```python
import torch

def _softmax(x, axis=-1):
    return torch.softmax(x, dim=axis)
def _gelu(x):
    return torch.nn.functional.gelu(x, approximate="tanh")
def _silu(x):
    return torch.nn.functional.silu(x)
def _sigmoid(x):
    return torch.sigmoid(x)
def _layernorm(x, axis=-1, eps=1e-5):
    m = x.mean(dim=axis, keepdim=True)
    v = x.var(dim=axis, keepdim=True, unbiased=False)
    return (x - m) / torch.sqrt(v + eps)
def _scatter_add(tgt, idx, src, axis=0):
    return tgt.clone().index_add_(axis, idx, src)
def _segment_sum(src, seg, n):
    out = torch.zeros((n,) + src.shape[1:], dtype=src.dtype)
    return out.index_add_(0, seg, src)
def _pad(x, pw):
    flat = tuple(v for pair in reversed(pw) for v in pair)
    return torch.nn.functional.pad(x, flat)

kernel_name = 'gated_mix__compressed_kv_cla__moe_arch_mod__prompt'

def setup_inputs():

    _w = (lambda a: (lambda t: t.to(torch.bfloat16) if t.is_floating_point() else t)(torch.as_tensor(a)))
    def _A_setup():
        B, H, Sq, Sk, D, M = 1, 16, 2048, 2048, 256, 448
        Dc = 4 * D
        return {
            "x": _w((lambda *s: torch.randn(*s, dtype=torch.bfloat16) * 0.05)(B, Sq, M)),
            "c_kv": _w((lambda *s: torch.randn(*s, dtype=torch.bfloat16) * 0.8)(B, Sk, Dc)),
            "w_q": _w((lambda *s: torch.randn(*s, dtype=torch.bfloat16) * 6.4)(M, H, D) * (M ** -0.5)),
            "w_uk": _w((lambda *s: torch.randn(*s, dtype=torch.bfloat16) * 6.4)(Dc, H, D) * (Dc ** -0.5)),
            "w_uv": _w((lambda *s: torch.randn(*s, dtype=torch.bfloat16) * 0.8)(Dc, H, D) * (Dc ** -0.5)),
            "w_o": _w((lambda *s: torch.randn(*s, dtype=torch.bfloat16) * 0.8)(H, D, M) * ((H * D) ** -0.5)),
        }
    _A_in = _A_setup()

    def _B_setup():
        B, S, D, F, C = 1, 2048, 448, 1792, 2
        return {
            "x": _w((lambda *s: torch.randn(*s, dtype=torch.bfloat16) * 0.05)(B, S, D)),
            "w_r": _w((lambda *s: torch.randn(*s, dtype=torch.bfloat16) * 3.2)(D)),
            "w_gate": _w((lambda *s: torch.randn(*s, dtype=torch.bfloat16) * 0.4)(D, F)),
            "w_up": _w((lambda *s: torch.randn(*s, dtype=torch.bfloat16) * 0.4)(D, F)),
            "w_down": _w((lambda *s: torch.randn(*s, dtype=torch.bfloat16) * 0.4)(F, D)),
        }
    _B_in = _B_setup()

    return {
        "A_x": _A_in["x"],
        "A_c_kv": _A_in["c_kv"],
        "A_w_q": _A_in["w_q"],
        "A_w_uk": _A_in["w_uk"],
        "A_w_uv": _A_in["w_uv"],
        "A_w_o": _A_in["w_o"],
        "B_x": _B_in["x"],
        "B_w_r": _B_in["w_r"],
        "B_w_gate": _B_in["w_gate"],
        "B_w_up": _B_in["w_up"],
        "B_w_down": _B_in["w_down"],
        "gate_w": _w((lambda *s: torch.randn(*s, dtype=torch.bfloat16) * 0.05)(448) * 16.0),
    }

def reference(A_x, A_c_kv, A_w_q, A_w_uk, A_w_uv, A_w_o, B_x, B_w_r, B_w_gate, B_w_up, B_w_down, gate_w):
    def _A_ref(x, c_kv, w_q, w_uk, w_uv, w_o):
        B, Sq, M = x.shape[0], x.shape[1], x.shape[2]
        Sk = c_kv.shape[1]
        H, D = w_q.shape[1], w_q.shape[2]
        q = torch.einsum("bqm,mhd->bhqd", x, w_q)
        k = torch.einsum("bkc,chd->bhkd", c_kv, w_uk)
        v = torch.einsum("bkc,chd->bhkd", c_kv, w_uv)
        scores = torch.einsum("bhqd,bhkd->bhqk", q, k) * (D ** -0.5)
        off = Sk - Sq
        qi = torch.arange(Sq)[:, None]
        kj = torch.arange(Sk)[None, :]
        cmask = kj <= (qi + off)
        scores = torch.where(cmask[None, None], scores, -1e9)
        p = _softmax(scores, axis=-1)
        ctx = torch.einsum("bhqk,bhkd->bhqd", p, v)
        return torch.einsum("bhqd,hdm->bqm", ctx, w_o)

    def _B_ref(x, w_r, w_gate, w_up, w_down):
        B, S, D = x.shape
        C = 2
        r = x @ w_r
        idx = torch.argsort(-r, axis=-1)[:, :C]
        idx_e = (lambda x, n, axis: torch.repeat_interleave(x, n, dim=axis))(idx.reshape(B, C, 1), D, axis=2)
        x_sel = (lambda x, i, axis: torch.take_along_dim(x, i, dim=axis))(x, idx_e, axis=1)
        g = _silu(x_sel @ w_gate)
        u = x_sel @ w_up
        y = (g * u) @ w_down
        r_sel = _sigmoid((lambda x, i, axis: torch.take_along_dim(x, i, dim=axis))(r, idx, axis=1))
        y = y * r_sel.reshape(B, C, 1)
        b_off = (torch.arange(B) * S).reshape(B, 1)
        flat_idx = (idx + b_off).reshape(B * C)
        zero = (x * 0.0).reshape(B * S, D)
        upd = _scatter_add(zero, flat_idx, y.reshape(B * C, D), axis=0)
        return x + upd.reshape(B, S, D)

    x = A_x
    g = _sigmoid((x * gate_w).sum(-1, keepdims=True))
    a = _A_ref(x, A_c_kv, A_w_q, A_w_uk, A_w_uv, A_w_o)
    b = _B_ref(x, B_w_r, B_w_gate, B_w_up, B_w_down)
    return g * a + (1 - g) * b
```

```cpp
#include <hip/hip_runtime.h>
#include <hip/hip_bf16.h>
#include <stdint.h>
#include <utility>

typedef __attribute__((ext_vector_type(4))) float f32x4;
typedef __attribute__((ext_vector_type(16))) float f32x16;
typedef __attribute__((ext_vector_type(4))) unsigned u32x4;
typedef __attribute__((ext_vector_type(2))) unsigned u32x2;
typedef unsigned short u16;

constexpr int S_ = 2048;
constexpr int M_ = 448;
constexpr int H_ = 16;
constexpr int D_ = 256;
constexpr int DC = 1024;
constexpr int HD = H_ * D_;
constexpr int F_ = 1792;
constexpr int FFN_N = F_ / 16;

constexpr size_t WS_Q = 0;
constexpr size_t WS_K = WS_Q + (size_t)S_ * HD * 2;
constexpr size_t WS_V = WS_K + (size_t)S_ * HD * 2;
constexpr size_t WS_Y = WS_V + (size_t)S_ * HD * 2;
constexpr size_t WS_X = WS_Y + (size_t)2 * H_ * 8 * 65536;
constexpr size_t WS_ML = WS_X + (size_t)2 * H_ * 8 * 65536;
constexpr size_t WS_WO = WS_ML + (size_t)H_ * 8 * 128 * 8;
constexpr size_t WS_GATE = WS_WO + (size_t)H_ * 262144;
constexpr size_t WS_R = WS_GATE + S_ * 4;
constexpr size_t WS_KEY = WS_R + S_ * 4;
constexpr size_t WS_IDX = WS_KEY + S_ * 8;
constexpr size_t WS_EP = WS_IDX + 768;
constexpr size_t WS_KVF = WS_EP + 256;
constexpr size_t WS_WF = WS_KVF + H_ * 256;
constexpr size_t WS_DF = WS_WF + (size_t)2 * H_ * 8 * 4 * 16;
constexpr size_t WS_BB = WS_DF + 4096;
constexpr size_t WS_YF = WS_BB + 2048;
constexpr size_t WS_END = WS_YF + (size_t)FFN_N * 2 * M_ * 4;
constexpr unsigned ML_SENT = 0xBF800000u;

typedef __attribute__((ext_vector_type(2))) float f32x2;
typedef __attribute__((ext_vector_type(2))) __bf16 bfx2;
__device__ __forceinline__ unsigned cvtpk(float lo, float hi) {
  bfx2 v = __builtin_convertvector((f32x2){lo, hi}, bfx2);
  return __builtin_bit_cast(unsigned, v);
}
__device__ __forceinline__ float rbf(float f) { return __uint_as_float(cvtpk(f, f) << 16); }
__device__ __forceinline__ unsigned lds_addr(const void* p) { return (unsigned)(uintptr_t)(const __attribute__((address_space(3))) char*)p; }
#define DI __device__ __forceinline__
typedef unsigned u32;
typedef __amdgpu_buffer_rsrc_t srd_t;
DI srd_t mk_srd(const void* p, unsigned bytes) { return __builtin_amdgcn_make_buffer_rsrc((void*)p, (short)0, (int)bytes, 0x00020000); }
DI u32 rfl(u32 x) { return __builtin_amdgcn_readfirstlane(x); }
DI int lane_id() { int l; asm volatile("v_mbcnt_lo_u32_b32 %0, -1, 0\n\tv_mbcnt_hi_u32_b32 %0, -1, %0" : "=v"(l)); return l; }
template <int... Is> using iseq = std::integer_sequence<int, Is...>;
template <int N> using make_iseq = std::make_integer_sequence<int, N>;
#define WT " sc0 sc1"
#define DV " sc1"
constexpr u32 OOB = 0x40000000u;
#define GAP() __builtin_amdgcn_sched_barrier(0)

template <int MO, int SO> DI void dma16(u32 m0base, u32 sbase, u32 voff, srd_t srd) {
  u32 t;
  asm volatile("s_add_u32 m0, %1, %2\n\ts_add_u32 %0, %3, %4\n\tbuffer_load_dwordx4 %5, %6, %0 offen lds"
               : "=&s"(t) : "s"(m0base), "i"(MO), "s"(sbase), "i"(SO), "v"(voff), "s"(srd) : "memory", "scc");
}
template <int MO, int SO, int J, bool FIRST = (J & 3) == 0> DI void dma16q(u32 m0base, u32 sbase, u32 voff, srd_t srd, u32& st) {
  if constexpr (FIRST)
    asm volatile("s_add_u32 m0, %1, %2\n\ts_add_u32 %0, %3, %4\n\tbuffer_load_dwordx4 %5, %6, %0 offen offset:%7" DV " lds"
                 : "=&s"(st) : "s"(m0base), "i"(MO + (J & 4) * 1024), "s"(sbase), "i"(SO + (J & 4) * 1024), "v"(voff), "s"(srd), "i"((J & 3) * 1024) : "memory", "scc");
  else
    asm volatile("buffer_load_dwordx4 %0, %1, %2 offen offset:%3" DV " lds" :: "v"(voff), "s"(srd), "s"(st), "i"((J & 3) * 1024) : "memory");
}
template <int OFF> DI void st16(u32x4 v, u32 voff, srd_t srd, u32 soff) {
  asm volatile("buffer_store_dwordx4 %0, %1, %2, %3 offen offset:%4" WT "\n\ts_nop 1" :: "v"(v), "v"(voff), "s"(srd), "s"(soff), "i"(OFF) : "memory");
}
#define CODE_WARM(SELF, END, m0, soff, voff) CODE_WARM_(SELF, END, m0, soff, voff)
#define CODE_WARM_(SELF, END, m0, soff, voff) do { \
    u32 lo_; int len_; unsigned long long pc_; \
    asm volatile("s_getpc_b64 %0\n.Lpc%=:\n\ts_mov_b32 %1, .Lpc%=-" #SELF "\n\ts_mov_b32 %2, " #END "-" #SELF : "=&s"(pc_), "=&s"(lo_), "=&s"(len_)); \
    dma16<0, 0>(m0, soff, voff, mk_srd((const void*)(pc_ - lo_), (len_ > 0 && len_ <= 393216) ? (u32)len_ & ~15u : 0u)); } while (0)
#define PSTAMP(i) do { } while (0)
#define ACL "a0","a1","a2","a3","a4","a5","a6","a7","a8","a9","a10","a11","a12","a13","a14","a15","a16","a17","a18","a19","a20","a21","a22","a23","a24","a25","a26","a27","a28","a29","a30","a31","a32","a33","a34","a35","a36","a37","a38","a39","a40","a41","a42","a43","a44","a45","a46","a47","a48","a49","a50","a51","a52","a53","a54","a55","a56","a57","a58","a59","a60","a61","a62","a63","a64","a65","a66","a67","a68","a69","a70","a71","a72","a73","a74","a75","a76","a77","a78","a79","a80","a81","a82","a83","a84","a85","a86","a87","a88","a89","a90","a91","a92","a93","a94","a95","a96","a97","a98","a99","a100","a101","a102","a103","a104","a105","a106","a107","a108","a109","a110","a111","a112","a113","a114","a115","a116","a117","a118","a119","a120","a121","a122","a123","a124","a125","a126","a127","a128","a129","a130","a131","a132","a133","a134","a135","a136","a137","a138","a139","a140","a141","a142","a143","a144","a145","a146","a147","a148","a149","a150","a151","a152","a153","a154","a155","a156","a157","a158","a159","a160","a161","a162","a163","a164","a165","a166","a167","a168","a169","a170","a171","a172","a173","a174","a175","a176","a177","a178","a179","a180","a181","a182","a183","a184","a185","a186","a187","a188","a189","a190","a191","a192","a193","a194","a195","a196","a197","a198","a199","a200","a201","a202","a203","a204","a205","a206","a207","a208","a209","a210","a211","a212","a213","a214","a215","a216","a217","a218","a219","a220","a221","a222","a223","a224","a225","a226","a227","a228","a229","a230","a231","a232","a233","a234","a235","a236","a237","a238","a239","a240","a241","a242","a243","a244","a245","a246","a247","a248","a249","a250","a251","a252","a253","a254","a255"
template <int R> DI float acc_rd() { float r; asm volatile("v_accvgpr_read_b32 %0, a[%c1]" : "=v"(r) : "i"(R)); return r; }
template <int R> DI void acc_wr(u32 v) { asm volatile("v_accvgpr_write_b32 a[%c1], %0" :: "v"(v), "i"(R) : ACL); }
template <int R> DI void acc_mul(float f) { float t; asm volatile("v_accvgpr_read_b32 %0, a[%c2]\n\tv_mul_f32 %0, %1, %0\n\tv_accvgpr_write_b32 a[%c2], %0" : "=&v"(t) : "v"(f), "i"(R) : ACL); }
template <int... Rs> DI void acc_mul_all(float f, iseq<Rs...>) { (acc_mul<Rs>(f), ...); }
template <int R> DI u32 acc_pk() {
  u32 w; float t;
  asm volatile("v_accvgpr_read_b32 %0, a[%c2]\n\tv_accvgpr_read_b32 %1, a[%c3]\n\tv_cvt_pk_bf16_f32 %0, %0, %1" : "=&v"(w), "=&v"(t) : "i"(R), "i"(R + 1));
  return w;
}
#define BF_LO(u) __uint_as_float((u) << 16)
#define BF_HI(u) __uint_as_float((u) & 0xffff0000u)
#define PINF(x) asm volatile("" : "+v"(x))
template <int CTRL> DI float dpp_add(float v) { return v + __uint_as_float(__builtin_amdgcn_update_dpp(0, __float_as_uint(v), CTRL, 0xf, 0xf, false)); }
template <bool W32> DI float sw_add(float a, float b) {
  if constexpr (W32) { const auto r = __builtin_amdgcn_permlane32_swap(__float_as_uint(a), __float_as_uint(b), false, false); return __uint_as_float(r[0]) + __uint_as_float(r[1]); }
  else { const auto r = __builtin_amdgcn_permlane16_swap(__float_as_uint(a), __float_as_uint(b), false, false); return __uint_as_float(r[0]) + __uint_as_float(r[1]); }
}
#define SPINB 18
constexpr u32 SPIN_MAX = 1u << SPINB;
constexpr int AT_X = 131072, AT_LDS = 163840;
constexpr int PJ_W0 = 65536;
struct PJ {
  u32x4 af[2][8];
  u32x2 wl[2][8], wh[2][8];
  u32 aKV[2], wb[8];
  u32 vAkv[4], vAq[4], vW[4], vWq[4], st;
  u32 m0A, m0W, m0Aq;
  u32 sAkv, sAq, sWkv, sWq;
  srd_t rA, rW, rX, rWq, rO;
  u32x4 pv[2], pl; float pt[2];
  u32 vO, sO, dO[5];
  u32x4 px[4]; float sg[2], sr[2], gv; u32 sRow, sM;
  const u16 *pwo, *pg, *pu, *pd; u32 sP, sPw, sPq, sPf, sPd, m0P, vP, vT;
  u32x2 wlo[4], whi[4]; u32 wra, sWo;
  u32 tag, sFl, vFl, poll;
  u32 sK0, sV0, vL, m0K;
  u32 wid;
  f32x16 qa[8];
  u32x2 ql[2][16], qh[2][16];
  u32x4 xf[2][4];
  u32 qb[4], xb[4];
  u32 nQf, sQf; u32x4 ones;
  u32 sQo; u32x2 iv;
};
template <bool VT> constexpr int pjT(int mt, int nt) { return VT ? 16 * (mt >> 1) + 2 * nt + (mt & 1) : 8 * mt + nt; }
template <bool VT> constexpr int pjMt(int T) { return VT ? 2 * (T >> 4) + (T & 1) : T >> 3; }
template <bool VT> constexpr int pjNt(int T) { return VT ? (T >> 1) & 7 : T & 7; }
template <bool SWAP, bool FIRST, int SET, int mt, int nt> DI void pj_mfma(PJ& s) {
  constexpr int ac = 4 * pjT<SWAP>(mt, nt);
  const u32x4 w = {s.wl[SET][nt][0], s.wl[SET][nt][1], s.wh[SET][nt][0], s.wh[SET][nt][1]};
  if constexpr (FIRST) {
    if constexpr (!SWAP) asm volatile("v_mfma_f32_16x16x32_bf16 a[%c2:%c3], %0, %1, 0" :: "v"(w), "v"(s.af[SET][mt]), "i"(ac), "i"(ac + 3) : ACL);
    else                 asm volatile("v_mfma_f32_16x16x32_bf16 a[%c2:%c3], %0, %1, 0" :: "v"(s.af[SET][mt]), "v"(w), "i"(ac), "i"(ac + 3) : ACL);
  } else {
    if constexpr (!SWAP) asm volatile("v_mfma_f32_16x16x32_bf16 a[%c2:%c3], %0, %1, a[%c2:%c3]" :: "v"(w), "v"(s.af[SET][mt]), "i"(ac), "i"(ac + 3) : ACL);
    else                 asm volatile("v_mfma_f32_16x16x32_bf16 a[%c2:%c3], %0, %1, a[%c2:%c3]" :: "v"(s.af[SET][mt]), "v"(w), "i"(ac), "i"(ac + 3) : ACL);
  }
}
template <int SET, int STG, int KS, int R> DI void pj_read(PJ& s) {
  if constexpr (R < 16) {
    constexpr int NT = R >> 1, OFF = STG * 32768 + KS * 16384;
    if constexpr ((R & 1) == 0) asm volatile("ds_read_b64_tr_b16 %0, %1 offset:%2" : "=v"(s.wl[SET][NT]) : "v"(s.wb[NT]), "i"(OFF) : "memory");
    else                        asm volatile("ds_read_b64_tr_b16 %0, %1 offset:%2" : "=v"(s.wh[SET][NT]) : "v"(s.wb[NT]), "i"(OFF + 2048) : "memory");
  } else if constexpr (R < 24) {
    constexpr int mt = R - 16;
    asm volatile("ds_read_b128 %0, %1 offset:%2" : "=v"(s.af[SET][mt]) : "v"(s.aKV[KS]), "i"(STG * 32768 + (mt & 1) * 2048 + (mt >> 1) * 8192) : "memory");
  }
}
template <int MO, int SO, int J, bool FIRST = (J == 0)> DI void dma16g(u32 m0base, u32 sbase, u32 voff, srd_t srd, u32& st) {
  if constexpr (FIRST)
    asm volatile("s_add_u32 m0, %1, %2\n\ts_add_u32 %0, %3, %4\n\tbuffer_load_dwordx4 %5, %6, %0 offen offset:%7 lds"
                 : "=&s"(st) : "s"(m0base), "i"(MO), "s"(sbase), "i"(SO), "v"(voff), "s"(srd), "i"(J * 1024) : "memory", "scc");
  else
    asm volatile("buffer_load_dwordx4 %0, %1, %2 offen offset:%3 lds" :: "v"(voff), "s"(srd), "s"(st), "i"(J * 1024) : "memory");
}
template <int STG, int KS, int J> DI void pj_dmaW(PJ& s, u32 sW) { dma16g<STG * 32768 + KS * 16384, KS * 262144, J>(s.m0W, sW, s.vW[J], s.rW, s.st); }
template <int STG, int J> DI void pj_dmaA(PJ& s, u32 sA) { dma16g<STG * 32768 + (J >> 2) * 4096, (J >> 2) * 65536, (J & 3)>(s.m0A, sA, s.vAkv[J & 3], s.rA, s.st); }
#define PJ_PIN(S) "+v"(s.wl[S][0]), "+v"(s.wl[S][1]), "+v"(s.wl[S][2]), "+v"(s.wl[S][3]), "+v"(s.wl[S][4]), "+v"(s.wl[S][5]), "+v"(s.wl[S][6]), "+v"(s.wl[S][7]), \
                  "+v"(s.wh[S][0]), "+v"(s.wh[S][1]), "+v"(s.wh[S][2]), "+v"(s.wh[S][3]), "+v"(s.wh[S][4]), "+v"(s.wh[S][5]), "+v"(s.wh[S][6]), "+v"(s.wh[S][7])
#define PJ_PIN4(S) PJ_PIN(S), "+v"(s.af[S][0]), "+v"(s.af[S][1]), "+v"(s.af[S][2]), "+v"(s.af[S][3])
#define PJ_PIN8(S) PJ_PIN4(S), "+v"(s.af[S][4]), "+v"(s.af[S][5]), "+v"(s.af[S][6]), "+v"(s.af[S][7])

DI void pj_wo_read(PJ& s) {
  asm volatile("ds_read_b64_tr_b16 %0, %8\n\tds_read_b64_tr_b16 %1, %8 offset:7168\n\tds_read_b64_tr_b16 %2, %8 offset:256\n\tds_read_b64_tr_b16 %3, %8 offset:7424\n\t"
               "ds_read_b64_tr_b16 %4, %8 offset:512\n\tds_read_b64_tr_b16 %5, %8 offset:7680\n\tds_read_b64_tr_b16 %6, %8 offset:768\n\tds_read_b64_tr_b16 %7, %8 offset:7936"
               : "=&v"(s.wlo[0]), "=&v"(s.whi[0]), "=&v"(s.wlo[1]), "=&v"(s.whi[1]), "=&v"(s.wlo[2]), "=&v"(s.whi[2]), "=&v"(s.wlo[3]), "=&v"(s.whi[3]) : "v"(s.wra) : "memory");
}
template <int Q> DI void pj_wo_store(PJ& s) {
  if constexpr (Q == 0) asm volatile("" : "+v"(s.wlo[0]), "+v"(s.whi[0]), "+v"(s.wlo[1]), "+v"(s.whi[1]), "+v"(s.wlo[2]), "+v"(s.whi[2]), "+v"(s.wlo[3]), "+v"(s.whi[3]));
  const u32 nt = s.wid + 4 * Q;
  const u32 so = nt < 14 ? s.sWo + nt * 1024 + (nt >= 7 ? 131072 - 7168 : 0) : OOB;
  st16<0>((u32x4){s.wlo[Q][0], s.wlo[Q][1], s.whi[Q][0], s.whi[Q][1]}, s.vP, s.rO, rfl(so));
}
template <int U> DI void gr_prod(PJ& s) {
  constexpr int e = U >> 1, rI = U & 1;
  const float gw = (e & 1) ? BF_HI(s.px[2][e >> 1]) : BF_LO(s.px[2][e >> 1]), wr = (e & 1) ? BF_HI(s.px[3][e >> 1]) : BF_LO(s.px[3][e >> 1]);
  const float xv = (e & 1) ? BF_HI(s.px[rI][e >> 1]) : BF_LO(s.px[rI][e >> 1]);
  if constexpr (e == 0) { s.sg[rI] = rbf(xv * gw); s.sr[rI] = xv * wr; } else { s.sg[rI] += rbf(xv * gw); s.sr[rI] = __builtin_fmaf(xv, wr, s.sr[rI]); }
  PINF(s.sg[rI]); PINF(s.sr[rI]);
}
template <int U> DI void gr_red(PJ& s) {
  if constexpr (U == 0) { s.sg[0] = sw_add<true>(s.sg[0], s.sg[1]); s.sr[0] = sw_add<true>(s.sr[0], s.sr[1]); PINF(s.sg[0]); PINF(s.sr[0]); }
  if constexpr (U == 1) { s.gv = sw_add<false>(s.sg[0], s.sr[0]); PINF(s.gv); }
  if constexpr (U >= 2 && U < 6) { s.gv = dpp_add<(U == 2 ? 0x128 : U == 3 ? 0x124 : U == 4 ? 0x122 : 0x121)>(s.gv); PINF(s.gv); }
  if constexpr (U == 6) { s.gv = rbf(s.gv); s.sg[0] = 1.f + __expf(-s.gv); PINF(s.gv); PINF(s.sg[0]); }
  if constexpr (U == 7) { s.sg[0] = rbf(__builtin_amdgcn_rcpf(s.sg[0])); PINF(s.sg[0]); }
}
DI void gr_store(PJ& s) {
  const int lane = lane_id(), rt = (lane >> 4) & 1;
  const u32 row = s.sRow + (lane >> 5);
  const bool ld = (lane & 15) == 0;
  const float v = rt ? s.gv : s.sg[0];
  const unsigned u = __float_as_uint(s.gv + 0.f) & 0xffff0000u;
  const u32x2 key = {((u & 0x80000000u) ? (~u & 0xffff0000u) : (u | 0x80000000u)) | (65535u - row), s.tag};
  const u32x2 ml = {0u, ML_SENT};
  asm volatile("buffer_store_dword %0, %1, %6, %7 offen" WT "\n\tbuffer_store_dwordx2 %2, %3, %6, %8 offen" WT "\n\tbuffer_store_dwordx2 %4, %5, %6, %9 offen" WT "\n\ts_nop 0"
               :: "v"(v), "v"(ld ? row * 4 + rt * 8192 : OOB), "v"(key), "v"((ld && rt) ? row * 8 : OOB), "v"(ml), "v"(lane < 32 ? (u32)lane * 8 : OOB),
                  "s"(s.rO), "s"((u32)WS_GATE), "s"((u32)WS_KEY), "s"(s.sM) : "memory");
}
template <bool SWAP, bool FIRST, int STG, bool EX, int M> DI void pj_gap0(PJ& s, u32 sWn, u32 sWd) {
  pj_mfma<SWAP, FIRST, 0, (M & 7), (M >> 3)>(s);
  if constexpr ((M & 1) == 0) pj_read<1, STG, 1, M / 2>(s);
  if constexpr (M % 8 == 5) {
    constexpr int q = M / 8;
    if constexpr (q < 4) pj_dmaW<STG ^ 1, 1, q>(s, sWn); else pj_dmaW<STG, 0, q - 4>(s, sWd);
  }
  if constexpr (EX && (M & 3) == 3) gr_prod<(M >> 2)>(s);
  if constexpr (EX) GAP();
}
template <bool SWAP, int STG, bool EX, int M> DI void pj_gap1(PJ& s, u32 sA) {
  pj_mfma<SWAP, false, 1, (M & 7), (M >> 3)>(s);
  if constexpr ((M & 1) == 0) pj_read<0, STG ^ 1, 0, M / 2>(s);
  if constexpr (M % 8 == 5) pj_dmaA<STG, M / 8>(s, sA);
  if constexpr (EX && (M & 3) == 3 && (M >> 2) < 8) gr_red<(M >> 2)>(s);
  if constexpr (EX) GAP();
}
template <bool SWAP, bool FIRST, int STG, bool EX, int VM, int... Ms> DI void pj_sub0(PJ& s, u32 sWn, u32 sWd, iseq<Ms...>) {
  asm volatile("s_waitcnt vmcnt(%24) lgkmcnt(0)\n\ts_barrier" : PJ_PIN8(0) : "i"(VM) : "memory");
  (pj_gap0<SWAP, FIRST, STG, EX, Ms>(s, sWn, sWd), ...);
}
template <bool SWAP, int STG, bool EX, int VM, int... Ms> DI void pj_sub1(PJ& s, u32 sA, iseq<Ms...>) {
  asm volatile("s_waitcnt vmcnt(%24) lgkmcnt(0)\n\ts_barrier" : PJ_PIN8(1) : "i"(VM) : "memory");
  (pj_gap1<SWAP, STG, EX, Ms>(s, sA), ...);
}
template <bool SWAP, bool FIRST, int STG, int P0, int P1, bool EX = false>
DI void pj_step(PJ& s, u32 sWn, u32 sWd, u32 sAd, srd_t rT, u32 sT0, u32 sT1, u32 vT) {
  pj_sub0<SWAP, FIRST, STG, EX, 4 + 8 + P0 + P1>(s, sWn, sWd, make_iseq<64>{});
  dma16<0, 0>(s.m0P, sT0, vT, rT);
  pj_sub1<SWAP, STG, EX, P1 + 4 + 4 + 1>(s, sAd, make_iseq<64>{});
  dma16<0, 0>(s.m0P, sT1, vT, rT);
}
template <int STG, int KS, int... Js> DI void pj_dmaW_all(PJ& s, u32 sW, iseq<Js...>) { (pj_dmaW<STG, KS, Js>(s, sW), ...); }
template <int STG, int... Js> DI void pj_dmaA_all(PJ& s, u32 sA, iseq<Js...>) { (pj_dmaA<STG, Js>(s, sA), ...); }
template <int... Rs> DI void pj_read_all(PJ& s, iseq<Rs...>) { (pj_read<0, 0, 0, Rs>(s), ...); }

constexpr int Z0 = 24, ZQ = 32, ZA = 46;
constexpr int zGaps(int z) { return z < ZQ ? 64 : z < ZA ? 16 : 32; }
constexpr int zBar(int z) { return z < ZQ ? 0 : z == ZQ + 12 ? -1 : z < ZA ? 8 : 24; }
#define ZF0 34
#define ZC0 40
constexpr int ZF3 = ZA, ZB = 47, ZE = 48;
#define QPAT "W.S.x.x.S.W.W.W."
constexpr char qPat(int M) { return QPAT[M]; }
constexpr int qCnt(char c, int M) { int n = 0; for (int i = 8; (i & 15) != M; ++i) n += qPat(i & 15) == c; return n; }
constexpr int zD(int z, int M) {
  if (z < ZQ) {
    if (z == 31) return (M & 1) && M < 8 ? 8 * 35 + M / 2 : -1;
    if (M % 8 != 5) return -1;
    const int q = M / 8;
    if (!(z & 1)) return 8 * (z + 3 + (q >> 2)) + (q & 3);
    return z < 29 ? 512 + 8 * (z / 2 + 2) + q : 512 + 8 * (16 + (q >> 2)) + (q & 3);
  }
  if (z == ZA) return M == 0 ? 1024 + 23 : -1;
  if (z == ZB) return M & 1 ? -1 : 1024 + 24 + M / 2;
  if (z == ZE) return M % 4 == 2 ? 1024 + 40 + M / 4 : -1;
  const int iv = z - ZQ - (M < 8);
  const char c = qPat(M);
  if (c == 'W' && iv >= 0 && iv <= 9) return 8 * (ZQ + iv + 4) + qCnt('W', M);
  if (c == 'x' && iv <= 8) return 512 + 8 * (18 + (iv + 1) / 2) + 2 * ((iv + 1) & 1) + qCnt('x', M);
  if (iv < 9 || (c != 'W' && c != 'x')) return -1;
  return 1024 + 6 * (iv - 10) + 2 + qCnt('W', M) + qCnt('x', M);
}
constexpr bool zDfirst(int z, int M) {
  const int d = zD(z, M);
  if ((d & 3) == 0) return true;
  for (;;) { if (--M < 0) { --z; M = zGaps(z) - 1; } if (zD(z, M) >= 0) return zD(z, M) != d - 1; }
}
constexpr int zS(int z, int M) {
  if (z == 31) return M % 8 == 7 ? M / 8 : -1;
  if (z < ZQ || z >= ZA || qPat(M) != 'S') return -1;
  const int iv = z - ZQ - (M < 8);
  return iv >= 0 && iv < 12 ? 8 + 2 * iv + qCnt('S', M) : -1;
}
constexpr int zPk(int z, int M) {
  if (z < ZQ || !(M & 1)) return -1;
  const int d = 32 + 8 * (z - ZQ) + M / 2;
  return d < 128 ? d : -1;
}
constexpr int zWo(int z, int M) { return (z == Z0 + 1 && M >= 41 && M % 6 == 5) ? (M - 41) / 6 : -1; }
constexpr int zPoll(int z, int M) { return 2 * (z == ZC0 - 2 && M == 9) + (z == ZE && (M == 15 || M == 17)); }
constexpr int qcvN(int J) { const int n = J <= 5 ? 0 : ((J - 5) * 64 + 55) / 56; return n > 64 ? 64 : n; }
constexpr bool qcv_ok() {
  for (int k = 0; k < 64; ++k) { int J = 0; while (qcvN(J + 1) <= k) ++J; if (J < 4 * (k >> 3) + 5 || J > 30 + 2 * (k >> 2)) return false; }
  return true;
}
static_assert(qcv_ok(), "q conversion: two products behind the tile's last, two gaps before the fragment's use");
constexpr int zJ(int z, int M) { return z >= ZA ? 32 * (z - ZA + 1) + M : z >= ZQ + 12 ? 16 * (z - ZQ - 12) + M : -1; }
#define QS0 19
#define QSD 3
constexpr int zQs(int z, int M) { const int J = zJ(z, M) - QS0; return J >= 0 && J % QSD == 0 && J / QSD < 16 ? J / QSD : -1; }
constexpr bool qs_ok() { for (int f = 0; f < 16; ++f) if (qcvN(QS0 + QSD * f) < 4 * f + 4) return false; return QS0 + QSD * 7 < 56 && QS0 + QSD * 15 < 80; }
static_assert(qs_ok(), "q rows are stored behind their conversion");
constexpr int zFlag(int z, int M) { return (z == ZF0 && M == 8) + (z == ZA && M == 24); }
constexpr int zOps(int z, int M) { return zFlag(z, M) + (zD(z, M) >= 0) + (zS(z, M) >= 0) + (zWo(z, M) >= 0) + (zQs(z, M) >= 0) + zPoll(z, M); }
constexpr int zYoung(int z0, int M0, int KIND, int z1, int M1) {
  int n = (KIND < 1 ? (zS(z0, M0) >= 0) : 0) + (KIND < 2 ? (zWo(z0, M0) >= 0) + (zQs(z0, M0) >= 0) : 0) + (KIND < 3 ? zPoll(z0, M0) : 0);
  for (int z = z0; z <= z1; ++z)
    for (int M = z == z0 ? M0 + 1 : 0; M < (z == z1 ? M1 : zGaps(z)); ++M) n += zOps(z, M);
  return n > 63 ? 63 : n;
}
constexpr int zYoungD(int d, int z1, int M1) { for (int z = Z0; z <= ZE; ++z) for (int M = 0; M < zGaps(z); ++M) if (zD(z, M) == d) return (z < z1 || (z == z1 && M < M1)) ? zYoung(z, M, 0, z1, M1) : -1000; return -1000; }
constexpr int zYoungS(int I, int z1, int M1) { for (int z = Z0; z <= ZA; ++z) for (int M = 0; M < zGaps(z); ++M) if (zS(z, M) == I) return (z < z1 || (z == z1 && M < M1)) ? zYoung(z, M, 1, z1, M1) : -1000; return -1000; }
constexpr int zYoungQ(int F, int z1, int M1) { for (int z = Z0; z <= ZE; ++z) for (int M = 0; M < zGaps(z); ++M) if (zQs(z, M) == F) return zYoung(z, M, 2, z1, M1); return -1000; }
constexpr int imin(int a, int b) { return a < b ? a : b; }
constexpr int imax(int a, int b) { return a > b ? a : b; }
constexpr int zWait(int z) {
  if (z == Z0) return 4 + 1 + 8 + 2;
  if (z == Z0 + 1) return 2 + 8;
  const int B = zBar(z); int n = 63;
  if (z < ZQ + 12) n = imin(n, zYoungD(8 * (z + 1) + 3, z, B));
  if (z == ZQ + 11) n = imin(n, zYoungD(8 * (z + 2) + 3, z, B));
  if ((z & 1) && z < ZQ + 12) n = imin(n, zYoungD(512 + 8 * (z + 1) / 2 + (z + 1 < ZQ ? 7 : 3), z, B));
  if (z == ZF0) n = imin(n, zYoungS(7, z, B));
  if (z == ZF3) n = imin(n, zYoungS(31, z, B));
  if (z == ZC0) n = imin(n, zYoung(ZC0 - 2, 9, 3, z, B));
  if (z == ZQ + 13) n = imin(n, zYoungD(1024 + 7, z, B));
  if (z == ZA) n = imin(n, zYoungD(1024 + 15, z, B));
  if (z == ZB) n = imin(n, zYoungD(1024 + 23, z, B));
  if (z == ZE) n = imin(n, zYoungD(1024 + 39, z, B));
  return n;
}
constexpr bool z_ok() {
  for (int z = Z0; z <= ZE; ++z) if (zBar(z) >= 0 && zWait(z) < 0) return false;
  int pk = 32, st = 8;
  for (int z = ZQ; z < ZA; ++z) for (int M = 0; M < 16; ++M) {
    if (zS(z, M) >= 0) { if (zS(z, M) != st || pk < 4 * st + 4) return false; ++st; }
    if (zPk(z, M) >= 0) { if (zPk(z, M) != pk || pk / 4 >= st + 2) return false; ++pk; }
  }
  return pk == 128 && st == 32 && ZC0 <= ZQ + 9 && zD(ZQ + 13, 14) == 1024 + 22 && zD(ZQ + 11, 7) < 1024 + 16;
}
static_assert(z_ok(), "what a barrier waits for was requested before it");
DI void kv_check(u32& poll, u32 tag, unsigned long long need, u32 vFl, srd_t r, u32 sFl) {
  unsigned long long t; u32 n = SPIN_MAX;
  asm volatile(".Lk%=:\n\tv_cmp_eq_u32_e32 vcc, %3, %0\n\ts_nop 3\n\ts_and_b64 %1, vcc, %4\n\ts_cmp_eq_u64 %1, %4\n\ts_cbranch_scc1 .Lg%=\n\t"
               "s_sub_u32 %2, %2, 1\n\ts_cbranch_scc1 .Lg%=\n\t"
               "s_sleep 1\n\tbuffer_load_dword %0, %5, %6, %7 offen sc0 sc1\n\ts_waitcnt vmcnt(0)\n\ts_branch .Lk%=\n.Lg%=:"
               : "+v"(poll), "=&s"(t), "+s"(n) : "s"(tag), "s"(need), "v"(vFl), "s"(r), "s"(sFl) : "memory", "scc", "vcc");
}
DI void kv_check_all(u32& poll, u32 tag, u32 vFl, srd_t r, u32 sFl) {
  u32 n = SPIN_MAX;
  asm volatile(".Lk%=:\n\tv_cmp_eq_u32_e32 vcc, %2, %0\n\ts_nop 3\n\ts_cmp_eq_u64 vcc, -1\n\ts_cbranch_scc1 .Lg%=\n\t"
               "s_sub_u32 %1, %1, 1\n\ts_cbranch_scc1 .Lg%=\n\t"
               "s_sleep 1\n\tbuffer_load_dword %0, %3, %4, %5 offen sc0 sc1\n\ts_waitcnt vmcnt(0)\n\ts_branch .Lk%=\n.Lg%=:"
               : "+v"(poll), "+s"(n) : "s"(tag), "v"(vFl), "s"(r), "s"(sFl) : "memory", "scc", "vcc");
}
DI void kv_poll(u32& poll, u32 vFl, srd_t r, u32 sFl) { asm volatile("buffer_load_dword %0, %1, %2, %3 offen sc0 sc1" : "=v"(poll) : "v"(vFl), "s"(r), "s"(sFl) : "memory"); }
template <int Z, int M> DI void z_dma(PJ& s) {
  constexpr int d = zD(Z, M);
  constexpr bool F = zDfirst(Z, M);
  if constexpr (d >= 1024) {
    constexpr int p = d - 1024, J = p & 7;
    dma16q<(p < 8 ? AT_X : p < 16 ? 32768 : 98304), (p >= 8 && p < 16 ? 32768 : 0), J, F>(s.m0K, p < 16 ? s.sK0 : s.sV0, s.vL, s.rO, s.st);
  } else if constexpr (d >= 512) {
    constexpr int g = (d - 512) >> 3, J = d & 7;
    if constexpr (g < 16) dma16g<(g & 1) * 32768 + (J >> 2) * 4096, g * 128 + (J >> 2) * 65536, (J & 3)>(s.m0A, s.sAkv, s.vAkv[J & 3], s.rA, s.st);
    else dma16g<(g & 1) * 16384, (g - 16) * 128, J, F>(s.m0Aq, s.sAq, s.vAq[J], s.rX, s.st);
  } else if constexpr (d >= 0) {
    constexpr int v = d >> 3, J = d & 7;
    if constexpr (v < ZQ) dma16g<(v & 3) * 16384, v * 262144, J>(s.m0W, s.sWkv, s.vW[J], s.rW, s.st);
    else dma16g<(v & 3) * 16384, (v - ZQ) * 262144, J>(s.m0W, s.sWq, s.vWq[J], s.rWq, s.st);
  }
}
#define STMASK 0xffffffffu
template <int I> DI void z_store(PJ& s, u32x4 v) {
  u32 vO = s.vO; if constexpr (!((STMASK >> I) & 1)) vO |= OOB;
  if constexpr (I == 0) asm volatile("buffer_store_dwordx4 %0, %1, %2, %3 offen" WT :: "v"(v), "v"(vO), "s"(s.rO), "s"(s.sO) : "memory");
  else asm volatile("s_add_u32 %0, %0, %4\n\tbuffer_store_dwordx4 %1, %2, %3, %0 offen" WT
                    : "+s"(s.sO) : "v"(v), "v"(vO), "s"(s.rO), "s"(s.dO[(I & 3) ? 0 : (I & 7) ? 1 : 1 + I / 8]) : "memory", "scc");
}
template <int LV> DI void z_flag(PJ& s) {
  asm volatile("buffer_store_dword %0, %1, %2, %3 offen offset:%4" WT :: "v"(s.tag), "v"(s.vP ? OOB : 0u), "s"(s.rO), "s"(s.wid ? OOB : s.sFl), "i"(4 * LV) : "memory");
}
template <int U, int F, int H> DI void q_read(PJ& s) {
  constexpr bool JT = U >= 12;
  constexpr int dt = JT ? F >> 2 : F & 7, v = JT ? 12 + ((F >> 1) & 1) : U, kk = JT ? F & 1 : F >> 3, set = JT ? F >> 4 : U & 1, i = F & 15;
  constexpr int OFF = (v & 3) * 16384 + kk * 8192 + (dt >> 2) * 256 + H * 2048;
  if constexpr (H == 0) asm volatile("ds_read_b64_tr_b16 %0, %1 offset:%2" : "=v"(s.ql[set][i]) : "v"(s.qb[dt & 3]), "i"(OFF) : "memory");
  else                  asm volatile("ds_read_b64_tr_b16 %0, %1 offset:%2" : "=v"(s.qh[set][i]) : "v"(s.qb[dt & 3]), "i"(OFF) : "memory");
}
template <int G, int KK> DI void x_read(PJ& s) { asm volatile("ds_read_b128 %0, %1 offset:%2" : "=v"(s.xf[G & 1][KK]) : "v"(s.xb[KK]), "i"((G & 1) * 16384) : "memory"); }
template <bool VT, int Z, int M> DI void z_gap(PJ& s) {
  constexpr int SUB = Z & 1, STG = (Z >> 1) & 1;
  if constexpr (M == 0) {
    asm volatile("s_waitcnt vmcnt(%24) lgkmcnt(0)\n\ts_barrier" : PJ_PIN8(SUB) : "i"(zWait(Z)) : "memory");
  }
  if constexpr (Z == 30 && M == 48) asm volatile("s_waitcnt lgkmcnt(6)" : PJ_PIN(1), "+v"(s.af[1][0]), "+v"(s.af[1][1]) :: "memory");
  if constexpr (Z == 30) {
    if constexpr (M < 48) pj_mfma<VT, false, 0, M % 6, M / 6>(s); else pj_mfma<VT, false, 1, pjMt<VT>(M - 48), pjNt<VT>(M - 48)>(s);
  } else if constexpr (Z == 31) {
    constexpr int T = M < 16 ? 16 + M : M < 32 ? 32 + M : M;
    pj_mfma<VT, false, (M >= 16 && M < 32) ? 0 : 1, pjMt<VT>(T), pjNt<VT>(T)>(s);
  } else pj_mfma<VT, false, SUB, (M & 7), (M >> 3)>(s);
  if constexpr (Z < 31) { if constexpr ((M & 1) == 0) pj_read<SUB ^ 1, STG ^ SUB, SUB ^ 1, M / 2>(s); }
  else {
    if constexpr ((M & 1) && M >= 9 && M < 49) { constexpr int r = (M - 9) / 2; if constexpr (r < 4) x_read<16, r>(s); else q_read<0, (r - 4) / 2, (r - 4) & 1>(s); }
    if constexpr ((M & 1) == 0) asm volatile("v_accvgpr_read_b32 %0, a[%c2]\n\tv_accvgpr_read_b32 %1, a[%c3]" : "=v"(s.pt[0]), "=v"(s.pt[1]) : "i"(M), "i"(M + 1));
    else { u32 w; asm volatile("v_cvt_pk_bf16_f32 %0, %1, %2" : "=v"(w) : "v"(s.pt[0]), "v"(s.pt[1])); s.pv[0][(M >> 1) & 3] = w; }
  }
  z_dma<Z, M>(s);
  if constexpr (Z == 31 && M > 0 && zS(Z, M - 1) >= 0) asm volatile("" :: "v"(s.pl));
  if constexpr (zS(Z, M) >= 0) { z_store<zS(Z, M)>(s, s.pv[0]); s.pl = s.pv[0]; }
  if constexpr (Z == Z0 && M == 51) pj_wo_read(s);
  if constexpr (zWo(Z, M) >= 0) pj_wo_store<zWo(Z, M)>(s);
  if constexpr (Z == 31) GAP();
}
template <bool VT, int Z, int... Ms> DI void z_sub(PJ& s, iseq<Ms...>) { (z_gap<VT, Z, Ms>(s), ...); }
template <bool VT, int... Zs> DI void z_all(PJ& s, iseq<Zs...>) { (z_sub<VT, Z0 + Zs>(s, make_iseq<64>{}), ...); }

template <bool VT> DI void pj_body1(PJ& s) {
  constexpr u32 WS = 524288;
  asm volatile("s_waitcnt vmcnt(16) lgkmcnt(0)\n\ts_barrier" : "+v"(s.px[0]), "+v"(s.px[1]), "+v"(s.px[2]), "+v"(s.px[3]), "+s"(s.tag) :: "memory");
  s.tag = rfl(s.tag * 1664525u + 1013904223u);
  asm volatile("" : "+s"(s.tag));
  pj_read_all(s, make_iseq<24>{});
  PSTAMP(1);
  pj_step<VT, true, 0, 0, 0, true>(s, s.sWkv + 1 * WS, s.sWkv + 2 * WS, s.sAkv + 2 * 128, s.rA, s.sP, s.sP + 1048576u, s.vP);
  gr_store(s);
  pj_step<VT, false, 1, 1, 4>(s, s.sWkv + 2 * WS, s.sWkv + 3 * WS, s.sAkv + 3 * 128, s.rA, s.sP + 2097152u, s.sP + 3145728u, s.vP);
  CODE_WARM(k_main, .Lk_code_end, s.m0P, ((blockIdx.x >> 3) * 4 + s.wid) * 1024 + 131072, s.vP);
  for (int t = 2; t < Z0 / 2; t += 2) {
    PSTAMP(1 + t / 2);
    const bool ff = t == 10;
    const srd_t rT0 = t < 8 ? s.rW : t == 8 ? s.rWq : mk_srd(s.pg, M_ * F_ * 2), rT1 = ff ? mk_srd(s.pu, M_ * F_ * 2) : rT0;
    const u32 dT = t < 8 ? WS : ff ? 917504u : 1048576u, sT0 = t < 8 ? s.sPw + (2 * t + 1) * WS : t == 8 ? s.sPq : s.sPf, sT1 = ff ? sT0 : sT0 + 2 * dT, vT = ff ? s.vT : s.vP;
    const srd_t rP = t == 6 ? s.rX : ff ? mk_srd(s.pd, M_ * F_ * 2) : mk_srd(s.pwo, t < 6 ? 16 * M_ * 2 : 0);
    const u32 m3 = s.m0P + (t < 6 ? (u32)(t - 1) * 4096u : 0u), s3 = ff ? s.sPd : t >= 6 ? s.sP : (s.sP & 3072u) + (u32)(t - 2) * 4096u, d3 = t < 6 ? 4096u : 0u, e3 = ff ? 131072u : t >= 6 ? 1048576u : 4096u;
    pj_step<VT, false, 0, 1, 2>(s, s.sWkv + (t + 1) * WS, s.sWkv + (t + 2) * WS, s.sAkv + (t + 2) * 128, rT0, sT0, sT0 + dT, vT);
    dma16<0, 0>(m3, s3, s.vP, rP);
    pj_step<VT, false, 1, 1, 2>(s, s.sWkv + (t + 1) * WS + WS, s.sWkv + (t + 2) * WS + WS, s.sAkv + (t + 3) * 128, rT1, sT1, sT1 + dT, vT);
    dma16<0, 0>(m3 + d3, s3 + e3, s.vP, rP);
    asm volatile("s_cmp_eq_u32 %0, 0\n\ts_cbranch_scc1 .Lq%=\n\tbuffer_store_dwordx4 %2, %3, %4, %1 offen" WT "\n\tbuffer_store_dwordx4 %2, %3, %4, %1 offen offset:1024" WT "\n\t"
                 "buffer_store_dwordx4 %2, %3, %4, %1 offen offset:2048" WT "\n\tbuffer_store_dwordx4 %2, %3, %4, %1 offen offset:3072" WT "\n\ts_add_u32 %1, %1, 4096\n\ts_sub_u32 %0, %0, 1\n.Lq%=:"
                 : "+s"(s.nQf), "+s"(s.sQf) : "v"(s.ones), "v"(s.vP), "s"(s.rO) : "memory", "scc");
  }
}
#define Q_PIN4(S, F) "+v"(s.ql[S][F]), "+v"(s.qh[S][F]), "+v"(s.ql[S][F + 1]), "+v"(s.qh[S][F + 1]), "+v"(s.ql[S][F + 2]), "+v"(s.qh[S][F + 2]), "+v"(s.ql[S][F + 3]), "+v"(s.qh[S][F + 3])
#define X_PIN(S) "+v"(s.xf[S][0]), "+v"(s.xf[S][1]), "+v"(s.xf[S][2]), "+v"(s.xf[S][3])
template <bool VT> DI void pj_body2(PJ& s) {
  PSTAMP(8);
  z_all<VT>(s, make_iseq<8>{});
  asm volatile("s_waitcnt lgkmcnt(0)\n\ts_nop 0" : Q_PIN4(0, 0), Q_PIN4(0, 4), X_PIN(0) : "v"(s.pl) : "memory");
}

constexpr int qrN(int u, int M) {
  if (u >= 12) { const int J = 16 * (u - 12) + M, n = 16 + (5 * J + 1) / 2; return 384 + (n > 64 ? 64 : n); }
  return 32 * u + (M <= 6 ? 16 + (5 * M + 1) / 2 : M <= 8 ? 32 : 32 + 2 * (M - 8));
}
static_assert(qrN(0, 0) == 16 && qrN(0, 16) == qrN(1, 0) && qrN(11, 16) == qrN(12, 0) && qrN(12, 16) == qrN(13, 0) && qrN(13, 5) == 448, "read sequence");
constexpr int qrX(int u, int M) { return ((u & 1) && u < 12 && M >= 8 && M < 12) ? M - 8 : -1; }
constexpr int qrYoung(int u, int M, int h) {
  int n = qrN(u, M) - h - 1;
  for (int g = 0; g < 16 * u + M; ++g) if (qrX(g / 16, g % 16) >= 0 && qrN(g / 16, g % 16) > h) ++n;
  return n;
}
template <int H0, int... Hs> DI void q_reads(PJ& s, iseq<Hs...>) {
  (q_read<((H0 + Hs) >= 384 ? 12 : (H0 + Hs) / 32), ((H0 + Hs) >= 384 ? (H0 + Hs - 384) / 2 : ((H0 + Hs) / 2) & 15), (H0 + Hs) & 1>(s), ...);
}
template <int K, int B = 192> DI void q_cv(PJ& s) {
  float t0, t1;
  asm volatile("v_mul_f32_e32 %0, %4, %2\n\tv_mul_f32_e32 %1, %4, %3\n\tv_cvt_pk_bf16_f32 %0, %0, %1\n\tv_accvgpr_write_b32 a[%c5], %0"
               : "=&v"(t0), "=&v"(t1) : "v"(s.qa[K >> 3][2 * (K & 7)]), "v"(s.qa[K >> 3][2 * (K & 7) + 1]), "s"(0.0625f * 1.4426950408889634f), "i"(B + K) : ACL);
}
template <int J> DI void q_cvs(PJ& s) {
  if constexpr (qcvN(J + 1) > qcvN(J)) q_cv<qcvN(J)>(s);
  if constexpr (qcvN(J + 1) > qcvN(J) + 1) q_cv<qcvN(J) + 1>(s);
}
template <int F> DI void q_store(u32 vl16, srd_t r, u32 so) {
  u32 t;
  asm volatile("s_add_u32 %0, %3, %4\n\tbuffer_store_dwordx4 a[%c5:%c6], %1, %2, %0 offen offset:%7" WT
               : "=&s"(t) : "v"(vl16), "s"(r), "s"(so), "i"((F >> 2) * 4096), "i"(192 + 4 * F), "i"(195 + 4 * F), "i"((F & 3) * 1024) : "memory", "scc");
}
template <int F> DI void at_kread0(u32x4 (&kf)[32], u32 kb0) {
  asm volatile("ds_read_b128 %0, %1 offset:%2" : "=v"(kf[F]) : "v"(kb0), "i"((F >> 1) * 2048 + (F & 1) * 512) : "memory");
}
template <int U, int M> DI void q_gap(PJ& s, u32x4 (&kf)[32], u32 kb0) {
  constexpr int Z = ZQ + U, J = zJ(Z, M);
  constexpr bool JT = U >= 12;
  constexpr int F = JT ? J : M, dt = JT ? F >> 2 : F & 7, kk = JT ? F & 3 : 2 * (U & 1) + (F >> 3), set = JT ? F >> 4 : U & 1, i = F & 15;
  if constexpr (M == zBar(Z)) {
    if constexpr (Z == ZC0) asm volatile("s_waitcnt vmcnt(%18) lgkmcnt(0)\n\ts_barrier" : Q_PIN4(set, 8), Q_PIN4(set, 12), "+v"(s.poll), "+v"(s.iv) : "i"(zWait(Z)) : "memory");
    else asm volatile("s_waitcnt vmcnt(%16) lgkmcnt(0)\n\ts_barrier" : Q_PIN4(set, 8), Q_PIN4(set, 12) : "i"(zWait(Z)) : "memory");
    if constexpr (Z == ZF0) z_flag<0>(s);
    if constexpr (Z == ZC0) kv_check_all(s.poll, s.tag, s.vFl, s.rO, s.sFl & ~255u);
  } else if constexpr (M % 4 == 0 && (JT ? J < 24 : M < 8) && !(U == 0 && M < 8)) {
    constexpr int n = imin(15, qrYoung(U, M, (JT ? 384 : 32 * U) + 2 * F + 7));
    static_assert(n >= 0, "lgkmcnt");
    if constexpr (M == 0 && !(U & 1)) asm volatile("s_waitcnt lgkmcnt(%12)" : Q_PIN4(set, i), X_PIN((U >> 1) & 1) : "i"(n) : "memory");
    else asm volatile("s_waitcnt lgkmcnt(%8)" : Q_PIN4(set, i) : "i"(n) : "memory");
  }
  {
    const u32x4 w = {s.ql[set][i][0], s.ql[set][i][1], s.qh[set][i][0], s.qh[set][i][1]};
    if constexpr (U == 0 && kk == 0) asm volatile("v_mfma_f32_32x32x16_bf16 %0, %1, %2, 0" : "=&v"(s.qa[dt]) : "v"(w), "v"(s.xf[0][0]));
    else asm volatile("v_mfma_f32_32x32x16_bf16 %0, %1, %2, %0" : "+v"(s.qa[dt]) : "v"(w), "v"(s.xf[(U >> 1) & 1][kk]));
  }
  if constexpr (qrX(U, M) >= 0) x_read<16 + (U + 1) / 2, qrX(U, M)>(s);
  q_reads<qrN(U, M)>(s, make_iseq<qrN(U, M + 1) - qrN(U, M)>{});
  if constexpr (U == 13 && M >= 8 && M < 12) { at_kread0<2 * (M - 8)>(kf, kb0); at_kread0<2 * (M - 8) + 1>(kf, kb0); }
  if constexpr (U == 13 && M >= 12) at_kread0<M - 4>(kf, kb0);
  z_dma<Z, M>(s);
  if constexpr (zPk(Z, M) >= 0) s.pv[(zPk(Z, M) >> 2) & 1][zPk(Z, M) & 3] = acc_pk<2 * zPk(Z, M)>();
  if constexpr (M > 0 ? zS(Z, M - 1) >= 0 : U > 0 && zS(Z - 1, 15) >= 0) asm volatile("" :: "v"(s.pl));
  if constexpr (zS(Z, M) >= 0) { z_store<zS(Z, M)>(s, s.pv[zS(Z, M) & 1]); s.pl = s.pv[zS(Z, M) & 1]; }
  if constexpr (JT) q_cvs<J>(s);
  if constexpr (zQs(Z, M) >= 0) q_store<zQs(Z, M)>(s.vP, s.rO, s.sQo);
  if constexpr (zPoll(Z, M)) {
    u32 z = 0u;
    asm volatile("buffer_load_dwordx2 %0, %1, %2, %3 offen sc0 sc1" : "=v"(s.iv) : "v"(z), "s"(s.rO), "s"((u32)WS_IDX) : "memory");
    kv_poll(s.poll, s.vFl, s.rO, s.sFl & ~255u);
  }
  GAP();
}
template <int U, int... Ms> DI void q_sub(PJ& s, u32x4 (&kf)[32], u32 kb0, iseq<Ms...>) { (q_gap<U, Ms>(s, kf, kb0), ...); }
template <int... Us> DI void q_all(PJ& s, u32x4 (&kf)[32], u32 kb0, iseq<Us...>) { (q_sub<Us>(s, kf, kb0, make_iseq<16>{}), ...); }
constexpr int FA = 0;
struct FF {
  float v[32], xs[8], t[3], hq; u32 rd[8]; f32x4 h0, h1;
  u32 vw, vx, vd, vy, lr, lw, lh, lg;
  u32 i0, i1;
  const u16 *x, *wg, *wu, *wd; srd_t rA;
};
template <int L> DI void ff_load(FF& f) {
  if constexpr (L < 8) {
    constexpr int q = L >> 1, mat = L & 1, R = FA + 16 * mat + 4 * q;
    asm volatile("buffer_load_dwordx4 a[%c3:%c4], %0, %1, %2 offen offset:%5" :: "v"(f.vw), "s"(mk_srd(mat ? f.wu : f.wg, M_ * F_ * 2)), "s"((q >> 1) * 7168), "i"(R), "i"(R + 3), "i"((q & 1) * 3584) : "memory", ACL);
  } else if constexpr (L < 10) {
    constexpr int R = FA + 48 + 2 * (L - 8);
    asm volatile("buffer_load_dwordx2 a[%c3:%c4], %0, %1, %2 offen" :: "v"(f.vx), "s"(mk_srd(f.x, S_ * M_ * 2)), "s"((L == 8 ? f.i0 : f.i1) * (M_ * 2)), "i"(R), "i"(R + 1) : "memory", ACL);
  } else {
    constexpr int j = L - 10, R = FA + 32 + 4 * j;
    asm volatile("buffer_load_dwordx4 a[%c2:%c3], %0, %1, 0 offen offset:%4" :: "v"(f.vd), "s"(mk_srd(f.wd, M_ * F_ * 2)), "i"(R), "i"(R + 3), "i"(j * 896) : "memory", ACL);
  }
}
template <int R, bool FIRST, int HALF> DI void ff_unit(FF& f, int b, float a0, float a1) {
  if constexpr (HALF == 0)
    asm volatile("v_accvgpr_read_b32 %1, a[%c2]\n\tv_lshlrev_b32_e32 %0, 16, %1\n\tv_and_b32_e32 %1, 0xffff0000, %1" : "=&v"(f.t[0]), "=&v"(f.t[1]) : "i"(R));
  else if constexpr (FIRST)
    asm volatile("v_mul_f32_e32 %0, %4, %6\n\tv_mul_f32_e32 %1, %4, %7\n\tv_mul_f32_e32 %2, %5, %6\n\tv_mul_f32_e32 %3, %5, %7"
                 : "=&v"(f.v[b]), "=&v"(f.v[b + 1]), "=&v"(f.v[b + 8]), "=&v"(f.v[b + 9]) : "v"(a0), "v"(a1), "v"(f.t[0]), "v"(f.t[1]));
  else
    asm volatile("v_fmac_f32_e32 %0, %4, %6\n\tv_fmac_f32_e32 %1, %4, %7\n\tv_fmac_f32_e32 %2, %5, %6\n\tv_fmac_f32_e32 %3, %5, %7"
                 : "+v"(f.v[b]), "+v"(f.v[b + 1]), "+v"(f.v[b + 8]), "+v"(f.v[b + 9]) : "v"(a0), "v"(a1), "v"(f.t[0]), "v"(f.t[1]));
}
template <int Q> DI void ff_step(FF& f) {
  if constexpr (Q == 56 || Q == 57) {
    constexpr int c = Q - 56;
    const u32 a = __float_as_uint(acc_rd<FA + 48 + 2 * c>()), b = __float_as_uint(acc_rd<FA + 49 + 2 * c>());
    f.xs[4 * c] = BF_LO(a); f.xs[4 * c + 1] = BF_HI(a); f.xs[4 * c + 2] = BF_LO(b); f.xs[4 * c + 3] = BF_HI(b);
    PINF(f.xs[4 * c]); PINF(f.xs[4 * c + 1]); PINF(f.xs[4 * c + 2]); PINF(f.xs[4 * c + 3]);
  }
  if constexpr (Q >= 58 && Q < 122) {
    constexpr int u = (Q - 58) >> 1, q = u >> 3, mat = (u >> 2) & 1, dw = u & 3;
    ff_unit<FA + 16 * mat + 4 * q + dw, q == 0, (Q & 1)>(f, 16 * mat + 2 * dw, f.xs[q], f.xs[4 + q]);
  }
  if constexpr (Q >= 122 && Q < 128) {
#pragma unroll
    for (int i = 3 * (Q - 122); i < 3 * (Q - 122) + 3 && i < 16; ++i) { f.v[i] = sw_add<true>(f.v[i], f.v[16 + i]); PINF(f.v[i]); }
  }
  if constexpr (Q >= 128 && Q < 131) {
#pragma unroll
    for (int i = 3 * (Q - 128); i < 3 * (Q - 128) + 3 && i < 8; ++i) { f.v[i] = sw_add<false>(f.v[i], f.v[8 + i]); PINF(f.v[i]); }
  }
  if constexpr (Q >= 131 && Q < 134) {
#pragma unroll
    for (int i = 0; i < 8; ++i) { f.v[i] = dpp_add<(Q == 131 ? 0x128 : Q == 132 ? 0x124 : 0x122)>(f.v[i]); PINF(f.v[i]); }
  }
  if constexpr (Q == 134) {
    const f32x4 a = {f.v[0], f.v[1], f.v[2], f.v[3]}, b = {f.v[4], f.v[5], f.v[6], f.v[7]};
    asm volatile("ds_write_b128 %0, %1\n\tds_write_b128 %0, %2 offset:16\n\tds_write_b128 %0, %1 offset:8192\n\tds_write_b128 %0, %2 offset:8208\n\t"
                 "ds_write_b128 %0, %1 offset:16384\n\tds_write_b128 %0, %2 offset:16400\n\tds_write_b128 %0, %1 offset:24576\n\tds_write_b128 %0, %2 offset:24592" :: "v"(f.lw), "v"(a), "v"(b) : "memory");
  }
  if constexpr (Q == 137)
    asm volatile("ds_read_b32 %0, %8\n\tds_read_b32 %1, %8 offset:256\n\tds_read_b32 %2, %8 offset:512\n\tds_read_b32 %3, %8 offset:768\n\t"
                 "ds_read_b32 %4, %8 offset:128\n\tds_read_b32 %5, %8 offset:384\n\tds_read_b32 %6, %8 offset:640\n\tds_read_b32 %7, %8 offset:896"
                 : "=&v"(f.rd[0]), "=&v"(f.rd[1]), "=&v"(f.rd[2]), "=&v"(f.rd[3]), "=&v"(f.rd[4]), "=&v"(f.rd[5]), "=&v"(f.rd[6]), "=&v"(f.rd[7]) : "v"(f.lr) : "memory");
  if constexpr (Q == 144) {
    asm volatile("s_waitcnt lgkmcnt(5)" : "+v"(f.rd[0]), "+v"(f.rd[1]), "+v"(f.rd[2]), "+v"(f.rd[3]), "+v"(f.rd[4]), "+v"(f.rd[5]), "+v"(f.rd[6]), "+v"(f.rd[7]));
    f.t[0] = rbf((__uint_as_float(f.rd[0]) + __uint_as_float(f.rd[1])) + (__uint_as_float(f.rd[2]) + __uint_as_float(f.rd[3])));
    f.t[1] = rbf((__uint_as_float(f.rd[4]) + __uint_as_float(f.rd[5])) + (__uint_as_float(f.rd[6]) + __uint_as_float(f.rd[7])));
    PINF(f.t[0]); PINF(f.t[1]);
  }
  if constexpr (Q == 145) { f.t[2] = f.t[0] * __builtin_amdgcn_rcpf(1.f + __expf(-f.t[0])); PINF(f.t[2]); }
  if constexpr (Q == 146) {
    f.hq = rbf(rbf(f.t[2]) * f.t[1]);
    asm volatile("ds_write_b32 %2, %3\n\tds_read_b128 %0, %4\n\tds_read_b128 %1, %4 offset:64" : "=&v"(f.h0), "=&v"(f.h1) : "v"(f.lh), "v"(f.hq), "v"(f.lg) : "memory");
  }
  if constexpr (Q == 156) asm volatile("s_waitcnt lgkmcnt(4)" : "+v"(f.h0), "+v"(f.h1));
  if constexpr (Q >= 156 && Q < 188) {
    constexpr int u = (Q - 156) >> 1, j = u >> 2, dw = u & 3;
    ff_unit<FA + 32 + 4 * j + dw, j == 0, (Q & 1)>(f, 2 * dw, f.h0[j], f.h1[j]);
  }
  if constexpr (Q >= 188 && Q < 191) {
#pragma unroll
    for (int i = 3 * (Q - 188); i < 3 * (Q - 188) + 3 && i < 8; ++i) { f.v[i] = sw_add<true>(f.v[i], f.v[8 + i]); PINF(f.v[i]); }
  }
  if constexpr (Q == 191) {
#pragma unroll
    for (int i = 0; i < 4; ++i) { f.v[i] = sw_add<false>(f.v[i], f.v[4 + i]); PINF(f.v[i]); }
  }
  if constexpr (Q == 192) {
    const f32x4 a = {f.v[0], f.v[1], f.v[2], f.v[3]};
    asm volatile("buffer_store_dwordx4 %0, %1, %2, %3 offen" WT "\n\ts_nop 1" :: "v"(a), "v"(f.vy), "s"(f.rA), "s"((u32)WS_YF) : "memory");
  }
}
template <int Q0, int... Qs> DI void ff_steps(FF& f, iseq<Qs...>) { (ff_step<Q0 + Qs>(f), ...); }

constexpr float AT_THR = 20.f;
constexpr int VS(int sl) { return (sl ^ 1) * 32768; }
DI float max2f(float a, float b) { float r; asm("v_max_f32_e32 %0, %1, %2" : "=v"(r) : "v"(a), "v"(b)); return r; }
DI float fadd_s(float a, float b) { float r; asm("v_add_f32_e32 %0, %1, %2" : "=v"(r) : "v"(a), "v"(b)); return r; }
DI float fsub_s(float a, float b) { float r; asm("v_sub_f32_e32 %0, %1, %2" : "=v"(r) : "v"(a), "v"(b)); return r; }

struct AT {
  f32x16 S[2][2];
  f32x16 negm;
  u32x4 P[4], Pz;
  u32x4 kf[32], vf[32];
  u32 kb_, vb_, kb0;
  u32 vL;
  u32 sW0;
  u32 m0K, m0V, m0X;
  srd_t rA;
  u32 sK, sV;
  u32 itn, v0, vtag;
  u32 sE0;
  int rem, mk;
  float l0; u32 pad0; float l1; u32 pad1; float mhat; u32 pad2; float mx[4], fct;
  u32 st;
  FF f; u32x4 ones; u32 sYf, sDf;
  u32x4 pb, pl; float mo, lo; u32 sM, v8;
  u32 sPB, vl16;
  int dq;
  bool pend; u32 nz;
  u32 tag, vFl, sFl;
};
template <int SL, int F> DI void at_kread(AT& s) {
  asm volatile("ds_read_b128 %0, %1 offset:%2" : "=v"(s.kf[F]) : "v"(SL == 2 ? s.kb0 : s.kb_), "i"((SL & 1) * 32768 + (F >> 1) * 2048 + (F & 1) * 512) : "memory");
}
template <int SL, int M, int F = M> DI void at_vread(AT& s) {
  asm volatile("ds_read_b128 %0, %1 offset:%2" : "=v"(s.vf[M]) : "v"(s.vb_), "i"(VS(SL) + (F >> 3) * 8192 + (F & 7) * 512) : "memory");
}
template <int X, int M, int QB, bool ZC> DI void at_qk(AT& s) {
  constexpr int kb = M & 1, ds = M >> 1, aq = QB + 4 * ds;
  if constexpr (ds == 0 && ZC) asm volatile("v_mfma_f32_32x32x16_bf16 %0, %1, a[%c2:%c3], 0" : "=&v"(s.S[X][kb]) : "v"(s.kf[M]), "i"(aq), "i"(aq + 3) : ACL);
  else if constexpr (ds == 0) asm volatile("v_mfma_f32_32x32x16_bf16 %0, %1, a[%c3:%c4], %2" : "=&v"(s.S[X][kb]) : "v"(s.kf[M]), "v"(s.negm), "i"(aq), "i"(aq + 3) : ACL);
  else                   asm volatile("v_mfma_f32_32x32x16_bf16 %0, %1, a[%c2:%c3], %0" : "+v"(s.S[X][kb]) : "v"(s.kf[M]), "i"(aq), "i"(aq + 3) : ACL);
}
template <int M, int F, bool ZC, bool PZ = false> DI void at_pv(AT& s) {
  constexpr int ks = F >> 3, ao = 16 * (F & 7);
  if constexpr (ZC && ks == 0) asm volatile("v_mfma_f32_32x32x16_bf16 a[%c2:%c3], %0, %1, 0" :: "v"(s.vf[M]), "v"(PZ ? s.Pz : s.P[ks]), "i"(ao), "i"(ao + 15) : ACL);
  else asm volatile("v_mfma_f32_32x32x16_bf16 a[%c2:%c3], %0, %1, a[%c2:%c3]" :: "v"(s.vf[M]), "v"(PZ && ks == 0 ? s.Pz : s.P[ks]), "i"(ao), "i"(ao + 15) : ACL);
}
constexpr int dbF(int M) { return 8 * (M & 3) + (M >> 2); }
template <int X, int KB, int E> DI void at_exp(AT& s) { float e = __builtin_amdgcn_exp2f(s.S[X][KB][E]); asm volatile("" : "+v"(e)); s.S[X][KB][E] = e; }
template <int X, int KB, int E> DI void at_sum(AT& s) {
  if constexpr (E & 1) asm volatile("v_add_f32_e32 %0, %0, %1" : "+v"(s.l1) : "v"(s.S[X][KB][E]));
  else                 asm volatile("v_add_f32_e32 %0, %0, %1" : "+v"(s.l0) : "v"(s.S[X][KB][E]));
}
template <int X, int KB, int C> DI void at_pack(AT& s) {
  u32 w = cvtpk(s.S[X][KB][2 * C], s.S[X][KB][2 * C + 1]); asm volatile("" : "+v"(w));
  s.P[2 * KB + (C >> 2)][C & 3] = w;
}
#define AT_PIN4(A, M) "+v"(s.A[M]), "+v"(s.A[M + 1]), "+v"(s.A[M + 2]), "+v"(s.A[M + 3])
#define AT_PIN8(A, M) AT_PIN4(A, M), AT_PIN4(A, M + 4)

constexpr int expB(int M) {
  if (M < 9 || M % 4 == 2 || (M >= 24 && M < 28)) return -1;
  int n = 0; for (int g = 9; g < M; ++g) if (!(g % 4 == 2 || (g >= 24 && g < 28))) ++n;
  return n;
}
constexpr int expA(int M) {
  if (M >= 24 || M % 4 == 2) return -1;
  int n = 14; for (int g = 0; g < M; ++g) if (g % 4 != 2) ++n;
  return n;
}
static_assert(expB(31) == 13 && expA(0) == 14 && expA(23) == 31, "exp schedule");
template <int X, int E> DI void at_e(AT& s) { if constexpr (E >= 0) at_exp<X, (E >> 4), (E & 15)>(s); }
template <int X, int E> DI void at_a(AT& s) { if constexpr (E >= 0) at_sum<X, (E >> 4), (E & 15)>(s); }
constexpr int dmaB(int M) { return M == 0 ? 0 : M == 6 ? 1 : (M >= 10 && M % 4 == 2) ? (M - 2) / 4 : -1; }

#define AT_NT0 18
constexpr int NT0 = AT_NT0;
template <int R> DI void acc_mov(void) { asm volatile("v_accvgpr_mov_b32 a[%c0], a[%c1]" :: "i"(192 + R), "i"(128 + R) : ACL); }
DI float at_lsum(float l0, float l1) {
  const float ls = fadd_s(l0, l1);
  const auto pr = __builtin_amdgcn_permlane32_swap(__float_as_uint(ls), __float_as_uint(ls), false, false);
  return fadd_s(__uint_as_float(pr[0]), __uint_as_float(pr[1]));
}
DI void at_mask16(f32x16& v, int dq, float ninf) {
  float e[16];
#pragma unroll
  for (int r = 0; r < 16; ++r) e[r] = v[r];
  asm volatile("v_cmpx_gt_i32_e32 vcc, 27, %16\n\tv_mov_b32_e32 %15, %17\n\t" "v_cmpx_gt_i32_e32 vcc, 26, %16\n\tv_mov_b32_e32 %14, %17\n\t"
               "v_cmpx_gt_i32_e32 vcc, 25, %16\n\tv_mov_b32_e32 %13, %17\n\t" "v_cmpx_gt_i32_e32 vcc, 24, %16\n\tv_mov_b32_e32 %12, %17\n\t"
               "v_cmpx_gt_i32_e32 vcc, 19, %16\n\tv_mov_b32_e32 %7, %17\n\t" "v_cmpx_gt_i32_e32 vcc, 18, %16\n\tv_mov_b32_e32 %6, %17\n\t"
               "v_cmpx_gt_i32_e32 vcc, 17, %16\n\tv_mov_b32_e32 %5, %17\n\t" "v_cmpx_gt_i32_e32 vcc, 16, %16\n\tv_mov_b32_e32 %4, %17\n\t"
               "v_cmpx_gt_i32_e32 vcc, 11, %16\n\tv_mov_b32_e32 %11, %17\n\t" "v_cmpx_gt_i32_e32 vcc, 10, %16\n\tv_mov_b32_e32 %10, %17\n\t"
               "v_cmpx_gt_i32_e32 vcc, 9, %16\n\tv_mov_b32_e32 %9, %17\n\t" "v_cmpx_gt_i32_e32 vcc, 8, %16\n\tv_mov_b32_e32 %8, %17\n\t"
               "v_cmpx_gt_i32_e32 vcc, 3, %16\n\tv_mov_b32_e32 %3, %17\n\t" "v_cmpx_gt_i32_e32 vcc, 2, %16\n\tv_mov_b32_e32 %2, %17\n\t"
               "v_cmpx_gt_i32_e32 vcc, 1, %16\n\tv_mov_b32_e32 %1, %17\n\t" "v_cmpx_gt_i32_e32 vcc, 0, %16\n\tv_mov_b32_e32 %0, %17\n\t"
               "s_mov_b64 exec, -1\n\ts_nop 2"
               : "+v"(e[0]), "+v"(e[1]), "+v"(e[2]), "+v"(e[3]), "+v"(e[4]), "+v"(e[5]), "+v"(e[6]), "+v"(e[7]), "+v"(e[8]), "+v"(e[9]), "+v"(e[10]), "+v"(e[11]), "+v"(e[12]), "+v"(e[13]), "+v"(e[14]), "+v"(e[15]) : "v"(dq), "v"(ninf) : "vcc");
#pragma unroll
  for (int r = 0; r < 16; ++r) v[r] = e[r];
}
template <int X, bool NEAR> DI void at_mask(AT& s, int rem) {
  if (__builtin_expect(rem <= s.mk, NEAR)) {
    const int dq = s.dq + 64 * rem - 128;
    at_mask16(s.S[X][1], dq - 32, -__builtin_inff()); at_mask16(s.S[X][0], dq, -__builtin_inff());
  }
}
template <int X, int M> DI void at_max(AT& s) {
#define SV(c, i) s.S[X][(c) >> 1][8 * ((c) & 1) + (i)]
  if constexpr (M == 2)
    asm volatile("v_max3_f32 %0, %4, %5, %6\n\tv_max3_f32 %1, %7, %8, %9\n\tv_max3_f32 %2, %10, %11, %12\n\tv_max3_f32 %3, %13, %14, %15"
                 : "=&v"(s.mx[0]), "=&v"(s.mx[1]), "=&v"(s.mx[2]), "=&v"(s.mx[3])
                 : "v"(SV(0, 0)), "v"(SV(0, 1)), "v"(SV(0, 2)), "v"(SV(1, 0)), "v"(SV(1, 1)), "v"(SV(1, 2)), "v"(SV(2, 0)), "v"(SV(2, 1)), "v"(SV(2, 2)), "v"(SV(3, 0)), "v"(SV(3, 1)), "v"(SV(3, 2)));
  if constexpr (M == 3 || M == 4)
    asm volatile("v_max3_f32 %0, %0, %4, %5\n\tv_max3_f32 %1, %1, %6, %7\n\tv_max3_f32 %2, %2, %8, %9\n\tv_max3_f32 %3, %3, %10, %11"
                 : "+v"(s.mx[0]), "+v"(s.mx[1]), "+v"(s.mx[2]), "+v"(s.mx[3])
                 : "v"(SV(0, 2 * M - 3)), "v"(SV(0, 2 * M - 2)), "v"(SV(1, 2 * M - 3)), "v"(SV(1, 2 * M - 2)), "v"(SV(2, 2 * M - 3)), "v"(SV(2, 2 * M - 2)), "v"(SV(3, 2 * M - 3)), "v"(SV(3, 2 * M - 2)));
  if constexpr (M == 5)
    asm volatile("v_max3_f32 %0, %0, %4, %5\n\tv_max3_f32 %2, %2, %6, %7\n\tv_max_f32_e32 %1, %1, %3"
                 : "+v"(s.mx[0]), "+v"(s.mx[1]), "+v"(s.mx[2]) : "v"(s.mx[3]), "v"(SV(0, 7)), "v"(SV(1, 7)), "v"(SV(2, 7)), "v"(SV(3, 7)));
  if constexpr (M == 6) asm volatile("v_max3_f32 %0, %0, %1, %2\n\tv_mov_b32_e32 %1, %0" : "+v"(s.mx[0]), "+v"(s.mx[1]) : "v"(s.mx[2]));
  if constexpr (M == 7) asm volatile("v_permlane32_swap_b32_e32 %0, %1\n\tv_max_f32_e32 %0, %0, %1" : "+v"(s.mx[0]), "+v"(s.mx[1]));
  if constexpr (M == 8) GAP();
#undef SV
}
DI void at_nextK(AT& s) {
  asm volatile("s_add_u32 %0, %0, 0x8000\n\ts_cmp_eq_u32 %1, 2\n\ts_cselect_b32 %0, %2, %0" : "+s"(s.sK) : "s"(s.rem), "s"(s.sE0) : "scc");
}
template <int X> DI void at_first(AT& s) {
  const float am = __builtin_fabsf(s.mx[0]);
  const bool out = am > AT_THR && am < 3.0e38f;
  if (__builtin_expect(__any(out || s.nz != 0u), 0)) {
    const float m = out ? s.mx[0] : 0.f;
    asm volatile("v_mov_b32_e32 %0, %1" : "+v"(s.mhat) : "v"(m));
#pragma unroll
    for (int kb = 0; kb < 2; ++kb)
#pragma unroll
      for (int r = 0; r < 16; ++r) { float e = s.S[X][kb][r]; asm volatile("v_sub_f32_e32 %0, %0, %1" : "+v"(e) : "v"(m)); s.S[X][kb][r] = e; }
#pragma unroll
    for (int r = 0; r < 16; ++r) { float e = s.negm[r]; asm volatile("v_sub_f32_e32 %0, 0, %1" : "+v"(e) : "v"(m)); s.negm[r] = e; }
    asm volatile("s_mov_b32 %0, 1" : "+s"(s.nz));
  }
}
constexpr int swN(int g) { const int n = g <= 5 ? 0 : ((g - 5) * 8 + 6) / 7; return n > 64 ? 64 : n; }
constexpr int swS(int g) { return g >= 1 && swN(g) / 4 > swN(g - 1) / 4 ? swN(g - 1) / 4 : -1; }
constexpr bool sw_ok() {
  for (int g = 0; g < 64; ++g) if (swN(g + 1) > swN(g) && (g < 4 * ((swN(g + 1) - 1) >> 3) + 5 || swN(g) / 4 != (swN(g + 1) - 1) / 4)) return false;
  return swS(61) == 15;
}
static_assert(sw_ok(), "the first job's O: behind the block's last product; a gap packs dwords of one fragment");
constexpr int swSt(int g0, int g1) { int n = 0; for (int g = g0; g < g1; ++g) n += swS(g) >= 0; return n; }
template <int G> DI void at_sw(AT& s) {
  if constexpr (swS(G) >= 0) {
    constexpr int F = swS(G);
    u32 t;
    asm volatile("s_add_u32 %0, %4, %5\n\tbuffer_store_dwordx4 %1, %2, %3, %0 offen offset:%6" WT
                 : "=&s"(t) : "v"(s.pb), "v"(s.vl16), "s"(s.rA), "s"(s.sPB), "i"((F >> 2) * 4096), "i"((F & 3) * 1024) : "memory", "scc");
    s.pl = s.pb;
  }
  if constexpr (swN(G + 1) > swN(G)) s.pb[swN(G) & 3] = acc_pk<2 * swN(G)>();
  if constexpr (swN(G + 1) > swN(G) + 1) s.pb[(swN(G) + 1) & 3] = acc_pk<2 * swN(G) + 2>();
  if constexpr (swS(G) >= 0) asm volatile("" :: "v"(s.pl));
}
template <int B, int F> DI void pb_ld(u32 vl16, srd_t r, u32 so) {
  u32 t;
  asm volatile("s_add_u32 %0, %3, %4\n\tbuffer_load_dwordx4 a[%c5:%c6], %1, %2, %0 offen offset:%7 sc0 sc1"
               : "=&s"(t) : "v"(vl16), "s"(r), "s"(so), "i"((F >> 2) * 4096), "i"(B + 4 * F), "i"(B + 3 + 4 * F), "i"((F & 3) * 1024) : "memory", "scc", ACL);
}
template <int OFF> DI void at_flag(AT& s, u32 so) { asm volatile("buffer_store_dword %0, %1, %2, %3 offen offset:%4" WT :: "v"(s.vtag), "v"(s.v0), "s"(s.rA), "s"(so), "i"(OFF) : "memory"); }
template <bool OWN, int M> DI void at_gap1(AT& s, PJ& j) {
  if constexpr (M % 4 == 0 && M < 24) asm volatile("s_waitcnt lgkmcnt(8)" : AT_PIN4(kf, M) :: "memory");
  if constexpr (M == 24) {
    asm volatile("s_waitcnt vmcnt(%8) lgkmcnt(0)\n\ts_barrier" : AT_PIN8(kf, 24) : "i"(OWN ? zWait(ZA) : 63) : "memory");
    if constexpr (OWN) z_flag<3>(j);
  }
  at_qk<0, M, 192, true>(s);
  if constexpr (M < 20) at_kread<2, M + 12>(s);
  if constexpr (M >= 24 && M < 28) { at_kread<1, 2 * (M - 24)>(s); at_kread<1, 2 * (M - 24) + 1>(s); }
  if constexpr (M >= 28) at_kread<1, M - 20>(s);
  if constexpr (OWN) {
    z_dma<ZA, M>(j);
    q_cvs<32 + M>(j);
    if constexpr (zQs(ZA, M) >= 0) q_store<zQs(ZA, M)>(s.vl16, s.rA, j.sQo);
  } else {
    if constexpr (M < 8) { ff_step<106 + 2 * M>(s.f); ff_step<107 + 2 * M>(s.f); }
    if constexpr (M >= 8 && M <= 20) ff_step<114 + M>(s.f);
    if constexpr (M == 24) ff_step<137>(s.f);
    if constexpr (M >= 29) ff_step<115 + M>(s.f);
  }
  if constexpr (M >= 28) {
#pragma unroll
    for (int r = 4 * (M - 28); r < 4 * (M - 27); ++r) { float e; asm volatile("v_mov_b32_e32 %0, 0" : "=v"(e)); s.negm[r] = e; }
  }
  if constexpr (M == 27) at_nextK(s);
  if constexpr (M == 29) asm volatile("s_sub_u32 %0, %0, 1" : "+s"(s.rem) :: "scc");
  if constexpr (M == 31) asm volatile("s_add_u32 %0, %0, 0x8000" : "+s"(s.sV) :: "scc");
  GAP();
}
template <bool OWN, int... Ms> DI void at_phase1(AT& s, PJ& j, iseq<Ms...>) { (at_gap1<OWN, Ms>(s, j), ...); }
template <int X, int MODE, int M> DI void at_gapA(AT& s) {
  if constexpr (M == 0) {
    if constexpr (MODE != 2 && MODE < 4) asm volatile("s_waitcnt lgkmcnt(8)\n\ts_nop 0" : AT_PIN4(kf, M), "+v"(s.negm) :: "memory");
    else asm volatile("s_waitcnt lgkmcnt(8)" : AT_PIN4(kf, M) :: "memory");
  }
  if constexpr (M % 4 == 0 && M > 0 && M < 24) asm volatile("s_waitcnt lgkmcnt(8)" : AT_PIN4(kf, M) :: "memory");
  if constexpr (M == 24) {
    constexpr int VM = MODE == 4 ? zWait(ZB) : MODE == 5 ? 63 : MODE == 3 ? 14 + swSt(0, 56) : 14;
    asm volatile("s_waitcnt vmcnt(%8) lgkmcnt(0)\n\ts_barrier" : AT_PIN8(kf, 24) : "i"(VM) : "memory");
  }
  at_qk<X, M, MODE == 2 ? 128 : 192, MODE == 2 || MODE >= 4>(s);
  if constexpr (M < 20) at_kread<X, M + 12>(s);
  if constexpr (MODE >= 4) {
    if constexpr ((M & 1) == 0 && M < 16) dma16q<VS(X), 0, M / 2>(s.m0V, s.sV, s.vL, s.rA, s.st);
    if constexpr ((M & 1) == 0 && M >= 16) dma16q<(X ^ 1) * 32768, 0, M / 2 - 8>(s.m0K, s.sK, s.vL, s.rA, s.st);
    if constexpr (MODE == 4 && zQs(ZB, M) >= 0) q_store<zQs(ZB, M)>(s.vl16, s.rA, s.sPB);
  } else if constexpr (M % 4 == 2) dma16q<VS(X), 0, M / 4>(s.m0V, s.sV, s.vL, s.rA, s.st);
  if constexpr (M == (MODE >= 4 ? 31 : 27)) at_nextK(s);
  if constexpr (M == 31) asm volatile("s_add_u32 %0, %0, 0x8000" : "+s"(s.sV) :: "scc");
  if constexpr (MODE == 3) at_sw<32 + M>(s);
  if constexpr (MODE == 5 && M >= 8 && M < 24) ff_step<148 + M>(s.f);
  if constexpr (MODE == 5 && M >= 24) { ff_step<124 + 2 * M>(s.f); ff_step<125 + 2 * M>(s.f); }
  if constexpr (MODE == 2) { acc_mov<2 * M>(); acc_mov<2 * M + 1>(); }
  constexpr bool DB = MODE == 2;
  if constexpr (M >= 24 && M < 28) { at_vread<X ^ 1, 2 * (M - 24), DB ? dbF(2 * (M - 24)) : 2 * (M - 24)>(s); at_vread<X ^ 1, 2 * (M - 24) + 1, DB ? dbF(2 * (M - 24) + 1) : 2 * (M - 24) + 1>(s); }
  if constexpr (M >= 28) at_vread<X ^ 1, M - 20, DB ? dbF(M - 20) : M - 20>(s);
  if constexpr (MODE >= 4) {
    if constexpr (M == 1 && MODE == 5) at_mask<X ^ 1, true>(s, s.rem + 1);
    at_max<X ^ 1, M>(s);
    if constexpr (M == 8) at_first<X ^ 1>(s);
    if constexpr (M >= 10) at_a<X ^ 1, M - 10>(s);
    if constexpr (M >= 9) at_e<X ^ 1, M - 9>(s);
    if constexpr (M >= 24) at_pack<X ^ 1, 0, M - 24>(s);
    if constexpr (M == 31) asm volatile("" : "+v"(s.P[0]), "+v"(s.P[1]), "+v"(s.l0), "+v"(s.l1));
  } else {
    if constexpr (M == 0) at_a<X ^ 1, 13>(s); else at_a<X ^ 1, expA(M - 1)>(s);
    at_e<X ^ 1, expA(M)>(s);
    if constexpr (M == 2 || M == 6) { at_pack<X ^ 1, 0, 4 + (M - 2) / 2>(s); at_pack<X ^ 1, 0, 5 + (M - 2) / 2>(s); }
    if constexpr (M == 14 || M == 18) { at_pack<X ^ 1, 1, (M - 14) / 2>(s); at_pack<X ^ 1, 1, 1 + (M - 14) / 2>(s); }
    if constexpr (M >= 28) at_pack<X ^ 1, 1, M - 24>(s);
    if constexpr (M == 31) asm volatile("" : "+v"(s.P[1]), "+v"(s.P[2]), "+v"(s.P[3]), "+v"(s.l0), "+v"(s.l1));
  }
  GAP();
}
template <int X, int MODE, int... Ms> DI void at_phaseA(AT& s, iseq<Ms...>) { (at_gapA<X, MODE, Ms>(s), ...); }

#define PPL(i, o) "buffer_load_dwordx4 a[" #i ":" #i "+3], %[vl], %[ra], %0 offen offset:" #o " sc0 sc1\n\t"
#define PPL16(B) "s_mov_b32 %0, " B "\n\t" PPL(128, 0) PPL(132, 1024) PPL(136, 2048) PPL(140, 3072) "s_add_u32 %0, " B ", 4096\n\t" PPL(144, 0) PPL(148, 1024) PPL(152, 2048) PPL(156, 3072) \
                 "s_add_u32 %0, " B ", 8192\n\t" PPL(160, 0) PPL(164, 1024) PPL(168, 2048) PPL(172, 3072) "s_add_u32 %0, " B ", 12288\n\t" PPL(176, 0) PPL(180, 1024) PPL(184, 2048) PPL(188, 3072)
template <int X, int MODE, int M> DI void at_gapB(AT& s) {
  constexpr bool DB = MODE == 2;
  if constexpr (DB && M == 0) s.Pz = s.P[0];
  if constexpr (M % 4 == 0 && M < 24) asm volatile("s_waitcnt lgkmcnt(8)" : AT_PIN4(vf, M) :: "memory");
  if constexpr (M == 24) {
    constexpr int VM = MODE == 4 ? zWait(ZE) : MODE == 5 ? 7 : MODE == 2 ? 14 + swSt(0, 24) : MODE == 3 ? 7 : 14;
    asm volatile("s_waitcnt vmcnt(%8) lgkmcnt(0)\n\ts_barrier" : AT_PIN8(vf, 24) : "i"(VM) : "memory");
    if constexpr (MODE == 3) { const f32x2 ml = {s.mo, s.lo}; asm volatile("buffer_store_dwordx2 %0, %1, %2, %3 offen" WT :: "v"(ml), "v"(s.v8), "s"(s.rA), "s"(s.sM) : "memory"); }
  }
  at_pv<M, DB ? dbF(M) : M, MODE >= 3, DB>(s);
  if constexpr (M < 20) at_vread<X ^ 1, M + 12, DB ? dbF(M + 12) : M + 12>(s);
  if constexpr (MODE >= 4) { if constexpr (M % 4 == 2) dma16q<X * 32768, 0, M / 4>(s.m0K, s.sK, s.vL, s.rA, s.st); }
  else if constexpr (dmaB(M) >= 0) dma16q<X * 32768, 0, dmaB(M)>(s.m0K, s.sK, s.vL, s.rA, s.st);
  if constexpr (MODE == 4 && M == 15) asm volatile("buffer_load_dwordx2 a[190:191], %0, %1, %2 offen sc0 sc1" :: "v"(s.v8), "s"(s.rA), "s"(s.sM) : "memory", ACL);
  if constexpr (MODE == 4 && M == 17) asm volatile("buffer_load_dword a189, %0, %1, %2 offen sc0 sc1" :: "v"(s.vFl), "s"(s.rA), "s"(s.sFl) : "memory", ACL);
  if constexpr (M >= 24 && M < 28) { at_kread<X ^ 1, 2 * (M - 24)>(s); at_kread<X ^ 1, 2 * (M - 24) + 1>(s); }
  if constexpr (M >= 28) at_kread<X ^ 1, M - 20>(s);
  if constexpr (MODE == 2 && M == 0) {
    s.lo = at_lsum(s.l0, s.l1); s.mo = s.mhat; s.l0 = 0.f; s.l1 = 0.f;
    asm volatile("" : "+v"(s.lo), "+v"(s.mo), "+v"(s.l0), "+v"(s.l1));
  }
  if constexpr (MODE == 2) at_sw<M>(s);
  if constexpr (MODE == 5 && M >= 16 && M <= 20) ff_step<172 + M>(s.f);
  if constexpr (MODE >= 4) {
    if constexpr (M == 0) { at_a<X ^ 1, 22>(s); at_e<X ^ 1, 23>(s); at_e<X ^ 1, 24>(s); }
    if constexpr (M == 1 || M == 2) { at_a<X ^ 1, 21 + 2 * M>(s); at_a<X ^ 1, 22 + 2 * M>(s); at_e<X ^ 1, 23 + 2 * M>(s); at_e<X ^ 1, 24 + 2 * M>(s); }
    if constexpr (M == 3) at_a<X ^ 1, 27>(s);
    if constexpr (M >= 3 && M <= 6) at_a<X ^ 1, 25 + M>(s);
    if constexpr (M >= 3 && M <= 5) at_e<X ^ 1, 26 + M>(s);
    if constexpr (M >= 7 && M < 15) at_pack<X ^ 1, 1, M - 7>(s);
    if constexpr (M == 14) asm volatile("" : "+v"(s.P[2]), "+v"(s.P[3]));
    if constexpr (M == 1) {
      if (__builtin_expect(s.nz != 0u, 0)) {
#pragma unroll
        for (int kb = 0; kb < 2; ++kb)
#pragma unroll
          for (int r = 0; r < 16; ++r) { float e = s.S[X][kb][r]; asm volatile("v_sub_f32_e32 %0, %0, %1" : "+v"(e) : "v"(s.mhat)); s.S[X][kb][r] = e; }
      }
    }
  }
  if constexpr (M == 1 && MODE != 4) at_mask<X, MODE != 0>(s, s.rem);
  at_max<X, M>(s);
  if constexpr (M == 8) {
    if constexpr (MODE == 2) at_first<X>(s);
    else if (__builtin_expect(__any(s.mx[0] > AT_THR), 0)) {
      const float dl = max2f(s.mx[0], 0.f);
      s.fct = __builtin_amdgcn_exp2f(-dl);
      asm volatile("v_add_f32_e32 %0, %0, %1" : "+v"(s.mhat) : "v"(dl));
#pragma unroll
      for (int kb = 0; kb < 2; ++kb)
#pragma unroll
        for (int r = 0; r < 16; ++r) { float e = s.S[X][kb][r]; asm volatile("v_sub_f32_e32 %0, %0, %1" : "+v"(e) : "v"(dl)); s.S[X][kb][r] = e; }
#pragma unroll
      for (int r = 0; r < 16; ++r) { float e = s.negm[r]; asm volatile("v_sub_f32_e32 %0, 0, %1" : "+v"(e) : "v"(s.mhat)); s.negm[r] = e; }
      asm volatile("s_nop 0\n\tv_mul_f32_e32 %0, %0, %2\n\tv_mul_f32_e32 %1, %1, %2" : "+v"(s.l0), "+v"(s.l1) : "v"(s.fct));
      s.pend = true; asm volatile("s_mov_b32 %0, 1" : "+s"(s.nz));
    }
  }
  if constexpr (M >= 1) at_a<X, expB(M - 1)>(s);
  at_e<X, expB(M)>(s);
  if constexpr (M == 22 || M == 26) { at_pack<X, 0, (M - 22) / 2>(s); at_pack<X, 0, 1 + (M - 22) / 2>(s); }
  if constexpr (M == 25) asm volatile("s_sub_u32 %0, %0, 1" : "+s"(s.rem) :: "scc");
  if constexpr (M == 31) asm volatile("" : "+v"(s.P[0]), "+v"(s.S[X][0]), "+v"(s.l0), "+v"(s.l1), "+v"(s.negm));
  GAP();
}
template <int X, int MODE, int... Ms> DI void at_phaseB(AT& s, iseq<Ms...>) {
  (at_gapB<X, MODE, Ms>(s), ...);
  if constexpr (MODE != 2) {
    if (__builtin_expect(s.pend, 0)) {
      asm volatile("s_nop 7\n\ts_nop 7" ::: "memory");
      acc_mul_all(s.fct, make_iseq<128>{});
      asm volatile("s_nop 1" ::: "memory");
      s.pend = false;
    }
    GAP();
  }
}
template <int X, int MODE> DI void at_period(AT& s) { at_phaseA<X, MODE>(s, make_iseq<32>{}); at_phaseB<X, MODE>(s, make_iseq<32>{}); }
template <bool BLOCK> DI void at_check(AT& s, u32& want) {
  u32 st, t, n = SPIN_MAX;
  asm volatile("" : "=v"(t));
  asm volatile("s_cmp_eq_u32 %1, 0\n\ts_cbranch_scc1 .Lce%=\n\ts_bitcmp0_b32 %1, 1\n\ts_cbranch_scc1 .Lcl%=\n"
               ".Lca%=:\n\tv_accvgpr_read_b32 %3, a189\n\t"
               "v_cmp_eq_u32_e32 vcc, %9, %3\n\ts_nop 3\n\ts_cmp_eq_u64 vcc, -1\n\ts_cbranch_scc1 .Lcb%=\n\ts_sub_u32 %2, %2, 1\n\ts_cbranch_scc1 .Lcb%=\n\t"
               "buffer_load_dword a189, %4, %6, %11 offen sc0 sc1\n\ts_waitcnt vmcnt(0)\n\ts_branch .Lca%=\n"
               ".Lcb%=:\n\tv_accvgpr_write_b32 a189, 0\n\ts_mov_b32 %2, %12\n\ts_and_b32 %1, %1, 1\n\ts_cbranch_scc0 .Lce%=\n"
               ".Lcl%=:\n\tv_accvgpr_read_b32 %3, a191\n\t"
               "v_cmp_ne_u32_e32 vcc, 0xbf800000, %3\n\ts_nop 3\n\ts_cmp_eq_u64 vcc, -1\n\ts_cbranch_scc1 .Lcg%=\n\t"
               ".if %10\n\ts_sub_u32 %2, %2, 1\n\ts_cbranch_scc1 .Lcg%=\n\t.endif\n\t"
               "buffer_load_dwordx2 a[190:191], %13, %6, %8 offen sc0 sc1\n\t"
               ".if %10\n\ts_waitcnt vmcnt(0)\n\ts_branch .Lcl%=\n\t.else\n\ts_branch .Lce%=\n\t.endif\n"
               ".Lcg%=:\n\ts_mov_b32 %1, 0\n\t" PPL16("%7")
               ".Lce%=:"
               : "=&s"(st), "+s"(want), "+s"(n) : "v"(t), "v"(s.vFl), [vl] "v"(s.vl16), [ra] "s"(s.rA), "s"(s.sPB), "s"(s.sM), "s"(s.tag), "i"(BLOCK ? 1 : 0), "s"(s.sFl), "i"(SPIN_MAX), "v"(s.v8)
               : "memory", "scc", "vcc", ACL);
}
#define QPL(i, o) "buffer_load_dwordx4 a[" #i ":" #i "+3], %5, %6, %1 offen offset:" #o " sc0 sc1\n\t"
#define QPM(i, k) "v_accvgpr_read_b32 %2, a[" #i "]\n\tv_accvgpr_read_b32 %3, a[" #k "]\n\tv_max3_u32 %4, %4, %2, %3\n\t"
DI void at_getq(AT& s) {
  u32 st, n = SPIN_MAX, t, u, m;
  asm volatile(".Lqg%=:\n\tv_mov_b32 %4, 0\n\t"
               QPM(195, 199) QPM(203, 207) QPM(211, 215) QPM(219, 223) QPM(227, 231) QPM(235, 239) QPM(243, 247) QPM(251, 255)
               "v_cmp_eq_u32_e32 vcc, -1, %4\n\ts_nop 3\n\ts_cmp_eq_u64 vcc, 0\n\ts_cbranch_scc1 .Lqe%=\n\t"
               "s_sub_u32 %0, %0, 1\n\ts_cbranch_scc1 .Lqe%=\n\ts_mov_b32 %1, %7\n\t"
               QPL(192, 0) QPL(196, 1024) QPL(200, 2048) QPL(204, 3072) "s_add_u32 %1, %7, 4096\n\t"
               QPL(208, 0) QPL(212, 1024) QPL(216, 2048) QPL(220, 3072) "s_add_u32 %1, %7, 8192\n\t"
               QPL(224, 0) QPL(228, 1024) QPL(232, 2048) QPL(236, 3072) "s_add_u32 %1, %7, 12288\n\t"
               QPL(240, 0) QPL(244, 1024) QPL(248, 2048) QPL(252, 3072)
               "s_waitcnt vmcnt(0)\n\ts_branch .Lqg%=\n"
               ".Lqe%=:"
               : "+s"(n), "=&s"(st), "=&v"(t), "=&v"(u), "=&v"(m) : "v"(s.vl16), "s"(s.rA), "s"(s.sPB) : "memory", "scc", "vcc", ACL);
}

struct RD { u32x2 xv[4], bv[4], fl; float gt[2]; u32 row, vx, vc, vf; srd_t rX; };
DI void rd_look(RD& r, srd_t rA) {
  asm volatile("buffer_load_dwordx2 %0, %1, %2, %3 offen sc0 sc1" : "=v"(r.fl) : "v"(r.vf), "s"(rA), "s"((u32)WS_IDX) : "memory");
}
DI void rd_check(RD& r, u32 tag, srd_t rA) {
  u32 n = SPIN_MAX;
  while (!__all(r.fl[1] == tag) && --n)
    asm volatile("s_sleep 1\n\tbuffer_load_dwordx2 %0, %1, %2, %3 offen sc0 sc1\n\ts_waitcnt vmcnt(0)" : "+v"(r.fl) : "v"(r.vf), "s"(rA), "s"((u32)WS_IDX) : "memory");
}
template <int U> DI void rd_x(RD& r) {
  asm volatile("buffer_load_dwordx2 %0, %2, %3, %4 offen\n\tbuffer_load_dwordx2 %1, %2, %3, %4 offen offset:32" : "=&v"(r.xv[2 * U]), "=&v"(r.xv[2 * U + 1]) : "v"(r.vx), "s"(r.rX), "s"(U * 16 * M_ * 2) : "memory");
}
template <int I> DI void rd_g(RD& r, srd_t rA) {
  if constexpr (I == 0)
    asm volatile("buffer_load_dword %0, %2, %3, %4 offen sc0 sc1\n\tbuffer_load_dword %1, %2, %3, %4 offen offset:64 sc0 sc1" : "=&v"(r.gt[0]), "=&v"(r.gt[1]) : "v"(r.row * 4), "s"(rA), "s"((u32)WS_GATE) : "memory");
  else {
    const u32 idx = __builtin_amdgcn_readlane(r.fl[0], 63), rw = r.row + 16 * (I - 1), c = r.vc;
    r.vf = rw == (idx & 0xffffu) ? c : rw == idx >> 16 ? c + M_ * 2 : OOB;
    asm volatile("buffer_load_dwordx2 %0, %2, %3, %4 offen sc0 sc1\n\tbuffer_load_dwordx2 %1, %2, %3, %4 offen offset:32 sc0 sc1" : "=&v"(r.bv[2 * I - 2]), "=&v"(r.bv[2 * I - 1]) : "v"(r.vf), "s"(rA), "s"((u32)WS_BB) : "memory");
  }
}

struct OP {
  u32x4 cf[16];
  u32x4 wf[28], st, id[2];
  f32x16 y[3];
  float inv;
  u32 sb[5];
  u32 vL, vl16, wofs, sW1, sq, sY, sX, sXp, tag, xp, sWf, sWfp;
  srd_t rA;
  RD r;
};
template <int W, int F> DI void op_read(OP& o, u32 rb) {
  asm volatile("ds_read_b128 %0, %1 offset:%2" : "=v"(o.wf[W]) : "v"(rb), "i"((F / 7) * 8192 + (F % 7) * 1024) : "memory");
}
template <int F> DI void op_xst(OP& o) {
  u32 t;
  asm volatile("s_add_u32 %0, %4, %5\n\tbuffer_store_dwordx4 %1, %2, %3, %0 offen offset:%6" WT
               : "=&s"(t) : "v"(o.cf[F]), "v"(o.vl16), "s"(o.rA), "s"(o.sX), "i"((F >> 2) * 4096), "i"((F & 3) * 1024) : "memory", "scc");
}
template <int D> DI void op_cv(OP& o) {
  u32 w; float t0;
  asm volatile("v_accvgpr_read_b32 %0, a[%c3]\n\tv_accvgpr_read_b32 %1, a[%c4]\n\tv_mul_f32_e32 %0, %0, %2\n\tv_mul_f32_e32 %1, %1, %2\n\tv_cvt_pk_bf16_f32 %0, %0, %1"
               : "=&v"(w), "=&v"(t0) : "v"(o.inv), "i"(2 * D), "i"(2 * D + 1));
  o.cf[D >> 2][D & 3] = w;
}
template <int D> DI void op_st(OP& o) {
  if constexpr (D >= 0 && D < 56) {
    constexpr int I = D >> 2, W = D & 3, nt = I >> 1, e = 8 * (I & 1) + 2 * W;
    u32 w;
    if constexpr (nt < 4) w = acc_pk<192 + 16 * nt + e>();
    else asm volatile("v_cvt_pk_bf16_f32 %0, %1, %2" : "=v"(w) : "v"(o.y[nt < 4 ? 0 : nt - 4][e]), "v"(o.y[nt < 4 ? 0 : nt - 4][e + 1]));
    o.st[W] = w;
    if constexpr (W == 3) asm volatile("buffer_store_dwordx4 %0, %1, %2, %3 offen offset:%4" WT "\n\ts_nop 1" :: "v"(o.st), "v"(o.vl16), "s"(o.rA), "s"(o.sY + (I >> 2) * 4096), "i"((I & 3) * 1024) : "memory");
  }
}
template <int PASS, int NT, int KS, int W> DI void op_mfma(OP& o) {
  constexpr int ay = 192 + 16 * NT;
  if constexpr (PASS == 0) {
    if constexpr (NT < 4) {
      if constexpr (KS == 0) asm volatile("v_mfma_f32_32x32x16_bf16 a[%c2:%c3], %0, %1, 0" :: "v"(o.wf[W]), "v"(o.cf[KS]), "i"(ay), "i"(ay + 15) : ACL);
      else                   asm volatile("v_mfma_f32_32x32x16_bf16 a[%c2:%c3], %0, %1, a[%c2:%c3]" :: "v"(o.wf[W]), "v"(o.cf[KS]), "i"(ay), "i"(ay + 15) : ACL);
    } else {
      if constexpr (KS == 0) asm volatile("v_mfma_f32_32x32x16_bf16 %0, %1, %2, 0" : "=&v"(o.y[NT - 4]) : "v"(o.wf[W]), "v"(o.cf[KS]));
      else                   asm volatile("v_mfma_f32_32x32x16_bf16 %0, %1, %2, %0" : "+v"(o.y[NT - 4]) : "v"(o.wf[W]), "v"(o.cf[KS]));
    }
  } else {
    if constexpr (NT < 4) asm volatile("v_mfma_f32_32x32x16_bf16 a[%c1:%c2], %0, a[%c3:%c4], a[%c1:%c2]" :: "v"(o.wf[W]), "i"(ay), "i"(ay + 15), "i"(4 * KS), "i"(4 * KS + 3) : ACL);
    else                  asm volatile("v_mfma_f32_32x32x16_bf16 %0, %1, a[%c2:%c3], %0" : "+v"(o.y[NT - 4]) : "v"(o.wf[W]), "i"(4 * KS), "i"(4 * KS + 3));
  }
}
constexpr int DR(bool mrg) { return mrg ? 48 : 32; }
constexpr int cvD(bool mrg, int q) { const int n = q + (mrg ? 27 : 13); return n < 0 ? 0 : n > 64 ? 64 : n; }
constexpr int stepI(int q, int q0, int d, int n) { return q >= q0 && (q - q0) % d == 0 && (q - q0) / d < n ? (q - q0) / d : -1; }
#define XS0 -9
#define XSD 4
#define XFA 40
#define XFB 72
#define XPA 60
#define XCA 76
#define XPB 92
constexpr int opXs(int q) { return stepI(q, XS0, XSD, 16); }
constexpr int opX(int q) { return stepI(q, XCA + 1, 4, 8) >= 0 ? stepI(q, XCA + 1, 4, 8) : stepI(q, 109, 4, 8) >= 0 ? 8 + stepI(q, 109, 4, 8) : -1; }
constexpr int opD(int q) { const int i = stepI(q, 1, 4, 14) >= 0 ? stepI(q, 1, 4, 14) : stepI(q, 55, 4, 14) >= 0 ? 14 + stepI(q, 55, 4, 14) : -1; return i >= 0 ? 32 + 8 * (i / 7) + i % 7 : -1; }
constexpr int opDq(int piece) { for (int q = 0; q < 168; ++q) if (opD(q) == piece) return q; return -1; }
constexpr int opN(bool mrg, int q) {
  return (q < 0 && (q + DR(mrg)) % 4 == 2 && q + DR(mrg) < 28) + (q == XFA) + (q == XFB) + (q == XPA) + (q == XPB) + (q == 141) + 2 * (q == 143 || q == 147) + (opD(q) >= 0) + (opX(q) >= 0) + (opXs(q) >= 0);
}
constexpr int opYoung(bool mrg, int q0, int q1) { int n = 0; for (int q = q0 + 1; q < q1; ++q) n += opN(mrg, q); return n > 63 ? 63 : n; }
constexpr bool op_ok() {
  for (int f = 0; f < 16; ++f) if (cvD(false, 7 * f - 1) < 4 * f + 4 || cvD(false, XS0 + XSD * f) < 4 * f + 4) return false;
  return opDq(40) > 24 && opDq(48) > 52 && opDq(56) > 80 && XS0 + 7 * XSD < XFA && XS0 + 15 * XSD < XFB && XPA < XCA && XCA + 13 < 108 && XPB < 108 && XPB > XCA && cvD(false, XCA) >= 32;
}
static_assert(op_ok(), "schedule of the out projection");
#define OP_PIN4(W) "+v"(o.wf[(W) % 28]), "+v"(o.wf[((W) + 1) % 28]), "+v"(o.wf[((W) + 2) % 28]), "+v"(o.wf[((W) + 3) % 28])
template <bool MRG, int Q> DI void op_ctx(OP& o) {
  if constexpr (opXs(Q) >= 0) op_xst<opXs(Q)>(o);
  if constexpr (cvD(MRG, Q + 1) > cvD(MRG, Q)) op_cv<cvD(MRG, Q)>(o);
  if constexpr (opXs(Q - 1) >= 0) asm volatile("" :: "v"(o.cf[opXs(Q - 1)]));
}
template <bool MRG, int C, int M> DI void op_gap(AT& s, OP& o, u32 rb, u32 rbn, u32 rbn2) {
  constexpr int ks = M / 7, nt = M % 7, q = 28 * C + M;
  if constexpr (M % 4 == 0 && M < 24) asm volatile("s_waitcnt lgkmcnt(4)" : OP_PIN4(M) :: "memory");
  if constexpr (q == XFA || q == XFB) { asm volatile("s_waitcnt vmcnt(%0)" :: "i"(opYoung(MRG, XS0 + XSD * (q == XFA ? 7 : 15), q)) : "memory"); at_flag<(q == XFA ? 8 : 12)>(s, o.sWf); }
  if constexpr (q == XCA) {
    asm volatile("s_waitcnt vmcnt(%1)" : "+v"(o.xp) : "i"(opYoung(MRG, XPA, q)) : "memory");
    u32 z = 0u; kv_check_all(o.xp, o.tag, z, o.rA, o.sWfp + 8u);
  }
  if constexpr (M == 24) {
    constexpr int q1 = C < 2 ? -99 : C == 2 ? 26 - DR(MRG) : C == 3 ? imax(imax(XCA + 13, opDq(38)), XPB) : C == 4 ? imax(XCA + 29, opDq(46)) : imax(141, opDq(62));
    constexpr int VM = q1 == -99 ? 63 : opYoung(MRG, q1, q);
    if constexpr (C == 3) {
      asm volatile("s_waitcnt vmcnt(%5) lgkmcnt(0)\n\ts_barrier" : OP_PIN4(24), "+v"(o.xp) : "i"(VM) : "memory");
      u32 z = 0u; kv_check_all(o.xp, o.tag, z, o.rA, o.sWfp + 12u);
    } else if constexpr (C == 5) {
      asm volatile("s_waitcnt vmcnt(%5) lgkmcnt(0)\n\ts_barrier" : OP_PIN4(24), "+v"(o.r.fl) : "i"(VM) : "memory");
      rd_check(o.r, o.tag, o.rA);
    } else asm volatile("s_waitcnt vmcnt(%4) lgkmcnt(0)\n\ts_barrier" : OP_PIN4(24) : "i"(VM) : "memory");
  }
  op_mfma<C / 4, nt, 4 * (C % 4) + ks, M>(o);
  if constexpr (M < 20) op_read<M + 8, M + 8>(o, rb);
  if constexpr (M >= 24) {
    constexpr int i = 2 * (M - 24);
    if constexpr (C < 5) { op_read<i, i>(o, rbn); op_read<i + 1, i + 1>(o, rbn); }
    else { op_read<i, 7 * (i & 3)>(o, i < 4 ? rbn : rbn2); op_read<i + 1, 7 * ((i + 1) & 3)>(o, i < 4 ? rbn : rbn2); }
  }
  if constexpr (opD(q) >= 0) dma16q<0, (opD(q) / 8 - 4) * 32768, opD(q) % 8>(o.sb[(opD(q) / 8) % 5] + o.wofs, o.sW1, o.vL, o.rA, o.sq);
  if constexpr (opX(q) >= 0) pb_ld<0, opX(q)>(o.vl16, o.rA, o.sXp);
  if constexpr (q == XPA || q == XPB) { u32 z = 0u; kv_poll(o.xp, z, o.rA, o.sWfp + (q == XPA ? 8u : 12u)); }
  if constexpr (q == 141) rd_look(o.r, o.rA);
  if constexpr (q >= 143 && q <= 147 && q % 4 == 3) rd_x<(q - 143) / 4>(o.r);
  if constexpr (q >= 165) rd_g<q - 165>(o.r, o.rA);
  op_ctx<MRG, q>(o);
  GAP();
}
template <bool MRG, int C, int... Ms> DI void op_chunk(AT& s, OP& o, iseq<Ms...>) {
  const u32 rb = o.vl16 + o.sb[C % 5], rbn = o.vl16 + o.sb[(C + 1) % 5], rbn2 = o.vl16 + o.sb[(C + 2) % 5];
  (op_gap<MRG, C, Ms>(s, o, rb, rbn, rbn2), ...);
}
template <int I> DI void op_gapL(OP& o, u32 rb6, u32 rb7) {
  constexpr int nt = I / 8, ks = I % 8, J = I + 8;
  if constexpr (I % 4 == 0) asm volatile("s_waitcnt lgkmcnt(%4)" : OP_PIN4(I) : "i"(I < 52 ? 4 : 0) : "memory");
  op_mfma<1, nt, 8 + ks, I % 28>(o);
  if constexpr (J < 56) op_read<J % 28, 7 * (J & 3) + J / 8>(o, (J & 4) ? rb7 : rb6);
  op_st<I - 10>(o);
  GAP();
}
template <int... Is> DI void op_last(OP& o, iseq<Is...>) { const u32 rb6 = o.vl16 + o.sb[1], rb7 = o.vl16 + o.sb[2]; (op_gapL<Is>(o, rb6, rb7), ...); }
template <int... Ds> DI void op_tail(OP& o, iseq<Ds...>) { (op_st<46 + Ds>(o), ...); }

DI u32 bf_scale(u32 w, float f) { return cvtpk(BF_LO(w) * f, BF_HI(w) * f); }
template <int... Rs> DI void pb_scale(float f, iseq<Rs...>) { (acc_wr<128 + Rs>(bf_scale(__float_as_uint(acc_rd<128 + Rs>()), f)), ...); }
constexpr int XL = 1;
constexpr int drF(int i) { return i < 16 ? i : 8 * (2 + (i & 1)) + ((i - 16) >> 1); }
constexpr int drI(bool mrg, int M) { return M < 16 || !mrg ? M : (M & 3) < 2 ? 16 + ((M - 16) >> 2) * 2 + (M & 1) : -1; }
template <bool MRG, int M> DI void at_gapD(AT& s, OP& o, u32 rb0) {
  constexpr int i = drI(MRG, M), q = M - DR(MRG), MB = DR(MRG) - 8;
  if constexpr (M % 4 == 0 && M < (MRG ? 16 : 24)) asm volatile("s_waitcnt lgkmcnt(8)" : AT_PIN4(vf, M) :: "memory");
  if constexpr (MRG && M % 4 == 0 && M >= 16 && M < MB) asm volatile("s_waitcnt lgkmcnt(%2)" : "+v"(s.vf[i]), "+v"(s.vf[i + 1]) : "i"(imin(i + 11, 31) - i - 1) : "memory");
  if constexpr (M == 16) {
    if constexpr (MRG) asm volatile("s_waitcnt vmcnt(4)" : "+v"(s.mo), "+v"(s.lo) :: "memory"); else asm volatile("s_waitcnt vmcnt(4)" ::: "memory");
  }
  if constexpr (M == MB) {
    if constexpr (MRG) asm volatile("s_waitcnt lgkmcnt(0)\n\ts_barrier" : AT_PIN4(vf, 28) :: "memory"); else asm volatile("s_waitcnt lgkmcnt(0)\n\ts_barrier" : AT_PIN8(vf, 24) :: "memory");
  }
  if constexpr (i >= 0) at_pv<i, drF(i), false>(s);
  else {
    constexpr int db = (M - 16) >> 2, F = 2 * db + (M & 1);
    asm volatile("v_mfma_f32_32x32x16_bf16 a[%c1:%c2], %0, a[%c3:%c4], a[%c1:%c2]" :: "v"(o.id[M & 1]), "i"(16 * db), "i"(16 * db + 15), "i"(128 + 4 * F), "i"(131 + 4 * F) : ACL);
  }
  if constexpr (i >= 0 && i < 20) at_vread<XL, i + 12, drF(i + 12)>(s);
  if constexpr (M % 4 == 2 && M < 28) dma16q<VS(XL ^ 1), 3 * 32768, M / 4>(s.m0V, s.sW0, s.vL, s.rA, s.st);
  if constexpr (M >= MB && M < MB + 4) { op_read<2 * (M - MB), 2 * (M - MB)>(o, rb0); op_read<2 * (M - MB) + 1, 2 * (M - MB) + 1>(o, rb0); }
  if constexpr (M <= 14) at_a<XL, 17 + M>(s);
  if constexpr (M <= 13) at_e<XL, 18 + M>(s);
  if constexpr (M >= 6 && M <= 9) at_pack<XL, 1, M - 6>(s);
  if constexpr (M == 8 || M == 10 || M == 12 || M == 14) at_pack<XL, 1, 4 + (M - 8) / 2>(s);
  if constexpr (M == 15) { s.l0 = at_lsum(s.l0, s.l1); asm volatile("" : "+v"(s.l0), "+v"(s.P[2]), "+v"(s.P[3])); }
  if constexpr (MRG && M == 17) {
    if (__builtin_expect(__any(s.lo > 0.f && s.mo != s.mhat), 0)) {
      const float r = max2f(s.lo > 0.f ? s.mo : s.mhat, s.mhat), f0 = __builtin_amdgcn_exp2f(s.mhat - r), f1 = __builtin_amdgcn_exp2f(s.lo > 0.f ? s.mo - r : 0.f);
      asm volatile("s_nop 7\n\ts_nop 7" ::: "memory");
      acc_mul_all(f0, make_iseq<128>{});
      pb_scale(f1, make_iseq<64>{});
      asm volatile("v_mul_f32_e32 %0, %0, %2\n\tv_mul_f32_e32 %1, %1, %3" : "+v"(s.l0), "+v"(s.lo) : "v"(f0), "v"(f1));
#pragma unroll
      for (int c = 0; c < 8; ++c) { u32 w = s.P[2 + (c >> 2)][c & 3]; const u32 v = bf_scale(w, f0); asm volatile("v_mov_b32_e32 %0, %1" : "+v"(w) : "v"(v)); s.P[2 + (c >> 2)][c & 3] = w; }
      asm volatile("s_nop 1" ::: "memory");
    }
  }
  if constexpr (MRG && M == 18) { s.l0 = fadd_s(s.l0, s.lo); asm volatile("" : "+v"(s.l0)); }
  if constexpr (M == (MRG ? 19 : 17)) { o.inv = __builtin_amdgcn_rcpf(s.l0); asm volatile("" : "+v"(o.inv)); }
  op_ctx<MRG, q>(o);
  GAP();
}
template <bool MRG, int... Ms> DI void at_drain(AT& s, OP& o, u32 rb0, iseq<Ms...>) { (at_gapD<MRG, Ms>(s, o, rb0), ...); }
template <int... Fs> DI void at_vread_first(AT& s, iseq<Fs...>) { (at_vread<XL, Fs>(s), ...); }
template <bool MRG> DI void at_finish(AT& s, OP& o, u32& want) {
  asm volatile("s_waitcnt vmcnt(8) lgkmcnt(0)\n\ts_barrier" ::: "memory");
  GAP();
  at_vread_first(s, make_iseq<12>{});
  if constexpr (MRG) {
    at_check<true>(s, want);
    asm volatile("buffer_load_dword %0, %2, %3, %4 offen sc0 sc1\n\tbuffer_load_dword %1, %2, %3, %4 offen offset:4 sc0 sc1" : "=&v"(s.mo), "=&v"(s.lo) : "v"(s.v8), "s"(s.rA), "s"(s.sM) : "memory");
  }
  at_a<XL, 13>(s); at_e<XL, 14>(s); at_e<XL, 15>(s); at_e<XL, 16>(s); at_e<XL, 17>(s); at_a<XL, 14>(s); at_a<XL, 15>(s); at_a<XL, 16>(s);
  at_pack<XL, 0, 4>(s); at_pack<XL, 0, 5>(s); at_pack<XL, 0, 6>(s); at_pack<XL, 0, 7>(s);
  asm volatile("" : "+v"(s.P[1]));
  GAP();
  PSTAMP(4);
  at_drain<MRG>(s, o, o.vl16 + o.sb[0], make_iseq<DR(MRG)>{});
  PSTAMP(5);
  op_chunk<MRG, 0>(s, o, make_iseq<28>{});
  PSTAMP(22);
  op_chunk<MRG, 1>(s, o, make_iseq<28>{});
  PSTAMP(6);
  op_chunk<MRG, 2>(s, o, make_iseq<28>{});
  PSTAMP(23);
  op_chunk<MRG, 3>(s, o, make_iseq<28>{});
  PSTAMP(7);
  op_chunk<MRG, 4>(s, o, make_iseq<28>{});
  PSTAMP(27);
  op_chunk<MRG, 5>(s, o, make_iseq<28>{});
  PSTAMP(8);
  op_last(o, make_iseq<56>{});
  PSTAMP(9);
  asm volatile("s_nop 7\n\ts_nop 3" ::: "memory"); GAP();
  op_tail(o, make_iseq<10>{});
  PSTAMP(10);
}

#define IDS(bid, lane, wid) \
  const int xcd = bid & 7, j = bid >> 3, h = 2 * xcd + (j & 1); \
  const int p = (j >> 1) & 7, role = (j >> 4) ^ 1;                  \
  const bool two = role && p < 7;                                   \
  const int qbH = 15 - p, qbO = role ? p : qbH;                     \
  const u32 lb = lds_addr(smem); \
  const u32 sM = (u32)WS_ML + (u32)((h * 8 + p) * 128 + wid * 32) * 8;     \
  const u32 sKb = (u32)WS_K + h * S_ * 512, sVb = (u32)WS_V + h * S_ * 512
#define SWF(rr, hh) ((u32)WS_WF + (((((rr) * 16 + (hh)) * 8 + p) * 4 + wid) << 4))
#define SPL(hh) ((u32)(((role * 16 + (hh)) * 8 + p) * 4 + wid) << 14)
DI int kv_tile(int rb, int l) { const int i = 3 * rb + l - 1; return l == 0 ? (rb < 5 ? rb : rb + 13) : i < 13 ? 5 + i : 8 + i; }
static_assert(WS_KVF % 512 == 0, "alignment of the words");
template <bool VT> DI void phase1(PJ& s, char* smem, const u16* x, const u16* ckv, const u16* wq, const u16* wkv, char* ws, const u16* gate_w, const u16* w_r, const u16* wo, int wid) {
  const int bid = blockIdx.x, lane = threadIdx.x & 63;
  IDS(bid, lane, wid);
  const srd_t rO = mk_srd(ws, (u32)WS_END);
  const int wm = wid >> 1, wn = wid & 1, fr = lane & 15, g = lane >> 4;
  constexpr int which = VT;
  const int rb = j >> 2;
  asm volatile("s_load_dword %0, %1, %2" : "=s"(s.tag) : "s"(ws), "s"((u32)WS_EP) : "memory");
  s.wid = wid;
  s.rA = mk_srd(ckv, S_ * DC * 2); s.rW = mk_srd(wkv, DC * HD * 2);
  s.rX = mk_srd(x, S_ * M_ * 2); s.rWq = mk_srd(wq, M_ * HD * 2); s.rO = rO;
  s.sRow = bid * 8 + wid * 2;
  {
    const u32 vo = lane < 56 ? (u32)lane * 16 : OOB;
    asm volatile("buffer_load_dwordx4 %0, %4, %5, %6 offen\n\tbuffer_load_dwordx4 %1, %4, %5, %6 offen offset:896\n\t"
                 "buffer_load_dwordx4 %2, %4, %7, 0 offen\n\tbuffer_load_dwordx4 %3, %4, %8, 0 offen"
                 : "=&v"(s.px[0]), "=&v"(s.px[1]), "=&v"(s.px[2]), "=&v"(s.px[3])
                 : "v"(vo), "s"(s.rX), "s"(rfl(s.sRow * 896)), "s"(mk_srd(gate_w, M_ * 2)), "s"(mk_srd(w_r, M_ * 2)) : "memory");
  }
  s.vP = lane * 16; s.m0P = lb + AT_X + wid * 1024;
  CODE_WARM(k_main, .Lk_code_end, s.m0P, (j * 4 + wid) * 1024, s.vP);
#pragma unroll
  for (int q = 0; q < 4; ++q) {
    const u32 c = ((lane & 7) ^ (((q & 1) << 2) | (lane >> 4))) << 4;
    s.vAkv[q] = (lane >> 3) * 2048 + c + q * 15360;
  }
#pragma unroll
  for (int v = 0; v < 4; ++v) { const u32 f = ((v & 1) << 1) | (lane >> 5) | ((wid & 1) << 2); s.vW[v] = (lane >> 5) * 8192 + (((lane & 31) ^ (f << 1)) << 4) + v * 15360; }
  s.m0A = lb + wid * 8192; s.m0W = lb + PJ_W0 + wid * 4096;
  s.sAkv = kv_tile(rb, wid) * 131072; s.sWkv = 8 * wid * 8192 + h * 512;
  pj_dmaW_all<0, 0>(s, s.sWkv, make_iseq<4>{});
  pj_dmaA_all<0>(s, s.sAkv, make_iseq<8>{});
  pj_dmaW_all<0, 1>(s, s.sWkv, make_iseq<4>{});
  pj_dmaA_all<1>(s, s.sAkv + 128, make_iseq<8>{});
  pj_dmaW_all<1, 0>(s, s.sWkv + 524288, make_iseq<4>{});
  GAP();
  s.pwo = wo + (size_t)(h * 256 + (8 * which + rb) * 16) * M_;
  s.sM = rfl(role ? OOB : sM);
  s.sPw = ((((j >> 2) * 2 + (j & 1)) * 8 + xcd) * 4 + wid) * 1024;
  s.sP = (bid * 4 + wid) * 1024; s.sPq = ((j * 4 + wid) * 8 + xcd) * 1024;
  s.sPf = (j * 4 + wid) * 7168 + xcd * 448; s.sPd = (j * 4 + wid) * 1024 + xcd * 200704; s.vT = lane < 28 ? lane * 16 : lane < 56 ? 3584 + (lane - 28) * 16 : OOB;
#pragma unroll
  for (int ks = 0; ks < 2; ++ks) s.aKV[ks] = lb + (32 * wm + fr) * 128 + (((4 * ks + g) ^ (fr >> 1)) << 4);
  { const int a4 = fr >> 2, f0 = a4 | ((g & 1) << 2);
#pragma unroll
    for (int nt = 0; nt < 8; ++nt) s.wb[nt] = lb + PJ_W0 + (8 * g + a4) * 512 + (8 * wn + (nt ^ f0)) * 32 + (lane & 3) * 8; }
  s.nQf = rfl(two ? 4 : 0); s.sQf = rfl((u32)WS_Q + ((h * 64 + qbH * 4 + wid) << 14));
  s.ones = (u32x4){0xffffffffu, 0xffffffffu, 0xffffffffu, 0xffffffffu};
  asm volatile("" : "+v"(s.ones));
  asm volatile("" : "+s"(s.pwo), "+s"(s.pg), "+s"(s.pu), "+s"(s.pd), "+s"(s.nQf), "+s"(s.sQf));
  pj_body1<VT>(s);
  asm volatile("s_waitcnt lgkmcnt(0)" : PJ_PIN8(0) :: "memory");
  {
    int bid = blockIdx.x, wid = s.wid;
    asm volatile("" : "+s"(bid), "+s"(wid));
    bid = rfl(bid); wid = rfl(wid);
    const int lane = lane_id();
    IDS(bid, lane, wid);
    const int wm = wid >> 1, wn = wid & 1, fr = lane & 15, g = lane >> 4, rb = j >> 2;
    s.wra = lb + AT_X + 4096u + (4 * (lane >> 5) + ((lane & 15) >> 2)) * 896 + wid * 64 + ((lane >> 4) & 1) * 32 + (lane & 3) * 8;
    s.sWo = (u32)WS_WO + h * 262144 + (8 * which + rb) * 8192;
    s.m0Aq = lb + wid * 4096;
    s.sAq = rfl((qbO * 128 + 32 * wid) * 896); s.sWq = 8 * wid * 8192 + h * 512;
#pragma unroll
    for (int q = 0; q < 4; ++q) {
      s.vAq[q] = (lane >> 3) * 896 + (((lane & 7) ^ (((q & 1) << 2) | (lane >> 4))) << 4) + q * 6144;
      s.vWq[q] = (lane >> 5) * 8192 + (((lane & 31) ^ ((2 * (q & 1) + (lane >> 5)) << 2)) << 4) + q * 15360;
      s.qb[q] = lb + PJ_W0 + (8 * (lane >> 5) + (fr >> 2)) * 512 + (q ^ (fr >> 2)) * 64 + (lane & 2) * 16 + (g & 1) * 8 + (lane & 1) * 16;
      s.xb[q] = lb + (32 * wid + (lane & 31)) * 128 + (((2 * q + (lane >> 5)) ^ ((lane >> 1) & 7)) << 4);
    }
    const u32 perm = (g & 1) * 2 + (g >> 1), t0 = h * 32 + kv_tile(rb, 0);
    if (which) { s.vO = g * 4096 + fr * 16; s.sO = (u32)WS_V + t0 * 32768 + wm * 16384 + wn * 2048; }
    else       { s.vO = perm * 1024 + fr * 16; s.sO = (u32)WS_K + t0 * 32768 + wn * 16384 + wm * 512; }
    s.sO = rfl(s.sO);
    if (bid == 112 && wid == 0) {
      u32x4 kv[16]; bool ok; u32 n = 1u << 16;
      do {
#pragma unroll
        for (int q = 0; q < 16; ++q)
          asm volatile("buffer_load_dwordx4 %0, %1, %2, %3 offen offset:%4 sc0 sc1" : "=v"(kv[q]) : "v"(s.vP), "s"(s.rO), "s"((u32)WS_KEY + (q >> 2) * 4096), "i"((q & 3) * 1024) : "memory");
        asm volatile("s_waitcnt vmcnt(0)" : "+v"(kv[0]), "+v"(kv[1]), "+v"(kv[2]), "+v"(kv[3]), "+v"(kv[4]), "+v"(kv[5]), "+v"(kv[6]), "+v"(kv[7]),
                     "+v"(kv[8]), "+v"(kv[9]), "+v"(kv[10]), "+v"(kv[11]), "+v"(kv[12]), "+v"(kv[13]), "+v"(kv[14]), "+v"(kv[15]) :: "memory");
        u32 d = 0u;
#pragma unroll
        for (int q = 0; q < 16; ++q) d |= (kv[q][1] ^ s.tag) | (kv[q][3] ^ s.tag);
        ok = __all(d == 0u);
      } while (!ok && --n);
      unsigned m1 = 0u, m2 = 0u;
#pragma unroll
      for (int q = 0; q < 16; ++q)
#pragma unroll
        for (int e = 0; e < 4; e += 2) { const unsigned k = kv[q][e]; m2 = max(m2, min(m1, k)); m1 = max(m1, k); }
#pragma unroll
      for (int o = 32; o > 0; o >>= 1) {
        const unsigned b1 = __shfl_xor(m1, o), b2 = __shfl_xor(m2, o);
        m2 = max(min(m1, b1), max(m2, b2)); m1 = max(m1, b1);
      }
      const u32x2 iv = {(65535u - (m1 & 0xffffu)) | ((65535u - (m2 & 0xffffu)) << 16), s.tag};
      asm volatile("buffer_store_dwordx2 %0, %1, %2, %3 offen" WT "\n\tbuffer_store_dword %4, %1, %2, %5 offen" WT "\n\ts_nop 0"
                   :: "v"(iv), "v"(lane ? OOB : 0u), "s"(s.rO), "s"((u32)WS_IDX), "v"(s.tag), "s"((u32)WS_EP) : "memory");
    }
    constexpr u32 a = which ? 256u : 4096u, b = which ? 1024u : 256u;
    s.dO[0] = a; s.dO[1] = b - 3u * a;
    GAP();
  }
  pj_body2<VT>(s);
}
DI void at_side(AT& s, u32 sT) {
  u32 st;
  asm volatile("s_cmp_gt_u32 %1, 3\n\ts_cbranch_scc1 .Lv%=\n\ts_lshl_b32 %0, %1, 11\n\ts_add_u32 m0, %3, %0\n\ts_add_u32 %0, %4, %0\n\t"
               "buffer_load_dwordx4 %5, %6, %0 offen" DV " lds\n\tbuffer_load_dwordx4 %5, %6, %0 offen offset:1024" DV " lds\n\t"
               "buffer_store_dwordx4 %9, %8, %6, %2 offen" WT "\n\tbuffer_store_dwordx4 %9, %8, %6, %2 offen offset:1024" WT "\n\tbuffer_store_dwordx4 %9, %8, %6, %2 offen offset:2048" WT "\n\t"
               "buffer_store_dwordx4 %9, %8, %6, %2 offen offset:3072" WT "\n\ts_add_u32 %2, %2, 4096\n\ts_branch .Lw%=\n"
               ".Lv%=:\n\ts_cmp_lg_u32 %1, 4\n\ts_cbranch_scc1 .Lw%=\n\tbuffer_store_dword %10, %11, %6, %12 offen" WT "\n\t"
               "s_add_u32 m0, %3, 7168\n\ts_add_u32 %0, %7, 1024\n\tbuffer_load_dwordx4 %8, %6, %7 offen lds\n\tbuffer_load_dwordx4 %8, %6, %0 offen lds\n"
               ".Lw%=:\n\ts_add_u32 %1, %1, 1"
               : "=&s"(st), "+s"(s.itn), "+s"(s.sYf) : "s"(s.m0X), "s"(s.sW0), "v"(s.vL), "s"(s.rA), "s"(sT), "v"(s.vl16), "v"(s.ones), "v"(s.vtag), "v"(s.v0), "s"(s.sDf) : "memory", "scc");
}
template <int... Rs> DI void acc_zero_mid(iseq<Rs...>) { (acc_wr<128 + Rs>(0u), ...); }
template <int K> DI void q_keep1(PJ& j, FF& f) {
  constexpr int L = K < 32 ? (K % 8 ? -1 : K / 8) : (K - 32) % 3 || K > 59 ? -1 : 4 + (K - 32) / 3;
  if constexpr (L >= 0) ff_load<(L < 2 ? 8 + L : L < 10 ? L - 2 : L)>(f);
  if constexpr (K < qcvN(32)) asm volatile("v_accvgpr_mov_b32 a[%c0], a[%c1]" :: "i"(128 + K), "i"(192 + K) : ACL); else q_cv<K, 128>(j);
}
template <int... Ks> DI void q_keep(PJ& j, FF& f, iseq<Ks...>) { (q_keep1<Ks>(j, f), ...); }
template <int F0, int... Fs> DI void q_get(AT& s, iseq<Fs...>) { (pb_ld<192, F0 + Fs>(s.vl16, s.rA, s.sPB), ...); }

DI void ff_row(AT& s, int task, int lane, srd_t rX) {
  u32 n = SPIN_MAX; u32x2 iv; bool ok;
  do {
    u32x4 a, b, c, d;
    asm volatile("buffer_load_dwordx4 %0, %5, %6, %7 offen sc0 sc1\n\tbuffer_load_dwordx4 %1, %5, %6, %7 offen offset:1024 sc0 sc1\n\tbuffer_load_dwordx4 %2, %5, %6, %7 offen offset:2048 sc0 sc1\n\t"
                 "buffer_load_dwordx4 %3, %5, %6, %7 offen offset:3072 sc0 sc1\n\tbuffer_load_dwordx2 %4, %8, %6, %9 offen sc0 sc1\n\ts_waitcnt vmcnt(0)"
                 : "=&v"(a), "=&v"(b), "=&v"(c), "=&v"(d), "=&v"(iv) : "v"(s.vl16), "s"(s.rA), "s"((u32)WS_DF), "v"(0u), "s"((u32)WS_IDX) : "memory");
    u32 x = iv[1] ^ s.tag;
#pragma unroll
    for (int e = 0; e < 4; ++e) x |= ((a[e] ^ s.tag) | (b[e] ^ s.tag)) | ((c[e] ^ s.tag) | (d[e] ^ s.tag));
    ok = __all(x == 0u);
    if (!ok) __builtin_amdgcn_s_sleep(4);
  } while (!ok && --n);
  const u32 idx = rfl(iv[0]), k = task >= 28, ir = (k ? idx >> 16 : idx) & 2047u, col = (task - 28 * k) * 16 + (lane & 3) * 4;
  f32x4 v[7]; u32x2 xv; float rv; const u32 sf = rfl((u32)WS_YF + task * (FFN_N * 64));
#pragma unroll
  for (int q = 0; q < 7; ++q) asm volatile("buffer_load_dwordx4 %0, %1, %2, %3 offen offset:%4 sc0 sc1" : "=v"(v[q]) : "v"(s.vl16), "s"(s.rA), "s"(sf + (q >> 2) * 4096), "i"((q & 3) * 1024) : "memory");
  asm volatile("buffer_load_dwordx2 %0, %9, %10, %11 offen\n\tbuffer_load_dword %1, %12, %13, %14 offen sc0 sc1\n\ts_waitcnt vmcnt(0)"
               : "=&v"(xv), "=&v"(rv), "+v"(v[0]), "+v"(v[1]), "+v"(v[2]), "+v"(v[3]), "+v"(v[4]), "+v"(v[5]), "+v"(v[6])
               : "v"(col * 2), "s"(rX), "s"(ir * (M_ * 2)), "v"(0u), "s"(s.rA), "s"((u32)WS_R + ir * 4) : "memory");
  const f32x4 fs = ((v[0] + v[1]) + (v[2] + v[3])) + ((v[4] + v[5]) + v[6]);
  const float sg = rbf(1.f / (1.f + __expf(-rv))), xs[4] = {BF_LO(xv[0]), BF_HI(xv[0]), BF_LO(xv[1]), BF_HI(xv[1])};
  float r[4];
#pragma unroll
  for (int e = 0; e < 4; ++e) {
    unsigned w = __float_as_uint(fs[e]);
    const auto s32 = __builtin_amdgcn_permlane32_swap(w, w, false, false);
    w = __float_as_uint(__uint_as_float(s32[0]) + __uint_as_float(s32[1]));
    const auto s16 = __builtin_amdgcn_permlane16_swap(w, w, false, false);
    float a = __uint_as_float(s16[0]) + __uint_as_float(s16[1]);
    a += __uint_as_float(__builtin_amdgcn_update_dpp(0u, __float_as_uint(a), 0x128, 0xf, 0xf, false));
    a += __uint_as_float(__builtin_amdgcn_update_dpp(0u, __float_as_uint(a), 0x124, 0xf, 0xf, false));
    r[e] = xs[e] + rbf(rbf(a) * sg);
  }
  const u32x2 o = {cvtpk(r[0], r[1]), cvtpk(r[2], r[3])};
  asm volatile("buffer_store_dwordx2 %0, %1, %2, %3 offen" WT "\n\ts_waitcnt vmcnt(0)\n\tbuffer_store_dword %4, %5, %2, %6 offen offset:12" WT
               :: "v"(o), "v"(lane < 4 ? col * 2 : OOB), "s"(s.rA), "s"((u32)WS_BB + k * (M_ * 2)), "v"(s.vtag), "v"(s.v0), "s"((u32)WS_IDX + task * 8) : "memory");
}
typedef __attribute__((ext_vector_type(8))) __bf16 bfx8;
#define RD_PIN "+v"(y[0]), "+v"(y[1]), "+v"(y[2]), "+v"(y[3]), "+v"(y[4]), "+v"(y[5]), "+v"(y[6]), "+v"(y[7]), "+v"(y[8]), "+v"(y[9]), "+v"(y[10]), "+v"(y[11]), "+v"(y[12]), "+v"(y[13]), "+v"(y[14]), "+v"(y[15])
#define RDL(i, b, o) "s_bitcmp0_b32 %16, " #i "\n\ts_cbranch_scc1 .Lr" #i "_%=\n\tbuffer_load_dwordx4 %" #i ", %17, %18, %" #b " offen offset:" #o " sc0 sc1\n.Lr" #i "_%=:\n\t"
template <int I> DI void rd_ld(u32x4 (&y)[16], u32 vy, srd_t rA, u32 sy) {
  asm volatile("buffer_load_dwordx4 %0, %1, %2, %3 offen offset:%4 sc0 sc1" : "=v"(y[I]) : "v"(vy), "s"(rA), "s"(sy + (I & 1) * (4u << 20)), "i"(((I >> 1) & 1) * 512 + ((I >> 2) & 1) * 1024 + (I >> 3) * 256) : "memory");
}
template <int... Is> DI void rd_ld_all(u32x4 (&y)[16], u32 vy, srd_t rA, u32 sy, iseq<Is...>) { (rd_ld<Is>(y, vy, rA, sy), ...); }
DI void rd_task(AT& s, RD& d, u32 sy, srd_t rOut) {
  const int lane = lane_id(), r = lane & 15, g = lane >> 4;
  const u32 vy = g * (1u << 20) + r * 16;
  u32x4 y[16];
  rd_ld_all(y, vy, s.rA, sy, make_iseq<16>{});
  asm volatile("s_waitcnt vmcnt(16)" : "+v"(d.gt[0]), "+v"(d.gt[1]), "+v"(d.xv[0]), "+v"(d.xv[1]), "+v"(d.xv[2]), "+v"(d.xv[3]), "+v"(d.bv[0]), "+v"(d.bv[1]), "+v"(d.bv[2]), "+v"(d.bv[3]) :: "memory");
  PSTAMP(28);
  u32x4 sel[2];
#pragma unroll
  for (int hi = 0; hi < 2; ++hi) {
    const int rr = r - 4 * hi, e = (rr & ~3) == 0 ? rr : (rr & ~3) == 8 ? rr - 4 : -1;
#pragma unroll
    for (int w = 0; w < 4; ++w) sel[hi][w] = e == 2 * w ? 0x3f80u : e == 2 * w + 1 ? 0x3f800000u : 0u;
  }
  const u32 idx = __builtin_amdgcn_readlane(d.fl[0], 63);
  float bb[4][4];
#pragma unroll
  for (int k = 0; k < 4; ++k) {
    const u32 rw = d.row + 16 * (k >> 1);
    const bool rt = rw == (idx & 0xffffu) || rw == idx >> 16;
    const u32 a = rt ? d.bv[k][0] : d.xv[k][0], b = rt ? d.bv[k][1] : d.xv[k][1];
    const float omg = rbf(1.f - d.gt[k >> 1]);
    bb[k][0] = rbf(omg * BF_LO(a)); bb[k][1] = rbf(omg * BF_HI(a)); bb[k][2] = rbf(omg * BF_LO(b)); bb[k][3] = rbf(omg * BF_HI(b));
    PINF(bb[k][0]); PINF(bb[k][1]); PINF(bb[k][2]); PINF(bb[k][3]);
  }
  asm volatile("" : "+v"(sel[0]), "+v"(sel[1]));
  asm volatile("s_waitcnt vmcnt(0)" : RD_PIN :: "memory");
  for (u32 n = SPIN_MAX;;) {
    u32 miss = 0u;
#pragma unroll
    for (int i = 0; i < 16; ++i) miss |= __any(y[i][3] == 0xffffffffu) ? 1u << i : 0u;
    if (!miss || !--n) break;
    asm volatile(RDL(0, 19, 0) RDL(1, 20, 0) RDL(2, 19, 512) RDL(3, 20, 512) RDL(4, 19, 1024) RDL(5, 20, 1024) RDL(6, 19, 1536) RDL(7, 20, 1536)
                 RDL(8, 19, 256) RDL(9, 20, 256) RDL(10, 19, 768) RDL(11, 20, 768) RDL(12, 19, 1280) RDL(13, 20, 1280) RDL(14, 19, 1792) RDL(15, 20, 1792) "s_waitcnt vmcnt(0)"
                 : RD_PIN : "s"(rfl(miss)), "v"(vy), "s"(s.rA), "s"(sy), "s"(sy + (4u << 20)) : "memory", "scc");
  }
  PSTAMP(29);
  u32 pk[4][2];
#pragma unroll
  for (int k = 0; k < 4; ++k) {
    f32x4 a = {0.f, 0.f, 0.f, 0.f};
#pragma unroll
    for (int i = 0; i < 4; ++i) a = __builtin_amdgcn_mfma_f32_16x16x32_bf16(__builtin_bit_cast(bfx8, sel[i >> 1]), __builtin_bit_cast(bfx8, y[8 * (k >> 1) + 4 * (k & 1) + i]), a, 0, 0, 0);
    const float gg = d.gt[k >> 1];
    pk[k][0] = cvtpk(rbf(gg * rbf(a[0])) + bb[k][0], rbf(gg * rbf(a[1])) + bb[k][1]); pk[k][1] = cvtpk(rbf(gg * rbf(a[2])) + bb[k][2], rbf(gg * rbf(a[3])) + bb[k][3]);
  }
#pragma unroll
  for (int u = 0; u < 2; ++u) {
    const auto r0 = __builtin_amdgcn_permlane16_swap(pk[2 * u][0], pk[2 * u + 1][0], false, false);
    const auto r1 = __builtin_amdgcn_permlane16_swap(pk[2 * u][1], pk[2 * u + 1][1], false, false);
    st16<0>((u32x4){r0[0], r1[0], r0[1], r1[1]}, d.vx - g * 8 + (g & 1) * 32 + (g >> 1) * 16, rOut, u * 16 * M_ * 2);
  }
  PSTAMP(30);
}

#define RSTAMP() do { } while (0)
#define FINISH(MRG) \
  o.sb[0] = lb + AT_X; o.sb[1] = lb; o.sb[2] = lb + 32768; o.sb[3] = lb + 98304; o.sb[4] = lb + 65536; \
  o.wofs = wid * 8192; o.rA = s.rA; o.vL = s.vL; o.vl16 = s.vl16; o.tag = s.tag; \
  o.sY = (u32)WS_Y + SPL(h); o.sX = (u32)WS_X + SPL(h); o.sXp = (u32)WS_X + SPL(h ^ 1); \
  o.sW1 = sW1; o.sWf = SWF(role, h); o.sWfp = SWF(role, h ^ 1); \
  const int task = (xcd * 14 + (j & 15)) * 4 + wid, qb = task / 14 + (role ? 0 : 32), t = task % 14, ch = t >= 7; \
  o.r.row = qb * 32 + (ln & 15); o.r.vc = (32 * t + 4 * (ln >> 4)) * 2; o.r.vx = o.r.row * (M_ * 2) + o.r.vc; o.r.vf = ln < 56 ? 8 + 8 * ln : 0; o.r.rX = mk_srd(x, S_ * M_ * 2); \
  at_finish<MRG>(s, o, want); \
    \
  if (p < 7) rd_task(s, o.r, rfl((u32)WS_Y + (((role * 16 + ch) * 8 + (role ? qb >> 2 : 15 - (qb >> 2))) * 4 + (qb & 3)) * 16384u + (t - 7 * ch) * 2048), mk_srd(out, S_ * M_ * 2))
#define LOOP_END \
  const u16* x; u16* out; \
  asm volatile("s_load_dwordx2 %0, %2, 0x0\n\ts_load_dwordx2 %1, %2, 0x60\n\ts_waitcnt lgkmcnt(0)" : "=&s"(x), "=&s"(out) : "s"(ka) : "memory"); \
  GAP(); PSTAMP(3);     \
  int ln = lane; asm volatile("" : "+v"(ln))
extern "C" __global__ __launch_bounds__(256, 1)
void k_main(const u16* x_, const u16* ckv, const u16* wq, const u16* wuk, const u16* wuv, char* ws,
            const u16* gate_w, const u16* w_r, const u16* wo, const u16* wg_, const u16* wu_, const u16* wd_, u16* out_) {
  extern __shared__ __attribute__((aligned(16))) char smem[];
  int wid = rfl(threadIdx.x >> 6);
  PJ pj;
  pj.pg = wg_; pj.pu = wu_; pj.pd = wd_;
  if (blockIdx.x & 16) phase1<true>(pj, smem, x_, ckv, wq, wuv, ws, gate_w, w_r, wo, wid);
  else                 phase1<false>(pj, smem, x_, ckv, wq, wuk, ws, gate_w, w_r, wo, wid);
  GAP();
  int bid = blockIdx.x;
  asm volatile("" : "+s"(bid), "+s"(wid));
  bid = rfl(bid); wid = rfl(wid);
  const int lane = lane_id();
  IDS(bid, lane, wid);
  const int l31 = lane & 31, hi = lane >> 5;
  AT s;
  typedef const __attribute__((address_space(4))) char* karg_t;
  karg_t ka = (karg_t)__builtin_amdgcn_kernarg_segment_ptr();
  const u32 sq = rfl(p < 7 ? (u32)WS_Q + ((h * 64 + qbH * 4 + wid) << 14) : OOB);
  { const int which = (j >> 1) & 1, rb = j >> 2;
    const u32 a = which ? 256u : 4096u, b = which ? 1024u : 256u;
    pj.dO[0] = rfl(a); pj.dO[1] = rfl(b - 3u * a);
#pragma unroll
    for (int l = 0; l < 3; ++l) pj.dO[2 + l] = rfl((u32)(kv_tile(rb, l + 1) - kv_tile(rb, l)) * 32768u - b - 3u * a);
    pj.sO = rfl(pj.sO); pj.tag = rfl(pj.tag); pj.wid = wid;
    pj.rX = mk_srd(x_, S_ * M_ * 2); pj.rWq = mk_srd(wq, M_ * HD * 2); pj.rO = mk_srd(ws, (u32)WS_END);
    pj.m0W = lb + PJ_W0 + wid * 4096; pj.m0Aq = lb + wid * 4096; pj.sAq = rfl((qbO * 128 + 32 * wid) * 896); pj.sWq = 8 * wid * 8192 + h * 512;
    pj.sFl = rfl((u32)WS_KVF + h * 256 + (2 * rb + which) * 16); pj.vFl = (lane & 15) * 16; pj.poll = 0u;
    pj.sK0 = rfl(sKb + (two ? NT0 * 32768u : 0u)); pj.sV0 = pj.sK0 + (u32)(WS_V - WS_K);
    pj.vL = lane * 16 + wid * 8192; pj.m0K = lb + wid * 8192;
    pj.sQo = rfl(role ? OOB : sq);
    const int sg = (l31 & 7) | ((l31 & 8) << 1) | ((l31 & 16) >> 1);
    s.kb_ = lb + hi * 1024 + sg * 16; s.kb0 = s.kb_ + AT_X;
    asm volatile("s_load_dwordx2 %0, %4, 0x0\n\ts_load_dwordx2 %1, %4, 0x48\n\ts_load_dwordx2 %2, %4, 0x50\n\ts_load_dwordx2 %3, %4, 0x58"
                 : "=&s"(s.f.x), "=&s"(s.f.wg), "=&s"(s.f.wu), "=&s"(s.f.wd) : "s"(ka) : "memory");
    PSTAMP(9);
    GAP();
    q_all(pj, s.kf, s.kb0, make_iseq<14>{});
    asm volatile("" : "+s"(s.f.x), "+s"(s.f.wg), "+s"(s.f.wu), "+s"(s.f.wd));
    PSTAMP(10);
  }
  s.rA = pj.rO; s.sW0 = (u32)WS_WO + h * 262144 + (h & 1) * 131072; s.tag = pj.tag;
  s.vL = pj.vL; s.vl16 = pj.vP;
  s.rem = rfl(two ? 14 - 2 * p : role ? 16 : NT0); s.mk = rfl((role || p == 7) ? 2 : 0);
  s.sE0 = rfl(two ? sKb : s.sW0 + 32768u);
  static_assert(NT0 == 18, "tiles");
  s.sK = pj.sK0 + 32768u; s.sV = pj.sV0;
  s.m0K = pj.m0K; s.m0V = lb + 65536 + wid * 8192; s.m0X = lb + AT_X + wid * 8192;
  s.vb_ = lb + 65536 + hi * 4096 + l31 * 16;
  s.dq = wid * 32 + l31 - 4 * hi;
  s.mhat = 0.f; s.l0 = 0.f; s.l1 = 0.f; s.fct = 1.f; s.pend = false; s.nz = 0u; s.itn = 0u;
  s.sFl = (pj.sFl & ~511u) + 12u; s.vFl = l31 * 16;
  s.v0 = lane ? OOB : 0u; s.vtag = s.tag; s.sPB = role && !two ? OOB : sq; s.sM = rfl(p < 7 ? sM : OOB); s.v8 = l31 * 8;
  const u32 sW1 = (u32)WS_WO + (h ^ 1) * 262144 + (h & 1) * 131072, sT = rfl(sW1 + ((j >> 1) * 4 + wid) * 2048);
  s.sYf = rfl((u32)WS_Y + SPL(h)); s.sDf = rfl((u32)WS_DF + (bid * 4 + wid) * 4);
  asm volatile("" : "+s"(s.rem), "+s"(s.mk), "+s"(s.sE0), "+s"(s.sPB), "+s"(s.sM), "+s"(s.sYf), "+s"(s.sDf));
  asm volatile("" : "+v"(s.v0), "+v"(s.vtag), "+v"(s.v8));
  OP o;
  GAP();
  if (two) {
    FF& f = s.f;
    z_dma<ZA, 0>(pj);
    const int sl = xcd * 14 + j, rp = lane >> 1, fq = lane >> 4, n8 = lane & 15;
    const u32 o1 = rp < 28 ? 0u : OOB, o2 = n8 < 14 ? 0u : OOB;
    f.rA = s.rA;
    f.vw = (u32)((112 * wid + 4 * rp) * F_ + sl * 16 + (lane & 1) * 8) * 2 | o1;
    f.vx = (u32)(112 * wid + 4 * rp) * 2 | o1;
    f.vd = (u32)((sl * 16 + 4 * fq) * M_ + 112 * wid + 8 * n8) * 2 | o2;
    const int n = 112 * wid + 8 * n8 + 4 * (fq & 1);
    f.vy = (u32)((((fq >> 1) * 28 + (n >> 4)) * FFN_N + sl) * 16 + (n & 15)) * 4 | o2;
    f.lw = lb + wid * 256 + fq * 64 + (lane & 1) * 32; f.lr = lb + wid * 8192 + (lane & 31) * 4;
    f.lh = f.lr + 1024; f.lg = lb + wid * 8192 + 1024 + fq * 16;
    { u32 c = SPIN_MAX, z = 0u;
      while (rfl(pj.iv[1]) != s.tag && --c)
        asm volatile("buffer_load_dwordx2 %0, %1, %2, %3 offen sc0 sc1\n\ts_waitcnt vmcnt(0)" : "+v"(pj.iv) : "v"(z), "s"(s.rA), "s"((u32)WS_IDX) : "memory");
      f.i0 = rfl(pj.iv[0]); f.i1 = (f.i0 >> 16) & 2047u; f.i0 &= 2047u; }
    asm volatile("" : "+s"(f.i0), "+s"(f.i1));
    asm volatile("s_waitcnt lgkmcnt(0)" : AT_PIN8(kf, 0), AT_PIN4(kf, 8) :: "memory");
    GAP();
    u32 kp;
    q_keep(pj, f, make_iseq<64>{});
    asm volatile("s_waitcnt vmcnt(14)\n\ts_barrier" ::: "memory");
    z_flag<3>(pj);
    PSTAMP(20);
    q_get<0>(s, make_iseq<16>{});
    kv_poll(kp, s.vFl, s.rA, s.sFl);
    PSTAMP(21);
    asm volatile("s_waitcnt vmcnt(17)" ::: "memory");
    ff_steps<56>(f, make_iseq<50>{});
    PSTAMP(19);
    asm volatile("s_waitcnt vmcnt(0)" : "+v"(kp) :: "memory");
    at_getq(s);
    kv_check_all(kp, s.tag, s.vFl, s.rA, s.sFl);
    GAP();
    PSTAMP(1);
    at_phase1<false>(s, pj, make_iseq<32>{});
    at_period<1, 5>(s);
    asm volatile("s_waitcnt lgkmcnt(0)" ::: "memory");
    GAP();
    PSTAMP(12);
    s.ones = (u32x4){0xffffffffu, 0xffffffffu, 0xffffffffu, 0xffffffffu};
    asm volatile("" : "+v"(s.ones));
    for (int seg = 0;; ++seg) {
      while (s.rem > 0) { RSTAMP(); at_period<0, 1>(s); at_side(s, sT); at_period<1, 1>(s); }
      if (seg) break;
      PSTAMP(2);
      s.rem = 2 * p + 2; s.sE0 = s.sW0 + 32768u; s.sV = sVb;
      PSTAMP(24);
      at_period<0, 2>(s);
      PSTAMP(25);
      at_period<1, 3>(s);
      asm volatile("s_waitcnt lgkmcnt(0)" ::: "memory");
      PSTAMP(26);
      GAP();
    }
    u32 want = 0u;
    LOOP_END;
    FINISH(false);
    __builtin_amdgcn_endpgm();
  }
  {
    PSTAMP(1);
    at_phase1<true>(s, pj, make_iseq<32>{});
    PSTAMP(24);
    if (p == 7) acc_zero_mid(make_iseq<64>{});
    at_phaseA<1, 4>(s, make_iseq<32>{});
    PSTAMP(25);
    at_phaseB<1, 4>(s, make_iseq<32>{});
    asm volatile("s_waitcnt vmcnt(4) lgkmcnt(0)" ::: "memory");
    GAP();
    PSTAMP(12);
    u32 want = 2u + (u32)((14 - p) >> 3);
    s.ones = (u32x4){0xffffffffu, 0xffffffffu, 0xffffffffu, 0xffffffffu};
    asm volatile("" : "+s"(want));
    asm volatile("" : "+v"(s.ones));
    while (s.rem > 0) { RSTAMP(); at_period<0, 0>(s); at_check<false>(s, want); at_side(s, sT); at_period<1, 0>(s); }
    LOOP_END;
    if (role && (xcd * 2 + (j & 1)) * 4 + wid < 56) ff_row(s, (xcd * 2 + (j & 1)) * 4 + wid, ln, mk_srd(x, S_ * M_ * 2));
#pragma unroll
    for (int a = 0; a < 2; ++a) {
      const int rr = (ln & 31) - 16 * a - 4 * (ln >> 5), e = (rr & ~3) == 0 ? rr : (rr & ~3) == 8 ? rr - 4 : -1;
#pragma unroll
      for (int w = 0; w < 4; ++w) o.id[a][w] = e == 2 * w ? 0x3f80u : e == 2 * w + 1 ? 0x3f800000u : 0u;
    }
    FINISH(true);
  }
  asm volatile(".Lk_code_end:");
}

extern "C" void kernel_launch(void* const* d_in, const int* in_sizes, int n_in,
                              void* d_out, int out_size, void* d_ws, size_t ws_size,
                              hipStream_t stream) {
  (void)hipFuncSetAttribute((const void*)k_main, hipFuncAttributeMaxDynamicSharedMemorySize, AT_LDS);
  k_main<<<256, 256, AT_LDS, stream>>>((const u16*)d_in[0], (const u16*)d_in[1], (const u16*)d_in[2], (const u16*)d_in[3], (const u16*)d_in[4],
      (char*)d_ws, (const u16*)d_in[11], (const u16*)d_in[7], (const u16*)d_in[5], (const u16*)d_in[8], (const u16*)d_in[9], (const u16*)d_in[10], (u16*)d_out);
}
```

```cpp
#include <hip/hip_runtime.h>
#include <stdint.h>

typedef __attribute__((ext_vector_type(4))) float f32x4;
typedef __attribute__((ext_vector_type(2))) float f32x2;
typedef __attribute__((ext_vector_type(4))) unsigned u32x4;
typedef __attribute__((ext_vector_type(2))) unsigned u32x2;
typedef __attribute__((ext_vector_type(2))) __bf16 bfx2;
typedef unsigned short u16;
typedef unsigned u32;
#define DI __device__ __forceinline__
constexpr int M_ = 448, F_ = 1792, FFN_N = 112;
constexpr size_t WS_K = 0u;
constexpr size_t WS_V = 16777216u;
constexpr size_t WS_Q = 33554432u;
constexpr size_t WS_PB = 41943040u;
constexpr size_t WS_Y = 50331648u;
constexpr size_t WS_WO = 83886080u;
constexpr size_t WS_ML = 88080384u;
constexpr size_t WS_KEY = 89128960u;
constexpr size_t WS_IDX = 89161728u;
constexpr size_t WS_EP = 89162752u;
constexpr size_t WS_BB = 89163776u;
constexpr size_t WS_BW = 89169920u;
constexpr size_t WS_FW = 89178112u;
constexpr size_t WS_KVF = 89194496u;
constexpr size_t WS_QF = 89227264u;
constexpr size_t WS_PF = 89260032u;
constexpr size_t WS_YW = 89325568u;
constexpr size_t WS_YF = 89456640u;
constexpr size_t WS_END = 89858048u;
DI unsigned cvtpk(float lo, float hi) { return __builtin_bit_cast(unsigned, __builtin_convertvector((f32x2){lo, hi}, bfx2)); }
DI float rbf(float f) { return __uint_as_float(cvtpk(f, f) << 16); }
#define BF_LO(u) __uint_as_float((u) << 16)
#define BF_HI(u) __uint_as_float((u) & 0xffff0000u)
typedef u32x4 srd_t;
DI u32 rfl(u32 x) { return __builtin_amdgcn_readfirstlane(x); }
DI srd_t rfl4(srd_t r) { return (srd_t){rfl(r[0]), rfl(r[1]), rfl(r[2]), rfl(r[3])}; }
constexpr u32 OOB = 0x40000000u;
template <int CTRL> DI float dpp_add(float v) { return v + __uint_as_float(__builtin_amdgcn_update_dpp(0, __float_as_uint(v), CTRL, 0xf, 0xf, false)); }
DI void await(u32 vo, srd_t r, u32 so, u32 tag, unsigned long long need) {
  u32 w, n = 1u << 17; unsigned long long t;
  asm volatile(".Lw%=:\n\tbuffer_load_dword %0, %3, %4, %5 offen sc0 sc1\n\ts_waitcnt vmcnt(0)\n\tv_cmp_eq_u32_e32 vcc, %6, %0\n\ts_andn2_b64 %1, %7, vcc\n\ts_cbranch_scc0 .Lg%=\n\t"
               "s_sub_u32 %2, %2, 1\n\ts_cbranch_scc0 .Lw%=\n.Lg%=:"
               : "=&v"(w), "=&s"(t), "+s"(n) : "v"(vo), "s"(r), "s"(so), "s"(tag), "s"(need) : "memory", "scc", "vcc");
}

#define V10(p) p "0", p "1", p "2", p "3", p "4", p "5", p "6", p "7", p "8", p "9"
#define V100(p) V10(p "0"), V10(p "1"), V10(p "2"), V10(p "3"), V10(p "4"), V10(p "5"), V10(p "6"), V10(p "7"), V10(p "8"), V10(p "9")
#define R256(p) V10(p "1"), V10(p "2"), V10(p "3"), V10(p "4"), V10(p "5"), V10(p "6"), V10(p "7"), V10(p "8"), V10(p "9"), V100(p "1"), \
                V10(p "20"), V10(p "21"), V10(p "22"), V10(p "23"), V10(p "24"), p "250", p "251", p "252", p "253", p "254", p "255"

#define ACL V10("a"), R256("a")
#define YL(k, c) "buffer_load_dwordx4 a[%c4+" #k "*16+" #c "*4:%c4+" #k "*16+" #c "*4+3], %1, %2, %0 offen offset:" #c "*256 sc0 sc1\n\t"
#define YK(k) "s_add_u32 %0, %3, " #k "*7340032\n\t" YL(k, 0) YL(k, 1) YL(k, 2) YL(k, 3)
template <int S> DI void y_load(u32 vy, srd_t r, u32 so) {
  u32 t;
  asm volatile(YK(0) YK(1) YK(2) YK(3) : "=&s"(t) : "v"(vy), "s"(r), "s"(so), "i"(64 * S) : "memory", "scc", ACL);
}
#define YM(k, c, d, s, z) "v_mfma_f32_16x16x32_bf16 %" #d ", %" #s ", a[%c4+" #k "*16+" #c "*4:%c4+" #k "*16+" #c "*4+3], " z "\n\t"
#define YS(k) YM(k, 0, 0, 2, "%0") YM(k, 2, 1, 2, "%1") YM(k, 1, 0, 3, "%0") YM(k, 3, 1, 3, "%1")
struct Task { u32x2 x[2]; float g, b[8]; u32 vx, sy; };
template <int S> DI void y_done(Task& k, const u32x4 (&sel)[2], srd_t rOut) {
  f32x4 c0, c1;
  asm volatile(YM(0, 0, 0, 2, "0") YM(0, 2, 1, 2, "0") YM(0, 1, 0, 3, "%0") YM(0, 3, 1, 3, "%1") YS(1) YS(2) YS(3) "s_nop 7"
               : "=&v"(c0), "=&v"(c1) : "v"(sel[0]), "v"(sel[1]), "i"(64 * S) : "memory");
  float r[8];
#pragma unroll
  for (int e = 0; e < 8; ++e) r[e] = rbf(k.g * rbf(e < 4 ? c0[e & 3] : c1[e & 3])) + k.b[e];
  const u32x2 o0 = {cvtpk(r[0], r[1]), cvtpk(r[2], r[3])}, o1 = {cvtpk(r[4], r[5]), cvtpk(r[6], r[7])};
  asm volatile("buffer_store_dwordx2 %0, %2, %3, 0 offen\n\tbuffer_store_dwordx2 %1, %2, %3, 0 offen offset:32" :: "v"(o0), "v"(o1), "v"(k.vx), "s"(rOut) : "memory");
}

extern "C" __global__ __launch_bounds__(256, 1)
void k_main(const u16* x_, const u16* ckv, const u16* wq, const u16* wuk, const u16* wuv, char* ws_,
            const u16* gate_w, const u16* w_r, const u16* wo, const u16* wg_, const u16* wu_, const u16* wd_, u16* out_) {
  typedef const __attribute__((address_space(4))) char* karg_t;
  karg_t ka = (karg_t)__builtin_amdgcn_kernarg_segment_ptr();
  u32 bid = blockIdx.x, tid = threadIdx.x, tag, sfw, idx;
  srd_t rX, rO, rOut;
  asm volatile(R"(
.macro Mv d,a,b
v_mfma_f32_16x16x32_bf16 v[\d:\d+3], a[\a:\a+3], a[\b:\b+3], v[\d:\d+3]
.endm
.macro Mp d,a,b
v_mfma_f32_16x16x32_bf16 v[\d:\d+3], a[\a:\a+3], v[\b:\b+3], v[\d:\d+3]
.endm
.macro Ma d,a,b
v_mfma_f32_16x16x32_bf16 a[\d:\d+3], a[\a:\a+3], a[\b:\b+3], a[\d:\d+3]
.endm
.macro Mz d,a,b
v_mfma_f32_16x16x32_bf16 v[\d:\d+3], a[\a:\a+3], a[\b:\b+3], 0
.endm
.macro Rb d,v,o
ds_read_b128 a[\d:\d+3], v\v offset:\o
.endm
.macro Rt d,v,o
ds_read_b64_tr_b16 a[\d:\d+1], v\v offset:\o
.endm
.macro Cv d,a,b
v_cvt_pk_bf16_f32 v\d, v\a, v\b
.endm
.LzSTART:
s_load_dwordx16 s[64:79], s[0:1], 0
s_load_dwordx2 s[80:81], s[0:1], 64
v_lshrrev_b32 v1, 6, v0
v_and_b32 v0, 63, v0
v_readfirstlane_b32 s33, v1
v_lshlrev_b32 v1, 4, v0
s_and_b32 s28, s2, 7
s_bfe_u32 s29, s2, 0x10003
s_lshl1_add_u32 s34, s28, s29
s_bfe_u32 s35, s2, 0x30004
s_bfe_u32 s85, s2, 0x10004
s_bfe_u32 s86, s2, 0x20005
s_lshl_b32 s36, s33, 13
s_mov_b32 s95, 40
s_bitcmp1_b32 s2, 7
s_cbranch_scc1 .LzB0
s_mov_b32 s84, 0x6040200
s_mov_b32 s28, 0x7050312
s_bitcmp1_b32 s2, 5
s_cmov_b32 s84, 0x18161401
s_cmov_b32 s28, 0x19171513
s_bitcmp1_b32 s2, 6
s_cmov_b32 s84, s28
s_lshl_b32 s28, s33, 3
s_lshr_b32 s28, s84, s28
s_and_b32 s28, s28, 255
s_lshl_b32 s38, s28, 17
s_add_u32 s38, s38, 0xff80
s_lshl_b32 s39, s33, 16
s_lshl_b32 s28, s34, 9
s_add_u32 s39, s39, s28
s_sub_u32 s39, s39, 0x40000
s_lshl_b32 s37, s33, 12
s_add_u32 s37, s37, 0x10000
v_lshrrev_b32 v28, 4, v0
v_and_b32 v29, 7, v0
v_xor_b32 v28, v28, v29
v_lshlrev_b32 v28, 4, v28
v_lshrrev_b32 v29, 3, v0
v_lshl_or_b32 v8, v29, 11, v28
v_xor_b32 v9, 64, v8
v_add_u32 v9, 15360, v9
v_add_u32 v10, 30720, v8
v_add_u32 v11, 30720, v9
s_and_b32 s28, s33, 1
s_lshl_b32 s28, s28, 7
v_lshrrev_b32 v29, 5, v0
v_mul_u32_u24 v29, 0x2220, v29
v_xor_b32 v29, v29, v1
v_xor_b32 v12, s28, v29
v_xor_b32 v13, 64, v12
v_add_u32 v13, 15360, v13
v_add_u32 v14, 30720, v12
v_add_u32 v15, 30720, v13
s_waitcnt lgkmcnt(0)
s_mov_b32 s4, s66
s_mov_b32 s5, s67
s_mov_b32 s6, 0x400000
s_mov_b32 s7, 0x20000
s_cmp_eq_u32 s85, 1
s_cselect_b32 s8, s72, s70
s_cselect_b32 s9, s73, s71
)" R"(
s_mov_b32 s10, 0x800000
s_mov_b32 s11, 0x20000
s_add_u32 m0, s37, 0
s_add_u32 s39, s39, 0x40000
buffer_load_dwordx4 v12, s[8:11], s39 offen offset:0 lds
buffer_load_dwordx4 v13, s[8:11], s39 offen offset:1024 lds
buffer_load_dwordx4 v14, s[8:11], s39 offen offset:2048 lds
buffer_load_dwordx4 v15, s[8:11], s39 offen offset:3072 lds
s_add_u32 m0, s36, 0
s_add_u32 s38, s38, 0xffff0080
buffer_load_dwordx4 v8, s[4:7], s38 offen offset:0 lds
buffer_load_dwordx4 v9, s[4:7], s38 offen offset:1024 lds
buffer_load_dwordx4 v10, s[4:7], s38 offen offset:2048 lds
buffer_load_dwordx4 v11, s[4:7], s38 offen offset:3072 lds
s_add_u32 m0, s36, 4096
s_add_u32 s38, s38, 0x10000
buffer_load_dwordx4 v8, s[4:7], s38 offen offset:0 lds
buffer_load_dwordx4 v9, s[4:7], s38 offen offset:1024 lds
buffer_load_dwordx4 v10, s[4:7], s38 offen offset:2048 lds
buffer_load_dwordx4 v11, s[4:7], s38 offen offset:3072 lds
s_add_u32 m0, s37, 16384
s_add_u32 s39, s39, 0x40000
buffer_load_dwordx4 v12, s[8:11], s39 offen offset:0 lds
buffer_load_dwordx4 v13, s[8:11], s39 offen offset:1024 lds
buffer_load_dwordx4 v14, s[8:11], s39 offen offset:2048 lds
buffer_load_dwordx4 v15, s[8:11], s39 offen offset:3072 lds
s_add_u32 m0, s36, 32768
s_add_u32 s38, s38, 0xffff0080
buffer_load_dwordx4 v8, s[4:7], s38 offen offset:0 lds
buffer_load_dwordx4 v9, s[4:7], s38 offen offset:1024 lds
buffer_load_dwordx4 v10, s[4:7], s38 offen offset:2048 lds
buffer_load_dwordx4 v11, s[4:7], s38 offen offset:3072 lds
s_add_u32 m0, s36, 36864
s_add_u32 s38, s38, 0x10000
buffer_load_dwordx4 v8, s[4:7], s38 offen offset:0 lds
buffer_load_dwordx4 v9, s[4:7], s38 offen offset:1024 lds
buffer_load_dwordx4 v10, s[4:7], s38 offen offset:2048 lds
buffer_load_dwordx4 v11, s[4:7], s38 offen offset:3072 lds
s_add_u32 m0, s37, 32768
s_add_u32 s39, s39, 0x40000
buffer_load_dwordx4 v12, s[8:11], s39 offen offset:0 lds
buffer_load_dwordx4 v13, s[8:11], s39 offen offset:1024 lds
buffer_load_dwordx4 v14, s[8:11], s39 offen offset:2048 lds
buffer_load_dwordx4 v15, s[8:11], s39 offen offset:3072 lds
s_getpc_b64 s[24:25]
.Lzpc0:
s_sub_u32 s24, s24, .Lzpc0-.LzSTART
s_subb_u32 s25, s25, 0
s_mov_b32 s26, .LzEND-.LzSTART+8192
s_mov_b32 s27, 0x20000
s_lshr_b32 s28, s2, 3
s_lshl2_add_u32 s28, s28, s33
s_lshl_b32 s28, s28, 11
v_lshrrev_b32 v5, 4, v0
v_min_u32 v5, 1, v5
v_lshlrev_b32 v5, 27, v5
v_add_lshl_u32 v5, v5, v1, 3
buffer_load_dword v5, v5, s[24:27], s28 offen
s_mov_b32 s28, 0x5508400
s_load_dword s3, s[74:75], s28
s_mov_b32 s20, s74
s_mov_b32 s21, s75
s_mov_b32 s22, 0x55b2000
s_mov_b32 s23, 0x20000
)" R"(
s_mov_b32 s12, s64
s_mov_b32 s13, s65
s_mov_b32 s14, 0x1c0000
s_mov_b32 s15, 0x20000
s_mov_b32 s16, s68
s_mov_b32 s17, s69
s_mov_b32 s18, 0x380000
s_mov_b32 s19, 0x20000
s_load_dwordx4 s[76:79], s[0:1], 72
s_load_dwordx2 s[70:71], s[0:1], 88
s_load_dwordx2 s[74:75], s[0:1], 96
v_bfe_u32 v28, v0, 1, 3
v_lshrrev_b32 v29, 4, v0
v_xor_b32 v28, v28, v29
v_lshlrev_b32 v28, 4, v28
v_and_b32 v29, 15, v0
v_lshl_or_b32 v28, v29, 7, v28
s_lshr_b32 s28, s33, 1
s_lshl_b32 s28, s28, 12
v_or_b32 v16, s28, v28
v_xor_b32 v17, 64, v16
v_bfe_u32 v28, v0, 2, 3
v_lshlrev_b32 v28, 5, v28
v_and_b32 v29, 3, v0
v_lshl_or_b32 v28, v29, 3, v28
v_bfe_u32 v29, v0, 2, 2
v_lshl_or_b32 v28, v29, 9, v28
v_lshrrev_b32 v29, 4, v0
v_lshl_or_b32 v28, v29, 12, v28
s_and_b32 s28, s33, 1
s_lshl_b32 s28, s28, 8
s_bitset1_b32 s28, 16
v_or_b32 v18, s28, v28
v_xor_b32 v19, 32, v18
v_xor_b32 v20, 64, v18
v_xor_b32 v21, 96, v18
v_xor_b32 v22, 128, v18
v_xor_b32 v23, 160, v18
v_xor_b32 v24, 192, v18
v_xor_b32 v25, 224, v18
s_lshl_b32 s28, s34, 20
s_lshl_b32 s29, s85, 24
s_add_u32 s28, s28, s29
s_bfe_u32 s29, s84, 0x80000
s_lshl_b32 s29, s29, 15
s_add_u32 s45, s28, s29
s_bfe_u32 s29, s84, 0x80008
s_lshl_b32 s29, s29, 15
s_add_u32 s46, s28, s29
s_bfe_u32 s29, s84, 0x80010
s_lshl_b32 s29, s29, 15
s_add_u32 s47, s28, s29
s_bfe_u32 s29, s84, 0x80018
s_lshl_b32 s29, s29, 15
s_add_u32 s48, s28, s29
s_lshl_b32 s50, s34, 11
s_add_u32 s50, s50, 0x5510000
s_lshl2_add_u32 s28, s85, s86
s_lshl_b32 s28, s28, 7
s_add_u32 s49, s28, s50
s_lshl3_add_u32 s28, s34, s35
s_lshl_b32 s52, s28, 7
s_add_u32 s52, s52, 0x5518000
s_lshl_b32 s51, s28, 16
)" R"(
s_add_u32 s51, s51, 0x2000000
s_cmp_lg_u32 s33, 0
s_cmov_b32 s49, 0x40000000
v_add_u32 v2, s36, v1
v_add_u32 v3, 0x1000, v2
v_min_u32 v7, 1, v0
v_lshlrev_b32 v7, 30, v7
s_mov_b32 s53, 0x40000
s_mov_b32 s54, 0x3db8aa3b
s_mul_i32 s41, s33, 7168
s_mul_i32 s42, s33, 0x70000
s_lshl_b32 s28, s34, 9
s_add_u32 s42, s42, s28
s_lshl_b32 s89, s34, 20
s_mov_b64 s[60:61], 0xff
s_mov_b32 s62, 0x4e800000
s_mov_b32 s63, 0
s_lshl_b32 s67, s34, 18
s_add_u32 s67, s67, 0x5000000
s_lshl3_add_u32 s28, s34, s35
s_lshl_b32 s64, s28, 16
s_add_u32 s64, s64, 0x2800000
s_lshl2_add_u32 s28, s28, s33
s_lshl_b32 s65, s28, 7
s_add_u32 s65, s65, 0x5520000
s_lshl_b32 s66, s28, 10
s_add_u32 s66, s66, 0x5400000
s_mov_b32 s28, s35
s_lshl2_add_u32 s28, s28, s33
s_lshl_b32 s29, s34, 6
s_add_u32 s29, s29, s28
s_mul_i32 s73, s29, 28672
s_add_u32 s73, s73, 0x3000000
s_lshl4_add_u32 s28, s28, s34
s_lshl_b32 s68, s28, 7
s_add_u32 s68, s68, 0x5530000
s_add_u32 s55, s89, 0x90000
s_lshl_b32 s28, s35, 1
s_sub_u32 s57, 14, s28
s_mov_b32 s59, s89
s_mov_b32 s58, 2
s_cmp_eq_u32 s35, 7
s_cmov_b32 s55, s89
s_cmp_eq_u32 s35, 7
s_cmov_b32 s57, 16
s_cmp_eq_u32 s35, 7
s_cmov_b32 s59, s67
s_add_u32 s56, s55, 0x1000000
s_waitcnt vmcnt(17) lgkmcnt(0)
s_barrier
Rt 0,18,0
Rt 2,18,2048
Rt 4,19,0
Rt 6,19,2048
Rt 8,20,0
Rt 10,20,2048
Rt 12,21,0
Rt 14,21,2048
Rt 16,22,0
Rt 18,22,2048
Rt 20,23,0
Rt 22,23,2048
Rt 24,24,0
Rt 26,24,2048
Rt 28,25,0
)" R"(
Rt 30,25,2048
Rb 32,16,0
Rb 36,16,2048
Rb 40,16,8192
Rb 44,16,10240
Rb 48,16,16384
Rb 52,16,18432
Rb 56,16,24576
Rb 60,16,26624
s_mul_i32 s3, s3, 1664525
s_add_u32 s3, s3, 1013904223
v_mov_b32 v6, s3
s_cmp_eq_u32 s85, 1
s_waitcnt vmcnt(13) lgkmcnt(0)
s_barrier
s_cbranch_scc1 .LzAV
Mz 32,0,32
Rt 64,18,16384
Mz 64,0,36
s_mov_b32 s24, s80
s_mov_b32 s25, s81
Mz 96,0,40
Rt 66,18,18432
s_mov_b32 s26, 0x380000
Mz 128,0,44
s_add_u32 s39, s39, 0x40000
s_mov_b32 s27, 0x20000
Mz 160,0,48
Rt 68,19,16384
s_add_u32 m0, s37, 49152
Mz 192,0,52
buffer_load_dwordx4 v12, s[8:11], s39 offen offset:0 lds
Mz 224,0,56
Rt 70,19,18432
s_bfe_u32 s28, s2, 0x30005
v_mfma_f32_16x16x32_bf16 a[224:227], a[0:3], a[60:63], 0
s_lshl3_add_u32 s29, s34, s28
s_lshl2_add_u32 s29, s29, s33
v_mfma_f32_16x16x32_bf16 a[228:231], a[4:7], a[60:63], 0
Rt 72,20,16384
s_mul_i32 s83, s29, 7168
Mz 228,4,56
s_mul_i32 s29, s85, 448
s_add_u32 s83, s83, s29
Mz 196,4,52
Rt 74,20,18432
s_lshl1_add_u32 s29, s34, s85
Mz 164,4,48
s_lshl_b32 s82, s29, 17
s_add_u32 s82, s82, 0x5000000
Mz 132,4,44
Rt 76,21,16384
s_mul_i32 s29, s28, 14
Mz 100,4,40
buffer_load_dwordx4 v13, s[8:11], s39 offen offset:1024 lds
Mz 68,4,36
Rt 78,21,18432
s_add_u32 s29, s29, s33
Mz 36,4,32
s_lshl3_add_u32 s28, s33, s28
Mz 40,8,32
Rt 80,22,16384
Mz 72,8,36
s_cmp_eq_u32 s85, 0
s_cselect_b32 s87, s29, s28
)" R"(
Mz 104,8,40
Rt 82,22,18432
Mz 136,8,44
s_cmp_eq_u32 s85, 0
s_cselect_b32 s92, 4, 32
Mz 168,8,48
Rt 84,23,16384
v_lshrrev_b32 v4, 5, v0
Mz 200,8,52
buffer_load_dwordx4 v14, s[8:11], s39 offen offset:2048 lds
Mz 232,8,56
Rt 86,23,18432
v_mul_u32_u24 v4, 384, v4
v_mfma_f32_16x16x32_bf16 a[232:235], a[8:11], a[60:63], 0
v_add_u32 v28, v4, v1
v_and_b32 v4, 31, v0
v_mfma_f32_16x16x32_bf16 a[236:239], a[12:15], a[60:63], 0
Rt 88,24,16384
v_cmp_lt_u32_e32 vcc, 27, v4
Mz 236,12,56
v_mov_b32 v4, 0x40000000
v_lshrrev_b32 v29, 2, v0
Mz 204,12,52
Rt 90,24,18432
v_cndmask_b32_e32 v28, v28, v4, vcc
Mz 172,12,48
v_and_b32 v4, 48, v1
v_lshl_or_b32 v29, v29, 9, v4
Mz 140,12,44
Rt 92,25,16384
s_lshr_b32 s28, s33, 1
Mz 108,12,40
buffer_load_dwordx4 v15, s[8:11], s39 offen offset:3072 lds
Mz 76,12,36
Rt 94,25,18432
s_lshl_b32 s28, s28, 6
Mz 44,12,32
s_and_b32 s29, s33, 1
s_lshl3_add_u32 s28, s29, s28
Mz 48,16,32
Rb 96,17,0
s_add_u32 s28, s28, 0x20000
Mz 80,16,36
v_add_u32 v29, s28, v29
Mz 112,16,40
Rb 100,17,2048
Mz 144,16,44
s_add_u32 s39, s39, 0x40000
Mz 176,16,48
Rb 104,17,8192
s_add_u32 m0, s37, 0
Mz 208,16,52
buffer_load_dwordx4 v12, s[8:11], s39 offen offset:0 lds
Mz 240,16,56
Rb 108,17,10240
v_mfma_f32_16x16x32_bf16 a[240:243], a[16:19], a[60:63], 0
v_mfma_f32_16x16x32_bf16 a[244:247], a[20:23], a[60:63], 0
Rb 112,17,16384
Mz 244,20,56
Mz 212,20,52
Rb 116,17,18432
Mz 180,20,48
Mz 148,20,44
Rb 120,17,24576
)" R"(
Mz 116,20,40
buffer_load_dwordx4 v13, s[8:11], s39 offen offset:1024 lds
Mz 84,20,36
Rb 124,17,26624
Mz 52,20,32
Mz 56,24,32
Mz 88,24,36
Mz 120,24,40
Mz 152,24,44
Mz 184,24,48
Mz 216,24,52
buffer_load_dwordx4 v14, s[8:11], s39 offen offset:2048 lds
Mz 248,24,56
v_mfma_f32_16x16x32_bf16 a[248:251], a[24:27], a[60:63], 0
v_mfma_f32_16x16x32_bf16 a[252:255], a[28:31], a[60:63], 0
Mz 252,28,56
Mz 220,28,52
Mz 188,28,48
Mz 156,28,44
Mz 124,28,40
buffer_load_dwordx4 v15, s[8:11], s39 offen offset:3072 lds
Mz 92,28,36
s_add_u32 m0, s37, 0x10000
s_add_u32 s29, s83, 0
buffer_load_dwordx4 v28, s[24:27], s29 offen offset:0 lds
Mz 60,28,32
s_add_u32 s29, s83, 768
s_waitcnt vmcnt(10) lgkmcnt(0)
s_barrier
Mv 32,64,96
Rt 0,18,32768
buffer_load_dwordx4 v28, s[24:27], s29 offen offset:1024 lds
s_add_u32 s29, s83, 1536
Mv 64,64,100
buffer_load_dwordx4 v28, s[24:27], s29 offen offset:2048 lds
s_add_u32 s29, s83, 2304
buffer_load_dwordx4 v28, s[24:27], s29 offen offset:3072 lds
Mv 96,64,104
Rt 2,18,34816
Mv 128,64,108
s_add_u32 s38, s38, 0xffff0080
Mv 160,64,112
Rt 4,19,32768
s_add_u32 m0, s36, 0
Mv 192,64,116
buffer_load_dwordx4 v8, s[4:7], s38 offen offset:0 lds
Mv 224,64,120
Rt 6,19,34816
Ma 224,64,124
Ma 228,68,124
Rt 8,20,32768
Mv 228,68,120
Mv 196,68,116
Rt 10,20,34816
Mv 164,68,112
Mv 132,68,108
Rt 12,21,32768
Mv 100,68,104
buffer_load_dwordx4 v9, s[4:7], s38 offen offset:1024 lds
Mv 68,68,100
Rt 14,21,34816
Mv 36,68,96
Mv 40,72,96
Rt 16,22,32768
Mv 72,72,100
)" R"(
Mv 104,72,104
Rt 18,22,34816
Mv 136,72,108
Mv 168,72,112
Rt 20,23,32768
Mv 200,72,116
buffer_load_dwordx4 v10, s[4:7], s38 offen offset:2048 lds
Mv 232,72,120
Rt 22,23,34816
Ma 232,72,124
Ma 236,76,124
Rt 24,24,32768
Mv 236,76,120
Mv 204,76,116
Rt 26,24,34816
Mv 172,76,112
Mv 140,76,108
Rt 28,25,32768
Mv 108,76,104
buffer_load_dwordx4 v11, s[4:7], s38 offen offset:3072 lds
Mv 76,76,100
Rt 30,25,34816
Mv 44,76,96
Mv 48,80,96
Rb 32,16,32768
Mv 80,80,100
Mv 112,80,104
Rb 36,16,34816
Mv 144,80,108
s_add_u32 s38, s38, 0x10000
Mv 176,80,112
Rb 40,16,40960
s_add_u32 m0, s36, 4096
Mv 208,80,116
buffer_load_dwordx4 v8, s[4:7], s38 offen offset:0 lds
Mv 240,80,120
Rb 44,16,43008
Ma 240,80,124
Ma 244,84,124
Rb 48,16,49152
Mv 244,84,120
Mv 212,84,116
Rb 52,16,51200
Mv 180,84,112
Mv 148,84,108
Rb 56,16,57344
Mv 116,84,104
buffer_load_dwordx4 v9, s[4:7], s38 offen offset:1024 lds
Mv 84,84,100
Rb 60,16,59392
Mv 52,84,96
Mv 56,88,96
Mv 88,88,100
Mv 120,88,104
Mv 152,88,108
Mv 184,88,112
Mv 216,88,116
buffer_load_dwordx4 v10, s[4:7], s38 offen offset:2048 lds
Mv 248,88,120
Ma 248,88,124
Ma 252,92,124
Mv 252,92,120
Mv 220,92,116
Mv 188,92,112
)" R"(
Mv 156,92,108
Mv 124,92,104
buffer_load_dwordx4 v11, s[4:7], s38 offen offset:3072 lds
Mv 92,92,100
Mv 60,92,96
s_waitcnt vmcnt(8) lgkmcnt(0)
s_barrier
Mv 32,0,32
Rt 64,18,49152
Mv 64,0,36
Mv 96,0,40
Rt 66,18,51200
Mv 128,0,44
s_add_u32 s39, s39, 0x40000
Mv 160,0,48
Rt 68,19,49152
s_add_u32 m0, s37, 16384
Mv 192,0,52
buffer_load_dwordx4 v12, s[8:11], s39 offen offset:0 lds
Mv 224,0,56
Rt 70,19,51200
Ma 224,0,60
Ma 228,4,60
Rt 72,20,49152
Mv 228,4,56
Mv 196,4,52
Rt 74,20,51200
Mv 164,4,48
Mv 132,4,44
Rt 76,21,49152
Mv 100,4,40
buffer_load_dwordx4 v13, s[8:11], s39 offen offset:1024 lds
Mv 68,4,36
Rt 78,21,51200
Mv 36,4,32
Mv 40,8,32
Rt 80,22,49152
Mv 72,8,36
Mv 104,8,40
Rt 82,22,51200
Mv 136,8,44
Mv 168,8,48
Rt 84,23,49152
Mv 200,8,52
buffer_load_dwordx4 v14, s[8:11], s39 offen offset:2048 lds
Mv 232,8,56
Rt 86,23,51200
Ma 232,8,60
Ma 236,12,60
Rt 88,24,49152
Mv 236,12,56
Mv 204,12,52
Rt 90,24,51200
Mv 172,12,48
Mv 140,12,44
Rt 92,25,49152
Mv 108,12,40
buffer_load_dwordx4 v15, s[8:11], s39 offen offset:3072 lds
Mv 76,12,36
Rt 94,25,51200
Mv 44,12,32
Mv 48,16,32
Rb 96,17,32768
Mv 80,16,36
Mv 112,16,40
)" R"(
Rb 100,17,34816
Mv 144,16,44
s_add_u32 s39, s39, 0x40000
Mv 176,16,48
Rb 104,17,40960
s_add_u32 m0, s37, 32768
Mv 208,16,52
buffer_load_dwordx4 v12, s[8:11], s39 offen offset:0 lds
Mv 240,16,56
Rb 108,17,43008
Ma 240,16,60
Ma 244,20,60
Rb 112,17,49152
Mv 244,20,56
Mv 212,20,52
Rb 116,17,51200
Mv 180,20,48
Mv 148,20,44
Rb 120,17,57344
Mv 116,20,40
buffer_load_dwordx4 v13, s[8:11], s39 offen offset:1024 lds
Mv 84,20,36
Rb 124,17,59392
Mv 52,20,32
Mv 56,24,32
Rt 176,29,0
Mv 88,24,36
Rt 178,29,8192
Mv 120,24,40
Rt 180,29,128
Mv 152,24,44
Rt 182,29,8320
Mv 184,24,48
Rt 184,29,256
Mv 216,24,52
buffer_load_dwordx4 v14, s[8:11], s39 offen offset:2048 lds
Rt 186,29,8448
Mv 248,24,56
Rt 188,29,384
Ma 248,24,60
Rt 190,29,8576
Ma 252,28,60
Mv 252,28,56
Mv 220,28,52
Mv 188,28,48
Mv 156,28,44
Mv 124,28,40
buffer_load_dwordx4 v15, s[8:11], s39 offen offset:3072 lds
Mv 92,28,36
Mv 60,28,32
s_waitcnt vmcnt(8) lgkmcnt(0)
s_barrier
Mv 32,64,96
Rt 0,18,0
s_mul_i32 s29, s87, 147
Mv 64,64,100
s_lshr_b32 s29, s29, 10
s_add_u32 s29, s29, s87
Mv 96,64,104
Rt 2,18,2048
s_lshl_b32 s29, s29, 10
Mv 128,64,108
s_add_u32 s38, s38, 0xffff0080
s_add_u32 s29, s29, s82
Mv 160,64,112
)" R"(
Rt 4,19,0
s_add_u32 m0, s36, 32768
Mv 192,64,116
buffer_load_dwordx4 v8, s[4:7], s38 offen offset:0 lds
Mv 224,64,120
Rt 6,19,2048
s_add_u32 s87, s87, s92
Ma 224,64,124
Ma 228,68,124
Rt 8,20,0
Mv 228,68,120
buffer_store_dwordx4 a[176:179], v1, s[20:23], s29 offen sc0 sc1
Mv 196,68,116
Rt 10,20,2048
Mv 164,68,112
Mv 132,68,108
Rt 12,21,0
Mv 100,68,104
buffer_load_dwordx4 v9, s[4:7], s38 offen offset:1024 lds
Mv 68,68,100
Rt 14,21,2048
Mv 36,68,96
Mv 40,72,96
Rt 16,22,0
s_mul_i32 s29, s87, 147
Mv 72,72,100
s_lshr_b32 s29, s29, 10
s_add_u32 s29, s29, s87
Mv 104,72,104
Rt 18,22,2048
s_lshl_b32 s29, s29, 10
Mv 136,72,108
s_add_u32 s29, s29, s82
s_add_u32 s87, s87, s92
Mv 168,72,112
Rt 20,23,0
Mv 200,72,116
buffer_load_dwordx4 v10, s[4:7], s38 offen offset:2048 lds
Mv 232,72,120
Rt 22,23,2048
Ma 232,72,124
Ma 236,76,124
Rt 24,24,0
Mv 236,76,120
buffer_store_dwordx4 a[180:183], v1, s[20:23], s29 offen sc0 sc1
Mv 204,76,116
Rt 26,24,2048
Mv 172,76,112
Mv 140,76,108
Rt 28,25,0
Mv 108,76,104
buffer_load_dwordx4 v11, s[4:7], s38 offen offset:3072 lds
Mv 76,76,100
Rt 30,25,2048
Mv 44,76,96
Mv 48,80,96
Rb 32,16,0
s_mul_i32 s29, s87, 147
Mv 80,80,100
s_lshr_b32 s29, s29, 10
s_add_u32 s29, s29, s87
Mv 112,80,104
Rb 36,16,2048
s_lshl_b32 s29, s29, 10
)" R"(
Mv 144,80,108
s_add_u32 s38, s38, 0x10000
s_add_u32 s29, s29, s82
Mv 176,80,112
Rb 40,16,8192
s_add_u32 m0, s36, 36864
Mv 208,80,116
buffer_load_dwordx4 v8, s[4:7], s38 offen offset:0 lds
Mv 240,80,120
Rb 44,16,10240
s_add_u32 s87, s87, s92
Ma 240,80,124
Ma 244,84,124
Rb 48,16,16384
Mv 244,84,120
buffer_store_dwordx4 a[184:187], v1, s[20:23], s29 offen sc0 sc1
Mv 212,84,116
Rb 52,16,18432
Mv 180,84,112
Mv 148,84,108
Rb 56,16,24576
Mv 116,84,104
buffer_load_dwordx4 v9, s[4:7], s38 offen offset:1024 lds
Mv 84,84,100
Rb 60,16,26624
Mv 52,84,96
Mv 56,88,96
s_mul_i32 s29, s87, 147
s_lshr_b32 s29, s29, 10
Mv 88,88,100
s_add_u32 s29, s29, s87
s_lshl_b32 s29, s29, 10
Mv 120,88,104
s_add_u32 s29, s29, s82
Mv 152,88,108
s_cmp_gt_u32 s33, 1
s_cmov_b32 s29, 0x40000000
Mv 184,88,112
Mv 216,88,116
buffer_load_dwordx4 v10, s[4:7], s38 offen offset:2048 lds
Mv 248,88,120
Ma 248,88,124
Ma 252,92,124
Mv 252,92,120
buffer_store_dwordx4 a[188:191], v1, s[20:23], s29 offen sc0 sc1
s_mov_b32 s40, 5
Mv 220,92,116
Mv 188,92,112
Mv 156,92,108
Mv 124,92,104
buffer_load_dwordx4 v11, s[4:7], s38 offen offset:3072 lds
Mv 92,92,100
Mv 60,92,96
s_waitcnt vmcnt(16) lgkmcnt(0)
s_barrier
s_mov_b32 s28, s35
s_lshl2_add_u32 s28, s28, s33
s_mul_i32 s43, s28, 28672
v_and_b32 v26, 0x70, v1
v_and_b32 v4, 48, v0
v_xor_b32 v26, v26, v4
v_lshrrev_b32 v4, 3, v0
v_mul_u32_u24 v4, 896, v4
v_add_u32 v26, v26, v4
v_xor_b32 v27, 64, v26
)" R"(
v_add_u32 v27, 6144, v27
v_bfe_u32 v28, v0, 1, 3
v_lshrrev_b32 v4, 4, v0
v_xor_b32 v28, v28, v4
v_lshlrev_b32 v28, 4, v28
v_and_b32 v4, 15, v0
v_lshl_or_b32 v28, v4, 7, v28
s_add_u32 s28, s36, 0x18000
v_add_u32 v28, s28, v28
v_xor_b32 v29, 64, v28
s_add_u32 m0, s36, 131072
s_add_u32 s44, s43, 0
buffer_load_dwordx4 v26, s[12:15], s44 offen offset:0 lds
buffer_load_dwordx4 v27, s[12:15], s44 offen offset:1024 lds
s_add_u32 s44, s43, 12288
buffer_load_dwordx4 v26, s[12:15], s44 offen offset:2048 lds
buffer_load_dwordx4 v27, s[12:15], s44 offen offset:3072 lds
s_add_u32 m0, s36, 135168
s_add_u32 s44, s43, 128
buffer_load_dwordx4 v26, s[12:15], s44 offen offset:0 lds
buffer_load_dwordx4 v27, s[12:15], s44 offen offset:1024 lds
s_add_u32 s44, s43, 12416
buffer_load_dwordx4 v26, s[12:15], s44 offen offset:2048 lds
buffer_load_dwordx4 v27, s[12:15], s44 offen offset:3072 lds
.LzPAK:
Mv 32,0,32
Rt 64,18,16384
Mv 64,0,36
Mv 96,0,40
Rt 66,18,18432
Mv 128,0,44
s_add_u32 s39, s39, 0x40000
Mv 160,0,48
Rt 68,19,16384
s_add_u32 m0, s37, 49152
Mv 192,0,52
buffer_load_dwordx4 v12, s[8:11], s39 offen offset:0 lds
Mv 224,0,56
Rt 70,19,18432
Ma 224,0,60
Ma 228,4,60
Rt 72,20,16384
Mv 228,4,56
Mv 196,4,52
Rt 74,20,18432
Mv 164,4,48
Mv 132,4,44
Rt 76,21,16384
Mv 100,4,40
buffer_load_dwordx4 v13, s[8:11], s39 offen offset:1024 lds
Mv 68,4,36
Rt 78,21,18432
Mv 36,4,32
Mv 40,8,32
Rt 80,22,16384
Mv 72,8,36
Mv 104,8,40
Rt 82,22,18432
Mv 136,8,44
Mv 168,8,48
Rt 84,23,16384
Mv 200,8,52
buffer_load_dwordx4 v14, s[8:11], s39 offen offset:2048 lds
Mv 232,8,56
)" R"(
Rt 86,23,18432
Ma 232,8,60
Ma 236,12,60
Rt 88,24,16384
Mv 236,12,56
Mv 204,12,52
Rt 90,24,18432
Mv 172,12,48
Mv 140,12,44
Rt 92,25,16384
Mv 108,12,40
buffer_load_dwordx4 v15, s[8:11], s39 offen offset:3072 lds
Mv 76,12,36
Rt 94,25,18432
Mv 44,12,32
Mv 48,16,32
Rb 96,17,0
Mv 80,16,36
Mv 112,16,40
Rb 100,17,2048
Mv 144,16,44
s_add_u32 s39, s39, 0x40000
Mv 176,16,48
Rb 104,17,8192
s_add_u32 m0, s37, 0
Mv 208,16,52
buffer_load_dwordx4 v12, s[8:11], s39 offen offset:0 lds
Mv 240,16,56
Rb 108,17,10240
Ma 240,16,60
Ma 244,20,60
Rb 112,17,16384
Mv 244,20,56
Mv 212,20,52
Rb 116,17,18432
Mv 180,20,48
Mv 148,20,44
Rb 120,17,24576
Mv 116,20,40
buffer_load_dwordx4 v13, s[8:11], s39 offen offset:1024 lds
Mv 84,20,36
Rb 124,17,26624
Mv 52,20,32
Mv 56,24,32
Mv 88,24,36
Mv 120,24,40
Mv 152,24,44
Mv 184,24,48
Mv 216,24,52
buffer_load_dwordx4 v14, s[8:11], s39 offen offset:2048 lds
Mv 248,24,56
Ma 248,24,60
Ma 252,28,60
Mv 252,28,56
Mv 220,28,52
Mv 188,28,48
Mv 156,28,44
Mv 124,28,40
buffer_load_dwordx4 v15, s[8:11], s39 offen offset:3072 lds
Mv 92,28,36
Mv 60,28,32
s_waitcnt vmcnt(8) lgkmcnt(0)
s_barrier
Mv 32,64,96
Rt 0,18,32768
)" R"(
Mv 64,64,100
Mv 96,64,104
Rt 2,18,34816
Mv 128,64,108
s_add_u32 s38, s38, 0xffff0080
Mv 160,64,112
Rt 4,19,32768
s_add_u32 m0, s36, 0
Mv 192,64,116
buffer_load_dwordx4 v8, s[4:7], s38 offen offset:0 lds
Mv 224,64,120
Rt 6,19,34816
Ma 224,64,124
Ma 228,68,124
Rt 8,20,32768
Mv 228,68,120
Mv 196,68,116
Rt 10,20,34816
Mv 164,68,112
Mv 132,68,108
Rt 12,21,32768
Mv 100,68,104
buffer_load_dwordx4 v9, s[4:7], s38 offen offset:1024 lds
Mv 68,68,100
Rt 14,21,34816
Mv 36,68,96
Mv 40,72,96
Rt 16,22,32768
Mv 72,72,100
Mv 104,72,104
Rt 18,22,34816
Mv 136,72,108
Mv 168,72,112
Rt 20,23,32768
Mv 200,72,116
buffer_load_dwordx4 v10, s[4:7], s38 offen offset:2048 lds
Mv 232,72,120
Rt 22,23,34816
Ma 232,72,124
Ma 236,76,124
Rt 24,24,32768
Mv 236,76,120
Mv 204,76,116
Rt 26,24,34816
Mv 172,76,112
Mv 140,76,108
Rt 28,25,32768
Mv 108,76,104
buffer_load_dwordx4 v11, s[4:7], s38 offen offset:3072 lds
Mv 76,76,100
Rt 30,25,34816
Mv 44,76,96
Mv 48,80,96
Rb 32,16,32768
Mv 80,80,100
Mv 112,80,104
Rb 36,16,34816
Mv 144,80,108
s_add_u32 s38, s38, 0x10000
Mv 176,80,112
Rb 40,16,40960
s_add_u32 m0, s36, 4096
Mv 208,80,116
buffer_load_dwordx4 v8, s[4:7], s38 offen offset:0 lds
)" R"(
Mv 240,80,120
Rb 44,16,43008
Ma 240,80,124
Ma 244,84,124
Rb 48,16,49152
Mv 244,84,120
Mv 212,84,116
Rb 52,16,51200
Mv 180,84,112
Mv 148,84,108
Rb 56,16,57344
Mv 116,84,104
buffer_load_dwordx4 v9, s[4:7], s38 offen offset:1024 lds
Mv 84,84,100
Rb 60,16,59392
Mv 52,84,96
Mv 56,88,96
Mv 88,88,100
Mv 120,88,104
Mv 152,88,108
Mv 184,88,112
Mv 216,88,116
buffer_load_dwordx4 v10, s[4:7], s38 offen offset:2048 lds
Mv 248,88,120
Ma 248,88,124
Ma 252,92,124
Mv 252,92,120
Mv 220,92,116
Mv 188,92,112
Mv 156,92,108
Mv 124,92,104
buffer_load_dwordx4 v11, s[4:7], s38 offen offset:3072 lds
Mv 92,92,100
Mv 60,92,96
s_waitcnt vmcnt(12) lgkmcnt(0)
s_barrier
Mv 32,0,32
Rt 64,18,49152
Mv 64,0,36
Mv 96,0,40
Rt 66,18,51200
Mv 128,0,44
s_add_u32 s39, s39, 0x40000
Mv 160,0,48
Rt 68,19,49152
s_add_u32 m0, s37, 16384
Mv 192,0,52
buffer_load_dwordx4 v12, s[8:11], s39 offen offset:0 lds
Mv 224,0,56
Rt 70,19,51200
Ma 224,0,60
Ma 228,4,60
Rt 72,20,49152
Mv 228,4,56
Mv 196,4,52
Rt 74,20,51200
Mv 164,4,48
Mv 132,4,44
Rt 76,21,49152
Mv 100,4,40
buffer_load_dwordx4 v13, s[8:11], s39 offen offset:1024 lds
Mv 68,4,36
Rt 78,21,51200
Mv 36,4,32
Mv 40,8,32
)" R"(
Rt 80,22,49152
Mv 72,8,36
Mv 104,8,40
Rt 82,22,51200
Mv 136,8,44
Mv 168,8,48
Rt 84,23,49152
Mv 200,8,52
buffer_load_dwordx4 v14, s[8:11], s39 offen offset:2048 lds
Mv 232,8,56
Rt 86,23,51200
Ma 232,8,60
Ma 236,12,60
Rt 88,24,49152
Mv 236,12,56
Mv 204,12,52
Rt 90,24,51200
Mv 172,12,48
Mv 140,12,44
Rt 92,25,49152
Mv 108,12,40
buffer_load_dwordx4 v15, s[8:11], s39 offen offset:3072 lds
Mv 76,12,36
Rt 94,25,51200
Mv 44,12,32
Mv 48,16,32
Rb 96,17,32768
Mv 80,16,36
Mv 112,16,40
Rb 100,17,34816
Mv 144,16,44
s_add_u32 s39, s39, 0x40000
Mv 176,16,48
Rb 104,17,40960
s_add_u32 m0, s37, 32768
Mv 208,16,52
buffer_load_dwordx4 v12, s[8:11], s39 offen offset:0 lds
Mv 240,16,56
Rb 108,17,43008
Ma 240,16,60
Ma 244,20,60
Rb 112,17,49152
Mv 244,20,56
Mv 212,20,52
Rb 116,17,51200
Mv 180,20,48
Mv 148,20,44
Rb 120,17,57344
Mv 116,20,40
buffer_load_dwordx4 v13, s[8:11], s39 offen offset:1024 lds
Mv 84,20,36
Rb 124,17,59392
Mv 52,20,32
Mv 56,24,32
Mv 88,24,36
Mv 120,24,40
Mv 152,24,44
Mv 184,24,48
Mv 216,24,52
buffer_load_dwordx4 v14, s[8:11], s39 offen offset:2048 lds
Mv 248,24,56
Ma 248,24,60
Ma 252,28,60
Mv 252,28,56
)" R"(
Mv 220,28,52
Mv 188,28,48
Mv 156,28,44
Mv 124,28,40
buffer_load_dwordx4 v15, s[8:11], s39 offen offset:3072 lds
Mv 92,28,36
Mv 60,28,32
s_waitcnt vmcnt(8) lgkmcnt(0)
s_barrier
Mv 32,64,96
Rt 0,18,0
Mv 64,64,100
Mv 96,64,104
Rt 2,18,2048
Mv 128,64,108
s_add_u32 s38, s38, 0xffff0080
Mv 160,64,112
Rt 4,19,0
s_add_u32 m0, s36, 32768
Mv 192,64,116
buffer_load_dwordx4 v8, s[4:7], s38 offen offset:0 lds
Mv 224,64,120
Rt 6,19,2048
Ma 224,64,124
Ma 228,68,124
Rt 8,20,0
Mv 228,68,120
Mv 196,68,116
Rt 10,20,2048
Mv 164,68,112
Mv 132,68,108
Rt 12,21,0
Mv 100,68,104
buffer_load_dwordx4 v9, s[4:7], s38 offen offset:1024 lds
Mv 68,68,100
Rt 14,21,2048
Mv 36,68,96
Mv 40,72,96
Rt 16,22,0
Mv 72,72,100
Mv 104,72,104
Rt 18,22,2048
Mv 136,72,108
Mv 168,72,112
Rt 20,23,0
Mv 200,72,116
buffer_load_dwordx4 v10, s[4:7], s38 offen offset:2048 lds
Mv 232,72,120
Rt 22,23,2048
Ma 232,72,124
Ma 236,76,124
Rt 24,24,0
Mv 236,76,120
Mv 204,76,116
Rt 26,24,2048
Mv 172,76,112
Mv 140,76,108
Rt 28,25,0
Mv 108,76,104
buffer_load_dwordx4 v11, s[4:7], s38 offen offset:3072 lds
Mv 76,76,100
Rt 30,25,2048
Mv 44,76,96
Mv 48,80,96
Rb 32,16,0
)" R"(
Mv 80,80,100
Mv 112,80,104
Rb 36,16,2048
Mv 144,80,108
s_add_u32 s38, s38, 0x10000
Mv 176,80,112
Rb 40,16,8192
s_add_u32 m0, s36, 36864
Mv 208,80,116
buffer_load_dwordx4 v8, s[4:7], s38 offen offset:0 lds
Mv 240,80,120
Rb 44,16,10240
Ma 240,80,124
Ma 244,84,124
Rb 48,16,16384
Mv 244,84,120
Mv 212,84,116
Rb 52,16,18432
Mv 180,84,112
Mv 148,84,108
Rb 56,16,24576
Mv 116,84,104
buffer_load_dwordx4 v9, s[4:7], s38 offen offset:1024 lds
Mv 84,84,100
Rb 60,16,26624
Mv 52,84,96
Mv 56,88,96
Mv 88,88,100
Mv 120,88,104
Mv 152,88,108
Mv 184,88,112
Mv 216,88,116
buffer_load_dwordx4 v10, s[4:7], s38 offen offset:2048 lds
Mv 248,88,120
Ma 248,88,124
Ma 252,92,124
Mv 252,92,120
Mv 220,92,116
s_sub_u32 s40, s40, 1
Mv 188,92,112
Mv 156,92,108
Mv 124,92,104
buffer_load_dwordx4 v11, s[4:7], s38 offen offset:3072 lds
Mv 92,92,100
Mv 60,92,96
s_cmp_lg_u32 s40, 0
s_waitcnt vmcnt(12) lgkmcnt(0)
s_barrier
s_cbranch_scc1 .LzPAK
Mv 32,0,32
Rt 64,18,16384
Mv 64,0,36
Rb 192,28,32768
Mv 96,0,40
Rt 66,18,18432
Mv 128,0,44
s_add_u32 s39, s39, 0x40000
Rb 196,28,34816
Mv 160,0,48
Rt 68,19,16384
s_add_u32 m0, s37, 49152
Mv 192,0,52
buffer_load_dwordx4 v12, s[8:11], s39 offen offset:0 lds
Mv 224,0,56
Rt 70,19,18432
)" R"(
Ma 224,0,60
Rb 200,29,32768
Ma 228,4,60
Rt 72,20,16384
Mv 228,4,56
Rb 204,29,34816
Mv 196,4,52
Rt 74,20,18432
Mv 164,4,48
Rb 208,28,36864
Mv 132,4,44
Rt 76,21,16384
Mv 100,4,40
buffer_load_dwordx4 v13, s[8:11], s39 offen offset:1024 lds
Mv 68,4,36
Rt 78,21,18432
Mv 36,4,32
Rb 212,28,38912
Mv 40,8,32
Rt 80,22,16384
Mv 72,8,36
Rb 216,29,36864
Mv 104,8,40
Rt 82,22,18432
Mv 136,8,44
Rb 220,29,38912
Mv 168,8,48
Rt 84,23,16384
Mv 200,8,52
buffer_load_dwordx4 v14, s[8:11], s39 offen offset:2048 lds
Mv 232,8,56
Rt 86,23,18432
Ma 232,8,60
Ma 236,12,60
Rt 88,24,16384
Mv 236,12,56
Mv 204,12,52
Rt 90,24,18432
Mv 172,12,48
Mv 140,12,44
Rt 92,25,16384
Mv 108,12,40
buffer_load_dwordx4 v15, s[8:11], s39 offen offset:3072 lds
Mv 76,12,36
Rt 94,25,18432
v_lshrrev_b32 v5, 2, v0
Mv 44,12,32
v_and_b32 v5, 8, v5
v_and_or_b32 v5, v0, 7, v5
Mv 48,16,32
Rb 96,17,0
v_lshlrev_b32 v5, 8, v5
Mv 80,16,36
v_and_or_b32 v5, v0, 24, v5
v_lshrrev_b32 v5, 1, v5
Mv 112,16,40
Rb 100,17,2048
Mv 144,16,44
s_add_u32 s39, s39, 0x40000
Mv 176,16,48
Rb 104,17,8192
s_add_u32 m0, s37, 0
Mv 208,16,52
buffer_load_dwordx4 v12, s[8:11], s39 offen offset:0 lds
)" R"(
Mv 240,16,56
Rb 108,17,10240
Ma 240,16,60
Ma 244,20,60
Rb 112,17,16384
Mv 244,20,56
Mv 212,20,52
Rb 116,17,18432
Mv 180,20,48
Mv 148,20,44
Rb 120,17,24576
Mv 116,20,40
buffer_load_dwordx4 v13, s[8:11], s39 offen offset:1024 lds
Mv 84,20,36
Rb 124,17,26624
Mv 52,20,32
Mv 56,24,32
Mv 88,24,36
Mv 120,24,40
Mv 152,24,44
Mv 184,24,48
Mv 216,24,52
buffer_load_dwordx4 v14, s[8:11], s39 offen offset:2048 lds
Mv 248,24,56
Ma 248,24,60
Ma 252,28,60
Mv 252,28,56
Mv 220,28,52
Mv 188,28,48
Mv 156,28,44
Mv 124,28,40
buffer_load_dwordx4 v15, s[8:11], s39 offen offset:3072 lds
Mv 92,28,36
Mv 60,28,32
s_waitcnt vmcnt(8) lgkmcnt(0)
s_barrier
s_add_u32 m0, s36, 131072
s_add_u32 s44, s43, 256
buffer_load_dwordx4 v26, s[12:15], s44 offen offset:0 lds
buffer_load_dwordx4 v27, s[12:15], s44 offen offset:1024 lds
s_add_u32 s44, s43, 12544
buffer_load_dwordx4 v26, s[12:15], s44 offen offset:2048 lds
buffer_load_dwordx4 v27, s[12:15], s44 offen offset:3072 lds
Mv 32,64,96
Rt 0,18,32768
Mv 64,64,100
Mv 96,64,104
Rt 2,18,34816
Mv 128,64,108
s_add_u32 s38, s38, 0xffff0080
Mv 160,64,112
Rt 4,19,32768
s_add_u32 m0, s36, 0
Mv 192,64,116
buffer_load_dwordx4 v8, s[4:7], s38 offen offset:0 lds
Mv 224,64,120
Rt 6,19,34816
Ma 224,64,124
Ma 228,68,124
Rt 8,20,32768
Mv 228,68,120
Mv 196,68,116
Rt 10,20,34816
Mv 164,68,112
Mv 132,68,108
)" R"(
Rt 12,21,32768
Mv 100,68,104
buffer_load_dwordx4 v9, s[4:7], s38 offen offset:1024 lds
Mv 68,68,100
Rt 14,21,34816
Mv 36,68,96
Mv 40,72,96
Rt 16,22,32768
Mv 72,72,100
Mv 104,72,104
Rt 18,22,34816
Mv 136,72,108
Mv 168,72,112
Rt 20,23,32768
Mv 200,72,116
buffer_load_dwordx4 v10, s[4:7], s38 offen offset:2048 lds
Mv 232,72,120
Rt 22,23,34816
Ma 232,72,124
Ma 236,76,124
Rt 24,24,32768
Mv 236,76,120
Mv 204,76,116
Rt 26,24,34816
Mv 172,76,112
Mv 140,76,108
Rt 28,25,32768
Mv 108,76,104
buffer_load_dwordx4 v11, s[4:7], s38 offen offset:3072 lds
Mv 76,76,100
Rt 30,25,34816
Mv 44,76,96
s_add_u32 m0, s36, 135168
s_add_u32 s44, s43, 384
buffer_load_dwordx4 v26, s[12:15], s44 offen offset:0 lds
buffer_load_dwordx4 v27, s[12:15], s44 offen offset:1024 lds
s_add_u32 s44, s43, 12672
buffer_load_dwordx4 v26, s[12:15], s44 offen offset:2048 lds
buffer_load_dwordx4 v27, s[12:15], s44 offen offset:3072 lds
Mv 48,80,96
Rb 32,16,32768
Mv 80,80,100
Mv 112,80,104
Rb 36,16,34816
Mv 144,80,108
s_add_u32 s38, s38, 0x10000
Mv 176,80,112
Rb 40,16,40960
s_add_u32 m0, s36, 4096
Mv 208,80,116
buffer_load_dwordx4 v8, s[4:7], s38 offen offset:0 lds
Mv 240,80,120
Rb 44,16,43008
Ma 240,80,124
Ma 244,84,124
Rb 48,16,49152
Mv 244,84,120
Mv 212,84,116
Rb 52,16,51200
Mv 180,84,112
Mv 148,84,108
Rb 56,16,57344
Mv 116,84,104
buffer_load_dwordx4 v9, s[4:7], s38 offen offset:1024 lds
)" R"(
Mv 84,84,100
Rb 60,16,59392
Mv 52,84,96
Mv 56,88,96
Mv 88,88,100
Mv 120,88,104
Mv 152,88,108
Mv 184,88,112
Mv 216,88,116
buffer_load_dwordx4 v10, s[4:7], s38 offen offset:2048 lds
Mv 248,88,120
Ma 248,88,124
Ma 252,92,124
Mv 252,92,120
Mv 220,92,116
Mv 188,92,112
Mv 156,92,108
Mv 124,92,104
buffer_load_dwordx4 v11, s[4:7], s38 offen offset:3072 lds
Mv 92,92,100
Mv 60,92,96
s_waitcnt vmcnt(4) lgkmcnt(0)
s_barrier
Mv 32,0,32
Rt 64,18,49152
Mv 64,0,36
Rb 128,28,32768
Mv 96,0,40
Rt 66,18,51200
Mv 128,0,44
s_add_u32 s39, s39, 0x40000
Rb 132,28,34816
Mv 160,0,48
Rt 68,19,49152
s_add_u32 m0, s37, 16384
Mv 192,0,52
buffer_load_dwordx4 v12, s[8:11], s39 offen offset:0 lds
Mv 224,0,56
Rt 70,19,51200
Ma 224,0,60
Rb 136,29,32768
Ma 228,4,60
Rt 72,20,49152
Mv 228,4,56
Rb 140,29,34816
Mv 196,4,52
Rt 74,20,51200
Mv 164,4,48
Rb 144,28,36864
Mv 132,4,44
Rt 76,21,49152
Mv 100,4,40
buffer_load_dwordx4 v13, s[8:11], s39 offen offset:1024 lds
Mv 68,4,36
Rt 78,21,51200
Mv 36,4,32
Rb 148,28,38912
Mv 40,8,32
Rt 80,22,49152
Mv 72,8,36
Rb 152,29,36864
Mv 104,8,40
Rt 82,22,51200
Mv 136,8,44
Rb 156,29,38912
)" R"(
Mv 168,8,48
Rt 84,23,49152
Mv 200,8,52
buffer_load_dwordx4 v14, s[8:11], s39 offen offset:2048 lds
Mv 232,8,56
Rt 86,23,51200
Ma 232,8,60
Ma 236,12,60
Rt 88,24,49152
Mv 236,12,56
Mv 204,12,52
Rt 90,24,51200
Mv 172,12,48
Mv 140,12,44
Rt 92,25,49152
Mv 108,12,40
buffer_load_dwordx4 v15, s[8:11], s39 offen offset:3072 lds
Mv 76,12,36
Rt 94,25,51200
Mv 44,12,32
Mv 48,16,32
Rb 96,17,32768
Mv 80,16,36
Mv 112,16,40
Rb 100,17,34816
Mv 144,16,44
s_add_u32 s39, s39, 0x40000
Mv 176,16,48
Rb 104,17,40960
s_add_u32 m0, s37, 32768
Mv 208,16,52
buffer_load_dwordx4 v12, s[8:11], s39 offen offset:0 lds
Mv 240,16,56
Rb 108,17,43008
Ma 240,16,60
Ma 244,20,60
Rb 112,17,49152
Mv 244,20,56
Mv 212,20,52
Rb 116,17,51200
Mv 180,20,48
Mv 148,20,44
Rb 120,17,57344
Mv 116,20,40
buffer_load_dwordx4 v13, s[8:11], s39 offen offset:1024 lds
Mv 84,20,36
Rb 124,17,59392
Mv 52,20,32
Mv 56,24,32
Mv 88,24,36
Mv 120,24,40
Mv 152,24,44
Mv 184,24,48
Mv 216,24,52
buffer_load_dwordx4 v14, s[8:11], s39 offen offset:2048 lds
Mv 248,24,56
Ma 248,24,60
Ma 252,28,60
Mv 252,28,56
Mv 220,28,52
Mv 188,28,48
Mv 156,28,44
Mv 124,28,40
buffer_load_dwordx4 v15, s[8:11], s39 offen offset:3072 lds
)" R"(
Mv 92,28,36
Mv 60,28,32
s_waitcnt vmcnt(8) lgkmcnt(0)
s_barrier
s_add_u32 m0, s36, 131072
s_add_u32 s44, s43, 512
buffer_load_dwordx4 v26, s[12:15], s44 offen offset:0 lds
buffer_load_dwordx4 v27, s[12:15], s44 offen offset:1024 lds
s_add_u32 s44, s43, 12800
buffer_load_dwordx4 v26, s[12:15], s44 offen offset:2048 lds
buffer_load_dwordx4 v27, s[12:15], s44 offen offset:3072 lds
Mv 32,64,96
Rt 0,18,0
Mv 64,64,100
Mv 96,64,104
Rt 2,18,2048
Mv 128,64,108
s_add_u32 s38, s38, 0xffff0080
Mv 160,64,112
Rt 4,19,0
s_add_u32 m0, s36, 32768
Mv 192,64,116
buffer_load_dwordx4 v8, s[4:7], s38 offen offset:0 lds
Mv 224,64,120
Rt 6,19,2048
Ma 224,64,124
Ma 228,68,124
Rt 8,20,0
Mv 228,68,120
Mv 196,68,116
Rt 10,20,2048
Mv 164,68,112
Mv 132,68,108
Rt 12,21,0
Mv 100,68,104
buffer_load_dwordx4 v9, s[4:7], s38 offen offset:1024 lds
Mv 68,68,100
Rt 14,21,2048
Mv 36,68,96
Mv 40,72,96
Rt 16,22,0
Mv 72,72,100
Mv 104,72,104
Rt 18,22,2048
Mv 136,72,108
Mv 168,72,112
Rt 20,23,0
Mv 200,72,116
buffer_load_dwordx4 v10, s[4:7], s38 offen offset:2048 lds
Mv 232,72,120
Rt 22,23,2048
Ma 232,72,124
Ma 236,76,124
Rt 24,24,0
Mv 236,76,120
Mv 204,76,116
Rt 26,24,2048
Mv 172,76,112
Mv 140,76,108
Rt 28,25,0
Mv 108,76,104
buffer_load_dwordx4 v11, s[4:7], s38 offen offset:3072 lds
Mv 76,76,100
Rt 30,25,2048
Mv 44,76,96
)" R"(
s_add_u32 m0, s36, 135168
s_add_u32 s44, s43, 640
buffer_load_dwordx4 v26, s[12:15], s44 offen offset:0 lds
buffer_load_dwordx4 v27, s[12:15], s44 offen offset:1024 lds
s_add_u32 s44, s43, 12928
buffer_load_dwordx4 v26, s[12:15], s44 offen offset:2048 lds
buffer_load_dwordx4 v27, s[12:15], s44 offen offset:3072 lds
Mv 48,80,96
Rb 32,16,0
Mv 80,80,100
Mv 112,80,104
Rb 36,16,2048
Mv 144,80,108
s_add_u32 s38, s38, 0x10000
Mv 176,80,112
Rb 40,16,8192
s_add_u32 m0, s36, 36864
Mv 208,80,116
buffer_load_dwordx4 v8, s[4:7], s38 offen offset:0 lds
Mv 240,80,120
Rb 44,16,10240
Ma 240,80,124
Ma 244,84,124
Rb 48,16,16384
Mv 244,84,120
Mv 212,84,116
Rb 52,16,18432
Mv 180,84,112
Mv 148,84,108
Rb 56,16,24576
Mv 116,84,104
buffer_load_dwordx4 v9, s[4:7], s38 offen offset:1024 lds
Mv 84,84,100
Rb 60,16,26624
Mv 52,84,96
Mv 56,88,96
Mv 88,88,100
Mv 120,88,104
Mv 152,88,108
Mv 184,88,112
Mv 216,88,116
buffer_load_dwordx4 v10, s[4:7], s38 offen offset:2048 lds
Mv 248,88,120
Ma 248,88,124
Ma 252,92,124
Mv 252,92,120
Mv 220,92,116
Mv 188,92,112
Mv 156,92,108
Mv 124,92,104
buffer_load_dwordx4 v11, s[4:7], s38 offen offset:3072 lds
Mv 92,92,100
Mv 60,92,96
s_waitcnt vmcnt(4) lgkmcnt(0)
s_barrier
Mv 32,0,32
Rt 64,18,16384
Mv 64,0,36
Rb 160,28,32768
Mv 96,0,40
Rt 66,18,18432
Mv 128,0,44
s_add_u32 s39, s39, 0x40000
Rb 164,28,34816
Mv 160,0,48
)" R"(
Rt 68,19,16384
s_add_u32 m0, s37, 49152
Mv 192,0,52
buffer_load_dwordx4 v12, s[8:11], s39 offen offset:0 lds
Mv 224,0,56
Rt 70,19,18432
Ma 224,0,60
Rb 168,29,32768
Ma 228,4,60
Rt 72,20,16384
Mv 228,4,56
Rb 172,29,34816
Mv 196,4,52
Rt 74,20,18432
Mv 164,4,48
Rb 176,28,36864
Mv 132,4,44
Rt 76,21,16384
Mv 100,4,40
buffer_load_dwordx4 v13, s[8:11], s39 offen offset:1024 lds
Mv 68,4,36
Rt 78,21,18432
Mv 36,4,32
Rb 180,28,38912
Mv 40,8,32
Rt 80,22,16384
Mv 72,8,36
Rb 184,29,36864
Mv 104,8,40
Rt 82,22,18432
Mv 136,8,44
Rb 188,29,38912
Mv 168,8,48
Rt 84,23,16384
Mv 200,8,52
buffer_load_dwordx4 v14, s[8:11], s39 offen offset:2048 lds
Mv 232,8,56
Rt 86,23,18432
Ma 232,8,60
Ma 236,12,60
Rt 88,24,16384
Mv 236,12,56
Mv 204,12,52
Rt 90,24,18432
Mv 172,12,48
Mv 140,12,44
Rt 92,25,16384
Mv 108,12,40
buffer_load_dwordx4 v15, s[8:11], s39 offen offset:3072 lds
Mv 76,12,36
Rt 94,25,18432
v_lshrrev_b32 v10, 3, v0
Mv 44,12,32
v_and_b32 v11, 0x70, v1
v_lshl_or_b32 v10, v10, 13, v11
Mv 48,16,32
Rb 96,17,0
v_lshlrev_b32 v11, 1, v0
Mv 80,16,36
v_and_b32 v11, 32, v11
v_xor_b32 v10, v10, v11
Mv 112,16,40
Rb 100,17,2048
s_and_b32 s28, s33, 1
)" R"(
Mv 144,16,44
s_lshl_b32 s28, s28, 6
v_xor_b32 v8, s28, v10
Mv 176,16,48
Rb 104,17,8192
v_xor_b32 v9, 64, v8
Mv 208,16,52
v_add_u32 v9, 0xfc00, v9
v_add_u32 v10, 0x1f800, v8
Mv 240,16,56
Rb 108,17,10240
v_add_u32 v11, 0x1f800, v9
Ma 240,16,60
Ma 244,20,60
Rb 112,17,16384
Mv 244,20,56
Mv 212,20,52
Rb 116,17,18432
Mv 180,20,48
Mv 148,20,44
Rb 120,17,24576
Mv 116,20,40
Mv 84,20,36
Rb 124,17,26624
Mv 52,20,32
Mv 56,24,32
Mv 88,24,36
Mv 120,24,40
Mv 152,24,44
Mv 184,24,48
Mv 216,24,52
Mv 248,24,56
Ma 248,24,60
Ma 252,28,60
Mv 252,28,56
Mv 220,28,52
Mv 188,28,48
Mv 156,28,44
Mv 124,28,40
Mv 92,28,36
Mv 60,28,32
s_waitcnt vmcnt(4) lgkmcnt(0)
s_barrier
Mv 32,64,96
Rt 0,18,32768
Mv 64,64,100
s_add_u32 s92, s42, 0x0
Mv 96,64,104
Rt 2,18,34816
s_add_u32 m0, s41, 131072
Mv 128,64,108
buffer_load_dwordx4 v8, s[16:19], s92 offen offset:0 lds
Mv 160,64,112
Rt 4,19,32768
Mv 192,64,116
Mv 224,64,120
Rt 6,19,34816
Ma 224,64,124
buffer_load_dwordx4 v9, s[16:19], s92 offen offset:1024 lds
Ma 228,68,124
Rt 8,20,32768
Mv 228,68,120
Mv 196,68,116
Rt 10,20,34816
Mv 164,68,112
)" R"(
buffer_load_dwordx4 v10, s[16:19], s92 offen offset:2048 lds
Mv 132,68,108
Rt 12,21,32768
Mv 100,68,104
Mv 68,68,100
Rt 14,21,34816
Mv 36,68,96
buffer_load_dwordx4 v11, s[16:19], s92 offen offset:3072 lds
Mv 40,72,96
Rt 16,22,32768
Mv 72,72,100
s_add_u32 s92, s42, 0x40000
Mv 104,72,104
Rt 18,22,34816
s_add_u32 m0, s41, 135168
Mv 136,72,108
buffer_load_dwordx4 v8, s[16:19], s92 offen offset:0 lds
Mv 168,72,112
Rt 20,23,32768
Mv 200,72,116
Mv 232,72,120
Rt 22,23,34816
Ma 232,72,124
buffer_load_dwordx4 v9, s[16:19], s92 offen offset:1024 lds
Ma 236,76,124
Rt 24,24,32768
Mv 236,76,120
Mv 204,76,116
Rt 26,24,34816
Mv 172,76,112
buffer_load_dwordx4 v10, s[16:19], s92 offen offset:2048 lds
Mv 140,76,108
Rt 28,25,32768
Mv 108,76,104
s_add_u32 s92, s42, 0x1c0000
Mv 76,76,100
Rt 30,25,34816
s_add_u32 m0, s41, 0
Mv 44,76,96
buffer_load_dwordx4 v8, s[16:19], s92 offen offset:0 lds
Mv 48,80,96
Rb 32,16,32768
Mv 80,80,100
Mv 112,80,104
Rb 36,16,34816
Mv 144,80,108
buffer_load_dwordx4 v9, s[16:19], s92 offen offset:1024 lds
Mv 176,80,112
Rb 40,16,40960
Mv 208,80,116
Mv 240,80,120
Rb 44,16,43008
Ma 240,80,124
buffer_load_dwordx4 v10, s[16:19], s92 offen offset:2048 lds
Ma 244,84,124
Rb 48,16,49152
Mv 244,84,120
Mv 212,84,116
Rb 52,16,51200
Mv 180,84,112
buffer_load_dwordx4 v11, s[16:19], s92 offen offset:3072 lds
Mv 148,84,108
Rb 56,16,57344
Mv 116,84,104
)" R"(
s_add_u32 s92, s42, 0x200000
Mv 84,84,100
Rb 60,16,59392
s_add_u32 m0, s41, 4096
Mv 52,84,96
buffer_load_dwordx4 v8, s[16:19], s92 offen offset:0 lds
Mv 56,88,96
Mv 88,88,100
Mv 120,88,104
Mv 152,88,108
buffer_load_dwordx4 v9, s[16:19], s92 offen offset:1024 lds
Mv 184,88,112
Mv 216,88,116
Mv 248,88,120
Ma 248,88,124
buffer_load_dwordx4 v10, s[16:19], s92 offen offset:2048 lds
Ma 252,92,124
Mv 252,92,120
Mv 220,92,116
Mv 188,92,112
Mv 156,92,108
Mv 124,92,104
Mv 92,92,100
Mv 60,92,96
s_waitcnt vmcnt(14) lgkmcnt(4)
s_barrier
Mv 32,0,32
Rt 64,18,49152
Mv 64,0,36
Rt 66,18,51200
s_add_u32 s92, s42, 0x80
Mv 96,0,40
Rt 68,19,49152
s_add_u32 m0, s41, 65536
Mv 128,0,44
Rt 70,19,51200
buffer_load_dwordx4 v8, s[16:19], s92 offen offset:0 lds
Mv 132,4,44
Rt 72,20,49152
Mv 100,4,40
Rt 74,20,51200
Mv 68,4,36
Rt 76,21,49152
Mv 36,4,32
Rt 78,21,51200
buffer_load_dwordx4 v9, s[16:19], s92 offen offset:1024 lds
Mv 40,8,32
Rt 80,22,49152
Mv 72,8,36
Rt 82,22,51200
Mv 104,8,40
Rt 84,23,49152
Mv 136,8,44
Rt 86,23,51200
buffer_load_dwordx4 v10, s[16:19], s92 offen offset:2048 lds
Mv 140,12,44
Rt 88,24,49152
Mv 108,12,40
Rt 90,24,51200
Mv 76,12,36
Rt 92,25,49152
Mv 44,12,32
Rt 94,25,51200
buffer_load_dwordx4 v11, s[16:19], s92 offen offset:3072 lds
Mv 48,16,32
)" R"(
Rb 96,17,32768
Mv 80,16,36
Rb 100,17,34816
s_add_u32 s92, s42, 0x40080
Mv 112,16,40
Rb 104,17,40960
s_add_u32 m0, s41, 69632
Mv 144,16,44
Rb 108,17,43008
buffer_load_dwordx4 v8, s[16:19], s92 offen offset:0 lds
Mv 148,20,44
Rb 112,17,49152
Mv 116,20,40
Rb 116,17,51200
Mv 84,20,36
Rb 120,17,57344
Mv 52,20,32
Rb 124,17,59392
buffer_load_dwordx4 v9, s[16:19], s92 offen offset:1024 lds
Mv 56,24,32
Mv 88,24,36
Mv 120,24,40
Mv 152,24,44
buffer_load_dwordx4 v10, s[16:19], s92 offen offset:2048 lds
Mv 156,28,44
Mv 124,28,40
Mv 92,28,36
Mv 60,28,32
s_waitcnt lgkmcnt(0)
s_barrier
Mv 32,64,96
v_bfe_u32 v12, v0, 3, 2
v_lshlrev_b32 v12, 5, v12
Mv 36,68,96
v_and_b32 v4, 3, v0
v_lshl_or_b32 v12, v4, 3, v12
Mv 40,72,96
s_add_u32 m0, s36, 98304
s_add_u32 s44, s43, 768
Mv 44,76,96
buffer_load_dwordx4 v26, s[12:15], s44 offen offset:0 lds
Mv 48,80,96
v_bfe_u32 v4, v0, 2, 2
v_lshl_or_b32 v12, v4, 7, v12
Mv 52,84,96
v_lshrrev_b32 v4, 4, v0
v_lshl_or_b32 v12, v4, 10, v12
Mv 56,88,96
v_xor_b32 v13, 32, v12
v_xor_b32 v14, 64, v12
Mv 60,92,96
buffer_load_dwordx4 v27, s[12:15], s44 offen offset:1024 lds
Mv 64,64,100
v_xor_b32 v15, 96, v12
v_add_u32 v20, 0x20000, v12
Mv 68,68,100
v_add_u32 v21, 0x20000, v13
v_add_u32 v22, 0x20000, v14
Mv 72,72,100
v_add_u32 v23, 0x20000, v15
v_add_u32 v16, 0x10000, v12
Mv 76,76,100
s_add_u32 s44, s43, 13056
buffer_load_dwordx4 v26, s[12:15], s44 offen offset:2048 lds
Mv 80,80,100
)" R"(
v_add_u32 v17, 0x10000, v13
v_add_u32 v18, 0x10000, v14
Mv 84,84,100
v_add_u32 v19, 0x10000, v15
Cv 32,32,33
Mv 88,88,100
Cv 33,34,35
Cv 34,36,37
Mv 92,92,100
buffer_load_dwordx4 v27, s[12:15], s44 offen offset:3072 lds
Mv 96,64,104
Cv 35,38,39
Cv 40,40,41
Mv 100,68,104
Cv 41,42,43
Cv 42,44,45
Mv 104,72,104
s_add_u32 m0, s41, 32768
s_add_u32 s92, s42, 0x1c0080
Mv 108,76,104
buffer_load_dwordx4 v8, s[16:19], s92 offen offset:0 lds
Mv 112,80,104
Cv 43,46,47
Cv 48,48,49
Mv 116,84,104
Cv 49,50,51
Cv 50,52,53
Mv 120,88,104
Cv 51,54,55
Cv 56,56,57
Mv 124,92,104
buffer_store_dwordx4 v[32:35], v2, s[20:23], s45 offen offset:0 sc0 sc1
Mv 128,64,108
Cv 57,58,59
Cv 58,60,61
Mv 132,68,108
Cv 59,62,63
Cv 64,64,65
Mv 136,72,108
Cv 65,66,67
Cv 66,68,69
Mv 140,76,108
buffer_load_dwordx4 v9, s[16:19], s92 offen offset:1024 lds
Mv 144,80,108
Cv 67,70,71
Cv 72,72,73
Mv 148,84,108
Cv 73,74,75
Cv 74,76,77
Mv 152,88,108
Cv 75,78,79
Cv 80,80,81
Mv 156,92,108
buffer_store_dwordx4 v[40:43], v2, s[20:23], s45 offen offset:1024 sc0 sc1
Mv 160,0,48
Cv 81,82,83
Cv 82,84,85
Mv 160,64,112
Cv 83,86,87
Cv 88,88,89
Mv 164,4,48
Cv 89,90,91
Cv 90,92,93
Mv 164,68,112
)" R"(
buffer_load_dwordx4 v10, s[16:19], s92 offen offset:2048 lds
Mv 168,8,48
Cv 91,94,95
Cv 96,96,97
Mv 168,72,112
Cv 97,98,99
Cv 98,100,101
Mv 172,12,48
Cv 99,102,103
Cv 104,104,105
Mv 172,76,112
buffer_store_dwordx4 v[48:51], v2, s[20:23], s45 offen offset:2048 sc0 sc1
Mv 176,16,48
Cv 105,106,107
Cv 106,108,109
Mv 176,80,112
Cv 107,110,111
Cv 112,112,113
Mv 180,20,48
Cv 113,114,115
Cv 114,116,117
Mv 180,84,112
buffer_load_dwordx4 v11, s[16:19], s92 offen offset:3072 lds
Mv 184,24,48
Cv 115,118,119
Cv 120,120,121
Mv 184,88,112
Cv 121,122,123
Cv 122,124,125
Mv 188,28,48
Cv 123,126,127
Cv 128,128,129
Mv 188,92,112
buffer_store_dwordx4 v[56:59], v2, s[20:23], s45 offen offset:3072 sc0 sc1
Mv 192,0,52
Cv 129,130,131
Cv 130,132,133
Mv 192,64,116
Cv 131,134,135
Cv 136,136,137
Mv 196,4,52
s_add_u32 m0, s41, 36864
s_add_u32 s92, s42, 0x200080
Mv 196,68,116
buffer_load_dwordx4 v8, s[16:19], s92 offen offset:0 lds
Mv 200,8,52
Cv 137,138,139
Cv 138,140,141
Mv 200,72,116
Cv 139,142,143
Cv 144,144,145
Mv 204,12,52
Cv 145,146,147
Cv 146,148,149
Mv 204,76,116
buffer_store_dwordx4 v[64:67], v3, s[20:23], s45 offen offset:0 sc0 sc1
Mv 208,16,52
Cv 147,150,151
Cv 152,152,153
Mv 208,80,116
Cv 153,154,155
Cv 154,156,157
Mv 212,20,52
Cv 155,158,159
)" R"(
Mv 212,84,116
buffer_load_dwordx4 v9, s[16:19], s92 offen offset:1024 lds
Mv 216,24,52
Mv 216,88,116
Mv 220,28,52
Mv 220,92,116
buffer_store_dwordx4 v[72:75], v3, s[20:23], s45 offen offset:1024 sc0 sc1
Mv 224,0,56
Ma 224,0,60
Ma 228,4,60
Mv 228,4,56
buffer_load_dwordx4 v10, s[16:19], s92 offen offset:2048 lds
Mv 232,8,56
Ma 232,8,60
Ma 236,12,60
Mv 236,12,56
s_waitcnt vmcnt(24)
s_barrier
Mv 240,16,56
buffer_store_dwordx4 v[80:83], v3, s[20:23], s45 offen offset:2048 sc0 sc1
Ma 240,16,60
Ma 244,20,60
Mv 244,20,56
Mv 248,24,56
Ma 248,24,60
Ma 252,28,60
Mv 252,28,56
buffer_store_dwordx4 v[88:91], v3, s[20:23], s45 offen offset:3072 sc0 sc1
Mv 224,64,120
Rt 0,20,0
Mv 228,68,120
Rt 2,20,512
Mv 232,72,120
Rt 4,21,0
Mv 236,76,120
Rt 6,21,512
Mv 240,80,120
Rt 8,22,0
Mv 244,84,120
Rt 10,22,512
Mv 248,88,120
Rt 12,23,0
Mv 252,92,120
Rt 14,23,512
Ma 224,64,124
Rt 16,20,4096
Ma 228,68,124
Rt 18,20,4608
Ma 232,72,124
Rt 20,21,4096
Ma 236,76,124
Rt 22,21,4608
Ma 240,80,124
Rt 24,22,4096
Ma 244,84,124
Rt 26,22,4608
Ma 248,88,124
Rt 28,23,4096
Ma 252,92,124
Rt 30,23,4608
s_waitcnt vmcnt(15) lgkmcnt(8)
s_branch .LzQA
.LzAV:
Mz 32,32,0
Rt 64,18,16384
)" R"(
Mz 36,36,0
s_mov_b32 s24, s80
s_mov_b32 s25, s81
Mz 96,40,0
Rt 66,18,18432
s_mov_b32 s26, 0x380000
Mz 100,44,0
s_add_u32 s39, s39, 0x40000
s_mov_b32 s27, 0x20000
Mz 160,48,0
Rt 68,19,16384
s_add_u32 m0, s37, 49152
Mz 164,52,0
buffer_load_dwordx4 v12, s[8:11], s39 offen offset:0 lds
Mz 224,56,0
Rt 70,19,18432
s_bfe_u32 s28, s2, 0x30005
Mz 228,60,0
s_lshl3_add_u32 s29, s34, s28
s_lshl2_add_u32 s29, s29, s33
Mz 236,60,4
Rt 72,20,16384
s_mul_i32 s83, s29, 7168
Mz 232,56,4
s_mul_i32 s29, s85, 448
s_add_u32 s83, s83, s29
Mz 172,52,4
Rt 74,20,18432
s_lshl1_add_u32 s29, s34, s85
Mz 168,48,4
s_lshl_b32 s82, s29, 17
s_add_u32 s82, s82, 0x5000000
Mz 108,44,4
Rt 76,21,16384
s_mul_i32 s29, s28, 14
Mz 104,40,4
buffer_load_dwordx4 v13, s[8:11], s39 offen offset:1024 lds
Mz 44,36,4
Rt 78,21,18432
s_add_u32 s29, s29, s33
Mz 40,32,4
s_lshl3_add_u32 s28, s33, s28
Mz 48,32,8
Rt 80,22,16384
Mz 52,36,8
s_cmp_eq_u32 s85, 0
s_cselect_b32 s87, s29, s28
Mz 112,40,8
Rt 82,22,18432
Mz 116,44,8
s_cmp_eq_u32 s85, 0
s_cselect_b32 s92, 4, 32
Mz 176,48,8
Rt 84,23,16384
v_lshrrev_b32 v4, 5, v0
Mz 180,52,8
buffer_load_dwordx4 v14, s[8:11], s39 offen offset:2048 lds
Mz 240,56,8
Rt 86,23,18432
v_mul_u32_u24 v4, 384, v4
Mz 244,60,8
v_add_u32 v28, v4, v1
v_and_b32 v4, 31, v0
Mz 252,60,12
)" R"(
Rt 88,24,16384
v_cmp_lt_u32_e32 vcc, 27, v4
Mz 248,56,12
v_mov_b32 v4, 0x40000000
v_lshrrev_b32 v29, 2, v0
Mz 188,52,12
Rt 90,24,18432
v_cndmask_b32_e32 v28, v28, v4, vcc
Mz 184,48,12
v_and_b32 v4, 48, v1
v_lshl_or_b32 v29, v29, 9, v4
Mz 124,44,12
Rt 92,25,16384
s_lshr_b32 s28, s33, 1
Mz 120,40,12
buffer_load_dwordx4 v15, s[8:11], s39 offen offset:3072 lds
Mz 60,36,12
Rt 94,25,18432
s_lshl_b32 s28, s28, 6
Mz 56,32,12
s_and_b32 s29, s33, 1
s_lshl3_add_u32 s28, s29, s28
Mz 64,32,16
Rb 96,17,0
s_add_u32 s28, s28, 0x20000
Mz 68,36,16
v_add_u32 v29, s28, v29
Mz 128,40,16
Rb 100,17,2048
Mz 132,44,16
s_add_u32 s39, s39, 0x40000
Mz 192,48,16
Rb 104,17,8192
s_add_u32 m0, s37, 0
Mz 196,52,16
buffer_load_dwordx4 v12, s[8:11], s39 offen offset:0 lds
v_mfma_f32_16x16x32_bf16 a[224:227], a[56:59], a[16:19], 0
Rb 108,17,10240
v_mfma_f32_16x16x32_bf16 a[228:231], a[60:63], a[16:19], 0
v_mfma_f32_16x16x32_bf16 a[236:239], a[60:63], a[20:23], 0
Rb 112,17,16384
v_mfma_f32_16x16x32_bf16 a[232:235], a[56:59], a[20:23], 0
Mz 204,52,20
Rb 116,17,18432
Mz 200,48,20
Mz 140,44,20
Rb 120,17,24576
Mz 136,40,20
buffer_load_dwordx4 v13, s[8:11], s39 offen offset:1024 lds
Mz 76,36,20
Rb 124,17,26624
Mz 72,32,20
Mz 80,32,24
Mz 84,36,24
Mz 144,40,24
Mz 148,44,24
Mz 208,48,24
Mz 212,52,24
buffer_load_dwordx4 v14, s[8:11], s39 offen offset:2048 lds
v_mfma_f32_16x16x32_bf16 a[240:243], a[56:59], a[24:27], 0
v_mfma_f32_16x16x32_bf16 a[244:247], a[60:63], a[24:27], 0
v_mfma_f32_16x16x32_bf16 a[252:255], a[60:63], a[28:31], 0
v_mfma_f32_16x16x32_bf16 a[248:251], a[56:59], a[28:31], 0
Mz 220,52,28
)" R"(
Mz 216,48,28
Mz 156,44,28
Mz 152,40,28
buffer_load_dwordx4 v15, s[8:11], s39 offen offset:3072 lds
Mz 92,36,28
s_add_u32 m0, s37, 0x10000
s_add_u32 s29, s83, 0
buffer_load_dwordx4 v28, s[24:27], s29 offen offset:0 lds
Mz 88,32,28
s_add_u32 s29, s83, 768
s_waitcnt vmcnt(10) lgkmcnt(0)
s_barrier
Mv 32,96,64
Rt 0,18,32768
buffer_load_dwordx4 v28, s[24:27], s29 offen offset:1024 lds
s_add_u32 s29, s83, 1536
Mv 36,100,64
buffer_load_dwordx4 v28, s[24:27], s29 offen offset:2048 lds
s_add_u32 s29, s83, 2304
buffer_load_dwordx4 v28, s[24:27], s29 offen offset:3072 lds
Mv 96,104,64
Rt 2,18,34816
Mv 100,108,64
s_add_u32 s38, s38, 0xffff0080
Mv 160,112,64
Rt 4,19,32768
s_add_u32 m0, s36, 0
Mv 164,116,64
buffer_load_dwordx4 v8, s[4:7], s38 offen offset:0 lds
Mv 224,120,64
Rt 6,19,34816
Mv 228,124,64
Mv 236,124,68
Rt 8,20,32768
Mv 232,120,68
Mv 172,116,68
Rt 10,20,34816
Mv 168,112,68
Mv 108,108,68
Rt 12,21,32768
Mv 104,104,68
buffer_load_dwordx4 v9, s[4:7], s38 offen offset:1024 lds
Mv 44,100,68
Rt 14,21,34816
Mv 40,96,68
Mv 48,96,72
Rt 16,22,32768
Mv 52,100,72
Mv 112,104,72
Rt 18,22,34816
Mv 116,108,72
Mv 176,112,72
Rt 20,23,32768
Mv 180,116,72
buffer_load_dwordx4 v10, s[4:7], s38 offen offset:2048 lds
Mv 240,120,72
Rt 22,23,34816
Mv 244,124,72
Mv 252,124,76
Rt 24,24,32768
Mv 248,120,76
Mv 188,116,76
Rt 26,24,34816
Mv 184,112,76
Mv 124,108,76
)" R"(
Rt 28,25,32768
Mv 120,104,76
buffer_load_dwordx4 v11, s[4:7], s38 offen offset:3072 lds
Mv 60,100,76
Rt 30,25,34816
Mv 56,96,76
Mv 64,96,80
Rb 32,16,32768
Mv 68,100,80
Mv 128,104,80
Rb 36,16,34816
Mv 132,108,80
s_add_u32 s38, s38, 0x10000
Mv 192,112,80
Rb 40,16,40960
s_add_u32 m0, s36, 4096
Mv 196,116,80
buffer_load_dwordx4 v8, s[4:7], s38 offen offset:0 lds
Ma 224,120,80
Rb 44,16,43008
Ma 228,124,80
Ma 236,124,84
Rb 48,16,49152
Ma 232,120,84
Mv 204,116,84
Rb 52,16,51200
Mv 200,112,84
Mv 140,108,84
Rb 56,16,57344
Mv 136,104,84
buffer_load_dwordx4 v9, s[4:7], s38 offen offset:1024 lds
Mv 76,100,84
Rb 60,16,59392
Mv 72,96,84
Mv 80,96,88
Mv 84,100,88
Mv 144,104,88
Mv 148,108,88
Mv 208,112,88
Mv 212,116,88
buffer_load_dwordx4 v10, s[4:7], s38 offen offset:2048 lds
Ma 240,120,88
Ma 244,124,88
Ma 252,124,92
Ma 248,120,92
Mv 220,116,92
Mv 216,112,92
Mv 156,108,92
Mv 152,104,92
buffer_load_dwordx4 v11, s[4:7], s38 offen offset:3072 lds
Mv 92,100,92
Mv 88,96,92
s_waitcnt vmcnt(8) lgkmcnt(0)
s_barrier
Mv 32,32,0
Rt 64,18,49152
Mv 36,36,0
Mv 96,40,0
Rt 66,18,51200
Mv 100,44,0
s_add_u32 s39, s39, 0x40000
Mv 160,48,0
Rt 68,19,49152
s_add_u32 m0, s37, 16384
Mv 164,52,0
)" R"(
buffer_load_dwordx4 v12, s[8:11], s39 offen offset:0 lds
Mv 224,56,0
Rt 70,19,51200
Mv 228,60,0
Mv 236,60,4
Rt 72,20,49152
Mv 232,56,4
Mv 172,52,4
Rt 74,20,51200
Mv 168,48,4
Mv 108,44,4
Rt 76,21,49152
Mv 104,40,4
buffer_load_dwordx4 v13, s[8:11], s39 offen offset:1024 lds
Mv 44,36,4
Rt 78,21,51200
Mv 40,32,4
Mv 48,32,8
Rt 80,22,49152
Mv 52,36,8
Mv 112,40,8
Rt 82,22,51200
Mv 116,44,8
Mv 176,48,8
Rt 84,23,49152
Mv 180,52,8
buffer_load_dwordx4 v14, s[8:11], s39 offen offset:2048 lds
Mv 240,56,8
Rt 86,23,51200
Mv 244,60,8
Mv 252,60,12
Rt 88,24,49152
Mv 248,56,12
Mv 188,52,12
Rt 90,24,51200
Mv 184,48,12
Mv 124,44,12
Rt 92,25,49152
Mv 120,40,12
buffer_load_dwordx4 v15, s[8:11], s39 offen offset:3072 lds
Mv 60,36,12
Rt 94,25,51200
Mv 56,32,12
Mv 64,32,16
Rb 96,17,32768
Mv 68,36,16
Mv 128,40,16
Rb 100,17,34816
Mv 132,44,16
s_add_u32 s39, s39, 0x40000
Mv 192,48,16
Rb 104,17,40960
s_add_u32 m0, s37, 32768
Mv 196,52,16
buffer_load_dwordx4 v12, s[8:11], s39 offen offset:0 lds
Ma 224,56,16
Rb 108,17,43008
Ma 228,60,16
Ma 236,60,20
Rb 112,17,49152
Ma 232,56,20
Mv 204,52,20
Rb 116,17,51200
Mv 200,48,20
)" R"(
Mv 140,44,20
Rb 120,17,57344
Mv 136,40,20
buffer_load_dwordx4 v13, s[8:11], s39 offen offset:1024 lds
Mv 76,36,20
Rb 124,17,59392
Mv 72,32,20
Mv 80,32,24
Rt 176,29,0
Mv 84,36,24
Rt 178,29,8192
Mv 144,40,24
Rt 180,29,128
Mv 148,44,24
Rt 182,29,8320
Mv 208,48,24
Rt 184,29,256
Mv 212,52,24
buffer_load_dwordx4 v14, s[8:11], s39 offen offset:2048 lds
Rt 186,29,8448
Ma 240,56,24
Rt 188,29,384
Ma 244,60,24
Rt 190,29,8576
Ma 252,60,28
Ma 248,56,28
Mv 220,52,28
Mv 216,48,28
Mv 156,44,28
Mv 152,40,28
buffer_load_dwordx4 v15, s[8:11], s39 offen offset:3072 lds
Mv 92,36,28
Mv 88,32,28
s_waitcnt vmcnt(8) lgkmcnt(0)
s_barrier
Mv 32,96,64
Rt 0,18,0
s_mul_i32 s29, s87, 147
Mv 36,100,64
s_lshr_b32 s29, s29, 10
s_add_u32 s29, s29, s87
Mv 96,104,64
Rt 2,18,2048
s_lshl_b32 s29, s29, 10
Mv 100,108,64
s_add_u32 s38, s38, 0xffff0080
s_add_u32 s29, s29, s82
Mv 160,112,64
Rt 4,19,0
s_add_u32 m0, s36, 32768
Mv 164,116,64
buffer_load_dwordx4 v8, s[4:7], s38 offen offset:0 lds
Mv 224,120,64
Rt 6,19,2048
s_add_u32 s87, s87, s92
Mv 228,124,64
Mv 236,124,68
Rt 8,20,0
Mv 232,120,68
buffer_store_dwordx4 a[176:179], v1, s[20:23], s29 offen sc0 sc1
Mv 172,116,68
Rt 10,20,2048
Mv 168,112,68
Mv 108,108,68
Rt 12,21,0
)" R"(
Mv 104,104,68
buffer_load_dwordx4 v9, s[4:7], s38 offen offset:1024 lds
Mv 44,100,68
Rt 14,21,2048
Mv 40,96,68
Mv 48,96,72
Rt 16,22,0
s_mul_i32 s29, s87, 147
Mv 52,100,72
s_lshr_b32 s29, s29, 10
s_add_u32 s29, s29, s87
Mv 112,104,72
Rt 18,22,2048
s_lshl_b32 s29, s29, 10
Mv 116,108,72
s_add_u32 s29, s29, s82
s_add_u32 s87, s87, s92
Mv 176,112,72
Rt 20,23,0
Mv 180,116,72
buffer_load_dwordx4 v10, s[4:7], s38 offen offset:2048 lds
Mv 240,120,72
Rt 22,23,2048
Mv 244,124,72
Mv 252,124,76
Rt 24,24,0
Mv 248,120,76
buffer_store_dwordx4 a[180:183], v1, s[20:23], s29 offen sc0 sc1
Mv 188,116,76
Rt 26,24,2048
Mv 184,112,76
Mv 124,108,76
Rt 28,25,0
Mv 120,104,76
buffer_load_dwordx4 v11, s[4:7], s38 offen offset:3072 lds
Mv 60,100,76
Rt 30,25,2048
Mv 56,96,76
Mv 64,96,80
Rb 32,16,0
s_mul_i32 s29, s87, 147
Mv 68,100,80
s_lshr_b32 s29, s29, 10
s_add_u32 s29, s29, s87
Mv 128,104,80
Rb 36,16,2048
s_lshl_b32 s29, s29, 10
Mv 132,108,80
s_add_u32 s38, s38, 0x10000
s_add_u32 s29, s29, s82
Mv 192,112,80
Rb 40,16,8192
s_add_u32 m0, s36, 36864
Mv 196,116,80
buffer_load_dwordx4 v8, s[4:7], s38 offen offset:0 lds
Ma 224,120,80
Rb 44,16,10240
s_add_u32 s87, s87, s92
Ma 228,124,80
Ma 236,124,84
Rb 48,16,16384
Ma 232,120,84
buffer_store_dwordx4 a[184:187], v1, s[20:23], s29 offen sc0 sc1
Mv 204,116,84
)" R"(
Rb 52,16,18432
Mv 200,112,84
Mv 140,108,84
Rb 56,16,24576
Mv 136,104,84
buffer_load_dwordx4 v9, s[4:7], s38 offen offset:1024 lds
Mv 76,100,84
Rb 60,16,26624
Mv 72,96,84
Mv 80,96,88
s_mul_i32 s29, s87, 147
s_lshr_b32 s29, s29, 10
Mv 84,100,88
s_add_u32 s29, s29, s87
s_lshl_b32 s29, s29, 10
Mv 144,104,88
s_add_u32 s29, s29, s82
Mv 148,108,88
s_cmp_gt_u32 s33, 1
s_cmov_b32 s29, 0x40000000
Mv 208,112,88
Mv 212,116,88
buffer_load_dwordx4 v10, s[4:7], s38 offen offset:2048 lds
Ma 240,120,88
Ma 244,124,88
Ma 252,124,92
Ma 248,120,92
buffer_store_dwordx4 a[188:191], v1, s[20:23], s29 offen sc0 sc1
s_mov_b32 s40, 5
Mv 220,116,92
Mv 216,112,92
Mv 156,108,92
Mv 152,104,92
buffer_load_dwordx4 v11, s[4:7], s38 offen offset:3072 lds
Mv 92,100,92
Mv 88,96,92
s_waitcnt vmcnt(16) lgkmcnt(0)
s_barrier
s_mov_b32 s28, s35
s_lshl2_add_u32 s28, s28, s33
s_mul_i32 s43, s28, 28672
v_and_b32 v26, 0x70, v1
v_and_b32 v4, 48, v0
v_xor_b32 v26, v26, v4
v_lshrrev_b32 v4, 3, v0
v_mul_u32_u24 v4, 896, v4
v_add_u32 v26, v26, v4
v_xor_b32 v27, 64, v26
v_add_u32 v27, 6144, v27
v_bfe_u32 v28, v0, 1, 3
v_lshrrev_b32 v4, 4, v0
v_xor_b32 v28, v28, v4
v_lshlrev_b32 v28, 4, v28
v_and_b32 v4, 15, v0
v_lshl_or_b32 v28, v4, 7, v28
s_add_u32 s28, s36, 0x18000
v_add_u32 v28, s28, v28
v_xor_b32 v29, 64, v28
s_add_u32 m0, s36, 131072
s_add_u32 s44, s43, 0
buffer_load_dwordx4 v26, s[12:15], s44 offen offset:0 lds
buffer_load_dwordx4 v27, s[12:15], s44 offen offset:1024 lds
s_add_u32 s44, s43, 12288
buffer_load_dwordx4 v26, s[12:15], s44 offen offset:2048 lds
buffer_load_dwordx4 v27, s[12:15], s44 offen offset:3072 lds
)" R"(
s_add_u32 m0, s36, 135168
s_add_u32 s44, s43, 128
buffer_load_dwordx4 v26, s[12:15], s44 offen offset:0 lds
buffer_load_dwordx4 v27, s[12:15], s44 offen offset:1024 lds
s_add_u32 s44, s43, 12416
buffer_load_dwordx4 v26, s[12:15], s44 offen offset:2048 lds
buffer_load_dwordx4 v27, s[12:15], s44 offen offset:3072 lds
.LzPAV:
Mv 32,32,0
Rt 64,18,16384
Mv 36,36,0
Mv 96,40,0
Rt 66,18,18432
Mv 100,44,0
s_add_u32 s39, s39, 0x40000
Mv 160,48,0
Rt 68,19,16384
s_add_u32 m0, s37, 49152
Mv 164,52,0
buffer_load_dwordx4 v12, s[8:11], s39 offen offset:0 lds
Mv 224,56,0
Rt 70,19,18432
Mv 228,60,0
Mv 236,60,4
Rt 72,20,16384
Mv 232,56,4
Mv 172,52,4
Rt 74,20,18432
Mv 168,48,4
Mv 108,44,4
Rt 76,21,16384
Mv 104,40,4
buffer_load_dwordx4 v13, s[8:11], s39 offen offset:1024 lds
Mv 44,36,4
Rt 78,21,18432
Mv 40,32,4
Mv 48,32,8
Rt 80,22,16384
Mv 52,36,8
Mv 112,40,8
Rt 82,22,18432
Mv 116,44,8
Mv 176,48,8
Rt 84,23,16384
Mv 180,52,8
buffer_load_dwordx4 v14, s[8:11], s39 offen offset:2048 lds
Mv 240,56,8
Rt 86,23,18432
Mv 244,60,8
Mv 252,60,12
Rt 88,24,16384
Mv 248,56,12
Mv 188,52,12
Rt 90,24,18432
Mv 184,48,12
Mv 124,44,12
Rt 92,25,16384
Mv 120,40,12
buffer_load_dwordx4 v15, s[8:11], s39 offen offset:3072 lds
Mv 60,36,12
Rt 94,25,18432
Mv 56,32,12
Mv 64,32,16
Rb 96,17,0
)" R"(
Mv 68,36,16
Mv 128,40,16
Rb 100,17,2048
Mv 132,44,16
s_add_u32 s39, s39, 0x40000
Mv 192,48,16
Rb 104,17,8192
s_add_u32 m0, s37, 0
Mv 196,52,16
buffer_load_dwordx4 v12, s[8:11], s39 offen offset:0 lds
Ma 224,56,16
Rb 108,17,10240
Ma 228,60,16
Ma 236,60,20
Rb 112,17,16384
Ma 232,56,20
Mv 204,52,20
Rb 116,17,18432
Mv 200,48,20
Mv 140,44,20
Rb 120,17,24576
Mv 136,40,20
buffer_load_dwordx4 v13, s[8:11], s39 offen offset:1024 lds
Mv 76,36,20
Rb 124,17,26624
Mv 72,32,20
Mv 80,32,24
Mv 84,36,24
Mv 144,40,24
Mv 148,44,24
Mv 208,48,24
Mv 212,52,24
buffer_load_dwordx4 v14, s[8:11], s39 offen offset:2048 lds
Ma 240,56,24
Ma 244,60,24
Ma 252,60,28
Ma 248,56,28
Mv 220,52,28
Mv 216,48,28
Mv 156,44,28
Mv 152,40,28
buffer_load_dwordx4 v15, s[8:11], s39 offen offset:3072 lds
Mv 92,36,28
Mv 88,32,28
s_waitcnt vmcnt(8) lgkmcnt(0)
s_barrier
Mv 32,96,64
Rt 0,18,32768
Mv 36,100,64
Mv 96,104,64
Rt 2,18,34816
Mv 100,108,64
s_add_u32 s38, s38, 0xffff0080
Mv 160,112,64
Rt 4,19,32768
s_add_u32 m0, s36, 0
Mv 164,116,64
buffer_load_dwordx4 v8, s[4:7], s38 offen offset:0 lds
Mv 224,120,64
Rt 6,19,34816
Mv 228,124,64
Mv 236,124,68
Rt 8,20,32768
Mv 232,120,68
Mv 172,116,68
)" R"(
Rt 10,20,34816
Mv 168,112,68
Mv 108,108,68
Rt 12,21,32768
Mv 104,104,68
buffer_load_dwordx4 v9, s[4:7], s38 offen offset:1024 lds
Mv 44,100,68
Rt 14,21,34816
Mv 40,96,68
Mv 48,96,72
Rt 16,22,32768
Mv 52,100,72
Mv 112,104,72
Rt 18,22,34816
Mv 116,108,72
Mv 176,112,72
Rt 20,23,32768
Mv 180,116,72
buffer_load_dwordx4 v10, s[4:7], s38 offen offset:2048 lds
Mv 240,120,72
Rt 22,23,34816
Mv 244,124,72
Mv 252,124,76
Rt 24,24,32768
Mv 248,120,76
Mv 188,116,76
Rt 26,24,34816
Mv 184,112,76
Mv 124,108,76
Rt 28,25,32768
Mv 120,104,76
buffer_load_dwordx4 v11, s[4:7], s38 offen offset:3072 lds
Mv 60,100,76
Rt 30,25,34816
Mv 56,96,76
Mv 64,96,80
Rb 32,16,32768
Mv 68,100,80
Mv 128,104,80
Rb 36,16,34816
Mv 132,108,80
s_add_u32 s38, s38, 0x10000
Mv 192,112,80
Rb 40,16,40960
s_add_u32 m0, s36, 4096
Mv 196,116,80
buffer_load_dwordx4 v8, s[4:7], s38 offen offset:0 lds
Ma 224,120,80
Rb 44,16,43008
Ma 228,124,80
Ma 236,124,84
Rb 48,16,49152
Ma 232,120,84
Mv 204,116,84
Rb 52,16,51200
Mv 200,112,84
Mv 140,108,84
Rb 56,16,57344
Mv 136,104,84
buffer_load_dwordx4 v9, s[4:7], s38 offen offset:1024 lds
Mv 76,100,84
Rb 60,16,59392
Mv 72,96,84
Mv 80,96,88
)" R"(
Mv 84,100,88
Mv 144,104,88
Mv 148,108,88
Mv 208,112,88
Mv 212,116,88
buffer_load_dwordx4 v10, s[4:7], s38 offen offset:2048 lds
Ma 240,120,88
Ma 244,124,88
Ma 252,124,92
Ma 248,120,92
Mv 220,116,92
Mv 216,112,92
Mv 156,108,92
Mv 152,104,92
buffer_load_dwordx4 v11, s[4:7], s38 offen offset:3072 lds
Mv 92,100,92
Mv 88,96,92
s_waitcnt vmcnt(12) lgkmcnt(0)
s_barrier
Mv 32,32,0
Rt 64,18,49152
Mv 36,36,0
Mv 96,40,0
Rt 66,18,51200
Mv 100,44,0
s_add_u32 s39, s39, 0x40000
Mv 160,48,0
Rt 68,19,49152
s_add_u32 m0, s37, 16384
Mv 164,52,0
buffer_load_dwordx4 v12, s[8:11], s39 offen offset:0 lds
Mv 224,56,0
Rt 70,19,51200
Mv 228,60,0
Mv 236,60,4
Rt 72,20,49152
Mv 232,56,4
Mv 172,52,4
Rt 74,20,51200
Mv 168,48,4
Mv 108,44,4
Rt 76,21,49152
Mv 104,40,4
buffer_load_dwordx4 v13, s[8:11], s39 offen offset:1024 lds
Mv 44,36,4
Rt 78,21,51200
Mv 40,32,4
Mv 48,32,8
Rt 80,22,49152
Mv 52,36,8
Mv 112,40,8
Rt 82,22,51200
Mv 116,44,8
Mv 176,48,8
Rt 84,23,49152
Mv 180,52,8
buffer_load_dwordx4 v14, s[8:11], s39 offen offset:2048 lds
Mv 240,56,8
Rt 86,23,51200
Mv 244,60,8
Mv 252,60,12
Rt 88,24,49152
Mv 248,56,12
Mv 188,52,12
Rt 90,24,51200
)" R"(
Mv 184,48,12
Mv 124,44,12
Rt 92,25,49152
Mv 120,40,12
buffer_load_dwordx4 v15, s[8:11], s39 offen offset:3072 lds
Mv 60,36,12
Rt 94,25,51200
Mv 56,32,12
Mv 64,32,16
Rb 96,17,32768
Mv 68,36,16
Mv 128,40,16
Rb 100,17,34816
Mv 132,44,16
s_add_u32 s39, s39, 0x40000
Mv 192,48,16
Rb 104,17,40960
s_add_u32 m0, s37, 32768
Mv 196,52,16
buffer_load_dwordx4 v12, s[8:11], s39 offen offset:0 lds
Ma 224,56,16
Rb 108,17,43008
Ma 228,60,16
Ma 236,60,20
Rb 112,17,49152
Ma 232,56,20
Mv 204,52,20
Rb 116,17,51200
Mv 200,48,20
Mv 140,44,20
Rb 120,17,57344
Mv 136,40,20
buffer_load_dwordx4 v13, s[8:11], s39 offen offset:1024 lds
Mv 76,36,20
Rb 124,17,59392
Mv 72,32,20
Mv 80,32,24
Mv 84,36,24
Mv 144,40,24
Mv 148,44,24
Mv 208,48,24
Mv 212,52,24
buffer_load_dwordx4 v14, s[8:11], s39 offen offset:2048 lds
Ma 240,56,24
Ma 244,60,24
Ma 252,60,28
Ma 248,56,28
Mv 220,52,28
Mv 216,48,28
Mv 156,44,28
Mv 152,40,28
buffer_load_dwordx4 v15, s[8:11], s39 offen offset:3072 lds
Mv 92,36,28
Mv 88,32,28
s_waitcnt vmcnt(8) lgkmcnt(0)
s_barrier
Mv 32,96,64
Rt 0,18,0
Mv 36,100,64
Mv 96,104,64
Rt 2,18,2048
Mv 100,108,64
s_add_u32 s38, s38, 0xffff0080
Mv 160,112,64
Rt 4,19,0
)" R"(
s_add_u32 m0, s36, 32768
Mv 164,116,64
buffer_load_dwordx4 v8, s[4:7], s38 offen offset:0 lds
Mv 224,120,64
Rt 6,19,2048
Mv 228,124,64
Mv 236,124,68
Rt 8,20,0
Mv 232,120,68
Mv 172,116,68
Rt 10,20,2048
Mv 168,112,68
Mv 108,108,68
Rt 12,21,0
Mv 104,104,68
buffer_load_dwordx4 v9, s[4:7], s38 offen offset:1024 lds
Mv 44,100,68
Rt 14,21,2048
Mv 40,96,68
Mv 48,96,72
Rt 16,22,0
Mv 52,100,72
Mv 112,104,72
Rt 18,22,2048
Mv 116,108,72
Mv 176,112,72
Rt 20,23,0
Mv 180,116,72
buffer_load_dwordx4 v10, s[4:7], s38 offen offset:2048 lds
Mv 240,120,72
Rt 22,23,2048
Mv 244,124,72
Mv 252,124,76
Rt 24,24,0
Mv 248,120,76
Mv 188,116,76
Rt 26,24,2048
Mv 184,112,76
Mv 124,108,76
Rt 28,25,0
Mv 120,104,76
buffer_load_dwordx4 v11, s[4:7], s38 offen offset:3072 lds
Mv 60,100,76
Rt 30,25,2048
Mv 56,96,76
Mv 64,96,80
Rb 32,16,0
Mv 68,100,80
Mv 128,104,80
Rb 36,16,2048
Mv 132,108,80
s_add_u32 s38, s38, 0x10000
Mv 192,112,80
Rb 40,16,8192
s_add_u32 m0, s36, 36864
Mv 196,116,80
buffer_load_dwordx4 v8, s[4:7], s38 offen offset:0 lds
Ma 224,120,80
Rb 44,16,10240
Ma 228,124,80
Ma 236,124,84
Rb 48,16,16384
Ma 232,120,84
Mv 204,116,84
)" R"(
Rb 52,16,18432
Mv 200,112,84
Mv 140,108,84
Rb 56,16,24576
Mv 136,104,84
buffer_load_dwordx4 v9, s[4:7], s38 offen offset:1024 lds
Mv 76,100,84
Rb 60,16,26624
Mv 72,96,84
Mv 80,96,88
Mv 84,100,88
Mv 144,104,88
Mv 148,108,88
Mv 208,112,88
Mv 212,116,88
buffer_load_dwordx4 v10, s[4:7], s38 offen offset:2048 lds
Ma 240,120,88
Ma 244,124,88
Ma 252,124,92
Ma 248,120,92
Mv 220,116,92
s_sub_u32 s40, s40, 1
Mv 216,112,92
Mv 156,108,92
Mv 152,104,92
buffer_load_dwordx4 v11, s[4:7], s38 offen offset:3072 lds
Mv 92,100,92
Mv 88,96,92
s_cmp_lg_u32 s40, 0
s_waitcnt vmcnt(12) lgkmcnt(0)
s_barrier
s_cbranch_scc1 .LzPAV
Mv 32,32,0
Rt 64,18,16384
Mv 36,36,0
Rb 192,28,32768
Mv 96,40,0
Rt 66,18,18432
Mv 100,44,0
s_add_u32 s39, s39, 0x40000
Rb 196,28,34816
Mv 160,48,0
Rt 68,19,16384
s_add_u32 m0, s37, 49152
Mv 164,52,0
buffer_load_dwordx4 v12, s[8:11], s39 offen offset:0 lds
Mv 224,56,0
Rt 70,19,18432
Mv 228,60,0
Rb 200,29,32768
Mv 236,60,4
Rt 72,20,16384
Mv 232,56,4
Rb 204,29,34816
Mv 172,52,4
Rt 74,20,18432
Mv 168,48,4
Rb 208,28,36864
Mv 108,44,4
Rt 76,21,16384
Mv 104,40,4
buffer_load_dwordx4 v13, s[8:11], s39 offen offset:1024 lds
Mv 44,36,4
Rt 78,21,18432
Mv 40,32,4
)" R"(
Rb 212,28,38912
Mv 48,32,8
Rt 80,22,16384
Mv 52,36,8
Rb 216,29,36864
Mv 112,40,8
Rt 82,22,18432
Mv 116,44,8
Rb 220,29,38912
Mv 176,48,8
Rt 84,23,16384
Mv 180,52,8
buffer_load_dwordx4 v14, s[8:11], s39 offen offset:2048 lds
Mv 240,56,8
Rt 86,23,18432
Mv 244,60,8
Mv 252,60,12
Rt 88,24,16384
Mv 248,56,12
Mv 188,52,12
Rt 90,24,18432
Mv 184,48,12
Mv 124,44,12
Rt 92,25,16384
Mv 120,40,12
buffer_load_dwordx4 v15, s[8:11], s39 offen offset:3072 lds
Mv 60,36,12
Rt 94,25,18432
v_lshrrev_b32 v5, 2, v0
Mv 56,32,12
v_and_b32 v5, 8, v5
v_and_or_b32 v5, v0, 7, v5
Mv 64,32,16
Rb 96,17,0
v_lshlrev_b32 v5, 8, v5
Mv 68,36,16
v_and_or_b32 v5, v0, 24, v5
v_lshrrev_b32 v5, 1, v5
Mv 128,40,16
Rb 100,17,2048
Mv 132,44,16
s_add_u32 s39, s39, 0x40000
Mv 192,48,16
Rb 104,17,8192
s_add_u32 m0, s37, 0
Mv 196,52,16
buffer_load_dwordx4 v12, s[8:11], s39 offen offset:0 lds
Ma 224,56,16
Rb 108,17,10240
Ma 228,60,16
Ma 236,60,20
Rb 112,17,16384
Ma 232,56,20
Mv 204,52,20
Rb 116,17,18432
Mv 200,48,20
Mv 140,44,20
Rb 120,17,24576
Mv 136,40,20
buffer_load_dwordx4 v13, s[8:11], s39 offen offset:1024 lds
Mv 76,36,20
Rb 124,17,26624
Mv 72,32,20
Mv 80,32,24
)" R"(
Mv 84,36,24
Mv 144,40,24
Mv 148,44,24
Mv 208,48,24
Mv 212,52,24
buffer_load_dwordx4 v14, s[8:11], s39 offen offset:2048 lds
Ma 240,56,24
Ma 244,60,24
Ma 252,60,28
Ma 248,56,28
Mv 220,52,28
Mv 216,48,28
Mv 156,44,28
Mv 152,40,28
buffer_load_dwordx4 v15, s[8:11], s39 offen offset:3072 lds
Mv 92,36,28
Mv 88,32,28
s_waitcnt vmcnt(8) lgkmcnt(0)
s_barrier
s_add_u32 m0, s36, 131072
s_add_u32 s44, s43, 256
buffer_load_dwordx4 v26, s[12:15], s44 offen offset:0 lds
buffer_load_dwordx4 v27, s[12:15], s44 offen offset:1024 lds
s_add_u32 s44, s43, 12544
buffer_load_dwordx4 v26, s[12:15], s44 offen offset:2048 lds
buffer_load_dwordx4 v27, s[12:15], s44 offen offset:3072 lds
Mv 32,96,64
Rt 0,18,32768
Mv 36,100,64
Mv 96,104,64
Rt 2,18,34816
Mv 100,108,64
s_add_u32 s38, s38, 0xffff0080
Mv 160,112,64
Rt 4,19,32768
s_add_u32 m0, s36, 0
Mv 164,116,64
buffer_load_dwordx4 v8, s[4:7], s38 offen offset:0 lds
Mv 224,120,64
Rt 6,19,34816
Mv 228,124,64
Mv 236,124,68
Rt 8,20,32768
Mv 232,120,68
Mv 172,116,68
Rt 10,20,34816
Mv 168,112,68
Mv 108,108,68
Rt 12,21,32768
Mv 104,104,68
buffer_load_dwordx4 v9, s[4:7], s38 offen offset:1024 lds
Mv 44,100,68
Rt 14,21,34816
Mv 40,96,68
Mv 48,96,72
Rt 16,22,32768
Mv 52,100,72
Mv 112,104,72
Rt 18,22,34816
Mv 116,108,72
Mv 176,112,72
Rt 20,23,32768
Mv 180,116,72
buffer_load_dwordx4 v10, s[4:7], s38 offen offset:2048 lds
Mv 240,120,72
)" R"(
Rt 22,23,34816
Mv 244,124,72
Mv 252,124,76
Rt 24,24,32768
Mv 248,120,76
Mv 188,116,76
Rt 26,24,34816
Mv 184,112,76
Mv 124,108,76
Rt 28,25,32768
Mv 120,104,76
buffer_load_dwordx4 v11, s[4:7], s38 offen offset:3072 lds
Mv 60,100,76
Rt 30,25,34816
Mv 56,96,76
s_add_u32 m0, s36, 135168
s_add_u32 s44, s43, 384
buffer_load_dwordx4 v26, s[12:15], s44 offen offset:0 lds
buffer_load_dwordx4 v27, s[12:15], s44 offen offset:1024 lds
s_add_u32 s44, s43, 12672
buffer_load_dwordx4 v26, s[12:15], s44 offen offset:2048 lds
buffer_load_dwordx4 v27, s[12:15], s44 offen offset:3072 lds
Mv 64,96,80
Rb 32,16,32768
Mv 68,100,80
Mv 128,104,80
Rb 36,16,34816
Mv 132,108,80
s_add_u32 s38, s38, 0x10000
Mv 192,112,80
Rb 40,16,40960
s_add_u32 m0, s36, 4096
Mv 196,116,80
buffer_load_dwordx4 v8, s[4:7], s38 offen offset:0 lds
Ma 224,120,80
Rb 44,16,43008
Ma 228,124,80
Ma 236,124,84
Rb 48,16,49152
Ma 232,120,84
Mv 204,116,84
Rb 52,16,51200
Mv 200,112,84
Mv 140,108,84
Rb 56,16,57344
Mv 136,104,84
buffer_load_dwordx4 v9, s[4:7], s38 offen offset:1024 lds
Mv 76,100,84
Rb 60,16,59392
Mv 72,96,84
Mv 80,96,88
Mv 84,100,88
Mv 144,104,88
Mv 148,108,88
Mv 208,112,88
Mv 212,116,88
buffer_load_dwordx4 v10, s[4:7], s38 offen offset:2048 lds
Ma 240,120,88
Ma 244,124,88
Ma 252,124,92
Ma 248,120,92
Mv 220,116,92
Mv 216,112,92
Mv 156,108,92
)" R"(
Mv 152,104,92
buffer_load_dwordx4 v11, s[4:7], s38 offen offset:3072 lds
Mv 92,100,92
Mv 88,96,92
s_waitcnt vmcnt(4) lgkmcnt(0)
s_barrier
Mv 32,32,0
Rt 64,18,49152
Mv 36,36,0
Rb 128,28,32768
Mv 96,40,0
Rt 66,18,51200
Mv 100,44,0
s_add_u32 s39, s39, 0x40000
Rb 132,28,34816
Mv 160,48,0
Rt 68,19,49152
s_add_u32 m0, s37, 16384
Mv 164,52,0
buffer_load_dwordx4 v12, s[8:11], s39 offen offset:0 lds
Mv 224,56,0
Rt 70,19,51200
Mv 228,60,0
Rb 136,29,32768
Mv 236,60,4
Rt 72,20,49152
Mv 232,56,4
Rb 140,29,34816
Mv 172,52,4
Rt 74,20,51200
Mv 168,48,4
Rb 144,28,36864
Mv 108,44,4
Rt 76,21,49152
Mv 104,40,4
buffer_load_dwordx4 v13, s[8:11], s39 offen offset:1024 lds
Mv 44,36,4
Rt 78,21,51200
Mv 40,32,4
Rb 148,28,38912
Mv 48,32,8
Rt 80,22,49152
Mv 52,36,8
Rb 152,29,36864
Mv 112,40,8
Rt 82,22,51200
Mv 116,44,8
Rb 156,29,38912
Mv 176,48,8
Rt 84,23,49152
Mv 180,52,8
buffer_load_dwordx4 v14, s[8:11], s39 offen offset:2048 lds
Mv 240,56,8
Rt 86,23,51200
Mv 244,60,8
Mv 252,60,12
Rt 88,24,49152
Mv 248,56,12
Mv 188,52,12
Rt 90,24,51200
Mv 184,48,12
Mv 124,44,12
Rt 92,25,49152
Mv 120,40,12
buffer_load_dwordx4 v15, s[8:11], s39 offen offset:3072 lds
)" R"(
Mv 60,36,12
Rt 94,25,51200
Mv 56,32,12
Mv 64,32,16
Rb 96,17,32768
Mv 68,36,16
Mv 128,40,16
Rb 100,17,34816
Mv 132,44,16
s_add_u32 s39, s39, 0x40000
Mv 192,48,16
Rb 104,17,40960
s_add_u32 m0, s37, 32768
Mv 196,52,16
buffer_load_dwordx4 v12, s[8:11], s39 offen offset:0 lds
Ma 224,56,16
Rb 108,17,43008
Ma 228,60,16
Ma 236,60,20
Rb 112,17,49152
Ma 232,56,20
Mv 204,52,20
Rb 116,17,51200
Mv 200,48,20
Mv 140,44,20
Rb 120,17,57344
Mv 136,40,20
buffer_load_dwordx4 v13, s[8:11], s39 offen offset:1024 lds
Mv 76,36,20
Rb 124,17,59392
Mv 72,32,20
Mv 80,32,24
Mv 84,36,24
Mv 144,40,24
Mv 148,44,24
Mv 208,48,24
Mv 212,52,24
buffer_load_dwordx4 v14, s[8:11], s39 offen offset:2048 lds
Ma 240,56,24
Ma 244,60,24
Ma 252,60,28
Ma 248,56,28
Mv 220,52,28
Mv 216,48,28
Mv 156,44,28
Mv 152,40,28
buffer_load_dwordx4 v15, s[8:11], s39 offen offset:3072 lds
Mv 92,36,28
Mv 88,32,28
s_waitcnt vmcnt(8) lgkmcnt(0)
s_barrier
s_add_u32 m0, s36, 131072
s_add_u32 s44, s43, 512
buffer_load_dwordx4 v26, s[12:15], s44 offen offset:0 lds
buffer_load_dwordx4 v27, s[12:15], s44 offen offset:1024 lds
s_add_u32 s44, s43, 12800
buffer_load_dwordx4 v26, s[12:15], s44 offen offset:2048 lds
buffer_load_dwordx4 v27, s[12:15], s44 offen offset:3072 lds
Mv 32,96,64
Rt 0,18,0
Mv 36,100,64
Mv 96,104,64
Rt 2,18,2048
Mv 100,108,64
s_add_u32 s38, s38, 0xffff0080
)" R"(
Mv 160,112,64
Rt 4,19,0
s_add_u32 m0, s36, 32768
Mv 164,116,64
buffer_load_dwordx4 v8, s[4:7], s38 offen offset:0 lds
Mv 224,120,64
Rt 6,19,2048
Mv 228,124,64
Mv 236,124,68
Rt 8,20,0
Mv 232,120,68
Mv 172,116,68
Rt 10,20,2048
Mv 168,112,68
Mv 108,108,68
Rt 12,21,0
Mv 104,104,68
buffer_load_dwordx4 v9, s[4:7], s38 offen offset:1024 lds
Mv 44,100,68
Rt 14,21,2048
Mv 40,96,68
Mv 48,96,72
Rt 16,22,0
Mv 52,100,72
Mv 112,104,72
Rt 18,22,2048
Mv 116,108,72
Mv 176,112,72
Rt 20,23,0
Mv 180,116,72
buffer_load_dwordx4 v10, s[4:7], s38 offen offset:2048 lds
Mv 240,120,72
Rt 22,23,2048
Mv 244,124,72
Mv 252,124,76
Rt 24,24,0
Mv 248,120,76
Mv 188,116,76
Rt 26,24,2048
Mv 184,112,76
Mv 124,108,76
Rt 28,25,0
Mv 120,104,76
buffer_load_dwordx4 v11, s[4:7], s38 offen offset:3072 lds
Mv 60,100,76
Rt 30,25,2048
Mv 56,96,76
s_add_u32 m0, s36, 135168
s_add_u32 s44, s43, 640
buffer_load_dwordx4 v26, s[12:15], s44 offen offset:0 lds
buffer_load_dwordx4 v27, s[12:15], s44 offen offset:1024 lds
s_add_u32 s44, s43, 12928
buffer_load_dwordx4 v26, s[12:15], s44 offen offset:2048 lds
buffer_load_dwordx4 v27, s[12:15], s44 offen offset:3072 lds
Mv 64,96,80
Rb 32,16,0
Mv 68,100,80
Mv 128,104,80
Rb 36,16,2048
Mv 132,108,80
s_add_u32 s38, s38, 0x10000
Mv 192,112,80
Rb 40,16,8192
s_add_u32 m0, s36, 36864
)" R"(
Mv 196,116,80
buffer_load_dwordx4 v8, s[4:7], s38 offen offset:0 lds
Ma 224,120,80
Rb 44,16,10240
Ma 228,124,80
Ma 236,124,84
Rb 48,16,16384
Ma 232,120,84
Mv 204,116,84
Rb 52,16,18432
Mv 200,112,84
Mv 140,108,84
Rb 56,16,24576
Mv 136,104,84
buffer_load_dwordx4 v9, s[4:7], s38 offen offset:1024 lds
Mv 76,100,84
Rb 60,16,26624
Mv 72,96,84
Mv 80,96,88
Mv 84,100,88
Mv 144,104,88
Mv 148,108,88
Mv 208,112,88
Mv 212,116,88
buffer_load_dwordx4 v10, s[4:7], s38 offen offset:2048 lds
Ma 240,120,88
Ma 244,124,88
Ma 252,124,92
Ma 248,120,92
Mv 220,116,92
Mv 216,112,92
Mv 156,108,92
Mv 152,104,92
buffer_load_dwordx4 v11, s[4:7], s38 offen offset:3072 lds
Mv 92,100,92
Mv 88,96,92
s_waitcnt vmcnt(4) lgkmcnt(0)
s_barrier
Mv 32,32,0
Rt 64,18,16384
Mv 36,36,0
Rb 160,28,32768
Mv 96,40,0
Rt 66,18,18432
Mv 100,44,0
s_add_u32 s39, s39, 0x40000
Rb 164,28,34816
Mv 160,48,0
Rt 68,19,16384
s_add_u32 m0, s37, 49152
Mv 164,52,0
buffer_load_dwordx4 v12, s[8:11], s39 offen offset:0 lds
Mv 224,56,0
Rt 70,19,18432
Mv 228,60,0
Rb 168,29,32768
Mv 236,60,4
Rt 72,20,16384
Mv 232,56,4
Rb 172,29,34816
Mv 172,52,4
Rt 74,20,18432
Mv 168,48,4
Rb 176,28,36864
Mv 108,44,4
)" R"(
Rt 76,21,16384
Mv 104,40,4
buffer_load_dwordx4 v13, s[8:11], s39 offen offset:1024 lds
Mv 44,36,4
Rt 78,21,18432
Mv 40,32,4
Rb 180,28,38912
Mv 48,32,8
Rt 80,22,16384
Mv 52,36,8
Rb 184,29,36864
Mv 112,40,8
Rt 82,22,18432
Mv 116,44,8
Rb 188,29,38912
Mv 176,48,8
Rt 84,23,16384
Mv 180,52,8
buffer_load_dwordx4 v14, s[8:11], s39 offen offset:2048 lds
Mv 240,56,8
Rt 86,23,18432
Mv 244,60,8
Mv 252,60,12
Rt 88,24,16384
Mv 248,56,12
Mv 188,52,12
Rt 90,24,18432
Mv 184,48,12
Mv 124,44,12
Rt 92,25,16384
Mv 120,40,12
buffer_load_dwordx4 v15, s[8:11], s39 offen offset:3072 lds
Mv 60,36,12
Rt 94,25,18432
v_lshrrev_b32 v10, 3, v0
Mv 56,32,12
v_and_b32 v11, 0x70, v1
v_lshl_or_b32 v10, v10, 13, v11
Mv 64,32,16
Rb 96,17,0
v_lshlrev_b32 v11, 1, v0
Mv 68,36,16
v_and_b32 v11, 32, v11
v_xor_b32 v10, v10, v11
Mv 128,40,16
Rb 100,17,2048
s_and_b32 s28, s33, 1
Mv 132,44,16
s_lshl_b32 s28, s28, 6
v_xor_b32 v8, s28, v10
Mv 192,48,16
Rb 104,17,8192
v_xor_b32 v9, 64, v8
Mv 196,52,16
v_add_u32 v9, 0xfc00, v9
v_add_u32 v10, 0x1f800, v8
Ma 224,56,16
Rb 108,17,10240
v_add_u32 v11, 0x1f800, v9
Ma 228,60,16
Ma 236,60,20
Rb 112,17,16384
Ma 232,56,20
Mv 204,52,20
)" R"(
Rb 116,17,18432
Mv 200,48,20
Mv 140,44,20
Rb 120,17,24576
Mv 136,40,20
Mv 76,36,20
Rb 124,17,26624
Mv 72,32,20
Mv 80,32,24
Mv 84,36,24
Mv 144,40,24
Mv 148,44,24
Mv 208,48,24
Mv 212,52,24
Ma 240,56,24
Ma 244,60,24
Ma 252,60,28
Ma 248,56,28
Mv 220,52,28
Mv 216,48,28
Mv 156,44,28
Mv 152,40,28
Mv 92,36,28
Mv 88,32,28
s_waitcnt vmcnt(4) lgkmcnt(0)
s_barrier
Mv 32,96,64
Rt 0,18,32768
Mv 36,100,64
s_add_u32 s92, s42, 0x0
Mv 96,104,64
Rt 2,18,34816
s_add_u32 m0, s41, 131072
Mv 100,108,64
buffer_load_dwordx4 v8, s[16:19], s92 offen offset:0 lds
Mv 160,112,64
Rt 4,19,32768
Mv 164,116,64
Mv 224,120,64
Rt 6,19,34816
Mv 228,124,64
buffer_load_dwordx4 v9, s[16:19], s92 offen offset:1024 lds
Mv 236,124,68
Rt 8,20,32768
Mv 232,120,68
Mv 172,116,68
Rt 10,20,34816
Mv 168,112,68
buffer_load_dwordx4 v10, s[16:19], s92 offen offset:2048 lds
Mv 108,108,68
Rt 12,21,32768
Mv 104,104,68
Mv 44,100,68
Rt 14,21,34816
Mv 40,96,68
buffer_load_dwordx4 v11, s[16:19], s92 offen offset:3072 lds
Mv 48,96,72
Rt 16,22,32768
Mv 52,100,72
s_add_u32 s92, s42, 0x40000
Mv 112,104,72
Rt 18,22,34816
s_add_u32 m0, s41, 135168
Mv 116,108,72
buffer_load_dwordx4 v8, s[16:19], s92 offen offset:0 lds
)" R"(
Mv 176,112,72
Rt 20,23,32768
Mv 180,116,72
Mv 240,120,72
Rt 22,23,34816
Mv 244,124,72
buffer_load_dwordx4 v9, s[16:19], s92 offen offset:1024 lds
Mv 252,124,76
Rt 24,24,32768
Mv 248,120,76
Mv 188,116,76
Rt 26,24,34816
Mv 184,112,76
buffer_load_dwordx4 v10, s[16:19], s92 offen offset:2048 lds
Mv 124,108,76
Rt 28,25,32768
Mv 120,104,76
s_add_u32 s92, s42, 0x1c0000
Mv 60,100,76
Rt 30,25,34816
s_add_u32 m0, s41, 0
Mv 56,96,76
buffer_load_dwordx4 v8, s[16:19], s92 offen offset:0 lds
Mv 64,96,80
Rb 32,16,32768
Mv 68,100,80
Mv 128,104,80
Rb 36,16,34816
Mv 132,108,80
buffer_load_dwordx4 v9, s[16:19], s92 offen offset:1024 lds
Mv 192,112,80
Rb 40,16,40960
Mv 196,116,80
Ma 224,120,80
Rb 44,16,43008
Ma 228,124,80
buffer_load_dwordx4 v10, s[16:19], s92 offen offset:2048 lds
Ma 236,124,84
Rb 48,16,49152
Ma 232,120,84
Mv 204,116,84
Rb 52,16,51200
Mv 200,112,84
buffer_load_dwordx4 v11, s[16:19], s92 offen offset:3072 lds
Mv 140,108,84
Rb 56,16,57344
Mv 136,104,84
s_add_u32 s92, s42, 0x200000
Mv 76,100,84
Rb 60,16,59392
s_add_u32 m0, s41, 4096
Mv 72,96,84
buffer_load_dwordx4 v8, s[16:19], s92 offen offset:0 lds
Mv 80,96,88
Mv 84,100,88
Mv 144,104,88
Mv 148,108,88
buffer_load_dwordx4 v9, s[16:19], s92 offen offset:1024 lds
Mv 208,112,88
Mv 212,116,88
Ma 240,120,88
Ma 244,124,88
buffer_load_dwordx4 v10, s[16:19], s92 offen offset:2048 lds
Ma 252,124,92
)" R"(
Ma 248,120,92
Mv 220,116,92
Mv 216,112,92
Mv 156,108,92
Mv 152,104,92
Mv 92,100,92
Mv 88,96,92
s_waitcnt vmcnt(14) lgkmcnt(4)
s_barrier
Mv 32,32,0
Rt 64,18,49152
Mv 36,36,0
Rt 66,18,51200
s_add_u32 s92, s42, 0x80
Mv 96,40,0
Rt 68,19,49152
s_add_u32 m0, s41, 65536
Mv 100,44,0
Rt 70,19,51200
buffer_load_dwordx4 v8, s[16:19], s92 offen offset:0 lds
Mv 108,44,4
Rt 72,20,49152
Mv 104,40,4
Rt 74,20,51200
Mv 44,36,4
Rt 76,21,49152
Mv 40,32,4
Rt 78,21,51200
buffer_load_dwordx4 v9, s[16:19], s92 offen offset:1024 lds
Mv 48,32,8
Rt 80,22,49152
Mv 52,36,8
Rt 82,22,51200
Mv 112,40,8
Rt 84,23,49152
Mv 116,44,8
Rt 86,23,51200
buffer_load_dwordx4 v10, s[16:19], s92 offen offset:2048 lds
Mv 124,44,12
Rt 88,24,49152
Mv 120,40,12
Rt 90,24,51200
Mv 60,36,12
Rt 92,25,49152
Mv 56,32,12
Rt 94,25,51200
buffer_load_dwordx4 v11, s[16:19], s92 offen offset:3072 lds
Mv 64,32,16
Rb 96,17,32768
Mv 68,36,16
Rb 100,17,34816
s_add_u32 s92, s42, 0x40080
Mv 128,40,16
Rb 104,17,40960
s_add_u32 m0, s41, 69632
Mv 132,44,16
Rb 108,17,43008
buffer_load_dwordx4 v8, s[16:19], s92 offen offset:0 lds
Mv 140,44,20
Rb 112,17,49152
Mv 136,40,20
Rb 116,17,51200
Mv 76,36,20
Rb 120,17,57344
Mv 72,32,20
)" R"(
Rb 124,17,59392
buffer_load_dwordx4 v9, s[16:19], s92 offen offset:1024 lds
Mv 80,32,24
Mv 84,36,24
Mv 144,40,24
Mv 148,44,24
buffer_load_dwordx4 v10, s[16:19], s92 offen offset:2048 lds
Mv 156,44,28
Mv 152,40,28
Mv 92,36,28
Mv 88,32,28
s_waitcnt lgkmcnt(0)
s_barrier
Mv 32,96,64
v_bfe_u32 v12, v0, 3, 2
v_lshlrev_b32 v12, 5, v12
Mv 36,100,64
v_and_b32 v4, 3, v0
v_lshl_or_b32 v12, v4, 3, v12
Mv 40,96,68
s_add_u32 m0, s36, 98304
s_add_u32 s44, s43, 768
Mv 44,100,68
buffer_load_dwordx4 v26, s[12:15], s44 offen offset:0 lds
Mv 48,96,72
v_bfe_u32 v4, v0, 2, 2
v_lshl_or_b32 v12, v4, 7, v12
Mv 52,100,72
v_lshrrev_b32 v4, 4, v0
v_lshl_or_b32 v12, v4, 10, v12
Mv 56,96,76
v_xor_b32 v13, 32, v12
v_xor_b32 v14, 64, v12
Mv 60,100,76
buffer_load_dwordx4 v27, s[12:15], s44 offen offset:1024 lds
Mv 64,96,80
v_xor_b32 v15, 96, v12
v_add_u32 v20, 0x20000, v12
Mv 68,100,80
v_add_u32 v21, 0x20000, v13
v_add_u32 v22, 0x20000, v14
Mv 72,96,84
v_add_u32 v23, 0x20000, v15
v_add_u32 v16, 0x10000, v12
Mv 76,100,84
s_add_u32 s44, s43, 13056
buffer_load_dwordx4 v26, s[12:15], s44 offen offset:2048 lds
Mv 80,96,88
v_add_u32 v17, 0x10000, v13
v_add_u32 v18, 0x10000, v14
Mv 84,100,88
v_add_u32 v19, 0x10000, v15
Cv 32,32,33
Mv 88,96,92
Cv 33,34,35
Cv 34,36,37
Mv 92,100,92
buffer_load_dwordx4 v27, s[12:15], s44 offen offset:3072 lds
Mv 96,104,64
Cv 35,38,39
Cv 40,40,41
Mv 100,108,64
Cv 41,42,43
Cv 42,44,45
Mv 104,104,68
)" R"(
s_add_u32 m0, s41, 32768
s_add_u32 s92, s42, 0x1c0080
Mv 108,108,68
buffer_load_dwordx4 v8, s[16:19], s92 offen offset:0 lds
Mv 112,104,72
Cv 43,46,47
Cv 48,48,49
Mv 116,108,72
Cv 49,50,51
Cv 50,52,53
Mv 120,104,76
Cv 51,54,55
Cv 56,56,57
Mv 124,108,76
buffer_store_dwordx4 v[32:35], v2, s[20:23], s45 offen offset:0 sc0 sc1
Mv 128,104,80
Cv 57,58,59
Cv 58,60,61
Mv 132,108,80
Cv 59,62,63
Cv 64,64,65
Mv 136,104,84
Cv 65,66,67
Cv 66,68,69
Mv 140,108,84
buffer_load_dwordx4 v9, s[16:19], s92 offen offset:1024 lds
Mv 144,104,88
Cv 67,70,71
Cv 72,72,73
Mv 148,108,88
Cv 73,74,75
Cv 74,76,77
Mv 152,104,92
Cv 75,78,79
Cv 80,80,81
Mv 156,108,92
buffer_store_dwordx4 v[40:43], v2, s[20:23], s45 offen offset:1024 sc0 sc1
Mv 160,48,0
Cv 81,82,83
Cv 82,84,85
Mv 160,112,64
Cv 83,86,87
Cv 88,88,89
Mv 164,52,0
Cv 89,90,91
Cv 90,92,93
Mv 164,116,64
buffer_load_dwordx4 v10, s[16:19], s92 offen offset:2048 lds
Mv 168,48,4
Cv 91,94,95
Cv 96,96,97
Mv 168,112,68
Cv 97,98,99
Cv 98,100,101
Mv 172,52,4
Cv 99,102,103
Cv 104,104,105
Mv 172,116,68
buffer_store_dwordx4 v[48:51], v2, s[20:23], s45 offen offset:2048 sc0 sc1
Mv 176,48,8
Cv 105,106,107
Cv 106,108,109
Mv 176,112,72
Cv 107,110,111
)" R"(
Cv 112,112,113
Mv 180,52,8
Cv 113,114,115
Cv 114,116,117
Mv 180,116,72
buffer_load_dwordx4 v11, s[16:19], s92 offen offset:3072 lds
Mv 184,48,12
Cv 115,118,119
Cv 120,120,121
Mv 184,112,76
Cv 121,122,123
Cv 122,124,125
Mv 188,52,12
Cv 123,126,127
Cv 128,128,129
Mv 188,116,76
buffer_store_dwordx4 v[56:59], v2, s[20:23], s45 offen offset:3072 sc0 sc1
Mv 192,48,16
Cv 129,130,131
Cv 130,132,133
Mv 192,112,80
Cv 131,134,135
Cv 136,136,137
Mv 196,52,16
s_add_u32 m0, s41, 36864
s_add_u32 s92, s42, 0x200080
Mv 196,116,80
buffer_load_dwordx4 v8, s[16:19], s92 offen offset:0 lds
Mv 200,48,20
Cv 137,138,139
Cv 138,140,141
Mv 200,112,84
Cv 139,142,143
Cv 144,144,145
Mv 204,52,20
Cv 145,146,147
Cv 146,148,149
Mv 204,116,84
buffer_store_dwordx4 v[64:67], v3, s[20:23], s45 offen offset:0 sc0 sc1
Mv 208,48,24
Cv 147,150,151
Cv 152,152,153
Mv 208,112,88
Cv 153,154,155
Cv 154,156,157
Mv 212,52,24
Cv 155,158,159
Mv 212,116,88
buffer_load_dwordx4 v9, s[16:19], s92 offen offset:1024 lds
Mv 216,48,28
Mv 216,112,92
Mv 220,52,28
Mv 220,116,92
buffer_store_dwordx4 v[72:75], v3, s[20:23], s45 offen offset:1024 sc0 sc1
Mv 224,56,0
Mv 228,60,0
Mv 236,60,4
Mv 232,56,4
buffer_load_dwordx4 v10, s[16:19], s92 offen offset:2048 lds
Mv 240,56,8
Mv 244,60,8
Mv 252,60,12
Mv 248,56,12
s_waitcnt vmcnt(24)
s_barrier
)" R"(
Ma 224,56,16
buffer_store_dwordx4 v[80:83], v3, s[20:23], s45 offen offset:2048 sc0 sc1
Ma 228,60,16
Ma 236,60,20
Ma 232,56,20
Ma 240,56,24
Ma 244,60,24
Ma 252,60,28
Ma 248,56,28
buffer_store_dwordx4 v[88:91], v3, s[20:23], s45 offen offset:3072 sc0 sc1
Mv 224,120,64
Rt 0,20,0
Mv 228,124,64
Rt 2,20,512
Mv 232,120,68
Rt 4,21,0
Mv 236,124,68
Rt 6,21,512
Mv 240,120,72
Rt 8,22,0
Mv 244,124,72
Rt 10,22,512
Mv 248,120,76
Rt 12,23,0
Mv 252,124,76
Rt 14,23,512
Ma 224,120,80
Rt 16,20,4096
Ma 228,124,80
Rt 18,20,4608
Ma 232,120,84
Rt 20,21,4096
Ma 236,124,84
Rt 22,21,4608
Ma 240,120,88
Rt 24,22,4096
Ma 244,124,88
Rt 26,22,4608
Ma 248,120,92
Rt 28,23,4096
Ma 252,124,92
Rt 30,23,4608
s_waitcnt vmcnt(15) lgkmcnt(8)
.LzQA:
s_cmp_eq_u32 s2, 112
s_cbranch_scc1 .LzT2S
.LzT2E:
Mz 32,0,192
Rt 32,20,8192
Cv 224,224,225
Mz 40,0,196
Rt 34,20,8704
Rb 48,28,0
Mz 44,4,196
Rt 36,21,8192
buffer_store_dwordx4 v[96:99], v2, s[20:23], s46 offen offset:0 sc0 sc1
Mz 36,4,192
Rt 38,21,8704
Rb 52,28,2048
Cv 225,226,227
Mz 48,8,192
Rt 40,22,8192
Cv 226,228,229
Mz 56,8,196
)" R"(
Rt 42,22,8704
Rb 56,29,0
Mz 60,12,196
Rt 44,23,8192
Cv 227,230,231
Cv 232,232,233
Mz 52,12,192
Rt 46,23,8704
Rb 60,29,2048
s_waitcnt lgkmcnt(12)
Mv 32,16,200
Rt 0,20,12288
Cv 233,234,235
Mv 40,16,204
Rt 2,20,12800
Cv 234,236,237
Cv 235,238,239
Mv 44,20,204
Rt 4,21,12288
Cv 240,240,241
Mv 36,20,200
Rt 6,21,12800
Cv 241,242,243
Mv 48,24,200
Rt 8,22,12288
buffer_store_dwordx4 v[104:107], v2, s[20:23], s46 offen offset:1024 sc0 sc1
Mv 56,24,204
Rt 10,22,12800
Cv 242,244,245
Mv 60,28,204
Rt 12,23,12288
Cv 243,246,247
Mv 52,28,200
Rt 14,23,12800
Cv 248,248,249
s_waitcnt lgkmcnt(9)
Mv 32,32,208
Rt 16,20,16384
Cv 249,250,251
Mv 40,32,212
Rt 18,20,16896
Cv 250,252,253
Mv 44,36,212
Rt 20,21,16384
Cv 251,254,255
Mv 36,36,208
Rt 22,21,16896
v_accvgpr_read_b32 v228, a224
v_accvgpr_read_b32 v24, a225
Mv 48,40,208
Rt 24,22,16384
Cv 228,228,24
Mv 56,40,212
Rt 26,22,16896
v_accvgpr_read_b32 v229, a226
Mv 60,44,212
Rt 28,23,16384
buffer_store_dwordx4 v[112:115], v2, s[20:23], s46 offen offset:2048 sc0 sc1
Mv 52,44,208
Rt 30,23,16896
s_waitcnt lgkmcnt(8)
Mv 32,0,216
Rt 32,20,20480
v_accvgpr_read_b32 v25, a227
)" R"(
Mv 40,0,220
Rt 34,20,20992
Cv 229,229,25
v_accvgpr_read_b32 v230, a228
Mv 44,4,220
Rt 36,21,20480
v_accvgpr_read_b32 v24, a229
Mv 36,4,216
Rt 38,21,20992
Cv 230,230,24
Mv 48,8,216
Rt 40,22,20480
v_accvgpr_read_b32 v231, a230
v_accvgpr_read_b32 v25, a231
Mv 56,8,220
Rt 42,22,20992
Cv 231,231,25
Mv 60,12,220
Rt 44,23,20480
v_accvgpr_read_b32 v236, a232
Mv 52,12,216
Rt 46,23,20992
v_accvgpr_read_b32 v24, a233
s_waitcnt lgkmcnt(8)
Mv 32,16,128
Rt 0,20,24576
buffer_store_dwordx4 v[120:123], v2, s[20:23], s46 offen offset:3072 sc0 sc1
Mv 40,16,132
Rt 2,20,25088
Cv 236,236,24
Mv 44,20,132
Rt 4,21,24576
v_accvgpr_read_b32 v237, a234
Mv 36,20,128
Rt 6,21,25088
v_accvgpr_read_b32 v25, a235
Cv 237,237,25
Mv 48,24,128
Rt 8,22,24576
v_accvgpr_read_b32 v238, a236
Mv 56,24,132
Rt 10,22,25088
v_accvgpr_read_b32 v24, a237
Mv 60,28,132
Rt 12,23,24576
Cv 238,238,24
v_accvgpr_read_b32 v239, a238
Mv 52,28,128
Rt 14,23,25088
s_waitcnt lgkmcnt(8)
Mv 32,32,136
Rt 16,12,0
v_accvgpr_read_b32 v25, a239
Mv 40,32,140
Rt 18,12,512
Cv 239,239,25
v_accvgpr_read_b32 v244, a240
Mv 44,36,140
Rt 20,13,0
buffer_store_dwordx4 v[128:131], v3, s[20:23], s46 offen offset:0 sc0 sc1
Mv 36,36,136
Rt 22,13,512
v_accvgpr_read_b32 v24, a241
Mv 48,40,136
)" R"(
Rt 24,14,0
Cv 244,244,24
v_accvgpr_read_b32 v245, a242
Mv 56,40,140
Rt 26,14,512
v_accvgpr_read_b32 v25, a243
Mv 60,44,140
Rt 28,15,0
Cv 245,245,25
Mv 52,44,136
Rt 30,15,512
s_waitcnt vmcnt(5) lgkmcnt(8)
s_barrier
buffer_store_dword v6, v7, s[20:23], s49 offen offset:0 sc0 sc1
Mv 32,0,144
Rt 32,12,4096
v_accvgpr_read_b32 v246, a244
Mv 40,0,148
Rt 34,12,4608
s_add_u32 s92, s42, 0x100
Mv 44,4,148
Rt 36,13,4096
s_add_u32 m0, s41, 98304
Mv 36,4,144
Rt 38,13,4608
buffer_load_dwordx4 v8, s[16:19], s92 offen offset:0 lds
Mv 48,8,144
Rt 40,14,4096
buffer_store_dwordx4 v[136:139], v3, s[20:23], s46 offen offset:1024 sc0 sc1
Mv 56,8,148
Rt 42,14,4608
v_accvgpr_read_b32 v24, a245
Mv 60,12,148
Rt 44,15,4096
Cv 246,246,24
v_accvgpr_read_b32 v247, a246
Mv 52,12,144
Rt 46,15,4608
buffer_load_dwordx4 v9, s[16:19], s92 offen offset:1024 lds
s_waitcnt lgkmcnt(8)
Mv 32,16,152
Rt 0,12,8192
v_accvgpr_read_b32 v25, a247
Mv 40,16,156
Rt 2,12,8704
Cv 247,247,25
v_accvgpr_read_b32 v252, a248
Mv 44,20,156
Rt 4,13,8192
v_accvgpr_read_b32 v24, a249
Mv 36,20,152
Rt 6,13,8704
buffer_load_dwordx4 v10, s[16:19], s92 offen offset:2048 lds
Mv 48,24,152
Rt 8,14,8192
Cv 252,252,24
v_accvgpr_read_b32 v253, a250
Mv 56,24,156
Rt 10,14,8704
v_accvgpr_read_b32 v25, a251
Mv 60,28,156
Rt 12,15,8192
buffer_store_dwordx4 v[144:147], v3, s[20:23], s46 offen offset:2048 sc0 sc1
Mv 52,28,152
Rt 14,15,8704
)" R"(
buffer_load_dwordx4 v11, s[16:19], s92 offen offset:3072 lds
s_waitcnt lgkmcnt(8)
Mv 32,32,160
Rt 16,12,12288
Cv 253,253,25
Mv 40,32,164
Rt 18,12,12800
s_add_u32 s92, s42, 0x40100
Mv 44,36,164
Rt 20,13,12288
s_add_u32 m0, s41, 102400
Mv 36,36,160
Rt 22,13,12800
buffer_load_dwordx4 v8, s[16:19], s92 offen offset:0 lds
Mv 48,40,160
Rt 24,14,12288
v_accvgpr_read_b32 v254, a252
Mv 56,40,164
Rt 26,14,12800
v_accvgpr_read_b32 v24, a253
Mv 60,44,164
Rt 28,15,12288
Cv 254,254,24
v_accvgpr_read_b32 v255, a254
Mv 52,44,160
Rt 30,15,12800
buffer_load_dwordx4 v9, s[16:19], s92 offen offset:1024 lds
s_waitcnt lgkmcnt(8)
Mv 32,0,168
Rt 32,12,16384
buffer_store_dwordx4 v[152:155], v3, s[20:23], s46 offen offset:3072 sc0 sc1
Mv 40,0,172
Rt 34,12,16896
v_accvgpr_read_b32 v25, a255
Cv 255,255,25
Mv 44,4,172
Rt 36,13,16384
Cv 160,160,161
Mv 36,4,168
Rt 38,13,16896
buffer_load_dwordx4 v10, s[16:19], s92 offen offset:2048 lds
Mv 48,8,168
Rt 40,14,16384
Cv 161,162,163
Cv 162,164,165
Mv 56,8,172
Rt 42,14,16896
s_add_u32 s92, s42, 0x1c0100
Mv 60,12,172
Rt 44,15,16384
s_add_u32 m0, s41, 131072
Mv 52,12,168
Rt 46,15,16896
buffer_load_dwordx4 v8, s[16:19], s92 offen offset:0 lds
s_waitcnt lgkmcnt(8)
Mv 32,16,176
Rt 0,12,20480
Cv 163,166,167
Mv 40,16,180
Rt 2,12,20992
Cv 168,168,169
Mv 44,20,180
Rt 4,13,20480
buffer_store_dwordx4 v[160:163], v2, s[20:23], s47 offen offset:0 sc0 sc1
)" R"(
Mv 36,20,176
Rt 6,13,20992
buffer_load_dwordx4 v9, s[16:19], s92 offen offset:1024 lds
Mv 48,24,176
Rt 8,14,20480
Cv 169,170,171
Mv 56,24,180
Rt 10,14,20992
Cv 170,172,173
Mv 60,28,180
Rt 12,15,20480
Cv 171,174,175
Cv 176,176,177
Mv 52,28,176
Rt 14,15,20992
buffer_load_dwordx4 v10, s[16:19], s92 offen offset:2048 lds
s_waitcnt lgkmcnt(8)
Mv 32,32,184
Rt 16,12,24576
Cv 177,178,179
Mv 40,32,188
Rt 18,12,25088
Cv 178,180,181
Cv 179,182,183
Mv 44,36,188
Rt 20,13,24576
Cv 184,184,185
Mv 36,36,184
Rt 22,13,25088
buffer_load_dwordx4 v11, s[16:19], s92 offen offset:3072 lds
Mv 48,40,184
Rt 24,14,24576
Cv 185,186,187
Cv 186,188,189
Mv 56,40,188
Rt 26,14,25088
s_add_u32 s92, s42, 0x200100
Mv 60,44,188
Rt 28,15,24576
s_add_u32 m0, s41, 135168
Mv 52,44,184
Rt 30,15,25088
buffer_load_dwordx4 v8, s[16:19], s92 offen offset:0 lds
s_waitcnt lgkmcnt(8)
Mv 32,0,48
Rt 32,16,0
Cv 187,190,191
Mv 40,0,52
Rt 34,16,512
Cv 192,192,193
Mv 44,4,52
Rt 36,17,0
buffer_store_dwordx4 v[168:171], v2, s[20:23], s47 offen offset:1024 sc0 sc1
Mv 36,4,48
Rt 38,17,512
buffer_load_dwordx4 v9, s[16:19], s92 offen offset:1024 lds
Mv 48,8,48
Rt 40,18,0
Cv 193,194,195
Mv 56,8,52
Rt 42,18,512
Cv 194,196,197
Mv 60,12,52
Rt 44,19,0
)" R"(
Cv 195,198,199
Cv 200,200,201
Mv 52,12,48
Rt 46,19,512
buffer_load_dwordx4 v10, s[16:19], s92 offen offset:2048 lds
s_waitcnt vmcnt(10) lgkmcnt(8)
s_barrier
buffer_store_dword v6, v7, s[20:23], s49 offen offset:4 sc0 sc1
Mv 32,16,56
Rt 0,16,4096
Cv 201,202,203
Mv 40,16,60
Rt 2,16,4608
Cv 202,204,205
Cv 203,206,207
Mv 44,20,60
Rt 4,17,4096
Cv 208,208,209
Mv 36,20,56
Rt 6,17,4608
s_add_u32 s92, s42, 0x180
Mv 48,24,56
Rt 8,18,4096
s_add_u32 m0, s41, 0
Cv 209,210,211
Mv 56,24,60
Rt 10,18,4608
buffer_load_dwordx4 v8, s[16:19], s92 offen offset:0 lds
Mv 60,28,60
Rt 12,19,4096
Cv 210,212,213
Mv 52,28,56
Rt 14,19,4608
Cv 211,214,215
s_waitcnt lgkmcnt(8)
Mz 64,32,192
Rt 16,16,8192
Cv 216,216,217
Mz 72,32,196
Rt 18,16,8704
Cv 217,218,219
Mz 76,36,196
Rt 20,17,8192
buffer_store_dwordx4 v[176:179], v2, s[20:23], s47 offen offset:2048 sc0 sc1
Mz 68,36,192
Rt 22,17,8704
Cv 218,220,221
Cv 219,222,223
Mz 80,40,192
Rt 24,18,8192
v_mul_f32_e32 v32, s54, v32
Mz 88,40,196
Rt 26,18,8704
buffer_load_dwordx4 v9, s[16:19], s92 offen offset:1024 lds
Mz 92,44,196
Rt 28,19,8192
v_mul_f32_e32 v33, s54, v33
Mz 84,44,192
Rt 30,19,8704
s_waitcnt lgkmcnt(8)
Mv 64,0,200
Rt 32,16,12288
Cv 32,32,33
Mv 72,0,204
Rt 34,16,12800
)" R"(
v_accvgpr_write_b32 a224, v32
Mv 76,4,204
Rt 36,17,12288
v_mul_f32_e32 v34, s54, v34
Mv 68,4,200
Rt 38,17,12800
v_mul_f32_e32 v35, s54, v35
Mv 80,8,200
Rt 40,18,12288
Cv 34,34,35
Mv 88,8,204
Rt 42,18,12800
buffer_load_dwordx4 v10, s[16:19], s92 offen offset:2048 lds
Mv 92,12,204
Rt 44,19,12288
v_accvgpr_write_b32 a225, v34
Mv 84,12,200
Rt 46,19,12800
s_waitcnt lgkmcnt(8)
Mv 64,16,208
Rt 0,16,16384
v_mul_f32_e32 v36, s54, v36
Mv 72,16,212
Rt 2,16,16896
v_mul_f32_e32 v37, s54, v37
Mv 76,20,212
Rt 4,17,16384
buffer_store_dwordx4 v[184:187], v2, s[20:23], s47 offen offset:3072 sc0 sc1
Mv 68,20,208
Rt 6,17,16896
Cv 36,36,37
Mv 80,24,208
Rt 8,18,16384
v_accvgpr_write_b32 a226, v36
Mv 88,24,212
Rt 10,18,16896
buffer_load_dwordx4 v11, s[16:19], s92 offen offset:3072 lds
Mv 92,28,212
Rt 12,19,16384
v_mul_f32_e32 v38, s54, v38
Mv 84,28,208
Rt 14,19,16896
s_waitcnt lgkmcnt(8)
Mv 64,32,216
Rt 16,16,20480
v_mul_f32_e32 v39, s54, v39
Mv 72,32,220
Rt 18,16,20992
Cv 38,38,39
Mv 76,36,220
Rt 20,17,20480
v_accvgpr_write_b32 a227, v38
Mv 68,36,216
Rt 22,17,20992
s_add_u32 s92, s42, 0x40180
Mv 80,40,216
Rt 24,18,20480
s_add_u32 m0, s41, 4096
Mv 88,40,220
Rt 26,18,20992
buffer_load_dwordx4 v8, s[16:19], s92 offen offset:0 lds
Mv 92,44,220
Rt 28,19,20480
v_mul_f32_e32 v40, s54, v40
)" R"(
Mv 84,44,216
Rt 30,19,20992
s_waitcnt lgkmcnt(8)
Mv 64,0,128
Rt 32,16,24576
v_mul_f32_e32 v41, s54, v41
Mv 72,0,132
Rt 34,16,25088
Cv 40,40,41
Mv 76,4,132
Rt 36,17,24576
buffer_store_dwordx4 v[192:195], v3, s[20:23], s47 offen offset:0 sc0 sc1
Mv 68,4,128
Rt 38,17,25088
v_accvgpr_write_b32 a228, v40
Mv 80,8,128
Rt 40,18,24576
v_mul_f32_e32 v42, s54, v42
Mv 88,8,132
Rt 42,18,25088
buffer_load_dwordx4 v9, s[16:19], s92 offen offset:1024 lds
Mv 92,12,132
Rt 44,19,24576
v_mul_f32_e32 v43, s54, v43
Mv 84,12,128
Rt 46,19,25088
s_waitcnt lgkmcnt(8)
Mv 64,16,136
Rt 0,12,32768
Cv 42,42,43
Mv 72,16,140
Rt 2,12,33280
v_accvgpr_write_b32 a229, v42
Mv 76,20,140
Rt 4,13,32768
v_mul_f32_e32 v44, s54, v44
Mv 68,20,136
Rt 6,13,33280
v_mul_f32_e32 v45, s54, v45
Mv 80,24,136
Rt 8,14,32768
Cv 44,44,45
Mv 88,24,140
Rt 10,14,33280
buffer_load_dwordx4 v10, s[16:19], s92 offen offset:2048 lds
Mv 92,28,140
Rt 12,15,32768
v_accvgpr_write_b32 a230, v44
Mv 84,28,136
Rt 14,15,33280
s_waitcnt vmcnt(20) lgkmcnt(8)
s_barrier
Mv 64,32,144
Rt 16,12,36864
v_mul_f32_e32 v46, s54, v46
Mv 72,32,148
Rt 18,12,37376
v_mul_f32_e32 v47, s54, v47
Mv 76,36,148
Rt 20,13,36864
buffer_store_dwordx4 v[200:203], v3, s[20:23], s47 offen offset:1024 sc0 sc1
Mv 68,36,144
Rt 22,13,37376
s_add_u32 s92, s42, 0x1c0180
Mv 80,40,144
)" R"(
Rt 24,14,36864
s_add_u32 m0, s41, 65536
Mv 88,40,148
Rt 26,14,37376
buffer_load_dwordx4 v8, s[16:19], s92 offen offset:0 lds
Mv 92,44,148
Rt 28,15,36864
Cv 46,46,47
Mv 84,44,144
Rt 30,15,37376
s_waitcnt lgkmcnt(8)
Mv 64,0,152
Rt 32,12,40960
v_accvgpr_write_b32 a231, v46
Mv 72,0,156
Rt 34,12,41472
v_mul_f32_e32 v48, s54, v48
Mv 76,4,156
Rt 36,13,40960
v_mul_f32_e32 v49, s54, v49
Mv 68,4,152
Rt 38,13,41472
Cv 48,48,49
Mv 80,8,152
Rt 40,14,40960
v_accvgpr_write_b32 a232, v48
Mv 88,8,156
Rt 42,14,41472
buffer_load_dwordx4 v9, s[16:19], s92 offen offset:1024 lds
Mv 92,12,156
Rt 44,15,40960
v_mul_f32_e32 v50, s54, v50
Mv 84,12,152
Rt 46,15,41472
s_waitcnt lgkmcnt(8)
Mv 64,16,160
Rt 0,12,45056
v_mul_f32_e32 v51, s54, v51
Mv 72,16,164
Rt 2,12,45568
Cv 50,50,51
Mv 76,20,164
Rt 4,13,45056
buffer_store_dwordx4 v[208:211], v3, s[20:23], s47 offen offset:2048 sc0 sc1
Mv 68,20,160
Rt 6,13,45568
v_accvgpr_write_b32 a233, v50
Mv 80,24,160
Rt 8,14,45056
v_mul_f32_e32 v52, s54, v52
Mv 88,24,164
Rt 10,14,45568
buffer_load_dwordx4 v10, s[16:19], s92 offen offset:2048 lds
Mv 92,28,164
Rt 12,15,45056
v_mul_f32_e32 v53, s54, v53
Mv 84,28,160
Rt 14,15,45568
s_waitcnt lgkmcnt(8)
Mv 64,32,168
Rt 16,12,49152
Cv 52,52,53
Mv 72,32,172
Rt 18,12,49664
)" R"(
v_accvgpr_write_b32 a234, v52
Mv 76,36,172
Rt 20,13,49152
v_mul_f32_e32 v54, s54, v54
Mv 68,36,168
Rt 22,13,49664
v_mul_f32_e32 v55, s54, v55
Mv 80,40,168
Rt 24,14,49152
buffer_load_dword v4, v5, s[20:23], s50 offen offset:0 sc0 sc1
Mv 88,40,172
Rt 26,14,49664
buffer_load_dwordx4 v11, s[16:19], s92 offen offset:3072 lds
Mv 92,44,172
Rt 28,15,49152
Cv 54,54,55
Mv 84,44,168
Rt 30,15,49664
s_waitcnt lgkmcnt(8)
Mv 64,0,176
Rt 32,12,53248
v_accvgpr_write_b32 a235, v54
Mv 72,0,180
Rt 34,12,53760
v_mul_f32_e32 v56, s54, v56
Mv 76,4,180
Rt 36,13,53248
buffer_store_dwordx4 v[216:219], v3, s[20:23], s47 offen offset:3072 sc0 sc1
Mv 68,4,176
Rt 38,13,53760
s_add_u32 s92, s42, 0x200180
Mv 80,8,176
Rt 40,14,53248
s_add_u32 m0, s41, 69632
Mv 88,8,180
Rt 42,14,53760
buffer_load_dwordx4 v8, s[16:19], s92 offen offset:0 lds
Mv 92,12,180
Rt 44,15,53248
v_mul_f32_e32 v57, s54, v57
Mv 84,12,176
Rt 46,15,53760
s_waitcnt lgkmcnt(8)
Mv 64,16,184
Rt 0,12,57344
Cv 56,56,57
Mv 72,16,188
Rt 2,12,57856
v_accvgpr_write_b32 a236, v56
Mv 76,20,188
Rt 4,13,57344
v_mul_f32_e32 v58, s54, v58
Mv 68,20,184
Rt 6,13,57856
v_mul_f32_e32 v59, s54, v59
Mv 80,24,184
Rt 8,14,57344
Cv 58,58,59
Mv 88,24,188
Rt 10,14,57856
buffer_load_dwordx4 v9, s[16:19], s92 offen offset:1024 lds
Mv 92,28,188
Rt 12,15,57344
v_accvgpr_write_b32 a237, v58
)" R"(
Mv 84,28,184
Rt 14,15,57856
s_waitcnt lgkmcnt(8)
Mv 64,32,48
Rt 16,16,32768
v_mul_f32_e32 v60, s54, v60
Mv 72,32,52
Rt 18,16,33280
v_mul_f32_e32 v61, s54, v61
Mv 76,36,52
Rt 20,17,32768
buffer_store_dwordx4 v[224:227], v2, s[20:23], s48 offen offset:0 sc0 sc1
Mv 68,36,48
Rt 22,17,33280
Cv 60,60,61
Mv 80,40,48
Rt 24,18,32768
v_accvgpr_write_b32 a238, v60
Mv 88,40,52
Rt 26,18,33280
buffer_load_dwordx4 v10, s[16:19], s92 offen offset:2048 lds
Mv 92,44,52
Rt 28,19,32768
v_mul_f32_e32 v62, s54, v62
Mv 84,44,48
Rt 30,19,33280
s_waitcnt vmcnt(6) lgkmcnt(8)
s_barrier
Mv 64,0,56
Rt 32,16,36864
v_cmp_eq_u32_e32 vcc, s3, v4
s_andn2_b64 s[28:29], s[60:61], vcc
s_cbranch_scc1 .Lzsp1
.Lzok1:
Mv 72,0,60
Rt 34,16,37376
s_add_u32 s40, s55, 0x8000
Mv 76,4,60
Rt 36,17,36864
v_mul_f32_e32 v63, s54, v63
Mv 68,4,56
Rt 38,17,37376
Cv 62,62,63
Mv 80,8,56
Rt 40,18,36864
s_add_u32 m0, s36, 32768
Mv 88,8,60
Rt 42,18,37376
buffer_load_dwordx4 v2, s[20:23], s40 offen offset:0 sc1 lds
Mv 92,12,60
Rt 44,19,36864
v_accvgpr_write_b32 a239, v62
Mv 84,12,56
Rt 46,19,37376
s_waitcnt lgkmcnt(8)
Mz 32,16,192
Rt 0,16,40960
Mz 40,16,196
Rt 2,16,41472
Mz 44,20,196
Rt 4,17,40960
buffer_store_dwordx4 v[232:235], v2, s[20:23], s48 offen offset:1024 sc0 sc1
Mz 36,20,192
Rt 6,17,41472
buffer_load_dwordx4 v2, s[20:23], s40 offen offset:1024 sc1 lds
)" R"(
Mz 48,24,192
Rt 8,18,40960
v_mul_f32_e32 v64, s54, v64
Mz 56,24,196
Rt 10,18,41472
v_mul_f32_e32 v65, s54, v65
Mz 60,28,196
Rt 12,19,40960
Cv 64,64,65
Mz 52,28,192
Rt 14,19,41472
s_waitcnt lgkmcnt(8)
Mv 32,32,200
Rt 16,16,45056
v_accvgpr_write_b32 a240, v64
Mv 40,32,204
Rt 18,16,45568
buffer_load_dwordx4 v2, s[20:23], s40 offen offset:2048 sc1 lds
Mv 44,36,204
Rt 20,17,45056
v_mul_f32_e32 v66, s54, v66
Mv 36,36,200
Rt 22,17,45568
v_mul_f32_e32 v67, s54, v67
Mv 48,40,200
Rt 24,18,45056
Cv 66,66,67
Mv 56,40,204
Rt 26,18,45568
v_accvgpr_write_b32 a241, v66
Mv 60,44,204
Rt 28,19,45056
v_mul_f32_e32 v68, s54, v68
Mv 52,44,200
Rt 30,19,45568
buffer_load_dwordx4 v2, s[20:23], s40 offen offset:3072 sc1 lds
s_waitcnt lgkmcnt(8)
Mv 32,0,208
Rt 32,16,49152
v_mul_f32_e32 v69, s54, v69
Mv 40,0,212
Rt 34,16,49664
Cv 68,68,69
Mv 44,4,212
Rt 36,17,49152
buffer_store_dwordx4 v[240:243], v2, s[20:23], s48 offen offset:2048 sc0 sc1
Mv 36,4,208
Rt 38,17,49664
v_accvgpr_write_b32 a242, v68
v_mul_f32_e32 v70, s54, v70
Mv 48,8,208
Rt 40,18,49152
s_add_u32 m0, s36, 36864
buffer_load_dword v4, v7, s[20:23], s52 offen offset:0 sc0 sc1
Mv 56,8,212
Rt 42,18,49664
buffer_load_dwordx4 v3, s[20:23], s40 offen offset:0 sc1 lds
Mv 60,12,212
Rt 44,19,49152
v_mul_f32_e32 v71, s54, v71
Mv 52,12,208
Rt 46,19,49664
s_waitcnt lgkmcnt(8)
Mv 32,16,216
)" R"(
Rt 0,16,53248
Cv 70,70,71
Mv 40,16,220
Rt 2,16,53760
v_accvgpr_write_b32 a243, v70
Mv 44,20,220
Rt 4,17,53248
v_mul_f32_e32 v72, s54, v72
Mv 36,20,216
Rt 6,17,53760
buffer_load_dwordx4 v3, s[20:23], s40 offen offset:1024 sc1 lds
Mv 48,24,216
Rt 8,18,53248
v_mul_f32_e32 v73, s54, v73
Mv 56,24,220
Rt 10,18,53760
Cv 72,72,73
Mv 60,28,220
Rt 12,19,53248
v_accvgpr_write_b32 a244, v72
Mv 52,28,216
Rt 14,19,53760
s_waitcnt lgkmcnt(8)
Mv 32,32,128
Rt 16,16,57344
v_mul_f32_e32 v74, s54, v74
Mv 40,32,132
Rt 18,16,57856
buffer_load_dwordx4 v3, s[20:23], s40 offen offset:2048 sc1 lds
Mv 44,36,132
Rt 20,17,57344
buffer_store_dwordx4 v[248:251], v2, s[20:23], s48 offen offset:3072 sc0 sc1
Mv 36,36,128
Rt 22,17,57856
v_mul_f32_e32 v75, s54, v75
Cv 74,74,75
Mv 48,40,128
Rt 24,18,57344
v_accvgpr_write_b32 a245, v74
Mv 56,40,132
Rt 26,18,57856
v_mul_f32_e32 v76, s54, v76
Mv 60,44,132
Rt 28,19,57344
v_mul_f32_e32 v77, s54, v77
Mv 52,44,128
Rt 30,19,57856
buffer_load_dwordx4 v3, s[20:23], s40 offen offset:3072 sc1 lds
s_waitcnt lgkmcnt(8)
Mv 32,0,136
Rt 32,20,0
Cv 76,76,77
Mv 40,0,140
Rt 34,20,512
v_accvgpr_write_b32 a246, v76
Mv 44,4,140
Rt 36,21,0
v_mul_f32_e32 v78, s54, v78
Mv 36,4,136
Rt 38,21,512
v_mul_f32_e32 v79, s54, v79
Mv 48,8,136
Rt 40,22,0
Cv 78,78,79
)" R"(
Mv 56,8,140
Rt 42,22,512
v_accvgpr_write_b32 a247, v78
Mv 60,12,140
Rt 44,23,0
v_mul_f32_e32 v80, s54, v80
Mv 52,12,136
Rt 46,23,512
s_waitcnt vmcnt(5) lgkmcnt(8)
s_barrier
buffer_store_dword v6, v7, s[20:23], s49 offen offset:8 sc0 sc1
Mv 32,16,144
Rt 0,20,4096
v_cmp_eq_u32_e32 vcc, s3, v4
s_andn2_b64 s[28:29], 1, vcc
s_cbranch_scc1 .Lzsp2
.Lzok2:
Mv 40,16,148
Rt 2,20,4608
v_mul_f32_e32 v81, s54, v81
Mv 44,20,148
Rt 4,21,4096
s_add_u32 m0, s36, 98304
buffer_store_dwordx4 v[228:231], v3, s[20:23], s48 offen offset:0 sc0 sc1
Mv 36,20,144
Rt 6,21,4608
buffer_load_dwordx4 v2, s[20:23], s55 offen offset:0 sc1 lds
Mv 48,24,144
Rt 8,22,4096
Cv 80,80,81
Mv 56,24,148
Rt 10,22,4608
v_accvgpr_write_b32 a248, v80
Mv 60,28,148
Rt 12,23,4096
buffer_load_dwordx4 a[64:67], v2, s[20:23], s51 offen offset:0 sc0 sc1
Mv 52,28,144
Rt 14,23,4608
s_waitcnt lgkmcnt(8)
Mv 32,32,152
Rt 16,20,8192
v_mul_f32_e32 v82, s54, v82
Mv 40,32,156
Rt 18,20,8704
buffer_load_dwordx4 v2, s[20:23], s55 offen offset:1024 sc1 lds
Mv 44,36,156
Rt 20,21,8192
v_mul_f32_e32 v83, s54, v83
Mv 36,36,152
Rt 22,21,8704
Cv 82,82,83
Mv 48,40,152
Rt 24,22,8192
buffer_load_dwordx4 a[68:71], v2, s[20:23], s51 offen offset:1024 sc0 sc1
Mv 56,40,156
Rt 26,22,8704
v_accvgpr_write_b32 a249, v82
Mv 60,44,156
Rt 28,23,8192
v_mul_f32_e32 v84, s54, v84
Mv 52,44,152
Rt 30,23,8704
buffer_load_dwordx4 v2, s[20:23], s55 offen offset:2048 sc1 lds
s_waitcnt lgkmcnt(8)
Mv 32,0,160
)" R"(
Rt 32,20,12288
v_mul_f32_e32 v85, s54, v85
Mv 40,0,164
Rt 34,20,12800
Cv 84,84,85
Mv 44,4,164
Rt 36,21,12288
buffer_load_dwordx4 a[72:75], v2, s[20:23], s51 offen offset:2048 sc0 sc1
buffer_store_dwordx4 v[236:239], v3, s[20:23], s48 offen offset:1024 sc0 sc1
Mv 36,4,160
Rt 38,21,12800
v_accvgpr_write_b32 a250, v84
v_mul_f32_e32 v86, s54, v86
Mv 48,8,160
Rt 40,22,12288
v_mul_f32_e32 v87, s54, v87
Mv 56,8,164
Rt 42,22,12800
buffer_load_dwordx4 v2, s[20:23], s55 offen offset:3072 sc1 lds
Mv 60,12,164
Rt 44,23,12288
Cv 86,86,87
Mv 52,12,160
Rt 46,23,12800
s_waitcnt lgkmcnt(8)
Mv 32,16,168
Rt 0,20,16384
buffer_load_dwordx4 a[76:79], v2, s[20:23], s51 offen offset:3072 sc0 sc1
Mv 40,16,172
Rt 2,20,16896
v_accvgpr_write_b32 a251, v86
Mv 44,20,172
Rt 4,21,16384
s_add_u32 m0, s36, 102400
Mv 36,20,168
Rt 6,21,16896
buffer_load_dwordx4 v3, s[20:23], s55 offen offset:0 sc1 lds
Mv 48,24,168
Rt 8,22,16384
v_mul_f32_e32 v88, s54, v88
Mv 56,24,172
Rt 10,22,16896
v_mul_f32_e32 v89, s54, v89
Mv 60,28,172
Rt 12,23,16384
buffer_load_dwordx4 a[80:83], v3, s[20:23], s51 offen offset:0 sc0 sc1
Mv 52,28,168
Rt 14,23,16896
s_waitcnt lgkmcnt(8)
Mv 32,32,176
Rt 16,20,20480
Cv 88,88,89
Mv 40,32,180
Rt 18,20,20992
buffer_load_dwordx4 v3, s[20:23], s55 offen offset:1024 sc1 lds
Mv 44,36,180
Rt 20,21,20480
buffer_store_dwordx4 v[244:247], v3, s[20:23], s48 offen offset:2048 sc0 sc1
Mv 36,36,176
Rt 22,21,20992
v_accvgpr_write_b32 a252, v88
v_mul_f32_e32 v90, s54, v90
Mv 48,40,176
Rt 24,22,20480
)" R"(
buffer_load_dwordx4 a[84:87], v3, s[20:23], s51 offen offset:1024 sc0 sc1
Mv 56,40,180
Rt 26,22,20992
v_mul_f32_e32 v91, s54, v91
Mv 60,44,180
Rt 28,23,20480
Cv 90,90,91
Mv 52,44,176
Rt 30,23,20992
buffer_load_dwordx4 v3, s[20:23], s55 offen offset:2048 sc1 lds
s_waitcnt lgkmcnt(8)
Mv 32,0,184
Rt 32,20,24576
v_accvgpr_write_b32 a253, v90
Mv 40,0,188
Rt 34,20,25088
v_mul_f32_e32 v92, s54, v92
Mv 44,4,188
Rt 36,21,24576
buffer_load_dwordx4 a[88:91], v3, s[20:23], s51 offen offset:2048 sc0 sc1
Mv 36,4,184
Rt 38,21,25088
v_mul_f32_e32 v93, s54, v93
Mv 48,8,184
Rt 40,22,24576
Cv 92,92,93
Mv 56,8,188
Rt 42,22,25088
buffer_load_dwordx4 v3, s[20:23], s55 offen offset:3072 sc1 lds
Mv 60,12,188
Rt 44,23,24576
v_accvgpr_write_b32 a254, v92
Mv 52,12,184
Rt 46,23,25088
s_waitcnt lgkmcnt(8)
Mv 32,16,48
Rt 0,12,0
buffer_load_dwordx4 a[92:95], v3, s[20:23], s51 offen offset:3072 sc0 sc1
Mv 40,16,52
Rt 2,12,512
v_mul_f32_e32 v94, s54, v94
Mv 44,20,52
Rt 4,13,0
buffer_store_dwordx4 v[252:255], v3, s[20:23], s48 offen offset:3072 sc0 sc1
Mv 36,20,48
Rt 6,13,512
v_mul_f32_e32 v95, s54, v95
Cv 94,94,95
Mv 48,24,48
Rt 8,14,0
v_accvgpr_write_b32 a255, v94
Mv 56,24,52
Rt 10,14,512
Mv 60,28,52
Rt 12,15,0
s_add_u32 s51, s51, 0x8000
buffer_load_dwordx4 a[96:99], v2, s[20:23], s51 offen offset:0 sc0 sc1
Mv 52,28,48
Rt 14,15,512
s_waitcnt lgkmcnt(8)
s_barrier
Mv 32,32,56
Rt 16,12,4096
Mv 40,32,60
Rt 18,12,4608
)" R"(
s_mov_b32 s83, 0x5508000
buffer_load_dword v4, v7, s[20:23], s83 offen offset:4 sc0 sc1
Mv 44,36,60
Rt 20,13,4096
s_add_u32 m0, s36, 131072
Mv 36,36,56
Rt 22,13,4608
buffer_load_dwordx4 v2, s[20:23], s56 offen offset:0 sc1 lds
Mv 48,40,56
Rt 24,14,4096
buffer_load_dwordx4 a[100:103], v2, s[20:23], s51 offen offset:1024 sc0 sc1
Mv 56,40,60
Rt 26,14,4608
Mv 60,44,60
Rt 28,15,4096
Mv 52,44,56
Rt 30,15,4608
s_waitcnt lgkmcnt(8)
Mz 96,0,192
Rt 32,12,8192
Mz 104,0,196
Rt 34,12,8704
buffer_load_dwordx4 v2, s[20:23], s56 offen offset:1024 sc1 lds
Mz 108,4,196
Rt 36,13,8192
buffer_load_dwordx4 a[104:107], v2, s[20:23], s51 offen offset:2048 sc0 sc1
Mz 100,4,192
Rt 38,13,8704
Mz 112,8,192
Rt 40,14,8192
v_mul_f32_e32 v32, s54, v32
Mz 120,8,196
Rt 42,14,8704
v_mul_f32_e32 v33, s54, v33
Mz 124,12,196
Rt 44,15,8192
Cv 32,32,33
Mz 116,12,192
Rt 46,15,8704
buffer_load_dwordx4 v2, s[20:23], s56 offen offset:2048 sc1 lds
s_waitcnt lgkmcnt(8)
Mv 96,16,200
Rt 0,12,12288
buffer_load_dwordx4 a[108:111], v2, s[20:23], s51 offen offset:3072 sc0 sc1
Mv 104,16,204
Rt 2,12,12800
v_accvgpr_write_b32 a192, v32
Mv 108,20,204
Rt 4,13,12288
v_mul_f32_e32 v34, s54, v34
Mv 100,20,200
Rt 6,13,12800
v_mul_f32_e32 v35, s54, v35
Mv 112,24,200
Rt 8,14,12288
Cv 34,34,35
Mv 120,24,204
Rt 10,14,12800
buffer_load_dwordx4 v2, s[20:23], s56 offen offset:3072 sc1 lds
Mv 124,28,204
Rt 12,15,12288
buffer_load_dwordx4 a[112:115], v3, s[20:23], s51 offen offset:0 sc0 sc1
Mv 116,28,200
Rt 14,15,12800
)" R"(
s_waitcnt lgkmcnt(8)
Mv 96,32,208
Rt 16,12,16384
v_accvgpr_write_b32 a193, v34
Mv 104,32,212
Rt 18,12,16896
v_mul_f32_e32 v36, s54, v36
Mv 108,36,212
Rt 20,13,16384
s_add_u32 m0, s36, 135168
Mv 100,36,208
Rt 22,13,16896
buffer_load_dwordx4 v3, s[20:23], s56 offen offset:0 sc1 lds
Mv 112,40,208
Rt 24,14,16384
buffer_load_dwordx4 a[116:119], v3, s[20:23], s51 offen offset:1024 sc0 sc1
Mv 120,40,212
Rt 26,14,16896
v_mul_f32_e32 v37, s54, v37
Mv 124,44,212
Rt 28,15,16384
Cv 36,36,37
Mv 116,44,208
Rt 30,15,16896
s_waitcnt lgkmcnt(8)
Mv 96,0,216
Rt 32,12,20480
v_accvgpr_write_b32 a194, v36
Mv 104,0,220
Rt 34,12,20992
buffer_load_dwordx4 v3, s[20:23], s56 offen offset:1024 sc1 lds
Mv 108,4,220
Rt 36,13,20480
buffer_load_dwordx4 a[120:123], v3, s[20:23], s51 offen offset:2048 sc0 sc1
Mv 100,4,216
Rt 38,13,20992
v_mul_f32_e32 v38, s54, v38
Mv 112,8,216
Rt 40,14,20480
v_mul_f32_e32 v39, s54, v39
Mv 120,8,220
Rt 42,14,20992
Cv 38,38,39
Mv 124,12,220
Rt 44,15,20480
v_accvgpr_write_b32 a195, v38
Mv 116,12,216
Rt 46,15,20992
buffer_load_dwordx4 v3, s[20:23], s56 offen offset:2048 sc1 lds
s_waitcnt lgkmcnt(8)
Mv 96,16,128
Rt 0,12,24576
buffer_load_dwordx4 a[124:127], v3, s[20:23], s51 offen offset:3072 sc0 sc1
Mv 104,16,132
Rt 2,12,25088
v_mul_f32_e32 v40, s54, v40
Mv 108,20,132
Rt 4,13,24576
v_mul_f32_e32 v41, s54, v41
Mv 100,20,128
Rt 6,13,25088
Cv 40,40,41
Mv 112,24,128
Rt 8,14,24576
)" R"(
v_accvgpr_write_b32 a196, v40
Mv 120,24,132
Rt 10,14,25088
buffer_load_dwordx4 v3, s[20:23], s56 offen offset:3072 sc1 lds
Mv 124,28,132
Rt 12,15,24576
v_mul_f32_e32 v42, s54, v42
Mv 116,28,128
Rt 14,15,25088
s_waitcnt lgkmcnt(8)
Mv 96,32,136
Rt 16,16,0
v_mul_f32_e32 v43, s54, v43
Mv 104,32,140
Rt 18,16,512
Cv 42,42,43
Mv 108,36,140
Rt 20,17,0
v_accvgpr_write_b32 a197, v42
Mv 100,36,136
Rt 22,17,512
v_mul_f32_e32 v44, s54, v44
Mv 112,40,136
Rt 24,18,0
v_mul_f32_e32 v45, s54, v45
Mv 120,40,140
Rt 26,18,512
Cv 44,44,45
Mv 124,44,140
Rt 28,19,0
v_accvgpr_write_b32 a198, v44
Mv 116,44,136
Rt 30,19,512
s_waitcnt vmcnt(15) lgkmcnt(8)
s_barrier
buffer_store_dword v6, v7, s[20:23], s49 offen offset:12 sc0 sc1
Mv 96,0,144
Rt 32,16,4096
v_cmp_eq_u32_e32 vcc, s3, v4
s_andn2_b64 s[28:29], 1, vcc
s_cbranch_scc1 .Lzsp4
.Lzok4:
Mv 104,0,148
Rt 34,16,4608
buffer_load_dword v4, v5, s[20:23], s50 offen offset:0 sc0 sc1
Mv 108,4,148
Rt 36,17,4096
s_lshl_b64 s[60:61], s[60:61], 8
v_add_u32 v8, 0x10000, v1
Mv 100,4,144
Rt 38,17,4608
s_or_b32 s60, s60, 0xff
v_add_u32 v10, 0x20000, v1
Mv 112,8,144
Rt 40,18,4096
v_mul_f32_e32 v46, s54, v46
v_lshrrev_b32 v9, 4, v0
Mv 120,8,148
Rt 42,18,4608
v_mul_f32_e32 v47, s54, v47
v_mul_i32_i24 v9, -20, v9
Mv 124,12,148
Rt 44,19,4096
Cv 46,46,47
v_add_u32 v9, v9, v0
)" R"(
s_lshl_b32 s28, s33, 5
Mv 116,12,144
Rt 46,19,4608
v_add_u32 v9, s28, v9
s_waitcnt lgkmcnt(8)
Mv 96,16,152
Rt 0,16,8192
v_accvgpr_write_b32 a199, v46
s_add_u32 s56, s56, 0x8000
Mv 104,16,156
Rt 2,16,8704
v_mul_f32_e32 v48, s54, v48
s_add_u32 s55, s55, 0x8000
Mv 108,20,156
Rt 4,17,8192
v_mul_f32_e32 v49, s54, v49
s_add_u32 s55, s55, 0x8000
Mv 100,20,152
Rt 6,17,8704
Cv 48,48,49
s_cmp_eq_u32 s57, 2
s_cselect_b32 s55, s59, s55
Mv 112,24,152
Rt 8,18,8192
v_accvgpr_write_b32 a200, v48
s_mov_b32 s93, 0
Mv 120,24,156
Rt 10,18,8704
v_mul_f32_e32 v50, s54, v50
s_mov_b32 s94, 1
Mv 124,28,156
Rt 12,19,8192
v_mul_f32_e32 v51, s54, v51
Mv 116,28,152
Rt 14,19,8704
s_waitcnt lgkmcnt(8)
Mv 96,32,160
Rt 16,16,12288
Cv 50,50,51
Mv 104,32,164
Rt 18,16,12800
v_accvgpr_write_b32 a201, v50
Mv 108,36,164
Rt 20,17,12288
v_mul_f32_e32 v52, s54, v52
Mv 100,36,160
Rt 22,17,12800
v_mul_f32_e32 v53, s54, v53
Mv 112,40,160
Rt 24,18,12288
Cv 52,52,53
Mv 120,40,164
Rt 26,18,12800
v_accvgpr_write_b32 a202, v52
Mv 124,44,164
Rt 28,19,12288
v_mul_f32_e32 v54, s54, v54
Mv 116,44,160
Rt 30,19,12800
s_waitcnt lgkmcnt(8)
Mv 96,0,168
Rt 32,16,16384
v_mul_f32_e32 v55, s54, v55
Mv 104,0,172
)" R"(
Rt 34,16,16896
Cv 54,54,55
Mv 108,4,172
Rt 36,17,16384
v_accvgpr_write_b32 a203, v54
Mv 100,4,168
Rt 38,17,16896
v_mul_f32_e32 v56, s54, v56
Mv 112,8,168
Rt 40,18,16384
v_mul_f32_e32 v57, s54, v57
Mv 120,8,172
Rt 42,18,16896
Cv 56,56,57
Mv 124,12,172
Rt 44,19,16384
v_accvgpr_write_b32 a204, v56
Mv 116,12,168
Rt 46,19,16896
s_waitcnt lgkmcnt(8)
Mv 96,16,176
Rt 0,16,20480
v_mul_f32_e32 v58, s54, v58
Mv 104,16,180
Rt 2,16,20992
v_mul_f32_e32 v59, s54, v59
Mv 108,20,180
Rt 4,17,20480
Cv 58,58,59
Mv 100,20,176
Rt 6,17,20992
v_accvgpr_write_b32 a205, v58
Mv 112,24,176
Rt 8,18,20480
v_mul_f32_e32 v60, s54, v60
Mv 120,24,180
Rt 10,18,20992
v_mul_f32_e32 v61, s54, v61
Mv 124,28,180
Rt 12,19,20480
Cv 60,60,61
Mv 116,28,176
Rt 14,19,20992
s_waitcnt vmcnt(0) lgkmcnt(8)
s_barrier
Mv 96,32,184
Rt 16,16,24576
v_cmp_eq_u32_e32 vcc, s3, v4
s_andn2_b64 s[28:29], s[60:61], vcc
s_cbranch_scc1 .Lzsp3
.Lzok3:
Mv 104,32,188
Rt 18,16,25088
v_mov_b32 v24, 0
buffer_load_dword v4, v24, s[20:23], s83 offen sc0 sc1
Mv 108,36,188
Rt 20,17,24576
v_accvgpr_write_b32 a206, v60
Mv 100,36,184
Rt 22,17,25088
v_mul_f32_e32 v62, s54, v62
Mv 112,40,184
Rt 24,18,24576
v_mul_f32_e32 v63, s54, v63
Mv 120,40,188
)" R"(
Rt 26,18,25088
Cv 62,62,63
Mv 124,44,188
Rt 28,19,24576
v_accvgpr_write_b32 a207, v62
Mv 116,44,184
Rt 30,19,25088
s_waitcnt lgkmcnt(8)
Mv 96,0,48
Mv 104,0,52
Rb 0,8,32768
Mv 108,4,52
Mv 100,4,48
Rb 4,8,33792
Mv 112,8,48
Mv 120,8,52
Rb 8,8,34816
Mv 124,12,52
Mv 116,12,48
Rb 12,8,35840
s_waitcnt lgkmcnt(4)
Mv 96,16,56
Mv 104,16,60
Rb 16,8,40960
Mv 108,20,60
Mv 100,20,56
Rb 20,8,41984
Mv 112,24,56
Mv 120,24,60
Rb 24,8,43008
Mv 124,28,60
Mv 116,28,56
Rb 28,8,44032
Rb 32,8,36864
Rb 36,8,37888
Rb 40,8,38912
Rb 44,8,39936
s_cmp_eq_u32 s35, 7
s_cbranch_scc1 .LzA7
.LzA7R:
s_waitcnt lgkmcnt(8)
Mz 64,0,64
Rb 48,8,45056
v_mul_f32_e32 v96, s54, v96
Mz 68,0,68
v_mul_f32_e32 v97, s54, v97
Cv 96,96,97
v_accvgpr_write_b32 a208, v96
Mv 68,4,76
Rb 52,8,46080
s_add_u32 m0, s36, 0
Mv 64,4,72
buffer_load_dwordx4 v2, s[20:23], s55 offen offset:0 sc1 lds
v_mul_f32_e32 v98, s54, v98
Mv 64,8,80
Rb 56,8,47104
v_mul_f32_e32 v99, s54, v99
Mv 68,8,84
Cv 98,98,99
v_accvgpr_write_b32 a209, v98
v_mul_f32_e32 v100, s54, v100
Mv 68,12,92
Rb 60,8,48128
s_waitcnt lgkmcnt(8)
)" R"(
Mv 64,12,88
v_mul_f32_e32 v101, s54, v101
Cv 100,100,101
v_accvgpr_write_b32 a210, v100
Mv 64,16,96
Rb 0,8,49152
v_mul_f32_e32 v102, s54, v102
v_mul_f32_e32 v103, s54, v103
Mv 68,16,100
Cv 102,102,103
v_accvgpr_write_b32 a211, v102
Mv 68,20,108
Rb 4,8,50176
v_mul_f32_e32 v104, s54, v104
v_mul_f32_e32 v105, s54, v105
Mv 64,20,104
buffer_load_dwordx4 v2, s[20:23], s55 offen offset:1024 sc1 lds
Mv 64,24,112
Rb 8,8,51200
Cv 104,104,105
v_accvgpr_write_b32 a212, v104
Mv 68,24,116
v_mul_f32_e32 v106, s54, v106
v_mul_f32_e32 v107, s54, v107
Mv 68,28,124
Rb 12,8,52224
Cv 106,106,107
s_waitcnt lgkmcnt(8)
Mv 64,28,120
v_accvgpr_write_b32 a213, v106
v_mul_f32_e32 v108, s54, v108
v_mul_f32_e32 v109, s54, v109
Mz 72,32,64
Rb 16,8,57344
Cv 108,108,109
Mz 76,32,68
v_accvgpr_write_b32 a214, v108
v_mul_f32_e32 v110, s54, v110
v_mul_f32_e32 v111, s54, v111
Mv 76,36,76
Rb 20,8,58368
Cv 110,110,111
Mv 72,36,72
buffer_load_dwordx4 v2, s[20:23], s55 offen offset:2048 sc1 lds
v_accvgpr_write_b32 a215, v110
Mv 72,40,80
Rb 24,8,59392
v_mul_f32_e32 v112, s54, v112
Mv 76,40,84
v_mul_f32_e32 v113, s54, v113
Cv 112,112,113
v_accvgpr_write_b32 a216, v112
Mv 76,44,92
Rb 28,8,60416
s_waitcnt vmcnt(3) lgkmcnt(8)
Mv 72,44,88
v_mul_f32_e32 v114, s54, v114
v_mul_f32_e32 v115, s54, v115
Cv 114,114,115
Mv 72,48,96
Rb 32,8,53248
v_accvgpr_write_b32 a217, v114
v_mul_f32_e32 v116, s54, v116
Mv 76,48,100
)" R"(
v_mul_f32_e32 v117, s54, v117
Cv 116,116,117
Mv 76,52,108
Rb 36,8,54272
v_accvgpr_write_b32 a218, v116
v_mul_f32_e32 v118, s54, v118
Mv 72,52,104
buffer_load_dwordx4 v2, s[20:23], s55 offen offset:3072 sc1 lds
Mv 72,56,112
Rb 40,8,55296
v_mul_f32_e32 v119, s54, v119
Cv 118,118,119
Mv 76,56,116
v_accvgpr_write_b32 a219, v118
v_mul_f32_e32 v120, s54, v120
Mv 76,60,124
Rb 44,8,56320
v_mul_f32_e32 v121, s54, v121
s_waitcnt lgkmcnt(8)
Mv 72,60,120
Cv 120,120,121
v_accvgpr_write_b32 a220, v120
v_mul_f32_e32 v122, s54, v122
Mz 80,0,64
Rb 48,8,61440
v_mul_f32_e32 v123, s54, v123
Mz 84,0,68
Cv 122,122,123
v_accvgpr_write_b32 a221, v122
v_mul_f32_e32 v124, s54, v124
Mv 84,4,76
Rb 52,8,62464
s_add_u32 m0, s36, 4096
Mv 80,4,72
buffer_load_dwordx4 v3, s[20:23], s55 offen offset:0 sc1 lds
v_mul_f32_e32 v125, s54, v125
Mv 80,8,80
Rb 56,8,63488
Cv 124,124,125
Mv 84,8,84
v_accvgpr_write_b32 a222, v124
v_mul_f32_e32 v126, s54, v126
v_mul_f32_e32 v127, s54, v127
Mv 84,12,92
Rb 60,8,64512
s_waitcnt lgkmcnt(8)
Mv 80,12,88
Cv 126,126,127
v_accvgpr_write_b32 a223, v126
v_mov_b32 v16, 0
Mv 80,16,96
v_mov_b32 v17, 0
v_mov_b32 v18, 0
v_mov_b32 v19, 0
Mv 84,16,100
v_mov_b32 v20, 0
v_mov_b32 v21, 0
Mv 84,20,108
v_mov_b32 v22, 0
v_mov_b32 v23, 0
v_readfirstlane_b32 s84, v4
Mv 80,20,104
buffer_load_dwordx4 v3, s[20:23], s55 offen offset:1024 sc1 lds
Mv 80,24,112
)" R"(
v_and_b32 v24, 16, v1
v_lshrrev_b32 v24, v24, v4
v_and_b32 v24, 0x7ff, v24
Mv 84,24,116
v_mul_u32_u24 v24, 896, v24
v_and_b32 v25, 14, v0
Mv 84,28,124
v_min_u32 v25, 1, v25
v_lshlrev_b32 v25, 30, v25
v_and_or_b32 v25, v0, 48, v25
Mv 80,28,120
v_add_u32 v24, v24, v25
s_waitcnt lgkmcnt(0)
s_barrier
Mz 88,32,64
Rb 0,1,32768
s_and_b32 s83, s33, 1
Mz 92,32,68
Rb 4,1,33792
s_mul_i32 s82, s83, 448
Mv 92,36,76
Rb 8,1,34816
Mv 88,36,72
Rb 12,1,35840
buffer_load_dwordx4 v3, s[20:23], s55 offen offset:2048 sc1 lds
Mv 88,40,80
Rb 16,1,40960
Mv 92,40,84
Rb 20,1,41984
buffer_load_dwordx4 a[128:131], v24, s[12:15], s82 offen offset:0
Mv 92,44,92
Rb 24,1,43008
Mv 88,44,88
Rb 28,1,44032
buffer_load_dwordx4 a[132:135], v24, s[12:15], s82 offen offset:64
Mv 88,48,96
Rb 32,1,36864
buffer_load_dwordx4 a[136:139], v24, s[12:15], s82 offen offset:128
Mv 92,48,100
Rb 36,1,37888
Mv 92,52,108
Rb 40,1,38912
buffer_load_dwordx4 a[140:143], v24, s[12:15], s82 offen offset:192
Mv 88,52,104
Rb 44,1,39936
buffer_load_dwordx4 v3, s[20:23], s55 offen offset:3072 sc1 lds
Mv 88,56,112
buffer_load_dwordx4 a[144:147], v24, s[12:15], s82 offen offset:256
Mv 92,56,116
buffer_load_dwordx4 a[148:151], v24, s[12:15], s82 offen offset:320
Mv 92,60,124
buffer_load_dwordx4 a[152:155], v24, s[12:15], s82 offen offset:384
s_waitcnt lgkmcnt(8)
Mv 88,60,120
Mz 96,0,64
Rb 48,1,45056
buffer_load_dword v4, v5, s[20:23], s50 offen offset:0 sc0 sc1
Mz 100,0,68
s_lshl_b64 s[60:61], s[60:61], 8
s_or_b32 s60, s60, 0xff
s_cmp_le_i32 s57, s58
s_cbranch_scc1 .Lzmk0
.Lzmr0:
Mv 100,4,76
Rb 52,1,46080
)" R"(
s_add_u32 m0, s36, 65536
s_sub_u32 s57, s57, 1
Mv 96,4,72
buffer_load_dwordx4 v2, s[20:23], s56 offen offset:0 sc1 lds
s_add_u32 s55, s55, 0x8000
Mv 96,8,80
Rb 56,1,47104
s_cmp_eq_u32 s57, 2
s_cselect_b32 s55, s59, s55
Mv 100,8,84
v_exp_f32_e32 v32, v64
Mv 100,12,92
Rb 60,1,48128
s_waitcnt lgkmcnt(8)
Mv 96,12,88
v_exp_f32_e32 v24, v65
Mv 96,16,96
Rb 0,1,49152
v_exp_f32_e32 v33, v66
Mv 100,16,100
v_exp_f32_e32 v25, v67
v_exp_f32_e32 v34, v72
Mv 100,20,108
Rb 4,1,50176
v_exp_f32_e32 v26, v73
Mv 96,20,104
buffer_load_dwordx4 v2, s[20:23], s56 offen offset:1024 sc1 lds
v_add_f32_e32 v14, v32, v24
Mv 96,24,112
Rb 8,1,51200
v_exp_f32_e32 v35, v74
Mv 100,24,116
v_exp_f32_e32 v27, v75
Cv 32,32,24
Mv 100,28,124
Rb 12,1,52224
v_add_f32_e32 v14, v14, v33
s_waitcnt lgkmcnt(8)
Mv 96,28,120
v_exp_f32_e32 v36, v68
v_add_f32_e32 v14, v14, v25
Mz 104,32,64
Rb 16,1,57344
v_exp_f32_e32 v28, v69
Mz 108,32,68
v_exp_f32_e32 v37, v70
v_exp_f32_e32 v29, v71
Mv 108,36,76
Rb 20,1,58368
v_exp_f32_e32 v38, v76
Mv 104,36,72
buffer_load_dwordx4 v2, s[20:23], s56 offen offset:2048 sc1 lds
Cv 33,33,25
Mv 104,40,80
Rb 24,1,59392
v_exp_f32_e32 v24, v77
Mv 108,40,84
v_exp_f32_e32 v39, v78
v_add_f32_e32 v14, v14, v34
Mv 108,44,92
Rb 28,1,60416
v_add_f32_e32 v14, v14, v26
s_waitcnt lgkmcnt(8)
Mv 104,44,88
)" R"(
v_exp_f32_e32 v25, v79
Cv 34,34,26
Mv 104,48,96
Rb 32,1,53248
v_exp_f32_e32 v40, v80
Mv 108,48,100
v_exp_f32_e32 v26, v81
v_add_f32_e32 v14, v14, v35
v_add_f32_e32 v14, v14, v27
Mv 108,52,108
Rb 36,1,54272
Cv 35,35,27
v_add_f32_e32 v15, v36, v28
Mv 104,52,104
buffer_load_dwordx4 v2, s[20:23], s56 offen offset:3072 sc1 lds
Cv 36,36,28
Mv 104,56,112
Rb 40,1,55296
v_exp_f32_e32 v41, v82
Mv 108,56,116
v_exp_f32_e32 v27, v83
v_add_f32_e32 v15, v15, v37
Mv 108,60,124
Rb 44,1,56320
v_add_f32_e32 v15, v15, v29
s_waitcnt lgkmcnt(8)
Mv 104,60,120
v_exp_f32_e32 v42, v88
Cv 37,37,29
Mz 112,0,64
Rb 48,1,61440
v_exp_f32_e32 v28, v89
Mz 116,0,68
v_exp_f32_e32 v43, v90
v_add_f32_e32 v15, v15, v38
v_add_f32_e32 v15, v15, v24
Mv 116,4,76
Rb 52,1,62464
s_add_u32 m0, s36, 69632
Cv 38,38,24
Mv 112,4,72
buffer_load_dwordx4 v3, s[20:23], s56 offen offset:0 sc1 lds
v_add_f32_e32 v15, v15, v39
Mv 112,8,80
Rb 56,1,63488
v_exp_f32_e32 v29, v91
Mv 116,8,84
v_exp_f32_e32 v44, v84
v_add_f32_e32 v15, v15, v25
Mv 116,12,92
Rb 60,1,64512
Cv 39,39,25
s_waitcnt lgkmcnt(8)
Mv 112,12,88
v_exp_f32_e32 v24, v85
v_add_f32_e32 v14, v14, v40
Mv 112,16,96
v_exp_f32_e32 v45, v86
v_add_f32_e32 v14, v14, v26
Mv 116,16,100
v_exp_f32_e32 v25, v87
Cv 40,40,26
v_add_f32_e32 v14, v14, v41
Mv 116,20,108
)" R"(
v_exp_f32_e32 v46, v92
v_add_f32_e32 v14, v14, v27
Mv 112,20,104
buffer_load_dwordx4 v3, s[20:23], s56 offen offset:1024 sc1 lds
Cv 41,41,27
Mv 112,24,112
v_exp_f32_e32 v26, v93
v_add_f32_e32 v14, v14, v42
Mv 116,24,116
v_exp_f32_e32 v47, v94
v_add_f32_e32 v14, v14, v28
Mv 116,28,124
v_exp_f32_e32 v27, v95
Cv 42,42,28
Mv 112,28,120
v_add_f32_e32 v14, v14, v43
s_waitcnt lgkmcnt(0)
s_barrier
Mz 120,32,64
Rb 0,10,0
v_add_f32_e32 v14, v14, v29
Cv 43,43,29
Mz 124,32,68
Rb 4,10,1024
v_add_f32_e32 v15, v15, v44
v_add_f32_e32 v15, v15, v24
Cv 44,44,24
Mv 124,36,76
Rb 8,10,2048
v_add_f32_e32 v15, v15, v45
v_add_f32_e32 v15, v15, v25
Mv 120,36,72
Rb 12,10,3072
buffer_load_dwordx4 v3, s[20:23], s56 offen offset:2048 sc1 lds
Mv 120,40,80
Rb 16,10,4096
Cv 45,45,25
v_add_f32_e32 v15, v15, v46
Mv 124,40,84
Rb 20,10,5120
v_add_f32_e32 v15, v15, v26
Cv 46,46,26
Mv 124,44,92
Rb 24,10,6144
v_add_f32_e32 v15, v15, v47
v_add_f32_e32 v15, v15, v27
Mv 120,44,88
Rb 28,10,7168
Cv 47,47,27
v_max_f32_e32 v11, v14, v15
Mv 120,48,96
Rb 32,10,8192
v_mov_b32 v24, v14
v_mov_b32 v25, v15
Mv 124,48,100
Rb 36,10,9216
v_permlane32_swap_b32_e32 v24, v25
v_add_f32_e32 v24, v24, v25
v_mov_b32 v25, v24
Mv 124,52,108
Rb 40,10,10240
v_permlane16_swap_b32_e32 v24, v25
v_add_f32_e32 v24, v24, v25
Mv 120,52,104
Rb 44,10,11264
)" R"(
buffer_load_dwordx4 v3, s[20:23], s56 offen offset:3072 sc1 lds
Mv 120,56,112
v_rcp_f32_e32 v24, v24
Mv 124,56,116
s_add_u32 s56, s56, 0x8000
v_max_f32_e32 v11, v11, v24
v_cmp_nge_f32_e32 vcc, s62, v11
Mv 124,60,124
s_waitcnt lgkmcnt(8)
s_cbranch_vccnz .Lzfx0
.Lzfr0:
Mv 120,60,120
v_mov_b32 v12, v14
v_mov_b32 v13, v15
v_mfma_f32_16x16x32_bf16 v[128:131], a[0:3], v[32:35], 0
Rb 48,10,12288
v_mfma_f32_16x16x32_bf16 v[136:139], a[0:3], v[36:39], 0
s_cmp_le_i32 s57, s58
s_cbranch_scc1 .Lzmk1
.Lzmr1:
v_mfma_f32_16x16x32_bf16 v[140:143], a[4:7], v[36:39], 0
Rb 52,10,13312
s_add_u32 m0, s36, 32768
v_mfma_f32_16x16x32_bf16 v[132:135], a[4:7], v[32:35], 0
buffer_load_dwordx4 v2, s[20:23], s55 offen offset:0 sc1 lds
v_mfma_f32_16x16x32_bf16 v[144:147], a[8:11], v[32:35], 0
Rb 56,10,14336
s_and_b32 s92, s2, 7
v_mfma_f32_16x16x32_bf16 v[152:155], a[8:11], v[36:39], 0
s_mul_i32 s92, s92, 14
s_lshr_b32 s82, s2, 3
v_mfma_f32_16x16x32_bf16 v[156:159], a[12:15], v[36:39], 0
Rb 60,10,15360
s_waitcnt lgkmcnt(8)
v_mfma_f32_16x16x32_bf16 v[148:151], a[12:15], v[32:35], 0
buffer_load_dwordx4 v2, s[20:23], s55 offen offset:1024 sc1 lds
v_mfma_f32_16x16x32_bf16 v[160:163], a[16:19], v[32:35], 0
Rb 0,10,16384
s_add_u32 s82, s82, s92
v_mfma_f32_16x16x32_bf16 v[168:171], a[16:19], v[36:39], 0
v_exp_f32_e32 v48, v96
v_mfma_f32_16x16x32_bf16 v[172:175], a[20:23], v[36:39], 0
Rb 4,10,17408
v_mfma_f32_16x16x32_bf16 v[164:167], a[20:23], v[32:35], 0
buffer_load_dwordx4 v2, s[20:23], s55 offen offset:2048 sc1 lds
v_mfma_f32_16x16x32_bf16 v[176:179], a[24:27], v[32:35], 0
Rb 8,10,18432
v_exp_f32_e32 v28, v97
v_mfma_f32_16x16x32_bf16 v[184:187], a[24:27], v[36:39], 0
v_exp_f32_e32 v49, v98
v_mfma_f32_16x16x32_bf16 v[188:191], a[28:31], v[36:39], 0
Rb 12,10,19456
s_waitcnt lgkmcnt(8)
v_mfma_f32_16x16x32_bf16 v[180:183], a[28:31], v[32:35], 0
buffer_load_dwordx4 v2, s[20:23], s55 offen offset:3072 sc1 lds
v_add_f32_e32 v14, v48, v28
v_mfma_f32_16x16x32_bf16 v[192:195], a[32:35], v[32:35], 0
Rb 16,10,20480
Cv 48,48,28
v_mfma_f32_16x16x32_bf16 v[200:203], a[32:35], v[36:39], 0
v_exp_f32_e32 v29, v99
v_add_f32_e32 v14, v14, v49
v_mfma_f32_16x16x32_bf16 v[204:207], a[36:39], v[36:39], 0
Rb 20,10,21504
)" R"(
s_add_u32 m0, s36, 36864
v_mfma_f32_16x16x32_bf16 v[196:199], a[36:39], v[32:35], 0
buffer_load_dwordx4 v3, s[20:23], s55 offen offset:0 sc1 lds
v_mfma_f32_16x16x32_bf16 v[208:211], a[40:43], v[32:35], 0
Rb 24,10,22528
v_exp_f32_e32 v50, v104
v_mfma_f32_16x16x32_bf16 v[216:219], a[40:43], v[36:39], 0
s_cmp_gt_u32 s33, 1
s_cselect_b32 s24, s78, s76
s_cselect_b32 s25, s79, s77
v_mfma_f32_16x16x32_bf16 v[220:223], a[44:47], v[36:39], 0
Rb 28,10,23552
s_waitcnt lgkmcnt(8)
v_mfma_f32_16x16x32_bf16 v[212:215], a[44:47], v[32:35], 0
buffer_load_dwordx4 v3, s[20:23], s55 offen offset:1024 sc1 lds
v_add_f32_e32 v14, v14, v29
v_mfma_f32_16x16x32_bf16 v[224:227], a[48:51], v[32:35], 0
Rb 32,10,24576
s_mov_b32 s27, 0x20000
v_mfma_f32_16x16x32_bf16 v[232:235], a[48:51], v[36:39], 0
s_cmp_lt_u32 s35, 7
s_cselect_b32 s26, 0x188000, 0
Cv 49,49,29
v_mfma_f32_16x16x32_bf16 v[236:239], a[52:55], v[36:39], 0
Rb 36,10,25600
s_mul_i32 s83, s83, 802816
v_mfma_f32_16x16x32_bf16 v[228:231], a[52:55], v[32:35], 0
buffer_load_dwordx4 v3, s[20:23], s55 offen offset:2048 sc1 lds
v_mfma_f32_16x16x32_bf16 v[240:243], a[56:59], v[32:35], 0
Rb 40,10,26624
s_lshl_b32 s92, s82, 5
v_add_f32_e32 v14, v14, v50
v_mfma_f32_16x16x32_bf16 v[248:251], a[56:59], v[36:39], 0
s_add_u32 s83, s83, s92
v_and_b32 v11, 62, v0
v_mul_u32_u24 v11, 0x6f0, v11
v_mfma_f32_16x16x32_bf16 v[252:255], a[60:63], v[36:39], 0
Rb 44,10,27648
s_waitcnt lgkmcnt(8)
v_mfma_f32_16x16x32_bf16 v[244:247], a[60:63], v[32:35], 0
buffer_load_dwordx4 v3, s[20:23], s55 offen offset:3072 sc1 lds
Mp 128,0,40
Rb 48,10,28672
v_add_u32 v11, v11, v1
Mp 136,0,44
v_exp_f32_e32 v24, v105
Mp 140,4,44
Rb 52,10,29696
v_add_f32_e32 v14, v14, v24
Mp 132,4,40
v_exp_f32_e32 v51, v106
Mp 144,8,40
Rb 56,10,30720
v_exp_f32_e32 v25, v107
Mp 152,8,44
v_exp_f32_e32 v52, v100
Mp 156,12,44
Rb 60,10,31744
s_waitcnt lgkmcnt(8)
Mp 148,12,40
v_exp_f32_e32 v26, v101
Cv 50,50,24
Mp 160,16,40
v_exp_f32_e32 v53, v102
)" R"(
Mp 168,16,44
v_exp_f32_e32 v27, v103
v_add_f32_e32 v14, v14, v51
Mp 172,20,44
v_exp_f32_e32 v54, v108
Mp 164,20,40
v_exp_f32_e32 v28, v109
Mp 176,24,40
v_exp_f32_e32 v55, v110
v_add_f32_e32 v14, v14, v25
Mp 184,24,44
v_exp_f32_e32 v29, v111
Mp 188,28,44
Cv 51,51,25
v_add_f32_e32 v15, v52, v26
Mp 180,28,40
Cv 52,52,26
s_waitcnt vmcnt(16) lgkmcnt(0)
s_barrier
Mp 192,32,40
Rb 0,1,0
s_add_u32 m0, s36, 131072
s_nop 0
buffer_load_dwordx4 v11, s[24:27], s83 offen offset:0 lds
Mp 200,32,44
Rb 4,1,1024
v_add_f32_e32 v15, v15, v53
v_add_f32_e32 v15, v15, v27
Mp 204,36,44
Rb 8,1,2048
s_add_u32 s83, s83, 0x1bc00
buffer_load_dwordx4 v11, s[24:27], s83 offen offset:1024 lds
Mp 196,36,40
Rb 12,1,3072
Cv 53,53,27
Mp 208,40,40
Rb 16,1,8192
s_add_u32 s83, s83, 0x1bc00
buffer_load_dwordx4 v11, s[24:27], s83 offen offset:2048 lds
Mp 216,40,44
Rb 20,1,9216
v_add_f32_e32 v15, v15, v54
Mp 220,44,44
Rb 24,1,10240
s_add_u32 s83, s83, 0x1bc00
buffer_load_dwordx4 v11, s[24:27], s83 offen offset:3072 lds
Mp 212,44,40
Rb 28,1,11264
v_add_f32_e32 v15, v15, v28
Cv 54,54,28
Mp 224,48,40
Rb 32,1,4096
s_add_u32 m0, s36, 135168
s_add_u32 s83, s83, 0x1cc00
buffer_load_dwordx4 v11, s[24:27], s83 offen offset:0 lds
Mp 232,48,44
Rb 36,1,5120
v_add_f32_e32 v15, v15, v55
v_add_f32_e32 v15, v15, v29
Mp 236,52,44
Rb 40,1,6144
s_add_u32 s83, s83, 0x1bc00
buffer_load_dwordx4 v11, s[24:27], s83 offen offset:1024 lds
Mp 228,52,40
Rb 44,1,7168
)" R"(
Cv 55,55,29
Mp 240,56,40
s_add_u32 s83, s83, 0x1bc00
buffer_load_dwordx4 v11, s[24:27], s83 offen offset:2048 lds
v_cmp_eq_u32_e32 vcc, s3, v4
s_andn2_b64 s[28:29], s[60:61], vcc
s_cbranch_scc1 .Lzsp5
.Lzok5:
Mp 248,56,44
s_sub_u32 s57, s57, 1
Mp 252,60,44
s_waitcnt lgkmcnt(8)
Mp 244,60,40
s_cmp_gt_i32 s57, s93
s_cbranch_scc0 .LzLX
.LzL:
v_mfma_f32_16x16x32_bf16 v[64:67], a[0:3], a[64:67], v[16:19]
Rb 48,1,12288
v_mfma_f32_16x16x32_bf16 v[68:71], a[0:3], a[68:71], v[20:23]
v_exp_f32_e32 v56, v112
Mv 68,4,76
Rb 52,1,13312
s_add_u32 m0, s36, 98304
Mv 64,4,72
buffer_load_dwordx4 v2, s[20:23], s56 offen offset:0 sc1 lds
Mv 64,8,80
Rb 56,1,14336
v_add_f32_e32 v14, v14, v56
Mv 68,8,84
v_exp_f32_e32 v24, v113
Mv 68,12,92
Rb 60,1,15360
s_waitcnt lgkmcnt(8)
Mv 64,12,88
v_exp_f32_e32 v57, v114
Mv 64,16,96
Rb 0,1,16384
v_add_f32_e32 v14, v14, v24
Mv 68,16,100
v_exp_f32_e32 v25, v115
Mv 68,20,108
Rb 4,1,17408
Cv 56,56,24
Mv 64,20,104
buffer_load_dwordx4 v2, s[20:23], s56 offen offset:1024 sc1 lds
Mv 64,24,112
Rb 8,1,18432
v_add_f32_e32 v14, v14, v57
Mv 68,24,116
v_exp_f32_e32 v58, v120
Mv 68,28,124
Rb 12,1,19456
s_waitcnt lgkmcnt(8)
Mv 64,28,120
v_exp_f32_e32 v26, v121
v_mfma_f32_16x16x32_bf16 v[72:75], a[32:35], a[64:67], v[16:19]
Rb 16,1,24576
v_add_f32_e32 v14, v14, v25
v_mfma_f32_16x16x32_bf16 v[76:79], a[32:35], a[68:71], v[20:23]
v_exp_f32_e32 v59, v122
Mv 76,36,76
Rb 20,1,25600
Cv 57,57,25
Mv 72,36,72
)" R"(
buffer_load_dwordx4 v2, s[20:23], s56 offen offset:2048 sc1 lds
Mv 72,40,80
Rb 24,1,26624
v_add_f32_e32 v14, v14, v58
Mv 76,40,84
v_exp_f32_e32 v27, v123
Mv 76,44,92
Rb 28,1,27648
s_waitcnt lgkmcnt(8)
Mv 72,44,88
v_exp_f32_e32 v60, v116
Mv 72,48,96
Rb 32,1,20480
v_add_f32_e32 v14, v14, v26
Mv 76,48,100
v_exp_f32_e32 v28, v117
Mv 76,52,108
Rb 36,1,21504
Cv 58,58,26
Mv 72,52,104
buffer_load_dwordx4 v2, s[20:23], s56 offen offset:3072 sc1 lds
Mv 72,56,112
Rb 40,1,22528
v_add_f32_e32 v14, v14, v59
Mv 76,56,116
v_exp_f32_e32 v61, v118
Mv 76,60,124
Rb 44,1,23552
s_waitcnt lgkmcnt(8)
Mv 72,60,120
v_exp_f32_e32 v29, v119
v_mfma_f32_16x16x32_bf16 v[80:83], a[0:3], a[64:67], v[16:19]
Rb 48,1,28672
v_add_f32_e32 v14, v14, v27
v_mfma_f32_16x16x32_bf16 v[84:87], a[0:3], a[68:71], v[20:23]
v_exp_f32_e32 v62, v124
Mv 84,4,76
Rb 52,1,29696
s_add_u32 m0, s36, 102400
Mv 80,4,72
buffer_load_dwordx4 v3, s[20:23], s56 offen offset:0 sc1 lds
Mv 80,8,80
Rb 56,1,30720
Cv 59,59,27
Mv 84,8,84
v_exp_f32_e32 v24, v125
Mv 84,12,92
Rb 60,1,31744
s_waitcnt lgkmcnt(8)
Mv 80,12,88
v_exp_f32_e32 v63, v126
Mv 80,16,96
v_exp_f32_e32 v25, v127
Mv 84,16,100
v_add_f32_e32 v15, v15, v60
v_add_f32_e32 v15, v15, v28
Mv 84,20,108
Cv 60,60,28
v_add_f32_e32 v15, v15, v61
Mv 80,20,104
buffer_load_dwordx4 v3, s[20:23], s56 offen offset:1024 sc1 lds
Mv 80,24,112
v_add_f32_e32 v15, v15, v29
Cv 61,61,29
)" R"(
Mv 84,24,116
v_add_f32_e32 v15, v15, v62
v_add_f32_e32 v15, v15, v24
Mv 84,28,124
Cv 62,62,24
v_add_f32_e32 v15, v15, v63
Mv 80,28,120
s_waitcnt vmcnt(14) lgkmcnt(0)
s_barrier
v_mfma_f32_16x16x32_bf16 v[88:91], a[32:35], a[64:67], v[16:19]
Rb 0,8,0
v_add_f32_e32 v15, v15, v25
v_mfma_f32_16x16x32_bf16 v[92:95], a[32:35], a[68:71], v[20:23]
Rb 4,8,1024
Cv 63,63,25
Mv 92,36,76
Rb 8,8,2048
v_max_f32_e32 v11, v14, v15
Mv 88,36,72
Rb 12,8,3072
buffer_load_dwordx4 v3, s[20:23], s56 offen offset:2048 sc1 lds
Mv 88,40,80
Rb 16,8,4096
v_cmp_nge_f32_e32 vcc, s62, v11
Mv 92,40,84
Rb 20,8,5120
s_add_u32 s55, s55, 0x8000
Mv 92,44,92
Rb 24,8,6144
Mv 88,44,88
Rb 28,8,7168
Mv 88,48,96
Rb 32,8,8192
Mv 92,48,100
Rb 36,8,9216
Mv 92,52,108
Rb 40,8,10240
Mv 88,52,104
Rb 44,8,11264
buffer_load_dwordx4 v3, s[20:23], s56 offen offset:3072 sc1 lds
Mv 88,56,112
s_cmp_eq_u32 s57, 2
s_cselect_b32 s55, s59, s55
Mv 92,56,116
s_add_u32 s56, s56, 0x8000
Mv 92,60,124
s_waitcnt lgkmcnt(8)
s_cbranch_vccnz .Lzfx1
.Lzfr1:
Mv 88,60,120
v_add_f32_e32 v12, v12, v14
v_add_f32_e32 v13, v13, v15
Mp 128,0,48
Rb 48,8,12288
Mp 136,0,52
s_cmp_le_i32 s57, s58
s_cbranch_scc1 .Lzmk2
.Lzmr2:
Mp 140,4,52
Rb 52,8,13312
s_add_u32 m0, s36, 0
Mp 132,4,48
buffer_load_dwordx4 v2, s[20:23], s55 offen offset:0 sc1 lds
Mp 144,8,48
Rb 56,8,14336
)" R"(
Mp 152,8,52
v_exp_f32_e32 v32, v64
Mp 156,12,52
Rb 60,8,15360
s_waitcnt lgkmcnt(8)
Mp 148,12,48
v_exp_f32_e32 v26, v65
Mp 160,16,48
Rb 0,8,16384
v_add_f32_e32 v14, v32, v26
Mp 168,16,52
v_exp_f32_e32 v33, v66
Mp 172,20,52
Rb 4,8,17408
Cv 32,32,26
Mp 164,20,48
buffer_load_dwordx4 v2, s[20:23], s55 offen offset:1024 sc1 lds
Mp 176,24,48
Rb 8,8,18432
v_add_f32_e32 v14, v14, v33
Mp 184,24,52
v_exp_f32_e32 v27, v67
Mp 188,28,52
Rb 12,8,19456
s_waitcnt lgkmcnt(8)
Mp 180,28,48
v_exp_f32_e32 v34, v72
Mp 192,32,48
Rb 16,8,20480
v_add_f32_e32 v14, v14, v27
Mp 200,32,52
v_exp_f32_e32 v28, v73
Mp 204,36,52
Rb 20,8,21504
Cv 33,33,27
Mp 196,36,48
buffer_load_dwordx4 v2, s[20:23], s55 offen offset:2048 sc1 lds
Mp 208,40,48
Rb 24,8,22528
v_add_f32_e32 v14, v14, v34
Mp 216,40,52
v_exp_f32_e32 v35, v74
Mp 220,44,52
Rb 28,8,23552
s_waitcnt lgkmcnt(8)
Mp 212,44,48
v_exp_f32_e32 v29, v75
Mp 224,48,48
Rb 32,8,24576
v_add_f32_e32 v14, v14, v28
Mp 232,48,52
v_exp_f32_e32 v36, v68
Mp 236,52,52
Rb 36,8,25600
Cv 34,34,28
Mp 228,52,48
buffer_load_dwordx4 v2, s[20:23], s55 offen offset:3072 sc1 lds
Mp 240,56,48
Rb 40,8,26624
v_add_f32_e32 v14, v14, v35
Mp 248,56,52
v_exp_f32_e32 v24, v69
Mp 252,60,52
Rb 44,8,27648
)" R"(
s_waitcnt lgkmcnt(8)
Mp 244,60,48
v_exp_f32_e32 v37, v70
Mp 128,0,56
Rb 48,8,28672
v_add_f32_e32 v14, v14, v29
Mp 136,0,60
v_exp_f32_e32 v25, v71
Mp 140,4,60
Rb 52,8,29696
s_add_u32 m0, s36, 4096
Mp 132,4,56
buffer_load_dwordx4 v3, s[20:23], s55 offen offset:0 sc1 lds
Mp 144,8,56
Rb 56,8,30720
Cv 35,35,29
Mp 152,8,60
v_exp_f32_e32 v38, v76
Mp 156,12,60
Rb 60,8,31744
s_waitcnt lgkmcnt(8)
Mp 148,12,56
v_exp_f32_e32 v26, v77
Mp 160,16,56
v_exp_f32_e32 v39, v78
Mp 168,16,60
v_exp_f32_e32 v27, v79
Mp 172,20,60
v_add_f32_e32 v15, v36, v24
Cv 36,36,24
Mp 164,20,56
buffer_load_dwordx4 v3, s[20:23], s55 offen offset:1024 sc1 lds
Mp 176,24,56
v_add_f32_e32 v15, v15, v37
v_add_f32_e32 v15, v15, v25
Mp 184,24,60
Cv 37,37,25
v_add_f32_e32 v15, v15, v38
Mp 188,28,60
v_add_f32_e32 v15, v15, v26
Cv 38,38,26
Mp 180,28,56
s_waitcnt vmcnt(14) lgkmcnt(0)
s_barrier
Mp 192,32,56
Rb 0,1,32768
v_add_f32_e32 v15, v15, v39
Mp 200,32,60
Rb 4,1,33792
v_add_f32_e32 v15, v15, v27
Mp 204,36,60
Rb 8,1,34816
Cv 39,39,27
Mp 196,36,56
Rb 12,1,35840
buffer_load_dwordx4 v3, s[20:23], s55 offen offset:2048 sc1 lds
Mp 208,40,56
Rb 16,1,40960
s_sub_u32 s57, s57, 1
Mp 216,40,60
Rb 20,1,41984
Mp 220,44,60
Rb 24,1,43008
Mp 212,44,56
Rb 28,1,44032
)" R"(
Mp 224,48,56
Rb 32,1,36864
Mp 232,48,60
Rb 36,1,37888
Mp 236,52,60
Rb 40,1,38912
Mp 228,52,56
Rb 44,1,39936
buffer_load_dwordx4 v3, s[20:23], s55 offen offset:3072 sc1 lds
Mp 240,56,56
Mp 248,56,60
Mp 252,60,60
s_waitcnt lgkmcnt(8)
Mp 244,60,56
v_mfma_f32_16x16x32_bf16 v[96:99], a[0:3], a[64:67], v[16:19]
Rb 48,1,45056
buffer_load_dword v4, v5, s[20:23], s50 offen offset:0 sc0 sc1
v_mfma_f32_16x16x32_bf16 v[100:103], a[0:3], a[68:71], v[20:23]
s_lshl_b64 s[60:61], s[60:61], 8
s_or_b32 s60, s60, 0xff
Mv 100,4,76
Rb 52,1,46080
s_add_u32 m0, s36, 65536
Mv 96,4,72
buffer_load_dwordx4 v2, s[20:23], s56 offen offset:0 sc1 lds
Mv 96,8,80
Rb 56,1,47104
Mv 100,8,84
v_exp_f32_e32 v40, v80
Mv 100,12,92
Rb 60,1,48128
s_waitcnt lgkmcnt(8)
Mv 96,12,88
v_exp_f32_e32 v28, v81
Mv 96,16,96
Rb 0,1,49152
v_add_f32_e32 v14, v14, v40
Mv 100,16,100
v_exp_f32_e32 v41, v82
Mv 100,20,108
Rb 4,1,50176
v_add_f32_e32 v14, v14, v28
Mv 96,20,104
buffer_load_dwordx4 v2, s[20:23], s56 offen offset:1024 sc1 lds
Mv 96,24,112
Rb 8,1,51200
Cv 40,40,28
Mv 100,24,116
v_exp_f32_e32 v29, v83
Mv 100,28,124
Rb 12,1,52224
s_waitcnt lgkmcnt(8)
Mv 96,28,120
v_exp_f32_e32 v42, v88
v_mfma_f32_16x16x32_bf16 v[104:107], a[32:35], a[64:67], v[16:19]
Rb 16,1,57344
v_add_f32_e32 v14, v14, v41
v_mfma_f32_16x16x32_bf16 v[108:111], a[32:35], a[68:71], v[20:23]
v_exp_f32_e32 v24, v89
Mv 108,36,76
Rb 20,1,58368
v_add_f32_e32 v14, v14, v29
Mv 104,36,72
buffer_load_dwordx4 v2, s[20:23], s56 offen offset:2048 sc1 lds
)" R"(
Mv 104,40,80
Rb 24,1,59392
Cv 41,41,29
Mv 108,40,84
v_exp_f32_e32 v43, v90
Mv 108,44,92
Rb 28,1,60416
s_waitcnt lgkmcnt(8)
Mv 104,44,88
v_exp_f32_e32 v25, v91
Mv 104,48,96
Rb 32,1,53248
v_add_f32_e32 v14, v14, v42
Mv 108,48,100
v_exp_f32_e32 v44, v84
Mv 108,52,108
Rb 36,1,54272
v_add_f32_e32 v14, v14, v24
Mv 104,52,104
buffer_load_dwordx4 v2, s[20:23], s56 offen offset:3072 sc1 lds
Mv 104,56,112
Rb 40,1,55296
Cv 42,42,24
Mv 108,56,116
v_exp_f32_e32 v26, v85
Mv 108,60,124
Rb 44,1,56320
s_waitcnt lgkmcnt(8)
Mv 104,60,120
v_exp_f32_e32 v45, v86
v_mfma_f32_16x16x32_bf16 v[112:115], a[0:3], a[64:67], v[16:19]
Rb 48,1,61440
v_add_f32_e32 v14, v14, v43
v_mfma_f32_16x16x32_bf16 v[116:119], a[0:3], a[68:71], v[20:23]
v_exp_f32_e32 v27, v87
Mv 116,4,76
Rb 52,1,62464
s_add_u32 m0, s36, 69632
Mv 112,4,72
buffer_load_dwordx4 v3, s[20:23], s56 offen offset:0 sc1 lds
Mv 112,8,80
Rb 56,1,63488
v_add_f32_e32 v14, v14, v25
Mv 116,8,84
v_exp_f32_e32 v46, v92
Mv 116,12,92
Rb 60,1,64512
s_waitcnt lgkmcnt(8)
Mv 112,12,88
v_exp_f32_e32 v28, v93
Mv 112,16,96
v_exp_f32_e32 v47, v94
Mv 116,16,100
v_exp_f32_e32 v29, v95
Mv 116,20,108
Cv 43,43,25
v_add_f32_e32 v15, v15, v44
Mv 112,20,104
buffer_load_dwordx4 v3, s[20:23], s56 offen offset:1024 sc1 lds
Mv 112,24,112
v_add_f32_e32 v15, v15, v26
Cv 44,44,26
Mv 116,24,116
v_add_f32_e32 v15, v15, v45
)" R"(
v_add_f32_e32 v15, v15, v27
Mv 116,28,124
Cv 45,45,27
v_add_f32_e32 v15, v15, v46
Mv 112,28,120
s_waitcnt vmcnt(15) lgkmcnt(0)
s_barrier
v_mfma_f32_16x16x32_bf16 v[120:123], a[32:35], a[64:67], v[16:19]
Rb 0,8,32768
v_add_f32_e32 v15, v15, v28
v_mfma_f32_16x16x32_bf16 v[124:127], a[32:35], a[68:71], v[20:23]
Rb 4,8,33792
Cv 46,46,28
Mv 124,36,76
Rb 8,8,34816
v_add_f32_e32 v15, v15, v47
Mv 120,36,72
Rb 12,8,35840
buffer_load_dwordx4 v3, s[20:23], s56 offen offset:2048 sc1 lds
Mv 120,40,80
Rb 16,8,36864
v_add_f32_e32 v15, v15, v29
Mv 124,40,84
Rb 20,8,37888
Cv 47,47,29
Mv 124,44,92
Rb 24,8,38912
v_max_f32_e32 v11, v14, v15
Mv 120,44,88
Rb 28,8,39936
v_cmp_nge_f32_e32 vcc, s62, v11
Mv 120,48,96
Rb 32,8,40960
s_add_u32 s55, s55, 0x8000
Mv 124,48,100
Rb 36,8,41984
Mv 124,52,108
Rb 40,8,43008
Mv 120,52,104
Rb 44,8,44032
buffer_load_dwordx4 v3, s[20:23], s56 offen offset:3072 sc1 lds
Mv 120,56,112
s_cmp_eq_u32 s57, 2
s_cselect_b32 s55, s59, s55
Mv 124,56,116
s_add_u32 s56, s56, 0x8000
Mv 124,60,124
s_waitcnt lgkmcnt(8)
s_cbranch_vccnz .Lzfx2
.Lzfr2:
Mv 120,60,120
v_add_f32_e32 v12, v12, v14
v_add_f32_e32 v13, v13, v15
Mp 128,0,32
Rb 48,8,45056
Mp 136,0,36
s_cmp_le_i32 s57, s58
s_cbranch_scc1 .Lzmk3
.Lzmr3:
Mp 140,4,36
Rb 52,8,46080
s_add_u32 m0, s36, 32768
Mp 132,4,32
buffer_load_dwordx4 v2, s[20:23], s55 offen offset:0 sc1 lds
Mp 144,8,32
)" R"(
Rb 56,8,47104
Mp 152,8,36
v_exp_f32_e32 v48, v96
Mp 156,12,36
Rb 60,8,48128
s_waitcnt lgkmcnt(8)
Mp 148,12,32
v_exp_f32_e32 v24, v97
Mp 160,16,32
Rb 0,8,49152
v_add_f32_e32 v14, v48, v24
Mp 168,16,36
v_exp_f32_e32 v49, v98
Mp 172,20,36
Rb 4,8,50176
Cv 48,48,24
Mp 164,20,32
buffer_load_dwordx4 v2, s[20:23], s55 offen offset:1024 sc1 lds
Mp 176,24,32
Rb 8,8,51200
v_add_f32_e32 v14, v14, v49
Mp 184,24,36
v_exp_f32_e32 v25, v99
Mp 188,28,36
Rb 12,8,52224
s_waitcnt lgkmcnt(8)
Mp 180,28,32
v_exp_f32_e32 v50, v104
Mp 192,32,32
Rb 16,8,53248
v_add_f32_e32 v14, v14, v25
Mp 200,32,36
v_exp_f32_e32 v26, v105
Mp 204,36,36
Rb 20,8,54272
Cv 49,49,25
Mp 196,36,32
buffer_load_dwordx4 v2, s[20:23], s55 offen offset:2048 sc1 lds
Mp 208,40,32
Rb 24,8,55296
v_add_f32_e32 v14, v14, v50
Mp 216,40,36
v_exp_f32_e32 v51, v106
Mp 220,44,36
Rb 28,8,56320
s_waitcnt lgkmcnt(8)
Mp 212,44,32
v_exp_f32_e32 v27, v107
Mp 224,48,32
Rb 32,8,57344
v_add_f32_e32 v14, v14, v26
Mp 232,48,36
v_exp_f32_e32 v52, v100
Mp 236,52,36
Rb 36,8,58368
Cv 50,50,26
Mp 228,52,32
buffer_load_dwordx4 v2, s[20:23], s55 offen offset:3072 sc1 lds
Mp 240,56,32
Rb 40,8,59392
v_add_f32_e32 v14, v14, v51
Mp 248,56,36
v_exp_f32_e32 v28, v101
Mp 252,60,36
)" R"(
Rb 44,8,60416
s_waitcnt lgkmcnt(8)
Mp 244,60,32
v_exp_f32_e32 v53, v102
Mp 128,0,40
Rb 48,8,61440
v_add_f32_e32 v14, v14, v27
Mp 136,0,44
v_exp_f32_e32 v29, v103
Mp 140,4,44
Rb 52,8,62464
s_add_u32 m0, s36, 36864
Mp 132,4,40
buffer_load_dwordx4 v3, s[20:23], s55 offen offset:0 sc1 lds
Mp 144,8,40
Rb 56,8,63488
Cv 51,51,27
Mp 152,8,44
v_exp_f32_e32 v54, v108
Mp 156,12,44
Rb 60,8,64512
s_waitcnt lgkmcnt(8)
Mp 148,12,40
v_exp_f32_e32 v24, v109
Mp 160,16,40
v_exp_f32_e32 v55, v110
Mp 168,16,44
v_exp_f32_e32 v25, v111
Mp 172,20,44
v_add_f32_e32 v15, v52, v28
Cv 52,52,28
Mp 164,20,40
buffer_load_dwordx4 v3, s[20:23], s55 offen offset:1024 sc1 lds
Mp 176,24,40
v_add_f32_e32 v15, v15, v53
v_add_f32_e32 v15, v15, v29
Mp 184,24,44
Cv 53,53,29
v_add_f32_e32 v15, v15, v54
Mp 188,28,44
v_add_f32_e32 v15, v15, v24
Cv 54,54,24
Mp 180,28,40
s_waitcnt vmcnt(14) lgkmcnt(0)
s_barrier
Mp 192,32,40
Rb 0,1,0
v_add_f32_e32 v15, v15, v55
Mp 200,32,44
Rb 4,1,1024
v_add_f32_e32 v15, v15, v25
Mp 204,36,44
Rb 8,1,2048
Cv 55,55,25
Mp 196,36,40
Rb 12,1,3072
buffer_load_dwordx4 v3, s[20:23], s55 offen offset:2048 sc1 lds
Mp 208,40,40
Rb 16,1,8192
s_sub_u32 s57, s57, 1
Mp 216,40,44
Rb 20,1,9216
Mp 220,44,44
Rb 24,1,10240
Mp 212,44,40
)" R"(
Rb 28,1,11264
Mp 224,48,40
Rb 32,1,4096
Mp 232,48,44
Rb 36,1,5120
Mp 236,52,44
Rb 40,1,6144
Mp 228,52,40
Rb 44,1,7168
buffer_load_dwordx4 v3, s[20:23], s55 offen offset:3072 sc1 lds
Mp 240,56,40
v_cmp_eq_u32_e32 vcc, s3, v4
s_andn2_b64 s[28:29], s[60:61], vcc
s_cbranch_scc1 .Lzsp6
.Lzok6:
Mp 248,56,44
Mp 252,60,44
s_waitcnt lgkmcnt(8)
Mp 244,60,40
s_cmp_gt_i32 s57, s93
s_cbranch_scc1 .LzL
.LzLX:
s_cmp_eq_u32 s94, 0
s_cbranch_scc1 .LzEB
s_cmp_eq_u32 s94, 2
s_cbranch_scc1 .LzDA
s_lshl1_add_u32 s57, s35, 2
s_mov_b32 s59, s67
s_add_u32 s56, s89, 0x1000000
s_mov_b32 s94, 2
Mz 64,0,224
Rb 48,1,12288
v_lshrrev_b32 v4, 4, v0
v_accvgpr_mov_b32 a64, a224
Mz 68,0,228
v_lshlrev_b32 v4, 4, v4
v_add_lshl_u32 v4, v4, v0, 3
v_accvgpr_mov_b32 a65, a225
Mv 68,4,236
Rb 52,1,13312
s_add_u32 m0, s36, 98304
v_accvgpr_mov_b32 a66, a226
Mv 64,4,232
buffer_load_dwordx4 v2, s[20:23], s56 offen offset:0 sc1 lds
v_accvgpr_mov_b32 a67, a227
Mv 64,8,240
Rb 56,1,14336
s_add_u32 s92, s36, 131072
v_accvgpr_mov_b32 a68, a228
Mv 68,8,244
v_add_u32 v4, s92, v4
v_accvgpr_mov_b32 a69, a229
s_waitcnt vmcnt(1)
Mv 68,12,252
Rb 60,1,15360
v_accvgpr_mov_b32 a70, a230
s_waitcnt lgkmcnt(8)
Mv 64,12,248
buffer_load_dwordx4 v2, s[20:23], s56 offen offset:1024 sc1 lds
Rt 156,4,0
Mv 64,16,192
Rb 0,1,16384
Rt 158,4,128
v_accvgpr_mov_b32 a71, a231
)" R"(
Mv 68,16,196
Rt 160,4,1024
v_exp_f32_e32 v56, v112
Mv 68,20,204
Rb 4,1,17408
Rt 162,4,1152
v_add_f32_e32 v14, v14, v56
Mv 64,20,200
buffer_load_dwordx4 v2, s[20:23], s56 offen offset:2048 sc1 lds
Rt 164,4,2048
Mv 64,24,208
Rb 8,1,18432
Rt 166,4,2176
v_accvgpr_mov_b32 a72, a232
Mv 68,24,212
Rt 168,4,3072
v_exp_f32_e32 v26, v113
Mv 68,28,220
Rb 12,1,19456
Rt 170,4,3200
Mv 64,28,216
buffer_load_dwordx4 v2, s[20:23], s56 offen offset:3072 sc1 lds
Rt 172,4,4096
Mz 72,32,224
Rb 16,1,24576
Rt 174,4,4224
v_accvgpr_mov_b32 a73, a233
Mz 76,32,228
Rt 176,4,5120
v_exp_f32_e32 v57, v114
Mv 76,36,236
Rb 20,1,25600
s_add_u32 m0, s36, 102400
Rt 178,4,5248
Mv 72,36,232
buffer_load_dwordx4 v3, s[20:23], s56 offen offset:0 sc1 lds
Rt 180,4,6144
Mv 72,40,240
Rb 24,1,26624
Rt 182,4,6272
v_add_f32_e32 v14, v14, v26
Mv 76,40,244
v_exp_f32_e32 v27, v115
Cv 56,56,26
Mv 76,44,252
Rb 28,1,27648
v_add_f32_e32 v14, v14, v57
Mv 72,44,248
buffer_load_dwordx4 v3, s[20:23], s56 offen offset:1024 sc1 lds
v_add_f32_e32 v14, v14, v27
Mv 72,48,192
Rb 32,1,20480
v_and_b32 v4, 14, v0
v_accvgpr_mov_b32 a74, a234
Mv 76,48,196
v_min_u32 v4, 1, v4
v_lshlrev_b32 v4, 8, v4
Cv 57,57,27
Mv 76,52,204
Rb 36,1,21504
v_and_or_b32 v4, v0, 48, v4
v_accvgpr_mov_b32 a75, a235
Mv 72,52,200
buffer_load_dwordx4 v3, s[20:23], s56 offen offset:2048 sc1 lds
)" R"(
v_accvgpr_mov_b32 a76, a236
Mv 72,56,208
Rb 40,1,22528
v_lshlrev_b32 v4, 1, v4
v_accvgpr_mov_b32 a77, a237
Mv 76,56,212
v_and_or_b32 v4, v1, 16, v4
s_lshl_b32 s92, s33, 7
v_accvgpr_mov_b32 a78, a238
Mv 76,60,220
Rb 44,1,23552
v_accvgpr_mov_b32 a79, a239
s_waitcnt lgkmcnt(5)
Mv 72,60,216
v_mfma_f32_16x16x32_bf16 a[184:187], a[156:159], a[128:131], 0
buffer_load_dwordx4 v3, s[20:23], s56 offen offset:3072 sc1 lds
v_accvgpr_mov_b32 a80, a240
Mz 80,0,224
Ma 184,160,132
Rb 48,1,28672
s_add_u32 s92, s92, 162816
v_accvgpr_mov_b32 a81, a241
Mz 84,0,228
Ma 184,164,136
v_add_u32 v4, s92, v4
v_exp_f32_e32 v58, v120
Mv 84,4,236
Ma 184,168,140
Rb 52,1,29696
v_exp_f32_e32 v28, v121
Mv 80,4,232
Ma 184,172,144
v_exp_f32_e32 v59, v122
v_add_f32_e32 v14, v14, v58
Mv 80,8,240
Ma 184,176,148
Rb 56,1,30720
s_mov_b32 s24, s70
s_mov_b32 s25, s71
Mv 84,8,244
Ma 184,180,152
s_mul_i32 s83, s82, 14336
s_lshl_b32 s92, s33, 12
v_add_f32_e32 v14, v14, v28
Mv 84,12,252
Rb 60,1,31744
Cv 58,58,28
s_waitcnt lgkmcnt(8)
Mv 80,12,248
s_add_u32 s83, s83, s92
s_add_u32 m0, s92, 131072
v_add_f32_e32 v14, v14, v59
Mv 80,16,192
v_exp_f32_e32 v29, v123
v_accvgpr_mov_b32 a82, a242
Mv 84,16,196
v_exp_f32_e32 v60, v116
v_add_f32_e32 v14, v14, v29
Mv 84,20,204
ds_write_b128 v4, a[184:187]
v_and_b32 v4, 0x70, v4
Cv 59,59,29
Mv 80,20,200
v_add_u32 v4, 0x27c00, v4
)" R"(
v_exp_f32_e32 v24, v117
Mv 80,24,208
v_exp_f32_e32 v61, v118
v_add_f32_e32 v15, v15, v60
Mv 84,24,212
v_exp_f32_e32 v25, v119
v_add_f32_e32 v15, v15, v24
Mv 84,28,220
v_exp_f32_e32 v62, v124
Cv 60,60,24
Mv 80,28,216
v_add_f32_e32 v15, v15, v61
s_waitcnt lgkmcnt(0)
s_barrier
Mz 88,32,224
Rb 0,8,0
v_add_f32_e32 v15, v15, v25
v_accvgpr_mov_b32 a83, a243
Mz 92,32,228
Rb 4,8,16384
buffer_load_dwordx4 v1, s[24:27], s83 offen offset:0 lds
Mv 92,36,236
Rb 8,8,1024
v_exp_f32_e32 v26, v125
Mv 88,36,232
Rb 12,8,17408
v_exp_f32_e32 v63, v126
Mv 88,40,240
Rb 16,8,2048
buffer_load_dwordx4 v1, s[24:27], s83 offen offset:1024 lds
Mv 92,40,244
Rb 20,8,18432
v_exp_f32_e32 v27, v127
Mv 92,44,252
Rb 24,8,3072
Cv 61,61,25
v_add_f32_e32 v15, v15, v62
Mv 88,44,248
Rb 28,8,19456
v_add_f32_e32 v15, v15, v26
Cv 62,62,26
Mv 88,48,192
Rb 32,8,4096
v_add_f32_e32 v15, v15, v63
v_accvgpr_mov_b32 a84, a244
Mv 92,48,196
Rb 36,8,20480
buffer_load_dwordx4 v1, s[24:27], s83 offen offset:2048 lds
Mv 92,52,204
Rb 40,8,5120
v_add_f32_e32 v15, v15, v27
Cv 63,63,27
Mv 88,52,200
Rb 44,8,21504
v_max_f32_e32 v11, v14, v15
v_cmp_nge_f32_e32 vcc, s62, v11
Mv 88,56,208
buffer_load_dwordx4 v1, s[24:27], s83 offen offset:3072 lds
s_add_u32 s55, s55, 0x8000
Mv 92,56,212
s_add_u32 s56, s56, 0x8000
s_cmp_eq_u32 s57, 2
s_cselect_b32 s55, s59, s55
Mv 92,60,220
v_accvgpr_mov_b32 a85, a245
)" R"(
s_waitcnt lgkmcnt(8)
s_cbranch_vccnz .Lzfx3
.Lzfr3:
Mv 88,60,216
v_add_f32_e32 v12, v12, v14
v_add_f32_e32 v13, v13, v15
v_mov_b32 v14, v16
Mp 128,0,48
Rb 48,8,6144
v_mov_b32 v15, v20
v_accvgpr_mov_b32 a86, a246
Mp 136,0,52
buffer_store_dwordx4 v[12:15], v1, s[20:23], s66 offen sc0 sc1
s_cmp_le_i32 s57, s58
s_cbranch_scc1 .Lzmk4
.Lzmr4:
Mp 136,4,60
Rb 52,8,22528
s_add_u32 m0, s36, 0
v_accvgpr_mov_b32 a87, a247
Mp 128,4,56
buffer_load_dwordx4 v2, s[20:23], s55 offen offset:0 sc1 lds
v_accvgpr_mov_b32 a88, a248
Mp 132,8,48
Rb 56,8,7168
v_exp_f32_e32 v32, v64
Mp 140,8,52
v_exp_f32_e32 v28, v65
v_accvgpr_mov_b32 a89, a249
Mp 140,12,60
Rb 60,8,23552
v_exp_f32_e32 v33, v66
s_waitcnt lgkmcnt(8)
Mp 132,12,56
v_exp_f32_e32 v29, v67
v_add_f32_e32 v14, v32, v28
Mp 144,16,48
Rb 0,8,8192
v_exp_f32_e32 v34, v72
Mp 152,16,52
v_exp_f32_e32 v24, v73
v_exp_f32_e32 v35, v74
Mp 152,20,60
Rb 4,8,24576
v_exp_f32_e32 v25, v75
Mp 144,20,56
buffer_load_dwordx4 v2, s[20:23], s55 offen offset:1024 sc1 lds
Cv 128,128,129
Mp 148,24,48
Rb 8,8,9216
Cv 129,130,131
Cv 130,132,133
Cv 32,32,28
Mp 156,24,52
Cv 131,134,135
Cv 136,136,137
v_add_f32_e32 v14, v14, v33
Mp 156,28,60
Rb 12,8,25600
Cv 137,138,139
s_waitcnt lgkmcnt(8)
Mp 148,28,56
Cv 138,140,141
Cv 139,142,143
)" R"(
v_exp_f32_e32 v36, v68
Mp 160,32,48
Rb 16,8,10240
v_exp_f32_e32 v26, v69
Mp 168,32,52
v_exp_f32_e32 v37, v70
v_add_f32_e32 v14, v14, v29
Mp 168,36,60
Rb 20,8,26624
v_exp_f32_e32 v27, v71
Cv 33,33,29
Mp 160,36,56
buffer_load_dwordx4 v2, s[20:23], s55 offen offset:2048 sc1 lds
Cv 144,144,145
Mp 164,40,48
Rb 24,8,11264
Cv 145,146,147
Cv 146,148,149
Mp 172,40,52
buffer_store_dwordx4 v[128:131], v2, s[20:23], s64 offen offset:0 sc0 sc1
v_add_f32_e32 v14, v14, v34
Mp 172,44,60
Rb 28,8,27648
Cv 147,150,151
v_add_f32_e32 v14, v14, v24
s_waitcnt lgkmcnt(8)
Mp 164,44,56
Cv 152,152,153
Cv 153,154,155
Cv 154,156,157
Mp 176,48,48
Rb 32,8,12288
Cv 155,158,159
Cv 34,34,24
Mp 184,48,52
v_exp_f32_e32 v38, v76
v_exp_f32_e32 v28, v77
Mp 184,52,60
Rb 36,8,28672
v_exp_f32_e32 v39, v78
Mp 176,52,56
buffer_load_dwordx4 v2, s[20:23], s55 offen offset:3072 sc1 lds
Cv 160,160,161
Mp 180,56,48
Rb 40,8,13312
Cv 161,162,163
Cv 162,164,165
v_add_f32_e32 v14, v14, v35
Mp 188,56,52
buffer_store_dwordx4 v[136:139], v2, s[20:23], s64 offen offset:1024 sc0 sc1
v_add_f32_e32 v14, v14, v25
Mp 188,60,60
Rb 44,8,29696
Cv 163,166,167
s_waitcnt lgkmcnt(8)
Mp 180,60,56
Cv 168,168,169
Cv 169,170,171
Cv 170,172,173
Cv 35,35,25
Mp 192,0,48
Rb 48,8,14336
Cv 171,174,175
v_add_f32_e32 v15, v36, v26
)" R"(
Mp 200,0,52
buffer_store_dwordx4 v[144:147], v2, s[20:23], s64 offen offset:2048 sc0 sc1
Cv 36,36,26
Mp 200,4,60
Rb 52,8,30720
s_add_u32 m0, s36, 4096
v_exp_f32_e32 v29, v79
Mp 192,4,56
buffer_load_dwordx4 v3, s[20:23], s55 offen offset:0 sc1 lds
Cv 176,176,177
Mp 196,8,48
Rb 56,8,15360
Cv 177,178,179
Cv 178,180,181
Mp 204,8,52
Cv 179,182,183
Cv 184,184,185
v_add_f32_e32 v15, v15, v37
Mp 204,12,60
Rb 60,8,31744
Cv 185,186,187
v_add_f32_e32 v15, v15, v27
s_waitcnt lgkmcnt(8)
Mp 196,12,56
buffer_store_dwordx4 v[152:155], v2, s[20:23], s64 offen offset:3072 sc0 sc1
Cv 186,188,189
Mp 208,16,48
ds_read_b128 v[132:135], v4 offset:0
Cv 187,190,191
Cv 37,37,27
Mp 216,16,52
ds_read_b128 v[140:143], v4 offset:128
v_add_f32_e32 v15, v15, v38
v_add_f32_e32 v15, v15, v28
Cv 38,38,28
Mp 216,20,60
ds_read_b128 v[148:151], v4 offset:256
v_add_f32_e32 v15, v15, v39
v_add_f32_e32 v15, v15, v29
Mp 208,20,56
buffer_load_dwordx4 v3, s[20:23], s55 offen offset:1024 sc1 lds
ds_read_b128 v[156:159], v4 offset:384
Mp 212,24,48
Cv 192,192,193
Cv 193,194,195
Cv 194,196,197
Cv 39,39,29
Mp 220,24,52
Cv 195,198,199
Cv 200,200,201
s_sub_u32 s57, s57, 1
Mp 220,28,60
buffer_store_dwordx4 v[160:163], v3, s[20:23], s64 offen offset:0 sc0 sc1
Cv 201,202,203
Mp 212,28,56
Cv 202,204,205
v_mov_b32 v16, 0
s_waitcnt lgkmcnt(0)
s_barrier
Mp 224,32,48
Rb 0,1,32768
v_add_f32_e32 v132, v132, v140
v_mov_b32 v17, 0
Mp 232,32,52
Rb 4,1,33792
)" R"(
v_add_f32_e32 v133, v133, v141
Cv 203,206,207
Mp 232,36,60
Rb 8,1,34816
v_add_f32_e32 v134, v134, v142
v_mov_b32 v18, 0
v_mov_b32 v19, 0
Mp 224,36,56
Rb 12,1,35840
buffer_load_dwordx4 v3, s[20:23], s55 offen offset:2048 sc1 lds
Mp 228,40,48
Rb 16,1,40960
v_add_f32_e32 v135, v135, v143
buffer_store_dwordx4 v[168:171], v3, s[20:23], s64 offen offset:1024 sc0 sc1
Mp 236,40,52
Rb 20,1,41984
v_add_f32_e32 v148, v148, v156
v_mov_b32 v20, 0
Mp 236,44,60
Rb 24,1,43008
v_add_f32_e32 v149, v149, v157
Cv 208,208,209
v_mov_b32 v21, 0
Mp 228,44,56
Rb 28,1,44032
v_add_f32_e32 v150, v150, v158
v_add_f32_e32 v151, v151, v159
Mp 240,48,48
Rb 32,1,36864
Cv 132,132,132
v_mov_b32 v22, 0
Mp 248,48,52
Rb 36,1,37888
v_lshlrev_b32 v132, 16, v132
Cv 209,210,211
v_mov_b32 v23, 0
Mp 248,52,60
Rb 40,1,38912
Cv 148,148,148
buffer_store_dwordx4 v[176:179], v3, s[20:23], s64 offen offset:2048 sc0 sc1
Mp 240,52,56
Rb 44,1,39936
buffer_load_dwordx4 v3, s[20:23], s55 offen offset:3072 sc1 lds
Mp 244,56,48
v_lshlrev_b32 v148, 16, v148
Cv 133,133,133
Cv 210,212,213
Mp 252,56,52
v_lshlrev_b32 v133, 16, v133
Cv 149,149,149
v_accvgpr_mov_b32 a90, a250
Mp 252,60,60
v_lshlrev_b32 v149, 16, v149
Cv 211,214,215
s_waitcnt lgkmcnt(8)
Mp 244,60,56
Cv 134,134,134
v_lshlrev_b32 v134, 16, v134
Cv 150,150,150
Mz 96,0,224
Rb 48,1,45056
buffer_load_dword v4, v5, s[20:23], s50 offen offset:0 sc0 sc1
buffer_store_dwordx4 v[184:187], v3, s[20:23], s64 offen offset:3072 sc0 sc1
Mz 100,0,228
)" R"(
s_lshl_b64 s[60:61], s[60:61], 8
s_or_b32 s60, s60, 0xff
Cv 216,216,217
Mv 100,4,236
Rb 52,1,46080
s_add_u32 m0, s36, 65536
v_exp_f32_e32 v40, v80
Mv 96,4,232
buffer_load_dwordx4 v2, s[20:23], s56 offen offset:0 sc1 lds
Cv 217,218,219
v_add_f32_e32 v14, v14, v40
Mv 96,8,240
Rb 56,1,47104
v_lshlrev_b32 v150, 16, v150
Cv 218,220,221
Mv 100,8,244
Cv 135,135,135
v_lshlrev_b32 v135, 16, v135
v_exp_f32_e32 v24, v81
Mv 100,12,252
Rb 60,1,48128
s_add_u32 s64, s64, 0x8000
buffer_store_dwordx4 v[192:195], v2, s[20:23], s64 offen offset:0 sc0 sc1
s_waitcnt lgkmcnt(8)
Mv 96,12,248
Cv 151,151,151
v_lshlrev_b32 v151, 16, v151
v_mul_f32_e32 v140, 0xbfb8aa3b, v132
v_add_f32_e32 v14, v14, v24
Mv 96,16,192
Rb 0,1,49152
v_mul_f32_e32 v141, 0xbfb8aa3b, v133
v_exp_f32_e32 v41, v82
Mv 100,16,196
v_exp_f32_e32 v140, v140
Cv 219,222,223
Cv 40,40,24
Mv 100,20,204
Rb 4,1,50176
v_mul_f32_e32 v142, 0xbfb8aa3b, v134
v_exp_f32_e32 v25, v83
Mv 96,20,200
buffer_load_dwordx4 v2, s[20:23], s56 offen offset:1024 sc1 lds
Cv 224,224,225
v_add_f32_e32 v14, v14, v41
Mv 96,24,208
Rb 8,1,51200
v_mul_f32_e32 v143, 0xbfb8aa3b, v135
buffer_store_dwordx4 v[200:203], v2, s[20:23], s64 offen offset:1024 sc0 sc1
Mv 100,24,212
v_exp_f32_e32 v141, v141
v_exp_f32_e32 v42, v88
Mv 100,28,220
Rb 12,1,52224
Cv 225,226,227
v_add_f32_e32 v14, v14, v25
s_waitcnt lgkmcnt(8)
Mv 96,28,216
v_exp_f32_e32 v142, v142
v_add_f32_e32 v140, 1.0, v140
Cv 41,41,25
Mz 104,32,224
Rb 16,1,57344
v_add_f32_e32 v141, 1.0, v141
)" R"(
v_add_f32_e32 v14, v14, v42
Mz 108,32,228
v_exp_f32_e32 v143, v143
Cv 226,228,229
Mv 108,36,236
Rb 20,1,58368
v_add_f32_e32 v142, 1.0, v142
buffer_store_dwordx4 v[208:211], v2, s[20:23], s64 offen offset:2048 sc0 sc1
Mv 104,36,232
buffer_load_dwordx4 v2, s[20:23], s56 offen offset:2048 sc1 lds
Cv 227,230,231
Mv 104,40,240
Rb 24,1,59392
v_add_f32_e32 v143, 1.0, v143
Cv 232,232,233
Mv 108,40,244
v_rcp_f32_e32 v140, v140
v_exp_f32_e32 v26, v89
Mv 108,44,252
Rb 28,1,60416
Cv 233,234,235
v_add_f32_e32 v14, v14, v26
s_waitcnt lgkmcnt(8)
Mv 104,44,248
v_rcp_f32_e32 v141, v141
v_mul_f32_e32 v132, v132, v140
Cv 42,42,26
Mv 104,48,192
Rb 32,1,53248
v_mul_f32_e32 v133, v133, v141
buffer_store_dwordx4 v[216:219], v2, s[20:23], s64 offen offset:3072 sc0 sc1
Mv 108,48,196
v_rcp_f32_e32 v142, v142
Cv 234,236,237
Mv 108,52,204
Rb 36,1,54272
v_mul_f32_e32 v134, v134, v142
v_exp_f32_e32 v43, v90
Mv 104,52,200
buffer_load_dwordx4 v2, s[20:23], s56 offen offset:3072 sc1 lds
Cv 235,238,239
v_add_f32_e32 v14, v14, v43
Mv 104,56,208
Rb 40,1,55296
Cv 132,132,132
Cv 240,240,241
Mv 108,56,212
v_rcp_f32_e32 v143, v143
v_exp_f32_e32 v27, v91
Mv 108,60,220
Rb 44,1,56320
buffer_store_dwordx4 v[224:227], v3, s[20:23], s64 offen offset:0 sc0 sc1
s_waitcnt lgkmcnt(8)
Mv 104,60,216
v_mul_f32_e32 v135, v135, v143
v_lshlrev_b32 v132, 16, v132
Cv 133,133,133
v_add_f32_e32 v14, v14, v27
Mz 112,0,224
Rb 48,1,61440
v_lshlrev_b32 v133, 16, v133
Cv 43,43,27
Mz 116,0,228
Cv 134,134,134
)" R"(
v_lshlrev_b32 v134, 16, v134
Cv 241,242,243
Mv 116,4,236
Rb 52,1,62464
s_add_u32 m0, s36, 69632
v_exp_f32_e32 v44, v84
Mv 112,4,232
buffer_load_dwordx4 v3, s[20:23], s56 offen offset:0 sc1 lds
Cv 242,244,245
v_add_f32_e32 v15, v15, v44
Mv 112,8,240
Rb 56,1,63488
Cv 135,135,135
buffer_store_dwordx4 v[232:235], v3, s[20:23], s64 offen offset:1024 sc0 sc1
Mv 116,8,244
v_lshlrev_b32 v135, 16, v135
v_mul_f32_e32 v132, v132, v148
v_exp_f32_e32 v28, v85
Mv 116,12,252
Rb 60,1,64512
Cv 243,246,247
v_add_f32_e32 v15, v15, v28
s_waitcnt lgkmcnt(8)
Mv 112,12,248
v_mul_f32_e32 v133, v133, v149
v_mul_f32_e32 v134, v134, v150
v_mul_f32_e32 v135, v135, v151
Cv 44,44,28
Mv 112,16,192
Cv 140,132,133
Cv 141,134,135
v_exp_f32_e32 v45, v86
Mv 116,16,196
v_accvgpr_write_b32 a188, v140
v_accvgpr_write_b32 a189, v141
Cv 248,248,249
v_add_f32_e32 v15, v15, v45
Mv 116,20,204
v_lshrrev_b32 v156, 2, v0
v_mul_u32_u24 v156, 864, v156
buffer_store_dwordx4 v[240:243], v3, s[20:23], s64 offen offset:2048 sc0 sc1
Mv 112,20,200
buffer_load_dwordx4 v3, s[20:23], s56 offen offset:1024 sc1 lds
Cv 249,250,251
Mv 112,24,208
v_lshl_add_u32 v156, v0, 3, v156
s_mul_i32 s92, s33, 224
Cv 250,252,253
Mv 116,24,212
s_add_u32 s92, s92, 131072
v_add_u32 v156, s92, v156
v_exp_f32_e32 v29, v87
Mv 116,28,220
Cv 251,254,255
v_exp_f32_e32 v46, v92
v_add_f32_e32 v15, v15, v29
Mv 112,28,216
v_exp_f32_e32 v24, v93
s_waitcnt vmcnt(31) lgkmcnt(0)
s_barrier
Mz 120,32,224
Rb 0,8,32768
Rt 156,156,0
buffer_store_dwordx4 v[248:251], v3, s[20:23], s64 offen offset:3072 sc0 sc1
Mz 124,32,228
)" R"(
Rb 4,8,33792
Rt 158,156,32
v_exp_f32_e32 v47, v94
Mv 124,36,236
Rb 8,8,34816
Rt 160,156,64
v_exp_f32_e32 v25, v95
Mv 120,36,232
Rb 12,8,35840
buffer_load_dwordx4 v3, s[20:23], s56 offen offset:2048 sc1 lds
Rt 162,156,96
Mv 120,40,240
Rb 16,8,36864
Rt 164,156,128
Cv 45,45,29
v_add_f32_e32 v15, v15, v46
Mv 124,40,244
Rb 20,8,37888
Rt 166,156,160
v_add_f32_e32 v15, v15, v24
Cv 46,46,24
Mv 124,44,252
Rb 24,8,38912
Rt 168,156,192
v_add_f32_e32 v15, v15, v47
v_add_f32_e32 v15, v15, v25
Mv 120,44,248
Rb 28,8,39936
Cv 47,47,25
v_max_f32_e32 v11, v14, v15
v_mov_b32 v24, v14
Mv 120,48,192
Rb 32,8,40960
v_mov_b32 v25, v15
s_add_u32 s55, s55, 0x8000
Mv 124,48,196
Rb 36,8,41984
s_cmp_eq_u32 s57, 2
s_cselect_b32 s55, s59, s55
v_permlane32_swap_b32_e32 v24, v25
Mv 124,52,204
Rb 40,8,43008
v_add_f32_e32 v24, v24, v25
v_mov_b32 v25, v24
Mv 120,52,200
Rb 44,8,44032
buffer_load_dwordx4 v3, s[20:23], s56 offen offset:3072 sc1 lds
v_permlane16_swap_b32_e32 v24, v25
Mv 120,56,208
v_add_f32_e32 v24, v24, v25
v_rcp_f32_e32 v24, v24
Mv 124,56,212
s_add_u32 s56, s56, 0x8000
v_max_f32_e32 v11, v11, v24
v_cmp_nge_f32_e32 vcc, s62, v11
Mv 124,60,220
v_accvgpr_mov_b32 a91, a251
s_waitcnt lgkmcnt(12)
s_cbranch_vccnz .Lzfx4
.Lzfr4:
Mv 120,60,216
v_mov_b32 v12, v14
v_mov_b32 v13, v15
v_accvgpr_mov_b32 a92, a252
)" R"(
v_mfma_f32_16x16x32_bf16 v[128:131], a[0:3], v[32:35], 0
Rb 48,8,45056
v_accvgpr_mov_b32 a93, a253
v_accvgpr_mov_b32 a94, a254
v_mfma_f32_16x16x32_bf16 v[136:139], a[0:3], v[36:39], 0
v_accvgpr_mov_b32 a95, a255
s_cmp_le_i32 s57, s58
s_cbranch_scc1 .Lzmk5
.Lzmr5:
v_mfma_f32_16x16x32_bf16 v[140:143], a[4:7], v[36:39], 0
Rb 52,8,46080
s_add_u32 m0, s36, 32768
v_accvgpr_mov_b32 a96, a192
v_mfma_f32_16x16x32_bf16 v[132:135], a[4:7], v[32:35], 0
buffer_load_dwordx4 v2, s[20:23], s55 offen offset:0 sc1 lds
v_accvgpr_mov_b32 a97, a193
v_mfma_f32_16x16x32_bf16 v[144:147], a[8:11], v[32:35], 0
Rb 56,8,47104
v_and_b32 v11, 14, v0
v_accvgpr_mov_b32 a98, a194
v_mfma_f32_16x16x32_bf16 v[152:155], a[8:11], v[36:39], 0
v_min_u32 v11, 1, v11
v_lshlrev_b32 v11, 13, v11
v_accvgpr_mov_b32 a99, a195
v_mfma_f32_16x16x32_bf16 v[156:159], a[12:15], v[36:39], 0
Rb 60,8,48128
v_accvgpr_mov_b32 a100, a196
s_waitcnt lgkmcnt(8)
v_mfma_f32_16x16x32_bf16 v[148:151], a[12:15], v[32:35], 0
v_mfma_f32_16x16x16_bf16 a[128:131], a[156:157], a[188:189], 0
v_and_or_b32 v11, v0, 1, v11
v_mul_u32_u24 v11, 0x31000, v11
v_accvgpr_mov_b32 a101, a197
v_mfma_f32_16x16x32_bf16 v[160:163], a[16:19], v[32:35], 0
v_mfma_f32_16x16x16_bf16 a[132:135], a[158:159], a[188:189], 0
Rb 0,8,49152
v_and_or_b32 v11, v0, 48, v11
v_accvgpr_mov_b32 a102, a198
v_mfma_f32_16x16x32_bf16 v[168:171], a[16:19], v[36:39], 0
v_mfma_f32_16x16x16_bf16 a[136:139], a[160:161], a[188:189], 0
s_mul_i32 s83, s33, 50176
s_lshl_b32 s92, s82, 6
v_accvgpr_mov_b32 a103, a199
v_mfma_f32_16x16x32_bf16 v[172:175], a[20:23], v[36:39], 0
v_mfma_f32_16x16x16_bf16 a[140:143], a[162:163], a[188:189], 0
Rb 4,8,50176
s_add_u32 s83, s83, s92
v_accvgpr_mov_b32 a104, a200
v_mfma_f32_16x16x32_bf16 v[164:167], a[20:23], v[32:35], 0
v_mfma_f32_16x16x16_bf16 a[144:147], a[164:165], a[188:189], 0
buffer_load_dwordx4 v2, s[20:23], s55 offen offset:1024 sc1 lds
v_accvgpr_mov_b32 a105, a201
v_mfma_f32_16x16x32_bf16 v[176:179], a[24:27], v[32:35], 0
v_mfma_f32_16x16x16_bf16 a[148:151], a[166:167], a[188:189], 0
Rb 8,8,51200
s_add_u32 s83, s83, 0x5550000
v_accvgpr_mov_b32 a106, a202
v_mfma_f32_16x16x32_bf16 v[184:187], a[24:27], v[36:39], 0
v_mfma_f32_16x16x16_bf16 a[152:155], a[168:169], a[188:189], 0
v_exp_f32_e32 v48, v96
v_accvgpr_mov_b32 a107, a203
v_mfma_f32_16x16x32_bf16 v[188:191], a[28:31], v[36:39], 0
Rb 12,8,52224
v_accvgpr_mov_b32 a108, a204
)" R"(
s_waitcnt lgkmcnt(8)
v_mfma_f32_16x16x32_bf16 v[180:183], a[28:31], v[32:35], 0
v_exp_f32_e32 v26, v97
v_accvgpr_mov_b32 a109, a205
v_mfma_f32_16x16x32_bf16 v[192:195], a[32:35], v[32:35], 0
Rb 16,8,53248
v_add_f32_e32 v14, v48, v26
v_accvgpr_mov_b32 a110, a206
v_mfma_f32_16x16x32_bf16 v[200:203], a[32:35], v[36:39], 0
v_exp_f32_e32 v49, v98
Cv 48,48,26
v_mfma_f32_16x16x32_bf16 v[204:207], a[36:39], v[36:39], 0
Rb 20,8,54272
v_add_f32_e32 v14, v14, v49
v_accvgpr_mov_b32 a111, a207
v_mfma_f32_16x16x32_bf16 v[196:199], a[36:39], v[32:35], 0
buffer_load_dwordx4 v2, s[20:23], s55 offen offset:2048 sc1 lds
v_accvgpr_mov_b32 a112, a208
v_mfma_f32_16x16x32_bf16 v[208:211], a[40:43], v[32:35], 0
Rb 24,8,55296
v_exp_f32_e32 v27, v99
v_mfma_f32_16x16x32_bf16 v[216:219], a[40:43], v[36:39], 0
buffer_store_dwordx4 a[128:131], v11, s[20:23], s83 offen sc0 sc1
v_accvgpr_mov_b32 a113, a209
v_mfma_f32_16x16x32_bf16 v[220:223], a[44:47], v[36:39], 0
Rb 28,8,56320
v_accvgpr_mov_b32 a114, a210
s_waitcnt lgkmcnt(8)
v_mfma_f32_16x16x32_bf16 v[212:215], a[44:47], v[32:35], 0
s_add_u32 s83, s83, 7168
v_exp_f32_e32 v50, v104
v_mfma_f32_16x16x32_bf16 v[224:227], a[48:51], v[32:35], 0
Rb 32,8,57344
v_add_f32_e32 v14, v14, v27
v_accvgpr_mov_b32 a115, a211
v_mfma_f32_16x16x32_bf16 v[232:235], a[48:51], v[36:39], 0
v_exp_f32_e32 v28, v105
Cv 49,49,27
v_mfma_f32_16x16x32_bf16 v[236:239], a[52:55], v[36:39], 0
Rb 36,8,58368
v_add_f32_e32 v14, v14, v50
v_accvgpr_mov_b32 a116, a212
v_mfma_f32_16x16x32_bf16 v[228:231], a[52:55], v[32:35], 0
buffer_load_dwordx4 v2, s[20:23], s55 offen offset:3072 sc1 lds
v_accvgpr_mov_b32 a117, a213
v_mfma_f32_16x16x32_bf16 v[240:243], a[56:59], v[32:35], 0
Rb 40,8,59392
v_exp_f32_e32 v51, v106
v_mfma_f32_16x16x32_bf16 v[248:251], a[56:59], v[36:39], 0
buffer_store_dwordx4 a[132:135], v11, s[20:23], s83 offen sc0 sc1
v_accvgpr_mov_b32 a118, a214
v_mfma_f32_16x16x32_bf16 v[252:255], a[60:63], v[36:39], 0
Rb 44,8,60416
v_accvgpr_mov_b32 a119, a215
s_waitcnt lgkmcnt(8)
v_mfma_f32_16x16x32_bf16 v[244:247], a[60:63], v[32:35], 0
s_add_u32 s83, s83, 7168
v_exp_f32_e32 v29, v107
Mp 128,0,40
Rb 48,8,61440
v_add_f32_e32 v14, v14, v28
v_accvgpr_mov_b32 a120, a216
Mp 136,0,44
buffer_store_dwordx4 a[136:139], v11, s[20:23], s83 offen sc0 sc1
)" R"(
s_add_u32 s83, s83, 7168
Mp 140,4,44
Rb 52,8,62464
s_add_u32 m0, s36, 36864
v_accvgpr_mov_b32 a121, a217
Mp 132,4,40
buffer_load_dwordx4 v3, s[20:23], s55 offen offset:0 sc1 lds
v_accvgpr_mov_b32 a122, a218
Mp 144,8,40
Rb 56,8,63488
v_exp_f32_e32 v52, v100
Mp 152,8,44
v_exp_f32_e32 v24, v101
v_accvgpr_mov_b32 a123, a219
Mp 156,12,44
Rb 60,8,64512
v_accvgpr_mov_b32 a124, a220
s_waitcnt lgkmcnt(8)
Mp 148,12,40
buffer_store_dwordx4 a[140:143], v11, s[20:23], s83 offen sc0 sc1
Cv 50,50,28
Mp 160,16,40
s_add_u32 s83, s83, 7168
v_add_f32_e32 v14, v14, v51
v_accvgpr_mov_b32 a125, a221
Mp 168,16,44
v_exp_f32_e32 v53, v102
v_add_f32_e32 v14, v14, v29
Mp 172,20,44
v_exp_f32_e32 v25, v103
v_accvgpr_mov_b32 a126, a222
Mp 164,20,40
buffer_load_dwordx4 v3, s[20:23], s55 offen offset:1024 sc1 lds
v_accvgpr_mov_b32 a127, a223
Mp 176,24,40
v_exp_f32_e32 v54, v108
Cv 51,51,29
Mp 184,24,44
buffer_store_dwordx4 a[144:147], v11, s[20:23], s83 offen sc0 sc1
Mp 188,28,44
s_add_u32 s83, s83, 7168
v_add_f32_e32 v15, v52, v24
Mp 180,28,40
Cv 52,52,24
s_waitcnt vmcnt(13) lgkmcnt(0)
s_barrier
buffer_store_dword v6, v7, s[20:23], s65 offen offset:0 sc0 sc1
Mp 192,32,40
Rb 0,1,0
v_add_f32_e32 v15, v15, v53
Mp 200,32,44
Rb 4,1,1024
v_exp_f32_e32 v26, v109
Mp 204,36,44
Rb 8,1,2048
v_add_f32_e32 v15, v15, v25
Mp 196,36,40
Rb 12,1,3072
buffer_load_dwordx4 v3, s[20:23], s55 offen offset:2048 sc1 lds
Mp 208,40,40
Rb 16,1,8192
v_exp_f32_e32 v55, v110
Mp 216,40,44
Rb 20,1,9216
Cv 53,53,25
)" R"(
Mp 220,44,44
Rb 24,1,10240
buffer_store_dwordx4 a[148:151], v11, s[20:23], s83 offen sc0 sc1
Mp 212,44,40
Rb 28,1,11264
s_add_u32 s83, s83, 7168
v_add_f32_e32 v15, v15, v54
Mp 224,48,40
Rb 32,1,4096
v_add_f32_e32 v15, v15, v26
Mp 232,48,44
Rb 36,1,5120
v_exp_f32_e32 v27, v111
Mp 236,52,44
Rb 40,1,6144
Cv 54,54,26
Mp 228,52,40
Rb 44,1,7168
buffer_load_dwordx4 v3, s[20:23], s55 offen offset:3072 sc1 lds
Mp 240,56,40
v_cmp_eq_u32_e32 vcc, s3, v4
s_andn2_b64 s[28:29], s[60:61], vcc
s_cbranch_scc1 .Lzsp7
.Lzok7:
Mp 248,56,44
buffer_store_dwordx4 a[152:155], v11, s[20:23], s83 offen sc0 sc1
Mp 252,60,44
v_add_f32_e32 v15, v15, v55
s_waitcnt lgkmcnt(8)
Mp 244,60,40
v_add_f32_e32 v15, v15, v27
Cv 55,55,27
s_sub_u32 s57, s57, 1
s_cmp_gt_i32 s57, s93
s_cbranch_scc1 .LzL
.LzDA:
s_waitcnt vmcnt(8) lgkmcnt(0)
s_barrier
Rb 0,8,0
Rb 4,8,16384
Rb 8,8,1024
Rb 12,8,17408
Rb 16,8,2048
Rb 20,8,18432
Rb 24,8,3072
Rb 28,8,19456
Rb 32,8,4096
Rb 36,8,20480
Rb 40,8,5120
Rb 44,8,21504
v_exp_f32_e32 v56, v112
v_exp_f32_e32 v28, v113
v_add_f32_e32 v14, v14, v56
v_add_f32_e32 v14, v14, v28
Cv 56,56,28
v_exp_f32_e32 v57, v114
v_exp_f32_e32 v29, v115
v_add_f32_e32 v14, v14, v57
v_add_f32_e32 v14, v14, v29
Cv 57,57,29
v_exp_f32_e32 v58, v120
v_exp_f32_e32 v24, v121
v_add_f32_e32 v14, v14, v58
v_add_f32_e32 v14, v14, v24
Cv 58,58,24
)" R"(
v_exp_f32_e32 v59, v122
v_exp_f32_e32 v25, v123
v_add_f32_e32 v14, v14, v59
v_add_f32_e32 v14, v14, v25
Cv 59,59,25
v_exp_f32_e32 v60, v116
v_exp_f32_e32 v26, v117
v_add_f32_e32 v15, v15, v60
v_add_f32_e32 v15, v15, v26
Cv 60,60,26
v_exp_f32_e32 v61, v118
v_exp_f32_e32 v27, v119
v_add_f32_e32 v15, v15, v61
v_add_f32_e32 v15, v15, v27
Cv 61,61,27
v_exp_f32_e32 v62, v124
v_exp_f32_e32 v28, v125
v_add_f32_e32 v15, v15, v62
v_add_f32_e32 v15, v15, v28
Cv 62,62,28
v_exp_f32_e32 v63, v126
v_exp_f32_e32 v29, v127
v_add_f32_e32 v15, v15, v63
v_add_f32_e32 v15, v15, v29
Cv 63,63,29
v_max_f32_e32 v11, v14, v15
v_cmp_nge_f32_e32 vcc, s62, v11
s_cbranch_vccnz .Lzfx5
.Lzfr5:
s_waitcnt lgkmcnt(8)
Mp 128,0,48
Rb 48,8,6144
s_add_u32 m0, s36, 131072
s_add_u32 s40, s67, 0x10000
s_lshl_b32 s82, s82, 7
Mp 136,0,52
buffer_load_dwordx4 v2, s[20:23], s40 offen offset:0 sc1 lds
s_add_u32 s82, s82, 0x550c000
Mp 136,4,60
Rb 52,8,22528
v_add_f32_e32 v12, v12, v14
Mp 128,4,56
buffer_load_dwordx4 v2, s[20:23], s40 offen offset:1024 sc1 lds
Mp 132,8,48
Rb 56,8,7168
v_add_f32_e32 v13, v13, v15
Mp 140,8,52
v_mov_b32 v24, v12
v_mov_b32 v25, v13
s_cmp_lg_u32 s33, 0
s_cmov_b32 s82, 0x40000000
Mp 140,12,60
Rb 60,8,23552
s_waitcnt lgkmcnt(8)
Mp 132,12,56
buffer_load_dwordx4 v2, s[20:23], s40 offen offset:2048 sc1 lds
Mp 144,16,48
Rb 0,8,8192
v_permlane32_swap_b32_e32 v12, v24
Mp 152,16,52
buffer_load_dwordx4 v2, s[20:23], s40 offen offset:3072 sc1 lds
Mp 152,20,60
Rb 4,8,24576
v_add_f32_e32 v12, v12, v24
)" R"(
Mp 144,20,56
v_mov_b32 v24, v12
v_permlane32_swap_b32_e32 v13, v25
Mp 148,24,48
Rb 8,8,9216
s_add_u32 m0, s36, 135168
Mp 156,24,52
buffer_load_dwordx4 v3, s[20:23], s40 offen offset:0 sc1 lds
s_cmp_eq_u32 s35, 7
s_cmov_b32 s82, 0x40000000
Mp 156,28,60
Rb 12,8,25600
s_waitcnt lgkmcnt(8)
Mp 148,28,56
buffer_load_dwordx4 v3, s[20:23], s40 offen offset:1024 sc1 lds
Mp 160,32,48
Rb 16,8,10240
v_permlane16_swap_b32_e32 v12, v24
Mp 168,32,52
v_add_f32_e32 v12, v12, v24
v_add_f32_e32 v13, v13, v25
Mp 168,36,60
Rb 20,8,26624
v_mov_b32 v25, v13
Mp 160,36,56
buffer_load_dwordx4 v3, s[20:23], s40 offen offset:2048 sc1 lds
Mp 164,40,48
Rb 24,8,11264
s_add_u32 m0, s36, 98304
s_add_u32 s40, s67, 0x18000
Mp 172,40,52
buffer_load_dwordx4 v2, s[20:23], s40 offen offset:0 sc1 lds
Mp 172,44,60
Rb 28,8,27648
s_waitcnt lgkmcnt(8)
Mp 164,44,56
v_rcp_f32_e32 v12, v12
Mp 176,48,48
Rb 32,8,12288
v_permlane16_swap_b32_e32 v13, v25
Mp 184,48,52
buffer_load_dwordx4 v2, s[20:23], s40 offen offset:1024 sc1 lds
Mp 184,52,60
Rb 36,8,28672
v_add_f32_e32 v13, v13, v25
Mp 176,52,56
buffer_load_dwordx4 v2, s[20:23], s40 offen offset:2048 sc1 lds
Mp 180,56,48
Rb 40,8,13312
v_mul_f32_e32 v128, v12, v128
Mp 188,56,52
v_rcp_f32_e32 v13, v13
Mp 188,60,60
Rb 44,8,29696
s_waitcnt lgkmcnt(8)
Mp 180,60,56
buffer_load_dwordx4 v2, s[20:23], s40 offen offset:3072 sc1 lds
Mp 192,0,48
Rb 48,8,14336
s_add_u32 m0, s36, 102400
Mp 200,0,52
buffer_load_dwordx4 v3, s[20:23], s40 offen offset:0 sc1 lds
Mp 200,4,60
Rb 52,8,30720
)" R"(
v_mul_f32_e32 v129, v12, v129
Mp 192,4,56
Cv 128,128,129
v_mul_f32_e32 v130, v12, v130
Mp 196,8,48
Rb 56,8,15360
v_mul_f32_e32 v131, v12, v131
Mp 204,8,52
buffer_load_dwordx4 v3, s[20:23], s40 offen offset:1024 sc1 lds
Mp 204,12,60
Rb 60,8,31744
s_waitcnt lgkmcnt(8)
Mp 196,12,56
buffer_load_dwordx4 v3, s[20:23], s40 offen offset:2048 sc1 lds
Mp 208,16,48
Rb 0,1,0
Cv 129,130,131
Mp 216,16,52
v_mul_f32_e32 v132, v12, v132
v_mul_f32_e32 v133, v12, v133
Mp 216,20,60
Rb 4,1,1024
Cv 130,132,133
Mp 208,20,56
v_mul_f32_e32 v134, v12, v134
v_mul_f32_e32 v135, v12, v135
Mp 212,24,48
Rb 8,1,2048
Cv 131,134,135
Mp 220,24,52
v_mul_f32_e32 v136, v13, v136
v_mul_f32_e32 v137, v13, v137
Mp 220,28,60
Rb 12,1,3072
s_waitcnt vmcnt(14) lgkmcnt(4)
s_barrier
Mp 212,28,56
s_add_u32 s40, s67, 0x20000
Cv 136,136,137
Mp 224,32,48
Rb 16,1,4096
s_add_u32 m0, s36, 65536
Mp 232,32,52
buffer_load_dwordx4 v2, s[20:23], s40 offen offset:0 sc1 lds
Mp 232,36,60
Rb 20,1,5120
v_mul_f32_e32 v138, v13, v138
Mp 224,36,56
v_mul_f32_e32 v139, v13, v139
Cv 137,138,139
Mp 228,40,48
Rb 24,1,6144
v_mul_f32_e32 v140, v13, v140
Mp 236,40,52
v_mul_f32_e32 v141, v13, v141
Cv 138,140,141
Mp 236,44,60
Rb 28,1,8192
Mp 228,44,56
buffer_load_dwordx4 v2, s[20:23], s40 offen offset:1024 sc1 lds
Mp 240,48,48
Rb 32,1,9216
v_mul_f32_e32 v142, v13, v142
Mp 248,48,52
v_mul_f32_e32 v143, v13, v143
)" R"(
Cv 139,142,143
Mp 248,52,60
Rb 36,1,10240
v_mul_f32_e32 v144, v12, v144
Mp 240,52,56
v_mul_f32_e32 v145, v12, v145
Cv 144,144,145
Mp 244,56,48
Rb 40,1,11264
v_mul_f32_e32 v146, v12, v146
Mp 252,56,52
buffer_load_dwordx4 v2, s[20:23], s40 offen offset:2048 sc1 lds
Mp 252,60,60
Rb 44,1,12288
s_waitcnt lgkmcnt(8)
Mp 244,60,56
v_mul_f32_e32 v147, v12, v147
Cv 145,146,147
v_mfma_f32_16x16x32_bf16 a[64:67], a[0:3], v[128:131], 0
Rb 48,1,13312
v_mul_f32_e32 v148, v12, v148
v_mfma_f32_16x16x32_bf16 a[72:75], a[0:3], v[136:139], 0
v_mul_f32_e32 v149, v12, v149
Cv 146,148,149
v_mfma_f32_16x16x32_bf16 a[76:79], a[4:7], v[136:139], 0
Rb 52,1,14336
v_mul_f32_e32 v150, v12, v150
v_mfma_f32_16x16x32_bf16 a[68:71], a[4:7], v[128:131], 0
buffer_load_dwordx4 v2, s[20:23], s40 offen offset:3072 sc1 lds
v_mfma_f32_16x16x32_bf16 a[80:83], a[8:11], v[128:131], 0
Rb 56,1,16384
v_mul_f32_e32 v151, v12, v151
v_mfma_f32_16x16x32_bf16 a[88:91], a[8:11], v[136:139], 0
Cv 147,150,151
v_mul_f32_e32 v152, v13, v152
v_mfma_f32_16x16x32_bf16 a[92:95], a[12:15], v[136:139], 0
Rb 60,1,17408
s_waitcnt lgkmcnt(8)
v_mfma_f32_16x16x32_bf16 a[84:87], a[12:15], v[128:131], 0
v_mul_f32_e32 v153, v13, v153
Cv 152,152,153
v_mfma_f32_16x16x32_bf16 a[96:99], a[16:19], v[128:131], 0
Rb 0,1,18432
s_add_u32 m0, s36, 69632
v_mfma_f32_16x16x32_bf16 a[104:107], a[16:19], v[136:139], 0
buffer_load_dwordx4 v3, s[20:23], s40 offen offset:0 sc1 lds
v_mfma_f32_16x16x32_bf16 a[108:111], a[20:23], v[136:139], 0
Rb 4,1,19456
v_mul_f32_e32 v154, v13, v154
v_mfma_f32_16x16x32_bf16 a[100:103], a[20:23], v[128:131], 0
v_mul_f32_e32 v155, v13, v155
Cv 153,154,155
v_mfma_f32_16x16x32_bf16 a[112:115], a[24:27], v[128:131], 0
Rb 8,1,20480
v_mul_f32_e32 v156, v13, v156
v_mfma_f32_16x16x32_bf16 a[120:123], a[24:27], v[136:139], 0
v_mul_f32_e32 v157, v13, v157
Cv 154,156,157
v_mfma_f32_16x16x32_bf16 a[124:127], a[28:31], v[136:139], 0
Rb 12,1,21504
s_waitcnt lgkmcnt(8)
v_mfma_f32_16x16x32_bf16 a[116:119], a[28:31], v[128:131], 0
buffer_load_dwordx4 v3, s[20:23], s40 offen offset:1024 sc1 lds
v_mfma_f32_16x16x32_bf16 a[128:131], a[32:35], v[128:131], 0
)" R"(
Rb 16,1,22528
v_mul_f32_e32 v158, v13, v158
v_mfma_f32_16x16x32_bf16 a[136:139], a[32:35], v[136:139], 0
v_mul_f32_e32 v159, v13, v159
Cv 155,158,159
v_mfma_f32_16x16x32_bf16 a[140:143], a[36:39], v[136:139], 0
Rb 20,1,24576
v_mul_f32_e32 v160, v12, v160
v_mfma_f32_16x16x32_bf16 a[132:135], a[36:39], v[128:131], 0
v_mul_f32_e32 v161, v12, v161
Cv 160,160,161
v_mfma_f32_16x16x32_bf16 a[144:147], a[40:43], v[128:131], 0
Rb 24,1,25600
v_mul_f32_e32 v162, v12, v162
v_mfma_f32_16x16x32_bf16 a[152:155], a[40:43], v[136:139], 0
buffer_load_dwordx4 v3, s[20:23], s40 offen offset:2048 sc1 lds
v_mfma_f32_16x16x32_bf16 a[156:159], a[44:47], v[136:139], 0
Rb 28,1,26624
s_waitcnt lgkmcnt(8)
v_mfma_f32_16x16x32_bf16 a[148:151], a[44:47], v[128:131], 0
v_mul_f32_e32 v163, v12, v163
Cv 161,162,163
v_mfma_f32_16x16x32_bf16 a[160:163], a[48:51], v[128:131], 0
Rb 32,1,27648
v_mul_f32_e32 v164, v12, v164
v_mfma_f32_16x16x32_bf16 a[168:171], a[48:51], v[136:139], 0
v_mul_f32_e32 v165, v12, v165
Cv 162,164,165
v_mfma_f32_16x16x32_bf16 a[172:175], a[52:55], v[136:139], 0
Rb 36,1,28672
v_mul_f32_e32 v166, v12, v166
v_mfma_f32_16x16x32_bf16 a[164:167], a[52:55], v[128:131], 0
v_mul_f32_e32 v167, v12, v167
Cv 163,166,167
v_mfma_f32_16x16x32_bf16 a[64:67], a[56:59], v[144:147], a[64:67]
Rb 40,1,29696
v_mul_f32_e32 v168, v13, v168
v_mfma_f32_16x16x32_bf16 a[72:75], a[56:59], v[152:155], a[72:75]
v_mul_f32_e32 v169, v13, v169
Cv 168,168,169
v_mfma_f32_16x16x32_bf16 a[76:79], a[60:63], v[152:155], a[76:79]
Rb 44,1,30720
s_waitcnt lgkmcnt(8)
v_mfma_f32_16x16x32_bf16 a[68:71], a[60:63], v[144:147], a[68:71]
v_mul_f32_e32 v170, v13, v170
v_mul_f32_e32 v171, v13, v171
v_mfma_f32_16x16x32_bf16 a[80:83], a[0:3], v[144:147], a[80:83]
Rb 48,1,32768
Cv 169,170,171
v_mfma_f32_16x16x32_bf16 a[88:91], a[0:3], v[152:155], a[88:91]
v_mul_f32_e32 v172, v13, v172
v_mul_f32_e32 v173, v13, v173
v_mfma_f32_16x16x32_bf16 a[92:95], a[4:7], v[152:155], a[92:95]
Rb 52,1,33792
Cv 170,172,173
v_mfma_f32_16x16x32_bf16 a[84:87], a[4:7], v[144:147], a[84:87]
v_mul_f32_e32 v174, v13, v174
v_mul_f32_e32 v175, v13, v175
v_mfma_f32_16x16x32_bf16 a[96:99], a[8:11], v[144:147], a[96:99]
Rb 56,1,34816
Cv 171,174,175
v_mfma_f32_16x16x32_bf16 a[104:107], a[8:11], v[152:155], a[104:107]
v_mul_f32_e32 v176, v12, v176
v_mul_f32_e32 v177, v12, v177
)" R"(
v_mfma_f32_16x16x32_bf16 a[108:111], a[12:15], v[152:155], a[108:111]
Rb 60,1,35840
s_waitcnt vmcnt(14) lgkmcnt(4)
s_barrier
buffer_store_dword v6, v7, s[20:23], s82 offen offset:0 sc0 sc1
v_mfma_f32_16x16x32_bf16 a[100:103], a[12:15], v[144:147], a[100:103]
s_add_u32 s40, s67, 0x28000
Cv 176,176,177
v_mfma_f32_16x16x32_bf16 a[112:115], a[16:19], v[144:147], a[112:115]
Rb 0,1,36864
s_add_u32 m0, s36, 0
v_mfma_f32_16x16x32_bf16 a[120:123], a[16:19], v[152:155], a[120:123]
buffer_load_dwordx4 v2, s[20:23], s40 offen offset:0 sc1 lds
v_mfma_f32_16x16x32_bf16 a[124:127], a[20:23], v[152:155], a[124:127]
Rb 4,1,37888
v_mul_f32_e32 v178, v12, v178
v_mfma_f32_16x16x32_bf16 a[116:119], a[20:23], v[144:147], a[116:119]
v_mul_f32_e32 v179, v12, v179
Cv 177,178,179
v_mfma_f32_16x16x32_bf16 a[128:131], a[24:27], v[144:147], a[128:131]
Rb 8,1,38912
v_mul_f32_e32 v180, v12, v180
v_mfma_f32_16x16x32_bf16 a[136:139], a[24:27], v[152:155], a[136:139]
v_mul_f32_e32 v181, v12, v181
Cv 178,180,181
v_mfma_f32_16x16x32_bf16 a[140:143], a[28:31], v[152:155], a[140:143]
Rb 12,1,40960
v_mfma_f32_16x16x32_bf16 a[132:135], a[28:31], v[144:147], a[132:135]
buffer_load_dwordx4 v2, s[20:23], s40 offen offset:1024 sc1 lds
v_mfma_f32_16x16x32_bf16 a[144:147], a[32:35], v[144:147], a[144:147]
Rb 16,1,41984
v_mul_f32_e32 v182, v12, v182
v_mfma_f32_16x16x32_bf16 a[152:155], a[32:35], v[152:155], a[152:155]
v_mul_f32_e32 v183, v12, v183
Cv 179,182,183
v_mfma_f32_16x16x32_bf16 a[156:159], a[36:39], v[152:155], a[156:159]
Rb 20,1,43008
v_mul_f32_e32 v184, v13, v184
v_mfma_f32_16x16x32_bf16 a[148:151], a[36:39], v[144:147], a[148:151]
v_mul_f32_e32 v185, v13, v185
Cv 184,184,185
v_mfma_f32_16x16x32_bf16 a[160:163], a[40:43], v[144:147], a[160:163]
Rb 24,1,44032
v_mul_f32_e32 v186, v13, v186
v_mfma_f32_16x16x32_bf16 a[168:171], a[40:43], v[152:155], a[168:171]
buffer_load_dwordx4 v2, s[20:23], s40 offen offset:2048 sc1 lds
v_mfma_f32_16x16x32_bf16 a[172:175], a[44:47], v[152:155], a[172:175]
Rb 28,1,45056
s_waitcnt lgkmcnt(8)
v_mfma_f32_16x16x32_bf16 a[164:167], a[44:47], v[144:147], a[164:167]
v_mul_f32_e32 v187, v13, v187
Cv 185,186,187
v_mfma_f32_16x16x32_bf16 a[64:67], a[48:51], v[160:163], a[64:67]
Rb 32,1,46080
v_mul_f32_e32 v188, v13, v188
v_mfma_f32_16x16x32_bf16 a[72:75], a[48:51], v[168:171], a[72:75]
v_mul_f32_e32 v189, v13, v189
Cv 186,188,189
v_mfma_f32_16x16x32_bf16 a[76:79], a[52:55], v[168:171], a[76:79]
Rb 36,1,47104
v_mul_f32_e32 v190, v13, v190
v_mfma_f32_16x16x32_bf16 a[68:71], a[52:55], v[160:163], a[68:71]
buffer_load_dwordx4 v2, s[20:23], s40 offen offset:3072 sc1 lds
v_mfma_f32_16x16x32_bf16 a[80:83], a[56:59], v[160:163], a[80:83]
Rb 40,1,49152
)" R"(
v_mul_f32_e32 v191, v13, v191
v_mfma_f32_16x16x32_bf16 a[88:91], a[56:59], v[168:171], a[88:91]
Cv 187,190,191
v_mul_f32_e32 v192, v12, v192
v_mfma_f32_16x16x32_bf16 a[92:95], a[60:63], v[168:171], a[92:95]
Rb 44,1,50176
s_waitcnt lgkmcnt(8)
v_mfma_f32_16x16x32_bf16 a[84:87], a[60:63], v[160:163], a[84:87]
v_mul_f32_e32 v193, v12, v193
Cv 192,192,193
v_mfma_f32_16x16x32_bf16 a[96:99], a[0:3], v[160:163], a[96:99]
Rb 48,1,51200
s_add_u32 m0, s36, 4096
v_mfma_f32_16x16x32_bf16 a[104:107], a[0:3], v[168:171], a[104:107]
buffer_load_dwordx4 v3, s[20:23], s40 offen offset:0 sc1 lds
v_mfma_f32_16x16x32_bf16 a[108:111], a[4:7], v[168:171], a[108:111]
Rb 52,1,52224
v_mul_f32_e32 v194, v12, v194
v_mfma_f32_16x16x32_bf16 a[100:103], a[4:7], v[160:163], a[100:103]
v_mul_f32_e32 v195, v12, v195
Cv 193,194,195
v_mfma_f32_16x16x32_bf16 a[112:115], a[8:11], v[160:163], a[112:115]
Rb 56,1,53248
v_mul_f32_e32 v196, v12, v196
v_mfma_f32_16x16x32_bf16 a[120:123], a[8:11], v[168:171], a[120:123]
v_mul_f32_e32 v197, v12, v197
Cv 194,196,197
v_mfma_f32_16x16x32_bf16 a[124:127], a[12:15], v[168:171], a[124:127]
Rb 60,1,54272
s_waitcnt lgkmcnt(8)
v_mfma_f32_16x16x32_bf16 a[116:119], a[12:15], v[160:163], a[116:119]
buffer_load_dwordx4 v3, s[20:23], s40 offen offset:1024 sc1 lds
v_mfma_f32_16x16x32_bf16 a[128:131], a[16:19], v[160:163], a[128:131]
Rb 0,1,55296
v_mul_f32_e32 v198, v12, v198
v_mfma_f32_16x16x32_bf16 a[136:139], a[16:19], v[168:171], a[136:139]
v_mul_f32_e32 v199, v12, v199
Cv 195,198,199
v_mfma_f32_16x16x32_bf16 a[140:143], a[20:23], v[168:171], a[140:143]
Rb 4,1,57344
v_mul_f32_e32 v200, v13, v200
v_mfma_f32_16x16x32_bf16 a[132:135], a[20:23], v[160:163], a[132:135]
v_mul_f32_e32 v201, v13, v201
Cv 200,200,201
v_mfma_f32_16x16x32_bf16 a[144:147], a[24:27], v[160:163], a[144:147]
Rb 8,1,58368
v_mul_f32_e32 v202, v13, v202
v_mfma_f32_16x16x32_bf16 a[152:155], a[24:27], v[168:171], a[152:155]
buffer_load_dwordx4 v3, s[20:23], s40 offen offset:2048 sc1 lds
v_mfma_f32_16x16x32_bf16 a[156:159], a[28:31], v[168:171], a[156:159]
Rb 12,1,59392
s_waitcnt lgkmcnt(8)
v_mfma_f32_16x16x32_bf16 a[148:151], a[28:31], v[160:163], a[148:151]
v_mul_f32_e32 v203, v13, v203
Cv 201,202,203
v_mfma_f32_16x16x32_bf16 a[160:163], a[32:35], v[160:163], a[160:163]
Rb 16,1,60416
v_mul_f32_e32 v204, v13, v204
v_mfma_f32_16x16x32_bf16 a[168:171], a[32:35], v[168:171], a[168:171]
v_mul_f32_e32 v205, v13, v205
Cv 202,204,205
v_mfma_f32_16x16x32_bf16 a[172:175], a[36:39], v[168:171], a[172:175]
Rb 20,1,61440
v_mul_f32_e32 v206, v13, v206
)" R"(
v_mfma_f32_16x16x32_bf16 a[164:167], a[36:39], v[160:163], a[164:167]
v_mul_f32_e32 v207, v13, v207
Cv 203,206,207
v_mfma_f32_16x16x32_bf16 a[64:67], a[40:43], v[176:179], a[64:67]
Rb 24,1,62464
v_mul_f32_e32 v208, v12, v208
v_mfma_f32_16x16x32_bf16 a[72:75], a[40:43], v[184:187], a[72:75]
v_mul_f32_e32 v209, v12, v209
Cv 208,208,209
v_mfma_f32_16x16x32_bf16 a[76:79], a[44:47], v[184:187], a[76:79]
Rb 28,1,63488
s_waitcnt lgkmcnt(8)
v_mfma_f32_16x16x32_bf16 a[68:71], a[44:47], v[176:179], a[68:71]
v_mul_f32_e32 v210, v12, v210
v_mul_f32_e32 v211, v12, v211
v_mfma_f32_16x16x32_bf16 a[80:83], a[48:51], v[176:179], a[80:83]
Rb 32,10,0
Cv 209,210,211
v_mfma_f32_16x16x32_bf16 a[88:91], a[48:51], v[184:187], a[88:91]
v_mul_f32_e32 v212, v12, v212
v_mul_f32_e32 v213, v12, v213
v_mfma_f32_16x16x32_bf16 a[92:95], a[52:55], v[184:187], a[92:95]
Rb 36,10,1024
Cv 210,212,213
v_mfma_f32_16x16x32_bf16 a[84:87], a[52:55], v[176:179], a[84:87]
v_mul_f32_e32 v214, v12, v214
v_mul_f32_e32 v215, v12, v215
v_mfma_f32_16x16x32_bf16 a[96:99], a[56:59], v[176:179], a[96:99]
Rb 40,10,2048
Cv 211,214,215
v_mfma_f32_16x16x32_bf16 a[104:107], a[56:59], v[184:187], a[104:107]
v_mul_f32_e32 v216, v13, v216
v_mul_f32_e32 v217, v13, v217
v_mfma_f32_16x16x32_bf16 a[108:111], a[60:63], v[184:187], a[108:111]
Rb 44,10,3072
s_waitcnt vmcnt(15) lgkmcnt(4)
s_barrier
v_mfma_f32_16x16x32_bf16 a[100:103], a[60:63], v[176:179], a[100:103]
s_add_u32 s40, s67, 0x30000
Cv 216,216,217
v_mfma_f32_16x16x32_bf16 a[112:115], a[0:3], v[176:179], a[112:115]
Rb 48,10,4096
s_add_u32 m0, s36, 32768
v_mfma_f32_16x16x32_bf16 a[120:123], a[0:3], v[184:187], a[120:123]
buffer_load_dwordx4 v2, s[20:23], s40 offen offset:0 sc1 lds
v_mfma_f32_16x16x32_bf16 a[124:127], a[4:7], v[184:187], a[124:127]
Rb 52,10,5120
v_mul_f32_e32 v218, v13, v218
v_mfma_f32_16x16x32_bf16 a[116:119], a[4:7], v[176:179], a[116:119]
v_mul_f32_e32 v219, v13, v219
Cv 217,218,219
v_mfma_f32_16x16x32_bf16 a[128:131], a[8:11], v[176:179], a[128:131]
Rb 56,10,6144
v_mul_f32_e32 v220, v13, v220
v_mfma_f32_16x16x32_bf16 a[136:139], a[8:11], v[184:187], a[136:139]
v_mul_f32_e32 v221, v13, v221
Cv 218,220,221
v_mfma_f32_16x16x32_bf16 a[140:143], a[12:15], v[184:187], a[140:143]
Rb 60,10,8192
v_mfma_f32_16x16x32_bf16 a[132:135], a[12:15], v[176:179], a[132:135]
buffer_load_dwordx4 v2, s[20:23], s40 offen offset:1024 sc1 lds
v_mfma_f32_16x16x32_bf16 a[144:147], a[16:19], v[176:179], a[144:147]
Rb 0,10,9216
v_mul_f32_e32 v222, v13, v222
v_mfma_f32_16x16x32_bf16 a[152:155], a[16:19], v[184:187], a[152:155]
)" R"(
v_mul_f32_e32 v223, v13, v223
Cv 219,222,223
v_mfma_f32_16x16x32_bf16 a[156:159], a[20:23], v[184:187], a[156:159]
Rb 4,10,10240
v_mul_f32_e32 v224, v12, v224
v_mfma_f32_16x16x32_bf16 a[148:151], a[20:23], v[176:179], a[148:151]
v_mul_f32_e32 v225, v12, v225
Cv 224,224,225
v_mfma_f32_16x16x32_bf16 a[160:163], a[24:27], v[176:179], a[160:163]
Rb 8,10,11264
v_mul_f32_e32 v226, v12, v226
v_mfma_f32_16x16x32_bf16 a[168:171], a[24:27], v[184:187], a[168:171]
buffer_load_dwordx4 v2, s[20:23], s40 offen offset:2048 sc1 lds
v_mfma_f32_16x16x32_bf16 a[172:175], a[28:31], v[184:187], a[172:175]
Rb 12,10,12288
s_waitcnt lgkmcnt(8)
v_mfma_f32_16x16x32_bf16 a[164:167], a[28:31], v[176:179], a[164:167]
v_mul_f32_e32 v227, v12, v227
Cv 225,226,227
v_mfma_f32_16x16x32_bf16 a[64:67], a[32:35], v[192:195], a[64:67]
Rb 16,10,13312
v_mul_f32_e32 v228, v12, v228
v_mfma_f32_16x16x32_bf16 a[72:75], a[32:35], v[200:203], a[72:75]
v_mul_f32_e32 v229, v12, v229
Cv 226,228,229
v_mfma_f32_16x16x32_bf16 a[76:79], a[36:39], v[200:203], a[76:79]
Rb 20,10,14336
v_mul_f32_e32 v230, v12, v230
v_mfma_f32_16x16x32_bf16 a[68:71], a[36:39], v[192:195], a[68:71]
buffer_load_dwordx4 v2, s[20:23], s40 offen offset:3072 sc1 lds
v_mfma_f32_16x16x32_bf16 a[80:83], a[40:43], v[192:195], a[80:83]
Rb 24,10,16384
v_mul_f32_e32 v231, v12, v231
v_mfma_f32_16x16x32_bf16 a[88:91], a[40:43], v[200:203], a[88:91]
Cv 227,230,231
v_mul_f32_e32 v232, v13, v232
v_mfma_f32_16x16x32_bf16 a[92:95], a[44:47], v[200:203], a[92:95]
Rb 28,10,17408
s_waitcnt lgkmcnt(8)
v_mfma_f32_16x16x32_bf16 a[84:87], a[44:47], v[192:195], a[84:87]
v_mul_f32_e32 v233, v13, v233
Cv 232,232,233
v_mfma_f32_16x16x32_bf16 a[96:99], a[48:51], v[192:195], a[96:99]
Rb 32,10,18432
s_add_u32 m0, s36, 36864
v_mfma_f32_16x16x32_bf16 a[104:107], a[48:51], v[200:203], a[104:107]
buffer_load_dwordx4 v3, s[20:23], s40 offen offset:0 sc1 lds
v_mfma_f32_16x16x32_bf16 a[108:111], a[52:55], v[200:203], a[108:111]
Rb 36,10,19456
v_mul_f32_e32 v234, v13, v234
v_mfma_f32_16x16x32_bf16 a[100:103], a[52:55], v[192:195], a[100:103]
v_mul_f32_e32 v235, v13, v235
Cv 233,234,235
v_mfma_f32_16x16x32_bf16 a[112:115], a[56:59], v[192:195], a[112:115]
Rb 40,10,20480
v_mul_f32_e32 v236, v13, v236
v_mfma_f32_16x16x32_bf16 a[120:123], a[56:59], v[200:203], a[120:123]
v_mul_f32_e32 v237, v13, v237
Cv 234,236,237
v_mfma_f32_16x16x32_bf16 a[124:127], a[60:63], v[200:203], a[124:127]
Rb 44,10,21504
s_waitcnt lgkmcnt(8)
v_mfma_f32_16x16x32_bf16 a[116:119], a[60:63], v[192:195], a[116:119]
buffer_load_dwordx4 v3, s[20:23], s40 offen offset:1024 sc1 lds
)" R"(
v_mfma_f32_16x16x32_bf16 a[128:131], a[0:3], v[192:195], a[128:131]
Rb 48,10,22528
v_mul_f32_e32 v238, v13, v238
v_mfma_f32_16x16x32_bf16 a[136:139], a[0:3], v[200:203], a[136:139]
v_mul_f32_e32 v239, v13, v239
Cv 235,238,239
v_mfma_f32_16x16x32_bf16 a[140:143], a[4:7], v[200:203], a[140:143]
Rb 52,10,24576
v_mul_f32_e32 v240, v12, v240
v_mfma_f32_16x16x32_bf16 a[132:135], a[4:7], v[192:195], a[132:135]
v_mul_f32_e32 v241, v12, v241
Cv 240,240,241
v_mfma_f32_16x16x32_bf16 a[144:147], a[8:11], v[192:195], a[144:147]
Rb 56,10,25600
v_mul_f32_e32 v242, v12, v242
v_mfma_f32_16x16x32_bf16 a[152:155], a[8:11], v[200:203], a[152:155]
buffer_load_dwordx4 v3, s[20:23], s40 offen offset:2048 sc1 lds
v_mfma_f32_16x16x32_bf16 a[156:159], a[12:15], v[200:203], a[156:159]
Rb 60,10,26624
s_waitcnt lgkmcnt(8)
v_mfma_f32_16x16x32_bf16 a[148:151], a[12:15], v[192:195], a[148:151]
v_mul_f32_e32 v243, v12, v243
Cv 241,242,243
v_mfma_f32_16x16x32_bf16 a[160:163], a[16:19], v[192:195], a[160:163]
Rb 0,10,27648
v_mul_f32_e32 v244, v12, v244
v_mfma_f32_16x16x32_bf16 a[168:171], a[16:19], v[200:203], a[168:171]
v_mul_f32_e32 v245, v12, v245
Cv 242,244,245
v_mfma_f32_16x16x32_bf16 a[172:175], a[20:23], v[200:203], a[172:175]
Rb 4,10,28672
v_mul_f32_e32 v246, v12, v246
v_mfma_f32_16x16x32_bf16 a[164:167], a[20:23], v[192:195], a[164:167]
v_mul_f32_e32 v247, v12, v247
Cv 243,246,247
v_mfma_f32_16x16x32_bf16 a[64:67], a[24:27], v[208:211], a[64:67]
Rb 8,10,29696
v_mul_f32_e32 v248, v13, v248
v_mfma_f32_16x16x32_bf16 a[72:75], a[24:27], v[216:219], a[72:75]
v_mul_f32_e32 v249, v13, v249
Cv 248,248,249
v_mfma_f32_16x16x32_bf16 a[76:79], a[28:31], v[216:219], a[76:79]
Rb 12,10,30720
s_waitcnt lgkmcnt(8)
v_mfma_f32_16x16x32_bf16 a[68:71], a[28:31], v[208:211], a[68:71]
v_mul_f32_e32 v250, v13, v250
v_mul_f32_e32 v251, v13, v251
v_mfma_f32_16x16x32_bf16 a[80:83], a[32:35], v[208:211], a[80:83]
Rb 16,8,32768
Cv 249,250,251
v_mfma_f32_16x16x32_bf16 a[88:91], a[32:35], v[216:219], a[88:91]
v_mul_f32_e32 v252, v13, v252
v_mul_f32_e32 v253, v13, v253
v_mfma_f32_16x16x32_bf16 a[92:95], a[36:39], v[216:219], a[92:95]
Rb 20,8,33792
Cv 250,252,253
v_mfma_f32_16x16x32_bf16 a[84:87], a[36:39], v[208:211], a[84:87]
v_mul_f32_e32 v254, v13, v254
v_mul_f32_e32 v255, v13, v255
v_mfma_f32_16x16x32_bf16 a[96:99], a[40:43], v[208:211], a[96:99]
Rb 24,8,34816
Cv 251,254,255
v_mfma_f32_16x16x32_bf16 a[104:107], a[40:43], v[216:219], a[104:107]
v_mfma_f32_16x16x32_bf16 a[108:111], a[44:47], v[216:219], a[108:111]
)" R"(
Rb 28,8,35840
s_waitcnt vmcnt(15) lgkmcnt(4)
s_barrier
v_mfma_f32_16x16x32_bf16 a[100:103], a[44:47], v[208:211], a[100:103]
s_add_u32 s40, s67, 0x38000
v_mfma_f32_16x16x32_bf16 a[112:115], a[48:51], v[208:211], a[112:115]
Rb 32,8,36864
s_add_u32 m0, s36, 131072
v_mfma_f32_16x16x32_bf16 a[120:123], a[48:51], v[216:219], a[120:123]
buffer_load_dwordx4 v2, s[20:23], s40 offen offset:0 sc1 lds
v_mfma_f32_16x16x32_bf16 a[124:127], a[52:55], v[216:219], a[124:127]
Rb 36,8,37888
v_mfma_f32_16x16x32_bf16 a[116:119], a[52:55], v[208:211], a[116:119]
v_mfma_f32_16x16x32_bf16 a[128:131], a[56:59], v[208:211], a[128:131]
Rb 40,8,38912
v_mfma_f32_16x16x32_bf16 a[136:139], a[56:59], v[216:219], a[136:139]
v_mfma_f32_16x16x32_bf16 a[140:143], a[60:63], v[216:219], a[140:143]
Rb 44,8,40960
v_mfma_f32_16x16x32_bf16 a[132:135], a[60:63], v[208:211], a[132:135]
buffer_load_dwordx4 v2, s[20:23], s40 offen offset:1024 sc1 lds
v_mfma_f32_16x16x32_bf16 a[144:147], a[0:3], v[208:211], a[144:147]
Rb 48,8,41984
v_mfma_f32_16x16x32_bf16 a[152:155], a[0:3], v[216:219], a[152:155]
v_mfma_f32_16x16x32_bf16 a[156:159], a[4:7], v[216:219], a[156:159]
Rb 52,8,43008
v_mfma_f32_16x16x32_bf16 a[148:151], a[4:7], v[208:211], a[148:151]
v_mfma_f32_16x16x32_bf16 a[160:163], a[8:11], v[208:211], a[160:163]
Rb 56,8,44032
v_mfma_f32_16x16x32_bf16 a[168:171], a[8:11], v[216:219], a[168:171]
buffer_load_dwordx4 v2, s[20:23], s40 offen offset:2048 sc1 lds
v_mfma_f32_16x16x32_bf16 a[172:175], a[12:15], v[216:219], a[172:175]
Rb 60,8,45056
s_waitcnt lgkmcnt(8)
v_mfma_f32_16x16x32_bf16 a[164:167], a[12:15], v[208:211], a[164:167]
v_mfma_f32_16x16x32_bf16 a[64:67], a[16:19], v[224:227], a[64:67]
Rb 0,8,46080
v_mfma_f32_16x16x32_bf16 a[72:75], a[16:19], v[232:235], a[72:75]
v_mfma_f32_16x16x32_bf16 a[76:79], a[20:23], v[232:235], a[76:79]
Rb 4,8,47104
v_mfma_f32_16x16x32_bf16 a[68:71], a[20:23], v[224:227], a[68:71]
buffer_load_dwordx4 v2, s[20:23], s40 offen offset:3072 sc1 lds
v_mfma_f32_16x16x32_bf16 a[80:83], a[24:27], v[224:227], a[80:83]
Rb 8,8,49152
v_mfma_f32_16x16x32_bf16 a[88:91], a[24:27], v[232:235], a[88:91]
v_mfma_f32_16x16x32_bf16 a[92:95], a[28:31], v[232:235], a[92:95]
Rb 12,8,50176
s_waitcnt lgkmcnt(8)
v_mfma_f32_16x16x32_bf16 a[84:87], a[28:31], v[224:227], a[84:87]
v_mfma_f32_16x16x32_bf16 a[96:99], a[32:35], v[224:227], a[96:99]
Rb 16,8,51200
s_add_u32 m0, s36, 135168
v_mfma_f32_16x16x32_bf16 a[104:107], a[32:35], v[232:235], a[104:107]
buffer_load_dwordx4 v3, s[20:23], s40 offen offset:0 sc1 lds
v_mfma_f32_16x16x32_bf16 a[108:111], a[36:39], v[232:235], a[108:111]
Rb 20,8,52224
v_mfma_f32_16x16x32_bf16 a[100:103], a[36:39], v[224:227], a[100:103]
v_mfma_f32_16x16x32_bf16 a[112:115], a[40:43], v[224:227], a[112:115]
Rb 24,8,53248
v_mfma_f32_16x16x32_bf16 a[120:123], a[40:43], v[232:235], a[120:123]
v_mfma_f32_16x16x32_bf16 a[124:127], a[44:47], v[232:235], a[124:127]
Rb 28,8,54272
s_waitcnt lgkmcnt(8)
v_mfma_f32_16x16x32_bf16 a[116:119], a[44:47], v[224:227], a[116:119]
buffer_load_dwordx4 v3, s[20:23], s40 offen offset:1024 sc1 lds
v_mfma_f32_16x16x32_bf16 a[128:131], a[48:51], v[224:227], a[128:131]
)" R"(
Rb 32,8,55296
v_mfma_f32_16x16x32_bf16 a[136:139], a[48:51], v[232:235], a[136:139]
v_mfma_f32_16x16x32_bf16 a[140:143], a[52:55], v[232:235], a[140:143]
Rb 36,8,57344
v_mfma_f32_16x16x32_bf16 a[132:135], a[52:55], v[224:227], a[132:135]
v_mfma_f32_16x16x32_bf16 a[144:147], a[56:59], v[224:227], a[144:147]
Rb 40,8,58368
v_mfma_f32_16x16x32_bf16 a[152:155], a[56:59], v[232:235], a[152:155]
buffer_load_dwordx4 v3, s[20:23], s40 offen offset:2048 sc1 lds
v_mfma_f32_16x16x32_bf16 a[156:159], a[60:63], v[232:235], a[156:159]
Rb 44,8,59392
s_waitcnt lgkmcnt(8)
v_mfma_f32_16x16x32_bf16 a[148:151], a[60:63], v[224:227], a[148:151]
v_mfma_f32_16x16x32_bf16 a[160:163], a[0:3], v[224:227], a[160:163]
Rb 48,8,60416
v_mfma_f32_16x16x32_bf16 a[168:171], a[0:3], v[232:235], a[168:171]
v_mfma_f32_16x16x32_bf16 a[172:175], a[4:7], v[232:235], a[172:175]
Rb 52,8,61440
v_mfma_f32_16x16x32_bf16 a[164:167], a[4:7], v[224:227], a[164:167]
v_mfma_f32_16x16x32_bf16 a[64:67], a[8:11], v[240:243], a[64:67]
Rb 56,8,62464
v_mfma_f32_16x16x32_bf16 a[72:75], a[8:11], v[248:251], a[72:75]
v_mfma_f32_16x16x32_bf16 a[76:79], a[12:15], v[248:251], a[76:79]
Rb 60,8,63488
s_waitcnt lgkmcnt(8)
v_mfma_f32_16x16x32_bf16 a[68:71], a[12:15], v[240:243], a[68:71]
v_mfma_f32_16x16x32_bf16 a[80:83], a[16:19], v[240:243], a[80:83]
Rb 0,8,0
v_mfma_f32_16x16x32_bf16 a[88:91], a[16:19], v[248:251], a[88:91]
v_mfma_f32_16x16x32_bf16 a[92:95], a[20:23], v[248:251], a[92:95]
Rb 4,8,1024
v_mfma_f32_16x16x32_bf16 a[84:87], a[20:23], v[240:243], a[84:87]
v_mfma_f32_16x16x32_bf16 a[96:99], a[24:27], v[240:243], a[96:99]
Rb 8,8,2048
v_mfma_f32_16x16x32_bf16 a[104:107], a[24:27], v[248:251], a[104:107]
v_mfma_f32_16x16x32_bf16 a[108:111], a[28:31], v[248:251], a[108:111]
Rb 12,8,3072
s_waitcnt vmcnt(14) lgkmcnt(8)
s_barrier
v_mfma_f32_16x16x32_bf16 a[100:103], a[28:31], v[240:243], a[100:103]
v_mfma_f32_16x16x32_bf16 a[112:115], a[32:35], v[240:243], a[112:115]
Rb 16,8,4096
v_mfma_f32_16x16x32_bf16 a[120:123], a[32:35], v[248:251], a[120:123]
v_mfma_f32_16x16x32_bf16 a[124:127], a[36:39], v[248:251], a[124:127]
Rb 20,8,5120
v_mfma_f32_16x16x32_bf16 a[116:119], a[36:39], v[240:243], a[116:119]
v_mfma_f32_16x16x32_bf16 a[128:131], a[40:43], v[240:243], a[128:131]
Rb 24,8,6144
v_mfma_f32_16x16x32_bf16 a[136:139], a[40:43], v[248:251], a[136:139]
v_mfma_f32_16x16x32_bf16 a[140:143], a[44:47], v[248:251], a[140:143]
Rb 28,8,8192
s_waitcnt lgkmcnt(8)
v_mfma_f32_16x16x32_bf16 a[132:135], a[44:47], v[240:243], a[132:135]
v_mfma_f32_16x16x32_bf16 a[144:147], a[48:51], v[240:243], a[144:147]
Rb 32,8,9216
v_mfma_f32_16x16x32_bf16 a[152:155], a[48:51], v[248:251], a[152:155]
v_mfma_f32_16x16x32_bf16 a[156:159], a[52:55], v[248:251], a[156:159]
Rb 36,8,10240
v_mfma_f32_16x16x32_bf16 a[148:151], a[52:55], v[240:243], a[148:151]
v_mfma_f32_16x16x32_bf16 a[160:163], a[56:59], v[240:243], a[160:163]
Rb 40,8,11264
v_mfma_f32_16x16x32_bf16 a[168:171], a[56:59], v[248:251], a[168:171]
v_mfma_f32_16x16x32_bf16 a[172:175], a[60:63], v[248:251], a[172:175]
Rb 44,8,12288
s_waitcnt lgkmcnt(8)
)" R"(
v_mfma_f32_16x16x32_bf16 a[164:167], a[60:63], v[240:243], a[164:167]
v_mfma_f32_16x16x32_bf16 v[32:35], a[0:3], v[128:131], 0
Rb 48,8,13312
v_mfma_f32_16x16x32_bf16 v[40:43], a[0:3], v[136:139], 0
Mp 40,4,152
Rb 52,8,14336
v_accvgpr_read_b32 v112, a64
Mp 32,4,144
v_accvgpr_read_b32 v24, a65
Cv 112,112,24
Mp 32,8,160
Rb 56,8,16384
v_accvgpr_read_b32 v113, a66
Mp 40,8,168
v_accvgpr_read_b32 v25, a67
Cv 113,113,25
Mp 40,12,184
Rb 60,8,17408
s_waitcnt lgkmcnt(8)
Mp 32,12,176
v_accvgpr_read_b32 v114, a68
v_accvgpr_read_b32 v26, a69
Mp 32,16,192
Rb 0,8,18432
Cv 114,114,26
Mp 40,16,200
v_accvgpr_read_b32 v115, a70
v_accvgpr_read_b32 v27, a71
Mp 40,20,216
Rb 4,8,19456
Cv 115,115,27
Mp 32,20,208
s_add_u32 s29, s73, 0x0
buffer_store_dwordx4 v[112:115], v1, s[20:23], s29 offen sc0 sc1
Mp 32,24,224
Rb 8,8,20480
v_accvgpr_read_b32 v116, a72
Mp 40,24,232
v_accvgpr_read_b32 v24, a73
Cv 116,116,24
Mp 40,28,248
Rb 12,8,21504
s_waitcnt lgkmcnt(8)
Mp 32,28,240
v_accvgpr_read_b32 v117, a74
v_accvgpr_read_b32 v25, a75
v_mfma_f32_16x16x32_bf16 v[36:39], a[32:35], v[128:131], 0
Rb 16,8,22528
Cv 117,117,25
v_mfma_f32_16x16x32_bf16 v[44:47], a[32:35], v[136:139], 0
v_accvgpr_read_b32 v118, a76
v_accvgpr_read_b32 v26, a77
Cv 118,118,26
Mp 44,36,152
Rb 20,8,24576
v_accvgpr_read_b32 v119, a78
Mp 36,36,144
v_accvgpr_read_b32 v27, a79
Cv 119,119,27
Mp 36,40,160
Rb 24,8,25600
v_accvgpr_read_b32 v120, a80
Mp 44,40,168
s_add_u32 s29, s73, 0x400
)" R"(
buffer_store_dwordx4 v[116:119], v1, s[20:23], s29 offen sc0 sc1
Mp 44,44,184
Rb 28,8,26624
s_waitcnt lgkmcnt(8)
Mp 36,44,176
v_accvgpr_read_b32 v24, a81
Cv 120,120,24
Mp 36,48,192
Rb 32,8,27648
v_accvgpr_read_b32 v121, a82
Mp 44,48,200
v_accvgpr_read_b32 v25, a83
Cv 121,121,25
Mp 44,52,216
Rb 36,8,28672
v_accvgpr_read_b32 v122, a84
Mp 36,52,208
v_accvgpr_read_b32 v26, a85
Cv 122,122,26
Mp 36,56,224
Rb 40,8,29696
v_accvgpr_read_b32 v123, a86
Mp 44,56,232
v_accvgpr_read_b32 v27, a87
Cv 123,123,27
Mp 44,60,248
Rb 44,8,30720
s_waitcnt lgkmcnt(8)
Mp 36,60,240
s_add_u32 s29, s73, 0x800
buffer_store_dwordx4 v[120:123], v1, s[20:23], s29 offen sc0 sc1
v_mfma_f32_16x16x32_bf16 v[48:51], a[0:3], v[128:131], 0
Rb 48,1,0
v_accvgpr_read_b32 v124, a88
v_mfma_f32_16x16x32_bf16 v[56:59], a[0:3], v[136:139], 0
v_accvgpr_read_b32 v24, a89
Cv 124,124,24
v_accvgpr_read_b32 v125, a90
Mp 56,4,152
Rb 52,1,1024
v_accvgpr_read_b32 v25, a91
Mp 48,4,144
Cv 125,125,25
v_accvgpr_read_b32 v126, a92
Mp 48,8,160
Rb 56,1,2048
Cv 32,32,33
Mp 56,8,168
Cv 33,34,35
Cv 34,36,37
Mp 56,12,184
Rb 60,1,3072
s_waitcnt vmcnt(10) lgkmcnt(8)
s_barrier
Mp 48,12,176
Cv 35,38,39
v_accvgpr_read_b32 v26, a93
Mp 48,16,192
Rb 0,1,4096
Cv 126,126,26
Mp 56,16,200
s_add_u32 s29, s73, 0x3800
buffer_store_dwordx4 v[32:35], v1, s[20:23], s29 offen sc0 sc1
Mp 56,20,216
Rb 4,1,5120
)" R"(
v_accvgpr_read_b32 v127, a94
Mp 48,20,208
v_accvgpr_read_b32 v27, a95
Cv 127,127,27
Mp 48,24,224
Rb 8,1,6144
v_accvgpr_read_b32 v112, a96
Mp 56,24,232
s_add_u32 s29, s73, 0xc00
buffer_store_dwordx4 v[124:127], v1, s[20:23], s29 offen sc0 sc1
Mp 56,28,248
Rb 12,1,8192
s_waitcnt lgkmcnt(8)
Mp 48,28,240
v_accvgpr_read_b32 v24, a97
Cv 112,112,24
v_mfma_f32_16x16x32_bf16 v[52:55], a[32:35], v[128:131], 0
Rb 16,1,9216
Cv 40,40,41
v_mfma_f32_16x16x32_bf16 v[60:63], a[32:35], v[136:139], 0
Cv 41,42,43
Cv 42,44,45
v_accvgpr_read_b32 v113, a98
Mp 60,36,152
Rb 20,1,10240
Cv 43,46,47
Mp 52,36,144
s_add_u32 s29, s73, 0x3c00
buffer_store_dwordx4 v[40:43], v1, s[20:23], s29 offen sc0 sc1
Mp 52,40,160
Rb 24,1,11264
v_accvgpr_read_b32 v25, a99
Mp 60,40,168
Cv 113,113,25
v_accvgpr_read_b32 v114, a100
Mp 60,44,184
Rb 28,1,12288
s_waitcnt lgkmcnt(8)
Mp 52,44,176
v_accvgpr_read_b32 v26, a101
Cv 114,114,26
Mp 52,48,192
Rb 32,1,13312
v_accvgpr_read_b32 v115, a102
Mp 60,48,200
v_accvgpr_read_b32 v27, a103
Cv 115,115,27
Mp 60,52,216
Rb 36,1,14336
v_accvgpr_read_b32 v116, a104
Mp 52,52,208
s_add_u32 s29, s73, 0x1000
buffer_store_dwordx4 v[112:115], v1, s[20:23], s29 offen sc0 sc1
Mp 52,56,224
Rb 40,1,16384
v_accvgpr_read_b32 v24, a105
Mp 60,56,232
Cv 116,116,24
v_accvgpr_read_b32 v117, a106
Mp 60,60,248
Rb 44,1,17408
s_waitcnt lgkmcnt(8)
Mp 52,60,240
v_accvgpr_read_b32 v25, a107
)" R"(
Cv 117,117,25
v_mfma_f32_16x16x32_bf16 v[64:67], a[0:3], v[128:131], 0
Rb 48,1,18432
v_accvgpr_read_b32 v118, a108
v_mfma_f32_16x16x32_bf16 v[72:75], a[0:3], v[136:139], 0
v_accvgpr_read_b32 v26, a109
Cv 118,118,26
v_accvgpr_read_b32 v119, a110
Mp 72,4,152
Rb 52,1,19456
v_accvgpr_read_b32 v27, a111
Mp 64,4,144
Cv 119,119,27
v_accvgpr_read_b32 v120, a112
Mp 64,8,160
Rb 56,1,20480
Cv 48,48,49
Mp 72,8,168
Cv 49,50,51
Cv 50,52,53
Mp 72,12,184
Rb 60,1,21504
s_waitcnt lgkmcnt(8)
Mp 64,12,176
Cv 51,54,55
v_accvgpr_read_b32 v24, a113
Mp 64,16,192
Rb 0,1,22528
Cv 120,120,24
Mp 72,16,200
s_add_u32 s29, s73, 0x4000
buffer_store_dwordx4 v[48:51], v1, s[20:23], s29 offen sc0 sc1
Mp 72,20,216
Rb 4,1,24576
v_accvgpr_read_b32 v121, a114
Mp 64,20,208
s_add_u32 s29, s73, 0x1400
buffer_store_dwordx4 v[116:119], v1, s[20:23], s29 offen sc0 sc1
Mp 64,24,224
Rb 8,1,25600
v_accvgpr_read_b32 v25, a115
Mp 72,24,232
Cv 121,121,25
v_accvgpr_read_b32 v122, a116
Mp 72,28,248
Rb 12,1,26624
s_waitcnt lgkmcnt(8)
Mp 64,28,240
v_accvgpr_read_b32 v26, a117
Cv 122,122,26
v_mfma_f32_16x16x32_bf16 v[68:71], a[32:35], v[128:131], 0
Rb 16,1,27648
Cv 56,56,57
v_mfma_f32_16x16x32_bf16 v[76:79], a[32:35], v[136:139], 0
Cv 57,58,59
Cv 58,60,61
v_accvgpr_read_b32 v123, a118
Mp 76,36,152
Rb 20,1,28672
Cv 59,62,63
Mp 68,36,144
s_add_u32 s29, s73, 0x4400
buffer_store_dwordx4 v[56:59], v1, s[20:23], s29 offen sc0 sc1
Mp 68,40,160
)" R"(
Rb 24,1,29696
v_accvgpr_read_b32 v27, a119
Mp 76,40,168
Cv 123,123,27
v_accvgpr_read_b32 v124, a120
Mp 76,44,184
Rb 28,1,30720
s_waitcnt lgkmcnt(8)
Mp 68,44,176
s_add_u32 s29, s73, 0x1800
buffer_store_dwordx4 v[120:123], v1, s[20:23], s29 offen sc0 sc1
Mp 68,48,192
Rb 32,1,32768
v_accvgpr_read_b32 v24, a121
Mp 76,48,200
Cv 124,124,24
v_accvgpr_read_b32 v125, a122
Mp 76,52,216
Rb 36,1,33792
v_accvgpr_read_b32 v25, a123
Mp 68,52,208
Cv 125,125,25
v_accvgpr_read_b32 v126, a124
Mp 68,56,224
Rb 40,1,34816
v_accvgpr_read_b32 v26, a125
Mp 76,56,232
Cv 126,126,26
v_accvgpr_read_b32 v127, a126
Mp 76,60,248
Rb 44,1,35840
s_waitcnt vmcnt(11) lgkmcnt(8)
s_barrier
Mp 68,60,240
v_accvgpr_read_b32 v27, a127
Cv 127,127,27
v_mfma_f32_16x16x32_bf16 v[80:83], a[0:3], v[128:131], 0
Rb 48,1,36864
v_accvgpr_read_b32 v112, a128
v_mfma_f32_16x16x32_bf16 v[88:91], a[0:3], v[136:139], 0
s_add_u32 s29, s73, 0x1c00
buffer_store_dwordx4 v[124:127], v1, s[20:23], s29 offen sc0 sc1
Mp 88,4,152
Rb 52,1,37888
v_accvgpr_read_b32 v24, a129
Mp 80,4,144
Cv 112,112,24
v_accvgpr_read_b32 v113, a130
Mp 80,8,160
Rb 56,1,38912
Cv 64,64,65
Mp 88,8,168
Cv 65,66,67
Cv 66,68,69
Mp 88,12,184
Rb 60,1,40960
s_waitcnt lgkmcnt(8)
Mp 80,12,176
Cv 67,70,71
v_accvgpr_read_b32 v25, a131
Mp 80,16,192
Rb 0,1,41984
Cv 113,113,25
Mp 88,16,200
s_add_u32 s29, s73, 0x4800
)" R"(
buffer_store_dwordx4 v[64:67], v1, s[20:23], s29 offen sc0 sc1
Mp 88,20,216
Rb 4,1,43008
v_accvgpr_read_b32 v114, a132
Mp 80,20,208
v_accvgpr_read_b32 v26, a133
Cv 114,114,26
Mp 80,24,224
Rb 8,1,44032
v_accvgpr_read_b32 v115, a134
Mp 88,24,232
v_accvgpr_read_b32 v27, a135
Cv 115,115,27
Mp 88,28,248
Rb 12,1,45056
s_waitcnt lgkmcnt(8)
Mp 80,28,240
s_add_u32 s29, s73, 0x2000
buffer_store_dwordx4 v[112:115], v1, s[20:23], s29 offen sc0 sc1
v_mfma_f32_16x16x32_bf16 v[84:87], a[32:35], v[128:131], 0
Rb 16,1,46080
Cv 72,72,73
v_mfma_f32_16x16x32_bf16 v[92:95], a[32:35], v[136:139], 0
Cv 73,74,75
Cv 74,76,77
v_accvgpr_read_b32 v116, a136
Mp 92,36,152
Rb 20,1,47104
Cv 75,78,79
Mp 84,36,144
s_add_u32 s29, s73, 0x4c00
buffer_store_dwordx4 v[72:75], v1, s[20:23], s29 offen sc0 sc1
Mp 84,40,160
Rb 24,1,49152
v_accvgpr_read_b32 v24, a137
Mp 92,40,168
Cv 116,116,24
v_accvgpr_read_b32 v117, a138
Mp 92,44,184
Rb 28,1,50176
s_waitcnt lgkmcnt(8)
Mp 84,44,176
v_accvgpr_read_b32 v25, a139
Cv 117,117,25
Mp 84,48,192
Rb 32,1,51200
v_accvgpr_read_b32 v118, a140
Mp 92,48,200
v_accvgpr_read_b32 v26, a141
Cv 118,118,26
Mp 92,52,216
Rb 36,1,52224
v_accvgpr_read_b32 v119, a142
Mp 84,52,208
v_accvgpr_read_b32 v27, a143
Cv 119,119,27
Mp 84,56,224
Rb 40,1,53248
v_accvgpr_read_b32 v120, a144
Mp 92,56,232
s_add_u32 s29, s73, 0x2400
buffer_store_dwordx4 v[116:119], v1, s[20:23], s29 offen sc0 sc1
Mp 92,60,248
Rb 44,1,54272
)" R"(
s_waitcnt lgkmcnt(8)
Mp 84,60,240
v_accvgpr_read_b32 v24, a145
Cv 120,120,24
v_mfma_f32_16x16x32_bf16 v[96:99], a[0:3], v[128:131], 0
Rb 48,1,55296
v_accvgpr_read_b32 v121, a146
v_mfma_f32_16x16x32_bf16 v[104:107], a[0:3], v[136:139], 0
v_accvgpr_read_b32 v25, a147
Cv 121,121,25
v_accvgpr_read_b32 v122, a148
Mp 104,4,152
Rb 52,1,57344
v_accvgpr_read_b32 v26, a149
Mp 96,4,144
Cv 122,122,26
v_accvgpr_read_b32 v123, a150
Mp 96,8,160
Rb 56,1,58368
Cv 80,80,81
Mp 104,8,168
Cv 81,82,83
Cv 82,84,85
Mp 104,12,184
Rb 60,1,59392
s_waitcnt lgkmcnt(8)
Mp 96,12,176
Cv 83,86,87
v_accvgpr_read_b32 v27, a151
Mp 96,16,192
Rb 0,1,60416
Cv 123,123,27
Mp 104,16,200
s_add_u32 s29, s73, 0x5000
buffer_store_dwordx4 v[80:83], v1, s[20:23], s29 offen sc0 sc1
Mp 104,20,216
Rb 4,1,61440
v_accvgpr_read_b32 v124, a152
Mp 96,20,208
s_add_u32 s29, s73, 0x2800
buffer_store_dwordx4 v[120:123], v1, s[20:23], s29 offen sc0 sc1
Mp 96,24,224
Rb 8,1,62464
v_accvgpr_read_b32 v24, a153
Mp 104,24,232
Cv 124,124,24
v_accvgpr_read_b32 v125, a154
Mp 104,28,248
Rb 12,1,63488
s_waitcnt lgkmcnt(8)
Mp 96,28,240
v_accvgpr_read_b32 v25, a155
Cv 125,125,25
v_mfma_f32_16x16x32_bf16 v[100:103], a[32:35], v[128:131], 0
Rb 16,10,0
Cv 88,88,89
v_mfma_f32_16x16x32_bf16 v[108:111], a[32:35], v[136:139], 0
Cv 89,90,91
Cv 90,92,93
v_accvgpr_read_b32 v126, a156
Mp 108,36,152
Rb 20,10,1024
Cv 91,94,95
Mp 100,36,144
)" R"(
s_add_u32 s29, s73, 0x5400
buffer_store_dwordx4 v[88:91], v1, s[20:23], s29 offen sc0 sc1
Mp 100,40,160
Rb 24,10,2048
v_accvgpr_read_b32 v26, a157
Mp 108,40,168
Cv 126,126,26
v_accvgpr_read_b32 v127, a158
Mp 108,44,184
Rb 28,10,3072
s_waitcnt lgkmcnt(8)
Mp 100,44,176
v_accvgpr_read_b32 v27, a159
Cv 127,127,27
Mp 100,48,192
Rb 32,10,4096
v_accvgpr_read_b32 v112, a160
Mp 108,48,200
s_add_u32 s29, s73, 0x2c00
buffer_store_dwordx4 v[124:127], v1, s[20:23], s29 offen sc0 sc1
Mp 108,52,216
Rb 36,10,5120
v_accvgpr_read_b32 v24, a161
Mp 100,52,208
Cv 112,112,24
v_accvgpr_read_b32 v113, a162
Mp 100,56,224
Rb 40,10,6144
v_accvgpr_read_b32 v25, a163
Mp 108,56,232
Cv 113,113,25
v_accvgpr_read_b32 v114, a164
Mp 108,60,248
Rb 44,10,8192
s_waitcnt lgkmcnt(8)
Mp 100,60,240
v_accvgpr_read_b32 v26, a165
Cv 114,114,26
v_mfma_f32_16x16x32_bf16 v[132:135], a[0:3], v[128:131], 0
Rb 48,10,9216
v_accvgpr_read_b32 v115, a166
v_mfma_f32_16x16x32_bf16 v[148:151], a[0:3], v[136:139], 0
v_accvgpr_read_b32 v27, a167
Cv 115,115,27
v_accvgpr_read_b32 v116, a168
Mp 148,4,152
Rb 52,10,10240
v_accvgpr_read_b32 v24, a169
Mp 132,4,144
s_add_u32 s29, s73, 0x3000
buffer_store_dwordx4 v[112:115], v1, s[20:23], s29 offen sc0 sc1
Mp 132,8,160
Rb 56,10,11264
Cv 96,96,97
Mp 148,8,168
Cv 97,98,99
Cv 98,100,101
Mp 148,12,184
Rb 60,10,12288
s_waitcnt lgkmcnt(8)
Mp 132,12,176
Cv 99,102,103
Cv 116,116,24
Mp 132,16,192
)" R"(
Rb 0,10,13312
v_accvgpr_read_b32 v117, a170
Mp 148,16,200
s_add_u32 s29, s73, 0x5800
buffer_store_dwordx4 v[96:99], v1, s[20:23], s29 offen sc0 sc1
Mp 148,20,216
Rb 4,10,14336
v_accvgpr_read_b32 v25, a171
Mp 132,20,208
Cv 117,117,25
v_accvgpr_read_b32 v118, a172
Mp 132,24,224
Rb 8,10,16384
v_accvgpr_read_b32 v26, a173
Mp 148,24,232
Cv 118,118,26
v_accvgpr_read_b32 v119, a174
Mp 148,28,248
Rb 12,10,17408
s_waitcnt lgkmcnt(8)
Mp 132,28,240
v_accvgpr_read_b32 v27, a175
Cv 119,119,27
v_mfma_f32_16x16x32_bf16 v[140:143], a[32:35], v[128:131], 0
Rb 16,10,18432
Cv 104,104,105
v_mfma_f32_16x16x32_bf16 v[156:159], a[32:35], v[136:139], 0
Cv 105,106,107
Cv 106,108,109
Mp 156,36,152
Rb 20,10,19456
Cv 107,110,111
Mp 140,36,144
s_add_u32 s29, s73, 0x5c00
buffer_store_dwordx4 v[104:107], v1, s[20:23], s29 offen sc0 sc1
Mp 140,40,160
Rb 24,10,20480
Mp 156,40,168
s_add_u32 s29, s73, 0x3400
buffer_store_dwordx4 v[116:119], v1, s[20:23], s29 offen sc0 sc1
Mp 156,44,184
Rb 28,10,21504
s_waitcnt lgkmcnt(8)
Mp 140,44,176
Mp 140,48,192
Rb 32,10,22528
Mp 156,48,200
Mp 156,52,216
Rb 36,10,24576
Mp 140,52,208
Mp 140,56,224
Rb 40,10,25600
Mp 156,56,232
Mp 156,60,248
Rb 44,10,26624
s_waitcnt lgkmcnt(8)
Mp 140,60,240
v_mfma_f32_16x16x32_bf16 v[164:167], a[0:3], v[128:131], 0
Rb 48,10,27648
v_mfma_f32_16x16x32_bf16 v[180:183], a[0:3], v[136:139], 0
Mp 180,4,152
Rb 52,10,28672
Mp 164,4,144
Mp 164,8,160
)" R"(
Rb 56,10,29696
Cv 132,132,133
Mp 180,8,168
Cv 133,134,135
Cv 134,140,141
Mp 180,12,184
Rb 60,10,30720
s_waitcnt lgkmcnt(8)
Mp 164,12,176
Cv 135,142,143
Mp 164,16,192
s_add_u32 s29, s73, 0x6000
buffer_store_dwordx4 v[132:135], v1, s[20:23], s29 offen sc0 sc1
Mp 180,16,200
Mp 180,20,216
Mp 164,20,208
Mp 164,24,224
Mp 180,24,232
Mp 180,28,248
s_waitcnt lgkmcnt(4)
Mp 164,28,240
v_mfma_f32_16x16x32_bf16 v[172:175], a[32:35], v[128:131], 0
Cv 148,148,149
Cv 149,150,151
v_mfma_f32_16x16x32_bf16 v[188:191], a[32:35], v[136:139], 0
Cv 150,156,157
Cv 151,158,159
Mp 188,36,152
s_add_u32 s29, s73, 0x6400
buffer_store_dwordx4 v[148:151], v1, s[20:23], s29 offen sc0 sc1
Mp 172,36,144
Mp 172,40,160
Mp 188,40,168
Mp 188,44,184
s_waitcnt lgkmcnt(0)
Mp 172,44,176
Mp 172,48,192
Mp 188,48,200
Mp 188,52,216
Mp 172,52,208
Mp 172,56,224
Mp 188,56,232
Mp 188,60,248
s_waitcnt vmcnt(2)
buffer_store_dword v6, v7, s[20:23], s68 offen offset:0 sc0 sc1
Mp 172,60,240
s_nop 7
Cv 164,164,165
Cv 165,166,167
Cv 166,172,173
Cv 167,174,175
s_add_u32 s29, s73, 0x6800
buffer_store_dwordx4 v[164:167], v1, s[20:23], s29 offen sc0 sc1
Cv 180,180,181
Cv 181,182,183
Cv 182,188,189
Cv 183,190,191
s_add_u32 s29, s73, 0x6c00
buffer_store_dwordx4 v[180:183], v1, s[20:23], s29 offen sc0 sc1
s_branch .LzEND
.LzMASK0:
s_lshl_b32 s28, s57, 6
s_sub_u32 s28, s28, 128
v_add_u32 v11, s28, v9
)" R"(
s_mov_b32 s29, 0xff800000
s_nop 2
v_cmpx_gt_i32_e32 vcc, 51, v11
s_cbranch_execz .LzMASKE0
v_mov_b32 v91, s29
v_cmpx_gt_i32_e32 vcc, 50, v11
v_mov_b32 v90, s29
v_cmpx_gt_i32_e32 vcc, 49, v11
v_mov_b32 v89, s29
v_cmpx_gt_i32_e32 vcc, 48, v11
v_mov_b32 v88, s29
v_cmpx_gt_i32_e32 vcc, 35, v11
s_cbranch_execz .LzMASKE0
v_mov_b32 v83, s29
v_mov_b32 v95, s29
v_cmpx_gt_i32_e32 vcc, 34, v11
v_mov_b32 v82, s29
v_mov_b32 v94, s29
v_cmpx_gt_i32_e32 vcc, 33, v11
v_mov_b32 v81, s29
v_mov_b32 v93, s29
v_cmpx_gt_i32_e32 vcc, 32, v11
v_mov_b32 v80, s29
v_mov_b32 v92, s29
v_cmpx_gt_i32_e32 vcc, 19, v11
s_cbranch_execz .LzMASKE0
v_mov_b32 v75, s29
v_mov_b32 v87, s29
v_cmpx_gt_i32_e32 vcc, 18, v11
v_mov_b32 v74, s29
v_mov_b32 v86, s29
v_cmpx_gt_i32_e32 vcc, 17, v11
v_mov_b32 v73, s29
v_mov_b32 v85, s29
v_cmpx_gt_i32_e32 vcc, 16, v11
v_mov_b32 v72, s29
v_mov_b32 v84, s29
v_cmpx_gt_i32_e32 vcc, 3, v11
s_cbranch_execz .LzMASKE0
v_mov_b32 v67, s29
v_mov_b32 v79, s29
v_cmpx_gt_i32_e32 vcc, 2, v11
v_mov_b32 v66, s29
v_mov_b32 v78, s29
v_cmpx_gt_i32_e32 vcc, 1, v11
v_mov_b32 v65, s29
v_mov_b32 v77, s29
v_cmpx_gt_i32_e32 vcc, 0, v11
v_mov_b32 v64, s29
v_mov_b32 v76, s29
v_cmpx_gt_i32_e32 vcc, -13, v11
s_cbranch_execz .LzMASKE0
v_mov_b32 v71, s29
v_cmpx_gt_i32_e32 vcc, -14, v11
v_mov_b32 v70, s29
v_cmpx_gt_i32_e32 vcc, -15, v11
v_mov_b32 v69, s29
v_cmpx_gt_i32_e32 vcc, -16, v11
v_mov_b32 v68, s29
.LzMASKE0:
s_mov_b64 exec, -1
s_nop 1
s_setpc_b64 s[30:31]
.LzFIX0:
)" R"(
s_nop 7
s_mov_b32 s63, 1
v_max_f32_e32 v28, v64, v65
v_max3_f32 v28, v28, v66, v67
v_max3_f32 v28, v28, v72, v73
v_max3_f32 v28, v28, v74, v75
v_max3_f32 v28, v28, v80, v81
v_max3_f32 v28, v28, v82, v83
v_max3_f32 v28, v28, v88, v89
v_max3_f32 v28, v28, v90, v91
v_mov_b32 v24, v28
s_nop 1
v_permlane32_swap_b32_e32 v28, v24
v_max_f32_e32 v28, v28, v24
v_mov_b32 v24, v28
s_nop 1
v_permlane16_swap_b32_e32 v28, v24
v_max_f32_e32 v28, v28, v24
v_max_f32_e32 v29, v68, v69
v_max3_f32 v29, v29, v70, v71
v_max3_f32 v29, v29, v76, v77
v_max3_f32 v29, v29, v78, v79
v_max3_f32 v29, v29, v84, v85
v_max3_f32 v29, v29, v86, v87
v_max3_f32 v29, v29, v92, v93
v_max3_f32 v29, v29, v94, v95
v_mov_b32 v24, v29
s_nop 1
v_permlane32_swap_b32_e32 v29, v24
v_max_f32_e32 v29, v29, v24
v_mov_b32 v24, v29
s_nop 1
v_permlane16_swap_b32_e32 v29, v24
v_max_f32_e32 v29, v29, v24
s_bitcmp1_b32 s88, 0
s_cbranch_scc1 .LzFIXF0
v_max_f32_e32 v28, 0, v28
v_sub_f32_e32 v24, 0, v28
v_exp_f32_e32 v24, v24
s_nop 0
v_mul_f32_e32 v12, v24, v12
v_mul_f32_e32 v128, v24, v128
v_mul_f32_e32 v129, v24, v129
v_mul_f32_e32 v130, v24, v130
v_mul_f32_e32 v131, v24, v131
v_mul_f32_e32 v132, v24, v132
v_mul_f32_e32 v133, v24, v133
v_mul_f32_e32 v134, v24, v134
v_mul_f32_e32 v135, v24, v135
v_mul_f32_e32 v144, v24, v144
v_mul_f32_e32 v145, v24, v145
v_mul_f32_e32 v146, v24, v146
v_mul_f32_e32 v147, v24, v147
v_mul_f32_e32 v148, v24, v148
v_mul_f32_e32 v149, v24, v149
v_mul_f32_e32 v150, v24, v150
v_mul_f32_e32 v151, v24, v151
v_mul_f32_e32 v160, v24, v160
v_mul_f32_e32 v161, v24, v161
v_mul_f32_e32 v162, v24, v162
v_mul_f32_e32 v163, v24, v163
v_mul_f32_e32 v164, v24, v164
v_mul_f32_e32 v165, v24, v165
v_mul_f32_e32 v166, v24, v166
)" R"(
v_mul_f32_e32 v167, v24, v167
v_mul_f32_e32 v176, v24, v176
v_mul_f32_e32 v177, v24, v177
v_mul_f32_e32 v178, v24, v178
v_mul_f32_e32 v179, v24, v179
v_mul_f32_e32 v180, v24, v180
v_mul_f32_e32 v181, v24, v181
v_mul_f32_e32 v182, v24, v182
v_mul_f32_e32 v183, v24, v183
v_mul_f32_e32 v192, v24, v192
v_mul_f32_e32 v193, v24, v193
v_mul_f32_e32 v194, v24, v194
v_mul_f32_e32 v195, v24, v195
v_mul_f32_e32 v196, v24, v196
v_mul_f32_e32 v197, v24, v197
v_mul_f32_e32 v198, v24, v198
v_mul_f32_e32 v199, v24, v199
v_mul_f32_e32 v208, v24, v208
v_mul_f32_e32 v209, v24, v209
v_mul_f32_e32 v210, v24, v210
v_mul_f32_e32 v211, v24, v211
v_mul_f32_e32 v212, v24, v212
v_mul_f32_e32 v213, v24, v213
v_mul_f32_e32 v214, v24, v214
v_mul_f32_e32 v215, v24, v215
v_mul_f32_e32 v224, v24, v224
v_mul_f32_e32 v225, v24, v225
v_mul_f32_e32 v226, v24, v226
v_mul_f32_e32 v227, v24, v227
v_mul_f32_e32 v228, v24, v228
v_mul_f32_e32 v229, v24, v229
v_mul_f32_e32 v230, v24, v230
v_mul_f32_e32 v231, v24, v231
v_mul_f32_e32 v240, v24, v240
v_mul_f32_e32 v241, v24, v241
v_mul_f32_e32 v242, v24, v242
v_mul_f32_e32 v243, v24, v243
v_mul_f32_e32 v244, v24, v244
v_mul_f32_e32 v245, v24, v245
v_mul_f32_e32 v246, v24, v246
v_mul_f32_e32 v247, v24, v247
v_max_f32_e32 v29, 0, v29
v_sub_f32_e32 v24, 0, v29
v_exp_f32_e32 v24, v24
s_nop 0
v_mul_f32_e32 v13, v24, v13
v_mul_f32_e32 v136, v24, v136
v_mul_f32_e32 v137, v24, v137
v_mul_f32_e32 v138, v24, v138
v_mul_f32_e32 v139, v24, v139
v_mul_f32_e32 v140, v24, v140
v_mul_f32_e32 v141, v24, v141
v_mul_f32_e32 v142, v24, v142
v_mul_f32_e32 v143, v24, v143
v_mul_f32_e32 v152, v24, v152
v_mul_f32_e32 v153, v24, v153
v_mul_f32_e32 v154, v24, v154
v_mul_f32_e32 v155, v24, v155
v_mul_f32_e32 v156, v24, v156
v_mul_f32_e32 v157, v24, v157
v_mul_f32_e32 v158, v24, v158
v_mul_f32_e32 v159, v24, v159
v_mul_f32_e32 v168, v24, v168
v_mul_f32_e32 v169, v24, v169
)" R"(
v_mul_f32_e32 v170, v24, v170
v_mul_f32_e32 v171, v24, v171
v_mul_f32_e32 v172, v24, v172
v_mul_f32_e32 v173, v24, v173
v_mul_f32_e32 v174, v24, v174
v_mul_f32_e32 v175, v24, v175
v_mul_f32_e32 v184, v24, v184
v_mul_f32_e32 v185, v24, v185
v_mul_f32_e32 v186, v24, v186
v_mul_f32_e32 v187, v24, v187
v_mul_f32_e32 v188, v24, v188
v_mul_f32_e32 v189, v24, v189
v_mul_f32_e32 v190, v24, v190
v_mul_f32_e32 v191, v24, v191
v_mul_f32_e32 v200, v24, v200
v_mul_f32_e32 v201, v24, v201
v_mul_f32_e32 v202, v24, v202
v_mul_f32_e32 v203, v24, v203
v_mul_f32_e32 v204, v24, v204
v_mul_f32_e32 v205, v24, v205
v_mul_f32_e32 v206, v24, v206
v_mul_f32_e32 v207, v24, v207
v_mul_f32_e32 v216, v24, v216
v_mul_f32_e32 v217, v24, v217
v_mul_f32_e32 v218, v24, v218
v_mul_f32_e32 v219, v24, v219
v_mul_f32_e32 v220, v24, v220
v_mul_f32_e32 v221, v24, v221
v_mul_f32_e32 v222, v24, v222
v_mul_f32_e32 v223, v24, v223
v_mul_f32_e32 v232, v24, v232
v_mul_f32_e32 v233, v24, v233
v_mul_f32_e32 v234, v24, v234
v_mul_f32_e32 v235, v24, v235
v_mul_f32_e32 v236, v24, v236
v_mul_f32_e32 v237, v24, v237
v_mul_f32_e32 v238, v24, v238
v_mul_f32_e32 v239, v24, v239
v_mul_f32_e32 v248, v24, v248
v_mul_f32_e32 v249, v24, v249
v_mul_f32_e32 v250, v24, v250
v_mul_f32_e32 v251, v24, v251
v_mul_f32_e32 v252, v24, v252
v_mul_f32_e32 v253, v24, v253
v_mul_f32_e32 v254, v24, v254
v_mul_f32_e32 v255, v24, v255
s_branch .LzFIXG0
.LzFIXF0:
v_cmp_lt_f32_e32 vcc, 0xff7fffff, v28
s_nop 1
v_cndmask_b32_e32 v28, 0, v28, vcc
v_cmp_lt_f32_e32 vcc, 0xff7fffff, v29
s_nop 1
v_cndmask_b32_e32 v29, 0, v29, vcc
.LzFIXG0:
v_sub_f32_e32 v16, v16, v28
v_sub_f32_e32 v17, v17, v28
v_sub_f32_e32 v18, v18, v28
v_sub_f32_e32 v19, v19, v28
v_sub_f32_e32 v64, v64, v28
v_sub_f32_e32 v65, v65, v28
v_sub_f32_e32 v66, v66, v28
v_sub_f32_e32 v67, v67, v28
v_sub_f32_e32 v72, v72, v28
)" R"(
v_sub_f32_e32 v73, v73, v28
v_sub_f32_e32 v74, v74, v28
v_sub_f32_e32 v75, v75, v28
v_sub_f32_e32 v80, v80, v28
v_sub_f32_e32 v81, v81, v28
v_sub_f32_e32 v82, v82, v28
v_sub_f32_e32 v83, v83, v28
v_sub_f32_e32 v88, v88, v28
v_sub_f32_e32 v89, v89, v28
v_sub_f32_e32 v90, v90, v28
v_sub_f32_e32 v91, v91, v28
v_sub_f32_e32 v20, v20, v29
v_sub_f32_e32 v21, v21, v29
v_sub_f32_e32 v22, v22, v29
v_sub_f32_e32 v23, v23, v29
v_sub_f32_e32 v68, v68, v29
v_sub_f32_e32 v69, v69, v29
v_sub_f32_e32 v70, v70, v29
v_sub_f32_e32 v71, v71, v29
v_sub_f32_e32 v76, v76, v29
v_sub_f32_e32 v77, v77, v29
v_sub_f32_e32 v78, v78, v29
v_sub_f32_e32 v79, v79, v29
v_sub_f32_e32 v84, v84, v29
v_sub_f32_e32 v85, v85, v29
v_sub_f32_e32 v86, v86, v29
v_sub_f32_e32 v87, v87, v29
v_sub_f32_e32 v92, v92, v29
v_sub_f32_e32 v93, v93, v29
v_sub_f32_e32 v94, v94, v29
v_sub_f32_e32 v95, v95, v29
s_bitcmp0_b32 s88, 1
s_cbranch_scc1 .LzFIXH0
v_sub_f32_e32 v96, v96, v28
v_sub_f32_e32 v97, v97, v28
v_sub_f32_e32 v98, v98, v28
v_sub_f32_e32 v99, v99, v28
v_sub_f32_e32 v104, v104, v28
v_sub_f32_e32 v105, v105, v28
v_sub_f32_e32 v106, v106, v28
v_sub_f32_e32 v107, v107, v28
v_sub_f32_e32 v112, v112, v28
v_sub_f32_e32 v113, v113, v28
v_sub_f32_e32 v114, v114, v28
v_sub_f32_e32 v115, v115, v28
v_sub_f32_e32 v120, v120, v28
v_sub_f32_e32 v121, v121, v28
v_sub_f32_e32 v122, v122, v28
v_sub_f32_e32 v123, v123, v28
v_sub_f32_e32 v100, v100, v29
v_sub_f32_e32 v101, v101, v29
v_sub_f32_e32 v102, v102, v29
v_sub_f32_e32 v103, v103, v29
v_sub_f32_e32 v108, v108, v29
v_sub_f32_e32 v109, v109, v29
v_sub_f32_e32 v110, v110, v29
v_sub_f32_e32 v111, v111, v29
v_sub_f32_e32 v116, v116, v29
v_sub_f32_e32 v117, v117, v29
v_sub_f32_e32 v118, v118, v29
v_sub_f32_e32 v119, v119, v29
v_sub_f32_e32 v124, v124, v29
v_sub_f32_e32 v125, v125, v29
v_sub_f32_e32 v126, v126, v29
)" R"(
v_sub_f32_e32 v127, v127, v29
.LzFIXH0:
v_exp_f32_e32 v32, v64
v_exp_f32_e32 v28, v65
s_nop 0
v_add_f32_e32 v14, v32, v28
Cv 32,32,28
v_exp_f32_e32 v33, v66
v_exp_f32_e32 v29, v67
v_add_f32_e32 v14, v14, v33
v_add_f32_e32 v14, v14, v29
Cv 33,33,29
v_exp_f32_e32 v34, v72
v_exp_f32_e32 v24, v73
v_add_f32_e32 v14, v14, v34
v_add_f32_e32 v14, v14, v24
Cv 34,34,24
v_exp_f32_e32 v35, v74
v_exp_f32_e32 v25, v75
v_add_f32_e32 v14, v14, v35
v_add_f32_e32 v14, v14, v25
Cv 35,35,25
v_exp_f32_e32 v36, v68
v_exp_f32_e32 v26, v69
s_nop 0
v_add_f32_e32 v15, v36, v26
Cv 36,36,26
v_exp_f32_e32 v37, v70
v_exp_f32_e32 v27, v71
v_add_f32_e32 v15, v15, v37
v_add_f32_e32 v15, v15, v27
Cv 37,37,27
v_exp_f32_e32 v38, v76
v_exp_f32_e32 v28, v77
v_add_f32_e32 v15, v15, v38
v_add_f32_e32 v15, v15, v28
Cv 38,38,28
v_exp_f32_e32 v39, v78
v_exp_f32_e32 v29, v79
v_add_f32_e32 v15, v15, v39
v_add_f32_e32 v15, v15, v29
Cv 39,39,29
v_exp_f32_e32 v40, v80
v_exp_f32_e32 v24, v81
v_add_f32_e32 v14, v14, v40
v_add_f32_e32 v14, v14, v24
Cv 40,40,24
v_exp_f32_e32 v41, v82
v_exp_f32_e32 v25, v83
v_add_f32_e32 v14, v14, v41
v_add_f32_e32 v14, v14, v25
Cv 41,41,25
v_exp_f32_e32 v42, v88
v_exp_f32_e32 v26, v89
v_add_f32_e32 v14, v14, v42
v_add_f32_e32 v14, v14, v26
Cv 42,42,26
v_exp_f32_e32 v43, v90
v_exp_f32_e32 v27, v91
v_add_f32_e32 v14, v14, v43
v_add_f32_e32 v14, v14, v27
Cv 43,43,27
v_exp_f32_e32 v44, v84
v_exp_f32_e32 v28, v85
)" R"(
v_add_f32_e32 v15, v15, v44
v_add_f32_e32 v15, v15, v28
Cv 44,44,28
v_exp_f32_e32 v45, v86
v_exp_f32_e32 v29, v87
v_add_f32_e32 v15, v15, v45
v_add_f32_e32 v15, v15, v29
Cv 45,45,29
v_exp_f32_e32 v46, v92
v_exp_f32_e32 v24, v93
v_add_f32_e32 v15, v15, v46
v_add_f32_e32 v15, v15, v24
Cv 46,46,24
v_exp_f32_e32 v47, v94
v_exp_f32_e32 v25, v95
v_add_f32_e32 v15, v15, v47
v_add_f32_e32 v15, v15, v25
Cv 47,47,25
s_nop 1
s_setpc_b64 s[30:31]
.LzMASK1:
s_lshl_b32 s28, s57, 6
s_sub_u32 s28, s28, 128
v_add_u32 v11, s28, v9
s_mov_b32 s29, 0xff800000
s_nop 2
v_cmpx_gt_i32_e32 vcc, 51, v11
s_cbranch_execz .LzMASKE1
v_mov_b32 v123, s29
v_cmpx_gt_i32_e32 vcc, 50, v11
v_mov_b32 v122, s29
v_cmpx_gt_i32_e32 vcc, 49, v11
v_mov_b32 v121, s29
v_cmpx_gt_i32_e32 vcc, 48, v11
v_mov_b32 v120, s29
v_cmpx_gt_i32_e32 vcc, 35, v11
s_cbranch_execz .LzMASKE1
v_mov_b32 v115, s29
v_mov_b32 v127, s29
v_cmpx_gt_i32_e32 vcc, 34, v11
v_mov_b32 v114, s29
v_mov_b32 v126, s29
v_cmpx_gt_i32_e32 vcc, 33, v11
v_mov_b32 v113, s29
v_mov_b32 v125, s29
v_cmpx_gt_i32_e32 vcc, 32, v11
v_mov_b32 v112, s29
v_mov_b32 v124, s29
v_cmpx_gt_i32_e32 vcc, 19, v11
s_cbranch_execz .LzMASKE1
v_mov_b32 v107, s29
v_mov_b32 v119, s29
v_cmpx_gt_i32_e32 vcc, 18, v11
v_mov_b32 v106, s29
v_mov_b32 v118, s29
v_cmpx_gt_i32_e32 vcc, 17, v11
v_mov_b32 v105, s29
v_mov_b32 v117, s29
v_cmpx_gt_i32_e32 vcc, 16, v11
v_mov_b32 v104, s29
v_mov_b32 v116, s29
v_cmpx_gt_i32_e32 vcc, 3, v11
s_cbranch_execz .LzMASKE1
v_mov_b32 v99, s29
)" R"(
v_mov_b32 v111, s29
v_cmpx_gt_i32_e32 vcc, 2, v11
v_mov_b32 v98, s29
v_mov_b32 v110, s29
v_cmpx_gt_i32_e32 vcc, 1, v11
v_mov_b32 v97, s29
v_mov_b32 v109, s29
v_cmpx_gt_i32_e32 vcc, 0, v11
v_mov_b32 v96, s29
v_mov_b32 v108, s29
v_cmpx_gt_i32_e32 vcc, -13, v11
s_cbranch_execz .LzMASKE1
v_mov_b32 v103, s29
v_cmpx_gt_i32_e32 vcc, -14, v11
v_mov_b32 v102, s29
v_cmpx_gt_i32_e32 vcc, -15, v11
v_mov_b32 v101, s29
v_cmpx_gt_i32_e32 vcc, -16, v11
v_mov_b32 v100, s29
.LzMASKE1:
s_mov_b64 exec, -1
s_nop 1
s_setpc_b64 s[30:31]
.LzFIX1:
s_nop 7
s_mov_b32 s63, 1
v_max_f32_e32 v28, v96, v97
v_max3_f32 v28, v28, v98, v99
v_max3_f32 v28, v28, v104, v105
v_max3_f32 v28, v28, v106, v107
v_max3_f32 v28, v28, v112, v113
v_max3_f32 v28, v28, v114, v115
v_max3_f32 v28, v28, v120, v121
v_max3_f32 v28, v28, v122, v123
v_mov_b32 v24, v28
s_nop 1
v_permlane32_swap_b32_e32 v28, v24
v_max_f32_e32 v28, v28, v24
v_mov_b32 v24, v28
s_nop 1
v_permlane16_swap_b32_e32 v28, v24
v_max_f32_e32 v28, v28, v24
v_max_f32_e32 v29, v100, v101
v_max3_f32 v29, v29, v102, v103
v_max3_f32 v29, v29, v108, v109
v_max3_f32 v29, v29, v110, v111
v_max3_f32 v29, v29, v116, v117
v_max3_f32 v29, v29, v118, v119
v_max3_f32 v29, v29, v124, v125
v_max3_f32 v29, v29, v126, v127
v_mov_b32 v24, v29
s_nop 1
v_permlane32_swap_b32_e32 v29, v24
v_max_f32_e32 v29, v29, v24
v_mov_b32 v24, v29
s_nop 1
v_permlane16_swap_b32_e32 v29, v24
v_max_f32_e32 v29, v29, v24
s_bitcmp1_b32 s88, 0
s_cbranch_scc1 .LzFIXF1
v_max_f32_e32 v28, 0, v28
v_sub_f32_e32 v24, 0, v28
v_exp_f32_e32 v24, v24
s_nop 0
)" R"(
v_mul_f32_e32 v12, v24, v12
v_mul_f32_e32 v128, v24, v128
v_mul_f32_e32 v129, v24, v129
v_mul_f32_e32 v130, v24, v130
v_mul_f32_e32 v131, v24, v131
v_mul_f32_e32 v132, v24, v132
v_mul_f32_e32 v133, v24, v133
v_mul_f32_e32 v134, v24, v134
v_mul_f32_e32 v135, v24, v135
v_mul_f32_e32 v144, v24, v144
v_mul_f32_e32 v145, v24, v145
v_mul_f32_e32 v146, v24, v146
v_mul_f32_e32 v147, v24, v147
v_mul_f32_e32 v148, v24, v148
v_mul_f32_e32 v149, v24, v149
v_mul_f32_e32 v150, v24, v150
v_mul_f32_e32 v151, v24, v151
v_mul_f32_e32 v160, v24, v160
v_mul_f32_e32 v161, v24, v161
v_mul_f32_e32 v162, v24, v162
v_mul_f32_e32 v163, v24, v163
v_mul_f32_e32 v164, v24, v164
v_mul_f32_e32 v165, v24, v165
v_mul_f32_e32 v166, v24, v166
v_mul_f32_e32 v167, v24, v167
v_mul_f32_e32 v176, v24, v176
v_mul_f32_e32 v177, v24, v177
v_mul_f32_e32 v178, v24, v178
v_mul_f32_e32 v179, v24, v179
v_mul_f32_e32 v180, v24, v180
v_mul_f32_e32 v181, v24, v181
v_mul_f32_e32 v182, v24, v182
v_mul_f32_e32 v183, v24, v183
v_mul_f32_e32 v192, v24, v192
v_mul_f32_e32 v193, v24, v193
v_mul_f32_e32 v194, v24, v194
v_mul_f32_e32 v195, v24, v195
v_mul_f32_e32 v196, v24, v196
v_mul_f32_e32 v197, v24, v197
v_mul_f32_e32 v198, v24, v198
v_mul_f32_e32 v199, v24, v199
v_mul_f32_e32 v208, v24, v208
v_mul_f32_e32 v209, v24, v209
v_mul_f32_e32 v210, v24, v210
v_mul_f32_e32 v211, v24, v211
v_mul_f32_e32 v212, v24, v212
v_mul_f32_e32 v213, v24, v213
v_mul_f32_e32 v214, v24, v214
v_mul_f32_e32 v215, v24, v215
v_mul_f32_e32 v224, v24, v224
v_mul_f32_e32 v225, v24, v225
v_mul_f32_e32 v226, v24, v226
v_mul_f32_e32 v227, v24, v227
v_mul_f32_e32 v228, v24, v228
v_mul_f32_e32 v229, v24, v229
v_mul_f32_e32 v230, v24, v230
v_mul_f32_e32 v231, v24, v231
v_mul_f32_e32 v240, v24, v240
v_mul_f32_e32 v241, v24, v241
v_mul_f32_e32 v242, v24, v242
v_mul_f32_e32 v243, v24, v243
v_mul_f32_e32 v244, v24, v244
v_mul_f32_e32 v245, v24, v245
v_mul_f32_e32 v246, v24, v246
)" R"(
v_mul_f32_e32 v247, v24, v247
v_max_f32_e32 v29, 0, v29
v_sub_f32_e32 v24, 0, v29
v_exp_f32_e32 v24, v24
s_nop 0
v_mul_f32_e32 v13, v24, v13
v_mul_f32_e32 v136, v24, v136
v_mul_f32_e32 v137, v24, v137
v_mul_f32_e32 v138, v24, v138
v_mul_f32_e32 v139, v24, v139
v_mul_f32_e32 v140, v24, v140
v_mul_f32_e32 v141, v24, v141
v_mul_f32_e32 v142, v24, v142
v_mul_f32_e32 v143, v24, v143
v_mul_f32_e32 v152, v24, v152
v_mul_f32_e32 v153, v24, v153
v_mul_f32_e32 v154, v24, v154
v_mul_f32_e32 v155, v24, v155
v_mul_f32_e32 v156, v24, v156
v_mul_f32_e32 v157, v24, v157
v_mul_f32_e32 v158, v24, v158
v_mul_f32_e32 v159, v24, v159
v_mul_f32_e32 v168, v24, v168
v_mul_f32_e32 v169, v24, v169
v_mul_f32_e32 v170, v24, v170
v_mul_f32_e32 v171, v24, v171
v_mul_f32_e32 v172, v24, v172
v_mul_f32_e32 v173, v24, v173
v_mul_f32_e32 v174, v24, v174
v_mul_f32_e32 v175, v24, v175
v_mul_f32_e32 v184, v24, v184
v_mul_f32_e32 v185, v24, v185
v_mul_f32_e32 v186, v24, v186
v_mul_f32_e32 v187, v24, v187
v_mul_f32_e32 v188, v24, v188
v_mul_f32_e32 v189, v24, v189
v_mul_f32_e32 v190, v24, v190
v_mul_f32_e32 v191, v24, v191
v_mul_f32_e32 v200, v24, v200
v_mul_f32_e32 v201, v24, v201
v_mul_f32_e32 v202, v24, v202
v_mul_f32_e32 v203, v24, v203
v_mul_f32_e32 v204, v24, v204
v_mul_f32_e32 v205, v24, v205
v_mul_f32_e32 v206, v24, v206
v_mul_f32_e32 v207, v24, v207
v_mul_f32_e32 v216, v24, v216
v_mul_f32_e32 v217, v24, v217
v_mul_f32_e32 v218, v24, v218
v_mul_f32_e32 v219, v24, v219
v_mul_f32_e32 v220, v24, v220
v_mul_f32_e32 v221, v24, v221
v_mul_f32_e32 v222, v24, v222
v_mul_f32_e32 v223, v24, v223
v_mul_f32_e32 v232, v24, v232
v_mul_f32_e32 v233, v24, v233
v_mul_f32_e32 v234, v24, v234
v_mul_f32_e32 v235, v24, v235
v_mul_f32_e32 v236, v24, v236
v_mul_f32_e32 v237, v24, v237
v_mul_f32_e32 v238, v24, v238
v_mul_f32_e32 v239, v24, v239
v_mul_f32_e32 v248, v24, v248
v_mul_f32_e32 v249, v24, v249
)" R"(
v_mul_f32_e32 v250, v24, v250
v_mul_f32_e32 v251, v24, v251
v_mul_f32_e32 v252, v24, v252
v_mul_f32_e32 v253, v24, v253
v_mul_f32_e32 v254, v24, v254
v_mul_f32_e32 v255, v24, v255
s_branch .LzFIXG1
.LzFIXF1:
v_cmp_lt_f32_e32 vcc, 0xff7fffff, v28
s_nop 1
v_cndmask_b32_e32 v28, 0, v28, vcc
v_cmp_lt_f32_e32 vcc, 0xff7fffff, v29
s_nop 1
v_cndmask_b32_e32 v29, 0, v29, vcc
.LzFIXG1:
v_sub_f32_e32 v16, v16, v28
v_sub_f32_e32 v17, v17, v28
v_sub_f32_e32 v18, v18, v28
v_sub_f32_e32 v19, v19, v28
v_sub_f32_e32 v96, v96, v28
v_sub_f32_e32 v97, v97, v28
v_sub_f32_e32 v98, v98, v28
v_sub_f32_e32 v99, v99, v28
v_sub_f32_e32 v104, v104, v28
v_sub_f32_e32 v105, v105, v28
v_sub_f32_e32 v106, v106, v28
v_sub_f32_e32 v107, v107, v28
v_sub_f32_e32 v112, v112, v28
v_sub_f32_e32 v113, v113, v28
v_sub_f32_e32 v114, v114, v28
v_sub_f32_e32 v115, v115, v28
v_sub_f32_e32 v120, v120, v28
v_sub_f32_e32 v121, v121, v28
v_sub_f32_e32 v122, v122, v28
v_sub_f32_e32 v123, v123, v28
v_sub_f32_e32 v20, v20, v29
v_sub_f32_e32 v21, v21, v29
v_sub_f32_e32 v22, v22, v29
v_sub_f32_e32 v23, v23, v29
v_sub_f32_e32 v100, v100, v29
v_sub_f32_e32 v101, v101, v29
v_sub_f32_e32 v102, v102, v29
v_sub_f32_e32 v103, v103, v29
v_sub_f32_e32 v108, v108, v29
v_sub_f32_e32 v109, v109, v29
v_sub_f32_e32 v110, v110, v29
v_sub_f32_e32 v111, v111, v29
v_sub_f32_e32 v116, v116, v29
v_sub_f32_e32 v117, v117, v29
v_sub_f32_e32 v118, v118, v29
v_sub_f32_e32 v119, v119, v29
v_sub_f32_e32 v124, v124, v29
v_sub_f32_e32 v125, v125, v29
v_sub_f32_e32 v126, v126, v29
v_sub_f32_e32 v127, v127, v29
s_bitcmp0_b32 s88, 1
s_cbranch_scc1 .LzFIXH1
v_sub_f32_e32 v64, v64, v28
v_sub_f32_e32 v65, v65, v28
v_sub_f32_e32 v66, v66, v28
v_sub_f32_e32 v67, v67, v28
v_sub_f32_e32 v72, v72, v28
v_sub_f32_e32 v73, v73, v28
v_sub_f32_e32 v74, v74, v28
)" R"(
v_sub_f32_e32 v75, v75, v28
v_sub_f32_e32 v80, v80, v28
v_sub_f32_e32 v81, v81, v28
v_sub_f32_e32 v82, v82, v28
v_sub_f32_e32 v83, v83, v28
v_sub_f32_e32 v88, v88, v28
v_sub_f32_e32 v89, v89, v28
v_sub_f32_e32 v90, v90, v28
v_sub_f32_e32 v91, v91, v28
v_sub_f32_e32 v68, v68, v29
v_sub_f32_e32 v69, v69, v29
v_sub_f32_e32 v70, v70, v29
v_sub_f32_e32 v71, v71, v29
v_sub_f32_e32 v76, v76, v29
v_sub_f32_e32 v77, v77, v29
v_sub_f32_e32 v78, v78, v29
v_sub_f32_e32 v79, v79, v29
v_sub_f32_e32 v84, v84, v29
v_sub_f32_e32 v85, v85, v29
v_sub_f32_e32 v86, v86, v29
v_sub_f32_e32 v87, v87, v29
v_sub_f32_e32 v92, v92, v29
v_sub_f32_e32 v93, v93, v29
v_sub_f32_e32 v94, v94, v29
v_sub_f32_e32 v95, v95, v29
.LzFIXH1:
v_exp_f32_e32 v48, v96
v_exp_f32_e32 v26, v97
s_nop 0
v_add_f32_e32 v14, v48, v26
Cv 48,48,26
v_exp_f32_e32 v49, v98
v_exp_f32_e32 v27, v99
v_add_f32_e32 v14, v14, v49
v_add_f32_e32 v14, v14, v27
Cv 49,49,27
v_exp_f32_e32 v50, v104
v_exp_f32_e32 v28, v105
v_add_f32_e32 v14, v14, v50
v_add_f32_e32 v14, v14, v28
Cv 50,50,28
v_exp_f32_e32 v51, v106
v_exp_f32_e32 v29, v107
v_add_f32_e32 v14, v14, v51
v_add_f32_e32 v14, v14, v29
Cv 51,51,29
v_exp_f32_e32 v52, v100
v_exp_f32_e32 v24, v101
s_nop 0
v_add_f32_e32 v15, v52, v24
Cv 52,52,24
v_exp_f32_e32 v53, v102
v_exp_f32_e32 v25, v103
v_add_f32_e32 v15, v15, v53
v_add_f32_e32 v15, v15, v25
Cv 53,53,25
v_exp_f32_e32 v54, v108
v_exp_f32_e32 v26, v109
v_add_f32_e32 v15, v15, v54
v_add_f32_e32 v15, v15, v26
Cv 54,54,26
v_exp_f32_e32 v55, v110
v_exp_f32_e32 v27, v111
v_add_f32_e32 v15, v15, v55
)" R"(
v_add_f32_e32 v15, v15, v27
Cv 55,55,27
v_exp_f32_e32 v56, v112
v_exp_f32_e32 v28, v113
v_add_f32_e32 v14, v14, v56
v_add_f32_e32 v14, v14, v28
Cv 56,56,28
v_exp_f32_e32 v57, v114
v_exp_f32_e32 v29, v115
v_add_f32_e32 v14, v14, v57
v_add_f32_e32 v14, v14, v29
Cv 57,57,29
v_exp_f32_e32 v58, v120
v_exp_f32_e32 v24, v121
v_add_f32_e32 v14, v14, v58
v_add_f32_e32 v14, v14, v24
Cv 58,58,24
v_exp_f32_e32 v59, v122
v_exp_f32_e32 v25, v123
v_add_f32_e32 v14, v14, v59
v_add_f32_e32 v14, v14, v25
Cv 59,59,25
v_exp_f32_e32 v60, v116
v_exp_f32_e32 v26, v117
v_add_f32_e32 v15, v15, v60
v_add_f32_e32 v15, v15, v26
Cv 60,60,26
v_exp_f32_e32 v61, v118
v_exp_f32_e32 v27, v119
v_add_f32_e32 v15, v15, v61
v_add_f32_e32 v15, v15, v27
Cv 61,61,27
v_exp_f32_e32 v62, v124
v_exp_f32_e32 v28, v125
v_add_f32_e32 v15, v15, v62
v_add_f32_e32 v15, v15, v28
Cv 62,62,28
v_exp_f32_e32 v63, v126
v_exp_f32_e32 v29, v127
v_add_f32_e32 v15, v15, v63
v_add_f32_e32 v15, v15, v29
Cv 63,63,29
s_nop 1
s_setpc_b64 s[30:31]
.Lzsp1:
s_sub_u32 s53, s53, 1
s_cbranch_scc1 .Lzsq1
s_sleep 1
buffer_load_dword v4, v5, s[20:23], s50 offen offset:0 sc0 sc1
s_waitcnt vmcnt(0)
v_cmp_eq_u32_e32 vcc, s3, v4
s_andn2_b64 s[28:29], s[60:61], vcc
s_cbranch_scc1 .Lzsp1
s_branch .Lzok1
.Lzsq1:
s_mov_b32 s53, 0
s_branch .Lzok1
.Lzsp2:
s_sub_u32 s53, s53, 1
s_cbranch_scc1 .Lzsq2
s_sleep 1
buffer_load_dword v4, v7, s[20:23], s52 offen offset:0 sc0 sc1
s_waitcnt vmcnt(0)
v_cmp_eq_u32_e32 vcc, s3, v4
)" R"(
s_andn2_b64 s[28:29], 1, vcc
s_cbranch_scc1 .Lzsp2
s_branch .Lzok2
.Lzsq2:
s_mov_b32 s53, 0
s_branch .Lzok2
.Lzsp3:
s_sub_u32 s53, s53, 1
s_cbranch_scc1 .Lzsq3
s_sleep 1
buffer_load_dword v4, v5, s[20:23], s50 offen offset:0 sc0 sc1
s_waitcnt vmcnt(0)
v_cmp_eq_u32_e32 vcc, s3, v4
s_andn2_b64 s[28:29], s[60:61], vcc
s_cbranch_scc1 .Lzsp3
s_branch .Lzok3
.Lzsq3:
s_mov_b32 s53, 0
s_branch .Lzok3
.Lzsp4:
s_sub_u32 s53, s53, 1
s_cbranch_scc1 .Lzsq4
s_sleep 1
buffer_load_dword v4, v7, s[20:23], s83 offen offset:4 sc0 sc1
s_waitcnt vmcnt(0)
v_cmp_eq_u32_e32 vcc, s3, v4
s_andn2_b64 s[28:29], 1, vcc
s_cbranch_scc1 .Lzsp4
s_branch .Lzok4
.Lzsq4:
s_mov_b32 s53, 0
s_branch .Lzok4
.LzT2S:
s_cmp_lg_u32 s33, 0
s_cbranch_scc1 .LzT2E
.LzT2:
s_mov_b32 s28, 0x5500000
buffer_load_dwordx2 v[32:33], v1, s[20:23], s28 offen offset:0 sc0 sc1
buffer_load_dwordx2 v[34:35], v1, s[20:23], s28 offen offset:1024 sc0 sc1
buffer_load_dwordx2 v[36:37], v1, s[20:23], s28 offen offset:2048 sc0 sc1
buffer_load_dwordx2 v[38:39], v1, s[20:23], s28 offen offset:3072 sc0 sc1
s_mov_b32 s28, 0x5501000
buffer_load_dwordx2 v[40:41], v1, s[20:23], s28 offen offset:0 sc0 sc1
buffer_load_dwordx2 v[42:43], v1, s[20:23], s28 offen offset:1024 sc0 sc1
buffer_load_dwordx2 v[44:45], v1, s[20:23], s28 offen offset:2048 sc0 sc1
buffer_load_dwordx2 v[46:47], v1, s[20:23], s28 offen offset:3072 sc0 sc1
s_mov_b32 s28, 0x5502000
buffer_load_dwordx2 v[48:49], v1, s[20:23], s28 offen offset:0 sc0 sc1
buffer_load_dwordx2 v[50:51], v1, s[20:23], s28 offen offset:1024 sc0 sc1
buffer_load_dwordx2 v[52:53], v1, s[20:23], s28 offen offset:2048 sc0 sc1
buffer_load_dwordx2 v[54:55], v1, s[20:23], s28 offen offset:3072 sc0 sc1
s_mov_b32 s28, 0x5503000
buffer_load_dwordx2 v[56:57], v1, s[20:23], s28 offen offset:0 sc0 sc1
buffer_load_dwordx2 v[58:59], v1, s[20:23], s28 offen offset:1024 sc0 sc1
buffer_load_dwordx2 v[60:61], v1, s[20:23], s28 offen offset:2048 sc0 sc1
buffer_load_dwordx2 v[62:63], v1, s[20:23], s28 offen offset:3072 sc0 sc1
s_mov_b32 s28, 0x5504000
buffer_load_dwordx2 v[64:65], v1, s[20:23], s28 offen offset:0 sc0 sc1
buffer_load_dwordx2 v[66:67], v1, s[20:23], s28 offen offset:1024 sc0 sc1
buffer_load_dwordx2 v[68:69], v1, s[20:23], s28 offen offset:2048 sc0 sc1
buffer_load_dwordx2 v[70:71], v1, s[20:23], s28 offen offset:3072 sc0 sc1
s_mov_b32 s28, 0x5505000
buffer_load_dwordx2 v[72:73], v1, s[20:23], s28 offen offset:0 sc0 sc1
buffer_load_dwordx2 v[74:75], v1, s[20:23], s28 offen offset:1024 sc0 sc1
)" R"(
buffer_load_dwordx2 v[76:77], v1, s[20:23], s28 offen offset:2048 sc0 sc1
buffer_load_dwordx2 v[78:79], v1, s[20:23], s28 offen offset:3072 sc0 sc1
s_mov_b32 s28, 0x5506000
buffer_load_dwordx2 v[80:81], v1, s[20:23], s28 offen offset:0 sc0 sc1
buffer_load_dwordx2 v[82:83], v1, s[20:23], s28 offen offset:1024 sc0 sc1
buffer_load_dwordx2 v[84:85], v1, s[20:23], s28 offen offset:2048 sc0 sc1
buffer_load_dwordx2 v[86:87], v1, s[20:23], s28 offen offset:3072 sc0 sc1
s_mov_b32 s28, 0x5507000
buffer_load_dwordx2 v[88:89], v1, s[20:23], s28 offen offset:0 sc0 sc1
buffer_load_dwordx2 v[90:91], v1, s[20:23], s28 offen offset:1024 sc0 sc1
buffer_load_dwordx2 v[92:93], v1, s[20:23], s28 offen offset:2048 sc0 sc1
buffer_load_dwordx2 v[94:95], v1, s[20:23], s28 offen offset:3072 sc0 sc1
v_mov_b32 v26, 0
v_mov_b32 v27, -1
s_waitcnt vmcnt(0)
v_max3_u32 v26, v26, v33, v35
v_min3_u32 v27, v27, v33, v35
v_max3_u32 v26, v26, v37, v39
v_min3_u32 v27, v27, v37, v39
v_max3_u32 v26, v26, v41, v43
v_min3_u32 v27, v27, v41, v43
v_max3_u32 v26, v26, v45, v47
v_min3_u32 v27, v27, v45, v47
v_max3_u32 v26, v26, v49, v51
v_min3_u32 v27, v27, v49, v51
v_max3_u32 v26, v26, v53, v55
v_min3_u32 v27, v27, v53, v55
v_max3_u32 v26, v26, v57, v59
v_min3_u32 v27, v27, v57, v59
v_max3_u32 v26, v26, v61, v63
v_min3_u32 v27, v27, v61, v63
v_max3_u32 v26, v26, v65, v67
v_min3_u32 v27, v27, v65, v67
v_max3_u32 v26, v26, v69, v71
v_min3_u32 v27, v27, v69, v71
v_max3_u32 v26, v26, v73, v75
v_min3_u32 v27, v27, v73, v75
v_max3_u32 v26, v26, v77, v79
v_min3_u32 v27, v27, v77, v79
v_max3_u32 v26, v26, v81, v83
v_min3_u32 v27, v27, v81, v83
v_max3_u32 v26, v26, v85, v87
v_min3_u32 v27, v27, v85, v87
v_max3_u32 v26, v26, v89, v91
v_min3_u32 v27, v27, v89, v91
v_max3_u32 v26, v26, v93, v95
v_min3_u32 v27, v27, v93, v95
v_cmp_ne_u32_e32 vcc, s3, v26
v_cmp_ne_u32_e64 s[28:29], s3, v27
s_or_b64 vcc, vcc, s[28:29]
s_cbranch_scc0 .LzT2G
s_sub_u32 s53, s53, 1
s_cbranch_scc0 .LzT2
s_mov_b32 s53, 0
.LzT2G:
v_max_u32 v26, v32, v34
v_min_u32 v27, v32, v34
v_med3_u32 v27, v26, v27, v36
v_max_u32 v26, v26, v36
v_med3_u32 v27, v26, v27, v38
v_max_u32 v26, v26, v38
v_med3_u32 v27, v26, v27, v40
v_max_u32 v26, v26, v40
v_med3_u32 v27, v26, v27, v42
)" R"(
v_max_u32 v26, v26, v42
v_med3_u32 v27, v26, v27, v44
v_max_u32 v26, v26, v44
v_med3_u32 v27, v26, v27, v46
v_max_u32 v26, v26, v46
v_med3_u32 v27, v26, v27, v48
v_max_u32 v26, v26, v48
v_med3_u32 v27, v26, v27, v50
v_max_u32 v26, v26, v50
v_med3_u32 v27, v26, v27, v52
v_max_u32 v26, v26, v52
v_med3_u32 v27, v26, v27, v54
v_max_u32 v26, v26, v54
v_med3_u32 v27, v26, v27, v56
v_max_u32 v26, v26, v56
v_med3_u32 v27, v26, v27, v58
v_max_u32 v26, v26, v58
v_med3_u32 v27, v26, v27, v60
v_max_u32 v26, v26, v60
v_med3_u32 v27, v26, v27, v62
v_max_u32 v26, v26, v62
v_med3_u32 v27, v26, v27, v64
v_max_u32 v26, v26, v64
v_med3_u32 v27, v26, v27, v66
v_max_u32 v26, v26, v66
v_med3_u32 v27, v26, v27, v68
v_max_u32 v26, v26, v68
v_med3_u32 v27, v26, v27, v70
v_max_u32 v26, v26, v70
v_med3_u32 v27, v26, v27, v72
v_max_u32 v26, v26, v72
v_med3_u32 v27, v26, v27, v74
v_max_u32 v26, v26, v74
v_med3_u32 v27, v26, v27, v76
v_max_u32 v26, v26, v76
v_med3_u32 v27, v26, v27, v78
v_max_u32 v26, v26, v78
v_med3_u32 v27, v26, v27, v80
v_max_u32 v26, v26, v80
v_med3_u32 v27, v26, v27, v82
v_max_u32 v26, v26, v82
v_med3_u32 v27, v26, v27, v84
v_max_u32 v26, v26, v84
v_med3_u32 v27, v26, v27, v86
v_max_u32 v26, v26, v86
v_med3_u32 v27, v26, v27, v88
v_max_u32 v26, v26, v88
v_med3_u32 v27, v26, v27, v90
v_max_u32 v26, v26, v90
v_med3_u32 v27, v26, v27, v92
v_max_u32 v26, v26, v92
v_med3_u32 v27, v26, v27, v94
v_max_u32 v26, v26, v94
v_mov_b32 v24, v26
v_mov_b32 v25, v27
s_nop 1
v_permlane32_swap_b32_e32 v26, v24
v_permlane32_swap_b32_e32 v27, v25
v_max_u32 v27, v27, v25
v_med3_u32 v27, v26, v27, v24
v_max_u32 v26, v26, v24
v_mov_b32 v24, v26
v_mov_b32 v25, v27
s_nop 1
)" R"(
v_permlane16_swap_b32_e32 v26, v24
v_permlane16_swap_b32_e32 v27, v25
v_max_u32 v27, v27, v25
v_med3_u32 v27, v26, v27, v24
v_max_u32 v26, v26, v24
s_nop 1
v_mov_b32_dpp v24, v26 row_ror:8 row_mask:0xf bank_mask:0xf
v_mov_b32_dpp v25, v27 row_ror:8 row_mask:0xf bank_mask:0xf
v_max_u32 v27, v27, v25
v_med3_u32 v27, v26, v27, v24
v_max_u32 v26, v26, v24
s_nop 1
v_mov_b32_dpp v24, v26 row_ror:4 row_mask:0xf bank_mask:0xf
v_mov_b32_dpp v25, v27 row_ror:4 row_mask:0xf bank_mask:0xf
v_max_u32 v27, v27, v25
v_med3_u32 v27, v26, v27, v24
v_max_u32 v26, v26, v24
s_nop 1
v_mov_b32_dpp v24, v26 row_ror:2 row_mask:0xf bank_mask:0xf
v_mov_b32_dpp v25, v27 row_ror:2 row_mask:0xf bank_mask:0xf
v_max_u32 v27, v27, v25
v_med3_u32 v27, v26, v27, v24
v_max_u32 v26, v26, v24
s_nop 1
v_mov_b32_dpp v24, v26 row_ror:1 row_mask:0xf bank_mask:0xf
v_mov_b32_dpp v25, v27 row_ror:1 row_mask:0xf bank_mask:0xf
v_max_u32 v27, v27, v25
v_med3_u32 v27, v26, v27, v24
v_max_u32 v26, v26, v24
v_and_b32 v26, 0xffff, v26
v_lshl_or_b32 v26, v27, 16, v26
v_not_b32 v24, v26
v_mov_b32 v25, s3
s_mov_b32 s28, 0x5508000
buffer_store_dwordx2 v[24:25], v7, s[20:23], s28 offen sc0 sc1
s_branch .LzT2E
.LzA7:
s_waitcnt vmcnt(0)
s_nop 7
v_mul_f32_e32 v96, s54, v96
v_mul_f32_e32 v97, s54, v97
Cv 96,96,97
v_accvgpr_write_b32 a112, v96
v_mul_f32_e32 v98, s54, v98
v_mul_f32_e32 v99, s54, v99
Cv 98,98,99
v_accvgpr_write_b32 a113, v98
v_mul_f32_e32 v100, s54, v100
v_mul_f32_e32 v101, s54, v101
Cv 100,100,101
v_accvgpr_write_b32 a114, v100
v_mul_f32_e32 v102, s54, v102
v_mul_f32_e32 v103, s54, v103
Cv 102,102,103
v_accvgpr_write_b32 a115, v102
v_mul_f32_e32 v104, s54, v104
v_mul_f32_e32 v105, s54, v105
Cv 104,104,105
v_accvgpr_write_b32 a116, v104
v_mul_f32_e32 v106, s54, v106
v_mul_f32_e32 v107, s54, v107
Cv 106,106,107
v_accvgpr_write_b32 a117, v106
v_mul_f32_e32 v108, s54, v108
)" R"(
v_mul_f32_e32 v109, s54, v109
Cv 108,108,109
v_accvgpr_write_b32 a118, v108
v_mul_f32_e32 v110, s54, v110
v_mul_f32_e32 v111, s54, v111
Cv 110,110,111
v_accvgpr_write_b32 a119, v110
v_mul_f32_e32 v112, s54, v112
v_mul_f32_e32 v113, s54, v113
Cv 112,112,113
v_accvgpr_write_b32 a120, v112
v_mul_f32_e32 v114, s54, v114
v_mul_f32_e32 v115, s54, v115
Cv 114,114,115
v_accvgpr_write_b32 a121, v114
v_mul_f32_e32 v116, s54, v116
v_mul_f32_e32 v117, s54, v117
Cv 116,116,117
v_accvgpr_write_b32 a122, v116
v_mul_f32_e32 v118, s54, v118
v_mul_f32_e32 v119, s54, v119
Cv 118,118,119
v_accvgpr_write_b32 a123, v118
v_mul_f32_e32 v120, s54, v120
v_mul_f32_e32 v121, s54, v121
Cv 120,120,121
v_accvgpr_write_b32 a124, v120
v_mul_f32_e32 v122, s54, v122
v_mul_f32_e32 v123, s54, v123
Cv 122,122,123
v_accvgpr_write_b32 a125, v122
v_mul_f32_e32 v124, s54, v124
v_mul_f32_e32 v125, s54, v125
Cv 124,124,125
v_accvgpr_write_b32 a126, v124
v_mul_f32_e32 v126, s54, v126
v_mul_f32_e32 v127, s54, v127
Cv 126,126,127
v_accvgpr_write_b32 a127, v126
v_accvgpr_mov_b32 a64, a224
v_accvgpr_mov_b32 a65, a225
v_accvgpr_mov_b32 a66, a226
v_accvgpr_mov_b32 a67, a227
v_accvgpr_mov_b32 a68, a228
v_accvgpr_mov_b32 a69, a229
v_accvgpr_mov_b32 a70, a230
v_accvgpr_mov_b32 a71, a231
v_accvgpr_mov_b32 a72, a232
v_accvgpr_mov_b32 a73, a233
v_accvgpr_mov_b32 a74, a234
v_accvgpr_mov_b32 a75, a235
v_accvgpr_mov_b32 a76, a236
v_accvgpr_mov_b32 a77, a237
v_accvgpr_mov_b32 a78, a238
v_accvgpr_mov_b32 a79, a239
v_accvgpr_mov_b32 a80, a240
v_accvgpr_mov_b32 a81, a241
v_accvgpr_mov_b32 a82, a242
v_accvgpr_mov_b32 a83, a243
v_accvgpr_mov_b32 a84, a244
v_accvgpr_mov_b32 a85, a245
v_accvgpr_mov_b32 a86, a246
v_accvgpr_mov_b32 a87, a247
v_accvgpr_mov_b32 a88, a248
)" R"(
v_accvgpr_mov_b32 a89, a249
v_accvgpr_mov_b32 a90, a250
v_accvgpr_mov_b32 a91, a251
v_accvgpr_mov_b32 a92, a252
v_accvgpr_mov_b32 a93, a253
v_accvgpr_mov_b32 a94, a254
v_accvgpr_mov_b32 a95, a255
v_accvgpr_mov_b32 a96, a192
v_accvgpr_mov_b32 a97, a193
v_accvgpr_mov_b32 a98, a194
v_accvgpr_mov_b32 a99, a195
v_accvgpr_mov_b32 a100, a196
v_accvgpr_mov_b32 a101, a197
v_accvgpr_mov_b32 a102, a198
v_accvgpr_mov_b32 a103, a199
v_accvgpr_mov_b32 a104, a200
v_accvgpr_mov_b32 a105, a201
v_accvgpr_mov_b32 a106, a202
v_accvgpr_mov_b32 a107, a203
v_accvgpr_mov_b32 a108, a204
v_accvgpr_mov_b32 a109, a205
v_accvgpr_mov_b32 a110, a206
v_accvgpr_mov_b32 a111, a207
s_mov_b32 s94, 2
s_nop 1
s_branch .LzA7R
.Lzsp5:
s_sub_u32 s53, s53, 1
s_cbranch_scc1 .Lzsq5
s_sleep 1
buffer_load_dword v4, v5, s[20:23], s50 offen offset:0 sc0 sc1
s_waitcnt vmcnt(0)
v_cmp_eq_u32_e32 vcc, s3, v4
s_andn2_b64 s[28:29], s[60:61], vcc
s_cbranch_scc1 .Lzsp5
s_branch .Lzok5
.Lzsq5:
s_mov_b32 s53, 0
s_branch .Lzok5
.Lzmk0:
s_call_b64 s[30:31], .LzMASK0
s_branch .Lzmr0
.Lzfx0:
s_mov_b32 s88, 3
s_call_b64 s[30:31], .LzFIX0
s_branch .Lzfr0
.Lzmk1:
s_call_b64 s[30:31], .LzMASK1
s_branch .Lzmr1
.Lzsp6:
s_sub_u32 s53, s53, 1
s_cbranch_scc1 .Lzsq6
s_sleep 1
buffer_load_dword v4, v5, s[20:23], s50 offen offset:0 sc0 sc1
s_waitcnt vmcnt(0)
v_cmp_eq_u32_e32 vcc, s3, v4
s_andn2_b64 s[28:29], s[60:61], vcc
s_cbranch_scc1 .Lzsp6
s_branch .Lzok6
.Lzsq6:
s_mov_b32 s53, 0
s_branch .Lzok6
.Lzfx1:
s_mov_b32 s88, 2
)" R"(
s_call_b64 s[30:31], .LzFIX1
s_branch .Lzfr1
.Lzmk2:
s_call_b64 s[30:31], .LzMASK0
s_branch .Lzmr2
.Lzfx2:
s_mov_b32 s88, 2
s_call_b64 s[30:31], .LzFIX0
s_branch .Lzfr2
.Lzmk3:
s_call_b64 s[30:31], .LzMASK1
s_branch .Lzmr3
.Lzsp7:
s_sub_u32 s53, s53, 1
s_cbranch_scc1 .Lzsq7
s_sleep 1
buffer_load_dword v4, v5, s[20:23], s50 offen offset:0 sc0 sc1
s_waitcnt vmcnt(0)
v_cmp_eq_u32_e32 vcc, s3, v4
s_andn2_b64 s[28:29], s[60:61], vcc
s_cbranch_scc1 .Lzsp7
s_branch .Lzok7
.Lzsq7:
s_mov_b32 s53, 0
s_branch .Lzok7
.Lzfx3:
s_mov_b32 s88, 0
s_call_b64 s[30:31], .LzFIX1
s_branch .Lzfr3
.Lzmk4:
s_call_b64 s[30:31], .LzMASK0
s_branch .Lzmr4
.Lzfx4:
s_mov_b32 s88, 3
s_call_b64 s[30:31], .LzFIX0
s_branch .Lzfr4
.Lzmk5:
s_call_b64 s[30:31], .LzMASK1
s_branch .Lzmr5
.Lzfx5:
s_mov_b32 s88, 0
s_call_b64 s[30:31], .LzFIX1
s_branch .Lzfr5
.Lzsp9:
s_sub_u32 s53, s53, 1
s_cbranch_scc1 .Lzsq9
s_sleep 1
buffer_load_dword v4, v5, s[20:23], s50 offen offset:0 sc0 sc1
s_waitcnt vmcnt(0)
v_cmp_eq_u32_e32 vcc, s3, v4
s_andn2_b64 s[28:29], s[60:61], vcc
s_cbranch_scc1 .Lzsp9
s_branch .Lzok9
.Lzsq9:
s_mov_b32 s53, 0
s_branch .Lzok9
.Lzsp10:
s_sub_u32 s53, s53, 1
s_cbranch_scc1 .Lzsq10
s_sleep 1
buffer_load_dword v4, v5, s[20:23], s50 offen offset:0 sc0 sc1
s_waitcnt vmcnt(0)
v_cmp_eq_u32_e32 vcc, s3, v4
s_andn2_b64 s[28:29], s[60:61], vcc
)" R"(
s_cbranch_scc1 .Lzsp10
s_branch .Lzok10
.Lzsq10:
s_mov_b32 s53, 0
s_branch .Lzok10
.Lzsp11:
s_sub_u32 s53, s53, 1
s_cbranch_scc1 .Lzsq11
s_sleep 1
buffer_load_dword v4, v5, s[20:23], s50 offen offset:0 sc0 sc1
s_waitcnt vmcnt(0)
v_cmp_eq_u32_e32 vcc, s3, v4
s_andn2_b64 s[28:29], s[60:61], vcc
s_cbranch_scc1 .Lzsp11
s_branch .Lzok11
.Lzsq11:
s_mov_b32 s53, 0
s_branch .Lzok11
.Lzsp12:
s_sub_u32 s53, s53, 1
s_cbranch_scc1 .Lzsq12
s_sleep 1
buffer_load_dword v4, v5, s[20:23], s50 offen offset:0 sc0 sc1
s_waitcnt vmcnt(0)
v_cmp_eq_u32_e32 vcc, s3, v4
s_andn2_b64 s[28:29], s[60:61], vcc
s_cbranch_scc1 .Lzsp12
s_branch .Lzok12
.Lzsq12:
s_mov_b32 s53, 0
s_branch .Lzok12
.Lzfx6:
s_mov_b32 s88, 3
s_call_b64 s[30:31], .LzFIX0
s_branch .Lzfr6
.Lzsp13:
s_sub_u32 s53, s53, 1
s_cbranch_scc1 .Lzsq13
s_sleep 1
buffer_load_dword v4, v5, s[20:23], s50 offen offset:0 sc0 sc1
s_waitcnt vmcnt(0)
v_cmp_eq_u32_e32 vcc, s3, v4
s_andn2_b64 s[28:29], s[60:61], vcc
s_cbranch_scc1 .Lzsp13
s_branch .Lzok13
.Lzsq13:
s_mov_b32 s53, 0
s_branch .Lzok13
.Lzfx7:
s_mov_b32 s88, 2
s_call_b64 s[30:31], .LzFIX1
s_branch .Lzfr7
.Lzmk6:
s_call_b64 s[30:31], .LzMASK0
s_branch .Lzmr6
.Lzfx8:
s_mov_b32 s88, 2
s_call_b64 s[30:31], .LzFIX0
s_branch .Lzfr8
.Lzmk7:
s_call_b64 s[30:31], .LzMASK1
s_branch .Lzmr7
.Lzsp14:
s_sub_u32 s53, s53, 1
)" R"(
s_cbranch_scc1 .Lzsq14
s_sleep 1
buffer_load_dword v4, v5, s[20:23], s50 offen offset:0 sc0 sc1
s_waitcnt vmcnt(0)
v_cmp_eq_u32_e32 vcc, s3, v4
s_andn2_b64 s[28:29], s[60:61], vcc
s_cbranch_scc1 .Lzsp14
s_branch .Lzok14
.Lzsq14:
s_mov_b32 s53, 0
s_branch .Lzok14
.Lzfx9:
s_mov_b32 s88, 2
s_call_b64 s[30:31], .LzFIX1
s_branch .Lzfr9
.Lzmk8:
s_call_b64 s[30:31], .LzMASK0
s_branch .Lzmr8
.Lzfx10:
s_mov_b32 s88, 2
s_call_b64 s[30:31], .LzFIX0
s_branch .Lzfr10
.Lzmk9:
s_call_b64 s[30:31], .LzMASK1
s_branch .Lzmr9
.Lzsp15:
s_sub_u32 s53, s53, 1
s_cbranch_scc1 .Lzsq15
s_sleep 1
buffer_load_dword v4, v7, s[20:23], s65 offen offset:0 sc0 sc1
s_waitcnt vmcnt(0)
v_cmp_eq_u32_e32 vcc, s3, v4
s_andn2_b64 s[28:29], s[60:61], vcc
s_cbranch_scc1 .Lzsp15
s_branch .Lzok15
.Lzsq15:
s_mov_b32 s53, 0
s_branch .Lzok15
.Lzfx11:
s_mov_b32 s88, 2
s_call_b64 s[30:31], .LzFIX1
s_branch .Lzfr11
.Lzmk10:
s_call_b64 s[30:31], .LzMASK0
s_branch .Lzmr10
.Lzfx12:
s_mov_b32 s88, 2
s_call_b64 s[30:31], .LzFIX0
s_branch .Lzfr12
.Lzmk11:
s_call_b64 s[30:31], .LzMASK1
s_branch .Lzmr11
.Lzfx13:
s_mov_b32 s88, 0
s_call_b64 s[30:31], .LzFIX1
s_branch .Lzfr13
.LzMRG:
s_waitcnt vmcnt(0)
s_nop 7
v_mov_b32 v25, v64
v_mov_b32 v24, v25
s_nop 1
v_permlane32_swap_b32_e32 v25, v24
v_add_f32_e32 v25, v25, v24
)" R"(
v_mov_b32 v24, v25
s_nop 1
v_permlane16_swap_b32_e32 v25, v24
v_add_f32_e32 v25, v25, v24
v_cmp_lt_f32_e32 vcc, 0, v25
s_nop 1
v_cndmask_b32_e32 v66, v16, v66, vcc
v_sub_f32_e32 v28, v16, v66
v_min_f32_e32 v26, 0, v28
v_exp_f32_e32 v26, v26
v_max_f32_e32 v28, 0, v28
v_sub_f32_e32 v24, 0, v28
v_exp_f32_e32 v24, v24
v_mul_f32_e32 v64, v26, v64
v_mul_f32_e32 v12, v24, v12
v_mul_f32_e32 v128, v24, v128
v_mul_f32_e32 v129, v24, v129
v_mul_f32_e32 v130, v24, v130
v_mul_f32_e32 v131, v24, v131
v_mul_f32_e32 v132, v24, v132
v_mul_f32_e32 v133, v24, v133
v_mul_f32_e32 v134, v24, v134
v_mul_f32_e32 v135, v24, v135
v_mul_f32_e32 v144, v24, v144
v_mul_f32_e32 v145, v24, v145
v_mul_f32_e32 v146, v24, v146
v_mul_f32_e32 v147, v24, v147
v_mul_f32_e32 v148, v24, v148
v_mul_f32_e32 v149, v24, v149
v_mul_f32_e32 v150, v24, v150
v_mul_f32_e32 v151, v24, v151
v_mul_f32_e32 v160, v24, v160
v_mul_f32_e32 v161, v24, v161
v_mul_f32_e32 v162, v24, v162
v_mul_f32_e32 v163, v24, v163
v_mul_f32_e32 v164, v24, v164
v_mul_f32_e32 v165, v24, v165
v_mul_f32_e32 v166, v24, v166
v_mul_f32_e32 v167, v24, v167
v_mul_f32_e32 v176, v24, v176
v_mul_f32_e32 v177, v24, v177
v_mul_f32_e32 v178, v24, v178
v_mul_f32_e32 v179, v24, v179
v_mul_f32_e32 v180, v24, v180
v_mul_f32_e32 v181, v24, v181
v_mul_f32_e32 v182, v24, v182
v_mul_f32_e32 v183, v24, v183
v_mul_f32_e32 v192, v24, v192
v_mul_f32_e32 v193, v24, v193
v_mul_f32_e32 v194, v24, v194
v_mul_f32_e32 v195, v24, v195
v_mul_f32_e32 v196, v24, v196
v_mul_f32_e32 v197, v24, v197
v_mul_f32_e32 v198, v24, v198
v_mul_f32_e32 v199, v24, v199
v_mul_f32_e32 v208, v24, v208
v_mul_f32_e32 v209, v24, v209
v_mul_f32_e32 v210, v24, v210
v_mul_f32_e32 v211, v24, v211
v_mul_f32_e32 v212, v24, v212
v_mul_f32_e32 v213, v24, v213
v_mul_f32_e32 v214, v24, v214
v_mul_f32_e32 v215, v24, v215
v_mul_f32_e32 v224, v24, v224
)" R"(
v_mul_f32_e32 v225, v24, v225
v_mul_f32_e32 v226, v24, v226
v_mul_f32_e32 v227, v24, v227
v_mul_f32_e32 v228, v24, v228
v_mul_f32_e32 v229, v24, v229
v_mul_f32_e32 v230, v24, v230
v_mul_f32_e32 v231, v24, v231
v_mul_f32_e32 v240, v24, v240
v_mul_f32_e32 v241, v24, v241
v_mul_f32_e32 v242, v24, v242
v_mul_f32_e32 v243, v24, v243
v_mul_f32_e32 v244, v24, v244
v_mul_f32_e32 v245, v24, v245
v_mul_f32_e32 v246, v24, v246
v_mul_f32_e32 v247, v24, v247
v_sub_f32_e32 v16, v16, v28
v_sub_f32_e32 v17, v17, v28
v_sub_f32_e32 v18, v18, v28
v_sub_f32_e32 v19, v19, v28
v_sub_f32_e32 v96, v96, v28
v_sub_f32_e32 v97, v97, v28
v_sub_f32_e32 v98, v98, v28
v_sub_f32_e32 v99, v99, v28
v_sub_f32_e32 v104, v104, v28
v_sub_f32_e32 v105, v105, v28
v_sub_f32_e32 v106, v106, v28
v_sub_f32_e32 v107, v107, v28
v_sub_f32_e32 v112, v112, v28
v_sub_f32_e32 v113, v113, v28
v_sub_f32_e32 v114, v114, v28
v_sub_f32_e32 v115, v115, v28
v_sub_f32_e32 v120, v120, v28
v_sub_f32_e32 v121, v121, v28
v_sub_f32_e32 v122, v122, v28
v_sub_f32_e32 v123, v123, v28
v_accvgpr_read_b32 v25, a128
v_lshlrev_b32 v24, 16, v25
v_and_b32 v25, 0xffff0000, v25
v_mul_f32_e32 v24, v26, v24
v_mul_f32_e32 v25, v26, v25
Cv 24,24,25
v_accvgpr_write_b32 a128, v24
v_accvgpr_read_b32 v25, a129
v_lshlrev_b32 v24, 16, v25
v_and_b32 v25, 0xffff0000, v25
v_mul_f32_e32 v24, v26, v24
v_mul_f32_e32 v25, v26, v25
Cv 24,24,25
v_accvgpr_write_b32 a129, v24
v_accvgpr_read_b32 v25, a130
v_lshlrev_b32 v24, 16, v25
v_and_b32 v25, 0xffff0000, v25
v_mul_f32_e32 v24, v26, v24
v_mul_f32_e32 v25, v26, v25
Cv 24,24,25
v_accvgpr_write_b32 a130, v24
v_accvgpr_read_b32 v25, a131
v_lshlrev_b32 v24, 16, v25
v_and_b32 v25, 0xffff0000, v25
v_mul_f32_e32 v24, v26, v24
v_mul_f32_e32 v25, v26, v25
Cv 24,24,25
v_accvgpr_write_b32 a131, v24
v_accvgpr_read_b32 v25, a136
)" R"(
v_lshlrev_b32 v24, 16, v25
v_and_b32 v25, 0xffff0000, v25
v_mul_f32_e32 v24, v26, v24
v_mul_f32_e32 v25, v26, v25
Cv 24,24,25
v_accvgpr_write_b32 a136, v24
v_accvgpr_read_b32 v25, a137
v_lshlrev_b32 v24, 16, v25
v_and_b32 v25, 0xffff0000, v25
v_mul_f32_e32 v24, v26, v24
v_mul_f32_e32 v25, v26, v25
Cv 24,24,25
v_accvgpr_write_b32 a137, v24
v_accvgpr_read_b32 v25, a138
v_lshlrev_b32 v24, 16, v25
v_and_b32 v25, 0xffff0000, v25
v_mul_f32_e32 v24, v26, v24
v_mul_f32_e32 v25, v26, v25
Cv 24,24,25
v_accvgpr_write_b32 a138, v24
v_accvgpr_read_b32 v25, a139
v_lshlrev_b32 v24, 16, v25
v_and_b32 v25, 0xffff0000, v25
v_mul_f32_e32 v24, v26, v24
v_mul_f32_e32 v25, v26, v25
Cv 24,24,25
v_accvgpr_write_b32 a139, v24
v_accvgpr_read_b32 v25, a144
v_lshlrev_b32 v24, 16, v25
v_and_b32 v25, 0xffff0000, v25
v_mul_f32_e32 v24, v26, v24
v_mul_f32_e32 v25, v26, v25
Cv 24,24,25
v_accvgpr_write_b32 a144, v24
v_accvgpr_read_b32 v25, a145
v_lshlrev_b32 v24, 16, v25
v_and_b32 v25, 0xffff0000, v25
v_mul_f32_e32 v24, v26, v24
v_mul_f32_e32 v25, v26, v25
Cv 24,24,25
v_accvgpr_write_b32 a145, v24
v_accvgpr_read_b32 v25, a146
v_lshlrev_b32 v24, 16, v25
v_and_b32 v25, 0xffff0000, v25
v_mul_f32_e32 v24, v26, v24
v_mul_f32_e32 v25, v26, v25
Cv 24,24,25
v_accvgpr_write_b32 a146, v24
v_accvgpr_read_b32 v25, a147
v_lshlrev_b32 v24, 16, v25
v_and_b32 v25, 0xffff0000, v25
v_mul_f32_e32 v24, v26, v24
v_mul_f32_e32 v25, v26, v25
Cv 24,24,25
v_accvgpr_write_b32 a147, v24
v_accvgpr_read_b32 v25, a152
v_lshlrev_b32 v24, 16, v25
v_and_b32 v25, 0xffff0000, v25
v_mul_f32_e32 v24, v26, v24
v_mul_f32_e32 v25, v26, v25
Cv 24,24,25
v_accvgpr_write_b32 a152, v24
v_accvgpr_read_b32 v25, a153
v_lshlrev_b32 v24, 16, v25
)" R"(
v_and_b32 v25, 0xffff0000, v25
v_mul_f32_e32 v24, v26, v24
v_mul_f32_e32 v25, v26, v25
Cv 24,24,25
v_accvgpr_write_b32 a153, v24
v_accvgpr_read_b32 v25, a154
v_lshlrev_b32 v24, 16, v25
v_and_b32 v25, 0xffff0000, v25
v_mul_f32_e32 v24, v26, v24
v_mul_f32_e32 v25, v26, v25
Cv 24,24,25
v_accvgpr_write_b32 a154, v24
v_accvgpr_read_b32 v25, a155
v_lshlrev_b32 v24, 16, v25
v_and_b32 v25, 0xffff0000, v25
v_mul_f32_e32 v24, v26, v24
v_mul_f32_e32 v25, v26, v25
Cv 24,24,25
v_accvgpr_write_b32 a155, v24
v_accvgpr_read_b32 v25, a160
v_lshlrev_b32 v24, 16, v25
v_and_b32 v25, 0xffff0000, v25
v_mul_f32_e32 v24, v26, v24
v_mul_f32_e32 v25, v26, v25
Cv 24,24,25
v_accvgpr_write_b32 a160, v24
v_accvgpr_read_b32 v25, a161
v_lshlrev_b32 v24, 16, v25
v_and_b32 v25, 0xffff0000, v25
v_mul_f32_e32 v24, v26, v24
v_mul_f32_e32 v25, v26, v25
Cv 24,24,25
v_accvgpr_write_b32 a161, v24
v_accvgpr_read_b32 v25, a162
v_lshlrev_b32 v24, 16, v25
v_and_b32 v25, 0xffff0000, v25
v_mul_f32_e32 v24, v26, v24
v_mul_f32_e32 v25, v26, v25
Cv 24,24,25
v_accvgpr_write_b32 a162, v24
v_accvgpr_read_b32 v25, a163
v_lshlrev_b32 v24, 16, v25
v_and_b32 v25, 0xffff0000, v25
v_mul_f32_e32 v24, v26, v24
v_mul_f32_e32 v25, v26, v25
Cv 24,24,25
v_accvgpr_write_b32 a163, v24
v_accvgpr_read_b32 v25, a168
v_lshlrev_b32 v24, 16, v25
v_and_b32 v25, 0xffff0000, v25
v_mul_f32_e32 v24, v26, v24
v_mul_f32_e32 v25, v26, v25
Cv 24,24,25
v_accvgpr_write_b32 a168, v24
v_accvgpr_read_b32 v25, a169
v_lshlrev_b32 v24, 16, v25
v_and_b32 v25, 0xffff0000, v25
v_mul_f32_e32 v24, v26, v24
v_mul_f32_e32 v25, v26, v25
Cv 24,24,25
v_accvgpr_write_b32 a169, v24
v_accvgpr_read_b32 v25, a170
v_lshlrev_b32 v24, 16, v25
v_and_b32 v25, 0xffff0000, v25
)" R"(
v_mul_f32_e32 v24, v26, v24
v_mul_f32_e32 v25, v26, v25
Cv 24,24,25
v_accvgpr_write_b32 a170, v24
v_accvgpr_read_b32 v25, a171
v_lshlrev_b32 v24, 16, v25
v_and_b32 v25, 0xffff0000, v25
v_mul_f32_e32 v24, v26, v24
v_mul_f32_e32 v25, v26, v25
Cv 24,24,25
v_accvgpr_write_b32 a171, v24
v_accvgpr_read_b32 v25, a176
v_lshlrev_b32 v24, 16, v25
v_and_b32 v25, 0xffff0000, v25
v_mul_f32_e32 v24, v26, v24
v_mul_f32_e32 v25, v26, v25
Cv 24,24,25
v_accvgpr_write_b32 a176, v24
v_accvgpr_read_b32 v25, a177
v_lshlrev_b32 v24, 16, v25
v_and_b32 v25, 0xffff0000, v25
v_mul_f32_e32 v24, v26, v24
v_mul_f32_e32 v25, v26, v25
Cv 24,24,25
v_accvgpr_write_b32 a177, v24
v_accvgpr_read_b32 v25, a178
v_lshlrev_b32 v24, 16, v25
v_and_b32 v25, 0xffff0000, v25
v_mul_f32_e32 v24, v26, v24
v_mul_f32_e32 v25, v26, v25
Cv 24,24,25
v_accvgpr_write_b32 a178, v24
v_accvgpr_read_b32 v25, a179
v_lshlrev_b32 v24, 16, v25
v_and_b32 v25, 0xffff0000, v25
v_mul_f32_e32 v24, v26, v24
v_mul_f32_e32 v25, v26, v25
Cv 24,24,25
v_accvgpr_write_b32 a179, v24
v_accvgpr_read_b32 v25, a184
v_lshlrev_b32 v24, 16, v25
v_and_b32 v25, 0xffff0000, v25
v_mul_f32_e32 v24, v26, v24
v_mul_f32_e32 v25, v26, v25
Cv 24,24,25
v_accvgpr_write_b32 a184, v24
v_accvgpr_read_b32 v25, a185
v_lshlrev_b32 v24, 16, v25
v_and_b32 v25, 0xffff0000, v25
v_mul_f32_e32 v24, v26, v24
v_mul_f32_e32 v25, v26, v25
Cv 24,24,25
v_accvgpr_write_b32 a185, v24
v_accvgpr_read_b32 v25, a186
v_lshlrev_b32 v24, 16, v25
v_and_b32 v25, 0xffff0000, v25
v_mul_f32_e32 v24, v26, v24
v_mul_f32_e32 v25, v26, v25
Cv 24,24,25
v_accvgpr_write_b32 a186, v24
v_accvgpr_read_b32 v25, a187
v_lshlrev_b32 v24, 16, v25
v_and_b32 v25, 0xffff0000, v25
v_mul_f32_e32 v24, v26, v24
)" R"(
v_mul_f32_e32 v25, v26, v25
Cv 24,24,25
v_accvgpr_write_b32 a187, v24
v_mov_b32 v25, v65
v_mov_b32 v24, v25
s_nop 1
v_permlane32_swap_b32_e32 v25, v24
v_add_f32_e32 v25, v25, v24
v_mov_b32 v24, v25
s_nop 1
v_permlane16_swap_b32_e32 v25, v24
v_add_f32_e32 v25, v25, v24
v_cmp_lt_f32_e32 vcc, 0, v25
s_nop 1
v_cndmask_b32_e32 v67, v20, v67, vcc
v_sub_f32_e32 v29, v20, v67
v_min_f32_e32 v27, 0, v29
v_exp_f32_e32 v27, v27
v_max_f32_e32 v29, 0, v29
v_sub_f32_e32 v24, 0, v29
v_exp_f32_e32 v24, v24
v_mul_f32_e32 v65, v27, v65
v_mul_f32_e32 v13, v24, v13
v_mul_f32_e32 v136, v24, v136
v_mul_f32_e32 v137, v24, v137
v_mul_f32_e32 v138, v24, v138
v_mul_f32_e32 v139, v24, v139
v_mul_f32_e32 v140, v24, v140
v_mul_f32_e32 v141, v24, v141
v_mul_f32_e32 v142, v24, v142
v_mul_f32_e32 v143, v24, v143
v_mul_f32_e32 v152, v24, v152
v_mul_f32_e32 v153, v24, v153
v_mul_f32_e32 v154, v24, v154
v_mul_f32_e32 v155, v24, v155
v_mul_f32_e32 v156, v24, v156
v_mul_f32_e32 v157, v24, v157
v_mul_f32_e32 v158, v24, v158
v_mul_f32_e32 v159, v24, v159
v_mul_f32_e32 v168, v24, v168
v_mul_f32_e32 v169, v24, v169
v_mul_f32_e32 v170, v24, v170
v_mul_f32_e32 v171, v24, v171
v_mul_f32_e32 v172, v24, v172
v_mul_f32_e32 v173, v24, v173
v_mul_f32_e32 v174, v24, v174
v_mul_f32_e32 v175, v24, v175
v_mul_f32_e32 v184, v24, v184
v_mul_f32_e32 v185, v24, v185
v_mul_f32_e32 v186, v24, v186
v_mul_f32_e32 v187, v24, v187
v_mul_f32_e32 v188, v24, v188
v_mul_f32_e32 v189, v24, v189
v_mul_f32_e32 v190, v24, v190
v_mul_f32_e32 v191, v24, v191
v_mul_f32_e32 v200, v24, v200
v_mul_f32_e32 v201, v24, v201
v_mul_f32_e32 v202, v24, v202
v_mul_f32_e32 v203, v24, v203
v_mul_f32_e32 v204, v24, v204
v_mul_f32_e32 v205, v24, v205
v_mul_f32_e32 v206, v24, v206
v_mul_f32_e32 v207, v24, v207
v_mul_f32_e32 v216, v24, v216
)" R"(
v_mul_f32_e32 v217, v24, v217
v_mul_f32_e32 v218, v24, v218
v_mul_f32_e32 v219, v24, v219
v_mul_f32_e32 v220, v24, v220
v_mul_f32_e32 v221, v24, v221
v_mul_f32_e32 v222, v24, v222
v_mul_f32_e32 v223, v24, v223
v_mul_f32_e32 v232, v24, v232
v_mul_f32_e32 v233, v24, v233
v_mul_f32_e32 v234, v24, v234
v_mul_f32_e32 v235, v24, v235
v_mul_f32_e32 v236, v24, v236
v_mul_f32_e32 v237, v24, v237
v_mul_f32_e32 v238, v24, v238
v_mul_f32_e32 v239, v24, v239
v_mul_f32_e32 v248, v24, v248
v_mul_f32_e32 v249, v24, v249
v_mul_f32_e32 v250, v24, v250
v_mul_f32_e32 v251, v24, v251
v_mul_f32_e32 v252, v24, v252
v_mul_f32_e32 v253, v24, v253
v_mul_f32_e32 v254, v24, v254
v_mul_f32_e32 v255, v24, v255
v_sub_f32_e32 v20, v20, v29
v_sub_f32_e32 v21, v21, v29
v_sub_f32_e32 v22, v22, v29
v_sub_f32_e32 v23, v23, v29
v_sub_f32_e32 v100, v100, v29
v_sub_f32_e32 v101, v101, v29
v_sub_f32_e32 v102, v102, v29
v_sub_f32_e32 v103, v103, v29
v_sub_f32_e32 v108, v108, v29
v_sub_f32_e32 v109, v109, v29
v_sub_f32_e32 v110, v110, v29
v_sub_f32_e32 v111, v111, v29
v_sub_f32_e32 v116, v116, v29
v_sub_f32_e32 v117, v117, v29
v_sub_f32_e32 v118, v118, v29
v_sub_f32_e32 v119, v119, v29
v_sub_f32_e32 v124, v124, v29
v_sub_f32_e32 v125, v125, v29
v_sub_f32_e32 v126, v126, v29
v_sub_f32_e32 v127, v127, v29
v_accvgpr_read_b32 v25, a132
v_lshlrev_b32 v24, 16, v25
v_and_b32 v25, 0xffff0000, v25
v_mul_f32_e32 v24, v27, v24
v_mul_f32_e32 v25, v27, v25
Cv 24,24,25
v_accvgpr_write_b32 a132, v24
v_accvgpr_read_b32 v25, a133
v_lshlrev_b32 v24, 16, v25
v_and_b32 v25, 0xffff0000, v25
v_mul_f32_e32 v24, v27, v24
v_mul_f32_e32 v25, v27, v25
Cv 24,24,25
v_accvgpr_write_b32 a133, v24
v_accvgpr_read_b32 v25, a134
v_lshlrev_b32 v24, 16, v25
v_and_b32 v25, 0xffff0000, v25
v_mul_f32_e32 v24, v27, v24
v_mul_f32_e32 v25, v27, v25
Cv 24,24,25
v_accvgpr_write_b32 a134, v24
)" R"(
v_accvgpr_read_b32 v25, a135
v_lshlrev_b32 v24, 16, v25
v_and_b32 v25, 0xffff0000, v25
v_mul_f32_e32 v24, v27, v24
v_mul_f32_e32 v25, v27, v25
Cv 24,24,25
v_accvgpr_write_b32 a135, v24
v_accvgpr_read_b32 v25, a140
v_lshlrev_b32 v24, 16, v25
v_and_b32 v25, 0xffff0000, v25
v_mul_f32_e32 v24, v27, v24
v_mul_f32_e32 v25, v27, v25
Cv 24,24,25
v_accvgpr_write_b32 a140, v24
v_accvgpr_read_b32 v25, a141
v_lshlrev_b32 v24, 16, v25
v_and_b32 v25, 0xffff0000, v25
v_mul_f32_e32 v24, v27, v24
v_mul_f32_e32 v25, v27, v25
Cv 24,24,25
v_accvgpr_write_b32 a141, v24
v_accvgpr_read_b32 v25, a142
v_lshlrev_b32 v24, 16, v25
v_and_b32 v25, 0xffff0000, v25
v_mul_f32_e32 v24, v27, v24
v_mul_f32_e32 v25, v27, v25
Cv 24,24,25
v_accvgpr_write_b32 a142, v24
v_accvgpr_read_b32 v25, a143
v_lshlrev_b32 v24, 16, v25
v_and_b32 v25, 0xffff0000, v25
v_mul_f32_e32 v24, v27, v24
v_mul_f32_e32 v25, v27, v25
Cv 24,24,25
v_accvgpr_write_b32 a143, v24
v_accvgpr_read_b32 v25, a148
v_lshlrev_b32 v24, 16, v25
v_and_b32 v25, 0xffff0000, v25
v_mul_f32_e32 v24, v27, v24
v_mul_f32_e32 v25, v27, v25
Cv 24,24,25
v_accvgpr_write_b32 a148, v24
v_accvgpr_read_b32 v25, a149
v_lshlrev_b32 v24, 16, v25
v_and_b32 v25, 0xffff0000, v25
v_mul_f32_e32 v24, v27, v24
v_mul_f32_e32 v25, v27, v25
Cv 24,24,25
v_accvgpr_write_b32 a149, v24
v_accvgpr_read_b32 v25, a150
v_lshlrev_b32 v24, 16, v25
v_and_b32 v25, 0xffff0000, v25
v_mul_f32_e32 v24, v27, v24
v_mul_f32_e32 v25, v27, v25
Cv 24,24,25
v_accvgpr_write_b32 a150, v24
v_accvgpr_read_b32 v25, a151
v_lshlrev_b32 v24, 16, v25
v_and_b32 v25, 0xffff0000, v25
v_mul_f32_e32 v24, v27, v24
v_mul_f32_e32 v25, v27, v25
Cv 24,24,25
v_accvgpr_write_b32 a151, v24
v_accvgpr_read_b32 v25, a156
)" R"(
v_lshlrev_b32 v24, 16, v25
v_and_b32 v25, 0xffff0000, v25
v_mul_f32_e32 v24, v27, v24
v_mul_f32_e32 v25, v27, v25
Cv 24,24,25
v_accvgpr_write_b32 a156, v24
v_accvgpr_read_b32 v25, a157
v_lshlrev_b32 v24, 16, v25
v_and_b32 v25, 0xffff0000, v25
v_mul_f32_e32 v24, v27, v24
v_mul_f32_e32 v25, v27, v25
Cv 24,24,25
v_accvgpr_write_b32 a157, v24
v_accvgpr_read_b32 v25, a158
v_lshlrev_b32 v24, 16, v25
v_and_b32 v25, 0xffff0000, v25
v_mul_f32_e32 v24, v27, v24
v_mul_f32_e32 v25, v27, v25
Cv 24,24,25
v_accvgpr_write_b32 a158, v24
v_accvgpr_read_b32 v25, a159
v_lshlrev_b32 v24, 16, v25
v_and_b32 v25, 0xffff0000, v25
v_mul_f32_e32 v24, v27, v24
v_mul_f32_e32 v25, v27, v25
Cv 24,24,25
v_accvgpr_write_b32 a159, v24
v_accvgpr_read_b32 v25, a164
v_lshlrev_b32 v24, 16, v25
v_and_b32 v25, 0xffff0000, v25
v_mul_f32_e32 v24, v27, v24
v_mul_f32_e32 v25, v27, v25
Cv 24,24,25
v_accvgpr_write_b32 a164, v24
v_accvgpr_read_b32 v25, a165
v_lshlrev_b32 v24, 16, v25
v_and_b32 v25, 0xffff0000, v25
v_mul_f32_e32 v24, v27, v24
v_mul_f32_e32 v25, v27, v25
Cv 24,24,25
v_accvgpr_write_b32 a165, v24
v_accvgpr_read_b32 v25, a166
v_lshlrev_b32 v24, 16, v25
v_and_b32 v25, 0xffff0000, v25
v_mul_f32_e32 v24, v27, v24
v_mul_f32_e32 v25, v27, v25
Cv 24,24,25
v_accvgpr_write_b32 a166, v24
v_accvgpr_read_b32 v25, a167
v_lshlrev_b32 v24, 16, v25
v_and_b32 v25, 0xffff0000, v25
v_mul_f32_e32 v24, v27, v24
v_mul_f32_e32 v25, v27, v25
Cv 24,24,25
v_accvgpr_write_b32 a167, v24
v_accvgpr_read_b32 v25, a172
v_lshlrev_b32 v24, 16, v25
v_and_b32 v25, 0xffff0000, v25
v_mul_f32_e32 v24, v27, v24
v_mul_f32_e32 v25, v27, v25
Cv 24,24,25
v_accvgpr_write_b32 a172, v24
v_accvgpr_read_b32 v25, a173
v_lshlrev_b32 v24, 16, v25
)" R"(
v_and_b32 v25, 0xffff0000, v25
v_mul_f32_e32 v24, v27, v24
v_mul_f32_e32 v25, v27, v25
Cv 24,24,25
v_accvgpr_write_b32 a173, v24
v_accvgpr_read_b32 v25, a174
v_lshlrev_b32 v24, 16, v25
v_and_b32 v25, 0xffff0000, v25
v_mul_f32_e32 v24, v27, v24
v_mul_f32_e32 v25, v27, v25
Cv 24,24,25
v_accvgpr_write_b32 a174, v24
v_accvgpr_read_b32 v25, a175
v_lshlrev_b32 v24, 16, v25
v_and_b32 v25, 0xffff0000, v25
v_mul_f32_e32 v24, v27, v24
v_mul_f32_e32 v25, v27, v25
Cv 24,24,25
v_accvgpr_write_b32 a175, v24
v_accvgpr_read_b32 v25, a180
v_lshlrev_b32 v24, 16, v25
v_and_b32 v25, 0xffff0000, v25
v_mul_f32_e32 v24, v27, v24
v_mul_f32_e32 v25, v27, v25
Cv 24,24,25
v_accvgpr_write_b32 a180, v24
v_accvgpr_read_b32 v25, a181
v_lshlrev_b32 v24, 16, v25
v_and_b32 v25, 0xffff0000, v25
v_mul_f32_e32 v24, v27, v24
v_mul_f32_e32 v25, v27, v25
Cv 24,24,25
v_accvgpr_write_b32 a181, v24
v_accvgpr_read_b32 v25, a182
v_lshlrev_b32 v24, 16, v25
v_and_b32 v25, 0xffff0000, v25
v_mul_f32_e32 v24, v27, v24
v_mul_f32_e32 v25, v27, v25
Cv 24,24,25
v_accvgpr_write_b32 a182, v24
v_accvgpr_read_b32 v25, a183
v_lshlrev_b32 v24, 16, v25
v_and_b32 v25, 0xffff0000, v25
v_mul_f32_e32 v24, v27, v24
v_mul_f32_e32 v25, v27, v25
Cv 24,24,25
v_accvgpr_write_b32 a183, v24
v_accvgpr_read_b32 v25, a188
v_lshlrev_b32 v24, 16, v25
v_and_b32 v25, 0xffff0000, v25
v_mul_f32_e32 v24, v27, v24
v_mul_f32_e32 v25, v27, v25
Cv 24,24,25
v_accvgpr_write_b32 a188, v24
v_accvgpr_read_b32 v25, a189
v_lshlrev_b32 v24, 16, v25
v_and_b32 v25, 0xffff0000, v25
v_mul_f32_e32 v24, v27, v24
v_mul_f32_e32 v25, v27, v25
Cv 24,24,25
v_accvgpr_write_b32 a189, v24
v_accvgpr_read_b32 v25, a190
v_lshlrev_b32 v24, 16, v25
v_and_b32 v25, 0xffff0000, v25
)" R"(
v_mul_f32_e32 v24, v27, v24
v_mul_f32_e32 v25, v27, v25
Cv 24,24,25
v_accvgpr_write_b32 a190, v24
v_accvgpr_read_b32 v25, a191
v_lshlrev_b32 v24, 16, v25
v_and_b32 v25, 0xffff0000, v25
v_mul_f32_e32 v24, v27, v24
v_mul_f32_e32 v25, v27, v25
Cv 24,24,25
v_accvgpr_write_b32 a191, v24
v_exp_f32_e32 v48, v96
v_exp_f32_e32 v24, v97
s_nop 0
v_add_f32_e32 v14, v48, v24
Cv 48,48,24
v_exp_f32_e32 v49, v98
v_exp_f32_e32 v25, v99
v_add_f32_e32 v14, v14, v49
v_add_f32_e32 v14, v14, v25
Cv 49,49,25
v_exp_f32_e32 v50, v104
v_exp_f32_e32 v26, v105
v_add_f32_e32 v14, v14, v50
v_add_f32_e32 v14, v14, v26
Cv 50,50,26
v_exp_f32_e32 v51, v106
v_exp_f32_e32 v27, v107
v_add_f32_e32 v14, v14, v51
v_add_f32_e32 v14, v14, v27
Cv 51,51,27
v_exp_f32_e32 v52, v100
v_exp_f32_e32 v28, v101
s_nop 0
v_add_f32_e32 v15, v52, v28
Cv 52,52,28
v_exp_f32_e32 v53, v102
v_exp_f32_e32 v29, v103
v_add_f32_e32 v15, v15, v53
v_add_f32_e32 v15, v15, v29
Cv 53,53,29
v_exp_f32_e32 v54, v108
v_exp_f32_e32 v24, v109
v_add_f32_e32 v15, v15, v54
v_add_f32_e32 v15, v15, v24
Cv 54,54,24
v_exp_f32_e32 v55, v110
v_exp_f32_e32 v25, v111
v_add_f32_e32 v15, v15, v55
v_add_f32_e32 v15, v15, v25
Cv 55,55,25
s_nop 1
s_branch .LzMRR
.Lzsp16:
s_sub_u32 s53, s53, 1
s_cbranch_scc1 .Lzsq16
s_sleep 1
buffer_load_dword v4, v7, s[20:23], s56 offen offset:4 sc0 sc1
s_waitcnt vmcnt(0)
v_cmp_eq_u32_e32 vcc, s3, v4
s_andn2_b64 s[28:29], 1, vcc
s_cbranch_scc1 .Lzsp16
s_branch .Lzok16
.Lzsq16:
)" R"(
s_mov_b32 s53, 0
s_branch .Lzok16
.LzB0:
s_mul_i32 s41, s33, 7168
s_mul_i32 s42, s33, 0x70000
s_lshl_b32 s28, s34, 9
s_add_u32 s42, s42, s28
v_lshrrev_b32 v28, 3, v0
v_and_b32 v29, 0x70, v1
v_lshl_or_b32 v28, v28, 13, v29
v_lshlrev_b32 v29, 1, v0
v_and_b32 v29, 32, v29
v_xor_b32 v28, v28, v29
s_and_b32 s28, s33, 1
s_lshl_b32 s28, s28, 6
v_xor_b32 v240, s28, v28
v_xor_b32 v241, 64, v240
v_add_u32 v241, 0xfc00, v241
v_add_u32 v242, 0x1f800, v240
v_add_u32 v243, 0x1f800, v241
s_sub_u32 s28, 15, s35
s_lshl2_add_u32 s28, s28, s33
s_mul_i32 s43, s28, 28672
v_and_b32 v96, 0x70, v1
v_and_b32 v28, 48, v0
v_xor_b32 v96, v96, v28
v_lshrrev_b32 v28, 3, v0
v_mul_u32_u24 v28, 896, v28
v_add_u32 v96, v96, v28
v_xor_b32 v97, 64, v96
v_add_u32 v97, 6144, v97
v_bfe_u32 v98, v0, 1, 3
v_lshrrev_b32 v28, 4, v0
v_xor_b32 v98, v98, v28
v_lshlrev_b32 v98, 4, v98
v_and_b32 v28, 15, v0
v_lshl_or_b32 v98, v28, 7, v98
s_add_u32 s28, s36, 0x0
v_add_u32 v98, s28, v98
v_xor_b32 v99, 64, v98
v_add_u32 v100, 0x10000, v98
v_add_u32 v101, 0x10000, v99
v_add_u32 v102, 0x20000, v98
v_add_u32 v103, 0x20000, v99
s_waitcnt lgkmcnt(0)
s_mov_b32 s16, s68
s_mov_b32 s17, s69
s_mov_b32 s18, 0x380000
s_mov_b32 s19, 0x20000
s_mov_b32 s12, s64
s_mov_b32 s13, s65
s_mov_b32 s14, 0x1c0000
s_mov_b32 s15, 0x20000
s_add_u32 m0, s36, 0
s_add_u32 s44, s43, 0
buffer_load_dwordx4 v96, s[12:15], s44 offen offset:0 lds
buffer_load_dwordx4 v97, s[12:15], s44 offen offset:1024 lds
s_add_u32 s44, s43, 12288
buffer_load_dwordx4 v96, s[12:15], s44 offen offset:2048 lds
buffer_load_dwordx4 v97, s[12:15], s44 offen offset:3072 lds
s_add_u32 m0, s41, 32768
s_add_u32 s92, s42, 0x0
buffer_load_dwordx4 v240, s[16:19], s92 offen offset:0 lds
buffer_load_dwordx4 v241, s[16:19], s92 offen offset:1024 lds
)" R"(
buffer_load_dwordx4 v242, s[16:19], s92 offen offset:2048 lds
buffer_load_dwordx4 v243, s[16:19], s92 offen offset:3072 lds
s_add_u32 m0, s41, 36864
s_add_u32 s92, s42, 0x40000
buffer_load_dwordx4 v240, s[16:19], s92 offen offset:0 lds
buffer_load_dwordx4 v241, s[16:19], s92 offen offset:1024 lds
buffer_load_dwordx4 v242, s[16:19], s92 offen offset:2048 lds
s_add_u32 m0, s36, 4096
s_add_u32 s44, s43, 128
buffer_load_dwordx4 v96, s[12:15], s44 offen offset:0 lds
buffer_load_dwordx4 v97, s[12:15], s44 offen offset:1024 lds
s_add_u32 s44, s43, 12416
buffer_load_dwordx4 v96, s[12:15], s44 offen offset:2048 lds
buffer_load_dwordx4 v97, s[12:15], s44 offen offset:3072 lds
s_add_u32 m0, s36, 65536
s_add_u32 s44, s43, 256
buffer_load_dwordx4 v96, s[12:15], s44 offen offset:0 lds
buffer_load_dwordx4 v97, s[12:15], s44 offen offset:1024 lds
s_add_u32 s44, s43, 12544
buffer_load_dwordx4 v96, s[12:15], s44 offen offset:2048 lds
buffer_load_dwordx4 v97, s[12:15], s44 offen offset:3072 lds
s_add_u32 m0, s36, 69632
s_add_u32 s44, s43, 384
buffer_load_dwordx4 v96, s[12:15], s44 offen offset:0 lds
buffer_load_dwordx4 v97, s[12:15], s44 offen offset:1024 lds
s_add_u32 s44, s43, 12672
buffer_load_dwordx4 v96, s[12:15], s44 offen offset:2048 lds
buffer_load_dwordx4 v97, s[12:15], s44 offen offset:3072 lds
s_add_u32 m0, s41, 98304
s_add_u32 s92, s42, 0x1c0000
buffer_load_dwordx4 v240, s[16:19], s92 offen offset:0 lds
buffer_load_dwordx4 v241, s[16:19], s92 offen offset:1024 lds
buffer_load_dwordx4 v242, s[16:19], s92 offen offset:2048 lds
buffer_load_dwordx4 v243, s[16:19], s92 offen offset:3072 lds
s_add_u32 m0, s41, 102400
s_add_u32 s92, s42, 0x200000
buffer_load_dwordx4 v240, s[16:19], s92 offen offset:0 lds
buffer_load_dwordx4 v241, s[16:19], s92 offen offset:1024 lds
buffer_load_dwordx4 v242, s[16:19], s92 offen offset:2048 lds
s_add_u32 m0, s36, 131072
s_add_u32 s44, s43, 512
buffer_load_dwordx4 v96, s[12:15], s44 offen offset:0 lds
buffer_load_dwordx4 v97, s[12:15], s44 offen offset:1024 lds
s_add_u32 s44, s43, 12800
buffer_load_dwordx4 v96, s[12:15], s44 offen offset:2048 lds
buffer_load_dwordx4 v97, s[12:15], s44 offen offset:3072 lds
s_add_u32 m0, s36, 135168
s_add_u32 s44, s43, 640
buffer_load_dwordx4 v96, s[12:15], s44 offen offset:0 lds
buffer_load_dwordx4 v97, s[12:15], s44 offen offset:1024 lds
s_add_u32 s44, s43, 12928
buffer_load_dwordx4 v96, s[12:15], s44 offen offset:2048 lds
buffer_load_dwordx4 v97, s[12:15], s44 offen offset:3072 lds
s_sub_u32 s87, s2, 128
s_lshl2_add_u32 s87, s87, s33
s_lshl_b32 s87, s87, 2
s_mul_i32 s29, s87, 896
v_mov_b32 v88, 0x40000000
v_cmp_gt_u32_e32 vcc, 56, v0
s_nop 1
v_cndmask_b32_e32 v88, v88, v1, vcc
buffer_load_dwordx4 v[64:67], v88, s[12:15], s29 offen offset:0
buffer_load_dwordx4 v[68:71], v88, s[12:15], s29 offen offset:896
buffer_load_dwordx4 v[72:75], v88, s[12:15], s29 offen offset:1792
)" R"(
buffer_load_dwordx4 v[76:79], v88, s[12:15], s29 offen offset:2688
s_mov_b32 s24, s76
s_mov_b32 s25, s77
s_mov_b32 s26, 0x380
s_mov_b32 s27, 0x20000
buffer_load_dwordx4 v[80:83], v88, s[24:27], 0 offen
s_mov_b32 s24, s78
s_mov_b32 s25, s79
s_mov_b32 s26, 0x380
s_mov_b32 s27, 0x20000
buffer_load_dwordx4 v[84:87], v88, s[24:27], 0 offen
s_getpc_b64 s[24:25]
.Lzpc8:
s_sub_u32 s24, s24, .Lzpc8-.LzSTART
s_subb_u32 s25, s25, 0
s_mov_b32 s26, .LzEND-.LzSTART+8192
s_mov_b32 s27, 0x20000
s_lshr_b32 s28, s2, 3
s_lshl2_add_u32 s28, s28, s33
s_lshl_b32 s28, s28, 11
v_lshrrev_b32 v5, 4, v0
v_min_u32 v5, 1, v5
v_lshlrev_b32 v5, 27, v5
v_add_lshl_u32 v5, v5, v1, 3
buffer_load_dword v5, v5, s[24:27], s28 offen
s_mov_b32 s28, 0x5508400
s_load_dword s3, s[74:75], s28
s_mov_b32 s20, s74
s_mov_b32 s21, s75
s_mov_b32 s22, 0x55b2000
s_mov_b32 s23, 0x20000
s_mov_b32 s4, s66
s_mov_b32 s5, s67
s_mov_b32 s6, 0x400000
s_mov_b32 s7, 0x20000
s_cmp_eq_u32 s85, 1
s_cselect_b32 s8, s72, s70
s_cselect_b32 s9, s73, s71
s_mov_b32 s10, 0x800000
s_mov_b32 s11, 0x20000
s_load_dwordx2 s[74:75], s[0:1], 96
v_bfe_u32 v244, v0, 3, 2
v_lshlrev_b32 v244, 5, v244
v_and_b32 v28, 3, v0
v_lshl_or_b32 v244, v28, 3, v244
v_bfe_u32 v28, v0, 2, 2
v_lshl_or_b32 v244, v28, 7, v244
v_lshrrev_b32 v28, 4, v0
v_lshl_or_b32 v244, v28, 10, v244
v_xor_b32 v245, 32, v244
v_xor_b32 v246, 64, v244
v_xor_b32 v247, 96, v244
v_add_u32 v252, 0x20000, v244
v_add_u32 v253, 0x20000, v245
v_add_u32 v254, 0x20000, v246
v_add_u32 v255, 0x20000, v247
v_add_u32 v248, 0x10000, v244
v_add_u32 v249, 0x10000, v245
v_add_u32 v250, 0x10000, v246
v_add_u32 v251, 0x10000, v247
s_mov_b32 s84, 0xe0c0a08
s_mov_b32 s28, 0x100d0b09
s_bitcmp1_b32 s2, 5
s_cmov_b32 s84, 0xf1e1c1a
)" R"(
s_cmov_b32 s28, 0x111f1d1b
s_bitcmp1_b32 s2, 6
s_cmov_b32 s84, s28
s_lshl_b32 s28, s33, 3
s_lshr_b32 s28, s84, s28
s_and_b32 s28, s28, 255
s_lshl_b32 s38, s28, 17
s_add_u32 s38, s38, 0xff80
s_lshl_b32 s39, s33, 16
s_lshl_b32 s28, s34, 9
s_add_u32 s39, s39, s28
s_sub_u32 s39, s39, 0x40000
s_lshl_b32 s37, s33, 12
s_add_u32 s37, s37, 0x10000
v_lshrrev_b32 v28, 4, v0
v_and_b32 v29, 7, v0
v_xor_b32 v28, v28, v29
v_lshlrev_b32 v28, 4, v28
v_lshrrev_b32 v29, 3, v0
v_lshl_or_b32 v8, v29, 11, v28
v_xor_b32 v9, 64, v8
v_add_u32 v9, 15360, v9
v_add_u32 v10, 30720, v8
v_add_u32 v11, 30720, v9
s_and_b32 s28, s33, 1
s_lshl_b32 s28, s28, 7
v_lshrrev_b32 v29, 5, v0
v_mul_u32_u24 v29, 0x2220, v29
v_xor_b32 v29, v29, v1
v_xor_b32 v12, s28, v29
v_xor_b32 v13, 64, v12
v_add_u32 v13, 15360, v13
v_add_u32 v14, 30720, v12
v_add_u32 v15, 30720, v13
v_bfe_u32 v28, v0, 1, 3
v_lshrrev_b32 v29, 4, v0
v_xor_b32 v28, v28, v29
v_lshlrev_b32 v28, 4, v28
v_and_b32 v29, 15, v0
v_lshl_or_b32 v28, v29, 7, v28
s_lshr_b32 s28, s33, 1
s_lshl_b32 s28, s28, 12
v_or_b32 v16, s28, v28
v_xor_b32 v17, 64, v16
v_bfe_u32 v28, v0, 2, 3
v_lshlrev_b32 v28, 5, v28
v_and_b32 v29, 3, v0
v_lshl_or_b32 v28, v29, 3, v28
v_bfe_u32 v29, v0, 2, 2
v_lshl_or_b32 v28, v29, 9, v28
v_lshrrev_b32 v29, 4, v0
v_lshl_or_b32 v28, v29, 12, v28
s_and_b32 s28, s33, 1
s_lshl_b32 s28, s28, 8
s_bitset1_b32 s28, 16
v_or_b32 v18, s28, v28
v_xor_b32 v19, 32, v18
v_xor_b32 v20, 64, v18
v_xor_b32 v21, 96, v18
v_xor_b32 v22, 128, v18
v_xor_b32 v23, 160, v18
v_xor_b32 v24, 192, v18
v_xor_b32 v25, 224, v18
s_lshl_b32 s28, s34, 20
)" R"(
s_lshl_b32 s29, s85, 24
s_add_u32 s28, s28, s29
s_bfe_u32 s29, s84, 0x80000
s_lshl_b32 s29, s29, 15
s_add_u32 s45, s28, s29
s_bfe_u32 s29, s84, 0x80008
s_lshl_b32 s29, s29, 15
s_add_u32 s46, s28, s29
s_bfe_u32 s29, s84, 0x80010
s_lshl_b32 s29, s29, 15
s_add_u32 s47, s28, s29
s_bfe_u32 s29, s84, 0x80018
s_lshl_b32 s29, s29, 15
s_add_u32 s48, s28, s29
s_lshl_b32 s50, s34, 11
s_add_u32 s50, s50, 0x5510000
s_lshl2_add_u32 s28, s85, s86
s_lshl_b32 s28, s28, 7
s_add_u32 s49, s28, s50
s_add_u32 s49, s49, 1024
s_lshl3_add_u32 s28, s34, s35
s_lshl_b32 s52, s28, 7
s_add_u32 s52, s52, 0x5518000
s_lshl_b32 s51, s28, 16
s_add_u32 s51, s51, 0x2000000
s_cmp_lg_u32 s33, 0
s_cmov_b32 s49, 0x40000000
s_cmov_b32 s52, 0x40000000
v_add_u32 v2, s36, v1
v_add_u32 v3, 0x1000, v2
v_min_u32 v7, 1, v0
v_lshlrev_b32 v7, 30, v7
s_mov_b32 s53, 0x40000
s_mov_b32 s54, 0x3db8aa3b
s_lshl_b32 s89, s34, 20
s_mov_b64 s[60:61], 0xff
s_mov_b32 s62, 0x4e800000
s_mov_b32 s63, 0
s_lshl_b32 s67, s34, 18
s_add_u32 s67, s67, 0x5000000
s_lshl3_add_u32 s28, s34, s35
s_lshl_b32 s64, s28, 16
s_add_u32 s64, s64, 0x2800000
s_lshl2_add_u32 s28, s28, s33
s_lshl_b32 s65, s28, 7
s_add_u32 s65, s65, 0x5520000
s_lshl_b32 s66, s28, 10
s_add_u32 s66, s66, 0x5400000
s_sub_u32 s28, 15, s35
s_lshl2_add_u32 s28, s28, s33
s_lshl_b32 s29, s34, 6
s_add_u32 s29, s29, s28
s_mul_i32 s73, s29, 28672
s_add_u32 s73, s73, 0x3000000
s_lshl4_add_u32 s28, s28, s34
s_lshl_b32 s68, s28, 7
s_add_u32 s68, s68, 0x5530000
s_mov_b32 s55, s89
s_mov_b32 s57, 18
s_mov_b32 s59, s67
s_mov_b32 s58, 0
s_cmp_eq_u32 s35, 7
s_cmov_b32 s58, 2
s_cmp_eq_u32 s35, 7
)" R"(
s_cmov_b32 s64, 0x40000000
s_cmp_eq_u32 s35, 7
s_cmov_b32 s66, 0x40000000
s_add_u32 s56, s55, 0x1000000
s_waitcnt vmcnt(30) lgkmcnt(0)
s_barrier
ds_read_b128 v[128:131], v98 offset:0
ds_read_b128 v[132:135], v98 offset:2048
ds_read_b128 v[136:139], v99 offset:0
ds_read_b128 v[140:143], v99 offset:2048
ds_read_b128 v[144:147], v98 offset:4096
ds_read_b128 v[148:151], v98 offset:6144
ds_read_b128 v[152:155], v99 offset:4096
ds_read_b128 v[156:159], v99 offset:6144
Rt 128,244,32768
Rt 130,244,33280
Rt 132,245,32768
Rt 134,245,33280
Rt 136,246,32768
Rt 138,246,33280
Rt 140,247,32768
Rt 142,247,33280
Rt 144,244,36864
Rt 146,244,37376
Rt 148,245,36864
Rt 150,245,37376
Rt 152,246,36864
Rt 154,246,37376
Rt 156,247,36864
Rt 158,247,37376
s_mul_i32 s3, s3, 1664525
s_add_u32 s3, s3, 1013904223
v_mov_b32 v6, s3
s_waitcnt lgkmcnt(8)
s_barrier
v_mfma_f32_16x16x32_bf16 v[32:35], a[128:131], v[128:131], 0
Rt 160,244,40960
s_add_u32 m0, s41, 0
s_add_u32 s92, s42, 0x80
v_mfma_f32_16x16x32_bf16 v[40:43], a[128:131], v[132:135], 0
Rt 162,244,41472
buffer_load_dwordx4 v240, s[16:19], s92 offen offset:0 lds
v_mfma_f32_16x16x32_bf16 v[44:47], a[132:135], v[132:135], 0
Rt 164,245,40960
v_mfma_f32_16x16x32_bf16 v[36:39], a[132:135], v[128:131], 0
Rt 166,245,41472
v_mfma_f32_16x16x32_bf16 v[48:51], a[136:139], v[128:131], 0
Rt 168,246,40960
v_mfma_f32_16x16x32_bf16 v[56:59], a[136:139], v[132:135], 0
Rt 170,246,41472
buffer_load_dwordx4 v241, s[16:19], s92 offen offset:1024 lds
v_mfma_f32_16x16x32_bf16 v[60:63], a[140:143], v[132:135], 0
Rt 172,247,40960
v_mfma_f32_16x16x32_bf16 v[52:55], a[140:143], v[128:131], 0
Rt 174,247,41472
s_waitcnt vmcnt(28) lgkmcnt(8)
Mp 32,144,136
Rt 128,244,45056
ds_read_b128 v[160:163], v100 offset:0
Mp 40,144,140
Rt 130,244,45568
buffer_load_dwordx4 v242, s[16:19], s92 offen offset:2048 lds
Mp 44,148,140
Rt 132,245,45056
ds_read_b128 v[164:167], v100 offset:2048
Mp 36,148,136
)" R"(
Rt 134,245,45568
Mp 48,152,136
Rt 136,246,45056
ds_read_b128 v[168:171], v101 offset:0
Mp 56,152,140
Rt 138,246,45568
buffer_load_dwordx4 v243, s[16:19], s92 offen offset:3072 lds
Mp 60,156,140
Rt 140,247,45056
ds_read_b128 v[172:175], v101 offset:2048
Mp 52,156,136
Rt 142,247,45568
s_waitcnt vmcnt(26) lgkmcnt(12)
Mp 32,160,144
Rt 144,244,49152
ds_read_b128 v[176:179], v100 offset:4096
s_add_u32 m0, s41, 4096
s_add_u32 s92, s42, 0x40080
Mp 40,160,148
Rt 146,244,49664
buffer_load_dwordx4 v240, s[16:19], s92 offen offset:0 lds
Mp 44,164,148
Rt 148,245,49152
ds_read_b128 v[180:183], v100 offset:6144
Mp 36,164,144
Rt 150,245,49664
Mp 48,168,144
Rt 152,246,49152
ds_read_b128 v[184:187], v101 offset:4096
Mp 56,168,148
Rt 154,246,49664
buffer_load_dwordx4 v241, s[16:19], s92 offen offset:1024 lds
Mp 60,172,148
Rt 156,247,49152
ds_read_b128 v[188:191], v101 offset:6144
Mp 52,172,144
Rt 158,247,49664
s_waitcnt lgkmcnt(12)
Mp 32,128,152
Rt 160,244,53248
Mp 40,128,156
Rt 162,244,53760
buffer_load_dwordx4 v242, s[16:19], s92 offen offset:2048 lds
Mp 44,132,156
Rt 164,245,53248
Mp 36,132,152
Rt 166,245,53760
s_add_u32 s44, s43, 768
Mp 48,136,152
Rt 168,246,53248
s_add_u32 m0, s36, 65536
Mp 56,136,156
Rt 170,246,53760
buffer_load_dwordx4 v96, s[12:15], s44 offen offset:0 lds
Mp 60,140,156
Rt 172,247,53248
Mp 52,140,152
Rt 174,247,53760
s_waitcnt lgkmcnt(8)
Mp 32,144,160
Rt 128,244,57344
Mp 40,144,164
Rt 130,244,57856
buffer_load_dwordx4 v97, s[12:15], s44 offen offset:1024 lds
)" R"(
Mp 44,148,164
Rt 132,245,57344
Mp 36,148,160
Rt 134,245,57856
Mp 48,152,160
Rt 136,246,57344
Mp 56,152,164
Rt 138,246,57856
s_add_u32 s44, s43, 13056
buffer_load_dwordx4 v96, s[12:15], s44 offen offset:2048 lds
Mp 60,156,164
Rt 140,247,57344
Mp 52,156,160
Rt 142,247,57856
s_waitcnt vmcnt(21) lgkmcnt(8)
s_barrier
Mp 32,160,168
Rt 144,248,32768
ds_read_b128 v[192:195], v102 offset:0
Mp 40,160,172
Rt 146,248,33280
buffer_load_dwordx4 v97, s[12:15], s44 offen offset:3072 lds
Mp 44,164,172
Rt 148,249,32768
ds_read_b128 v[196:199], v102 offset:2048
Mp 36,164,168
Rt 150,249,33280
Mp 48,168,168
Rt 152,250,32768
ds_read_b128 v[200:203], v103 offset:0
Mp 56,168,172
Rt 154,250,33280
Mp 60,172,172
Rt 156,251,32768
ds_read_b128 v[204:207], v103 offset:2048
Mp 52,172,168
Rt 158,251,33280
s_waitcnt vmcnt(18) lgkmcnt(12)
s_barrier
Mp 32,128,176
Rt 160,248,36864
ds_read_b128 v[208:211], v102 offset:4096
Mp 40,128,180
Rt 162,248,37376
Mp 44,132,180
Rt 164,249,36864
ds_read_b128 v[212:215], v102 offset:6144
Mp 36,132,176
Rt 166,249,37376
Mp 48,136,176
Rt 168,250,36864
ds_read_b128 v[216:219], v103 offset:4096
Mp 56,136,180
Rt 170,250,37376
Mp 60,140,180
Rt 172,251,36864
ds_read_b128 v[220:223], v103 offset:6144
Mp 52,140,176
Rt 174,251,37376
s_waitcnt vmcnt(12) lgkmcnt(12)
Mp 32,144,184
Rt 128,248,40960
v_dot2_f32_bf16 v88, v64, v80, 0
v_dot2_f32_bf16 v89, v68, v80, 0
Mp 40,144,188
Rt 130,248,41472
)" R"(
v_dot2_f32_bf16 v90, v72, v80, 0
v_dot2_f32_bf16 v91, v76, v80, 0
Mp 44,148,188
Rt 132,249,40960
v_dot2_f32_bf16 v92, v64, v84, 0
v_dot2_f32_bf16 v93, v68, v84, 0
Mp 36,148,184
Rt 134,249,41472
v_dot2_f32_bf16 v94, v72, v84, 0
v_dot2_f32_bf16 v95, v76, v84, 0
v_dot2_f32_bf16 v88, v65, v81, v88
Mp 48,152,184
Rt 136,250,40960
v_dot2_f32_bf16 v89, v69, v81, v89
v_dot2_f32_bf16 v90, v73, v81, v90
Mp 56,152,188
Rt 138,250,41472
v_dot2_f32_bf16 v91, v77, v81, v91
v_dot2_f32_bf16 v92, v65, v85, v92
Mp 60,156,188
Rt 140,251,40960
s_add_u32 s92, s42, 0x1c0080
v_dot2_f32_bf16 v93, v69, v85, v93
v_dot2_f32_bf16 v94, v73, v85, v94
Mp 52,156,184
Rt 142,251,41472
s_waitcnt lgkmcnt(8)
s_barrier
Mp 32,160,192
Rt 144,248,45056
s_add_u32 m0, s41, 131072
v_dot2_f32_bf16 v95, v77, v85, v95
Mp 40,160,196
Rt 146,248,45568
buffer_load_dwordx4 v240, s[16:19], s92 offen offset:0 lds
v_dot2_f32_bf16 v88, v66, v82, v88
Mp 44,164,196
Rt 148,249,45056
v_dot2_f32_bf16 v89, v70, v82, v89
v_dot2_f32_bf16 v90, v74, v82, v90
Mp 36,164,192
Rt 150,249,45568
v_dot2_f32_bf16 v91, v78, v82, v91
v_dot2_f32_bf16 v92, v66, v86, v92
Mp 48,168,192
Rt 152,250,45056
v_dot2_f32_bf16 v93, v70, v86, v93
v_dot2_f32_bf16 v94, v74, v86, v94
v_dot2_f32_bf16 v95, v78, v86, v95
Mp 56,168,196
Rt 154,250,45568
buffer_load_dwordx4 v241, s[16:19], s92 offen offset:1024 lds
Mp 60,172,196
Rt 156,251,45056
v_dot2_f32_bf16 v88, v67, v83, v88
v_dot2_f32_bf16 v89, v71, v83, v89
Mp 52,172,192
Rt 158,251,45568
v_dot2_f32_bf16 v90, v75, v83, v90
v_dot2_f32_bf16 v91, v79, v83, v91
s_waitcnt lgkmcnt(8)
Mp 32,128,200
Rt 160,248,49152
v_dot2_f32_bf16 v92, v67, v87, v92
v_dot2_f32_bf16 v93, v71, v87, v93
)" R"(
Mp 40,128,204
Rt 162,248,49664
buffer_load_dwordx4 v242, s[16:19], s92 offen offset:2048 lds
Mp 44,132,204
Rt 164,249,49152
v_dot2_f32_bf16 v94, v75, v87, v94
v_dot2_f32_bf16 v95, v79, v87, v95
Mp 36,132,200
Rt 166,249,49664
v_permlane32_swap_b32_e32 v88, v89
v_add_f32_e32 v88, v88, v89
v_permlane32_swap_b32_e32 v90, v91
Mp 48,136,200
Rt 168,250,49152
v_add_f32_e32 v90, v90, v91
v_permlane32_swap_b32_e32 v92, v93
Mp 56,136,204
Rt 170,250,49664
buffer_load_dwordx4 v243, s[16:19], s92 offen offset:3072 lds
Mp 60,140,204
Rt 172,251,49152
s_add_u32 m0, s41, 135168
v_add_f32_e32 v92, v92, v93
v_permlane32_swap_b32_e32 v94, v95
Mp 52,140,200
Rt 174,251,49664
v_add_f32_e32 v94, v94, v95
s_waitcnt vmcnt(4) lgkmcnt(8)
Mp 32,144,208
Rt 128,248,53248
ds_read_b128 v[224:227], v100 offset:0
s_add_u32 s92, s42, 0x200080
Mp 40,144,212
Rt 130,248,53760
buffer_load_dwordx4 v240, s[16:19], s92 offen offset:0 lds
v_permlane16_swap_b32_e32 v88, v90
Mp 44,148,212
Rt 132,249,53248
ds_read_b128 v[228:231], v100 offset:2048
v_add_f32_e32 v88, v88, v90
Mp 36,148,208
Rt 134,249,53760
v_permlane16_swap_b32_e32 v92, v94
v_add_f32_e32 v92, v92, v94
Mp 48,152,208
Rt 136,250,53248
ds_read_b128 v[232:235], v101 offset:0
v_add_f32_dpp v88, v88, v88 row_ror:8 row_mask:0xf bank_mask:0xf
v_add_f32_dpp v92, v92, v92 row_ror:8 row_mask:0xf bank_mask:0xf
Mp 56,152,212
Rt 138,250,53760
buffer_load_dwordx4 v241, s[16:19], s92 offen offset:1024 lds
Mp 60,156,212
Rt 140,251,53248
ds_read_b128 v[236:239], v101 offset:2048
v_add_f32_dpp v88, v88, v88 row_ror:4 row_mask:0xf bank_mask:0xf
Mp 52,156,208
Rt 142,251,53760
v_add_f32_dpp v92, v92, v92 row_ror:4 row_mask:0xf bank_mask:0xf
v_add_f32_dpp v88, v88, v88 row_ror:2 row_mask:0xf bank_mask:0xf
s_waitcnt lgkmcnt(12)
Mp 32,160,216
Rt 144,248,57344
v_add_f32_dpp v92, v92, v92 row_ror:2 row_mask:0xf bank_mask:0xf
)" R"(
v_add_f32_dpp v88, v88, v88 row_ror:1 row_mask:0xf bank_mask:0xf
Mp 40,160,220
Rt 146,248,57856
buffer_load_dwordx4 v242, s[16:19], s92 offen offset:2048 lds
Mp 44,164,220
Rt 148,249,57344
v_add_f32_dpp v92, v92, v92 row_ror:1 row_mask:0xf bank_mask:0xf
Cv 88,88,88
Mp 36,164,216
Rt 150,249,57856
v_lshlrev_b32 v88, 16, v88
Cv 92,92,92
v_lshlrev_b32 v92, 16, v92
Mp 48,168,216
Rt 152,250,57344
v_mul_f32_e32 v88, 0xbfb8aa3b, v88
v_lshrrev_b32 v89, 5, v0
Mp 56,168,220
Rt 154,250,57856
v_exp_f32_e32 v88, v88
Mp 60,172,220
Rt 156,251,57344
s_add_u32 s92, s42, 0x100
v_add_f32_e32 v88, 1.0, v88
v_bfe_u32 v90, v0, 4, 1
Mp 52,172,216
Rt 158,251,57856
s_waitcnt lgkmcnt(8)
s_barrier
Mp 32,128,224
Rt 160,244,0
s_add_u32 m0, s41, 65536
v_lshl_or_b32 v89, v90, 1, v89
Mp 40,128,228
Rt 162,244,512
buffer_load_dwordx4 v240, s[16:19], s92 offen offset:0 lds
v_add_u32 v89, s87, v89
Mp 44,132,228
Rt 164,245,0
v_rcp_f32_e32 v88, v88
Mp 36,132,224
Rt 166,245,512
Cv 88,88,88
v_lshlrev_b32 v88, 16, v88
Mp 48,136,224
Rt 168,246,0
v_add_f32_e32 v93, 0, v92
v_ashrrev_i32 v94, 31, v93
v_or_b32 v94, 0x80000000, v94
Mp 56,136,228
Rt 170,246,512
buffer_load_dwordx4 v241, s[16:19], s92 offen offset:1024 lds
Mp 60,140,228
Rt 172,247,0
v_xor_b32 v93, v93, v94
v_and_b32 v93, 0xffff0000, v93
Mp 52,140,224
Rt 174,247,512
v_sub_u32 v94, 0xffff, v89
s_waitcnt lgkmcnt(8)
s_barrier
Mp 32,144,232
Rt 128,244,4096
v_or_b32 v84, v93, v94
v_mov_b32 v85, s3
Mp 40,144,236
)" R"(
Rt 130,244,4608
buffer_load_dwordx4 v242, s[16:19], s92 offen offset:2048 lds
Mp 44,148,236
Rt 132,245,4096
v_mov_b32 v86, v88
v_mov_b32 v87, v92
Mp 36,148,232
Rt 134,245,4608
v_and_b32 v90, 15, v0
v_min_u32 v90, 1, v90
v_lshlrev_b32 v90, 30, v90
Mp 48,152,232
Rt 136,246,4096
v_lshl_or_b32 v90, v89, 4, v90
s_mov_b32 s87, 0x5500000
Mp 56,152,236
Rt 138,246,4608
buffer_load_dwordx4 v243, s[16:19], s92 offen offset:3072 lds
Mp 60,156,236
Rt 140,247,4096
s_add_u32 s92, s42, 0x40100
buffer_store_dwordx4 v[84:87], v90, s[20:23], s87 offen sc0 sc1
Mp 52,156,232
Rt 142,247,4608
s_waitcnt lgkmcnt(8)
v_mfma_f32_16x16x32_bf16 v[64:67], a[160:163], v[128:131], 0
Rt 144,244,8192
s_add_u32 m0, s41, 69632
v_mfma_f32_16x16x32_bf16 v[72:75], a[160:163], v[132:135], 0
Rt 146,244,8704
buffer_load_dwordx4 v240, s[16:19], s92 offen offset:0 lds
v_mfma_f32_16x16x32_bf16 v[76:79], a[164:167], v[132:135], 0
Rt 148,245,8192
v_mfma_f32_16x16x32_bf16 v[68:71], a[164:167], v[128:131], 0
Rt 150,245,8704
v_mfma_f32_16x16x32_bf16 v[80:83], a[168:171], v[128:131], 0
Rt 152,246,8192
v_mul_f32_e32 v32, s54, v32
v_mfma_f32_16x16x32_bf16 v[88:91], a[168:171], v[132:135], 0
Rt 154,246,8704
buffer_load_dwordx4 v241, s[16:19], s92 offen offset:1024 lds
v_mfma_f32_16x16x32_bf16 v[92:95], a[172:175], v[132:135], 0
Rt 156,247,8192
v_mul_f32_e32 v33, s54, v33
v_mfma_f32_16x16x32_bf16 v[84:87], a[172:175], v[128:131], 0
Rt 158,247,8704
s_waitcnt lgkmcnt(8)
Mp 64,128,136
Rt 160,244,12288
Cv 32,32,33
Mp 72,128,140
Rt 162,244,12800
buffer_load_dwordx4 v242, s[16:19], s92 offen offset:2048 lds
Mp 76,132,140
Rt 164,245,12288
v_accvgpr_write_b32 a64, v32
Mp 68,132,136
Rt 166,245,12800
s_add_u32 s92, s42, 0x1c0100
v_mul_f32_e32 v34, s54, v34
Mp 80,136,136
Rt 168,246,12288
s_add_u32 m0, s41, 32768
Mp 88,136,140
)" R"(
Rt 170,246,12800
buffer_load_dwordx4 v240, s[16:19], s92 offen offset:0 lds
Mp 92,140,140
Rt 172,247,12288
v_mul_f32_e32 v35, s54, v35
Mp 84,140,136
Rt 174,247,12800
s_waitcnt lgkmcnt(8)
Mp 64,144,144
Rt 128,244,16384
Cv 34,34,35
Mp 72,144,148
Rt 130,244,16896
buffer_load_dwordx4 v241, s[16:19], s92 offen offset:1024 lds
Mp 76,148,148
Rt 132,245,16384
v_accvgpr_write_b32 a65, v34
Mp 68,148,144
Rt 134,245,16896
v_mul_f32_e32 v36, s54, v36
v_mul_f32_e32 v37, s54, v37
Mp 80,152,144
Rt 136,246,16384
Cv 36,36,37
Mp 88,152,148
Rt 138,246,16896
buffer_load_dwordx4 v242, s[16:19], s92 offen offset:2048 lds
Mp 92,156,148
Rt 140,247,16384
v_accvgpr_write_b32 a66, v36
Mp 84,156,144
Rt 142,247,16896
s_waitcnt lgkmcnt(8)
Mp 64,160,152
Rt 144,244,20480
v_mul_f32_e32 v38, s54, v38
Mp 72,160,156
Rt 146,244,20992
buffer_load_dwordx4 v243, s[16:19], s92 offen offset:3072 lds
Mp 76,164,156
Rt 148,245,20480
v_mul_f32_e32 v39, s54, v39
Mp 68,164,152
Rt 150,245,20992
s_add_u32 s92, s42, 0x200100
Cv 38,38,39
Mp 80,168,152
Rt 152,246,20480
s_add_u32 m0, s41, 36864
Mp 88,168,156
Rt 154,246,20992
buffer_load_dwordx4 v240, s[16:19], s92 offen offset:0 lds
Mp 92,172,156
Rt 156,247,20480
v_accvgpr_write_b32 a67, v38
Mp 84,172,152
Rt 158,247,20992
s_waitcnt lgkmcnt(8)
Mp 64,128,160
Rt 160,244,24576
v_mul_f32_e32 v40, s54, v40
Mp 72,128,164
Rt 162,244,25088
buffer_load_dwordx4 v241, s[16:19], s92 offen offset:1024 lds
)" R"(
Mp 76,132,164
Rt 164,245,24576
v_mul_f32_e32 v41, s54, v41
Mp 68,132,160
Rt 166,245,25088
Cv 40,40,41
v_accvgpr_write_b32 a68, v40
Mp 80,136,160
Rt 168,246,24576
v_mul_f32_e32 v42, s54, v42
Mp 88,136,164
Rt 170,246,25088
buffer_load_dwordx4 v242, s[16:19], s92 offen offset:2048 lds
Mp 92,140,164
Rt 172,247,24576
s_add_u32 s92, s42, 0x180
Mp 84,140,160
Rt 174,247,25088
s_waitcnt vmcnt(15) lgkmcnt(8)
s_barrier
Mp 64,144,168
Rt 128,252,0
s_add_u32 m0, s41, 98304
Mp 72,144,172
Rt 130,252,512
buffer_load_dwordx4 v240, s[16:19], s92 offen offset:0 lds
Mp 76,148,172
Rt 132,253,0
v_mul_f32_e32 v43, s54, v43
Mp 68,148,168
Rt 134,253,512
Cv 42,42,43
v_accvgpr_write_b32 a69, v42
Mp 80,152,168
Rt 136,254,0
v_mul_f32_e32 v44, s54, v44
Mp 88,152,172
Rt 138,254,512
buffer_load_dwordx4 v241, s[16:19], s92 offen offset:1024 lds
Mp 92,156,172
Rt 140,255,0
v_mul_f32_e32 v45, s54, v45
Mp 84,156,168
Rt 142,255,512
s_waitcnt lgkmcnt(8)
s_barrier
Mp 64,160,176
Rt 144,252,4096
Cv 44,44,45
Mp 72,160,180
Rt 146,252,4608
buffer_load_dwordx4 v242, s[16:19], s92 offen offset:2048 lds
Mp 76,164,180
Rt 148,253,4096
v_accvgpr_write_b32 a70, v44
Mp 68,164,176
Rt 150,253,4608
v_mul_f32_e32 v46, s54, v46
v_mul_f32_e32 v47, s54, v47
Mp 80,168,176
Rt 152,254,4096
Cv 46,46,47
Mp 88,168,180
Rt 154,254,4608
buffer_load_dwordx4 v243, s[16:19], s92 offen offset:3072 lds
Mp 92,172,180
)" R"(
Rt 156,255,4096
s_add_u32 s92, s42, 0x40180
Mp 84,172,176
Rt 158,255,4608
s_waitcnt lgkmcnt(8)
Mp 64,128,184
Rt 160,252,8192
s_add_u32 m0, s41, 102400
Mp 72,128,188
Rt 162,252,8704
buffer_load_dwordx4 v240, s[16:19], s92 offen offset:0 lds
Mp 76,132,188
Rt 164,253,8192
v_accvgpr_write_b32 a71, v46
Mp 68,132,184
Rt 166,253,8704
v_mul_f32_e32 v48, s54, v48
v_mul_f32_e32 v49, s54, v49
Mp 80,136,184
Rt 168,254,8192
Cv 48,48,49
Mp 88,136,188
Rt 170,254,8704
buffer_load_dwordx4 v241, s[16:19], s92 offen offset:1024 lds
Mp 92,140,188
Rt 172,255,8192
v_accvgpr_write_b32 a72, v48
Mp 84,140,184
Rt 174,255,8704
s_waitcnt lgkmcnt(8)
Mp 64,144,192
Rt 128,252,12288
v_mul_f32_e32 v50, s54, v50
Mp 72,144,196
Rt 130,252,12800
buffer_load_dwordx4 v242, s[16:19], s92 offen offset:2048 lds
Mp 76,148,196
Rt 132,253,12288
v_mul_f32_e32 v51, s54, v51
Mp 68,148,192
Rt 134,253,12800
Cv 50,50,51
v_accvgpr_write_b32 a73, v50
Mp 80,152,192
Rt 136,254,12288
v_mul_f32_e32 v52, s54, v52
Mp 88,152,196
Rt 138,254,12800
v_mul_f32_e32 v53, s54, v53
Mp 92,156,196
Rt 140,255,12288
s_add_u32 s38, s38, 0xffff0080
Mp 84,156,192
Rt 142,255,12800
s_waitcnt lgkmcnt(8)
Mp 64,160,200
Rt 144,252,16384
s_add_u32 m0, s36, 0
Mp 72,160,204
Rt 146,252,16896
buffer_load_dwordx4 v8, s[4:7], s38 offen offset:0 lds
Mp 76,164,204
Rt 148,253,16384
Cv 52,52,53
)" R"(
Mp 68,164,200
Rt 150,253,16896
v_accvgpr_write_b32 a74, v52
v_mul_f32_e32 v54, s54, v54
Mp 80,168,200
Rt 152,254,16384
v_mul_f32_e32 v55, s54, v55
Mp 88,168,204
Rt 154,254,16896
buffer_load_dwordx4 v9, s[4:7], s38 offen offset:1024 lds
Mp 92,172,204
Rt 156,255,16384
Cv 54,54,55
Mp 84,172,200
Rt 158,255,16896
s_waitcnt lgkmcnt(8)
Mp 64,128,208
Rt 160,252,20480
v_accvgpr_write_b32 a75, v54
Mp 72,128,212
Rt 162,252,20992
buffer_load_dwordx4 v10, s[4:7], s38 offen offset:2048 lds
Mp 76,132,212
Rt 164,253,20480
v_mul_f32_e32 v56, s54, v56
Mp 68,132,208
Rt 166,253,20992
v_mul_f32_e32 v57, s54, v57
Cv 56,56,57
Mp 80,136,208
Rt 168,254,20480
v_accvgpr_write_b32 a76, v56
Mp 88,136,212
Rt 170,254,20992
buffer_load_dwordx4 v11, s[4:7], s38 offen offset:3072 lds
Mp 92,140,212
Rt 172,255,20480
s_add_u32 s38, s38, 0x10000
Mp 84,140,208
Rt 174,255,20992
s_waitcnt lgkmcnt(8)
Mp 64,144,216
Rt 128,252,24576
s_add_u32 m0, s36, 4096
Mp 72,144,220
Rt 130,252,25088
buffer_load_dwordx4 v8, s[4:7], s38 offen offset:0 lds
Mp 76,148,220
Rt 132,253,24576
v_mul_f32_e32 v58, s54, v58
Mp 68,148,216
Rt 134,253,25088
v_mul_f32_e32 v59, s54, v59
Cv 58,58,59
Mp 80,152,216
Rt 136,254,24576
v_accvgpr_write_b32 a77, v58
Mp 88,152,220
Rt 138,254,25088
buffer_load_dwordx4 v9, s[4:7], s38 offen offset:1024 lds
Mp 92,156,220
Rt 140,255,24576
v_mul_f32_e32 v60, s54, v60
Mp 84,156,216
)" R"(
Rt 142,255,25088
s_waitcnt vmcnt(20) lgkmcnt(8)
s_barrier
Mp 64,160,224
Rt 144,248,0
v_mul_f32_e32 v61, s54, v61
Mp 72,160,228
Rt 146,248,512
buffer_load_dwordx4 v10, s[4:7], s38 offen offset:2048 lds
Mp 76,164,228
Rt 148,249,0
Cv 60,60,61
Mp 68,164,224
Rt 150,249,512
v_accvgpr_write_b32 a78, v60
v_mul_f32_e32 v62, s54, v62
Mp 80,168,224
Rt 152,250,0
v_mul_f32_e32 v63, s54, v63
Mp 88,168,228
Rt 154,250,512
buffer_load_dwordx4 v11, s[4:7], s38 offen offset:3072 lds
Mp 92,172,228
Rt 156,251,0
Cv 62,62,63
Mp 84,172,224
Rt 158,251,512
s_waitcnt vmcnt(15) lgkmcnt(8)
s_barrier
Mp 64,128,232
Rt 160,248,4096
v_accvgpr_write_b32 a79, v62
Mp 72,128,236
Rt 162,248,4608
Mp 76,132,236
Rt 164,249,4096
Mp 68,132,232
Rt 166,249,4608
s_add_u32 s92, s42, 0x1c0180
Mp 80,136,232
Rt 168,250,4096
s_add_u32 m0, s41, 131072
Mp 88,136,236
Rt 170,250,4608
buffer_load_dwordx4 v240, s[16:19], s92 offen offset:0 lds
Mp 92,140,236
Rt 172,251,4096
Mp 84,140,232
Rt 174,251,4608
s_waitcnt lgkmcnt(8)
v_mfma_f32_16x16x32_bf16 v[32:35], a[144:147], v[128:131], 0
Rt 128,248,8192
v_mfma_f32_16x16x32_bf16 v[40:43], a[144:147], v[132:135], 0
Rt 130,248,8704
v_mfma_f32_16x16x32_bf16 v[44:47], a[148:151], v[132:135], 0
Rt 132,249,8192
v_mfma_f32_16x16x32_bf16 v[36:39], a[148:151], v[128:131], 0
Rt 134,249,8704
buffer_load_dwordx4 v241, s[16:19], s92 offen offset:1024 lds
v_mfma_f32_16x16x32_bf16 v[48:51], a[152:155], v[128:131], 0
Rt 136,250,8192
v_mul_f32_e32 v64, s54, v64
v_mfma_f32_16x16x32_bf16 v[56:59], a[152:155], v[132:135], 0
Rt 138,250,8704
v_mul_f32_e32 v65, s54, v65
v_mfma_f32_16x16x32_bf16 v[60:63], a[156:159], v[132:135], 0
)" R"(
Rt 140,251,8192
Cv 64,64,65
v_mfma_f32_16x16x32_bf16 v[52:55], a[156:159], v[128:131], 0
Rt 142,251,8704
s_waitcnt lgkmcnt(8)
Mp 32,160,136
Rt 144,248,12288
v_accvgpr_write_b32 a80, v64
Mp 40,160,140
Rt 146,248,12800
buffer_load_dwordx4 v242, s[16:19], s92 offen offset:2048 lds
Mp 44,164,140
Rt 148,249,12288
v_mul_f32_e32 v66, s54, v66
Mp 36,164,136
Rt 150,249,12800
v_mul_f32_e32 v67, s54, v67
Mp 48,168,136
Rt 152,250,12288
Cv 66,66,67
Mp 56,168,140
Rt 154,250,12800
v_accvgpr_write_b32 a81, v66
Mp 60,172,140
Rt 156,251,12288
v_mul_f32_e32 v68, s54, v68
Mp 52,172,136
Rt 158,251,12800
buffer_load_dwordx4 v243, s[16:19], s92 offen offset:3072 lds
s_waitcnt lgkmcnt(8)
Mp 32,128,144
Rt 160,248,16384
v_mul_f32_e32 v69, s54, v69
Mp 40,128,148
Rt 162,248,16896
Cv 68,68,69
Mp 44,132,148
Rt 164,249,16384
v_accvgpr_write_b32 a82, v68
Mp 36,132,144
Rt 166,249,16896
s_add_u32 s92, s42, 0x200180
Mp 48,136,144
Rt 168,250,16384
s_add_u32 m0, s41, 135168
Mp 56,136,148
Rt 170,250,16896
buffer_load_dwordx4 v240, s[16:19], s92 offen offset:0 lds
Mp 60,140,148
Rt 172,251,16384
v_mul_f32_e32 v70, s54, v70
Mp 52,140,144
Rt 174,251,16896
s_waitcnt lgkmcnt(8)
Mp 32,144,152
Rt 128,248,20480
v_mul_f32_e32 v71, s54, v71
Mp 40,144,156
Rt 130,248,20992
Cv 70,70,71
Mp 44,148,156
Rt 132,249,20480
v_accvgpr_write_b32 a83, v70
Mp 36,148,152
)" R"(
Rt 134,249,20992
buffer_load_dwordx4 v241, s[16:19], s92 offen offset:1024 lds
Mp 48,152,152
Rt 136,250,20480
v_mul_f32_e32 v72, s54, v72
Mp 56,152,156
Rt 138,250,20992
v_mul_f32_e32 v73, s54, v73
Mp 60,156,156
Rt 140,251,20480
Cv 72,72,73
Mp 52,156,152
Rt 142,251,20992
s_waitcnt lgkmcnt(8)
Mp 32,160,160
Rt 144,248,24576
v_accvgpr_write_b32 a84, v72
Mp 40,160,164
Rt 146,248,25088
buffer_load_dwordx4 v242, s[16:19], s92 offen offset:2048 lds
Mp 44,164,164
Rt 148,249,24576
v_mul_f32_e32 v74, s54, v74
Mp 36,164,160
Rt 150,249,25088
v_mul_f32_e32 v75, s54, v75
Mp 48,168,160
Rt 152,250,24576
Cv 74,74,75
Mp 56,168,164
Rt 154,250,25088
v_accvgpr_write_b32 a85, v74
Mp 60,172,164
Rt 156,251,24576
v_mul_f32_e32 v76, s54, v76
Mp 52,172,160
Rt 158,251,25088
s_waitcnt lgkmcnt(8)
Mp 32,128,168
Rt 160,244,32768
v_mul_f32_e32 v77, s54, v77
Mp 40,128,172
Rt 162,244,33280
Cv 76,76,77
Mp 44,132,172
Rt 164,245,32768
v_accvgpr_write_b32 a86, v76
Mp 36,132,168
Rt 166,245,33280
v_mul_f32_e32 v78, s54, v78
Mp 48,136,168
Rt 168,246,32768
v_mul_f32_e32 v79, s54, v79
Mp 56,136,172
Rt 170,246,33280
Cv 78,78,79
Mp 60,140,172
Rt 172,247,32768
v_accvgpr_write_b32 a87, v78
Mp 52,140,168
Rt 174,247,33280
s_waitcnt vmcnt(15) lgkmcnt(8)
s_barrier
Mp 32,144,176
Rt 128,244,36864
)" R"(
v_mul_f32_e32 v80, s54, v80
Mp 40,144,180
Rt 130,244,37376
s_add_u32 s39, s39, 0x40000
Mp 44,148,180
Rt 132,245,36864
s_add_u32 m0, s37, 0
Mp 36,148,176
Rt 134,245,37376
buffer_load_dwordx4 v12, s[8:11], s39 offen offset:0 lds
Mp 48,152,176
Rt 136,246,36864
v_mul_f32_e32 v81, s54, v81
Mp 56,152,180
Rt 138,246,37376
Cv 80,80,81
Mp 60,156,180
Rt 140,247,36864
v_accvgpr_write_b32 a88, v80
Mp 52,156,176
Rt 142,247,37376
s_waitcnt lgkmcnt(8)
Mp 32,160,184
Rt 144,244,40960
v_mul_f32_e32 v82, s54, v82
Mp 40,160,188
Rt 146,244,41472
buffer_load_dwordx4 v13, s[8:11], s39 offen offset:1024 lds
Mp 44,164,188
Rt 148,245,40960
v_mul_f32_e32 v83, s54, v83
Mp 36,164,184
Rt 150,245,41472
Cv 82,82,83
Mp 48,168,184
Rt 152,246,40960
v_accvgpr_write_b32 a89, v82
Mp 56,168,188
Rt 154,246,41472
v_mul_f32_e32 v84, s54, v84
Mp 60,172,188
Rt 156,247,40960
v_mul_f32_e32 v85, s54, v85
Mp 52,172,184
Rt 158,247,41472
buffer_load_dwordx4 v14, s[8:11], s39 offen offset:2048 lds
s_waitcnt lgkmcnt(8)
Mp 32,128,192
Rt 160,244,45056
Cv 84,84,85
Mp 40,128,196
Rt 162,244,45568
v_accvgpr_write_b32 a90, v84
Mp 44,132,196
Rt 164,245,45056
v_mul_f32_e32 v86, s54, v86
Mp 36,132,192
Rt 166,245,45568
v_mul_f32_e32 v87, s54, v87
Mp 48,136,192
Rt 168,246,45056
Cv 86,86,87
Mp 56,136,196
Rt 170,246,45568
)" R"(
buffer_load_dwordx4 v15, s[8:11], s39 offen offset:3072 lds
Mp 60,140,196
Rt 172,247,45056
v_accvgpr_write_b32 a91, v86
Mp 52,140,192
Rt 174,247,45568
s_waitcnt lgkmcnt(8)
Mp 32,144,200
Rt 128,244,49152
v_mul_f32_e32 v88, s54, v88
Mp 40,144,204
Rt 130,244,49664
s_add_u32 s39, s39, 0x40000
Mp 44,148,204
Rt 132,245,49152
s_add_u32 m0, s37, 16384
Mp 36,148,200
Rt 134,245,49664
buffer_load_dwordx4 v12, s[8:11], s39 offen offset:0 lds
Mp 48,152,200
Rt 136,246,49152
v_mul_f32_e32 v89, s54, v89
Mp 56,152,204
Rt 138,246,49664
Cv 88,88,89
Mp 60,156,204
Rt 140,247,49152
v_accvgpr_write_b32 a92, v88
Mp 52,156,200
Rt 142,247,49664
s_waitcnt lgkmcnt(8)
Mp 32,160,208
Rt 144,244,53248
v_mul_f32_e32 v90, s54, v90
Mp 40,160,212
Rt 146,244,53760
buffer_load_dwordx4 v13, s[8:11], s39 offen offset:1024 lds
Mp 44,164,212
Rt 148,245,53248
v_mul_f32_e32 v91, s54, v91
Mp 36,164,208
Rt 150,245,53760
Cv 90,90,91
Mp 48,168,208
Rt 152,246,53248
v_accvgpr_write_b32 a93, v90
Mp 56,168,212
Rt 154,246,53760
v_mul_f32_e32 v92, s54, v92
Mp 60,172,212
Rt 156,247,53248
v_mul_f32_e32 v93, s54, v93
Mp 52,172,208
Rt 158,247,53760
buffer_load_dwordx4 v14, s[8:11], s39 offen offset:2048 lds
s_waitcnt lgkmcnt(8)
Mp 32,128,216
Rt 160,244,57344
Cv 92,92,93
Mp 40,128,220
Rt 162,244,57856
v_accvgpr_write_b32 a94, v92
Mp 44,132,220
Rt 164,245,57344
)" R"(
v_mul_f32_e32 v94, s54, v94
Mp 36,132,216
Rt 166,245,57856
v_mul_f32_e32 v95, s54, v95
Mp 48,136,216
Rt 168,246,57344
Cv 94,94,95
Mp 56,136,220
Rt 170,246,57856
buffer_load_dwordx4 v15, s[8:11], s39 offen offset:3072 lds
Mp 60,140,220
Rt 172,247,57344
v_accvgpr_write_b32 a95, v94
Mp 52,140,216
Rt 174,247,57856
s_waitcnt lgkmcnt(8)
Mp 32,144,224
Rt 128,248,32768
Mp 40,144,228
Rt 130,248,33280
Mp 44,148,228
Rt 132,249,32768
Mp 36,148,224
Rt 134,249,33280
Mp 48,152,224
Rt 136,250,32768
Mp 56,152,228
Rt 138,250,33280
Mp 60,156,228
Rt 140,251,32768
Mp 52,156,224
Rt 142,251,33280
s_waitcnt vmcnt(8) lgkmcnt(8)
s_barrier
Mp 32,160,232
Rt 144,248,36864
Mp 40,160,236
Rt 146,248,37376
s_add_u32 s38, s38, 0xffff0080
Mp 44,164,236
Rt 148,249,36864
s_add_u32 m0, s36, 32768
Mp 36,164,232
Rt 150,249,37376
buffer_load_dwordx4 v8, s[4:7], s38 offen offset:0 lds
Mp 48,168,232
Rt 152,250,36864
Mp 56,168,236
Rt 154,250,37376
Mp 60,172,236
Rt 156,251,36864
Mp 52,172,232
Rt 158,251,37376
s_waitcnt lgkmcnt(8)
v_mfma_f32_16x16x32_bf16 v[64:67], a[128:131], v[128:131], 0
Rt 160,248,40960
v_mfma_f32_16x16x32_bf16 v[72:75], a[128:131], v[132:135], 0
Rt 162,248,41472
buffer_load_dwordx4 v9, s[4:7], s38 offen offset:1024 lds
v_mfma_f32_16x16x32_bf16 v[76:79], a[132:135], v[132:135], 0
Rt 164,249,40960
v_mfma_f32_16x16x32_bf16 v[68:71], a[132:135], v[128:131], 0
Rt 166,249,41472
v_mfma_f32_16x16x32_bf16 v[80:83], a[136:139], v[128:131], 0
Rt 168,250,40960
)" R"(
v_mul_f32_e32 v32, s54, v32
v_mfma_f32_16x16x32_bf16 v[88:91], a[136:139], v[132:135], 0
Rt 170,250,41472
v_mul_f32_e32 v33, s54, v33
v_mfma_f32_16x16x32_bf16 v[92:95], a[140:143], v[132:135], 0
Rt 172,251,40960
Cv 32,32,33
v_mfma_f32_16x16x32_bf16 v[84:87], a[140:143], v[128:131], 0
Rt 174,251,41472
buffer_load_dwordx4 v10, s[4:7], s38 offen offset:2048 lds
s_waitcnt lgkmcnt(8)
Mp 64,144,136
Rt 128,248,45056
v_accvgpr_write_b32 a96, v32
Mp 72,144,140
Rt 130,248,45568
v_mul_f32_e32 v34, s54, v34
Mp 76,148,140
Rt 132,249,45056
v_mul_f32_e32 v35, s54, v35
Mp 68,148,136
Rt 134,249,45568
Cv 34,34,35
Mp 80,152,136
Rt 136,250,45056
v_accvgpr_write_b32 a97, v34
Mp 88,152,140
Rt 138,250,45568
buffer_load_dwordx4 v11, s[4:7], s38 offen offset:3072 lds
Mp 92,156,140
Rt 140,251,45056
v_mul_f32_e32 v36, s54, v36
Mp 84,156,136
Rt 142,251,45568
s_waitcnt lgkmcnt(8)
Mp 64,160,144
Rt 144,248,49152
v_mul_f32_e32 v37, s54, v37
Mp 72,160,148
Rt 146,248,49664
s_add_u32 s38, s38, 0x10000
Mp 76,164,148
Rt 148,249,49152
s_add_u32 m0, s36, 36864
Mp 68,164,144
Rt 150,249,49664
buffer_load_dwordx4 v8, s[4:7], s38 offen offset:0 lds
Mp 80,168,144
Rt 152,250,49152
Cv 36,36,37
Mp 88,168,148
Rt 154,250,49664
v_accvgpr_write_b32 a98, v36
Mp 92,172,148
Rt 156,251,49152
v_mul_f32_e32 v38, s54, v38
Mp 84,172,144
Rt 158,251,49664
s_waitcnt lgkmcnt(8)
Mp 64,128,152
Rt 160,248,53248
v_mul_f32_e32 v39, s54, v39
Mp 72,128,156
Rt 162,248,53760
)" R"(
buffer_load_dwordx4 v9, s[4:7], s38 offen offset:1024 lds
Mp 76,132,156
Rt 164,249,53248
Cv 38,38,39
Mp 68,132,152
Rt 166,249,53760
v_accvgpr_write_b32 a99, v38
Mp 80,136,152
Rt 168,250,53248
v_mul_f32_e32 v40, s54, v40
Mp 88,136,156
Rt 170,250,53760
v_mul_f32_e32 v41, s54, v41
Mp 92,140,156
Rt 172,251,53248
Cv 40,40,41
Mp 84,140,152
Rt 174,251,53760
buffer_load_dwordx4 v10, s[4:7], s38 offen offset:2048 lds
s_waitcnt lgkmcnt(8)
Mp 64,144,160
Rt 128,248,57344
v_accvgpr_write_b32 a100, v40
Mp 72,144,164
Rt 130,248,57856
v_mul_f32_e32 v42, s54, v42
Mp 76,148,164
Rt 132,249,57344
v_mul_f32_e32 v43, s54, v43
Mp 68,148,160
Rt 134,249,57856
Cv 42,42,43
Mp 80,152,160
Rt 136,250,57344
v_accvgpr_write_b32 a101, v42
Mp 88,152,164
Rt 138,250,57856
buffer_load_dwordx4 v11, s[4:7], s38 offen offset:3072 lds
Mp 92,156,164
Rt 140,251,57344
v_mul_f32_e32 v44, s54, v44
Mp 84,156,160
Rt 142,251,57856
s_waitcnt lgkmcnt(8)
Mp 64,160,168
Rt 144,252,0
v_mul_f32_e32 v45, s54, v45
Mp 72,160,172
Rt 146,252,512
Cv 44,44,45
Mp 76,164,172
Rt 148,253,0
v_accvgpr_write_b32 a102, v44
Mp 68,164,168
Rt 150,253,512
v_mul_f32_e32 v46, s54, v46
Mp 80,168,168
Rt 152,254,0
v_mul_f32_e32 v47, s54, v47
Mp 88,168,172
Rt 154,254,512
Cv 46,46,47
Mp 92,172,172
Rt 156,255,0
)" R"(
v_accvgpr_write_b32 a103, v46
Mp 84,172,168
Rt 158,255,512
s_waitcnt vmcnt(12) lgkmcnt(8)
s_barrier
Mp 64,128,176
Rt 160,252,4096
v_mul_f32_e32 v48, s54, v48
Mp 72,128,180
Rt 162,252,4608
s_add_u32 s39, s39, 0x40000
Mp 76,132,180
Rt 164,253,4096
s_add_u32 m0, s37, 32768
Mp 68,132,176
Rt 166,253,4608
buffer_load_dwordx4 v12, s[8:11], s39 offen offset:0 lds
Mp 80,136,176
Rt 168,254,4096
v_mul_f32_e32 v49, s54, v49
Mp 88,136,180
Rt 170,254,4608
Cv 48,48,49
Mp 92,140,180
Rt 172,255,4096
v_accvgpr_write_b32 a104, v48
Mp 84,140,176
Rt 174,255,4608
s_waitcnt lgkmcnt(8)
Mp 64,144,184
Rt 128,252,8192
v_mul_f32_e32 v50, s54, v50
Mp 72,144,188
Rt 130,252,8704
buffer_load_dwordx4 v13, s[8:11], s39 offen offset:1024 lds
Mp 76,148,188
Rt 132,253,8192
v_mul_f32_e32 v51, s54, v51
Mp 68,148,184
Rt 134,253,8704
Cv 50,50,51
Mp 80,152,184
Rt 136,254,8192
v_accvgpr_write_b32 a105, v50
Mp 88,152,188
Rt 138,254,8704
v_mul_f32_e32 v52, s54, v52
Mp 92,156,188
Rt 140,255,8192
v_mul_f32_e32 v53, s54, v53
Mp 84,156,184
Rt 142,255,8704
buffer_load_dwordx4 v14, s[8:11], s39 offen offset:2048 lds
s_waitcnt lgkmcnt(8)
Mp 64,160,192
Rt 144,252,12288
Cv 52,52,53
Mp 72,160,196
Rt 146,252,12800
v_accvgpr_write_b32 a106, v52
Mp 76,164,196
Rt 148,253,12288
v_mul_f32_e32 v54, s54, v54
Mp 68,164,192
Rt 150,253,12800
)" R"(
v_mul_f32_e32 v55, s54, v55
Mp 80,168,192
Rt 152,254,12288
Cv 54,54,55
Mp 88,168,196
Rt 154,254,12800
buffer_load_dwordx4 v15, s[8:11], s39 offen offset:3072 lds
Mp 92,172,196
Rt 156,255,12288
v_accvgpr_write_b32 a107, v54
Mp 84,172,192
Rt 158,255,12800
s_waitcnt lgkmcnt(8)
Mp 64,128,200
Rt 160,252,16384
v_mul_f32_e32 v56, s54, v56
Mp 72,128,204
Rt 162,252,16896
v_mul_f32_e32 v57, s54, v57
Mp 76,132,204
Rt 164,253,16384
Cv 56,56,57
Mp 68,132,200
Rt 166,253,16896
v_accvgpr_write_b32 a108, v56
Mp 80,136,200
Rt 168,254,16384
v_mul_f32_e32 v58, s54, v58
Mp 88,136,204
Rt 170,254,16896
v_mul_f32_e32 v59, s54, v59
Mp 92,140,204
Rt 172,255,16384
Cv 58,58,59
Mp 84,140,200
Rt 174,255,16896
s_waitcnt lgkmcnt(8)
Mp 64,144,208
Rt 128,252,20480
Rt 0,18,0
Mp 72,144,212
Rt 130,252,20992
Rt 2,18,2048
Mp 76,148,212
Rt 132,253,20480
Rt 4,19,0
Mp 68,148,208
Rt 134,253,20992
Rt 6,19,2048
Mp 80,152,208
Rt 136,254,20480
Rt 8,20,0
Mp 88,152,212
Rt 138,254,20992
Rt 10,20,2048
Mp 92,156,212
Rt 140,255,20480
Rt 12,21,0
Mp 84,156,208
Rt 142,255,20992
Rt 14,21,2048
Mp 64,160,216
Rt 144,252,24576
Rt 16,22,0
)" R"(
Mp 72,160,220
Rt 146,252,25088
Rt 18,22,2048
Mp 76,164,220
Rt 148,253,24576
Rt 20,23,0
Mp 68,164,216
Rt 150,253,25088
Rt 22,23,2048
Mp 80,168,216
Rt 152,254,24576
Rt 24,24,0
Mp 88,168,220
Rt 154,254,25088
Rt 26,24,2048
Mp 92,172,220
Rt 156,255,24576
Rt 28,25,0
Mp 84,172,216
Rt 158,255,25088
Rt 30,25,2048
Mp 64,128,224
Rb 32,16,0
v_accvgpr_write_b32 a109, v58
Mp 72,128,228
Rb 36,16,2048
v_mul_f32_e32 v60, s54, v60
Mp 76,132,228
Rb 40,16,8192
v_mul_f32_e32 v61, s54, v61
Mp 68,132,224
Rb 44,16,10240
Cv 60,60,61
Mp 80,136,224
Rb 48,16,16384
v_accvgpr_write_b32 a110, v60
Mp 88,136,228
Rb 52,16,18432
v_mul_f32_e32 v62, s54, v62
Mp 92,140,228
Rb 56,16,24576
v_mul_f32_e32 v63, s54, v63
Mp 84,140,224
Rb 60,16,26624
s_waitcnt lgkmcnt(9)
Mp 64,144,232
Cv 62,62,63
v_accvgpr_write_b32 a111, v62
Mp 72,144,236
Mp 76,148,236
Mp 68,148,232
Mp 80,152,232
Mp 88,152,236
Mp 92,156,236
Mp 84,156,232
s_cmp_eq_u32 s85, 1
s_waitcnt vmcnt(12) lgkmcnt(0)
s_barrier
s_cbranch_scc1 .LzBV
Mz 32,0,32
Rt 128,18,16384
v_mul_f32_e32 v64, s54, v64
Mz 96,0,40
v_mul_f32_e32 v65, s54, v65
Cv 64,64,65
)" R"(
Mz 128,0,44
Rt 130,18,18432
v_accvgpr_write_b32 a112, v64
Mz 160,0,48
s_add_u32 s39, s39, 0x40000
v_mul_f32_e32 v66, s54, v66
Mz 192,0,52
Rt 132,19,16384
s_add_u32 m0, s37, 49152
Mz 224,0,56
buffer_load_dwordx4 v12, s[8:11], s39 offen offset:0 lds
v_mfma_f32_16x16x32_bf16 a[192:195], a[0:3], a[60:63], 0
Rt 134,19,18432
v_mul_f32_e32 v67, s54, v67
v_mfma_f32_16x16x32_bf16 a[196:199], a[4:7], a[60:63], 0
Cv 66,66,67
v_accvgpr_write_b32 a113, v66
Mz 228,4,56
Rt 136,20,16384
v_mul_f32_e32 v68, s54, v68
Mz 196,4,52
v_mul_f32_e32 v69, s54, v69
Cv 68,68,69
buffer_store_dwordx4 a[64:67], v2, s[20:23], s51 offen offset:0 sc0 sc1
Mz 164,4,48
Rt 138,20,18432
v_accvgpr_write_b32 a114, v68
Mz 132,4,44
v_mul_f32_e32 v70, s54, v70
v_mul_f32_e32 v71, s54, v71
Mz 100,4,40
Rt 140,21,16384
Cv 70,70,71
Mz 36,4,32
buffer_load_dwordx4 v13, s[8:11], s39 offen offset:1024 lds
Mz 40,8,32
Rt 142,21,18432
v_accvgpr_write_b32 a115, v70
Mz 104,8,40
v_mul_f32_e32 v72, s54, v72
v_mul_f32_e32 v73, s54, v73
Mz 136,8,44
Rt 144,22,16384
Cv 72,72,73
Mz 168,8,48
v_accvgpr_write_b32 a116, v72
v_mul_f32_e32 v74, s54, v74
buffer_store_dwordx4 a[68:71], v2, s[20:23], s51 offen offset:1024 sc0 sc1
Mz 200,8,52
Rt 146,22,18432
v_mul_f32_e32 v75, s54, v75
Mz 232,8,56
Cv 74,74,75
v_accvgpr_write_b32 a117, v74
v_mfma_f32_16x16x32_bf16 a[200:203], a[8:11], a[60:63], 0
Rt 148,23,16384
v_mul_f32_e32 v76, s54, v76
v_mfma_f32_16x16x32_bf16 a[204:207], a[12:15], a[60:63], 0
buffer_load_dwordx4 v14, s[8:11], s39 offen offset:2048 lds
Mz 236,12,56
Rt 150,23,18432
v_mul_f32_e32 v77, s54, v77
Mz 204,12,52
Cv 76,76,77
)" R"(
v_accvgpr_write_b32 a118, v76
Mz 172,12,48
Rt 152,24,16384
v_mul_f32_e32 v78, s54, v78
Mz 140,12,44
v_mul_f32_e32 v79, s54, v79
Cv 78,78,79
buffer_store_dwordx4 a[72:75], v2, s[20:23], s51 offen offset:2048 sc0 sc1
Mz 108,12,40
Rt 154,24,18432
v_accvgpr_write_b32 a119, v78
Mz 44,12,32
v_mul_f32_e32 v80, s54, v80
v_mul_f32_e32 v81, s54, v81
Mz 48,16,32
Rt 156,25,16384
Cv 80,80,81
Mz 112,16,40
buffer_load_dwordx4 v15, s[8:11], s39 offen offset:3072 lds
Mz 144,16,44
Rt 158,25,18432
v_accvgpr_write_b32 a120, v80
Mz 176,16,48
v_mul_f32_e32 v82, s54, v82
v_mul_f32_e32 v83, s54, v83
Mz 208,16,52
Rb 160,17,0
Cv 82,82,83
Mz 240,16,56
v_accvgpr_write_b32 a121, v82
v_mul_f32_e32 v84, s54, v84
buffer_store_dwordx4 a[76:79], v2, s[20:23], s51 offen offset:3072 sc0 sc1
v_mfma_f32_16x16x32_bf16 a[208:211], a[16:19], a[60:63], 0
Rb 164,17,2048
v_mul_f32_e32 v85, s54, v85
v_mfma_f32_16x16x32_bf16 a[212:215], a[20:23], a[60:63], 0
s_add_u32 s39, s39, 0x40000
Cv 84,84,85
Mz 244,20,56
Rb 168,17,8192
s_add_u32 m0, s37, 0
Mz 212,20,52
buffer_load_dwordx4 v12, s[8:11], s39 offen offset:0 lds
Mz 180,20,48
Rb 172,17,10240
v_accvgpr_write_b32 a122, v84
Mz 148,20,44
v_mul_f32_e32 v86, s54, v86
v_mul_f32_e32 v87, s54, v87
Mz 116,20,40
Rb 176,17,16384
Cv 86,86,87
Mz 52,20,32
v_accvgpr_write_b32 a123, v86
v_mul_f32_e32 v88, s54, v88
buffer_store_dwordx4 a[80:83], v3, s[20:23], s51 offen offset:0 sc0 sc1
Mz 56,24,32
Rb 180,17,18432
v_mul_f32_e32 v89, s54, v89
Mz 120,24,40
Cv 88,88,89
v_accvgpr_write_b32 a124, v88
Mz 152,24,44
Rb 184,17,24576
)" R"(
v_mul_f32_e32 v90, s54, v90
Mz 184,24,48
buffer_load_dwordx4 v13, s[8:11], s39 offen offset:1024 lds
Mz 216,24,52
Rb 188,17,26624
v_mul_f32_e32 v91, s54, v91
Mz 248,24,56
Cv 90,90,91
v_accvgpr_write_b32 a125, v90
v_mfma_f32_16x16x32_bf16 a[216:219], a[24:27], a[60:63], 0
v_mul_f32_e32 v92, s54, v92
v_mul_f32_e32 v93, s54, v93
v_mfma_f32_16x16x32_bf16 a[220:223], a[28:31], a[60:63], 0
Cv 92,92,93
v_accvgpr_write_b32 a126, v92
buffer_store_dwordx4 a[84:87], v3, s[20:23], s51 offen offset:1024 sc0 sc1
Mz 252,28,56
v_mul_f32_e32 v94, s54, v94
v_mul_f32_e32 v95, s54, v95
Mz 220,28,52
Cv 94,94,95
v_accvgpr_write_b32 a127, v94
Mz 188,28,48
Mz 156,28,44
buffer_load_dwordx4 v14, s[8:11], s39 offen offset:2048 lds
Mz 124,28,40
Mz 60,28,32
Mz 64,0,36
Mz 68,4,36
buffer_store_dwordx4 a[88:91], v3, s[20:23], s51 offen offset:2048 sc0 sc1
Mz 72,8,36
Mz 76,12,36
Mz 80,16,36
Mz 84,20,36
buffer_load_dwordx4 v15, s[8:11], s39 offen offset:3072 lds
Mz 88,24,36
Mz 92,28,36
s_waitcnt vmcnt(15) lgkmcnt(0)
s_barrier
Mv 32,128,160
Rt 0,18,32768
Mv 64,128,164
buffer_store_dwordx4 a[92:95], v3, s[20:23], s51 offen offset:3072 sc0 sc1
Mv 96,128,168
Rt 2,18,34816
Mv 128,128,172
s_add_u32 s38, s38, 0xffff0080
Mv 160,128,176
Rt 4,19,32768
s_add_u32 m0, s36, 0
Mv 192,128,180
buffer_load_dwordx4 v8, s[4:7], s38 offen offset:0 lds
Mv 224,128,184
Rt 6,19,34816
Ma 192,128,188
Ma 196,132,188
Rt 8,20,32768
Mv 228,132,184
s_add_u32 s51, s51, 0x8000
buffer_store_dwordx4 a[96:99], v2, s[20:23], s51 offen offset:0 sc0 sc1
Mv 196,132,180
Rt 10,20,34816
Mv 164,132,176
Mv 132,132,172
Rt 12,21,32768
)" R"(
Mv 100,132,168
buffer_load_dwordx4 v9, s[4:7], s38 offen offset:1024 lds
Mv 68,132,164
Rt 14,21,34816
Mv 36,132,160
Mv 40,136,160
Rt 16,22,32768
Mv 72,136,164
buffer_store_dwordx4 a[100:103], v2, s[20:23], s51 offen offset:1024 sc0 sc1
Mv 104,136,168
Rt 18,22,34816
Mv 136,136,172
Mv 168,136,176
Rt 20,23,32768
Mv 200,136,180
buffer_load_dwordx4 v10, s[4:7], s38 offen offset:2048 lds
Mv 232,136,184
Rt 22,23,34816
Ma 200,136,188
Ma 204,140,188
Rt 24,24,32768
Mv 236,140,184
buffer_store_dwordx4 a[104:107], v2, s[20:23], s51 offen offset:2048 sc0 sc1
Mv 204,140,180
Rt 26,24,34816
Mv 172,140,176
Mv 140,140,172
Rt 28,25,32768
Mv 108,140,168
buffer_load_dwordx4 v11, s[4:7], s38 offen offset:3072 lds
Mv 76,140,164
Rt 30,25,34816
Mv 44,140,160
Mv 48,144,160
Rb 32,16,32768
Mv 80,144,164
buffer_store_dwordx4 a[108:111], v2, s[20:23], s51 offen offset:3072 sc0 sc1
Mv 112,144,168
Rb 36,16,34816
Mv 144,144,172
s_add_u32 s38, s38, 0x10000
Mv 176,144,176
Rb 40,16,40960
s_add_u32 m0, s36, 4096
Mv 208,144,180
buffer_load_dwordx4 v8, s[4:7], s38 offen offset:0 lds
Mv 240,144,184
Rb 44,16,43008
Ma 208,144,188
Ma 212,148,188
Rb 48,16,49152
Mv 244,148,184
buffer_store_dwordx4 a[112:115], v3, s[20:23], s51 offen offset:0 sc0 sc1
Mv 212,148,180
Rb 52,16,51200
Mv 180,148,176
Mv 148,148,172
Rb 56,16,57344
Mv 116,148,168
buffer_load_dwordx4 v9, s[4:7], s38 offen offset:1024 lds
Mv 84,148,164
Rb 60,16,59392
Mv 52,148,160
Mv 56,152,160
)" R"(
Mv 88,152,164
buffer_store_dwordx4 a[116:119], v3, s[20:23], s51 offen offset:1024 sc0 sc1
Mv 120,152,168
Mv 152,152,172
Mv 184,152,176
Mv 216,152,180
buffer_load_dwordx4 v10, s[4:7], s38 offen offset:2048 lds
Mv 248,152,184
Ma 216,152,188
Ma 220,156,188
Mv 252,156,184
buffer_store_dwordx4 a[120:123], v3, s[20:23], s51 offen offset:2048 sc0 sc1
Mv 220,156,180
Mv 188,156,176
Mv 156,156,172
Mv 124,156,168
buffer_load_dwordx4 v11, s[4:7], s38 offen offset:3072 lds
Mv 92,156,164
Mv 60,156,160
s_waitcnt vmcnt(24) lgkmcnt(0)
s_barrier
Mv 32,0,32
Rt 128,18,49152
Mv 64,0,36
buffer_store_dwordx4 a[124:127], v3, s[20:23], s51 offen offset:3072 sc0 sc1
Mv 96,0,40
Rt 130,18,51200
Mv 128,0,44
s_add_u32 s39, s39, 0x40000
Mv 160,0,48
Rt 132,19,49152
s_add_u32 m0, s37, 16384
Mv 192,0,52
buffer_load_dwordx4 v12, s[8:11], s39 offen offset:0 lds
Mv 224,0,56
Rt 134,19,51200
Ma 192,0,60
Ma 196,4,60
Rt 136,20,49152
Mv 228,4,56
Mv 196,4,52
Rt 138,20,51200
Mv 164,4,48
Mv 132,4,44
Rt 140,21,49152
Mv 100,4,40
buffer_load_dwordx4 v13, s[8:11], s39 offen offset:1024 lds
Mv 68,4,36
Rt 142,21,51200
Mv 36,4,32
Mv 40,8,32
Rt 144,22,49152
Mv 72,8,36
Mv 104,8,40
Rt 146,22,51200
Mv 136,8,44
Mv 168,8,48
Rt 148,23,49152
Mv 200,8,52
buffer_load_dwordx4 v14, s[8:11], s39 offen offset:2048 lds
Mv 232,8,56
Rt 150,23,51200
Ma 200,8,60
Ma 204,12,60
Rt 152,24,49152
)" R"(
Mv 236,12,56
Mv 204,12,52
Rt 154,24,51200
Mv 172,12,48
Mv 140,12,44
Rt 156,25,49152
Mv 108,12,40
buffer_load_dwordx4 v15, s[8:11], s39 offen offset:3072 lds
Mv 76,12,36
Rt 158,25,51200
Mv 44,12,32
Mv 48,16,32
Rb 160,17,32768
Mv 80,16,36
Mv 112,16,40
Rb 164,17,34816
Mv 144,16,44
s_add_u32 s39, s39, 0x40000
Mv 176,16,48
Rb 168,17,40960
s_add_u32 m0, s37, 32768
Mv 208,16,52
buffer_load_dwordx4 v12, s[8:11], s39 offen offset:0 lds
Mv 240,16,56
Rb 172,17,43008
Ma 208,16,60
Ma 212,20,60
Rb 176,17,49152
Mv 244,20,56
Mv 212,20,52
Rb 180,17,51200
Mv 180,20,48
Mv 148,20,44
Rb 184,17,57344
Mv 116,20,40
buffer_load_dwordx4 v13, s[8:11], s39 offen offset:1024 lds
Mv 84,20,36
Rb 188,17,59392
Mv 52,20,32
Mv 56,24,32
Mv 88,24,36
Mv 120,24,40
Mv 152,24,44
Mv 184,24,48
Mv 216,24,52
buffer_load_dwordx4 v14, s[8:11], s39 offen offset:2048 lds
Mv 248,24,56
Ma 216,24,60
Ma 220,28,60
Mv 252,28,56
Mv 220,28,52
Mv 188,28,48
Mv 156,28,44
Mv 124,28,40
buffer_load_dwordx4 v15, s[8:11], s39 offen offset:3072 lds
Mv 92,28,36
Mv 60,28,32
s_waitcnt vmcnt(8) lgkmcnt(0)
s_barrier
buffer_store_dword v6, v7, s[20:23], s52 offen offset:0 sc0 sc1
Mv 32,128,160
Rt 0,18,0
Mv 64,128,164
Mv 96,128,168
Rt 2,18,2048
)" R"(
Mv 128,128,172
s_add_u32 s38, s38, 0xffff0080
Mv 160,128,176
Rt 4,19,0
s_add_u32 m0, s36, 32768
Mv 192,128,180
buffer_load_dwordx4 v8, s[4:7], s38 offen offset:0 lds
Mv 224,128,184
Rt 6,19,2048
Ma 192,128,188
Ma 196,132,188
Rt 8,20,0
s_mov_b32 s40, 5
Mv 228,132,184
Mv 196,132,180
Rt 10,20,2048
Mv 164,132,176
Mv 132,132,172
Rt 12,21,0
Mv 100,132,168
buffer_load_dwordx4 v9, s[4:7], s38 offen offset:1024 lds
Mv 68,132,164
Rt 14,21,2048
Mv 36,132,160
Mv 40,136,160
Rt 16,22,0
Mv 72,136,164
Mv 104,136,168
Rt 18,22,2048
Mv 136,136,172
Mv 168,136,176
Rt 20,23,0
Mv 200,136,180
buffer_load_dwordx4 v10, s[4:7], s38 offen offset:2048 lds
Mv 232,136,184
Rt 22,23,2048
Ma 200,136,188
Ma 204,140,188
Rt 24,24,0
Mv 236,140,184
Mv 204,140,180
Rt 26,24,2048
Mv 172,140,176
Mv 140,140,172
Rt 28,25,0
Mv 108,140,168
buffer_load_dwordx4 v11, s[4:7], s38 offen offset:3072 lds
Mv 76,140,164
Rt 30,25,2048
Mv 44,140,160
Mv 48,144,160
Rb 32,16,0
Mv 80,144,164
Mv 112,144,168
Rb 36,16,2048
Mv 144,144,172
s_add_u32 s38, s38, 0x10000
Mv 176,144,176
Rb 40,16,8192
s_add_u32 m0, s36, 36864
Mv 208,144,180
buffer_load_dwordx4 v8, s[4:7], s38 offen offset:0 lds
Mv 240,144,184
Rb 44,16,10240
)" R"(
Ma 208,144,188
Ma 212,148,188
Rb 48,16,16384
Mv 244,148,184
Mv 212,148,180
Rb 52,16,18432
Mv 180,148,176
Mv 148,148,172
Rb 56,16,24576
Mv 116,148,168
buffer_load_dwordx4 v9, s[4:7], s38 offen offset:1024 lds
Mv 84,148,164
Rb 60,16,26624
Mv 52,148,160
Mv 56,152,160
Mv 88,152,164
Mv 120,152,168
Mv 152,152,172
Mv 184,152,176
Mv 216,152,180
buffer_load_dwordx4 v10, s[4:7], s38 offen offset:2048 lds
Mv 248,152,184
Ma 216,152,188
Ma 220,156,188
Mv 252,156,184
Mv 220,156,180
Mv 188,156,176
Mv 156,156,172
Mv 124,156,168
buffer_load_dwordx4 v11, s[4:7], s38 offen offset:3072 lds
Mv 92,156,164
Mv 60,156,160
s_waitcnt vmcnt(13) lgkmcnt(0)
s_barrier
.LzPBK:
Mv 32,0,32
Rt 128,18,16384
Mv 64,0,36
Mv 96,0,40
Rt 130,18,18432
Mv 128,0,44
s_add_u32 s39, s39, 0x40000
Mv 160,0,48
Rt 132,19,16384
s_add_u32 m0, s37, 49152
Mv 192,0,52
buffer_load_dwordx4 v12, s[8:11], s39 offen offset:0 lds
Mv 224,0,56
Rt 134,19,18432
Ma 192,0,60
Ma 196,4,60
Rt 136,20,16384
Mv 228,4,56
Mv 196,4,52
Rt 138,20,18432
Mv 164,4,48
Mv 132,4,44
Rt 140,21,16384
Mv 100,4,40
buffer_load_dwordx4 v13, s[8:11], s39 offen offset:1024 lds
Mv 68,4,36
Rt 142,21,18432
Mv 36,4,32
Mv 40,8,32
Rt 144,22,16384
)" R"(
Mv 72,8,36
Mv 104,8,40
Rt 146,22,18432
Mv 136,8,44
Mv 168,8,48
Rt 148,23,16384
Mv 200,8,52
buffer_load_dwordx4 v14, s[8:11], s39 offen offset:2048 lds
Mv 232,8,56
Rt 150,23,18432
Ma 200,8,60
Ma 204,12,60
Rt 152,24,16384
Mv 236,12,56
Mv 204,12,52
Rt 154,24,18432
Mv 172,12,48
Mv 140,12,44
Rt 156,25,16384
Mv 108,12,40
buffer_load_dwordx4 v15, s[8:11], s39 offen offset:3072 lds
Mv 76,12,36
Rt 158,25,18432
Mv 44,12,32
Mv 48,16,32
Rb 160,17,0
Mv 80,16,36
Mv 112,16,40
Rb 164,17,2048
Mv 144,16,44
s_add_u32 s39, s39, 0x40000
Mv 176,16,48
Rb 168,17,8192
s_add_u32 m0, s37, 0
Mv 208,16,52
buffer_load_dwordx4 v12, s[8:11], s39 offen offset:0 lds
Mv 240,16,56
Rb 172,17,10240
Ma 208,16,60
Ma 212,20,60
Rb 176,17,16384
Mv 244,20,56
Mv 212,20,52
Rb 180,17,18432
Mv 180,20,48
Mv 148,20,44
Rb 184,17,24576
Mv 116,20,40
buffer_load_dwordx4 v13, s[8:11], s39 offen offset:1024 lds
Mv 84,20,36
Rb 188,17,26624
Mv 52,20,32
Mv 56,24,32
Mv 88,24,36
Mv 120,24,40
Mv 152,24,44
Mv 184,24,48
Mv 216,24,52
buffer_load_dwordx4 v14, s[8:11], s39 offen offset:2048 lds
Mv 248,24,56
Ma 216,24,60
Ma 220,28,60
Mv 252,28,56
Mv 220,28,52
)" R"(
Mv 188,28,48
Mv 156,28,44
Mv 124,28,40
buffer_load_dwordx4 v15, s[8:11], s39 offen offset:3072 lds
Mv 92,28,36
Mv 60,28,32
s_waitcnt vmcnt(8) lgkmcnt(0)
s_barrier
Mv 32,128,160
Rt 0,18,32768
Mv 64,128,164
Mv 96,128,168
Rt 2,18,34816
Mv 128,128,172
s_add_u32 s38, s38, 0xffff0080
Mv 160,128,176
Rt 4,19,32768
s_add_u32 m0, s36, 0
Mv 192,128,180
buffer_load_dwordx4 v8, s[4:7], s38 offen offset:0 lds
Mv 224,128,184
Rt 6,19,34816
Ma 192,128,188
Ma 196,132,188
Rt 8,20,32768
Mv 228,132,184
Mv 196,132,180
Rt 10,20,34816
Mv 164,132,176
Mv 132,132,172
Rt 12,21,32768
Mv 100,132,168
buffer_load_dwordx4 v9, s[4:7], s38 offen offset:1024 lds
Mv 68,132,164
Rt 14,21,34816
Mv 36,132,160
Mv 40,136,160
Rt 16,22,32768
Mv 72,136,164
Mv 104,136,168
Rt 18,22,34816
Mv 136,136,172
Mv 168,136,176
Rt 20,23,32768
Mv 200,136,180
buffer_load_dwordx4 v10, s[4:7], s38 offen offset:2048 lds
Mv 232,136,184
Rt 22,23,34816
Ma 200,136,188
Ma 204,140,188
Rt 24,24,32768
Mv 236,140,184
Mv 204,140,180
Rt 26,24,34816
Mv 172,140,176
Mv 140,140,172
Rt 28,25,32768
Mv 108,140,168
buffer_load_dwordx4 v11, s[4:7], s38 offen offset:3072 lds
Mv 76,140,164
Rt 30,25,34816
Mv 44,140,160
Mv 48,144,160
Rb 32,16,32768
Mv 80,144,164
)" R"(
Mv 112,144,168
Rb 36,16,34816
Mv 144,144,172
s_add_u32 s38, s38, 0x10000
Mv 176,144,176
Rb 40,16,40960
s_add_u32 m0, s36, 4096
Mv 208,144,180
buffer_load_dwordx4 v8, s[4:7], s38 offen offset:0 lds
Mv 240,144,184
Rb 44,16,43008
Ma 208,144,188
Ma 212,148,188
Rb 48,16,49152
Mv 244,148,184
Mv 212,148,180
Rb 52,16,51200
Mv 180,148,176
Mv 148,148,172
Rb 56,16,57344
Mv 116,148,168
buffer_load_dwordx4 v9, s[4:7], s38 offen offset:1024 lds
Mv 84,148,164
Rb 60,16,59392
Mv 52,148,160
Mv 56,152,160
Mv 88,152,164
Mv 120,152,168
Mv 152,152,172
Mv 184,152,176
Mv 216,152,180
buffer_load_dwordx4 v10, s[4:7], s38 offen offset:2048 lds
Mv 248,152,184
Ma 216,152,188
Ma 220,156,188
Mv 252,156,184
Mv 220,156,180
Mv 188,156,176
Mv 156,156,172
Mv 124,156,168
buffer_load_dwordx4 v11, s[4:7], s38 offen offset:3072 lds
Mv 92,156,164
Mv 60,156,160
s_waitcnt vmcnt(12) lgkmcnt(0)
s_barrier
Mv 32,0,32
Rt 128,18,49152
Mv 64,0,36
Mv 96,0,40
Rt 130,18,51200
Mv 128,0,44
s_add_u32 s39, s39, 0x40000
Mv 160,0,48
Rt 132,19,49152
s_add_u32 m0, s37, 16384
Mv 192,0,52
buffer_load_dwordx4 v12, s[8:11], s39 offen offset:0 lds
Mv 224,0,56
Rt 134,19,51200
Ma 192,0,60
Ma 196,4,60
Rt 136,20,49152
Mv 228,4,56
Mv 196,4,52
Rt 138,20,51200
)" R"(
Mv 164,4,48
Mv 132,4,44
Rt 140,21,49152
Mv 100,4,40
buffer_load_dwordx4 v13, s[8:11], s39 offen offset:1024 lds
Mv 68,4,36
Rt 142,21,51200
Mv 36,4,32
Mv 40,8,32
Rt 144,22,49152
Mv 72,8,36
Mv 104,8,40
Rt 146,22,51200
Mv 136,8,44
Mv 168,8,48
Rt 148,23,49152
Mv 200,8,52
buffer_load_dwordx4 v14, s[8:11], s39 offen offset:2048 lds
Mv 232,8,56
Rt 150,23,51200
Ma 200,8,60
Ma 204,12,60
Rt 152,24,49152
Mv 236,12,56
Mv 204,12,52
Rt 154,24,51200
Mv 172,12,48
Mv 140,12,44
Rt 156,25,49152
Mv 108,12,40
buffer_load_dwordx4 v15, s[8:11], s39 offen offset:3072 lds
Mv 76,12,36
Rt 158,25,51200
Mv 44,12,32
Mv 48,16,32
Rb 160,17,32768
Mv 80,16,36
Mv 112,16,40
Rb 164,17,34816
Mv 144,16,44
s_add_u32 s39, s39, 0x40000
Mv 176,16,48
Rb 168,17,40960
s_add_u32 m0, s37, 32768
Mv 208,16,52
buffer_load_dwordx4 v12, s[8:11], s39 offen offset:0 lds
Mv 240,16,56
Rb 172,17,43008
Ma 208,16,60
Ma 212,20,60
Rb 176,17,49152
Mv 244,20,56
Mv 212,20,52
Rb 180,17,51200
Mv 180,20,48
Mv 148,20,44
Rb 184,17,57344
Mv 116,20,40
buffer_load_dwordx4 v13, s[8:11], s39 offen offset:1024 lds
Mv 84,20,36
Rb 188,17,59392
Mv 52,20,32
Mv 56,24,32
Mv 88,24,36
)" R"(
Mv 120,24,40
Mv 152,24,44
Mv 184,24,48
Mv 216,24,52
buffer_load_dwordx4 v14, s[8:11], s39 offen offset:2048 lds
Mv 248,24,56
Ma 216,24,60
Ma 220,28,60
Mv 252,28,56
Mv 220,28,52
Mv 188,28,48
Mv 156,28,44
Mv 124,28,40
buffer_load_dwordx4 v15, s[8:11], s39 offen offset:3072 lds
Mv 92,28,36
Mv 60,28,32
s_waitcnt vmcnt(8) lgkmcnt(0)
s_barrier
Mv 32,128,160
Rt 0,18,0
Mv 64,128,164
Mv 96,128,168
Rt 2,18,2048
Mv 128,128,172
s_add_u32 s38, s38, 0xffff0080
Mv 160,128,176
Rt 4,19,0
s_add_u32 m0, s36, 32768
Mv 192,128,180
buffer_load_dwordx4 v8, s[4:7], s38 offen offset:0 lds
Mv 224,128,184
Rt 6,19,2048
Ma 192,128,188
Ma 196,132,188
Rt 8,20,0
Mv 228,132,184
Mv 196,132,180
Rt 10,20,2048
Mv 164,132,176
Mv 132,132,172
Rt 12,21,0
Mv 100,132,168
buffer_load_dwordx4 v9, s[4:7], s38 offen offset:1024 lds
Mv 68,132,164
Rt 14,21,2048
Mv 36,132,160
Mv 40,136,160
Rt 16,22,0
Mv 72,136,164
Mv 104,136,168
Rt 18,22,2048
Mv 136,136,172
Mv 168,136,176
Rt 20,23,0
Mv 200,136,180
buffer_load_dwordx4 v10, s[4:7], s38 offen offset:2048 lds
Mv 232,136,184
Rt 22,23,2048
Ma 200,136,188
Ma 204,140,188
Rt 24,24,0
Mv 236,140,184
Mv 204,140,180
Rt 26,24,2048
Mv 172,140,176
)" R"(
Mv 140,140,172
Rt 28,25,0
Mv 108,140,168
buffer_load_dwordx4 v11, s[4:7], s38 offen offset:3072 lds
Mv 76,140,164
Rt 30,25,2048
Mv 44,140,160
Mv 48,144,160
Rb 32,16,0
Mv 80,144,164
Mv 112,144,168
Rb 36,16,2048
Mv 144,144,172
s_add_u32 s38, s38, 0x10000
Mv 176,144,176
Rb 40,16,8192
s_add_u32 m0, s36, 36864
Mv 208,144,180
buffer_load_dwordx4 v8, s[4:7], s38 offen offset:0 lds
Mv 240,144,184
Rb 44,16,10240
Ma 208,144,188
Ma 212,148,188
Rb 48,16,16384
Mv 244,148,184
Mv 212,148,180
Rb 52,16,18432
Mv 180,148,176
Mv 148,148,172
Rb 56,16,24576
Mv 116,148,168
buffer_load_dwordx4 v9, s[4:7], s38 offen offset:1024 lds
Mv 84,148,164
Rb 60,16,26624
Mv 52,148,160
Mv 56,152,160
Mv 88,152,164
Mv 120,152,168
Mv 152,152,172
Mv 184,152,176
Mv 216,152,180
buffer_load_dwordx4 v10, s[4:7], s38 offen offset:2048 lds
Mv 248,152,184
Ma 216,152,188
Ma 220,156,188
Mv 252,156,184
Mv 220,156,180
s_sub_u32 s40, s40, 1
Mv 188,156,176
Mv 156,156,172
Mv 124,156,168
buffer_load_dwordx4 v11, s[4:7], s38 offen offset:3072 lds
Mv 92,156,164
Mv 60,156,160
s_cmp_lg_u32 s40, 0
s_waitcnt vmcnt(12) lgkmcnt(0)
s_barrier
s_cbranch_scc1 .LzPBK
Mv 32,0,32
Rt 128,18,16384
Mv 64,0,36
s_mov_b32 s24, s80
s_mov_b32 s25, s81
Mv 96,0,40
Rt 130,18,18432
)" R"(
s_mov_b32 s26, 0x380000
Mv 128,0,44
s_add_u32 s39, s39, 0x40000
s_mov_b32 s27, 0x20000
Mv 160,0,48
Rt 132,19,16384
s_add_u32 m0, s37, 49152
Mv 192,0,52
buffer_load_dwordx4 v12, s[8:11], s39 offen offset:0 lds
Mv 224,0,56
Rt 134,19,18432
s_bfe_u32 s28, s2, 0x30005
Ma 192,0,60
s_lshl3_add_u32 s29, s34, s28
s_lshl2_add_u32 s29, s29, s33
Ma 196,4,60
Rt 136,20,16384
s_mul_i32 s83, s29, 7168
Mv 228,4,56
s_mul_i32 s29, s85, 448
s_add_u32 s83, s83, s29
Mv 196,4,52
Rt 138,20,18432
s_lshl1_add_u32 s29, s34, s85
Mv 164,4,48
s_lshl_b32 s82, s29, 17
s_add_u32 s82, s82, 0x5000000
Mv 132,4,44
Rt 140,21,16384
s_mul_i32 s29, s28, 14
Mv 100,4,40
buffer_load_dwordx4 v13, s[8:11], s39 offen offset:1024 lds
Mv 68,4,36
Rt 142,21,18432
s_add_u32 s29, s29, s33
Mv 36,4,32
s_lshl3_add_u32 s28, s33, s28
Mv 40,8,32
Rt 144,22,16384
Mv 72,8,36
s_cmp_eq_u32 s85, 0
s_cselect_b32 s87, s29, s28
Mv 104,8,40
Rt 146,22,18432
Mv 136,8,44
s_cmp_eq_u32 s85, 0
s_cselect_b32 s92, 4, 32
Mv 168,8,48
Rt 148,23,16384
v_lshrrev_b32 v4, 5, v0
Mv 200,8,52
buffer_load_dwordx4 v14, s[8:11], s39 offen offset:2048 lds
Mv 232,8,56
Rt 150,23,18432
v_mul_u32_u24 v4, 384, v4
Ma 200,8,60
v_add_u32 v28, v4, v1
v_and_b32 v4, 31, v0
Ma 204,12,60
Rt 152,24,16384
v_cmp_lt_u32_e32 vcc, 27, v4
Mv 236,12,56
v_mov_b32 v4, 0x40000000
v_lshrrev_b32 v29, 2, v0
)" R"(
Mv 204,12,52
Rt 154,24,18432
v_cndmask_b32_e32 v28, v28, v4, vcc
Mv 172,12,48
v_and_b32 v4, 48, v1
v_lshl_or_b32 v29, v29, 9, v4
Mv 140,12,44
Rt 156,25,16384
s_lshr_b32 s28, s33, 1
Mv 108,12,40
buffer_load_dwordx4 v15, s[8:11], s39 offen offset:3072 lds
Mv 76,12,36
Rt 158,25,18432
s_lshl_b32 s28, s28, 6
Mv 44,12,32
s_and_b32 s29, s33, 1
s_lshl3_add_u32 s28, s29, s28
Mv 48,16,32
Rb 160,17,0
s_add_u32 s28, s28, 0x20000
Mv 80,16,36
v_add_u32 v29, s28, v29
Mv 112,16,40
Rb 164,17,2048
Mv 144,16,44
s_add_u32 s39, s39, 0x40000
Mv 176,16,48
Rb 168,17,8192
s_add_u32 m0, s37, 0
Mv 208,16,52
buffer_load_dwordx4 v12, s[8:11], s39 offen offset:0 lds
Mv 240,16,56
Rb 172,17,10240
Ma 208,16,60
Ma 212,20,60
Rb 176,17,16384
Mv 244,20,56
Mv 212,20,52
Rb 180,17,18432
Mv 180,20,48
Mv 148,20,44
Rb 184,17,24576
Mv 116,20,40
buffer_load_dwordx4 v13, s[8:11], s39 offen offset:1024 lds
Mv 84,20,36
Rb 188,17,26624
Mv 52,20,32
Mv 56,24,32
Mv 88,24,36
Mv 120,24,40
Mv 152,24,44
Mv 184,24,48
Mv 216,24,52
buffer_load_dwordx4 v14, s[8:11], s39 offen offset:2048 lds
Mv 248,24,56
Ma 216,24,60
Ma 220,28,60
Mv 252,28,56
Mv 220,28,52
Mv 188,28,48
Mv 156,28,44
Mv 124,28,40
buffer_load_dwordx4 v15, s[8:11], s39 offen offset:3072 lds
Mv 92,28,36
)" R"(
s_add_u32 m0, s37, 0x10000
s_add_u32 s29, s83, 0
buffer_load_dwordx4 v28, s[24:27], s29 offen offset:0 lds
Mv 60,28,32
s_add_u32 s29, s83, 768
s_waitcnt vmcnt(9) lgkmcnt(0)
s_barrier
Mv 32,128,160
Rt 0,18,32768
buffer_load_dwordx4 v28, s[24:27], s29 offen offset:1024 lds
s_add_u32 s29, s83, 1536
Mv 64,128,164
buffer_load_dwordx4 v28, s[24:27], s29 offen offset:2048 lds
s_add_u32 s29, s83, 2304
buffer_load_dwordx4 v28, s[24:27], s29 offen offset:3072 lds
Mv 96,128,168
Rt 2,18,34816
Mv 128,128,172
s_add_u32 s38, s38, 0xffff0080
Mv 160,128,176
Rt 4,19,32768
s_add_u32 m0, s36, 0
Mv 192,128,180
buffer_load_dwordx4 v8, s[4:7], s38 offen offset:0 lds
Mv 224,128,184
Rt 6,19,34816
Ma 192,128,188
Ma 196,132,188
Rt 8,20,32768
Mv 228,132,184
Mv 196,132,180
Rt 10,20,34816
Mv 164,132,176
Mv 132,132,172
Rt 12,21,32768
Mv 100,132,168
buffer_load_dwordx4 v9, s[4:7], s38 offen offset:1024 lds
Mv 68,132,164
Rt 14,21,34816
Mv 36,132,160
Mv 40,136,160
Rt 16,22,32768
Mv 72,136,164
Mv 104,136,168
Rt 18,22,34816
Mv 136,136,172
Mv 168,136,176
Rt 20,23,32768
Mv 200,136,180
buffer_load_dwordx4 v10, s[4:7], s38 offen offset:2048 lds
Mv 232,136,184
Rt 22,23,34816
Ma 200,136,188
Ma 204,140,188
Rt 24,24,32768
Mv 236,140,184
Mv 204,140,180
Rt 26,24,34816
Mv 172,140,176
Mv 140,140,172
Rt 28,25,32768
Mv 108,140,168
buffer_load_dwordx4 v11, s[4:7], s38 offen offset:3072 lds
Mv 76,140,164
Rt 30,25,34816
)" R"(
Mv 44,140,160
Mv 48,144,160
Rb 32,16,32768
Mv 80,144,164
Mv 112,144,168
Rb 36,16,34816
Mv 144,144,172
s_add_u32 s38, s38, 0x10000
Mv 176,144,176
Rb 40,16,40960
s_add_u32 m0, s36, 4096
Mv 208,144,180
buffer_load_dwordx4 v8, s[4:7], s38 offen offset:0 lds
Mv 240,144,184
Rb 44,16,43008
Ma 208,144,188
Ma 212,148,188
Rb 48,16,49152
Mv 244,148,184
Mv 212,148,180
Rb 52,16,51200
Mv 180,148,176
Mv 148,148,172
Rb 56,16,57344
Mv 116,148,168
buffer_load_dwordx4 v9, s[4:7], s38 offen offset:1024 lds
Mv 84,148,164
Rb 60,16,59392
Mv 52,148,160
Mv 56,152,160
Mv 88,152,164
Mv 120,152,168
Mv 152,152,172
Mv 184,152,176
Mv 216,152,180
buffer_load_dwordx4 v10, s[4:7], s38 offen offset:2048 lds
Mv 248,152,184
Ma 216,152,188
Ma 220,156,188
Mv 252,156,184
Mv 220,156,180
Mv 188,156,176
Mv 156,156,172
Mv 124,156,168
buffer_load_dwordx4 v11, s[4:7], s38 offen offset:3072 lds
Mv 92,156,164
Mv 60,156,160
s_waitcnt vmcnt(8) lgkmcnt(0)
s_barrier
Mv 32,0,32
Rt 128,18,49152
Mv 64,0,36
Mv 96,0,40
Rt 130,18,51200
v_lshrrev_b32 v5, 2, v0
Mv 128,0,44
s_add_u32 s39, s39, 0x40000
v_and_b32 v5, 8, v5
Mv 160,0,48
Rt 132,19,49152
s_add_u32 m0, s37, 16384
Mv 192,0,52
buffer_load_dwordx4 v12, s[8:11], s39 offen offset:0 lds
Mv 224,0,56
Rt 134,19,51200
)" R"(
v_and_or_b32 v5, v0, 7, v5
Ma 192,0,60
v_lshlrev_b32 v5, 8, v5
v_and_or_b32 v5, v0, 24, v5
Ma 196,4,60
Rt 136,20,49152
v_lshrrev_b32 v5, 1, v5
Mv 228,4,56
Mv 196,4,52
Rt 138,20,51200
Mv 164,4,48
Mv 132,4,44
Rt 140,21,49152
Mv 100,4,40
buffer_load_dwordx4 v13, s[8:11], s39 offen offset:1024 lds
Mv 68,4,36
Rt 142,21,51200
Mv 36,4,32
Mv 40,8,32
Rt 144,22,49152
Mv 72,8,36
Mv 104,8,40
Rt 146,22,51200
Mv 136,8,44
Mv 168,8,48
Rt 148,23,49152
Mv 200,8,52
buffer_load_dwordx4 v14, s[8:11], s39 offen offset:2048 lds
Mv 232,8,56
Rt 150,23,51200
Ma 200,8,60
Ma 204,12,60
Rt 152,24,49152
Mv 236,12,56
Mv 204,12,52
Rt 154,24,51200
Mv 172,12,48
Mv 140,12,44
Rt 156,25,49152
Mv 108,12,40
buffer_load_dwordx4 v15, s[8:11], s39 offen offset:3072 lds
Mv 76,12,36
Rt 158,25,51200
Mv 44,12,32
Mv 48,16,32
Rb 160,17,32768
Mv 80,16,36
Mv 112,16,40
Rb 164,17,34816
Mv 144,16,44
s_add_u32 s39, s39, 0x40000
Mv 176,16,48
Rb 168,17,40960
s_add_u32 m0, s37, 32768
Mv 208,16,52
buffer_load_dwordx4 v12, s[8:11], s39 offen offset:0 lds
Mv 240,16,56
Rb 172,17,43008
Ma 208,16,60
Ma 212,20,60
Rb 176,17,49152
Mv 244,20,56
Mv 212,20,52
Rb 180,17,51200
)" R"(
Mv 180,20,48
Mv 148,20,44
Rb 184,17,57344
Mv 116,20,40
buffer_load_dwordx4 v13, s[8:11], s39 offen offset:1024 lds
Mv 84,20,36
Rb 188,17,59392
Mv 52,20,32
Mv 56,24,32
Rt 224,29,0
Mv 88,24,36
Rt 226,29,8192
Mv 120,24,40
Rt 228,29,128
Mv 152,24,44
Rt 230,29,8320
Mv 184,24,48
Rt 232,29,256
Mv 216,24,52
buffer_load_dwordx4 v14, s[8:11], s39 offen offset:2048 lds
Rt 234,29,8448
Mv 248,24,56
Rt 236,29,384
Ma 216,24,60
Rt 238,29,8576
Ma 220,28,60
Mv 252,28,56
Mv 220,28,52
Mv 188,28,48
Mv 156,28,44
Mv 124,28,40
buffer_load_dwordx4 v15, s[8:11], s39 offen offset:3072 lds
Mv 92,28,36
Mv 60,28,32
s_waitcnt vmcnt(8) lgkmcnt(0)
s_barrier
Mv 32,128,160
Rt 0,18,0
s_mul_i32 s29, s87, 147
Mv 64,128,164
s_lshr_b32 s29, s29, 10
s_add_u32 s29, s29, s87
Mv 96,128,168
Rt 2,18,2048
s_lshl_b32 s29, s29, 10
Mv 128,128,172
s_add_u32 s38, s38, 0xffff0080
s_add_u32 s29, s29, s82
Mv 160,128,176
Rt 4,19,0
s_add_u32 m0, s36, 32768
Mv 192,128,180
buffer_load_dwordx4 v8, s[4:7], s38 offen offset:0 lds
Mv 224,128,184
Rt 6,19,2048
s_add_u32 s87, s87, s92
Ma 192,128,188
Ma 196,132,188
Rt 8,20,0
Mv 228,132,184
buffer_store_dwordx4 a[224:227], v1, s[20:23], s29 offen sc0 sc1
Mv 196,132,180
Rt 10,20,2048
Mv 164,132,176
Mv 132,132,172
)" R"(
Rt 12,21,0
Mv 100,132,168
buffer_load_dwordx4 v9, s[4:7], s38 offen offset:1024 lds
Mv 68,132,164
Rt 14,21,2048
Mv 36,132,160
Mv 40,136,160
Rt 16,22,0
s_mul_i32 s29, s87, 147
Mv 72,136,164
s_lshr_b32 s29, s29, 10
s_add_u32 s29, s29, s87
Mv 104,136,168
Rt 18,22,2048
s_lshl_b32 s29, s29, 10
Mv 136,136,172
s_add_u32 s29, s29, s82
s_add_u32 s87, s87, s92
Mv 168,136,176
Rt 20,23,0
Mv 200,136,180
buffer_load_dwordx4 v10, s[4:7], s38 offen offset:2048 lds
Mv 232,136,184
Rt 22,23,2048
Ma 200,136,188
Ma 204,140,188
Rt 24,24,0
Mv 236,140,184
buffer_store_dwordx4 a[228:231], v1, s[20:23], s29 offen sc0 sc1
Mv 204,140,180
Rt 26,24,2048
Mv 172,140,176
Mv 140,140,172
Rt 28,25,0
Mv 108,140,168
buffer_load_dwordx4 v11, s[4:7], s38 offen offset:3072 lds
Mv 76,140,164
Rt 30,25,2048
Mv 44,140,160
Mv 48,144,160
Rb 32,16,0
s_mul_i32 s29, s87, 147
Mv 80,144,164
s_lshr_b32 s29, s29, 10
s_add_u32 s29, s29, s87
Mv 112,144,168
Rb 36,16,2048
s_lshl_b32 s29, s29, 10
Mv 144,144,172
s_add_u32 s38, s38, 0x10000
s_add_u32 s29, s29, s82
Mv 176,144,176
Rb 40,16,8192
s_add_u32 m0, s36, 36864
Mv 208,144,180
buffer_load_dwordx4 v8, s[4:7], s38 offen offset:0 lds
Mv 240,144,184
Rb 44,16,10240
s_add_u32 s87, s87, s92
Ma 208,144,188
Ma 212,148,188
Rb 48,16,16384
Mv 244,148,184
buffer_store_dwordx4 a[232:235], v1, s[20:23], s29 offen sc0 sc1
)" R"(
Mv 212,148,180
Rb 52,16,18432
Mv 180,148,176
Mv 148,148,172
Rb 56,16,24576
Mv 116,148,168
buffer_load_dwordx4 v9, s[4:7], s38 offen offset:1024 lds
Mv 84,148,164
Rb 60,16,26624
Mv 52,148,160
Mv 56,152,160
s_mul_i32 s29, s87, 147
s_lshr_b32 s29, s29, 10
Mv 88,152,164
s_add_u32 s29, s29, s87
s_lshl_b32 s29, s29, 10
Mv 120,152,168
s_add_u32 s29, s29, s82
Mv 152,152,172
s_cmp_gt_u32 s33, 1
s_cmov_b32 s29, 0x40000000
Mv 184,152,176
Mv 216,152,180
buffer_load_dwordx4 v10, s[4:7], s38 offen offset:2048 lds
Mv 248,152,184
Ma 216,152,188
Ma 220,156,188
Mv 252,156,184
buffer_store_dwordx4 a[236:239], v1, s[20:23], s29 offen sc0 sc1
Mv 220,156,180
Mv 188,156,176
Mv 156,156,172
Mv 124,156,168
buffer_load_dwordx4 v11, s[4:7], s38 offen offset:3072 lds
Mv 92,156,164
Mv 60,156,160
s_waitcnt vmcnt(16) lgkmcnt(0)
s_barrier
Mv 32,0,32
Rt 128,18,16384
Mv 64,0,36
Mv 96,0,40
Rt 130,18,18432
Mv 128,0,44
s_add_u32 s39, s39, 0x40000
Mv 160,0,48
Rt 132,19,16384
s_add_u32 m0, s37, 49152
Mv 192,0,52
buffer_load_dwordx4 v12, s[8:11], s39 offen offset:0 lds
Mv 224,0,56
Rt 134,19,18432
Ma 192,0,60
Ma 196,4,60
Rt 136,20,16384
Mv 228,4,56
Mv 196,4,52
Rt 138,20,18432
Mv 164,4,48
Mv 132,4,44
Rt 140,21,16384
Mv 100,4,40
buffer_load_dwordx4 v13, s[8:11], s39 offen offset:1024 lds
Mv 68,4,36
Rt 142,21,18432
)" R"(
Mv 36,4,32
Mv 40,8,32
Rt 144,22,16384
Mv 72,8,36
Mv 104,8,40
Rt 146,22,18432
Mv 136,8,44
Mv 168,8,48
Rt 148,23,16384
Mv 200,8,52
buffer_load_dwordx4 v14, s[8:11], s39 offen offset:2048 lds
Mv 232,8,56
Rt 150,23,18432
Ma 200,8,60
Ma 204,12,60
Rt 152,24,16384
Mv 236,12,56
Mv 204,12,52
Rt 154,24,18432
Mv 172,12,48
Mv 140,12,44
Rt 156,25,16384
Mv 108,12,40
buffer_load_dwordx4 v15, s[8:11], s39 offen offset:3072 lds
Mv 76,12,36
Rt 158,25,18432
Mv 44,12,32
Mv 48,16,32
Rb 160,17,0
Mv 80,16,36
buffer_load_dword v4, v5, s[20:23], s50 offen offset:0 sc0 sc1
Mv 112,16,40
Rb 164,17,2048
Mv 144,16,44
Mv 176,16,48
Rb 168,17,8192
Mv 208,16,52
Mv 240,16,56
Rb 172,17,10240
Ma 208,16,60
Ma 212,20,60
Rb 176,17,16384
Mv 244,20,56
Mv 212,20,52
Rb 180,17,18432
Mv 180,20,48
Mv 148,20,44
Rb 184,17,24576
Mv 116,20,40
Mv 84,20,36
Rb 188,17,26624
Mv 52,20,32
Mv 56,24,32
Mv 88,24,36
Mv 120,24,40
Mv 152,24,44
Mv 184,24,48
Mv 216,24,52
Mv 248,24,56
Ma 216,24,60
Ma 220,28,60
Mv 252,28,56
Mv 220,28,52
Mv 188,28,48
)" R"(
Mv 156,28,44
Mv 124,28,40
Mv 92,28,36
Mv 60,28,32
s_waitcnt vmcnt(0) lgkmcnt(0)
s_barrier
Mv 32,128,160
Rt 0,18,32768
v_cmp_eq_u32_e32 vcc, s3, v4
s_andn2_b64 s[28:29], s[60:61], vcc
s_cbranch_scc1 .Lzsp9
.Lzok9:
Mv 64,128,164
Mv 96,128,168
Rt 2,18,34816
Mv 128,128,172
Mv 160,128,176
Rt 4,19,32768
s_add_u32 m0, s36, 0
Mv 192,128,180
buffer_load_dwordx4 v2, s[20:23], s55 offen offset:0 sc1 lds
Mv 224,128,184
Rt 6,19,34816
Ma 192,128,188
Ma 196,132,188
Rt 8,20,32768
Mv 228,132,184
Mv 196,132,180
Rt 10,20,34816
Mv 164,132,176
buffer_load_dwordx4 v2, s[20:23], s55 offen offset:1024 sc1 lds
Mv 132,132,172
Rt 12,21,32768
Mv 100,132,168
Mv 68,132,164
Rt 14,21,34816
Mv 36,132,160
Mv 40,136,160
Rt 16,22,32768
Mv 72,136,164
buffer_load_dwordx4 v2, s[20:23], s55 offen offset:2048 sc1 lds
Mv 104,136,168
Rt 18,22,34816
Mv 136,136,172
Mv 168,136,176
Rt 20,23,32768
Mv 200,136,180
Mv 232,136,184
Rt 22,23,34816
Ma 200,136,188
buffer_load_dwordx4 v2, s[20:23], s55 offen offset:3072 sc1 lds
Ma 204,140,188
Rt 24,24,32768
Mv 236,140,184
Mv 204,140,180
Rt 26,24,34816
Mv 172,140,176
Mv 140,140,172
Rt 28,25,32768
s_add_u32 m0, s36, 4096
Mv 108,140,168
buffer_load_dwordx4 v3, s[20:23], s55 offen offset:0 sc1 lds
Mv 76,140,164
Rt 30,25,34816
Mv 44,140,160
)" R"(
Mv 48,144,160
Rb 32,16,32768
Mv 80,144,164
Mv 112,144,168
Rb 36,16,34816
Mv 144,144,172
buffer_load_dwordx4 v3, s[20:23], s55 offen offset:1024 sc1 lds
Mv 176,144,176
Rb 40,16,40960
Mv 208,144,180
Mv 240,144,184
Rb 44,16,43008
Ma 208,144,188
Ma 212,148,188
Rb 48,16,49152
Mv 244,148,184
buffer_load_dwordx4 v3, s[20:23], s55 offen offset:2048 sc1 lds
Mv 212,148,180
Rb 52,16,51200
Mv 180,148,176
Mv 148,148,172
Rb 56,16,57344
Mv 116,148,168
Mv 84,148,164
Rb 60,16,59392
Mv 52,148,160
buffer_load_dwordx4 v3, s[20:23], s55 offen offset:3072 sc1 lds
Mv 56,152,160
Mv 88,152,164
Mv 120,152,168
Mv 152,152,172
Mv 184,152,176
Mv 216,152,180
Mv 248,152,184
Ma 216,152,188
Ma 220,156,188
s_add_u32 s55, s55, 0x8000
Mv 252,156,184
Mv 220,156,180
Mv 188,156,176
Mv 156,156,172
Mv 124,156,168
Mv 92,156,164
Mv 60,156,160
s_waitcnt lgkmcnt(4)
s_barrier
Mv 32,0,32
Rt 128,18,49152
Mv 64,0,36
Rt 130,18,51200
Mv 96,0,40
Rt 132,19,49152
Mv 128,0,44
Rt 134,19,51200
Mv 132,4,44
Rt 136,20,49152
Mv 100,4,40
Rt 138,20,51200
Mv 68,4,36
Rt 140,21,49152
Mv 36,4,32
Rt 142,21,51200
Mv 40,8,32
Rt 144,22,49152
Mv 72,8,36
)" R"(
Rt 146,22,51200
Mv 104,8,40
Rt 148,23,49152
Mv 136,8,44
Rt 150,23,51200
Mv 140,12,44
Rt 152,24,49152
Mv 108,12,40
Rt 154,24,51200
Mv 76,12,36
Rt 156,25,49152
Mv 44,12,32
Rt 158,25,51200
Mv 48,16,32
Rb 160,17,32768
Mv 80,16,36
Rb 164,17,34816
Mv 112,16,40
Rb 168,17,40960
Mv 144,16,44
Rb 172,17,43008
Mv 148,20,44
Rb 176,17,49152
Mv 116,20,40
Rb 180,17,51200
Mv 84,20,36
Rb 184,17,57344
Mv 52,20,32
Rb 188,17,59392
Mv 56,24,32
v_add_u32 v8, 0x10000, v1
v_add_u32 v10, 0x20000, v1
Mv 88,24,36
v_lshrrev_b32 v9, 4, v0
v_mul_i32_i24 v9, -20, v9
Mv 120,24,40
v_add_u32 v9, v9, v0
s_lshl_b32 s28, s33, 5
Mv 152,24,44
v_add_u32 v9, s28, v9
s_mov_b32 s93, 2
Mv 156,28,44
s_mov_b32 s94, 0
Mv 124,28,40
Mv 92,28,36
Mv 60,28,32
s_waitcnt lgkmcnt(0)
s_barrier
Mv 32,128,160
Mv 36,132,160
Mv 40,136,160
s_add_u32 m0, s36, 32768
Mv 44,140,160
buffer_load_dwordx4 v2, s[20:23], s55 offen offset:0 sc1 lds
Mv 48,144,160
Mv 52,148,160
s_nop 0
Cv 32,32,33
Cv 33,34,35
Mv 56,152,160
Cv 34,36,37
Cv 35,38,39
Mv 60,156,160
buffer_load_dwordx4 v2, s[20:23], s55 offen offset:1024 sc1 lds
Mv 64,128,164
)" R"(
Cv 40,40,41
Cv 41,42,43
Mv 68,132,164
Cv 42,44,45
Cv 43,46,47
Mv 72,136,164
Cv 48,48,49
Cv 49,50,51
Mv 76,140,164
buffer_load_dwordx4 v2, s[20:23], s55 offen offset:2048 sc1 lds
Mv 80,144,164
Cv 50,52,53
Cv 51,54,55
Mv 84,148,164
Cv 56,56,57
Cv 57,58,59
Mv 88,152,164
Cv 58,60,61
Cv 59,62,63
Mv 92,156,164
buffer_load_dwordx4 v2, s[20:23], s55 offen offset:3072 sc1 lds
Mv 96,128,168
Cv 64,64,65
Cv 65,66,67
Mv 100,132,168
Cv 66,68,69
Cv 67,70,71
Mv 104,136,168
s_add_u32 m0, s36, 36864
Cv 72,72,73
Mv 108,140,168
buffer_load_dwordx4 v3, s[20:23], s55 offen offset:0 sc1 lds
Mv 112,144,168
Cv 73,74,75
Cv 74,76,77
Mv 116,148,168
Cv 75,78,79
Cv 80,80,81
Mv 120,152,168
Cv 81,82,83
Cv 82,84,85
Mv 124,156,168
buffer_store_dwordx4 v[32:35], v2, s[20:23], s45 offen offset:0 sc0 sc1
Mv 128,128,172
Cv 83,86,87
Cv 88,88,89
Mv 132,132,172
Cv 89,90,91
Cv 90,92,93
Mv 136,136,172
Cv 91,94,95
Mv 140,140,172
buffer_load_dwordx4 v3, s[20:23], s55 offen offset:1024 sc1 lds
Mv 144,144,172
Mv 148,148,172
Cv 128,128,129
Cv 129,130,131
Mv 152,152,172
Cv 130,132,133
Cv 131,134,135
Mv 156,156,172
buffer_store_dwordx4 v[40:43], v2, s[20:23], s45 offen offset:1024 sc0 sc1
Mv 160,0,48
Cv 136,136,137
)" R"(
Cv 137,138,139
Mv 160,128,176
Cv 138,140,141
Cv 139,142,143
Mv 164,4,48
Cv 144,144,145
Cv 145,146,147
Mv 164,132,176
buffer_load_dwordx4 v3, s[20:23], s55 offen offset:2048 sc1 lds
Mv 168,8,48
Cv 146,148,149
Cv 147,150,151
Mv 168,136,176
Cv 152,152,153
Cv 153,154,155
Mv 172,12,48
Cv 154,156,157
Cv 155,158,159
Mv 172,140,176
buffer_store_dwordx4 v[48:51], v2, s[20:23], s45 offen offset:2048 sc0 sc1
Mv 176,16,48
Cv 132,96,97
Cv 133,98,99
Mv 176,144,176
Cv 134,100,101
Cv 135,102,103
Mv 180,20,48
Cv 140,104,105
Cv 141,106,107
Mv 180,148,176
buffer_load_dwordx4 v3, s[20:23], s55 offen offset:3072 sc1 lds
Mv 184,24,48
Cv 142,108,109
Cv 143,110,111
Mv 184,152,176
Cv 148,112,113
Cv 149,114,115
Mv 188,28,48
Cv 150,116,117
Cv 151,118,119
Mv 188,156,176
buffer_store_dwordx4 v[56:59], v2, s[20:23], s45 offen offset:3072 sc0 sc1
Mv 192,0,52
Cv 156,120,121
Cv 157,122,123
Mv 192,128,180
Cv 158,124,125
Cv 159,126,127
Mv 196,4,52
s_add_u32 m0, s36, 98304
Cv 160,160,161
Mv 196,132,180
buffer_load_dwordx4 v2, s[20:23], s56 offen offset:0 sc1 lds
Mv 200,8,52
Cv 161,162,163
Cv 162,164,165
Mv 200,136,180
Cv 163,166,167
v_accvgpr_write_b32 a224, v160
Mv 204,12,52
v_accvgpr_write_b32 a225, v161
v_accvgpr_write_b32 a226, v162
Mv 204,140,180
buffer_store_dwordx4 v[64:67], v3, s[20:23], s45 offen offset:0 sc0 sc1
)" R"(
Mv 208,16,52
v_accvgpr_write_b32 a227, v163
Cv 168,168,169
Mv 208,144,180
Cv 169,170,171
Cv 170,172,173
Mv 212,20,52
Cv 171,174,175
v_accvgpr_write_b32 a228, v168
Mv 212,148,180
buffer_load_dwordx4 v2, s[20:23], s56 offen offset:1024 sc1 lds
Mv 216,24,52
v_accvgpr_write_b32 a229, v169
v_accvgpr_write_b32 a230, v170
Mv 216,152,180
v_accvgpr_write_b32 a231, v171
Cv 176,176,177
Mv 220,28,52
Cv 177,178,179
Cv 178,180,181
Mv 220,156,180
buffer_store_dwordx4 v[72:75], v3, s[20:23], s45 offen offset:1024 sc0 sc1
Mv 224,0,56
Cv 179,182,183
v_accvgpr_write_b32 a232, v176
Ma 192,0,60
v_accvgpr_write_b32 a233, v177
v_accvgpr_write_b32 a234, v178
Ma 196,4,60
v_accvgpr_write_b32 a235, v179
Cv 184,184,185
Mv 228,4,56
buffer_load_dwordx4 v2, s[20:23], s56 offen offset:2048 sc1 lds
Mv 232,8,56
Cv 185,186,187
Cv 186,188,189
Ma 200,8,60
Cv 187,190,191
v_accvgpr_write_b32 a236, v184
Ma 204,12,60
v_accvgpr_write_b32 a237, v185
v_accvgpr_write_b32 a238, v186
Mv 236,12,56
s_waitcnt vmcnt(17)
s_barrier
Mv 240,16,56
buffer_store_dwordx4 v[80:83], v3, s[20:23], s45 offen offset:2048 sc0 sc1
Ma 208,16,60
v_accvgpr_write_b32 a239, v187
Ma 212,20,60
Mv 244,20,56
buffer_load_dwordx4 v2, s[20:23], s56 offen offset:3072 sc1 lds
Mv 248,24,56
Ma 216,24,60
Ma 220,28,60
Mv 252,28,56
buffer_store_dwordx4 v[88:91], v3, s[20:23], s45 offen offset:3072 sc0 sc1
Mv 224,128,184
Rb 0,1,0
Mv 228,132,184
Rb 4,1,1024
Mv 232,136,184
s_add_u32 m0, s36, 102400
Rb 8,1,2048
Mv 236,140,184
)" R"(
buffer_load_dwordx4 v3, s[20:23], s56 offen offset:0 sc1 lds
Rb 12,1,3072
Mv 240,144,184
Rb 16,1,8192
Mv 244,148,184
Rb 20,1,9216
Mv 248,152,184
Rb 24,1,10240
Mv 252,156,184
Rb 28,1,11264
Ma 192,128,188
Rb 32,1,4096
Ma 196,132,188
Rb 36,1,5120
Ma 200,136,188
Rb 40,1,6144
Ma 204,140,188
buffer_load_dwordx4 v3, s[20:23], s56 offen offset:1024 sc1 lds
Rb 44,1,7168
Ma 208,144,188
Ma 212,148,188
buffer_load_dwordx4 v3, s[20:23], s56 offen offset:2048 sc1 lds
Ma 216,152,188
Ma 220,156,188
buffer_load_dwordx4 v3, s[20:23], s56 offen offset:3072 sc1 lds
s_add_u32 s56, s56, 0x8000
s_branch .LzFB
.LzBV:
Mz 32,32,0
Rt 128,18,16384
v_mul_f32_e32 v64, s54, v64
Mz 36,36,0
v_mul_f32_e32 v65, s54, v65
Cv 64,64,65
Mz 96,40,0
Rt 130,18,18432
v_accvgpr_write_b32 a112, v64
Mz 100,44,0
s_add_u32 s39, s39, 0x40000
v_mul_f32_e32 v66, s54, v66
Mz 160,48,0
Rt 132,19,16384
s_add_u32 m0, s37, 49152
Mz 164,52,0
buffer_load_dwordx4 v12, s[8:11], s39 offen offset:0 lds
Mz 224,56,0
Rt 134,19,18432
v_mul_f32_e32 v67, s54, v67
Mz 228,60,0
Cv 66,66,67
v_accvgpr_write_b32 a113, v66
Mz 236,60,4
Rt 136,20,16384
v_mul_f32_e32 v68, s54, v68
Mz 232,56,4
v_mul_f32_e32 v69, s54, v69
Cv 68,68,69
buffer_store_dwordx4 a[64:67], v2, s[20:23], s51 offen offset:0 sc0 sc1
Mz 172,52,4
Rt 138,20,18432
v_accvgpr_write_b32 a114, v68
Mz 168,48,4
v_mul_f32_e32 v70, s54, v70
v_mul_f32_e32 v71, s54, v71
)" R"(
Mz 108,44,4
Rt 140,21,16384
Cv 70,70,71
Mz 104,40,4
buffer_load_dwordx4 v13, s[8:11], s39 offen offset:1024 lds
Mz 44,36,4
Rt 142,21,18432
v_accvgpr_write_b32 a115, v70
Mz 40,32,4
v_mul_f32_e32 v72, s54, v72
v_mul_f32_e32 v73, s54, v73
Mz 48,32,8
Rt 144,22,16384
Cv 72,72,73
Mz 52,36,8
v_accvgpr_write_b32 a116, v72
v_mul_f32_e32 v74, s54, v74
buffer_store_dwordx4 a[68:71], v2, s[20:23], s51 offen offset:1024 sc0 sc1
Mz 112,40,8
Rt 146,22,18432
v_mul_f32_e32 v75, s54, v75
Mz 116,44,8
Cv 74,74,75
v_accvgpr_write_b32 a117, v74
Mz 176,48,8
Rt 148,23,16384
v_mul_f32_e32 v76, s54, v76
Mz 180,52,8
buffer_load_dwordx4 v14, s[8:11], s39 offen offset:2048 lds
Mz 240,56,8
Rt 150,23,18432
v_mul_f32_e32 v77, s54, v77
Mz 244,60,8
Cv 76,76,77
v_accvgpr_write_b32 a118, v76
Mz 252,60,12
Rt 152,24,16384
v_mul_f32_e32 v78, s54, v78
Mz 248,56,12
v_mul_f32_e32 v79, s54, v79
Cv 78,78,79
buffer_store_dwordx4 a[72:75], v2, s[20:23], s51 offen offset:2048 sc0 sc1
Mz 188,52,12
Rt 154,24,18432
v_accvgpr_write_b32 a119, v78
Mz 184,48,12
v_mul_f32_e32 v80, s54, v80
v_mul_f32_e32 v81, s54, v81
Mz 124,44,12
Rt 156,25,16384
Cv 80,80,81
Mz 120,40,12
buffer_load_dwordx4 v15, s[8:11], s39 offen offset:3072 lds
Mz 60,36,12
Rt 158,25,18432
v_accvgpr_write_b32 a120, v80
Mz 56,32,12
v_mul_f32_e32 v82, s54, v82
v_mul_f32_e32 v83, s54, v83
Mz 128,40,16
Rb 160,17,0
Cv 82,82,83
Mz 132,44,16
v_accvgpr_write_b32 a121, v82
)" R"(
v_mul_f32_e32 v84, s54, v84
buffer_store_dwordx4 a[76:79], v2, s[20:23], s51 offen offset:3072 sc0 sc1
Mz 192,48,16
Rb 164,17,2048
v_mul_f32_e32 v85, s54, v85
Mz 196,52,16
s_add_u32 s39, s39, 0x40000
Cv 84,84,85
v_mfma_f32_16x16x32_bf16 a[192:195], a[56:59], a[16:19], 0
Rb 168,17,8192
s_add_u32 m0, s37, 0
v_mfma_f32_16x16x32_bf16 a[196:199], a[60:63], a[16:19], 0
buffer_load_dwordx4 v12, s[8:11], s39 offen offset:0 lds
v_mfma_f32_16x16x32_bf16 a[204:207], a[60:63], a[20:23], 0
Rb 172,17,10240
v_accvgpr_write_b32 a122, v84
v_mfma_f32_16x16x32_bf16 a[200:203], a[56:59], a[20:23], 0
v_mul_f32_e32 v86, s54, v86
v_mul_f32_e32 v87, s54, v87
Mz 204,52,20
Rb 176,17,16384
Cv 86,86,87
Mz 200,48,20
v_accvgpr_write_b32 a123, v86
v_mul_f32_e32 v88, s54, v88
buffer_store_dwordx4 a[80:83], v3, s[20:23], s51 offen offset:0 sc0 sc1
Mz 140,44,20
Rb 180,17,18432
v_mul_f32_e32 v89, s54, v89
Mz 136,40,20
Cv 88,88,89
v_accvgpr_write_b32 a124, v88
Mz 144,40,24
Rb 184,17,24576
v_mul_f32_e32 v90, s54, v90
Mz 148,44,24
buffer_load_dwordx4 v13, s[8:11], s39 offen offset:1024 lds
Mz 208,48,24
Rb 188,17,26624
v_mul_f32_e32 v91, s54, v91
Mz 212,52,24
Cv 90,90,91
v_accvgpr_write_b32 a125, v90
v_mfma_f32_16x16x32_bf16 a[208:211], a[56:59], a[24:27], 0
v_mul_f32_e32 v92, s54, v92
v_mul_f32_e32 v93, s54, v93
v_mfma_f32_16x16x32_bf16 a[212:215], a[60:63], a[24:27], 0
Cv 92,92,93
v_accvgpr_write_b32 a126, v92
buffer_store_dwordx4 a[84:87], v3, s[20:23], s51 offen offset:1024 sc0 sc1
v_mfma_f32_16x16x32_bf16 a[220:223], a[60:63], a[28:31], 0
v_mul_f32_e32 v94, s54, v94
v_mul_f32_e32 v95, s54, v95
v_mfma_f32_16x16x32_bf16 a[216:219], a[56:59], a[28:31], 0
Cv 94,94,95
v_accvgpr_write_b32 a127, v94
Mz 220,52,28
Mz 216,48,28
buffer_load_dwordx4 v14, s[8:11], s39 offen offset:2048 lds
Mz 156,44,28
Mz 152,40,28
Mz 64,32,16
Mz 68,36,16
buffer_store_dwordx4 a[88:91], v3, s[20:23], s51 offen offset:2048 sc0 sc1
)" R"(
Mz 72,32,20
Mz 76,36,20
Mz 80,32,24
Mz 84,36,24
buffer_load_dwordx4 v15, s[8:11], s39 offen offset:3072 lds
Mz 88,32,28
Mz 92,36,28
s_waitcnt vmcnt(15) lgkmcnt(0)
s_barrier
Mv 32,160,128
Rt 0,18,32768
Mv 36,164,128
buffer_store_dwordx4 a[92:95], v3, s[20:23], s51 offen offset:3072 sc0 sc1
Mv 96,168,128
Rt 2,18,34816
Mv 100,172,128
s_add_u32 s38, s38, 0xffff0080
Mv 160,176,128
Rt 4,19,32768
s_add_u32 m0, s36, 0
Mv 164,180,128
buffer_load_dwordx4 v8, s[4:7], s38 offen offset:0 lds
Mv 224,184,128
Rt 6,19,34816
Mv 228,188,128
Mv 236,188,132
Rt 8,20,32768
Mv 232,184,132
s_add_u32 s51, s51, 0x8000
buffer_store_dwordx4 a[96:99], v2, s[20:23], s51 offen offset:0 sc0 sc1
Mv 172,180,132
Rt 10,20,34816
Mv 168,176,132
Mv 108,172,132
Rt 12,21,32768
Mv 104,168,132
buffer_load_dwordx4 v9, s[4:7], s38 offen offset:1024 lds
Mv 44,164,132
Rt 14,21,34816
Mv 40,160,132
Mv 48,160,136
Rt 16,22,32768
Mv 52,164,136
buffer_store_dwordx4 a[100:103], v2, s[20:23], s51 offen offset:1024 sc0 sc1
Mv 112,168,136
Rt 18,22,34816
Mv 116,172,136
Mv 176,176,136
Rt 20,23,32768
Mv 180,180,136
buffer_load_dwordx4 v10, s[4:7], s38 offen offset:2048 lds
Mv 240,184,136
Rt 22,23,34816
Mv 244,188,136
Mv 252,188,140
Rt 24,24,32768
Mv 248,184,140
buffer_store_dwordx4 a[104:107], v2, s[20:23], s51 offen offset:2048 sc0 sc1
Mv 188,180,140
Rt 26,24,34816
Mv 184,176,140
Mv 124,172,140
Rt 28,25,32768
Mv 120,168,140
buffer_load_dwordx4 v11, s[4:7], s38 offen offset:3072 lds
)" R"(
Mv 60,164,140
Rt 30,25,34816
Mv 56,160,140
Mv 64,160,144
Rb 32,16,32768
Mv 68,164,144
buffer_store_dwordx4 a[108:111], v2, s[20:23], s51 offen offset:3072 sc0 sc1
Mv 128,168,144
Rb 36,16,34816
Mv 132,172,144
s_add_u32 s38, s38, 0x10000
Mv 192,176,144
Rb 40,16,40960
s_add_u32 m0, s36, 4096
Mv 196,180,144
buffer_load_dwordx4 v8, s[4:7], s38 offen offset:0 lds
Ma 192,184,144
Rb 44,16,43008
Ma 196,188,144
Ma 204,188,148
Rb 48,16,49152
Ma 200,184,148
buffer_store_dwordx4 a[112:115], v3, s[20:23], s51 offen offset:0 sc0 sc1
Mv 204,180,148
Rb 52,16,51200
Mv 200,176,148
Mv 140,172,148
Rb 56,16,57344
Mv 136,168,148
buffer_load_dwordx4 v9, s[4:7], s38 offen offset:1024 lds
Mv 76,164,148
Rb 60,16,59392
Mv 72,160,148
Mv 80,160,152
Mv 84,164,152
buffer_store_dwordx4 a[116:119], v3, s[20:23], s51 offen offset:1024 sc0 sc1
Mv 144,168,152
Mv 148,172,152
Mv 208,176,152
Mv 212,180,152
buffer_load_dwordx4 v10, s[4:7], s38 offen offset:2048 lds
Ma 208,184,152
Ma 212,188,152
Ma 220,188,156
Ma 216,184,156
buffer_store_dwordx4 a[120:123], v3, s[20:23], s51 offen offset:2048 sc0 sc1
Mv 220,180,156
Mv 216,176,156
Mv 156,172,156
Mv 152,168,156
buffer_load_dwordx4 v11, s[4:7], s38 offen offset:3072 lds
Mv 92,164,156
Mv 88,160,156
s_waitcnt vmcnt(24) lgkmcnt(0)
s_barrier
Mv 32,32,0
Rt 128,18,49152
Mv 36,36,0
buffer_store_dwordx4 a[124:127], v3, s[20:23], s51 offen offset:3072 sc0 sc1
Mv 96,40,0
Rt 130,18,51200
Mv 100,44,0
s_add_u32 s39, s39, 0x40000
Mv 160,48,0
Rt 132,19,49152
)" R"(
s_add_u32 m0, s37, 16384
Mv 164,52,0
buffer_load_dwordx4 v12, s[8:11], s39 offen offset:0 lds
Mv 224,56,0
Rt 134,19,51200
Mv 228,60,0
Mv 236,60,4
Rt 136,20,49152
Mv 232,56,4
Mv 172,52,4
Rt 138,20,51200
Mv 168,48,4
Mv 108,44,4
Rt 140,21,49152
Mv 104,40,4
buffer_load_dwordx4 v13, s[8:11], s39 offen offset:1024 lds
Mv 44,36,4
Rt 142,21,51200
Mv 40,32,4
Mv 48,32,8
Rt 144,22,49152
Mv 52,36,8
Mv 112,40,8
Rt 146,22,51200
Mv 116,44,8
Mv 176,48,8
Rt 148,23,49152
Mv 180,52,8
buffer_load_dwordx4 v14, s[8:11], s39 offen offset:2048 lds
Mv 240,56,8
Rt 150,23,51200
Mv 244,60,8
Mv 252,60,12
Rt 152,24,49152
Mv 248,56,12
Mv 188,52,12
Rt 154,24,51200
Mv 184,48,12
Mv 124,44,12
Rt 156,25,49152
Mv 120,40,12
buffer_load_dwordx4 v15, s[8:11], s39 offen offset:3072 lds
Mv 60,36,12
Rt 158,25,51200
Mv 56,32,12
Mv 64,32,16
Rb 160,17,32768
Mv 68,36,16
Mv 128,40,16
Rb 164,17,34816
Mv 132,44,16
s_add_u32 s39, s39, 0x40000
Mv 192,48,16
Rb 168,17,40960
s_add_u32 m0, s37, 32768
Mv 196,52,16
buffer_load_dwordx4 v12, s[8:11], s39 offen offset:0 lds
Ma 192,56,16
Rb 172,17,43008
Ma 196,60,16
Ma 204,60,20
Rb 176,17,49152
Ma 200,56,20
Mv 204,52,20
)" R"(
Rb 180,17,51200
Mv 200,48,20
Mv 140,44,20
Rb 184,17,57344
Mv 136,40,20
buffer_load_dwordx4 v13, s[8:11], s39 offen offset:1024 lds
Mv 76,36,20
Rb 188,17,59392
Mv 72,32,20
Mv 80,32,24
Mv 84,36,24
Mv 144,40,24
Mv 148,44,24
Mv 208,48,24
Mv 212,52,24
buffer_load_dwordx4 v14, s[8:11], s39 offen offset:2048 lds
Ma 208,56,24
Ma 212,60,24
Ma 220,60,28
Ma 216,56,28
Mv 220,52,28
Mv 216,48,28
Mv 156,44,28
Mv 152,40,28
buffer_load_dwordx4 v15, s[8:11], s39 offen offset:3072 lds
Mv 92,36,28
Mv 88,32,28
s_waitcnt vmcnt(8) lgkmcnt(0)
s_barrier
buffer_store_dword v6, v7, s[20:23], s52 offen offset:0 sc0 sc1
Mv 32,160,128
Rt 0,18,0
Mv 36,164,128
Mv 96,168,128
Rt 2,18,2048
Mv 100,172,128
s_add_u32 s38, s38, 0xffff0080
Mv 160,176,128
Rt 4,19,0
s_add_u32 m0, s36, 32768
Mv 164,180,128
buffer_load_dwordx4 v8, s[4:7], s38 offen offset:0 lds
Mv 224,184,128
Rt 6,19,2048
Mv 228,188,128
Mv 236,188,132
Rt 8,20,0
s_mov_b32 s40, 5
Mv 232,184,132
Mv 172,180,132
Rt 10,20,2048
Mv 168,176,132
Mv 108,172,132
Rt 12,21,0
Mv 104,168,132
buffer_load_dwordx4 v9, s[4:7], s38 offen offset:1024 lds
Mv 44,164,132
Rt 14,21,2048
Mv 40,160,132
Mv 48,160,136
Rt 16,22,0
Mv 52,164,136
Mv 112,168,136
Rt 18,22,2048
Mv 116,172,136
)" R"(
Mv 176,176,136
Rt 20,23,0
Mv 180,180,136
buffer_load_dwordx4 v10, s[4:7], s38 offen offset:2048 lds
Mv 240,184,136
Rt 22,23,2048
Mv 244,188,136
Mv 252,188,140
Rt 24,24,0
Mv 248,184,140
Mv 188,180,140
Rt 26,24,2048
Mv 184,176,140
Mv 124,172,140
Rt 28,25,0
Mv 120,168,140
buffer_load_dwordx4 v11, s[4:7], s38 offen offset:3072 lds
Mv 60,164,140
Rt 30,25,2048
Mv 56,160,140
Mv 64,160,144
Rb 32,16,0
Mv 68,164,144
Mv 128,168,144
Rb 36,16,2048
Mv 132,172,144
s_add_u32 s38, s38, 0x10000
Mv 192,176,144
Rb 40,16,8192
s_add_u32 m0, s36, 36864
Mv 196,180,144
buffer_load_dwordx4 v8, s[4:7], s38 offen offset:0 lds
Ma 192,184,144
Rb 44,16,10240
Ma 196,188,144
Ma 204,188,148
Rb 48,16,16384
Ma 200,184,148
Mv 204,180,148
Rb 52,16,18432
Mv 200,176,148
Mv 140,172,148
Rb 56,16,24576
Mv 136,168,148
buffer_load_dwordx4 v9, s[4:7], s38 offen offset:1024 lds
Mv 76,164,148
Rb 60,16,26624
Mv 72,160,148
Mv 80,160,152
Mv 84,164,152
Mv 144,168,152
Mv 148,172,152
Mv 208,176,152
Mv 212,180,152
buffer_load_dwordx4 v10, s[4:7], s38 offen offset:2048 lds
Ma 208,184,152
Ma 212,188,152
Ma 220,188,156
Ma 216,184,156
Mv 220,180,156
Mv 216,176,156
Mv 156,172,156
Mv 152,168,156
buffer_load_dwordx4 v11, s[4:7], s38 offen offset:3072 lds
)" R"(
Mv 92,164,156
Mv 88,160,156
s_waitcnt vmcnt(13) lgkmcnt(0)
s_barrier
.LzPBV:
Mv 32,32,0
Rt 128,18,16384
Mv 36,36,0
Mv 96,40,0
Rt 130,18,18432
Mv 100,44,0
s_add_u32 s39, s39, 0x40000
Mv 160,48,0
Rt 132,19,16384
s_add_u32 m0, s37, 49152
Mv 164,52,0
buffer_load_dwordx4 v12, s[8:11], s39 offen offset:0 lds
Mv 224,56,0
Rt 134,19,18432
Mv 228,60,0
Mv 236,60,4
Rt 136,20,16384
Mv 232,56,4
Mv 172,52,4
Rt 138,20,18432
Mv 168,48,4
Mv 108,44,4
Rt 140,21,16384
Mv 104,40,4
buffer_load_dwordx4 v13, s[8:11], s39 offen offset:1024 lds
Mv 44,36,4
Rt 142,21,18432
Mv 40,32,4
Mv 48,32,8
Rt 144,22,16384
Mv 52,36,8
Mv 112,40,8
Rt 146,22,18432
Mv 116,44,8
Mv 176,48,8
Rt 148,23,16384
Mv 180,52,8
buffer_load_dwordx4 v14, s[8:11], s39 offen offset:2048 lds
Mv 240,56,8
Rt 150,23,18432
Mv 244,60,8
Mv 252,60,12
Rt 152,24,16384
Mv 248,56,12
Mv 188,52,12
Rt 154,24,18432
Mv 184,48,12
Mv 124,44,12
Rt 156,25,16384
Mv 120,40,12
buffer_load_dwordx4 v15, s[8:11], s39 offen offset:3072 lds
Mv 60,36,12
Rt 158,25,18432
Mv 56,32,12
Mv 64,32,16
Rb 160,17,0
Mv 68,36,16
Mv 128,40,16
Rb 164,17,2048
Mv 132,44,16
)" R"(
s_add_u32 s39, s39, 0x40000
Mv 192,48,16
Rb 168,17,8192
s_add_u32 m0, s37, 0
Mv 196,52,16
buffer_load_dwordx4 v12, s[8:11], s39 offen offset:0 lds
Ma 192,56,16
Rb 172,17,10240
Ma 196,60,16
Ma 204,60,20
Rb 176,17,16384
Ma 200,56,20
Mv 204,52,20
Rb 180,17,18432
Mv 200,48,20
Mv 140,44,20
Rb 184,17,24576
Mv 136,40,20
buffer_load_dwordx4 v13, s[8:11], s39 offen offset:1024 lds
Mv 76,36,20
Rb 188,17,26624
Mv 72,32,20
Mv 80,32,24
Mv 84,36,24
Mv 144,40,24
Mv 148,44,24
Mv 208,48,24
Mv 212,52,24
buffer_load_dwordx4 v14, s[8:11], s39 offen offset:2048 lds
Ma 208,56,24
Ma 212,60,24
Ma 220,60,28
Ma 216,56,28
Mv 220,52,28
Mv 216,48,28
Mv 156,44,28
Mv 152,40,28
buffer_load_dwordx4 v15, s[8:11], s39 offen offset:3072 lds
Mv 92,36,28
Mv 88,32,28
s_waitcnt vmcnt(8) lgkmcnt(0)
s_barrier
Mv 32,160,128
Rt 0,18,32768
Mv 36,164,128
Mv 96,168,128
Rt 2,18,34816
Mv 100,172,128
s_add_u32 s38, s38, 0xffff0080
Mv 160,176,128
Rt 4,19,32768
s_add_u32 m0, s36, 0
Mv 164,180,128
buffer_load_dwordx4 v8, s[4:7], s38 offen offset:0 lds
Mv 224,184,128
Rt 6,19,34816
Mv 228,188,128
Mv 236,188,132
Rt 8,20,32768
Mv 232,184,132
Mv 172,180,132
Rt 10,20,34816
Mv 168,176,132
Mv 108,172,132
Rt 12,21,32768
)" R"(
Mv 104,168,132
buffer_load_dwordx4 v9, s[4:7], s38 offen offset:1024 lds
Mv 44,164,132
Rt 14,21,34816
Mv 40,160,132
Mv 48,160,136
Rt 16,22,32768
Mv 52,164,136
Mv 112,168,136
Rt 18,22,34816
Mv 116,172,136
Mv 176,176,136
Rt 20,23,32768
Mv 180,180,136
buffer_load_dwordx4 v10, s[4:7], s38 offen offset:2048 lds
Mv 240,184,136
Rt 22,23,34816
Mv 244,188,136
Mv 252,188,140
Rt 24,24,32768
Mv 248,184,140
Mv 188,180,140
Rt 26,24,34816
Mv 184,176,140
Mv 124,172,140
Rt 28,25,32768
Mv 120,168,140
buffer_load_dwordx4 v11, s[4:7], s38 offen offset:3072 lds
Mv 60,164,140
Rt 30,25,34816
Mv 56,160,140
Mv 64,160,144
Rb 32,16,32768
Mv 68,164,144
Mv 128,168,144
Rb 36,16,34816
Mv 132,172,144
s_add_u32 s38, s38, 0x10000
Mv 192,176,144
Rb 40,16,40960
s_add_u32 m0, s36, 4096
Mv 196,180,144
buffer_load_dwordx4 v8, s[4:7], s38 offen offset:0 lds
Ma 192,184,144
Rb 44,16,43008
Ma 196,188,144
Ma 204,188,148
Rb 48,16,49152
Ma 200,184,148
Mv 204,180,148
Rb 52,16,51200
Mv 200,176,148
Mv 140,172,148
Rb 56,16,57344
Mv 136,168,148
buffer_load_dwordx4 v9, s[4:7], s38 offen offset:1024 lds
Mv 76,164,148
Rb 60,16,59392
Mv 72,160,148
Mv 80,160,152
Mv 84,164,152
Mv 144,168,152
Mv 148,172,152
Mv 208,176,152
)" R"(
Mv 212,180,152
buffer_load_dwordx4 v10, s[4:7], s38 offen offset:2048 lds
Ma 208,184,152
Ma 212,188,152
Ma 220,188,156
Ma 216,184,156
Mv 220,180,156
Mv 216,176,156
Mv 156,172,156
Mv 152,168,156
buffer_load_dwordx4 v11, s[4:7], s38 offen offset:3072 lds
Mv 92,164,156
Mv 88,160,156
s_waitcnt vmcnt(12) lgkmcnt(0)
s_barrier
Mv 32,32,0
Rt 128,18,49152
Mv 36,36,0
Mv 96,40,0
Rt 130,18,51200
Mv 100,44,0
s_add_u32 s39, s39, 0x40000
Mv 160,48,0
Rt 132,19,49152
s_add_u32 m0, s37, 16384
Mv 164,52,0
buffer_load_dwordx4 v12, s[8:11], s39 offen offset:0 lds
Mv 224,56,0
Rt 134,19,51200
Mv 228,60,0
Mv 236,60,4
Rt 136,20,49152
Mv 232,56,4
Mv 172,52,4
Rt 138,20,51200
Mv 168,48,4
Mv 108,44,4
Rt 140,21,49152
Mv 104,40,4
buffer_load_dwordx4 v13, s[8:11], s39 offen offset:1024 lds
Mv 44,36,4
Rt 142,21,51200
Mv 40,32,4
Mv 48,32,8
Rt 144,22,49152
Mv 52,36,8
Mv 112,40,8
Rt 146,22,51200
Mv 116,44,8
Mv 176,48,8
Rt 148,23,49152
Mv 180,52,8
buffer_load_dwordx4 v14, s[8:11], s39 offen offset:2048 lds
Mv 240,56,8
Rt 150,23,51200
Mv 244,60,8
Mv 252,60,12
Rt 152,24,49152
Mv 248,56,12
Mv 188,52,12
Rt 154,24,51200
Mv 184,48,12
Mv 124,44,12
Rt 156,25,49152
Mv 120,40,12
)" R"(
buffer_load_dwordx4 v15, s[8:11], s39 offen offset:3072 lds
Mv 60,36,12
Rt 158,25,51200
Mv 56,32,12
Mv 64,32,16
Rb 160,17,32768
Mv 68,36,16
Mv 128,40,16
Rb 164,17,34816
Mv 132,44,16
s_add_u32 s39, s39, 0x40000
Mv 192,48,16
Rb 168,17,40960
s_add_u32 m0, s37, 32768
Mv 196,52,16
buffer_load_dwordx4 v12, s[8:11], s39 offen offset:0 lds
Ma 192,56,16
Rb 172,17,43008
Ma 196,60,16
Ma 204,60,20
Rb 176,17,49152
Ma 200,56,20
Mv 204,52,20
Rb 180,17,51200
Mv 200,48,20
Mv 140,44,20
Rb 184,17,57344
Mv 136,40,20
buffer_load_dwordx4 v13, s[8:11], s39 offen offset:1024 lds
Mv 76,36,20
Rb 188,17,59392
Mv 72,32,20
Mv 80,32,24
Mv 84,36,24
Mv 144,40,24
Mv 148,44,24
Mv 208,48,24
Mv 212,52,24
buffer_load_dwordx4 v14, s[8:11], s39 offen offset:2048 lds
Ma 208,56,24
Ma 212,60,24
Ma 220,60,28
Ma 216,56,28
Mv 220,52,28
Mv 216,48,28
Mv 156,44,28
Mv 152,40,28
buffer_load_dwordx4 v15, s[8:11], s39 offen offset:3072 lds
Mv 92,36,28
Mv 88,32,28
s_waitcnt vmcnt(8) lgkmcnt(0)
s_barrier
Mv 32,160,128
Rt 0,18,0
Mv 36,164,128
Mv 96,168,128
Rt 2,18,2048
Mv 100,172,128
s_add_u32 s38, s38, 0xffff0080
Mv 160,176,128
Rt 4,19,0
s_add_u32 m0, s36, 32768
Mv 164,180,128
buffer_load_dwordx4 v8, s[4:7], s38 offen offset:0 lds
Mv 224,184,128
)" R"(
Rt 6,19,2048
Mv 228,188,128
Mv 236,188,132
Rt 8,20,0
Mv 232,184,132
Mv 172,180,132
Rt 10,20,2048
Mv 168,176,132
Mv 108,172,132
Rt 12,21,0
Mv 104,168,132
buffer_load_dwordx4 v9, s[4:7], s38 offen offset:1024 lds
Mv 44,164,132
Rt 14,21,2048
Mv 40,160,132
Mv 48,160,136
Rt 16,22,0
Mv 52,164,136
Mv 112,168,136
Rt 18,22,2048
Mv 116,172,136
Mv 176,176,136
Rt 20,23,0
Mv 180,180,136
buffer_load_dwordx4 v10, s[4:7], s38 offen offset:2048 lds
Mv 240,184,136
Rt 22,23,2048
Mv 244,188,136
Mv 252,188,140
Rt 24,24,0
Mv 248,184,140
Mv 188,180,140
Rt 26,24,2048
Mv 184,176,140
Mv 124,172,140
Rt 28,25,0
Mv 120,168,140
buffer_load_dwordx4 v11, s[4:7], s38 offen offset:3072 lds
Mv 60,164,140
Rt 30,25,2048
Mv 56,160,140
Mv 64,160,144
Rb 32,16,0
Mv 68,164,144
Mv 128,168,144
Rb 36,16,2048
Mv 132,172,144
s_add_u32 s38, s38, 0x10000
Mv 192,176,144
Rb 40,16,8192
s_add_u32 m0, s36, 36864
Mv 196,180,144
buffer_load_dwordx4 v8, s[4:7], s38 offen offset:0 lds
Ma 192,184,144
Rb 44,16,10240
Ma 196,188,144
Ma 204,188,148
Rb 48,16,16384
Ma 200,184,148
Mv 204,180,148
Rb 52,16,18432
Mv 200,176,148
Mv 140,172,148
Rb 56,16,24576
)" R"(
Mv 136,168,148
buffer_load_dwordx4 v9, s[4:7], s38 offen offset:1024 lds
Mv 76,164,148
Rb 60,16,26624
Mv 72,160,148
Mv 80,160,152
Mv 84,164,152
Mv 144,168,152
Mv 148,172,152
Mv 208,176,152
Mv 212,180,152
buffer_load_dwordx4 v10, s[4:7], s38 offen offset:2048 lds
Ma 208,184,152
Ma 212,188,152
Ma 220,188,156
Ma 216,184,156
Mv 220,180,156
s_sub_u32 s40, s40, 1
Mv 216,176,156
Mv 156,172,156
Mv 152,168,156
buffer_load_dwordx4 v11, s[4:7], s38 offen offset:3072 lds
Mv 92,164,156
Mv 88,160,156
s_cmp_lg_u32 s40, 0
s_waitcnt vmcnt(12) lgkmcnt(0)
s_barrier
s_cbranch_scc1 .LzPBV
Mv 32,32,0
Rt 128,18,16384
Mv 36,36,0
s_mov_b32 s24, s80
s_mov_b32 s25, s81
Mv 96,40,0
Rt 130,18,18432
s_mov_b32 s26, 0x380000
Mv 100,44,0
s_add_u32 s39, s39, 0x40000
s_mov_b32 s27, 0x20000
Mv 160,48,0
Rt 132,19,16384
s_add_u32 m0, s37, 49152
Mv 164,52,0
buffer_load_dwordx4 v12, s[8:11], s39 offen offset:0 lds
Mv 224,56,0
Rt 134,19,18432
s_bfe_u32 s28, s2, 0x30005
Mv 228,60,0
s_lshl3_add_u32 s29, s34, s28
s_lshl2_add_u32 s29, s29, s33
Mv 236,60,4
Rt 136,20,16384
s_mul_i32 s83, s29, 7168
Mv 232,56,4
s_mul_i32 s29, s85, 448
s_add_u32 s83, s83, s29
Mv 172,52,4
Rt 138,20,18432
s_lshl1_add_u32 s29, s34, s85
Mv 168,48,4
s_lshl_b32 s82, s29, 17
s_add_u32 s82, s82, 0x5000000
Mv 108,44,4
Rt 140,21,16384
s_mul_i32 s29, s28, 14
)" R"(
Mv 104,40,4
buffer_load_dwordx4 v13, s[8:11], s39 offen offset:1024 lds
Mv 44,36,4
Rt 142,21,18432
s_add_u32 s29, s29, s33
Mv 40,32,4
s_lshl3_add_u32 s28, s33, s28
Mv 48,32,8
Rt 144,22,16384
Mv 52,36,8
s_cmp_eq_u32 s85, 0
s_cselect_b32 s87, s29, s28
Mv 112,40,8
Rt 146,22,18432
Mv 116,44,8
s_cmp_eq_u32 s85, 0
s_cselect_b32 s92, 4, 32
Mv 176,48,8
Rt 148,23,16384
v_lshrrev_b32 v4, 5, v0
Mv 180,52,8
buffer_load_dwordx4 v14, s[8:11], s39 offen offset:2048 lds
Mv 240,56,8
Rt 150,23,18432
v_mul_u32_u24 v4, 384, v4
Mv 244,60,8
v_add_u32 v28, v4, v1
v_and_b32 v4, 31, v0
Mv 252,60,12
Rt 152,24,16384
v_cmp_lt_u32_e32 vcc, 27, v4
Mv 248,56,12
v_mov_b32 v4, 0x40000000
v_lshrrev_b32 v29, 2, v0
Mv 188,52,12
Rt 154,24,18432
v_cndmask_b32_e32 v28, v28, v4, vcc
Mv 184,48,12
v_and_b32 v4, 48, v1
v_lshl_or_b32 v29, v29, 9, v4
Mv 124,44,12
Rt 156,25,16384
s_lshr_b32 s28, s33, 1
Mv 120,40,12
buffer_load_dwordx4 v15, s[8:11], s39 offen offset:3072 lds
Mv 60,36,12
Rt 158,25,18432
s_lshl_b32 s28, s28, 6
Mv 56,32,12
s_and_b32 s29, s33, 1
s_lshl3_add_u32 s28, s29, s28
Mv 64,32,16
Rb 160,17,0
s_add_u32 s28, s28, 0x20000
Mv 68,36,16
v_add_u32 v29, s28, v29
Mv 128,40,16
Rb 164,17,2048
Mv 132,44,16
s_add_u32 s39, s39, 0x40000
Mv 192,48,16
Rb 168,17,8192
s_add_u32 m0, s37, 0
Mv 196,52,16
)" R"(
buffer_load_dwordx4 v12, s[8:11], s39 offen offset:0 lds
Ma 192,56,16
Rb 172,17,10240
Ma 196,60,16
Ma 204,60,20
Rb 176,17,16384
Ma 200,56,20
Mv 204,52,20
Rb 180,17,18432
Mv 200,48,20
Mv 140,44,20
Rb 184,17,24576
Mv 136,40,20
buffer_load_dwordx4 v13, s[8:11], s39 offen offset:1024 lds
Mv 76,36,20
Rb 188,17,26624
Mv 72,32,20
Mv 80,32,24
Mv 84,36,24
Mv 144,40,24
Mv 148,44,24
Mv 208,48,24
Mv 212,52,24
buffer_load_dwordx4 v14, s[8:11], s39 offen offset:2048 lds
Ma 208,56,24
Ma 212,60,24
Ma 220,60,28
Ma 216,56,28
Mv 220,52,28
Mv 216,48,28
Mv 156,44,28
Mv 152,40,28
buffer_load_dwordx4 v15, s[8:11], s39 offen offset:3072 lds
Mv 92,36,28
s_add_u32 m0, s37, 0x10000
s_add_u32 s29, s83, 0
buffer_load_dwordx4 v28, s[24:27], s29 offen offset:0 lds
Mv 88,32,28
s_add_u32 s29, s83, 768
s_waitcnt vmcnt(9) lgkmcnt(0)
s_barrier
Mv 32,160,128
Rt 0,18,32768
buffer_load_dwordx4 v28, s[24:27], s29 offen offset:1024 lds
s_add_u32 s29, s83, 1536
Mv 36,164,128
buffer_load_dwordx4 v28, s[24:27], s29 offen offset:2048 lds
s_add_u32 s29, s83, 2304
buffer_load_dwordx4 v28, s[24:27], s29 offen offset:3072 lds
Mv 96,168,128
Rt 2,18,34816
Mv 100,172,128
s_add_u32 s38, s38, 0xffff0080
Mv 160,176,128
Rt 4,19,32768
s_add_u32 m0, s36, 0
Mv 164,180,128
buffer_load_dwordx4 v8, s[4:7], s38 offen offset:0 lds
Mv 224,184,128
Rt 6,19,34816
Mv 228,188,128
Mv 236,188,132
Rt 8,20,32768
Mv 232,184,132
Mv 172,180,132
)" R"(
Rt 10,20,34816
Mv 168,176,132
Mv 108,172,132
Rt 12,21,32768
Mv 104,168,132
buffer_load_dwordx4 v9, s[4:7], s38 offen offset:1024 lds
Mv 44,164,132
Rt 14,21,34816
Mv 40,160,132
Mv 48,160,136
Rt 16,22,32768
Mv 52,164,136
Mv 112,168,136
Rt 18,22,34816
Mv 116,172,136
Mv 176,176,136
Rt 20,23,32768
Mv 180,180,136
buffer_load_dwordx4 v10, s[4:7], s38 offen offset:2048 lds
Mv 240,184,136
Rt 22,23,34816
Mv 244,188,136
Mv 252,188,140
Rt 24,24,32768
Mv 248,184,140
Mv 188,180,140
Rt 26,24,34816
Mv 184,176,140
Mv 124,172,140
Rt 28,25,32768
Mv 120,168,140
buffer_load_dwordx4 v11, s[4:7], s38 offen offset:3072 lds
Mv 60,164,140
Rt 30,25,34816
Mv 56,160,140
Mv 64,160,144
Rb 32,16,32768
Mv 68,164,144
Mv 128,168,144
Rb 36,16,34816
Mv 132,172,144
s_add_u32 s38, s38, 0x10000
Mv 192,176,144
Rb 40,16,40960
s_add_u32 m0, s36, 4096
Mv 196,180,144
buffer_load_dwordx4 v8, s[4:7], s38 offen offset:0 lds
Ma 192,184,144
Rb 44,16,43008
Ma 196,188,144
Ma 204,188,148
Rb 48,16,49152
Ma 200,184,148
Mv 204,180,148
Rb 52,16,51200
Mv 200,176,148
Mv 140,172,148
Rb 56,16,57344
Mv 136,168,148
buffer_load_dwordx4 v9, s[4:7], s38 offen offset:1024 lds
Mv 76,164,148
Rb 60,16,59392
Mv 72,160,148
Mv 80,160,152
)" R"(
Mv 84,164,152
Mv 144,168,152
Mv 148,172,152
Mv 208,176,152
Mv 212,180,152
buffer_load_dwordx4 v10, s[4:7], s38 offen offset:2048 lds
Ma 208,184,152
Ma 212,188,152
Ma 220,188,156
Ma 216,184,156
Mv 220,180,156
Mv 216,176,156
Mv 156,172,156
Mv 152,168,156
buffer_load_dwordx4 v11, s[4:7], s38 offen offset:3072 lds
Mv 92,164,156
Mv 88,160,156
s_waitcnt vmcnt(8) lgkmcnt(0)
s_barrier
Mv 32,32,0
Rt 128,18,49152
Mv 36,36,0
Mv 96,40,0
Rt 130,18,51200
v_lshrrev_b32 v5, 2, v0
Mv 100,44,0
s_add_u32 s39, s39, 0x40000
v_and_b32 v5, 8, v5
Mv 160,48,0
Rt 132,19,49152
s_add_u32 m0, s37, 16384
Mv 164,52,0
buffer_load_dwordx4 v12, s[8:11], s39 offen offset:0 lds
Mv 224,56,0
Rt 134,19,51200
v_and_or_b32 v5, v0, 7, v5
Mv 228,60,0
v_lshlrev_b32 v5, 8, v5
v_and_or_b32 v5, v0, 24, v5
Mv 236,60,4
Rt 136,20,49152
v_lshrrev_b32 v5, 1, v5
Mv 232,56,4
Mv 172,52,4
Rt 138,20,51200
Mv 168,48,4
Mv 108,44,4
Rt 140,21,49152
Mv 104,40,4
buffer_load_dwordx4 v13, s[8:11], s39 offen offset:1024 lds
Mv 44,36,4
Rt 142,21,51200
Mv 40,32,4
Mv 48,32,8
Rt 144,22,49152
Mv 52,36,8
Mv 112,40,8
Rt 146,22,51200
Mv 116,44,8
Mv 176,48,8
Rt 148,23,49152
Mv 180,52,8
buffer_load_dwordx4 v14, s[8:11], s39 offen offset:2048 lds
Mv 240,56,8
Rt 150,23,51200
)" R"(
Mv 244,60,8
Mv 252,60,12
Rt 152,24,49152
Mv 248,56,12
Mv 188,52,12
Rt 154,24,51200
Mv 184,48,12
Mv 124,44,12
Rt 156,25,49152
Mv 120,40,12
buffer_load_dwordx4 v15, s[8:11], s39 offen offset:3072 lds
Mv 60,36,12
Rt 158,25,51200
Mv 56,32,12
Mv 64,32,16
Rb 160,17,32768
Mv 68,36,16
Mv 128,40,16
Rb 164,17,34816
Mv 132,44,16
s_add_u32 s39, s39, 0x40000
Mv 192,48,16
Rb 168,17,40960
s_add_u32 m0, s37, 32768
Mv 196,52,16
buffer_load_dwordx4 v12, s[8:11], s39 offen offset:0 lds
Ma 192,56,16
Rb 172,17,43008
Ma 196,60,16
Ma 204,60,20
Rb 176,17,49152
Ma 200,56,20
Mv 204,52,20
Rb 180,17,51200
Mv 200,48,20
Mv 140,44,20
Rb 184,17,57344
Mv 136,40,20
buffer_load_dwordx4 v13, s[8:11], s39 offen offset:1024 lds
Mv 76,36,20
Rb 188,17,59392
Mv 72,32,20
Mv 80,32,24
Rt 224,29,0
Mv 84,36,24
Rt 226,29,8192
Mv 144,40,24
Rt 228,29,128
Mv 148,44,24
Rt 230,29,8320
Mv 208,48,24
Rt 232,29,256
Mv 212,52,24
buffer_load_dwordx4 v14, s[8:11], s39 offen offset:2048 lds
Rt 234,29,8448
Ma 208,56,24
Rt 236,29,384
Ma 212,60,24
Rt 238,29,8576
Ma 220,60,28
Ma 216,56,28
Mv 220,52,28
Mv 216,48,28
Mv 156,44,28
)" R"(
Mv 152,40,28
buffer_load_dwordx4 v15, s[8:11], s39 offen offset:3072 lds
Mv 92,36,28
Mv 88,32,28
s_waitcnt vmcnt(8) lgkmcnt(0)
s_barrier
Mv 32,160,128
Rt 0,18,0
s_mul_i32 s29, s87, 147
Mv 36,164,128
s_lshr_b32 s29, s29, 10
s_add_u32 s29, s29, s87
Mv 96,168,128
Rt 2,18,2048
s_lshl_b32 s29, s29, 10
Mv 100,172,128
s_add_u32 s38, s38, 0xffff0080
s_add_u32 s29, s29, s82
Mv 160,176,128
Rt 4,19,0
s_add_u32 m0, s36, 32768
Mv 164,180,128
buffer_load_dwordx4 v8, s[4:7], s38 offen offset:0 lds
Mv 224,184,128
Rt 6,19,2048
s_add_u32 s87, s87, s92
Mv 228,188,128
Mv 236,188,132
Rt 8,20,0
Mv 232,184,132
buffer_store_dwordx4 a[224:227], v1, s[20:23], s29 offen sc0 sc1
Mv 172,180,132
Rt 10,20,2048
Mv 168,176,132
Mv 108,172,132
Rt 12,21,0
Mv 104,168,132
buffer_load_dwordx4 v9, s[4:7], s38 offen offset:1024 lds
Mv 44,164,132
Rt 14,21,2048
Mv 40,160,132
Mv 48,160,136
Rt 16,22,0
s_mul_i32 s29, s87, 147
Mv 52,164,136
s_lshr_b32 s29, s29, 10
s_add_u32 s29, s29, s87
Mv 112,168,136
Rt 18,22,2048
s_lshl_b32 s29, s29, 10
Mv 116,172,136
s_add_u32 s29, s29, s82
s_add_u32 s87, s87, s92
Mv 176,176,136
Rt 20,23,0
Mv 180,180,136
buffer_load_dwordx4 v10, s[4:7], s38 offen offset:2048 lds
Mv 240,184,136
Rt 22,23,2048
Mv 244,188,136
Mv 252,188,140
Rt 24,24,0
Mv 248,184,140
buffer_store_dwordx4 a[228:231], v1, s[20:23], s29 offen sc0 sc1
Mv 188,180,140
)" R"(
Rt 26,24,2048
Mv 184,176,140
Mv 124,172,140
Rt 28,25,0
Mv 120,168,140
buffer_load_dwordx4 v11, s[4:7], s38 offen offset:3072 lds
Mv 60,164,140
Rt 30,25,2048
Mv 56,160,140
Mv 64,160,144
Rb 32,16,0
s_mul_i32 s29, s87, 147
Mv 68,164,144
s_lshr_b32 s29, s29, 10
s_add_u32 s29, s29, s87
Mv 128,168,144
Rb 36,16,2048
s_lshl_b32 s29, s29, 10
Mv 132,172,144
s_add_u32 s38, s38, 0x10000
s_add_u32 s29, s29, s82
Mv 192,176,144
Rb 40,16,8192
s_add_u32 m0, s36, 36864
Mv 196,180,144
buffer_load_dwordx4 v8, s[4:7], s38 offen offset:0 lds
Ma 192,184,144
Rb 44,16,10240
s_add_u32 s87, s87, s92
Ma 196,188,144
Ma 204,188,148
Rb 48,16,16384
Ma 200,184,148
buffer_store_dwordx4 a[232:235], v1, s[20:23], s29 offen sc0 sc1
Mv 204,180,148
Rb 52,16,18432
Mv 200,176,148
Mv 140,172,148
Rb 56,16,24576
Mv 136,168,148
buffer_load_dwordx4 v9, s[4:7], s38 offen offset:1024 lds
Mv 76,164,148
Rb 60,16,26624
Mv 72,160,148
Mv 80,160,152
s_mul_i32 s29, s87, 147
s_lshr_b32 s29, s29, 10
Mv 84,164,152
s_add_u32 s29, s29, s87
s_lshl_b32 s29, s29, 10
Mv 144,168,152
s_add_u32 s29, s29, s82
Mv 148,172,152
s_cmp_gt_u32 s33, 1
s_cmov_b32 s29, 0x40000000
Mv 208,176,152
Mv 212,180,152
buffer_load_dwordx4 v10, s[4:7], s38 offen offset:2048 lds
Ma 208,184,152
Ma 212,188,152
Ma 220,188,156
Ma 216,184,156
buffer_store_dwordx4 a[236:239], v1, s[20:23], s29 offen sc0 sc1
Mv 220,180,156
)" R"(
Mv 216,176,156
Mv 156,172,156
Mv 152,168,156
buffer_load_dwordx4 v11, s[4:7], s38 offen offset:3072 lds
Mv 92,164,156
Mv 88,160,156
s_waitcnt vmcnt(16) lgkmcnt(0)
s_barrier
Mv 32,32,0
Rt 128,18,16384
Mv 36,36,0
Mv 96,40,0
Rt 130,18,18432
Mv 100,44,0
s_add_u32 s39, s39, 0x40000
Mv 160,48,0
Rt 132,19,16384
s_add_u32 m0, s37, 49152
Mv 164,52,0
buffer_load_dwordx4 v12, s[8:11], s39 offen offset:0 lds
Mv 224,56,0
Rt 134,19,18432
Mv 228,60,0
Mv 236,60,4
Rt 136,20,16384
Mv 232,56,4
Mv 172,52,4
Rt 138,20,18432
Mv 168,48,4
Mv 108,44,4
Rt 140,21,16384
Mv 104,40,4
buffer_load_dwordx4 v13, s[8:11], s39 offen offset:1024 lds
Mv 44,36,4
Rt 142,21,18432
Mv 40,32,4
Mv 48,32,8
Rt 144,22,16384
Mv 52,36,8
Mv 112,40,8
Rt 146,22,18432
Mv 116,44,8
Mv 176,48,8
Rt 148,23,16384
Mv 180,52,8
buffer_load_dwordx4 v14, s[8:11], s39 offen offset:2048 lds
Mv 240,56,8
Rt 150,23,18432
Mv 244,60,8
Mv 252,60,12
Rt 152,24,16384
Mv 248,56,12
Mv 188,52,12
Rt 154,24,18432
Mv 184,48,12
Mv 124,44,12
Rt 156,25,16384
Mv 120,40,12
buffer_load_dwordx4 v15, s[8:11], s39 offen offset:3072 lds
Mv 60,36,12
Rt 158,25,18432
Mv 56,32,12
Mv 64,32,16
Rb 160,17,0
Mv 68,36,16
)" R"(
buffer_load_dword v4, v5, s[20:23], s50 offen offset:0 sc0 sc1
Mv 128,40,16
Rb 164,17,2048
Mv 132,44,16
Mv 192,48,16
Rb 168,17,8192
Mv 196,52,16
Ma 192,56,16
Rb 172,17,10240
Ma 196,60,16
Ma 204,60,20
Rb 176,17,16384
Ma 200,56,20
Mv 204,52,20
Rb 180,17,18432
Mv 200,48,20
Mv 140,44,20
Rb 184,17,24576
Mv 136,40,20
Mv 76,36,20
Rb 188,17,26624
Mv 72,32,20
Mv 80,32,24
Mv 84,36,24
Mv 144,40,24
Mv 148,44,24
Mv 208,48,24
Mv 212,52,24
Ma 208,56,24
Ma 212,60,24
Ma 220,60,28
Ma 216,56,28
Mv 220,52,28
Mv 216,48,28
Mv 156,44,28
Mv 152,40,28
Mv 92,36,28
Mv 88,32,28
s_waitcnt vmcnt(0) lgkmcnt(0)
s_barrier
Mv 32,160,128
Rt 0,18,32768
v_cmp_eq_u32_e32 vcc, s3, v4
s_andn2_b64 s[28:29], s[60:61], vcc
s_cbranch_scc1 .Lzsp10
.Lzok10:
Mv 36,164,128
Mv 96,168,128
Rt 2,18,34816
Mv 100,172,128
Mv 160,176,128
Rt 4,19,32768
s_add_u32 m0, s36, 0
Mv 164,180,128
buffer_load_dwordx4 v2, s[20:23], s55 offen offset:0 sc1 lds
Mv 224,184,128
Rt 6,19,34816
Mv 228,188,128
Mv 236,188,132
Rt 8,20,32768
Mv 232,184,132
Mv 172,180,132
Rt 10,20,34816
Mv 168,176,132
buffer_load_dwordx4 v2, s[20:23], s55 offen offset:1024 sc1 lds
)" R"(
Mv 108,172,132
Rt 12,21,32768
Mv 104,168,132
Mv 44,164,132
Rt 14,21,34816
Mv 40,160,132
Mv 48,160,136
Rt 16,22,32768
Mv 52,164,136
buffer_load_dwordx4 v2, s[20:23], s55 offen offset:2048 sc1 lds
Mv 112,168,136
Rt 18,22,34816
Mv 116,172,136
Mv 176,176,136
Rt 20,23,32768
Mv 180,180,136
Mv 240,184,136
Rt 22,23,34816
Mv 244,188,136
buffer_load_dwordx4 v2, s[20:23], s55 offen offset:3072 sc1 lds
Mv 252,188,140
Rt 24,24,32768
Mv 248,184,140
Mv 188,180,140
Rt 26,24,34816
Mv 184,176,140
Mv 124,172,140
Rt 28,25,32768
s_add_u32 m0, s36, 4096
Mv 120,168,140
buffer_load_dwordx4 v3, s[20:23], s55 offen offset:0 sc1 lds
Mv 60,164,140
Rt 30,25,34816
Mv 56,160,140
Mv 64,160,144
Rb 32,16,32768
Mv 68,164,144
Mv 128,168,144
Rb 36,16,34816
Mv 132,172,144
buffer_load_dwordx4 v3, s[20:23], s55 offen offset:1024 sc1 lds
Mv 192,176,144
Rb 40,16,40960
Mv 196,180,144
Ma 192,184,144
Rb 44,16,43008
Ma 196,188,144
Ma 204,188,148
Rb 48,16,49152
Ma 200,184,148
buffer_load_dwordx4 v3, s[20:23], s55 offen offset:2048 sc1 lds
Mv 204,180,148
Rb 52,16,51200
Mv 200,176,148
Mv 140,172,148
Rb 56,16,57344
Mv 136,168,148
Mv 76,164,148
Rb 60,16,59392
Mv 72,160,148
buffer_load_dwordx4 v3, s[20:23], s55 offen offset:3072 sc1 lds
Mv 80,160,152
Mv 84,164,152
Mv 144,168,152
)" R"(
Mv 148,172,152
Mv 208,176,152
Mv 212,180,152
Ma 208,184,152
Ma 212,188,152
Ma 220,188,156
s_add_u32 s55, s55, 0x8000
Ma 216,184,156
Mv 220,180,156
Mv 216,176,156
Mv 156,172,156
Mv 152,168,156
Mv 92,164,156
Mv 88,160,156
s_waitcnt lgkmcnt(4)
s_barrier
Mv 32,32,0
Rt 128,18,49152
Mv 36,36,0
Rt 130,18,51200
Mv 96,40,0
Rt 132,19,49152
Mv 100,44,0
Rt 134,19,51200
Mv 108,44,4
Rt 136,20,49152
Mv 104,40,4
Rt 138,20,51200
Mv 44,36,4
Rt 140,21,49152
Mv 40,32,4
Rt 142,21,51200
Mv 48,32,8
Rt 144,22,49152
Mv 52,36,8
Rt 146,22,51200
Mv 112,40,8
Rt 148,23,49152
Mv 116,44,8
Rt 150,23,51200
Mv 124,44,12
Rt 152,24,49152
Mv 120,40,12
Rt 154,24,51200
Mv 60,36,12
Rt 156,25,49152
Mv 56,32,12
Rt 158,25,51200
Mv 64,32,16
Rb 160,17,32768
Mv 68,36,16
Rb 164,17,34816
Mv 128,40,16
Rb 168,17,40960
Mv 132,44,16
Rb 172,17,43008
Mv 140,44,20
Rb 176,17,49152
Mv 136,40,20
Rb 180,17,51200
Mv 76,36,20
Rb 184,17,57344
Mv 72,32,20
Rb 188,17,59392
Mv 80,32,24
)" R"(
v_add_u32 v8, 0x10000, v1
v_add_u32 v10, 0x20000, v1
Mv 84,36,24
v_lshrrev_b32 v9, 4, v0
v_mul_i32_i24 v9, -20, v9
Mv 144,40,24
v_add_u32 v9, v9, v0
s_lshl_b32 s28, s33, 5
Mv 148,44,24
v_add_u32 v9, s28, v9
s_mov_b32 s93, 2
Mv 156,44,28
s_mov_b32 s94, 0
Mv 152,40,28
Mv 92,36,28
Mv 88,32,28
s_waitcnt lgkmcnt(0)
s_barrier
Mv 32,160,128
Mv 36,164,128
Mv 40,160,132
s_add_u32 m0, s36, 32768
Mv 44,164,132
buffer_load_dwordx4 v2, s[20:23], s55 offen offset:0 sc1 lds
Mv 48,160,136
Mv 52,164,136
s_nop 0
Cv 32,32,33
Cv 33,34,35
Mv 56,160,140
Cv 34,36,37
Cv 35,38,39
Mv 60,164,140
buffer_load_dwordx4 v2, s[20:23], s55 offen offset:1024 sc1 lds
Mv 64,160,144
Cv 40,40,41
Cv 41,42,43
Mv 68,164,144
Cv 42,44,45
Cv 43,46,47
Mv 72,160,148
Cv 48,48,49
Cv 49,50,51
Mv 76,164,148
buffer_load_dwordx4 v2, s[20:23], s55 offen offset:2048 sc1 lds
Mv 80,160,152
Cv 50,52,53
Cv 51,54,55
Mv 84,164,152
Cv 56,56,57
Cv 57,58,59
Mv 88,160,156
Cv 58,60,61
Cv 59,62,63
Mv 92,164,156
buffer_load_dwordx4 v2, s[20:23], s55 offen offset:3072 sc1 lds
Mv 96,168,128
Cv 64,64,65
Cv 65,66,67
Mv 100,172,128
Cv 66,68,69
Cv 67,70,71
Mv 104,168,132
s_add_u32 m0, s36, 36864
Cv 72,72,73
)" R"(
Mv 108,172,132
buffer_load_dwordx4 v3, s[20:23], s55 offen offset:0 sc1 lds
Mv 112,168,136
Cv 73,74,75
Cv 74,76,77
Mv 116,172,136
Cv 75,78,79
Cv 80,80,81
Mv 120,168,140
Cv 81,82,83
Cv 82,84,85
Mv 124,172,140
buffer_store_dwordx4 v[32:35], v2, s[20:23], s45 offen offset:0 sc0 sc1
Mv 128,168,144
Cv 83,86,87
Cv 88,88,89
Mv 132,172,144
Cv 89,90,91
Cv 90,92,93
Mv 136,168,148
Cv 91,94,95
Mv 140,172,148
buffer_load_dwordx4 v3, s[20:23], s55 offen offset:1024 sc1 lds
Mv 144,168,152
Mv 148,172,152
Cv 128,128,129
Cv 129,130,131
Mv 152,168,156
Cv 130,132,133
Cv 131,134,135
Mv 156,172,156
buffer_store_dwordx4 v[40:43], v2, s[20:23], s45 offen offset:1024 sc0 sc1
Mv 160,48,0
Cv 136,136,137
Cv 137,138,139
Mv 160,176,128
Cv 138,140,141
Cv 139,142,143
Mv 164,52,0
Cv 144,144,145
Cv 145,146,147
Mv 164,180,128
buffer_load_dwordx4 v3, s[20:23], s55 offen offset:2048 sc1 lds
Mv 168,48,4
Cv 146,148,149
Cv 147,150,151
Mv 168,176,132
Cv 152,152,153
Cv 153,154,155
Mv 172,52,4
Cv 154,156,157
Cv 155,158,159
Mv 172,180,132
buffer_store_dwordx4 v[48:51], v2, s[20:23], s45 offen offset:2048 sc0 sc1
Mv 176,48,8
Cv 132,96,97
Cv 133,98,99
Mv 176,176,136
Cv 134,100,101
Cv 135,102,103
Mv 180,52,8
Cv 140,104,105
Cv 141,106,107
Mv 180,180,136
)" R"(
buffer_load_dwordx4 v3, s[20:23], s55 offen offset:3072 sc1 lds
Mv 184,48,12
Cv 142,108,109
Cv 143,110,111
Mv 184,176,140
Cv 148,112,113
Cv 149,114,115
Mv 188,52,12
Cv 150,116,117
Cv 151,118,119
Mv 188,180,140
buffer_store_dwordx4 v[56:59], v2, s[20:23], s45 offen offset:3072 sc0 sc1
Mv 192,48,16
Cv 156,120,121
Cv 157,122,123
Mv 192,176,144
Cv 158,124,125
Cv 159,126,127
Mv 196,52,16
s_add_u32 m0, s36, 98304
Cv 160,160,161
Mv 196,180,144
buffer_load_dwordx4 v2, s[20:23], s56 offen offset:0 sc1 lds
Mv 200,48,20
Cv 161,162,163
Cv 162,164,165
Mv 200,176,148
Cv 163,166,167
v_accvgpr_write_b32 a224, v160
Mv 204,52,20
v_accvgpr_write_b32 a225, v161
v_accvgpr_write_b32 a226, v162
Mv 204,180,148
buffer_store_dwordx4 v[64:67], v3, s[20:23], s45 offen offset:0 sc0 sc1
Mv 208,48,24
v_accvgpr_write_b32 a227, v163
Cv 168,168,169
Mv 208,176,152
Cv 169,170,171
Cv 170,172,173
Mv 212,52,24
Cv 171,174,175
v_accvgpr_write_b32 a228, v168
Mv 212,180,152
buffer_load_dwordx4 v2, s[20:23], s56 offen offset:1024 sc1 lds
Mv 216,48,28
v_accvgpr_write_b32 a229, v169
v_accvgpr_write_b32 a230, v170
Mv 216,176,156
v_accvgpr_write_b32 a231, v171
Cv 176,176,177
Mv 220,52,28
Cv 177,178,179
Cv 178,180,181
Mv 220,180,156
buffer_store_dwordx4 v[72:75], v3, s[20:23], s45 offen offset:1024 sc0 sc1
Mv 224,56,0
Cv 179,182,183
v_accvgpr_write_b32 a232, v176
Mv 228,60,0
v_accvgpr_write_b32 a233, v177
v_accvgpr_write_b32 a234, v178
Mv 236,60,4
v_accvgpr_write_b32 a235, v179
)" R"(
Cv 184,184,185
Mv 232,56,4
buffer_load_dwordx4 v2, s[20:23], s56 offen offset:2048 sc1 lds
Mv 240,56,8
Cv 185,186,187
Cv 186,188,189
Mv 244,60,8
Cv 187,190,191
v_accvgpr_write_b32 a236, v184
Mv 252,60,12
v_accvgpr_write_b32 a237, v185
v_accvgpr_write_b32 a238, v186
Mv 248,56,12
s_waitcnt vmcnt(17)
s_barrier
Ma 192,56,16
buffer_store_dwordx4 v[80:83], v3, s[20:23], s45 offen offset:2048 sc0 sc1
Ma 196,60,16
v_accvgpr_write_b32 a239, v187
Ma 204,60,20
Ma 200,56,20
buffer_load_dwordx4 v2, s[20:23], s56 offen offset:3072 sc1 lds
Ma 208,56,24
Ma 212,60,24
Ma 220,60,28
Ma 216,56,28
buffer_store_dwordx4 v[88:91], v3, s[20:23], s45 offen offset:3072 sc0 sc1
Mv 224,184,128
Rb 0,1,0
Mv 228,188,128
Rb 4,1,1024
Mv 232,184,132
s_add_u32 m0, s36, 102400
Rb 8,1,2048
Mv 236,188,132
buffer_load_dwordx4 v3, s[20:23], s56 offen offset:0 sc1 lds
Rb 12,1,3072
Mv 240,184,136
Rb 16,1,8192
Mv 244,188,136
Rb 20,1,9216
Mv 248,184,140
Rb 24,1,10240
Mv 252,188,140
Rb 28,1,11264
Ma 192,184,144
Rb 32,1,4096
Ma 196,188,144
Rb 36,1,5120
Ma 200,184,148
Rb 40,1,6144
Ma 204,188,148
buffer_load_dwordx4 v3, s[20:23], s56 offen offset:1024 sc1 lds
Rb 44,1,7168
Ma 208,184,152
Ma 212,188,152
buffer_load_dwordx4 v3, s[20:23], s56 offen offset:2048 sc1 lds
Ma 216,184,156
Ma 220,188,156
buffer_load_dwordx4 v3, s[20:23], s56 offen offset:3072 sc1 lds
s_add_u32 s56, s56, 0x8000
.LzFB:
s_waitcnt lgkmcnt(8)
Mz 64,0,64
Rb 48,1,12288
)" R"(
buffer_load_dword v4, v5, s[20:23], s50 offen offset:0 sc0 sc1
Mz 68,0,68
s_lshl_b64 s[60:61], s[60:61], 8
s_or_b32 s60, s60, 0xff
Mv 68,4,76
Rb 52,1,13312
s_add_u32 m0, s36, 65536
Mv 64,4,72
buffer_load_dwordx4 v2, s[20:23], s56 offen offset:0 sc1 lds
Mv 64,8,80
Rb 56,1,14336
s_add_u32 s55, s55, 0x8000
Cv 192,192,193
Mv 68,8,84
buffer_store_dwordx4 v[132:135], v2, s[20:23], s46 offen offset:0 sc0 sc1
s_cmp_eq_u32 s57, 2
s_cselect_b32 s55, s59, s55
Mv 68,12,92
Rb 60,1,15360
s_sub_u32 s57, s57, 1
s_waitcnt lgkmcnt(8)
Mv 64,12,88
Cv 193,194,195
Cv 194,196,197
Cv 195,198,199
v_accvgpr_write_b32 a240, v192
Mv 64,16,96
Rb 0,1,16384
v_accvgpr_write_b32 a241, v193
v_accvgpr_write_b32 a242, v194
Mv 68,16,100
v_accvgpr_write_b32 a243, v195
Cv 200,200,201
Cv 201,202,203
Cv 202,204,205
Mv 68,20,108
Rb 4,1,17408
Cv 203,206,207
v_accvgpr_write_b32 a244, v200
Mv 64,20,104
buffer_load_dwordx4 v2, s[20:23], s56 offen offset:1024 sc1 lds
v_accvgpr_write_b32 a245, v201
v_accvgpr_write_b32 a246, v202
Mv 64,24,112
Rb 8,1,18432
v_accvgpr_write_b32 a247, v203
Cv 208,208,209
Cv 209,210,211
Mv 68,24,116
Cv 210,212,213
Cv 211,214,215
v_accvgpr_write_b32 a248, v208
Mv 68,28,124
Rb 12,1,19456
v_accvgpr_write_b32 a249, v209
v_accvgpr_write_b32 a250, v210
s_waitcnt lgkmcnt(8)
Mv 64,28,120
v_accvgpr_write_b32 a251, v211
Cv 216,216,217
Cv 217,218,219
Mz 72,32,64
Rb 16,1,24576
Cv 218,220,221
)" R"(
Cv 219,222,223
v_accvgpr_write_b32 a252, v216
Mz 76,32,68
v_accvgpr_write_b32 a253, v217
v_accvgpr_write_b32 a254, v218
v_accvgpr_write_b32 a255, v219
Mv 76,36,76
Rb 20,1,25600
Cv 224,224,225
Cv 225,226,227
Cv 226,228,229
Mv 72,36,72
buffer_load_dwordx4 v2, s[20:23], s56 offen offset:2048 sc1 lds
Cv 227,230,231
v_accvgpr_write_b32 a128, v224
Mv 72,40,80
Rb 24,1,26624
v_accvgpr_write_b32 a129, v225
v_accvgpr_write_b32 a130, v226
Mv 76,40,84
buffer_store_dwordx4 v[140:143], v2, s[20:23], s46 offen offset:1024 sc0 sc1
v_accvgpr_write_b32 a131, v227
Cv 232,232,233
Mv 76,44,92
Rb 28,1,27648
Cv 233,234,235
s_waitcnt lgkmcnt(8)
Mv 72,44,88
Cv 234,236,237
Cv 235,238,239
v_accvgpr_write_b32 a132, v232
v_accvgpr_write_b32 a133, v233
Mv 72,48,96
Rb 32,1,20480
v_accvgpr_write_b32 a134, v234
v_accvgpr_write_b32 a135, v235
Mv 76,48,100
Cv 240,240,241
Cv 241,242,243
Cv 242,244,245
Cv 243,246,247
Mv 76,52,108
Rb 36,1,21504
v_accvgpr_write_b32 a136, v240
v_accvgpr_write_b32 a137, v241
Mv 72,52,104
buffer_load_dwordx4 v2, s[20:23], s56 offen offset:3072 sc1 lds
v_accvgpr_write_b32 a138, v242
v_accvgpr_write_b32 a139, v243
Mv 72,56,112
Rb 40,1,22528
Cv 248,248,249
Cv 249,250,251
Cv 250,252,253
Mv 76,56,116
Cv 251,254,255
v_accvgpr_write_b32 a140, v248
v_accvgpr_write_b32 a141, v249
Mv 76,60,124
Rb 44,1,23552
v_accvgpr_write_b32 a142, v250
v_accvgpr_write_b32 a143, v251
s_waitcnt lgkmcnt(8)
Mv 72,60,120
)" R"(
v_accvgpr_read_b32 v160, a192
v_accvgpr_read_b32 v28, a193
Cv 160,160,28
Mz 80,0,64
Rb 48,1,28672
v_accvgpr_read_b32 v161, a194
v_accvgpr_read_b32 v29, a195
Cv 161,161,29
Mz 84,0,68
v_accvgpr_read_b32 v162, a196
v_accvgpr_read_b32 v28, a197
Cv 162,162,28
Mv 84,4,76
Rb 52,1,29696
s_add_u32 m0, s36, 69632
v_accvgpr_read_b32 v163, a198
v_accvgpr_read_b32 v29, a199
Mv 80,4,72
buffer_load_dwordx4 v3, s[20:23], s56 offen offset:0 sc1 lds
Cv 163,163,29
v_accvgpr_write_b32 a144, v160
Mv 80,8,80
Rb 56,1,30720
v_accvgpr_write_b32 a145, v161
v_accvgpr_write_b32 a146, v162
Mv 84,8,84
buffer_store_dwordx4 v[148:151], v2, s[20:23], s46 offen offset:2048 sc0 sc1
v_accvgpr_write_b32 a147, v163
v_accvgpr_read_b32 v168, a200
Mv 84,12,92
Rb 60,1,31744
v_accvgpr_read_b32 v28, a201
s_waitcnt lgkmcnt(8)
Mv 80,12,88
Cv 168,168,28
v_accvgpr_read_b32 v169, a202
v_accvgpr_read_b32 v29, a203
Cv 169,169,29
Mv 80,16,96
v_accvgpr_read_b32 v170, a204
v_accvgpr_read_b32 v28, a205
Cv 170,170,28
Mv 84,16,100
v_accvgpr_read_b32 v171, a206
v_accvgpr_read_b32 v29, a207
Cv 171,171,29
v_accvgpr_write_b32 a148, v168
Mv 84,20,108
v_accvgpr_write_b32 a149, v169
v_accvgpr_write_b32 a150, v170
v_accvgpr_write_b32 a151, v171
Mv 80,20,104
buffer_load_dwordx4 v3, s[20:23], s56 offen offset:1024 sc1 lds
v_accvgpr_read_b32 v176, a208
v_accvgpr_read_b32 v28, a209
Mv 80,24,112
Cv 176,176,28
v_accvgpr_read_b32 v177, a210
v_accvgpr_read_b32 v29, a211
Cv 177,177,29
Mv 84,24,116
v_accvgpr_read_b32 v178, a212
v_accvgpr_read_b32 v28, a213
Cv 178,178,28
)" R"(
Mv 84,28,124
v_accvgpr_read_b32 v179, a214
v_accvgpr_read_b32 v29, a215
Cv 179,179,29
v_accvgpr_write_b32 a152, v176
Mv 80,28,120
s_waitcnt vmcnt(9) lgkmcnt(0)
s_barrier
buffer_store_dword v6, v7, s[20:23], s49 offen offset:0 sc0 sc1
Mz 88,32,64
Rb 0,1,32768
v_accvgpr_write_b32 a153, v177
v_accvgpr_write_b32 a154, v178
v_accvgpr_write_b32 a155, v179
Mz 92,32,68
Rb 4,1,33792
v_accvgpr_read_b32 v184, a216
v_accvgpr_read_b32 v28, a217
Mv 92,36,76
Rb 8,1,34816
Cv 184,184,28
v_accvgpr_read_b32 v185, a218
v_accvgpr_read_b32 v29, a219
Mv 88,36,72
Rb 12,1,35840
buffer_load_dwordx4 v3, s[20:23], s56 offen offset:2048 sc1 lds
Cv 185,185,29
Mv 88,40,80
Rb 16,1,40960
v_accvgpr_read_b32 v186, a220
v_accvgpr_read_b32 v28, a221
Mv 92,40,84
Rb 20,1,41984
buffer_store_dwordx4 v[156:159], v2, s[20:23], s46 offen offset:3072 sc0 sc1
Cv 186,186,28
Mv 92,44,92
Rb 24,1,43008
v_accvgpr_read_b32 v187, a222
v_accvgpr_read_b32 v29, a223
Mv 88,44,88
Rb 28,1,44032
Cv 187,187,29
v_accvgpr_write_b32 a156, v184
v_accvgpr_write_b32 a157, v185
Mv 88,48,96
Rb 32,1,36864
v_accvgpr_write_b32 a158, v186
v_accvgpr_write_b32 a159, v187
Mv 92,48,100
Rb 36,1,37888
v_mov_b32 v16, 0
v_mov_b32 v17, 0
v_mov_b32 v18, 0
Mv 92,52,108
Rb 40,1,38912
v_mov_b32 v19, 0
v_mov_b32 v20, 0
Mv 88,52,104
Rb 44,1,39936
buffer_load_dwordx4 v3, s[20:23], s56 offen offset:3072 sc1 lds
v_mov_b32 v21, 0
Mv 88,56,112
v_mov_b32 v22, 0
v_cmp_eq_u32_e32 vcc, s3, v4
s_andn2_b64 s[28:29], s[60:61], vcc
)" R"(
s_cbranch_scc1 .Lzsp11
.Lzok11:
Mv 92,56,116
s_add_u32 s56, s56, 0x8000
v_mov_b32 v23, 0
Mv 92,60,124
s_waitcnt lgkmcnt(8)
Mv 88,60,120
v_exp_f32_e32 v32, v64
Mz 96,0,64
Rb 48,1,45056
buffer_load_dword v4, v5, s[20:23], s50 offen offset:0 sc0 sc1
Mz 100,0,68
s_lshl_b64 s[60:61], s[60:61], 8
s_or_b32 s60, s60, 0xff
Mv 100,4,76
Rb 52,1,46080
s_add_u32 m0, s36, 0
Mv 96,4,72
buffer_load_dwordx4 v2, s[20:23], s55 offen offset:0 sc1 lds
Mv 96,8,80
Rb 56,1,47104
v_exp_f32_e32 v24, v65
Mv 100,8,84
buffer_store_dwordx4 v[128:131], v3, s[20:23], s46 offen offset:0 sc0 sc1
v_add_f32_e32 v14, v32, v24
Mv 100,12,92
Rb 60,1,48128
Cv 32,32,24
s_waitcnt lgkmcnt(8)
Mv 96,12,88
v_exp_f32_e32 v33, v66
Mv 96,16,96
Rb 0,1,49152
v_exp_f32_e32 v25, v67
Mv 100,16,100
v_exp_f32_e32 v34, v72
v_exp_f32_e32 v26, v73
Mv 100,20,108
Rb 4,1,50176
v_exp_f32_e32 v35, v74
Mv 96,20,104
buffer_load_dwordx4 v2, s[20:23], s55 offen offset:1024 sc1 lds
v_add_f32_e32 v14, v14, v33
Mv 96,24,112
Rb 8,1,51200
v_exp_f32_e32 v27, v75
Mv 100,24,116
v_exp_f32_e32 v36, v68
v_add_f32_e32 v14, v14, v25
Mv 100,28,124
Rb 12,1,52224
v_exp_f32_e32 v28, v69
s_waitcnt lgkmcnt(8)
Mv 96,28,120
v_exp_f32_e32 v37, v70
Cv 33,33,25
Mz 104,32,64
Rb 16,1,57344
v_exp_f32_e32 v29, v71
Mz 108,32,68
v_exp_f32_e32 v38, v76
v_add_f32_e32 v14, v14, v34
Mv 108,36,76
)" R"(
Rb 20,1,58368
v_exp_f32_e32 v24, v77
Mv 104,36,72
buffer_load_dwordx4 v2, s[20:23], s55 offen offset:2048 sc1 lds
v_add_f32_e32 v14, v14, v26
Mv 104,40,80
Rb 24,1,59392
v_exp_f32_e32 v39, v78
Cv 34,34,26
Mv 108,40,84
buffer_store_dwordx4 v[136:139], v3, s[20:23], s46 offen offset:1024 sc0 sc1
v_add_f32_e32 v14, v14, v35
Mv 108,44,92
Rb 28,1,60416
v_add_f32_e32 v14, v14, v27
s_waitcnt lgkmcnt(8)
Mv 104,44,88
v_exp_f32_e32 v25, v79
Cv 35,35,27
Mv 104,48,96
Rb 32,1,53248
v_exp_f32_e32 v40, v80
Mv 108,48,100
v_exp_f32_e32 v26, v81
v_exp_f32_e32 v41, v82
Mv 108,52,108
Rb 36,1,54272
v_exp_f32_e32 v27, v83
Mv 104,52,104
buffer_load_dwordx4 v2, s[20:23], s55 offen offset:3072 sc1 lds
v_add_f32_e32 v15, v36, v28
Mv 104,56,112
Rb 40,1,55296
Cv 36,36,28
v_add_f32_e32 v15, v15, v37
Mv 108,56,116
v_exp_f32_e32 v42, v88
v_add_f32_e32 v15, v15, v29
Mv 108,60,124
Rb 44,1,56320
v_exp_f32_e32 v28, v89
s_waitcnt lgkmcnt(8)
Mv 104,60,120
Cv 37,37,29
v_exp_f32_e32 v43, v90
Mz 112,0,64
Rb 48,1,61440
v_add_f32_e32 v15, v15, v38
v_add_f32_e32 v15, v15, v24
Mz 116,0,68
v_exp_f32_e32 v29, v91
Cv 38,38,24
Mv 116,4,76
Rb 52,1,62464
s_add_u32 m0, s36, 4096
v_add_f32_e32 v15, v15, v39
Mv 112,4,72
buffer_load_dwordx4 v3, s[20:23], s55 offen offset:0 sc1 lds
v_add_f32_e32 v15, v15, v25
Mv 112,8,80
Rb 56,1,63488
v_exp_f32_e32 v44, v84
Cv 39,39,25
Mv 116,8,84
)" R"(
buffer_store_dwordx4 v[144:147], v3, s[20:23], s46 offen offset:2048 sc0 sc1
v_add_f32_e32 v14, v14, v40
Mv 116,12,92
Rb 60,1,64512
v_add_f32_e32 v14, v14, v26
s_waitcnt lgkmcnt(8)
Mv 112,12,88
v_exp_f32_e32 v24, v85
Cv 40,40,26
Mv 112,16,96
v_exp_f32_e32 v45, v86
v_add_f32_e32 v14, v14, v41
Mv 116,16,100
v_exp_f32_e32 v25, v87
v_exp_f32_e32 v46, v92
Mv 116,20,108
v_exp_f32_e32 v26, v93
v_add_f32_e32 v14, v14, v27
Mv 112,20,104
buffer_load_dwordx4 v3, s[20:23], s55 offen offset:1024 sc1 lds
Cv 41,41,27
Mv 112,24,112
v_exp_f32_e32 v47, v94
v_add_f32_e32 v14, v14, v42
Mv 116,24,116
v_exp_f32_e32 v27, v95
v_add_f32_e32 v14, v14, v28
Mv 116,28,124
Cv 42,42,28
v_add_f32_e32 v14, v14, v43
v_add_f32_e32 v14, v14, v29
Cv 43,43,29
Mv 112,28,120
v_add_f32_e32 v15, v15, v44
s_waitcnt lgkmcnt(0)
s_barrier
Mz 120,32,64
Rb 0,8,32768
v_add_f32_e32 v15, v15, v24
Cv 44,44,24
Mz 124,32,68
Rb 4,8,33792
v_add_f32_e32 v15, v15, v45
v_add_f32_e32 v15, v15, v25
Mv 124,36,76
Rb 8,8,34816
Cv 45,45,25
v_add_f32_e32 v15, v15, v46
Mv 120,36,72
Rb 12,8,35840
buffer_load_dwordx4 v3, s[20:23], s55 offen offset:2048 sc1 lds
Mv 120,40,80
Rb 16,8,36864
v_add_f32_e32 v15, v15, v26
Cv 46,46,26
v_add_f32_e32 v15, v15, v47
Mv 124,40,84
Rb 20,8,37888
buffer_store_dwordx4 v[152:155], v3, s[20:23], s46 offen offset:3072 sc0 sc1
Mv 124,44,92
Rb 24,8,38912
v_add_f32_e32 v15, v15, v27
Cv 47,47,27
Mv 120,44,88
Rb 28,8,39936
)" R"(
v_max_f32_e32 v11, v14, v15
v_mov_b32 v24, v14
Mv 120,48,96
Rb 32,8,40960
v_mov_b32 v25, v15
Mv 124,48,100
Rb 36,8,41984
v_permlane32_swap_b32_e32 v24, v25
v_add_f32_e32 v24, v24, v25
v_mov_b32 v25, v24
Mv 124,52,108
Rb 40,8,43008
v_permlane16_swap_b32_e32 v24, v25
v_add_f32_e32 v24, v24, v25
Mv 120,52,104
Rb 44,8,44032
buffer_load_dwordx4 v3, s[20:23], s55 offen offset:3072 sc1 lds
Mv 120,56,112
v_rcp_f32_e32 v24, v24
s_add_u32 s55, s55, 0x8000
Mv 124,56,116
s_cmp_eq_u32 s57, 2
s_cselect_b32 s55, s59, s55
v_max_f32_e32 v11, v11, v24
Mv 124,60,124
v_cmp_nge_f32_e32 vcc, s62, v11
s_waitcnt lgkmcnt(8)
s_cbranch_vccnz .Lzfx6
.Lzfr6:
Mv 120,60,120
v_mov_b32 v12, v14
v_mov_b32 v13, v15
v_mfma_f32_16x16x32_bf16 v[128:131], a[0:3], v[32:35], 0
Rb 48,8,45056
v_mfma_f32_16x16x32_bf16 v[136:139], a[0:3], v[36:39], 0
v_mfma_f32_16x16x32_bf16 v[140:143], a[4:7], v[36:39], 0
Rb 52,8,46080
s_add_u32 m0, s36, 32768
v_mfma_f32_16x16x32_bf16 v[132:135], a[4:7], v[32:35], 0
buffer_load_dwordx4 v2, s[20:23], s55 offen offset:0 sc1 lds
v_mfma_f32_16x16x32_bf16 v[144:147], a[8:11], v[32:35], 0
Rb 56,8,47104
v_mfma_f32_16x16x32_bf16 v[152:155], a[8:11], v[36:39], 0
v_exp_f32_e32 v48, v96
v_mfma_f32_16x16x32_bf16 v[156:159], a[12:15], v[36:39], 0
Rb 60,8,48128
s_waitcnt lgkmcnt(8)
v_mfma_f32_16x16x32_bf16 v[148:151], a[12:15], v[32:35], 0
v_exp_f32_e32 v28, v97
v_mfma_f32_16x16x32_bf16 v[160:163], a[16:19], v[32:35], 0
Rb 0,8,49152
v_add_f32_e32 v14, v48, v28
v_mfma_f32_16x16x32_bf16 v[168:171], a[16:19], v[36:39], 0
v_exp_f32_e32 v49, v98
v_mfma_f32_16x16x32_bf16 v[172:175], a[20:23], v[36:39], 0
Rb 4,8,50176
Cv 48,48,28
v_mfma_f32_16x16x32_bf16 v[164:167], a[20:23], v[32:35], 0
buffer_load_dwordx4 v2, s[20:23], s55 offen offset:1024 sc1 lds
v_mfma_f32_16x16x32_bf16 v[176:179], a[24:27], v[32:35], 0
Rb 8,8,51200
v_add_f32_e32 v14, v14, v49
v_mfma_f32_16x16x32_bf16 v[184:187], a[24:27], v[36:39], 0
v_exp_f32_e32 v29, v99
)" R"(
v_mfma_f32_16x16x32_bf16 v[188:191], a[28:31], v[36:39], 0
Rb 12,8,52224
s_waitcnt lgkmcnt(8)
v_mfma_f32_16x16x32_bf16 v[180:183], a[28:31], v[32:35], 0
v_exp_f32_e32 v50, v104
v_mfma_f32_16x16x32_bf16 v[192:195], a[32:35], v[32:35], 0
Rb 16,8,53248
v_add_f32_e32 v14, v14, v29
v_mfma_f32_16x16x32_bf16 v[200:203], a[32:35], v[36:39], 0
v_exp_f32_e32 v24, v105
v_mfma_f32_16x16x32_bf16 v[204:207], a[36:39], v[36:39], 0
Rb 20,8,54272
Cv 49,49,29
v_mfma_f32_16x16x32_bf16 v[196:199], a[36:39], v[32:35], 0
buffer_load_dwordx4 v2, s[20:23], s55 offen offset:2048 sc1 lds
v_mfma_f32_16x16x32_bf16 v[208:211], a[40:43], v[32:35], 0
Rb 24,8,55296
v_add_f32_e32 v14, v14, v50
v_mfma_f32_16x16x32_bf16 v[216:219], a[40:43], v[36:39], 0
v_exp_f32_e32 v51, v106
v_mfma_f32_16x16x32_bf16 v[220:223], a[44:47], v[36:39], 0
Rb 28,8,56320
s_waitcnt lgkmcnt(8)
v_mfma_f32_16x16x32_bf16 v[212:215], a[44:47], v[32:35], 0
v_exp_f32_e32 v25, v107
v_mfma_f32_16x16x32_bf16 v[224:227], a[48:51], v[32:35], 0
Rb 32,8,57344
v_add_f32_e32 v14, v14, v24
v_mfma_f32_16x16x32_bf16 v[232:235], a[48:51], v[36:39], 0
v_exp_f32_e32 v52, v100
v_mfma_f32_16x16x32_bf16 v[236:239], a[52:55], v[36:39], 0
Rb 36,8,58368
Cv 50,50,24
v_mfma_f32_16x16x32_bf16 v[228:231], a[52:55], v[32:35], 0
buffer_load_dwordx4 v2, s[20:23], s55 offen offset:3072 sc1 lds
v_mfma_f32_16x16x32_bf16 v[240:243], a[56:59], v[32:35], 0
Rb 40,8,59392
v_add_f32_e32 v14, v14, v51
v_mfma_f32_16x16x32_bf16 v[248:251], a[56:59], v[36:39], 0
v_exp_f32_e32 v26, v101
v_mfma_f32_16x16x32_bf16 v[252:255], a[60:63], v[36:39], 0
Rb 44,8,60416
s_waitcnt lgkmcnt(8)
v_mfma_f32_16x16x32_bf16 v[244:247], a[60:63], v[32:35], 0
v_exp_f32_e32 v53, v102
Mp 128,0,40
Rb 48,8,61440
v_add_f32_e32 v14, v14, v25
Mp 136,0,44
v_exp_f32_e32 v27, v103
Mp 140,4,44
Rb 52,8,62464
s_add_u32 m0, s36, 36864
Mp 132,4,40
buffer_load_dwordx4 v3, s[20:23], s55 offen offset:0 sc1 lds
Mp 144,8,40
Rb 56,8,63488
Cv 51,51,25
Mp 152,8,44
v_exp_f32_e32 v54, v108
Mp 156,12,44
Rb 60,8,64512
s_waitcnt lgkmcnt(8)
Mp 148,12,40
)" R"(
v_exp_f32_e32 v28, v109
Mp 160,16,40
v_exp_f32_e32 v55, v110
Mp 168,16,44
v_exp_f32_e32 v29, v111
Mp 172,20,44
v_add_f32_e32 v15, v52, v26
Cv 52,52,26
Mp 164,20,40
buffer_load_dwordx4 v3, s[20:23], s55 offen offset:1024 sc1 lds
Mp 176,24,40
v_add_f32_e32 v15, v15, v53
v_add_f32_e32 v15, v15, v27
Mp 184,24,44
Cv 53,53,27
v_add_f32_e32 v15, v15, v54
Mp 188,28,44
v_add_f32_e32 v15, v15, v28
Cv 54,54,28
Mp 180,28,40
s_waitcnt vmcnt(6) lgkmcnt(0)
s_barrier
buffer_store_dword v6, v7, s[20:23], s49 offen offset:4 sc0 sc1
Mp 192,32,40
Rb 0,1,0
v_add_f32_e32 v15, v15, v55
Mp 200,32,44
Rb 4,1,1024
v_add_f32_e32 v15, v15, v29
Mp 204,36,44
Rb 8,1,2048
Cv 55,55,29
Mp 196,36,40
Rb 12,1,3072
buffer_load_dwordx4 v3, s[20:23], s55 offen offset:2048 sc1 lds
Mp 208,40,40
Rb 16,1,8192
s_sub_u32 s57, s57, 1
Mp 216,40,44
Rb 20,1,9216
Mp 220,44,44
Rb 24,1,10240
Mp 212,44,40
Rb 28,1,11264
Mp 224,48,40
Rb 32,1,4096
Mp 232,48,44
Rb 36,1,5120
Mp 236,52,44
Rb 40,1,6144
Mp 228,52,40
Rb 44,1,7168
buffer_load_dwordx4 v3, s[20:23], s55 offen offset:3072 sc1 lds
Mp 240,56,40
v_cmp_eq_u32_e32 vcc, s3, v4
s_andn2_b64 s[28:29], s[60:61], vcc
s_cbranch_scc1 .Lzsp12
.Lzok12:
Mp 248,56,44
Mp 252,60,44
s_waitcnt lgkmcnt(8)
Mp 244,60,40
v_mfma_f32_16x16x32_bf16 v[64:67], a[0:3], a[64:67], v[16:19]
Rb 48,1,12288
v_mfma_f32_16x16x32_bf16 v[68:71], a[0:3], a[68:71], v[20:23]
)" R"(
v_exp_f32_e32 v56, v112
Mv 68,4,76
Rb 52,1,13312
s_add_u32 m0, s36, 98304
Mv 64,4,72
buffer_load_dwordx4 v2, s[20:23], s56 offen offset:0 sc1 lds
Mv 64,8,80
Rb 56,1,14336
v_add_f32_e32 v14, v14, v56
Mv 68,8,84
buffer_store_dwordx4 a[224:227], v2, s[20:23], s47 offen offset:0 sc0 sc1
Mv 68,12,92
Rb 60,1,15360
s_waitcnt lgkmcnt(8)
Mv 64,12,88
v_exp_f32_e32 v24, v113
Mv 64,16,96
Rb 0,1,16384
v_add_f32_e32 v14, v14, v24
Mv 68,16,100
v_exp_f32_e32 v57, v114
Mv 68,20,108
Rb 4,1,17408
Cv 56,56,24
Mv 64,20,104
buffer_load_dwordx4 v2, s[20:23], s56 offen offset:1024 sc1 lds
Mv 64,24,112
Rb 8,1,18432
v_add_f32_e32 v14, v14, v57
Mv 68,24,116
v_exp_f32_e32 v25, v115
Mv 68,28,124
Rb 12,1,19456
s_waitcnt lgkmcnt(8)
Mv 64,28,120
v_exp_f32_e32 v58, v120
v_mfma_f32_16x16x32_bf16 v[72:75], a[32:35], a[64:67], v[16:19]
Rb 16,1,24576
v_add_f32_e32 v14, v14, v25
v_mfma_f32_16x16x32_bf16 v[76:79], a[32:35], a[68:71], v[20:23]
v_exp_f32_e32 v26, v121
Mv 76,36,76
Rb 20,1,25600
Cv 57,57,25
Mv 72,36,72
buffer_load_dwordx4 v2, s[20:23], s56 offen offset:2048 sc1 lds
Mv 72,40,80
Rb 24,1,26624
v_add_f32_e32 v14, v14, v58
Mv 76,40,84
v_exp_f32_e32 v59, v122
Mv 76,44,92
Rb 28,1,27648
s_waitcnt lgkmcnt(8)
Mv 72,44,88
v_exp_f32_e32 v27, v123
Mv 72,48,96
Rb 32,1,20480
v_add_f32_e32 v14, v14, v26
Mv 76,48,100
v_exp_f32_e32 v60, v116
Mv 76,52,108
Rb 36,1,21504
Cv 58,58,26
)" R"(
Mv 72,52,104
buffer_load_dwordx4 v2, s[20:23], s56 offen offset:3072 sc1 lds
Mv 72,56,112
Rb 40,1,22528
v_add_f32_e32 v14, v14, v59
Mv 76,56,116
buffer_store_dwordx4 a[228:231], v2, s[20:23], s47 offen offset:1024 sc0 sc1
Mv 76,60,124
Rb 44,1,23552
s_waitcnt lgkmcnt(8)
Mv 72,60,120
v_exp_f32_e32 v28, v117
v_mfma_f32_16x16x32_bf16 v[80:83], a[0:3], a[64:67], v[16:19]
Rb 48,1,28672
v_add_f32_e32 v14, v14, v27
v_mfma_f32_16x16x32_bf16 v[84:87], a[0:3], a[68:71], v[20:23]
v_exp_f32_e32 v61, v118
Mv 84,4,76
Rb 52,1,29696
s_add_u32 m0, s36, 102400
Mv 80,4,72
buffer_load_dwordx4 v3, s[20:23], s56 offen offset:0 sc1 lds
Mv 80,8,80
Rb 56,1,30720
Cv 59,59,27
Mv 84,8,84
v_exp_f32_e32 v29, v119
Mv 84,12,92
Rb 60,1,31744
s_waitcnt lgkmcnt(8)
Mv 80,12,88
v_exp_f32_e32 v62, v124
Mv 80,16,96
v_exp_f32_e32 v24, v125
Mv 84,16,100
v_exp_f32_e32 v63, v126
Mv 84,20,108
v_exp_f32_e32 v25, v127
Mv 80,20,104
buffer_load_dwordx4 v3, s[20:23], s56 offen offset:1024 sc1 lds
Mv 80,24,112
v_add_f32_e32 v15, v15, v60
v_add_f32_e32 v15, v15, v28
Mv 84,24,116
Cv 60,60,28
v_add_f32_e32 v15, v15, v61
Mv 84,28,124
v_add_f32_e32 v15, v15, v29
Cv 61,61,29
Mv 80,28,120
s_waitcnt lgkmcnt(0)
s_barrier
v_mfma_f32_16x16x32_bf16 v[88:91], a[32:35], a[64:67], v[16:19]
Rb 0,8,0
v_add_f32_e32 v15, v15, v62
v_mfma_f32_16x16x32_bf16 v[92:95], a[32:35], a[68:71], v[20:23]
Rb 4,8,1024
v_add_f32_e32 v15, v15, v24
Mv 92,36,76
Rb 8,8,2048
Cv 62,62,24
Mv 88,36,72
Rb 12,8,3072
buffer_load_dwordx4 v3, s[20:23], s56 offen offset:2048 sc1 lds
Mv 88,40,80
)" R"(
Rb 16,8,4096
v_add_f32_e32 v15, v15, v63
Mv 92,40,84
Rb 20,8,5120
buffer_store_dwordx4 a[232:235], v2, s[20:23], s47 offen offset:2048 sc0 sc1
Mv 92,44,92
Rb 24,8,6144
v_add_f32_e32 v15, v15, v25
Mv 88,44,88
Rb 28,8,7168
Cv 63,63,25
Mv 88,48,96
Rb 32,8,8192
v_max_f32_e32 v11, v14, v15
Mv 92,48,100
Rb 36,8,9216
v_cmp_nge_f32_e32 vcc, s62, v11
Mv 92,52,108
Rb 40,8,10240
s_add_u32 s55, s55, 0x8000
Mv 88,52,104
Rb 44,8,11264
buffer_load_dwordx4 v3, s[20:23], s56 offen offset:3072 sc1 lds
Mv 88,56,112
s_cmp_eq_u32 s57, 2
s_cselect_b32 s55, s59, s55
Mv 92,56,116
s_add_u32 s56, s56, 0x8000
Mv 92,60,124
s_waitcnt lgkmcnt(8)
s_cbranch_vccnz .Lzfx7
.Lzfr7:
Mv 88,60,120
v_add_f32_e32 v12, v12, v14
v_add_f32_e32 v13, v13, v15
Mp 128,0,48
Rb 48,8,12288
Mp 136,0,52
s_cmp_le_i32 s57, s58
s_cbranch_scc1 .Lzmk6
.Lzmr6:
Mp 140,4,52
Rb 52,8,13312
s_add_u32 m0, s36, 0
Mp 132,4,48
buffer_load_dwordx4 v2, s[20:23], s55 offen offset:0 sc1 lds
Mp 144,8,48
Rb 56,8,14336
Mp 152,8,52
v_exp_f32_e32 v32, v64
Mp 156,12,52
Rb 60,8,15360
s_waitcnt lgkmcnt(8)
Mp 148,12,48
v_exp_f32_e32 v26, v65
Mp 160,16,48
Rb 0,8,16384
v_add_f32_e32 v14, v32, v26
Mp 168,16,52
v_exp_f32_e32 v33, v66
Mp 172,20,52
Rb 4,8,17408
Cv 32,32,26
Mp 164,20,48
)" R"(
buffer_load_dwordx4 v2, s[20:23], s55 offen offset:1024 sc1 lds
Mp 176,24,48
Rb 8,8,18432
v_add_f32_e32 v14, v14, v33
Mp 184,24,52
buffer_store_dwordx4 a[236:239], v2, s[20:23], s47 offen offset:3072 sc0 sc1
Mp 188,28,52
Rb 12,8,19456
s_waitcnt lgkmcnt(8)
Mp 180,28,48
v_exp_f32_e32 v27, v67
Mp 192,32,48
Rb 16,8,20480
v_add_f32_e32 v14, v14, v27
Mp 200,32,52
v_exp_f32_e32 v34, v72
Mp 204,36,52
Rb 20,8,21504
Cv 33,33,27
Mp 196,36,48
buffer_load_dwordx4 v2, s[20:23], s55 offen offset:2048 sc1 lds
Mp 208,40,48
Rb 24,8,22528
v_add_f32_e32 v14, v14, v34
Mp 216,40,52
v_exp_f32_e32 v28, v73
Mp 220,44,52
Rb 28,8,23552
s_waitcnt lgkmcnt(8)
Mp 212,44,48
v_exp_f32_e32 v35, v74
Mp 224,48,48
Rb 32,8,24576
v_add_f32_e32 v14, v14, v28
Mp 232,48,52
v_exp_f32_e32 v29, v75
Mp 236,52,52
Rb 36,8,25600
Cv 34,34,28
Mp 228,52,48
buffer_load_dwordx4 v2, s[20:23], s55 offen offset:3072 sc1 lds
Mp 240,56,48
Rb 40,8,26624
v_add_f32_e32 v14, v14, v35
Mp 248,56,52
v_exp_f32_e32 v36, v68
Mp 252,60,52
Rb 44,8,27648
s_waitcnt lgkmcnt(8)
Mp 244,60,48
v_exp_f32_e32 v24, v69
Mp 128,0,56
Rb 48,8,28672
v_add_f32_e32 v14, v14, v29
Mp 136,0,60
v_exp_f32_e32 v37, v70
Mp 140,4,60
Rb 52,8,29696
s_add_u32 m0, s36, 4096
Mp 132,4,56
buffer_load_dwordx4 v3, s[20:23], s55 offen offset:0 sc1 lds
Mp 144,8,56
Rb 56,8,30720
Cv 35,35,29
)" R"(
Mp 152,8,60
buffer_store_dwordx4 a[240:243], v3, s[20:23], s47 offen offset:0 sc0 sc1
Mp 156,12,60
Rb 60,8,31744
s_waitcnt lgkmcnt(8)
Mp 148,12,56
v_exp_f32_e32 v25, v71
Mp 160,16,56
v_exp_f32_e32 v38, v76
Mp 168,16,60
v_exp_f32_e32 v26, v77
Mp 172,20,60
v_exp_f32_e32 v39, v78
Mp 164,20,56
buffer_load_dwordx4 v3, s[20:23], s55 offen offset:1024 sc1 lds
Mp 176,24,56
v_exp_f32_e32 v27, v79
Mp 184,24,60
v_add_f32_e32 v15, v36, v24
Cv 36,36,24
Mp 188,28,60
v_add_f32_e32 v15, v15, v37
v_add_f32_e32 v15, v15, v25
Mp 180,28,56
s_waitcnt vmcnt(19) lgkmcnt(0)
s_barrier
Mp 192,32,56
Rb 0,1,32768
Cv 37,37,25
Mp 200,32,60
Rb 4,1,33792
v_add_f32_e32 v15, v15, v38
Mp 204,36,60
Rb 8,1,34816
v_add_f32_e32 v15, v15, v26
Mp 196,36,56
Rb 12,1,35840
buffer_load_dwordx4 v3, s[20:23], s55 offen offset:2048 sc1 lds
Mp 208,40,56
Rb 16,1,40960
Cv 38,38,26
Mp 216,40,60
Rb 20,1,41984
v_add_f32_e32 v15, v15, v39
Mp 220,44,60
Rb 24,1,43008
v_add_f32_e32 v15, v15, v27
Mp 212,44,56
Rb 28,1,44032
Cv 39,39,27
Mp 224,48,56
Rb 32,1,36864
s_sub_u32 s57, s57, 1
Mp 232,48,60
Rb 36,1,37888
Mp 236,52,60
Rb 40,1,38912
Mp 228,52,56
Rb 44,1,39936
buffer_load_dwordx4 v3, s[20:23], s55 offen offset:3072 sc1 lds
Mp 240,56,56
Mp 248,56,60
buffer_store_dwordx4 a[244:247], v3, s[20:23], s47 offen offset:1024 sc0 sc1
Mp 252,60,60
s_waitcnt lgkmcnt(8)
)" R"(
Mp 244,60,56
v_mfma_f32_16x16x32_bf16 v[96:99], a[0:3], a[64:67], v[16:19]
Rb 48,1,45056
buffer_load_dword v4, v5, s[20:23], s50 offen offset:0 sc0 sc1
v_mfma_f32_16x16x32_bf16 v[100:103], a[0:3], a[68:71], v[20:23]
s_lshl_b64 s[60:61], s[60:61], 8
s_or_b32 s60, s60, 0xff
Mv 100,4,76
Rb 52,1,46080
s_add_u32 m0, s36, 65536
Mv 96,4,72
buffer_load_dwordx4 v2, s[20:23], s56 offen offset:0 sc1 lds
Mv 96,8,80
Rb 56,1,47104
Mv 100,8,84
v_exp_f32_e32 v40, v80
Mv 100,12,92
Rb 60,1,48128
s_waitcnt lgkmcnt(8)
Mv 96,12,88
v_exp_f32_e32 v28, v81
Mv 96,16,96
Rb 0,1,49152
v_add_f32_e32 v14, v14, v40
Mv 100,16,100
v_exp_f32_e32 v41, v82
Mv 100,20,108
Rb 4,1,50176
v_add_f32_e32 v14, v14, v28
Mv 96,20,104
buffer_load_dwordx4 v2, s[20:23], s56 offen offset:1024 sc1 lds
Mv 96,24,112
Rb 8,1,51200
Cv 40,40,28
Mv 100,24,116
v_exp_f32_e32 v29, v83
Mv 100,28,124
Rb 12,1,52224
s_waitcnt lgkmcnt(8)
Mv 96,28,120
v_exp_f32_e32 v42, v88
v_add_f32_e32 v14, v14, v41
v_mfma_f32_16x16x32_bf16 v[104:107], a[32:35], a[64:67], v[16:19]
Rb 16,1,57344
v_add_f32_e32 v14, v14, v29
v_mfma_f32_16x16x32_bf16 v[108:111], a[32:35], a[68:71], v[20:23]
v_exp_f32_e32 v24, v89
Mv 108,36,76
Rb 20,1,58368
Cv 41,41,29
Mv 104,36,72
buffer_load_dwordx4 v2, s[20:23], s56 offen offset:2048 sc1 lds
Mv 104,40,80
Rb 24,1,59392
v_add_f32_e32 v14, v14, v42
Mv 108,40,84
buffer_store_dwordx4 a[248:251], v3, s[20:23], s47 offen offset:2048 sc0 sc1
Mv 108,44,92
Rb 28,1,60416
s_waitcnt lgkmcnt(8)
Mv 104,44,88
v_exp_f32_e32 v43, v90
Mv 104,48,96
Rb 32,1,53248
)" R"(
v_add_f32_e32 v14, v14, v24
Mv 108,48,100
v_exp_f32_e32 v25, v91
Mv 108,52,108
Rb 36,1,54272
Cv 42,42,24
Mv 104,52,104
buffer_load_dwordx4 v2, s[20:23], s56 offen offset:3072 sc1 lds
Mv 104,56,112
Rb 40,1,55296
v_add_f32_e32 v14, v14, v43
Mv 108,56,116
v_exp_f32_e32 v44, v84
Mv 108,60,124
Rb 44,1,56320
s_waitcnt lgkmcnt(8)
Mv 104,60,120
v_exp_f32_e32 v26, v85
v_add_f32_e32 v14, v14, v25
v_mfma_f32_16x16x32_bf16 v[112:115], a[0:3], a[64:67], v[16:19]
Rb 48,1,61440
Cv 43,43,25
v_mfma_f32_16x16x32_bf16 v[116:119], a[0:3], a[68:71], v[20:23]
v_exp_f32_e32 v45, v86
Mv 116,4,76
Rb 52,1,62464
s_add_u32 m0, s36, 69632
Mv 112,4,72
buffer_load_dwordx4 v3, s[20:23], s56 offen offset:0 sc1 lds
Mv 112,8,80
Rb 56,1,63488
v_add_f32_e32 v15, v15, v44
Mv 116,8,84
v_exp_f32_e32 v27, v87
Mv 116,12,92
Rb 60,1,64512
s_waitcnt lgkmcnt(8)
Mv 112,12,88
v_exp_f32_e32 v46, v92
Mv 112,16,96
v_exp_f32_e32 v28, v93
Mv 116,16,100
v_exp_f32_e32 v47, v94
Mv 116,20,108
v_exp_f32_e32 v29, v95
Mv 112,20,104
buffer_load_dwordx4 v3, s[20:23], s56 offen offset:1024 sc1 lds
Mv 112,24,112
v_add_f32_e32 v15, v15, v26
Cv 44,44,26
Mv 116,24,116
buffer_store_dwordx4 a[252:255], v3, s[20:23], s47 offen offset:3072 sc0 sc1
Mv 116,28,124
v_add_f32_e32 v15, v15, v45
v_add_f32_e32 v15, v15, v27
Mv 112,28,120
Cv 45,45,27
s_waitcnt vmcnt(20) lgkmcnt(0)
s_barrier
v_mfma_f32_16x16x32_bf16 v[120:123], a[32:35], a[64:67], v[16:19]
Rb 0,8,32768
v_add_f32_e32 v15, v15, v46
v_mfma_f32_16x16x32_bf16 v[124:127], a[32:35], a[68:71], v[20:23]
Rb 4,8,33792
v_add_f32_e32 v15, v15, v28
)" R"(
Mv 124,36,76
Rb 8,8,34816
Cv 46,46,28
Mv 120,36,72
Rb 12,8,35840
buffer_load_dwordx4 v3, s[20:23], s56 offen offset:2048 sc1 lds
Mv 120,40,80
Rb 16,8,36864
v_add_f32_e32 v15, v15, v47
Mv 124,40,84
Rb 20,8,37888
v_add_f32_e32 v15, v15, v29
Mv 124,44,92
Rb 24,8,38912
Cv 47,47,29
Mv 120,44,88
Rb 28,8,39936
v_max_f32_e32 v11, v14, v15
Mv 120,48,96
Rb 32,8,40960
v_cmp_nge_f32_e32 vcc, s62, v11
Mv 124,48,100
Rb 36,8,41984
s_add_u32 s55, s55, 0x8000
Mv 124,52,108
Rb 40,8,43008
Mv 120,52,104
Rb 44,8,44032
buffer_load_dwordx4 v3, s[20:23], s56 offen offset:3072 sc1 lds
Mv 120,56,112
s_cmp_eq_u32 s57, 2
s_cselect_b32 s55, s59, s55
Mv 124,56,116
s_add_u32 s56, s56, 0x8000
Mv 124,60,124
s_waitcnt lgkmcnt(8)
s_cbranch_vccnz .Lzfx8
.Lzfr8:
Mv 120,60,120
v_add_f32_e32 v12, v12, v14
v_add_f32_e32 v13, v13, v15
Mp 128,0,32
Rb 48,8,45056
Mp 136,0,36
s_cmp_le_i32 s57, s58
s_cbranch_scc1 .Lzmk7
.Lzmr7:
Mp 140,4,36
Rb 52,8,46080
s_add_u32 m0, s36, 32768
Mp 132,4,32
buffer_load_dwordx4 v2, s[20:23], s55 offen offset:0 sc1 lds
Mp 144,8,32
Rb 56,8,47104
Mp 152,8,36
v_exp_f32_e32 v48, v96
Mp 156,12,36
Rb 60,8,48128
s_waitcnt lgkmcnt(8)
Mp 148,12,32
v_exp_f32_e32 v24, v97
Mp 160,16,32
Rb 0,8,49152
v_add_f32_e32 v14, v48, v24
)" R"(
Mp 168,16,36
v_exp_f32_e32 v49, v98
Mp 172,20,36
Rb 4,8,50176
Cv 48,48,24
Mp 164,20,32
buffer_load_dwordx4 v2, s[20:23], s55 offen offset:1024 sc1 lds
Mp 176,24,32
Rb 8,8,51200
v_add_f32_e32 v14, v14, v49
Mp 184,24,36
v_exp_f32_e32 v25, v99
Mp 188,28,36
Rb 12,8,52224
s_waitcnt lgkmcnt(8)
Mp 180,28,32
v_exp_f32_e32 v50, v104
Mp 192,32,32
Rb 16,8,53248
v_add_f32_e32 v14, v14, v25
Mp 200,32,36
v_exp_f32_e32 v26, v105
Mp 204,36,36
Rb 20,8,54272
Cv 49,49,25
Mp 196,36,32
buffer_load_dwordx4 v2, s[20:23], s55 offen offset:2048 sc1 lds
Mp 208,40,32
Rb 24,8,55296
v_add_f32_e32 v14, v14, v50
Mp 216,40,36
v_exp_f32_e32 v51, v106
Mp 220,44,36
Rb 28,8,56320
s_waitcnt lgkmcnt(8)
Mp 212,44,32
v_exp_f32_e32 v27, v107
Mp 224,48,32
Rb 32,8,57344
v_add_f32_e32 v14, v14, v26
Mp 232,48,36
v_exp_f32_e32 v52, v100
Mp 236,52,36
Rb 36,8,58368
Cv 50,50,26
Mp 228,52,32
buffer_load_dwordx4 v2, s[20:23], s55 offen offset:3072 sc1 lds
Mp 240,56,32
Rb 40,8,59392
v_add_f32_e32 v14, v14, v51
Mp 248,56,36
v_exp_f32_e32 v28, v101
Mp 252,60,36
Rb 44,8,60416
s_waitcnt lgkmcnt(8)
Mp 244,60,32
v_exp_f32_e32 v53, v102
Mp 128,0,40
Rb 48,8,61440
v_add_f32_e32 v14, v14, v27
Mp 136,0,44
v_exp_f32_e32 v29, v103
Mp 140,4,44
Rb 52,8,62464
)" R"(
s_add_u32 m0, s36, 36864
Mp 132,4,40
buffer_load_dwordx4 v3, s[20:23], s55 offen offset:0 sc1 lds
Mp 144,8,40
Rb 56,8,63488
Cv 51,51,27
Mp 152,8,44
v_exp_f32_e32 v54, v108
Mp 156,12,44
Rb 60,8,64512
s_waitcnt lgkmcnt(8)
Mp 148,12,40
v_exp_f32_e32 v24, v109
Mp 160,16,40
v_exp_f32_e32 v55, v110
Mp 168,16,44
v_exp_f32_e32 v25, v111
Mp 172,20,44
v_add_f32_e32 v15, v52, v28
Cv 52,52,28
Mp 164,20,40
buffer_load_dwordx4 v3, s[20:23], s55 offen offset:1024 sc1 lds
Mp 176,24,40
v_add_f32_e32 v15, v15, v53
v_add_f32_e32 v15, v15, v29
Mp 184,24,44
Cv 53,53,29
v_add_f32_e32 v15, v15, v54
Mp 188,28,44
v_add_f32_e32 v15, v15, v24
Cv 54,54,24
Mp 180,28,40
s_waitcnt vmcnt(8) lgkmcnt(0)
s_barrier
buffer_store_dword v6, v7, s[20:23], s49 offen offset:8 sc0 sc1
Mp 192,32,40
Rb 0,1,0
v_add_f32_e32 v15, v15, v55
Mp 200,32,44
Rb 4,1,1024
v_add_f32_e32 v15, v15, v25
Mp 204,36,44
Rb 8,1,2048
Cv 55,55,25
Mp 196,36,40
Rb 12,1,3072
buffer_load_dwordx4 v3, s[20:23], s55 offen offset:2048 sc1 lds
Mp 208,40,40
Rb 16,1,8192
s_sub_u32 s57, s57, 1
Mp 216,40,44
Rb 20,1,9216
Mp 220,44,44
Rb 24,1,10240
Mp 212,44,40
Rb 28,1,11264
Mp 224,48,40
Rb 32,1,4096
Mp 232,48,44
Rb 36,1,5120
Mp 236,52,44
Rb 40,1,6144
Mp 228,52,40
Rb 44,1,7168
buffer_load_dwordx4 v3, s[20:23], s55 offen offset:3072 sc1 lds
)" R"(
Mp 240,56,40
v_cmp_eq_u32_e32 vcc, s3, v4
s_andn2_b64 s[28:29], s[60:61], vcc
s_cbranch_scc1 .Lzsp13
.Lzok13:
Mp 248,56,44
Mp 252,60,44
s_waitcnt lgkmcnt(8)
Mp 244,60,40
v_mfma_f32_16x16x32_bf16 v[64:67], a[0:3], a[64:67], v[16:19]
Rb 48,1,12288
v_mfma_f32_16x16x32_bf16 v[68:71], a[0:3], a[68:71], v[20:23]
v_exp_f32_e32 v56, v112
Mv 68,4,76
Rb 52,1,13312
s_add_u32 m0, s36, 98304
Mv 64,4,72
buffer_load_dwordx4 v2, s[20:23], s56 offen offset:0 sc1 lds
Mv 64,8,80
Rb 56,1,14336
v_add_f32_e32 v14, v14, v56
Mv 68,8,84
buffer_store_dwordx4 a[128:131], v2, s[20:23], s48 offen offset:0 sc0 sc1
Mv 68,12,92
Rb 60,1,15360
s_waitcnt lgkmcnt(8)
Mv 64,12,88
v_exp_f32_e32 v26, v113
Mv 64,16,96
Rb 0,1,16384
v_add_f32_e32 v14, v14, v26
Mv 68,16,100
v_exp_f32_e32 v57, v114
Mv 68,20,108
Rb 4,1,17408
Cv 56,56,26
Mv 64,20,104
buffer_load_dwordx4 v2, s[20:23], s56 offen offset:1024 sc1 lds
Mv 64,24,112
Rb 8,1,18432
v_add_f32_e32 v14, v14, v57
Mv 68,24,116
v_exp_f32_e32 v27, v115
Mv 68,28,124
Rb 12,1,19456
s_waitcnt lgkmcnt(8)
Mv 64,28,120
v_exp_f32_e32 v58, v120
v_mfma_f32_16x16x32_bf16 v[72:75], a[32:35], a[64:67], v[16:19]
Rb 16,1,24576
v_add_f32_e32 v14, v14, v27
v_mfma_f32_16x16x32_bf16 v[76:79], a[32:35], a[68:71], v[20:23]
v_exp_f32_e32 v28, v121
Mv 76,36,76
Rb 20,1,25600
Cv 57,57,27
Mv 72,36,72
buffer_load_dwordx4 v2, s[20:23], s56 offen offset:2048 sc1 lds
Mv 72,40,80
Rb 24,1,26624
v_add_f32_e32 v14, v14, v58
Mv 76,40,84
v_exp_f32_e32 v59, v122
Mv 76,44,92
)" R"(
Rb 28,1,27648
s_waitcnt lgkmcnt(8)
Mv 72,44,88
v_exp_f32_e32 v29, v123
Mv 72,48,96
Rb 32,1,20480
v_add_f32_e32 v14, v14, v28
Mv 76,48,100
v_exp_f32_e32 v60, v116
Mv 76,52,108
Rb 36,1,21504
Cv 58,58,28
Mv 72,52,104
buffer_load_dwordx4 v2, s[20:23], s56 offen offset:3072 sc1 lds
Mv 72,56,112
Rb 40,1,22528
v_add_f32_e32 v14, v14, v59
Mv 76,56,116
buffer_store_dwordx4 a[132:135], v2, s[20:23], s48 offen offset:1024 sc0 sc1
Mv 76,60,124
Rb 44,1,23552
s_waitcnt lgkmcnt(8)
Mv 72,60,120
v_exp_f32_e32 v24, v117
v_mfma_f32_16x16x32_bf16 v[80:83], a[0:3], a[64:67], v[16:19]
Rb 48,1,28672
v_add_f32_e32 v14, v14, v29
v_mfma_f32_16x16x32_bf16 v[84:87], a[0:3], a[68:71], v[20:23]
v_exp_f32_e32 v61, v118
Mv 84,4,76
Rb 52,1,29696
s_add_u32 m0, s36, 102400
Mv 80,4,72
buffer_load_dwordx4 v3, s[20:23], s56 offen offset:0 sc1 lds
Mv 80,8,80
Rb 56,1,30720
Cv 59,59,29
Mv 84,8,84
v_exp_f32_e32 v25, v119
Mv 84,12,92
Rb 60,1,31744
s_waitcnt lgkmcnt(8)
Mv 80,12,88
v_exp_f32_e32 v62, v124
Mv 80,16,96
v_exp_f32_e32 v26, v125
Mv 84,16,100
v_exp_f32_e32 v63, v126
Mv 84,20,108
v_exp_f32_e32 v27, v127
Mv 80,20,104
buffer_load_dwordx4 v3, s[20:23], s56 offen offset:1024 sc1 lds
Mv 80,24,112
v_add_f32_e32 v15, v15, v60
v_add_f32_e32 v15, v15, v24
Mv 84,24,116
Cv 60,60,24
v_add_f32_e32 v15, v15, v61
Mv 84,28,124
v_add_f32_e32 v15, v15, v25
Cv 61,61,25
Mv 80,28,120
s_waitcnt vmcnt(17) lgkmcnt(0)
s_barrier
v_mfma_f32_16x16x32_bf16 v[88:91], a[32:35], a[64:67], v[16:19]
)" R"(
Rb 0,8,0
v_add_f32_e32 v15, v15, v62
v_mfma_f32_16x16x32_bf16 v[92:95], a[32:35], a[68:71], v[20:23]
Rb 4,8,1024
v_add_f32_e32 v15, v15, v26
Mv 92,36,76
Rb 8,8,2048
Cv 62,62,26
Mv 88,36,72
Rb 12,8,3072
buffer_load_dwordx4 v3, s[20:23], s56 offen offset:2048 sc1 lds
Mv 88,40,80
Rb 16,8,4096
v_add_f32_e32 v15, v15, v63
Mv 92,40,84
Rb 20,8,5120
buffer_store_dwordx4 a[136:139], v2, s[20:23], s48 offen offset:2048 sc0 sc1
Mv 92,44,92
Rb 24,8,6144
v_add_f32_e32 v15, v15, v27
Mv 88,44,88
Rb 28,8,7168
Cv 63,63,27
Mv 88,48,96
Rb 32,8,8192
v_max_f32_e32 v11, v14, v15
Mv 92,48,100
Rb 36,8,9216
v_cmp_nge_f32_e32 vcc, s62, v11
Mv 92,52,108
Rb 40,8,10240
s_add_u32 s55, s55, 0x8000
Mv 88,52,104
Rb 44,8,11264
buffer_load_dwordx4 v3, s[20:23], s56 offen offset:3072 sc1 lds
Mv 88,56,112
s_cmp_eq_u32 s57, 2
s_cselect_b32 s55, s59, s55
Mv 92,56,116
s_add_u32 s56, s56, 0x8000
Mv 92,60,124
s_waitcnt lgkmcnt(8)
s_cbranch_vccnz .Lzfx9
.Lzfr9:
Mv 88,60,120
v_add_f32_e32 v12, v12, v14
v_add_f32_e32 v13, v13, v15
Mp 128,0,48
Rb 48,8,12288
Mp 136,0,52
s_cmp_le_i32 s57, s58
s_cbranch_scc1 .Lzmk8
.Lzmr8:
Mp 140,4,52
Rb 52,8,13312
s_add_u32 m0, s36, 0
Mp 132,4,48
buffer_load_dwordx4 v2, s[20:23], s55 offen offset:0 sc1 lds
Mp 144,8,48
Rb 56,8,14336
Mp 152,8,52
v_exp_f32_e32 v32, v64
Mp 156,12,52
Rb 60,8,15360
)" R"(
s_waitcnt lgkmcnt(8)
Mp 148,12,48
v_exp_f32_e32 v28, v65
Mp 160,16,48
Rb 0,8,16384
v_add_f32_e32 v14, v32, v28
Mp 168,16,52
v_exp_f32_e32 v33, v66
Mp 172,20,52
Rb 4,8,17408
Cv 32,32,28
Mp 164,20,48
buffer_load_dwordx4 v2, s[20:23], s55 offen offset:1024 sc1 lds
Mp 176,24,48
Rb 8,8,18432
v_add_f32_e32 v14, v14, v33
Mp 184,24,52
buffer_store_dwordx4 a[140:143], v2, s[20:23], s48 offen offset:3072 sc0 sc1
Mp 188,28,52
Rb 12,8,19456
s_waitcnt lgkmcnt(8)
Mp 180,28,48
v_exp_f32_e32 v29, v67
Mp 192,32,48
Rb 16,8,20480
v_add_f32_e32 v14, v14, v29
Mp 200,32,52
v_exp_f32_e32 v34, v72
Mp 204,36,52
Rb 20,8,21504
Cv 33,33,29
Mp 196,36,48
buffer_load_dwordx4 v2, s[20:23], s55 offen offset:2048 sc1 lds
Mp 208,40,48
Rb 24,8,22528
v_add_f32_e32 v14, v14, v34
Mp 216,40,52
v_exp_f32_e32 v24, v73
Mp 220,44,52
Rb 28,8,23552
s_waitcnt lgkmcnt(8)
Mp 212,44,48
v_exp_f32_e32 v35, v74
Mp 224,48,48
Rb 32,8,24576
v_add_f32_e32 v14, v14, v24
Mp 232,48,52
v_exp_f32_e32 v25, v75
Mp 236,52,52
Rb 36,8,25600
Cv 34,34,24
Mp 228,52,48
buffer_load_dwordx4 v2, s[20:23], s55 offen offset:3072 sc1 lds
Mp 240,56,48
Rb 40,8,26624
v_add_f32_e32 v14, v14, v35
Mp 248,56,52
v_exp_f32_e32 v36, v68
Mp 252,60,52
Rb 44,8,27648
s_waitcnt lgkmcnt(8)
Mp 244,60,48
v_exp_f32_e32 v26, v69
Mp 128,0,56
)" R"(
Rb 48,8,28672
v_add_f32_e32 v14, v14, v25
Mp 136,0,60
v_exp_f32_e32 v37, v70
Mp 140,4,60
Rb 52,8,29696
s_add_u32 m0, s36, 4096
Mp 132,4,56
buffer_load_dwordx4 v3, s[20:23], s55 offen offset:0 sc1 lds
Mp 144,8,56
Rb 56,8,30720
Cv 35,35,25
Mp 152,8,60
buffer_store_dwordx4 a[144:147], v3, s[20:23], s48 offen offset:0 sc0 sc1
Mp 156,12,60
Rb 60,8,31744
s_waitcnt lgkmcnt(8)
Mp 148,12,56
v_exp_f32_e32 v27, v71
Mp 160,16,56
v_exp_f32_e32 v38, v76
Mp 168,16,60
v_exp_f32_e32 v28, v77
Mp 172,20,60
v_exp_f32_e32 v39, v78
Mp 164,20,56
buffer_load_dwordx4 v3, s[20:23], s55 offen offset:1024 sc1 lds
Mp 176,24,56
v_exp_f32_e32 v29, v79
Mp 184,24,60
v_add_f32_e32 v15, v36, v26
Cv 36,36,26
Mp 188,28,60
v_add_f32_e32 v15, v15, v37
v_add_f32_e32 v15, v15, v27
Mp 180,28,56
s_waitcnt vmcnt(19) lgkmcnt(0)
s_barrier
Mp 192,32,56
Rb 0,1,32768
Cv 37,37,27
Mp 200,32,60
Rb 4,1,33792
v_add_f32_e32 v15, v15, v38
Mp 204,36,60
Rb 8,1,34816
v_add_f32_e32 v15, v15, v28
Mp 196,36,56
Rb 12,1,35840
buffer_load_dwordx4 v3, s[20:23], s55 offen offset:2048 sc1 lds
Mp 208,40,56
Rb 16,1,40960
Cv 38,38,28
Mp 216,40,60
Rb 20,1,41984
v_add_f32_e32 v15, v15, v39
Mp 220,44,60
Rb 24,1,43008
v_add_f32_e32 v15, v15, v29
Mp 212,44,56
Rb 28,1,44032
Cv 39,39,29
Mp 224,48,56
Rb 32,1,36864
s_sub_u32 s57, s57, 1
)" R"(
Mp 232,48,60
Rb 36,1,37888
Mp 236,52,60
Rb 40,1,38912
Mp 228,52,56
Rb 44,1,39936
buffer_load_dwordx4 v3, s[20:23], s55 offen offset:3072 sc1 lds
Mp 240,56,56
Mp 248,56,60
buffer_store_dwordx4 a[148:151], v3, s[20:23], s48 offen offset:1024 sc0 sc1
Mp 252,60,60
s_waitcnt lgkmcnt(8)
Mp 244,60,56
v_mfma_f32_16x16x32_bf16 v[96:99], a[0:3], a[64:67], v[16:19]
Rb 48,1,45056
buffer_load_dword v4, v5, s[20:23], s50 offen offset:0 sc0 sc1
v_mfma_f32_16x16x32_bf16 v[100:103], a[0:3], a[68:71], v[20:23]
s_lshl_b64 s[60:61], s[60:61], 8
s_or_b32 s60, s60, 0xff
Mv 100,4,76
Rb 52,1,46080
s_add_u32 m0, s36, 65536
Mv 96,4,72
buffer_load_dwordx4 v2, s[20:23], s56 offen offset:0 sc1 lds
Mv 96,8,80
Rb 56,1,47104
Mv 100,8,84
v_exp_f32_e32 v40, v80
Mv 100,12,92
Rb 60,1,48128
s_waitcnt lgkmcnt(8)
Mv 96,12,88
v_exp_f32_e32 v24, v81
Mv 96,16,96
Rb 0,1,49152
v_add_f32_e32 v14, v14, v40
Mv 100,16,100
v_exp_f32_e32 v41, v82
Mv 100,20,108
Rb 4,1,50176
v_add_f32_e32 v14, v14, v24
Mv 96,20,104
buffer_load_dwordx4 v2, s[20:23], s56 offen offset:1024 sc1 lds
Mv 96,24,112
Rb 8,1,51200
Cv 40,40,24
Mv 100,24,116
v_exp_f32_e32 v25, v83
Mv 100,28,124
Rb 12,1,52224
s_waitcnt lgkmcnt(8)
Mv 96,28,120
v_exp_f32_e32 v42, v88
v_add_f32_e32 v14, v14, v41
v_mfma_f32_16x16x32_bf16 v[104:107], a[32:35], a[64:67], v[16:19]
Rb 16,1,57344
v_add_f32_e32 v14, v14, v25
v_mfma_f32_16x16x32_bf16 v[108:111], a[32:35], a[68:71], v[20:23]
v_exp_f32_e32 v26, v89
Mv 108,36,76
Rb 20,1,58368
Cv 41,41,25
Mv 104,36,72
buffer_load_dwordx4 v2, s[20:23], s56 offen offset:2048 sc1 lds
)" R"(
Mv 104,40,80
Rb 24,1,59392
v_add_f32_e32 v14, v14, v42
Mv 108,40,84
buffer_store_dwordx4 a[152:155], v3, s[20:23], s48 offen offset:2048 sc0 sc1
Mv 108,44,92
Rb 28,1,60416
s_waitcnt lgkmcnt(8)
Mv 104,44,88
v_exp_f32_e32 v43, v90
Mv 104,48,96
Rb 32,1,53248
v_add_f32_e32 v14, v14, v26
Mv 108,48,100
v_exp_f32_e32 v27, v91
Mv 108,52,108
Rb 36,1,54272
Cv 42,42,26
Mv 104,52,104
buffer_load_dwordx4 v2, s[20:23], s56 offen offset:3072 sc1 lds
Mv 104,56,112
Rb 40,1,55296
v_add_f32_e32 v14, v14, v43
Mv 108,56,116
v_exp_f32_e32 v44, v84
Mv 108,60,124
Rb 44,1,56320
s_waitcnt lgkmcnt(8)
Mv 104,60,120
v_exp_f32_e32 v28, v85
v_add_f32_e32 v14, v14, v27
v_mfma_f32_16x16x32_bf16 v[112:115], a[0:3], a[64:67], v[16:19]
Rb 48,1,61440
Cv 43,43,27
v_mfma_f32_16x16x32_bf16 v[116:119], a[0:3], a[68:71], v[20:23]
v_exp_f32_e32 v45, v86
Mv 116,4,76
Rb 52,1,62464
s_add_u32 m0, s36, 69632
Mv 112,4,72
buffer_load_dwordx4 v3, s[20:23], s56 offen offset:0 sc1 lds
Mv 112,8,80
Rb 56,1,63488
v_add_f32_e32 v15, v15, v44
Mv 116,8,84
v_exp_f32_e32 v29, v87
Mv 116,12,92
Rb 60,1,64512
s_waitcnt lgkmcnt(8)
Mv 112,12,88
v_exp_f32_e32 v46, v92
Mv 112,16,96
v_exp_f32_e32 v24, v93
Mv 116,16,100
v_exp_f32_e32 v47, v94
Mv 116,20,108
v_exp_f32_e32 v25, v95
Mv 112,20,104
buffer_load_dwordx4 v3, s[20:23], s56 offen offset:1024 sc1 lds
Mv 112,24,112
v_add_f32_e32 v15, v15, v28
Cv 44,44,28
Mv 116,24,116
buffer_store_dwordx4 a[156:159], v3, s[20:23], s48 offen offset:3072 sc0 sc1
)" R"(
Mv 116,28,124
v_add_f32_e32 v15, v15, v45
v_add_f32_e32 v15, v15, v29
Mv 112,28,120
Cv 45,45,29
s_waitcnt vmcnt(20) lgkmcnt(0)
s_barrier
v_mfma_f32_16x16x32_bf16 v[120:123], a[32:35], a[64:67], v[16:19]
Rb 0,8,32768
v_add_f32_e32 v15, v15, v46
v_mfma_f32_16x16x32_bf16 v[124:127], a[32:35], a[68:71], v[20:23]
Rb 4,8,33792
v_add_f32_e32 v15, v15, v24
Mv 124,36,76
Rb 8,8,34816
Cv 46,46,24
Mv 120,36,72
Rb 12,8,35840
buffer_load_dwordx4 v3, s[20:23], s56 offen offset:2048 sc1 lds
Mv 120,40,80
Rb 16,8,36864
v_add_f32_e32 v15, v15, v47
Mv 124,40,84
Rb 20,8,37888
v_add_f32_e32 v15, v15, v25
Mv 124,44,92
Rb 24,8,38912
Cv 47,47,25
Mv 120,44,88
Rb 28,8,39936
v_max_f32_e32 v11, v14, v15
Mv 120,48,96
Rb 32,8,40960
v_cmp_nge_f32_e32 vcc, s62, v11
Mv 124,48,100
Rb 36,8,41984
s_add_u32 s55, s55, 0x8000
Mv 124,52,108
Rb 40,8,43008
Mv 120,52,104
Rb 44,8,44032
buffer_load_dwordx4 v3, s[20:23], s56 offen offset:3072 sc1 lds
Mv 120,56,112
s_cmp_eq_u32 s57, 2
s_cselect_b32 s55, s59, s55
Mv 124,56,116
s_add_u32 s56, s56, 0x8000
Mv 124,60,124
s_waitcnt lgkmcnt(8)
s_cbranch_vccnz .Lzfx10
.Lzfr10:
Mv 120,60,120
v_add_f32_e32 v12, v12, v14
v_add_f32_e32 v13, v13, v15
Mp 128,0,32
Rb 48,8,45056
Mp 136,0,36
s_cmp_le_i32 s57, s58
s_cbranch_scc1 .Lzmk9
.Lzmr9:
Mp 140,4,36
Rb 52,8,46080
s_add_u32 m0, s36, 32768
Mp 132,4,32
buffer_load_dwordx4 v2, s[20:23], s55 offen offset:0 sc1 lds
)" R"(
Mp 144,8,32
Rb 56,8,47104
Mp 152,8,36
v_exp_f32_e32 v48, v96
Mp 156,12,36
Rb 60,8,48128
s_waitcnt lgkmcnt(8)
Mp 148,12,32
v_exp_f32_e32 v26, v97
Mp 160,16,32
Rb 0,8,49152
v_add_f32_e32 v14, v48, v26
Mp 168,16,36
v_exp_f32_e32 v49, v98
Mp 172,20,36
Rb 4,8,50176
Cv 48,48,26
Mp 164,20,32
buffer_load_dwordx4 v2, s[20:23], s55 offen offset:1024 sc1 lds
Mp 176,24,32
Rb 8,8,51200
v_add_f32_e32 v14, v14, v49
Mp 184,24,36
v_exp_f32_e32 v27, v99
Mp 188,28,36
Rb 12,8,52224
s_waitcnt lgkmcnt(8)
Mp 180,28,32
v_exp_f32_e32 v50, v104
Mp 192,32,32
Rb 16,8,53248
v_add_f32_e32 v14, v14, v27
Mp 200,32,36
v_exp_f32_e32 v28, v105
Mp 204,36,36
Rb 20,8,54272
Cv 49,49,27
Mp 196,36,32
buffer_load_dwordx4 v2, s[20:23], s55 offen offset:2048 sc1 lds
Mp 208,40,32
Rb 24,8,55296
v_add_f32_e32 v14, v14, v50
Mp 216,40,36
v_exp_f32_e32 v51, v106
Mp 220,44,36
Rb 28,8,56320
s_waitcnt lgkmcnt(8)
Mp 212,44,32
v_exp_f32_e32 v29, v107
Mp 224,48,32
Rb 32,8,57344
v_add_f32_e32 v14, v14, v28
Mp 232,48,36
v_exp_f32_e32 v52, v100
Mp 236,52,36
Rb 36,8,58368
Cv 50,50,28
Mp 228,52,32
buffer_load_dwordx4 v2, s[20:23], s55 offen offset:3072 sc1 lds
Mp 240,56,32
Rb 40,8,59392
v_add_f32_e32 v14, v14, v51
Mp 248,56,36
v_exp_f32_e32 v24, v101
)" R"(
Mp 252,60,36
Rb 44,8,60416
s_waitcnt lgkmcnt(8)
Mp 244,60,32
v_exp_f32_e32 v53, v102
Mp 128,0,40
Rb 48,8,61440
v_add_f32_e32 v14, v14, v29
Mp 136,0,44
v_exp_f32_e32 v25, v103
Mp 140,4,44
Rb 52,8,62464
s_add_u32 m0, s36, 36864
Mp 132,4,40
buffer_load_dwordx4 v3, s[20:23], s55 offen offset:0 sc1 lds
Mp 144,8,40
Rb 56,8,63488
Cv 51,51,29
Mp 152,8,44
v_exp_f32_e32 v54, v108
Mp 156,12,44
Rb 60,8,64512
s_waitcnt lgkmcnt(8)
Mp 148,12,40
v_exp_f32_e32 v26, v109
Mp 160,16,40
v_exp_f32_e32 v55, v110
Mp 168,16,44
v_exp_f32_e32 v27, v111
Mp 172,20,44
v_add_f32_e32 v15, v52, v24
Cv 52,52,24
Mp 164,20,40
buffer_load_dwordx4 v3, s[20:23], s55 offen offset:1024 sc1 lds
Mp 176,24,40
v_add_f32_e32 v15, v15, v53
v_add_f32_e32 v15, v15, v25
Mp 184,24,44
Cv 53,53,25
v_add_f32_e32 v15, v15, v54
Mp 188,28,44
v_add_f32_e32 v15, v15, v26
Cv 54,54,26
Mp 180,28,40
s_waitcnt vmcnt(8) lgkmcnt(0)
s_barrier
buffer_store_dword v6, v7, s[20:23], s49 offen offset:12 sc0 sc1
Mp 192,32,40
Rb 0,1,0
v_add_f32_e32 v15, v15, v55
Mp 200,32,44
Rb 4,1,1024
v_add_f32_e32 v15, v15, v27
Mp 204,36,44
Rb 8,1,2048
Cv 55,55,27
Mp 196,36,40
Rb 12,1,3072
buffer_load_dwordx4 v3, s[20:23], s55 offen offset:2048 sc1 lds
Mp 208,40,40
Rb 16,1,8192
s_sub_u32 s57, s57, 1
Mp 216,40,44
Rb 20,1,9216
Mp 220,44,44
)" R"(
Rb 24,1,10240
Mp 212,44,40
Rb 28,1,11264
Mp 224,48,40
Rb 32,1,4096
Mp 232,48,44
Rb 36,1,5120
Mp 236,52,44
Rb 40,1,6144
Mp 228,52,40
Rb 44,1,7168
buffer_load_dwordx4 v3, s[20:23], s55 offen offset:3072 sc1 lds
Mp 240,56,40
v_cmp_eq_u32_e32 vcc, s3, v4
s_andn2_b64 s[28:29], s[60:61], vcc
s_cbranch_scc1 .Lzsp14
.Lzok14:
Mp 248,56,44
Mp 252,60,44
s_waitcnt lgkmcnt(8)
Mp 244,60,40
s_branch .LzL
.LzEB:
v_mfma_f32_16x16x32_bf16 v[64:67], a[0:3], a[64:67], v[16:19]
Rb 48,1,12288
v_mfma_f32_16x16x32_bf16 v[68:71], a[0:3], a[68:71], v[20:23]
s_cmp_lg_u32 s35, 7
s_cselect_b64 s[60:61], 1, 0
Mv 68,4,76
Rb 52,1,13312
s_add_u32 m0, s36, 98304
Mv 64,4,72
buffer_load_dwordx4 v2, s[20:23], s56 offen offset:0 sc1 lds
Mv 64,8,80
Rb 56,1,14336
Mv 68,8,84
v_exp_f32_e32 v56, v112
Mv 68,12,92
Rb 60,1,15360
s_waitcnt lgkmcnt(8)
Mv 64,12,88
v_exp_f32_e32 v28, v113
Mv 64,16,96
Rb 0,1,16384
v_add_f32_e32 v14, v14, v56
Mv 68,16,100
v_exp_f32_e32 v57, v114
Mv 68,20,108
Rb 4,1,17408
v_add_f32_e32 v14, v14, v28
Mv 64,20,104
buffer_load_dwordx4 v2, s[20:23], s56 offen offset:1024 sc1 lds
Mv 64,24,112
Rb 8,1,18432
Cv 56,56,28
Mv 68,24,116
v_exp_f32_e32 v29, v115
Mv 68,28,124
Rb 12,1,19456
s_waitcnt lgkmcnt(8)
Mv 64,28,120
v_exp_f32_e32 v58, v120
v_mfma_f32_16x16x32_bf16 v[72:75], a[32:35], a[64:67], v[16:19]
Rb 16,1,24576
)" R"(
v_add_f32_e32 v14, v14, v57
v_mfma_f32_16x16x32_bf16 v[76:79], a[32:35], a[68:71], v[20:23]
v_exp_f32_e32 v24, v121
Mv 76,36,76
Rb 20,1,25600
v_add_f32_e32 v14, v14, v29
Mv 72,36,72
buffer_load_dwordx4 v2, s[20:23], s56 offen offset:2048 sc1 lds
Mv 72,40,80
Rb 24,1,26624
Cv 57,57,29
Mv 76,40,84
v_exp_f32_e32 v59, v122
Mv 76,44,92
Rb 28,1,27648
s_waitcnt lgkmcnt(8)
Mv 72,44,88
v_exp_f32_e32 v25, v123
Mv 72,48,96
Rb 32,1,20480
v_add_f32_e32 v14, v14, v58
Mv 76,48,100
v_exp_f32_e32 v60, v116
Mv 76,52,108
Rb 36,1,21504
v_add_f32_e32 v14, v14, v24
Mv 72,52,104
buffer_load_dwordx4 v2, s[20:23], s56 offen offset:3072 sc1 lds
Mv 72,56,112
Rb 40,1,22528
Cv 58,58,24
Mv 76,56,116
v_exp_f32_e32 v26, v117
Mv 76,60,124
Rb 44,1,23552
s_waitcnt lgkmcnt(8)
Mv 72,60,120
v_exp_f32_e32 v61, v118
v_mfma_f32_16x16x32_bf16 v[80:83], a[0:3], a[64:67], v[16:19]
Rb 48,1,28672
v_add_f32_e32 v14, v14, v59
v_mfma_f32_16x16x32_bf16 v[84:87], a[0:3], a[68:71], v[20:23]
v_exp_f32_e32 v27, v119
Mv 84,4,76
Rb 52,1,29696
s_add_u32 m0, s36, 102400
Mv 80,4,72
buffer_load_dwordx4 v3, s[20:23], s56 offen offset:0 sc1 lds
Mv 80,8,80
Rb 56,1,30720
v_add_f32_e32 v14, v14, v25
Mv 84,8,84
v_exp_f32_e32 v62, v124
Mv 84,12,92
Rb 60,1,31744
s_waitcnt lgkmcnt(8)
Mv 80,12,88
v_exp_f32_e32 v28, v125
Mv 80,16,96
v_exp_f32_e32 v63, v126
Mv 84,16,100
v_exp_f32_e32 v29, v127
Mv 84,20,108
Cv 59,59,25
)" R"(
v_add_f32_e32 v15, v15, v60
Mv 80,20,104
buffer_load_dwordx4 v3, s[20:23], s56 offen offset:1024 sc1 lds
Mv 80,24,112
v_add_f32_e32 v15, v15, v26
Cv 60,60,26
Mv 84,24,116
v_add_f32_e32 v15, v15, v61
v_add_f32_e32 v15, v15, v27
Mv 84,28,124
Cv 61,61,27
v_add_f32_e32 v15, v15, v62
Mv 80,28,120
s_waitcnt vmcnt(14) lgkmcnt(0)
s_barrier
v_mfma_f32_16x16x32_bf16 v[88:91], a[32:35], a[64:67], v[16:19]
Rb 0,8,0
v_add_f32_e32 v15, v15, v28
v_mfma_f32_16x16x32_bf16 v[92:95], a[32:35], a[68:71], v[20:23]
Rb 4,8,1024
Cv 62,62,28
Mv 92,36,76
Rb 8,8,2048
v_add_f32_e32 v15, v15, v63
Mv 88,36,72
Rb 12,8,3072
buffer_load_dwordx4 v3, s[20:23], s56 offen offset:2048 sc1 lds
Mv 88,40,80
Rb 16,8,4096
v_add_f32_e32 v15, v15, v29
Mv 92,40,84
Rb 20,8,5120
Cv 63,63,29
Mv 92,44,92
Rb 24,8,6144
v_max_f32_e32 v11, v14, v15
Mv 88,44,88
Rb 28,8,7168
v_cmp_nge_f32_e32 vcc, s62, v11
Mv 88,48,96
Rb 32,8,8192
s_add_u32 s55, s55, 0x8000
Mv 92,48,100
Rb 36,8,9216
Mv 92,52,108
Rb 40,8,10240
Mv 88,52,104
Rb 44,8,11264
buffer_load_dwordx4 v3, s[20:23], s56 offen offset:3072 sc1 lds
Mv 88,56,112
s_cmp_eq_u32 s57, 2
s_cselect_b32 s55, s59, s55
Mv 92,56,116
s_add_u32 s56, s56, 0x8000
Mv 92,60,124
s_waitcnt lgkmcnt(8)
s_cbranch_vccnz .Lzfx11
.Lzfr11:
Mv 88,60,120
v_add_f32_e32 v12, v12, v14
v_add_f32_e32 v13, v13, v15
Mp 128,0,48
Rb 48,8,12288
Mp 136,0,52
s_cmp_le_i32 s57, s58
)" R"(
s_cbranch_scc1 .Lzmk10
.Lzmr10:
Mp 140,4,52
Rb 52,8,13312
s_add_u32 m0, s36, 0
Mp 132,4,48
buffer_load_dwordx4 v2, s[20:23], s55 offen offset:0 sc1 lds
Mp 144,8,48
Rb 56,8,14336
Mp 152,8,52
v_exp_f32_e32 v32, v64
Mp 156,12,52
Rb 60,8,15360
s_waitcnt lgkmcnt(8)
Mp 148,12,48
v_exp_f32_e32 v24, v65
Mp 160,16,48
Rb 0,8,16384
v_add_f32_e32 v14, v32, v24
Mp 168,16,52
v_exp_f32_e32 v33, v66
Cv 32,32,24
Mp 172,20,52
Rb 4,8,17408
v_add_f32_e32 v14, v14, v33
Mp 164,20,48
buffer_load_dwordx4 v2, s[20:23], s55 offen offset:1024 sc1 lds
Mp 176,24,48
Rb 8,8,18432
Mp 184,24,52
v_exp_f32_e32 v25, v67
Mp 188,28,52
Rb 12,8,19456
s_waitcnt lgkmcnt(8)
Mp 180,28,48
v_exp_f32_e32 v34, v72
Mp 192,32,48
Rb 16,8,20480
v_add_f32_e32 v14, v14, v25
Mp 200,32,52
v_exp_f32_e32 v26, v73
Cv 33,33,25
Mp 204,36,52
Rb 20,8,21504
v_add_f32_e32 v14, v14, v34
Mp 196,36,48
buffer_load_dwordx4 v2, s[20:23], s55 offen offset:2048 sc1 lds
Mp 208,40,48
Rb 24,8,22528
v_add_f32_e32 v14, v14, v26
Mp 216,40,52
v_exp_f32_e32 v35, v74
Mp 220,44,52
Rb 28,8,23552
s_waitcnt lgkmcnt(8)
Mp 212,44,48
v_exp_f32_e32 v27, v75
Mp 224,48,48
Rb 32,8,24576
Cv 34,34,26
Mp 232,48,52
v_exp_f32_e32 v36, v68
v_add_f32_e32 v14, v14, v35
Mp 236,52,52
)" R"(
Rb 36,8,25600
v_add_f32_e32 v14, v14, v27
Mp 228,52,48
buffer_load_dwordx4 v2, s[20:23], s55 offen offset:3072 sc1 lds
Mp 240,56,48
Rb 40,8,26624
Cv 35,35,27
Mp 248,56,52
v_exp_f32_e32 v28, v69
Mp 252,60,52
Rb 44,8,27648
s_waitcnt lgkmcnt(8)
Mp 244,60,48
v_exp_f32_e32 v37, v70
Mp 128,0,56
Rb 48,8,28672
v_add_f32_e32 v15, v36, v28
Mp 136,0,60
v_exp_f32_e32 v29, v71
Cv 36,36,28
Mp 140,4,60
Rb 52,8,29696
s_add_u32 m0, s36, 4096
Mp 132,4,56
buffer_load_dwordx4 v3, s[20:23], s55 offen offset:0 sc1 lds
Mp 144,8,56
Rb 56,8,30720
v_add_f32_e32 v15, v15, v37
Mp 152,8,60
v_exp_f32_e32 v38, v76
Mp 156,12,60
Rb 60,8,31744
s_waitcnt lgkmcnt(8)
Mp 148,12,56
v_exp_f32_e32 v24, v77
Mp 160,16,56
v_exp_f32_e32 v39, v78
Mp 168,16,60
v_exp_f32_e32 v25, v79
v_add_f32_e32 v15, v15, v29
Mp 172,20,60
Cv 37,37,29
v_add_f32_e32 v15, v15, v38
Mp 164,20,56
buffer_load_dwordx4 v3, s[20:23], s55 offen offset:1024 sc1 lds
Mp 176,24,56
v_add_f32_e32 v15, v15, v24
Cv 38,38,24
Mp 184,24,60
v_add_f32_e32 v15, v15, v39
v_add_f32_e32 v15, v15, v25
Mp 188,28,60
Cv 39,39,25
s_sub_u32 s57, s57, 1
Mp 180,28,56
s_waitcnt vmcnt(14) lgkmcnt(0)
s_barrier
Mp 192,32,56
Rb 0,1,32768
v_bfe_u32 v11, v0, 2, 2
Mp 200,32,60
Rb 4,1,33792
v_lshrrev_b32 v4, 4, v0
v_xor_b32 v11, v11, v4
Mp 204,36,60
)" R"(
Rb 8,1,34816
v_bfe_u32 v4, v0, 1, 1
Mp 196,36,56
Rb 12,1,35840
buffer_load_dwordx4 v3, s[20:23], s55 offen offset:2048 sc1 lds
Mp 208,40,56
Rb 16,1,40960
v_lshl_or_b32 v11, v11, 1, v4
Mp 216,40,60
Rb 20,1,41984
v_and_b32 v4, 1, v0
Mp 220,44,60
Rb 24,1,43008
v_mul_u32_u24 v4, 0xffff, v4
Mp 212,44,56
Rb 28,1,44032
v_add_u32 v4, 1, v4
Mp 224,48,56
Rb 32,1,36864
v_mul_u32_u24 v4, 0x3f80, v4
Mp 232,48,60
Rb 36,1,37888
v_cmp_eq_u32_e32 vcc, 0, v11
v_cmp_eq_u32_e64 s[28:29], 1, v11
Mp 236,52,60
Rb 40,1,38912
v_accvgpr_write_b32 a192, 0
Mp 228,52,56
Rb 44,1,39936
buffer_load_dwordx4 v3, s[20:23], s55 offen offset:3072 sc1 lds
Mp 240,56,56
v_accvgpr_write_b32 a193, 0
v_accvgpr_write_b32 a196, 0
Mp 248,56,60
v_accvgpr_write_b32 a197, 0
v_cndmask_b32_e32 v11, 0, v4, vcc
Mp 252,60,60
v_cndmask_b32_e64 v4, 0, v4, s[28:29]
s_waitcnt lgkmcnt(8)
Mp 244,60,56
v_accvgpr_write_b32 a194, v11
v_accvgpr_write_b32 a195, v4
v_mfma_f32_16x16x32_bf16 v[96:99], a[0:3], a[64:67], v[16:19]
Rb 48,1,45056
v_mfma_f32_16x16x32_bf16 v[100:103], a[0:3], a[68:71], v[20:23]
v_exp_f32_e32 v40, v80
Mv 100,4,76
Rb 52,1,46080
s_add_u32 m0, s36, 65536
Mv 96,4,72
buffer_load_dwordx4 v2, s[20:23], s56 offen offset:0 sc1 lds
Mv 96,8,80
Rb 56,1,47104
v_add_f32_e32 v14, v14, v40
Mv 100,8,84
v_exp_f32_e32 v26, v81
Mv 100,12,92
Rb 60,1,48128
s_waitcnt lgkmcnt(8)
Mv 96,12,88
v_exp_f32_e32 v41, v82
Mv 96,16,96
Rb 0,1,49152
v_add_f32_e32 v14, v14, v26
)" R"(
Mv 100,16,100
v_exp_f32_e32 v27, v83
Mv 100,20,108
Rb 4,1,50176
Cv 40,40,26
Mv 96,20,104
buffer_load_dwordx4 v2, s[20:23], s56 offen offset:1024 sc1 lds
Mv 96,24,112
Rb 8,1,51200
v_add_f32_e32 v14, v14, v41
Mv 100,24,116
v_exp_f32_e32 v42, v88
Mv 100,28,124
Rb 12,1,52224
s_waitcnt lgkmcnt(8)
Mv 96,28,120
v_exp_f32_e32 v28, v89
v_mfma_f32_16x16x32_bf16 v[104:107], a[32:35], a[64:67], v[16:19]
Rb 16,1,57344
v_add_f32_e32 v14, v14, v27
v_mfma_f32_16x16x32_bf16 v[108:111], a[32:35], a[68:71], v[20:23]
v_exp_f32_e32 v43, v90
Mv 108,36,76
Rb 20,1,58368
Cv 41,41,27
Mv 104,36,72
buffer_load_dwordx4 v2, s[20:23], s56 offen offset:2048 sc1 lds
Mv 104,40,80
Rb 24,1,59392
buffer_load_dword v4, v7, s[20:23], s65 offen offset:0 sc0 sc1
Mv 108,40,84
v_exp_f32_e32 v29, v91
Mv 108,44,92
Rb 28,1,60416
s_waitcnt lgkmcnt(8)
Mv 104,44,88
v_exp_f32_e32 v44, v84
Mv 104,48,96
Rb 32,1,53248
v_add_f32_e32 v14, v14, v42
Mv 108,48,100
v_exp_f32_e32 v24, v85
Mv 108,52,108
Rb 36,1,54272
v_add_f32_e32 v14, v14, v28
Mv 104,52,104
buffer_load_dwordx4 v2, s[20:23], s56 offen offset:3072 sc1 lds
Mv 104,56,112
Rb 40,1,55296
Cv 42,42,28
Mv 108,56,116
v_exp_f32_e32 v45, v86
Mv 108,60,124
Rb 44,1,56320
s_waitcnt lgkmcnt(8)
Mv 104,60,120
v_exp_f32_e32 v25, v87
v_mfma_f32_16x16x32_bf16 v[112:115], a[0:3], a[64:67], v[16:19]
Rb 48,1,61440
v_add_f32_e32 v14, v14, v43
v_mfma_f32_16x16x32_bf16 v[116:119], a[0:3], a[68:71], v[20:23]
v_exp_f32_e32 v46, v92
Mv 116,4,76
Rb 52,1,62464
)" R"(
s_add_u32 m0, s36, 69632
Mv 112,4,72
buffer_load_dwordx4 v3, s[20:23], s56 offen offset:0 sc1 lds
Mv 112,8,80
Rb 56,1,63488
v_add_f32_e32 v14, v14, v29
Mv 116,8,84
v_exp_f32_e32 v26, v93
Mv 116,12,92
Rb 60,1,64512
s_waitcnt lgkmcnt(8)
Mv 112,12,88
v_exp_f32_e32 v47, v94
Mv 112,16,96
v_exp_f32_e32 v27, v95
Mv 116,16,100
Cv 43,43,29
v_add_f32_e32 v15, v15, v44
Mv 116,20,108
v_add_f32_e32 v15, v15, v24
Cv 44,44,24
Mv 112,20,104
buffer_load_dwordx4 v3, s[20:23], s56 offen offset:1024 sc1 lds
Mv 112,24,112
v_add_f32_e32 v15, v15, v45
v_add_f32_e32 v15, v15, v25
Mv 116,24,116
Cv 45,45,25
v_add_f32_e32 v15, v15, v46
Mv 116,28,124
v_add_f32_e32 v15, v15, v26
Cv 46,46,26
Mv 112,28,120
s_waitcnt vmcnt(3) lgkmcnt(0)
s_barrier
v_mfma_f32_16x16x32_bf16 v[120:123], a[32:35], a[64:67], v[16:19]
Rb 0,8,32768
v_add_f32_e32 v15, v15, v47
v_mfma_f32_16x16x32_bf16 v[124:127], a[32:35], a[68:71], v[20:23]
Rb 4,8,33792
v_add_f32_e32 v15, v15, v27
Mv 124,36,76
Rb 8,8,34816
Cv 47,47,27
Mv 120,36,72
Rb 12,8,35840
buffer_load_dwordx4 v3, s[20:23], s56 offen offset:2048 sc1 lds
Mv 120,40,80
Rb 16,8,36864
v_max_f32_e32 v11, v14, v15
Mv 124,40,84
Rb 20,8,37888
v_cmp_nge_f32_e32 vcc, s62, v11
Mv 124,44,92
Rb 24,8,38912
s_add_u32 s55, s55, 0x8000
Mv 120,44,88
Rb 28,8,39936
Mv 120,48,96
Rb 32,8,40960
Mv 124,48,100
Rb 36,8,41984
Mv 124,52,108
Rb 40,8,43008
Mv 120,52,104
)" R"(
Rb 44,8,44032
buffer_load_dwordx4 v3, s[20:23], s56 offen offset:3072 sc1 lds
Mv 120,56,112
s_cmp_eq_u32 s57, 2
s_cselect_b32 s55, s59, s55
Mv 124,56,116
s_add_u32 s56, s56, 0x8000
Mv 124,60,124
s_waitcnt lgkmcnt(8)
s_cbranch_vccnz .Lzfx12
.Lzfr12:
Mv 120,60,120
v_cmp_eq_u32_e32 vcc, s3, v4
s_andn2_b64 s[28:29], s[60:61], vcc
s_cbranch_scc1 .Lzsp15
.Lzok15:
Mp 128,0,32
Rb 48,8,45056
v_add_f32_e32 v12, v12, v14
Mp 136,0,36
v_add_f32_e32 v13, v13, v15
s_cmp_le_i32 s57, s58
s_cbranch_scc1 .Lzmk11
.Lzmr11:
Mp 140,4,36
Rb 52,8,46080
s_add_u32 m0, s36, 32768
buffer_load_dwordx4 v[64:67], v1, s[20:23], s66 offen sc0 sc1
Mp 132,4,32
buffer_load_dwordx4 v2, s[20:23], s55 offen offset:0 sc1 lds
Mp 144,8,32
Rb 56,8,47104
v_exp_f32_e32 v48, v96
Mp 152,8,36
buffer_load_dwordx4 a[128:131], v2, s[20:23], s64 offen offset:0 sc0 sc1
Mp 156,12,36
Rb 60,8,48128
s_waitcnt lgkmcnt(8)
Mp 148,12,32
v_exp_f32_e32 v28, v97
Mp 160,16,32
Rb 0,8,49152
buffer_load_dwordx4 a[132:135], v2, s[20:23], s64 offen offset:1024 sc0 sc1
Mp 168,16,36
v_exp_f32_e32 v49, v98
v_add_f32_e32 v14, v48, v28
Mp 172,20,36
Rb 4,8,50176
Cv 48,48,28
Mp 164,20,32
buffer_load_dwordx4 v2, s[20:23], s55 offen offset:1024 sc1 lds
Mp 176,24,32
Rb 8,8,51200
v_exp_f32_e32 v29, v99
Mp 184,24,36
buffer_load_dwordx4 a[136:139], v2, s[20:23], s64 offen offset:2048 sc0 sc1
Mp 188,28,36
Rb 12,8,52224
s_waitcnt lgkmcnt(8)
Mp 180,28,32
v_exp_f32_e32 v50, v104
v_add_f32_e32 v14, v14, v49
Mp 192,32,32
Rb 16,8,53248
)" R"(
buffer_load_dwordx4 a[140:143], v2, s[20:23], s64 offen offset:3072 sc0 sc1
Mp 200,32,36
v_exp_f32_e32 v24, v105
v_add_f32_e32 v14, v14, v29
Mp 204,36,36
Rb 20,8,54272
Cv 49,49,29
Mp 196,36,32
buffer_load_dwordx4 v2, s[20:23], s55 offen offset:2048 sc1 lds
Mp 208,40,32
Rb 24,8,55296
v_exp_f32_e32 v51, v106
Mp 216,40,36
buffer_load_dwordx4 a[144:147], v3, s[20:23], s64 offen offset:0 sc0 sc1
Mp 220,44,36
Rb 28,8,56320
s_waitcnt lgkmcnt(8)
Mp 212,44,32
v_exp_f32_e32 v25, v107
v_add_f32_e32 v14, v14, v50
Mp 224,48,32
Rb 32,8,57344
buffer_load_dwordx4 a[148:151], v3, s[20:23], s64 offen offset:1024 sc0 sc1
Mp 232,48,36
v_exp_f32_e32 v52, v100
v_add_f32_e32 v14, v14, v24
Mp 236,52,36
Rb 36,8,58368
Cv 50,50,24
Mp 228,52,32
buffer_load_dwordx4 v2, s[20:23], s55 offen offset:3072 sc1 lds
Mp 240,56,32
Rb 40,8,59392
v_exp_f32_e32 v26, v101
Mp 248,56,36
buffer_load_dwordx4 a[152:155], v3, s[20:23], s64 offen offset:2048 sc0 sc1
Mp 252,60,36
Rb 44,8,60416
s_waitcnt lgkmcnt(8)
Mp 244,60,32
v_exp_f32_e32 v53, v102
v_add_f32_e32 v14, v14, v51
Mp 128,0,40
Rb 48,8,61440
buffer_load_dwordx4 a[156:159], v3, s[20:23], s64 offen offset:3072 sc0 sc1
Mp 136,0,44
v_exp_f32_e32 v27, v103
v_add_f32_e32 v14, v14, v25
Mp 140,4,44
Rb 52,8,62464
s_add_u32 m0, s36, 36864
Mp 132,4,40
buffer_load_dwordx4 v3, s[20:23], s55 offen offset:0 sc1 lds
Mp 144,8,40
Rb 56,8,63488
v_exp_f32_e32 v54, v108
Mp 152,8,44
s_add_u32 s64, s64, 0x8000
buffer_load_dwordx4 a[160:163], v2, s[20:23], s64 offen offset:0 sc0 sc1
Mp 156,12,44
Rb 60,8,64512
s_waitcnt lgkmcnt(8)
Mp 148,12,40
v_exp_f32_e32 v28, v109
)" R"(
Cv 51,51,25
Mp 160,16,40
buffer_load_dwordx4 a[164:167], v2, s[20:23], s64 offen offset:1024 sc0 sc1
Mp 168,16,44
v_exp_f32_e32 v55, v110
v_add_f32_e32 v15, v52, v26
Mp 172,20,44
v_exp_f32_e32 v29, v111
Mp 164,20,40
buffer_load_dwordx4 v3, s[20:23], s55 offen offset:1024 sc1 lds
Mp 176,24,40
Cv 52,52,26
v_add_f32_e32 v15, v15, v53
v_add_f32_e32 v15, v15, v27
Mp 184,24,44
buffer_load_dwordx4 a[168:171], v2, s[20:23], s64 offen offset:2048 sc0 sc1
Mp 188,28,44
Cv 53,53,27
v_add_f32_e32 v15, v15, v54
Mp 180,28,40
v_add_f32_e32 v15, v15, v28
s_waitcnt vmcnt(18) lgkmcnt(0)
s_barrier
Mp 192,32,40
Rb 0,8,0
buffer_load_dwordx4 a[172:175], v2, s[20:23], s64 offen offset:3072 sc0 sc1
Mp 200,32,44
Rb 4,8,16384
Cv 54,54,28
v_add_f32_e32 v15, v15, v55
Mp 204,36,44
Rb 8,8,1024
v_add_f32_e32 v15, v15, v29
Mp 196,36,40
Rb 12,8,17408
buffer_load_dwordx4 v3, s[20:23], s55 offen offset:2048 sc1 lds
Mp 208,40,40
Rb 16,8,2048
Cv 55,55,29
s_sub_u32 s57, s57, 1
Mp 216,40,44
Rb 20,8,18432
buffer_load_dwordx4 a[176:179], v3, s[20:23], s64 offen offset:0 sc0 sc1
Mp 220,44,44
Rb 24,8,3072
Mp 212,44,40
Rb 28,8,19456
Mp 224,48,40
Rb 32,8,4096
buffer_load_dwordx4 a[180:183], v3, s[20:23], s64 offen offset:1024 sc0 sc1
Mp 232,48,44
Rb 36,8,20480
Mp 236,52,44
Rb 40,8,5120
Mp 228,52,40
Rb 44,8,21504
buffer_load_dwordx4 v3, s[20:23], s55 offen offset:3072 sc1 lds
Mp 240,56,40
buffer_load_dwordx4 a[188:191], v3, s[20:23], s64 offen offset:3072 sc0 sc1
Mp 248,56,44
buffer_load_dwordx4 a[184:187], v3, s[20:23], s64 offen offset:2048 sc0 sc1
Mp 252,60,44
Mp 244,60,40
s_waitcnt vmcnt(18)
v_cmp_neq_f32_e32 vcc, v16, v66
)" R"(
v_cmp_neq_f32_e64 s[28:29], v20, v67
s_or_b64 vcc, vcc, s[28:29]
s_cbranch_scc1 .LzMRG
.LzMRR:
Mv 128,194,128
v_exp_f32_e32 v56, v112
Mv 136,194,132
v_exp_f32_e32 v26, v113
v_add_f32_e32 v12, v12, v64
Mv 132,192,128
s_add_u32 m0, s36, 131072
s_add_u32 s40, s67, 0x10000
v_add_f32_e32 v13, v13, v65
Mv 140,192,132
buffer_load_dwordx4 v2, s[20:23], s40 offen offset:0 sc1 lds
Mv 144,194,136
v_exp_f32_e32 v57, v114
v_add_f32_e32 v14, v14, v56
Mv 152,194,140
v_exp_f32_e32 v27, v115
v_add_f32_e32 v14, v14, v26
Mv 148,192,136
Cv 56,56,26
s_waitcnt vmcnt(13)
Mv 156,192,140
buffer_load_dwordx4 v2, s[20:23], s40 offen offset:1024 sc1 lds
v_add_f32_e32 v14, v14, v57
Mv 160,194,144
v_exp_f32_e32 v58, v120
v_add_f32_e32 v14, v14, v27
Mv 168,194,148
v_exp_f32_e32 v28, v121
Mv 164,192,144
v_exp_f32_e32 v59, v122
Cv 57,57,27
Mv 172,192,148
buffer_load_dwordx4 v2, s[20:23], s40 offen offset:2048 sc1 lds
v_add_f32_e32 v14, v14, v58
Mv 176,194,152
v_exp_f32_e32 v29, v123
Mv 184,194,156
v_exp_f32_e32 v60, v116
v_add_f32_e32 v14, v14, v28
Mv 180,192,152
v_exp_f32_e32 v24, v117
s_waitcnt vmcnt(9)
Mv 188,192,156
buffer_load_dwordx4 v2, s[20:23], s40 offen offset:3072 sc1 lds
Cv 58,58,28
Mv 192,194,160
v_exp_f32_e32 v61, v118
Mv 200,194,164
v_exp_f32_e32 v25, v119
v_add_f32_e32 v14, v14, v59
Mv 196,192,160
s_add_u32 m0, s36, 135168
v_exp_f32_e32 v62, v124
Mv 204,192,164
buffer_load_dwordx4 v3, s[20:23], s40 offen offset:0 sc1 lds
Mv 208,194,168
v_exp_f32_e32 v26, v125
v_add_f32_e32 v14, v14, v29
Mv 216,194,172
v_exp_f32_e32 v63, v126
)" R"(
Cv 59,59,29
Mv 212,192,168
v_add_f32_e32 v15, v15, v60
s_waitcnt vmcnt(5)
Mv 220,192,172
buffer_load_dwordx4 v3, s[20:23], s40 offen offset:1024 sc1 lds
v_add_f32_e32 v15, v15, v24
Mv 224,194,176
v_exp_f32_e32 v27, v127
Cv 60,60,24
Mv 232,194,180
v_add_f32_e32 v15, v15, v61
v_add_f32_e32 v15, v15, v25
Mv 228,192,176
Cv 61,61,25
v_add_f32_e32 v15, v15, v62
v_add_f32_e32 v15, v15, v26
Mv 236,192,180
buffer_load_dwordx4 v3, s[20:23], s40 offen offset:2048 sc1 lds
Cv 62,62,26
Mv 240,194,184
v_add_f32_e32 v15, v15, v63
v_add_f32_e32 v15, v15, v27
Mv 248,194,188
s_add_u32 s40, s67, 0x18000
Cv 63,63,27
v_max_f32_e32 v11, v14, v15
Mv 244,192,184
s_add_u32 m0, s36, 98304
v_cmp_nge_f32_e32 vcc, s62, v11
s_waitcnt lgkmcnt(8)
Mv 252,192,188
buffer_load_dwordx4 v2, s[20:23], s40 offen offset:0 sc1 lds
s_cbranch_vccnz .Lzfx13
.Lzfr13:
Mp 128,0,48
s_mov_b32 s56, 0x5508000
buffer_load_dword v4, v7, s[20:23], s56 offen offset:4 sc0 sc1
Rb 48,8,6144
v_add_f32_e32 v12, v12, v14
Mp 136,0,52
v_add_f32_e32 v13, v13, v15
v_mov_b32 v24, v12
Mp 136,4,60
Rb 52,8,22528
v_permlane32_swap_b32_e32 v12, v24
Mp 128,4,56
buffer_load_dwordx4 v2, s[20:23], s40 offen offset:1024 sc1 lds
Mp 132,8,48
Rb 56,8,7168
v_add_f32_e32 v12, v12, v24
Mp 140,8,52
v_mov_b32 v24, v12
v_mov_b32 v25, v13
Mp 140,12,60
Rb 60,8,23552
s_waitcnt lgkmcnt(8)
Mp 132,12,56
buffer_load_dwordx4 v2, s[20:23], s40 offen offset:2048 sc1 lds
Mp 144,16,48
Rb 0,8,8192
v_permlane16_swap_b32_e32 v12, v24
Mp 152,16,52
v_add_f32_e32 v12, v12, v24
)" R"(
v_permlane32_swap_b32_e32 v13, v25
Mp 152,20,60
Rb 4,8,24576
v_add_f32_e32 v13, v13, v25
Mp 144,20,56
buffer_load_dwordx4 v2, s[20:23], s40 offen offset:3072 sc1 lds
Mp 148,24,48
Rb 8,8,9216
v_mov_b32 v25, v13
Mp 156,24,52
s_add_u32 m0, s36, 102400
v_permlane16_swap_b32_e32 v13, v25
Mp 156,28,60
Rb 12,8,25600
s_waitcnt lgkmcnt(8)
Mp 148,28,56
buffer_load_dwordx4 v3, s[20:23], s40 offen offset:0 sc1 lds
Mp 160,32,48
Rb 16,8,10240
v_add_f32_e32 v13, v13, v25
Mp 168,32,52
v_rcp_f32_e32 v12, v12
Mp 168,36,60
Rb 20,8,26624
v_mul_f32_e32 v128, v12, v128
Mp 160,36,56
buffer_load_dwordx4 v3, s[20:23], s40 offen offset:1024 sc1 lds
Mp 164,40,48
Rb 24,8,11264
v_mul_f32_e32 v129, v12, v129
Mp 172,40,52
v_rcp_f32_e32 v13, v13
Mp 172,44,60
Rb 28,8,27648
s_waitcnt lgkmcnt(8)
Mp 164,44,56
buffer_load_dwordx4 v3, s[20:23], s40 offen offset:2048 sc1 lds
Mp 176,48,48
Rb 32,8,12288
Cv 128,128,129
Mp 184,48,52
v_mul_f32_e32 v130, v12, v130
v_mul_f32_e32 v131, v12, v131
Mp 184,52,60
Rb 36,8,28672
Cv 129,130,131
Mp 176,52,56
v_mul_f32_e32 v132, v12, v132
v_mul_f32_e32 v133, v12, v133
Mp 180,56,48
Rb 40,8,13312
Cv 130,132,133
Mp 188,56,52
v_mul_f32_e32 v134, v12, v134
v_mul_f32_e32 v135, v12, v135
Mp 188,60,60
Rb 44,8,29696
s_waitcnt lgkmcnt(8)
Mp 180,60,56
Cv 131,134,135
v_mul_f32_e32 v136, v13, v136
Mp 192,0,48
Rb 48,8,14336
v_mul_f32_e32 v137, v13, v137
)" R"(
Mp 200,0,52
Cv 136,136,137
v_mul_f32_e32 v138, v13, v138
Mp 200,4,60
Rb 52,8,30720
v_mul_f32_e32 v139, v13, v139
Mp 192,4,56
Cv 137,138,139
v_mul_f32_e32 v140, v13, v140
Mp 196,8,48
Rb 56,8,15360
v_mul_f32_e32 v141, v13, v141
Mp 204,8,52
Cv 138,140,141
v_mul_f32_e32 v142, v13, v142
Mp 204,12,60
Rb 60,8,31744
s_waitcnt lgkmcnt(8)
Mp 196,12,56
v_mul_f32_e32 v143, v13, v143
Cv 139,142,143
Mp 208,16,48
Rb 0,1,0
v_mul_f32_e32 v144, v12, v144
Mp 216,16,52
v_mul_f32_e32 v145, v12, v145
Cv 144,144,145
Mp 216,20,60
Rb 4,1,1024
v_mul_f32_e32 v146, v12, v146
Mp 208,20,56
v_mul_f32_e32 v147, v12, v147
Cv 145,146,147
Mp 212,24,48
Rb 8,1,2048
v_mul_f32_e32 v148, v12, v148
Mp 220,24,52
v_mul_f32_e32 v149, v12, v149
Cv 146,148,149
Mp 220,28,60
Rb 12,1,3072
s_waitcnt vmcnt(6) lgkmcnt(4)
s_barrier
Mp 212,28,56
s_add_u32 s40, s67, 0x20000
v_mul_f32_e32 v150, v12, v150
v_cmp_eq_u32_e32 vcc, s3, v4
s_andn2_b64 s[28:29], 1, vcc
s_cbranch_scc1 .Lzsp16
.Lzok16:
Mp 224,32,48
Rb 16,1,4096
s_add_u32 m0, s36, 65536
buffer_load_dword v4, v7, s[20:23], s56 offen sc0 sc1
Mp 232,32,52
buffer_load_dwordx4 v2, s[20:23], s40 offen offset:0 sc1 lds
Mp 232,36,60
Rb 20,1,5120
v_mul_f32_e32 v151, v12, v151
Mp 224,36,56
Cv 147,150,151
v_mul_f32_e32 v152, v13, v152
Mp 228,40,48
Rb 24,1,6144
v_mul_f32_e32 v153, v13, v153
)" R"(
Mp 236,40,52
Cv 152,152,153
v_mul_f32_e32 v154, v13, v154
Mp 236,44,60
Rb 28,1,8192
Mp 228,44,56
buffer_load_dwordx4 v2, s[20:23], s40 offen offset:1024 sc1 lds
Mp 240,48,48
Rb 32,1,9216
v_mul_f32_e32 v155, v13, v155
Mp 248,48,52
Cv 153,154,155
v_mul_f32_e32 v156, v13, v156
Mp 248,52,60
Rb 36,1,10240
v_mul_f32_e32 v157, v13, v157
Mp 240,52,56
Cv 154,156,157
v_mul_f32_e32 v158, v13, v158
Mp 244,56,48
Rb 40,1,11264
v_mul_f32_e32 v159, v13, v159
Mp 252,56,52
buffer_load_dwordx4 v2, s[20:23], s40 offen offset:2048 sc1 lds
Mp 252,60,60
Rb 44,1,12288
s_waitcnt lgkmcnt(8)
Mp 244,60,56
Cv 155,158,159
v_mul_f32_e32 v160, v12, v160
v_mfma_f32_16x16x32_bf16 a[64:67], a[0:3], v[128:131], 0
Rb 48,1,13312
v_mul_f32_e32 v161, v12, v161
v_mfma_f32_16x16x32_bf16 a[72:75], a[0:3], v[136:139], 0
Cv 160,160,161
v_mul_f32_e32 v162, v12, v162
v_mfma_f32_16x16x32_bf16 a[76:79], a[4:7], v[136:139], 0
Rb 52,1,14336
v_mul_f32_e32 v163, v12, v163
v_mfma_f32_16x16x32_bf16 a[68:71], a[4:7], v[128:131], 0
buffer_load_dwordx4 v2, s[20:23], s40 offen offset:3072 sc1 lds
v_mfma_f32_16x16x32_bf16 a[80:83], a[8:11], v[128:131], 0
Rb 56,1,16384
Cv 161,162,163
v_mfma_f32_16x16x32_bf16 a[88:91], a[8:11], v[136:139], 0
v_mul_f32_e32 v164, v12, v164
v_mul_f32_e32 v165, v12, v165
v_mfma_f32_16x16x32_bf16 a[92:95], a[12:15], v[136:139], 0
Rb 60,1,17408
s_waitcnt lgkmcnt(8)
v_mfma_f32_16x16x32_bf16 a[84:87], a[12:15], v[128:131], 0
Cv 162,164,165
v_mul_f32_e32 v166, v12, v166
v_mfma_f32_16x16x32_bf16 a[96:99], a[16:19], v[128:131], 0
Rb 0,1,18432
s_add_u32 m0, s36, 69632
v_mfma_f32_16x16x32_bf16 a[104:107], a[16:19], v[136:139], 0
buffer_load_dwordx4 v3, s[20:23], s40 offen offset:0 sc1 lds
v_mfma_f32_16x16x32_bf16 a[108:111], a[20:23], v[136:139], 0
Rb 4,1,19456
v_mul_f32_e32 v167, v12, v167
v_mfma_f32_16x16x32_bf16 a[100:103], a[20:23], v[128:131], 0
Cv 163,166,167
v_mul_f32_e32 v168, v13, v168
)" R"(
v_mfma_f32_16x16x32_bf16 a[112:115], a[24:27], v[128:131], 0
Rb 8,1,20480
v_mul_f32_e32 v169, v13, v169
v_mfma_f32_16x16x32_bf16 a[120:123], a[24:27], v[136:139], 0
Cv 168,168,169
v_mul_f32_e32 v170, v13, v170
v_mfma_f32_16x16x32_bf16 a[124:127], a[28:31], v[136:139], 0
Rb 12,1,21504
s_waitcnt lgkmcnt(8)
v_mfma_f32_16x16x32_bf16 a[116:119], a[28:31], v[128:131], 0
buffer_load_dwordx4 v3, s[20:23], s40 offen offset:1024 sc1 lds
v_mfma_f32_16x16x32_bf16 a[128:131], a[32:35], v[128:131], 0
Rb 16,1,22528
v_mul_f32_e32 v171, v13, v171
v_mfma_f32_16x16x32_bf16 a[136:139], a[32:35], v[136:139], 0
Cv 169,170,171
v_mul_f32_e32 v172, v13, v172
v_mfma_f32_16x16x32_bf16 a[140:143], a[36:39], v[136:139], 0
Rb 20,1,24576
v_mul_f32_e32 v173, v13, v173
v_mfma_f32_16x16x32_bf16 a[132:135], a[36:39], v[128:131], 0
Cv 170,172,173
v_mul_f32_e32 v174, v13, v174
v_mfma_f32_16x16x32_bf16 a[144:147], a[40:43], v[128:131], 0
Rb 24,1,25600
v_mul_f32_e32 v175, v13, v175
v_mfma_f32_16x16x32_bf16 a[152:155], a[40:43], v[136:139], 0
buffer_load_dwordx4 v3, s[20:23], s40 offen offset:2048 sc1 lds
v_mfma_f32_16x16x32_bf16 a[156:159], a[44:47], v[136:139], 0
Rb 28,1,26624
s_waitcnt lgkmcnt(8)
v_mfma_f32_16x16x32_bf16 a[148:151], a[44:47], v[128:131], 0
Cv 171,174,175
v_mul_f32_e32 v176, v12, v176
v_mfma_f32_16x16x32_bf16 a[160:163], a[48:51], v[128:131], 0
Rb 32,1,27648
v_mul_f32_e32 v177, v12, v177
v_mfma_f32_16x16x32_bf16 a[168:171], a[48:51], v[136:139], 0
Cv 176,176,177
v_mul_f32_e32 v178, v12, v178
v_mfma_f32_16x16x32_bf16 a[172:175], a[52:55], v[136:139], 0
Rb 36,1,28672
v_mul_f32_e32 v179, v12, v179
v_mfma_f32_16x16x32_bf16 a[164:167], a[52:55], v[128:131], 0
Cv 177,178,179
v_mul_f32_e32 v180, v12, v180
v_mfma_f32_16x16x32_bf16 a[64:67], a[56:59], v[144:147], a[64:67]
Rb 40,1,29696
v_mul_f32_e32 v181, v12, v181
v_mfma_f32_16x16x32_bf16 a[72:75], a[56:59], v[152:155], a[72:75]
Cv 178,180,181
v_mul_f32_e32 v182, v12, v182
v_mfma_f32_16x16x32_bf16 a[76:79], a[60:63], v[152:155], a[76:79]
Rb 44,1,30720
s_waitcnt lgkmcnt(8)
v_mfma_f32_16x16x32_bf16 a[68:71], a[60:63], v[144:147], a[68:71]
v_mul_f32_e32 v183, v12, v183
Cv 179,182,183
v_mfma_f32_16x16x32_bf16 a[80:83], a[0:3], v[144:147], a[80:83]
Rb 48,1,32768
v_mul_f32_e32 v184, v13, v184
v_mfma_f32_16x16x32_bf16 a[88:91], a[0:3], v[152:155], a[88:91]
v_mul_f32_e32 v185, v13, v185
Cv 184,184,185
)" R"(
v_mfma_f32_16x16x32_bf16 a[92:95], a[4:7], v[152:155], a[92:95]
Rb 52,1,33792
v_mul_f32_e32 v186, v13, v186
v_mfma_f32_16x16x32_bf16 a[84:87], a[4:7], v[144:147], a[84:87]
v_mul_f32_e32 v187, v13, v187
Cv 185,186,187
v_mfma_f32_16x16x32_bf16 a[96:99], a[8:11], v[144:147], a[96:99]
Rb 56,1,34816
v_mul_f32_e32 v188, v13, v188
v_mfma_f32_16x16x32_bf16 a[104:107], a[8:11], v[152:155], a[104:107]
v_mul_f32_e32 v189, v13, v189
Cv 186,188,189
v_mfma_f32_16x16x32_bf16 a[108:111], a[12:15], v[152:155], a[108:111]
Rb 60,1,35840
s_waitcnt lgkmcnt(4)
s_barrier
v_mfma_f32_16x16x32_bf16 a[100:103], a[12:15], v[144:147], a[100:103]
s_add_u32 s40, s67, 0x28000
v_mul_f32_e32 v190, v13, v190
v_mfma_f32_16x16x32_bf16 a[112:115], a[16:19], v[144:147], a[112:115]
Rb 0,1,36864
s_add_u32 m0, s36, 0
v_mfma_f32_16x16x32_bf16 a[120:123], a[16:19], v[152:155], a[120:123]
buffer_load_dwordx4 v2, s[20:23], s40 offen offset:0 sc1 lds
v_mfma_f32_16x16x32_bf16 a[124:127], a[20:23], v[152:155], a[124:127]
Rb 4,1,37888
v_mul_f32_e32 v191, v13, v191
v_mfma_f32_16x16x32_bf16 a[116:119], a[20:23], v[144:147], a[116:119]
Cv 187,190,191
v_mul_f32_e32 v192, v12, v192
v_mfma_f32_16x16x32_bf16 a[128:131], a[24:27], v[144:147], a[128:131]
Rb 8,1,38912
v_mul_f32_e32 v193, v12, v193
v_mfma_f32_16x16x32_bf16 a[136:139], a[24:27], v[152:155], a[136:139]
Cv 192,192,193
v_mul_f32_e32 v194, v12, v194
v_mfma_f32_16x16x32_bf16 a[140:143], a[28:31], v[152:155], a[140:143]
Rb 12,1,40960
v_mfma_f32_16x16x32_bf16 a[132:135], a[28:31], v[144:147], a[132:135]
buffer_load_dwordx4 v2, s[20:23], s40 offen offset:1024 sc1 lds
v_mfma_f32_16x16x32_bf16 a[144:147], a[32:35], v[144:147], a[144:147]
Rb 16,1,41984
v_mul_f32_e32 v195, v12, v195
v_mfma_f32_16x16x32_bf16 a[152:155], a[32:35], v[152:155], a[152:155]
Cv 193,194,195
v_mul_f32_e32 v196, v12, v196
v_mfma_f32_16x16x32_bf16 a[156:159], a[36:39], v[152:155], a[156:159]
Rb 20,1,43008
v_mul_f32_e32 v197, v12, v197
v_mfma_f32_16x16x32_bf16 a[148:151], a[36:39], v[144:147], a[148:151]
Cv 194,196,197
v_mul_f32_e32 v198, v12, v198
v_mfma_f32_16x16x32_bf16 a[160:163], a[40:43], v[144:147], a[160:163]
Rb 24,1,44032
v_mul_f32_e32 v199, v12, v199
v_mfma_f32_16x16x32_bf16 a[168:171], a[40:43], v[152:155], a[168:171]
buffer_load_dwordx4 v2, s[20:23], s40 offen offset:2048 sc1 lds
v_mfma_f32_16x16x32_bf16 a[172:175], a[44:47], v[152:155], a[172:175]
Rb 28,1,45056
s_waitcnt lgkmcnt(8)
v_mfma_f32_16x16x32_bf16 a[164:167], a[44:47], v[144:147], a[164:167]
Cv 195,198,199
v_mul_f32_e32 v200, v13, v200
v_mfma_f32_16x16x32_bf16 a[64:67], a[48:51], v[160:163], a[64:67]
Rb 32,1,46080
)" R"(
v_mul_f32_e32 v201, v13, v201
v_mfma_f32_16x16x32_bf16 a[72:75], a[48:51], v[168:171], a[72:75]
Cv 200,200,201
v_mul_f32_e32 v202, v13, v202
v_mfma_f32_16x16x32_bf16 a[76:79], a[52:55], v[168:171], a[76:79]
Rb 36,1,47104
v_mul_f32_e32 v203, v13, v203
v_mfma_f32_16x16x32_bf16 a[68:71], a[52:55], v[160:163], a[68:71]
buffer_load_dwordx4 v2, s[20:23], s40 offen offset:3072 sc1 lds
v_mfma_f32_16x16x32_bf16 a[80:83], a[56:59], v[160:163], a[80:83]
Rb 40,1,49152
Cv 201,202,203
v_mfma_f32_16x16x32_bf16 a[88:91], a[56:59], v[168:171], a[88:91]
v_mul_f32_e32 v204, v13, v204
v_mul_f32_e32 v205, v13, v205
v_mfma_f32_16x16x32_bf16 a[92:95], a[60:63], v[168:171], a[92:95]
Rb 44,1,50176
s_waitcnt lgkmcnt(8)
v_mfma_f32_16x16x32_bf16 a[84:87], a[60:63], v[160:163], a[84:87]
Cv 202,204,205
v_mul_f32_e32 v206, v13, v206
v_mfma_f32_16x16x32_bf16 a[96:99], a[0:3], v[160:163], a[96:99]
Rb 48,1,51200
s_add_u32 m0, s36, 4096
v_mfma_f32_16x16x32_bf16 a[104:107], a[0:3], v[168:171], a[104:107]
buffer_load_dwordx4 v3, s[20:23], s40 offen offset:0 sc1 lds
v_mfma_f32_16x16x32_bf16 a[108:111], a[4:7], v[168:171], a[108:111]
Rb 52,1,52224
v_mul_f32_e32 v207, v13, v207
v_mfma_f32_16x16x32_bf16 a[100:103], a[4:7], v[160:163], a[100:103]
Cv 203,206,207
v_mul_f32_e32 v208, v12, v208
v_mfma_f32_16x16x32_bf16 a[112:115], a[8:11], v[160:163], a[112:115]
Rb 56,1,53248
v_mul_f32_e32 v209, v12, v209
v_mfma_f32_16x16x32_bf16 a[120:123], a[8:11], v[168:171], a[120:123]
Cv 208,208,209
v_mul_f32_e32 v210, v12, v210
v_mfma_f32_16x16x32_bf16 a[124:127], a[12:15], v[168:171], a[124:127]
Rb 60,1,54272
s_waitcnt lgkmcnt(8)
v_mfma_f32_16x16x32_bf16 a[116:119], a[12:15], v[160:163], a[116:119]
buffer_load_dwordx4 v3, s[20:23], s40 offen offset:1024 sc1 lds
v_mfma_f32_16x16x32_bf16 a[128:131], a[16:19], v[160:163], a[128:131]
Rb 0,1,55296
v_mul_f32_e32 v211, v12, v211
v_mfma_f32_16x16x32_bf16 a[136:139], a[16:19], v[168:171], a[136:139]
Cv 209,210,211
v_mul_f32_e32 v212, v12, v212
v_mfma_f32_16x16x32_bf16 a[140:143], a[20:23], v[168:171], a[140:143]
Rb 4,1,57344
v_mul_f32_e32 v213, v12, v213
v_mfma_f32_16x16x32_bf16 a[132:135], a[20:23], v[160:163], a[132:135]
Cv 210,212,213
v_mul_f32_e32 v214, v12, v214
v_mfma_f32_16x16x32_bf16 a[144:147], a[24:27], v[160:163], a[144:147]
Rb 8,1,58368
v_mul_f32_e32 v215, v12, v215
v_mfma_f32_16x16x32_bf16 a[152:155], a[24:27], v[168:171], a[152:155]
buffer_load_dwordx4 v3, s[20:23], s40 offen offset:2048 sc1 lds
v_mfma_f32_16x16x32_bf16 a[156:159], a[28:31], v[168:171], a[156:159]
Rb 12,1,59392
s_waitcnt lgkmcnt(8)
v_mfma_f32_16x16x32_bf16 a[148:151], a[28:31], v[160:163], a[148:151]
)" R"(
Cv 211,214,215
v_mul_f32_e32 v216, v13, v216
v_mfma_f32_16x16x32_bf16 a[160:163], a[32:35], v[160:163], a[160:163]
Rb 16,1,60416
v_mul_f32_e32 v217, v13, v217
v_mfma_f32_16x16x32_bf16 a[168:171], a[32:35], v[168:171], a[168:171]
Cv 216,216,217
v_mul_f32_e32 v218, v13, v218
v_mfma_f32_16x16x32_bf16 a[172:175], a[36:39], v[168:171], a[172:175]
Rb 20,1,61440
v_mul_f32_e32 v219, v13, v219
v_mfma_f32_16x16x32_bf16 a[164:167], a[36:39], v[160:163], a[164:167]
Cv 217,218,219
v_mul_f32_e32 v220, v13, v220
v_mfma_f32_16x16x32_bf16 a[64:67], a[40:43], v[176:179], a[64:67]
Rb 24,1,62464
v_mul_f32_e32 v221, v13, v221
v_mfma_f32_16x16x32_bf16 a[72:75], a[40:43], v[184:187], a[72:75]
Cv 218,220,221
v_mul_f32_e32 v222, v13, v222
v_mfma_f32_16x16x32_bf16 a[76:79], a[44:47], v[184:187], a[76:79]
Rb 28,1,63488
s_waitcnt lgkmcnt(8)
v_mfma_f32_16x16x32_bf16 a[68:71], a[44:47], v[176:179], a[68:71]
v_mul_f32_e32 v223, v13, v223
Cv 219,222,223
v_mfma_f32_16x16x32_bf16 a[80:83], a[48:51], v[176:179], a[80:83]
Rb 32,10,0
v_mul_f32_e32 v224, v12, v224
v_mfma_f32_16x16x32_bf16 a[88:91], a[48:51], v[184:187], a[88:91]
v_mul_f32_e32 v225, v12, v225
Cv 224,224,225
v_mfma_f32_16x16x32_bf16 a[92:95], a[52:55], v[184:187], a[92:95]
Rb 36,10,1024
v_mul_f32_e32 v226, v12, v226
v_mfma_f32_16x16x32_bf16 a[84:87], a[52:55], v[176:179], a[84:87]
v_mul_f32_e32 v227, v12, v227
Cv 225,226,227
v_mfma_f32_16x16x32_bf16 a[96:99], a[56:59], v[176:179], a[96:99]
Rb 40,10,2048
v_mul_f32_e32 v228, v12, v228
v_mfma_f32_16x16x32_bf16 a[104:107], a[56:59], v[184:187], a[104:107]
v_mul_f32_e32 v229, v12, v229
Cv 226,228,229
v_mfma_f32_16x16x32_bf16 a[108:111], a[60:63], v[184:187], a[108:111]
Rb 44,10,3072
s_waitcnt vmcnt(14) lgkmcnt(4)
s_barrier
v_mfma_f32_16x16x32_bf16 a[100:103], a[60:63], v[176:179], a[100:103]
s_add_u32 s40, s67, 0x30000
v_mul_f32_e32 v230, v12, v230
v_readfirstlane_b32 s84, v4
v_mfma_f32_16x16x32_bf16 a[112:115], a[0:3], v[176:179], a[112:115]
Rb 48,10,4096
s_add_u32 m0, s36, 32768
v_mfma_f32_16x16x32_bf16 a[120:123], a[0:3], v[184:187], a[120:123]
buffer_load_dwordx4 v2, s[20:23], s40 offen offset:0 sc1 lds
v_mfma_f32_16x16x32_bf16 a[124:127], a[4:7], v[184:187], a[124:127]
Rb 52,10,5120
v_mul_f32_e32 v231, v12, v231
v_mfma_f32_16x16x32_bf16 a[116:119], a[4:7], v[176:179], a[116:119]
Cv 227,230,231
v_mul_f32_e32 v232, v13, v232
v_mfma_f32_16x16x32_bf16 a[128:131], a[8:11], v[176:179], a[128:131]
Rb 56,10,6144
)" R"(
v_mul_f32_e32 v233, v13, v233
v_mfma_f32_16x16x32_bf16 a[136:139], a[8:11], v[184:187], a[136:139]
Cv 232,232,233
v_mul_f32_e32 v234, v13, v234
v_mfma_f32_16x16x32_bf16 a[140:143], a[12:15], v[184:187], a[140:143]
Rb 60,10,8192
v_mfma_f32_16x16x32_bf16 a[132:135], a[12:15], v[176:179], a[132:135]
buffer_load_dwordx4 v2, s[20:23], s40 offen offset:1024 sc1 lds
v_mfma_f32_16x16x32_bf16 a[144:147], a[16:19], v[176:179], a[144:147]
Rb 0,10,9216
v_mul_f32_e32 v235, v13, v235
v_mfma_f32_16x16x32_bf16 a[152:155], a[16:19], v[184:187], a[152:155]
Cv 233,234,235
v_mul_f32_e32 v236, v13, v236
v_mfma_f32_16x16x32_bf16 a[156:159], a[20:23], v[184:187], a[156:159]
Rb 4,10,10240
v_mul_f32_e32 v237, v13, v237
v_mfma_f32_16x16x32_bf16 a[148:151], a[20:23], v[176:179], a[148:151]
Cv 234,236,237
v_mul_f32_e32 v238, v13, v238
v_mfma_f32_16x16x32_bf16 a[160:163], a[24:27], v[176:179], a[160:163]
Rb 8,10,11264
v_mul_f32_e32 v239, v13, v239
v_mfma_f32_16x16x32_bf16 a[168:171], a[24:27], v[184:187], a[168:171]
buffer_load_dwordx4 v2, s[20:23], s40 offen offset:2048 sc1 lds
v_mfma_f32_16x16x32_bf16 a[172:175], a[28:31], v[184:187], a[172:175]
Rb 12,10,12288
s_waitcnt lgkmcnt(8)
v_mfma_f32_16x16x32_bf16 a[164:167], a[28:31], v[176:179], a[164:167]
Cv 235,238,239
v_mul_f32_e32 v240, v12, v240
v_mfma_f32_16x16x32_bf16 a[64:67], a[32:35], v[192:195], a[64:67]
Rb 16,10,13312
v_mul_f32_e32 v241, v12, v241
v_mfma_f32_16x16x32_bf16 a[72:75], a[32:35], v[200:203], a[72:75]
Cv 240,240,241
v_mul_f32_e32 v242, v12, v242
v_mfma_f32_16x16x32_bf16 a[76:79], a[36:39], v[200:203], a[76:79]
Rb 20,10,14336
v_mul_f32_e32 v243, v12, v243
v_mfma_f32_16x16x32_bf16 a[68:71], a[36:39], v[192:195], a[68:71]
buffer_load_dwordx4 v2, s[20:23], s40 offen offset:3072 sc1 lds
v_mfma_f32_16x16x32_bf16 a[80:83], a[40:43], v[192:195], a[80:83]
Rb 24,10,16384
Cv 241,242,243
v_mfma_f32_16x16x32_bf16 a[88:91], a[40:43], v[200:203], a[88:91]
v_mul_f32_e32 v244, v12, v244
v_mul_f32_e32 v245, v12, v245
v_mfma_f32_16x16x32_bf16 a[92:95], a[44:47], v[200:203], a[92:95]
Rb 28,10,17408
s_waitcnt lgkmcnt(8)
v_mfma_f32_16x16x32_bf16 a[84:87], a[44:47], v[192:195], a[84:87]
Cv 242,244,245
v_mul_f32_e32 v246, v12, v246
v_mfma_f32_16x16x32_bf16 a[96:99], a[48:51], v[192:195], a[96:99]
Rb 32,10,18432
s_add_u32 m0, s36, 36864
v_mfma_f32_16x16x32_bf16 a[104:107], a[48:51], v[200:203], a[104:107]
buffer_load_dwordx4 v3, s[20:23], s40 offen offset:0 sc1 lds
v_mfma_f32_16x16x32_bf16 a[108:111], a[52:55], v[200:203], a[108:111]
Rb 36,10,19456
v_mul_f32_e32 v247, v12, v247
v_mfma_f32_16x16x32_bf16 a[100:103], a[52:55], v[192:195], a[100:103]
Cv 243,246,247
)" R"(
v_mul_f32_e32 v248, v13, v248
v_mfma_f32_16x16x32_bf16 a[112:115], a[56:59], v[192:195], a[112:115]
Rb 40,10,20480
v_mul_f32_e32 v249, v13, v249
v_mfma_f32_16x16x32_bf16 a[120:123], a[56:59], v[200:203], a[120:123]
Cv 248,248,249
v_mul_f32_e32 v250, v13, v250
v_mfma_f32_16x16x32_bf16 a[124:127], a[60:63], v[200:203], a[124:127]
Rb 44,10,21504
s_waitcnt lgkmcnt(8)
v_mfma_f32_16x16x32_bf16 a[116:119], a[60:63], v[192:195], a[116:119]
buffer_load_dwordx4 v3, s[20:23], s40 offen offset:1024 sc1 lds
v_mfma_f32_16x16x32_bf16 a[128:131], a[0:3], v[192:195], a[128:131]
Rb 48,10,22528
v_mul_f32_e32 v251, v13, v251
v_mfma_f32_16x16x32_bf16 a[136:139], a[0:3], v[200:203], a[136:139]
Cv 249,250,251
v_mul_f32_e32 v252, v13, v252
v_mfma_f32_16x16x32_bf16 a[140:143], a[4:7], v[200:203], a[140:143]
Rb 52,10,24576
v_mul_f32_e32 v253, v13, v253
v_mfma_f32_16x16x32_bf16 a[132:135], a[4:7], v[192:195], a[132:135]
Cv 250,252,253
v_mul_f32_e32 v254, v13, v254
v_mfma_f32_16x16x32_bf16 a[144:147], a[8:11], v[192:195], a[144:147]
Rb 56,10,25600
v_mul_f32_e32 v255, v13, v255
v_mfma_f32_16x16x32_bf16 a[152:155], a[8:11], v[200:203], a[152:155]
buffer_load_dwordx4 v3, s[20:23], s40 offen offset:2048 sc1 lds
v_mfma_f32_16x16x32_bf16 a[156:159], a[12:15], v[200:203], a[156:159]
Rb 60,10,26624
s_waitcnt lgkmcnt(8)
v_mfma_f32_16x16x32_bf16 a[148:151], a[12:15], v[192:195], a[148:151]
Cv 251,254,255
v_mfma_f32_16x16x32_bf16 a[160:163], a[16:19], v[192:195], a[160:163]
Rb 0,10,27648
v_mfma_f32_16x16x32_bf16 a[168:171], a[16:19], v[200:203], a[168:171]
v_mfma_f32_16x16x32_bf16 a[172:175], a[20:23], v[200:203], a[172:175]
Rb 4,10,28672
v_mfma_f32_16x16x32_bf16 a[164:167], a[20:23], v[192:195], a[164:167]
v_mfma_f32_16x16x32_bf16 a[64:67], a[24:27], v[208:211], a[64:67]
Rb 8,10,29696
v_mfma_f32_16x16x32_bf16 a[72:75], a[24:27], v[216:219], a[72:75]
v_mfma_f32_16x16x32_bf16 a[76:79], a[28:31], v[216:219], a[76:79]
Rb 12,10,30720
s_waitcnt lgkmcnt(8)
v_mfma_f32_16x16x32_bf16 a[68:71], a[28:31], v[208:211], a[68:71]
v_mfma_f32_16x16x32_bf16 a[80:83], a[32:35], v[208:211], a[80:83]
Rb 16,8,32768
v_mfma_f32_16x16x32_bf16 a[88:91], a[32:35], v[216:219], a[88:91]
v_mfma_f32_16x16x32_bf16 a[92:95], a[36:39], v[216:219], a[92:95]
Rb 20,8,33792
v_mfma_f32_16x16x32_bf16 a[84:87], a[36:39], v[208:211], a[84:87]
v_mfma_f32_16x16x32_bf16 a[96:99], a[40:43], v[208:211], a[96:99]
Rb 24,8,34816
v_mfma_f32_16x16x32_bf16 a[104:107], a[40:43], v[216:219], a[104:107]
v_mfma_f32_16x16x32_bf16 a[108:111], a[44:47], v[216:219], a[108:111]
Rb 28,8,35840
s_waitcnt vmcnt(14) lgkmcnt(4)
s_barrier
v_mfma_f32_16x16x32_bf16 a[100:103], a[44:47], v[208:211], a[100:103]
s_add_u32 s40, s67, 0x38000
v_mfma_f32_16x16x32_bf16 a[112:115], a[48:51], v[208:211], a[112:115]
Rb 32,8,36864
s_add_u32 m0, s36, 131072
)" R"(
v_mfma_f32_16x16x32_bf16 a[120:123], a[48:51], v[216:219], a[120:123]
buffer_load_dwordx4 v2, s[20:23], s40 offen offset:0 sc1 lds
v_mfma_f32_16x16x32_bf16 a[124:127], a[52:55], v[216:219], a[124:127]
Rb 36,8,37888
v_mfma_f32_16x16x32_bf16 a[116:119], a[52:55], v[208:211], a[116:119]
v_mfma_f32_16x16x32_bf16 a[128:131], a[56:59], v[208:211], a[128:131]
Rb 40,8,38912
v_mfma_f32_16x16x32_bf16 a[136:139], a[56:59], v[216:219], a[136:139]
v_mfma_f32_16x16x32_bf16 a[140:143], a[60:63], v[216:219], a[140:143]
Rb 44,8,40960
v_mfma_f32_16x16x32_bf16 a[132:135], a[60:63], v[208:211], a[132:135]
buffer_load_dwordx4 v2, s[20:23], s40 offen offset:1024 sc1 lds
v_mfma_f32_16x16x32_bf16 a[144:147], a[0:3], v[208:211], a[144:147]
Rb 48,8,41984
v_mfma_f32_16x16x32_bf16 a[152:155], a[0:3], v[216:219], a[152:155]
v_mfma_f32_16x16x32_bf16 a[156:159], a[4:7], v[216:219], a[156:159]
Rb 52,8,43008
v_mfma_f32_16x16x32_bf16 a[148:151], a[4:7], v[208:211], a[148:151]
v_mfma_f32_16x16x32_bf16 a[160:163], a[8:11], v[208:211], a[160:163]
Rb 56,8,44032
v_mfma_f32_16x16x32_bf16 a[168:171], a[8:11], v[216:219], a[168:171]
buffer_load_dwordx4 v2, s[20:23], s40 offen offset:2048 sc1 lds
v_mfma_f32_16x16x32_bf16 a[172:175], a[12:15], v[216:219], a[172:175]
Rb 60,8,45056
s_waitcnt lgkmcnt(8)
v_mfma_f32_16x16x32_bf16 a[164:167], a[12:15], v[208:211], a[164:167]
v_mfma_f32_16x16x32_bf16 a[64:67], a[16:19], v[224:227], a[64:67]
Rb 0,8,46080
v_mfma_f32_16x16x32_bf16 a[72:75], a[16:19], v[232:235], a[72:75]
v_mfma_f32_16x16x32_bf16 a[76:79], a[20:23], v[232:235], a[76:79]
Rb 4,8,47104
v_mfma_f32_16x16x32_bf16 a[68:71], a[20:23], v[224:227], a[68:71]
buffer_load_dwordx4 v2, s[20:23], s40 offen offset:3072 sc1 lds
v_mfma_f32_16x16x32_bf16 a[80:83], a[24:27], v[224:227], a[80:83]
Rb 8,8,49152
v_mfma_f32_16x16x32_bf16 a[88:91], a[24:27], v[232:235], a[88:91]
v_mfma_f32_16x16x32_bf16 a[92:95], a[28:31], v[232:235], a[92:95]
Rb 12,8,50176
s_waitcnt lgkmcnt(8)
v_mfma_f32_16x16x32_bf16 a[84:87], a[28:31], v[224:227], a[84:87]
v_mfma_f32_16x16x32_bf16 a[96:99], a[32:35], v[224:227], a[96:99]
Rb 16,8,51200
s_add_u32 m0, s36, 135168
v_mfma_f32_16x16x32_bf16 a[104:107], a[32:35], v[232:235], a[104:107]
buffer_load_dwordx4 v3, s[20:23], s40 offen offset:0 sc1 lds
v_mfma_f32_16x16x32_bf16 a[108:111], a[36:39], v[232:235], a[108:111]
Rb 20,8,52224
v_mfma_f32_16x16x32_bf16 a[100:103], a[36:39], v[224:227], a[100:103]
v_mfma_f32_16x16x32_bf16 a[112:115], a[40:43], v[224:227], a[112:115]
Rb 24,8,53248
v_mfma_f32_16x16x32_bf16 a[120:123], a[40:43], v[232:235], a[120:123]
v_mfma_f32_16x16x32_bf16 a[124:127], a[44:47], v[232:235], a[124:127]
Rb 28,8,54272
s_waitcnt lgkmcnt(8)
v_mfma_f32_16x16x32_bf16 a[116:119], a[44:47], v[224:227], a[116:119]
buffer_load_dwordx4 v3, s[20:23], s40 offen offset:1024 sc1 lds
v_mfma_f32_16x16x32_bf16 a[128:131], a[48:51], v[224:227], a[128:131]
Rb 32,8,55296
v_mfma_f32_16x16x32_bf16 a[136:139], a[48:51], v[232:235], a[136:139]
v_mfma_f32_16x16x32_bf16 a[140:143], a[52:55], v[232:235], a[140:143]
Rb 36,8,57344
v_mfma_f32_16x16x32_bf16 a[132:135], a[52:55], v[224:227], a[132:135]
v_mfma_f32_16x16x32_bf16 a[144:147], a[56:59], v[224:227], a[144:147]
Rb 40,8,58368
)" R"(
v_mfma_f32_16x16x32_bf16 a[152:155], a[56:59], v[232:235], a[152:155]
buffer_load_dwordx4 v3, s[20:23], s40 offen offset:2048 sc1 lds
v_mfma_f32_16x16x32_bf16 a[156:159], a[60:63], v[232:235], a[156:159]
Rb 44,8,59392
s_waitcnt lgkmcnt(8)
v_mfma_f32_16x16x32_bf16 a[148:151], a[60:63], v[224:227], a[148:151]
v_mfma_f32_16x16x32_bf16 a[160:163], a[0:3], v[224:227], a[160:163]
Rb 48,8,60416
v_mfma_f32_16x16x32_bf16 a[168:171], a[0:3], v[232:235], a[168:171]
v_mfma_f32_16x16x32_bf16 a[172:175], a[4:7], v[232:235], a[172:175]
Rb 52,8,61440
v_mfma_f32_16x16x32_bf16 a[164:167], a[4:7], v[224:227], a[164:167]
v_mfma_f32_16x16x32_bf16 a[64:67], a[8:11], v[240:243], a[64:67]
Rb 56,8,62464
v_mfma_f32_16x16x32_bf16 a[72:75], a[8:11], v[248:251], a[72:75]
v_mfma_f32_16x16x32_bf16 a[76:79], a[12:15], v[248:251], a[76:79]
Rb 60,8,63488
s_waitcnt lgkmcnt(8)
v_mfma_f32_16x16x32_bf16 a[68:71], a[12:15], v[240:243], a[68:71]
v_mfma_f32_16x16x32_bf16 a[80:83], a[16:19], v[240:243], a[80:83]
Rb 0,8,0
v_mfma_f32_16x16x32_bf16 a[88:91], a[16:19], v[248:251], a[88:91]
v_mfma_f32_16x16x32_bf16 a[92:95], a[20:23], v[248:251], a[92:95]
Rb 4,8,1024
v_mfma_f32_16x16x32_bf16 a[84:87], a[20:23], v[240:243], a[84:87]
v_mfma_f32_16x16x32_bf16 a[96:99], a[24:27], v[240:243], a[96:99]
Rb 8,8,2048
v_mfma_f32_16x16x32_bf16 a[104:107], a[24:27], v[248:251], a[104:107]
v_mfma_f32_16x16x32_bf16 a[108:111], a[28:31], v[248:251], a[108:111]
Rb 12,8,3072
s_waitcnt vmcnt(14) lgkmcnt(8)
s_barrier
v_mfma_f32_16x16x32_bf16 a[100:103], a[28:31], v[240:243], a[100:103]
v_mfma_f32_16x16x32_bf16 a[112:115], a[32:35], v[240:243], a[112:115]
Rb 16,8,4096
v_mfma_f32_16x16x32_bf16 a[120:123], a[32:35], v[248:251], a[120:123]
v_mfma_f32_16x16x32_bf16 a[124:127], a[36:39], v[248:251], a[124:127]
Rb 20,8,5120
v_mfma_f32_16x16x32_bf16 a[116:119], a[36:39], v[240:243], a[116:119]
v_mfma_f32_16x16x32_bf16 a[128:131], a[40:43], v[240:243], a[128:131]
Rb 24,8,6144
v_mfma_f32_16x16x32_bf16 a[136:139], a[40:43], v[248:251], a[136:139]
v_mfma_f32_16x16x32_bf16 a[140:143], a[44:47], v[248:251], a[140:143]
Rb 28,8,8192
s_waitcnt lgkmcnt(8)
v_mfma_f32_16x16x32_bf16 a[132:135], a[44:47], v[240:243], a[132:135]
v_mfma_f32_16x16x32_bf16 a[144:147], a[48:51], v[240:243], a[144:147]
Rb 32,8,9216
v_mfma_f32_16x16x32_bf16 a[152:155], a[48:51], v[248:251], a[152:155]
v_mfma_f32_16x16x32_bf16 a[156:159], a[52:55], v[248:251], a[156:159]
Rb 36,8,10240
v_mfma_f32_16x16x32_bf16 a[148:151], a[52:55], v[240:243], a[148:151]
v_mfma_f32_16x16x32_bf16 a[160:163], a[56:59], v[240:243], a[160:163]
Rb 40,8,11264
v_mfma_f32_16x16x32_bf16 a[168:171], a[56:59], v[248:251], a[168:171]
v_mfma_f32_16x16x32_bf16 a[172:175], a[60:63], v[248:251], a[172:175]
Rb 44,8,12288
s_waitcnt lgkmcnt(8)
v_mfma_f32_16x16x32_bf16 a[164:167], a[60:63], v[240:243], a[164:167]
v_mfma_f32_16x16x32_bf16 v[32:35], a[0:3], v[128:131], 0
Rb 48,8,13312
v_mfma_f32_16x16x32_bf16 v[40:43], a[0:3], v[136:139], 0
Mp 40,4,152
Rb 52,8,14336
v_accvgpr_read_b32 v112, a64
)" R"(
Mp 32,4,144
v_accvgpr_read_b32 v24, a65
Cv 112,112,24
Mp 32,8,160
Rb 56,8,16384
v_accvgpr_read_b32 v113, a66
Mp 40,8,168
v_accvgpr_read_b32 v25, a67
Cv 113,113,25
Mp 40,12,184
Rb 60,8,17408
s_waitcnt lgkmcnt(8)
Mp 32,12,176
v_accvgpr_read_b32 v114, a68
v_accvgpr_read_b32 v26, a69
Mp 32,16,192
Rb 0,8,18432
Cv 114,114,26
Mp 40,16,200
v_accvgpr_read_b32 v115, a70
v_accvgpr_read_b32 v27, a71
Mp 40,20,216
Rb 4,8,19456
Cv 115,115,27
Mp 32,20,208
s_add_u32 s29, s73, 0x0
buffer_store_dwordx4 v[112:115], v1, s[20:23], s29 offen sc0 sc1
Mp 32,24,224
Rb 8,8,20480
v_accvgpr_read_b32 v116, a72
Mp 40,24,232
v_accvgpr_read_b32 v24, a73
Cv 116,116,24
Mp 40,28,248
Rb 12,8,21504
s_waitcnt lgkmcnt(8)
Mp 32,28,240
v_accvgpr_read_b32 v117, a74
v_accvgpr_read_b32 v25, a75
v_mfma_f32_16x16x32_bf16 v[36:39], a[32:35], v[128:131], 0
Rb 16,8,22528
Cv 117,117,25
v_mfma_f32_16x16x32_bf16 v[44:47], a[32:35], v[136:139], 0
v_accvgpr_read_b32 v118, a76
v_accvgpr_read_b32 v26, a77
Cv 118,118,26
Mp 44,36,152
Rb 20,8,24576
v_accvgpr_read_b32 v119, a78
Mp 36,36,144
v_accvgpr_read_b32 v27, a79
Cv 119,119,27
Mp 36,40,160
Rb 24,8,25600
v_accvgpr_read_b32 v120, a80
Mp 44,40,168
s_add_u32 s29, s73, 0x400
buffer_store_dwordx4 v[116:119], v1, s[20:23], s29 offen sc0 sc1
Mp 44,44,184
Rb 28,8,26624
s_waitcnt lgkmcnt(8)
Mp 36,44,176
v_accvgpr_read_b32 v24, a81
Cv 120,120,24
)" R"(
Mp 36,48,192
Rb 32,8,27648
v_accvgpr_read_b32 v121, a82
Mp 44,48,200
v_accvgpr_read_b32 v25, a83
Cv 121,121,25
Mp 44,52,216
Rb 36,8,28672
v_accvgpr_read_b32 v122, a84
Mp 36,52,208
v_accvgpr_read_b32 v26, a85
Cv 122,122,26
Mp 36,56,224
Rb 40,8,29696
v_accvgpr_read_b32 v123, a86
Mp 44,56,232
v_accvgpr_read_b32 v27, a87
Cv 123,123,27
Mp 44,60,248
Rb 44,8,30720
s_waitcnt lgkmcnt(8)
Mp 36,60,240
s_add_u32 s29, s73, 0x800
buffer_store_dwordx4 v[120:123], v1, s[20:23], s29 offen sc0 sc1
v_mfma_f32_16x16x32_bf16 v[48:51], a[0:3], v[128:131], 0
Rb 48,1,0
v_accvgpr_read_b32 v124, a88
v_mfma_f32_16x16x32_bf16 v[56:59], a[0:3], v[136:139], 0
v_accvgpr_read_b32 v24, a89
Cv 124,124,24
v_accvgpr_read_b32 v125, a90
Mp 56,4,152
Rb 52,1,1024
v_accvgpr_read_b32 v25, a91
Mp 48,4,144
Cv 125,125,25
v_accvgpr_read_b32 v126, a92
Mp 48,8,160
Rb 56,1,2048
Cv 32,32,33
Mp 56,8,168
Cv 33,34,35
Cv 34,36,37
Mp 56,12,184
Rb 60,1,3072
s_waitcnt vmcnt(10) lgkmcnt(8)
s_barrier
Mp 48,12,176
Cv 35,38,39
v_accvgpr_read_b32 v26, a93
Mp 48,16,192
Rb 0,1,4096
Cv 126,126,26
Mp 56,16,200
s_add_u32 s29, s73, 0x3800
buffer_store_dwordx4 v[32:35], v1, s[20:23], s29 offen sc0 sc1
Mp 56,20,216
Rb 4,1,5120
v_accvgpr_read_b32 v127, a94
Mp 48,20,208
v_accvgpr_read_b32 v27, a95
Cv 127,127,27
Mp 48,24,224
Rb 8,1,6144
v_accvgpr_read_b32 v112, a96
)" R"(
Mp 56,24,232
s_add_u32 s29, s73, 0xc00
buffer_store_dwordx4 v[124:127], v1, s[20:23], s29 offen sc0 sc1
Mp 56,28,248
Rb 12,1,8192
s_waitcnt lgkmcnt(8)
Mp 48,28,240
v_accvgpr_read_b32 v24, a97
Cv 112,112,24
v_mfma_f32_16x16x32_bf16 v[52:55], a[32:35], v[128:131], 0
Rb 16,1,9216
Cv 40,40,41
v_mfma_f32_16x16x32_bf16 v[60:63], a[32:35], v[136:139], 0
Cv 41,42,43
Cv 42,44,45
v_accvgpr_read_b32 v113, a98
Mp 60,36,152
Rb 20,1,10240
Cv 43,46,47
Mp 52,36,144
s_add_u32 s29, s73, 0x3c00
buffer_store_dwordx4 v[40:43], v1, s[20:23], s29 offen sc0 sc1
Mp 52,40,160
Rb 24,1,11264
v_accvgpr_read_b32 v25, a99
Mp 60,40,168
Cv 113,113,25
v_accvgpr_read_b32 v114, a100
Mp 60,44,184
Rb 28,1,12288
s_waitcnt lgkmcnt(8)
Mp 52,44,176
v_accvgpr_read_b32 v26, a101
Cv 114,114,26
Mp 52,48,192
Rb 32,1,13312
v_accvgpr_read_b32 v115, a102
Mp 60,48,200
v_accvgpr_read_b32 v27, a103
Cv 115,115,27
Mp 60,52,216
Rb 36,1,14336
v_accvgpr_read_b32 v116, a104
Mp 52,52,208
s_add_u32 s29, s73, 0x1000
buffer_store_dwordx4 v[112:115], v1, s[20:23], s29 offen sc0 sc1
Mp 52,56,224
Rb 40,1,16384
v_accvgpr_read_b32 v24, a105
Mp 60,56,232
Cv 116,116,24
v_accvgpr_read_b32 v117, a106
Mp 60,60,248
Rb 44,1,17408
s_waitcnt lgkmcnt(8)
Mp 52,60,240
v_accvgpr_read_b32 v25, a107
Cv 117,117,25
v_mfma_f32_16x16x32_bf16 v[64:67], a[0:3], v[128:131], 0
Rb 48,1,18432
v_accvgpr_read_b32 v118, a108
v_mfma_f32_16x16x32_bf16 v[72:75], a[0:3], v[136:139], 0
v_accvgpr_read_b32 v26, a109
Cv 118,118,26
)" R"(
v_accvgpr_read_b32 v119, a110
Mp 72,4,152
Rb 52,1,19456
v_accvgpr_read_b32 v27, a111
Mp 64,4,144
Cv 119,119,27
v_accvgpr_read_b32 v120, a112
Mp 64,8,160
Rb 56,1,20480
Cv 48,48,49
Mp 72,8,168
Cv 49,50,51
Cv 50,52,53
Mp 72,12,184
Rb 60,1,21504
s_waitcnt lgkmcnt(8)
Mp 64,12,176
Cv 51,54,55
v_accvgpr_read_b32 v24, a113
Mp 64,16,192
Rb 0,1,22528
Cv 120,120,24
Mp 72,16,200
s_add_u32 s29, s73, 0x4000
buffer_store_dwordx4 v[48:51], v1, s[20:23], s29 offen sc0 sc1
Mp 72,20,216
Rb 4,1,24576
v_accvgpr_read_b32 v121, a114
Mp 64,20,208
s_add_u32 s29, s73, 0x1400
buffer_store_dwordx4 v[116:119], v1, s[20:23], s29 offen sc0 sc1
Mp 64,24,224
Rb 8,1,25600
v_accvgpr_read_b32 v25, a115
Mp 72,24,232
Cv 121,121,25
v_accvgpr_read_b32 v122, a116
Mp 72,28,248
Rb 12,1,26624
s_waitcnt lgkmcnt(8)
Mp 64,28,240
v_accvgpr_read_b32 v26, a117
Cv 122,122,26
v_mfma_f32_16x16x32_bf16 v[68:71], a[32:35], v[128:131], 0
Rb 16,1,27648
Cv 56,56,57
v_mfma_f32_16x16x32_bf16 v[76:79], a[32:35], v[136:139], 0
Cv 57,58,59
Cv 58,60,61
v_accvgpr_read_b32 v123, a118
Mp 76,36,152
Rb 20,1,28672
Cv 59,62,63
Mp 68,36,144
s_add_u32 s29, s73, 0x4400
buffer_store_dwordx4 v[56:59], v1, s[20:23], s29 offen sc0 sc1
Mp 68,40,160
Rb 24,1,29696
v_accvgpr_read_b32 v27, a119
Mp 76,40,168
Cv 123,123,27
v_accvgpr_read_b32 v124, a120
Mp 76,44,184
Rb 28,1,30720
)" R"(
s_waitcnt lgkmcnt(8)
Mp 68,44,176
s_add_u32 s29, s73, 0x1800
buffer_store_dwordx4 v[120:123], v1, s[20:23], s29 offen sc0 sc1
Mp 68,48,192
Rb 32,1,32768
v_accvgpr_read_b32 v24, a121
Mp 76,48,200
Cv 124,124,24
v_accvgpr_read_b32 v125, a122
Mp 76,52,216
Rb 36,1,33792
v_accvgpr_read_b32 v25, a123
Mp 68,52,208
Cv 125,125,25
v_accvgpr_read_b32 v126, a124
Mp 68,56,224
Rb 40,1,34816
v_accvgpr_read_b32 v26, a125
Mp 76,56,232
Cv 126,126,26
v_accvgpr_read_b32 v127, a126
Mp 76,60,248
Rb 44,1,35840
s_waitcnt vmcnt(11) lgkmcnt(8)
s_barrier
Mp 68,60,240
v_accvgpr_read_b32 v27, a127
Cv 127,127,27
v_mfma_f32_16x16x32_bf16 v[80:83], a[0:3], v[128:131], 0
Rb 48,1,36864
v_accvgpr_read_b32 v112, a128
v_mfma_f32_16x16x32_bf16 v[88:91], a[0:3], v[136:139], 0
s_add_u32 s29, s73, 0x1c00
buffer_store_dwordx4 v[124:127], v1, s[20:23], s29 offen sc0 sc1
Mp 88,4,152
Rb 52,1,37888
v_accvgpr_read_b32 v24, a129
Mp 80,4,144
Cv 112,112,24
v_accvgpr_read_b32 v113, a130
Mp 80,8,160
Rb 56,1,38912
Cv 64,64,65
Mp 88,8,168
Cv 65,66,67
Cv 66,68,69
Mp 88,12,184
Rb 60,1,40960
s_waitcnt lgkmcnt(8)
Mp 80,12,176
Cv 67,70,71
v_accvgpr_read_b32 v25, a131
Mp 80,16,192
Rb 0,1,41984
Cv 113,113,25
Mp 88,16,200
s_add_u32 s29, s73, 0x4800
buffer_store_dwordx4 v[64:67], v1, s[20:23], s29 offen sc0 sc1
Mp 88,20,216
Rb 4,1,43008
v_accvgpr_read_b32 v114, a132
Mp 80,20,208
v_accvgpr_read_b32 v26, a133
Cv 114,114,26
)" R"(
Mp 80,24,224
Rb 8,1,44032
v_accvgpr_read_b32 v115, a134
Mp 88,24,232
v_accvgpr_read_b32 v27, a135
Cv 115,115,27
Mp 88,28,248
Rb 12,1,45056
s_waitcnt lgkmcnt(8)
Mp 80,28,240
s_add_u32 s29, s73, 0x2000
buffer_store_dwordx4 v[112:115], v1, s[20:23], s29 offen sc0 sc1
v_mfma_f32_16x16x32_bf16 v[84:87], a[32:35], v[128:131], 0
Rb 16,1,46080
Cv 72,72,73
v_mfma_f32_16x16x32_bf16 v[92:95], a[32:35], v[136:139], 0
Cv 73,74,75
Cv 74,76,77
v_accvgpr_read_b32 v116, a136
Mp 92,36,152
Rb 20,1,47104
Cv 75,78,79
Mp 84,36,144
s_add_u32 s29, s73, 0x4c00
buffer_store_dwordx4 v[72:75], v1, s[20:23], s29 offen sc0 sc1
Mp 84,40,160
Rb 24,1,49152
v_accvgpr_read_b32 v24, a137
Mp 92,40,168
Cv 116,116,24
v_accvgpr_read_b32 v117, a138
Mp 92,44,184
Rb 28,1,50176
s_waitcnt lgkmcnt(8)
Mp 84,44,176
v_accvgpr_read_b32 v25, a139
Cv 117,117,25
Mp 84,48,192
Rb 32,1,51200
v_accvgpr_read_b32 v118, a140
Mp 92,48,200
v_accvgpr_read_b32 v26, a141
Cv 118,118,26
Mp 92,52,216
Rb 36,1,52224
v_accvgpr_read_b32 v119, a142
Mp 84,52,208
v_accvgpr_read_b32 v27, a143
Cv 119,119,27
Mp 84,56,224
Rb 40,1,53248
v_accvgpr_read_b32 v120, a144
Mp 92,56,232
s_add_u32 s29, s73, 0x2400
buffer_store_dwordx4 v[116:119], v1, s[20:23], s29 offen sc0 sc1
Mp 92,60,248
Rb 44,1,54272
s_waitcnt lgkmcnt(8)
Mp 84,60,240
v_accvgpr_read_b32 v24, a145
Cv 120,120,24
v_mfma_f32_16x16x32_bf16 v[96:99], a[0:3], v[128:131], 0
Rb 48,1,55296
v_accvgpr_read_b32 v121, a146
)" R"(
v_mfma_f32_16x16x32_bf16 v[104:107], a[0:3], v[136:139], 0
v_accvgpr_read_b32 v25, a147
Cv 121,121,25
v_accvgpr_read_b32 v122, a148
Mp 104,4,152
Rb 52,1,57344
v_accvgpr_read_b32 v26, a149
Mp 96,4,144
Cv 122,122,26
v_accvgpr_read_b32 v123, a150
Mp 96,8,160
Rb 56,1,58368
Cv 80,80,81
Mp 104,8,168
Cv 81,82,83
Cv 82,84,85
Mp 104,12,184
Rb 60,1,59392
s_waitcnt lgkmcnt(8)
Mp 96,12,176
Cv 83,86,87
v_accvgpr_read_b32 v27, a151
Mp 96,16,192
Rb 0,1,60416
Cv 123,123,27
Mp 104,16,200
s_add_u32 s29, s73, 0x5000
buffer_store_dwordx4 v[80:83], v1, s[20:23], s29 offen sc0 sc1
Mp 104,20,216
Rb 4,1,61440
v_accvgpr_read_b32 v124, a152
Mp 96,20,208
s_add_u32 s29, s73, 0x2800
buffer_store_dwordx4 v[120:123], v1, s[20:23], s29 offen sc0 sc1
Mp 96,24,224
Rb 8,1,62464
v_accvgpr_read_b32 v24, a153
Mp 104,24,232
Cv 124,124,24
v_accvgpr_read_b32 v125, a154
Mp 104,28,248
Rb 12,1,63488
s_waitcnt lgkmcnt(8)
Mp 96,28,240
v_accvgpr_read_b32 v25, a155
Cv 125,125,25
v_mfma_f32_16x16x32_bf16 v[100:103], a[32:35], v[128:131], 0
Rb 16,10,0
Cv 88,88,89
v_mfma_f32_16x16x32_bf16 v[108:111], a[32:35], v[136:139], 0
Cv 89,90,91
Cv 90,92,93
v_accvgpr_read_b32 v126, a156
Mp 108,36,152
Rb 20,10,1024
Cv 91,94,95
Mp 100,36,144
s_add_u32 s29, s73, 0x5400
buffer_store_dwordx4 v[88:91], v1, s[20:23], s29 offen sc0 sc1
Mp 100,40,160
Rb 24,10,2048
v_accvgpr_read_b32 v26, a157
Mp 108,40,168
Cv 126,126,26
)" R"(
v_accvgpr_read_b32 v127, a158
Mp 108,44,184
Rb 28,10,3072
s_waitcnt lgkmcnt(8)
Mp 100,44,176
v_accvgpr_read_b32 v27, a159
Cv 127,127,27
Mp 100,48,192
Rb 32,10,4096
v_accvgpr_read_b32 v112, a160
Mp 108,48,200
s_add_u32 s29, s73, 0x2c00
buffer_store_dwordx4 v[124:127], v1, s[20:23], s29 offen sc0 sc1
Mp 108,52,216
Rb 36,10,5120
v_accvgpr_read_b32 v24, a161
Mp 100,52,208
Cv 112,112,24
v_accvgpr_read_b32 v113, a162
Mp 100,56,224
Rb 40,10,6144
v_accvgpr_read_b32 v25, a163
Mp 108,56,232
Cv 113,113,25
v_accvgpr_read_b32 v114, a164
Mp 108,60,248
Rb 44,10,8192
s_waitcnt lgkmcnt(8)
Mp 100,60,240
v_accvgpr_read_b32 v26, a165
Cv 114,114,26
v_mfma_f32_16x16x32_bf16 v[132:135], a[0:3], v[128:131], 0
Rb 48,10,9216
v_accvgpr_read_b32 v115, a166
v_mfma_f32_16x16x32_bf16 v[148:151], a[0:3], v[136:139], 0
v_accvgpr_read_b32 v27, a167
Cv 115,115,27
v_accvgpr_read_b32 v116, a168
Mp 148,4,152
Rb 52,10,10240
v_accvgpr_read_b32 v24, a169
Mp 132,4,144
s_add_u32 s29, s73, 0x3000
buffer_store_dwordx4 v[112:115], v1, s[20:23], s29 offen sc0 sc1
Mp 132,8,160
Rb 56,10,11264
Cv 96,96,97
Mp 148,8,168
Cv 97,98,99
Cv 98,100,101
Mp 148,12,184
Rb 60,10,12288
s_waitcnt lgkmcnt(8)
Mp 132,12,176
Cv 99,102,103
Cv 116,116,24
Mp 132,16,192
Rb 0,10,13312
v_accvgpr_read_b32 v117, a170
Mp 148,16,200
s_add_u32 s29, s73, 0x5800
buffer_store_dwordx4 v[96:99], v1, s[20:23], s29 offen sc0 sc1
Mp 148,20,216
Rb 4,10,14336
)" R"(
v_accvgpr_read_b32 v25, a171
Mp 132,20,208
Cv 117,117,25
v_accvgpr_read_b32 v118, a172
Mp 132,24,224
Rb 8,10,16384
v_accvgpr_read_b32 v26, a173
Mp 148,24,232
Cv 118,118,26
v_accvgpr_read_b32 v119, a174
Mp 148,28,248
Rb 12,10,17408
s_waitcnt lgkmcnt(8)
Mp 132,28,240
v_accvgpr_read_b32 v27, a175
Cv 119,119,27
v_mfma_f32_16x16x32_bf16 v[140:143], a[32:35], v[128:131], 0
Rb 16,10,18432
Cv 104,104,105
v_mfma_f32_16x16x32_bf16 v[156:159], a[32:35], v[136:139], 0
Cv 105,106,107
Cv 106,108,109
Mp 156,36,152
Rb 20,10,19456
Cv 107,110,111
Mp 140,36,144
s_add_u32 s29, s73, 0x5c00
buffer_store_dwordx4 v[104:107], v1, s[20:23], s29 offen sc0 sc1
Mp 140,40,160
Rb 24,10,20480
Mp 156,40,168
s_add_u32 s29, s73, 0x3400
buffer_store_dwordx4 v[116:119], v1, s[20:23], s29 offen sc0 sc1
Mp 156,44,184
Rb 28,10,21504
s_waitcnt lgkmcnt(8)
Mp 140,44,176
Mp 140,48,192
Rb 32,10,22528
Mp 156,48,200
Mp 156,52,216
Rb 36,10,24576
Mp 140,52,208
Mp 140,56,224
Rb 40,10,25600
Mp 156,56,232
Mp 156,60,248
Rb 44,10,26624
s_waitcnt lgkmcnt(8)
Mp 140,60,240
v_mfma_f32_16x16x32_bf16 v[164:167], a[0:3], v[128:131], 0
Rb 48,10,27648
v_mfma_f32_16x16x32_bf16 v[180:183], a[0:3], v[136:139], 0
Mp 180,4,152
Rb 52,10,28672
Mp 164,4,144
Mp 164,8,160
Rb 56,10,29696
Cv 132,132,133
Mp 180,8,168
Cv 133,134,135
Cv 134,140,141
Mp 180,12,184
Rb 60,10,30720
)" R"(
s_waitcnt lgkmcnt(8)
Mp 164,12,176
Cv 135,142,143
Mp 164,16,192
s_add_u32 s29, s73, 0x6000
buffer_store_dwordx4 v[132:135], v1, s[20:23], s29 offen sc0 sc1
Mp 180,16,200
Mp 180,20,216
Mp 164,20,208
Mp 164,24,224
Mp 180,24,232
Mp 180,28,248
s_waitcnt lgkmcnt(4)
Mp 164,28,240
v_mfma_f32_16x16x32_bf16 v[172:175], a[32:35], v[128:131], 0
Cv 148,148,149
Cv 149,150,151
v_mfma_f32_16x16x32_bf16 v[188:191], a[32:35], v[136:139], 0
Cv 150,156,157
Cv 151,158,159
Mp 188,36,152
s_add_u32 s29, s73, 0x6400
buffer_store_dwordx4 v[148:151], v1, s[20:23], s29 offen sc0 sc1
Mp 172,36,144
Mp 172,40,160
Mp 188,40,168
Mp 188,44,184
s_waitcnt lgkmcnt(0)
Mp 172,44,176
Mp 172,48,192
Mp 188,48,200
Mp 188,52,216
Mp 172,52,208
Mp 172,56,224
Mp 188,56,232
Mp 188,60,248
s_waitcnt vmcnt(2)
buffer_store_dword v6, v7, s[20:23], s68 offen offset:0 sc0 sc1
Mp 172,60,240
s_nop 7
Cv 164,164,165
Cv 165,166,167
Cv 166,172,173
Cv 167,174,175
s_add_u32 s29, s73, 0x6800
buffer_store_dwordx4 v[164:167], v1, s[20:23], s29 offen sc0 sc1
Cv 180,180,181
Cv 181,182,183
Cv 182,188,189
Cv 183,190,191
s_add_u32 s29, s73, 0x6c00
buffer_store_dwordx4 v[180:183], v1, s[20:23], s29 offen sc0 sc1
.LzEND:
v_lshl_or_b32 v0, s33, 6, v0
s_add_u32 s4, s68, 4
s_mov_b32 s5, s84
s_mov_b32 s8, s74
s_mov_b32 s9, s75
s_mov_b32 s10, 0x1c0000
s_mov_b32 s11, 0x20000
)" : "+{s[0:1]}"(ka), "+{s2}"(bid), "+{v0}"(tid), "={s3}"(tag), "={s4}"(sfw), "={s5}"(idx), "={s[12:15]}"(rX), "={s[20:23]}"(rO), "={s[8:11]}"(rOut) :
   : "memory", "scc", "vcc", "v1", "v2", "v3", "v4", "v5", "v6", "v7", "v8", "v9", R256("v"), ACL,
     "s6", "s7", "s16", "s17", "s18", "s19", "s24", "s25", "s26", "s27", "s28", "s29", "s30", "s31", "s33", "s34", "s35", "s36", "s37", "s38", "s39",
     V10("s4"), V10("s5"), V10("s6"), V10("s7"), V10("s8"), V10("s9"));
  bid = rfl(bid); tag = rfl(tag); sfw = rfl(sfw); rX = rfl4(rX); rO = rfl4(rO); rOut = rfl4(rOut);
  const u32 lane = tid & 63, wid = rfl(tid >> 6), r = lane & 15, g = lane >> 4, w = (bid & 127u) + 128u * wid;
  const bool B = bid >= 128u;
  idx = rfl(idx);
  Task k[3]; u32 vf = OOB, rt = 0u, pend = 0u;
#pragma unroll
  for (int s = 0; s < 3; ++s) {
    const u32 i = B ? (s ? 1792u : 1344u + w) : w + 512u * s, cls = i / 448u, e = i - cls * 448u;
    const bool on = i < (B ? 1792u : 1344u);
    const u32 blk = (cls >> 1) * 32u + e / 14u, G = e % 14u + (cls & 1u) * 14u, r0 = blk * 32u + (G & 1u) * 16u;
    k[s].sy = rfl((u32)WS_Y + (blk * 28u + G) * 1024u);
    k[s].vx = on ? (r0 + r) * (M_ * 2) + (G >> 1) * 64u + g * 8u : OOB;
    if (g == s) vf = (blk * 16u + r) * 128u + (cls & 1u) * 4u;
    if (on) pend |= 1u << s;
    if (on) rt |= (((idx & 0xffffu) >> 4 == r0 >> 4 ? 1u : 0u) | (idx >> 20 == r0 >> 4 ? 2u : 0u)) << (2 * s);
    asm volatile("buffer_load_dwordx2 %0, %3, %4, 0 offen\n\tbuffer_load_dwordx2 %1, %3, %4, 0 offen offset:32\n\tbuffer_load_dword %2, %5, %6, %7 offen sc0 sc1"
                 : "=&v"(k[s].x[0]), "=&v"(k[s].x[1]), "=&v"(k[s].g) : "v"(k[s].vx), "s"(rX), "v"((r0 + r) * 16u), "s"(rO), "s"((u32)WS_KEY + 8u) : "memory");
  }
  u32x4 sel[2];
#pragma unroll
  for (int b = 0; b < 2; ++b)
#pragma unroll
    for (int q = 0; q < 4; ++q) sel[b][q] = (r >> 1) == 4u * b + q ? ((r & 1u) ? 0x3f800000u : 0x3f80u) : 0u;
  asm volatile("s_waitcnt vmcnt(9)" : "+v"(sel[0]), "+v"(sel[1]) :: "memory");
  asm volatile("buffer_store_dword %0, %1, %2, %3 offen sc0 sc1" :: "v"(tag), "v"(lane ? OOB : 0u), "s"(rO), "s"(sfw) : "memory");
  asm volatile("s_waitcnt vmcnt(1)" : "+v"(k[0].x[0]), "+v"(k[0].x[1]), "+v"(k[0].g), "+v"(k[1].x[0]), "+v"(k[1].x[1]), "+v"(k[1].g), "+v"(k[2].x[0]), "+v"(k[2].x[1]), "+v"(k[2].g) :: "memory");
  const u32 vy = g * 1835008u + r * 16u;
  pend = rfl(pend); rt = rfl(rt);
  if (bid == 127u && wid == 3u) {
    for (u32 q = 0; q < 4u; ++q) await(lane * 128u, rO, (u32)WS_KVF + q * 8192u, tag, ~0ull);
    asm volatile("buffer_store_dword %0, %1, %2, %3 offen sc0 sc1" :: "v"(tag), "v"(lane ? OOB : 0u), "s"(rO), "s"((u32)WS_EP) : "memory");
  }
  if (rt) {
    await(lane * 128u, rO, (u32)WS_FW, tag, ~0ull); await(lane * 128u + 8192u, rO, (u32)WS_FW, tag, 0xffffffffffffull);
    for (u32 q = 0; q < 6u; ++q) {
      if (!((rt >> q) & 1u)) continue;
      const u32 s = q >> 1, c = q & 1u, ir = (c ? idx >> 16 : idx) & 2047u, vx = s == 0u ? k[0].vx : s == 1u ? k[1].vx : k[2].vx;
      const u32 sf = (u32)WS_YF + (c * 28u + (rfl(vx) % (M_ * 2)) / 32u) * 7168u;
      f32x4 v[14]; float rv;
#pragma unroll
      for (int e = 0; e < 14; ++e) asm volatile("buffer_load_dwordx4 %0, %1, %2, %3 offen offset:%4 sc0 sc1" : "=v"(v[e]) : "v"(lane * 16u), "s"(rO), "s"(sf + (e >> 2) * 4096), "i"((e & 3) * 1024) : "memory");
      asm volatile("buffer_load_dword %0, %15, %16, %17 offen sc0 sc1\n\ts_waitcnt vmcnt(0)"
                   : "=&v"(rv), "+v"(v[0]), "+v"(v[1]), "+v"(v[2]), "+v"(v[3]), "+v"(v[4]), "+v"(v[5]), "+v"(v[6]), "+v"(v[7]), "+v"(v[8]), "+v"(v[9]), "+v"(v[10]), "+v"(v[11]), "+v"(v[12]), "+v"(v[13])
                   : "v"(0u), "s"(rO), "s"((u32)WS_KEY + 12u + ir * 16u) : "memory");
      const float sg = rbf(1.f / (1.f + __expf(-rv)));
#pragma unroll
      for (int t = 0; t < 2; ++t) {
        const f32x4 fs = ((v[7 * t] + v[7 * t + 1]) + (v[7 * t + 2] + v[7 * t + 3])) + ((v[7 * t + 4] + v[7 * t + 5]) + v[7 * t + 6]);
        const u32x2 xv = s == 0u ? k[0].x[t] : s == 1u ? k[1].x[t] : k[2].x[t];
        float o[4];
#pragma unroll
        for (int e = 0; e < 4; ++e) {
          unsigned u = __float_as_uint(fs[e]);
          const auto s32 = __builtin_amdgcn_permlane32_swap(u, u, false, false);
          u = __float_as_uint(__uint_as_float(s32[0]) + __uint_as_float(s32[1]));
          const auto s16 = __builtin_amdgcn_permlane16_swap(u, u, false, false);
          const float y = __shfl(dpp_add<0x124>(dpp_add<0x128>(__uint_as_float(s16[0]) + __uint_as_float(s16[1]))), g);
          o[e] = ((e & 1) ? BF_HI(xv[e >> 1]) : BF_LO(xv[e >> 1])) + rbf(rbf(y) * sg);
        }
        const u32x2 ov = {cvtpk(o[0], o[1]), cvtpk(o[2], o[3])};
        if (r == (ir & 15u)) { if (s == 0u) k[0].x[t] = ov; else if (s == 1u) k[1].x[t] = ov; else k[2].x[t] = ov; }
      }
    }
  }
#pragma unroll
  for (int s = 0; s < 3; ++s) {
    const float omg = rbf(1.f - k[s].g);
#pragma unroll
    for (int e = 0; e < 4; ++e) { k[s].b[2 * e] = rbf(omg * BF_LO(k[s].x[e >> 1][e & 1])); k[s].b[2 * e + 1] = rbf(omg * BF_HI(k[s].x[e >> 1][e & 1])); }
  }
  for (u32 fly = 0u, n = 1u << 17; pend | fly; --n) {
    u32 f; unsigned long long m;
    asm volatile("buffer_load_dword %0, %2, %3, %4 offen sc0 sc1\n\ts_waitcnt vmcnt(0)\n\tv_cmp_ne_u32_e64 %1, %5, %0" : "=&v"(f), "=s"(m) : "v"((pend >> g) & 1u ? vf : OOB), "s"(rO), "s"((u32)WS_YW), "s"(tag) : "memory");
    const u32 lo = rfl((u32)m), hi = rfl((u32)(m >> 32));
    const u32 rdy = pend & (n ? ((lo & 0xffffu) ? 0u : 1u) | ((lo >> 16) ? 0u : 2u) | ((hi & 0xffffu) ? 0u : 4u) : 7u);
    if (rdy & 1u) y_load<0>(vy, rO, k[0].sy);
    if (rdy & 2u) y_load<1>(vy, rO, k[1].sy);
    if (rdy & 4u) y_load<2>(vy, rO, k[2].sy);
    if (fly & 1u) y_done<0>(k[0], sel, rOut);
    if (fly & 2u) y_done<1>(k[1], sel, rOut);
    if (fly & 4u) y_done<2>(k[2], sel, rOut);
    fly = rdy; pend &= ~rdy;
  }
  asm volatile(".LzCEND:");
}

extern "C" void kernel_launch(void* const* d_in, const int* in_sizes, int n_in, void* d_out, int out_size, void* d_ws, size_t ws_size, hipStream_t stream) {
  (void)hipFuncSetAttribute((const void*)k_main, hipFuncAttributeMaxDynamicSharedMemorySize, 163840);
  k_main<<<256, 256, 163840, stream>>>((const u16*)d_in[0], (const u16*)d_in[1], (const u16*)d_in[2], (const u16*)d_in[3], (const u16*)d_in[4],
      (char*)d_ws, (const u16*)d_in[11], (const u16*)d_in[7], (const u16*)d_in[5], (const u16*)d_in[8], (const u16*)d_in[9], (const u16*)d_in[10], (u16*)d_out);
}
```

```cpp
#include <hip/hip_runtime.h>
#include <stdint.h>

typedef __attribute__((ext_vector_type(4))) float f32x4;
typedef __attribute__((ext_vector_type(2))) float f32x2;
typedef __attribute__((ext_vector_type(4))) unsigned u32x4;
typedef __attribute__((ext_vector_type(2))) unsigned u32x2;
typedef __attribute__((ext_vector_type(2))) __bf16 bfx2;
typedef unsigned short u16;
typedef unsigned u32;
#define DI __device__ __forceinline__
constexpr int M_ = 448, F_ = 1792, FFN_N = 112;
constexpr size_t WS_K = 0u;
constexpr size_t WS_V = 33554432u;
constexpr size_t WS_Q = 67108864u;
constexpr size_t WS_PB = 68157440u;
constexpr size_t WS_WO = 100663296u;
constexpr size_t WS_Y = 134217728u;
constexpr size_t WS_ML = 201326592u;
constexpr size_t WS_KEY = 202375168u;
constexpr size_t WS_IDX = 202407936u;
constexpr size_t WS_EP = 202408960u;
constexpr size_t WS_PAR = 202409984u;
constexpr size_t WS_FW = 202424320u;
constexpr size_t WS_KVF = 202440704u;
constexpr size_t WS_QF = 202473472u;
constexpr size_t WS_PF = 202506240u;
constexpr size_t WS_YW = 202571776u;
constexpr size_t WS_YF = 202702848u;
constexpr size_t WS_END = 203104256u;
DI unsigned cvtpk(float lo, float hi) { return __builtin_bit_cast(unsigned, __builtin_convertvector((f32x2){lo, hi}, bfx2)); }
DI float rbf(float f) { return __uint_as_float(cvtpk(f, f) << 16); }
#define BF_LO(u) __uint_as_float((u) << 16)
#define BF_HI(u) __uint_as_float((u) & 0xffff0000u)
typedef u32x4 srd_t;
DI u32 rfl(u32 x) { return __builtin_amdgcn_readfirstlane(x); }
DI srd_t rfl4(srd_t r) { return (srd_t){rfl(r[0]), rfl(r[1]), rfl(r[2]), rfl(r[3])}; }
constexpr u32 OOB = 0x40000000u;
template <int CTRL> DI float dpp_add(float v) { return v + __uint_as_float(__builtin_amdgcn_update_dpp(0, __float_as_uint(v), CTRL, 0xf, 0xf, false)); }
DI void await(u32 vo, srd_t r, u32 so, u32 tag, unsigned long long need) {
  u32 w, n = 1u << 17; unsigned long long t;
  asm volatile(".Lw%=:\n\tbuffer_load_dword %0, %3, %4, %5 offen sc0 sc1\n\ts_waitcnt vmcnt(0)\n\tv_cmp_eq_u32_e32 vcc, %6, %0\n\ts_andn2_b64 %1, %7, vcc\n\ts_cbranch_scc0 .Lg%=\n\t"
               "s_sub_u32 %2, %2, 1\n\ts_cbranch_scc0 .Lw%=\n.Lg%=:"
               : "=&v"(w), "=&s"(t), "+s"(n) : "v"(vo), "s"(r), "s"(so), "s"(tag), "s"(need) : "memory", "scc", "vcc");
}

#define V10(p) p "0", p "1", p "2", p "3", p "4", p "5", p "6", p "7", p "8", p "9"
#define V100(p) V10(p "0"), V10(p "1"), V10(p "2"), V10(p "3"), V10(p "4"), V10(p "5"), V10(p "6"), V10(p "7"), V10(p "8"), V10(p "9")
#define R256(p) V10(p "1"), V10(p "2"), V10(p "3"), V10(p "4"), V10(p "5"), V10(p "6"), V10(p "7"), V10(p "8"), V10(p "9"), V100(p "1"), \
                V10(p "20"), V10(p "21"), V10(p "22"), V10(p "23"), V10(p "24"), p "250", p "251", p "252", p "253", p "254", p "255"

#define ACL V10("a"), R256("a")
#define YL(k, c) "buffer_load_dwordx4 a[%c4+" #k "*16+" #c "*4:%c4+" #k "*16+" #c "*4+3], %1, %2, %0 offen offset:" #c "*256 sc0 sc1\n\t"
#define YK(k) "s_bitcmp0_b32 %5, " #k "*4\n\ts_cbranch_scc1 .Ly" #k "_%=\n\ts_add_u32 %0, %3, " #k "*16777216\n\t" YL(k, 0) YL(k, 1) YL(k, 2) YL(k, 3) ".Ly" #k "_%=:\n\t"
template <int S> DI void y_load(u32 vy, srd_t r, u32 so, u32 rdy) {
  u32 t;
  asm volatile(YK(0) YK(1) YK(2) YK(3) : "=&s"(t) : "v"(vy), "s"(r), "s"(so), "i"(64 * S), "s"(rdy) : "memory", "scc", ACL);
}
#define YM(k, c, d, s, z) "v_mfma_f32_16x16x32_bf16 %" #d ", %" #s ", a[%c4+" #k "*16+" #c "*4:%c4+" #k "*16+" #c "*4+3], " z "\n\t"
#define YS(k) YM(k, 0, 0, 2, "%0") YM(k, 2, 1, 2, "%1") YM(k, 1, 0, 3, "%0") YM(k, 3, 1, 3, "%1")
constexpr int NS = 2, SLP = 8;
struct Task { u32x2 x[2]; float g, b[8]; u32 vx, sy; };
template <int S> DI void y_done(Task& k, const u32x4 (&sel)[2], srd_t rOut) {
  f32x4 c0, c1;
  asm volatile(YM(0, 0, 0, 2, "0") YM(0, 2, 1, 2, "0") YM(0, 1, 0, 3, "%0") YM(0, 3, 1, 3, "%1") YS(1) YS(2) YS(3) "s_nop 7"
               : "=&v"(c0), "=&v"(c1) : "v"(sel[0]), "v"(sel[1]), "i"(64 * S) : "memory");
  float r[8];
#pragma unroll
  for (int e = 0; e < 8; ++e) r[e] = rbf(k.g * rbf(e < 4 ? c0[e & 3] : c1[e & 3])) + k.b[e];
  const u32x2 o0 = {cvtpk(r[0], r[1]), cvtpk(r[2], r[3])}, o1 = {cvtpk(r[4], r[5]), cvtpk(r[6], r[7])};
  asm volatile("buffer_store_dwordx2 %0, %2, %3, 0 offen\n\tbuffer_store_dwordx2 %1, %2, %3, 0 offen offset:32" :: "v"(o0), "v"(o1), "v"(k.vx), "s"(rOut) : "memory");
}

extern "C" __global__ __launch_bounds__(256, 1)
void k_main(const u16* x_, const u16* ckv, const u16* wq, const u16* wuk, const u16* wuv, char* ws_,
            const u16* gate_w, const u16* w_r, const u16* wo, const u16* wg_, const u16* wu_, const u16* wd_, u16* out_) {
  typedef const __attribute__((address_space(4))) char* karg_t;
  karg_t ka = (karg_t)__builtin_amdgcn_kernarg_segment_ptr();
  u32 bid = blockIdx.x, tid = threadIdx.x, tag, sfw, idx, tk;
  srd_t rX, rO, rOut;
  asm volatile(R"(
.macro Mv d,a,b
v_mfma_f32_16x16x32_bf16 v[\d:\d+3], a[\a:\a+3], a[\b:\b+3], v[\d:\d+3]
.endm
.macro Mp d,a,b
v_mfma_f32_16x16x32_bf16 v[\d:\d+3], a[\a:\a+3], v[\b:\b+3], v[\d:\d+3]
.endm
.macro Ma d,a,b
v_mfma_f32_16x16x32_bf16 a[\d:\d+3], a[\a:\a+3], a[\b:\b+3], a[\d:\d+3]
.endm
.macro Mz d,a,b
v_mfma_f32_16x16x32_bf16 v[\d:\d+3], a[\a:\a+3], a[\b:\b+3], 0
.endm
.macro Rb d,v,o
ds_read_b128 a[\d:\d+3], v\v offset:\o
.endm
.macro Rt d,v,o
ds_read_b64_tr_b16 a[\d:\d+1], v\v offset:\o
.endm
.macro Cv d,a,b
v_cvt_pk_bf16_f32 v\d, v\a, v\b
.endm
.LzSTART:
s_load_dwordx16 s[64:79], s[0:1], 0
s_load_dwordx2 s[80:81], s[0:1], 64
v_lshrrev_b32 v1, 6, v0
v_and_b32 v0, 63, v0
v_readfirstlane_b32 s33, v1
v_lshlrev_b32 v1, 4, v0
s_and_b32 s28, s2, 7
s_bfe_u32 s29, s2, 0x10003
s_lshl1_add_u32 s34, s28, s29
s_bfe_u32 s35, s2, 0x30004
s_bfe_u32 s91, s2, 0x10004
s_bfe_u32 s93, s2, 0x20005
s_lshl_b32 s36, s33, 13
s_mov_b32 s95, 40
s_bitcmp1_b32 s2, 7
s_cbranch_scc1 .LzB0
s_branch .LzA0
.LzPRB:
s_lshl_b32 s24, s34, 21
s_mul_i32 s25, s33, 2304
s_add_u32 s24, s24, s25
s_add_u32 s24, s24, 0x6100000
s_xor_b32 s25, s24, 0x2000
buffer_store_dword v0, v7, s[20:23], s24 offen sc0 sc1
buffer_store_dword v0, v7, s[20:23], s25 offen sc0 sc1
s_mov_b32 s26, -1
s_mov_b32 s27, -1
s_waitcnt vmcnt(0)
s_memtime s[28:29]
s_waitcnt lgkmcnt(0)
buffer_store_dword v0, v7, s[20:23], s24 offen sc0 sc1
s_waitcnt vmcnt(0)
s_memtime s[30:31]
s_waitcnt lgkmcnt(0)
s_sub_u32 s30, s30, s28
s_min_u32 s26, s26, s30
s_memtime s[28:29]
s_waitcnt lgkmcnt(0)
buffer_store_dword v0, v7, s[20:23], s25 offen sc0 sc1
s_waitcnt vmcnt(0)
s_memtime s[30:31]
s_waitcnt lgkmcnt(0)
s_sub_u32 s30, s30, s28
s_min_u32 s27, s27, s30
s_memtime s[28:29]
s_waitcnt lgkmcnt(0)
buffer_store_dword v0, v7, s[20:23], s24 offen sc0 sc1
s_waitcnt vmcnt(0)
s_memtime s[30:31]
s_waitcnt lgkmcnt(0)
s_sub_u32 s30, s30, s28
s_min_u32 s26, s26, s30
s_memtime s[28:29]
s_waitcnt lgkmcnt(0)
buffer_store_dword v0, v7, s[20:23], s25 offen sc0 sc1
s_waitcnt vmcnt(0)
s_memtime s[30:31]
s_waitcnt lgkmcnt(0)
s_sub_u32 s30, s30, s28
s_min_u32 s27, s27, s30
v_mov_b32 v32, s26
v_mov_b32 v33, s27
)" R"(
s_lshl_b32 s24, s33, 3
v_mov_b32 v34, s24
v_mov_b32 v35, 0
ds_write_b64 v34, v[32:33]
s_waitcnt lgkmcnt(0)
s_barrier
ds_read_b128 v[36:39], v35
ds_read_b128 v[40:43], v35 offset:16
s_mul_i32 s24, s3, 1664525
s_add_u32 s24, s24, 1013904223
s_and_b32 s24, s24, -2
s_lshl_b32 s25, s34, 6
s_add_u32 s25, s25, 0xc108800
s_cmp_lg_u32 s33, 0
s_cmov_b32 s25, 0x40000000
s_waitcnt lgkmcnt(0)
v_min3_u32 v32, v36, v38, v40
v_min3_u32 v33, v37, v39, v41
v_min_u32 v32, v32, v42
v_min_u32 v33, v33, v43
v_cmp_gt_u32_e32 vcc, v32, v33
v_mov_b32 v34, s24
s_nop 0
v_addc_co_u32_e32 v34, vcc, 0, v34, vcc
buffer_store_dword v34, v7, s[20:23], s25 offen sc0 sc1
s_barrier
s_branch .LzPRR
.LzA0:
s_mov_b32 s63, 0x6040200
s_mov_b32 s28, 0x7050312
s_bitcmp1_b32 s2, 5
s_cmov_b32 s63, 0x18161401
s_cmov_b32 s28, 0x19171513
s_bitcmp1_b32 s2, 6
s_cmov_b32 s63, s28
s_lshl_b32 s28, s33, 3
s_lshr_b32 s28, s63, s28
s_and_b32 s28, s28, 255
s_lshl_b32 s38, s28, 17
s_add_u32 s38, s38, 0xff80
s_lshl_b32 s39, s33, 16
s_lshl_b32 s28, s34, 9
s_add_u32 s39, s39, s28
s_sub_u32 s39, s39, 0x40000
s_lshl_b32 s37, s33, 12
s_add_u32 s37, s37, 0x10000
v_lshrrev_b32 v28, 4, v0
v_and_b32 v29, 7, v0
v_xor_b32 v28, v28, v29
v_lshlrev_b32 v28, 4, v28
v_lshrrev_b32 v29, 3, v0
v_lshl_or_b32 v8, v29, 11, v28
v_xor_b32 v9, 64, v8
v_add_u32 v9, 15360, v9
v_add_u32 v10, 30720, v8
v_add_u32 v11, 30720, v9
s_and_b32 s28, s33, 1
s_lshl_b32 s28, s28, 7
v_lshrrev_b32 v29, 5, v0
v_mul_u32_u24 v29, 0x2220, v29
v_xor_b32 v29, v29, v1
v_xor_b32 v12, s28, v29
v_xor_b32 v13, 64, v12
v_add_u32 v13, 15360, v13
)" R"(
v_add_u32 v14, 30720, v12
v_add_u32 v15, 30720, v13
v_min_u32 v7, 1, v0
v_lshlrev_b32 v7, 30, v7
s_waitcnt lgkmcnt(0)
s_mov_b32 s28, 0xc108400
s_load_dword s3, s[74:75], s28
s_mov_b32 s20, s74
s_mov_b32 s21, s75
s_mov_b32 s22, 0xc1b2000
s_mov_b32 s23, 0x20000
s_mov_b32 s4, s66
s_mov_b32 s5, s67
s_mov_b32 s6, 0x400000
s_mov_b32 s7, 0x20000
s_cmp_eq_u32 s91, 1
s_cselect_b32 s8, s72, s70
s_cselect_b32 s9, s73, s71
s_mov_b32 s10, 0x800000
s_mov_b32 s11, 0x20000
s_lshr_b32 s28, s2, 3
s_cmp_eq_u32 s28, 14
s_cbranch_scc1 .LzPRB
.LzPRR:
s_add_u32 m0, s37, 0
s_add_u32 s39, s39, 0x40000
buffer_load_dwordx4 v12, s[8:11], s39 offen offset:0 lds
buffer_load_dwordx4 v13, s[8:11], s39 offen offset:1024 lds
buffer_load_dwordx4 v14, s[8:11], s39 offen offset:2048 lds
buffer_load_dwordx4 v15, s[8:11], s39 offen offset:3072 lds
s_add_u32 m0, s36, 0
s_add_u32 s38, s38, 0xffff0080
buffer_load_dwordx4 v8, s[4:7], s38 offen offset:0 lds
buffer_load_dwordx4 v9, s[4:7], s38 offen offset:1024 lds
buffer_load_dwordx4 v10, s[4:7], s38 offen offset:2048 lds
buffer_load_dwordx4 v11, s[4:7], s38 offen offset:3072 lds
s_add_u32 m0, s36, 4096
s_add_u32 s38, s38, 0x10000
buffer_load_dwordx4 v8, s[4:7], s38 offen offset:0 lds
buffer_load_dwordx4 v9, s[4:7], s38 offen offset:1024 lds
buffer_load_dwordx4 v10, s[4:7], s38 offen offset:2048 lds
buffer_load_dwordx4 v11, s[4:7], s38 offen offset:3072 lds
s_add_u32 m0, s37, 16384
s_add_u32 s39, s39, 0x40000
buffer_load_dwordx4 v12, s[8:11], s39 offen offset:0 lds
buffer_load_dwordx4 v13, s[8:11], s39 offen offset:1024 lds
buffer_load_dwordx4 v14, s[8:11], s39 offen offset:2048 lds
buffer_load_dwordx4 v15, s[8:11], s39 offen offset:3072 lds
s_add_u32 m0, s36, 32768
s_add_u32 s38, s38, 0xffff0080
buffer_load_dwordx4 v8, s[4:7], s38 offen offset:0 lds
buffer_load_dwordx4 v9, s[4:7], s38 offen offset:1024 lds
buffer_load_dwordx4 v10, s[4:7], s38 offen offset:2048 lds
buffer_load_dwordx4 v11, s[4:7], s38 offen offset:3072 lds
s_add_u32 m0, s36, 36864
s_add_u32 s38, s38, 0x10000
buffer_load_dwordx4 v8, s[4:7], s38 offen offset:0 lds
buffer_load_dwordx4 v9, s[4:7], s38 offen offset:1024 lds
buffer_load_dwordx4 v10, s[4:7], s38 offen offset:2048 lds
buffer_load_dwordx4 v11, s[4:7], s38 offen offset:3072 lds
s_add_u32 m0, s37, 32768
s_add_u32 s39, s39, 0x40000
buffer_load_dwordx4 v12, s[8:11], s39 offen offset:0 lds
buffer_load_dwordx4 v13, s[8:11], s39 offen offset:1024 lds
)" R"(
buffer_load_dwordx4 v14, s[8:11], s39 offen offset:2048 lds
buffer_load_dwordx4 v15, s[8:11], s39 offen offset:3072 lds
s_getpc_b64 s[24:25]
.Lzpc0:
s_sub_u32 s24, s24, .Lzpc0-.LzSTART
s_subb_u32 s25, s25, 0
s_mov_b32 s26, .LzEND-.LzSTART+8192
s_mov_b32 s27, 0x20000
s_lshr_b32 s28, s2, 3
s_lshl2_add_u32 s28, s28, s33
s_lshl_b32 s28, s28, 11
v_lshrrev_b32 v6, 4, v0
v_min_u32 v6, 1, v6
v_lshlrev_b32 v6, 27, v6
v_add_lshl_u32 v6, v6, v1, 3
buffer_load_dword v6, v6, s[24:27], s28 offen
s_mov_b32 s12, s64
s_mov_b32 s13, s65
s_mov_b32 s14, 0x1c0000
s_mov_b32 s15, 0x20000
s_mov_b32 s16, s68
s_mov_b32 s17, s69
s_mov_b32 s18, 0x380000
s_mov_b32 s19, 0x20000
s_load_dwordx4 s[76:79], s[0:1], 72
s_load_dwordx2 s[70:71], s[0:1], 88
s_load_dwordx2 s[74:75], s[0:1], 96
v_bfe_u32 v28, v0, 1, 3
v_lshrrev_b32 v29, 4, v0
v_xor_b32 v28, v28, v29
v_lshlrev_b32 v28, 4, v28
v_and_b32 v29, 15, v0
v_lshl_or_b32 v28, v29, 7, v28
s_lshr_b32 s28, s33, 1
s_lshl_b32 s28, s28, 12
v_or_b32 v16, s28, v28
v_xor_b32 v17, 64, v16
v_bfe_u32 v28, v0, 2, 3
v_lshlrev_b32 v28, 5, v28
v_and_b32 v29, 3, v0
v_lshl_or_b32 v28, v29, 3, v28
v_bfe_u32 v29, v0, 2, 2
v_lshl_or_b32 v28, v29, 9, v28
v_lshrrev_b32 v29, 4, v0
v_lshl_or_b32 v28, v29, 12, v28
s_and_b32 s28, s33, 1
s_lshl_b32 s28, s28, 8
s_bitset1_b32 s28, 16
v_or_b32 v18, s28, v28
v_xor_b32 v19, 32, v18
v_xor_b32 v20, 64, v18
v_xor_b32 v21, 96, v18
v_xor_b32 v22, 128, v18
v_xor_b32 v23, 160, v18
v_xor_b32 v24, 192, v18
v_xor_b32 v25, 224, v18
s_mov_b32 s24, s80
s_mov_b32 s25, s81
s_mov_b32 s26, 0x380000
s_mov_b32 s27, 0x20000
s_bfe_u32 s28, s2, 0x30005
s_lshl3_add_u32 s29, s34, s28
s_lshl2_add_u32 s29, s29, s33
s_mul_i32 s83, s29, 7168
)" R"(
s_mul_i32 s29, s91, 448
s_add_u32 s83, s83, s29
s_lshl3_add_u32 s29, s34, s91
s_lshl_b32 s82, s29, 18
s_add_u32 s82, s82, 0x6000000
s_mul_i32 s29, s28, 14
s_add_u32 s29, s29, s33
s_lshl3_add_u32 s28, s33, s28
s_cmp_eq_u32 s91, 0
s_cselect_b32 s94, s29, s28
s_cmp_eq_u32 s91, 0
s_cselect_b32 s92, 4, 32
v_lshrrev_b32 v4, 5, v0
v_mul_u32_u24 v4, 384, v4
v_add_u32 v28, v4, v1
v_and_b32 v4, 31, v0
v_cmp_lt_u32_e32 vcc, 27, v4
v_mov_b32 v4, 0x40000000
v_lshrrev_b32 v29, 2, v0
v_cndmask_b32_e32 v28, v28, v4, vcc
v_and_b32 v4, 48, v1
v_lshl_or_b32 v29, v29, 9, v4
s_lshr_b32 s28, s33, 1
s_lshl_b32 s28, s28, 6
s_and_b32 s29, s33, 1
s_lshl3_add_u32 s28, s29, s28
s_add_u32 s28, s28, 0x20000
v_add_u32 v29, s28, v29
s_add_u32 m0, s37, 0x10000
s_add_u32 s29, s83, 0
buffer_load_dwordx4 v28, s[24:27], s29 offen offset:0 lds
s_add_u32 s29, s83, 768
buffer_load_dwordx4 v28, s[24:27], s29 offen offset:1024 lds
s_add_u32 s29, s83, 1536
buffer_load_dwordx4 v28, s[24:27], s29 offen offset:2048 lds
s_add_u32 s29, s83, 2304
buffer_load_dwordx4 v28, s[24:27], s29 offen offset:3072 lds
s_lshl_b32 s28, s34, 21
s_lshl_b32 s29, s91, 25
s_add_u32 s28, s28, s29
s_bfe_u32 s29, s63, 0x80000
s_lshl_b32 s29, s29, 16
s_add_u32 s45, s28, s29
s_bfe_u32 s29, s63, 0x80008
s_lshl_b32 s29, s29, 16
s_add_u32 s46, s28, s29
s_bfe_u32 s29, s63, 0x80010
s_lshl_b32 s29, s29, 16
s_add_u32 s47, s28, s29
s_bfe_u32 s29, s63, 0x80018
s_lshl_b32 s29, s29, 16
s_add_u32 s48, s28, s29
s_lshl_b32 s50, s34, 11
s_add_u32 s50, s50, 0xc110000
s_lshl2_add_u32 s28, s91, s93
s_lshl_b32 s28, s28, 7
s_add_u32 s49, s28, s50
s_lshl3_add_u32 s28, s34, s35
s_lshl_b32 s52, s28, 7
s_add_u32 s52, s52, 0xc118000
s_lshl4_add_u32 s28, s34, s35
s_lshl_b32 s51, s28, 17
s_add_u32 s51, s51, 0x4000000
s_cmp_lg_u32 s33, 0
)" R"(
s_cmov_b32 s49, 0x40000000
s_mov_b32 s53, 0x40000
s_mov_b32 s54, 0x3db8aa3b
s_mul_i32 s41, s33, 7168
s_mul_i32 s42, s33, 0x70000
s_lshl_b32 s28, s34, 9
s_add_u32 s42, s42, s28
s_lshl_b32 s89, s34, 21
s_mov_b64 s[60:61], 0xff
s_mov_b32 s62, 0x4e800000
s_add_u32 s67, s89, 0x6000000
s_lshl3_add_u32 s28, s34, s35
s_add_u32 s64, s51, 0x100000
s_lshl2_add_u32 s28, s28, s33
s_lshl_b32 s65, s28, 7
s_add_u32 s65, s65, 0xc120000
s_lshl_b32 s66, s28, 10
s_add_u32 s66, s66, 0xc000000
s_mov_b32 s28, s35
s_lshl2_add_u32 s28, s28, s33
s_lshl_b32 s29, s34, 6
s_add_u32 s29, s29, s28
s_lshl_b32 s73, s29, 16
s_add_u32 s73, s73, 0x8000000
s_lshl4_add_u32 s28, s28, s34
s_lshl_b32 s68, s28, 7
s_add_u32 s68, s68, 0xc130000
s_add_u32 s55, s89, 0x120000
s_lshl_b32 s28, s35, 1
s_sub_u32 s57, 14, s28
s_mov_b32 s59, s89
s_mov_b32 s58, 2
s_cmp_eq_u32 s35, 7
s_cmov_b32 s55, s89
s_cmp_eq_u32 s35, 7
s_cmov_b32 s57, 16
s_cmp_eq_u32 s35, 7
s_cmov_b32 s59, s67
s_add_u32 s56, s55, 0x2000000
s_waitcnt vmcnt(21) lgkmcnt(0)
s_barrier
Rt 0,18,0
Rt 2,18,2048
Rt 4,19,0
Rt 6,19,2048
Rt 8,20,0
Rt 10,20,2048
Rt 12,21,0
Rt 14,21,2048
Rt 16,22,0
Rt 18,22,2048
Rt 20,23,0
Rt 22,23,2048
Rt 24,24,0
Rt 26,24,2048
Rt 28,25,0
Rt 30,25,2048
Rb 32,16,0
Rb 36,16,2048
Rb 40,16,8192
Rb 44,16,10240
Rb 48,16,16384
Rb 52,16,18432
Rb 56,16,24576
Rb 60,16,26624
)" R"(
s_mul_i32 s3, s3, 1664525
s_add_u32 s3, s3, 1013904223
s_cmp_eq_u32 s91, 1
s_waitcnt vmcnt(17) lgkmcnt(0)
s_barrier
s_cbranch_scc1 .LzAV
Mz 32,0,32
Rt 64,18,16384
Mz 64,0,36
Mz 96,0,40
Rt 66,18,18432
Mz 128,0,44
s_add_u32 s39, s39, 0x40000
Mz 160,0,48
Rt 68,19,16384
s_add_u32 m0, s37, 49152
Mz 192,0,52
buffer_load_dwordx4 v12, s[8:11], s39 offen offset:0 lds
Mz 224,0,56
Rt 70,19,18432
v_mfma_f32_16x16x32_bf16 a[224:227], a[0:3], a[60:63], 0
v_mfma_f32_16x16x32_bf16 a[228:231], a[4:7], a[60:63], 0
Rt 72,20,16384
Mz 228,4,56
Mz 196,4,52
Rt 74,20,18432
Mz 164,4,48
Mz 132,4,44
Rt 76,21,16384
Mz 100,4,40
buffer_load_dwordx4 v13, s[8:11], s39 offen offset:1024 lds
Mz 68,4,36
Rt 78,21,18432
Mz 36,4,32
Mz 40,8,32
Rt 80,22,16384
Mz 72,8,36
Mz 104,8,40
Rt 82,22,18432
Mz 136,8,44
Mz 168,8,48
Rt 84,23,16384
Mz 200,8,52
buffer_load_dwordx4 v14, s[8:11], s39 offen offset:2048 lds
Mz 232,8,56
Rt 86,23,18432
v_mfma_f32_16x16x32_bf16 a[232:235], a[8:11], a[60:63], 0
v_mfma_f32_16x16x32_bf16 a[236:239], a[12:15], a[60:63], 0
Rt 88,24,16384
Mz 236,12,56
Mz 204,12,52
Rt 90,24,18432
Mz 172,12,48
Mz 140,12,44
Rt 92,25,16384
Mz 108,12,40
buffer_load_dwordx4 v15, s[8:11], s39 offen offset:3072 lds
Mz 76,12,36
Rt 94,25,18432
Mz 44,12,32
Mz 48,16,32
Rb 96,17,0
Mz 80,16,36
Mz 112,16,40
Rb 100,17,2048
)" R"(
Mz 144,16,44
s_add_u32 s39, s39, 0x40000
Mz 176,16,48
Rb 104,17,8192
s_add_u32 m0, s37, 0
Mz 208,16,52
buffer_load_dwordx4 v12, s[8:11], s39 offen offset:0 lds
Mz 240,16,56
Rb 108,17,10240
v_mfma_f32_16x16x32_bf16 a[240:243], a[16:19], a[60:63], 0
v_mfma_f32_16x16x32_bf16 a[244:247], a[20:23], a[60:63], 0
Rb 112,17,16384
Mz 244,20,56
Mz 212,20,52
Rb 116,17,18432
Mz 180,20,48
Mz 148,20,44
Rb 120,17,24576
Mz 116,20,40
buffer_load_dwordx4 v13, s[8:11], s39 offen offset:1024 lds
Mz 84,20,36
Rb 124,17,26624
Mz 52,20,32
Mz 56,24,32
Mz 88,24,36
Mz 120,24,40
s_lshl2_add_u32 s90, s2, s33
s_lshl_b32 s90, s90, 13
v_lshlrev_b32 v5, 7, v0
Mz 152,24,44
Mz 184,24,48
Mz 216,24,52
buffer_load_dwordx4 v14, s[8:11], s39 offen offset:2048 lds
Mz 248,24,56
v_mfma_f32_16x16x32_bf16 a[248:251], a[24:27], a[60:63], 0
v_mfma_f32_16x16x32_bf16 a[252:255], a[28:31], a[60:63], 0
Mz 252,28,56
Mz 220,28,52
Mz 188,28,48
Mz 156,28,44
Mz 124,28,40
buffer_load_dwordx4 v15, s[8:11], s39 offen offset:3072 lds
Mz 92,28,36
Mz 60,28,32
s_waitcnt vmcnt(13) lgkmcnt(0)
s_barrier
Mv 32,64,96
Rt 0,18,32768
Mv 64,64,100
s_add_u32 s38, s38, 0xffff0080
Mv 96,64,104
Rt 2,18,34816
s_add_u32 m0, s36, 0
Mv 128,64,108
buffer_load_dwordx4 v8, s[4:7], s38 offen offset:0 lds
Mv 160,64,112
Rt 4,19,32768
Mv 192,64,116
Mv 224,64,120
Rt 6,19,34816
Ma 224,64,124
buffer_load_dwordx4 v9, s[4:7], s38 offen offset:1024 lds
Ma 228,68,124
Rt 8,20,32768
Mv 228,68,120
)" R"(
Mv 196,68,116
Rt 10,20,34816
Mv 164,68,112
buffer_load_dwordx4 v10, s[4:7], s38 offen offset:2048 lds
Mv 132,68,108
Rt 12,21,32768
Mv 100,68,104
Mv 68,68,100
Rt 14,21,34816
Mv 36,68,96
buffer_load_dwordx4 v11, s[4:7], s38 offen offset:3072 lds
Mv 40,72,96
Rt 16,22,32768
Mv 72,72,100
s_add_u32 s38, s38, 0x10000
Mv 104,72,104
Rt 18,22,34816
s_add_u32 m0, s36, 4096
Mv 136,72,108
buffer_load_dwordx4 v8, s[4:7], s38 offen offset:0 lds
Mv 168,72,112
Rt 20,23,32768
Mv 200,72,116
Mv 232,72,120
Rt 22,23,34816
Ma 232,72,124
buffer_load_dwordx4 v9, s[4:7], s38 offen offset:1024 lds
Ma 236,76,124
Rt 24,24,32768
Mv 236,76,120
Mv 204,76,116
Rt 26,24,34816
Mv 172,76,112
buffer_load_dwordx4 v10, s[4:7], s38 offen offset:2048 lds
Mv 140,76,108
Rt 28,25,32768
Mv 108,76,104
Mv 76,76,100
Rt 30,25,34816
Mv 44,76,96
buffer_load_dwordx4 v11, s[4:7], s38 offen offset:3072 lds
Mv 48,80,96
Rb 32,16,32768
Mv 80,80,100
Mv 112,80,104
Rb 36,16,34816
Mv 144,80,108
Mv 176,80,112
Rb 40,16,40960
Mv 208,80,116
buffer_load_dword v6, v5, s[4:7], s90 offen
s_lshr_b32 s90, s2, 5
Mv 240,80,120
Rb 44,16,43008
s_and_b32 s40, s2, 15
s_lshl4_add_u32 s90, s90, s40
s_lshl2_add_u32 s90, s90, s33
Ma 240,80,124
s_lshl_b32 s90, s90, 12
s_add_u32 s90, s90, 0x200000
v_lshrrev_b32 v4, 5, v0
Ma 244,84,124
Rb 48,16,49152
v_lshl_or_b32 v5, v4, 30, v5
)" R"(
Mv 244,84,120
Mv 212,84,116
Rb 52,16,51200
Mv 180,84,112
Mv 148,84,108
Rb 56,16,57344
Mv 116,84,104
Mv 84,84,100
Rb 60,16,59392
Mv 52,84,96
Mv 56,88,96
Mv 88,88,100
Mv 120,88,104
Mv 152,88,108
Mv 184,88,112
Mv 216,88,116
Mv 248,88,120
Ma 248,88,124
Ma 252,92,124
Mv 252,92,120
Mv 220,92,116
Mv 188,92,112
Mv 156,92,108
Mv 124,92,104
Mv 92,92,100
Mv 60,92,96
s_waitcnt vmcnt(13) lgkmcnt(0)
s_barrier
Mv 32,0,32
Rt 64,18,49152
s_mov_b32 s85, 0xc108800
v_and_b32 v2, 7, v0
Mv 64,0,36
v_lshlrev_b32 v2, 7, v2
Mv 96,0,40
Rt 66,18,51200
buffer_load_dword v4, v2, s[20:23], s85 offen sc0 sc1
Mv 128,0,44
s_add_u32 s39, s39, 0x40000
Mv 160,0,48
Rt 68,19,49152
s_add_u32 m0, s37, 16384
Mv 192,0,52
buffer_load_dwordx4 v12, s[8:11], s39 offen offset:0 lds
Mv 224,0,56
Rt 70,19,51200
Ma 224,0,60
Ma 228,4,60
Rt 72,20,49152
Mv 228,4,56
Mv 196,4,52
Rt 74,20,51200
Mv 164,4,48
Mv 132,4,44
Rt 76,21,49152
Mv 100,4,40
buffer_load_dwordx4 v13, s[8:11], s39 offen offset:1024 lds
Mv 68,4,36
Rt 78,21,51200
Mv 36,4,32
Mv 40,8,32
Rt 80,22,49152
Mv 72,8,36
Mv 104,8,40
Rt 82,22,51200
)" R"(
Mv 136,8,44
Mv 168,8,48
Rt 84,23,49152
Mv 200,8,52
buffer_load_dwordx4 v14, s[8:11], s39 offen offset:2048 lds
Mv 232,8,56
Rt 86,23,51200
Ma 232,8,60
Ma 236,12,60
Rt 88,24,49152
Mv 236,12,56
Mv 204,12,52
Rt 90,24,51200
Mv 172,12,48
Mv 140,12,44
Rt 92,25,49152
Mv 108,12,40
buffer_load_dwordx4 v15, s[8:11], s39 offen offset:3072 lds
Mv 76,12,36
Rt 94,25,51200
Mv 44,12,32
Mv 48,16,32
Rb 96,17,32768
Mv 80,16,36
Mv 112,16,40
Rb 100,17,34816
Mv 144,16,44
s_add_u32 s39, s39, 0x40000
Mv 176,16,48
Rb 104,17,40960
s_add_u32 m0, s37, 32768
Mv 208,16,52
buffer_load_dwordx4 v12, s[8:11], s39 offen offset:0 lds
Mv 240,16,56
Rb 108,17,43008
Ma 240,16,60
Ma 244,20,60
Rb 112,17,49152
Mv 244,20,56
Mv 212,20,52
Rb 116,17,51200
Mv 180,20,48
Mv 148,20,44
Rb 120,17,57344
Mv 116,20,40
buffer_load_dwordx4 v13, s[8:11], s39 offen offset:1024 lds
Mv 84,20,36
Rb 124,17,59392
Mv 52,20,32
Mv 56,24,32
Rt 176,29,0
Mv 88,24,36
Rt 178,29,8192
Mv 120,24,40
Rt 180,29,128
Mv 152,24,44
Rt 182,29,8320
Mv 184,24,48
Rt 184,29,256
Mv 216,24,52
buffer_load_dwordx4 v14, s[8:11], s39 offen offset:2048 lds
Rt 186,29,8448
Mv 248,24,56
Rt 188,29,384
)" R"(
Ma 248,24,60
Rt 190,29,8576
Ma 252,28,60
Mv 252,28,56
Mv 220,28,52
Mv 188,28,48
Mv 156,28,44
Mv 124,28,40
buffer_load_dwordx4 v15, s[8:11], s39 offen offset:3072 lds
Mv 92,28,36
Mv 60,28,32
s_waitcnt vmcnt(8) lgkmcnt(0)
s_barrier
Mv 32,64,96
Rt 0,18,0
v_xor_b32 v4, s3, v4
v_cmp_gt_u32_e32 vcc, 2, v4
s_andn2_b32 s28, 0xff, vcc_lo
s_cbranch_scc1 .Lzsp1
.Lzok1:
Mv 64,64,100
s_add_u32 s38, s38, 0xffff0080
v_xor_b32 v4, s3, v4
Mv 96,64,104
Rt 2,18,2048
s_add_u32 m0, s36, 32768
Mv 128,64,108
buffer_load_dwordx4 v8, s[4:7], s38 offen offset:0 lds
Mv 160,64,112
Rt 4,19,0
v_and_b32 v4, 1, v4
Mv 192,64,116
v_cmp_eq_u32_e32 vcc, 1, v4
s_mov_b32 s85, vcc_lo
Mv 224,64,120
Rt 6,19,2048
s_and_b32 s28, s2, 7
Ma 224,64,124
buffer_load_dwordx4 v9, s[4:7], s38 offen offset:1024 lds
Ma 228,68,124
Rt 8,20,0
s_lshr_b32 s28, s85, s28
Mv 228,68,120
s_xor_b32 s28, s28, s34
s_and_b32 s28, s28, 1
Mv 196,68,116
Rt 10,20,2048
s_lshl_b32 s84, s28, 13
Mv 164,68,112
buffer_load_dwordx4 v10, s[4:7], s38 offen offset:2048 lds
Mv 132,68,108
Rt 12,21,0
s_lshl1_add_u32 s28, s36, s84
Mv 100,68,104
v_add_u32 v2, s28, v1
v_add_u32 v3, 0x1000, v2
Mv 68,68,100
Rt 14,21,2048
s_bfe_u32 s28, s73, 0x30010
Mv 36,68,96
buffer_load_dwordx4 v11, s[4:7], s38 offen offset:3072 lds
Mv 40,72,96
Rt 16,22,0
s_lshr_b32 s28, s85, s28
Mv 72,72,100
)" R"(
s_add_u32 s38, s38, 0x10000
s_and_b32 s28, s28, 1
Mv 104,72,104
Rt 18,22,2048
s_add_u32 m0, s36, 36864
Mv 136,72,108
buffer_load_dwordx4 v8, s[4:7], s38 offen offset:0 lds
Mv 168,72,112
Rt 20,23,0
s_lshl_b32 s28, s28, 13
Mv 200,72,116
s_add_u32 s73, s73, s28
Mv 232,72,120
Rt 22,23,2048
Ma 232,72,124
buffer_load_dwordx4 v9, s[4:7], s38 offen offset:1024 lds
Ma 236,76,124
Rt 24,24,0
Mv 236,76,120
Mv 204,76,116
Rt 26,24,2048
Mv 172,76,112
buffer_load_dwordx4 v10, s[4:7], s38 offen offset:2048 lds
Mv 140,76,108
Rt 28,25,0
s_mul_i32 s29, s94, 147
Mv 108,76,104
s_lshr_b32 s29, s29, 10
s_mul_i32 s29, s29, 9
Mv 76,76,100
Rt 30,25,2048
s_add_u32 s29, s29, s94
Mv 44,76,96
buffer_load_dwordx4 v11, s[4:7], s38 offen offset:3072 lds
Mv 48,80,96
Rb 32,16,0
s_lshl_b32 s29, s29, 10
Mv 80,80,100
s_add_u32 s29, s29, s82
s_add_u32 s29, s29, s84
buffer_load_dword v6, v5, s[8:11], s90 offen
Mv 112,80,104
Rb 36,16,2048
s_add_u32 s94, s94, s92
Mv 144,80,108
buffer_store_dwordx4 a[176:179], v1, s[20:23], s29 offen sc0 sc1
Mv 176,80,112
Rb 40,16,8192
s_mul_i32 s29, s94, 147
Mv 208,80,116
s_lshr_b32 s29, s29, 10
s_mul_i32 s29, s29, 9
Mv 240,80,120
Rb 44,16,10240
s_add_u32 s29, s29, s94
Ma 240,80,124
s_lshl_b32 s29, s29, 10
s_add_u32 s29, s29, s82
Ma 244,84,124
Rb 48,16,16384
s_add_u32 s29, s29, s84
Mv 244,84,120
s_add_u32 s94, s94, s92
Mv 212,84,116
)" R"(
Rb 52,16,18432
Mv 180,84,112
buffer_store_dwordx4 a[180:183], v1, s[20:23], s29 offen sc0 sc1
Mv 148,84,108
Rb 56,16,24576
s_mul_i32 s29, s94, 147
Mv 116,84,104
s_lshr_b32 s29, s29, 10
s_mul_i32 s29, s29, 9
Mv 84,84,100
Rb 60,16,26624
s_add_u32 s29, s29, s94
Mv 52,84,96
s_lshl_b32 s29, s29, 10
s_add_u32 s29, s29, s82
Mv 56,88,96
s_add_u32 s29, s29, s84
s_add_u32 s94, s94, s92
Mv 88,88,100
buffer_store_dwordx4 a[184:187], v1, s[20:23], s29 offen sc0 sc1
Mv 120,88,104
Mv 152,88,108
Mv 184,88,112
s_mul_i32 s29, s94, 147
s_lshr_b32 s29, s29, 10
Mv 216,88,116
s_mul_i32 s29, s29, 9
s_add_u32 s29, s29, s94
Mv 248,88,120
s_lshl_b32 s29, s29, 10
s_add_u32 s29, s29, s82
Ma 248,88,124
s_add_u32 s29, s29, s84
Ma 252,92,124
s_cmp_gt_u32 s33, 1
s_cmov_b32 s29, 0x40000000
Mv 252,92,120
buffer_store_dwordx4 a[188:191], v1, s[20:23], s29 offen sc0 sc1
s_mov_b32 s40, 5
Mv 220,92,116
Mv 188,92,112
Mv 156,92,108
Mv 124,92,104
Mv 92,92,100
Mv 60,92,96
s_waitcnt vmcnt(17) lgkmcnt(0)
s_barrier
s_mov_b32 s28, s35
s_lshl2_add_u32 s28, s28, s33
s_mul_i32 s43, s28, 28672
v_and_b32 v26, 0x70, v1
v_and_b32 v4, 48, v0
v_xor_b32 v26, v26, v4
v_lshrrev_b32 v4, 3, v0
v_mul_u32_u24 v4, 896, v4
v_add_u32 v26, v26, v4
v_xor_b32 v27, 64, v26
v_add_u32 v27, 6144, v27
v_bfe_u32 v28, v0, 1, 3
v_lshrrev_b32 v4, 4, v0
v_xor_b32 v28, v28, v4
v_lshlrev_b32 v28, 4, v28
v_and_b32 v4, 15, v0
v_lshl_or_b32 v28, v4, 7, v28
s_add_u32 s28, s36, 0x18000
)" R"(
v_add_u32 v28, s28, v28
v_xor_b32 v29, 64, v28
s_add_u32 m0, s36, 131072
s_add_u32 s44, s43, 0
buffer_load_dwordx4 v26, s[12:15], s44 offen offset:0 lds
buffer_load_dwordx4 v27, s[12:15], s44 offen offset:1024 lds
s_add_u32 s44, s43, 12288
buffer_load_dwordx4 v26, s[12:15], s44 offen offset:2048 lds
buffer_load_dwordx4 v27, s[12:15], s44 offen offset:3072 lds
s_add_u32 m0, s36, 135168
s_add_u32 s44, s43, 128
buffer_load_dwordx4 v26, s[12:15], s44 offen offset:0 lds
buffer_load_dwordx4 v27, s[12:15], s44 offen offset:1024 lds
s_add_u32 s44, s43, 12416
buffer_load_dwordx4 v26, s[12:15], s44 offen offset:2048 lds
buffer_load_dwordx4 v27, s[12:15], s44 offen offset:3072 lds
.LzPAK:
Mv 32,0,32
Rt 64,18,16384
Mv 64,0,36
Mv 96,0,40
Rt 66,18,18432
Mv 128,0,44
s_add_u32 s39, s39, 0x40000
Mv 160,0,48
Rt 68,19,16384
s_add_u32 m0, s37, 49152
Mv 192,0,52
buffer_load_dwordx4 v12, s[8:11], s39 offen offset:0 lds
Mv 224,0,56
Rt 70,19,18432
Ma 224,0,60
Ma 228,4,60
Rt 72,20,16384
Mv 228,4,56
Mv 196,4,52
Rt 74,20,18432
Mv 164,4,48
Mv 132,4,44
Rt 76,21,16384
Mv 100,4,40
buffer_load_dwordx4 v13, s[8:11], s39 offen offset:1024 lds
Mv 68,4,36
Rt 78,21,18432
Mv 36,4,32
Mv 40,8,32
Rt 80,22,16384
Mv 72,8,36
Mv 104,8,40
Rt 82,22,18432
Mv 136,8,44
Mv 168,8,48
Rt 84,23,16384
Mv 200,8,52
buffer_load_dwordx4 v14, s[8:11], s39 offen offset:2048 lds
Mv 232,8,56
Rt 86,23,18432
Ma 232,8,60
Ma 236,12,60
Rt 88,24,16384
Mv 236,12,56
Mv 204,12,52
Rt 90,24,18432
Mv 172,12,48
)" R"(
Mv 140,12,44
Rt 92,25,16384
Mv 108,12,40
buffer_load_dwordx4 v15, s[8:11], s39 offen offset:3072 lds
Mv 76,12,36
Rt 94,25,18432
Mv 44,12,32
Mv 48,16,32
Rb 96,17,0
Mv 80,16,36
Mv 112,16,40
Rb 100,17,2048
Mv 144,16,44
s_add_u32 s39, s39, 0x40000
Mv 176,16,48
Rb 104,17,8192
s_add_u32 m0, s37, 0
Mv 208,16,52
buffer_load_dwordx4 v12, s[8:11], s39 offen offset:0 lds
Mv 240,16,56
Rb 108,17,10240
Ma 240,16,60
Ma 244,20,60
Rb 112,17,16384
Mv 244,20,56
Mv 212,20,52
Rb 116,17,18432
Mv 180,20,48
Mv 148,20,44
Rb 120,17,24576
Mv 116,20,40
buffer_load_dwordx4 v13, s[8:11], s39 offen offset:1024 lds
Mv 84,20,36
Rb 124,17,26624
Mv 52,20,32
Mv 56,24,32
Mv 88,24,36
Mv 120,24,40
Mv 152,24,44
Mv 184,24,48
Mv 216,24,52
buffer_load_dwordx4 v14, s[8:11], s39 offen offset:2048 lds
Mv 248,24,56
Ma 248,24,60
Ma 252,28,60
Mv 252,28,56
Mv 220,28,52
Mv 188,28,48
Mv 156,28,44
Mv 124,28,40
buffer_load_dwordx4 v15, s[8:11], s39 offen offset:3072 lds
Mv 92,28,36
Mv 60,28,32
s_waitcnt vmcnt(9) lgkmcnt(0)
s_barrier
Mv 32,64,96
Rt 0,18,32768
Mv 64,64,100
s_add_u32 s38, s38, 0xffff0080
Mv 96,64,104
Rt 2,18,34816
s_add_u32 m0, s36, 0
Mv 128,64,108
buffer_load_dwordx4 v8, s[4:7], s38 offen offset:0 lds
Mv 160,64,112
)" R"(
Rt 4,19,32768
Mv 192,64,116
Mv 224,64,120
Rt 6,19,34816
Ma 224,64,124
buffer_load_dwordx4 v9, s[4:7], s38 offen offset:1024 lds
Ma 228,68,124
Rt 8,20,32768
Mv 228,68,120
Mv 196,68,116
Rt 10,20,34816
Mv 164,68,112
buffer_load_dwordx4 v10, s[4:7], s38 offen offset:2048 lds
Mv 132,68,108
Rt 12,21,32768
Mv 100,68,104
Mv 68,68,100
Rt 14,21,34816
Mv 36,68,96
buffer_load_dwordx4 v11, s[4:7], s38 offen offset:3072 lds
Mv 40,72,96
Rt 16,22,32768
Mv 72,72,100
s_add_u32 s38, s38, 0x10000
Mv 104,72,104
Rt 18,22,34816
s_add_u32 m0, s36, 4096
Mv 136,72,108
buffer_load_dwordx4 v8, s[4:7], s38 offen offset:0 lds
Mv 168,72,112
Rt 20,23,32768
Mv 200,72,116
Mv 232,72,120
Rt 22,23,34816
Ma 232,72,124
buffer_load_dwordx4 v9, s[4:7], s38 offen offset:1024 lds
Ma 236,76,124
Rt 24,24,32768
Mv 236,76,120
Mv 204,76,116
Rt 26,24,34816
Mv 172,76,112
buffer_load_dwordx4 v10, s[4:7], s38 offen offset:2048 lds
Mv 140,76,108
Rt 28,25,32768
Mv 108,76,104
Mv 76,76,100
Rt 30,25,34816
Mv 44,76,96
buffer_load_dwordx4 v11, s[4:7], s38 offen offset:3072 lds
Mv 48,80,96
Rb 32,16,32768
Mv 80,80,100
Mv 112,80,104
Rb 36,16,34816
Mv 144,80,108
s_add_u32 s90, s90, 0x100000
Mv 176,80,112
Rb 40,16,40960
Mv 208,80,116
buffer_load_dword v6, v5, s[8:11], s90 offen
Mv 240,80,120
Rb 44,16,43008
Ma 240,80,124
)" R"(
Ma 244,84,124
Rb 48,16,49152
Mv 244,84,120
Mv 212,84,116
Rb 52,16,51200
Mv 180,84,112
Mv 148,84,108
Rb 56,16,57344
Mv 116,84,104
Mv 84,84,100
Rb 60,16,59392
Mv 52,84,96
Mv 56,88,96
Mv 88,88,100
Mv 120,88,104
Mv 152,88,108
Mv 184,88,112
Mv 216,88,116
Mv 248,88,120
Ma 248,88,124
Ma 252,92,124
Mv 252,92,120
Mv 220,92,116
Mv 188,92,112
Mv 156,92,108
Mv 124,92,104
Mv 92,92,100
Mv 60,92,96
s_waitcnt vmcnt(13) lgkmcnt(0)
s_barrier
Mv 32,0,32
Rt 64,18,49152
Mv 64,0,36
Mv 96,0,40
Rt 66,18,51200
Mv 128,0,44
s_add_u32 s39, s39, 0x40000
Mv 160,0,48
Rt 68,19,49152
s_add_u32 m0, s37, 16384
Mv 192,0,52
buffer_load_dwordx4 v12, s[8:11], s39 offen offset:0 lds
Mv 224,0,56
Rt 70,19,51200
Ma 224,0,60
Ma 228,4,60
Rt 72,20,49152
Mv 228,4,56
Mv 196,4,52
Rt 74,20,51200
Mv 164,4,48
Mv 132,4,44
Rt 76,21,49152
Mv 100,4,40
buffer_load_dwordx4 v13, s[8:11], s39 offen offset:1024 lds
Mv 68,4,36
Rt 78,21,51200
Mv 36,4,32
Mv 40,8,32
Rt 80,22,49152
Mv 72,8,36
Mv 104,8,40
Rt 82,22,51200
Mv 136,8,44
Mv 168,8,48
)" R"(
Rt 84,23,49152
Mv 200,8,52
buffer_load_dwordx4 v14, s[8:11], s39 offen offset:2048 lds
Mv 232,8,56
Rt 86,23,51200
Ma 232,8,60
Ma 236,12,60
Rt 88,24,49152
Mv 236,12,56
Mv 204,12,52
Rt 90,24,51200
Mv 172,12,48
Mv 140,12,44
Rt 92,25,49152
Mv 108,12,40
buffer_load_dwordx4 v15, s[8:11], s39 offen offset:3072 lds
Mv 76,12,36
Rt 94,25,51200
Mv 44,12,32
Mv 48,16,32
Rb 96,17,32768
Mv 80,16,36
Mv 112,16,40
Rb 100,17,34816
Mv 144,16,44
s_add_u32 s39, s39, 0x40000
Mv 176,16,48
Rb 104,17,40960
s_add_u32 m0, s37, 32768
Mv 208,16,52
buffer_load_dwordx4 v12, s[8:11], s39 offen offset:0 lds
Mv 240,16,56
Rb 108,17,43008
Ma 240,16,60
Ma 244,20,60
Rb 112,17,49152
Mv 244,20,56
Mv 212,20,52
Rb 116,17,51200
Mv 180,20,48
Mv 148,20,44
Rb 120,17,57344
Mv 116,20,40
buffer_load_dwordx4 v13, s[8:11], s39 offen offset:1024 lds
Mv 84,20,36
Rb 124,17,59392
Mv 52,20,32
Mv 56,24,32
Mv 88,24,36
Mv 120,24,40
Mv 152,24,44
Mv 184,24,48
Mv 216,24,52
buffer_load_dwordx4 v14, s[8:11], s39 offen offset:2048 lds
Mv 248,24,56
Ma 248,24,60
Ma 252,28,60
Mv 252,28,56
Mv 220,28,52
Mv 188,28,48
Mv 156,28,44
Mv 124,28,40
buffer_load_dwordx4 v15, s[8:11], s39 offen offset:3072 lds
Mv 92,28,36
)" R"(
Mv 60,28,32
s_waitcnt vmcnt(9) lgkmcnt(0)
s_barrier
Mv 32,64,96
Rt 0,18,0
Mv 64,64,100
s_add_u32 s38, s38, 0xffff0080
Mv 96,64,104
Rt 2,18,2048
s_add_u32 m0, s36, 32768
Mv 128,64,108
buffer_load_dwordx4 v8, s[4:7], s38 offen offset:0 lds
Mv 160,64,112
Rt 4,19,0
Mv 192,64,116
Mv 224,64,120
Rt 6,19,2048
Ma 224,64,124
buffer_load_dwordx4 v9, s[4:7], s38 offen offset:1024 lds
Ma 228,68,124
Rt 8,20,0
Mv 228,68,120
Mv 196,68,116
Rt 10,20,2048
Mv 164,68,112
buffer_load_dwordx4 v10, s[4:7], s38 offen offset:2048 lds
Mv 132,68,108
Rt 12,21,0
Mv 100,68,104
Mv 68,68,100
Rt 14,21,2048
Mv 36,68,96
buffer_load_dwordx4 v11, s[4:7], s38 offen offset:3072 lds
Mv 40,72,96
Rt 16,22,0
Mv 72,72,100
s_add_u32 s38, s38, 0x10000
Mv 104,72,104
Rt 18,22,2048
s_add_u32 m0, s36, 36864
Mv 136,72,108
buffer_load_dwordx4 v8, s[4:7], s38 offen offset:0 lds
Mv 168,72,112
Rt 20,23,0
Mv 200,72,116
Mv 232,72,120
Rt 22,23,2048
Ma 232,72,124
buffer_load_dwordx4 v9, s[4:7], s38 offen offset:1024 lds
Ma 236,76,124
Rt 24,24,0
Mv 236,76,120
Mv 204,76,116
Rt 26,24,2048
Mv 172,76,112
buffer_load_dwordx4 v10, s[4:7], s38 offen offset:2048 lds
Mv 140,76,108
Rt 28,25,0
Mv 108,76,104
Mv 76,76,100
Rt 30,25,2048
Mv 44,76,96
buffer_load_dwordx4 v11, s[4:7], s38 offen offset:3072 lds
Mv 48,80,96
Rb 32,16,0
)" R"(
Mv 80,80,100
Mv 112,80,104
Rb 36,16,2048
Mv 144,80,108
s_add_u32 s90, s90, 0x100000
Mv 176,80,112
Rb 40,16,8192
Mv 208,80,116
buffer_load_dword v6, v5, s[8:11], s90 offen
Mv 240,80,120
Rb 44,16,10240
Ma 240,80,124
Ma 244,84,124
Rb 48,16,16384
Mv 244,84,120
Mv 212,84,116
Rb 52,16,18432
Mv 180,84,112
Mv 148,84,108
Rb 56,16,24576
Mv 116,84,104
Mv 84,84,100
Rb 60,16,26624
Mv 52,84,96
Mv 56,88,96
Mv 88,88,100
Mv 120,88,104
Mv 152,88,108
Mv 184,88,112
Mv 216,88,116
Mv 248,88,120
Ma 248,88,124
Ma 252,92,124
Mv 252,92,120
Mv 220,92,116
s_sub_u32 s40, s40, 1
Mv 188,92,112
Mv 156,92,108
Mv 124,92,104
Mv 92,92,100
Mv 60,92,96
s_cmp_lg_u32 s40, 0
s_waitcnt vmcnt(13) lgkmcnt(0)
s_barrier
s_cbranch_scc1 .LzPAK
s_branch .LzPAKX
.LzAV:
Mz 32,32,0
Rt 64,18,16384
Mz 36,36,0
Mz 96,40,0
Rt 66,18,18432
Mz 100,44,0
s_add_u32 s39, s39, 0x40000
Mz 160,48,0
Rt 68,19,16384
s_add_u32 m0, s37, 49152
Mz 164,52,0
buffer_load_dwordx4 v12, s[8:11], s39 offen offset:0 lds
Mz 224,56,0
Rt 70,19,18432
Mz 228,60,0
Mz 236,60,4
Rt 72,20,16384
Mz 232,56,4
)" R"(
Mz 172,52,4
Rt 74,20,18432
Mz 168,48,4
Mz 108,44,4
Rt 76,21,16384
Mz 104,40,4
buffer_load_dwordx4 v13, s[8:11], s39 offen offset:1024 lds
Mz 44,36,4
Rt 78,21,18432
Mz 40,32,4
Mz 48,32,8
Rt 80,22,16384
Mz 52,36,8
Mz 112,40,8
Rt 82,22,18432
Mz 116,44,8
Mz 176,48,8
Rt 84,23,16384
Mz 180,52,8
buffer_load_dwordx4 v14, s[8:11], s39 offen offset:2048 lds
Mz 240,56,8
Rt 86,23,18432
Mz 244,60,8
Mz 252,60,12
Rt 88,24,16384
Mz 248,56,12
Mz 188,52,12
Rt 90,24,18432
Mz 184,48,12
Mz 124,44,12
Rt 92,25,16384
Mz 120,40,12
buffer_load_dwordx4 v15, s[8:11], s39 offen offset:3072 lds
Mz 60,36,12
Rt 94,25,18432
Mz 56,32,12
Mz 64,32,16
Rb 96,17,0
Mz 68,36,16
Mz 128,40,16
Rb 100,17,2048
Mz 132,44,16
s_add_u32 s39, s39, 0x40000
Mz 192,48,16
Rb 104,17,8192
s_add_u32 m0, s37, 0
Mz 196,52,16
buffer_load_dwordx4 v12, s[8:11], s39 offen offset:0 lds
v_mfma_f32_16x16x32_bf16 a[224:227], a[56:59], a[16:19], 0
Rb 108,17,10240
v_mfma_f32_16x16x32_bf16 a[228:231], a[60:63], a[16:19], 0
v_mfma_f32_16x16x32_bf16 a[236:239], a[60:63], a[20:23], 0
Rb 112,17,16384
v_mfma_f32_16x16x32_bf16 a[232:235], a[56:59], a[20:23], 0
Mz 204,52,20
Rb 116,17,18432
Mz 200,48,20
Mz 140,44,20
Rb 120,17,24576
Mz 136,40,20
buffer_load_dwordx4 v13, s[8:11], s39 offen offset:1024 lds
Mz 76,36,20
Rb 124,17,26624
Mz 72,32,20
)" R"(
Mz 80,32,24
Mz 84,36,24
Mz 144,40,24
s_lshl2_add_u32 s90, s2, s33
s_lshl_b32 s90, s90, 13
v_lshlrev_b32 v5, 7, v0
Mz 148,44,24
Mz 208,48,24
Mz 212,52,24
buffer_load_dwordx4 v14, s[8:11], s39 offen offset:2048 lds
v_mfma_f32_16x16x32_bf16 a[240:243], a[56:59], a[24:27], 0
v_mfma_f32_16x16x32_bf16 a[244:247], a[60:63], a[24:27], 0
v_mfma_f32_16x16x32_bf16 a[252:255], a[60:63], a[28:31], 0
v_mfma_f32_16x16x32_bf16 a[248:251], a[56:59], a[28:31], 0
Mz 220,52,28
Mz 216,48,28
Mz 156,44,28
Mz 152,40,28
buffer_load_dwordx4 v15, s[8:11], s39 offen offset:3072 lds
Mz 92,36,28
Mz 88,32,28
s_waitcnt vmcnt(13) lgkmcnt(0)
s_barrier
Mv 32,96,64
Rt 0,18,32768
Mv 36,100,64
s_add_u32 s38, s38, 0xffff0080
Mv 96,104,64
Rt 2,18,34816
s_add_u32 m0, s36, 0
Mv 100,108,64
buffer_load_dwordx4 v8, s[4:7], s38 offen offset:0 lds
Mv 160,112,64
Rt 4,19,32768
Mv 164,116,64
Mv 224,120,64
Rt 6,19,34816
Mv 228,124,64
buffer_load_dwordx4 v9, s[4:7], s38 offen offset:1024 lds
Mv 236,124,68
Rt 8,20,32768
Mv 232,120,68
Mv 172,116,68
Rt 10,20,34816
Mv 168,112,68
buffer_load_dwordx4 v10, s[4:7], s38 offen offset:2048 lds
Mv 108,108,68
Rt 12,21,32768
Mv 104,104,68
Mv 44,100,68
Rt 14,21,34816
Mv 40,96,68
buffer_load_dwordx4 v11, s[4:7], s38 offen offset:3072 lds
Mv 48,96,72
Rt 16,22,32768
Mv 52,100,72
s_add_u32 s38, s38, 0x10000
Mv 112,104,72
Rt 18,22,34816
s_add_u32 m0, s36, 4096
Mv 116,108,72
buffer_load_dwordx4 v8, s[4:7], s38 offen offset:0 lds
Mv 176,112,72
Rt 20,23,32768
Mv 180,116,72
)" R"(
Mv 240,120,72
Rt 22,23,34816
Mv 244,124,72
buffer_load_dwordx4 v9, s[4:7], s38 offen offset:1024 lds
Mv 252,124,76
Rt 24,24,32768
Mv 248,120,76
Mv 188,116,76
Rt 26,24,34816
Mv 184,112,76
buffer_load_dwordx4 v10, s[4:7], s38 offen offset:2048 lds
Mv 124,108,76
Rt 28,25,32768
Mv 120,104,76
Mv 60,100,76
Rt 30,25,34816
Mv 56,96,76
buffer_load_dwordx4 v11, s[4:7], s38 offen offset:3072 lds
Mv 64,96,80
Rb 32,16,32768
Mv 68,100,80
Mv 128,104,80
Rb 36,16,34816
Mv 132,108,80
Mv 192,112,80
Rb 40,16,40960
Mv 196,116,80
buffer_load_dword v6, v5, s[4:7], s90 offen
s_lshr_b32 s90, s2, 5
Ma 224,120,80
Rb 44,16,43008
s_and_b32 s40, s2, 15
s_lshl4_add_u32 s90, s90, s40
s_lshl2_add_u32 s90, s90, s33
Ma 228,124,80
s_lshl_b32 s90, s90, 12
s_add_u32 s90, s90, 0x200000
v_lshrrev_b32 v4, 5, v0
Ma 236,124,84
Rb 48,16,49152
v_lshl_or_b32 v5, v4, 30, v5
Ma 232,120,84
Mv 204,116,84
Rb 52,16,51200
Mv 200,112,84
Mv 140,108,84
Rb 56,16,57344
Mv 136,104,84
Mv 76,100,84
Rb 60,16,59392
Mv 72,96,84
Mv 80,96,88
Mv 84,100,88
Mv 144,104,88
Mv 148,108,88
Mv 208,112,88
Mv 212,116,88
Ma 240,120,88
Ma 244,124,88
Ma 252,124,92
Ma 248,120,92
Mv 220,116,92
Mv 216,112,92
Mv 156,108,92
)" R"(
Mv 152,104,92
Mv 92,100,92
Mv 88,96,92
s_waitcnt vmcnt(13) lgkmcnt(0)
s_barrier
Mv 32,32,0
Rt 64,18,49152
s_mov_b32 s85, 0xc108800
v_and_b32 v2, 7, v0
Mv 36,36,0
v_lshlrev_b32 v2, 7, v2
Mv 96,40,0
Rt 66,18,51200
buffer_load_dword v4, v2, s[20:23], s85 offen sc0 sc1
Mv 100,44,0
s_add_u32 s39, s39, 0x40000
Mv 160,48,0
Rt 68,19,49152
s_add_u32 m0, s37, 16384
Mv 164,52,0
buffer_load_dwordx4 v12, s[8:11], s39 offen offset:0 lds
Mv 224,56,0
Rt 70,19,51200
Mv 228,60,0
Mv 236,60,4
Rt 72,20,49152
Mv 232,56,4
Mv 172,52,4
Rt 74,20,51200
Mv 168,48,4
Mv 108,44,4
Rt 76,21,49152
Mv 104,40,4
buffer_load_dwordx4 v13, s[8:11], s39 offen offset:1024 lds
Mv 44,36,4
Rt 78,21,51200
Mv 40,32,4
Mv 48,32,8
Rt 80,22,49152
Mv 52,36,8
Mv 112,40,8
Rt 82,22,51200
Mv 116,44,8
Mv 176,48,8
Rt 84,23,49152
Mv 180,52,8
buffer_load_dwordx4 v14, s[8:11], s39 offen offset:2048 lds
Mv 240,56,8
Rt 86,23,51200
Mv 244,60,8
Mv 252,60,12
Rt 88,24,49152
Mv 248,56,12
Mv 188,52,12
Rt 90,24,51200
Mv 184,48,12
Mv 124,44,12
Rt 92,25,49152
Mv 120,40,12
buffer_load_dwordx4 v15, s[8:11], s39 offen offset:3072 lds
Mv 60,36,12
Rt 94,25,51200
Mv 56,32,12
Mv 64,32,16
Rb 96,17,32768
)" R"(
Mv 68,36,16
Mv 128,40,16
Rb 100,17,34816
Mv 132,44,16
s_add_u32 s39, s39, 0x40000
Mv 192,48,16
Rb 104,17,40960
s_add_u32 m0, s37, 32768
Mv 196,52,16
buffer_load_dwordx4 v12, s[8:11], s39 offen offset:0 lds
Ma 224,56,16
Rb 108,17,43008
Ma 228,60,16
Ma 236,60,20
Rb 112,17,49152
Ma 232,56,20
Mv 204,52,20
Rb 116,17,51200
Mv 200,48,20
Mv 140,44,20
Rb 120,17,57344
Mv 136,40,20
buffer_load_dwordx4 v13, s[8:11], s39 offen offset:1024 lds
Mv 76,36,20
Rb 124,17,59392
Mv 72,32,20
Mv 80,32,24
Rt 176,29,0
Mv 84,36,24
Rt 178,29,8192
Mv 144,40,24
Rt 180,29,128
Mv 148,44,24
Rt 182,29,8320
Mv 208,48,24
Rt 184,29,256
Mv 212,52,24
buffer_load_dwordx4 v14, s[8:11], s39 offen offset:2048 lds
Rt 186,29,8448
Ma 240,56,24
Rt 188,29,384
Ma 244,60,24
Rt 190,29,8576
Ma 252,60,28
Ma 248,56,28
Mv 220,52,28
Mv 216,48,28
Mv 156,44,28
Mv 152,40,28
buffer_load_dwordx4 v15, s[8:11], s39 offen offset:3072 lds
Mv 92,36,28
Mv 88,32,28
s_waitcnt vmcnt(8) lgkmcnt(0)
s_barrier
Mv 32,96,64
Rt 0,18,0
v_xor_b32 v4, s3, v4
v_cmp_gt_u32_e32 vcc, 2, v4
s_andn2_b32 s28, 0xff, vcc_lo
s_cbranch_scc1 .Lzsp2
.Lzok2:
Mv 36,100,64
s_add_u32 s38, s38, 0xffff0080
v_xor_b32 v4, s3, v4
Mv 96,104,64
)" R"(
Rt 2,18,2048
s_add_u32 m0, s36, 32768
Mv 100,108,64
buffer_load_dwordx4 v8, s[4:7], s38 offen offset:0 lds
Mv 160,112,64
Rt 4,19,0
v_and_b32 v4, 1, v4
Mv 164,116,64
v_cmp_eq_u32_e32 vcc, 1, v4
s_mov_b32 s85, vcc_lo
Mv 224,120,64
Rt 6,19,2048
s_and_b32 s28, s2, 7
Mv 228,124,64
buffer_load_dwordx4 v9, s[4:7], s38 offen offset:1024 lds
Mv 236,124,68
Rt 8,20,0
s_lshr_b32 s28, s85, s28
Mv 232,120,68
s_xor_b32 s28, s28, s34
s_and_b32 s28, s28, 1
Mv 172,116,68
Rt 10,20,2048
s_lshl_b32 s84, s28, 13
Mv 168,112,68
buffer_load_dwordx4 v10, s[4:7], s38 offen offset:2048 lds
Mv 108,108,68
Rt 12,21,0
s_lshl1_add_u32 s28, s36, s84
Mv 104,104,68
v_add_u32 v2, s28, v1
v_add_u32 v3, 0x1000, v2
Mv 44,100,68
Rt 14,21,2048
s_bfe_u32 s28, s73, 0x30010
Mv 40,96,68
buffer_load_dwordx4 v11, s[4:7], s38 offen offset:3072 lds
Mv 48,96,72
Rt 16,22,0
s_lshr_b32 s28, s85, s28
Mv 52,100,72
s_add_u32 s38, s38, 0x10000
s_and_b32 s28, s28, 1
Mv 112,104,72
Rt 18,22,2048
s_add_u32 m0, s36, 36864
Mv 116,108,72
buffer_load_dwordx4 v8, s[4:7], s38 offen offset:0 lds
Mv 176,112,72
Rt 20,23,0
s_lshl_b32 s28, s28, 13
Mv 180,116,72
s_add_u32 s73, s73, s28
Mv 240,120,72
Rt 22,23,2048
Mv 244,124,72
buffer_load_dwordx4 v9, s[4:7], s38 offen offset:1024 lds
Mv 252,124,76
Rt 24,24,0
Mv 248,120,76
Mv 188,116,76
Rt 26,24,2048
Mv 184,112,76
buffer_load_dwordx4 v10, s[4:7], s38 offen offset:2048 lds
)" R"(
Mv 124,108,76
Rt 28,25,0
s_mul_i32 s29, s94, 147
Mv 120,104,76
s_lshr_b32 s29, s29, 10
s_mul_i32 s29, s29, 9
Mv 60,100,76
Rt 30,25,2048
s_add_u32 s29, s29, s94
Mv 56,96,76
buffer_load_dwordx4 v11, s[4:7], s38 offen offset:3072 lds
Mv 64,96,80
Rb 32,16,0
s_lshl_b32 s29, s29, 10
Mv 68,100,80
s_add_u32 s29, s29, s82
s_add_u32 s29, s29, s84
buffer_load_dword v6, v5, s[8:11], s90 offen
Mv 128,104,80
Rb 36,16,2048
s_add_u32 s94, s94, s92
Mv 132,108,80
buffer_store_dwordx4 a[176:179], v1, s[20:23], s29 offen sc0 sc1
Mv 192,112,80
Rb 40,16,8192
s_mul_i32 s29, s94, 147
Mv 196,116,80
s_lshr_b32 s29, s29, 10
s_mul_i32 s29, s29, 9
Ma 224,120,80
Rb 44,16,10240
s_add_u32 s29, s29, s94
Ma 228,124,80
s_lshl_b32 s29, s29, 10
s_add_u32 s29, s29, s82
Ma 236,124,84
Rb 48,16,16384
s_add_u32 s29, s29, s84
Ma 232,120,84
s_add_u32 s94, s94, s92
Mv 204,116,84
Rb 52,16,18432
Mv 200,112,84
buffer_store_dwordx4 a[180:183], v1, s[20:23], s29 offen sc0 sc1
Mv 140,108,84
Rb 56,16,24576
s_mul_i32 s29, s94, 147
Mv 136,104,84
s_lshr_b32 s29, s29, 10
s_mul_i32 s29, s29, 9
Mv 76,100,84
Rb 60,16,26624
s_add_u32 s29, s29, s94
Mv 72,96,84
s_lshl_b32 s29, s29, 10
s_add_u32 s29, s29, s82
Mv 80,96,88
s_add_u32 s29, s29, s84
s_add_u32 s94, s94, s92
Mv 84,100,88
buffer_store_dwordx4 a[184:187], v1, s[20:23], s29 offen sc0 sc1
Mv 144,104,88
Mv 148,108,88
Mv 208,112,88
)" R"(
s_mul_i32 s29, s94, 147
s_lshr_b32 s29, s29, 10
Mv 212,116,88
s_mul_i32 s29, s29, 9
s_add_u32 s29, s29, s94
Ma 240,120,88
s_lshl_b32 s29, s29, 10
s_add_u32 s29, s29, s82
Ma 244,124,88
s_add_u32 s29, s29, s84
Ma 252,124,92
s_cmp_gt_u32 s33, 1
s_cmov_b32 s29, 0x40000000
Ma 248,120,92
buffer_store_dwordx4 a[188:191], v1, s[20:23], s29 offen sc0 sc1
s_mov_b32 s40, 5
Mv 220,116,92
Mv 216,112,92
Mv 156,108,92
Mv 152,104,92
Mv 92,100,92
Mv 88,96,92
s_waitcnt vmcnt(17) lgkmcnt(0)
s_barrier
s_mov_b32 s28, s35
s_lshl2_add_u32 s28, s28, s33
s_mul_i32 s43, s28, 28672
v_and_b32 v26, 0x70, v1
v_and_b32 v4, 48, v0
v_xor_b32 v26, v26, v4
v_lshrrev_b32 v4, 3, v0
v_mul_u32_u24 v4, 896, v4
v_add_u32 v26, v26, v4
v_xor_b32 v27, 64, v26
v_add_u32 v27, 6144, v27
v_bfe_u32 v28, v0, 1, 3
v_lshrrev_b32 v4, 4, v0
v_xor_b32 v28, v28, v4
v_lshlrev_b32 v28, 4, v28
v_and_b32 v4, 15, v0
v_lshl_or_b32 v28, v4, 7, v28
s_add_u32 s28, s36, 0x18000
v_add_u32 v28, s28, v28
v_xor_b32 v29, 64, v28
s_add_u32 m0, s36, 131072
s_add_u32 s44, s43, 0
buffer_load_dwordx4 v26, s[12:15], s44 offen offset:0 lds
buffer_load_dwordx4 v27, s[12:15], s44 offen offset:1024 lds
s_add_u32 s44, s43, 12288
buffer_load_dwordx4 v26, s[12:15], s44 offen offset:2048 lds
buffer_load_dwordx4 v27, s[12:15], s44 offen offset:3072 lds
s_add_u32 m0, s36, 135168
s_add_u32 s44, s43, 128
buffer_load_dwordx4 v26, s[12:15], s44 offen offset:0 lds
buffer_load_dwordx4 v27, s[12:15], s44 offen offset:1024 lds
s_add_u32 s44, s43, 12416
buffer_load_dwordx4 v26, s[12:15], s44 offen offset:2048 lds
buffer_load_dwordx4 v27, s[12:15], s44 offen offset:3072 lds
.LzPAV:
Mv 32,32,0
Rt 64,18,16384
Mv 36,36,0
Mv 96,40,0
Rt 66,18,18432
Mv 100,44,0
)" R"(
s_add_u32 s39, s39, 0x40000
Mv 160,48,0
Rt 68,19,16384
s_add_u32 m0, s37, 49152
Mv 164,52,0
buffer_load_dwordx4 v12, s[8:11], s39 offen offset:0 lds
Mv 224,56,0
Rt 70,19,18432
Mv 228,60,0
Mv 236,60,4
Rt 72,20,16384
Mv 232,56,4
Mv 172,52,4
Rt 74,20,18432
Mv 168,48,4
Mv 108,44,4
Rt 76,21,16384
Mv 104,40,4
buffer_load_dwordx4 v13, s[8:11], s39 offen offset:1024 lds
Mv 44,36,4
Rt 78,21,18432
Mv 40,32,4
Mv 48,32,8
Rt 80,22,16384
Mv 52,36,8
Mv 112,40,8
Rt 82,22,18432
Mv 116,44,8
Mv 176,48,8
Rt 84,23,16384
Mv 180,52,8
buffer_load_dwordx4 v14, s[8:11], s39 offen offset:2048 lds
Mv 240,56,8
Rt 86,23,18432
Mv 244,60,8
Mv 252,60,12
Rt 88,24,16384
Mv 248,56,12
Mv 188,52,12
Rt 90,24,18432
Mv 184,48,12
Mv 124,44,12
Rt 92,25,16384
Mv 120,40,12
buffer_load_dwordx4 v15, s[8:11], s39 offen offset:3072 lds
Mv 60,36,12
Rt 94,25,18432
Mv 56,32,12
Mv 64,32,16
Rb 96,17,0
Mv 68,36,16
Mv 128,40,16
Rb 100,17,2048
Mv 132,44,16
s_add_u32 s39, s39, 0x40000
Mv 192,48,16
Rb 104,17,8192
s_add_u32 m0, s37, 0
Mv 196,52,16
buffer_load_dwordx4 v12, s[8:11], s39 offen offset:0 lds
Ma 224,56,16
Rb 108,17,10240
Ma 228,60,16
Ma 236,60,20
)" R"(
Rb 112,17,16384
Ma 232,56,20
Mv 204,52,20
Rb 116,17,18432
Mv 200,48,20
Mv 140,44,20
Rb 120,17,24576
Mv 136,40,20
buffer_load_dwordx4 v13, s[8:11], s39 offen offset:1024 lds
Mv 76,36,20
Rb 124,17,26624
Mv 72,32,20
Mv 80,32,24
Mv 84,36,24
Mv 144,40,24
Mv 148,44,24
Mv 208,48,24
Mv 212,52,24
buffer_load_dwordx4 v14, s[8:11], s39 offen offset:2048 lds
Ma 240,56,24
Ma 244,60,24
Ma 252,60,28
Ma 248,56,28
Mv 220,52,28
Mv 216,48,28
Mv 156,44,28
Mv 152,40,28
buffer_load_dwordx4 v15, s[8:11], s39 offen offset:3072 lds
Mv 92,36,28
Mv 88,32,28
s_waitcnt vmcnt(9) lgkmcnt(0)
s_barrier
Mv 32,96,64
Rt 0,18,32768
Mv 36,100,64
s_add_u32 s38, s38, 0xffff0080
Mv 96,104,64
Rt 2,18,34816
s_add_u32 m0, s36, 0
Mv 100,108,64
buffer_load_dwordx4 v8, s[4:7], s38 offen offset:0 lds
Mv 160,112,64
Rt 4,19,32768
Mv 164,116,64
Mv 224,120,64
Rt 6,19,34816
Mv 228,124,64
buffer_load_dwordx4 v9, s[4:7], s38 offen offset:1024 lds
Mv 236,124,68
Rt 8,20,32768
Mv 232,120,68
Mv 172,116,68
Rt 10,20,34816
Mv 168,112,68
buffer_load_dwordx4 v10, s[4:7], s38 offen offset:2048 lds
Mv 108,108,68
Rt 12,21,32768
Mv 104,104,68
Mv 44,100,68
Rt 14,21,34816
Mv 40,96,68
buffer_load_dwordx4 v11, s[4:7], s38 offen offset:3072 lds
Mv 48,96,72
Rt 16,22,32768
Mv 52,100,72
)" R"(
s_add_u32 s38, s38, 0x10000
Mv 112,104,72
Rt 18,22,34816
s_add_u32 m0, s36, 4096
Mv 116,108,72
buffer_load_dwordx4 v8, s[4:7], s38 offen offset:0 lds
Mv 176,112,72
Rt 20,23,32768
Mv 180,116,72
Mv 240,120,72
Rt 22,23,34816
Mv 244,124,72
buffer_load_dwordx4 v9, s[4:7], s38 offen offset:1024 lds
Mv 252,124,76
Rt 24,24,32768
Mv 248,120,76
Mv 188,116,76
Rt 26,24,34816
Mv 184,112,76
buffer_load_dwordx4 v10, s[4:7], s38 offen offset:2048 lds
Mv 124,108,76
Rt 28,25,32768
Mv 120,104,76
Mv 60,100,76
Rt 30,25,34816
Mv 56,96,76
buffer_load_dwordx4 v11, s[4:7], s38 offen offset:3072 lds
Mv 64,96,80
Rb 32,16,32768
Mv 68,100,80
Mv 128,104,80
Rb 36,16,34816
Mv 132,108,80
s_add_u32 s90, s90, 0x100000
Mv 192,112,80
Rb 40,16,40960
Mv 196,116,80
buffer_load_dword v6, v5, s[8:11], s90 offen
Ma 224,120,80
Rb 44,16,43008
Ma 228,124,80
Ma 236,124,84
Rb 48,16,49152
Ma 232,120,84
Mv 204,116,84
Rb 52,16,51200
Mv 200,112,84
Mv 140,108,84
Rb 56,16,57344
Mv 136,104,84
Mv 76,100,84
Rb 60,16,59392
Mv 72,96,84
Mv 80,96,88
Mv 84,100,88
Mv 144,104,88
Mv 148,108,88
Mv 208,112,88
Mv 212,116,88
Ma 240,120,88
Ma 244,124,88
Ma 252,124,92
Ma 248,120,92
Mv 220,116,92
)" R"(
Mv 216,112,92
Mv 156,108,92
Mv 152,104,92
Mv 92,100,92
Mv 88,96,92
s_waitcnt vmcnt(13) lgkmcnt(0)
s_barrier
Mv 32,32,0
Rt 64,18,49152
Mv 36,36,0
Mv 96,40,0
Rt 66,18,51200
Mv 100,44,0
s_add_u32 s39, s39, 0x40000
Mv 160,48,0
Rt 68,19,49152
s_add_u32 m0, s37, 16384
Mv 164,52,0
buffer_load_dwordx4 v12, s[8:11], s39 offen offset:0 lds
Mv 224,56,0
Rt 70,19,51200
Mv 228,60,0
Mv 236,60,4
Rt 72,20,49152
Mv 232,56,4
Mv 172,52,4
Rt 74,20,51200
Mv 168,48,4
Mv 108,44,4
Rt 76,21,49152
Mv 104,40,4
buffer_load_dwordx4 v13, s[8:11], s39 offen offset:1024 lds
Mv 44,36,4
Rt 78,21,51200
Mv 40,32,4
Mv 48,32,8
Rt 80,22,49152
Mv 52,36,8
Mv 112,40,8
Rt 82,22,51200
Mv 116,44,8
Mv 176,48,8
Rt 84,23,49152
Mv 180,52,8
buffer_load_dwordx4 v14, s[8:11], s39 offen offset:2048 lds
Mv 240,56,8
Rt 86,23,51200
Mv 244,60,8
Mv 252,60,12
Rt 88,24,49152
Mv 248,56,12
Mv 188,52,12
Rt 90,24,51200
Mv 184,48,12
Mv 124,44,12
Rt 92,25,49152
Mv 120,40,12
buffer_load_dwordx4 v15, s[8:11], s39 offen offset:3072 lds
Mv 60,36,12
Rt 94,25,51200
Mv 56,32,12
Mv 64,32,16
Rb 96,17,32768
Mv 68,36,16
Mv 128,40,16
)" R"(
Rb 100,17,34816
Mv 132,44,16
s_add_u32 s39, s39, 0x40000
Mv 192,48,16
Rb 104,17,40960
s_add_u32 m0, s37, 32768
Mv 196,52,16
buffer_load_dwordx4 v12, s[8:11], s39 offen offset:0 lds
Ma 224,56,16
Rb 108,17,43008
Ma 228,60,16
Ma 236,60,20
Rb 112,17,49152
Ma 232,56,20
Mv 204,52,20
Rb 116,17,51200
Mv 200,48,20
Mv 140,44,20
Rb 120,17,57344
Mv 136,40,20
buffer_load_dwordx4 v13, s[8:11], s39 offen offset:1024 lds
Mv 76,36,20
Rb 124,17,59392
Mv 72,32,20
Mv 80,32,24
Mv 84,36,24
Mv 144,40,24
Mv 148,44,24
Mv 208,48,24
Mv 212,52,24
buffer_load_dwordx4 v14, s[8:11], s39 offen offset:2048 lds
Ma 240,56,24
Ma 244,60,24
Ma 252,60,28
Ma 248,56,28
Mv 220,52,28
Mv 216,48,28
Mv 156,44,28
Mv 152,40,28
buffer_load_dwordx4 v15, s[8:11], s39 offen offset:3072 lds
Mv 92,36,28
Mv 88,32,28
s_waitcnt vmcnt(9) lgkmcnt(0)
s_barrier
Mv 32,96,64
Rt 0,18,0
Mv 36,100,64
s_add_u32 s38, s38, 0xffff0080
Mv 96,104,64
Rt 2,18,2048
s_add_u32 m0, s36, 32768
Mv 100,108,64
buffer_load_dwordx4 v8, s[4:7], s38 offen offset:0 lds
Mv 160,112,64
Rt 4,19,0
Mv 164,116,64
Mv 224,120,64
Rt 6,19,2048
Mv 228,124,64
buffer_load_dwordx4 v9, s[4:7], s38 offen offset:1024 lds
Mv 236,124,68
Rt 8,20,0
Mv 232,120,68
Mv 172,116,68
Rt 10,20,2048
)" R"(
Mv 168,112,68
buffer_load_dwordx4 v10, s[4:7], s38 offen offset:2048 lds
Mv 108,108,68
Rt 12,21,0
Mv 104,104,68
Mv 44,100,68
Rt 14,21,2048
Mv 40,96,68
buffer_load_dwordx4 v11, s[4:7], s38 offen offset:3072 lds
Mv 48,96,72
Rt 16,22,0
Mv 52,100,72
s_add_u32 s38, s38, 0x10000
Mv 112,104,72
Rt 18,22,2048
s_add_u32 m0, s36, 36864
Mv 116,108,72
buffer_load_dwordx4 v8, s[4:7], s38 offen offset:0 lds
Mv 176,112,72
Rt 20,23,0
Mv 180,116,72
Mv 240,120,72
Rt 22,23,2048
Mv 244,124,72
buffer_load_dwordx4 v9, s[4:7], s38 offen offset:1024 lds
Mv 252,124,76
Rt 24,24,0
Mv 248,120,76
Mv 188,116,76
Rt 26,24,2048
Mv 184,112,76
buffer_load_dwordx4 v10, s[4:7], s38 offen offset:2048 lds
Mv 124,108,76
Rt 28,25,0
Mv 120,104,76
Mv 60,100,76
Rt 30,25,2048
Mv 56,96,76
buffer_load_dwordx4 v11, s[4:7], s38 offen offset:3072 lds
Mv 64,96,80
Rb 32,16,0
Mv 68,100,80
Mv 128,104,80
Rb 36,16,2048
Mv 132,108,80
s_add_u32 s90, s90, 0x100000
Mv 192,112,80
Rb 40,16,8192
Mv 196,116,80
buffer_load_dword v6, v5, s[8:11], s90 offen
Ma 224,120,80
Rb 44,16,10240
Ma 228,124,80
Ma 236,124,84
Rb 48,16,16384
Ma 232,120,84
Mv 204,116,84
Rb 52,16,18432
Mv 200,112,84
Mv 140,108,84
Rb 56,16,24576
Mv 136,104,84
Mv 76,100,84
Rb 60,16,26624
)" R"(
Mv 72,96,84
Mv 80,96,88
Mv 84,100,88
Mv 144,104,88
Mv 148,108,88
Mv 208,112,88
Mv 212,116,88
Ma 240,120,88
Ma 244,124,88
Ma 252,124,92
Ma 248,120,92
Mv 220,116,92
s_sub_u32 s40, s40, 1
Mv 216,112,92
Mv 156,108,92
Mv 152,104,92
Mv 92,100,92
Mv 88,96,92
s_cmp_lg_u32 s40, 0
s_waitcnt vmcnt(13) lgkmcnt(0)
s_barrier
s_cbranch_scc1 .LzPAV
s_branch .LzPAVX
.LzB0:
s_mul_i32 s41, s33, 7168
s_mul_i32 s42, s33, 0x70000
s_lshl_b32 s28, s34, 9
s_add_u32 s42, s42, s28
v_lshrrev_b32 v28, 3, v0
v_and_b32 v29, 0x70, v1
v_lshl_or_b32 v28, v28, 13, v29
v_lshlrev_b32 v29, 1, v0
v_and_b32 v29, 32, v29
v_xor_b32 v28, v28, v29
s_and_b32 s28, s33, 1
s_lshl_b32 s28, s28, 6
v_xor_b32 v240, s28, v28
v_xor_b32 v241, 64, v240
v_add_u32 v241, 0xfc00, v241
v_add_u32 v242, 0x1f800, v240
v_add_u32 v243, 0x1f800, v241
s_sub_u32 s28, 15, s35
s_lshl2_add_u32 s28, s28, s33
s_mul_i32 s43, s28, 28672
v_and_b32 v96, 0x70, v1
v_and_b32 v28, 48, v0
v_xor_b32 v96, v96, v28
v_lshrrev_b32 v28, 3, v0
v_mul_u32_u24 v28, 896, v28
v_add_u32 v96, v96, v28
v_xor_b32 v97, 64, v96
v_add_u32 v97, 6144, v97
v_bfe_u32 v98, v0, 1, 3
v_lshrrev_b32 v28, 4, v0
v_xor_b32 v98, v98, v28
v_lshlrev_b32 v98, 4, v98
v_and_b32 v28, 15, v0
v_lshl_or_b32 v98, v28, 7, v98
s_add_u32 s28, s36, 0x0
v_add_u32 v98, s28, v98
v_xor_b32 v99, 64, v98
v_add_u32 v100, 0x10000, v98
v_add_u32 v101, 0x10000, v99
v_add_u32 v102, 0x20000, v98
v_add_u32 v103, 0x20000, v99
)" R"(
s_waitcnt lgkmcnt(0)
s_mov_b32 s16, s68
s_mov_b32 s17, s69
s_mov_b32 s18, 0x380000
s_mov_b32 s19, 0x20000
s_mov_b32 s12, s64
s_mov_b32 s13, s65
s_mov_b32 s14, 0x1c0000
s_mov_b32 s15, 0x20000
s_add_u32 m0, s36, 0
s_add_u32 s44, s43, 0
buffer_load_dwordx4 v96, s[12:15], s44 offen offset:0 lds
buffer_load_dwordx4 v97, s[12:15], s44 offen offset:1024 lds
s_add_u32 s44, s43, 12288
buffer_load_dwordx4 v96, s[12:15], s44 offen offset:2048 lds
buffer_load_dwordx4 v97, s[12:15], s44 offen offset:3072 lds
s_add_u32 m0, s41, 32768
s_add_u32 s92, s42, 0x0
buffer_load_dwordx4 v240, s[16:19], s92 offen offset:0 lds
buffer_load_dwordx4 v241, s[16:19], s92 offen offset:1024 lds
buffer_load_dwordx4 v242, s[16:19], s92 offen offset:2048 lds
buffer_load_dwordx4 v243, s[16:19], s92 offen offset:3072 lds
s_add_u32 m0, s41, 36864
s_add_u32 s92, s42, 0x40000
buffer_load_dwordx4 v240, s[16:19], s92 offen offset:0 lds
buffer_load_dwordx4 v241, s[16:19], s92 offen offset:1024 lds
buffer_load_dwordx4 v242, s[16:19], s92 offen offset:2048 lds
s_add_u32 m0, s36, 4096
s_add_u32 s44, s43, 128
buffer_load_dwordx4 v96, s[12:15], s44 offen offset:0 lds
buffer_load_dwordx4 v97, s[12:15], s44 offen offset:1024 lds
s_add_u32 s44, s43, 12416
buffer_load_dwordx4 v96, s[12:15], s44 offen offset:2048 lds
buffer_load_dwordx4 v97, s[12:15], s44 offen offset:3072 lds
s_add_u32 m0, s36, 65536
s_add_u32 s44, s43, 256
buffer_load_dwordx4 v96, s[12:15], s44 offen offset:0 lds
buffer_load_dwordx4 v97, s[12:15], s44 offen offset:1024 lds
s_add_u32 s44, s43, 12544
buffer_load_dwordx4 v96, s[12:15], s44 offen offset:2048 lds
buffer_load_dwordx4 v97, s[12:15], s44 offen offset:3072 lds
s_add_u32 m0, s36, 69632
s_add_u32 s44, s43, 384
buffer_load_dwordx4 v96, s[12:15], s44 offen offset:0 lds
buffer_load_dwordx4 v97, s[12:15], s44 offen offset:1024 lds
s_add_u32 s44, s43, 12672
buffer_load_dwordx4 v96, s[12:15], s44 offen offset:2048 lds
buffer_load_dwordx4 v97, s[12:15], s44 offen offset:3072 lds
s_add_u32 m0, s41, 98304
s_add_u32 s92, s42, 0x1c0000
buffer_load_dwordx4 v240, s[16:19], s92 offen offset:0 lds
buffer_load_dwordx4 v241, s[16:19], s92 offen offset:1024 lds
buffer_load_dwordx4 v242, s[16:19], s92 offen offset:2048 lds
buffer_load_dwordx4 v243, s[16:19], s92 offen offset:3072 lds
s_add_u32 m0, s41, 102400
s_add_u32 s92, s42, 0x200000
buffer_load_dwordx4 v240, s[16:19], s92 offen offset:0 lds
buffer_load_dwordx4 v241, s[16:19], s92 offen offset:1024 lds
buffer_load_dwordx4 v242, s[16:19], s92 offen offset:2048 lds
s_add_u32 m0, s36, 131072
s_add_u32 s44, s43, 512
buffer_load_dwordx4 v96, s[12:15], s44 offen offset:0 lds
buffer_load_dwordx4 v97, s[12:15], s44 offen offset:1024 lds
s_add_u32 s44, s43, 12800
)" R"(
buffer_load_dwordx4 v96, s[12:15], s44 offen offset:2048 lds
buffer_load_dwordx4 v97, s[12:15], s44 offen offset:3072 lds
s_add_u32 m0, s36, 135168
s_add_u32 s44, s43, 640
buffer_load_dwordx4 v96, s[12:15], s44 offen offset:0 lds
buffer_load_dwordx4 v97, s[12:15], s44 offen offset:1024 lds
s_add_u32 s44, s43, 12928
buffer_load_dwordx4 v96, s[12:15], s44 offen offset:2048 lds
buffer_load_dwordx4 v97, s[12:15], s44 offen offset:3072 lds
s_sub_u32 s90, s2, 128
s_lshl2_add_u32 s90, s90, s33
s_lshl_b32 s28, s90, 13
v_lshlrev_b32 v26, 7, v0
s_lshl_b32 s90, s90, 12
v_lshrrev_b32 v28, 5, v0
v_lshl_or_b32 v26, v28, 30, v26
s_sub_u32 s94, s2, 128
s_lshl2_add_u32 s94, s94, s33
s_lshl_b32 s94, s94, 2
s_mul_i32 s29, s94, 896
v_mov_b32 v88, 0x40000000
v_cmp_gt_u32_e32 vcc, 56, v0
s_nop 1
v_cndmask_b32_e32 v88, v88, v1, vcc
buffer_load_dwordx4 v[64:67], v88, s[12:15], s29 offen offset:0
buffer_load_dwordx4 v[68:71], v88, s[12:15], s29 offen offset:896
buffer_load_dwordx4 v[72:75], v88, s[12:15], s29 offen offset:1792
buffer_load_dwordx4 v[76:79], v88, s[12:15], s29 offen offset:2688
s_mov_b32 s24, s76
s_mov_b32 s25, s77
s_mov_b32 s26, 0x380
s_mov_b32 s27, 0x20000
buffer_load_dwordx4 v[80:83], v88, s[24:27], 0 offen
s_mov_b32 s24, s78
s_mov_b32 s25, s79
s_mov_b32 s26, 0x380
s_mov_b32 s27, 0x20000
buffer_load_dwordx4 v[84:87], v88, s[24:27], 0 offen
s_getpc_b64 s[24:25]
.Lzpc10:
s_sub_u32 s24, s24, .Lzpc10-.LzSTART
s_subb_u32 s25, s25, 0
s_mov_b32 s26, .LzEND-.LzSTART+8192
s_mov_b32 s27, 0x20000
s_lshr_b32 s28, s2, 3
s_lshl2_add_u32 s28, s28, s33
s_lshl_b32 s28, s28, 11
v_lshrrev_b32 v5, 4, v0
v_min_u32 v5, 1, v5
v_lshlrev_b32 v5, 27, v5
v_add_lshl_u32 v5, v5, v1, 3
buffer_load_dword v5, v5, s[24:27], s28 offen
s_mov_b32 s28, 0xc108400
s_load_dword s3, s[74:75], s28
s_mov_b32 s20, s74
s_mov_b32 s21, s75
s_mov_b32 s22, 0xc1b2000
s_mov_b32 s23, 0x20000
s_mov_b32 s4, s66
s_mov_b32 s5, s67
s_mov_b32 s6, 0x400000
s_mov_b32 s7, 0x20000
s_cmp_eq_u32 s91, 1
s_cselect_b32 s8, s72, s70
)" R"(
s_cselect_b32 s9, s73, s71
s_mov_b32 s10, 0x800000
s_mov_b32 s11, 0x20000
s_load_dwordx2 s[74:75], s[0:1], 96
s_load_dwordx4 s[76:79], s[0:1], 72
s_load_dwordx2 s[70:71], s[0:1], 88
v_bfe_u32 v244, v0, 3, 2
v_lshlrev_b32 v244, 5, v244
v_and_b32 v28, 3, v0
v_lshl_or_b32 v244, v28, 3, v244
v_bfe_u32 v28, v0, 2, 2
v_lshl_or_b32 v244, v28, 7, v244
v_lshrrev_b32 v28, 4, v0
v_lshl_or_b32 v244, v28, 10, v244
v_xor_b32 v245, 32, v244
v_xor_b32 v246, 64, v244
v_xor_b32 v247, 96, v244
v_add_u32 v252, 0x20000, v244
v_add_u32 v253, 0x20000, v245
v_add_u32 v254, 0x20000, v246
v_add_u32 v255, 0x20000, v247
v_add_u32 v248, 0x10000, v244
v_add_u32 v249, 0x10000, v245
v_add_u32 v250, 0x10000, v246
v_add_u32 v251, 0x10000, v247
s_mov_b32 s63, 0xe0c0a08
s_mov_b32 s28, 0x100d0b09
s_bitcmp1_b32 s2, 5
s_cmov_b32 s63, 0xf1e1c1a
s_cmov_b32 s28, 0x111f1d1b
s_bitcmp1_b32 s2, 6
s_cmov_b32 s63, s28
s_lshl_b32 s28, s33, 3
s_lshr_b32 s28, s63, s28
s_and_b32 s28, s28, 255
s_lshl_b32 s38, s28, 17
s_add_u32 s38, s38, 0xff80
s_lshl_b32 s39, s33, 16
s_lshl_b32 s28, s34, 9
s_add_u32 s39, s39, s28
s_sub_u32 s39, s39, 0x40000
s_lshl_b32 s37, s33, 12
s_add_u32 s37, s37, 0x10000
v_lshrrev_b32 v28, 4, v0
v_and_b32 v29, 7, v0
v_xor_b32 v28, v28, v29
v_lshlrev_b32 v28, 4, v28
v_lshrrev_b32 v29, 3, v0
v_lshl_or_b32 v8, v29, 11, v28
v_xor_b32 v9, 64, v8
v_add_u32 v9, 15360, v9
v_add_u32 v10, 30720, v8
v_add_u32 v11, 30720, v9
s_and_b32 s28, s33, 1
s_lshl_b32 s28, s28, 7
v_lshrrev_b32 v29, 5, v0
v_mul_u32_u24 v29, 0x2220, v29
v_xor_b32 v29, v29, v1
v_xor_b32 v12, s28, v29
v_xor_b32 v13, 64, v12
v_add_u32 v13, 15360, v13
v_add_u32 v14, 30720, v12
v_add_u32 v15, 30720, v13
v_bfe_u32 v28, v0, 1, 3
)" R"(
v_lshrrev_b32 v29, 4, v0
v_xor_b32 v28, v28, v29
v_lshlrev_b32 v28, 4, v28
v_and_b32 v29, 15, v0
v_lshl_or_b32 v28, v29, 7, v28
s_lshr_b32 s28, s33, 1
s_lshl_b32 s28, s28, 12
v_or_b32 v16, s28, v28
v_xor_b32 v17, 64, v16
v_bfe_u32 v28, v0, 2, 3
v_lshlrev_b32 v28, 5, v28
v_and_b32 v29, 3, v0
v_lshl_or_b32 v28, v29, 3, v28
v_bfe_u32 v29, v0, 2, 2
v_lshl_or_b32 v28, v29, 9, v28
v_lshrrev_b32 v29, 4, v0
v_lshl_or_b32 v28, v29, 12, v28
s_and_b32 s28, s33, 1
s_lshl_b32 s28, s28, 8
s_bitset1_b32 s28, 16
v_or_b32 v18, s28, v28
v_xor_b32 v19, 32, v18
v_xor_b32 v20, 64, v18
v_xor_b32 v21, 96, v18
v_xor_b32 v22, 128, v18
v_xor_b32 v23, 160, v18
v_xor_b32 v24, 192, v18
v_xor_b32 v25, 224, v18
s_lshl_b32 s28, s34, 21
s_lshl_b32 s29, s91, 25
s_add_u32 s28, s28, s29
s_bfe_u32 s29, s63, 0x80000
s_lshl_b32 s29, s29, 16
s_add_u32 s45, s28, s29
s_bfe_u32 s29, s63, 0x80008
s_lshl_b32 s29, s29, 16
s_add_u32 s46, s28, s29
s_bfe_u32 s29, s63, 0x80010
s_lshl_b32 s29, s29, 16
s_add_u32 s47, s28, s29
s_bfe_u32 s29, s63, 0x80018
s_lshl_b32 s29, s29, 16
s_add_u32 s48, s28, s29
s_lshl_b32 s50, s34, 11
s_add_u32 s50, s50, 0xc110000
s_lshl2_add_u32 s28, s91, s93
s_lshl_b32 s28, s28, 7
s_add_u32 s49, s28, s50
s_add_u32 s49, s49, 1024
s_lshl3_add_u32 s28, s34, s35
s_lshl_b32 s52, s28, 7
s_add_u32 s52, s52, 0xc118000
s_lshl4_add_u32 s28, s34, s35
s_lshl_b32 s51, s28, 17
s_add_u32 s51, s51, 0x4000000
s_cmp_lg_u32 s33, 0
s_cmov_b32 s49, 0x40000000
s_cmov_b32 s52, 0x40000000
s_mov_b32 s53, 0x40000
s_mov_b32 s54, 0x3db8aa3b
s_lshl_b32 s89, s34, 21
s_mov_b64 s[60:61], 0xff
s_mov_b32 s62, 0x4e800000
s_add_u32 s67, s89, 0x6000000
)" R"(
s_lshl3_add_u32 s28, s34, s35
s_add_u32 s64, s51, 0x100000
s_lshl2_add_u32 s28, s28, s33
s_lshl_b32 s65, s28, 7
s_add_u32 s65, s65, 0xc120000
s_lshl_b32 s66, s28, 10
s_add_u32 s66, s66, 0xc000000
s_sub_u32 s28, 15, s35
s_lshl2_add_u32 s28, s28, s33
s_lshl_b32 s29, s34, 6
s_add_u32 s29, s29, s28
s_lshl_b32 s73, s29, 16
s_add_u32 s73, s73, 0x8000000
s_lshl4_add_u32 s28, s28, s34
s_lshl_b32 s68, s28, 7
s_add_u32 s68, s68, 0xc130000
s_mov_b32 s55, s89
s_mov_b32 s57, 18
s_mov_b32 s59, s67
s_mov_b32 s58, 0
s_cmp_eq_u32 s35, 7
s_cmov_b32 s58, 2
s_cmp_eq_u32 s35, 7
s_cmov_b32 s64, 0x40000000
s_cmp_eq_u32 s35, 7
s_cmov_b32 s66, 0x40000000
s_add_u32 s56, s55, 0x2000000
s_waitcnt vmcnt(30) lgkmcnt(0)
s_barrier
ds_read_b128 v[128:131], v98 offset:0
ds_read_b128 v[132:135], v98 offset:2048
ds_read_b128 v[136:139], v99 offset:0
ds_read_b128 v[140:143], v99 offset:2048
ds_read_b128 v[144:147], v98 offset:4096
ds_read_b128 v[148:151], v98 offset:6144
ds_read_b128 v[152:155], v99 offset:4096
ds_read_b128 v[156:159], v99 offset:6144
Rt 128,244,32768
Rt 130,244,33280
Rt 132,245,32768
Rt 134,245,33280
Rt 136,246,32768
Rt 138,246,33280
Rt 140,247,32768
Rt 142,247,33280
Rt 144,244,36864
Rt 146,244,37376
Rt 148,245,36864
Rt 150,245,37376
Rt 152,246,36864
Rt 154,246,37376
Rt 156,247,36864
Rt 158,247,37376
s_mul_i32 s3, s3, 1664525
s_add_u32 s3, s3, 1013904223
v_mov_b32 v6, s3
v_min_u32 v7, 1, v0
v_lshlrev_b32 v7, 30, v7
s_waitcnt lgkmcnt(8)
s_barrier
v_mfma_f32_16x16x32_bf16 v[32:35], a[128:131], v[128:131], 0
Rt 160,244,40960
s_add_u32 m0, s41, 0
s_add_u32 s92, s42, 0x80
v_mfma_f32_16x16x32_bf16 v[40:43], a[128:131], v[132:135], 0
Rt 162,244,41472
)" R"(
buffer_load_dwordx4 v240, s[16:19], s92 offen offset:0 lds
v_mfma_f32_16x16x32_bf16 v[44:47], a[132:135], v[132:135], 0
Rt 164,245,40960
v_mfma_f32_16x16x32_bf16 v[36:39], a[132:135], v[128:131], 0
Rt 166,245,41472
v_mfma_f32_16x16x32_bf16 v[48:51], a[136:139], v[128:131], 0
Rt 168,246,40960
v_mfma_f32_16x16x32_bf16 v[56:59], a[136:139], v[132:135], 0
Rt 170,246,41472
buffer_load_dwordx4 v241, s[16:19], s92 offen offset:1024 lds
v_mfma_f32_16x16x32_bf16 v[60:63], a[140:143], v[132:135], 0
Rt 172,247,40960
v_mfma_f32_16x16x32_bf16 v[52:55], a[140:143], v[128:131], 0
Rt 174,247,41472
s_waitcnt vmcnt(28) lgkmcnt(8)
Mp 32,144,136
Rt 128,244,45056
ds_read_b128 v[160:163], v100 offset:0
Mp 40,144,140
Rt 130,244,45568
buffer_load_dwordx4 v242, s[16:19], s92 offen offset:2048 lds
Mp 44,148,140
Rt 132,245,45056
ds_read_b128 v[164:167], v100 offset:2048
Mp 36,148,136
Rt 134,245,45568
Mp 48,152,136
Rt 136,246,45056
ds_read_b128 v[168:171], v101 offset:0
Mp 56,152,140
Rt 138,246,45568
buffer_load_dwordx4 v243, s[16:19], s92 offen offset:3072 lds
Mp 60,156,140
Rt 140,247,45056
ds_read_b128 v[172:175], v101 offset:2048
Mp 52,156,136
Rt 142,247,45568
s_waitcnt vmcnt(26) lgkmcnt(12)
Mp 32,160,144
Rt 144,244,49152
ds_read_b128 v[176:179], v100 offset:4096
s_add_u32 m0, s41, 4096
s_add_u32 s92, s42, 0x40080
Mp 40,160,148
Rt 146,244,49664
buffer_load_dwordx4 v240, s[16:19], s92 offen offset:0 lds
Mp 44,164,148
Rt 148,245,49152
ds_read_b128 v[180:183], v100 offset:6144
Mp 36,164,144
Rt 150,245,49664
Mp 48,168,144
Rt 152,246,49152
ds_read_b128 v[184:187], v101 offset:4096
Mp 56,168,148
Rt 154,246,49664
buffer_load_dwordx4 v241, s[16:19], s92 offen offset:1024 lds
Mp 60,172,148
Rt 156,247,49152
ds_read_b128 v[188:191], v101 offset:6144
Mp 52,172,144
Rt 158,247,49664
s_waitcnt lgkmcnt(12)
Mp 32,128,152
)" R"(
Rt 160,244,53248
Mp 40,128,156
Rt 162,244,53760
buffer_load_dwordx4 v242, s[16:19], s92 offen offset:2048 lds
Mp 44,132,156
Rt 164,245,53248
Mp 36,132,152
Rt 166,245,53760
s_add_u32 s44, s43, 768
Mp 48,136,152
Rt 168,246,53248
s_add_u32 m0, s36, 65536
Mp 56,136,156
Rt 170,246,53760
buffer_load_dwordx4 v96, s[12:15], s44 offen offset:0 lds
Mp 60,140,156
Rt 172,247,53248
Mp 52,140,152
Rt 174,247,53760
s_waitcnt lgkmcnt(8)
Mp 32,144,160
Rt 128,244,57344
Mp 40,144,164
Rt 130,244,57856
buffer_load_dwordx4 v97, s[12:15], s44 offen offset:1024 lds
Mp 44,148,164
Rt 132,245,57344
Mp 36,148,160
Rt 134,245,57856
Mp 48,152,160
Rt 136,246,57344
Mp 56,152,164
Rt 138,246,57856
s_add_u32 s44, s43, 13056
buffer_load_dwordx4 v96, s[12:15], s44 offen offset:2048 lds
Mp 60,156,164
Rt 140,247,57344
Mp 52,156,160
Rt 142,247,57856
s_waitcnt vmcnt(21) lgkmcnt(8)
s_barrier
Mp 32,160,168
Rt 144,248,32768
ds_read_b128 v[192:195], v102 offset:0
Mp 40,160,172
Rt 146,248,33280
buffer_load_dwordx4 v97, s[12:15], s44 offen offset:3072 lds
Mp 44,164,172
Rt 148,249,32768
ds_read_b128 v[196:199], v102 offset:2048
Mp 36,164,168
Rt 150,249,33280
Mp 48,168,168
Rt 152,250,32768
ds_read_b128 v[200:203], v103 offset:0
Mp 56,168,172
Rt 154,250,33280
Mp 60,172,172
Rt 156,251,32768
ds_read_b128 v[204:207], v103 offset:2048
Mp 52,172,168
Rt 158,251,33280
s_waitcnt vmcnt(18) lgkmcnt(12)
s_barrier
Mp 32,128,176
Rt 160,248,36864
)" R"(
ds_read_b128 v[208:211], v102 offset:4096
Mp 40,128,180
Rt 162,248,37376
Mp 44,132,180
Rt 164,249,36864
ds_read_b128 v[212:215], v102 offset:6144
Mp 36,132,176
Rt 166,249,37376
Mp 48,136,176
Rt 168,250,36864
ds_read_b128 v[216:219], v103 offset:4096
Mp 56,136,180
Rt 170,250,37376
Mp 60,140,180
Rt 172,251,36864
ds_read_b128 v[220:223], v103 offset:6144
Mp 52,140,176
Rt 174,251,37376
s_waitcnt vmcnt(12) lgkmcnt(12)
Mp 32,144,184
Rt 128,248,40960
v_dot2_f32_bf16 v88, v64, v80, 0
v_dot2_f32_bf16 v89, v68, v80, 0
Mp 40,144,188
Rt 130,248,41472
v_dot2_f32_bf16 v90, v72, v80, 0
v_dot2_f32_bf16 v91, v76, v80, 0
Mp 44,148,188
Rt 132,249,40960
v_dot2_f32_bf16 v92, v64, v84, 0
v_dot2_f32_bf16 v93, v68, v84, 0
Mp 36,148,184
Rt 134,249,41472
v_dot2_f32_bf16 v94, v72, v84, 0
v_dot2_f32_bf16 v95, v76, v84, 0
v_dot2_f32_bf16 v88, v65, v81, v88
Mp 48,152,184
Rt 136,250,40960
v_dot2_f32_bf16 v89, v69, v81, v89
v_dot2_f32_bf16 v90, v73, v81, v90
Mp 56,152,188
Rt 138,250,41472
v_dot2_f32_bf16 v91, v77, v81, v91
v_dot2_f32_bf16 v92, v65, v85, v92
Mp 60,156,188
Rt 140,251,40960
s_add_u32 s92, s42, 0x1c0080
v_dot2_f32_bf16 v93, v69, v85, v93
v_dot2_f32_bf16 v94, v73, v85, v94
Mp 52,156,184
Rt 142,251,41472
s_waitcnt lgkmcnt(8)
s_barrier
Mp 32,160,192
Rt 144,248,45056
s_add_u32 m0, s41, 131072
v_dot2_f32_bf16 v95, v77, v85, v95
Mp 40,160,196
Rt 146,248,45568
buffer_load_dwordx4 v240, s[16:19], s92 offen offset:0 lds
v_dot2_f32_bf16 v88, v66, v82, v88
Mp 44,164,196
Rt 148,249,45056
v_dot2_f32_bf16 v89, v70, v82, v89
v_dot2_f32_bf16 v90, v74, v82, v90
)" R"(
Mp 36,164,192
Rt 150,249,45568
v_dot2_f32_bf16 v91, v78, v82, v91
v_dot2_f32_bf16 v92, v66, v86, v92
Mp 48,168,192
Rt 152,250,45056
v_dot2_f32_bf16 v93, v70, v86, v93
v_dot2_f32_bf16 v94, v74, v86, v94
v_dot2_f32_bf16 v95, v78, v86, v95
Mp 56,168,196
Rt 154,250,45568
buffer_load_dwordx4 v241, s[16:19], s92 offen offset:1024 lds
Mp 60,172,196
Rt 156,251,45056
v_dot2_f32_bf16 v88, v67, v83, v88
v_dot2_f32_bf16 v89, v71, v83, v89
Mp 52,172,192
Rt 158,251,45568
v_dot2_f32_bf16 v90, v75, v83, v90
v_dot2_f32_bf16 v91, v79, v83, v91
s_waitcnt lgkmcnt(8)
Mp 32,128,200
Rt 160,248,49152
v_dot2_f32_bf16 v92, v67, v87, v92
v_dot2_f32_bf16 v93, v71, v87, v93
Mp 40,128,204
Rt 162,248,49664
buffer_load_dwordx4 v242, s[16:19], s92 offen offset:2048 lds
Mp 44,132,204
Rt 164,249,49152
v_dot2_f32_bf16 v94, v75, v87, v94
v_dot2_f32_bf16 v95, v79, v87, v95
Mp 36,132,200
Rt 166,249,49664
v_permlane32_swap_b32_e32 v88, v89
v_add_f32_e32 v88, v88, v89
v_permlane32_swap_b32_e32 v90, v91
Mp 48,136,200
Rt 168,250,49152
v_add_f32_e32 v90, v90, v91
v_permlane32_swap_b32_e32 v92, v93
Mp 56,136,204
Rt 170,250,49664
buffer_load_dwordx4 v243, s[16:19], s92 offen offset:3072 lds
Mp 60,140,204
Rt 172,251,49152
s_add_u32 m0, s41, 135168
v_add_f32_e32 v92, v92, v93
v_permlane32_swap_b32_e32 v94, v95
Mp 52,140,200
Rt 174,251,49664
v_add_f32_e32 v94, v94, v95
s_waitcnt vmcnt(4) lgkmcnt(8)
Mp 32,144,208
Rt 128,248,53248
ds_read_b128 v[224:227], v100 offset:0
s_add_u32 s92, s42, 0x200080
Mp 40,144,212
Rt 130,248,53760
buffer_load_dwordx4 v240, s[16:19], s92 offen offset:0 lds
v_permlane16_swap_b32_e32 v88, v90
Mp 44,148,212
Rt 132,249,53248
ds_read_b128 v[228:231], v100 offset:2048
)" R"(
v_add_f32_e32 v88, v88, v90
Mp 36,148,208
Rt 134,249,53760
v_permlane16_swap_b32_e32 v92, v94
v_add_f32_e32 v92, v92, v94
Mp 48,152,208
Rt 136,250,53248
ds_read_b128 v[232:235], v101 offset:0
v_add_f32_dpp v88, v88, v88 row_ror:8 row_mask:0xf bank_mask:0xf
v_add_f32_dpp v92, v92, v92 row_ror:8 row_mask:0xf bank_mask:0xf
Mp 56,152,212
Rt 138,250,53760
buffer_load_dwordx4 v241, s[16:19], s92 offen offset:1024 lds
Mp 60,156,212
Rt 140,251,53248
ds_read_b128 v[236:239], v101 offset:2048
v_add_f32_dpp v88, v88, v88 row_ror:4 row_mask:0xf bank_mask:0xf
Mp 52,156,208
Rt 142,251,53760
v_add_f32_dpp v92, v92, v92 row_ror:4 row_mask:0xf bank_mask:0xf
v_add_f32_dpp v88, v88, v88 row_ror:2 row_mask:0xf bank_mask:0xf
s_waitcnt lgkmcnt(12)
Mp 32,160,216
Rt 144,248,57344
v_add_f32_dpp v92, v92, v92 row_ror:2 row_mask:0xf bank_mask:0xf
v_add_f32_dpp v88, v88, v88 row_ror:1 row_mask:0xf bank_mask:0xf
Mp 40,160,220
Rt 146,248,57856
buffer_load_dwordx4 v242, s[16:19], s92 offen offset:2048 lds
Mp 44,164,220
Rt 148,249,57344
v_add_f32_dpp v92, v92, v92 row_ror:1 row_mask:0xf bank_mask:0xf
Cv 88,88,88
Mp 36,164,216
Rt 150,249,57856
v_lshlrev_b32 v88, 16, v88
Cv 92,92,92
v_lshlrev_b32 v92, 16, v92
Mp 48,168,216
Rt 152,250,57344
v_mul_f32_e32 v88, 0xbfb8aa3b, v88
v_lshrrev_b32 v89, 5, v0
Mp 56,168,220
Rt 154,250,57856
v_exp_f32_e32 v88, v88
Mp 60,172,220
Rt 156,251,57344
s_add_u32 s92, s42, 0x100
v_add_f32_e32 v88, 1.0, v88
v_bfe_u32 v90, v0, 4, 1
Mp 52,172,216
Rt 158,251,57856
s_waitcnt lgkmcnt(8)
s_barrier
Mp 32,128,224
Rt 160,244,0
s_add_u32 m0, s41, 65536
v_lshl_or_b32 v89, v90, 1, v89
Mp 40,128,228
Rt 162,244,512
buffer_load_dwordx4 v240, s[16:19], s92 offen offset:0 lds
v_add_u32 v89, s94, v89
Mp 44,132,228
Rt 164,245,0
v_rcp_f32_e32 v88, v88
)" R"(
Mp 36,132,224
Rt 166,245,512
Cv 88,88,88
v_lshlrev_b32 v88, 16, v88
Mp 48,136,224
Rt 168,246,0
v_add_f32_e32 v93, 0, v92
v_ashrrev_i32 v94, 31, v93
v_or_b32 v94, 0x80000000, v94
Mp 56,136,228
Rt 170,246,512
buffer_load_dwordx4 v241, s[16:19], s92 offen offset:1024 lds
Mp 60,140,228
Rt 172,247,0
v_xor_b32 v93, v93, v94
v_and_b32 v93, 0xffff0000, v93
Mp 52,140,224
Rt 174,247,512
v_sub_u32 v94, 0xffff, v89
s_waitcnt lgkmcnt(8)
s_barrier
Mp 32,144,232
Rt 128,244,4096
v_or_b32 v84, v93, v94
v_mov_b32 v85, s3
Mp 40,144,236
Rt 130,244,4608
buffer_load_dwordx4 v242, s[16:19], s92 offen offset:2048 lds
Mp 44,148,236
Rt 132,245,4096
v_mov_b32 v86, v88
v_mov_b32 v87, v92
Mp 36,148,232
Rt 134,245,4608
v_and_b32 v90, 15, v0
v_min_u32 v90, 1, v90
v_lshlrev_b32 v90, 30, v90
Mp 48,152,232
Rt 136,246,4096
v_lshl_or_b32 v90, v89, 4, v90
s_mov_b32 s94, 0xc100000
Mp 56,152,236
Rt 138,246,4608
buffer_load_dwordx4 v243, s[16:19], s92 offen offset:3072 lds
Mp 60,156,236
Rt 140,247,4096
s_add_u32 s92, s42, 0x40100
buffer_store_dwordx4 v[84:87], v90, s[20:23], s94 offen sc0 sc1
Mp 52,156,232
Rt 142,247,4608
s_waitcnt lgkmcnt(8)
v_mfma_f32_16x16x32_bf16 v[64:67], a[160:163], v[128:131], 0
Rt 144,244,8192
s_add_u32 m0, s41, 69632
v_mfma_f32_16x16x32_bf16 v[72:75], a[160:163], v[132:135], 0
Rt 146,244,8704
buffer_load_dwordx4 v240, s[16:19], s92 offen offset:0 lds
v_mfma_f32_16x16x32_bf16 v[76:79], a[164:167], v[132:135], 0
Rt 148,245,8192
v_mfma_f32_16x16x32_bf16 v[68:71], a[164:167], v[128:131], 0
Rt 150,245,8704
v_mfma_f32_16x16x32_bf16 v[80:83], a[168:171], v[128:131], 0
Rt 152,246,8192
v_mul_f32_e32 v32, s54, v32
v_mfma_f32_16x16x32_bf16 v[88:91], a[168:171], v[132:135], 0
)" R"(
Rt 154,246,8704
buffer_load_dwordx4 v241, s[16:19], s92 offen offset:1024 lds
v_mfma_f32_16x16x32_bf16 v[92:95], a[172:175], v[132:135], 0
Rt 156,247,8192
v_mul_f32_e32 v33, s54, v33
v_mfma_f32_16x16x32_bf16 v[84:87], a[172:175], v[128:131], 0
Rt 158,247,8704
s_waitcnt lgkmcnt(8)
Mp 64,128,136
Rt 160,244,12288
Cv 32,32,33
Mp 72,128,140
Rt 162,244,12800
buffer_load_dwordx4 v242, s[16:19], s92 offen offset:2048 lds
Mp 76,132,140
Rt 164,245,12288
v_accvgpr_write_b32 a64, v32
Mp 68,132,136
Rt 166,245,12800
s_add_u32 s92, s42, 0x1c0100
v_mul_f32_e32 v34, s54, v34
Mp 80,136,136
Rt 168,246,12288
s_add_u32 m0, s41, 32768
Mp 88,136,140
Rt 170,246,12800
buffer_load_dwordx4 v240, s[16:19], s92 offen offset:0 lds
Mp 92,140,140
Rt 172,247,12288
v_mul_f32_e32 v35, s54, v35
Mp 84,140,136
Rt 174,247,12800
s_waitcnt lgkmcnt(8)
Mp 64,144,144
Rt 128,244,16384
Cv 34,34,35
Mp 72,144,148
Rt 130,244,16896
buffer_load_dwordx4 v241, s[16:19], s92 offen offset:1024 lds
Mp 76,148,148
Rt 132,245,16384
v_accvgpr_write_b32 a65, v34
Mp 68,148,144
Rt 134,245,16896
v_mul_f32_e32 v36, s54, v36
v_mul_f32_e32 v37, s54, v37
Mp 80,152,144
Rt 136,246,16384
Cv 36,36,37
Mp 88,152,148
Rt 138,246,16896
buffer_load_dwordx4 v242, s[16:19], s92 offen offset:2048 lds
Mp 92,156,148
Rt 140,247,16384
v_accvgpr_write_b32 a66, v36
Mp 84,156,144
Rt 142,247,16896
s_waitcnt lgkmcnt(8)
Mp 64,160,152
Rt 144,244,20480
v_mul_f32_e32 v38, s54, v38
Mp 72,160,156
Rt 146,244,20992
buffer_load_dwordx4 v243, s[16:19], s92 offen offset:3072 lds
)" R"(
Mp 76,164,156
Rt 148,245,20480
v_mul_f32_e32 v39, s54, v39
Mp 68,164,152
Rt 150,245,20992
s_add_u32 s92, s42, 0x200100
Cv 38,38,39
Mp 80,168,152
Rt 152,246,20480
s_add_u32 m0, s41, 36864
Mp 88,168,156
Rt 154,246,20992
buffer_load_dwordx4 v240, s[16:19], s92 offen offset:0 lds
Mp 92,172,156
Rt 156,247,20480
v_accvgpr_write_b32 a67, v38
Mp 84,172,152
Rt 158,247,20992
s_waitcnt lgkmcnt(8)
Mp 64,128,160
Rt 160,244,24576
v_mul_f32_e32 v40, s54, v40
Mp 72,128,164
Rt 162,244,25088
buffer_load_dwordx4 v241, s[16:19], s92 offen offset:1024 lds
Mp 76,132,164
Rt 164,245,24576
v_mul_f32_e32 v41, s54, v41
Mp 68,132,160
Rt 166,245,25088
Cv 40,40,41
v_accvgpr_write_b32 a68, v40
Mp 80,136,160
Rt 168,246,24576
v_mul_f32_e32 v42, s54, v42
Mp 88,136,164
Rt 170,246,25088
buffer_load_dwordx4 v242, s[16:19], s92 offen offset:2048 lds
Mp 92,140,164
Rt 172,247,24576
s_add_u32 s92, s42, 0x180
Mp 84,140,160
Rt 174,247,25088
s_waitcnt vmcnt(15) lgkmcnt(8)
s_barrier
Mp 64,144,168
Rt 128,252,0
s_add_u32 m0, s41, 98304
Mp 72,144,172
Rt 130,252,512
buffer_load_dwordx4 v240, s[16:19], s92 offen offset:0 lds
Mp 76,148,172
Rt 132,253,0
v_mul_f32_e32 v43, s54, v43
Mp 68,148,168
Rt 134,253,512
Cv 42,42,43
v_accvgpr_write_b32 a69, v42
Mp 80,152,168
Rt 136,254,0
v_mul_f32_e32 v44, s54, v44
Mp 88,152,172
Rt 138,254,512
buffer_load_dwordx4 v241, s[16:19], s92 offen offset:1024 lds
Mp 92,156,172
)" R"(
Rt 140,255,0
v_mul_f32_e32 v45, s54, v45
Mp 84,156,168
Rt 142,255,512
s_waitcnt lgkmcnt(8)
s_barrier
Mp 64,160,176
Rt 144,252,4096
Cv 44,44,45
Mp 72,160,180
Rt 146,252,4608
buffer_load_dwordx4 v242, s[16:19], s92 offen offset:2048 lds
Mp 76,164,180
Rt 148,253,4096
v_accvgpr_write_b32 a70, v44
Mp 68,164,176
Rt 150,253,4608
v_mul_f32_e32 v46, s54, v46
v_mul_f32_e32 v47, s54, v47
Mp 80,168,176
Rt 152,254,4096
Cv 46,46,47
Mp 88,168,180
Rt 154,254,4608
buffer_load_dwordx4 v243, s[16:19], s92 offen offset:3072 lds
Mp 92,172,180
Rt 156,255,4096
s_add_u32 s92, s42, 0x40180
Mp 84,172,176
Rt 158,255,4608
s_waitcnt lgkmcnt(8)
Mp 64,128,184
Rt 160,252,8192
s_add_u32 m0, s41, 102400
Mp 72,128,188
Rt 162,252,8704
buffer_load_dwordx4 v240, s[16:19], s92 offen offset:0 lds
Mp 76,132,188
Rt 164,253,8192
v_accvgpr_write_b32 a71, v46
Mp 68,132,184
Rt 166,253,8704
v_mul_f32_e32 v48, s54, v48
v_mul_f32_e32 v49, s54, v49
Mp 80,136,184
Rt 168,254,8192
Cv 48,48,49
Mp 88,136,188
Rt 170,254,8704
buffer_load_dwordx4 v241, s[16:19], s92 offen offset:1024 lds
Mp 92,140,188
Rt 172,255,8192
v_accvgpr_write_b32 a72, v48
Mp 84,140,184
Rt 174,255,8704
s_waitcnt lgkmcnt(8)
Mp 64,144,192
Rt 128,252,12288
v_mul_f32_e32 v50, s54, v50
Mp 72,144,196
Rt 130,252,12800
buffer_load_dwordx4 v242, s[16:19], s92 offen offset:2048 lds
Mp 76,148,196
Rt 132,253,12288
v_mul_f32_e32 v51, s54, v51
)" R"(
Mp 68,148,192
Rt 134,253,12800
Cv 50,50,51
v_accvgpr_write_b32 a73, v50
Mp 80,152,192
Rt 136,254,12288
v_mul_f32_e32 v52, s54, v52
Mp 88,152,196
Rt 138,254,12800
v_mul_f32_e32 v53, s54, v53
Mp 92,156,196
Rt 140,255,12288
s_add_u32 s38, s38, 0xffff0080
Mp 84,156,192
Rt 142,255,12800
s_waitcnt lgkmcnt(8)
Mp 64,160,200
Rt 144,252,16384
s_add_u32 m0, s36, 0
Mp 72,160,204
Rt 146,252,16896
buffer_load_dwordx4 v8, s[4:7], s38 offen offset:0 lds
Mp 76,164,204
Rt 148,253,16384
Cv 52,52,53
Mp 68,164,200
Rt 150,253,16896
v_accvgpr_write_b32 a74, v52
v_mul_f32_e32 v54, s54, v54
Mp 80,168,200
Rt 152,254,16384
v_mul_f32_e32 v55, s54, v55
Mp 88,168,204
Rt 154,254,16896
buffer_load_dwordx4 v9, s[4:7], s38 offen offset:1024 lds
Mp 92,172,204
Rt 156,255,16384
Cv 54,54,55
Mp 84,172,200
Rt 158,255,16896
s_waitcnt lgkmcnt(8)
Mp 64,128,208
Rt 160,252,20480
v_accvgpr_write_b32 a75, v54
Mp 72,128,212
Rt 162,252,20992
buffer_load_dwordx4 v10, s[4:7], s38 offen offset:2048 lds
Mp 76,132,212
Rt 164,253,20480
v_mul_f32_e32 v56, s54, v56
Mp 68,132,208
Rt 166,253,20992
v_mul_f32_e32 v57, s54, v57
Cv 56,56,57
Mp 80,136,208
Rt 168,254,20480
v_accvgpr_write_b32 a76, v56
Mp 88,136,212
Rt 170,254,20992
buffer_load_dwordx4 v11, s[4:7], s38 offen offset:3072 lds
Mp 92,140,212
Rt 172,255,20480
s_add_u32 s38, s38, 0x10000
Mp 84,140,208
)" R"(
Rt 174,255,20992
s_waitcnt lgkmcnt(8)
Mp 64,144,216
Rt 128,252,24576
s_add_u32 m0, s36, 4096
Mp 72,144,220
Rt 130,252,25088
buffer_load_dwordx4 v8, s[4:7], s38 offen offset:0 lds
Mp 76,148,220
Rt 132,253,24576
v_mul_f32_e32 v58, s54, v58
Mp 68,148,216
Rt 134,253,25088
v_mul_f32_e32 v59, s54, v59
Cv 58,58,59
Mp 80,152,216
Rt 136,254,24576
v_accvgpr_write_b32 a77, v58
Mp 88,152,220
Rt 138,254,25088
buffer_load_dwordx4 v9, s[4:7], s38 offen offset:1024 lds
Mp 92,156,220
Rt 140,255,24576
v_mul_f32_e32 v60, s54, v60
Mp 84,156,216
Rt 142,255,25088
s_waitcnt vmcnt(20) lgkmcnt(8)
s_barrier
Mp 64,160,224
Rt 144,248,0
v_mul_f32_e32 v61, s54, v61
Mp 72,160,228
Rt 146,248,512
buffer_load_dwordx4 v10, s[4:7], s38 offen offset:2048 lds
Mp 76,164,228
Rt 148,249,0
Cv 60,60,61
Mp 68,164,224
Rt 150,249,512
v_accvgpr_write_b32 a78, v60
v_mul_f32_e32 v62, s54, v62
Mp 80,168,224
Rt 152,250,0
v_mul_f32_e32 v63, s54, v63
Mp 88,168,228
Rt 154,250,512
buffer_load_dwordx4 v11, s[4:7], s38 offen offset:3072 lds
Mp 92,172,228
Rt 156,251,0
Cv 62,62,63
Mp 84,172,224
Rt 158,251,512
s_waitcnt vmcnt(15) lgkmcnt(8)
s_barrier
Mp 64,128,232
Rt 160,248,4096
v_accvgpr_write_b32 a79, v62
Mp 72,128,236
Rt 162,248,4608
Mp 76,132,236
Rt 164,249,4096
Mp 68,132,232
Rt 166,249,4608
s_add_u32 s92, s42, 0x1c0180
Mp 80,136,232
Rt 168,250,4096
)" R"(
s_add_u32 m0, s41, 131072
Mp 88,136,236
Rt 170,250,4608
buffer_load_dwordx4 v240, s[16:19], s92 offen offset:0 lds
Mp 92,140,236
Rt 172,251,4096
Mp 84,140,232
Rt 174,251,4608
s_waitcnt lgkmcnt(8)
v_mfma_f32_16x16x32_bf16 v[32:35], a[144:147], v[128:131], 0
Rt 128,248,8192
v_mfma_f32_16x16x32_bf16 v[40:43], a[144:147], v[132:135], 0
Rt 130,248,8704
v_mfma_f32_16x16x32_bf16 v[44:47], a[148:151], v[132:135], 0
Rt 132,249,8192
v_mfma_f32_16x16x32_bf16 v[36:39], a[148:151], v[128:131], 0
Rt 134,249,8704
buffer_load_dwordx4 v241, s[16:19], s92 offen offset:1024 lds
v_mfma_f32_16x16x32_bf16 v[48:51], a[152:155], v[128:131], 0
Rt 136,250,8192
v_mul_f32_e32 v64, s54, v64
v_mfma_f32_16x16x32_bf16 v[56:59], a[152:155], v[132:135], 0
Rt 138,250,8704
v_mul_f32_e32 v65, s54, v65
v_mfma_f32_16x16x32_bf16 v[60:63], a[156:159], v[132:135], 0
Rt 140,251,8192
Cv 64,64,65
v_mfma_f32_16x16x32_bf16 v[52:55], a[156:159], v[128:131], 0
Rt 142,251,8704
s_waitcnt lgkmcnt(8)
Mp 32,160,136
Rt 144,248,12288
v_accvgpr_write_b32 a80, v64
Mp 40,160,140
Rt 146,248,12800
buffer_load_dwordx4 v242, s[16:19], s92 offen offset:2048 lds
Mp 44,164,140
Rt 148,249,12288
v_mul_f32_e32 v66, s54, v66
Mp 36,164,136
Rt 150,249,12800
v_mul_f32_e32 v67, s54, v67
Mp 48,168,136
Rt 152,250,12288
Cv 66,66,67
Mp 56,168,140
Rt 154,250,12800
v_accvgpr_write_b32 a81, v66
Mp 60,172,140
Rt 156,251,12288
v_mul_f32_e32 v68, s54, v68
Mp 52,172,136
Rt 158,251,12800
buffer_load_dwordx4 v243, s[16:19], s92 offen offset:3072 lds
s_waitcnt lgkmcnt(8)
Mp 32,128,144
Rt 160,248,16384
v_mul_f32_e32 v69, s54, v69
Mp 40,128,148
Rt 162,248,16896
Cv 68,68,69
Mp 44,132,148
Rt 164,249,16384
v_accvgpr_write_b32 a82, v68
)" R"(
Mp 36,132,144
Rt 166,249,16896
s_add_u32 s92, s42, 0x200180
Mp 48,136,144
Rt 168,250,16384
s_add_u32 m0, s41, 135168
Mp 56,136,148
Rt 170,250,16896
buffer_load_dwordx4 v240, s[16:19], s92 offen offset:0 lds
Mp 60,140,148
Rt 172,251,16384
v_mul_f32_e32 v70, s54, v70
Mp 52,140,144
Rt 174,251,16896
s_waitcnt lgkmcnt(8)
Mp 32,144,152
Rt 128,248,20480
v_mul_f32_e32 v71, s54, v71
Mp 40,144,156
Rt 130,248,20992
Cv 70,70,71
Mp 44,148,156
Rt 132,249,20480
v_accvgpr_write_b32 a83, v70
Mp 36,148,152
Rt 134,249,20992
buffer_load_dwordx4 v241, s[16:19], s92 offen offset:1024 lds
Mp 48,152,152
Rt 136,250,20480
v_mul_f32_e32 v72, s54, v72
Mp 56,152,156
Rt 138,250,20992
v_mul_f32_e32 v73, s54, v73
Mp 60,156,156
Rt 140,251,20480
Cv 72,72,73
Mp 52,156,152
Rt 142,251,20992
s_waitcnt lgkmcnt(8)
Mp 32,160,160
Rt 144,248,24576
v_accvgpr_write_b32 a84, v72
Mp 40,160,164
Rt 146,248,25088
buffer_load_dwordx4 v242, s[16:19], s92 offen offset:2048 lds
Mp 44,164,164
Rt 148,249,24576
v_mul_f32_e32 v74, s54, v74
Mp 36,164,160
Rt 150,249,25088
v_mul_f32_e32 v75, s54, v75
Mp 48,168,160
Rt 152,250,24576
Cv 74,74,75
Mp 56,168,164
Rt 154,250,25088
v_accvgpr_write_b32 a85, v74
Mp 60,172,164
Rt 156,251,24576
v_mul_f32_e32 v76, s54, v76
s_mov_b32 s85, 0xc108800
Mp 52,172,160
Rt 158,251,25088
v_and_b32 v2, 7, v0
)" R"(
s_waitcnt lgkmcnt(8)
Mp 32,128,168
Rt 160,244,32768
v_mul_f32_e32 v77, s54, v77
v_lshlrev_b32 v2, 7, v2
Mp 40,128,172
Rt 162,244,33280
Cv 76,76,77
Mp 44,132,172
Rt 164,245,32768
v_accvgpr_write_b32 a86, v76
Mp 36,132,168
Rt 166,245,33280
v_mul_f32_e32 v78, s54, v78
buffer_load_dword v4, v2, s[20:23], s85 offen sc0 sc1
Mp 48,136,168
Rt 168,246,32768
v_mul_f32_e32 v79, s54, v79
Mp 56,136,172
Rt 170,246,33280
Cv 78,78,79
Mp 60,140,172
Rt 172,247,32768
v_accvgpr_write_b32 a87, v78
Mp 52,140,168
Rt 174,247,33280
s_waitcnt vmcnt(16) lgkmcnt(8)
s_barrier
Mp 32,144,176
Rt 128,244,36864
v_mul_f32_e32 v80, s54, v80
Mp 40,144,180
Rt 130,244,37376
s_add_u32 s39, s39, 0x40000
Mp 44,148,180
Rt 132,245,36864
s_add_u32 m0, s37, 0
Mp 36,148,176
Rt 134,245,37376
buffer_load_dwordx4 v12, s[8:11], s39 offen offset:0 lds
Mp 48,152,176
Rt 136,246,36864
v_mul_f32_e32 v81, s54, v81
Mp 56,152,180
Rt 138,246,37376
Cv 80,80,81
Mp 60,156,180
Rt 140,247,36864
v_accvgpr_write_b32 a88, v80
Mp 52,156,176
Rt 142,247,37376
s_waitcnt lgkmcnt(8)
Mp 32,160,184
Rt 144,244,40960
v_mul_f32_e32 v82, s54, v82
Mp 40,160,188
Rt 146,244,41472
buffer_load_dwordx4 v13, s[8:11], s39 offen offset:1024 lds
Mp 44,164,188
Rt 148,245,40960
v_mul_f32_e32 v83, s54, v83
Mp 36,164,184
Rt 150,245,41472
Cv 82,82,83
Mp 48,168,184
)" R"(
Rt 152,246,40960
v_accvgpr_write_b32 a89, v82
Mp 56,168,188
Rt 154,246,41472
v_mul_f32_e32 v84, s54, v84
Mp 60,172,188
Rt 156,247,40960
v_mul_f32_e32 v85, s54, v85
Mp 52,172,184
Rt 158,247,41472
buffer_load_dwordx4 v14, s[8:11], s39 offen offset:2048 lds
s_waitcnt lgkmcnt(8)
Mp 32,128,192
Rt 160,244,45056
Cv 84,84,85
Mp 40,128,196
Rt 162,244,45568
v_accvgpr_write_b32 a90, v84
Mp 44,132,196
Rt 164,245,45056
v_mul_f32_e32 v86, s54, v86
Mp 36,132,192
Rt 166,245,45568
v_mul_f32_e32 v87, s54, v87
Mp 48,136,192
Rt 168,246,45056
Cv 86,86,87
Mp 56,136,196
Rt 170,246,45568
buffer_load_dwordx4 v15, s[8:11], s39 offen offset:3072 lds
Mp 60,140,196
Rt 172,247,45056
v_accvgpr_write_b32 a91, v86
Mp 52,140,192
Rt 174,247,45568
s_waitcnt lgkmcnt(8)
Mp 32,144,200
Rt 128,244,49152
v_mul_f32_e32 v88, s54, v88
Mp 40,144,204
Rt 130,244,49664
s_add_u32 s39, s39, 0x40000
Mp 44,148,204
Rt 132,245,49152
s_add_u32 m0, s37, 16384
Mp 36,148,200
Rt 134,245,49664
buffer_load_dwordx4 v12, s[8:11], s39 offen offset:0 lds
Mp 48,152,200
Rt 136,246,49152
v_mul_f32_e32 v89, s54, v89
Mp 56,152,204
Rt 138,246,49664
Cv 88,88,89
Mp 60,156,204
Rt 140,247,49152
v_accvgpr_write_b32 a92, v88
Mp 52,156,200
Rt 142,247,49664
s_waitcnt lgkmcnt(8)
Mp 32,160,208
Rt 144,244,53248
v_mul_f32_e32 v90, s54, v90
Mp 40,160,212
)" R"(
Rt 146,244,53760
buffer_load_dwordx4 v13, s[8:11], s39 offen offset:1024 lds
Mp 44,164,212
Rt 148,245,53248
v_mul_f32_e32 v91, s54, v91
Mp 36,164,208
Rt 150,245,53760
Cv 90,90,91
Mp 48,168,208
Rt 152,246,53248
v_accvgpr_write_b32 a93, v90
Mp 56,168,212
Rt 154,246,53760
v_mul_f32_e32 v92, s54, v92
Mp 60,172,212
Rt 156,247,53248
v_mul_f32_e32 v93, s54, v93
Mp 52,172,208
Rt 158,247,53760
buffer_load_dwordx4 v14, s[8:11], s39 offen offset:2048 lds
s_waitcnt lgkmcnt(8)
Mp 32,128,216
Rt 160,244,57344
Cv 92,92,93
Mp 40,128,220
Rt 162,244,57856
v_accvgpr_write_b32 a94, v92
Mp 44,132,220
Rt 164,245,57344
v_mul_f32_e32 v94, s54, v94
Mp 36,132,216
Rt 166,245,57856
v_mul_f32_e32 v95, s54, v95
Mp 48,136,216
Rt 168,246,57344
Cv 94,94,95
Mp 56,136,220
Rt 170,246,57856
buffer_load_dwordx4 v15, s[8:11], s39 offen offset:3072 lds
Mp 60,140,220
Rt 172,247,57344
v_accvgpr_write_b32 a95, v94
Mp 52,140,216
Rt 174,247,57856
s_waitcnt lgkmcnt(8)
Mp 32,144,224
Rt 128,248,32768
Mp 40,144,228
Rt 130,248,33280
Mp 44,148,228
Rt 132,249,32768
Mp 36,148,224
Rt 134,249,33280
Mp 48,152,224
Rt 136,250,32768
Mp 56,152,228
Rt 138,250,33280
Mp 60,156,228
Rt 140,251,32768
Mp 52,156,224
Rt 142,251,33280
s_waitcnt vmcnt(8) lgkmcnt(8)
s_barrier
Mp 32,160,232
Rt 144,248,36864
)" R"(
v_xor_b32 v4, s3, v4
v_cmp_gt_u32_e32 vcc, 2, v4
s_andn2_b32 s28, 0xff, vcc_lo
s_cbranch_scc1 .Lzsp11
.Lzok11:
Mp 40,160,236
Rt 146,248,37376
s_add_u32 s38, s38, 0xffff0080
v_xor_b32 v4, s3, v4
Mp 44,164,236
Rt 148,249,36864
s_add_u32 m0, s36, 32768
Mp 36,164,232
Rt 150,249,37376
buffer_load_dwordx4 v8, s[4:7], s38 offen offset:0 lds
Mp 48,168,232
Rt 152,250,36864
v_and_b32 v4, 1, v4
Mp 56,168,236
Rt 154,250,37376
Mp 60,172,236
Rt 156,251,36864
v_cmp_eq_u32_e32 vcc, 1, v4
s_mov_b32 s85, vcc_lo
Mp 52,172,232
Rt 158,251,37376
s_and_b32 s28, s2, 7
s_waitcnt lgkmcnt(8)
v_mfma_f32_16x16x32_bf16 v[64:67], a[128:131], v[128:131], 0
Rt 160,248,40960
s_lshr_b32 s28, s85, s28
v_mfma_f32_16x16x32_bf16 v[72:75], a[128:131], v[132:135], 0
Rt 162,248,41472
buffer_load_dwordx4 v9, s[4:7], s38 offen offset:1024 lds
v_mfma_f32_16x16x32_bf16 v[76:79], a[132:135], v[132:135], 0
Rt 164,249,40960
s_xor_b32 s28, s28, s34
v_mfma_f32_16x16x32_bf16 v[68:71], a[132:135], v[128:131], 0
Rt 166,249,41472
s_and_b32 s28, s28, 1
v_mfma_f32_16x16x32_bf16 v[80:83], a[136:139], v[128:131], 0
Rt 168,250,40960
v_mul_f32_e32 v32, s54, v32
s_lshl_b32 s84, s28, 13
v_mfma_f32_16x16x32_bf16 v[88:91], a[136:139], v[132:135], 0
Rt 170,250,41472
v_mul_f32_e32 v33, s54, v33
v_mfma_f32_16x16x32_bf16 v[92:95], a[140:143], v[132:135], 0
Rt 172,251,40960
Cv 32,32,33
s_lshl1_add_u32 s28, s36, s84
v_mfma_f32_16x16x32_bf16 v[84:87], a[140:143], v[128:131], 0
Rt 174,251,41472
buffer_load_dwordx4 v10, s[4:7], s38 offen offset:2048 lds
s_waitcnt lgkmcnt(8)
Mp 64,144,136
Rt 128,248,45056
v_accvgpr_write_b32 a96, v32
Mp 72,144,140
Rt 130,248,45568
v_mul_f32_e32 v34, s54, v34
v_add_u32 v2, s28, v1
Mp 76,148,140
Rt 132,249,45056
)" R"(
v_mul_f32_e32 v35, s54, v35
Mp 68,148,136
Rt 134,249,45568
Cv 34,34,35
v_add_u32 v3, 0x1000, v2
Mp 80,152,136
Rt 136,250,45056
v_accvgpr_write_b32 a97, v34
s_bfe_u32 s28, s73, 0x30010
Mp 88,152,140
Rt 138,250,45568
buffer_load_dwordx4 v11, s[4:7], s38 offen offset:3072 lds
Mp 92,156,140
Rt 140,251,45056
v_mul_f32_e32 v36, s54, v36
s_lshr_b32 s28, s85, s28
Mp 84,156,136
Rt 142,251,45568
s_waitcnt lgkmcnt(8)
Mp 64,160,144
Rt 144,248,49152
v_mul_f32_e32 v37, s54, v37
Mp 72,160,148
Rt 146,248,49664
s_add_u32 s38, s38, 0x10000
s_not_b32 s28, s28
Mp 76,164,148
Rt 148,249,49152
s_add_u32 m0, s36, 36864
Mp 68,164,144
Rt 150,249,49664
buffer_load_dwordx4 v8, s[4:7], s38 offen offset:0 lds
Mp 80,168,144
Rt 152,250,49152
Cv 36,36,37
s_and_b32 s28, s28, 1
Mp 88,168,148
Rt 154,250,49664
v_accvgpr_write_b32 a98, v36
Mp 92,172,148
Rt 156,251,49152
v_mul_f32_e32 v38, s54, v38
s_lshl_b32 s28, s28, 13
Mp 84,172,144
Rt 158,251,49664
s_waitcnt lgkmcnt(8)
Mp 64,128,152
Rt 160,248,53248
v_mul_f32_e32 v39, s54, v39
s_add_u32 s73, s73, s28
Mp 72,128,156
Rt 162,248,53760
buffer_load_dwordx4 v9, s[4:7], s38 offen offset:1024 lds
Mp 76,132,156
Rt 164,249,53248
Cv 38,38,39
Mp 68,132,152
Rt 166,249,53760
v_accvgpr_write_b32 a99, v38
Mp 80,136,152
Rt 168,250,53248
v_mul_f32_e32 v40, s54, v40
Mp 88,136,156
Rt 170,250,53760
)" R"(
v_mul_f32_e32 v41, s54, v41
Mp 92,140,156
Rt 172,251,53248
Cv 40,40,41
Mp 84,140,152
Rt 174,251,53760
buffer_load_dwordx4 v10, s[4:7], s38 offen offset:2048 lds
s_waitcnt lgkmcnt(8)
Mp 64,144,160
Rt 128,248,57344
v_accvgpr_write_b32 a100, v40
Mp 72,144,164
Rt 130,248,57856
v_mul_f32_e32 v42, s54, v42
Mp 76,148,164
Rt 132,249,57344
v_mul_f32_e32 v43, s54, v43
Mp 68,148,160
Rt 134,249,57856
Cv 42,42,43
Mp 80,152,160
Rt 136,250,57344
v_accvgpr_write_b32 a101, v42
Mp 88,152,164
Rt 138,250,57856
buffer_load_dwordx4 v11, s[4:7], s38 offen offset:3072 lds
Mp 92,156,164
Rt 140,251,57344
v_mul_f32_e32 v44, s54, v44
Mp 84,156,160
Rt 142,251,57856
s_waitcnt lgkmcnt(8)
Mp 64,160,168
Rt 144,252,0
v_mul_f32_e32 v45, s54, v45
Mp 72,160,172
Rt 146,252,512
Cv 44,44,45
Mp 76,164,172
Rt 148,253,0
v_accvgpr_write_b32 a102, v44
Mp 68,164,168
Rt 150,253,512
v_mul_f32_e32 v46, s54, v46
Mp 80,168,168
Rt 152,254,0
v_mul_f32_e32 v47, s54, v47
Mp 88,168,172
Rt 154,254,512
Cv 46,46,47
Mp 92,172,172
Rt 156,255,0
v_accvgpr_write_b32 a103, v46
Mp 84,172,168
Rt 158,255,512
s_waitcnt vmcnt(12) lgkmcnt(8)
s_barrier
Mp 64,128,176
Rt 160,252,4096
v_mul_f32_e32 v48, s54, v48
Mp 72,128,180
Rt 162,252,4608
s_add_u32 s39, s39, 0x40000
Mp 76,132,180
Rt 164,253,4096
)" R"(
s_add_u32 m0, s37, 32768
Mp 68,132,176
Rt 166,253,4608
buffer_load_dwordx4 v12, s[8:11], s39 offen offset:0 lds
Mp 80,136,176
Rt 168,254,4096
v_mul_f32_e32 v49, s54, v49
Mp 88,136,180
Rt 170,254,4608
Cv 48,48,49
Mp 92,140,180
Rt 172,255,4096
v_accvgpr_write_b32 a104, v48
Mp 84,140,176
Rt 174,255,4608
s_waitcnt lgkmcnt(8)
Mp 64,144,184
Rt 128,252,8192
v_mul_f32_e32 v50, s54, v50
Mp 72,144,188
Rt 130,252,8704
buffer_load_dwordx4 v13, s[8:11], s39 offen offset:1024 lds
Mp 76,148,188
Rt 132,253,8192
v_mul_f32_e32 v51, s54, v51
Mp 68,148,184
Rt 134,253,8704
Cv 50,50,51
Mp 80,152,184
Rt 136,254,8192
v_accvgpr_write_b32 a105, v50
Mp 88,152,188
Rt 138,254,8704
v_mul_f32_e32 v52, s54, v52
Mp 92,156,188
Rt 140,255,8192
v_mul_f32_e32 v53, s54, v53
Mp 84,156,184
Rt 142,255,8704
buffer_load_dwordx4 v14, s[8:11], s39 offen offset:2048 lds
s_waitcnt lgkmcnt(8)
Mp 64,160,192
Rt 144,252,12288
Cv 52,52,53
Mp 72,160,196
Rt 146,252,12800
v_accvgpr_write_b32 a106, v52
Mp 76,164,196
Rt 148,253,12288
v_mul_f32_e32 v54, s54, v54
Mp 68,164,192
Rt 150,253,12800
v_mul_f32_e32 v55, s54, v55
Mp 80,168,192
Rt 152,254,12288
Cv 54,54,55
Mp 88,168,196
Rt 154,254,12800
buffer_load_dwordx4 v15, s[8:11], s39 offen offset:3072 lds
Mp 92,172,196
Rt 156,255,12288
v_accvgpr_write_b32 a107, v54
Mp 84,172,192
Rt 158,255,12800
)" R"(
s_waitcnt lgkmcnt(8)
Mp 64,128,200
Rt 160,252,16384
v_mul_f32_e32 v56, s54, v56
Mp 72,128,204
Rt 162,252,16896
v_mul_f32_e32 v57, s54, v57
Mp 76,132,204
Rt 164,253,16384
Cv 56,56,57
Mp 68,132,200
Rt 166,253,16896
v_accvgpr_write_b32 a108, v56
Mp 80,136,200
Rt 168,254,16384
v_mul_f32_e32 v58, s54, v58
Mp 88,136,204
Rt 170,254,16896
v_mul_f32_e32 v59, s54, v59
Mp 92,140,204
Rt 172,255,16384
Cv 58,58,59
Mp 84,140,200
Rt 174,255,16896
s_waitcnt lgkmcnt(8)
Mp 64,144,208
Rt 128,252,20480
Rt 0,18,0
Mp 72,144,212
Rt 130,252,20992
Rt 2,18,2048
Mp 76,148,212
Rt 132,253,20480
Rt 4,19,0
Mp 68,148,208
Rt 134,253,20992
Rt 6,19,2048
Mp 80,152,208
Rt 136,254,20480
Rt 8,20,0
Mp 88,152,212
Rt 138,254,20992
Rt 10,20,2048
Mp 92,156,212
Rt 140,255,20480
Rt 12,21,0
Mp 84,156,208
Rt 142,255,20992
Rt 14,21,2048
Mp 64,160,216
Rt 144,252,24576
Rt 16,22,0
Mp 72,160,220
Rt 146,252,25088
Rt 18,22,2048
Mp 76,164,220
Rt 148,253,24576
Rt 20,23,0
Mp 68,164,216
Rt 150,253,25088
Rt 22,23,2048
Mp 80,168,216
Rt 152,254,24576
Rt 24,24,0
)" R"(
Mp 88,168,220
Rt 154,254,25088
Rt 26,24,2048
Mp 92,172,220
Rt 156,255,24576
Rt 28,25,0
Mp 84,172,216
Rt 158,255,25088
Rt 30,25,2048
Mp 64,128,224
Rb 32,16,0
v_accvgpr_write_b32 a109, v58
Mp 72,128,228
Rb 36,16,2048
v_mul_f32_e32 v60, s54, v60
Mp 76,132,228
Rb 40,16,8192
v_mul_f32_e32 v61, s54, v61
Mp 68,132,224
Rb 44,16,10240
Cv 60,60,61
Mp 80,136,224
Rb 48,16,16384
v_accvgpr_write_b32 a110, v60
Mp 88,136,228
Rb 52,16,18432
v_mul_f32_e32 v62, s54, v62
Mp 92,140,228
Rb 56,16,24576
v_mul_f32_e32 v63, s54, v63
Mp 84,140,224
Rb 60,16,26624
s_waitcnt lgkmcnt(9)
Mp 64,144,232
Cv 62,62,63
v_accvgpr_write_b32 a111, v62
Mp 72,144,236
Mp 76,148,236
Mp 68,148,232
Mp 80,152,232
Mp 88,152,236
Mp 92,156,236
Mp 84,156,232
s_cmp_eq_u32 s91, 1
s_waitcnt vmcnt(12) lgkmcnt(0)
s_barrier
s_cbranch_scc1 .LzBV
Mz 32,0,32
Rt 128,18,16384
v_mul_f32_e32 v64, s54, v64
Mz 96,0,40
v_mul_f32_e32 v65, s54, v65
Cv 64,64,65
Mz 128,0,44
Rt 130,18,18432
v_accvgpr_write_b32 a112, v64
Mz 160,0,48
s_add_u32 s39, s39, 0x40000
v_mul_f32_e32 v66, s54, v66
Mz 192,0,52
Rt 132,19,16384
s_add_u32 m0, s37, 49152
Mz 224,0,56
buffer_load_dwordx4 v12, s[8:11], s39 offen offset:0 lds
v_mfma_f32_16x16x32_bf16 a[192:195], a[0:3], a[60:63], 0
)" R"(
Rt 134,19,18432
v_mul_f32_e32 v67, s54, v67
v_mfma_f32_16x16x32_bf16 a[196:199], a[4:7], a[60:63], 0
Cv 66,66,67
v_accvgpr_write_b32 a113, v66
Mz 228,4,56
Rt 136,20,16384
v_mul_f32_e32 v68, s54, v68
Mz 196,4,52
v_mul_f32_e32 v69, s54, v69
Cv 68,68,69
buffer_store_dwordx4 a[64:67], v2, s[20:23], s51 offen offset:0 sc0 sc1
Mz 164,4,48
Rt 138,20,18432
v_accvgpr_write_b32 a114, v68
Mz 132,4,44
v_mul_f32_e32 v70, s54, v70
v_mul_f32_e32 v71, s54, v71
Mz 100,4,40
Rt 140,21,16384
Cv 70,70,71
Mz 36,4,32
buffer_load_dwordx4 v13, s[8:11], s39 offen offset:1024 lds
Mz 40,8,32
Rt 142,21,18432
v_accvgpr_write_b32 a115, v70
Mz 104,8,40
v_mul_f32_e32 v72, s54, v72
v_mul_f32_e32 v73, s54, v73
Mz 136,8,44
Rt 144,22,16384
Cv 72,72,73
Mz 168,8,48
v_accvgpr_write_b32 a116, v72
v_mul_f32_e32 v74, s54, v74
buffer_store_dwordx4 a[68:71], v2, s[20:23], s51 offen offset:1024 sc0 sc1
Mz 200,8,52
Rt 146,22,18432
v_mul_f32_e32 v75, s54, v75
Mz 232,8,56
Cv 74,74,75
v_accvgpr_write_b32 a117, v74
v_mfma_f32_16x16x32_bf16 a[200:203], a[8:11], a[60:63], 0
Rt 148,23,16384
v_mul_f32_e32 v76, s54, v76
v_mfma_f32_16x16x32_bf16 a[204:207], a[12:15], a[60:63], 0
buffer_load_dwordx4 v14, s[8:11], s39 offen offset:2048 lds
Mz 236,12,56
Rt 150,23,18432
v_mul_f32_e32 v77, s54, v77
Mz 204,12,52
Cv 76,76,77
v_accvgpr_write_b32 a118, v76
Mz 172,12,48
Rt 152,24,16384
v_mul_f32_e32 v78, s54, v78
Mz 140,12,44
v_mul_f32_e32 v79, s54, v79
Cv 78,78,79
buffer_store_dwordx4 a[72:75], v2, s[20:23], s51 offen offset:2048 sc0 sc1
Mz 108,12,40
Rt 154,24,18432
v_accvgpr_write_b32 a119, v78
Mz 44,12,32
)" R"(
v_mul_f32_e32 v80, s54, v80
v_mul_f32_e32 v81, s54, v81
Mz 48,16,32
Rt 156,25,16384
Cv 80,80,81
Mz 112,16,40
buffer_load_dwordx4 v15, s[8:11], s39 offen offset:3072 lds
Mz 144,16,44
Rt 158,25,18432
v_accvgpr_write_b32 a120, v80
Mz 176,16,48
v_mul_f32_e32 v82, s54, v82
v_mul_f32_e32 v83, s54, v83
Mz 208,16,52
Rb 160,17,0
Cv 82,82,83
Mz 240,16,56
v_accvgpr_write_b32 a121, v82
v_mul_f32_e32 v84, s54, v84
buffer_store_dwordx4 a[76:79], v2, s[20:23], s51 offen offset:3072 sc0 sc1
v_mfma_f32_16x16x32_bf16 a[208:211], a[16:19], a[60:63], 0
Rb 164,17,2048
v_mul_f32_e32 v85, s54, v85
v_mfma_f32_16x16x32_bf16 a[212:215], a[20:23], a[60:63], 0
s_add_u32 s39, s39, 0x40000
Cv 84,84,85
Mz 244,20,56
Rb 168,17,8192
s_add_u32 m0, s37, 0
Mz 212,20,52
buffer_load_dwordx4 v12, s[8:11], s39 offen offset:0 lds
Mz 180,20,48
Rb 172,17,10240
v_accvgpr_write_b32 a122, v84
Mz 148,20,44
v_mul_f32_e32 v86, s54, v86
v_mul_f32_e32 v87, s54, v87
Mz 116,20,40
Rb 176,17,16384
Cv 86,86,87
Mz 52,20,32
v_accvgpr_write_b32 a123, v86
v_mul_f32_e32 v88, s54, v88
buffer_store_dwordx4 a[80:83], v3, s[20:23], s51 offen offset:0 sc0 sc1
Mz 56,24,32
Rb 180,17,18432
v_mul_f32_e32 v89, s54, v89
Mz 120,24,40
Cv 88,88,89
v_accvgpr_write_b32 a124, v88
Mz 152,24,44
Rb 184,17,24576
v_mul_f32_e32 v90, s54, v90
Mz 184,24,48
buffer_load_dwordx4 v13, s[8:11], s39 offen offset:1024 lds
Mz 216,24,52
Rb 188,17,26624
v_mul_f32_e32 v91, s54, v91
Mz 248,24,56
Cv 90,90,91
v_accvgpr_write_b32 a125, v90
v_mfma_f32_16x16x32_bf16 a[216:219], a[24:27], a[60:63], 0
v_mul_f32_e32 v92, s54, v92
v_mul_f32_e32 v93, s54, v93
)" R"(
v_mfma_f32_16x16x32_bf16 a[220:223], a[28:31], a[60:63], 0
Cv 92,92,93
v_accvgpr_write_b32 a126, v92
buffer_store_dwordx4 a[84:87], v3, s[20:23], s51 offen offset:1024 sc0 sc1
Mz 252,28,56
v_mul_f32_e32 v94, s54, v94
v_mul_f32_e32 v95, s54, v95
Mz 220,28,52
Cv 94,94,95
v_accvgpr_write_b32 a127, v94
Mz 188,28,48
Mz 156,28,44
buffer_load_dwordx4 v14, s[8:11], s39 offen offset:2048 lds
Mz 124,28,40
Mz 60,28,32
Mz 64,0,36
s_mov_b32 s24, s80
s_mov_b32 s25, s81
Mz 68,4,36
buffer_store_dwordx4 a[88:91], v3, s[20:23], s51 offen offset:2048 sc0 sc1
Mz 72,8,36
s_mov_b32 s26, 0x380000
s_mov_b32 s27, 0x20000
Mz 76,12,36
Mz 80,16,36
Mz 84,20,36
buffer_load_dwordx4 v15, s[8:11], s39 offen offset:3072 lds
Mz 88,24,36
Mz 92,28,36
s_waitcnt vmcnt(15) lgkmcnt(0)
s_barrier
Mv 32,128,160
Rt 0,18,32768
Mv 64,128,164
s_add_u32 s38, s38, 0xffff0080
Mv 96,128,168
Rt 2,18,34816
s_add_u32 m0, s36, 0
Mv 128,128,172
buffer_load_dwordx4 v8, s[4:7], s38 offen offset:0 lds
Mv 160,128,176
Rt 4,19,32768
Mv 192,128,180
Mv 224,128,184
Rt 6,19,34816
Ma 192,128,188
buffer_load_dwordx4 v9, s[4:7], s38 offen offset:1024 lds
Ma 196,132,188
Rt 8,20,32768
Mv 228,132,184
Mv 196,132,180
Rt 10,20,34816
Mv 164,132,176
buffer_load_dwordx4 v10, s[4:7], s38 offen offset:2048 lds
Mv 132,132,172
Rt 12,21,32768
Mv 100,132,168
Mv 68,132,164
Rt 14,21,34816
Mv 36,132,160
buffer_load_dwordx4 v11, s[4:7], s38 offen offset:3072 lds
Mv 40,136,160
Rt 16,22,32768
Mv 72,136,164
s_add_u32 s38, s38, 0x10000
)" R"(
Mv 104,136,168
Rt 18,22,34816
s_add_u32 m0, s36, 4096
Mv 136,136,172
buffer_load_dwordx4 v8, s[4:7], s38 offen offset:0 lds
Mv 168,136,176
Rt 20,23,32768
Mv 200,136,180
Mv 232,136,184
Rt 22,23,34816
Ma 200,136,188
buffer_load_dwordx4 v9, s[4:7], s38 offen offset:1024 lds
Ma 204,140,188
Rt 24,24,32768
Mv 236,140,184
Mv 204,140,180
Rt 26,24,34816
Mv 172,140,176
buffer_load_dwordx4 v10, s[4:7], s38 offen offset:2048 lds
Mv 140,140,172
Rt 28,25,32768
Mv 108,140,168
Mv 76,140,164
Rt 30,25,34816
Mv 44,140,160
buffer_load_dwordx4 v11, s[4:7], s38 offen offset:3072 lds
Mv 48,144,160
Rb 32,16,32768
Mv 80,144,164
Mv 112,144,168
Rb 36,16,34816
Mv 144,144,172
Mv 176,144,176
Rb 40,16,40960
Mv 208,144,180
buffer_load_dword v27, v26, s[24:27], s90 offen
Mv 240,144,184
Rb 44,16,43008
s_add_u32 s28, s90, 0x200000
Ma 208,144,188
Ma 212,148,188
Rb 48,16,49152
Mv 244,148,184
buffer_store_dwordx4 a[92:95], v3, s[20:23], s51 offen offset:3072 sc0 sc1
Mv 212,148,180
Rb 52,16,51200
Mv 180,148,176
Mv 148,148,172
Rb 56,16,57344
Mv 116,148,168
Mv 84,148,164
Rb 60,16,59392
Mv 52,148,160
Mv 56,152,160
Mv 88,152,164
s_add_u32 s51, s51, 0x10000
buffer_store_dwordx4 a[96:99], v2, s[20:23], s51 offen offset:0 sc0 sc1
Mv 120,152,168
Mv 152,152,172
Mv 184,152,176
Mv 216,152,180
Mv 248,152,184
Ma 216,152,188
Ma 220,156,188
)" R"(
Mv 252,156,184
buffer_store_dwordx4 a[100:103], v2, s[20:23], s51 offen offset:1024 sc0 sc1
Mv 220,156,180
Mv 188,156,176
Mv 156,156,172
Mv 124,156,168
Mv 92,156,164
Mv 60,156,160
s_waitcnt vmcnt(20) lgkmcnt(0)
s_barrier
Mv 32,0,32
Rt 128,18,49152
Mv 64,0,36
Mv 96,0,40
Rt 130,18,51200
Mv 128,0,44
s_add_u32 s39, s39, 0x40000
Mv 160,0,48
Rt 132,19,49152
s_add_u32 m0, s37, 16384
Mv 192,0,52
buffer_load_dwordx4 v12, s[8:11], s39 offen offset:0 lds
Mv 224,0,56
Rt 134,19,51200
Ma 192,0,60
Ma 196,4,60
Rt 136,20,49152
Mv 228,4,56
buffer_store_dwordx4 a[104:107], v2, s[20:23], s51 offen offset:2048 sc0 sc1
Mv 196,4,52
Rt 138,20,51200
Mv 164,4,48
Mv 132,4,44
Rt 140,21,49152
Mv 100,4,40
buffer_load_dwordx4 v13, s[8:11], s39 offen offset:1024 lds
Mv 68,4,36
Rt 142,21,51200
Mv 36,4,32
Mv 40,8,32
Rt 144,22,49152
Mv 72,8,36
buffer_store_dwordx4 a[108:111], v2, s[20:23], s51 offen offset:3072 sc0 sc1
Mv 104,8,40
Rt 146,22,51200
Mv 136,8,44
Mv 168,8,48
Rt 148,23,49152
Mv 200,8,52
buffer_load_dwordx4 v14, s[8:11], s39 offen offset:2048 lds
Mv 232,8,56
Rt 150,23,51200
Ma 200,8,60
Ma 204,12,60
Rt 152,24,49152
Mv 236,12,56
buffer_store_dwordx4 a[112:115], v3, s[20:23], s51 offen offset:0 sc0 sc1
Mv 204,12,52
Rt 154,24,51200
Mv 172,12,48
Mv 140,12,44
Rt 156,25,49152
Mv 108,12,40
buffer_load_dwordx4 v15, s[8:11], s39 offen offset:3072 lds
Mv 76,12,36
)" R"(
Rt 158,25,51200
Mv 44,12,32
Mv 48,16,32
Rb 160,17,32768
Mv 80,16,36
buffer_store_dwordx4 a[116:119], v3, s[20:23], s51 offen offset:1024 sc0 sc1
Mv 112,16,40
Rb 164,17,34816
Mv 144,16,44
s_add_u32 s39, s39, 0x40000
Mv 176,16,48
Rb 168,17,40960
s_add_u32 m0, s37, 32768
Mv 208,16,52
buffer_load_dwordx4 v12, s[8:11], s39 offen offset:0 lds
Mv 240,16,56
Rb 172,17,43008
Ma 208,16,60
Ma 212,20,60
Rb 176,17,49152
Mv 244,20,56
buffer_store_dwordx4 a[120:123], v3, s[20:23], s51 offen offset:2048 sc0 sc1
Mv 212,20,52
Rb 180,17,51200
Mv 180,20,48
Mv 148,20,44
Rb 184,17,57344
Mv 116,20,40
buffer_load_dwordx4 v13, s[8:11], s39 offen offset:1024 lds
Mv 84,20,36
Rb 188,17,59392
Mv 52,20,32
Mv 56,24,32
Mv 88,24,36
buffer_store_dwordx4 a[124:127], v3, s[20:23], s51 offen offset:3072 sc0 sc1
Mv 120,24,40
Mv 152,24,44
Mv 184,24,48
Mv 216,24,52
buffer_load_dwordx4 v14, s[8:11], s39 offen offset:2048 lds
Mv 248,24,56
Ma 216,24,60
Ma 220,28,60
Mv 252,28,56
Mv 220,28,52
Mv 188,28,48
Mv 156,28,44
Mv 124,28,40
buffer_load_dwordx4 v15, s[8:11], s39 offen offset:3072 lds
Mv 92,28,36
Mv 60,28,32
s_waitcnt vmcnt(18) lgkmcnt(0)
s_barrier
Mv 32,128,160
Rt 0,18,0
Mv 64,128,164
s_add_u32 s38, s38, 0xffff0080
Mv 96,128,168
Rt 2,18,2048
s_add_u32 m0, s36, 32768
Mv 128,128,172
buffer_load_dwordx4 v8, s[4:7], s38 offen offset:0 lds
Mv 160,128,176
Rt 4,19,0
Mv 192,128,180
)" R"(
Mv 224,128,184
Rt 6,19,2048
Ma 192,128,188
buffer_load_dwordx4 v9, s[4:7], s38 offen offset:1024 lds
Ma 196,132,188
Rt 8,20,0
s_mov_b32 s40, 5
Mv 228,132,184
Mv 196,132,180
Rt 10,20,2048
Mv 164,132,176
buffer_load_dwordx4 v10, s[4:7], s38 offen offset:2048 lds
Mv 132,132,172
Rt 12,21,0
Mv 100,132,168
Mv 68,132,164
Rt 14,21,2048
Mv 36,132,160
buffer_load_dwordx4 v11, s[4:7], s38 offen offset:3072 lds
Mv 40,136,160
Rt 16,22,0
Mv 72,136,164
s_add_u32 s38, s38, 0x10000
Mv 104,136,168
Rt 18,22,2048
s_add_u32 m0, s36, 36864
Mv 136,136,172
buffer_load_dwordx4 v8, s[4:7], s38 offen offset:0 lds
Mv 168,136,176
Rt 20,23,0
Mv 200,136,180
Mv 232,136,184
Rt 22,23,2048
Ma 200,136,188
buffer_load_dwordx4 v9, s[4:7], s38 offen offset:1024 lds
Ma 204,140,188
Rt 24,24,0
Mv 236,140,184
Mv 204,140,180
Rt 26,24,2048
Mv 172,140,176
buffer_load_dwordx4 v10, s[4:7], s38 offen offset:2048 lds
Mv 140,140,172
Rt 28,25,0
Mv 108,140,168
Mv 76,140,164
Rt 30,25,2048
Mv 44,140,160
buffer_load_dwordx4 v11, s[4:7], s38 offen offset:3072 lds
Mv 48,144,160
Rb 32,16,0
Mv 80,144,164
Mv 112,144,168
Rb 36,16,2048
Mv 144,144,172
Mv 176,144,176
Rb 40,16,8192
Mv 208,144,180
buffer_load_dword v27, v26, s[24:27], s28 offen
Mv 240,144,184
Rb 44,16,10240
Ma 208,144,188
Ma 212,148,188
Rb 48,16,16384
)" R"(
Mv 244,148,184
Mv 212,148,180
Rb 52,16,18432
Mv 180,148,176
Mv 148,148,172
Rb 56,16,24576
Mv 116,148,168
Mv 84,148,164
Rb 60,16,26624
Mv 52,148,160
Mv 56,152,160
Mv 88,152,164
Mv 120,152,168
Mv 152,152,172
Mv 184,152,176
Mv 216,152,180
Mv 248,152,184
Ma 216,152,188
Ma 220,156,188
Mv 252,156,184
Mv 220,156,180
Mv 188,156,176
Mv 156,156,172
Mv 124,156,168
Mv 92,156,164
Mv 60,156,160
s_waitcnt vmcnt(11) lgkmcnt(0)
s_barrier
buffer_store_dword v6, v7, s[20:23], s52 offen offset:0 sc0 sc1
.LzPBK:
Mv 32,0,32
Rt 128,18,16384
Mv 64,0,36
Mv 96,0,40
Rt 130,18,18432
Mv 128,0,44
s_add_u32 s39, s39, 0x40000
Mv 160,0,48
Rt 132,19,16384
s_add_u32 m0, s37, 49152
Mv 192,0,52
buffer_load_dwordx4 v12, s[8:11], s39 offen offset:0 lds
Mv 224,0,56
Rt 134,19,18432
Ma 192,0,60
Ma 196,4,60
Rt 136,20,16384
Mv 228,4,56
Mv 196,4,52
Rt 138,20,18432
Mv 164,4,48
Mv 132,4,44
Rt 140,21,16384
Mv 100,4,40
buffer_load_dwordx4 v13, s[8:11], s39 offen offset:1024 lds
Mv 68,4,36
Rt 142,21,18432
Mv 36,4,32
Mv 40,8,32
Rt 144,22,16384
Mv 72,8,36
Mv 104,8,40
Rt 146,22,18432
Mv 136,8,44
Mv 168,8,48
)" R"(
Rt 148,23,16384
Mv 200,8,52
buffer_load_dwordx4 v14, s[8:11], s39 offen offset:2048 lds
Mv 232,8,56
Rt 150,23,18432
Ma 200,8,60
Ma 204,12,60
Rt 152,24,16384
Mv 236,12,56
Mv 204,12,52
Rt 154,24,18432
Mv 172,12,48
Mv 140,12,44
Rt 156,25,16384
Mv 108,12,40
buffer_load_dwordx4 v15, s[8:11], s39 offen offset:3072 lds
Mv 76,12,36
Rt 158,25,18432
Mv 44,12,32
Mv 48,16,32
Rb 160,17,0
Mv 80,16,36
Mv 112,16,40
Rb 164,17,2048
Mv 144,16,44
s_add_u32 s39, s39, 0x40000
Mv 176,16,48
Rb 168,17,8192
s_add_u32 m0, s37, 0
Mv 208,16,52
buffer_load_dwordx4 v12, s[8:11], s39 offen offset:0 lds
Mv 240,16,56
Rb 172,17,10240
Ma 208,16,60
Ma 212,20,60
Rb 176,17,16384
Mv 244,20,56
Mv 212,20,52
Rb 180,17,18432
Mv 180,20,48
Mv 148,20,44
Rb 184,17,24576
Mv 116,20,40
buffer_load_dwordx4 v13, s[8:11], s39 offen offset:1024 lds
Mv 84,20,36
Rb 188,17,26624
Mv 52,20,32
Mv 56,24,32
Mv 88,24,36
Mv 120,24,40
Mv 152,24,44
Mv 184,24,48
Mv 216,24,52
buffer_load_dwordx4 v14, s[8:11], s39 offen offset:2048 lds
Mv 248,24,56
Ma 216,24,60
Ma 220,28,60
Mv 252,28,56
Mv 220,28,52
Mv 188,28,48
Mv 156,28,44
Mv 124,28,40
buffer_load_dwordx4 v15, s[8:11], s39 offen offset:3072 lds
Mv 92,28,36
)" R"(
Mv 60,28,32
s_waitcnt vmcnt(8) lgkmcnt(0)
s_barrier
Mv 32,128,160
Rt 0,18,32768
Mv 64,128,164
s_add_u32 s38, s38, 0xffff0080
Mv 96,128,168
Rt 2,18,34816
s_add_u32 m0, s36, 0
Mv 128,128,172
buffer_load_dwordx4 v8, s[4:7], s38 offen offset:0 lds
Mv 160,128,176
Rt 4,19,32768
Mv 192,128,180
Mv 224,128,184
Rt 6,19,34816
Ma 192,128,188
buffer_load_dwordx4 v9, s[4:7], s38 offen offset:1024 lds
Ma 196,132,188
Rt 8,20,32768
Mv 228,132,184
Mv 196,132,180
Rt 10,20,34816
Mv 164,132,176
buffer_load_dwordx4 v10, s[4:7], s38 offen offset:2048 lds
Mv 132,132,172
Rt 12,21,32768
Mv 100,132,168
Mv 68,132,164
Rt 14,21,34816
Mv 36,132,160
buffer_load_dwordx4 v11, s[4:7], s38 offen offset:3072 lds
Mv 40,136,160
Rt 16,22,32768
Mv 72,136,164
s_add_u32 s38, s38, 0x10000
Mv 104,136,168
Rt 18,22,34816
s_add_u32 m0, s36, 4096
Mv 136,136,172
buffer_load_dwordx4 v8, s[4:7], s38 offen offset:0 lds
Mv 168,136,176
Rt 20,23,32768
Mv 200,136,180
Mv 232,136,184
Rt 22,23,34816
Ma 200,136,188
buffer_load_dwordx4 v9, s[4:7], s38 offen offset:1024 lds
Ma 204,140,188
Rt 24,24,32768
Mv 236,140,184
Mv 204,140,180
Rt 26,24,34816
Mv 172,140,176
buffer_load_dwordx4 v10, s[4:7], s38 offen offset:2048 lds
Mv 140,140,172
Rt 28,25,32768
Mv 108,140,168
Mv 76,140,164
Rt 30,25,34816
Mv 44,140,160
buffer_load_dwordx4 v11, s[4:7], s38 offen offset:3072 lds
Mv 48,144,160
Rb 32,16,32768
)" R"(
Mv 80,144,164
Mv 112,144,168
Rb 36,16,34816
Mv 144,144,172
Mv 176,144,176
Rb 40,16,40960
Mv 208,144,180
Mv 240,144,184
Rb 44,16,43008
Ma 208,144,188
Ma 212,148,188
Rb 48,16,49152
Mv 244,148,184
Mv 212,148,180
Rb 52,16,51200
Mv 180,148,176
Mv 148,148,172
Rb 56,16,57344
Mv 116,148,168
Mv 84,148,164
Rb 60,16,59392
Mv 52,148,160
Mv 56,152,160
Mv 88,152,164
Mv 120,152,168
Mv 152,152,172
Mv 184,152,176
Mv 216,152,180
Mv 248,152,184
Ma 216,152,188
Ma 220,156,188
Mv 252,156,184
Mv 220,156,180
Mv 188,156,176
Mv 156,156,172
Mv 124,156,168
Mv 92,156,164
Mv 60,156,160
s_waitcnt vmcnt(12) lgkmcnt(0)
s_barrier
Mv 32,0,32
Rt 128,18,49152
Mv 64,0,36
Mv 96,0,40
Rt 130,18,51200
Mv 128,0,44
s_add_u32 s39, s39, 0x40000
Mv 160,0,48
Rt 132,19,49152
s_add_u32 m0, s37, 16384
Mv 192,0,52
buffer_load_dwordx4 v12, s[8:11], s39 offen offset:0 lds
Mv 224,0,56
Rt 134,19,51200
Ma 192,0,60
Ma 196,4,60
Rt 136,20,49152
Mv 228,4,56
Mv 196,4,52
Rt 138,20,51200
Mv 164,4,48
Mv 132,4,44
Rt 140,21,49152
Mv 100,4,40
buffer_load_dwordx4 v13, s[8:11], s39 offen offset:1024 lds
)" R"(
Mv 68,4,36
Rt 142,21,51200
Mv 36,4,32
Mv 40,8,32
Rt 144,22,49152
Mv 72,8,36
Mv 104,8,40
Rt 146,22,51200
Mv 136,8,44
Mv 168,8,48
Rt 148,23,49152
Mv 200,8,52
buffer_load_dwordx4 v14, s[8:11], s39 offen offset:2048 lds
Mv 232,8,56
Rt 150,23,51200
Ma 200,8,60
Ma 204,12,60
Rt 152,24,49152
Mv 236,12,56
Mv 204,12,52
Rt 154,24,51200
Mv 172,12,48
Mv 140,12,44
Rt 156,25,49152
Mv 108,12,40
buffer_load_dwordx4 v15, s[8:11], s39 offen offset:3072 lds
Mv 76,12,36
Rt 158,25,51200
Mv 44,12,32
Mv 48,16,32
Rb 160,17,32768
Mv 80,16,36
Mv 112,16,40
Rb 164,17,34816
Mv 144,16,44
s_add_u32 s39, s39, 0x40000
Mv 176,16,48
Rb 168,17,40960
s_add_u32 m0, s37, 32768
Mv 208,16,52
buffer_load_dwordx4 v12, s[8:11], s39 offen offset:0 lds
Mv 240,16,56
Rb 172,17,43008
Ma 208,16,60
Ma 212,20,60
Rb 176,17,49152
Mv 244,20,56
Mv 212,20,52
Rb 180,17,51200
Mv 180,20,48
Mv 148,20,44
Rb 184,17,57344
Mv 116,20,40
buffer_load_dwordx4 v13, s[8:11], s39 offen offset:1024 lds
Mv 84,20,36
Rb 188,17,59392
Mv 52,20,32
Mv 56,24,32
Mv 88,24,36
Mv 120,24,40
Mv 152,24,44
Mv 184,24,48
Mv 216,24,52
buffer_load_dwordx4 v14, s[8:11], s39 offen offset:2048 lds
)" R"(
Mv 248,24,56
Ma 216,24,60
Ma 220,28,60
Mv 252,28,56
Mv 220,28,52
Mv 188,28,48
Mv 156,28,44
Mv 124,28,40
buffer_load_dwordx4 v15, s[8:11], s39 offen offset:3072 lds
Mv 92,28,36
Mv 60,28,32
s_waitcnt vmcnt(8) lgkmcnt(0)
s_barrier
Mv 32,128,160
Rt 0,18,0
Mv 64,128,164
s_add_u32 s38, s38, 0xffff0080
Mv 96,128,168
Rt 2,18,2048
s_add_u32 m0, s36, 32768
Mv 128,128,172
buffer_load_dwordx4 v8, s[4:7], s38 offen offset:0 lds
Mv 160,128,176
Rt 4,19,0
Mv 192,128,180
Mv 224,128,184
Rt 6,19,2048
Ma 192,128,188
buffer_load_dwordx4 v9, s[4:7], s38 offen offset:1024 lds
Ma 196,132,188
Rt 8,20,0
Mv 228,132,184
Mv 196,132,180
Rt 10,20,2048
Mv 164,132,176
buffer_load_dwordx4 v10, s[4:7], s38 offen offset:2048 lds
Mv 132,132,172
Rt 12,21,0
Mv 100,132,168
Mv 68,132,164
Rt 14,21,2048
Mv 36,132,160
buffer_load_dwordx4 v11, s[4:7], s38 offen offset:3072 lds
Mv 40,136,160
Rt 16,22,0
Mv 72,136,164
s_add_u32 s38, s38, 0x10000
Mv 104,136,168
Rt 18,22,2048
s_add_u32 m0, s36, 36864
Mv 136,136,172
buffer_load_dwordx4 v8, s[4:7], s38 offen offset:0 lds
Mv 168,136,176
Rt 20,23,0
Mv 200,136,180
Mv 232,136,184
Rt 22,23,2048
Ma 200,136,188
buffer_load_dwordx4 v9, s[4:7], s38 offen offset:1024 lds
Ma 204,140,188
Rt 24,24,0
Mv 236,140,184
Mv 204,140,180
Rt 26,24,2048
Mv 172,140,176
)" R"(
buffer_load_dwordx4 v10, s[4:7], s38 offen offset:2048 lds
Mv 140,140,172
Rt 28,25,0
Mv 108,140,168
Mv 76,140,164
Rt 30,25,2048
Mv 44,140,160
buffer_load_dwordx4 v11, s[4:7], s38 offen offset:3072 lds
Mv 48,144,160
Rb 32,16,0
Mv 80,144,164
Mv 112,144,168
Rb 36,16,2048
Mv 144,144,172
Mv 176,144,176
Rb 40,16,8192
Mv 208,144,180
Mv 240,144,184
Rb 44,16,10240
Ma 208,144,188
Ma 212,148,188
Rb 48,16,16384
Mv 244,148,184
Mv 212,148,180
Rb 52,16,18432
Mv 180,148,176
Mv 148,148,172
Rb 56,16,24576
Mv 116,148,168
Mv 84,148,164
Rb 60,16,26624
Mv 52,148,160
Mv 56,152,160
Mv 88,152,164
Mv 120,152,168
Mv 152,152,172
Mv 184,152,176
Mv 216,152,180
Mv 248,152,184
Ma 216,152,188
Ma 220,156,188
Mv 252,156,184
Mv 220,156,180
s_sub_u32 s40, s40, 1
Mv 188,156,176
Mv 156,156,172
Mv 124,156,168
Mv 92,156,164
Mv 60,156,160
s_cmp_lg_u32 s40, 0
s_waitcnt vmcnt(12) lgkmcnt(0)
s_barrier
s_cbranch_scc1 .LzPBK
s_branch .LzPBKX
.LzBV:
Mz 32,32,0
Rt 128,18,16384
v_mul_f32_e32 v64, s54, v64
Mz 36,36,0
v_mul_f32_e32 v65, s54, v65
Cv 64,64,65
Mz 96,40,0
Rt 130,18,18432
v_accvgpr_write_b32 a112, v64
Mz 100,44,0
)" R"(
s_add_u32 s39, s39, 0x40000
v_mul_f32_e32 v66, s54, v66
Mz 160,48,0
Rt 132,19,16384
s_add_u32 m0, s37, 49152
Mz 164,52,0
buffer_load_dwordx4 v12, s[8:11], s39 offen offset:0 lds
Mz 224,56,0
Rt 134,19,18432
v_mul_f32_e32 v67, s54, v67
Mz 228,60,0
Cv 66,66,67
v_accvgpr_write_b32 a113, v66
Mz 236,60,4
Rt 136,20,16384
v_mul_f32_e32 v68, s54, v68
Mz 232,56,4
v_mul_f32_e32 v69, s54, v69
Cv 68,68,69
buffer_store_dwordx4 a[64:67], v2, s[20:23], s51 offen offset:0 sc0 sc1
Mz 172,52,4
Rt 138,20,18432
v_accvgpr_write_b32 a114, v68
Mz 168,48,4
v_mul_f32_e32 v70, s54, v70
v_mul_f32_e32 v71, s54, v71
Mz 108,44,4
Rt 140,21,16384
Cv 70,70,71
Mz 104,40,4
buffer_load_dwordx4 v13, s[8:11], s39 offen offset:1024 lds
Mz 44,36,4
Rt 142,21,18432
v_accvgpr_write_b32 a115, v70
Mz 40,32,4
v_mul_f32_e32 v72, s54, v72
v_mul_f32_e32 v73, s54, v73
Mz 48,32,8
Rt 144,22,16384
Cv 72,72,73
Mz 52,36,8
v_accvgpr_write_b32 a116, v72
v_mul_f32_e32 v74, s54, v74
buffer_store_dwordx4 a[68:71], v2, s[20:23], s51 offen offset:1024 sc0 sc1
Mz 112,40,8
Rt 146,22,18432
v_mul_f32_e32 v75, s54, v75
Mz 116,44,8
Cv 74,74,75
v_accvgpr_write_b32 a117, v74
Mz 176,48,8
Rt 148,23,16384
v_mul_f32_e32 v76, s54, v76
Mz 180,52,8
buffer_load_dwordx4 v14, s[8:11], s39 offen offset:2048 lds
Mz 240,56,8
Rt 150,23,18432
v_mul_f32_e32 v77, s54, v77
Mz 244,60,8
Cv 76,76,77
v_accvgpr_write_b32 a118, v76
Mz 252,60,12
Rt 152,24,16384
v_mul_f32_e32 v78, s54, v78
)" R"(
Mz 248,56,12
v_mul_f32_e32 v79, s54, v79
Cv 78,78,79
buffer_store_dwordx4 a[72:75], v2, s[20:23], s51 offen offset:2048 sc0 sc1
Mz 188,52,12
Rt 154,24,18432
v_accvgpr_write_b32 a119, v78
Mz 184,48,12
v_mul_f32_e32 v80, s54, v80
v_mul_f32_e32 v81, s54, v81
Mz 124,44,12
Rt 156,25,16384
Cv 80,80,81
Mz 120,40,12
buffer_load_dwordx4 v15, s[8:11], s39 offen offset:3072 lds
Mz 60,36,12
Rt 158,25,18432
v_accvgpr_write_b32 a120, v80
Mz 56,32,12
v_mul_f32_e32 v82, s54, v82
v_mul_f32_e32 v83, s54, v83
Mz 128,40,16
Rb 160,17,0
Cv 82,82,83
Mz 132,44,16
v_accvgpr_write_b32 a121, v82
v_mul_f32_e32 v84, s54, v84
buffer_store_dwordx4 a[76:79], v2, s[20:23], s51 offen offset:3072 sc0 sc1
Mz 192,48,16
Rb 164,17,2048
v_mul_f32_e32 v85, s54, v85
Mz 196,52,16
s_add_u32 s39, s39, 0x40000
Cv 84,84,85
v_mfma_f32_16x16x32_bf16 a[192:195], a[56:59], a[16:19], 0
Rb 168,17,8192
s_add_u32 m0, s37, 0
v_mfma_f32_16x16x32_bf16 a[196:199], a[60:63], a[16:19], 0
buffer_load_dwordx4 v12, s[8:11], s39 offen offset:0 lds
v_mfma_f32_16x16x32_bf16 a[204:207], a[60:63], a[20:23], 0
Rb 172,17,10240
v_accvgpr_write_b32 a122, v84
v_mfma_f32_16x16x32_bf16 a[200:203], a[56:59], a[20:23], 0
v_mul_f32_e32 v86, s54, v86
v_mul_f32_e32 v87, s54, v87
Mz 204,52,20
Rb 176,17,16384
Cv 86,86,87
Mz 200,48,20
v_accvgpr_write_b32 a123, v86
v_mul_f32_e32 v88, s54, v88
buffer_store_dwordx4 a[80:83], v3, s[20:23], s51 offen offset:0 sc0 sc1
Mz 140,44,20
Rb 180,17,18432
v_mul_f32_e32 v89, s54, v89
Mz 136,40,20
Cv 88,88,89
v_accvgpr_write_b32 a124, v88
Mz 144,40,24
Rb 184,17,24576
v_mul_f32_e32 v90, s54, v90
Mz 148,44,24
buffer_load_dwordx4 v13, s[8:11], s39 offen offset:1024 lds
Mz 208,48,24
)" R"(
Rb 188,17,26624
v_mul_f32_e32 v91, s54, v91
Mz 212,52,24
Cv 90,90,91
v_accvgpr_write_b32 a125, v90
v_mfma_f32_16x16x32_bf16 a[208:211], a[56:59], a[24:27], 0
v_mul_f32_e32 v92, s54, v92
v_mul_f32_e32 v93, s54, v93
v_mfma_f32_16x16x32_bf16 a[212:215], a[60:63], a[24:27], 0
Cv 92,92,93
v_accvgpr_write_b32 a126, v92
buffer_store_dwordx4 a[84:87], v3, s[20:23], s51 offen offset:1024 sc0 sc1
v_mfma_f32_16x16x32_bf16 a[220:223], a[60:63], a[28:31], 0
v_mul_f32_e32 v94, s54, v94
v_mul_f32_e32 v95, s54, v95
v_mfma_f32_16x16x32_bf16 a[216:219], a[56:59], a[28:31], 0
Cv 94,94,95
v_accvgpr_write_b32 a127, v94
Mz 220,52,28
Mz 216,48,28
buffer_load_dwordx4 v14, s[8:11], s39 offen offset:2048 lds
Mz 156,44,28
Mz 152,40,28
Mz 64,32,16
s_mov_b32 s24, s80
s_mov_b32 s25, s81
Mz 68,36,16
buffer_store_dwordx4 a[88:91], v3, s[20:23], s51 offen offset:2048 sc0 sc1
Mz 72,32,20
s_mov_b32 s26, 0x380000
s_mov_b32 s27, 0x20000
Mz 76,36,20
Mz 80,32,24
Mz 84,36,24
buffer_load_dwordx4 v15, s[8:11], s39 offen offset:3072 lds
Mz 88,32,28
Mz 92,36,28
s_waitcnt vmcnt(15) lgkmcnt(0)
s_barrier
Mv 32,160,128
Rt 0,18,32768
Mv 36,164,128
s_add_u32 s38, s38, 0xffff0080
Mv 96,168,128
Rt 2,18,34816
s_add_u32 m0, s36, 0
Mv 100,172,128
buffer_load_dwordx4 v8, s[4:7], s38 offen offset:0 lds
Mv 160,176,128
Rt 4,19,32768
Mv 164,180,128
Mv 224,184,128
Rt 6,19,34816
Mv 228,188,128
buffer_load_dwordx4 v9, s[4:7], s38 offen offset:1024 lds
Mv 236,188,132
Rt 8,20,32768
Mv 232,184,132
Mv 172,180,132
Rt 10,20,34816
Mv 168,176,132
buffer_load_dwordx4 v10, s[4:7], s38 offen offset:2048 lds
Mv 108,172,132
Rt 12,21,32768
Mv 104,168,132
)" R"(
Mv 44,164,132
Rt 14,21,34816
Mv 40,160,132
buffer_load_dwordx4 v11, s[4:7], s38 offen offset:3072 lds
Mv 48,160,136
Rt 16,22,32768
Mv 52,164,136
s_add_u32 s38, s38, 0x10000
Mv 112,168,136
Rt 18,22,34816
s_add_u32 m0, s36, 4096
Mv 116,172,136
buffer_load_dwordx4 v8, s[4:7], s38 offen offset:0 lds
Mv 176,176,136
Rt 20,23,32768
Mv 180,180,136
Mv 240,184,136
Rt 22,23,34816
Mv 244,188,136
buffer_load_dwordx4 v9, s[4:7], s38 offen offset:1024 lds
Mv 252,188,140
Rt 24,24,32768
Mv 248,184,140
Mv 188,180,140
Rt 26,24,34816
Mv 184,176,140
buffer_load_dwordx4 v10, s[4:7], s38 offen offset:2048 lds
Mv 124,172,140
Rt 28,25,32768
Mv 120,168,140
Mv 60,164,140
Rt 30,25,34816
Mv 56,160,140
buffer_load_dwordx4 v11, s[4:7], s38 offen offset:3072 lds
Mv 64,160,144
Rb 32,16,32768
Mv 68,164,144
Mv 128,168,144
Rb 36,16,34816
Mv 132,172,144
Mv 192,176,144
Rb 40,16,40960
Mv 196,180,144
buffer_load_dword v27, v26, s[24:27], s90 offen
Ma 192,184,144
Rb 44,16,43008
s_add_u32 s28, s90, 0x200000
Ma 196,188,144
Ma 204,188,148
Rb 48,16,49152
Ma 200,184,148
buffer_store_dwordx4 a[92:95], v3, s[20:23], s51 offen offset:3072 sc0 sc1
Mv 204,180,148
Rb 52,16,51200
Mv 200,176,148
Mv 140,172,148
Rb 56,16,57344
Mv 136,168,148
Mv 76,164,148
Rb 60,16,59392
Mv 72,160,148
Mv 80,160,152
Mv 84,164,152
s_add_u32 s51, s51, 0x10000
)" R"(
buffer_store_dwordx4 a[96:99], v2, s[20:23], s51 offen offset:0 sc0 sc1
Mv 144,168,152
Mv 148,172,152
Mv 208,176,152
Mv 212,180,152
Ma 208,184,152
Ma 212,188,152
Ma 220,188,156
Ma 216,184,156
buffer_store_dwordx4 a[100:103], v2, s[20:23], s51 offen offset:1024 sc0 sc1
Mv 220,180,156
Mv 216,176,156
Mv 156,172,156
Mv 152,168,156
Mv 92,164,156
Mv 88,160,156
s_waitcnt vmcnt(20) lgkmcnt(0)
s_barrier
Mv 32,32,0
Rt 128,18,49152
Mv 36,36,0
Mv 96,40,0
Rt 130,18,51200
Mv 100,44,0
s_add_u32 s39, s39, 0x40000
Mv 160,48,0
Rt 132,19,49152
s_add_u32 m0, s37, 16384
Mv 164,52,0
buffer_load_dwordx4 v12, s[8:11], s39 offen offset:0 lds
Mv 224,56,0
Rt 134,19,51200
Mv 228,60,0
Mv 236,60,4
Rt 136,20,49152
Mv 232,56,4
buffer_store_dwordx4 a[104:107], v2, s[20:23], s51 offen offset:2048 sc0 sc1
Mv 172,52,4
Rt 138,20,51200
Mv 168,48,4
Mv 108,44,4
Rt 140,21,49152
Mv 104,40,4
buffer_load_dwordx4 v13, s[8:11], s39 offen offset:1024 lds
Mv 44,36,4
Rt 142,21,51200
Mv 40,32,4
Mv 48,32,8
Rt 144,22,49152
Mv 52,36,8
buffer_store_dwordx4 a[108:111], v2, s[20:23], s51 offen offset:3072 sc0 sc1
Mv 112,40,8
Rt 146,22,51200
Mv 116,44,8
Mv 176,48,8
Rt 148,23,49152
Mv 180,52,8
buffer_load_dwordx4 v14, s[8:11], s39 offen offset:2048 lds
Mv 240,56,8
Rt 150,23,51200
Mv 244,60,8
Mv 252,60,12
Rt 152,24,49152
Mv 248,56,12
buffer_store_dwordx4 a[112:115], v3, s[20:23], s51 offen offset:0 sc0 sc1
)" R"(
Mv 188,52,12
Rt 154,24,51200
Mv 184,48,12
Mv 124,44,12
Rt 156,25,49152
Mv 120,40,12
buffer_load_dwordx4 v15, s[8:11], s39 offen offset:3072 lds
Mv 60,36,12
Rt 158,25,51200
Mv 56,32,12
Mv 64,32,16
Rb 160,17,32768
Mv 68,36,16
buffer_store_dwordx4 a[116:119], v3, s[20:23], s51 offen offset:1024 sc0 sc1
Mv 128,40,16
Rb 164,17,34816
Mv 132,44,16
s_add_u32 s39, s39, 0x40000
Mv 192,48,16
Rb 168,17,40960
s_add_u32 m0, s37, 32768
Mv 196,52,16
buffer_load_dwordx4 v12, s[8:11], s39 offen offset:0 lds
Ma 192,56,16
Rb 172,17,43008
Ma 196,60,16
Ma 204,60,20
Rb 176,17,49152
Ma 200,56,20
buffer_store_dwordx4 a[120:123], v3, s[20:23], s51 offen offset:2048 sc0 sc1
Mv 204,52,20
Rb 180,17,51200
Mv 200,48,20
Mv 140,44,20
Rb 184,17,57344
Mv 136,40,20
buffer_load_dwordx4 v13, s[8:11], s39 offen offset:1024 lds
Mv 76,36,20
Rb 188,17,59392
Mv 72,32,20
Mv 80,32,24
Mv 84,36,24
buffer_store_dwordx4 a[124:127], v3, s[20:23], s51 offen offset:3072 sc0 sc1
Mv 144,40,24
Mv 148,44,24
Mv 208,48,24
Mv 212,52,24
buffer_load_dwordx4 v14, s[8:11], s39 offen offset:2048 lds
Ma 208,56,24
Ma 212,60,24
Ma 220,60,28
Ma 216,56,28
Mv 220,52,28
Mv 216,48,28
Mv 156,44,28
Mv 152,40,28
buffer_load_dwordx4 v15, s[8:11], s39 offen offset:3072 lds
Mv 92,36,28
Mv 88,32,28
s_waitcnt vmcnt(18) lgkmcnt(0)
s_barrier
Mv 32,160,128
Rt 0,18,0
Mv 36,164,128
s_add_u32 s38, s38, 0xffff0080
)" R"(
Mv 96,168,128
Rt 2,18,2048
s_add_u32 m0, s36, 32768
Mv 100,172,128
buffer_load_dwordx4 v8, s[4:7], s38 offen offset:0 lds
Mv 160,176,128
Rt 4,19,0
Mv 164,180,128
Mv 224,184,128
Rt 6,19,2048
Mv 228,188,128
buffer_load_dwordx4 v9, s[4:7], s38 offen offset:1024 lds
Mv 236,188,132
Rt 8,20,0
s_mov_b32 s40, 5
Mv 232,184,132
Mv 172,180,132
Rt 10,20,2048
Mv 168,176,132
buffer_load_dwordx4 v10, s[4:7], s38 offen offset:2048 lds
Mv 108,172,132
Rt 12,21,0
Mv 104,168,132
Mv 44,164,132
Rt 14,21,2048
Mv 40,160,132
buffer_load_dwordx4 v11, s[4:7], s38 offen offset:3072 lds
Mv 48,160,136
Rt 16,22,0
Mv 52,164,136
s_add_u32 s38, s38, 0x10000
Mv 112,168,136
Rt 18,22,2048
s_add_u32 m0, s36, 36864
Mv 116,172,136
buffer_load_dwordx4 v8, s[4:7], s38 offen offset:0 lds
Mv 176,176,136
Rt 20,23,0
Mv 180,180,136
Mv 240,184,136
Rt 22,23,2048
Mv 244,188,136
buffer_load_dwordx4 v9, s[4:7], s38 offen offset:1024 lds
Mv 252,188,140
Rt 24,24,0
Mv 248,184,140
Mv 188,180,140
Rt 26,24,2048
Mv 184,176,140
buffer_load_dwordx4 v10, s[4:7], s38 offen offset:2048 lds
Mv 124,172,140
Rt 28,25,0
Mv 120,168,140
Mv 60,164,140
Rt 30,25,2048
Mv 56,160,140
buffer_load_dwordx4 v11, s[4:7], s38 offen offset:3072 lds
Mv 64,160,144
Rb 32,16,0
Mv 68,164,144
Mv 128,168,144
Rb 36,16,2048
Mv 132,172,144
Mv 192,176,144
)" R"(
Rb 40,16,8192
Mv 196,180,144
buffer_load_dword v27, v26, s[24:27], s28 offen
Ma 192,184,144
Rb 44,16,10240
Ma 196,188,144
Ma 204,188,148
Rb 48,16,16384
Ma 200,184,148
Mv 204,180,148
Rb 52,16,18432
Mv 200,176,148
Mv 140,172,148
Rb 56,16,24576
Mv 136,168,148
Mv 76,164,148
Rb 60,16,26624
Mv 72,160,148
Mv 80,160,152
Mv 84,164,152
Mv 144,168,152
Mv 148,172,152
Mv 208,176,152
Mv 212,180,152
Ma 208,184,152
Ma 212,188,152
Ma 220,188,156
Ma 216,184,156
Mv 220,180,156
Mv 216,176,156
Mv 156,172,156
Mv 152,168,156
Mv 92,164,156
Mv 88,160,156
s_waitcnt vmcnt(11) lgkmcnt(0)
s_barrier
buffer_store_dword v6, v7, s[20:23], s52 offen offset:0 sc0 sc1
.LzPBV:
Mv 32,32,0
Rt 128,18,16384
Mv 36,36,0
Mv 96,40,0
Rt 130,18,18432
Mv 100,44,0
s_add_u32 s39, s39, 0x40000
Mv 160,48,0
Rt 132,19,16384
s_add_u32 m0, s37, 49152
Mv 164,52,0
buffer_load_dwordx4 v12, s[8:11], s39 offen offset:0 lds
Mv 224,56,0
Rt 134,19,18432
Mv 228,60,0
Mv 236,60,4
Rt 136,20,16384
Mv 232,56,4
Mv 172,52,4
Rt 138,20,18432
Mv 168,48,4
Mv 108,44,4
Rt 140,21,16384
Mv 104,40,4
buffer_load_dwordx4 v13, s[8:11], s39 offen offset:1024 lds
Mv 44,36,4
Rt 142,21,18432
)" R"(
Mv 40,32,4
Mv 48,32,8
Rt 144,22,16384
Mv 52,36,8
Mv 112,40,8
Rt 146,22,18432
Mv 116,44,8
Mv 176,48,8
Rt 148,23,16384
Mv 180,52,8
buffer_load_dwordx4 v14, s[8:11], s39 offen offset:2048 lds
Mv 240,56,8
Rt 150,23,18432
Mv 244,60,8
Mv 252,60,12
Rt 152,24,16384
Mv 248,56,12
Mv 188,52,12
Rt 154,24,18432
Mv 184,48,12
Mv 124,44,12
Rt 156,25,16384
Mv 120,40,12
buffer_load_dwordx4 v15, s[8:11], s39 offen offset:3072 lds
Mv 60,36,12
Rt 158,25,18432
Mv 56,32,12
Mv 64,32,16
Rb 160,17,0
Mv 68,36,16
Mv 128,40,16
Rb 164,17,2048
Mv 132,44,16
s_add_u32 s39, s39, 0x40000
Mv 192,48,16
Rb 168,17,8192
s_add_u32 m0, s37, 0
Mv 196,52,16
buffer_load_dwordx4 v12, s[8:11], s39 offen offset:0 lds
Ma 192,56,16
Rb 172,17,10240
Ma 196,60,16
Ma 204,60,20
Rb 176,17,16384
Ma 200,56,20
Mv 204,52,20
Rb 180,17,18432
Mv 200,48,20
Mv 140,44,20
Rb 184,17,24576
Mv 136,40,20
buffer_load_dwordx4 v13, s[8:11], s39 offen offset:1024 lds
Mv 76,36,20
Rb 188,17,26624
Mv 72,32,20
Mv 80,32,24
Mv 84,36,24
Mv 144,40,24
Mv 148,44,24
Mv 208,48,24
Mv 212,52,24
buffer_load_dwordx4 v14, s[8:11], s39 offen offset:2048 lds
Ma 208,56,24
Ma 212,60,24
)" R"(
Ma 220,60,28
Ma 216,56,28
Mv 220,52,28
Mv 216,48,28
Mv 156,44,28
Mv 152,40,28
buffer_load_dwordx4 v15, s[8:11], s39 offen offset:3072 lds
Mv 92,36,28
Mv 88,32,28
s_waitcnt vmcnt(8) lgkmcnt(0)
s_barrier
Mv 32,160,128
Rt 0,18,32768
Mv 36,164,128
s_add_u32 s38, s38, 0xffff0080
Mv 96,168,128
Rt 2,18,34816
s_add_u32 m0, s36, 0
Mv 100,172,128
buffer_load_dwordx4 v8, s[4:7], s38 offen offset:0 lds
Mv 160,176,128
Rt 4,19,32768
Mv 164,180,128
Mv 224,184,128
Rt 6,19,34816
Mv 228,188,128
buffer_load_dwordx4 v9, s[4:7], s38 offen offset:1024 lds
Mv 236,188,132
Rt 8,20,32768
Mv 232,184,132
Mv 172,180,132
Rt 10,20,34816
Mv 168,176,132
buffer_load_dwordx4 v10, s[4:7], s38 offen offset:2048 lds
Mv 108,172,132
Rt 12,21,32768
Mv 104,168,132
Mv 44,164,132
Rt 14,21,34816
Mv 40,160,132
buffer_load_dwordx4 v11, s[4:7], s38 offen offset:3072 lds
Mv 48,160,136
Rt 16,22,32768
Mv 52,164,136
s_add_u32 s38, s38, 0x10000
Mv 112,168,136
Rt 18,22,34816
s_add_u32 m0, s36, 4096
Mv 116,172,136
buffer_load_dwordx4 v8, s[4:7], s38 offen offset:0 lds
Mv 176,176,136
Rt 20,23,32768
Mv 180,180,136
Mv 240,184,136
Rt 22,23,34816
Mv 244,188,136
buffer_load_dwordx4 v9, s[4:7], s38 offen offset:1024 lds
Mv 252,188,140
Rt 24,24,32768
Mv 248,184,140
Mv 188,180,140
Rt 26,24,34816
Mv 184,176,140
buffer_load_dwordx4 v10, s[4:7], s38 offen offset:2048 lds
Mv 124,172,140
)" R"(
Rt 28,25,32768
Mv 120,168,140
Mv 60,164,140
Rt 30,25,34816
Mv 56,160,140
buffer_load_dwordx4 v11, s[4:7], s38 offen offset:3072 lds
Mv 64,160,144
Rb 32,16,32768
Mv 68,164,144
Mv 128,168,144
Rb 36,16,34816
Mv 132,172,144
Mv 192,176,144
Rb 40,16,40960
Mv 196,180,144
Ma 192,184,144
Rb 44,16,43008
Ma 196,188,144
Ma 204,188,148
Rb 48,16,49152
Ma 200,184,148
Mv 204,180,148
Rb 52,16,51200
Mv 200,176,148
Mv 140,172,148
Rb 56,16,57344
Mv 136,168,148
Mv 76,164,148
Rb 60,16,59392
Mv 72,160,148
Mv 80,160,152
Mv 84,164,152
Mv 144,168,152
Mv 148,172,152
Mv 208,176,152
Mv 212,180,152
Ma 208,184,152
Ma 212,188,152
Ma 220,188,156
Ma 216,184,156
Mv 220,180,156
Mv 216,176,156
Mv 156,172,156
Mv 152,168,156
Mv 92,164,156
Mv 88,160,156
s_waitcnt vmcnt(12) lgkmcnt(0)
s_barrier
Mv 32,32,0
Rt 128,18,49152
Mv 36,36,0
Mv 96,40,0
Rt 130,18,51200
Mv 100,44,0
s_add_u32 s39, s39, 0x40000
Mv 160,48,0
Rt 132,19,49152
s_add_u32 m0, s37, 16384
Mv 164,52,0
buffer_load_dwordx4 v12, s[8:11], s39 offen offset:0 lds
Mv 224,56,0
Rt 134,19,51200
Mv 228,60,0
Mv 236,60,4
Rt 136,20,49152
)" R"(
Mv 232,56,4
Mv 172,52,4
Rt 138,20,51200
Mv 168,48,4
Mv 108,44,4
Rt 140,21,49152
Mv 104,40,4
buffer_load_dwordx4 v13, s[8:11], s39 offen offset:1024 lds
Mv 44,36,4
Rt 142,21,51200
Mv 40,32,4
Mv 48,32,8
Rt 144,22,49152
Mv 52,36,8
Mv 112,40,8
Rt 146,22,51200
Mv 116,44,8
Mv 176,48,8
Rt 148,23,49152
Mv 180,52,8
buffer_load_dwordx4 v14, s[8:11], s39 offen offset:2048 lds
Mv 240,56,8
Rt 150,23,51200
Mv 244,60,8
Mv 252,60,12
Rt 152,24,49152
Mv 248,56,12
Mv 188,52,12
Rt 154,24,51200
Mv 184,48,12
Mv 124,44,12
Rt 156,25,49152
Mv 120,40,12
buffer_load_dwordx4 v15, s[8:11], s39 offen offset:3072 lds
Mv 60,36,12
Rt 158,25,51200
Mv 56,32,12
Mv 64,32,16
Rb 160,17,32768
Mv 68,36,16
Mv 128,40,16
Rb 164,17,34816
Mv 132,44,16
s_add_u32 s39, s39, 0x40000
Mv 192,48,16
Rb 168,17,40960
s_add_u32 m0, s37, 32768
Mv 196,52,16
buffer_load_dwordx4 v12, s[8:11], s39 offen offset:0 lds
Ma 192,56,16
Rb 172,17,43008
Ma 196,60,16
Ma 204,60,20
Rb 176,17,49152
Ma 200,56,20
Mv 204,52,20
Rb 180,17,51200
Mv 200,48,20
Mv 140,44,20
Rb 184,17,57344
Mv 136,40,20
buffer_load_dwordx4 v13, s[8:11], s39 offen offset:1024 lds
Mv 76,36,20
Rb 188,17,59392
)" R"(
Mv 72,32,20
Mv 80,32,24
Mv 84,36,24
Mv 144,40,24
Mv 148,44,24
Mv 208,48,24
Mv 212,52,24
buffer_load_dwordx4 v14, s[8:11], s39 offen offset:2048 lds
Ma 208,56,24
Ma 212,60,24
Ma 220,60,28
Ma 216,56,28
Mv 220,52,28
Mv 216,48,28
Mv 156,44,28
Mv 152,40,28
buffer_load_dwordx4 v15, s[8:11], s39 offen offset:3072 lds
Mv 92,36,28
Mv 88,32,28
s_waitcnt vmcnt(8) lgkmcnt(0)
s_barrier
Mv 32,160,128
Rt 0,18,0
Mv 36,164,128
s_add_u32 s38, s38, 0xffff0080
Mv 96,168,128
Rt 2,18,2048
s_add_u32 m0, s36, 32768
Mv 100,172,128
buffer_load_dwordx4 v8, s[4:7], s38 offen offset:0 lds
Mv 160,176,128
Rt 4,19,0
Mv 164,180,128
Mv 224,184,128
Rt 6,19,2048
Mv 228,188,128
buffer_load_dwordx4 v9, s[4:7], s38 offen offset:1024 lds
Mv 236,188,132
Rt 8,20,0
Mv 232,184,132
Mv 172,180,132
Rt 10,20,2048
Mv 168,176,132
buffer_load_dwordx4 v10, s[4:7], s38 offen offset:2048 lds
Mv 108,172,132
Rt 12,21,0
Mv 104,168,132
Mv 44,164,132
Rt 14,21,2048
Mv 40,160,132
buffer_load_dwordx4 v11, s[4:7], s38 offen offset:3072 lds
Mv 48,160,136
Rt 16,22,0
Mv 52,164,136
s_add_u32 s38, s38, 0x10000
Mv 112,168,136
Rt 18,22,2048
s_add_u32 m0, s36, 36864
Mv 116,172,136
buffer_load_dwordx4 v8, s[4:7], s38 offen offset:0 lds
Mv 176,176,136
Rt 20,23,0
Mv 180,180,136
Mv 240,184,136
Rt 22,23,2048
)" R"(
Mv 244,188,136
buffer_load_dwordx4 v9, s[4:7], s38 offen offset:1024 lds
Mv 252,188,140
Rt 24,24,0
Mv 248,184,140
Mv 188,180,140
Rt 26,24,2048
Mv 184,176,140
buffer_load_dwordx4 v10, s[4:7], s38 offen offset:2048 lds
Mv 124,172,140
Rt 28,25,0
Mv 120,168,140
Mv 60,164,140
Rt 30,25,2048
Mv 56,160,140
buffer_load_dwordx4 v11, s[4:7], s38 offen offset:3072 lds
Mv 64,160,144
Rb 32,16,0
Mv 68,164,144
Mv 128,168,144
Rb 36,16,2048
Mv 132,172,144
Mv 192,176,144
Rb 40,16,8192
Mv 196,180,144
Ma 192,184,144
Rb 44,16,10240
Ma 196,188,144
Ma 204,188,148
Rb 48,16,16384
Ma 200,184,148
Mv 204,180,148
Rb 52,16,18432
Mv 200,176,148
Mv 140,172,148
Rb 56,16,24576
Mv 136,168,148
Mv 76,164,148
Rb 60,16,26624
Mv 72,160,148
Mv 80,160,152
Mv 84,164,152
Mv 144,168,152
Mv 148,172,152
Mv 208,176,152
Mv 212,180,152
Ma 208,184,152
Ma 212,188,152
Ma 220,188,156
Ma 216,184,156
Mv 220,180,156
s_sub_u32 s40, s40, 1
Mv 216,176,156
Mv 156,172,156
Mv 152,168,156
Mv 92,164,156
Mv 88,160,156
s_cmp_lg_u32 s40, 0
s_waitcnt vmcnt(12) lgkmcnt(0)
s_barrier
s_cbranch_scc1 .LzPBV
s_branch .LzPBVX
.LzWIN:
.LzPAKX:
Mv 32,0,32
)" R"(
Rt 64,18,16384
Mv 64,0,36
Rb 192,28,32768
Mv 96,0,40
Rt 66,18,18432
Mv 128,0,44
s_add_u32 s39, s39, 0x40000
Rb 196,28,34816
Mv 160,0,48
Rt 68,19,16384
s_add_u32 m0, s37, 49152
Mv 192,0,52
buffer_load_dwordx4 v12, s[8:11], s39 offen offset:0 lds
Mv 224,0,56
Rt 70,19,18432
Ma 224,0,60
Rb 200,29,32768
Ma 228,4,60
Rt 72,20,16384
Mv 228,4,56
Rb 204,29,34816
Mv 196,4,52
Rt 74,20,18432
Mv 164,4,48
Rb 208,28,36864
Mv 132,4,44
Rt 76,21,16384
Mv 100,4,40
buffer_load_dwordx4 v13, s[8:11], s39 offen offset:1024 lds
Mv 68,4,36
Rt 78,21,18432
Mv 36,4,32
Rb 212,28,38912
Mv 40,8,32
Rt 80,22,16384
Mv 72,8,36
Rb 216,29,36864
Mv 104,8,40
Rt 82,22,18432
Mv 136,8,44
Rb 220,29,38912
Mv 168,8,48
Rt 84,23,16384
Mv 200,8,52
buffer_load_dwordx4 v14, s[8:11], s39 offen offset:2048 lds
Mv 232,8,56
Rt 86,23,18432
Ma 232,8,60
Ma 236,12,60
Rt 88,24,16384
Mv 236,12,56
Mv 204,12,52
Rt 90,24,18432
Mv 172,12,48
Mv 140,12,44
Rt 92,25,16384
Mv 108,12,40
buffer_load_dwordx4 v15, s[8:11], s39 offen offset:3072 lds
Mv 76,12,36
Rt 94,25,18432
Mv 44,12,32
Mv 48,16,32
Rb 96,17,0
Mv 80,16,36
)" R"(
Mv 112,16,40
Rb 100,17,2048
Mv 144,16,44
s_add_u32 s39, s39, 0x40000
Mv 176,16,48
Rb 104,17,8192
s_add_u32 m0, s37, 0
Mv 208,16,52
buffer_load_dwordx4 v12, s[8:11], s39 offen offset:0 lds
Mv 240,16,56
Rb 108,17,10240
Ma 240,16,60
Ma 244,20,60
Rb 112,17,16384
Mv 244,20,56
Mv 212,20,52
Rb 116,17,18432
Mv 180,20,48
Mv 148,20,44
Rb 120,17,24576
Mv 116,20,40
buffer_load_dwordx4 v13, s[8:11], s39 offen offset:1024 lds
Mv 84,20,36
Rb 124,17,26624
Mv 52,20,32
Mv 56,24,32
Mv 88,24,36
Mv 120,24,40
Mv 152,24,44
Mv 184,24,48
Mv 216,24,52
buffer_load_dwordx4 v14, s[8:11], s39 offen offset:2048 lds
Mv 248,24,56
Ma 248,24,60
Ma 252,28,60
Mv 252,28,56
Mv 220,28,52
Mv 188,28,48
Mv 156,28,44
Mv 124,28,40
buffer_load_dwordx4 v15, s[8:11], s39 offen offset:3072 lds
Mv 92,28,36
Mv 60,28,32
s_waitcnt vmcnt(9) lgkmcnt(0)
s_barrier
s_add_u32 m0, s36, 131072
s_add_u32 s44, s43, 256
buffer_load_dwordx4 v26, s[12:15], s44 offen offset:0 lds
buffer_load_dwordx4 v27, s[12:15], s44 offen offset:1024 lds
s_add_u32 s44, s43, 12544
buffer_load_dwordx4 v26, s[12:15], s44 offen offset:2048 lds
buffer_load_dwordx4 v27, s[12:15], s44 offen offset:3072 lds
Mv 32,64,96
Rt 0,18,32768
Mv 64,64,100
s_add_u32 s38, s38, 0xffff0080
Mv 96,64,104
Rt 2,18,34816
s_add_u32 m0, s36, 0
Mv 128,64,108
buffer_load_dwordx4 v8, s[4:7], s38 offen offset:0 lds
Mv 160,64,112
Rt 4,19,32768
Mv 192,64,116
Mv 224,64,120
)" R"(
Rt 6,19,34816
Ma 224,64,124
buffer_load_dwordx4 v9, s[4:7], s38 offen offset:1024 lds
Ma 228,68,124
Rt 8,20,32768
Mv 228,68,120
Mv 196,68,116
Rt 10,20,34816
Mv 164,68,112
buffer_load_dwordx4 v10, s[4:7], s38 offen offset:2048 lds
Mv 132,68,108
Rt 12,21,32768
Mv 100,68,104
Mv 68,68,100
Rt 14,21,34816
Mv 36,68,96
buffer_load_dwordx4 v11, s[4:7], s38 offen offset:3072 lds
Mv 40,72,96
Rt 16,22,32768
Mv 72,72,100
s_add_u32 s38, s38, 0x10000
Mv 104,72,104
Rt 18,22,34816
s_add_u32 m0, s36, 4096
Mv 136,72,108
buffer_load_dwordx4 v8, s[4:7], s38 offen offset:0 lds
Mv 168,72,112
Rt 20,23,32768
Mv 200,72,116
Mv 232,72,120
Rt 22,23,34816
Ma 232,72,124
buffer_load_dwordx4 v9, s[4:7], s38 offen offset:1024 lds
Ma 236,76,124
Rt 24,24,32768
Mv 236,76,120
Mv 204,76,116
Rt 26,24,34816
Mv 172,76,112
buffer_load_dwordx4 v10, s[4:7], s38 offen offset:2048 lds
Mv 140,76,108
Rt 28,25,32768
Mv 108,76,104
Mv 76,76,100
Rt 30,25,34816
Mv 44,76,96
buffer_load_dwordx4 v11, s[4:7], s38 offen offset:3072 lds
Mv 48,80,96
Rb 32,16,32768
Mv 80,80,100
Mv 112,80,104
Rb 36,16,34816
Mv 144,80,108
s_add_u32 s44, s43, 384
Mv 176,80,112
Rb 40,16,40960
s_add_u32 m0, s36, 135168
Mv 208,80,116
buffer_load_dwordx4 v26, s[12:15], s44 offen offset:0 lds
Mv 240,80,120
Rb 44,16,43008
Ma 240,80,124
Ma 244,84,124
Rb 48,16,49152
)" R"(
Mv 244,84,120
buffer_load_dwordx4 v27, s[12:15], s44 offen offset:1024 lds
Mv 212,84,116
Rb 52,16,51200
Mv 180,84,112
Mv 148,84,108
Rb 56,16,57344
Mv 116,84,104
s_add_u32 s44, s43, 12672
buffer_load_dwordx4 v26, s[12:15], s44 offen offset:2048 lds
Mv 84,84,100
Rb 60,16,59392
Mv 52,84,96
Mv 56,88,96
Mv 88,88,100
buffer_load_dwordx4 v27, s[12:15], s44 offen offset:3072 lds
Mv 120,88,104
Mv 152,88,108
Mv 184,88,112
Mv 216,88,116
Mv 248,88,120
Ma 248,88,124
Ma 252,92,124
Mv 252,92,120
Mv 220,92,116
Mv 188,92,112
Mv 156,92,108
Mv 124,92,104
Mv 92,92,100
Mv 60,92,96
s_waitcnt vmcnt(0) lgkmcnt(0)
s_barrier
Mv 32,0,32
Rt 64,18,49152
Mv 64,0,36
Rb 128,28,32768
Mv 96,0,40
Rt 66,18,51200
Mv 128,0,44
s_add_u32 s39, s39, 0x40000
Rb 132,28,34816
Mv 160,0,48
Rt 68,19,49152
s_add_u32 m0, s37, 16384
Mv 192,0,52
buffer_load_dwordx4 v12, s[8:11], s39 offen offset:0 lds
Mv 224,0,56
Rt 70,19,51200
Ma 224,0,60
Rb 136,29,32768
Ma 228,4,60
Rt 72,20,49152
Mv 228,4,56
Rb 140,29,34816
Mv 196,4,52
Rt 74,20,51200
Mv 164,4,48
Rb 144,28,36864
Mv 132,4,44
Rt 76,21,49152
Mv 100,4,40
buffer_load_dwordx4 v13, s[8:11], s39 offen offset:1024 lds
Mv 68,4,36
Rt 78,21,51200
Mv 36,4,32
)" R"(
Rb 148,28,38912
Mv 40,8,32
Rt 80,22,49152
Mv 72,8,36
Rb 152,29,36864
Mv 104,8,40
Rt 82,22,51200
Mv 136,8,44
Rb 156,29,38912
Mv 168,8,48
Rt 84,23,49152
Mv 200,8,52
buffer_load_dwordx4 v14, s[8:11], s39 offen offset:2048 lds
Mv 232,8,56
Rt 86,23,51200
Ma 232,8,60
Ma 236,12,60
Rt 88,24,49152
Mv 236,12,56
Mv 204,12,52
Rt 90,24,51200
Mv 172,12,48
Mv 140,12,44
Rt 92,25,49152
Mv 108,12,40
buffer_load_dwordx4 v15, s[8:11], s39 offen offset:3072 lds
Mv 76,12,36
Rt 94,25,51200
Mv 44,12,32
Mv 48,16,32
Rb 96,17,32768
Mv 80,16,36
Mv 112,16,40
Rb 100,17,34816
Mv 144,16,44
s_add_u32 s39, s39, 0x40000
Mv 176,16,48
Rb 104,17,40960
s_add_u32 m0, s37, 32768
Mv 208,16,52
buffer_load_dwordx4 v12, s[8:11], s39 offen offset:0 lds
Mv 240,16,56
Rb 108,17,43008
Ma 240,16,60
Ma 244,20,60
Rb 112,17,49152
Mv 244,20,56
Mv 212,20,52
Rb 116,17,51200
Mv 180,20,48
Mv 148,20,44
Rb 120,17,57344
Mv 116,20,40
buffer_load_dwordx4 v13, s[8:11], s39 offen offset:1024 lds
Mv 84,20,36
Rb 124,17,59392
Mv 52,20,32
Mv 56,24,32
Mv 88,24,36
Mv 120,24,40
Mv 152,24,44
Mv 184,24,48
Mv 216,24,52
buffer_load_dwordx4 v14, s[8:11], s39 offen offset:2048 lds
)" R"(
Mv 248,24,56
Ma 248,24,60
Ma 252,28,60
Mv 252,28,56
Mv 220,28,52
Mv 188,28,48
Mv 156,28,44
Mv 124,28,40
buffer_load_dwordx4 v15, s[8:11], s39 offen offset:3072 lds
Mv 92,28,36
Mv 60,28,32
s_waitcnt lgkmcnt(0)
s_barrier
s_add_u32 m0, s36, 131072
s_add_u32 s44, s43, 512
buffer_load_dwordx4 v26, s[12:15], s44 offen offset:0 lds
buffer_load_dwordx4 v27, s[12:15], s44 offen offset:1024 lds
s_add_u32 s44, s43, 12800
buffer_load_dwordx4 v26, s[12:15], s44 offen offset:2048 lds
buffer_load_dwordx4 v27, s[12:15], s44 offen offset:3072 lds
Mv 32,64,96
Rt 0,18,0
Mv 64,64,100
s_add_u32 s38, s38, 0xffff0080
Mv 96,64,104
Rt 2,18,2048
s_add_u32 m0, s36, 32768
Mv 128,64,108
buffer_load_dwordx4 v8, s[4:7], s38 offen offset:0 lds
Mv 160,64,112
Rt 4,19,0
Mv 192,64,116
Mv 224,64,120
Rt 6,19,2048
Ma 224,64,124
buffer_load_dwordx4 v9, s[4:7], s38 offen offset:1024 lds
Ma 228,68,124
Rt 8,20,0
Mv 228,68,120
Mv 196,68,116
Rt 10,20,2048
Mv 164,68,112
buffer_load_dwordx4 v10, s[4:7], s38 offen offset:2048 lds
Mv 132,68,108
Rt 12,21,0
Mv 100,68,104
Mv 68,68,100
Rt 14,21,2048
Mv 36,68,96
buffer_load_dwordx4 v11, s[4:7], s38 offen offset:3072 lds
Mv 40,72,96
Rt 16,22,0
Mv 72,72,100
s_add_u32 s38, s38, 0x10000
Mv 104,72,104
Rt 18,22,2048
s_add_u32 m0, s36, 36864
Mv 136,72,108
buffer_load_dwordx4 v8, s[4:7], s38 offen offset:0 lds
Mv 168,72,112
Rt 20,23,0
Mv 200,72,116
Mv 232,72,120
Rt 22,23,2048
Ma 232,72,124
)" R"(
buffer_load_dwordx4 v9, s[4:7], s38 offen offset:1024 lds
Ma 236,76,124
Rt 24,24,0
Mv 236,76,120
Mv 204,76,116
Rt 26,24,2048
Mv 172,76,112
buffer_load_dwordx4 v10, s[4:7], s38 offen offset:2048 lds
Mv 140,76,108
Rt 28,25,0
Mv 108,76,104
Mv 76,76,100
Rt 30,25,2048
Mv 44,76,96
buffer_load_dwordx4 v11, s[4:7], s38 offen offset:3072 lds
Mv 48,80,96
Rb 32,16,0
Mv 80,80,100
Mv 112,80,104
Rb 36,16,2048
Mv 144,80,108
s_add_u32 s44, s43, 640
Mv 176,80,112
Rb 40,16,8192
s_add_u32 m0, s36, 135168
Mv 208,80,116
buffer_load_dwordx4 v26, s[12:15], s44 offen offset:0 lds
Mv 240,80,120
Rb 44,16,10240
Ma 240,80,124
Ma 244,84,124
Rb 48,16,16384
Mv 244,84,120
buffer_load_dwordx4 v27, s[12:15], s44 offen offset:1024 lds
Mv 212,84,116
Rb 52,16,18432
Mv 180,84,112
Mv 148,84,108
Rb 56,16,24576
Mv 116,84,104
s_add_u32 s44, s43, 12928
buffer_load_dwordx4 v26, s[12:15], s44 offen offset:2048 lds
Mv 84,84,100
Rb 60,16,26624
Mv 52,84,96
Mv 56,88,96
Mv 88,88,100
buffer_load_dwordx4 v27, s[12:15], s44 offen offset:3072 lds
Mv 120,88,104
Mv 152,88,108
Mv 184,88,112
Mv 216,88,116
Mv 248,88,120
Ma 248,88,124
Ma 252,92,124
Mv 252,92,120
Mv 220,92,116
Mv 188,92,112
Mv 156,92,108
Mv 124,92,104
Mv 92,92,100
Mv 60,92,96
s_waitcnt vmcnt(0) lgkmcnt(0)
s_barrier
Mv 32,0,32
)" R"(
Rt 64,18,16384
Mv 64,0,36
Rb 160,28,32768
v_lshrrev_b32 v5, 2, v0
Mv 96,0,40
Rt 66,18,18432
v_and_b32 v5, 8, v5
Mv 128,0,44
s_add_u32 s39, s39, 0x40000
Rb 164,28,34816
Mv 160,0,48
Rt 68,19,16384
s_add_u32 m0, s37, 49152
Mv 192,0,52
buffer_load_dwordx4 v12, s[8:11], s39 offen offset:0 lds
Mv 224,0,56
Rt 70,19,18432
v_and_or_b32 v5, v0, 7, v5
Ma 224,0,60
Rb 168,29,32768
v_lshlrev_b32 v5, 8, v5
Ma 228,4,60
Rt 72,20,16384
v_and_or_b32 v5, v0, 24, v5
Mv 228,4,56
Rb 172,29,34816
v_lshrrev_b32 v5, 1, v5
Mv 196,4,52
Rt 74,20,18432
v_mov_b32 v6, s3
Mv 164,4,48
Rb 176,28,36864
Mv 132,4,44
Rt 76,21,16384
Mv 100,4,40
buffer_load_dwordx4 v13, s[8:11], s39 offen offset:1024 lds
Mv 68,4,36
Rt 78,21,18432
Mv 36,4,32
Rb 180,28,38912
Mv 40,8,32
Rt 80,22,16384
Mv 72,8,36
Rb 184,29,36864
Mv 104,8,40
Rt 82,22,18432
Mv 136,8,44
Rb 188,29,38912
Mv 168,8,48
Rt 84,23,16384
Mv 200,8,52
buffer_load_dwordx4 v14, s[8:11], s39 offen offset:2048 lds
Mv 232,8,56
Rt 86,23,18432
Ma 232,8,60
Ma 236,12,60
Rt 88,24,16384
Mv 236,12,56
Mv 204,12,52
Rt 90,24,18432
Mv 172,12,48
Mv 140,12,44
Rt 92,25,16384
Mv 108,12,40
)" R"(
buffer_load_dwordx4 v15, s[8:11], s39 offen offset:3072 lds
Mv 76,12,36
Rt 94,25,18432
v_lshrrev_b32 v10, 3, v0
Mv 44,12,32
v_and_b32 v11, 0x70, v1
v_lshl_or_b32 v10, v10, 13, v11
Mv 48,16,32
Rb 96,17,0
v_lshlrev_b32 v11, 1, v0
Mv 80,16,36
v_and_b32 v11, 32, v11
v_xor_b32 v10, v10, v11
Mv 112,16,40
Rb 100,17,2048
s_and_b32 s28, s33, 1
Mv 144,16,44
s_lshl_b32 s28, s28, 6
v_xor_b32 v8, s28, v10
Mv 176,16,48
Rb 104,17,8192
v_xor_b32 v9, 64, v8
Mv 208,16,52
v_add_u32 v9, 0xfc00, v9
v_add_u32 v10, 0x1f800, v8
Mv 240,16,56
Rb 108,17,10240
v_add_u32 v11, 0x1f800, v9
Ma 240,16,60
Ma 244,20,60
Rb 112,17,16384
Mv 244,20,56
Mv 212,20,52
Rb 116,17,18432
Mv 180,20,48
Mv 148,20,44
Rb 120,17,24576
Mv 116,20,40
Mv 84,20,36
Rb 124,17,26624
Mv 52,20,32
Mv 56,24,32
Mv 88,24,36
Mv 120,24,40
Mv 152,24,44
Mv 184,24,48
Mv 216,24,52
Mv 248,24,56
Ma 248,24,60
Ma 252,28,60
Mv 252,28,56
Mv 220,28,52
Mv 188,28,48
Mv 156,28,44
Mv 124,28,40
Mv 92,28,36
Mv 60,28,32
s_waitcnt lgkmcnt(0)
s_barrier
Mv 32,64,96
Rt 0,18,32768
Mv 64,64,100
s_add_u32 s92, s42, 0x0
Mv 96,64,104
Rt 2,18,34816
)" R"(
s_add_u32 m0, s41, 131072
Mv 128,64,108
buffer_load_dwordx4 v8, s[16:19], s92 offen offset:0 lds
Mv 160,64,112
Rt 4,19,32768
Mv 192,64,116
Mv 224,64,120
Rt 6,19,34816
Ma 224,64,124
buffer_load_dwordx4 v9, s[16:19], s92 offen offset:1024 lds
Ma 228,68,124
Rt 8,20,32768
Mv 228,68,120
Mv 196,68,116
Rt 10,20,34816
Mv 164,68,112
buffer_load_dwordx4 v10, s[16:19], s92 offen offset:2048 lds
Mv 132,68,108
Rt 12,21,32768
Mv 100,68,104
Mv 68,68,100
Rt 14,21,34816
Mv 36,68,96
buffer_load_dwordx4 v11, s[16:19], s92 offen offset:3072 lds
Mv 40,72,96
Rt 16,22,32768
Mv 72,72,100
s_add_u32 s92, s42, 0x40000
Mv 104,72,104
Rt 18,22,34816
s_add_u32 m0, s41, 135168
Mv 136,72,108
buffer_load_dwordx4 v8, s[16:19], s92 offen offset:0 lds
Mv 168,72,112
Rt 20,23,32768
Mv 200,72,116
Mv 232,72,120
Rt 22,23,34816
Ma 232,72,124
buffer_load_dwordx4 v9, s[16:19], s92 offen offset:1024 lds
Ma 236,76,124
Rt 24,24,32768
Mv 236,76,120
Mv 204,76,116
Rt 26,24,34816
Mv 172,76,112
buffer_load_dwordx4 v10, s[16:19], s92 offen offset:2048 lds
Mv 140,76,108
Rt 28,25,32768
Mv 108,76,104
s_add_u32 s92, s42, 0x1c0000
Mv 76,76,100
Rt 30,25,34816
s_add_u32 m0, s41, 0
Mv 44,76,96
buffer_load_dwordx4 v8, s[16:19], s92 offen offset:0 lds
Mv 48,80,96
Rb 32,16,32768
Mv 80,80,100
Mv 112,80,104
Rb 36,16,34816
Mv 144,80,108
buffer_load_dwordx4 v9, s[16:19], s92 offen offset:1024 lds
Mv 176,80,112
)" R"(
Rb 40,16,40960
Mv 208,80,116
Mv 240,80,120
Rb 44,16,43008
Ma 240,80,124
buffer_load_dwordx4 v10, s[16:19], s92 offen offset:2048 lds
Ma 244,84,124
Rb 48,16,49152
Mv 244,84,120
Mv 212,84,116
Rb 52,16,51200
Mv 180,84,112
buffer_load_dwordx4 v11, s[16:19], s92 offen offset:3072 lds
Mv 148,84,108
Rb 56,16,57344
Mv 116,84,104
s_add_u32 s92, s42, 0x200000
Mv 84,84,100
Rb 60,16,59392
s_add_u32 m0, s41, 4096
Mv 52,84,96
buffer_load_dwordx4 v8, s[16:19], s92 offen offset:0 lds
Mv 56,88,96
Mv 88,88,100
Mv 120,88,104
Mv 152,88,108
buffer_load_dwordx4 v9, s[16:19], s92 offen offset:1024 lds
Mv 184,88,112
Mv 216,88,116
Mv 248,88,120
Ma 248,88,124
buffer_load_dwordx4 v10, s[16:19], s92 offen offset:2048 lds
Ma 252,92,124
Mv 252,92,120
Mv 220,92,116
Mv 188,92,112
Mv 156,92,108
Mv 124,92,104
Mv 92,92,100
Mv 60,92,96
s_waitcnt vmcnt(14) lgkmcnt(4)
s_barrier
Mv 32,0,32
Rt 64,18,49152
Mv 64,0,36
Rt 66,18,51200
s_add_u32 s92, s42, 0x80
Mv 96,0,40
Rt 68,19,49152
s_add_u32 m0, s41, 65536
Mv 128,0,44
Rt 70,19,51200
buffer_load_dwordx4 v8, s[16:19], s92 offen offset:0 lds
Mv 132,4,44
Rt 72,20,49152
Mv 100,4,40
Rt 74,20,51200
Mv 68,4,36
Rt 76,21,49152
Mv 36,4,32
Rt 78,21,51200
buffer_load_dwordx4 v9, s[16:19], s92 offen offset:1024 lds
Mv 40,8,32
Rt 80,22,49152
Mv 72,8,36
)" R"(
Rt 82,22,51200
Mv 104,8,40
Rt 84,23,49152
Mv 136,8,44
Rt 86,23,51200
buffer_load_dwordx4 v10, s[16:19], s92 offen offset:2048 lds
Mv 140,12,44
Rt 88,24,49152
Mv 108,12,40
Rt 90,24,51200
Mv 76,12,36
Rt 92,25,49152
Mv 44,12,32
Rt 94,25,51200
buffer_load_dwordx4 v11, s[16:19], s92 offen offset:3072 lds
Mv 48,16,32
Rb 96,17,32768
Mv 80,16,36
Rb 100,17,34816
s_add_u32 s92, s42, 0x40080
Mv 112,16,40
Rb 104,17,40960
s_add_u32 m0, s41, 69632
Mv 144,16,44
Rb 108,17,43008
buffer_load_dwordx4 v8, s[16:19], s92 offen offset:0 lds
Mv 148,20,44
Rb 112,17,49152
Mv 116,20,40
Rb 116,17,51200
Mv 84,20,36
Rb 120,17,57344
Mv 52,20,32
Rb 124,17,59392
buffer_load_dwordx4 v9, s[16:19], s92 offen offset:1024 lds
Mv 56,24,32
Mv 88,24,36
Mv 120,24,40
Mv 152,24,44
buffer_load_dwordx4 v10, s[16:19], s92 offen offset:2048 lds
Mv 156,28,44
Mv 124,28,40
Mv 92,28,36
Mv 60,28,32
s_waitcnt lgkmcnt(0)
s_barrier
Mv 32,64,96
v_bfe_u32 v12, v0, 3, 2
v_lshlrev_b32 v12, 5, v12
Mv 36,68,96
v_and_b32 v4, 3, v0
v_lshl_or_b32 v12, v4, 3, v12
Mv 40,72,96
s_add_u32 m0, s36, 98304
s_add_u32 s44, s43, 768
Mv 44,76,96
buffer_load_dwordx4 v26, s[12:15], s44 offen offset:0 lds
Mv 48,80,96
v_bfe_u32 v4, v0, 2, 2
v_lshl_or_b32 v12, v4, 7, v12
Mv 52,84,96
v_lshrrev_b32 v4, 4, v0
v_lshl_or_b32 v12, v4, 10, v12
Mv 56,88,96
v_xor_b32 v13, 32, v12
)" R"(
v_xor_b32 v14, 64, v12
Mv 60,92,96
buffer_load_dwordx4 v27, s[12:15], s44 offen offset:1024 lds
Mv 64,64,100
v_xor_b32 v15, 96, v12
v_add_u32 v20, 0x20000, v12
Mv 68,68,100
v_add_u32 v21, 0x20000, v13
v_add_u32 v22, 0x20000, v14
Mv 72,72,100
v_add_u32 v23, 0x20000, v15
v_add_u32 v16, 0x10000, v12
Mv 76,76,100
s_add_u32 s44, s43, 13056
buffer_load_dwordx4 v26, s[12:15], s44 offen offset:2048 lds
Mv 80,80,100
v_add_u32 v17, 0x10000, v13
v_add_u32 v18, 0x10000, v14
Mv 84,84,100
v_add_u32 v19, 0x10000, v15
Cv 32,32,33
Mv 88,88,100
Cv 33,34,35
Cv 34,36,37
Mv 92,92,100
buffer_load_dwordx4 v27, s[12:15], s44 offen offset:3072 lds
Mv 96,64,104
Cv 35,38,39
Cv 40,40,41
Mv 100,68,104
Cv 41,42,43
Cv 42,44,45
Mv 104,72,104
s_add_u32 m0, s41, 32768
s_add_u32 s92, s42, 0x1c0080
Mv 108,76,104
buffer_load_dwordx4 v8, s[16:19], s92 offen offset:0 lds
Mv 112,80,104
Cv 43,46,47
Cv 48,48,49
Mv 116,84,104
Cv 49,50,51
Cv 50,52,53
Mv 120,88,104
Cv 51,54,55
Cv 56,56,57
Mv 124,92,104
buffer_store_dwordx4 v[32:35], v2, s[20:23], s45 offen offset:0 sc0 sc1
Mv 128,64,108
Cv 57,58,59
Cv 58,60,61
Mv 132,68,108
Cv 59,62,63
Cv 64,64,65
Mv 136,72,108
Cv 65,66,67
Cv 66,68,69
Mv 140,76,108
buffer_load_dwordx4 v9, s[16:19], s92 offen offset:1024 lds
Mv 144,80,108
Cv 67,70,71
Cv 72,72,73
Mv 148,84,108
Cv 73,74,75
)" R"(
Cv 74,76,77
Mv 152,88,108
Cv 75,78,79
Cv 80,80,81
Mv 156,92,108
buffer_store_dwordx4 v[40:43], v2, s[20:23], s45 offen offset:1024 sc0 sc1
Mv 160,0,48
Cv 81,82,83
Cv 82,84,85
Mv 160,64,112
Cv 83,86,87
Cv 88,88,89
Mv 164,4,48
Cv 89,90,91
Cv 90,92,93
Mv 164,68,112
buffer_load_dwordx4 v10, s[16:19], s92 offen offset:2048 lds
Mv 168,8,48
Cv 91,94,95
Cv 96,96,97
Mv 168,72,112
Cv 97,98,99
Cv 98,100,101
Mv 172,12,48
Cv 99,102,103
Cv 104,104,105
Mv 172,76,112
buffer_store_dwordx4 v[48:51], v2, s[20:23], s45 offen offset:2048 sc0 sc1
Mv 176,16,48
Cv 105,106,107
Cv 106,108,109
Mv 176,80,112
Cv 107,110,111
Cv 112,112,113
Mv 180,20,48
Cv 113,114,115
Cv 114,116,117
Mv 180,84,112
buffer_load_dwordx4 v11, s[16:19], s92 offen offset:3072 lds
Mv 184,24,48
Cv 115,118,119
Cv 120,120,121
Mv 184,88,112
Cv 121,122,123
Cv 122,124,125
Mv 188,28,48
Cv 123,126,127
Cv 128,128,129
Mv 188,92,112
buffer_store_dwordx4 v[56:59], v2, s[20:23], s45 offen offset:3072 sc0 sc1
Mv 192,0,52
Cv 129,130,131
Cv 130,132,133
Mv 192,64,116
Cv 131,134,135
Cv 136,136,137
Mv 196,4,52
s_add_u32 m0, s41, 36864
s_add_u32 s92, s42, 0x200080
Mv 196,68,116
buffer_load_dwordx4 v8, s[16:19], s92 offen offset:0 lds
Mv 200,8,52
Cv 137,138,139
Cv 138,140,141
)" R"(
Mv 200,72,116
Cv 139,142,143
Cv 144,144,145
Mv 204,12,52
Cv 145,146,147
Cv 146,148,149
Mv 204,76,116
buffer_store_dwordx4 v[64:67], v3, s[20:23], s45 offen offset:0 sc0 sc1
Mv 208,16,52
Cv 147,150,151
Cv 152,152,153
Mv 208,80,116
Cv 153,154,155
Cv 154,156,157
Mv 212,20,52
Cv 155,158,159
Mv 212,84,116
buffer_load_dwordx4 v9, s[16:19], s92 offen offset:1024 lds
Mv 216,24,52
Mv 216,88,116
Mv 220,28,52
Mv 220,92,116
buffer_store_dwordx4 v[72:75], v3, s[20:23], s45 offen offset:1024 sc0 sc1
Mv 224,0,56
Ma 224,0,60
Ma 228,4,60
Mv 228,4,56
buffer_load_dwordx4 v10, s[16:19], s92 offen offset:2048 lds
Mv 232,8,56
Ma 232,8,60
Ma 236,12,60
Mv 236,12,56
s_waitcnt vmcnt(24)
s_barrier
Mv 240,16,56
buffer_store_dwordx4 v[80:83], v3, s[20:23], s45 offen offset:2048 sc0 sc1
Ma 240,16,60
Ma 244,20,60
Mv 244,20,56
Mv 248,24,56
Ma 248,24,60
Ma 252,28,60
Mv 252,28,56
buffer_store_dwordx4 v[88:91], v3, s[20:23], s45 offen offset:3072 sc0 sc1
Mv 224,64,120
Rt 0,20,0
Mv 228,68,120
Rt 2,20,512
Mv 232,72,120
Rt 4,21,0
Mv 236,76,120
Rt 6,21,512
Mv 240,80,120
Rt 8,22,0
Mv 244,84,120
Rt 10,22,512
Mv 248,88,120
Rt 12,23,0
Mv 252,92,120
Rt 14,23,512
Ma 224,64,124
Rt 16,20,4096
Ma 228,68,124
Rt 18,20,4608
Ma 232,72,124
)" R"(
Rt 20,21,4096
Ma 236,76,124
Rt 22,21,4608
Ma 240,80,124
Rt 24,22,4096
Ma 244,84,124
Rt 26,22,4608
Ma 248,88,124
Rt 28,23,4096
Ma 252,92,124
Rt 30,23,4608
s_waitcnt vmcnt(15) lgkmcnt(8)
s_branch .LzQA
.LzPAVX:
Mv 32,32,0
Rt 64,18,16384
Mv 36,36,0
Rb 192,28,32768
Mv 96,40,0
Rt 66,18,18432
Mv 100,44,0
s_add_u32 s39, s39, 0x40000
Rb 196,28,34816
Mv 160,48,0
Rt 68,19,16384
s_add_u32 m0, s37, 49152
Mv 164,52,0
buffer_load_dwordx4 v12, s[8:11], s39 offen offset:0 lds
Mv 224,56,0
Rt 70,19,18432
Mv 228,60,0
Rb 200,29,32768
Mv 236,60,4
Rt 72,20,16384
Mv 232,56,4
Rb 204,29,34816
Mv 172,52,4
Rt 74,20,18432
Mv 168,48,4
Rb 208,28,36864
Mv 108,44,4
Rt 76,21,16384
Mv 104,40,4
buffer_load_dwordx4 v13, s[8:11], s39 offen offset:1024 lds
Mv 44,36,4
Rt 78,21,18432
Mv 40,32,4
Rb 212,28,38912
Mv 48,32,8
Rt 80,22,16384
Mv 52,36,8
Rb 216,29,36864
Mv 112,40,8
Rt 82,22,18432
Mv 116,44,8
Rb 220,29,38912
Mv 176,48,8
Rt 84,23,16384
Mv 180,52,8
buffer_load_dwordx4 v14, s[8:11], s39 offen offset:2048 lds
Mv 240,56,8
Rt 86,23,18432
Mv 244,60,8
Mv 252,60,12
)" R"(
Rt 88,24,16384
Mv 248,56,12
Mv 188,52,12
Rt 90,24,18432
Mv 184,48,12
Mv 124,44,12
Rt 92,25,16384
Mv 120,40,12
buffer_load_dwordx4 v15, s[8:11], s39 offen offset:3072 lds
Mv 60,36,12
Rt 94,25,18432
Mv 56,32,12
Mv 64,32,16
Rb 96,17,0
Mv 68,36,16
Mv 128,40,16
Rb 100,17,2048
Mv 132,44,16
s_add_u32 s39, s39, 0x40000
Mv 192,48,16
Rb 104,17,8192
s_add_u32 m0, s37, 0
Mv 196,52,16
buffer_load_dwordx4 v12, s[8:11], s39 offen offset:0 lds
Ma 224,56,16
Rb 108,17,10240
Ma 228,60,16
Ma 236,60,20
Rb 112,17,16384
Ma 232,56,20
Mv 204,52,20
Rb 116,17,18432
Mv 200,48,20
Mv 140,44,20
Rb 120,17,24576
Mv 136,40,20
buffer_load_dwordx4 v13, s[8:11], s39 offen offset:1024 lds
Mv 76,36,20
Rb 124,17,26624
Mv 72,32,20
Mv 80,32,24
Mv 84,36,24
Mv 144,40,24
Mv 148,44,24
Mv 208,48,24
Mv 212,52,24
buffer_load_dwordx4 v14, s[8:11], s39 offen offset:2048 lds
Ma 240,56,24
Ma 244,60,24
Ma 252,60,28
Ma 248,56,28
Mv 220,52,28
Mv 216,48,28
Mv 156,44,28
Mv 152,40,28
buffer_load_dwordx4 v15, s[8:11], s39 offen offset:3072 lds
Mv 92,36,28
Mv 88,32,28
s_waitcnt vmcnt(9) lgkmcnt(0)
s_barrier
s_add_u32 m0, s36, 131072
s_add_u32 s44, s43, 256
buffer_load_dwordx4 v26, s[12:15], s44 offen offset:0 lds
buffer_load_dwordx4 v27, s[12:15], s44 offen offset:1024 lds
s_add_u32 s44, s43, 12544
)" R"(
buffer_load_dwordx4 v26, s[12:15], s44 offen offset:2048 lds
buffer_load_dwordx4 v27, s[12:15], s44 offen offset:3072 lds
Mv 32,96,64
Rt 0,18,32768
Mv 36,100,64
s_add_u32 s38, s38, 0xffff0080
Mv 96,104,64
Rt 2,18,34816
s_add_u32 m0, s36, 0
Mv 100,108,64
buffer_load_dwordx4 v8, s[4:7], s38 offen offset:0 lds
Mv 160,112,64
Rt 4,19,32768
Mv 164,116,64
Mv 224,120,64
Rt 6,19,34816
Mv 228,124,64
buffer_load_dwordx4 v9, s[4:7], s38 offen offset:1024 lds
Mv 236,124,68
Rt 8,20,32768
Mv 232,120,68
Mv 172,116,68
Rt 10,20,34816
Mv 168,112,68
buffer_load_dwordx4 v10, s[4:7], s38 offen offset:2048 lds
Mv 108,108,68
Rt 12,21,32768
Mv 104,104,68
Mv 44,100,68
Rt 14,21,34816
Mv 40,96,68
buffer_load_dwordx4 v11, s[4:7], s38 offen offset:3072 lds
Mv 48,96,72
Rt 16,22,32768
Mv 52,100,72
s_add_u32 s38, s38, 0x10000
Mv 112,104,72
Rt 18,22,34816
s_add_u32 m0, s36, 4096
Mv 116,108,72
buffer_load_dwordx4 v8, s[4:7], s38 offen offset:0 lds
Mv 176,112,72
Rt 20,23,32768
Mv 180,116,72
Mv 240,120,72
Rt 22,23,34816
Mv 244,124,72
buffer_load_dwordx4 v9, s[4:7], s38 offen offset:1024 lds
Mv 252,124,76
Rt 24,24,32768
Mv 248,120,76
Mv 188,116,76
Rt 26,24,34816
Mv 184,112,76
buffer_load_dwordx4 v10, s[4:7], s38 offen offset:2048 lds
Mv 124,108,76
Rt 28,25,32768
Mv 120,104,76
Mv 60,100,76
Rt 30,25,34816
Mv 56,96,76
buffer_load_dwordx4 v11, s[4:7], s38 offen offset:3072 lds
Mv 64,96,80
Rb 32,16,32768
)" R"(
Mv 68,100,80
Mv 128,104,80
Rb 36,16,34816
Mv 132,108,80
s_add_u32 s44, s43, 384
Mv 192,112,80
Rb 40,16,40960
s_add_u32 m0, s36, 135168
Mv 196,116,80
buffer_load_dwordx4 v26, s[12:15], s44 offen offset:0 lds
Ma 224,120,80
Rb 44,16,43008
Ma 228,124,80
Ma 236,124,84
Rb 48,16,49152
Ma 232,120,84
buffer_load_dwordx4 v27, s[12:15], s44 offen offset:1024 lds
Mv 204,116,84
Rb 52,16,51200
Mv 200,112,84
Mv 140,108,84
Rb 56,16,57344
Mv 136,104,84
s_add_u32 s44, s43, 12672
buffer_load_dwordx4 v26, s[12:15], s44 offen offset:2048 lds
Mv 76,100,84
Rb 60,16,59392
Mv 72,96,84
Mv 80,96,88
Mv 84,100,88
buffer_load_dwordx4 v27, s[12:15], s44 offen offset:3072 lds
Mv 144,104,88
Mv 148,108,88
Mv 208,112,88
Mv 212,116,88
Ma 240,120,88
Ma 244,124,88
Ma 252,124,92
Ma 248,120,92
Mv 220,116,92
Mv 216,112,92
Mv 156,108,92
Mv 152,104,92
Mv 92,100,92
Mv 88,96,92
s_waitcnt vmcnt(0) lgkmcnt(0)
s_barrier
Mv 32,32,0
Rt 64,18,49152
Mv 36,36,0
Rb 128,28,32768
Mv 96,40,0
Rt 66,18,51200
Mv 100,44,0
s_add_u32 s39, s39, 0x40000
Rb 132,28,34816
Mv 160,48,0
Rt 68,19,49152
s_add_u32 m0, s37, 16384
Mv 164,52,0
buffer_load_dwordx4 v12, s[8:11], s39 offen offset:0 lds
Mv 224,56,0
Rt 70,19,51200
Mv 228,60,0
Rb 136,29,32768
)" R"(
Mv 236,60,4
Rt 72,20,49152
Mv 232,56,4
Rb 140,29,34816
Mv 172,52,4
Rt 74,20,51200
Mv 168,48,4
Rb 144,28,36864
Mv 108,44,4
Rt 76,21,49152
Mv 104,40,4
buffer_load_dwordx4 v13, s[8:11], s39 offen offset:1024 lds
Mv 44,36,4
Rt 78,21,51200
Mv 40,32,4
Rb 148,28,38912
Mv 48,32,8
Rt 80,22,49152
Mv 52,36,8
Rb 152,29,36864
Mv 112,40,8
Rt 82,22,51200
Mv 116,44,8
Rb 156,29,38912
Mv 176,48,8
Rt 84,23,49152
Mv 180,52,8
buffer_load_dwordx4 v14, s[8:11], s39 offen offset:2048 lds
Mv 240,56,8
Rt 86,23,51200
Mv 244,60,8
Mv 252,60,12
Rt 88,24,49152
Mv 248,56,12
Mv 188,52,12
Rt 90,24,51200
Mv 184,48,12
Mv 124,44,12
Rt 92,25,49152
Mv 120,40,12
buffer_load_dwordx4 v15, s[8:11], s39 offen offset:3072 lds
Mv 60,36,12
Rt 94,25,51200
Mv 56,32,12
Mv 64,32,16
Rb 96,17,32768
Mv 68,36,16
Mv 128,40,16
Rb 100,17,34816
Mv 132,44,16
s_add_u32 s39, s39, 0x40000
Mv 192,48,16
Rb 104,17,40960
s_add_u32 m0, s37, 32768
Mv 196,52,16
buffer_load_dwordx4 v12, s[8:11], s39 offen offset:0 lds
Ma 224,56,16
Rb 108,17,43008
Ma 228,60,16
Ma 236,60,20
Rb 112,17,49152
Ma 232,56,20
Mv 204,52,20
Rb 116,17,51200
)" R"(
Mv 200,48,20
Mv 140,44,20
Rb 120,17,57344
Mv 136,40,20
buffer_load_dwordx4 v13, s[8:11], s39 offen offset:1024 lds
Mv 76,36,20
Rb 124,17,59392
Mv 72,32,20
Mv 80,32,24
Mv 84,36,24
Mv 144,40,24
Mv 148,44,24
Mv 208,48,24
Mv 212,52,24
buffer_load_dwordx4 v14, s[8:11], s39 offen offset:2048 lds
Ma 240,56,24
Ma 244,60,24
Ma 252,60,28
Ma 248,56,28
Mv 220,52,28
Mv 216,48,28
Mv 156,44,28
Mv 152,40,28
buffer_load_dwordx4 v15, s[8:11], s39 offen offset:3072 lds
Mv 92,36,28
Mv 88,32,28
s_waitcnt lgkmcnt(0)
s_barrier
s_add_u32 m0, s36, 131072
s_add_u32 s44, s43, 512
buffer_load_dwordx4 v26, s[12:15], s44 offen offset:0 lds
buffer_load_dwordx4 v27, s[12:15], s44 offen offset:1024 lds
s_add_u32 s44, s43, 12800
buffer_load_dwordx4 v26, s[12:15], s44 offen offset:2048 lds
buffer_load_dwordx4 v27, s[12:15], s44 offen offset:3072 lds
Mv 32,96,64
Rt 0,18,0
Mv 36,100,64
s_add_u32 s38, s38, 0xffff0080
Mv 96,104,64
Rt 2,18,2048
s_add_u32 m0, s36, 32768
Mv 100,108,64
buffer_load_dwordx4 v8, s[4:7], s38 offen offset:0 lds
Mv 160,112,64
Rt 4,19,0
Mv 164,116,64
Mv 224,120,64
Rt 6,19,2048
Mv 228,124,64
buffer_load_dwordx4 v9, s[4:7], s38 offen offset:1024 lds
Mv 236,124,68
Rt 8,20,0
Mv 232,120,68
Mv 172,116,68
Rt 10,20,2048
Mv 168,112,68
buffer_load_dwordx4 v10, s[4:7], s38 offen offset:2048 lds
Mv 108,108,68
Rt 12,21,0
Mv 104,104,68
Mv 44,100,68
Rt 14,21,2048
Mv 40,96,68
buffer_load_dwordx4 v11, s[4:7], s38 offen offset:3072 lds
)" R"(
Mv 48,96,72
Rt 16,22,0
Mv 52,100,72
s_add_u32 s38, s38, 0x10000
Mv 112,104,72
Rt 18,22,2048
s_add_u32 m0, s36, 36864
Mv 116,108,72
buffer_load_dwordx4 v8, s[4:7], s38 offen offset:0 lds
Mv 176,112,72
Rt 20,23,0
Mv 180,116,72
Mv 240,120,72
Rt 22,23,2048
Mv 244,124,72
buffer_load_dwordx4 v9, s[4:7], s38 offen offset:1024 lds
Mv 252,124,76
Rt 24,24,0
Mv 248,120,76
Mv 188,116,76
Rt 26,24,2048
Mv 184,112,76
buffer_load_dwordx4 v10, s[4:7], s38 offen offset:2048 lds
Mv 124,108,76
Rt 28,25,0
Mv 120,104,76
Mv 60,100,76
Rt 30,25,2048
Mv 56,96,76
buffer_load_dwordx4 v11, s[4:7], s38 offen offset:3072 lds
Mv 64,96,80
Rb 32,16,0
Mv 68,100,80
Mv 128,104,80
Rb 36,16,2048
Mv 132,108,80
s_add_u32 s44, s43, 640
Mv 192,112,80
Rb 40,16,8192
s_add_u32 m0, s36, 135168
Mv 196,116,80
buffer_load_dwordx4 v26, s[12:15], s44 offen offset:0 lds
Ma 224,120,80
Rb 44,16,10240
Ma 228,124,80
Ma 236,124,84
Rb 48,16,16384
Ma 232,120,84
buffer_load_dwordx4 v27, s[12:15], s44 offen offset:1024 lds
Mv 204,116,84
Rb 52,16,18432
Mv 200,112,84
Mv 140,108,84
Rb 56,16,24576
Mv 136,104,84
s_add_u32 s44, s43, 12928
buffer_load_dwordx4 v26, s[12:15], s44 offen offset:2048 lds
Mv 76,100,84
Rb 60,16,26624
Mv 72,96,84
Mv 80,96,88
Mv 84,100,88
buffer_load_dwordx4 v27, s[12:15], s44 offen offset:3072 lds
Mv 144,104,88
)" R"(
Mv 148,108,88
Mv 208,112,88
Mv 212,116,88
Ma 240,120,88
Ma 244,124,88
Ma 252,124,92
Ma 248,120,92
Mv 220,116,92
Mv 216,112,92
Mv 156,108,92
Mv 152,104,92
Mv 92,100,92
Mv 88,96,92
s_waitcnt vmcnt(0) lgkmcnt(0)
s_barrier
Mv 32,32,0
Rt 64,18,16384
Mv 36,36,0
Rb 160,28,32768
v_lshrrev_b32 v5, 2, v0
Mv 96,40,0
Rt 66,18,18432
v_and_b32 v5, 8, v5
Mv 100,44,0
s_add_u32 s39, s39, 0x40000
Rb 164,28,34816
Mv 160,48,0
Rt 68,19,16384
s_add_u32 m0, s37, 49152
Mv 164,52,0
buffer_load_dwordx4 v12, s[8:11], s39 offen offset:0 lds
Mv 224,56,0
Rt 70,19,18432
v_and_or_b32 v5, v0, 7, v5
Mv 228,60,0
Rb 168,29,32768
v_lshlrev_b32 v5, 8, v5
Mv 236,60,4
Rt 72,20,16384
v_and_or_b32 v5, v0, 24, v5
Mv 232,56,4
Rb 172,29,34816
v_lshrrev_b32 v5, 1, v5
Mv 172,52,4
Rt 74,20,18432
v_mov_b32 v6, s3
Mv 168,48,4
Rb 176,28,36864
Mv 108,44,4
Rt 76,21,16384
Mv 104,40,4
buffer_load_dwordx4 v13, s[8:11], s39 offen offset:1024 lds
Mv 44,36,4
Rt 78,21,18432
Mv 40,32,4
Rb 180,28,38912
Mv 48,32,8
Rt 80,22,16384
Mv 52,36,8
Rb 184,29,36864
Mv 112,40,8
Rt 82,22,18432
Mv 116,44,8
Rb 188,29,38912
Mv 176,48,8
)" R"(
Rt 84,23,16384
Mv 180,52,8
buffer_load_dwordx4 v14, s[8:11], s39 offen offset:2048 lds
Mv 240,56,8
Rt 86,23,18432
Mv 244,60,8
Mv 252,60,12
Rt 88,24,16384
Mv 248,56,12
Mv 188,52,12
Rt 90,24,18432
Mv 184,48,12
Mv 124,44,12
Rt 92,25,16384
Mv 120,40,12
buffer_load_dwordx4 v15, s[8:11], s39 offen offset:3072 lds
Mv 60,36,12
Rt 94,25,18432
v_lshrrev_b32 v10, 3, v0
Mv 56,32,12
v_and_b32 v11, 0x70, v1
v_lshl_or_b32 v10, v10, 13, v11
Mv 64,32,16
Rb 96,17,0
v_lshlrev_b32 v11, 1, v0
Mv 68,36,16
v_and_b32 v11, 32, v11
v_xor_b32 v10, v10, v11
Mv 128,40,16
Rb 100,17,2048
s_and_b32 s28, s33, 1
Mv 132,44,16
s_lshl_b32 s28, s28, 6
v_xor_b32 v8, s28, v10
Mv 192,48,16
Rb 104,17,8192
v_xor_b32 v9, 64, v8
Mv 196,52,16
v_add_u32 v9, 0xfc00, v9
v_add_u32 v10, 0x1f800, v8
Ma 224,56,16
Rb 108,17,10240
v_add_u32 v11, 0x1f800, v9
Ma 228,60,16
Ma 236,60,20
Rb 112,17,16384
Ma 232,56,20
Mv 204,52,20
Rb 116,17,18432
Mv 200,48,20
Mv 140,44,20
Rb 120,17,24576
Mv 136,40,20
Mv 76,36,20
Rb 124,17,26624
Mv 72,32,20
Mv 80,32,24
Mv 84,36,24
Mv 144,40,24
Mv 148,44,24
Mv 208,48,24
Mv 212,52,24
Ma 240,56,24
Ma 244,60,24
)" R"(
Ma 252,60,28
Ma 248,56,28
Mv 220,52,28
Mv 216,48,28
Mv 156,44,28
Mv 152,40,28
Mv 92,36,28
Mv 88,32,28
s_waitcnt lgkmcnt(0)
s_barrier
Mv 32,96,64
Rt 0,18,32768
Mv 36,100,64
s_add_u32 s92, s42, 0x0
Mv 96,104,64
Rt 2,18,34816
s_add_u32 m0, s41, 131072
Mv 100,108,64
buffer_load_dwordx4 v8, s[16:19], s92 offen offset:0 lds
Mv 160,112,64
Rt 4,19,32768
Mv 164,116,64
Mv 224,120,64
Rt 6,19,34816
Mv 228,124,64
buffer_load_dwordx4 v9, s[16:19], s92 offen offset:1024 lds
Mv 236,124,68
Rt 8,20,32768
Mv 232,120,68
Mv 172,116,68
Rt 10,20,34816
Mv 168,112,68
buffer_load_dwordx4 v10, s[16:19], s92 offen offset:2048 lds
Mv 108,108,68
Rt 12,21,32768
Mv 104,104,68
Mv 44,100,68
Rt 14,21,34816
Mv 40,96,68
buffer_load_dwordx4 v11, s[16:19], s92 offen offset:3072 lds
Mv 48,96,72
Rt 16,22,32768
Mv 52,100,72
s_add_u32 s92, s42, 0x40000
Mv 112,104,72
Rt 18,22,34816
s_add_u32 m0, s41, 135168
Mv 116,108,72
buffer_load_dwordx4 v8, s[16:19], s92 offen offset:0 lds
Mv 176,112,72
Rt 20,23,32768
Mv 180,116,72
Mv 240,120,72
Rt 22,23,34816
Mv 244,124,72
buffer_load_dwordx4 v9, s[16:19], s92 offen offset:1024 lds
Mv 252,124,76
Rt 24,24,32768
Mv 248,120,76
Mv 188,116,76
Rt 26,24,34816
Mv 184,112,76
buffer_load_dwordx4 v10, s[16:19], s92 offen offset:2048 lds
Mv 124,108,76
Rt 28,25,32768
)" R"(
Mv 120,104,76
s_add_u32 s92, s42, 0x1c0000
Mv 60,100,76
Rt 30,25,34816
s_add_u32 m0, s41, 0
Mv 56,96,76
buffer_load_dwordx4 v8, s[16:19], s92 offen offset:0 lds
Mv 64,96,80
Rb 32,16,32768
Mv 68,100,80
Mv 128,104,80
Rb 36,16,34816
Mv 132,108,80
buffer_load_dwordx4 v9, s[16:19], s92 offen offset:1024 lds
Mv 192,112,80
Rb 40,16,40960
Mv 196,116,80
Ma 224,120,80
Rb 44,16,43008
Ma 228,124,80
buffer_load_dwordx4 v10, s[16:19], s92 offen offset:2048 lds
Ma 236,124,84
Rb 48,16,49152
Ma 232,120,84
Mv 204,116,84
Rb 52,16,51200
Mv 200,112,84
buffer_load_dwordx4 v11, s[16:19], s92 offen offset:3072 lds
Mv 140,108,84
Rb 56,16,57344
Mv 136,104,84
s_add_u32 s92, s42, 0x200000
Mv 76,100,84
Rb 60,16,59392
s_add_u32 m0, s41, 4096
Mv 72,96,84
buffer_load_dwordx4 v8, s[16:19], s92 offen offset:0 lds
Mv 80,96,88
Mv 84,100,88
Mv 144,104,88
Mv 148,108,88
buffer_load_dwordx4 v9, s[16:19], s92 offen offset:1024 lds
Mv 208,112,88
Mv 212,116,88
Ma 240,120,88
Ma 244,124,88
buffer_load_dwordx4 v10, s[16:19], s92 offen offset:2048 lds
Ma 252,124,92
Ma 248,120,92
Mv 220,116,92
Mv 216,112,92
Mv 156,108,92
Mv 152,104,92
Mv 92,100,92
Mv 88,96,92
s_waitcnt vmcnt(14) lgkmcnt(4)
s_barrier
Mv 32,32,0
Rt 64,18,49152
Mv 36,36,0
Rt 66,18,51200
s_add_u32 s92, s42, 0x80
Mv 96,40,0
Rt 68,19,49152
s_add_u32 m0, s41, 65536
)" R"(
Mv 100,44,0
Rt 70,19,51200
buffer_load_dwordx4 v8, s[16:19], s92 offen offset:0 lds
Mv 108,44,4
Rt 72,20,49152
Mv 104,40,4
Rt 74,20,51200
Mv 44,36,4
Rt 76,21,49152
Mv 40,32,4
Rt 78,21,51200
buffer_load_dwordx4 v9, s[16:19], s92 offen offset:1024 lds
Mv 48,32,8
Rt 80,22,49152
Mv 52,36,8
Rt 82,22,51200
Mv 112,40,8
Rt 84,23,49152
Mv 116,44,8
Rt 86,23,51200
buffer_load_dwordx4 v10, s[16:19], s92 offen offset:2048 lds
Mv 124,44,12
Rt 88,24,49152
Mv 120,40,12
Rt 90,24,51200
Mv 60,36,12
Rt 92,25,49152
Mv 56,32,12
Rt 94,25,51200
buffer_load_dwordx4 v11, s[16:19], s92 offen offset:3072 lds
Mv 64,32,16
Rb 96,17,32768
Mv 68,36,16
Rb 100,17,34816
s_add_u32 s92, s42, 0x40080
Mv 128,40,16
Rb 104,17,40960
s_add_u32 m0, s41, 69632
Mv 132,44,16
Rb 108,17,43008
buffer_load_dwordx4 v8, s[16:19], s92 offen offset:0 lds
Mv 140,44,20
Rb 112,17,49152
Mv 136,40,20
Rb 116,17,51200
Mv 76,36,20
Rb 120,17,57344
Mv 72,32,20
Rb 124,17,59392
buffer_load_dwordx4 v9, s[16:19], s92 offen offset:1024 lds
Mv 80,32,24
Mv 84,36,24
Mv 144,40,24
Mv 148,44,24
buffer_load_dwordx4 v10, s[16:19], s92 offen offset:2048 lds
Mv 156,44,28
Mv 152,40,28
Mv 92,36,28
Mv 88,32,28
s_waitcnt lgkmcnt(0)
s_barrier
Mv 32,96,64
v_bfe_u32 v12, v0, 3, 2
v_lshlrev_b32 v12, 5, v12
Mv 36,100,64
)" R"(
v_and_b32 v4, 3, v0
v_lshl_or_b32 v12, v4, 3, v12
Mv 40,96,68
s_add_u32 m0, s36, 98304
s_add_u32 s44, s43, 768
Mv 44,100,68
buffer_load_dwordx4 v26, s[12:15], s44 offen offset:0 lds
Mv 48,96,72
v_bfe_u32 v4, v0, 2, 2
v_lshl_or_b32 v12, v4, 7, v12
Mv 52,100,72
v_lshrrev_b32 v4, 4, v0
v_lshl_or_b32 v12, v4, 10, v12
Mv 56,96,76
v_xor_b32 v13, 32, v12
v_xor_b32 v14, 64, v12
Mv 60,100,76
buffer_load_dwordx4 v27, s[12:15], s44 offen offset:1024 lds
Mv 64,96,80
v_xor_b32 v15, 96, v12
v_add_u32 v20, 0x20000, v12
Mv 68,100,80
v_add_u32 v21, 0x20000, v13
v_add_u32 v22, 0x20000, v14
Mv 72,96,84
v_add_u32 v23, 0x20000, v15
v_add_u32 v16, 0x10000, v12
Mv 76,100,84
s_add_u32 s44, s43, 13056
buffer_load_dwordx4 v26, s[12:15], s44 offen offset:2048 lds
Mv 80,96,88
v_add_u32 v17, 0x10000, v13
v_add_u32 v18, 0x10000, v14
Mv 84,100,88
v_add_u32 v19, 0x10000, v15
Cv 32,32,33
Mv 88,96,92
Cv 33,34,35
Cv 34,36,37
Mv 92,100,92
buffer_load_dwordx4 v27, s[12:15], s44 offen offset:3072 lds
Mv 96,104,64
Cv 35,38,39
Cv 40,40,41
Mv 100,108,64
Cv 41,42,43
Cv 42,44,45
Mv 104,104,68
s_add_u32 m0, s41, 32768
s_add_u32 s92, s42, 0x1c0080
Mv 108,108,68
buffer_load_dwordx4 v8, s[16:19], s92 offen offset:0 lds
Mv 112,104,72
Cv 43,46,47
Cv 48,48,49
Mv 116,108,72
Cv 49,50,51
Cv 50,52,53
Mv 120,104,76
Cv 51,54,55
Cv 56,56,57
Mv 124,108,76
buffer_store_dwordx4 v[32:35], v2, s[20:23], s45 offen offset:0 sc0 sc1
Mv 128,104,80
)" R"(
Cv 57,58,59
Cv 58,60,61
Mv 132,108,80
Cv 59,62,63
Cv 64,64,65
Mv 136,104,84
Cv 65,66,67
Cv 66,68,69
Mv 140,108,84
buffer_load_dwordx4 v9, s[16:19], s92 offen offset:1024 lds
Mv 144,104,88
Cv 67,70,71
Cv 72,72,73
Mv 148,108,88
Cv 73,74,75
Cv 74,76,77
Mv 152,104,92
Cv 75,78,79
Cv 80,80,81
Mv 156,108,92
buffer_store_dwordx4 v[40:43], v2, s[20:23], s45 offen offset:1024 sc0 sc1
Mv 160,48,0
Cv 81,82,83
Cv 82,84,85
Mv 160,112,64
Cv 83,86,87
Cv 88,88,89
Mv 164,52,0
Cv 89,90,91
Cv 90,92,93
Mv 164,116,64
buffer_load_dwordx4 v10, s[16:19], s92 offen offset:2048 lds
Mv 168,48,4
Cv 91,94,95
Cv 96,96,97
Mv 168,112,68
Cv 97,98,99
Cv 98,100,101
Mv 172,52,4
Cv 99,102,103
Cv 104,104,105
Mv 172,116,68
buffer_store_dwordx4 v[48:51], v2, s[20:23], s45 offen offset:2048 sc0 sc1
Mv 176,48,8
Cv 105,106,107
Cv 106,108,109
Mv 176,112,72
Cv 107,110,111
Cv 112,112,113
Mv 180,52,8
Cv 113,114,115
Cv 114,116,117
Mv 180,116,72
buffer_load_dwordx4 v11, s[16:19], s92 offen offset:3072 lds
Mv 184,48,12
Cv 115,118,119
Cv 120,120,121
Mv 184,112,76
Cv 121,122,123
Cv 122,124,125
Mv 188,52,12
Cv 123,126,127
Cv 128,128,129
Mv 188,116,76
)" R"(
buffer_store_dwordx4 v[56:59], v2, s[20:23], s45 offen offset:3072 sc0 sc1
Mv 192,48,16
Cv 129,130,131
Cv 130,132,133
Mv 192,112,80
Cv 131,134,135
Cv 136,136,137
Mv 196,52,16
s_add_u32 m0, s41, 36864
s_add_u32 s92, s42, 0x200080
Mv 196,116,80
buffer_load_dwordx4 v8, s[16:19], s92 offen offset:0 lds
Mv 200,48,20
Cv 137,138,139
Cv 138,140,141
Mv 200,112,84
Cv 139,142,143
Cv 144,144,145
Mv 204,52,20
Cv 145,146,147
Cv 146,148,149
Mv 204,116,84
buffer_store_dwordx4 v[64:67], v3, s[20:23], s45 offen offset:0 sc0 sc1
Mv 208,48,24
Cv 147,150,151
Cv 152,152,153
Mv 208,112,88
Cv 153,154,155
Cv 154,156,157
Mv 212,52,24
Cv 155,158,159
Mv 212,116,88
buffer_load_dwordx4 v9, s[16:19], s92 offen offset:1024 lds
Mv 216,48,28
Mv 216,112,92
Mv 220,52,28
Mv 220,116,92
buffer_store_dwordx4 v[72:75], v3, s[20:23], s45 offen offset:1024 sc0 sc1
Mv 224,56,0
Mv 228,60,0
Mv 236,60,4
Mv 232,56,4
buffer_load_dwordx4 v10, s[16:19], s92 offen offset:2048 lds
Mv 240,56,8
Mv 244,60,8
Mv 252,60,12
Mv 248,56,12
s_waitcnt vmcnt(24)
s_barrier
Ma 224,56,16
buffer_store_dwordx4 v[80:83], v3, s[20:23], s45 offen offset:2048 sc0 sc1
Ma 228,60,16
Ma 236,60,20
Ma 232,56,20
Ma 240,56,24
Ma 244,60,24
Ma 252,60,28
Ma 248,56,28
buffer_store_dwordx4 v[88:91], v3, s[20:23], s45 offen offset:3072 sc0 sc1
Mv 224,120,64
Rt 0,20,0
Mv 228,124,64
Rt 2,20,512
Mv 232,120,68
Rt 4,21,0
)" R"(
Mv 236,124,68
Rt 6,21,512
Mv 240,120,72
Rt 8,22,0
Mv 244,124,72
Rt 10,22,512
Mv 248,120,76
Rt 12,23,0
Mv 252,124,76
Rt 14,23,512
Ma 224,120,80
Rt 16,20,4096
Ma 228,124,80
Rt 18,20,4608
Ma 232,120,84
Rt 20,21,4096
Ma 236,124,84
Rt 22,21,4608
Ma 240,120,88
Rt 24,22,4096
Ma 244,124,88
Rt 26,22,4608
Ma 248,120,92
Rt 28,23,4096
Ma 252,124,92
Rt 30,23,4608
s_waitcnt vmcnt(15) lgkmcnt(8)
.LzQA:
s_cmp_eq_u32 s2, 112
s_cbranch_scc1 .LzT2S
.LzT2E:
Mz 32,0,192
Rt 32,20,8192
Cv 224,224,225
Mz 40,0,196
Rt 34,20,8704
Rb 48,28,0
Mz 44,4,196
Rt 36,21,8192
buffer_store_dwordx4 v[96:99], v2, s[20:23], s46 offen offset:0 sc0 sc1
Mz 36,4,192
Rt 38,21,8704
Rb 52,28,2048
Cv 225,226,227
Mz 48,8,192
Rt 40,22,8192
Cv 226,228,229
Mz 56,8,196
Rt 42,22,8704
Rb 56,29,0
Mz 60,12,196
Rt 44,23,8192
Cv 227,230,231
Cv 232,232,233
Mz 52,12,192
Rt 46,23,8704
Rb 60,29,2048
s_waitcnt lgkmcnt(12)
Mv 32,16,200
Rt 0,20,12288
Cv 233,234,235
Mv 40,16,204
Rt 2,20,12800
Cv 234,236,237
)" R"(
Cv 235,238,239
Mv 44,20,204
Rt 4,21,12288
Cv 240,240,241
Mv 36,20,200
Rt 6,21,12800
Cv 241,242,243
Mv 48,24,200
Rt 8,22,12288
buffer_store_dwordx4 v[104:107], v2, s[20:23], s46 offen offset:1024 sc0 sc1
Mv 56,24,204
Rt 10,22,12800
Cv 242,244,245
Mv 60,28,204
Rt 12,23,12288
Cv 243,246,247
Mv 52,28,200
Rt 14,23,12800
Cv 248,248,249
s_waitcnt lgkmcnt(9)
Mv 32,32,208
Rt 16,20,16384
Cv 249,250,251
Mv 40,32,212
Rt 18,20,16896
Cv 250,252,253
Mv 44,36,212
Rt 20,21,16384
Cv 251,254,255
Mv 36,36,208
Rt 22,21,16896
v_accvgpr_read_b32 v228, a224
v_accvgpr_read_b32 v24, a225
Mv 48,40,208
Rt 24,22,16384
Cv 228,228,24
Mv 56,40,212
Rt 26,22,16896
v_accvgpr_read_b32 v229, a226
Mv 60,44,212
Rt 28,23,16384
buffer_store_dwordx4 v[112:115], v2, s[20:23], s46 offen offset:2048 sc0 sc1
Mv 52,44,208
Rt 30,23,16896
s_waitcnt lgkmcnt(8)
Mv 32,0,216
Rt 32,20,20480
v_accvgpr_read_b32 v25, a227
Mv 40,0,220
Rt 34,20,20992
Cv 229,229,25
v_accvgpr_read_b32 v230, a228
Mv 44,4,220
Rt 36,21,20480
v_accvgpr_read_b32 v24, a229
Mv 36,4,216
Rt 38,21,20992
Cv 230,230,24
Mv 48,8,216
Rt 40,22,20480
v_accvgpr_read_b32 v231, a230
v_accvgpr_read_b32 v25, a231
Mv 56,8,220
Rt 42,22,20992
)" R"(
Cv 231,231,25
Mv 60,12,220
Rt 44,23,20480
v_accvgpr_read_b32 v236, a232
Mv 52,12,216
Rt 46,23,20992
v_accvgpr_read_b32 v24, a233
s_waitcnt lgkmcnt(8)
Mv 32,16,128
Rt 0,20,24576
buffer_store_dwordx4 v[120:123], v2, s[20:23], s46 offen offset:3072 sc0 sc1
Mv 40,16,132
Rt 2,20,25088
Cv 236,236,24
Mv 44,20,132
Rt 4,21,24576
v_accvgpr_read_b32 v237, a234
Mv 36,20,128
Rt 6,21,25088
v_accvgpr_read_b32 v25, a235
Cv 237,237,25
Mv 48,24,128
Rt 8,22,24576
v_accvgpr_read_b32 v238, a236
Mv 56,24,132
Rt 10,22,25088
v_accvgpr_read_b32 v24, a237
Mv 60,28,132
Rt 12,23,24576
Cv 238,238,24
v_accvgpr_read_b32 v239, a238
Mv 52,28,128
Rt 14,23,25088
s_waitcnt lgkmcnt(8)
Mv 32,32,136
Rt 16,12,0
v_accvgpr_read_b32 v25, a239
Mv 40,32,140
Rt 18,12,512
Cv 239,239,25
v_accvgpr_read_b32 v244, a240
Mv 44,36,140
Rt 20,13,0
buffer_store_dwordx4 v[128:131], v3, s[20:23], s46 offen offset:0 sc0 sc1
Mv 36,36,136
Rt 22,13,512
v_accvgpr_read_b32 v24, a241
Mv 48,40,136
Rt 24,14,0
Cv 244,244,24
v_accvgpr_read_b32 v245, a242
Mv 56,40,140
Rt 26,14,512
v_accvgpr_read_b32 v25, a243
Mv 60,44,140
Rt 28,15,0
Cv 245,245,25
Mv 52,44,136
Rt 30,15,512
s_waitcnt vmcnt(5) lgkmcnt(8)
s_barrier
buffer_store_dword v6, v7, s[20:23], s49 offen offset:0 sc0 sc1
Mv 32,0,144
Rt 32,12,4096
v_accvgpr_read_b32 v246, a244
)" R"(
Mv 40,0,148
Rt 34,12,4608
s_add_u32 s92, s42, 0x100
Mv 44,4,148
Rt 36,13,4096
s_add_u32 m0, s41, 98304
Mv 36,4,144
Rt 38,13,4608
buffer_load_dwordx4 v8, s[16:19], s92 offen offset:0 lds
Mv 48,8,144
Rt 40,14,4096
buffer_store_dwordx4 v[136:139], v3, s[20:23], s46 offen offset:1024 sc0 sc1
Mv 56,8,148
Rt 42,14,4608
v_accvgpr_read_b32 v24, a245
Mv 60,12,148
Rt 44,15,4096
Cv 246,246,24
v_accvgpr_read_b32 v247, a246
Mv 52,12,144
Rt 46,15,4608
buffer_load_dwordx4 v9, s[16:19], s92 offen offset:1024 lds
s_waitcnt lgkmcnt(8)
Mv 32,16,152
Rt 0,12,8192
v_accvgpr_read_b32 v25, a247
Mv 40,16,156
Rt 2,12,8704
Cv 247,247,25
v_accvgpr_read_b32 v252, a248
Mv 44,20,156
Rt 4,13,8192
v_accvgpr_read_b32 v24, a249
Mv 36,20,152
Rt 6,13,8704
buffer_load_dwordx4 v10, s[16:19], s92 offen offset:2048 lds
Mv 48,24,152
Rt 8,14,8192
Cv 252,252,24
v_accvgpr_read_b32 v253, a250
Mv 56,24,156
Rt 10,14,8704
v_accvgpr_read_b32 v25, a251
Mv 60,28,156
Rt 12,15,8192
buffer_store_dwordx4 v[144:147], v3, s[20:23], s46 offen offset:2048 sc0 sc1
Mv 52,28,152
Rt 14,15,8704
buffer_load_dwordx4 v11, s[16:19], s92 offen offset:3072 lds
s_waitcnt lgkmcnt(8)
Mv 32,32,160
Rt 16,12,12288
Cv 253,253,25
Mv 40,32,164
Rt 18,12,12800
s_add_u32 s92, s42, 0x40100
Mv 44,36,164
Rt 20,13,12288
s_add_u32 m0, s41, 102400
Mv 36,36,160
Rt 22,13,12800
buffer_load_dwordx4 v8, s[16:19], s92 offen offset:0 lds
Mv 48,40,160
Rt 24,14,12288
)" R"(
v_accvgpr_read_b32 v254, a252
Mv 56,40,164
Rt 26,14,12800
v_accvgpr_read_b32 v24, a253
Mv 60,44,164
Rt 28,15,12288
Cv 254,254,24
v_accvgpr_read_b32 v255, a254
Mv 52,44,160
Rt 30,15,12800
buffer_load_dwordx4 v9, s[16:19], s92 offen offset:1024 lds
s_waitcnt lgkmcnt(8)
Mv 32,0,168
Rt 32,12,16384
buffer_store_dwordx4 v[152:155], v3, s[20:23], s46 offen offset:3072 sc0 sc1
Mv 40,0,172
Rt 34,12,16896
v_accvgpr_read_b32 v25, a255
Cv 255,255,25
Mv 44,4,172
Rt 36,13,16384
Cv 160,160,161
Mv 36,4,168
Rt 38,13,16896
buffer_load_dwordx4 v10, s[16:19], s92 offen offset:2048 lds
Mv 48,8,168
Rt 40,14,16384
Cv 161,162,163
Cv 162,164,165
Mv 56,8,172
Rt 42,14,16896
s_add_u32 s92, s42, 0x1c0100
Mv 60,12,172
Rt 44,15,16384
s_add_u32 m0, s41, 131072
Mv 52,12,168
Rt 46,15,16896
buffer_load_dwordx4 v8, s[16:19], s92 offen offset:0 lds
s_waitcnt lgkmcnt(8)
Mv 32,16,176
Rt 0,12,20480
Cv 163,166,167
Mv 40,16,180
Rt 2,12,20992
Cv 168,168,169
Mv 44,20,180
Rt 4,13,20480
buffer_store_dwordx4 v[160:163], v2, s[20:23], s47 offen offset:0 sc0 sc1
Mv 36,20,176
Rt 6,13,20992
buffer_load_dwordx4 v9, s[16:19], s92 offen offset:1024 lds
Mv 48,24,176
Rt 8,14,20480
Cv 169,170,171
Mv 56,24,180
Rt 10,14,20992
Cv 170,172,173
Mv 60,28,180
Rt 12,15,20480
Cv 171,174,175
Cv 176,176,177
Mv 52,28,176
Rt 14,15,20992
buffer_load_dwordx4 v10, s[16:19], s92 offen offset:2048 lds
)" R"(
s_waitcnt lgkmcnt(8)
Mv 32,32,184
Rt 16,12,24576
Cv 177,178,179
Mv 40,32,188
Rt 18,12,25088
Cv 178,180,181
Cv 179,182,183
Mv 44,36,188
Rt 20,13,24576
Cv 184,184,185
Mv 36,36,184
Rt 22,13,25088
buffer_load_dwordx4 v11, s[16:19], s92 offen offset:3072 lds
Mv 48,40,184
Rt 24,14,24576
Cv 185,186,187
Cv 186,188,189
Mv 56,40,188
Rt 26,14,25088
s_add_u32 s92, s42, 0x200100
Mv 60,44,188
Rt 28,15,24576
s_add_u32 m0, s41, 135168
Mv 52,44,184
Rt 30,15,25088
buffer_load_dwordx4 v8, s[16:19], s92 offen offset:0 lds
s_waitcnt lgkmcnt(8)
Mv 32,0,48
Rt 32,16,0
Cv 187,190,191
Mv 40,0,52
Rt 34,16,512
Cv 192,192,193
Mv 44,4,52
Rt 36,17,0
buffer_store_dwordx4 v[168:171], v2, s[20:23], s47 offen offset:1024 sc0 sc1
Mv 36,4,48
Rt 38,17,512
buffer_load_dwordx4 v9, s[16:19], s92 offen offset:1024 lds
Mv 48,8,48
Rt 40,18,0
Cv 193,194,195
Mv 56,8,52
Rt 42,18,512
Cv 194,196,197
Mv 60,12,52
Rt 44,19,0
Cv 195,198,199
Cv 200,200,201
Mv 52,12,48
Rt 46,19,512
buffer_load_dwordx4 v10, s[16:19], s92 offen offset:2048 lds
s_waitcnt vmcnt(10) lgkmcnt(8)
s_barrier
buffer_store_dword v6, v7, s[20:23], s49 offen offset:4 sc0 sc1
Mv 32,16,56
Rt 0,16,4096
Cv 201,202,203
Mv 40,16,60
Rt 2,16,4608
Cv 202,204,205
Cv 203,206,207
Mv 44,20,60
Rt 4,17,4096
)" R"(
Cv 208,208,209
Mv 36,20,56
Rt 6,17,4608
s_add_u32 s92, s42, 0x180
Mv 48,24,56
Rt 8,18,4096
s_add_u32 m0, s41, 0
Cv 209,210,211
Mv 56,24,60
Rt 10,18,4608
buffer_load_dwordx4 v8, s[16:19], s92 offen offset:0 lds
Mv 60,28,60
Rt 12,19,4096
Cv 210,212,213
Mv 52,28,56
Rt 14,19,4608
Cv 211,214,215
s_waitcnt lgkmcnt(8)
Mz 64,32,192
Rt 16,16,8192
Cv 216,216,217
Mz 72,32,196
Rt 18,16,8704
Cv 217,218,219
Mz 76,36,196
Rt 20,17,8192
buffer_store_dwordx4 v[176:179], v2, s[20:23], s47 offen offset:2048 sc0 sc1
Mz 68,36,192
Rt 22,17,8704
Cv 218,220,221
Cv 219,222,223
Mz 80,40,192
Rt 24,18,8192
v_mul_f32_e32 v32, s54, v32
Mz 88,40,196
Rt 26,18,8704
buffer_load_dwordx4 v9, s[16:19], s92 offen offset:1024 lds
Mz 92,44,196
Rt 28,19,8192
v_mul_f32_e32 v33, s54, v33
Mz 84,44,192
Rt 30,19,8704
s_waitcnt lgkmcnt(8)
Mv 64,0,200
Rt 32,16,12288
Cv 32,32,33
Mv 72,0,204
Rt 34,16,12800
v_accvgpr_write_b32 a224, v32
Mv 76,4,204
Rt 36,17,12288
v_mul_f32_e32 v34, s54, v34
Mv 68,4,200
Rt 38,17,12800
v_mul_f32_e32 v35, s54, v35
Mv 80,8,200
Rt 40,18,12288
Cv 34,34,35
Mv 88,8,204
Rt 42,18,12800
buffer_load_dwordx4 v10, s[16:19], s92 offen offset:2048 lds
Mv 92,12,204
Rt 44,19,12288
v_accvgpr_write_b32 a225, v34
)" R"(
Mv 84,12,200
Rt 46,19,12800
s_waitcnt lgkmcnt(8)
Mv 64,16,208
Rt 0,16,16384
v_mul_f32_e32 v36, s54, v36
Mv 72,16,212
Rt 2,16,16896
v_mul_f32_e32 v37, s54, v37
Mv 76,20,212
Rt 4,17,16384
buffer_store_dwordx4 v[184:187], v2, s[20:23], s47 offen offset:3072 sc0 sc1
Mv 68,20,208
Rt 6,17,16896
Cv 36,36,37
Mv 80,24,208
Rt 8,18,16384
v_accvgpr_write_b32 a226, v36
Mv 88,24,212
Rt 10,18,16896
buffer_load_dwordx4 v11, s[16:19], s92 offen offset:3072 lds
Mv 92,28,212
Rt 12,19,16384
v_mul_f32_e32 v38, s54, v38
Mv 84,28,208
Rt 14,19,16896
s_waitcnt lgkmcnt(8)
Mv 64,32,216
Rt 16,16,20480
v_mul_f32_e32 v39, s54, v39
Mv 72,32,220
Rt 18,16,20992
Cv 38,38,39
Mv 76,36,220
Rt 20,17,20480
v_accvgpr_write_b32 a227, v38
Mv 68,36,216
Rt 22,17,20992
s_add_u32 s92, s42, 0x40180
Mv 80,40,216
Rt 24,18,20480
s_add_u32 m0, s41, 4096
Mv 88,40,220
Rt 26,18,20992
buffer_load_dwordx4 v8, s[16:19], s92 offen offset:0 lds
Mv 92,44,220
Rt 28,19,20480
v_mul_f32_e32 v40, s54, v40
Mv 84,44,216
Rt 30,19,20992
s_waitcnt lgkmcnt(8)
Mv 64,0,128
Rt 32,16,24576
v_mul_f32_e32 v41, s54, v41
Mv 72,0,132
Rt 34,16,25088
Cv 40,40,41
Mv 76,4,132
Rt 36,17,24576
buffer_store_dwordx4 v[192:195], v3, s[20:23], s47 offen offset:0 sc0 sc1
Mv 68,4,128
Rt 38,17,25088
v_accvgpr_write_b32 a228, v40
Mv 80,8,128
)" R"(
Rt 40,18,24576
v_mul_f32_e32 v42, s54, v42
Mv 88,8,132
Rt 42,18,25088
buffer_load_dwordx4 v9, s[16:19], s92 offen offset:1024 lds
Mv 92,12,132
Rt 44,19,24576
v_mul_f32_e32 v43, s54, v43
Mv 84,12,128
Rt 46,19,25088
s_waitcnt lgkmcnt(8)
Mv 64,16,136
Rt 0,12,32768
Cv 42,42,43
Mv 72,16,140
Rt 2,12,33280
v_accvgpr_write_b32 a229, v42
Mv 76,20,140
Rt 4,13,32768
v_mul_f32_e32 v44, s54, v44
Mv 68,20,136
Rt 6,13,33280
v_mul_f32_e32 v45, s54, v45
Mv 80,24,136
Rt 8,14,32768
Cv 44,44,45
Mv 88,24,140
Rt 10,14,33280
buffer_load_dwordx4 v10, s[16:19], s92 offen offset:2048 lds
Mv 92,28,140
Rt 12,15,32768
v_accvgpr_write_b32 a230, v44
Mv 84,28,136
Rt 14,15,33280
s_waitcnt vmcnt(20) lgkmcnt(8)
s_barrier
Mv 64,32,144
Rt 16,12,36864
v_mul_f32_e32 v46, s54, v46
Mv 72,32,148
Rt 18,12,37376
v_mul_f32_e32 v47, s54, v47
Mv 76,36,148
Rt 20,13,36864
buffer_store_dwordx4 v[200:203], v3, s[20:23], s47 offen offset:1024 sc0 sc1
Mv 68,36,144
Rt 22,13,37376
s_add_u32 s92, s42, 0x1c0180
Mv 80,40,144
Rt 24,14,36864
s_add_u32 m0, s41, 65536
Mv 88,40,148
Rt 26,14,37376
buffer_load_dwordx4 v8, s[16:19], s92 offen offset:0 lds
Mv 92,44,148
Rt 28,15,36864
Cv 46,46,47
Mv 84,44,144
Rt 30,15,37376
s_waitcnt lgkmcnt(8)
Mv 64,0,152
Rt 32,12,40960
v_accvgpr_write_b32 a231, v46
Mv 72,0,156
Rt 34,12,41472
)" R"(
v_mul_f32_e32 v48, s54, v48
Mv 76,4,156
Rt 36,13,40960
v_mul_f32_e32 v49, s54, v49
Mv 68,4,152
Rt 38,13,41472
Cv 48,48,49
Mv 80,8,152
Rt 40,14,40960
v_accvgpr_write_b32 a232, v48
Mv 88,8,156
Rt 42,14,41472
buffer_load_dwordx4 v9, s[16:19], s92 offen offset:1024 lds
Mv 92,12,156
Rt 44,15,40960
v_mul_f32_e32 v50, s54, v50
Mv 84,12,152
Rt 46,15,41472
s_waitcnt lgkmcnt(8)
Mv 64,16,160
Rt 0,12,45056
v_mul_f32_e32 v51, s54, v51
Mv 72,16,164
Rt 2,12,45568
Cv 50,50,51
Mv 76,20,164
Rt 4,13,45056
buffer_store_dwordx4 v[208:211], v3, s[20:23], s47 offen offset:2048 sc0 sc1
Mv 68,20,160
Rt 6,13,45568
v_accvgpr_write_b32 a233, v50
Mv 80,24,160
Rt 8,14,45056
v_mul_f32_e32 v52, s54, v52
Mv 88,24,164
Rt 10,14,45568
buffer_load_dwordx4 v10, s[16:19], s92 offen offset:2048 lds
Mv 92,28,164
Rt 12,15,45056
v_mul_f32_e32 v53, s54, v53
Mv 84,28,160
Rt 14,15,45568
s_waitcnt lgkmcnt(8)
Mv 64,32,168
Rt 16,12,49152
Cv 52,52,53
Mv 72,32,172
Rt 18,12,49664
v_accvgpr_write_b32 a234, v52
Mv 76,36,172
Rt 20,13,49152
v_mul_f32_e32 v54, s54, v54
Mv 68,36,168
Rt 22,13,49664
v_mul_f32_e32 v55, s54, v55
Mv 80,40,168
Rt 24,14,49152
buffer_load_dword v4, v5, s[20:23], s50 offen offset:0 sc0 sc1
Mv 88,40,172
Rt 26,14,49664
buffer_load_dwordx4 v11, s[16:19], s92 offen offset:3072 lds
Mv 92,44,172
Rt 28,15,49152
Cv 54,54,55
)" R"(
Mv 84,44,168
Rt 30,15,49664
s_waitcnt lgkmcnt(8)
Mv 64,0,176
Rt 32,12,53248
v_accvgpr_write_b32 a235, v54
Mv 72,0,180
Rt 34,12,53760
v_mul_f32_e32 v56, s54, v56
Mv 76,4,180
Rt 36,13,53248
buffer_store_dwordx4 v[216:219], v3, s[20:23], s47 offen offset:3072 sc0 sc1
Mv 68,4,176
Rt 38,13,53760
s_add_u32 s92, s42, 0x200180
Mv 80,8,176
Rt 40,14,53248
s_add_u32 m0, s41, 69632
Mv 88,8,180
Rt 42,14,53760
buffer_load_dwordx4 v8, s[16:19], s92 offen offset:0 lds
Mv 92,12,180
Rt 44,15,53248
v_mul_f32_e32 v57, s54, v57
Mv 84,12,176
Rt 46,15,53760
s_waitcnt lgkmcnt(8)
Mv 64,16,184
Rt 0,12,57344
Cv 56,56,57
Mv 72,16,188
Rt 2,12,57856
v_accvgpr_write_b32 a236, v56
Mv 76,20,188
Rt 4,13,57344
v_mul_f32_e32 v58, s54, v58
Mv 68,20,184
Rt 6,13,57856
v_mul_f32_e32 v59, s54, v59
Mv 80,24,184
Rt 8,14,57344
Cv 58,58,59
Mv 88,24,188
Rt 10,14,57856
buffer_load_dwordx4 v9, s[16:19], s92 offen offset:1024 lds
Mv 92,28,188
Rt 12,15,57344
v_accvgpr_write_b32 a237, v58
Mv 84,28,184
Rt 14,15,57856
s_waitcnt lgkmcnt(8)
Mv 64,32,48
Rt 16,16,32768
v_mul_f32_e32 v60, s54, v60
Mv 72,32,52
Rt 18,16,33280
v_mul_f32_e32 v61, s54, v61
Mv 76,36,52
Rt 20,17,32768
buffer_store_dwordx4 v[224:227], v2, s[20:23], s48 offen offset:0 sc0 sc1
Mv 68,36,48
Rt 22,17,33280
Cv 60,60,61
Mv 80,40,48
)" R"(
Rt 24,18,32768
v_accvgpr_write_b32 a238, v60
Mv 88,40,52
Rt 26,18,33280
buffer_load_dwordx4 v10, s[16:19], s92 offen offset:2048 lds
Mv 92,44,52
Rt 28,19,32768
v_mul_f32_e32 v62, s54, v62
Mv 84,44,48
Rt 30,19,33280
s_waitcnt vmcnt(6) lgkmcnt(8)
s_barrier
Mv 64,0,56
Rt 32,16,36864
v_cmp_eq_u32_e32 vcc, s3, v4
s_andn2_b64 s[28:29], s[60:61], vcc
s_cbranch_scc1 .Lzsp3
.Lzok3:
Mv 72,0,60
Rt 34,16,37376
s_add_u32 s40, s55, 0x10000
Mv 76,4,60
Rt 36,17,36864
v_mul_f32_e32 v63, s54, v63
Mv 68,4,56
Rt 38,17,37376
Cv 62,62,63
Mv 80,8,56
Rt 40,18,36864
s_add_u32 m0, s36, 32768
Mv 88,8,60
Rt 42,18,37376
buffer_load_dwordx4 v2, s[20:23], s40 offen offset:0 sc1 lds
Mv 92,12,60
Rt 44,19,36864
v_accvgpr_write_b32 a239, v62
Mv 84,12,56
Rt 46,19,37376
s_waitcnt lgkmcnt(8)
Mz 32,16,192
Rt 0,16,40960
Mz 40,16,196
Rt 2,16,41472
Mz 44,20,196
Rt 4,17,40960
buffer_store_dwordx4 v[232:235], v2, s[20:23], s48 offen offset:1024 sc0 sc1
Mz 36,20,192
Rt 6,17,41472
buffer_load_dwordx4 v2, s[20:23], s40 offen offset:1024 sc1 lds
Mz 48,24,192
Rt 8,18,40960
v_mul_f32_e32 v64, s54, v64
Mz 56,24,196
Rt 10,18,41472
v_mul_f32_e32 v65, s54, v65
Mz 60,28,196
Rt 12,19,40960
Cv 64,64,65
Mz 52,28,192
Rt 14,19,41472
s_waitcnt lgkmcnt(8)
Mv 32,32,200
Rt 16,16,45056
v_accvgpr_write_b32 a240, v64
Mv 40,32,204
)" R"(
Rt 18,16,45568
buffer_load_dwordx4 v2, s[20:23], s40 offen offset:2048 sc1 lds
Mv 44,36,204
Rt 20,17,45056
v_mul_f32_e32 v66, s54, v66
Mv 36,36,200
Rt 22,17,45568
v_mul_f32_e32 v67, s54, v67
Mv 48,40,200
Rt 24,18,45056
Cv 66,66,67
Mv 56,40,204
Rt 26,18,45568
v_accvgpr_write_b32 a241, v66
Mv 60,44,204
Rt 28,19,45056
v_mul_f32_e32 v68, s54, v68
Mv 52,44,200
Rt 30,19,45568
buffer_load_dwordx4 v2, s[20:23], s40 offen offset:3072 sc1 lds
s_waitcnt lgkmcnt(8)
Mv 32,0,208
Rt 32,16,49152
v_mul_f32_e32 v69, s54, v69
Mv 40,0,212
Rt 34,16,49664
Cv 68,68,69
Mv 44,4,212
Rt 36,17,49152
buffer_store_dwordx4 v[240:243], v2, s[20:23], s48 offen offset:2048 sc0 sc1
Mv 36,4,208
Rt 38,17,49664
v_accvgpr_write_b32 a242, v68
v_mul_f32_e32 v70, s54, v70
Mv 48,8,208
Rt 40,18,49152
s_add_u32 m0, s36, 36864
buffer_load_dword v4, v7, s[20:23], s52 offen offset:0 sc0 sc1
Mv 56,8,212
Rt 42,18,49664
buffer_load_dwordx4 v3, s[20:23], s40 offen offset:0 sc1 lds
Mv 60,12,212
Rt 44,19,49152
v_mul_f32_e32 v71, s54, v71
Mv 52,12,208
Rt 46,19,49664
s_waitcnt lgkmcnt(8)
Mv 32,16,216
Rt 0,16,53248
Cv 70,70,71
Mv 40,16,220
Rt 2,16,53760
v_accvgpr_write_b32 a243, v70
Mv 44,20,220
Rt 4,17,53248
v_mul_f32_e32 v72, s54, v72
Mv 36,20,216
Rt 6,17,53760
buffer_load_dwordx4 v3, s[20:23], s40 offen offset:1024 sc1 lds
Mv 48,24,216
Rt 8,18,53248
v_mul_f32_e32 v73, s54, v73
Mv 56,24,220
Rt 10,18,53760
)" R"(
Cv 72,72,73
Mv 60,28,220
Rt 12,19,53248
v_accvgpr_write_b32 a244, v72
Mv 52,28,216
Rt 14,19,53760
s_waitcnt lgkmcnt(8)
Mv 32,32,128
Rt 16,16,57344
v_mul_f32_e32 v74, s54, v74
Mv 40,32,132
Rt 18,16,57856
buffer_load_dwordx4 v3, s[20:23], s40 offen offset:2048 sc1 lds
Mv 44,36,132
Rt 20,17,57344
buffer_store_dwordx4 v[248:251], v2, s[20:23], s48 offen offset:3072 sc0 sc1
Mv 36,36,128
Rt 22,17,57856
v_mul_f32_e32 v75, s54, v75
Cv 74,74,75
Mv 48,40,128
Rt 24,18,57344
v_accvgpr_write_b32 a245, v74
Mv 56,40,132
Rt 26,18,57856
v_mul_f32_e32 v76, s54, v76
Mv 60,44,132
Rt 28,19,57344
v_mul_f32_e32 v77, s54, v77
Mv 52,44,128
Rt 30,19,57856
buffer_load_dwordx4 v3, s[20:23], s40 offen offset:3072 sc1 lds
s_waitcnt lgkmcnt(8)
Mv 32,0,136
Rt 32,20,0
Cv 76,76,77
Mv 40,0,140
Rt 34,20,512
v_accvgpr_write_b32 a246, v76
Mv 44,4,140
Rt 36,21,0
v_mul_f32_e32 v78, s54, v78
Mv 36,4,136
Rt 38,21,512
v_mul_f32_e32 v79, s54, v79
Mv 48,8,136
Rt 40,22,0
Cv 78,78,79
Mv 56,8,140
Rt 42,22,512
v_accvgpr_write_b32 a247, v78
Mv 60,12,140
Rt 44,23,0
v_mul_f32_e32 v80, s54, v80
Mv 52,12,136
Rt 46,23,512
s_waitcnt vmcnt(5) lgkmcnt(8)
s_barrier
buffer_store_dword v6, v7, s[20:23], s49 offen offset:8 sc0 sc1
Mv 32,16,144
Rt 0,20,4096
v_cmp_eq_u32_e32 vcc, s3, v4
s_andn2_b64 s[28:29], 1, vcc
s_cbranch_scc1 .Lzsp4
.Lzok4:
)" R"(
Mv 40,16,148
Rt 2,20,4608
v_mul_f32_e32 v81, s54, v81
Mv 44,20,148
Rt 4,21,4096
s_add_u32 m0, s36, 98304
buffer_store_dwordx4 v[228:231], v3, s[20:23], s48 offen offset:0 sc0 sc1
Mv 36,20,144
Rt 6,21,4608
buffer_load_dwordx4 v2, s[20:23], s55 offen offset:0 sc1 lds
Mv 48,24,144
Rt 8,22,4096
Cv 80,80,81
Mv 56,24,148
Rt 10,22,4608
v_accvgpr_write_b32 a248, v80
Mv 60,28,148
Rt 12,23,4096
buffer_load_dwordx4 a[64:67], v2, s[20:23], s51 offen offset:0 sc0 sc1
Mv 52,28,144
Rt 14,23,4608
s_waitcnt lgkmcnt(8)
Mv 32,32,152
Rt 16,20,8192
v_mul_f32_e32 v82, s54, v82
Mv 40,32,156
Rt 18,20,8704
buffer_load_dwordx4 v2, s[20:23], s55 offen offset:1024 sc1 lds
Mv 44,36,156
Rt 20,21,8192
v_mul_f32_e32 v83, s54, v83
Mv 36,36,152
Rt 22,21,8704
Cv 82,82,83
Mv 48,40,152
Rt 24,22,8192
buffer_load_dwordx4 a[68:71], v2, s[20:23], s51 offen offset:1024 sc0 sc1
Mv 56,40,156
Rt 26,22,8704
v_accvgpr_write_b32 a249, v82
Mv 60,44,156
Rt 28,23,8192
v_mul_f32_e32 v84, s54, v84
Mv 52,44,152
Rt 30,23,8704
buffer_load_dwordx4 v2, s[20:23], s55 offen offset:2048 sc1 lds
s_waitcnt lgkmcnt(8)
Mv 32,0,160
Rt 32,20,12288
v_mul_f32_e32 v85, s54, v85
Mv 40,0,164
Rt 34,20,12800
Cv 84,84,85
Mv 44,4,164
Rt 36,21,12288
buffer_load_dwordx4 a[72:75], v2, s[20:23], s51 offen offset:2048 sc0 sc1
buffer_store_dwordx4 v[236:239], v3, s[20:23], s48 offen offset:1024 sc0 sc1
Mv 36,4,160
Rt 38,21,12800
v_accvgpr_write_b32 a250, v84
v_mul_f32_e32 v86, s54, v86
Mv 48,8,160
Rt 40,22,12288
v_mul_f32_e32 v87, s54, v87
)" R"(
Mv 56,8,164
Rt 42,22,12800
buffer_load_dwordx4 v2, s[20:23], s55 offen offset:3072 sc1 lds
Mv 60,12,164
Rt 44,23,12288
Cv 86,86,87
Mv 52,12,160
Rt 46,23,12800
s_waitcnt lgkmcnt(8)
Mv 32,16,168
Rt 0,20,16384
buffer_load_dwordx4 a[76:79], v2, s[20:23], s51 offen offset:3072 sc0 sc1
Mv 40,16,172
Rt 2,20,16896
v_accvgpr_write_b32 a251, v86
Mv 44,20,172
Rt 4,21,16384
s_add_u32 m0, s36, 102400
Mv 36,20,168
Rt 6,21,16896
buffer_load_dwordx4 v3, s[20:23], s55 offen offset:0 sc1 lds
Mv 48,24,168
Rt 8,22,16384
v_mul_f32_e32 v88, s54, v88
Mv 56,24,172
Rt 10,22,16896
v_mul_f32_e32 v89, s54, v89
Mv 60,28,172
Rt 12,23,16384
buffer_load_dwordx4 a[80:83], v3, s[20:23], s51 offen offset:0 sc0 sc1
Mv 52,28,168
Rt 14,23,16896
s_waitcnt lgkmcnt(8)
Mv 32,32,176
Rt 16,20,20480
Cv 88,88,89
Mv 40,32,180
Rt 18,20,20992
buffer_load_dwordx4 v3, s[20:23], s55 offen offset:1024 sc1 lds
Mv 44,36,180
Rt 20,21,20480
buffer_store_dwordx4 v[244:247], v3, s[20:23], s48 offen offset:2048 sc0 sc1
Mv 36,36,176
Rt 22,21,20992
v_accvgpr_write_b32 a252, v88
v_mul_f32_e32 v90, s54, v90
Mv 48,40,176
Rt 24,22,20480
buffer_load_dwordx4 a[84:87], v3, s[20:23], s51 offen offset:1024 sc0 sc1
Mv 56,40,180
Rt 26,22,20992
v_mul_f32_e32 v91, s54, v91
Mv 60,44,180
Rt 28,23,20480
Cv 90,90,91
Mv 52,44,176
Rt 30,23,20992
buffer_load_dwordx4 v3, s[20:23], s55 offen offset:2048 sc1 lds
s_waitcnt lgkmcnt(8)
Mv 32,0,184
Rt 32,20,24576
v_accvgpr_write_b32 a253, v90
Mv 40,0,188
Rt 34,20,25088
)" R"(
v_mul_f32_e32 v92, s54, v92
Mv 44,4,188
Rt 36,21,24576
buffer_load_dwordx4 a[88:91], v3, s[20:23], s51 offen offset:2048 sc0 sc1
Mv 36,4,184
Rt 38,21,25088
v_mul_f32_e32 v93, s54, v93
Mv 48,8,184
Rt 40,22,24576
Cv 92,92,93
Mv 56,8,188
Rt 42,22,25088
buffer_load_dwordx4 v3, s[20:23], s55 offen offset:3072 sc1 lds
Mv 60,12,188
Rt 44,23,24576
v_accvgpr_write_b32 a254, v92
Mv 52,12,184
Rt 46,23,25088
s_waitcnt lgkmcnt(8)
Mv 32,16,48
Rt 0,12,0
buffer_load_dwordx4 a[92:95], v3, s[20:23], s51 offen offset:3072 sc0 sc1
Mv 40,16,52
Rt 2,12,512
v_mul_f32_e32 v94, s54, v94
Mv 44,20,52
Rt 4,13,0
buffer_store_dwordx4 v[252:255], v3, s[20:23], s48 offen offset:3072 sc0 sc1
Mv 36,20,48
Rt 6,13,512
v_mul_f32_e32 v95, s54, v95
Cv 94,94,95
Mv 48,24,48
Rt 8,14,0
v_accvgpr_write_b32 a255, v94
Mv 56,24,52
Rt 10,14,512
Mv 60,28,52
Rt 12,15,0
s_add_u32 s51, s51, 0x10000
buffer_load_dwordx4 a[96:99], v2, s[20:23], s51 offen offset:0 sc0 sc1
Mv 52,28,48
Rt 14,15,512
s_waitcnt lgkmcnt(8)
s_barrier
Mv 32,32,56
Rt 16,12,4096
Mv 40,32,60
Rt 18,12,4608
s_mov_b32 s83, 0xc108000
buffer_load_dword v4, v7, s[20:23], s83 offen offset:4 sc0 sc1
Mv 44,36,60
Rt 20,13,4096
s_add_u32 m0, s36, 131072
Mv 36,36,56
Rt 22,13,4608
buffer_load_dwordx4 v2, s[20:23], s56 offen offset:0 sc1 lds
Mv 48,40,56
Rt 24,14,4096
buffer_load_dwordx4 a[100:103], v2, s[20:23], s51 offen offset:1024 sc0 sc1
Mv 56,40,60
Rt 26,14,4608
Mv 60,44,60
Rt 28,15,4096
Mv 52,44,56
)" R"(
Rt 30,15,4608
s_waitcnt lgkmcnt(8)
Mz 96,0,192
Rt 32,12,8192
Mz 104,0,196
Rt 34,12,8704
buffer_load_dwordx4 v2, s[20:23], s56 offen offset:1024 sc1 lds
Mz 108,4,196
Rt 36,13,8192
buffer_load_dwordx4 a[104:107], v2, s[20:23], s51 offen offset:2048 sc0 sc1
Mz 100,4,192
Rt 38,13,8704
Mz 112,8,192
Rt 40,14,8192
v_mul_f32_e32 v32, s54, v32
Mz 120,8,196
Rt 42,14,8704
v_mul_f32_e32 v33, s54, v33
Mz 124,12,196
Rt 44,15,8192
Cv 32,32,33
Mz 116,12,192
Rt 46,15,8704
buffer_load_dwordx4 v2, s[20:23], s56 offen offset:2048 sc1 lds
s_waitcnt lgkmcnt(8)
Mv 96,16,200
Rt 0,12,12288
buffer_load_dwordx4 a[108:111], v2, s[20:23], s51 offen offset:3072 sc0 sc1
Mv 104,16,204
Rt 2,12,12800
v_accvgpr_write_b32 a192, v32
Mv 108,20,204
Rt 4,13,12288
v_mul_f32_e32 v34, s54, v34
Mv 100,20,200
Rt 6,13,12800
v_mul_f32_e32 v35, s54, v35
Mv 112,24,200
Rt 8,14,12288
Cv 34,34,35
Mv 120,24,204
Rt 10,14,12800
buffer_load_dwordx4 v2, s[20:23], s56 offen offset:3072 sc1 lds
Mv 124,28,204
Rt 12,15,12288
buffer_load_dwordx4 a[112:115], v3, s[20:23], s51 offen offset:0 sc0 sc1
Mv 116,28,200
Rt 14,15,12800
s_waitcnt lgkmcnt(8)
Mv 96,32,208
Rt 16,12,16384
v_accvgpr_write_b32 a193, v34
Mv 104,32,212
Rt 18,12,16896
v_mul_f32_e32 v36, s54, v36
Mv 108,36,212
Rt 20,13,16384
s_add_u32 m0, s36, 135168
Mv 100,36,208
Rt 22,13,16896
buffer_load_dwordx4 v3, s[20:23], s56 offen offset:0 sc1 lds
Mv 112,40,208
Rt 24,14,16384
buffer_load_dwordx4 a[116:119], v3, s[20:23], s51 offen offset:1024 sc0 sc1
)" R"(
Mv 120,40,212
Rt 26,14,16896
v_mul_f32_e32 v37, s54, v37
Mv 124,44,212
Rt 28,15,16384
Cv 36,36,37
Mv 116,44,208
Rt 30,15,16896
s_waitcnt lgkmcnt(8)
Mv 96,0,216
Rt 32,12,20480
v_accvgpr_write_b32 a194, v36
Mv 104,0,220
Rt 34,12,20992
buffer_load_dwordx4 v3, s[20:23], s56 offen offset:1024 sc1 lds
Mv 108,4,220
Rt 36,13,20480
buffer_load_dwordx4 a[120:123], v3, s[20:23], s51 offen offset:2048 sc0 sc1
Mv 100,4,216
Rt 38,13,20992
v_mul_f32_e32 v38, s54, v38
Mv 112,8,216
Rt 40,14,20480
v_mul_f32_e32 v39, s54, v39
Mv 120,8,220
Rt 42,14,20992
Cv 38,38,39
Mv 124,12,220
Rt 44,15,20480
v_accvgpr_write_b32 a195, v38
Mv 116,12,216
Rt 46,15,20992
buffer_load_dwordx4 v3, s[20:23], s56 offen offset:2048 sc1 lds
s_waitcnt lgkmcnt(8)
Mv 96,16,128
Rt 0,12,24576
buffer_load_dwordx4 a[124:127], v3, s[20:23], s51 offen offset:3072 sc0 sc1
Mv 104,16,132
Rt 2,12,25088
v_mul_f32_e32 v40, s54, v40
Mv 108,20,132
Rt 4,13,24576
v_mul_f32_e32 v41, s54, v41
Mv 100,20,128
Rt 6,13,25088
Cv 40,40,41
Mv 112,24,128
Rt 8,14,24576
v_accvgpr_write_b32 a196, v40
Mv 120,24,132
Rt 10,14,25088
buffer_load_dwordx4 v3, s[20:23], s56 offen offset:3072 sc1 lds
Mv 124,28,132
Rt 12,15,24576
v_mul_f32_e32 v42, s54, v42
Mv 116,28,128
Rt 14,15,25088
s_waitcnt lgkmcnt(8)
Mv 96,32,136
Rt 16,16,0
v_mul_f32_e32 v43, s54, v43
Mv 104,32,140
Rt 18,16,512
Cv 42,42,43
)" R"(
Mv 108,36,140
Rt 20,17,0
v_accvgpr_write_b32 a197, v42
Mv 100,36,136
Rt 22,17,512
v_mul_f32_e32 v44, s54, v44
Mv 112,40,136
Rt 24,18,0
v_mul_f32_e32 v45, s54, v45
Mv 120,40,140
Rt 26,18,512
Cv 44,44,45
Mv 124,44,140
Rt 28,19,0
v_accvgpr_write_b32 a198, v44
Mv 116,44,136
Rt 30,19,512
s_waitcnt vmcnt(15) lgkmcnt(8)
s_barrier
buffer_store_dword v6, v7, s[20:23], s49 offen offset:12 sc0 sc1
Mv 96,0,144
Rt 32,16,4096
v_cmp_eq_u32_e32 vcc, s3, v4
s_andn2_b64 s[28:29], 1, vcc
s_cbranch_scc1 .Lzsp6
.Lzok6:
Mv 104,0,148
Rt 34,16,4608
buffer_load_dword v4, v5, s[20:23], s50 offen offset:0 sc0 sc1
Mv 108,4,148
Rt 36,17,4096
s_lshl_b64 s[60:61], s[60:61], 8
v_add_u32 v8, 0x10000, v1
Mv 100,4,144
Rt 38,17,4608
s_or_b32 s60, s60, 0xff
v_add_u32 v10, 0x20000, v1
Mv 112,8,144
Rt 40,18,4096
v_mul_f32_e32 v46, s54, v46
s_add_u32 s56, s56, 0x10000
Mv 120,8,148
Rt 42,18,4608
v_mul_f32_e32 v47, s54, v47
s_add_u32 s55, s55, 0x10000
Mv 124,12,148
Rt 44,19,4096
Cv 46,46,47
s_add_u32 s55, s55, 0x10000
Mv 116,12,144
Rt 46,19,4608
s_waitcnt lgkmcnt(8)
Mv 96,16,152
Rt 0,16,8192
v_accvgpr_write_b32 a199, v46
Mv 104,16,156
Rt 2,16,8704
v_mul_f32_e32 v48, s54, v48
Mv 108,20,156
Rt 4,17,8192
v_mul_f32_e32 v49, s54, v49
Mv 100,20,152
Rt 6,17,8704
Cv 48,48,49
s_cmp_eq_u32 s57, 2
)" R"(
s_cselect_b32 s55, s59, s55
Mv 112,24,152
Rt 8,18,8192
v_accvgpr_write_b32 a200, v48
s_mov_b32 s93, 0
Mv 120,24,156
Rt 10,18,8704
v_mul_f32_e32 v50, s54, v50
s_mov_b32 s94, 1
Mv 124,28,156
Rt 12,19,8192
v_mul_f32_e32 v51, s54, v51
Mv 116,28,152
Rt 14,19,8704
s_waitcnt lgkmcnt(8)
Mv 96,32,160
Rt 16,16,12288
Cv 50,50,51
Mv 104,32,164
Rt 18,16,12800
v_accvgpr_write_b32 a201, v50
Mv 108,36,164
Rt 20,17,12288
v_mul_f32_e32 v52, s54, v52
Mv 100,36,160
Rt 22,17,12800
v_mul_f32_e32 v53, s54, v53
Mv 112,40,160
Rt 24,18,12288
Cv 52,52,53
Mv 120,40,164
Rt 26,18,12800
v_accvgpr_write_b32 a202, v52
Mv 124,44,164
Rt 28,19,12288
v_mul_f32_e32 v54, s54, v54
Mv 116,44,160
Rt 30,19,12800
s_waitcnt lgkmcnt(8)
Mv 96,0,168
Rt 32,16,16384
v_mul_f32_e32 v55, s54, v55
Mv 104,0,172
Rt 34,16,16896
Cv 54,54,55
Mv 108,4,172
Rt 36,17,16384
v_accvgpr_write_b32 a203, v54
Mv 100,4,168
Rt 38,17,16896
v_mul_f32_e32 v56, s54, v56
Mv 112,8,168
Rt 40,18,16384
v_mul_f32_e32 v57, s54, v57
Mv 120,8,172
Rt 42,18,16896
Cv 56,56,57
Mv 124,12,172
Rt 44,19,16384
v_accvgpr_write_b32 a204, v56
Mv 116,12,168
Rt 46,19,16896
s_waitcnt lgkmcnt(8)
Mv 96,16,176
)" R"(
Rt 0,16,20480
v_mul_f32_e32 v58, s54, v58
Mv 104,16,180
Rt 2,16,20992
v_mul_f32_e32 v59, s54, v59
Mv 108,20,180
Rt 4,17,20480
Cv 58,58,59
Mv 100,20,176
Rt 6,17,20992
v_accvgpr_write_b32 a205, v58
Mv 112,24,176
Rt 8,18,20480
v_mul_f32_e32 v60, s54, v60
Mv 120,24,180
Rt 10,18,20992
v_mul_f32_e32 v61, s54, v61
Mv 124,28,180
Rt 12,19,20480
Cv 60,60,61
Mv 116,28,176
Rt 14,19,20992
s_waitcnt vmcnt(0) lgkmcnt(8)
s_barrier
Mv 96,32,184
Rt 16,16,24576
v_cmp_eq_u32_e32 vcc, s3, v4
s_andn2_b64 s[28:29], s[60:61], vcc
s_cbranch_scc1 .Lzsp5
.Lzok5:
Mv 104,32,188
Rt 18,16,25088
v_mov_b32 v24, 0
buffer_load_dword v4, v24, s[20:23], s83 offen sc0 sc1
Mv 108,36,188
Rt 20,17,24576
v_accvgpr_write_b32 a206, v60
Mv 100,36,184
Rt 22,17,25088
v_mul_f32_e32 v62, s54, v62
Mv 112,40,184
Rt 24,18,24576
v_mul_f32_e32 v63, s54, v63
Mv 120,40,188
Rt 26,18,25088
Cv 62,62,63
Mv 124,44,188
Rt 28,19,24576
v_accvgpr_write_b32 a207, v62
Mv 116,44,184
Rt 30,19,25088
s_waitcnt lgkmcnt(8)
Mv 96,0,48
Mv 104,0,52
Rb 0,8,32768
Mv 108,4,52
Mv 100,4,48
Rb 4,8,33792
Mv 112,8,48
Mv 120,8,52
Rb 8,8,34816
Mv 124,12,52
Mv 116,12,48
Rb 12,8,35840
s_waitcnt lgkmcnt(4)
)" R"(
Mv 96,16,56
Mv 104,16,60
Rb 16,8,40960
Mv 108,20,60
Mv 100,20,56
Rb 20,8,41984
Mv 112,24,56
Mv 120,24,60
Rb 24,8,43008
Mv 124,28,60
Mv 116,28,56
Rb 28,8,44032
Rb 32,8,36864
Rb 36,8,37888
Rb 40,8,38912
Rb 44,8,39936
s_cmp_eq_u32 s35, 7
s_cbranch_scc1 .LzA7
.LzA7R:
s_waitcnt lgkmcnt(8)
Mz 64,0,64
Rb 48,8,45056
v_mul_f32_e32 v96, s54, v96
Mz 68,0,68
v_mul_f32_e32 v97, s54, v97
Cv 96,96,97
v_accvgpr_write_b32 a208, v96
Mv 68,4,76
Rb 52,8,46080
s_add_u32 m0, s36, 0
Mv 64,4,72
buffer_load_dwordx4 v2, s[20:23], s55 offen offset:0 sc1 lds
v_mul_f32_e32 v98, s54, v98
Mv 64,8,80
Rb 56,8,47104
v_mul_f32_e32 v99, s54, v99
Mv 68,8,84
Cv 98,98,99
v_accvgpr_write_b32 a209, v98
v_mul_f32_e32 v100, s54, v100
Mv 68,12,92
Rb 60,8,48128
s_waitcnt lgkmcnt(8)
Mv 64,12,88
v_mul_f32_e32 v101, s54, v101
Cv 100,100,101
v_accvgpr_write_b32 a210, v100
Mv 64,16,96
Rb 0,8,49152
v_mul_f32_e32 v102, s54, v102
v_mul_f32_e32 v103, s54, v103
Mv 68,16,100
Cv 102,102,103
v_accvgpr_write_b32 a211, v102
Mv 68,20,108
Rb 4,8,50176
v_mul_f32_e32 v104, s54, v104
v_mul_f32_e32 v105, s54, v105
Mv 64,20,104
buffer_load_dwordx4 v2, s[20:23], s55 offen offset:1024 sc1 lds
Mv 64,24,112
Rb 8,8,51200
Cv 104,104,105
v_accvgpr_write_b32 a212, v104
)" R"(
Mv 68,24,116
v_mul_f32_e32 v106, s54, v106
v_mul_f32_e32 v107, s54, v107
Mv 68,28,124
Rb 12,8,52224
Cv 106,106,107
s_waitcnt lgkmcnt(8)
Mv 64,28,120
v_accvgpr_write_b32 a213, v106
v_mul_f32_e32 v108, s54, v108
v_mul_f32_e32 v109, s54, v109
Mz 72,32,64
Rb 16,8,57344
Cv 108,108,109
Mz 76,32,68
v_accvgpr_write_b32 a214, v108
v_mul_f32_e32 v110, s54, v110
v_mul_f32_e32 v111, s54, v111
Mv 76,36,76
Rb 20,8,58368
Cv 110,110,111
Mv 72,36,72
buffer_load_dwordx4 v2, s[20:23], s55 offen offset:2048 sc1 lds
v_accvgpr_write_b32 a215, v110
Mv 72,40,80
Rb 24,8,59392
v_mul_f32_e32 v112, s54, v112
Mv 76,40,84
v_mul_f32_e32 v113, s54, v113
Cv 112,112,113
v_accvgpr_write_b32 a216, v112
Mv 76,44,92
Rb 28,8,60416
s_waitcnt vmcnt(3) lgkmcnt(8)
Mv 72,44,88
v_mul_f32_e32 v114, s54, v114
v_mul_f32_e32 v115, s54, v115
Cv 114,114,115
Mv 72,48,96
Rb 32,8,53248
v_accvgpr_write_b32 a217, v114
v_mul_f32_e32 v116, s54, v116
Mv 76,48,100
v_mul_f32_e32 v117, s54, v117
Cv 116,116,117
Mv 76,52,108
Rb 36,8,54272
v_accvgpr_write_b32 a218, v116
v_mul_f32_e32 v118, s54, v118
Mv 72,52,104
buffer_load_dwordx4 v2, s[20:23], s55 offen offset:3072 sc1 lds
Mv 72,56,112
Rb 40,8,55296
v_mul_f32_e32 v119, s54, v119
Cv 118,118,119
Mv 76,56,116
v_accvgpr_write_b32 a219, v118
v_mul_f32_e32 v120, s54, v120
Mv 76,60,124
Rb 44,8,56320
v_mul_f32_e32 v121, s54, v121
s_waitcnt lgkmcnt(8)
Mv 72,60,120
Cv 120,120,121
)" R"(
v_accvgpr_write_b32 a220, v120
v_mul_f32_e32 v122, s54, v122
Mz 80,0,64
Rb 48,8,61440
v_mul_f32_e32 v123, s54, v123
Mz 84,0,68
Cv 122,122,123
v_accvgpr_write_b32 a221, v122
v_mul_f32_e32 v124, s54, v124
Mv 84,4,76
Rb 52,8,62464
s_add_u32 m0, s36, 4096
Mv 80,4,72
buffer_load_dwordx4 v3, s[20:23], s55 offen offset:0 sc1 lds
v_mul_f32_e32 v125, s54, v125
Mv 80,8,80
Rb 56,8,63488
Cv 124,124,125
Mv 84,8,84
v_accvgpr_write_b32 a222, v124
v_mul_f32_e32 v126, s54, v126
v_mul_f32_e32 v127, s54, v127
Mv 84,12,92
Rb 60,8,64512
s_waitcnt lgkmcnt(8)
Mv 80,12,88
Cv 126,126,127
v_accvgpr_write_b32 a223, v126
v_mov_b32 v16, 0
Mv 80,16,96
v_mov_b32 v17, 0
v_mov_b32 v18, 0
v_mov_b32 v19, 0
Mv 84,16,100
v_mov_b32 v20, 0
v_mov_b32 v21, 0
Mv 84,20,108
v_mov_b32 v22, 0
v_mov_b32 v23, 0
v_readfirstlane_b32 s63, v4
Mv 80,20,104
buffer_load_dwordx4 v3, s[20:23], s55 offen offset:1024 sc1 lds
Mv 80,24,112
v_and_b32 v24, 16, v1
v_lshrrev_b32 v24, v24, v4
v_and_b32 v24, 0x7ff, v24
Mv 84,24,116
v_mul_u32_u24 v24, 896, v24
v_and_b32 v25, 14, v0
Mv 84,28,124
v_min_u32 v25, 1, v25
v_lshlrev_b32 v25, 30, v25
v_and_or_b32 v25, v0, 48, v25
Mv 80,28,120
v_add_u32 v24, v24, v25
s_waitcnt lgkmcnt(0)
s_barrier
Mz 88,32,64
Rb 0,1,32768
s_and_b32 s83, s33, 1
Mz 92,32,68
Rb 4,1,33792
s_mul_i32 s82, s83, 448
Mv 92,36,76
Rb 8,1,34816
)" R"(
Mv 88,36,72
Rb 12,1,35840
buffer_load_dwordx4 v3, s[20:23], s55 offen offset:2048 sc1 lds
Mv 88,40,80
Rb 16,1,40960
Mv 92,40,84
Rb 20,1,41984
buffer_load_dwordx4 a[128:131], v24, s[12:15], s82 offen offset:0
Mv 92,44,92
Rb 24,1,43008
Mv 88,44,88
Rb 28,1,44032
buffer_load_dwordx4 a[132:135], v24, s[12:15], s82 offen offset:64
Mv 88,48,96
Rb 32,1,36864
buffer_load_dwordx4 a[136:139], v24, s[12:15], s82 offen offset:128
Mv 92,48,100
Rb 36,1,37888
Mv 92,52,108
Rb 40,1,38912
buffer_load_dwordx4 a[140:143], v24, s[12:15], s82 offen offset:192
Mv 88,52,104
Rb 44,1,39936
buffer_load_dwordx4 v3, s[20:23], s55 offen offset:3072 sc1 lds
Mv 88,56,112
buffer_load_dwordx4 a[144:147], v24, s[12:15], s82 offen offset:256
Mv 92,56,116
buffer_load_dwordx4 a[148:151], v24, s[12:15], s82 offen offset:320
Mv 92,60,124
buffer_load_dwordx4 a[152:155], v24, s[12:15], s82 offen offset:384
s_waitcnt lgkmcnt(8)
Mv 88,60,120
Mz 96,0,64
Rb 48,1,45056
buffer_load_dword v4, v5, s[20:23], s50 offen offset:0 sc0 sc1
Mz 100,0,68
s_lshl_b64 s[60:61], s[60:61], 8
s_or_b32 s60, s60, 0xff
s_cmp_le_i32 s57, s58
s_cbranch_scc1 .Lzmk0
.Lzmr0:
Mv 100,4,76
Rb 52,1,46080
s_add_u32 m0, s36, 65536
s_sub_u32 s57, s57, 1
Mv 96,4,72
buffer_load_dwordx4 v2, s[20:23], s56 offen offset:0 sc1 lds
s_add_u32 s55, s55, 0x10000
Mv 96,8,80
Rb 56,1,47104
s_cmp_eq_u32 s57, 2
s_cselect_b32 s55, s59, s55
Mv 100,8,84
v_exp_f32_e32 v32, v64
Mv 100,12,92
Rb 60,1,48128
s_waitcnt lgkmcnt(8)
Mv 96,12,88
v_exp_f32_e32 v24, v65
Mv 96,16,96
Rb 0,1,49152
v_exp_f32_e32 v33, v66
Mv 100,16,100
v_exp_f32_e32 v25, v67
)" R"(
v_exp_f32_e32 v34, v72
Mv 100,20,108
Rb 4,1,50176
v_exp_f32_e32 v26, v73
Mv 96,20,104
buffer_load_dwordx4 v2, s[20:23], s56 offen offset:1024 sc1 lds
v_add_f32_e32 v14, v32, v24
Mv 96,24,112
Rb 8,1,51200
v_exp_f32_e32 v35, v74
Mv 100,24,116
v_exp_f32_e32 v27, v75
Cv 32,32,24
Mv 100,28,124
Rb 12,1,52224
v_add_f32_e32 v14, v14, v33
s_waitcnt lgkmcnt(8)
Mv 96,28,120
v_exp_f32_e32 v36, v68
v_add_f32_e32 v14, v14, v25
Mz 104,32,64
Rb 16,1,57344
v_exp_f32_e32 v28, v69
Mz 108,32,68
v_exp_f32_e32 v37, v70
v_exp_f32_e32 v29, v71
Mv 108,36,76
Rb 20,1,58368
v_exp_f32_e32 v38, v76
Mv 104,36,72
buffer_load_dwordx4 v2, s[20:23], s56 offen offset:2048 sc1 lds
Cv 33,33,25
Mv 104,40,80
Rb 24,1,59392
v_exp_f32_e32 v24, v77
Mv 108,40,84
v_exp_f32_e32 v39, v78
v_add_f32_e32 v14, v14, v34
Mv 108,44,92
Rb 28,1,60416
v_add_f32_e32 v14, v14, v26
s_waitcnt lgkmcnt(8)
Mv 104,44,88
v_exp_f32_e32 v25, v79
Cv 34,34,26
Mv 104,48,96
Rb 32,1,53248
v_exp_f32_e32 v40, v80
Mv 108,48,100
v_exp_f32_e32 v26, v81
v_add_f32_e32 v14, v14, v35
v_add_f32_e32 v14, v14, v27
Mv 108,52,108
Rb 36,1,54272
Cv 35,35,27
v_add_f32_e32 v15, v36, v28
Mv 104,52,104
buffer_load_dwordx4 v2, s[20:23], s56 offen offset:3072 sc1 lds
Cv 36,36,28
Mv 104,56,112
Rb 40,1,55296
v_exp_f32_e32 v41, v82
Mv 108,56,116
v_exp_f32_e32 v27, v83
)" R"(
v_add_f32_e32 v15, v15, v37
Mv 108,60,124
Rb 44,1,56320
v_add_f32_e32 v15, v15, v29
s_waitcnt lgkmcnt(8)
Mv 104,60,120
v_exp_f32_e32 v42, v88
Cv 37,37,29
Mz 112,0,64
Rb 48,1,61440
v_exp_f32_e32 v28, v89
Mz 116,0,68
v_exp_f32_e32 v43, v90
v_add_f32_e32 v15, v15, v38
v_add_f32_e32 v15, v15, v24
Mv 116,4,76
Rb 52,1,62464
s_add_u32 m0, s36, 69632
Cv 38,38,24
Mv 112,4,72
buffer_load_dwordx4 v3, s[20:23], s56 offen offset:0 sc1 lds
v_add_f32_e32 v15, v15, v39
Mv 112,8,80
Rb 56,1,63488
v_exp_f32_e32 v29, v91
Mv 116,8,84
v_exp_f32_e32 v44, v84
v_add_f32_e32 v15, v15, v25
Mv 116,12,92
Rb 60,1,64512
Cv 39,39,25
s_waitcnt lgkmcnt(8)
Mv 112,12,88
v_exp_f32_e32 v24, v85
v_add_f32_e32 v14, v14, v40
Mv 112,16,96
v_exp_f32_e32 v45, v86
v_add_f32_e32 v14, v14, v26
Mv 116,16,100
v_exp_f32_e32 v25, v87
Cv 40,40,26
v_add_f32_e32 v14, v14, v41
Mv 116,20,108
v_exp_f32_e32 v46, v92
v_add_f32_e32 v14, v14, v27
Mv 112,20,104
buffer_load_dwordx4 v3, s[20:23], s56 offen offset:1024 sc1 lds
Cv 41,41,27
Mv 112,24,112
v_exp_f32_e32 v26, v93
v_add_f32_e32 v14, v14, v42
Mv 116,24,116
v_exp_f32_e32 v47, v94
v_add_f32_e32 v14, v14, v28
Mv 116,28,124
v_exp_f32_e32 v27, v95
Cv 42,42,28
Mv 112,28,120
v_add_f32_e32 v14, v14, v43
s_waitcnt lgkmcnt(0)
s_barrier
Mz 120,32,64
Rb 0,10,0
v_add_f32_e32 v14, v14, v29
Cv 43,43,29
)" R"(
Mz 124,32,68
Rb 4,10,1024
v_add_f32_e32 v15, v15, v44
v_add_f32_e32 v15, v15, v24
Cv 44,44,24
Mv 124,36,76
Rb 8,10,2048
v_add_f32_e32 v15, v15, v45
v_add_f32_e32 v15, v15, v25
Mv 120,36,72
Rb 12,10,3072
buffer_load_dwordx4 v3, s[20:23], s56 offen offset:2048 sc1 lds
Mv 120,40,80
Rb 16,10,4096
Cv 45,45,25
v_add_f32_e32 v15, v15, v46
Mv 124,40,84
Rb 20,10,5120
v_add_f32_e32 v15, v15, v26
Cv 46,46,26
Mv 124,44,92
Rb 24,10,6144
v_add_f32_e32 v15, v15, v47
v_add_f32_e32 v15, v15, v27
Mv 120,44,88
Rb 28,10,7168
Cv 47,47,27
v_max_f32_e32 v11, v14, v15
Mv 120,48,96
Rb 32,10,8192
v_mov_b32 v24, v14
v_mov_b32 v25, v15
Mv 124,48,100
Rb 36,10,9216
v_permlane32_swap_b32_e32 v24, v25
v_add_f32_e32 v24, v24, v25
v_mov_b32 v25, v24
Mv 124,52,108
Rb 40,10,10240
v_permlane16_swap_b32_e32 v24, v25
v_add_f32_e32 v24, v24, v25
Mv 120,52,104
Rb 44,10,11264
buffer_load_dwordx4 v3, s[20:23], s56 offen offset:3072 sc1 lds
Mv 120,56,112
v_rcp_f32_e32 v24, v24
Mv 124,56,116
s_add_u32 s56, s56, 0x10000
v_max_f32_e32 v11, v11, v24
v_cmp_nge_f32_e32 vcc, s62, v11
Mv 124,60,124
s_waitcnt lgkmcnt(8)
s_cbranch_vccnz .Lzfx0
.Lzfr0:
Mv 120,60,120
v_mov_b32 v12, v14
v_mov_b32 v13, v15
v_mfma_f32_16x16x32_bf16 v[128:131], a[0:3], v[32:35], 0
Rb 48,10,12288
v_mfma_f32_16x16x32_bf16 v[136:139], a[0:3], v[36:39], 0
s_cmp_le_i32 s57, s58
s_cbranch_scc1 .Lzmk1
.Lzmr1:
v_mfma_f32_16x16x32_bf16 v[140:143], a[4:7], v[36:39], 0
)" R"(
Rb 52,10,13312
s_add_u32 m0, s36, 32768
v_mfma_f32_16x16x32_bf16 v[132:135], a[4:7], v[32:35], 0
buffer_load_dwordx4 v2, s[20:23], s55 offen offset:0 sc1 lds
v_mfma_f32_16x16x32_bf16 v[144:147], a[8:11], v[32:35], 0
Rb 56,10,14336
s_and_b32 s92, s2, 7
v_mfma_f32_16x16x32_bf16 v[152:155], a[8:11], v[36:39], 0
s_mul_i32 s92, s92, 14
s_lshr_b32 s82, s2, 3
v_mfma_f32_16x16x32_bf16 v[156:159], a[12:15], v[36:39], 0
Rb 60,10,15360
s_waitcnt lgkmcnt(8)
v_mfma_f32_16x16x32_bf16 v[148:151], a[12:15], v[32:35], 0
buffer_load_dwordx4 v2, s[20:23], s55 offen offset:1024 sc1 lds
v_mfma_f32_16x16x32_bf16 v[160:163], a[16:19], v[32:35], 0
Rb 0,10,16384
s_add_u32 s82, s82, s92
v_mfma_f32_16x16x32_bf16 v[168:171], a[16:19], v[36:39], 0
v_exp_f32_e32 v48, v96
v_mfma_f32_16x16x32_bf16 v[172:175], a[20:23], v[36:39], 0
Rb 4,10,17408
v_mfma_f32_16x16x32_bf16 v[164:167], a[20:23], v[32:35], 0
buffer_load_dwordx4 v2, s[20:23], s55 offen offset:2048 sc1 lds
v_mfma_f32_16x16x32_bf16 v[176:179], a[24:27], v[32:35], 0
Rb 8,10,18432
v_exp_f32_e32 v28, v97
v_mfma_f32_16x16x32_bf16 v[184:187], a[24:27], v[36:39], 0
v_exp_f32_e32 v49, v98
v_mfma_f32_16x16x32_bf16 v[188:191], a[28:31], v[36:39], 0
Rb 12,10,19456
s_waitcnt lgkmcnt(8)
v_mfma_f32_16x16x32_bf16 v[180:183], a[28:31], v[32:35], 0
buffer_load_dwordx4 v2, s[20:23], s55 offen offset:3072 sc1 lds
v_add_f32_e32 v14, v48, v28
v_mfma_f32_16x16x32_bf16 v[192:195], a[32:35], v[32:35], 0
Rb 16,10,20480
Cv 48,48,28
v_mfma_f32_16x16x32_bf16 v[200:203], a[32:35], v[36:39], 0
v_exp_f32_e32 v29, v99
v_add_f32_e32 v14, v14, v49
v_mfma_f32_16x16x32_bf16 v[204:207], a[36:39], v[36:39], 0
Rb 20,10,21504
s_add_u32 m0, s36, 36864
v_mfma_f32_16x16x32_bf16 v[196:199], a[36:39], v[32:35], 0
buffer_load_dwordx4 v3, s[20:23], s55 offen offset:0 sc1 lds
v_mfma_f32_16x16x32_bf16 v[208:211], a[40:43], v[32:35], 0
Rb 24,10,22528
v_exp_f32_e32 v50, v104
v_mfma_f32_16x16x32_bf16 v[216:219], a[40:43], v[36:39], 0
s_cmp_gt_u32 s33, 1
s_cselect_b32 s24, s78, s76
s_cselect_b32 s25, s79, s77
v_mfma_f32_16x16x32_bf16 v[220:223], a[44:47], v[36:39], 0
Rb 28,10,23552
s_waitcnt lgkmcnt(8)
v_mfma_f32_16x16x32_bf16 v[212:215], a[44:47], v[32:35], 0
buffer_load_dwordx4 v3, s[20:23], s55 offen offset:1024 sc1 lds
v_add_f32_e32 v14, v14, v29
v_mfma_f32_16x16x32_bf16 v[224:227], a[48:51], v[32:35], 0
Rb 32,10,24576
s_mov_b32 s27, 0x20000
v_mfma_f32_16x16x32_bf16 v[232:235], a[48:51], v[36:39], 0
s_cmp_lt_u32 s35, 7
)" R"(
s_cselect_b32 s26, 0x188000, 0
Cv 49,49,29
v_mfma_f32_16x16x32_bf16 v[236:239], a[52:55], v[36:39], 0
Rb 36,10,25600
s_mul_i32 s83, s83, 802816
v_mfma_f32_16x16x32_bf16 v[228:231], a[52:55], v[32:35], 0
buffer_load_dwordx4 v3, s[20:23], s55 offen offset:2048 sc1 lds
v_mfma_f32_16x16x32_bf16 v[240:243], a[56:59], v[32:35], 0
Rb 40,10,26624
s_lshl_b32 s92, s82, 5
v_add_f32_e32 v14, v14, v50
v_mfma_f32_16x16x32_bf16 v[248:251], a[56:59], v[36:39], 0
s_add_u32 s83, s83, s92
v_and_b32 v11, 62, v0
v_mul_u32_u24 v11, 0x6f0, v11
v_mfma_f32_16x16x32_bf16 v[252:255], a[60:63], v[36:39], 0
Rb 44,10,27648
s_waitcnt lgkmcnt(8)
v_mfma_f32_16x16x32_bf16 v[244:247], a[60:63], v[32:35], 0
buffer_load_dwordx4 v3, s[20:23], s55 offen offset:3072 sc1 lds
Mp 128,0,40
Rb 48,10,28672
v_add_u32 v11, v11, v1
Mp 136,0,44
v_exp_f32_e32 v24, v105
Mp 140,4,44
Rb 52,10,29696
v_add_f32_e32 v14, v14, v24
Mp 132,4,40
v_exp_f32_e32 v51, v106
Mp 144,8,40
Rb 56,10,30720
v_exp_f32_e32 v25, v107
Mp 152,8,44
v_exp_f32_e32 v52, v100
Mp 156,12,44
Rb 60,10,31744
s_waitcnt lgkmcnt(8)
Mp 148,12,40
v_exp_f32_e32 v26, v101
Cv 50,50,24
Mp 160,16,40
v_exp_f32_e32 v53, v102
Mp 168,16,44
v_exp_f32_e32 v27, v103
v_add_f32_e32 v14, v14, v51
Mp 172,20,44
v_exp_f32_e32 v54, v108
Mp 164,20,40
v_exp_f32_e32 v28, v109
Mp 176,24,40
v_exp_f32_e32 v55, v110
v_add_f32_e32 v14, v14, v25
Mp 184,24,44
v_exp_f32_e32 v29, v111
Mp 188,28,44
Cv 51,51,25
v_add_f32_e32 v15, v52, v26
Mp 180,28,40
Cv 52,52,26
s_waitcnt vmcnt(16) lgkmcnt(0)
s_barrier
Mp 192,32,40
Rb 0,1,0
s_add_u32 m0, s36, 131072
)" R"(
s_nop 0
buffer_load_dwordx4 v11, s[24:27], s83 offen offset:0 lds
Mp 200,32,44
Rb 4,1,1024
v_add_f32_e32 v15, v15, v53
v_add_f32_e32 v15, v15, v27
Mp 204,36,44
Rb 8,1,2048
s_add_u32 s83, s83, 0x1bc00
buffer_load_dwordx4 v11, s[24:27], s83 offen offset:1024 lds
Mp 196,36,40
Rb 12,1,3072
Cv 53,53,27
Mp 208,40,40
Rb 16,1,8192
s_add_u32 s83, s83, 0x1bc00
buffer_load_dwordx4 v11, s[24:27], s83 offen offset:2048 lds
Mp 216,40,44
Rb 20,1,9216
v_add_f32_e32 v15, v15, v54
Mp 220,44,44
Rb 24,1,10240
s_add_u32 s83, s83, 0x1bc00
buffer_load_dwordx4 v11, s[24:27], s83 offen offset:3072 lds
Mp 212,44,40
Rb 28,1,11264
v_add_f32_e32 v15, v15, v28
Cv 54,54,28
Mp 224,48,40
Rb 32,1,4096
s_add_u32 m0, s36, 135168
s_add_u32 s83, s83, 0x1cc00
buffer_load_dwordx4 v11, s[24:27], s83 offen offset:0 lds
Mp 232,48,44
Rb 36,1,5120
v_add_f32_e32 v15, v15, v55
v_add_f32_e32 v15, v15, v29
Mp 236,52,44
Rb 40,1,6144
s_add_u32 s83, s83, 0x1bc00
buffer_load_dwordx4 v11, s[24:27], s83 offen offset:1024 lds
Mp 228,52,40
Rb 44,1,7168
Cv 55,55,29
Mp 240,56,40
s_add_u32 s83, s83, 0x1bc00
buffer_load_dwordx4 v11, s[24:27], s83 offen offset:2048 lds
v_cmp_eq_u32_e32 vcc, s3, v4
s_andn2_b64 s[28:29], s[60:61], vcc
s_cbranch_scc1 .Lzsp7
.Lzok7:
Mp 248,56,44
s_sub_u32 s57, s57, 1
Mp 252,60,44
s_waitcnt lgkmcnt(8)
Mp 244,60,40
s_cmp_gt_i32 s57, s93
s_cbranch_scc0 .LzLX
.LzL:
v_mfma_f32_16x16x32_bf16 v[64:67], a[0:3], a[64:67], v[16:19]
Rb 48,1,12288
v_mfma_f32_16x16x32_bf16 v[68:71], a[0:3], a[68:71], v[20:23]
v_exp_f32_e32 v56, v112
Mv 68,4,76
)" R"(
Rb 52,1,13312
s_add_u32 m0, s36, 98304
Mv 64,4,72
buffer_load_dwordx4 v2, s[20:23], s56 offen offset:0 sc1 lds
Mv 64,8,80
Rb 56,1,14336
v_add_f32_e32 v14, v14, v56
Mv 68,8,84
v_exp_f32_e32 v24, v113
Mv 68,12,92
Rb 60,1,15360
s_waitcnt lgkmcnt(8)
Mv 64,12,88
v_exp_f32_e32 v57, v114
Mv 64,16,96
Rb 0,1,16384
v_add_f32_e32 v14, v14, v24
Mv 68,16,100
v_exp_f32_e32 v25, v115
Mv 68,20,108
Rb 4,1,17408
Cv 56,56,24
Mv 64,20,104
buffer_load_dwordx4 v2, s[20:23], s56 offen offset:1024 sc1 lds
Mv 64,24,112
Rb 8,1,18432
v_add_f32_e32 v14, v14, v57
Mv 68,24,116
v_exp_f32_e32 v58, v120
Mv 68,28,124
Rb 12,1,19456
s_waitcnt lgkmcnt(8)
Mv 64,28,120
v_exp_f32_e32 v26, v121
v_mfma_f32_16x16x32_bf16 v[72:75], a[32:35], a[64:67], v[16:19]
Rb 16,1,24576
v_add_f32_e32 v14, v14, v25
v_mfma_f32_16x16x32_bf16 v[76:79], a[32:35], a[68:71], v[20:23]
v_exp_f32_e32 v59, v122
Mv 76,36,76
Rb 20,1,25600
Cv 57,57,25
Mv 72,36,72
buffer_load_dwordx4 v2, s[20:23], s56 offen offset:2048 sc1 lds
Mv 72,40,80
Rb 24,1,26624
v_add_f32_e32 v14, v14, v58
Mv 76,40,84
v_exp_f32_e32 v27, v123
Mv 76,44,92
Rb 28,1,27648
s_waitcnt lgkmcnt(8)
Mv 72,44,88
v_exp_f32_e32 v60, v116
Mv 72,48,96
Rb 32,1,20480
v_add_f32_e32 v14, v14, v26
Mv 76,48,100
v_exp_f32_e32 v28, v117
Mv 76,52,108
Rb 36,1,21504
Cv 58,58,26
Mv 72,52,104
buffer_load_dwordx4 v2, s[20:23], s56 offen offset:3072 sc1 lds
)" R"(
Mv 72,56,112
Rb 40,1,22528
v_add_f32_e32 v14, v14, v59
Mv 76,56,116
v_exp_f32_e32 v61, v118
Mv 76,60,124
Rb 44,1,23552
s_waitcnt lgkmcnt(8)
Mv 72,60,120
v_exp_f32_e32 v29, v119
v_mfma_f32_16x16x32_bf16 v[80:83], a[0:3], a[64:67], v[16:19]
Rb 48,1,28672
v_add_f32_e32 v14, v14, v27
v_mfma_f32_16x16x32_bf16 v[84:87], a[0:3], a[68:71], v[20:23]
v_exp_f32_e32 v62, v124
Mv 84,4,76
Rb 52,1,29696
s_add_u32 m0, s36, 102400
Mv 80,4,72
buffer_load_dwordx4 v3, s[20:23], s56 offen offset:0 sc1 lds
Mv 80,8,80
Rb 56,1,30720
Cv 59,59,27
Mv 84,8,84
v_exp_f32_e32 v24, v125
Mv 84,12,92
Rb 60,1,31744
s_waitcnt lgkmcnt(8)
Mv 80,12,88
v_exp_f32_e32 v63, v126
Mv 80,16,96
v_exp_f32_e32 v25, v127
Mv 84,16,100
v_add_f32_e32 v15, v15, v60
v_add_f32_e32 v15, v15, v28
Mv 84,20,108
Cv 60,60,28
v_add_f32_e32 v15, v15, v61
Mv 80,20,104
buffer_load_dwordx4 v3, s[20:23], s56 offen offset:1024 sc1 lds
Mv 80,24,112
v_add_f32_e32 v15, v15, v29
Cv 61,61,29
Mv 84,24,116
v_add_f32_e32 v15, v15, v62
v_add_f32_e32 v15, v15, v24
Mv 84,28,124
Cv 62,62,24
v_add_f32_e32 v15, v15, v63
Mv 80,28,120
s_waitcnt vmcnt(14) lgkmcnt(0)
s_barrier
v_mfma_f32_16x16x32_bf16 v[88:91], a[32:35], a[64:67], v[16:19]
Rb 0,8,0
v_add_f32_e32 v15, v15, v25
v_mfma_f32_16x16x32_bf16 v[92:95], a[32:35], a[68:71], v[20:23]
Rb 4,8,1024
Cv 63,63,25
Mv 92,36,76
Rb 8,8,2048
v_max_f32_e32 v11, v14, v15
Mv 88,36,72
Rb 12,8,3072
buffer_load_dwordx4 v3, s[20:23], s56 offen offset:2048 sc1 lds
Mv 88,40,80
)" R"(
Rb 16,8,4096
v_cmp_nge_f32_e32 vcc, s62, v11
Mv 92,40,84
Rb 20,8,5120
s_add_u32 s55, s55, 0x10000
Mv 92,44,92
Rb 24,8,6144
Mv 88,44,88
Rb 28,8,7168
Mv 88,48,96
Rb 32,8,8192
Mv 92,48,100
Rb 36,8,9216
Mv 92,52,108
Rb 40,8,10240
Mv 88,52,104
Rb 44,8,11264
buffer_load_dwordx4 v3, s[20:23], s56 offen offset:3072 sc1 lds
Mv 88,56,112
s_cmp_eq_u32 s57, 2
s_cselect_b32 s55, s59, s55
Mv 92,56,116
s_add_u32 s56, s56, 0x10000
Mv 92,60,124
s_waitcnt lgkmcnt(8)
s_cbranch_vccnz .Lzfx1
.Lzfr1:
Mv 88,60,120
v_add_f32_e32 v12, v12, v14
v_add_f32_e32 v13, v13, v15
Mp 128,0,48
Rb 48,8,12288
Mp 136,0,52
s_cmp_le_i32 s57, s58
s_cbranch_scc1 .Lzmk2
.Lzmr2:
Mp 140,4,52
Rb 52,8,13312
s_add_u32 m0, s36, 0
Mp 132,4,48
buffer_load_dwordx4 v2, s[20:23], s55 offen offset:0 sc1 lds
Mp 144,8,48
Rb 56,8,14336
Mp 152,8,52
v_exp_f32_e32 v32, v64
Mp 156,12,52
Rb 60,8,15360
s_waitcnt lgkmcnt(8)
Mp 148,12,48
v_exp_f32_e32 v26, v65
Mp 160,16,48
Rb 0,8,16384
v_add_f32_e32 v14, v32, v26
Mp 168,16,52
v_exp_f32_e32 v33, v66
Mp 172,20,52
Rb 4,8,17408
Cv 32,32,26
Mp 164,20,48
buffer_load_dwordx4 v2, s[20:23], s55 offen offset:1024 sc1 lds
Mp 176,24,48
Rb 8,8,18432
v_add_f32_e32 v14, v14, v33
Mp 184,24,52
)" R"(
v_exp_f32_e32 v27, v67
Mp 188,28,52
Rb 12,8,19456
s_waitcnt lgkmcnt(8)
Mp 180,28,48
v_exp_f32_e32 v34, v72
Mp 192,32,48
Rb 16,8,20480
v_add_f32_e32 v14, v14, v27
Mp 200,32,52
v_exp_f32_e32 v28, v73
Mp 204,36,52
Rb 20,8,21504
Cv 33,33,27
Mp 196,36,48
buffer_load_dwordx4 v2, s[20:23], s55 offen offset:2048 sc1 lds
Mp 208,40,48
Rb 24,8,22528
v_add_f32_e32 v14, v14, v34
Mp 216,40,52
v_exp_f32_e32 v35, v74
Mp 220,44,52
Rb 28,8,23552
s_waitcnt lgkmcnt(8)
Mp 212,44,48
v_exp_f32_e32 v29, v75
Mp 224,48,48
Rb 32,8,24576
v_add_f32_e32 v14, v14, v28
Mp 232,48,52
v_exp_f32_e32 v36, v68
Mp 236,52,52
Rb 36,8,25600
Cv 34,34,28
Mp 228,52,48
buffer_load_dwordx4 v2, s[20:23], s55 offen offset:3072 sc1 lds
Mp 240,56,48
Rb 40,8,26624
v_add_f32_e32 v14, v14, v35
Mp 248,56,52
v_exp_f32_e32 v24, v69
Mp 252,60,52
Rb 44,8,27648
s_waitcnt lgkmcnt(8)
Mp 244,60,48
v_exp_f32_e32 v37, v70
Mp 128,0,56
Rb 48,8,28672
v_add_f32_e32 v14, v14, v29
Mp 136,0,60
v_exp_f32_e32 v25, v71
Mp 140,4,60
Rb 52,8,29696
s_add_u32 m0, s36, 4096
Mp 132,4,56
buffer_load_dwordx4 v3, s[20:23], s55 offen offset:0 sc1 lds
Mp 144,8,56
Rb 56,8,30720
Cv 35,35,29
Mp 152,8,60
v_exp_f32_e32 v38, v76
Mp 156,12,60
Rb 60,8,31744
s_waitcnt lgkmcnt(8)
)" R"(
Mp 148,12,56
v_exp_f32_e32 v26, v77
Mp 160,16,56
v_exp_f32_e32 v39, v78
Mp 168,16,60
v_exp_f32_e32 v27, v79
Mp 172,20,60
v_add_f32_e32 v15, v36, v24
Cv 36,36,24
Mp 164,20,56
buffer_load_dwordx4 v3, s[20:23], s55 offen offset:1024 sc1 lds
Mp 176,24,56
v_add_f32_e32 v15, v15, v37
v_add_f32_e32 v15, v15, v25
Mp 184,24,60
Cv 37,37,25
v_add_f32_e32 v15, v15, v38
Mp 188,28,60
v_add_f32_e32 v15, v15, v26
Cv 38,38,26
Mp 180,28,56
s_waitcnt vmcnt(14) lgkmcnt(0)
s_barrier
Mp 192,32,56
Rb 0,1,32768
v_add_f32_e32 v15, v15, v39
Mp 200,32,60
Rb 4,1,33792
v_add_f32_e32 v15, v15, v27
Mp 204,36,60
Rb 8,1,34816
Cv 39,39,27
Mp 196,36,56
Rb 12,1,35840
buffer_load_dwordx4 v3, s[20:23], s55 offen offset:2048 sc1 lds
Mp 208,40,56
Rb 16,1,40960
s_sub_u32 s57, s57, 1
Mp 216,40,60
Rb 20,1,41984
Mp 220,44,60
Rb 24,1,43008
Mp 212,44,56
Rb 28,1,44032
Mp 224,48,56
Rb 32,1,36864
Mp 232,48,60
Rb 36,1,37888
Mp 236,52,60
Rb 40,1,38912
Mp 228,52,56
Rb 44,1,39936
buffer_load_dwordx4 v3, s[20:23], s55 offen offset:3072 sc1 lds
Mp 240,56,56
Mp 248,56,60
Mp 252,60,60
s_waitcnt lgkmcnt(8)
Mp 244,60,56
v_mfma_f32_16x16x32_bf16 v[96:99], a[0:3], a[64:67], v[16:19]
Rb 48,1,45056
buffer_load_dword v4, v5, s[20:23], s50 offen offset:0 sc0 sc1
v_mfma_f32_16x16x32_bf16 v[100:103], a[0:3], a[68:71], v[20:23]
s_lshl_b64 s[60:61], s[60:61], 8
s_or_b32 s60, s60, 0xff
Mv 100,4,76
)" R"(
Rb 52,1,46080
s_add_u32 m0, s36, 65536
Mv 96,4,72
buffer_load_dwordx4 v2, s[20:23], s56 offen offset:0 sc1 lds
Mv 96,8,80
Rb 56,1,47104
Mv 100,8,84
v_exp_f32_e32 v40, v80
Mv 100,12,92
Rb 60,1,48128
s_waitcnt lgkmcnt(8)
Mv 96,12,88
v_exp_f32_e32 v28, v81
Mv 96,16,96
Rb 0,1,49152
v_add_f32_e32 v14, v14, v40
Mv 100,16,100
v_exp_f32_e32 v41, v82
Mv 100,20,108
Rb 4,1,50176
v_add_f32_e32 v14, v14, v28
Mv 96,20,104
buffer_load_dwordx4 v2, s[20:23], s56 offen offset:1024 sc1 lds
Mv 96,24,112
Rb 8,1,51200
Cv 40,40,28
Mv 100,24,116
v_exp_f32_e32 v29, v83
Mv 100,28,124
Rb 12,1,52224
s_waitcnt lgkmcnt(8)
Mv 96,28,120
v_exp_f32_e32 v42, v88
v_mfma_f32_16x16x32_bf16 v[104:107], a[32:35], a[64:67], v[16:19]
Rb 16,1,57344
v_add_f32_e32 v14, v14, v41
v_mfma_f32_16x16x32_bf16 v[108:111], a[32:35], a[68:71], v[20:23]
v_exp_f32_e32 v24, v89
Mv 108,36,76
Rb 20,1,58368
v_add_f32_e32 v14, v14, v29
Mv 104,36,72
buffer_load_dwordx4 v2, s[20:23], s56 offen offset:2048 sc1 lds
Mv 104,40,80
Rb 24,1,59392
Cv 41,41,29
Mv 108,40,84
v_exp_f32_e32 v43, v90
Mv 108,44,92
Rb 28,1,60416
s_waitcnt lgkmcnt(8)
Mv 104,44,88
v_exp_f32_e32 v25, v91
Mv 104,48,96
Rb 32,1,53248
v_add_f32_e32 v14, v14, v42
Mv 108,48,100
v_exp_f32_e32 v44, v84
Mv 108,52,108
Rb 36,1,54272
v_add_f32_e32 v14, v14, v24
Mv 104,52,104
buffer_load_dwordx4 v2, s[20:23], s56 offen offset:3072 sc1 lds
Mv 104,56,112
)" R"(
Rb 40,1,55296
Cv 42,42,24
Mv 108,56,116
v_exp_f32_e32 v26, v85
Mv 108,60,124
Rb 44,1,56320
s_waitcnt lgkmcnt(8)
Mv 104,60,120
v_exp_f32_e32 v45, v86
v_mfma_f32_16x16x32_bf16 v[112:115], a[0:3], a[64:67], v[16:19]
Rb 48,1,61440
v_add_f32_e32 v14, v14, v43
v_mfma_f32_16x16x32_bf16 v[116:119], a[0:3], a[68:71], v[20:23]
v_exp_f32_e32 v27, v87
Mv 116,4,76
Rb 52,1,62464
s_add_u32 m0, s36, 69632
Mv 112,4,72
buffer_load_dwordx4 v3, s[20:23], s56 offen offset:0 sc1 lds
Mv 112,8,80
Rb 56,1,63488
v_add_f32_e32 v14, v14, v25
Mv 116,8,84
v_exp_f32_e32 v46, v92
Mv 116,12,92
Rb 60,1,64512
s_waitcnt lgkmcnt(8)
Mv 112,12,88
v_exp_f32_e32 v28, v93
Mv 112,16,96
v_exp_f32_e32 v47, v94
Mv 116,16,100
v_exp_f32_e32 v29, v95
Mv 116,20,108
Cv 43,43,25
v_add_f32_e32 v15, v15, v44
Mv 112,20,104
buffer_load_dwordx4 v3, s[20:23], s56 offen offset:1024 sc1 lds
Mv 112,24,112
v_add_f32_e32 v15, v15, v26
Cv 44,44,26
Mv 116,24,116
v_add_f32_e32 v15, v15, v45
v_add_f32_e32 v15, v15, v27
Mv 116,28,124
Cv 45,45,27
v_add_f32_e32 v15, v15, v46
Mv 112,28,120
s_waitcnt vmcnt(15) lgkmcnt(0)
s_barrier
v_mfma_f32_16x16x32_bf16 v[120:123], a[32:35], a[64:67], v[16:19]
Rb 0,8,32768
v_add_f32_e32 v15, v15, v28
v_mfma_f32_16x16x32_bf16 v[124:127], a[32:35], a[68:71], v[20:23]
Rb 4,8,33792
Cv 46,46,28
Mv 124,36,76
Rb 8,8,34816
v_add_f32_e32 v15, v15, v47
Mv 120,36,72
Rb 12,8,35840
buffer_load_dwordx4 v3, s[20:23], s56 offen offset:2048 sc1 lds
Mv 120,40,80
Rb 16,8,36864
v_add_f32_e32 v15, v15, v29
)" R"(
Mv 124,40,84
Rb 20,8,37888
Cv 47,47,29
Mv 124,44,92
Rb 24,8,38912
v_max_f32_e32 v11, v14, v15
Mv 120,44,88
Rb 28,8,39936
v_cmp_nge_f32_e32 vcc, s62, v11
Mv 120,48,96
Rb 32,8,40960
s_add_u32 s55, s55, 0x10000
Mv 124,48,100
Rb 36,8,41984
Mv 124,52,108
Rb 40,8,43008
Mv 120,52,104
Rb 44,8,44032
buffer_load_dwordx4 v3, s[20:23], s56 offen offset:3072 sc1 lds
Mv 120,56,112
s_cmp_eq_u32 s57, 2
s_cselect_b32 s55, s59, s55
Mv 124,56,116
s_add_u32 s56, s56, 0x10000
Mv 124,60,124
s_waitcnt lgkmcnt(8)
s_cbranch_vccnz .Lzfx2
.Lzfr2:
Mv 120,60,120
v_add_f32_e32 v12, v12, v14
v_add_f32_e32 v13, v13, v15
Mp 128,0,32
Rb 48,8,45056
Mp 136,0,36
s_cmp_le_i32 s57, s58
s_cbranch_scc1 .Lzmk3
.Lzmr3:
Mp 140,4,36
Rb 52,8,46080
s_add_u32 m0, s36, 32768
Mp 132,4,32
buffer_load_dwordx4 v2, s[20:23], s55 offen offset:0 sc1 lds
Mp 144,8,32
Rb 56,8,47104
Mp 152,8,36
v_exp_f32_e32 v48, v96
Mp 156,12,36
Rb 60,8,48128
s_waitcnt lgkmcnt(8)
Mp 148,12,32
v_exp_f32_e32 v24, v97
Mp 160,16,32
Rb 0,8,49152
v_add_f32_e32 v14, v48, v24
Mp 168,16,36
v_exp_f32_e32 v49, v98
Mp 172,20,36
Rb 4,8,50176
Cv 48,48,24
Mp 164,20,32
buffer_load_dwordx4 v2, s[20:23], s55 offen offset:1024 sc1 lds
Mp 176,24,32
Rb 8,8,51200
v_add_f32_e32 v14, v14, v49
)" R"(
Mp 184,24,36
v_exp_f32_e32 v25, v99
Mp 188,28,36
Rb 12,8,52224
s_waitcnt lgkmcnt(8)
Mp 180,28,32
v_exp_f32_e32 v50, v104
Mp 192,32,32
Rb 16,8,53248
v_add_f32_e32 v14, v14, v25
Mp 200,32,36
v_exp_f32_e32 v26, v105
Mp 204,36,36
Rb 20,8,54272
Cv 49,49,25
Mp 196,36,32
buffer_load_dwordx4 v2, s[20:23], s55 offen offset:2048 sc1 lds
Mp 208,40,32
Rb 24,8,55296
v_add_f32_e32 v14, v14, v50
Mp 216,40,36
v_exp_f32_e32 v51, v106
Mp 220,44,36
Rb 28,8,56320
s_waitcnt lgkmcnt(8)
Mp 212,44,32
v_exp_f32_e32 v27, v107
Mp 224,48,32
Rb 32,8,57344
v_add_f32_e32 v14, v14, v26
Mp 232,48,36
v_exp_f32_e32 v52, v100
Mp 236,52,36
Rb 36,8,58368
Cv 50,50,26
Mp 228,52,32
buffer_load_dwordx4 v2, s[20:23], s55 offen offset:3072 sc1 lds
Mp 240,56,32
Rb 40,8,59392
v_add_f32_e32 v14, v14, v51
Mp 248,56,36
v_exp_f32_e32 v28, v101
Mp 252,60,36
Rb 44,8,60416
s_waitcnt lgkmcnt(8)
Mp 244,60,32
v_exp_f32_e32 v53, v102
Mp 128,0,40
Rb 48,8,61440
v_add_f32_e32 v14, v14, v27
Mp 136,0,44
v_exp_f32_e32 v29, v103
Mp 140,4,44
Rb 52,8,62464
s_add_u32 m0, s36, 36864
Mp 132,4,40
buffer_load_dwordx4 v3, s[20:23], s55 offen offset:0 sc1 lds
Mp 144,8,40
Rb 56,8,63488
Cv 51,51,27
Mp 152,8,44
v_exp_f32_e32 v54, v108
Mp 156,12,44
Rb 60,8,64512
)" R"(
s_waitcnt lgkmcnt(8)
Mp 148,12,40
v_exp_f32_e32 v24, v109
Mp 160,16,40
v_exp_f32_e32 v55, v110
Mp 168,16,44
v_exp_f32_e32 v25, v111
Mp 172,20,44
v_add_f32_e32 v15, v52, v28
Cv 52,52,28
Mp 164,20,40
buffer_load_dwordx4 v3, s[20:23], s55 offen offset:1024 sc1 lds
Mp 176,24,40
v_add_f32_e32 v15, v15, v53
v_add_f32_e32 v15, v15, v29
Mp 184,24,44
Cv 53,53,29
v_add_f32_e32 v15, v15, v54
Mp 188,28,44
v_add_f32_e32 v15, v15, v24
Cv 54,54,24
Mp 180,28,40
s_waitcnt vmcnt(14) lgkmcnt(0)
s_barrier
Mp 192,32,40
Rb 0,1,0
v_add_f32_e32 v15, v15, v55
Mp 200,32,44
Rb 4,1,1024
v_add_f32_e32 v15, v15, v25
Mp 204,36,44
Rb 8,1,2048
Cv 55,55,25
Mp 196,36,40
Rb 12,1,3072
buffer_load_dwordx4 v3, s[20:23], s55 offen offset:2048 sc1 lds
Mp 208,40,40
Rb 16,1,8192
s_sub_u32 s57, s57, 1
Mp 216,40,44
Rb 20,1,9216
Mp 220,44,44
Rb 24,1,10240
Mp 212,44,40
Rb 28,1,11264
Mp 224,48,40
Rb 32,1,4096
Mp 232,48,44
Rb 36,1,5120
Mp 236,52,44
Rb 40,1,6144
Mp 228,52,40
Rb 44,1,7168
buffer_load_dwordx4 v3, s[20:23], s55 offen offset:3072 sc1 lds
Mp 240,56,40
v_cmp_eq_u32_e32 vcc, s3, v4
s_andn2_b64 s[28:29], s[60:61], vcc
s_cbranch_scc1 .Lzsp8
.Lzok8:
Mp 248,56,44
Mp 252,60,44
s_waitcnt lgkmcnt(8)
Mp 244,60,40
s_cmp_gt_i32 s57, s93
s_cbranch_scc1 .LzL
)" R"(
.LzLX:
s_cmp_eq_u32 s94, 0
s_cbranch_scc1 .LzEB
s_cmp_eq_u32 s94, 2
s_cbranch_scc1 .LzDA
s_lshl1_add_u32 s57, s35, 2
s_mov_b32 s59, s67
s_add_u32 s56, s89, 0x2000000
s_mov_b32 s94, 2
Mz 64,0,224
Rb 48,1,12288
v_lshrrev_b32 v4, 4, v0
v_accvgpr_mov_b32 a64, a224
Mz 68,0,228
v_lshlrev_b32 v4, 4, v4
v_add_lshl_u32 v4, v4, v0, 3
v_accvgpr_mov_b32 a65, a225
Mv 68,4,236
Rb 52,1,13312
s_add_u32 m0, s36, 98304
v_accvgpr_mov_b32 a66, a226
Mv 64,4,232
buffer_load_dwordx4 v2, s[20:23], s56 offen offset:0 sc1 lds
v_accvgpr_mov_b32 a67, a227
Mv 64,8,240
Rb 56,1,14336
s_add_u32 s92, s36, 131072
v_accvgpr_mov_b32 a68, a228
Mv 68,8,244
v_add_u32 v4, s92, v4
v_accvgpr_mov_b32 a69, a229
s_waitcnt vmcnt(1)
Mv 68,12,252
Rb 60,1,15360
v_accvgpr_mov_b32 a70, a230
s_waitcnt lgkmcnt(8)
Mv 64,12,248
buffer_load_dwordx4 v2, s[20:23], s56 offen offset:1024 sc1 lds
Rt 156,4,0
Mv 64,16,192
Rb 0,1,16384
Rt 158,4,128
v_accvgpr_mov_b32 a71, a231
Mv 68,16,196
Rt 160,4,1024
v_exp_f32_e32 v56, v112
Mv 68,20,204
Rb 4,1,17408
Rt 162,4,1152
v_add_f32_e32 v14, v14, v56
Mv 64,20,200
buffer_load_dwordx4 v2, s[20:23], s56 offen offset:2048 sc1 lds
Rt 164,4,2048
Mv 64,24,208
Rb 8,1,18432
Rt 166,4,2176
v_accvgpr_mov_b32 a72, a232
Mv 68,24,212
Rt 168,4,3072
v_exp_f32_e32 v26, v113
Mv 68,28,220
Rb 12,1,19456
Rt 170,4,3200
Mv 64,28,216
)" R"(
buffer_load_dwordx4 v2, s[20:23], s56 offen offset:3072 sc1 lds
Rt 172,4,4096
Mz 72,32,224
Rb 16,1,24576
Rt 174,4,4224
v_accvgpr_mov_b32 a73, a233
Mz 76,32,228
Rt 176,4,5120
v_exp_f32_e32 v57, v114
Mv 76,36,236
Rb 20,1,25600
s_add_u32 m0, s36, 102400
Rt 178,4,5248
Mv 72,36,232
buffer_load_dwordx4 v3, s[20:23], s56 offen offset:0 sc1 lds
Rt 180,4,6144
Mv 72,40,240
Rb 24,1,26624
Rt 182,4,6272
v_add_f32_e32 v14, v14, v26
Mv 76,40,244
v_exp_f32_e32 v27, v115
Cv 56,56,26
Mv 76,44,252
Rb 28,1,27648
v_add_f32_e32 v14, v14, v57
Mv 72,44,248
buffer_load_dwordx4 v3, s[20:23], s56 offen offset:1024 sc1 lds
v_add_f32_e32 v14, v14, v27
Mv 72,48,192
Rb 32,1,20480
v_and_b32 v4, 14, v0
v_accvgpr_mov_b32 a74, a234
Mv 76,48,196
v_min_u32 v4, 1, v4
v_lshlrev_b32 v4, 8, v4
Cv 57,57,27
Mv 76,52,204
Rb 36,1,21504
v_and_or_b32 v4, v0, 48, v4
v_accvgpr_mov_b32 a75, a235
Mv 72,52,200
buffer_load_dwordx4 v3, s[20:23], s56 offen offset:2048 sc1 lds
v_accvgpr_mov_b32 a76, a236
Mv 72,56,208
Rb 40,1,22528
v_lshlrev_b32 v4, 1, v4
v_accvgpr_mov_b32 a77, a237
Mv 76,56,212
v_and_or_b32 v4, v1, 16, v4
s_lshl_b32 s92, s33, 7
v_accvgpr_mov_b32 a78, a238
Mv 76,60,220
Rb 44,1,23552
v_accvgpr_mov_b32 a79, a239
s_waitcnt lgkmcnt(5)
Mv 72,60,216
v_mfma_f32_16x16x32_bf16 a[184:187], a[156:159], a[128:131], 0
buffer_load_dwordx4 v3, s[20:23], s56 offen offset:3072 sc1 lds
v_accvgpr_mov_b32 a80, a240
Mz 80,0,224
Ma 184,160,132
Rb 48,1,28672
s_add_u32 s92, s92, 162816
)" R"(
v_accvgpr_mov_b32 a81, a241
Mz 84,0,228
Ma 184,164,136
v_add_u32 v4, s92, v4
v_exp_f32_e32 v58, v120
Mv 84,4,236
Ma 184,168,140
Rb 52,1,29696
v_exp_f32_e32 v28, v121
Mv 80,4,232
Ma 184,172,144
v_exp_f32_e32 v59, v122
v_add_f32_e32 v14, v14, v58
Mv 80,8,240
Ma 184,176,148
Rb 56,1,30720
s_mov_b32 s24, s70
s_mov_b32 s25, s71
Mv 84,8,244
Ma 184,180,152
s_mul_i32 s83, s82, 14336
s_lshl_b32 s92, s33, 12
v_add_f32_e32 v14, v14, v28
Mv 84,12,252
Rb 60,1,31744
Cv 58,58,28
s_waitcnt lgkmcnt(8)
Mv 80,12,248
s_add_u32 s83, s83, s92
s_add_u32 m0, s92, 131072
v_add_f32_e32 v14, v14, v59
Mv 80,16,192
v_exp_f32_e32 v29, v123
v_accvgpr_mov_b32 a82, a242
Mv 84,16,196
v_exp_f32_e32 v60, v116
v_add_f32_e32 v14, v14, v29
Mv 84,20,204
ds_write_b128 v4, a[184:187]
v_and_b32 v4, 0x70, v4
Cv 59,59,29
Mv 80,20,200
v_add_u32 v4, 0x27c00, v4
v_exp_f32_e32 v24, v117
Mv 80,24,208
v_exp_f32_e32 v61, v118
v_add_f32_e32 v15, v15, v60
Mv 84,24,212
v_exp_f32_e32 v25, v119
v_add_f32_e32 v15, v15, v24
Mv 84,28,220
v_exp_f32_e32 v62, v124
Cv 60,60,24
Mv 80,28,216
v_add_f32_e32 v15, v15, v61
s_waitcnt lgkmcnt(0)
s_barrier
Mz 88,32,224
Rb 0,8,0
v_add_f32_e32 v15, v15, v25
v_accvgpr_mov_b32 a83, a243
Mz 92,32,228
Rb 4,8,16384
buffer_load_dwordx4 v1, s[24:27], s83 offen offset:0 lds
Mv 92,36,236
)" R"(
Rb 8,8,1024
v_exp_f32_e32 v26, v125
Mv 88,36,232
Rb 12,8,17408
v_exp_f32_e32 v63, v126
Mv 88,40,240
Rb 16,8,2048
buffer_load_dwordx4 v1, s[24:27], s83 offen offset:1024 lds
Mv 92,40,244
Rb 20,8,18432
v_exp_f32_e32 v27, v127
Mv 92,44,252
Rb 24,8,3072
Cv 61,61,25
v_add_f32_e32 v15, v15, v62
Mv 88,44,248
Rb 28,8,19456
v_add_f32_e32 v15, v15, v26
Cv 62,62,26
Mv 88,48,192
Rb 32,8,4096
v_add_f32_e32 v15, v15, v63
v_accvgpr_mov_b32 a84, a244
Mv 92,48,196
Rb 36,8,20480
buffer_load_dwordx4 v1, s[24:27], s83 offen offset:2048 lds
Mv 92,52,204
Rb 40,8,5120
v_add_f32_e32 v15, v15, v27
Cv 63,63,27
Mv 88,52,200
Rb 44,8,21504
v_max_f32_e32 v11, v14, v15
v_cmp_nge_f32_e32 vcc, s62, v11
Mv 88,56,208
buffer_load_dwordx4 v1, s[24:27], s83 offen offset:3072 lds
s_add_u32 s55, s55, 0x10000
Mv 92,56,212
s_add_u32 s56, s56, 0x10000
s_cmp_eq_u32 s57, 2
s_cselect_b32 s55, s59, s55
Mv 92,60,220
v_accvgpr_mov_b32 a85, a245
s_waitcnt lgkmcnt(8)
s_cbranch_vccnz .Lzfx3
.Lzfr3:
Mv 88,60,216
v_add_f32_e32 v12, v12, v14
v_add_f32_e32 v13, v13, v15
v_mov_b32 v14, v16
Mp 128,0,48
Rb 48,8,6144
v_mov_b32 v15, v20
v_accvgpr_mov_b32 a86, a246
Mp 136,0,52
buffer_store_dwordx4 v[12:15], v1, s[20:23], s66 offen sc0 sc1
s_cmp_le_i32 s57, s58
s_cbranch_scc1 .Lzmk4
.Lzmr4:
Mp 136,4,60
Rb 52,8,22528
s_add_u32 m0, s36, 0
v_accvgpr_mov_b32 a87, a247
Mp 128,4,56
)" R"(
buffer_load_dwordx4 v2, s[20:23], s55 offen offset:0 sc1 lds
v_accvgpr_mov_b32 a88, a248
Mp 132,8,48
Rb 56,8,7168
v_exp_f32_e32 v32, v64
Mp 140,8,52
v_exp_f32_e32 v28, v65
v_accvgpr_mov_b32 a89, a249
Mp 140,12,60
Rb 60,8,23552
v_exp_f32_e32 v33, v66
s_waitcnt lgkmcnt(8)
Mp 132,12,56
v_exp_f32_e32 v29, v67
v_add_f32_e32 v14, v32, v28
Mp 144,16,48
Rb 0,8,8192
v_exp_f32_e32 v34, v72
Mp 152,16,52
v_exp_f32_e32 v24, v73
v_exp_f32_e32 v35, v74
Mp 152,20,60
Rb 4,8,24576
v_exp_f32_e32 v25, v75
Mp 144,20,56
buffer_load_dwordx4 v2, s[20:23], s55 offen offset:1024 sc1 lds
Cv 128,128,129
Mp 148,24,48
Rb 8,8,9216
Cv 129,130,131
Cv 130,132,133
Cv 32,32,28
Mp 156,24,52
Cv 131,134,135
Cv 136,136,137
v_add_f32_e32 v14, v14, v33
Mp 156,28,60
Rb 12,8,25600
Cv 137,138,139
s_waitcnt lgkmcnt(8)
Mp 148,28,56
Cv 138,140,141
Cv 139,142,143
v_exp_f32_e32 v36, v68
Mp 160,32,48
Rb 16,8,10240
v_exp_f32_e32 v26, v69
Mp 168,32,52
v_exp_f32_e32 v37, v70
v_add_f32_e32 v14, v14, v29
Mp 168,36,60
Rb 20,8,26624
v_exp_f32_e32 v27, v71
Cv 33,33,29
Mp 160,36,56
buffer_load_dwordx4 v2, s[20:23], s55 offen offset:2048 sc1 lds
Cv 144,144,145
Mp 164,40,48
Rb 24,8,11264
Cv 145,146,147
Cv 146,148,149
Mp 172,40,52
buffer_store_dwordx4 v[128:131], v2, s[20:23], s64 offen offset:0 sc0 sc1
v_add_f32_e32 v14, v14, v34
)" R"(
Mp 172,44,60
Rb 28,8,27648
Cv 147,150,151
v_add_f32_e32 v14, v14, v24
s_waitcnt lgkmcnt(8)
Mp 164,44,56
Cv 152,152,153
Cv 153,154,155
Cv 154,156,157
Mp 176,48,48
Rb 32,8,12288
Cv 155,158,159
Cv 34,34,24
Mp 184,48,52
v_exp_f32_e32 v38, v76
v_exp_f32_e32 v28, v77
Mp 184,52,60
Rb 36,8,28672
v_exp_f32_e32 v39, v78
Mp 176,52,56
buffer_load_dwordx4 v2, s[20:23], s55 offen offset:3072 sc1 lds
Cv 160,160,161
Mp 180,56,48
Rb 40,8,13312
Cv 161,162,163
Cv 162,164,165
v_add_f32_e32 v14, v14, v35
Mp 188,56,52
buffer_store_dwordx4 v[136:139], v2, s[20:23], s64 offen offset:1024 sc0 sc1
v_add_f32_e32 v14, v14, v25
Mp 188,60,60
Rb 44,8,29696
Cv 163,166,167
s_waitcnt lgkmcnt(8)
Mp 180,60,56
Cv 168,168,169
Cv 169,170,171
Cv 170,172,173
Cv 35,35,25
Mp 192,0,48
Rb 48,8,14336
Cv 171,174,175
v_add_f32_e32 v15, v36, v26
Mp 200,0,52
buffer_store_dwordx4 v[144:147], v2, s[20:23], s64 offen offset:2048 sc0 sc1
Cv 36,36,26
Mp 200,4,60
Rb 52,8,30720
s_add_u32 m0, s36, 4096
v_exp_f32_e32 v29, v79
Mp 192,4,56
buffer_load_dwordx4 v3, s[20:23], s55 offen offset:0 sc1 lds
Cv 176,176,177
Mp 196,8,48
Rb 56,8,15360
Cv 177,178,179
Cv 178,180,181
Mp 204,8,52
Cv 179,182,183
Cv 184,184,185
v_add_f32_e32 v15, v15, v37
Mp 204,12,60
Rb 60,8,31744
Cv 185,186,187
)" R"(
v_add_f32_e32 v15, v15, v27
s_waitcnt lgkmcnt(8)
Mp 196,12,56
buffer_store_dwordx4 v[152:155], v2, s[20:23], s64 offen offset:3072 sc0 sc1
Cv 186,188,189
Mp 208,16,48
ds_read_b128 v[132:135], v4 offset:0
Cv 187,190,191
Cv 37,37,27
Mp 216,16,52
ds_read_b128 v[140:143], v4 offset:128
v_add_f32_e32 v15, v15, v38
v_add_f32_e32 v15, v15, v28
Cv 38,38,28
Mp 216,20,60
ds_read_b128 v[148:151], v4 offset:256
v_add_f32_e32 v15, v15, v39
v_add_f32_e32 v15, v15, v29
Mp 208,20,56
buffer_load_dwordx4 v3, s[20:23], s55 offen offset:1024 sc1 lds
ds_read_b128 v[156:159], v4 offset:384
Mp 212,24,48
Cv 192,192,193
Cv 193,194,195
Cv 194,196,197
Cv 39,39,29
Mp 220,24,52
Cv 195,198,199
Cv 200,200,201
s_sub_u32 s57, s57, 1
Mp 220,28,60
buffer_store_dwordx4 v[160:163], v3, s[20:23], s64 offen offset:0 sc0 sc1
Cv 201,202,203
Mp 212,28,56
Cv 202,204,205
v_mov_b32 v16, 0
s_waitcnt lgkmcnt(0)
s_barrier
Mp 224,32,48
Rb 0,1,32768
v_add_f32_e32 v132, v132, v140
v_mov_b32 v17, 0
Mp 232,32,52
Rb 4,1,33792
v_add_f32_e32 v133, v133, v141
Cv 203,206,207
Mp 232,36,60
Rb 8,1,34816
v_add_f32_e32 v134, v134, v142
v_mov_b32 v18, 0
v_mov_b32 v19, 0
Mp 224,36,56
Rb 12,1,35840
buffer_load_dwordx4 v3, s[20:23], s55 offen offset:2048 sc1 lds
Mp 228,40,48
Rb 16,1,40960
v_add_f32_e32 v135, v135, v143
buffer_store_dwordx4 v[168:171], v3, s[20:23], s64 offen offset:1024 sc0 sc1
Mp 236,40,52
Rb 20,1,41984
v_add_f32_e32 v148, v148, v156
v_mov_b32 v20, 0
Mp 236,44,60
Rb 24,1,43008
v_add_f32_e32 v149, v149, v157
)" R"(
Cv 208,208,209
v_mov_b32 v21, 0
Mp 228,44,56
Rb 28,1,44032
v_add_f32_e32 v150, v150, v158
v_add_f32_e32 v151, v151, v159
Mp 240,48,48
Rb 32,1,36864
Cv 132,132,132
v_mov_b32 v22, 0
Mp 248,48,52
Rb 36,1,37888
v_lshlrev_b32 v132, 16, v132
Cv 209,210,211
v_mov_b32 v23, 0
Mp 248,52,60
Rb 40,1,38912
Cv 148,148,148
buffer_store_dwordx4 v[176:179], v3, s[20:23], s64 offen offset:2048 sc0 sc1
Mp 240,52,56
Rb 44,1,39936
buffer_load_dwordx4 v3, s[20:23], s55 offen offset:3072 sc1 lds
Mp 244,56,48
v_lshlrev_b32 v148, 16, v148
Cv 133,133,133
Cv 210,212,213
Mp 252,56,52
v_lshlrev_b32 v133, 16, v133
Cv 149,149,149
v_accvgpr_mov_b32 a90, a250
Mp 252,60,60
v_lshlrev_b32 v149, 16, v149
Cv 211,214,215
s_waitcnt lgkmcnt(8)
Mp 244,60,56
Cv 134,134,134
v_lshlrev_b32 v134, 16, v134
Cv 150,150,150
Mz 96,0,224
Rb 48,1,45056
buffer_load_dword v4, v5, s[20:23], s50 offen offset:0 sc0 sc1
buffer_store_dwordx4 v[184:187], v3, s[20:23], s64 offen offset:3072 sc0 sc1
Mz 100,0,228
s_lshl_b64 s[60:61], s[60:61], 8
s_or_b32 s60, s60, 0xff
Cv 216,216,217
Mv 100,4,236
Rb 52,1,46080
s_add_u32 m0, s36, 65536
v_exp_f32_e32 v40, v80
Mv 96,4,232
buffer_load_dwordx4 v2, s[20:23], s56 offen offset:0 sc1 lds
Cv 217,218,219
v_add_f32_e32 v14, v14, v40
Mv 96,8,240
Rb 56,1,47104
v_lshlrev_b32 v150, 16, v150
Cv 218,220,221
Mv 100,8,244
Cv 135,135,135
v_lshlrev_b32 v135, 16, v135
v_exp_f32_e32 v24, v81
Mv 100,12,252
Rb 60,1,48128
)" R"(
s_add_u32 s64, s64, 0x10000
buffer_store_dwordx4 v[192:195], v2, s[20:23], s64 offen offset:0 sc0 sc1
s_waitcnt lgkmcnt(8)
Mv 96,12,248
Cv 151,151,151
v_lshlrev_b32 v151, 16, v151
v_mul_f32_e32 v140, 0xbfb8aa3b, v132
v_add_f32_e32 v14, v14, v24
Mv 96,16,192
Rb 0,1,49152
v_mul_f32_e32 v141, 0xbfb8aa3b, v133
v_exp_f32_e32 v41, v82
Mv 100,16,196
v_exp_f32_e32 v140, v140
Cv 219,222,223
Cv 40,40,24
Mv 100,20,204
Rb 4,1,50176
v_mul_f32_e32 v142, 0xbfb8aa3b, v134
v_exp_f32_e32 v25, v83
Mv 96,20,200
buffer_load_dwordx4 v2, s[20:23], s56 offen offset:1024 sc1 lds
Cv 224,224,225
v_add_f32_e32 v14, v14, v41
Mv 96,24,208
Rb 8,1,51200
v_mul_f32_e32 v143, 0xbfb8aa3b, v135
buffer_store_dwordx4 v[200:203], v2, s[20:23], s64 offen offset:1024 sc0 sc1
Mv 100,24,212
v_exp_f32_e32 v141, v141
v_exp_f32_e32 v42, v88
Mv 100,28,220
Rb 12,1,52224
Cv 225,226,227
v_add_f32_e32 v14, v14, v25
s_waitcnt lgkmcnt(8)
Mv 96,28,216
v_exp_f32_e32 v142, v142
v_add_f32_e32 v140, 1.0, v140
Cv 41,41,25
Mz 104,32,224
Rb 16,1,57344
v_add_f32_e32 v141, 1.0, v141
v_add_f32_e32 v14, v14, v42
Mz 108,32,228
v_exp_f32_e32 v143, v143
Cv 226,228,229
Mv 108,36,236
Rb 20,1,58368
v_add_f32_e32 v142, 1.0, v142
buffer_store_dwordx4 v[208:211], v2, s[20:23], s64 offen offset:2048 sc0 sc1
Mv 104,36,232
buffer_load_dwordx4 v2, s[20:23], s56 offen offset:2048 sc1 lds
Cv 227,230,231
Mv 104,40,240
Rb 24,1,59392
v_add_f32_e32 v143, 1.0, v143
Cv 232,232,233
Mv 108,40,244
v_rcp_f32_e32 v140, v140
v_exp_f32_e32 v26, v89
Mv 108,44,252
Rb 28,1,60416
Cv 233,234,235
)" R"(
v_add_f32_e32 v14, v14, v26
s_waitcnt lgkmcnt(8)
Mv 104,44,248
v_rcp_f32_e32 v141, v141
v_mul_f32_e32 v132, v132, v140
Cv 42,42,26
Mv 104,48,192
Rb 32,1,53248
v_mul_f32_e32 v133, v133, v141
buffer_store_dwordx4 v[216:219], v2, s[20:23], s64 offen offset:3072 sc0 sc1
Mv 108,48,196
v_rcp_f32_e32 v142, v142
Cv 234,236,237
Mv 108,52,204
Rb 36,1,54272
v_mul_f32_e32 v134, v134, v142
v_exp_f32_e32 v43, v90
Mv 104,52,200
buffer_load_dwordx4 v2, s[20:23], s56 offen offset:3072 sc1 lds
Cv 235,238,239
v_add_f32_e32 v14, v14, v43
Mv 104,56,208
Rb 40,1,55296
Cv 132,132,132
Cv 240,240,241
Mv 108,56,212
v_rcp_f32_e32 v143, v143
v_exp_f32_e32 v27, v91
Mv 108,60,220
Rb 44,1,56320
buffer_store_dwordx4 v[224:227], v3, s[20:23], s64 offen offset:0 sc0 sc1
s_waitcnt lgkmcnt(8)
Mv 104,60,216
v_mul_f32_e32 v135, v135, v143
v_lshlrev_b32 v132, 16, v132
Cv 133,133,133
v_add_f32_e32 v14, v14, v27
Mz 112,0,224
Rb 48,1,61440
v_lshlrev_b32 v133, 16, v133
Cv 43,43,27
Mz 116,0,228
Cv 134,134,134
v_lshlrev_b32 v134, 16, v134
Cv 241,242,243
Mv 116,4,236
Rb 52,1,62464
s_add_u32 m0, s36, 69632
v_exp_f32_e32 v44, v84
Mv 112,4,232
buffer_load_dwordx4 v3, s[20:23], s56 offen offset:0 sc1 lds
Cv 242,244,245
v_add_f32_e32 v15, v15, v44
Mv 112,8,240
Rb 56,1,63488
Cv 135,135,135
buffer_store_dwordx4 v[232:235], v3, s[20:23], s64 offen offset:1024 sc0 sc1
Mv 116,8,244
v_lshlrev_b32 v135, 16, v135
v_mul_f32_e32 v132, v132, v148
v_exp_f32_e32 v28, v85
Mv 116,12,252
Rb 60,1,64512
Cv 243,246,247
)" R"(
v_add_f32_e32 v15, v15, v28
s_waitcnt lgkmcnt(8)
Mv 112,12,248
v_mul_f32_e32 v133, v133, v149
v_mul_f32_e32 v134, v134, v150
v_mul_f32_e32 v135, v135, v151
Cv 44,44,28
Mv 112,16,192
Cv 140,132,133
Cv 141,134,135
v_exp_f32_e32 v45, v86
Mv 116,16,196
v_accvgpr_write_b32 a188, v140
v_accvgpr_write_b32 a189, v141
Cv 248,248,249
v_add_f32_e32 v15, v15, v45
Mv 116,20,204
v_lshrrev_b32 v156, 2, v0
v_mul_u32_u24 v156, 864, v156
buffer_store_dwordx4 v[240:243], v3, s[20:23], s64 offen offset:2048 sc0 sc1
Mv 112,20,200
buffer_load_dwordx4 v3, s[20:23], s56 offen offset:1024 sc1 lds
Cv 249,250,251
Mv 112,24,208
v_lshl_add_u32 v156, v0, 3, v156
s_mul_i32 s92, s33, 224
Cv 250,252,253
Mv 116,24,212
s_add_u32 s92, s92, 131072
v_add_u32 v156, s92, v156
v_exp_f32_e32 v29, v87
Mv 116,28,220
Cv 251,254,255
v_exp_f32_e32 v46, v92
v_add_f32_e32 v15, v15, v29
Mv 112,28,216
v_exp_f32_e32 v24, v93
s_waitcnt vmcnt(31) lgkmcnt(0)
s_barrier
Mz 120,32,224
Rb 0,8,32768
Rt 156,156,0
buffer_store_dwordx4 v[248:251], v3, s[20:23], s64 offen offset:3072 sc0 sc1
Mz 124,32,228
Rb 4,8,33792
Rt 158,156,32
v_exp_f32_e32 v47, v94
Mv 124,36,236
Rb 8,8,34816
Rt 160,156,64
v_exp_f32_e32 v25, v95
Mv 120,36,232
Rb 12,8,35840
buffer_load_dwordx4 v3, s[20:23], s56 offen offset:2048 sc1 lds
Rt 162,156,96
Mv 120,40,240
Rb 16,8,36864
Rt 164,156,128
Cv 45,45,29
v_add_f32_e32 v15, v15, v46
Mv 124,40,244
Rb 20,8,37888
Rt 166,156,160
v_add_f32_e32 v15, v15, v24
Cv 46,46,24
)" R"(
Mv 124,44,252
Rb 24,8,38912
Rt 168,156,192
v_add_f32_e32 v15, v15, v47
v_add_f32_e32 v15, v15, v25
Mv 120,44,248
Rb 28,8,39936
Cv 47,47,25
v_max_f32_e32 v11, v14, v15
v_mov_b32 v24, v14
Mv 120,48,192
Rb 32,8,40960
v_mov_b32 v25, v15
s_add_u32 s55, s55, 0x10000
Mv 124,48,196
Rb 36,8,41984
s_cmp_eq_u32 s57, 2
s_cselect_b32 s55, s59, s55
v_permlane32_swap_b32_e32 v24, v25
Mv 124,52,204
Rb 40,8,43008
v_add_f32_e32 v24, v24, v25
v_mov_b32 v25, v24
Mv 120,52,200
Rb 44,8,44032
buffer_load_dwordx4 v3, s[20:23], s56 offen offset:3072 sc1 lds
v_permlane16_swap_b32_e32 v24, v25
Mv 120,56,208
v_add_f32_e32 v24, v24, v25
v_rcp_f32_e32 v24, v24
Mv 124,56,212
s_add_u32 s56, s56, 0x10000
v_max_f32_e32 v11, v11, v24
v_cmp_nge_f32_e32 vcc, s62, v11
Mv 124,60,220
v_accvgpr_mov_b32 a91, a251
s_waitcnt lgkmcnt(12)
s_cbranch_vccnz .Lzfx4
.Lzfr4:
Mv 120,60,216
v_mov_b32 v12, v14
v_mov_b32 v13, v15
v_accvgpr_mov_b32 a92, a252
v_mfma_f32_16x16x32_bf16 v[128:131], a[0:3], v[32:35], 0
Rb 48,8,45056
v_accvgpr_mov_b32 a93, a253
v_accvgpr_mov_b32 a94, a254
v_mfma_f32_16x16x32_bf16 v[136:139], a[0:3], v[36:39], 0
v_accvgpr_mov_b32 a95, a255
s_cmp_le_i32 s57, s58
s_cbranch_scc1 .Lzmk5
.Lzmr5:
v_mfma_f32_16x16x32_bf16 v[140:143], a[4:7], v[36:39], 0
Rb 52,8,46080
s_add_u32 m0, s36, 32768
v_accvgpr_mov_b32 a96, a192
v_mfma_f32_16x16x32_bf16 v[132:135], a[4:7], v[32:35], 0
buffer_load_dwordx4 v2, s[20:23], s55 offen offset:0 sc1 lds
v_accvgpr_mov_b32 a97, a193
v_mfma_f32_16x16x32_bf16 v[144:147], a[8:11], v[32:35], 0
Rb 56,8,47104
v_and_b32 v11, 14, v0
v_accvgpr_mov_b32 a98, a194
v_mfma_f32_16x16x32_bf16 v[152:155], a[8:11], v[36:39], 0
)" R"(
v_min_u32 v11, 1, v11
v_lshlrev_b32 v11, 13, v11
v_accvgpr_mov_b32 a99, a195
v_mfma_f32_16x16x32_bf16 v[156:159], a[12:15], v[36:39], 0
Rb 60,8,48128
v_accvgpr_mov_b32 a100, a196
s_waitcnt lgkmcnt(8)
v_mfma_f32_16x16x32_bf16 v[148:151], a[12:15], v[32:35], 0
v_mfma_f32_16x16x16_bf16 a[128:131], a[156:157], a[188:189], 0
v_and_or_b32 v11, v0, 1, v11
v_mul_u32_u24 v11, 0x31000, v11
v_accvgpr_mov_b32 a101, a197
v_mfma_f32_16x16x32_bf16 v[160:163], a[16:19], v[32:35], 0
v_mfma_f32_16x16x16_bf16 a[132:135], a[158:159], a[188:189], 0
Rb 0,8,49152
v_and_or_b32 v11, v0, 48, v11
v_accvgpr_mov_b32 a102, a198
v_mfma_f32_16x16x32_bf16 v[168:171], a[16:19], v[36:39], 0
v_mfma_f32_16x16x16_bf16 a[136:139], a[160:161], a[188:189], 0
s_mul_i32 s83, s33, 50176
s_lshl_b32 s92, s82, 6
v_accvgpr_mov_b32 a103, a199
v_mfma_f32_16x16x32_bf16 v[172:175], a[20:23], v[36:39], 0
v_mfma_f32_16x16x16_bf16 a[140:143], a[162:163], a[188:189], 0
Rb 4,8,50176
s_add_u32 s83, s83, s92
v_accvgpr_mov_b32 a104, a200
v_mfma_f32_16x16x32_bf16 v[164:167], a[20:23], v[32:35], 0
v_mfma_f32_16x16x16_bf16 a[144:147], a[164:165], a[188:189], 0
buffer_load_dwordx4 v2, s[20:23], s55 offen offset:1024 sc1 lds
v_accvgpr_mov_b32 a105, a201
v_mfma_f32_16x16x32_bf16 v[176:179], a[24:27], v[32:35], 0
v_mfma_f32_16x16x16_bf16 a[148:151], a[166:167], a[188:189], 0
Rb 8,8,51200
s_add_u32 s83, s83, 0xc150000
v_accvgpr_mov_b32 a106, a202
v_mfma_f32_16x16x32_bf16 v[184:187], a[24:27], v[36:39], 0
v_mfma_f32_16x16x16_bf16 a[152:155], a[168:169], a[188:189], 0
v_exp_f32_e32 v48, v96
v_accvgpr_mov_b32 a107, a203
v_mfma_f32_16x16x32_bf16 v[188:191], a[28:31], v[36:39], 0
Rb 12,8,52224
v_accvgpr_mov_b32 a108, a204
s_waitcnt lgkmcnt(8)
v_mfma_f32_16x16x32_bf16 v[180:183], a[28:31], v[32:35], 0
v_exp_f32_e32 v26, v97
v_accvgpr_mov_b32 a109, a205
v_mfma_f32_16x16x32_bf16 v[192:195], a[32:35], v[32:35], 0
Rb 16,8,53248
v_add_f32_e32 v14, v48, v26
v_accvgpr_mov_b32 a110, a206
v_mfma_f32_16x16x32_bf16 v[200:203], a[32:35], v[36:39], 0
v_exp_f32_e32 v49, v98
Cv 48,48,26
v_mfma_f32_16x16x32_bf16 v[204:207], a[36:39], v[36:39], 0
Rb 20,8,54272
v_add_f32_e32 v14, v14, v49
v_accvgpr_mov_b32 a111, a207
v_mfma_f32_16x16x32_bf16 v[196:199], a[36:39], v[32:35], 0
buffer_load_dwordx4 v2, s[20:23], s55 offen offset:2048 sc1 lds
v_accvgpr_mov_b32 a112, a208
v_mfma_f32_16x16x32_bf16 v[208:211], a[40:43], v[32:35], 0
Rb 24,8,55296
v_exp_f32_e32 v27, v99
)" R"(
v_mfma_f32_16x16x32_bf16 v[216:219], a[40:43], v[36:39], 0
buffer_store_dwordx4 a[128:131], v11, s[20:23], s83 offen sc0 sc1
v_accvgpr_mov_b32 a113, a209
v_mfma_f32_16x16x32_bf16 v[220:223], a[44:47], v[36:39], 0
Rb 28,8,56320
v_accvgpr_mov_b32 a114, a210
s_waitcnt lgkmcnt(8)
v_mfma_f32_16x16x32_bf16 v[212:215], a[44:47], v[32:35], 0
s_add_u32 s83, s83, 7168
v_exp_f32_e32 v50, v104
v_mfma_f32_16x16x32_bf16 v[224:227], a[48:51], v[32:35], 0
Rb 32,8,57344
v_add_f32_e32 v14, v14, v27
v_accvgpr_mov_b32 a115, a211
v_mfma_f32_16x16x32_bf16 v[232:235], a[48:51], v[36:39], 0
v_exp_f32_e32 v28, v105
Cv 49,49,27
v_mfma_f32_16x16x32_bf16 v[236:239], a[52:55], v[36:39], 0
Rb 36,8,58368
v_add_f32_e32 v14, v14, v50
v_accvgpr_mov_b32 a116, a212
v_mfma_f32_16x16x32_bf16 v[228:231], a[52:55], v[32:35], 0
buffer_load_dwordx4 v2, s[20:23], s55 offen offset:3072 sc1 lds
v_accvgpr_mov_b32 a117, a213
v_mfma_f32_16x16x32_bf16 v[240:243], a[56:59], v[32:35], 0
Rb 40,8,59392
v_exp_f32_e32 v51, v106
v_mfma_f32_16x16x32_bf16 v[248:251], a[56:59], v[36:39], 0
buffer_store_dwordx4 a[132:135], v11, s[20:23], s83 offen sc0 sc1
v_accvgpr_mov_b32 a118, a214
v_mfma_f32_16x16x32_bf16 v[252:255], a[60:63], v[36:39], 0
Rb 44,8,60416
v_accvgpr_mov_b32 a119, a215
s_waitcnt lgkmcnt(8)
v_mfma_f32_16x16x32_bf16 v[244:247], a[60:63], v[32:35], 0
s_add_u32 s83, s83, 7168
v_exp_f32_e32 v29, v107
Mp 128,0,40
Rb 48,8,61440
v_add_f32_e32 v14, v14, v28
v_accvgpr_mov_b32 a120, a216
Mp 136,0,44
buffer_store_dwordx4 a[136:139], v11, s[20:23], s83 offen sc0 sc1
s_add_u32 s83, s83, 7168
Mp 140,4,44
Rb 52,8,62464
s_add_u32 m0, s36, 36864
v_accvgpr_mov_b32 a121, a217
Mp 132,4,40
buffer_load_dwordx4 v3, s[20:23], s55 offen offset:0 sc1 lds
v_accvgpr_mov_b32 a122, a218
Mp 144,8,40
Rb 56,8,63488
v_exp_f32_e32 v52, v100
Mp 152,8,44
v_exp_f32_e32 v24, v101
v_accvgpr_mov_b32 a123, a219
Mp 156,12,44
Rb 60,8,64512
v_accvgpr_mov_b32 a124, a220
s_waitcnt lgkmcnt(8)
Mp 148,12,40
buffer_store_dwordx4 a[140:143], v11, s[20:23], s83 offen sc0 sc1
Cv 50,50,28
)" R"(
Mp 160,16,40
s_add_u32 s83, s83, 7168
v_add_f32_e32 v14, v14, v51
v_accvgpr_mov_b32 a125, a221
Mp 168,16,44
v_exp_f32_e32 v53, v102
v_add_f32_e32 v14, v14, v29
Mp 172,20,44
v_exp_f32_e32 v25, v103
v_accvgpr_mov_b32 a126, a222
Mp 164,20,40
buffer_load_dwordx4 v3, s[20:23], s55 offen offset:1024 sc1 lds
v_accvgpr_mov_b32 a127, a223
Mp 176,24,40
v_exp_f32_e32 v54, v108
Cv 51,51,29
Mp 184,24,44
buffer_store_dwordx4 a[144:147], v11, s[20:23], s83 offen sc0 sc1
Mp 188,28,44
s_add_u32 s83, s83, 7168
v_add_f32_e32 v15, v52, v24
Mp 180,28,40
Cv 52,52,24
s_waitcnt vmcnt(13) lgkmcnt(0)
s_barrier
buffer_store_dword v6, v7, s[20:23], s65 offen offset:0 sc0 sc1
Mp 192,32,40
Rb 0,1,0
v_add_f32_e32 v15, v15, v53
Mp 200,32,44
Rb 4,1,1024
v_exp_f32_e32 v26, v109
Mp 204,36,44
Rb 8,1,2048
v_add_f32_e32 v15, v15, v25
Mp 196,36,40
Rb 12,1,3072
buffer_load_dwordx4 v3, s[20:23], s55 offen offset:2048 sc1 lds
Mp 208,40,40
Rb 16,1,8192
v_exp_f32_e32 v55, v110
Mp 216,40,44
Rb 20,1,9216
Cv 53,53,25
Mp 220,44,44
Rb 24,1,10240
buffer_store_dwordx4 a[148:151], v11, s[20:23], s83 offen sc0 sc1
Mp 212,44,40
Rb 28,1,11264
s_add_u32 s83, s83, 7168
v_add_f32_e32 v15, v15, v54
Mp 224,48,40
Rb 32,1,4096
v_add_f32_e32 v15, v15, v26
Mp 232,48,44
Rb 36,1,5120
v_exp_f32_e32 v27, v111
Mp 236,52,44
Rb 40,1,6144
Cv 54,54,26
Mp 228,52,40
Rb 44,1,7168
buffer_load_dwordx4 v3, s[20:23], s55 offen offset:3072 sc1 lds
Mp 240,56,40
v_cmp_eq_u32_e32 vcc, s3, v4
)" R"(
s_andn2_b64 s[28:29], s[60:61], vcc
s_cbranch_scc1 .Lzsp9
.Lzok9:
Mp 248,56,44
buffer_store_dwordx4 a[152:155], v11, s[20:23], s83 offen sc0 sc1
Mp 252,60,44
v_add_f32_e32 v15, v15, v55
s_waitcnt lgkmcnt(8)
Mp 244,60,40
v_add_f32_e32 v15, v15, v27
Cv 55,55,27
s_sub_u32 s57, s57, 1
s_cmp_gt_i32 s57, s93
s_cbranch_scc1 .LzL
.LzDA:
s_waitcnt vmcnt(8) lgkmcnt(0)
s_barrier
Rb 0,8,0
Rb 4,8,16384
Rb 8,8,1024
Rb 12,8,17408
Rb 16,8,2048
Rb 20,8,18432
Rb 24,8,3072
Rb 28,8,19456
Rb 32,8,4096
Rb 36,8,20480
Rb 40,8,5120
Rb 44,8,21504
v_exp_f32_e32 v56, v112
v_exp_f32_e32 v28, v113
v_add_f32_e32 v14, v14, v56
v_add_f32_e32 v14, v14, v28
Cv 56,56,28
v_exp_f32_e32 v57, v114
v_exp_f32_e32 v29, v115
v_add_f32_e32 v14, v14, v57
v_add_f32_e32 v14, v14, v29
Cv 57,57,29
v_exp_f32_e32 v58, v120
v_exp_f32_e32 v24, v121
v_add_f32_e32 v14, v14, v58
v_add_f32_e32 v14, v14, v24
Cv 58,58,24
v_exp_f32_e32 v59, v122
v_exp_f32_e32 v25, v123
v_add_f32_e32 v14, v14, v59
v_add_f32_e32 v14, v14, v25
Cv 59,59,25
v_exp_f32_e32 v60, v116
v_exp_f32_e32 v26, v117
v_add_f32_e32 v15, v15, v60
v_add_f32_e32 v15, v15, v26
Cv 60,60,26
v_exp_f32_e32 v61, v118
v_exp_f32_e32 v27, v119
v_add_f32_e32 v15, v15, v61
v_add_f32_e32 v15, v15, v27
Cv 61,61,27
v_exp_f32_e32 v62, v124
v_exp_f32_e32 v28, v125
v_add_f32_e32 v15, v15, v62
v_add_f32_e32 v15, v15, v28
Cv 62,62,28
v_exp_f32_e32 v63, v126
)" R"(
v_exp_f32_e32 v29, v127
v_add_f32_e32 v15, v15, v63
v_add_f32_e32 v15, v15, v29
Cv 63,63,29
v_max_f32_e32 v11, v14, v15
v_cmp_nge_f32_e32 vcc, s62, v11
s_cbranch_vccnz .Lzfx5
.Lzfr5:
s_waitcnt lgkmcnt(8)
Mp 128,0,48
Rb 48,8,6144
s_add_u32 m0, s36, 131072
s_add_u32 s40, s67, 0x20000
s_lshl_b32 s82, s82, 7
Mp 136,0,52
buffer_load_dwordx4 v2, s[20:23], s40 offen offset:0 sc1 lds
s_add_u32 s82, s82, 0xc10c000
Mp 136,4,60
Rb 52,8,22528
v_add_f32_e32 v12, v12, v14
Mp 128,4,56
buffer_load_dwordx4 v2, s[20:23], s40 offen offset:1024 sc1 lds
Mp 132,8,48
Rb 56,8,7168
v_add_f32_e32 v13, v13, v15
Mp 140,8,52
v_mov_b32 v24, v12
v_mov_b32 v25, v13
s_cmp_lg_u32 s33, 0
s_cmov_b32 s82, 0x40000000
Mp 140,12,60
Rb 60,8,23552
s_waitcnt lgkmcnt(8)
Mp 132,12,56
buffer_load_dwordx4 v2, s[20:23], s40 offen offset:2048 sc1 lds
Mp 144,16,48
Rb 0,8,8192
v_permlane32_swap_b32_e32 v12, v24
Mp 152,16,52
buffer_load_dwordx4 v2, s[20:23], s40 offen offset:3072 sc1 lds
Mp 152,20,60
Rb 4,8,24576
v_add_f32_e32 v12, v12, v24
Mp 144,20,56
v_mov_b32 v24, v12
v_permlane32_swap_b32_e32 v13, v25
Mp 148,24,48
Rb 8,8,9216
s_add_u32 m0, s36, 135168
Mp 156,24,52
buffer_load_dwordx4 v3, s[20:23], s40 offen offset:0 sc1 lds
s_cmp_eq_u32 s35, 7
s_cmov_b32 s82, 0x40000000
Mp 156,28,60
Rb 12,8,25600
s_waitcnt lgkmcnt(8)
Mp 148,28,56
buffer_load_dwordx4 v3, s[20:23], s40 offen offset:1024 sc1 lds
Mp 160,32,48
Rb 16,8,10240
v_permlane16_swap_b32_e32 v12, v24
Mp 168,32,52
v_add_f32_e32 v12, v12, v24
v_add_f32_e32 v13, v13, v25
)" R"(
Mp 168,36,60
Rb 20,8,26624
v_mov_b32 v25, v13
Mp 160,36,56
buffer_load_dwordx4 v3, s[20:23], s40 offen offset:2048 sc1 lds
Mp 164,40,48
Rb 24,8,11264
s_add_u32 m0, s36, 98304
s_add_u32 s40, s67, 0x30000
Mp 172,40,52
buffer_load_dwordx4 v2, s[20:23], s40 offen offset:0 sc1 lds
Mp 172,44,60
Rb 28,8,27648
s_waitcnt lgkmcnt(8)
Mp 164,44,56
v_rcp_f32_e32 v12, v12
Mp 176,48,48
Rb 32,8,12288
v_permlane16_swap_b32_e32 v13, v25
Mp 184,48,52
buffer_load_dwordx4 v2, s[20:23], s40 offen offset:1024 sc1 lds
Mp 184,52,60
Rb 36,8,28672
v_add_f32_e32 v13, v13, v25
Mp 176,52,56
buffer_load_dwordx4 v2, s[20:23], s40 offen offset:2048 sc1 lds
Mp 180,56,48
Rb 40,8,13312
v_mul_f32_e32 v128, v12, v128
Mp 188,56,52
v_rcp_f32_e32 v13, v13
Mp 188,60,60
Rb 44,8,29696
s_waitcnt lgkmcnt(8)
Mp 180,60,56
buffer_load_dwordx4 v2, s[20:23], s40 offen offset:3072 sc1 lds
Mp 192,0,48
Rb 48,8,14336
s_add_u32 m0, s36, 102400
Mp 200,0,52
buffer_load_dwordx4 v3, s[20:23], s40 offen offset:0 sc1 lds
Mp 200,4,60
Rb 52,8,30720
v_mul_f32_e32 v129, v12, v129
Mp 192,4,56
Cv 128,128,129
v_mul_f32_e32 v130, v12, v130
Mp 196,8,48
Rb 56,8,15360
v_mul_f32_e32 v131, v12, v131
Mp 204,8,52
buffer_load_dwordx4 v3, s[20:23], s40 offen offset:1024 sc1 lds
Mp 204,12,60
Rb 60,8,31744
s_waitcnt lgkmcnt(8)
Mp 196,12,56
buffer_load_dwordx4 v3, s[20:23], s40 offen offset:2048 sc1 lds
Mp 208,16,48
Rb 0,1,0
Cv 129,130,131
Mp 216,16,52
v_mul_f32_e32 v132, v12, v132
v_mul_f32_e32 v133, v12, v133
Mp 216,20,60
)" R"(
Rb 4,1,1024
Cv 130,132,133
Mp 208,20,56
v_mul_f32_e32 v134, v12, v134
v_mul_f32_e32 v135, v12, v135
Mp 212,24,48
Rb 8,1,2048
Cv 131,134,135
Mp 220,24,52
v_mul_f32_e32 v136, v13, v136
v_mul_f32_e32 v137, v13, v137
Mp 220,28,60
Rb 12,1,3072
s_waitcnt vmcnt(14) lgkmcnt(4)
s_barrier
Mp 212,28,56
s_add_u32 s40, s67, 0x40000
Cv 136,136,137
Mp 224,32,48
Rb 16,1,4096
s_add_u32 m0, s36, 65536
Mp 232,32,52
buffer_load_dwordx4 v2, s[20:23], s40 offen offset:0 sc1 lds
Mp 232,36,60
Rb 20,1,5120
v_mul_f32_e32 v138, v13, v138
Mp 224,36,56
v_mul_f32_e32 v139, v13, v139
Cv 137,138,139
Mp 228,40,48
Rb 24,1,6144
v_mul_f32_e32 v140, v13, v140
Mp 236,40,52
v_mul_f32_e32 v141, v13, v141
Cv 138,140,141
Mp 236,44,60
Rb 28,1,8192
Mp 228,44,56
buffer_load_dwordx4 v2, s[20:23], s40 offen offset:1024 sc1 lds
Mp 240,48,48
Rb 32,1,9216
v_mul_f32_e32 v142, v13, v142
Mp 248,48,52
v_mul_f32_e32 v143, v13, v143
Cv 139,142,143
Mp 248,52,60
Rb 36,1,10240
v_mul_f32_e32 v144, v12, v144
Mp 240,52,56
v_mul_f32_e32 v145, v12, v145
Cv 144,144,145
Mp 244,56,48
Rb 40,1,11264
v_mul_f32_e32 v146, v12, v146
Mp 252,56,52
buffer_load_dwordx4 v2, s[20:23], s40 offen offset:2048 sc1 lds
Mp 252,60,60
Rb 44,1,12288
s_waitcnt lgkmcnt(8)
Mp 244,60,56
v_mul_f32_e32 v147, v12, v147
Cv 145,146,147
v_mfma_f32_16x16x32_bf16 a[64:67], a[0:3], v[128:131], 0
Rb 48,1,13312
v_mul_f32_e32 v148, v12, v148
)" R"(
v_mfma_f32_16x16x32_bf16 a[72:75], a[0:3], v[136:139], 0
v_mul_f32_e32 v149, v12, v149
Cv 146,148,149
v_mfma_f32_16x16x32_bf16 a[76:79], a[4:7], v[136:139], 0
Rb 52,1,14336
v_mul_f32_e32 v150, v12, v150
v_mfma_f32_16x16x32_bf16 a[68:71], a[4:7], v[128:131], 0
buffer_load_dwordx4 v2, s[20:23], s40 offen offset:3072 sc1 lds
v_mfma_f32_16x16x32_bf16 a[80:83], a[8:11], v[128:131], 0
Rb 56,1,16384
v_mul_f32_e32 v151, v12, v151
v_mfma_f32_16x16x32_bf16 a[88:91], a[8:11], v[136:139], 0
Cv 147,150,151
v_mul_f32_e32 v152, v13, v152
v_mfma_f32_16x16x32_bf16 a[92:95], a[12:15], v[136:139], 0
Rb 60,1,17408
s_waitcnt lgkmcnt(8)
v_mfma_f32_16x16x32_bf16 a[84:87], a[12:15], v[128:131], 0
v_mul_f32_e32 v153, v13, v153
Cv 152,152,153
v_mfma_f32_16x16x32_bf16 a[96:99], a[16:19], v[128:131], 0
Rb 0,1,18432
s_add_u32 m0, s36, 69632
v_mfma_f32_16x16x32_bf16 a[104:107], a[16:19], v[136:139], 0
buffer_load_dwordx4 v3, s[20:23], s40 offen offset:0 sc1 lds
v_mfma_f32_16x16x32_bf16 a[108:111], a[20:23], v[136:139], 0
Rb 4,1,19456
v_mul_f32_e32 v154, v13, v154
v_mfma_f32_16x16x32_bf16 a[100:103], a[20:23], v[128:131], 0
v_mul_f32_e32 v155, v13, v155
Cv 153,154,155
v_mfma_f32_16x16x32_bf16 a[112:115], a[24:27], v[128:131], 0
Rb 8,1,20480
v_mul_f32_e32 v156, v13, v156
v_mfma_f32_16x16x32_bf16 a[120:123], a[24:27], v[136:139], 0
v_mul_f32_e32 v157, v13, v157
Cv 154,156,157
v_mfma_f32_16x16x32_bf16 a[124:127], a[28:31], v[136:139], 0
Rb 12,1,21504
s_waitcnt lgkmcnt(8)
v_mfma_f32_16x16x32_bf16 a[116:119], a[28:31], v[128:131], 0
buffer_load_dwordx4 v3, s[20:23], s40 offen offset:1024 sc1 lds
v_mfma_f32_16x16x32_bf16 a[128:131], a[32:35], v[128:131], 0
Rb 16,1,22528
v_mul_f32_e32 v158, v13, v158
v_mfma_f32_16x16x32_bf16 a[136:139], a[32:35], v[136:139], 0
v_mul_f32_e32 v159, v13, v159
Cv 155,158,159
v_mfma_f32_16x16x32_bf16 a[140:143], a[36:39], v[136:139], 0
Rb 20,1,24576
v_mul_f32_e32 v160, v12, v160
v_mfma_f32_16x16x32_bf16 a[132:135], a[36:39], v[128:131], 0
v_mul_f32_e32 v161, v12, v161
Cv 160,160,161
v_mfma_f32_16x16x32_bf16 a[144:147], a[40:43], v[128:131], 0
Rb 24,1,25600
v_mul_f32_e32 v162, v12, v162
v_mfma_f32_16x16x32_bf16 a[152:155], a[40:43], v[136:139], 0
buffer_load_dwordx4 v3, s[20:23], s40 offen offset:2048 sc1 lds
v_mfma_f32_16x16x32_bf16 a[156:159], a[44:47], v[136:139], 0
Rb 28,1,26624
s_waitcnt lgkmcnt(8)
v_mfma_f32_16x16x32_bf16 a[148:151], a[44:47], v[128:131], 0
v_mul_f32_e32 v163, v12, v163
)" R"(
Cv 161,162,163
v_mfma_f32_16x16x32_bf16 a[160:163], a[48:51], v[128:131], 0
Rb 32,1,27648
v_mul_f32_e32 v164, v12, v164
v_mfma_f32_16x16x32_bf16 a[168:171], a[48:51], v[136:139], 0
v_mul_f32_e32 v165, v12, v165
Cv 162,164,165
v_mfma_f32_16x16x32_bf16 a[172:175], a[52:55], v[136:139], 0
Rb 36,1,28672
v_mul_f32_e32 v166, v12, v166
v_mfma_f32_16x16x32_bf16 a[164:167], a[52:55], v[128:131], 0
v_mul_f32_e32 v167, v12, v167
Cv 163,166,167
v_mfma_f32_16x16x32_bf16 a[64:67], a[56:59], v[144:147], a[64:67]
Rb 40,1,29696
v_mul_f32_e32 v168, v13, v168
v_mfma_f32_16x16x32_bf16 a[72:75], a[56:59], v[152:155], a[72:75]
v_mul_f32_e32 v169, v13, v169
Cv 168,168,169
v_mfma_f32_16x16x32_bf16 a[76:79], a[60:63], v[152:155], a[76:79]
Rb 44,1,30720
s_waitcnt lgkmcnt(8)
v_mfma_f32_16x16x32_bf16 a[68:71], a[60:63], v[144:147], a[68:71]
v_mul_f32_e32 v170, v13, v170
v_mul_f32_e32 v171, v13, v171
v_mfma_f32_16x16x32_bf16 a[80:83], a[0:3], v[144:147], a[80:83]
Rb 48,1,32768
Cv 169,170,171
v_mfma_f32_16x16x32_bf16 a[88:91], a[0:3], v[152:155], a[88:91]
v_mul_f32_e32 v172, v13, v172
v_mul_f32_e32 v173, v13, v173
v_mfma_f32_16x16x32_bf16 a[92:95], a[4:7], v[152:155], a[92:95]
Rb 52,1,33792
Cv 170,172,173
v_mfma_f32_16x16x32_bf16 a[84:87], a[4:7], v[144:147], a[84:87]
v_mul_f32_e32 v174, v13, v174
v_mul_f32_e32 v175, v13, v175
v_mfma_f32_16x16x32_bf16 a[96:99], a[8:11], v[144:147], a[96:99]
Rb 56,1,34816
Cv 171,174,175
v_mfma_f32_16x16x32_bf16 a[104:107], a[8:11], v[152:155], a[104:107]
v_mul_f32_e32 v176, v12, v176
v_mul_f32_e32 v177, v12, v177
v_mfma_f32_16x16x32_bf16 a[108:111], a[12:15], v[152:155], a[108:111]
Rb 60,1,35840
s_waitcnt vmcnt(14) lgkmcnt(4)
s_barrier
buffer_store_dword v6, v7, s[20:23], s82 offen offset:0 sc0 sc1
v_mfma_f32_16x16x32_bf16 a[100:103], a[12:15], v[144:147], a[100:103]
s_add_u32 s40, s67, 0x50000
Cv 176,176,177
v_mfma_f32_16x16x32_bf16 a[112:115], a[16:19], v[144:147], a[112:115]
Rb 0,1,36864
s_add_u32 m0, s36, 0
v_mfma_f32_16x16x32_bf16 a[120:123], a[16:19], v[152:155], a[120:123]
buffer_load_dwordx4 v2, s[20:23], s40 offen offset:0 sc1 lds
v_mfma_f32_16x16x32_bf16 a[124:127], a[20:23], v[152:155], a[124:127]
Rb 4,1,37888
v_mul_f32_e32 v178, v12, v178
v_mfma_f32_16x16x32_bf16 a[116:119], a[20:23], v[144:147], a[116:119]
v_mul_f32_e32 v179, v12, v179
Cv 177,178,179
v_mfma_f32_16x16x32_bf16 a[128:131], a[24:27], v[144:147], a[128:131]
Rb 8,1,38912
v_mul_f32_e32 v180, v12, v180
)" R"(
v_mfma_f32_16x16x32_bf16 a[136:139], a[24:27], v[152:155], a[136:139]
v_mul_f32_e32 v181, v12, v181
Cv 178,180,181
v_mfma_f32_16x16x32_bf16 a[140:143], a[28:31], v[152:155], a[140:143]
Rb 12,1,40960
v_mfma_f32_16x16x32_bf16 a[132:135], a[28:31], v[144:147], a[132:135]
buffer_load_dwordx4 v2, s[20:23], s40 offen offset:1024 sc1 lds
v_mfma_f32_16x16x32_bf16 a[144:147], a[32:35], v[144:147], a[144:147]
Rb 16,1,41984
v_mul_f32_e32 v182, v12, v182
v_mfma_f32_16x16x32_bf16 a[152:155], a[32:35], v[152:155], a[152:155]
v_mul_f32_e32 v183, v12, v183
Cv 179,182,183
v_mfma_f32_16x16x32_bf16 a[156:159], a[36:39], v[152:155], a[156:159]
Rb 20,1,43008
v_mul_f32_e32 v184, v13, v184
v_mfma_f32_16x16x32_bf16 a[148:151], a[36:39], v[144:147], a[148:151]
v_mul_f32_e32 v185, v13, v185
Cv 184,184,185
v_mfma_f32_16x16x32_bf16 a[160:163], a[40:43], v[144:147], a[160:163]
Rb 24,1,44032
v_mul_f32_e32 v186, v13, v186
v_mfma_f32_16x16x32_bf16 a[168:171], a[40:43], v[152:155], a[168:171]
buffer_load_dwordx4 v2, s[20:23], s40 offen offset:2048 sc1 lds
v_mfma_f32_16x16x32_bf16 a[172:175], a[44:47], v[152:155], a[172:175]
Rb 28,1,45056
s_waitcnt lgkmcnt(8)
v_mfma_f32_16x16x32_bf16 a[164:167], a[44:47], v[144:147], a[164:167]
v_mul_f32_e32 v187, v13, v187
Cv 185,186,187
v_mfma_f32_16x16x32_bf16 a[64:67], a[48:51], v[160:163], a[64:67]
Rb 32,1,46080
v_mul_f32_e32 v188, v13, v188
v_mfma_f32_16x16x32_bf16 a[72:75], a[48:51], v[168:171], a[72:75]
v_mul_f32_e32 v189, v13, v189
Cv 186,188,189
v_mfma_f32_16x16x32_bf16 a[76:79], a[52:55], v[168:171], a[76:79]
Rb 36,1,47104
v_mul_f32_e32 v190, v13, v190
v_mfma_f32_16x16x32_bf16 a[68:71], a[52:55], v[160:163], a[68:71]
buffer_load_dwordx4 v2, s[20:23], s40 offen offset:3072 sc1 lds
v_mfma_f32_16x16x32_bf16 a[80:83], a[56:59], v[160:163], a[80:83]
Rb 40,1,49152
v_mul_f32_e32 v191, v13, v191
v_mfma_f32_16x16x32_bf16 a[88:91], a[56:59], v[168:171], a[88:91]
Cv 187,190,191
v_mul_f32_e32 v192, v12, v192
v_mfma_f32_16x16x32_bf16 a[92:95], a[60:63], v[168:171], a[92:95]
Rb 44,1,50176
s_waitcnt lgkmcnt(8)
v_mfma_f32_16x16x32_bf16 a[84:87], a[60:63], v[160:163], a[84:87]
v_mul_f32_e32 v193, v12, v193
Cv 192,192,193
v_mfma_f32_16x16x32_bf16 a[96:99], a[0:3], v[160:163], a[96:99]
Rb 48,1,51200
s_add_u32 m0, s36, 4096
v_mfma_f32_16x16x32_bf16 a[104:107], a[0:3], v[168:171], a[104:107]
buffer_load_dwordx4 v3, s[20:23], s40 offen offset:0 sc1 lds
v_mfma_f32_16x16x32_bf16 a[108:111], a[4:7], v[168:171], a[108:111]
Rb 52,1,52224
v_mul_f32_e32 v194, v12, v194
v_mfma_f32_16x16x32_bf16 a[100:103], a[4:7], v[160:163], a[100:103]
v_mul_f32_e32 v195, v12, v195
Cv 193,194,195
)" R"(
v_mfma_f32_16x16x32_bf16 a[112:115], a[8:11], v[160:163], a[112:115]
Rb 56,1,53248
v_mul_f32_e32 v196, v12, v196
v_mfma_f32_16x16x32_bf16 a[120:123], a[8:11], v[168:171], a[120:123]
v_mul_f32_e32 v197, v12, v197
Cv 194,196,197
v_mfma_f32_16x16x32_bf16 a[124:127], a[12:15], v[168:171], a[124:127]
Rb 60,1,54272
s_waitcnt lgkmcnt(8)
v_mfma_f32_16x16x32_bf16 a[116:119], a[12:15], v[160:163], a[116:119]
buffer_load_dwordx4 v3, s[20:23], s40 offen offset:1024 sc1 lds
v_mfma_f32_16x16x32_bf16 a[128:131], a[16:19], v[160:163], a[128:131]
Rb 0,1,55296
v_mul_f32_e32 v198, v12, v198
v_mfma_f32_16x16x32_bf16 a[136:139], a[16:19], v[168:171], a[136:139]
v_mul_f32_e32 v199, v12, v199
Cv 195,198,199
v_mfma_f32_16x16x32_bf16 a[140:143], a[20:23], v[168:171], a[140:143]
Rb 4,1,57344
v_mul_f32_e32 v200, v13, v200
v_mfma_f32_16x16x32_bf16 a[132:135], a[20:23], v[160:163], a[132:135]
v_mul_f32_e32 v201, v13, v201
Cv 200,200,201
v_mfma_f32_16x16x32_bf16 a[144:147], a[24:27], v[160:163], a[144:147]
Rb 8,1,58368
v_mul_f32_e32 v202, v13, v202
v_mfma_f32_16x16x32_bf16 a[152:155], a[24:27], v[168:171], a[152:155]
buffer_load_dwordx4 v3, s[20:23], s40 offen offset:2048 sc1 lds
v_mfma_f32_16x16x32_bf16 a[156:159], a[28:31], v[168:171], a[156:159]
Rb 12,1,59392
s_waitcnt lgkmcnt(8)
v_mfma_f32_16x16x32_bf16 a[148:151], a[28:31], v[160:163], a[148:151]
v_mul_f32_e32 v203, v13, v203
Cv 201,202,203
v_mfma_f32_16x16x32_bf16 a[160:163], a[32:35], v[160:163], a[160:163]
Rb 16,1,60416
v_mul_f32_e32 v204, v13, v204
v_mfma_f32_16x16x32_bf16 a[168:171], a[32:35], v[168:171], a[168:171]
v_mul_f32_e32 v205, v13, v205
Cv 202,204,205
v_mfma_f32_16x16x32_bf16 a[172:175], a[36:39], v[168:171], a[172:175]
Rb 20,1,61440
v_mul_f32_e32 v206, v13, v206
v_mfma_f32_16x16x32_bf16 a[164:167], a[36:39], v[160:163], a[164:167]
v_mul_f32_e32 v207, v13, v207
Cv 203,206,207
v_mfma_f32_16x16x32_bf16 a[64:67], a[40:43], v[176:179], a[64:67]
Rb 24,1,62464
v_mul_f32_e32 v208, v12, v208
v_mfma_f32_16x16x32_bf16 a[72:75], a[40:43], v[184:187], a[72:75]
v_mul_f32_e32 v209, v12, v209
Cv 208,208,209
v_mfma_f32_16x16x32_bf16 a[76:79], a[44:47], v[184:187], a[76:79]
Rb 28,1,63488
s_waitcnt lgkmcnt(8)
v_mfma_f32_16x16x32_bf16 a[68:71], a[44:47], v[176:179], a[68:71]
v_mul_f32_e32 v210, v12, v210
v_mul_f32_e32 v211, v12, v211
v_mfma_f32_16x16x32_bf16 a[80:83], a[48:51], v[176:179], a[80:83]
Rb 32,10,0
Cv 209,210,211
v_mfma_f32_16x16x32_bf16 a[88:91], a[48:51], v[184:187], a[88:91]
v_mul_f32_e32 v212, v12, v212
v_mul_f32_e32 v213, v12, v213
)" R"(
v_mfma_f32_16x16x32_bf16 a[92:95], a[52:55], v[184:187], a[92:95]
Rb 36,10,1024
Cv 210,212,213
v_mfma_f32_16x16x32_bf16 a[84:87], a[52:55], v[176:179], a[84:87]
v_mul_f32_e32 v214, v12, v214
v_mul_f32_e32 v215, v12, v215
v_mfma_f32_16x16x32_bf16 a[96:99], a[56:59], v[176:179], a[96:99]
Rb 40,10,2048
Cv 211,214,215
v_mfma_f32_16x16x32_bf16 a[104:107], a[56:59], v[184:187], a[104:107]
v_mul_f32_e32 v216, v13, v216
v_mul_f32_e32 v217, v13, v217
v_mfma_f32_16x16x32_bf16 a[108:111], a[60:63], v[184:187], a[108:111]
Rb 44,10,3072
s_waitcnt vmcnt(15) lgkmcnt(4)
s_barrier
v_mfma_f32_16x16x32_bf16 a[100:103], a[60:63], v[176:179], a[100:103]
s_add_u32 s40, s67, 0x60000
Cv 216,216,217
v_mfma_f32_16x16x32_bf16 a[112:115], a[0:3], v[176:179], a[112:115]
Rb 48,10,4096
s_add_u32 m0, s36, 32768
v_mfma_f32_16x16x32_bf16 a[120:123], a[0:3], v[184:187], a[120:123]
buffer_load_dwordx4 v2, s[20:23], s40 offen offset:0 sc1 lds
v_mfma_f32_16x16x32_bf16 a[124:127], a[4:7], v[184:187], a[124:127]
Rb 52,10,5120
v_mul_f32_e32 v218, v13, v218
v_mfma_f32_16x16x32_bf16 a[116:119], a[4:7], v[176:179], a[116:119]
v_mul_f32_e32 v219, v13, v219
Cv 217,218,219
v_mfma_f32_16x16x32_bf16 a[128:131], a[8:11], v[176:179], a[128:131]
Rb 56,10,6144
v_mul_f32_e32 v220, v13, v220
v_mfma_f32_16x16x32_bf16 a[136:139], a[8:11], v[184:187], a[136:139]
v_mul_f32_e32 v221, v13, v221
Cv 218,220,221
v_mfma_f32_16x16x32_bf16 a[140:143], a[12:15], v[184:187], a[140:143]
Rb 60,10,8192
v_mfma_f32_16x16x32_bf16 a[132:135], a[12:15], v[176:179], a[132:135]
buffer_load_dwordx4 v2, s[20:23], s40 offen offset:1024 sc1 lds
v_mfma_f32_16x16x32_bf16 a[144:147], a[16:19], v[176:179], a[144:147]
Rb 0,10,9216
v_mul_f32_e32 v222, v13, v222
v_mfma_f32_16x16x32_bf16 a[152:155], a[16:19], v[184:187], a[152:155]
v_mul_f32_e32 v223, v13, v223
Cv 219,222,223
v_mfma_f32_16x16x32_bf16 a[156:159], a[20:23], v[184:187], a[156:159]
Rb 4,10,10240
v_mul_f32_e32 v224, v12, v224
v_mfma_f32_16x16x32_bf16 a[148:151], a[20:23], v[176:179], a[148:151]
v_mul_f32_e32 v225, v12, v225
Cv 224,224,225
v_mfma_f32_16x16x32_bf16 a[160:163], a[24:27], v[176:179], a[160:163]
Rb 8,10,11264
v_mul_f32_e32 v226, v12, v226
v_mfma_f32_16x16x32_bf16 a[168:171], a[24:27], v[184:187], a[168:171]
buffer_load_dwordx4 v2, s[20:23], s40 offen offset:2048 sc1 lds
v_mfma_f32_16x16x32_bf16 a[172:175], a[28:31], v[184:187], a[172:175]
Rb 12,10,12288
s_waitcnt lgkmcnt(8)
v_mfma_f32_16x16x32_bf16 a[164:167], a[28:31], v[176:179], a[164:167]
v_mul_f32_e32 v227, v12, v227
Cv 225,226,227
v_mfma_f32_16x16x32_bf16 a[64:67], a[32:35], v[192:195], a[64:67]
Rb 16,10,13312
)" R"(
v_mul_f32_e32 v228, v12, v228
v_mfma_f32_16x16x32_bf16 a[72:75], a[32:35], v[200:203], a[72:75]
v_mul_f32_e32 v229, v12, v229
Cv 226,228,229
v_mfma_f32_16x16x32_bf16 a[76:79], a[36:39], v[200:203], a[76:79]
Rb 20,10,14336
v_mul_f32_e32 v230, v12, v230
v_mfma_f32_16x16x32_bf16 a[68:71], a[36:39], v[192:195], a[68:71]
buffer_load_dwordx4 v2, s[20:23], s40 offen offset:3072 sc1 lds
v_mfma_f32_16x16x32_bf16 a[80:83], a[40:43], v[192:195], a[80:83]
Rb 24,10,16384
v_mul_f32_e32 v231, v12, v231
v_mfma_f32_16x16x32_bf16 a[88:91], a[40:43], v[200:203], a[88:91]
Cv 227,230,231
v_mul_f32_e32 v232, v13, v232
v_mfma_f32_16x16x32_bf16 a[92:95], a[44:47], v[200:203], a[92:95]
Rb 28,10,17408
s_waitcnt lgkmcnt(8)
v_mfma_f32_16x16x32_bf16 a[84:87], a[44:47], v[192:195], a[84:87]
v_mul_f32_e32 v233, v13, v233
Cv 232,232,233
v_mfma_f32_16x16x32_bf16 a[96:99], a[48:51], v[192:195], a[96:99]
Rb 32,10,18432
s_add_u32 m0, s36, 36864
v_mfma_f32_16x16x32_bf16 a[104:107], a[48:51], v[200:203], a[104:107]
buffer_load_dwordx4 v3, s[20:23], s40 offen offset:0 sc1 lds
v_mfma_f32_16x16x32_bf16 a[108:111], a[52:55], v[200:203], a[108:111]
Rb 36,10,19456
v_mul_f32_e32 v234, v13, v234
v_mfma_f32_16x16x32_bf16 a[100:103], a[52:55], v[192:195], a[100:103]
v_mul_f32_e32 v235, v13, v235
Cv 233,234,235
v_mfma_f32_16x16x32_bf16 a[112:115], a[56:59], v[192:195], a[112:115]
Rb 40,10,20480
v_mul_f32_e32 v236, v13, v236
v_mfma_f32_16x16x32_bf16 a[120:123], a[56:59], v[200:203], a[120:123]
v_mul_f32_e32 v237, v13, v237
Cv 234,236,237
v_mfma_f32_16x16x32_bf16 a[124:127], a[60:63], v[200:203], a[124:127]
Rb 44,10,21504
s_waitcnt lgkmcnt(8)
v_mfma_f32_16x16x32_bf16 a[116:119], a[60:63], v[192:195], a[116:119]
buffer_load_dwordx4 v3, s[20:23], s40 offen offset:1024 sc1 lds
v_mfma_f32_16x16x32_bf16 a[128:131], a[0:3], v[192:195], a[128:131]
Rb 48,10,22528
v_mul_f32_e32 v238, v13, v238
v_mfma_f32_16x16x32_bf16 a[136:139], a[0:3], v[200:203], a[136:139]
v_mul_f32_e32 v239, v13, v239
Cv 235,238,239
v_mfma_f32_16x16x32_bf16 a[140:143], a[4:7], v[200:203], a[140:143]
Rb 52,10,24576
v_mul_f32_e32 v240, v12, v240
v_mfma_f32_16x16x32_bf16 a[132:135], a[4:7], v[192:195], a[132:135]
v_mul_f32_e32 v241, v12, v241
Cv 240,240,241
v_mfma_f32_16x16x32_bf16 a[144:147], a[8:11], v[192:195], a[144:147]
Rb 56,10,25600
v_mul_f32_e32 v242, v12, v242
v_mfma_f32_16x16x32_bf16 a[152:155], a[8:11], v[200:203], a[152:155]
buffer_load_dwordx4 v3, s[20:23], s40 offen offset:2048 sc1 lds
v_mfma_f32_16x16x32_bf16 a[156:159], a[12:15], v[200:203], a[156:159]
Rb 60,10,26624
s_waitcnt lgkmcnt(8)
v_mfma_f32_16x16x32_bf16 a[148:151], a[12:15], v[192:195], a[148:151]
)" R"(
v_mul_f32_e32 v243, v12, v243
Cv 241,242,243
v_mfma_f32_16x16x32_bf16 a[160:163], a[16:19], v[192:195], a[160:163]
Rb 0,10,27648
v_mul_f32_e32 v244, v12, v244
v_mfma_f32_16x16x32_bf16 a[168:171], a[16:19], v[200:203], a[168:171]
v_mul_f32_e32 v245, v12, v245
Cv 242,244,245
v_mfma_f32_16x16x32_bf16 a[172:175], a[20:23], v[200:203], a[172:175]
Rb 4,10,28672
v_mul_f32_e32 v246, v12, v246
v_mfma_f32_16x16x32_bf16 a[164:167], a[20:23], v[192:195], a[164:167]
v_mul_f32_e32 v247, v12, v247
Cv 243,246,247
v_mfma_f32_16x16x32_bf16 a[64:67], a[24:27], v[208:211], a[64:67]
Rb 8,10,29696
v_mul_f32_e32 v248, v13, v248
v_mfma_f32_16x16x32_bf16 a[72:75], a[24:27], v[216:219], a[72:75]
v_mul_f32_e32 v249, v13, v249
Cv 248,248,249
v_mfma_f32_16x16x32_bf16 a[76:79], a[28:31], v[216:219], a[76:79]
Rb 12,10,30720
s_waitcnt lgkmcnt(8)
v_mfma_f32_16x16x32_bf16 a[68:71], a[28:31], v[208:211], a[68:71]
v_mul_f32_e32 v250, v13, v250
v_mul_f32_e32 v251, v13, v251
v_mfma_f32_16x16x32_bf16 a[80:83], a[32:35], v[208:211], a[80:83]
Rb 16,8,32768
Cv 249,250,251
v_mfma_f32_16x16x32_bf16 a[88:91], a[32:35], v[216:219], a[88:91]
v_mul_f32_e32 v252, v13, v252
v_mul_f32_e32 v253, v13, v253
v_mfma_f32_16x16x32_bf16 a[92:95], a[36:39], v[216:219], a[92:95]
Rb 20,8,33792
Cv 250,252,253
v_mfma_f32_16x16x32_bf16 a[84:87], a[36:39], v[208:211], a[84:87]
v_mul_f32_e32 v254, v13, v254
v_mul_f32_e32 v255, v13, v255
v_mfma_f32_16x16x32_bf16 a[96:99], a[40:43], v[208:211], a[96:99]
Rb 24,8,34816
Cv 251,254,255
v_mfma_f32_16x16x32_bf16 a[104:107], a[40:43], v[216:219], a[104:107]
v_mfma_f32_16x16x32_bf16 a[108:111], a[44:47], v[216:219], a[108:111]
Rb 28,8,35840
s_waitcnt vmcnt(15) lgkmcnt(4)
s_barrier
v_mfma_f32_16x16x32_bf16 a[100:103], a[44:47], v[208:211], a[100:103]
s_add_u32 s40, s67, 0x70000
v_mfma_f32_16x16x32_bf16 a[112:115], a[48:51], v[208:211], a[112:115]
Rb 32,8,36864
s_add_u32 m0, s36, 131072
v_mfma_f32_16x16x32_bf16 a[120:123], a[48:51], v[216:219], a[120:123]
buffer_load_dwordx4 v2, s[20:23], s40 offen offset:0 sc1 lds
v_mfma_f32_16x16x32_bf16 a[124:127], a[52:55], v[216:219], a[124:127]
Rb 36,8,37888
v_mfma_f32_16x16x32_bf16 a[116:119], a[52:55], v[208:211], a[116:119]
v_mfma_f32_16x16x32_bf16 a[128:131], a[56:59], v[208:211], a[128:131]
Rb 40,8,38912
v_mfma_f32_16x16x32_bf16 a[136:139], a[56:59], v[216:219], a[136:139]
v_mfma_f32_16x16x32_bf16 a[140:143], a[60:63], v[216:219], a[140:143]
Rb 44,8,40960
v_mfma_f32_16x16x32_bf16 a[132:135], a[60:63], v[208:211], a[132:135]
buffer_load_dwordx4 v2, s[20:23], s40 offen offset:1024 sc1 lds
v_mfma_f32_16x16x32_bf16 a[144:147], a[0:3], v[208:211], a[144:147]
Rb 48,8,41984
)" R"(
v_mfma_f32_16x16x32_bf16 a[152:155], a[0:3], v[216:219], a[152:155]
v_mfma_f32_16x16x32_bf16 a[156:159], a[4:7], v[216:219], a[156:159]
Rb 52,8,43008
v_mfma_f32_16x16x32_bf16 a[148:151], a[4:7], v[208:211], a[148:151]
v_mfma_f32_16x16x32_bf16 a[160:163], a[8:11], v[208:211], a[160:163]
Rb 56,8,44032
v_mfma_f32_16x16x32_bf16 a[168:171], a[8:11], v[216:219], a[168:171]
buffer_load_dwordx4 v2, s[20:23], s40 offen offset:2048 sc1 lds
v_mfma_f32_16x16x32_bf16 a[172:175], a[12:15], v[216:219], a[172:175]
Rb 60,8,45056
s_waitcnt lgkmcnt(8)
v_mfma_f32_16x16x32_bf16 a[164:167], a[12:15], v[208:211], a[164:167]
v_mfma_f32_16x16x32_bf16 a[64:67], a[16:19], v[224:227], a[64:67]
Rb 0,8,46080
v_mfma_f32_16x16x32_bf16 a[72:75], a[16:19], v[232:235], a[72:75]
v_mfma_f32_16x16x32_bf16 a[76:79], a[20:23], v[232:235], a[76:79]
Rb 4,8,47104
v_mfma_f32_16x16x32_bf16 a[68:71], a[20:23], v[224:227], a[68:71]
buffer_load_dwordx4 v2, s[20:23], s40 offen offset:3072 sc1 lds
v_mfma_f32_16x16x32_bf16 a[80:83], a[24:27], v[224:227], a[80:83]
Rb 8,8,49152
v_mfma_f32_16x16x32_bf16 a[88:91], a[24:27], v[232:235], a[88:91]
v_mfma_f32_16x16x32_bf16 a[92:95], a[28:31], v[232:235], a[92:95]
Rb 12,8,50176
s_waitcnt lgkmcnt(8)
v_mfma_f32_16x16x32_bf16 a[84:87], a[28:31], v[224:227], a[84:87]
v_mfma_f32_16x16x32_bf16 a[96:99], a[32:35], v[224:227], a[96:99]
Rb 16,8,51200
s_add_u32 m0, s36, 135168
v_mfma_f32_16x16x32_bf16 a[104:107], a[32:35], v[232:235], a[104:107]
buffer_load_dwordx4 v3, s[20:23], s40 offen offset:0 sc1 lds
v_mfma_f32_16x16x32_bf16 a[108:111], a[36:39], v[232:235], a[108:111]
Rb 20,8,52224
v_mfma_f32_16x16x32_bf16 a[100:103], a[36:39], v[224:227], a[100:103]
v_mfma_f32_16x16x32_bf16 a[112:115], a[40:43], v[224:227], a[112:115]
Rb 24,8,53248
v_mfma_f32_16x16x32_bf16 a[120:123], a[40:43], v[232:235], a[120:123]
v_mfma_f32_16x16x32_bf16 a[124:127], a[44:47], v[232:235], a[124:127]
Rb 28,8,54272
s_waitcnt lgkmcnt(8)
v_mfma_f32_16x16x32_bf16 a[116:119], a[44:47], v[224:227], a[116:119]
buffer_load_dwordx4 v3, s[20:23], s40 offen offset:1024 sc1 lds
v_mfma_f32_16x16x32_bf16 a[128:131], a[48:51], v[224:227], a[128:131]
Rb 32,8,55296
v_mfma_f32_16x16x32_bf16 a[136:139], a[48:51], v[232:235], a[136:139]
v_mfma_f32_16x16x32_bf16 a[140:143], a[52:55], v[232:235], a[140:143]
Rb 36,8,57344
v_mfma_f32_16x16x32_bf16 a[132:135], a[52:55], v[224:227], a[132:135]
v_mfma_f32_16x16x32_bf16 a[144:147], a[56:59], v[224:227], a[144:147]
Rb 40,8,58368
v_mfma_f32_16x16x32_bf16 a[152:155], a[56:59], v[232:235], a[152:155]
buffer_load_dwordx4 v3, s[20:23], s40 offen offset:2048 sc1 lds
v_mfma_f32_16x16x32_bf16 a[156:159], a[60:63], v[232:235], a[156:159]
Rb 44,8,59392
s_waitcnt lgkmcnt(8)
v_mfma_f32_16x16x32_bf16 a[148:151], a[60:63], v[224:227], a[148:151]
v_mfma_f32_16x16x32_bf16 a[160:163], a[0:3], v[224:227], a[160:163]
Rb 48,8,60416
v_mfma_f32_16x16x32_bf16 a[168:171], a[0:3], v[232:235], a[168:171]
v_mfma_f32_16x16x32_bf16 a[172:175], a[4:7], v[232:235], a[172:175]
Rb 52,8,61440
v_mfma_f32_16x16x32_bf16 a[164:167], a[4:7], v[224:227], a[164:167]
v_mfma_f32_16x16x32_bf16 a[64:67], a[8:11], v[240:243], a[64:67]
Rb 56,8,62464
)" R"(
v_mfma_f32_16x16x32_bf16 a[72:75], a[8:11], v[248:251], a[72:75]
v_mfma_f32_16x16x32_bf16 a[76:79], a[12:15], v[248:251], a[76:79]
Rb 60,8,63488
s_waitcnt lgkmcnt(8)
v_mfma_f32_16x16x32_bf16 a[68:71], a[12:15], v[240:243], a[68:71]
v_mfma_f32_16x16x32_bf16 a[80:83], a[16:19], v[240:243], a[80:83]
Rb 0,8,0
v_mfma_f32_16x16x32_bf16 a[88:91], a[16:19], v[248:251], a[88:91]
v_mfma_f32_16x16x32_bf16 a[92:95], a[20:23], v[248:251], a[92:95]
Rb 4,8,1024
v_mfma_f32_16x16x32_bf16 a[84:87], a[20:23], v[240:243], a[84:87]
v_mfma_f32_16x16x32_bf16 a[96:99], a[24:27], v[240:243], a[96:99]
Rb 8,8,2048
v_mfma_f32_16x16x32_bf16 a[104:107], a[24:27], v[248:251], a[104:107]
v_mfma_f32_16x16x32_bf16 a[108:111], a[28:31], v[248:251], a[108:111]
Rb 12,8,3072
s_waitcnt vmcnt(14) lgkmcnt(8)
s_barrier
v_mfma_f32_16x16x32_bf16 a[100:103], a[28:31], v[240:243], a[100:103]
v_mfma_f32_16x16x32_bf16 a[112:115], a[32:35], v[240:243], a[112:115]
Rb 16,8,4096
v_mfma_f32_16x16x32_bf16 a[120:123], a[32:35], v[248:251], a[120:123]
v_mfma_f32_16x16x32_bf16 a[124:127], a[36:39], v[248:251], a[124:127]
Rb 20,8,5120
v_mfma_f32_16x16x32_bf16 a[116:119], a[36:39], v[240:243], a[116:119]
v_mfma_f32_16x16x32_bf16 a[128:131], a[40:43], v[240:243], a[128:131]
Rb 24,8,6144
v_mfma_f32_16x16x32_bf16 a[136:139], a[40:43], v[248:251], a[136:139]
v_mfma_f32_16x16x32_bf16 a[140:143], a[44:47], v[248:251], a[140:143]
Rb 28,8,8192
s_waitcnt lgkmcnt(8)
v_mfma_f32_16x16x32_bf16 a[132:135], a[44:47], v[240:243], a[132:135]
v_mfma_f32_16x16x32_bf16 a[144:147], a[48:51], v[240:243], a[144:147]
Rb 32,8,9216
v_mfma_f32_16x16x32_bf16 a[152:155], a[48:51], v[248:251], a[152:155]
v_mfma_f32_16x16x32_bf16 a[156:159], a[52:55], v[248:251], a[156:159]
Rb 36,8,10240
v_mfma_f32_16x16x32_bf16 a[148:151], a[52:55], v[240:243], a[148:151]
v_mfma_f32_16x16x32_bf16 a[160:163], a[56:59], v[240:243], a[160:163]
Rb 40,8,11264
v_mfma_f32_16x16x32_bf16 a[168:171], a[56:59], v[248:251], a[168:171]
v_mfma_f32_16x16x32_bf16 a[172:175], a[60:63], v[248:251], a[172:175]
Rb 44,8,12288
s_waitcnt lgkmcnt(8)
v_mfma_f32_16x16x32_bf16 a[164:167], a[60:63], v[240:243], a[164:167]
v_mfma_f32_16x16x32_bf16 v[32:35], a[0:3], v[128:131], 0
Rb 48,8,13312
v_mfma_f32_16x16x32_bf16 v[40:43], a[0:3], v[136:139], 0
Mp 40,4,152
Rb 52,8,14336
v_accvgpr_read_b32 v112, a64
Mp 32,4,144
v_accvgpr_read_b32 v24, a65
Cv 112,112,24
Mp 32,8,160
Rb 56,8,16384
v_accvgpr_read_b32 v113, a66
Mp 40,8,168
v_accvgpr_read_b32 v25, a67
Cv 113,113,25
Mp 40,12,184
Rb 60,8,17408
s_waitcnt lgkmcnt(8)
Mp 32,12,176
v_accvgpr_read_b32 v114, a68
)" R"(
v_accvgpr_read_b32 v26, a69
Mp 32,16,192
Rb 0,8,18432
Cv 114,114,26
Mp 40,16,200
v_accvgpr_read_b32 v115, a70
v_accvgpr_read_b32 v27, a71
Mp 40,20,216
Rb 4,8,19456
Cv 115,115,27
Mp 32,20,208
s_add_u32 s29, s73, 0x0
buffer_store_dwordx4 v[112:115], v1, s[20:23], s29 offen sc0 sc1
Mp 32,24,224
Rb 8,8,20480
v_accvgpr_read_b32 v116, a72
Mp 40,24,232
v_accvgpr_read_b32 v24, a73
Cv 116,116,24
Mp 40,28,248
Rb 12,8,21504
s_waitcnt lgkmcnt(8)
Mp 32,28,240
v_accvgpr_read_b32 v117, a74
v_accvgpr_read_b32 v25, a75
v_mfma_f32_16x16x32_bf16 v[36:39], a[32:35], v[128:131], 0
Rb 16,8,22528
Cv 117,117,25
v_mfma_f32_16x16x32_bf16 v[44:47], a[32:35], v[136:139], 0
v_accvgpr_read_b32 v118, a76
v_accvgpr_read_b32 v26, a77
Cv 118,118,26
Mp 44,36,152
Rb 20,8,24576
v_accvgpr_read_b32 v119, a78
Mp 36,36,144
v_accvgpr_read_b32 v27, a79
Cv 119,119,27
Mp 36,40,160
Rb 24,8,25600
v_accvgpr_read_b32 v120, a80
Mp 44,40,168
s_add_u32 s29, s73, 0x400
buffer_store_dwordx4 v[116:119], v1, s[20:23], s29 offen sc0 sc1
Mp 44,44,184
Rb 28,8,26624
s_waitcnt lgkmcnt(8)
Mp 36,44,176
v_accvgpr_read_b32 v24, a81
Cv 120,120,24
Mp 36,48,192
Rb 32,8,27648
v_accvgpr_read_b32 v121, a82
Mp 44,48,200
v_accvgpr_read_b32 v25, a83
Cv 121,121,25
Mp 44,52,216
Rb 36,8,28672
v_accvgpr_read_b32 v122, a84
Mp 36,52,208
v_accvgpr_read_b32 v26, a85
Cv 122,122,26
Mp 36,56,224
Rb 40,8,29696
)" R"(
v_accvgpr_read_b32 v123, a86
Mp 44,56,232
v_accvgpr_read_b32 v27, a87
Cv 123,123,27
Mp 44,60,248
Rb 44,8,30720
s_waitcnt lgkmcnt(8)
Mp 36,60,240
s_add_u32 s29, s73, 0x800
buffer_store_dwordx4 v[120:123], v1, s[20:23], s29 offen sc0 sc1
v_mfma_f32_16x16x32_bf16 v[48:51], a[0:3], v[128:131], 0
Rb 48,1,0
v_accvgpr_read_b32 v124, a88
v_mfma_f32_16x16x32_bf16 v[56:59], a[0:3], v[136:139], 0
v_accvgpr_read_b32 v24, a89
Cv 124,124,24
v_accvgpr_read_b32 v125, a90
Mp 56,4,152
Rb 52,1,1024
v_accvgpr_read_b32 v25, a91
Mp 48,4,144
Cv 125,125,25
v_accvgpr_read_b32 v126, a92
Mp 48,8,160
Rb 56,1,2048
Cv 32,32,33
Mp 56,8,168
Cv 33,34,35
Cv 34,36,37
Mp 56,12,184
Rb 60,1,3072
s_waitcnt vmcnt(10) lgkmcnt(8)
s_barrier
Mp 48,12,176
Cv 35,38,39
v_accvgpr_read_b32 v26, a93
Mp 48,16,192
Rb 0,1,4096
Cv 126,126,26
Mp 56,16,200
s_add_u32 s29, s73, 0x8000
buffer_store_dwordx4 v[32:35], v1, s[20:23], s29 offen sc0 sc1
Mp 56,20,216
Rb 4,1,5120
v_accvgpr_read_b32 v127, a94
Mp 48,20,208
v_accvgpr_read_b32 v27, a95
Cv 127,127,27
Mp 48,24,224
Rb 8,1,6144
v_accvgpr_read_b32 v112, a96
Mp 56,24,232
s_add_u32 s29, s73, 0xc00
buffer_store_dwordx4 v[124:127], v1, s[20:23], s29 offen sc0 sc1
Mp 56,28,248
Rb 12,1,8192
s_waitcnt lgkmcnt(8)
Mp 48,28,240
v_accvgpr_read_b32 v24, a97
Cv 112,112,24
v_mfma_f32_16x16x32_bf16 v[52:55], a[32:35], v[128:131], 0
Rb 16,1,9216
Cv 40,40,41
v_mfma_f32_16x16x32_bf16 v[60:63], a[32:35], v[136:139], 0
Cv 41,42,43
)" R"(
Cv 42,44,45
v_accvgpr_read_b32 v113, a98
Mp 60,36,152
Rb 20,1,10240
Cv 43,46,47
Mp 52,36,144
s_add_u32 s29, s73, 0x8400
buffer_store_dwordx4 v[40:43], v1, s[20:23], s29 offen sc0 sc1
Mp 52,40,160
Rb 24,1,11264
v_accvgpr_read_b32 v25, a99
Mp 60,40,168
Cv 113,113,25
v_accvgpr_read_b32 v114, a100
Mp 60,44,184
Rb 28,1,12288
s_waitcnt lgkmcnt(8)
Mp 52,44,176
v_accvgpr_read_b32 v26, a101
Cv 114,114,26
Mp 52,48,192
Rb 32,1,13312
v_accvgpr_read_b32 v115, a102
Mp 60,48,200
v_accvgpr_read_b32 v27, a103
Cv 115,115,27
Mp 60,52,216
Rb 36,1,14336
v_accvgpr_read_b32 v116, a104
Mp 52,52,208
s_add_u32 s29, s73, 0x1000
buffer_store_dwordx4 v[112:115], v1, s[20:23], s29 offen sc0 sc1
Mp 52,56,224
Rb 40,1,16384
v_accvgpr_read_b32 v24, a105
Mp 60,56,232
Cv 116,116,24
v_accvgpr_read_b32 v117, a106
Mp 60,60,248
Rb 44,1,17408
s_waitcnt lgkmcnt(8)
Mp 52,60,240
v_accvgpr_read_b32 v25, a107
Cv 117,117,25
v_mfma_f32_16x16x32_bf16 v[64:67], a[0:3], v[128:131], 0
Rb 48,1,18432
v_accvgpr_read_b32 v118, a108
v_mfma_f32_16x16x32_bf16 v[72:75], a[0:3], v[136:139], 0
v_accvgpr_read_b32 v26, a109
Cv 118,118,26
v_accvgpr_read_b32 v119, a110
Mp 72,4,152
Rb 52,1,19456
v_accvgpr_read_b32 v27, a111
Mp 64,4,144
Cv 119,119,27
v_accvgpr_read_b32 v120, a112
Mp 64,8,160
Rb 56,1,20480
Cv 48,48,49
Mp 72,8,168
Cv 49,50,51
Cv 50,52,53
Mp 72,12,184
)" R"(
Rb 60,1,21504
s_waitcnt lgkmcnt(8)
Mp 64,12,176
Cv 51,54,55
v_accvgpr_read_b32 v24, a113
Mp 64,16,192
Rb 0,1,22528
Cv 120,120,24
Mp 72,16,200
s_add_u32 s29, s73, 0x8800
buffer_store_dwordx4 v[48:51], v1, s[20:23], s29 offen sc0 sc1
Mp 72,20,216
Rb 4,1,24576
v_accvgpr_read_b32 v121, a114
Mp 64,20,208
s_add_u32 s29, s73, 0x1400
buffer_store_dwordx4 v[116:119], v1, s[20:23], s29 offen sc0 sc1
Mp 64,24,224
Rb 8,1,25600
v_accvgpr_read_b32 v25, a115
Mp 72,24,232
Cv 121,121,25
v_accvgpr_read_b32 v122, a116
Mp 72,28,248
Rb 12,1,26624
s_waitcnt lgkmcnt(8)
Mp 64,28,240
v_accvgpr_read_b32 v26, a117
Cv 122,122,26
v_mfma_f32_16x16x32_bf16 v[68:71], a[32:35], v[128:131], 0
Rb 16,1,27648
Cv 56,56,57
v_mfma_f32_16x16x32_bf16 v[76:79], a[32:35], v[136:139], 0
Cv 57,58,59
Cv 58,60,61
v_accvgpr_read_b32 v123, a118
Mp 76,36,152
Rb 20,1,28672
Cv 59,62,63
Mp 68,36,144
s_add_u32 s29, s73, 0x8c00
buffer_store_dwordx4 v[56:59], v1, s[20:23], s29 offen sc0 sc1
Mp 68,40,160
Rb 24,1,29696
v_accvgpr_read_b32 v27, a119
Mp 76,40,168
Cv 123,123,27
v_accvgpr_read_b32 v124, a120
Mp 76,44,184
Rb 28,1,30720
s_waitcnt lgkmcnt(8)
Mp 68,44,176
s_add_u32 s29, s73, 0x1800
buffer_store_dwordx4 v[120:123], v1, s[20:23], s29 offen sc0 sc1
Mp 68,48,192
Rb 32,1,32768
v_accvgpr_read_b32 v24, a121
Mp 76,48,200
Cv 124,124,24
v_accvgpr_read_b32 v125, a122
Mp 76,52,216
Rb 36,1,33792
v_accvgpr_read_b32 v25, a123
Mp 68,52,208
)" R"(
Cv 125,125,25
v_accvgpr_read_b32 v126, a124
Mp 68,56,224
Rb 40,1,34816
v_accvgpr_read_b32 v26, a125
Mp 76,56,232
Cv 126,126,26
v_accvgpr_read_b32 v127, a126
Mp 76,60,248
Rb 44,1,35840
s_waitcnt vmcnt(11) lgkmcnt(8)
s_barrier
Mp 68,60,240
v_accvgpr_read_b32 v27, a127
Cv 127,127,27
v_mfma_f32_16x16x32_bf16 v[80:83], a[0:3], v[128:131], 0
Rb 48,1,36864
v_accvgpr_read_b32 v112, a128
v_mfma_f32_16x16x32_bf16 v[88:91], a[0:3], v[136:139], 0
s_add_u32 s29, s73, 0x4000
buffer_store_dwordx4 v[124:127], v1, s[20:23], s29 offen sc0 sc1
Mp 88,4,152
Rb 52,1,37888
v_accvgpr_read_b32 v24, a129
Mp 80,4,144
Cv 112,112,24
v_accvgpr_read_b32 v113, a130
Mp 80,8,160
Rb 56,1,38912
Cv 64,64,65
Mp 88,8,168
Cv 65,66,67
Cv 66,68,69
Mp 88,12,184
Rb 60,1,40960
s_waitcnt lgkmcnt(8)
Mp 80,12,176
Cv 67,70,71
v_accvgpr_read_b32 v25, a131
Mp 80,16,192
Rb 0,1,41984
Cv 113,113,25
Mp 88,16,200
s_add_u32 s29, s73, 0x9000
buffer_store_dwordx4 v[64:67], v1, s[20:23], s29 offen sc0 sc1
Mp 88,20,216
Rb 4,1,43008
v_accvgpr_read_b32 v114, a132
Mp 80,20,208
v_accvgpr_read_b32 v26, a133
Cv 114,114,26
Mp 80,24,224
Rb 8,1,44032
v_accvgpr_read_b32 v115, a134
Mp 88,24,232
v_accvgpr_read_b32 v27, a135
Cv 115,115,27
Mp 88,28,248
Rb 12,1,45056
s_waitcnt lgkmcnt(8)
Mp 80,28,240
s_add_u32 s29, s73, 0x4400
buffer_store_dwordx4 v[112:115], v1, s[20:23], s29 offen sc0 sc1
v_mfma_f32_16x16x32_bf16 v[84:87], a[32:35], v[128:131], 0
Rb 16,1,46080
)" R"(
Cv 72,72,73
v_mfma_f32_16x16x32_bf16 v[92:95], a[32:35], v[136:139], 0
Cv 73,74,75
Cv 74,76,77
v_accvgpr_read_b32 v116, a136
Mp 92,36,152
Rb 20,1,47104
Cv 75,78,79
Mp 84,36,144
s_add_u32 s29, s73, 0x9400
buffer_store_dwordx4 v[72:75], v1, s[20:23], s29 offen sc0 sc1
Mp 84,40,160
Rb 24,1,49152
v_accvgpr_read_b32 v24, a137
Mp 92,40,168
Cv 116,116,24
v_accvgpr_read_b32 v117, a138
Mp 92,44,184
Rb 28,1,50176
s_waitcnt lgkmcnt(8)
Mp 84,44,176
v_accvgpr_read_b32 v25, a139
Cv 117,117,25
Mp 84,48,192
Rb 32,1,51200
v_accvgpr_read_b32 v118, a140
Mp 92,48,200
v_accvgpr_read_b32 v26, a141
Cv 118,118,26
Mp 92,52,216
Rb 36,1,52224
v_accvgpr_read_b32 v119, a142
Mp 84,52,208
v_accvgpr_read_b32 v27, a143
Cv 119,119,27
Mp 84,56,224
Rb 40,1,53248
v_accvgpr_read_b32 v120, a144
Mp 92,56,232
s_add_u32 s29, s73, 0x4800
buffer_store_dwordx4 v[116:119], v1, s[20:23], s29 offen sc0 sc1
Mp 92,60,248
Rb 44,1,54272
s_waitcnt lgkmcnt(8)
Mp 84,60,240
v_accvgpr_read_b32 v24, a145
Cv 120,120,24
v_mfma_f32_16x16x32_bf16 v[96:99], a[0:3], v[128:131], 0
Rb 48,1,55296
v_accvgpr_read_b32 v121, a146
v_mfma_f32_16x16x32_bf16 v[104:107], a[0:3], v[136:139], 0
v_accvgpr_read_b32 v25, a147
Cv 121,121,25
v_accvgpr_read_b32 v122, a148
Mp 104,4,152
Rb 52,1,57344
v_accvgpr_read_b32 v26, a149
Mp 96,4,144
Cv 122,122,26
v_accvgpr_read_b32 v123, a150
Mp 96,8,160
Rb 56,1,58368
Cv 80,80,81
Mp 104,8,168
)" R"(
Cv 81,82,83
Cv 82,84,85
Mp 104,12,184
Rb 60,1,59392
s_waitcnt lgkmcnt(8)
Mp 96,12,176
Cv 83,86,87
v_accvgpr_read_b32 v27, a151
Mp 96,16,192
Rb 0,1,60416
Cv 123,123,27
Mp 104,16,200
s_add_u32 s29, s73, 0x9800
buffer_store_dwordx4 v[80:83], v1, s[20:23], s29 offen sc0 sc1
Mp 104,20,216
Rb 4,1,61440
v_accvgpr_read_b32 v124, a152
Mp 96,20,208
s_add_u32 s29, s73, 0x4c00
buffer_store_dwordx4 v[120:123], v1, s[20:23], s29 offen sc0 sc1
Mp 96,24,224
Rb 8,1,62464
v_accvgpr_read_b32 v24, a153
Mp 104,24,232
Cv 124,124,24
v_accvgpr_read_b32 v125, a154
Mp 104,28,248
Rb 12,1,63488
s_waitcnt lgkmcnt(8)
Mp 96,28,240
v_accvgpr_read_b32 v25, a155
Cv 125,125,25
v_mfma_f32_16x16x32_bf16 v[100:103], a[32:35], v[128:131], 0
Rb 16,10,0
Cv 88,88,89
v_mfma_f32_16x16x32_bf16 v[108:111], a[32:35], v[136:139], 0
Cv 89,90,91
Cv 90,92,93
v_accvgpr_read_b32 v126, a156
Mp 108,36,152
Rb 20,10,1024
Cv 91,94,95
Mp 100,36,144
s_add_u32 s29, s73, 0xc000
buffer_store_dwordx4 v[88:91], v1, s[20:23], s29 offen sc0 sc1
Mp 100,40,160
Rb 24,10,2048
v_accvgpr_read_b32 v26, a157
Mp 108,40,168
Cv 126,126,26
v_accvgpr_read_b32 v127, a158
Mp 108,44,184
Rb 28,10,3072
s_waitcnt lgkmcnt(8)
Mp 100,44,176
v_accvgpr_read_b32 v27, a159
Cv 127,127,27
Mp 100,48,192
Rb 32,10,4096
v_accvgpr_read_b32 v112, a160
Mp 108,48,200
s_add_u32 s29, s73, 0x5000
buffer_store_dwordx4 v[124:127], v1, s[20:23], s29 offen sc0 sc1
Mp 108,52,216
)" R"(
Rb 36,10,5120
v_accvgpr_read_b32 v24, a161
Mp 100,52,208
Cv 112,112,24
v_accvgpr_read_b32 v113, a162
Mp 100,56,224
Rb 40,10,6144
v_accvgpr_read_b32 v25, a163
Mp 108,56,232
Cv 113,113,25
v_accvgpr_read_b32 v114, a164
Mp 108,60,248
Rb 44,10,8192
s_waitcnt lgkmcnt(8)
Mp 100,60,240
v_accvgpr_read_b32 v26, a165
Cv 114,114,26
v_mfma_f32_16x16x32_bf16 v[132:135], a[0:3], v[128:131], 0
Rb 48,10,9216
v_accvgpr_read_b32 v115, a166
v_mfma_f32_16x16x32_bf16 v[148:151], a[0:3], v[136:139], 0
v_accvgpr_read_b32 v27, a167
Cv 115,115,27
v_accvgpr_read_b32 v116, a168
Mp 148,4,152
Rb 52,10,10240
v_accvgpr_read_b32 v24, a169
Mp 132,4,144
s_add_u32 s29, s73, 0x5400
buffer_store_dwordx4 v[112:115], v1, s[20:23], s29 offen sc0 sc1
Mp 132,8,160
Rb 56,10,11264
Cv 96,96,97
Mp 148,8,168
Cv 97,98,99
Cv 98,100,101
Mp 148,12,184
Rb 60,10,12288
s_waitcnt lgkmcnt(8)
Mp 132,12,176
Cv 99,102,103
Cv 116,116,24
Mp 132,16,192
Rb 0,10,13312
v_accvgpr_read_b32 v117, a170
Mp 148,16,200
s_add_u32 s29, s73, 0xc400
buffer_store_dwordx4 v[96:99], v1, s[20:23], s29 offen sc0 sc1
Mp 148,20,216
Rb 4,10,14336
v_accvgpr_read_b32 v25, a171
Mp 132,20,208
Cv 117,117,25
v_accvgpr_read_b32 v118, a172
Mp 132,24,224
Rb 8,10,16384
v_accvgpr_read_b32 v26, a173
Mp 148,24,232
Cv 118,118,26
v_accvgpr_read_b32 v119, a174
Mp 148,28,248
Rb 12,10,17408
s_waitcnt lgkmcnt(8)
Mp 132,28,240
)" R"(
v_accvgpr_read_b32 v27, a175
Cv 119,119,27
v_mfma_f32_16x16x32_bf16 v[140:143], a[32:35], v[128:131], 0
Rb 16,10,18432
Cv 104,104,105
v_mfma_f32_16x16x32_bf16 v[156:159], a[32:35], v[136:139], 0
Cv 105,106,107
Cv 106,108,109
Mp 156,36,152
Rb 20,10,19456
Cv 107,110,111
Mp 140,36,144
s_add_u32 s29, s73, 0xc800
buffer_store_dwordx4 v[104:107], v1, s[20:23], s29 offen sc0 sc1
Mp 140,40,160
Rb 24,10,20480
Mp 156,40,168
s_add_u32 s29, s73, 0x5800
buffer_store_dwordx4 v[116:119], v1, s[20:23], s29 offen sc0 sc1
Mp 156,44,184
Rb 28,10,21504
s_waitcnt lgkmcnt(8)
Mp 140,44,176
Mp 140,48,192
Rb 32,10,22528
Mp 156,48,200
Mp 156,52,216
Rb 36,10,24576
Mp 140,52,208
Mp 140,56,224
Rb 40,10,25600
Mp 156,56,232
Mp 156,60,248
Rb 44,10,26624
s_waitcnt lgkmcnt(8)
Mp 140,60,240
v_mfma_f32_16x16x32_bf16 v[164:167], a[0:3], v[128:131], 0
Rb 48,10,27648
v_mfma_f32_16x16x32_bf16 v[180:183], a[0:3], v[136:139], 0
Mp 180,4,152
Rb 52,10,28672
Mp 164,4,144
Mp 164,8,160
Rb 56,10,29696
Cv 132,132,133
Mp 180,8,168
Cv 133,134,135
Cv 134,140,141
Mp 180,12,184
Rb 60,10,30720
s_waitcnt lgkmcnt(8)
Mp 164,12,176
Cv 135,142,143
Mp 164,16,192
s_add_u32 s29, s73, 0xcc00
buffer_store_dwordx4 v[132:135], v1, s[20:23], s29 offen sc0 sc1
Mp 180,16,200
Mp 180,20,216
Mp 164,20,208
Mp 164,24,224
Mp 180,24,232
Mp 180,28,248
s_waitcnt lgkmcnt(4)
Mp 164,28,240
)" R"(
v_mfma_f32_16x16x32_bf16 v[172:175], a[32:35], v[128:131], 0
Cv 148,148,149
Cv 149,150,151
v_mfma_f32_16x16x32_bf16 v[188:191], a[32:35], v[136:139], 0
Cv 150,156,157
Cv 151,158,159
Mp 188,36,152
s_add_u32 s29, s73, 0xd000
buffer_store_dwordx4 v[148:151], v1, s[20:23], s29 offen sc0 sc1
Mp 172,36,144
Mp 172,40,160
Mp 188,40,168
Mp 188,44,184
s_waitcnt lgkmcnt(0)
Mp 172,44,176
Mp 172,48,192
Mp 188,48,200
Mp 188,52,216
Mp 172,52,208
Mp 172,56,224
Mp 188,56,232
Mp 188,60,248
s_waitcnt vmcnt(2)
buffer_store_dword v6, v7, s[20:23], s68 offen offset:0 sc0 sc1
Mp 172,60,240
s_nop 7
Cv 164,164,165
Cv 165,166,167
Cv 166,172,173
Cv 167,174,175
s_add_u32 s29, s73, 0xd400
buffer_store_dwordx4 v[164:167], v1, s[20:23], s29 offen sc0 sc1
Cv 180,180,181
Cv 181,182,183
Cv 182,188,189
Cv 183,190,191
s_add_u32 s29, s73, 0xd800
buffer_store_dwordx4 v[180:183], v1, s[20:23], s29 offen sc0 sc1
s_branch .LzEND
.LzMASK0:
s_lshl1_add_u32 s28, s57, s33
s_mov_b32 s29, 0xff800000
s_cmp_gt_u32 s28, 5
s_cbranch_scc1 .LzMASKE0
s_cmp_eq_u32 s28, 5
s_cbranch_scc1 .LzMASKB0
v_mov_b32 v72, s29
v_mov_b32 v73, s29
v_mov_b32 v74, s29
v_mov_b32 v75, s29
v_mov_b32 v80, s29
v_mov_b32 v81, s29
v_mov_b32 v82, s29
v_mov_b32 v83, s29
v_mov_b32 v84, s29
v_mov_b32 v85, s29
v_mov_b32 v86, s29
v_mov_b32 v87, s29
v_mov_b32 v88, s29
v_mov_b32 v89, s29
v_mov_b32 v90, s29
v_mov_b32 v91, s29
v_mov_b32 v92, s29
v_mov_b32 v93, s29
)" R"(
v_mov_b32 v94, s29
v_mov_b32 v95, s29
s_cmp_eq_u32 s28, 4
s_cbranch_scc1 .LzMASKA0
v_mov_b32 v64, s29
v_mov_b32 v65, s29
v_mov_b32 v66, s29
v_mov_b32 v67, s29
v_mov_b32 v68, s29
v_mov_b32 v69, s29
v_mov_b32 v70, s29
v_mov_b32 v71, s29
v_mov_b32 v76, s29
v_mov_b32 v77, s29
v_mov_b32 v78, s29
v_mov_b32 v79, s29
.LzMASKE0:
s_setpc_b64 s[30:31]
.LzMASKA0:
s_mov_b32 exec_lo, 0xf0000
s_mov_b32 exec_hi, 0xfff00ff
v_mov_b32 v64, s29
v_mov_b32 v76, s29
s_mov_b32 exec_lo, 0x1f0001
s_mov_b32 exec_hi, 0x1fff01ff
v_mov_b32 v65, s29
v_mov_b32 v77, s29
s_mov_b32 exec_lo, 0x3f0003
s_mov_b32 exec_hi, 0x3fff03ff
v_mov_b32 v66, s29
v_mov_b32 v78, s29
s_mov_b32 exec_lo, 0x7f0007
s_mov_b32 exec_hi, 0x7fff07ff
v_mov_b32 v67, s29
v_mov_b32 v79, s29
s_mov_b64 exec, -1
s_setpc_b64 s[30:31]
.LzMASKB0:
s_nop 1
v_mov_b32 v88, s29
v_mov_b32 v89, s29
v_mov_b32 v90, s29
v_mov_b32 v91, s29
s_mov_b32 exec_lo, 0xf0000
s_mov_b32 exec_hi, 0xfff00ff
v_mov_b32 v80, s29
v_mov_b32 v92, s29
s_mov_b32 exec_lo, 0x1f0001
s_mov_b32 exec_hi, 0x1fff01ff
v_mov_b32 v81, s29
v_mov_b32 v93, s29
s_mov_b32 exec_lo, 0x3f0003
s_mov_b32 exec_hi, 0x3fff03ff
v_mov_b32 v82, s29
v_mov_b32 v94, s29
s_mov_b32 exec_lo, 0x7f0007
s_mov_b32 exec_hi, 0x7fff07ff
v_mov_b32 v83, s29
v_mov_b32 v95, s29
s_mov_b64 exec, -1
s_setpc_b64 s[30:31]
.LzFIX0:
s_nop 7
v_max_f32_e32 v28, v64, v65
)" R"(
v_max3_f32 v28, v28, v66, v67
v_max3_f32 v28, v28, v72, v73
v_max3_f32 v28, v28, v74, v75
v_max3_f32 v28, v28, v80, v81
v_max3_f32 v28, v28, v82, v83
v_max3_f32 v28, v28, v88, v89
v_max3_f32 v28, v28, v90, v91
v_mov_b32 v24, v28
s_nop 1
v_permlane32_swap_b32_e32 v28, v24
v_max_f32_e32 v28, v28, v24
v_mov_b32 v24, v28
s_nop 1
v_permlane16_swap_b32_e32 v28, v24
v_max_f32_e32 v28, v28, v24
v_max_f32_e32 v29, v68, v69
v_max3_f32 v29, v29, v70, v71
v_max3_f32 v29, v29, v76, v77
v_max3_f32 v29, v29, v78, v79
v_max3_f32 v29, v29, v84, v85
v_max3_f32 v29, v29, v86, v87
v_max3_f32 v29, v29, v92, v93
v_max3_f32 v29, v29, v94, v95
v_mov_b32 v24, v29
s_nop 1
v_permlane32_swap_b32_e32 v29, v24
v_max_f32_e32 v29, v29, v24
v_mov_b32 v24, v29
s_nop 1
v_permlane16_swap_b32_e32 v29, v24
v_max_f32_e32 v29, v29, v24
s_bitcmp1_b32 s88, 0
s_cbranch_scc1 .LzFIXF0
v_max_f32_e32 v28, 0, v28
v_sub_f32_e32 v24, 0, v28
v_exp_f32_e32 v24, v24
s_nop 0
v_mul_f32_e32 v12, v24, v12
v_mul_f32_e32 v128, v24, v128
v_mul_f32_e32 v129, v24, v129
v_mul_f32_e32 v130, v24, v130
v_mul_f32_e32 v131, v24, v131
v_mul_f32_e32 v132, v24, v132
v_mul_f32_e32 v133, v24, v133
v_mul_f32_e32 v134, v24, v134
v_mul_f32_e32 v135, v24, v135
v_mul_f32_e32 v144, v24, v144
v_mul_f32_e32 v145, v24, v145
v_mul_f32_e32 v146, v24, v146
v_mul_f32_e32 v147, v24, v147
v_mul_f32_e32 v148, v24, v148
v_mul_f32_e32 v149, v24, v149
v_mul_f32_e32 v150, v24, v150
v_mul_f32_e32 v151, v24, v151
v_mul_f32_e32 v160, v24, v160
v_mul_f32_e32 v161, v24, v161
v_mul_f32_e32 v162, v24, v162
v_mul_f32_e32 v163, v24, v163
v_mul_f32_e32 v164, v24, v164
v_mul_f32_e32 v165, v24, v165
v_mul_f32_e32 v166, v24, v166
v_mul_f32_e32 v167, v24, v167
v_mul_f32_e32 v176, v24, v176
v_mul_f32_e32 v177, v24, v177
)" R"(
v_mul_f32_e32 v178, v24, v178
v_mul_f32_e32 v179, v24, v179
v_mul_f32_e32 v180, v24, v180
v_mul_f32_e32 v181, v24, v181
v_mul_f32_e32 v182, v24, v182
v_mul_f32_e32 v183, v24, v183
v_mul_f32_e32 v192, v24, v192
v_mul_f32_e32 v193, v24, v193
v_mul_f32_e32 v194, v24, v194
v_mul_f32_e32 v195, v24, v195
v_mul_f32_e32 v196, v24, v196
v_mul_f32_e32 v197, v24, v197
v_mul_f32_e32 v198, v24, v198
v_mul_f32_e32 v199, v24, v199
v_mul_f32_e32 v208, v24, v208
v_mul_f32_e32 v209, v24, v209
v_mul_f32_e32 v210, v24, v210
v_mul_f32_e32 v211, v24, v211
v_mul_f32_e32 v212, v24, v212
v_mul_f32_e32 v213, v24, v213
v_mul_f32_e32 v214, v24, v214
v_mul_f32_e32 v215, v24, v215
v_mul_f32_e32 v224, v24, v224
v_mul_f32_e32 v225, v24, v225
v_mul_f32_e32 v226, v24, v226
v_mul_f32_e32 v227, v24, v227
v_mul_f32_e32 v228, v24, v228
v_mul_f32_e32 v229, v24, v229
v_mul_f32_e32 v230, v24, v230
v_mul_f32_e32 v231, v24, v231
v_mul_f32_e32 v240, v24, v240
v_mul_f32_e32 v241, v24, v241
v_mul_f32_e32 v242, v24, v242
v_mul_f32_e32 v243, v24, v243
v_mul_f32_e32 v244, v24, v244
v_mul_f32_e32 v245, v24, v245
v_mul_f32_e32 v246, v24, v246
v_mul_f32_e32 v247, v24, v247
v_max_f32_e32 v29, 0, v29
v_sub_f32_e32 v24, 0, v29
v_exp_f32_e32 v24, v24
s_nop 0
v_mul_f32_e32 v13, v24, v13
v_mul_f32_e32 v136, v24, v136
v_mul_f32_e32 v137, v24, v137
v_mul_f32_e32 v138, v24, v138
v_mul_f32_e32 v139, v24, v139
v_mul_f32_e32 v140, v24, v140
v_mul_f32_e32 v141, v24, v141
v_mul_f32_e32 v142, v24, v142
v_mul_f32_e32 v143, v24, v143
v_mul_f32_e32 v152, v24, v152
v_mul_f32_e32 v153, v24, v153
v_mul_f32_e32 v154, v24, v154
v_mul_f32_e32 v155, v24, v155
v_mul_f32_e32 v156, v24, v156
v_mul_f32_e32 v157, v24, v157
v_mul_f32_e32 v158, v24, v158
v_mul_f32_e32 v159, v24, v159
v_mul_f32_e32 v168, v24, v168
v_mul_f32_e32 v169, v24, v169
v_mul_f32_e32 v170, v24, v170
v_mul_f32_e32 v171, v24, v171
v_mul_f32_e32 v172, v24, v172
)" R"(
v_mul_f32_e32 v173, v24, v173
v_mul_f32_e32 v174, v24, v174
v_mul_f32_e32 v175, v24, v175
v_mul_f32_e32 v184, v24, v184
v_mul_f32_e32 v185, v24, v185
v_mul_f32_e32 v186, v24, v186
v_mul_f32_e32 v187, v24, v187
v_mul_f32_e32 v188, v24, v188
v_mul_f32_e32 v189, v24, v189
v_mul_f32_e32 v190, v24, v190
v_mul_f32_e32 v191, v24, v191
v_mul_f32_e32 v200, v24, v200
v_mul_f32_e32 v201, v24, v201
v_mul_f32_e32 v202, v24, v202
v_mul_f32_e32 v203, v24, v203
v_mul_f32_e32 v204, v24, v204
v_mul_f32_e32 v205, v24, v205
v_mul_f32_e32 v206, v24, v206
v_mul_f32_e32 v207, v24, v207
v_mul_f32_e32 v216, v24, v216
v_mul_f32_e32 v217, v24, v217
v_mul_f32_e32 v218, v24, v218
v_mul_f32_e32 v219, v24, v219
v_mul_f32_e32 v220, v24, v220
v_mul_f32_e32 v221, v24, v221
v_mul_f32_e32 v222, v24, v222
v_mul_f32_e32 v223, v24, v223
v_mul_f32_e32 v232, v24, v232
v_mul_f32_e32 v233, v24, v233
v_mul_f32_e32 v234, v24, v234
v_mul_f32_e32 v235, v24, v235
v_mul_f32_e32 v236, v24, v236
v_mul_f32_e32 v237, v24, v237
v_mul_f32_e32 v238, v24, v238
v_mul_f32_e32 v239, v24, v239
v_mul_f32_e32 v248, v24, v248
v_mul_f32_e32 v249, v24, v249
v_mul_f32_e32 v250, v24, v250
v_mul_f32_e32 v251, v24, v251
v_mul_f32_e32 v252, v24, v252
v_mul_f32_e32 v253, v24, v253
v_mul_f32_e32 v254, v24, v254
v_mul_f32_e32 v255, v24, v255
s_branch .LzFIXG0
.LzFIXF0:
v_cmp_lt_f32_e32 vcc, 0xff7fffff, v28
s_nop 1
v_cndmask_b32_e32 v28, 0, v28, vcc
v_cmp_lt_f32_e32 vcc, 0xff7fffff, v29
s_nop 1
v_cndmask_b32_e32 v29, 0, v29, vcc
.LzFIXG0:
v_sub_f32_e32 v16, v16, v28
v_sub_f32_e32 v17, v17, v28
v_sub_f32_e32 v18, v18, v28
v_sub_f32_e32 v19, v19, v28
v_sub_f32_e32 v64, v64, v28
v_sub_f32_e32 v65, v65, v28
v_sub_f32_e32 v66, v66, v28
v_sub_f32_e32 v67, v67, v28
v_sub_f32_e32 v72, v72, v28
v_sub_f32_e32 v73, v73, v28
v_sub_f32_e32 v74, v74, v28
v_sub_f32_e32 v75, v75, v28
)" R"(
v_sub_f32_e32 v80, v80, v28
v_sub_f32_e32 v81, v81, v28
v_sub_f32_e32 v82, v82, v28
v_sub_f32_e32 v83, v83, v28
v_sub_f32_e32 v88, v88, v28
v_sub_f32_e32 v89, v89, v28
v_sub_f32_e32 v90, v90, v28
v_sub_f32_e32 v91, v91, v28
v_sub_f32_e32 v20, v20, v29
v_sub_f32_e32 v21, v21, v29
v_sub_f32_e32 v22, v22, v29
v_sub_f32_e32 v23, v23, v29
v_sub_f32_e32 v68, v68, v29
v_sub_f32_e32 v69, v69, v29
v_sub_f32_e32 v70, v70, v29
v_sub_f32_e32 v71, v71, v29
v_sub_f32_e32 v76, v76, v29
v_sub_f32_e32 v77, v77, v29
v_sub_f32_e32 v78, v78, v29
v_sub_f32_e32 v79, v79, v29
v_sub_f32_e32 v84, v84, v29
v_sub_f32_e32 v85, v85, v29
v_sub_f32_e32 v86, v86, v29
v_sub_f32_e32 v87, v87, v29
v_sub_f32_e32 v92, v92, v29
v_sub_f32_e32 v93, v93, v29
v_sub_f32_e32 v94, v94, v29
v_sub_f32_e32 v95, v95, v29
s_bitcmp0_b32 s88, 1
s_cbranch_scc1 .LzFIXH0
v_sub_f32_e32 v96, v96, v28
v_sub_f32_e32 v97, v97, v28
v_sub_f32_e32 v98, v98, v28
v_sub_f32_e32 v99, v99, v28
v_sub_f32_e32 v104, v104, v28
v_sub_f32_e32 v105, v105, v28
v_sub_f32_e32 v106, v106, v28
v_sub_f32_e32 v107, v107, v28
v_sub_f32_e32 v112, v112, v28
v_sub_f32_e32 v113, v113, v28
v_sub_f32_e32 v114, v114, v28
v_sub_f32_e32 v115, v115, v28
v_sub_f32_e32 v120, v120, v28
v_sub_f32_e32 v121, v121, v28
v_sub_f32_e32 v122, v122, v28
v_sub_f32_e32 v123, v123, v28
v_sub_f32_e32 v100, v100, v29
v_sub_f32_e32 v101, v101, v29
v_sub_f32_e32 v102, v102, v29
v_sub_f32_e32 v103, v103, v29
v_sub_f32_e32 v108, v108, v29
v_sub_f32_e32 v109, v109, v29
v_sub_f32_e32 v110, v110, v29
v_sub_f32_e32 v111, v111, v29
v_sub_f32_e32 v116, v116, v29
v_sub_f32_e32 v117, v117, v29
v_sub_f32_e32 v118, v118, v29
v_sub_f32_e32 v119, v119, v29
v_sub_f32_e32 v124, v124, v29
v_sub_f32_e32 v125, v125, v29
v_sub_f32_e32 v126, v126, v29
v_sub_f32_e32 v127, v127, v29
.LzFIXH0:
v_exp_f32_e32 v32, v64
)" R"(
v_exp_f32_e32 v28, v65
s_nop 0
v_add_f32_e32 v14, v32, v28
Cv 32,32,28
v_exp_f32_e32 v33, v66
v_exp_f32_e32 v29, v67
v_add_f32_e32 v14, v14, v33
v_add_f32_e32 v14, v14, v29
Cv 33,33,29
v_exp_f32_e32 v34, v72
v_exp_f32_e32 v24, v73
v_add_f32_e32 v14, v14, v34
v_add_f32_e32 v14, v14, v24
Cv 34,34,24
v_exp_f32_e32 v35, v74
v_exp_f32_e32 v25, v75
v_add_f32_e32 v14, v14, v35
v_add_f32_e32 v14, v14, v25
Cv 35,35,25
v_exp_f32_e32 v36, v68
v_exp_f32_e32 v26, v69
s_nop 0
v_add_f32_e32 v15, v36, v26
Cv 36,36,26
v_exp_f32_e32 v37, v70
v_exp_f32_e32 v27, v71
v_add_f32_e32 v15, v15, v37
v_add_f32_e32 v15, v15, v27
Cv 37,37,27
v_exp_f32_e32 v38, v76
v_exp_f32_e32 v28, v77
v_add_f32_e32 v15, v15, v38
v_add_f32_e32 v15, v15, v28
Cv 38,38,28
v_exp_f32_e32 v39, v78
v_exp_f32_e32 v29, v79
v_add_f32_e32 v15, v15, v39
v_add_f32_e32 v15, v15, v29
Cv 39,39,29
v_exp_f32_e32 v40, v80
v_exp_f32_e32 v24, v81
v_add_f32_e32 v14, v14, v40
v_add_f32_e32 v14, v14, v24
Cv 40,40,24
v_exp_f32_e32 v41, v82
v_exp_f32_e32 v25, v83
v_add_f32_e32 v14, v14, v41
v_add_f32_e32 v14, v14, v25
Cv 41,41,25
v_exp_f32_e32 v42, v88
v_exp_f32_e32 v26, v89
v_add_f32_e32 v14, v14, v42
v_add_f32_e32 v14, v14, v26
Cv 42,42,26
v_exp_f32_e32 v43, v90
v_exp_f32_e32 v27, v91
v_add_f32_e32 v14, v14, v43
v_add_f32_e32 v14, v14, v27
Cv 43,43,27
v_exp_f32_e32 v44, v84
v_exp_f32_e32 v28, v85
v_add_f32_e32 v15, v15, v44
v_add_f32_e32 v15, v15, v28
Cv 44,44,28
)" R"(
v_exp_f32_e32 v45, v86
v_exp_f32_e32 v29, v87
v_add_f32_e32 v15, v15, v45
v_add_f32_e32 v15, v15, v29
Cv 45,45,29
v_exp_f32_e32 v46, v92
v_exp_f32_e32 v24, v93
v_add_f32_e32 v15, v15, v46
v_add_f32_e32 v15, v15, v24
Cv 46,46,24
v_exp_f32_e32 v47, v94
v_exp_f32_e32 v25, v95
v_add_f32_e32 v15, v15, v47
v_add_f32_e32 v15, v15, v25
Cv 47,47,25
s_nop 1
s_setpc_b64 s[30:31]
.LzMASK1:
s_lshl1_add_u32 s28, s57, s33
s_mov_b32 s29, 0xff800000
s_cmp_gt_u32 s28, 5
s_cbranch_scc1 .LzMASKE1
s_cmp_eq_u32 s28, 5
s_cbranch_scc1 .LzMASKB1
v_mov_b32 v104, s29
v_mov_b32 v105, s29
v_mov_b32 v106, s29
v_mov_b32 v107, s29
v_mov_b32 v112, s29
v_mov_b32 v113, s29
v_mov_b32 v114, s29
v_mov_b32 v115, s29
v_mov_b32 v116, s29
v_mov_b32 v117, s29
v_mov_b32 v118, s29
v_mov_b32 v119, s29
v_mov_b32 v120, s29
v_mov_b32 v121, s29
v_mov_b32 v122, s29
v_mov_b32 v123, s29
v_mov_b32 v124, s29
v_mov_b32 v125, s29
v_mov_b32 v126, s29
v_mov_b32 v127, s29
s_cmp_eq_u32 s28, 4
s_cbranch_scc1 .LzMASKA1
v_mov_b32 v96, s29
v_mov_b32 v97, s29
v_mov_b32 v98, s29
v_mov_b32 v99, s29
v_mov_b32 v100, s29
v_mov_b32 v101, s29
v_mov_b32 v102, s29
v_mov_b32 v103, s29
v_mov_b32 v108, s29
v_mov_b32 v109, s29
v_mov_b32 v110, s29
v_mov_b32 v111, s29
.LzMASKE1:
s_setpc_b64 s[30:31]
.LzMASKA1:
s_mov_b32 exec_lo, 0xf0000
s_mov_b32 exec_hi, 0xfff00ff
v_mov_b32 v96, s29
)" R"(
v_mov_b32 v108, s29
s_mov_b32 exec_lo, 0x1f0001
s_mov_b32 exec_hi, 0x1fff01ff
v_mov_b32 v97, s29
v_mov_b32 v109, s29
s_mov_b32 exec_lo, 0x3f0003
s_mov_b32 exec_hi, 0x3fff03ff
v_mov_b32 v98, s29
v_mov_b32 v110, s29
s_mov_b32 exec_lo, 0x7f0007
s_mov_b32 exec_hi, 0x7fff07ff
v_mov_b32 v99, s29
v_mov_b32 v111, s29
s_mov_b64 exec, -1
s_setpc_b64 s[30:31]
.LzMASKB1:
s_nop 1
v_mov_b32 v120, s29
v_mov_b32 v121, s29
v_mov_b32 v122, s29
v_mov_b32 v123, s29
s_mov_b32 exec_lo, 0xf0000
s_mov_b32 exec_hi, 0xfff00ff
v_mov_b32 v112, s29
v_mov_b32 v124, s29
s_mov_b32 exec_lo, 0x1f0001
s_mov_b32 exec_hi, 0x1fff01ff
v_mov_b32 v113, s29
v_mov_b32 v125, s29
s_mov_b32 exec_lo, 0x3f0003
s_mov_b32 exec_hi, 0x3fff03ff
v_mov_b32 v114, s29
v_mov_b32 v126, s29
s_mov_b32 exec_lo, 0x7f0007
s_mov_b32 exec_hi, 0x7fff07ff
v_mov_b32 v115, s29
v_mov_b32 v127, s29
s_mov_b64 exec, -1
s_setpc_b64 s[30:31]
.LzFIX1:
s_nop 7
v_max_f32_e32 v28, v96, v97
v_max3_f32 v28, v28, v98, v99
v_max3_f32 v28, v28, v104, v105
v_max3_f32 v28, v28, v106, v107
v_max3_f32 v28, v28, v112, v113
v_max3_f32 v28, v28, v114, v115
v_max3_f32 v28, v28, v120, v121
v_max3_f32 v28, v28, v122, v123
v_mov_b32 v24, v28
s_nop 1
v_permlane32_swap_b32_e32 v28, v24
v_max_f32_e32 v28, v28, v24
v_mov_b32 v24, v28
s_nop 1
v_permlane16_swap_b32_e32 v28, v24
v_max_f32_e32 v28, v28, v24
v_max_f32_e32 v29, v100, v101
v_max3_f32 v29, v29, v102, v103
v_max3_f32 v29, v29, v108, v109
v_max3_f32 v29, v29, v110, v111
v_max3_f32 v29, v29, v116, v117
v_max3_f32 v29, v29, v118, v119
v_max3_f32 v29, v29, v124, v125
)" R"(
v_max3_f32 v29, v29, v126, v127
v_mov_b32 v24, v29
s_nop 1
v_permlane32_swap_b32_e32 v29, v24
v_max_f32_e32 v29, v29, v24
v_mov_b32 v24, v29
s_nop 1
v_permlane16_swap_b32_e32 v29, v24
v_max_f32_e32 v29, v29, v24
s_bitcmp1_b32 s88, 0
s_cbranch_scc1 .LzFIXF1
v_max_f32_e32 v28, 0, v28
v_sub_f32_e32 v24, 0, v28
v_exp_f32_e32 v24, v24
s_nop 0
v_mul_f32_e32 v12, v24, v12
v_mul_f32_e32 v128, v24, v128
v_mul_f32_e32 v129, v24, v129
v_mul_f32_e32 v130, v24, v130
v_mul_f32_e32 v131, v24, v131
v_mul_f32_e32 v132, v24, v132
v_mul_f32_e32 v133, v24, v133
v_mul_f32_e32 v134, v24, v134
v_mul_f32_e32 v135, v24, v135
v_mul_f32_e32 v144, v24, v144
v_mul_f32_e32 v145, v24, v145
v_mul_f32_e32 v146, v24, v146
v_mul_f32_e32 v147, v24, v147
v_mul_f32_e32 v148, v24, v148
v_mul_f32_e32 v149, v24, v149
v_mul_f32_e32 v150, v24, v150
v_mul_f32_e32 v151, v24, v151
v_mul_f32_e32 v160, v24, v160
v_mul_f32_e32 v161, v24, v161
v_mul_f32_e32 v162, v24, v162
v_mul_f32_e32 v163, v24, v163
v_mul_f32_e32 v164, v24, v164
v_mul_f32_e32 v165, v24, v165
v_mul_f32_e32 v166, v24, v166
v_mul_f32_e32 v167, v24, v167
v_mul_f32_e32 v176, v24, v176
v_mul_f32_e32 v177, v24, v177
v_mul_f32_e32 v178, v24, v178
v_mul_f32_e32 v179, v24, v179
v_mul_f32_e32 v180, v24, v180
v_mul_f32_e32 v181, v24, v181
v_mul_f32_e32 v182, v24, v182
v_mul_f32_e32 v183, v24, v183
v_mul_f32_e32 v192, v24, v192
v_mul_f32_e32 v193, v24, v193
v_mul_f32_e32 v194, v24, v194
v_mul_f32_e32 v195, v24, v195
v_mul_f32_e32 v196, v24, v196
v_mul_f32_e32 v197, v24, v197
v_mul_f32_e32 v198, v24, v198
v_mul_f32_e32 v199, v24, v199
v_mul_f32_e32 v208, v24, v208
v_mul_f32_e32 v209, v24, v209
v_mul_f32_e32 v210, v24, v210
v_mul_f32_e32 v211, v24, v211
v_mul_f32_e32 v212, v24, v212
v_mul_f32_e32 v213, v24, v213
v_mul_f32_e32 v214, v24, v214
v_mul_f32_e32 v215, v24, v215
)" R"(
v_mul_f32_e32 v224, v24, v224
v_mul_f32_e32 v225, v24, v225
v_mul_f32_e32 v226, v24, v226
v_mul_f32_e32 v227, v24, v227
v_mul_f32_e32 v228, v24, v228
v_mul_f32_e32 v229, v24, v229
v_mul_f32_e32 v230, v24, v230
v_mul_f32_e32 v231, v24, v231
v_mul_f32_e32 v240, v24, v240
v_mul_f32_e32 v241, v24, v241
v_mul_f32_e32 v242, v24, v242
v_mul_f32_e32 v243, v24, v243
v_mul_f32_e32 v244, v24, v244
v_mul_f32_e32 v245, v24, v245
v_mul_f32_e32 v246, v24, v246
v_mul_f32_e32 v247, v24, v247
v_max_f32_e32 v29, 0, v29
v_sub_f32_e32 v24, 0, v29
v_exp_f32_e32 v24, v24
s_nop 0
v_mul_f32_e32 v13, v24, v13
v_mul_f32_e32 v136, v24, v136
v_mul_f32_e32 v137, v24, v137
v_mul_f32_e32 v138, v24, v138
v_mul_f32_e32 v139, v24, v139
v_mul_f32_e32 v140, v24, v140
v_mul_f32_e32 v141, v24, v141
v_mul_f32_e32 v142, v24, v142
v_mul_f32_e32 v143, v24, v143
v_mul_f32_e32 v152, v24, v152
v_mul_f32_e32 v153, v24, v153
v_mul_f32_e32 v154, v24, v154
v_mul_f32_e32 v155, v24, v155
v_mul_f32_e32 v156, v24, v156
v_mul_f32_e32 v157, v24, v157
v_mul_f32_e32 v158, v24, v158
v_mul_f32_e32 v159, v24, v159
v_mul_f32_e32 v168, v24, v168
v_mul_f32_e32 v169, v24, v169
v_mul_f32_e32 v170, v24, v170
v_mul_f32_e32 v171, v24, v171
v_mul_f32_e32 v172, v24, v172
v_mul_f32_e32 v173, v24, v173
v_mul_f32_e32 v174, v24, v174
v_mul_f32_e32 v175, v24, v175
v_mul_f32_e32 v184, v24, v184
v_mul_f32_e32 v185, v24, v185
v_mul_f32_e32 v186, v24, v186
v_mul_f32_e32 v187, v24, v187
v_mul_f32_e32 v188, v24, v188
v_mul_f32_e32 v189, v24, v189
v_mul_f32_e32 v190, v24, v190
v_mul_f32_e32 v191, v24, v191
v_mul_f32_e32 v200, v24, v200
v_mul_f32_e32 v201, v24, v201
v_mul_f32_e32 v202, v24, v202
v_mul_f32_e32 v203, v24, v203
v_mul_f32_e32 v204, v24, v204
v_mul_f32_e32 v205, v24, v205
v_mul_f32_e32 v206, v24, v206
v_mul_f32_e32 v207, v24, v207
v_mul_f32_e32 v216, v24, v216
v_mul_f32_e32 v217, v24, v217
v_mul_f32_e32 v218, v24, v218
)" R"(
v_mul_f32_e32 v219, v24, v219
v_mul_f32_e32 v220, v24, v220
v_mul_f32_e32 v221, v24, v221
v_mul_f32_e32 v222, v24, v222
v_mul_f32_e32 v223, v24, v223
v_mul_f32_e32 v232, v24, v232
v_mul_f32_e32 v233, v24, v233
v_mul_f32_e32 v234, v24, v234
v_mul_f32_e32 v235, v24, v235
v_mul_f32_e32 v236, v24, v236
v_mul_f32_e32 v237, v24, v237
v_mul_f32_e32 v238, v24, v238
v_mul_f32_e32 v239, v24, v239
v_mul_f32_e32 v248, v24, v248
v_mul_f32_e32 v249, v24, v249
v_mul_f32_e32 v250, v24, v250
v_mul_f32_e32 v251, v24, v251
v_mul_f32_e32 v252, v24, v252
v_mul_f32_e32 v253, v24, v253
v_mul_f32_e32 v254, v24, v254
v_mul_f32_e32 v255, v24, v255
s_branch .LzFIXG1
.LzFIXF1:
v_cmp_lt_f32_e32 vcc, 0xff7fffff, v28
s_nop 1
v_cndmask_b32_e32 v28, 0, v28, vcc
v_cmp_lt_f32_e32 vcc, 0xff7fffff, v29
s_nop 1
v_cndmask_b32_e32 v29, 0, v29, vcc
.LzFIXG1:
v_sub_f32_e32 v16, v16, v28
v_sub_f32_e32 v17, v17, v28
v_sub_f32_e32 v18, v18, v28
v_sub_f32_e32 v19, v19, v28
v_sub_f32_e32 v96, v96, v28
v_sub_f32_e32 v97, v97, v28
v_sub_f32_e32 v98, v98, v28
v_sub_f32_e32 v99, v99, v28
v_sub_f32_e32 v104, v104, v28
v_sub_f32_e32 v105, v105, v28
v_sub_f32_e32 v106, v106, v28
v_sub_f32_e32 v107, v107, v28
v_sub_f32_e32 v112, v112, v28
v_sub_f32_e32 v113, v113, v28
v_sub_f32_e32 v114, v114, v28
v_sub_f32_e32 v115, v115, v28
v_sub_f32_e32 v120, v120, v28
v_sub_f32_e32 v121, v121, v28
v_sub_f32_e32 v122, v122, v28
v_sub_f32_e32 v123, v123, v28
v_sub_f32_e32 v20, v20, v29
v_sub_f32_e32 v21, v21, v29
v_sub_f32_e32 v22, v22, v29
v_sub_f32_e32 v23, v23, v29
v_sub_f32_e32 v100, v100, v29
v_sub_f32_e32 v101, v101, v29
v_sub_f32_e32 v102, v102, v29
v_sub_f32_e32 v103, v103, v29
v_sub_f32_e32 v108, v108, v29
v_sub_f32_e32 v109, v109, v29
v_sub_f32_e32 v110, v110, v29
v_sub_f32_e32 v111, v111, v29
v_sub_f32_e32 v116, v116, v29
v_sub_f32_e32 v117, v117, v29
)" R"(
v_sub_f32_e32 v118, v118, v29
v_sub_f32_e32 v119, v119, v29
v_sub_f32_e32 v124, v124, v29
v_sub_f32_e32 v125, v125, v29
v_sub_f32_e32 v126, v126, v29
v_sub_f32_e32 v127, v127, v29
s_bitcmp0_b32 s88, 1
s_cbranch_scc1 .LzFIXH1
v_sub_f32_e32 v64, v64, v28
v_sub_f32_e32 v65, v65, v28
v_sub_f32_e32 v66, v66, v28
v_sub_f32_e32 v67, v67, v28
v_sub_f32_e32 v72, v72, v28
v_sub_f32_e32 v73, v73, v28
v_sub_f32_e32 v74, v74, v28
v_sub_f32_e32 v75, v75, v28
v_sub_f32_e32 v80, v80, v28
v_sub_f32_e32 v81, v81, v28
v_sub_f32_e32 v82, v82, v28
v_sub_f32_e32 v83, v83, v28
v_sub_f32_e32 v88, v88, v28
v_sub_f32_e32 v89, v89, v28
v_sub_f32_e32 v90, v90, v28
v_sub_f32_e32 v91, v91, v28
v_sub_f32_e32 v68, v68, v29
v_sub_f32_e32 v69, v69, v29
v_sub_f32_e32 v70, v70, v29
v_sub_f32_e32 v71, v71, v29
v_sub_f32_e32 v76, v76, v29
v_sub_f32_e32 v77, v77, v29
v_sub_f32_e32 v78, v78, v29
v_sub_f32_e32 v79, v79, v29
v_sub_f32_e32 v84, v84, v29
v_sub_f32_e32 v85, v85, v29
v_sub_f32_e32 v86, v86, v29
v_sub_f32_e32 v87, v87, v29
v_sub_f32_e32 v92, v92, v29
v_sub_f32_e32 v93, v93, v29
v_sub_f32_e32 v94, v94, v29
v_sub_f32_e32 v95, v95, v29
.LzFIXH1:
v_exp_f32_e32 v48, v96
v_exp_f32_e32 v26, v97
s_nop 0
v_add_f32_e32 v14, v48, v26
Cv 48,48,26
v_exp_f32_e32 v49, v98
v_exp_f32_e32 v27, v99
v_add_f32_e32 v14, v14, v49
v_add_f32_e32 v14, v14, v27
Cv 49,49,27
v_exp_f32_e32 v50, v104
v_exp_f32_e32 v28, v105
v_add_f32_e32 v14, v14, v50
v_add_f32_e32 v14, v14, v28
Cv 50,50,28
v_exp_f32_e32 v51, v106
v_exp_f32_e32 v29, v107
v_add_f32_e32 v14, v14, v51
v_add_f32_e32 v14, v14, v29
Cv 51,51,29
v_exp_f32_e32 v52, v100
v_exp_f32_e32 v24, v101
s_nop 0
)" R"(
v_add_f32_e32 v15, v52, v24
Cv 52,52,24
v_exp_f32_e32 v53, v102
v_exp_f32_e32 v25, v103
v_add_f32_e32 v15, v15, v53
v_add_f32_e32 v15, v15, v25
Cv 53,53,25
v_exp_f32_e32 v54, v108
v_exp_f32_e32 v26, v109
v_add_f32_e32 v15, v15, v54
v_add_f32_e32 v15, v15, v26
Cv 54,54,26
v_exp_f32_e32 v55, v110
v_exp_f32_e32 v27, v111
v_add_f32_e32 v15, v15, v55
v_add_f32_e32 v15, v15, v27
Cv 55,55,27
v_exp_f32_e32 v56, v112
v_exp_f32_e32 v28, v113
v_add_f32_e32 v14, v14, v56
v_add_f32_e32 v14, v14, v28
Cv 56,56,28
v_exp_f32_e32 v57, v114
v_exp_f32_e32 v29, v115
v_add_f32_e32 v14, v14, v57
v_add_f32_e32 v14, v14, v29
Cv 57,57,29
v_exp_f32_e32 v58, v120
v_exp_f32_e32 v24, v121
v_add_f32_e32 v14, v14, v58
v_add_f32_e32 v14, v14, v24
Cv 58,58,24
v_exp_f32_e32 v59, v122
v_exp_f32_e32 v25, v123
v_add_f32_e32 v14, v14, v59
v_add_f32_e32 v14, v14, v25
Cv 59,59,25
v_exp_f32_e32 v60, v116
v_exp_f32_e32 v26, v117
v_add_f32_e32 v15, v15, v60
v_add_f32_e32 v15, v15, v26
Cv 60,60,26
v_exp_f32_e32 v61, v118
v_exp_f32_e32 v27, v119
v_add_f32_e32 v15, v15, v61
v_add_f32_e32 v15, v15, v27
Cv 61,61,27
v_exp_f32_e32 v62, v124
v_exp_f32_e32 v28, v125
v_add_f32_e32 v15, v15, v62
v_add_f32_e32 v15, v15, v28
Cv 62,62,28
v_exp_f32_e32 v63, v126
v_exp_f32_e32 v29, v127
v_add_f32_e32 v15, v15, v63
v_add_f32_e32 v15, v15, v29
Cv 63,63,29
s_nop 1
s_setpc_b64 s[30:31]
.Lzsp1:
s_sub_u32 s53, s53, 1
s_cbranch_scc1 .Lzsq1
s_sleep 1
buffer_load_dword v4, v2, s[20:23], s85 offen sc0 sc1
)" R"(
s_waitcnt vmcnt(0)
v_xor_b32 v4, s3, v4
v_cmp_gt_u32_e32 vcc, 2, v4
s_andn2_b32 s28, 0xff, vcc_lo
s_cbranch_scc1 .Lzsp1
s_branch .Lzok1
.Lzsq1:
s_mov_b32 s53, 0
s_branch .Lzok1
.Lzsp2:
s_sub_u32 s53, s53, 1
s_cbranch_scc1 .Lzsq2
s_sleep 1
buffer_load_dword v4, v2, s[20:23], s85 offen sc0 sc1
s_waitcnt vmcnt(0)
v_xor_b32 v4, s3, v4
v_cmp_gt_u32_e32 vcc, 2, v4
s_andn2_b32 s28, 0xff, vcc_lo
s_cbranch_scc1 .Lzsp2
s_branch .Lzok2
.Lzsq2:
s_mov_b32 s53, 0
s_branch .Lzok2
.Lzsp3:
s_sub_u32 s53, s53, 1
s_cbranch_scc1 .Lzsq3
s_sleep 1
buffer_load_dword v4, v5, s[20:23], s50 offen offset:0 sc0 sc1
s_waitcnt vmcnt(0)
v_cmp_eq_u32_e32 vcc, s3, v4
s_andn2_b64 s[28:29], s[60:61], vcc
s_cbranch_scc1 .Lzsp3
s_branch .Lzok3
.Lzsq3:
s_mov_b32 s53, 0
s_branch .Lzok3
.Lzsp4:
s_sub_u32 s53, s53, 1
s_cbranch_scc1 .Lzsq4
s_sleep 1
buffer_load_dword v4, v7, s[20:23], s52 offen offset:0 sc0 sc1
s_waitcnt vmcnt(0)
v_cmp_eq_u32_e32 vcc, s3, v4
s_andn2_b64 s[28:29], 1, vcc
s_cbranch_scc1 .Lzsp4
s_branch .Lzok4
.Lzsq4:
s_mov_b32 s53, 0
s_branch .Lzok4
.Lzsp5:
s_sub_u32 s53, s53, 1
s_cbranch_scc1 .Lzsq5
s_sleep 1
buffer_load_dword v4, v5, s[20:23], s50 offen offset:0 sc0 sc1
s_waitcnt vmcnt(0)
v_cmp_eq_u32_e32 vcc, s3, v4
s_andn2_b64 s[28:29], s[60:61], vcc
s_cbranch_scc1 .Lzsp5
s_branch .Lzok5
.Lzsq5:
s_mov_b32 s53, 0
s_branch .Lzok5
.Lzsp6:
s_sub_u32 s53, s53, 1
)" R"(
s_cbranch_scc1 .Lzsq6
s_sleep 1
buffer_load_dword v4, v7, s[20:23], s83 offen offset:4 sc0 sc1
s_waitcnt vmcnt(0)
v_cmp_eq_u32_e32 vcc, s3, v4
s_andn2_b64 s[28:29], 1, vcc
s_cbranch_scc1 .Lzsp6
s_branch .Lzok6
.Lzsq6:
s_mov_b32 s53, 0
s_branch .Lzok6
.LzT2S:
s_cmp_lg_u32 s33, 0
s_cbranch_scc1 .LzT2E
.LzT2:
s_mov_b32 s28, 0xc100000
buffer_load_dwordx2 v[32:33], v1, s[20:23], s28 offen offset:0 sc0 sc1
buffer_load_dwordx2 v[34:35], v1, s[20:23], s28 offen offset:1024 sc0 sc1
buffer_load_dwordx2 v[36:37], v1, s[20:23], s28 offen offset:2048 sc0 sc1
buffer_load_dwordx2 v[38:39], v1, s[20:23], s28 offen offset:3072 sc0 sc1
s_mov_b32 s28, 0xc101000
buffer_load_dwordx2 v[40:41], v1, s[20:23], s28 offen offset:0 sc0 sc1
buffer_load_dwordx2 v[42:43], v1, s[20:23], s28 offen offset:1024 sc0 sc1
buffer_load_dwordx2 v[44:45], v1, s[20:23], s28 offen offset:2048 sc0 sc1
buffer_load_dwordx2 v[46:47], v1, s[20:23], s28 offen offset:3072 sc0 sc1
s_mov_b32 s28, 0xc102000
buffer_load_dwordx2 v[48:49], v1, s[20:23], s28 offen offset:0 sc0 sc1
buffer_load_dwordx2 v[50:51], v1, s[20:23], s28 offen offset:1024 sc0 sc1
buffer_load_dwordx2 v[52:53], v1, s[20:23], s28 offen offset:2048 sc0 sc1
buffer_load_dwordx2 v[54:55], v1, s[20:23], s28 offen offset:3072 sc0 sc1
s_mov_b32 s28, 0xc103000
buffer_load_dwordx2 v[56:57], v1, s[20:23], s28 offen offset:0 sc0 sc1
buffer_load_dwordx2 v[58:59], v1, s[20:23], s28 offen offset:1024 sc0 sc1
buffer_load_dwordx2 v[60:61], v1, s[20:23], s28 offen offset:2048 sc0 sc1
buffer_load_dwordx2 v[62:63], v1, s[20:23], s28 offen offset:3072 sc0 sc1
s_mov_b32 s28, 0xc104000
buffer_load_dwordx2 v[64:65], v1, s[20:23], s28 offen offset:0 sc0 sc1
buffer_load_dwordx2 v[66:67], v1, s[20:23], s28 offen offset:1024 sc0 sc1
buffer_load_dwordx2 v[68:69], v1, s[20:23], s28 offen offset:2048 sc0 sc1
buffer_load_dwordx2 v[70:71], v1, s[20:23], s28 offen offset:3072 sc0 sc1
s_mov_b32 s28, 0xc105000
buffer_load_dwordx2 v[72:73], v1, s[20:23], s28 offen offset:0 sc0 sc1
buffer_load_dwordx2 v[74:75], v1, s[20:23], s28 offen offset:1024 sc0 sc1
buffer_load_dwordx2 v[76:77], v1, s[20:23], s28 offen offset:2048 sc0 sc1
buffer_load_dwordx2 v[78:79], v1, s[20:23], s28 offen offset:3072 sc0 sc1
s_mov_b32 s28, 0xc106000
buffer_load_dwordx2 v[80:81], v1, s[20:23], s28 offen offset:0 sc0 sc1
buffer_load_dwordx2 v[82:83], v1, s[20:23], s28 offen offset:1024 sc0 sc1
buffer_load_dwordx2 v[84:85], v1, s[20:23], s28 offen offset:2048 sc0 sc1
buffer_load_dwordx2 v[86:87], v1, s[20:23], s28 offen offset:3072 sc0 sc1
s_mov_b32 s28, 0xc107000
buffer_load_dwordx2 v[88:89], v1, s[20:23], s28 offen offset:0 sc0 sc1
buffer_load_dwordx2 v[90:91], v1, s[20:23], s28 offen offset:1024 sc0 sc1
buffer_load_dwordx2 v[92:93], v1, s[20:23], s28 offen offset:2048 sc0 sc1
buffer_load_dwordx2 v[94:95], v1, s[20:23], s28 offen offset:3072 sc0 sc1
v_mov_b32 v26, 0
v_mov_b32 v27, -1
s_waitcnt vmcnt(0)
v_max3_u32 v26, v26, v33, v35
v_min3_u32 v27, v27, v33, v35
v_max3_u32 v26, v26, v37, v39
v_min3_u32 v27, v27, v37, v39
v_max3_u32 v26, v26, v41, v43
v_min3_u32 v27, v27, v41, v43
)" R"(
v_max3_u32 v26, v26, v45, v47
v_min3_u32 v27, v27, v45, v47
v_max3_u32 v26, v26, v49, v51
v_min3_u32 v27, v27, v49, v51
v_max3_u32 v26, v26, v53, v55
v_min3_u32 v27, v27, v53, v55
v_max3_u32 v26, v26, v57, v59
v_min3_u32 v27, v27, v57, v59
v_max3_u32 v26, v26, v61, v63
v_min3_u32 v27, v27, v61, v63
v_max3_u32 v26, v26, v65, v67
v_min3_u32 v27, v27, v65, v67
v_max3_u32 v26, v26, v69, v71
v_min3_u32 v27, v27, v69, v71
v_max3_u32 v26, v26, v73, v75
v_min3_u32 v27, v27, v73, v75
v_max3_u32 v26, v26, v77, v79
v_min3_u32 v27, v27, v77, v79
v_max3_u32 v26, v26, v81, v83
v_min3_u32 v27, v27, v81, v83
v_max3_u32 v26, v26, v85, v87
v_min3_u32 v27, v27, v85, v87
v_max3_u32 v26, v26, v89, v91
v_min3_u32 v27, v27, v89, v91
v_max3_u32 v26, v26, v93, v95
v_min3_u32 v27, v27, v93, v95
v_cmp_ne_u32_e32 vcc, s3, v26
v_cmp_ne_u32_e64 s[28:29], s3, v27
s_or_b64 vcc, vcc, s[28:29]
s_cbranch_scc0 .LzT2G
s_sub_u32 s53, s53, 1
s_cbranch_scc0 .LzT2
s_mov_b32 s53, 0
.LzT2G:
v_max_u32 v26, v32, v34
v_min_u32 v27, v32, v34
v_med3_u32 v27, v26, v27, v36
v_max_u32 v26, v26, v36
v_med3_u32 v27, v26, v27, v38
v_max_u32 v26, v26, v38
v_med3_u32 v27, v26, v27, v40
v_max_u32 v26, v26, v40
v_med3_u32 v27, v26, v27, v42
v_max_u32 v26, v26, v42
v_med3_u32 v27, v26, v27, v44
v_max_u32 v26, v26, v44
v_med3_u32 v27, v26, v27, v46
v_max_u32 v26, v26, v46
v_med3_u32 v27, v26, v27, v48
v_max_u32 v26, v26, v48
v_med3_u32 v27, v26, v27, v50
v_max_u32 v26, v26, v50
v_med3_u32 v27, v26, v27, v52
v_max_u32 v26, v26, v52
v_med3_u32 v27, v26, v27, v54
v_max_u32 v26, v26, v54
v_med3_u32 v27, v26, v27, v56
v_max_u32 v26, v26, v56
v_med3_u32 v27, v26, v27, v58
v_max_u32 v26, v26, v58
v_med3_u32 v27, v26, v27, v60
v_max_u32 v26, v26, v60
v_med3_u32 v27, v26, v27, v62
v_max_u32 v26, v26, v62
)" R"(
v_med3_u32 v27, v26, v27, v64
v_max_u32 v26, v26, v64
v_med3_u32 v27, v26, v27, v66
v_max_u32 v26, v26, v66
v_med3_u32 v27, v26, v27, v68
v_max_u32 v26, v26, v68
v_med3_u32 v27, v26, v27, v70
v_max_u32 v26, v26, v70
v_med3_u32 v27, v26, v27, v72
v_max_u32 v26, v26, v72
v_med3_u32 v27, v26, v27, v74
v_max_u32 v26, v26, v74
v_med3_u32 v27, v26, v27, v76
v_max_u32 v26, v26, v76
v_med3_u32 v27, v26, v27, v78
v_max_u32 v26, v26, v78
v_med3_u32 v27, v26, v27, v80
v_max_u32 v26, v26, v80
v_med3_u32 v27, v26, v27, v82
v_max_u32 v26, v26, v82
v_med3_u32 v27, v26, v27, v84
v_max_u32 v26, v26, v84
v_med3_u32 v27, v26, v27, v86
v_max_u32 v26, v26, v86
v_med3_u32 v27, v26, v27, v88
v_max_u32 v26, v26, v88
v_med3_u32 v27, v26, v27, v90
v_max_u32 v26, v26, v90
v_med3_u32 v27, v26, v27, v92
v_max_u32 v26, v26, v92
v_med3_u32 v27, v26, v27, v94
v_max_u32 v26, v26, v94
v_mov_b32 v24, v26
v_mov_b32 v25, v27
s_nop 1
v_permlane32_swap_b32_e32 v26, v24
v_permlane32_swap_b32_e32 v27, v25
v_max_u32 v27, v27, v25
v_med3_u32 v27, v26, v27, v24
v_max_u32 v26, v26, v24
v_mov_b32 v24, v26
v_mov_b32 v25, v27
s_nop 1
v_permlane16_swap_b32_e32 v26, v24
v_permlane16_swap_b32_e32 v27, v25
v_max_u32 v27, v27, v25
v_med3_u32 v27, v26, v27, v24
v_max_u32 v26, v26, v24
s_nop 1
v_mov_b32_dpp v24, v26 row_ror:8 row_mask:0xf bank_mask:0xf
v_mov_b32_dpp v25, v27 row_ror:8 row_mask:0xf bank_mask:0xf
v_max_u32 v27, v27, v25
v_med3_u32 v27, v26, v27, v24
v_max_u32 v26, v26, v24
s_nop 1
v_mov_b32_dpp v24, v26 row_ror:4 row_mask:0xf bank_mask:0xf
v_mov_b32_dpp v25, v27 row_ror:4 row_mask:0xf bank_mask:0xf
v_max_u32 v27, v27, v25
v_med3_u32 v27, v26, v27, v24
v_max_u32 v26, v26, v24
s_nop 1
v_mov_b32_dpp v24, v26 row_ror:2 row_mask:0xf bank_mask:0xf
v_mov_b32_dpp v25, v27 row_ror:2 row_mask:0xf bank_mask:0xf
v_max_u32 v27, v27, v25
)" R"(
v_med3_u32 v27, v26, v27, v24
v_max_u32 v26, v26, v24
s_nop 1
v_mov_b32_dpp v24, v26 row_ror:1 row_mask:0xf bank_mask:0xf
v_mov_b32_dpp v25, v27 row_ror:1 row_mask:0xf bank_mask:0xf
v_max_u32 v27, v27, v25
v_med3_u32 v27, v26, v27, v24
v_max_u32 v26, v26, v24
v_and_b32 v26, 0xffff, v26
v_lshl_or_b32 v26, v27, 16, v26
v_not_b32 v24, v26
v_mov_b32 v25, s3
s_mov_b32 s28, 0xc108000
buffer_store_dwordx2 v[24:25], v7, s[20:23], s28 offen sc0 sc1
s_branch .LzT2E
.LzA7:
s_waitcnt vmcnt(0)
s_nop 7
v_mul_f32_e32 v96, s54, v96
v_mul_f32_e32 v97, s54, v97
Cv 96,96,97
v_accvgpr_write_b32 a112, v96
v_mul_f32_e32 v98, s54, v98
v_mul_f32_e32 v99, s54, v99
Cv 98,98,99
v_accvgpr_write_b32 a113, v98
v_mul_f32_e32 v100, s54, v100
v_mul_f32_e32 v101, s54, v101
Cv 100,100,101
v_accvgpr_write_b32 a114, v100
v_mul_f32_e32 v102, s54, v102
v_mul_f32_e32 v103, s54, v103
Cv 102,102,103
v_accvgpr_write_b32 a115, v102
v_mul_f32_e32 v104, s54, v104
v_mul_f32_e32 v105, s54, v105
Cv 104,104,105
v_accvgpr_write_b32 a116, v104
v_mul_f32_e32 v106, s54, v106
v_mul_f32_e32 v107, s54, v107
Cv 106,106,107
v_accvgpr_write_b32 a117, v106
v_mul_f32_e32 v108, s54, v108
v_mul_f32_e32 v109, s54, v109
Cv 108,108,109
v_accvgpr_write_b32 a118, v108
v_mul_f32_e32 v110, s54, v110
v_mul_f32_e32 v111, s54, v111
Cv 110,110,111
v_accvgpr_write_b32 a119, v110
v_mul_f32_e32 v112, s54, v112
v_mul_f32_e32 v113, s54, v113
Cv 112,112,113
v_accvgpr_write_b32 a120, v112
v_mul_f32_e32 v114, s54, v114
v_mul_f32_e32 v115, s54, v115
Cv 114,114,115
v_accvgpr_write_b32 a121, v114
v_mul_f32_e32 v116, s54, v116
v_mul_f32_e32 v117, s54, v117
Cv 116,116,117
v_accvgpr_write_b32 a122, v116
v_mul_f32_e32 v118, s54, v118
v_mul_f32_e32 v119, s54, v119
)" R"(
Cv 118,118,119
v_accvgpr_write_b32 a123, v118
v_mul_f32_e32 v120, s54, v120
v_mul_f32_e32 v121, s54, v121
Cv 120,120,121
v_accvgpr_write_b32 a124, v120
v_mul_f32_e32 v122, s54, v122
v_mul_f32_e32 v123, s54, v123
Cv 122,122,123
v_accvgpr_write_b32 a125, v122
v_mul_f32_e32 v124, s54, v124
v_mul_f32_e32 v125, s54, v125
Cv 124,124,125
v_accvgpr_write_b32 a126, v124
v_mul_f32_e32 v126, s54, v126
v_mul_f32_e32 v127, s54, v127
Cv 126,126,127
v_accvgpr_write_b32 a127, v126
v_accvgpr_mov_b32 a64, a224
v_accvgpr_mov_b32 a65, a225
v_accvgpr_mov_b32 a66, a226
v_accvgpr_mov_b32 a67, a227
v_accvgpr_mov_b32 a68, a228
v_accvgpr_mov_b32 a69, a229
v_accvgpr_mov_b32 a70, a230
v_accvgpr_mov_b32 a71, a231
v_accvgpr_mov_b32 a72, a232
v_accvgpr_mov_b32 a73, a233
v_accvgpr_mov_b32 a74, a234
v_accvgpr_mov_b32 a75, a235
v_accvgpr_mov_b32 a76, a236
v_accvgpr_mov_b32 a77, a237
v_accvgpr_mov_b32 a78, a238
v_accvgpr_mov_b32 a79, a239
v_accvgpr_mov_b32 a80, a240
v_accvgpr_mov_b32 a81, a241
v_accvgpr_mov_b32 a82, a242
v_accvgpr_mov_b32 a83, a243
v_accvgpr_mov_b32 a84, a244
v_accvgpr_mov_b32 a85, a245
v_accvgpr_mov_b32 a86, a246
v_accvgpr_mov_b32 a87, a247
v_accvgpr_mov_b32 a88, a248
v_accvgpr_mov_b32 a89, a249
v_accvgpr_mov_b32 a90, a250
v_accvgpr_mov_b32 a91, a251
v_accvgpr_mov_b32 a92, a252
v_accvgpr_mov_b32 a93, a253
v_accvgpr_mov_b32 a94, a254
v_accvgpr_mov_b32 a95, a255
v_accvgpr_mov_b32 a96, a192
v_accvgpr_mov_b32 a97, a193
v_accvgpr_mov_b32 a98, a194
v_accvgpr_mov_b32 a99, a195
v_accvgpr_mov_b32 a100, a196
v_accvgpr_mov_b32 a101, a197
v_accvgpr_mov_b32 a102, a198
v_accvgpr_mov_b32 a103, a199
v_accvgpr_mov_b32 a104, a200
v_accvgpr_mov_b32 a105, a201
v_accvgpr_mov_b32 a106, a202
v_accvgpr_mov_b32 a107, a203
v_accvgpr_mov_b32 a108, a204
v_accvgpr_mov_b32 a109, a205
)" R"(
v_accvgpr_mov_b32 a110, a206
v_accvgpr_mov_b32 a111, a207
s_mov_b32 s94, 2
s_nop 1
s_branch .LzA7R
.Lzsp7:
s_sub_u32 s53, s53, 1
s_cbranch_scc1 .Lzsq7
s_sleep 1
buffer_load_dword v4, v5, s[20:23], s50 offen offset:0 sc0 sc1
s_waitcnt vmcnt(0)
v_cmp_eq_u32_e32 vcc, s3, v4
s_andn2_b64 s[28:29], s[60:61], vcc
s_cbranch_scc1 .Lzsp7
s_branch .Lzok7
.Lzsq7:
s_mov_b32 s53, 0
s_branch .Lzok7
.Lzmk0:
s_call_b64 s[30:31], .LzMASK0
s_branch .Lzmr0
.Lzfx0:
s_mov_b32 s88, 3
s_call_b64 s[30:31], .LzFIX0
s_branch .Lzfr0
.Lzmk1:
s_call_b64 s[30:31], .LzMASK1
s_branch .Lzmr1
.Lzsp8:
s_sub_u32 s53, s53, 1
s_cbranch_scc1 .Lzsq8
s_sleep 1
buffer_load_dword v4, v5, s[20:23], s50 offen offset:0 sc0 sc1
s_waitcnt vmcnt(0)
v_cmp_eq_u32_e32 vcc, s3, v4
s_andn2_b64 s[28:29], s[60:61], vcc
s_cbranch_scc1 .Lzsp8
s_branch .Lzok8
.Lzsq8:
s_mov_b32 s53, 0
s_branch .Lzok8
.Lzfx1:
s_mov_b32 s88, 2
s_call_b64 s[30:31], .LzFIX1
s_branch .Lzfr1
.Lzmk2:
s_call_b64 s[30:31], .LzMASK0
s_branch .Lzmr2
.Lzfx2:
s_mov_b32 s88, 2
s_call_b64 s[30:31], .LzFIX0
s_branch .Lzfr2
.Lzmk3:
s_call_b64 s[30:31], .LzMASK1
s_branch .Lzmr3
.Lzsp9:
s_sub_u32 s53, s53, 1
s_cbranch_scc1 .Lzsq9
s_sleep 1
buffer_load_dword v4, v5, s[20:23], s50 offen offset:0 sc0 sc1
s_waitcnt vmcnt(0)
v_cmp_eq_u32_e32 vcc, s3, v4
s_andn2_b64 s[28:29], s[60:61], vcc
s_cbranch_scc1 .Lzsp9
)" R"(
s_branch .Lzok9
.Lzsq9:
s_mov_b32 s53, 0
s_branch .Lzok9
.Lzfx3:
s_mov_b32 s88, 0
s_call_b64 s[30:31], .LzFIX1
s_branch .Lzfr3
.Lzmk4:
s_call_b64 s[30:31], .LzMASK0
s_branch .Lzmr4
.Lzfx4:
s_mov_b32 s88, 3
s_call_b64 s[30:31], .LzFIX0
s_branch .Lzfr4
.Lzmk5:
s_call_b64 s[30:31], .LzMASK1
s_branch .Lzmr5
.Lzfx5:
s_mov_b32 s88, 0
s_call_b64 s[30:31], .LzFIX1
s_branch .Lzfr5
.Lzsp11:
s_sub_u32 s53, s53, 1
s_cbranch_scc1 .Lzsq11
s_sleep 1
buffer_load_dword v4, v2, s[20:23], s85 offen sc0 sc1
s_waitcnt vmcnt(0)
v_xor_b32 v4, s3, v4
v_cmp_gt_u32_e32 vcc, 2, v4
s_andn2_b32 s28, 0xff, vcc_lo
s_cbranch_scc1 .Lzsp11
s_branch .Lzok11
.Lzsq11:
s_mov_b32 s53, 0
s_branch .Lzok11
.Lzsp12:
s_sub_u32 s53, s53, 1
s_cbranch_scc1 .Lzsq12
s_sleep 1
buffer_load_dword v4, v5, s[20:23], s50 offen offset:0 sc0 sc1
s_waitcnt vmcnt(0)
v_cmp_eq_u32_e32 vcc, s3, v4
s_andn2_b64 s[28:29], s[60:61], vcc
s_cbranch_scc1 .Lzsp12
s_branch .Lzok12
.Lzsq12:
s_mov_b32 s53, 0
s_branch .Lzok12
.Lzsp13:
s_sub_u32 s53, s53, 1
s_cbranch_scc1 .Lzsq13
s_sleep 1
buffer_load_dword v4, v5, s[20:23], s50 offen offset:0 sc0 sc1
s_waitcnt vmcnt(0)
v_cmp_eq_u32_e32 vcc, s3, v4
s_andn2_b64 s[28:29], s[60:61], vcc
s_cbranch_scc1 .Lzsp13
s_branch .Lzok13
.Lzsq13:
s_mov_b32 s53, 0
s_branch .Lzok13
.Lzsp14:
s_sub_u32 s53, s53, 1
)" R"(
s_cbranch_scc1 .Lzsq14
s_sleep 1
buffer_load_dword v4, v5, s[20:23], s50 offen offset:0 sc0 sc1
s_waitcnt vmcnt(0)
v_cmp_eq_u32_e32 vcc, s3, v4
s_andn2_b64 s[28:29], s[60:61], vcc
s_cbranch_scc1 .Lzsp14
s_branch .Lzok14
.Lzsq14:
s_mov_b32 s53, 0
s_branch .Lzok14
.Lzsp15:
s_sub_u32 s53, s53, 1
s_cbranch_scc1 .Lzsq15
s_sleep 1
buffer_load_dword v4, v5, s[20:23], s50 offen offset:0 sc0 sc1
s_waitcnt vmcnt(0)
v_cmp_eq_u32_e32 vcc, s3, v4
s_andn2_b64 s[28:29], s[60:61], vcc
s_cbranch_scc1 .Lzsp15
s_branch .Lzok15
.Lzsq15:
s_mov_b32 s53, 0
s_branch .Lzok15
.Lzfx6:
s_mov_b32 s88, 3
s_call_b64 s[30:31], .LzFIX0
s_branch .Lzfr6
.Lzsp16:
s_sub_u32 s53, s53, 1
s_cbranch_scc1 .Lzsq16
s_sleep 1
buffer_load_dword v4, v5, s[20:23], s50 offen offset:0 sc0 sc1
s_waitcnt vmcnt(0)
v_cmp_eq_u32_e32 vcc, s3, v4
s_andn2_b64 s[28:29], s[60:61], vcc
s_cbranch_scc1 .Lzsp16
s_branch .Lzok16
.Lzsq16:
s_mov_b32 s53, 0
s_branch .Lzok16
.Lzfx7:
s_mov_b32 s88, 2
s_call_b64 s[30:31], .LzFIX1
s_branch .Lzfr7
.Lzmk6:
s_call_b64 s[30:31], .LzMASK0
s_branch .Lzmr6
.Lzfx8:
s_mov_b32 s88, 2
s_call_b64 s[30:31], .LzFIX0
s_branch .Lzfr8
.Lzmk7:
s_call_b64 s[30:31], .LzMASK1
s_branch .Lzmr7
.Lzsp17:
s_sub_u32 s53, s53, 1
s_cbranch_scc1 .Lzsq17
s_sleep 1
buffer_load_dword v4, v5, s[20:23], s50 offen offset:0 sc0 sc1
s_waitcnt vmcnt(0)
v_cmp_eq_u32_e32 vcc, s3, v4
s_andn2_b64 s[28:29], s[60:61], vcc
s_cbranch_scc1 .Lzsp17
)" R"(
s_branch .Lzok17
.Lzsq17:
s_mov_b32 s53, 0
s_branch .Lzok17
.Lzfx9:
s_mov_b32 s88, 2
s_call_b64 s[30:31], .LzFIX1
s_branch .Lzfr9
.Lzmk8:
s_call_b64 s[30:31], .LzMASK0
s_branch .Lzmr8
.Lzfx10:
s_mov_b32 s88, 2
s_call_b64 s[30:31], .LzFIX0
s_branch .Lzfr10
.Lzmk9:
s_call_b64 s[30:31], .LzMASK1
s_branch .Lzmr9
.Lzsp18:
s_sub_u32 s53, s53, 1
s_cbranch_scc1 .Lzsq18
s_sleep 1
buffer_load_dword v4, v7, s[20:23], s65 offen offset:0 sc0 sc1
s_waitcnt vmcnt(0)
v_cmp_eq_u32_e32 vcc, s3, v4
s_andn2_b64 s[28:29], s[60:61], vcc
s_cbranch_scc1 .Lzsp18
s_branch .Lzok18
.Lzsq18:
s_mov_b32 s53, 0
s_branch .Lzok18
.Lzfx11:
s_mov_b32 s88, 2
s_call_b64 s[30:31], .LzFIX1
s_branch .Lzfr11
.Lzmk10:
s_call_b64 s[30:31], .LzMASK0
s_branch .Lzmr10
.Lzfx12:
s_mov_b32 s88, 2
s_call_b64 s[30:31], .LzFIX0
s_branch .Lzfr12
.Lzmk11:
s_call_b64 s[30:31], .LzMASK1
s_branch .Lzmr11
.Lzfx13:
s_mov_b32 s88, 0
s_call_b64 s[30:31], .LzFIX1
s_branch .Lzfr13
.LzMRG:
s_waitcnt vmcnt(0)
s_nop 7
v_mov_b32 v25, v64
v_mov_b32 v24, v25
s_nop 1
v_permlane32_swap_b32_e32 v25, v24
v_add_f32_e32 v25, v25, v24
v_mov_b32 v24, v25
s_nop 1
v_permlane16_swap_b32_e32 v25, v24
v_add_f32_e32 v25, v25, v24
v_cmp_lt_f32_e32 vcc, 0, v25
s_nop 1
v_cndmask_b32_e32 v66, v16, v66, vcc
)" R"(
v_sub_f32_e32 v28, v16, v66
v_min_f32_e32 v26, 0, v28
v_exp_f32_e32 v26, v26
v_max_f32_e32 v28, 0, v28
v_sub_f32_e32 v24, 0, v28
v_exp_f32_e32 v24, v24
v_mul_f32_e32 v64, v26, v64
v_mul_f32_e32 v12, v24, v12
v_mul_f32_e32 v128, v24, v128
v_mul_f32_e32 v129, v24, v129
v_mul_f32_e32 v130, v24, v130
v_mul_f32_e32 v131, v24, v131
v_mul_f32_e32 v132, v24, v132
v_mul_f32_e32 v133, v24, v133
v_mul_f32_e32 v134, v24, v134
v_mul_f32_e32 v135, v24, v135
v_mul_f32_e32 v144, v24, v144
v_mul_f32_e32 v145, v24, v145
v_mul_f32_e32 v146, v24, v146
v_mul_f32_e32 v147, v24, v147
v_mul_f32_e32 v148, v24, v148
v_mul_f32_e32 v149, v24, v149
v_mul_f32_e32 v150, v24, v150
v_mul_f32_e32 v151, v24, v151
v_mul_f32_e32 v160, v24, v160
v_mul_f32_e32 v161, v24, v161
v_mul_f32_e32 v162, v24, v162
v_mul_f32_e32 v163, v24, v163
v_mul_f32_e32 v164, v24, v164
v_mul_f32_e32 v165, v24, v165
v_mul_f32_e32 v166, v24, v166
v_mul_f32_e32 v167, v24, v167
v_mul_f32_e32 v176, v24, v176
v_mul_f32_e32 v177, v24, v177
v_mul_f32_e32 v178, v24, v178
v_mul_f32_e32 v179, v24, v179
v_mul_f32_e32 v180, v24, v180
v_mul_f32_e32 v181, v24, v181
v_mul_f32_e32 v182, v24, v182
v_mul_f32_e32 v183, v24, v183
v_mul_f32_e32 v192, v24, v192
v_mul_f32_e32 v193, v24, v193
v_mul_f32_e32 v194, v24, v194
v_mul_f32_e32 v195, v24, v195
v_mul_f32_e32 v196, v24, v196
v_mul_f32_e32 v197, v24, v197
v_mul_f32_e32 v198, v24, v198
v_mul_f32_e32 v199, v24, v199
v_mul_f32_e32 v208, v24, v208
v_mul_f32_e32 v209, v24, v209
v_mul_f32_e32 v210, v24, v210
v_mul_f32_e32 v211, v24, v211
v_mul_f32_e32 v212, v24, v212
v_mul_f32_e32 v213, v24, v213
v_mul_f32_e32 v214, v24, v214
v_mul_f32_e32 v215, v24, v215
v_mul_f32_e32 v224, v24, v224
v_mul_f32_e32 v225, v24, v225
v_mul_f32_e32 v226, v24, v226
v_mul_f32_e32 v227, v24, v227
v_mul_f32_e32 v228, v24, v228
v_mul_f32_e32 v229, v24, v229
v_mul_f32_e32 v230, v24, v230
v_mul_f32_e32 v231, v24, v231
)" R"(
v_mul_f32_e32 v240, v24, v240
v_mul_f32_e32 v241, v24, v241
v_mul_f32_e32 v242, v24, v242
v_mul_f32_e32 v243, v24, v243
v_mul_f32_e32 v244, v24, v244
v_mul_f32_e32 v245, v24, v245
v_mul_f32_e32 v246, v24, v246
v_mul_f32_e32 v247, v24, v247
v_sub_f32_e32 v16, v16, v28
v_sub_f32_e32 v17, v17, v28
v_sub_f32_e32 v18, v18, v28
v_sub_f32_e32 v19, v19, v28
v_sub_f32_e32 v96, v96, v28
v_sub_f32_e32 v97, v97, v28
v_sub_f32_e32 v98, v98, v28
v_sub_f32_e32 v99, v99, v28
v_sub_f32_e32 v104, v104, v28
v_sub_f32_e32 v105, v105, v28
v_sub_f32_e32 v106, v106, v28
v_sub_f32_e32 v107, v107, v28
v_sub_f32_e32 v112, v112, v28
v_sub_f32_e32 v113, v113, v28
v_sub_f32_e32 v114, v114, v28
v_sub_f32_e32 v115, v115, v28
v_sub_f32_e32 v120, v120, v28
v_sub_f32_e32 v121, v121, v28
v_sub_f32_e32 v122, v122, v28
v_sub_f32_e32 v123, v123, v28
v_accvgpr_read_b32 v25, a128
v_lshlrev_b32 v24, 16, v25
v_and_b32 v25, 0xffff0000, v25
v_mul_f32_e32 v24, v26, v24
v_mul_f32_e32 v25, v26, v25
Cv 24,24,25
v_accvgpr_write_b32 a128, v24
v_accvgpr_read_b32 v25, a129
v_lshlrev_b32 v24, 16, v25
v_and_b32 v25, 0xffff0000, v25
v_mul_f32_e32 v24, v26, v24
v_mul_f32_e32 v25, v26, v25
Cv 24,24,25
v_accvgpr_write_b32 a129, v24
v_accvgpr_read_b32 v25, a130
v_lshlrev_b32 v24, 16, v25
v_and_b32 v25, 0xffff0000, v25
v_mul_f32_e32 v24, v26, v24
v_mul_f32_e32 v25, v26, v25
Cv 24,24,25
v_accvgpr_write_b32 a130, v24
v_accvgpr_read_b32 v25, a131
v_lshlrev_b32 v24, 16, v25
v_and_b32 v25, 0xffff0000, v25
v_mul_f32_e32 v24, v26, v24
v_mul_f32_e32 v25, v26, v25
Cv 24,24,25
v_accvgpr_write_b32 a131, v24
v_accvgpr_read_b32 v25, a136
v_lshlrev_b32 v24, 16, v25
v_and_b32 v25, 0xffff0000, v25
v_mul_f32_e32 v24, v26, v24
v_mul_f32_e32 v25, v26, v25
Cv 24,24,25
v_accvgpr_write_b32 a136, v24
v_accvgpr_read_b32 v25, a137
)" R"(
v_lshlrev_b32 v24, 16, v25
v_and_b32 v25, 0xffff0000, v25
v_mul_f32_e32 v24, v26, v24
v_mul_f32_e32 v25, v26, v25
Cv 24,24,25
v_accvgpr_write_b32 a137, v24
v_accvgpr_read_b32 v25, a138
v_lshlrev_b32 v24, 16, v25
v_and_b32 v25, 0xffff0000, v25
v_mul_f32_e32 v24, v26, v24
v_mul_f32_e32 v25, v26, v25
Cv 24,24,25
v_accvgpr_write_b32 a138, v24
v_accvgpr_read_b32 v25, a139
v_lshlrev_b32 v24, 16, v25
v_and_b32 v25, 0xffff0000, v25
v_mul_f32_e32 v24, v26, v24
v_mul_f32_e32 v25, v26, v25
Cv 24,24,25
v_accvgpr_write_b32 a139, v24
v_accvgpr_read_b32 v25, a144
v_lshlrev_b32 v24, 16, v25
v_and_b32 v25, 0xffff0000, v25
v_mul_f32_e32 v24, v26, v24
v_mul_f32_e32 v25, v26, v25
Cv 24,24,25
v_accvgpr_write_b32 a144, v24
v_accvgpr_read_b32 v25, a145
v_lshlrev_b32 v24, 16, v25
v_and_b32 v25, 0xffff0000, v25
v_mul_f32_e32 v24, v26, v24
v_mul_f32_e32 v25, v26, v25
Cv 24,24,25
v_accvgpr_write_b32 a145, v24
v_accvgpr_read_b32 v25, a146
v_lshlrev_b32 v24, 16, v25
v_and_b32 v25, 0xffff0000, v25
v_mul_f32_e32 v24, v26, v24
v_mul_f32_e32 v25, v26, v25
Cv 24,24,25
v_accvgpr_write_b32 a146, v24
v_accvgpr_read_b32 v25, a147
v_lshlrev_b32 v24, 16, v25
v_and_b32 v25, 0xffff0000, v25
v_mul_f32_e32 v24, v26, v24
v_mul_f32_e32 v25, v26, v25
Cv 24,24,25
v_accvgpr_write_b32 a147, v24
v_accvgpr_read_b32 v25, a152
v_lshlrev_b32 v24, 16, v25
v_and_b32 v25, 0xffff0000, v25
v_mul_f32_e32 v24, v26, v24
v_mul_f32_e32 v25, v26, v25
Cv 24,24,25
v_accvgpr_write_b32 a152, v24
v_accvgpr_read_b32 v25, a153
v_lshlrev_b32 v24, 16, v25
v_and_b32 v25, 0xffff0000, v25
v_mul_f32_e32 v24, v26, v24
v_mul_f32_e32 v25, v26, v25
Cv 24,24,25
v_accvgpr_write_b32 a153, v24
v_accvgpr_read_b32 v25, a154
v_lshlrev_b32 v24, 16, v25
)" R"(
v_and_b32 v25, 0xffff0000, v25
v_mul_f32_e32 v24, v26, v24
v_mul_f32_e32 v25, v26, v25
Cv 24,24,25
v_accvgpr_write_b32 a154, v24
v_accvgpr_read_b32 v25, a155
v_lshlrev_b32 v24, 16, v25
v_and_b32 v25, 0xffff0000, v25
v_mul_f32_e32 v24, v26, v24
v_mul_f32_e32 v25, v26, v25
Cv 24,24,25
v_accvgpr_write_b32 a155, v24
v_accvgpr_read_b32 v25, a160
v_lshlrev_b32 v24, 16, v25
v_and_b32 v25, 0xffff0000, v25
v_mul_f32_e32 v24, v26, v24
v_mul_f32_e32 v25, v26, v25
Cv 24,24,25
v_accvgpr_write_b32 a160, v24
v_accvgpr_read_b32 v25, a161
v_lshlrev_b32 v24, 16, v25
v_and_b32 v25, 0xffff0000, v25
v_mul_f32_e32 v24, v26, v24
v_mul_f32_e32 v25, v26, v25
Cv 24,24,25
v_accvgpr_write_b32 a161, v24
v_accvgpr_read_b32 v25, a162
v_lshlrev_b32 v24, 16, v25
v_and_b32 v25, 0xffff0000, v25
v_mul_f32_e32 v24, v26, v24
v_mul_f32_e32 v25, v26, v25
Cv 24,24,25
v_accvgpr_write_b32 a162, v24
v_accvgpr_read_b32 v25, a163
v_lshlrev_b32 v24, 16, v25
v_and_b32 v25, 0xffff0000, v25
v_mul_f32_e32 v24, v26, v24
v_mul_f32_e32 v25, v26, v25
Cv 24,24,25
v_accvgpr_write_b32 a163, v24
v_accvgpr_read_b32 v25, a168
v_lshlrev_b32 v24, 16, v25
v_and_b32 v25, 0xffff0000, v25
v_mul_f32_e32 v24, v26, v24
v_mul_f32_e32 v25, v26, v25
Cv 24,24,25
v_accvgpr_write_b32 a168, v24
v_accvgpr_read_b32 v25, a169
v_lshlrev_b32 v24, 16, v25
v_and_b32 v25, 0xffff0000, v25
v_mul_f32_e32 v24, v26, v24
v_mul_f32_e32 v25, v26, v25
Cv 24,24,25
v_accvgpr_write_b32 a169, v24
v_accvgpr_read_b32 v25, a170
v_lshlrev_b32 v24, 16, v25
v_and_b32 v25, 0xffff0000, v25
v_mul_f32_e32 v24, v26, v24
v_mul_f32_e32 v25, v26, v25
Cv 24,24,25
v_accvgpr_write_b32 a170, v24
v_accvgpr_read_b32 v25, a171
v_lshlrev_b32 v24, 16, v25
v_and_b32 v25, 0xffff0000, v25
)" R"(
v_mul_f32_e32 v24, v26, v24
v_mul_f32_e32 v25, v26, v25
Cv 24,24,25
v_accvgpr_write_b32 a171, v24
v_accvgpr_read_b32 v25, a176
v_lshlrev_b32 v24, 16, v25
v_and_b32 v25, 0xffff0000, v25
v_mul_f32_e32 v24, v26, v24
v_mul_f32_e32 v25, v26, v25
Cv 24,24,25
v_accvgpr_write_b32 a176, v24
v_accvgpr_read_b32 v25, a177
v_lshlrev_b32 v24, 16, v25
v_and_b32 v25, 0xffff0000, v25
v_mul_f32_e32 v24, v26, v24
v_mul_f32_e32 v25, v26, v25
Cv 24,24,25
v_accvgpr_write_b32 a177, v24
v_accvgpr_read_b32 v25, a178
v_lshlrev_b32 v24, 16, v25
v_and_b32 v25, 0xffff0000, v25
v_mul_f32_e32 v24, v26, v24
v_mul_f32_e32 v25, v26, v25
Cv 24,24,25
v_accvgpr_write_b32 a178, v24
v_accvgpr_read_b32 v25, a179
v_lshlrev_b32 v24, 16, v25
v_and_b32 v25, 0xffff0000, v25
v_mul_f32_e32 v24, v26, v24
v_mul_f32_e32 v25, v26, v25
Cv 24,24,25
v_accvgpr_write_b32 a179, v24
v_accvgpr_read_b32 v25, a184
v_lshlrev_b32 v24, 16, v25
v_and_b32 v25, 0xffff0000, v25
v_mul_f32_e32 v24, v26, v24
v_mul_f32_e32 v25, v26, v25
Cv 24,24,25
v_accvgpr_write_b32 a184, v24
v_accvgpr_read_b32 v25, a185
v_lshlrev_b32 v24, 16, v25
v_and_b32 v25, 0xffff0000, v25
v_mul_f32_e32 v24, v26, v24
v_mul_f32_e32 v25, v26, v25
Cv 24,24,25
v_accvgpr_write_b32 a185, v24
v_accvgpr_read_b32 v25, a186
v_lshlrev_b32 v24, 16, v25
v_and_b32 v25, 0xffff0000, v25
v_mul_f32_e32 v24, v26, v24
v_mul_f32_e32 v25, v26, v25
Cv 24,24,25
v_accvgpr_write_b32 a186, v24
v_accvgpr_read_b32 v25, a187
v_lshlrev_b32 v24, 16, v25
v_and_b32 v25, 0xffff0000, v25
v_mul_f32_e32 v24, v26, v24
v_mul_f32_e32 v25, v26, v25
Cv 24,24,25
v_accvgpr_write_b32 a187, v24
v_mov_b32 v25, v65
v_mov_b32 v24, v25
s_nop 1
v_permlane32_swap_b32_e32 v25, v24
)" R"(
v_add_f32_e32 v25, v25, v24
v_mov_b32 v24, v25
s_nop 1
v_permlane16_swap_b32_e32 v25, v24
v_add_f32_e32 v25, v25, v24
v_cmp_lt_f32_e32 vcc, 0, v25
s_nop 1
v_cndmask_b32_e32 v67, v20, v67, vcc
v_sub_f32_e32 v29, v20, v67
v_min_f32_e32 v27, 0, v29
v_exp_f32_e32 v27, v27
v_max_f32_e32 v29, 0, v29
v_sub_f32_e32 v24, 0, v29
v_exp_f32_e32 v24, v24
v_mul_f32_e32 v65, v27, v65
v_mul_f32_e32 v13, v24, v13
v_mul_f32_e32 v136, v24, v136
v_mul_f32_e32 v137, v24, v137
v_mul_f32_e32 v138, v24, v138
v_mul_f32_e32 v139, v24, v139
v_mul_f32_e32 v140, v24, v140
v_mul_f32_e32 v141, v24, v141
v_mul_f32_e32 v142, v24, v142
v_mul_f32_e32 v143, v24, v143
v_mul_f32_e32 v152, v24, v152
v_mul_f32_e32 v153, v24, v153
v_mul_f32_e32 v154, v24, v154
v_mul_f32_e32 v155, v24, v155
v_mul_f32_e32 v156, v24, v156
v_mul_f32_e32 v157, v24, v157
v_mul_f32_e32 v158, v24, v158
v_mul_f32_e32 v159, v24, v159
v_mul_f32_e32 v168, v24, v168
v_mul_f32_e32 v169, v24, v169
v_mul_f32_e32 v170, v24, v170
v_mul_f32_e32 v171, v24, v171
v_mul_f32_e32 v172, v24, v172
v_mul_f32_e32 v173, v24, v173
v_mul_f32_e32 v174, v24, v174
v_mul_f32_e32 v175, v24, v175
v_mul_f32_e32 v184, v24, v184
v_mul_f32_e32 v185, v24, v185
v_mul_f32_e32 v186, v24, v186
v_mul_f32_e32 v187, v24, v187
v_mul_f32_e32 v188, v24, v188
v_mul_f32_e32 v189, v24, v189
v_mul_f32_e32 v190, v24, v190
v_mul_f32_e32 v191, v24, v191
v_mul_f32_e32 v200, v24, v200
v_mul_f32_e32 v201, v24, v201
v_mul_f32_e32 v202, v24, v202
v_mul_f32_e32 v203, v24, v203
v_mul_f32_e32 v204, v24, v204
v_mul_f32_e32 v205, v24, v205
v_mul_f32_e32 v206, v24, v206
v_mul_f32_e32 v207, v24, v207
v_mul_f32_e32 v216, v24, v216
v_mul_f32_e32 v217, v24, v217
v_mul_f32_e32 v218, v24, v218
v_mul_f32_e32 v219, v24, v219
v_mul_f32_e32 v220, v24, v220
v_mul_f32_e32 v221, v24, v221
v_mul_f32_e32 v222, v24, v222
v_mul_f32_e32 v223, v24, v223
)" R"(
v_mul_f32_e32 v232, v24, v232
v_mul_f32_e32 v233, v24, v233
v_mul_f32_e32 v234, v24, v234
v_mul_f32_e32 v235, v24, v235
v_mul_f32_e32 v236, v24, v236
v_mul_f32_e32 v237, v24, v237
v_mul_f32_e32 v238, v24, v238
v_mul_f32_e32 v239, v24, v239
v_mul_f32_e32 v248, v24, v248
v_mul_f32_e32 v249, v24, v249
v_mul_f32_e32 v250, v24, v250
v_mul_f32_e32 v251, v24, v251
v_mul_f32_e32 v252, v24, v252
v_mul_f32_e32 v253, v24, v253
v_mul_f32_e32 v254, v24, v254
v_mul_f32_e32 v255, v24, v255
v_sub_f32_e32 v20, v20, v29
v_sub_f32_e32 v21, v21, v29
v_sub_f32_e32 v22, v22, v29
v_sub_f32_e32 v23, v23, v29
v_sub_f32_e32 v100, v100, v29
v_sub_f32_e32 v101, v101, v29
v_sub_f32_e32 v102, v102, v29
v_sub_f32_e32 v103, v103, v29
v_sub_f32_e32 v108, v108, v29
v_sub_f32_e32 v109, v109, v29
v_sub_f32_e32 v110, v110, v29
v_sub_f32_e32 v111, v111, v29
v_sub_f32_e32 v116, v116, v29
v_sub_f32_e32 v117, v117, v29
v_sub_f32_e32 v118, v118, v29
v_sub_f32_e32 v119, v119, v29
v_sub_f32_e32 v124, v124, v29
v_sub_f32_e32 v125, v125, v29
v_sub_f32_e32 v126, v126, v29
v_sub_f32_e32 v127, v127, v29
v_accvgpr_read_b32 v25, a132
v_lshlrev_b32 v24, 16, v25
v_and_b32 v25, 0xffff0000, v25
v_mul_f32_e32 v24, v27, v24
v_mul_f32_e32 v25, v27, v25
Cv 24,24,25
v_accvgpr_write_b32 a132, v24
v_accvgpr_read_b32 v25, a133
v_lshlrev_b32 v24, 16, v25
v_and_b32 v25, 0xffff0000, v25
v_mul_f32_e32 v24, v27, v24
v_mul_f32_e32 v25, v27, v25
Cv 24,24,25
v_accvgpr_write_b32 a133, v24
v_accvgpr_read_b32 v25, a134
v_lshlrev_b32 v24, 16, v25
v_and_b32 v25, 0xffff0000, v25
v_mul_f32_e32 v24, v27, v24
v_mul_f32_e32 v25, v27, v25
Cv 24,24,25
v_accvgpr_write_b32 a134, v24
v_accvgpr_read_b32 v25, a135
v_lshlrev_b32 v24, 16, v25
v_and_b32 v25, 0xffff0000, v25
v_mul_f32_e32 v24, v27, v24
v_mul_f32_e32 v25, v27, v25
Cv 24,24,25
v_accvgpr_write_b32 a135, v24
)" R"(
v_accvgpr_read_b32 v25, a140
v_lshlrev_b32 v24, 16, v25
v_and_b32 v25, 0xffff0000, v25
v_mul_f32_e32 v24, v27, v24
v_mul_f32_e32 v25, v27, v25
Cv 24,24,25
v_accvgpr_write_b32 a140, v24
v_accvgpr_read_b32 v25, a141
v_lshlrev_b32 v24, 16, v25
v_and_b32 v25, 0xffff0000, v25
v_mul_f32_e32 v24, v27, v24
v_mul_f32_e32 v25, v27, v25
Cv 24,24,25
v_accvgpr_write_b32 a141, v24
v_accvgpr_read_b32 v25, a142
v_lshlrev_b32 v24, 16, v25
v_and_b32 v25, 0xffff0000, v25
v_mul_f32_e32 v24, v27, v24
v_mul_f32_e32 v25, v27, v25
Cv 24,24,25
v_accvgpr_write_b32 a142, v24
v_accvgpr_read_b32 v25, a143
v_lshlrev_b32 v24, 16, v25
v_and_b32 v25, 0xffff0000, v25
v_mul_f32_e32 v24, v27, v24
v_mul_f32_e32 v25, v27, v25
Cv 24,24,25
v_accvgpr_write_b32 a143, v24
v_accvgpr_read_b32 v25, a148
v_lshlrev_b32 v24, 16, v25
v_and_b32 v25, 0xffff0000, v25
v_mul_f32_e32 v24, v27, v24
v_mul_f32_e32 v25, v27, v25
Cv 24,24,25
v_accvgpr_write_b32 a148, v24
v_accvgpr_read_b32 v25, a149
v_lshlrev_b32 v24, 16, v25
v_and_b32 v25, 0xffff0000, v25
v_mul_f32_e32 v24, v27, v24
v_mul_f32_e32 v25, v27, v25
Cv 24,24,25
v_accvgpr_write_b32 a149, v24
v_accvgpr_read_b32 v25, a150
v_lshlrev_b32 v24, 16, v25
v_and_b32 v25, 0xffff0000, v25
v_mul_f32_e32 v24, v27, v24
v_mul_f32_e32 v25, v27, v25
Cv 24,24,25
v_accvgpr_write_b32 a150, v24
v_accvgpr_read_b32 v25, a151
v_lshlrev_b32 v24, 16, v25
v_and_b32 v25, 0xffff0000, v25
v_mul_f32_e32 v24, v27, v24
v_mul_f32_e32 v25, v27, v25
Cv 24,24,25
v_accvgpr_write_b32 a151, v24
v_accvgpr_read_b32 v25, a156
v_lshlrev_b32 v24, 16, v25
v_and_b32 v25, 0xffff0000, v25
v_mul_f32_e32 v24, v27, v24
v_mul_f32_e32 v25, v27, v25
Cv 24,24,25
v_accvgpr_write_b32 a156, v24
v_accvgpr_read_b32 v25, a157
)" R"(
v_lshlrev_b32 v24, 16, v25
v_and_b32 v25, 0xffff0000, v25
v_mul_f32_e32 v24, v27, v24
v_mul_f32_e32 v25, v27, v25
Cv 24,24,25
v_accvgpr_write_b32 a157, v24
v_accvgpr_read_b32 v25, a158
v_lshlrev_b32 v24, 16, v25
v_and_b32 v25, 0xffff0000, v25
v_mul_f32_e32 v24, v27, v24
v_mul_f32_e32 v25, v27, v25
Cv 24,24,25
v_accvgpr_write_b32 a158, v24
v_accvgpr_read_b32 v25, a159
v_lshlrev_b32 v24, 16, v25
v_and_b32 v25, 0xffff0000, v25
v_mul_f32_e32 v24, v27, v24
v_mul_f32_e32 v25, v27, v25
Cv 24,24,25
v_accvgpr_write_b32 a159, v24
v_accvgpr_read_b32 v25, a164
v_lshlrev_b32 v24, 16, v25
v_and_b32 v25, 0xffff0000, v25
v_mul_f32_e32 v24, v27, v24
v_mul_f32_e32 v25, v27, v25
Cv 24,24,25
v_accvgpr_write_b32 a164, v24
v_accvgpr_read_b32 v25, a165
v_lshlrev_b32 v24, 16, v25
v_and_b32 v25, 0xffff0000, v25
v_mul_f32_e32 v24, v27, v24
v_mul_f32_e32 v25, v27, v25
Cv 24,24,25
v_accvgpr_write_b32 a165, v24
v_accvgpr_read_b32 v25, a166
v_lshlrev_b32 v24, 16, v25
v_and_b32 v25, 0xffff0000, v25
v_mul_f32_e32 v24, v27, v24
v_mul_f32_e32 v25, v27, v25
Cv 24,24,25
v_accvgpr_write_b32 a166, v24
v_accvgpr_read_b32 v25, a167
v_lshlrev_b32 v24, 16, v25
v_and_b32 v25, 0xffff0000, v25
v_mul_f32_e32 v24, v27, v24
v_mul_f32_e32 v25, v27, v25
Cv 24,24,25
v_accvgpr_write_b32 a167, v24
v_accvgpr_read_b32 v25, a172
v_lshlrev_b32 v24, 16, v25
v_and_b32 v25, 0xffff0000, v25
v_mul_f32_e32 v24, v27, v24
v_mul_f32_e32 v25, v27, v25
Cv 24,24,25
v_accvgpr_write_b32 a172, v24
v_accvgpr_read_b32 v25, a173
v_lshlrev_b32 v24, 16, v25
v_and_b32 v25, 0xffff0000, v25
v_mul_f32_e32 v24, v27, v24
v_mul_f32_e32 v25, v27, v25
Cv 24,24,25
v_accvgpr_write_b32 a173, v24
v_accvgpr_read_b32 v25, a174
v_lshlrev_b32 v24, 16, v25
)" R"(
v_and_b32 v25, 0xffff0000, v25
v_mul_f32_e32 v24, v27, v24
v_mul_f32_e32 v25, v27, v25
Cv 24,24,25
v_accvgpr_write_b32 a174, v24
v_accvgpr_read_b32 v25, a175
v_lshlrev_b32 v24, 16, v25
v_and_b32 v25, 0xffff0000, v25
v_mul_f32_e32 v24, v27, v24
v_mul_f32_e32 v25, v27, v25
Cv 24,24,25
v_accvgpr_write_b32 a175, v24
v_accvgpr_read_b32 v25, a180
v_lshlrev_b32 v24, 16, v25
v_and_b32 v25, 0xffff0000, v25
v_mul_f32_e32 v24, v27, v24
v_mul_f32_e32 v25, v27, v25
Cv 24,24,25
v_accvgpr_write_b32 a180, v24
v_accvgpr_read_b32 v25, a181
v_lshlrev_b32 v24, 16, v25
v_and_b32 v25, 0xffff0000, v25
v_mul_f32_e32 v24, v27, v24
v_mul_f32_e32 v25, v27, v25
Cv 24,24,25
v_accvgpr_write_b32 a181, v24
v_accvgpr_read_b32 v25, a182
v_lshlrev_b32 v24, 16, v25
v_and_b32 v25, 0xffff0000, v25
v_mul_f32_e32 v24, v27, v24
v_mul_f32_e32 v25, v27, v25
Cv 24,24,25
v_accvgpr_write_b32 a182, v24
v_accvgpr_read_b32 v25, a183
v_lshlrev_b32 v24, 16, v25
v_and_b32 v25, 0xffff0000, v25
v_mul_f32_e32 v24, v27, v24
v_mul_f32_e32 v25, v27, v25
Cv 24,24,25
v_accvgpr_write_b32 a183, v24
v_accvgpr_read_b32 v25, a188
v_lshlrev_b32 v24, 16, v25
v_and_b32 v25, 0xffff0000, v25
v_mul_f32_e32 v24, v27, v24
v_mul_f32_e32 v25, v27, v25
Cv 24,24,25
v_accvgpr_write_b32 a188, v24
v_accvgpr_read_b32 v25, a189
v_lshlrev_b32 v24, 16, v25
v_and_b32 v25, 0xffff0000, v25
v_mul_f32_e32 v24, v27, v24
v_mul_f32_e32 v25, v27, v25
Cv 24,24,25
v_accvgpr_write_b32 a189, v24
v_accvgpr_read_b32 v25, a190
v_lshlrev_b32 v24, 16, v25
v_and_b32 v25, 0xffff0000, v25
v_mul_f32_e32 v24, v27, v24
v_mul_f32_e32 v25, v27, v25
Cv 24,24,25
v_accvgpr_write_b32 a190, v24
v_accvgpr_read_b32 v25, a191
v_lshlrev_b32 v24, 16, v25
v_and_b32 v25, 0xffff0000, v25
)" R"(
v_mul_f32_e32 v24, v27, v24
v_mul_f32_e32 v25, v27, v25
Cv 24,24,25
v_accvgpr_write_b32 a191, v24
v_exp_f32_e32 v48, v96
v_exp_f32_e32 v24, v97
s_nop 0
v_add_f32_e32 v14, v48, v24
Cv 48,48,24
v_exp_f32_e32 v49, v98
v_exp_f32_e32 v25, v99
v_add_f32_e32 v14, v14, v49
v_add_f32_e32 v14, v14, v25
Cv 49,49,25
v_exp_f32_e32 v50, v104
v_exp_f32_e32 v26, v105
v_add_f32_e32 v14, v14, v50
v_add_f32_e32 v14, v14, v26
Cv 50,50,26
v_exp_f32_e32 v51, v106
v_exp_f32_e32 v27, v107
v_add_f32_e32 v14, v14, v51
v_add_f32_e32 v14, v14, v27
Cv 51,51,27
v_exp_f32_e32 v52, v100
v_exp_f32_e32 v28, v101
s_nop 0
v_add_f32_e32 v15, v52, v28
Cv 52,52,28
v_exp_f32_e32 v53, v102
v_exp_f32_e32 v29, v103
v_add_f32_e32 v15, v15, v53
v_add_f32_e32 v15, v15, v29
Cv 53,53,29
v_exp_f32_e32 v54, v108
v_exp_f32_e32 v24, v109
v_add_f32_e32 v15, v15, v54
v_add_f32_e32 v15, v15, v24
Cv 54,54,24
v_exp_f32_e32 v55, v110
v_exp_f32_e32 v25, v111
v_add_f32_e32 v15, v15, v55
v_add_f32_e32 v15, v15, v25
Cv 55,55,25
s_nop 1
s_branch .LzMRR
.Lzsp19:
s_sub_u32 s53, s53, 1
s_cbranch_scc1 .Lzsq19
s_sleep 1
buffer_load_dword v4, v7, s[20:23], s56 offen offset:4 sc0 sc1
s_waitcnt vmcnt(0)
v_cmp_eq_u32_e32 vcc, s3, v4
s_andn2_b64 s[28:29], 1, vcc
s_cbranch_scc1 .Lzsp19
s_branch .Lzok19
.Lzsq19:
s_mov_b32 s53, 0
s_branch .Lzok19
.LzPBKX:
Mv 32,0,32
Rt 128,18,16384
Mv 64,0,36
s_mov_b32 s24, s80
)" R"(
s_mov_b32 s25, s81
Mv 96,0,40
Rt 130,18,18432
s_mov_b32 s26, 0x380000
Mv 128,0,44
s_add_u32 s39, s39, 0x40000
s_mov_b32 s27, 0x20000
Mv 160,0,48
Rt 132,19,16384
s_add_u32 m0, s37, 49152
Mv 192,0,52
buffer_load_dwordx4 v12, s[8:11], s39 offen offset:0 lds
Mv 224,0,56
Rt 134,19,18432
s_bfe_u32 s28, s2, 0x30005
Ma 192,0,60
s_lshl3_add_u32 s29, s34, s28
s_lshl2_add_u32 s29, s29, s33
Ma 196,4,60
Rt 136,20,16384
s_mul_i32 s83, s29, 7168
Mv 228,4,56
s_mul_i32 s29, s91, 448
s_add_u32 s83, s83, s29
Mv 196,4,52
Rt 138,20,18432
s_lshl3_add_u32 s29, s34, s91
Mv 164,4,48
s_lshl_b32 s82, s29, 18
s_add_u32 s82, s82, 0x6000000
Mv 132,4,44
Rt 140,21,16384
s_mul_i32 s29, s28, 14
Mv 100,4,40
buffer_load_dwordx4 v13, s[8:11], s39 offen offset:1024 lds
Mv 68,4,36
Rt 142,21,18432
s_add_u32 s29, s29, s33
Mv 36,4,32
s_lshl3_add_u32 s28, s33, s28
Mv 40,8,32
Rt 144,22,16384
Mv 72,8,36
s_cmp_eq_u32 s91, 0
s_cselect_b32 s94, s29, s28
Mv 104,8,40
Rt 146,22,18432
Mv 136,8,44
s_cmp_eq_u32 s91, 0
s_cselect_b32 s92, 4, 32
Mv 168,8,48
Rt 148,23,16384
v_lshrrev_b32 v4, 5, v0
Mv 200,8,52
buffer_load_dwordx4 v14, s[8:11], s39 offen offset:2048 lds
Mv 232,8,56
Rt 150,23,18432
v_mul_u32_u24 v4, 384, v4
Ma 200,8,60
v_add_u32 v28, v4, v1
v_and_b32 v4, 31, v0
Ma 204,12,60
Rt 152,24,16384
v_cmp_lt_u32_e32 vcc, 27, v4
)" R"(
Mv 236,12,56
v_mov_b32 v4, 0x40000000
v_lshrrev_b32 v29, 2, v0
Mv 204,12,52
Rt 154,24,18432
v_cndmask_b32_e32 v28, v28, v4, vcc
Mv 172,12,48
v_and_b32 v4, 48, v1
v_lshl_or_b32 v29, v29, 9, v4
Mv 140,12,44
Rt 156,25,16384
s_lshr_b32 s28, s33, 1
Mv 108,12,40
buffer_load_dwordx4 v15, s[8:11], s39 offen offset:3072 lds
Mv 76,12,36
Rt 158,25,18432
s_lshl_b32 s28, s28, 6
Mv 44,12,32
s_and_b32 s29, s33, 1
s_lshl3_add_u32 s28, s29, s28
Mv 48,16,32
Rb 160,17,0
s_add_u32 s28, s28, 0x20000
Mv 80,16,36
v_add_u32 v29, s28, v29
Mv 112,16,40
Rb 164,17,2048
Mv 144,16,44
s_add_u32 s39, s39, 0x40000
Mv 176,16,48
Rb 168,17,8192
s_add_u32 m0, s37, 0
Mv 208,16,52
buffer_load_dwordx4 v12, s[8:11], s39 offen offset:0 lds
Mv 240,16,56
Rb 172,17,10240
Ma 208,16,60
Ma 212,20,60
Rb 176,17,16384
Mv 244,20,56
Mv 212,20,52
Rb 180,17,18432
Mv 180,20,48
Mv 148,20,44
Rb 184,17,24576
Mv 116,20,40
buffer_load_dwordx4 v13, s[8:11], s39 offen offset:1024 lds
Mv 84,20,36
Rb 188,17,26624
Mv 52,20,32
Mv 56,24,32
Mv 88,24,36
Mv 120,24,40
Mv 152,24,44
Mv 184,24,48
Mv 216,24,52
buffer_load_dwordx4 v14, s[8:11], s39 offen offset:2048 lds
Mv 248,24,56
Ma 216,24,60
Ma 220,28,60
Mv 252,28,56
Mv 220,28,52
Mv 188,28,48
Mv 156,28,44
)" R"(
Mv 124,28,40
buffer_load_dwordx4 v15, s[8:11], s39 offen offset:3072 lds
Mv 92,28,36
s_add_u32 m0, s37, 0x10000
s_add_u32 s29, s83, 0
buffer_load_dwordx4 v28, s[24:27], s29 offen offset:0 lds
s_add_u32 s29, s83, 768
Mv 60,28,32
buffer_load_dwordx4 v28, s[24:27], s29 offen offset:1024 lds
s_waitcnt vmcnt(10) lgkmcnt(0)
s_barrier
Mv 32,128,160
Rt 0,18,32768
s_add_u32 s29, s83, 1536
buffer_load_dwordx4 v28, s[24:27], s29 offen offset:2048 lds
Mv 64,128,164
s_add_u32 s38, s38, 0xffff0080
s_add_u32 s29, s83, 2304
buffer_load_dwordx4 v28, s[24:27], s29 offen offset:3072 lds
Mv 96,128,168
Rt 2,18,34816
s_add_u32 m0, s36, 0
Mv 128,128,172
buffer_load_dwordx4 v8, s[4:7], s38 offen offset:0 lds
Mv 160,128,176
Rt 4,19,32768
Mv 192,128,180
Mv 224,128,184
Rt 6,19,34816
s_mov_b32 s24, s76
Ma 192,128,188
buffer_load_dwordx4 v9, s[4:7], s38 offen offset:1024 lds
Ma 196,132,188
Rt 8,20,32768
s_mov_b32 s25, s77
Mv 228,132,184
s_mov_b32 s26, 0x188000
s_mov_b32 s27, 0x20000
Mv 196,132,180
Rt 10,20,34816
Mv 164,132,176
buffer_load_dwordx4 v10, s[4:7], s38 offen offset:2048 lds
Mv 132,132,172
Rt 12,21,32768
Mv 100,132,168
Mv 68,132,164
Rt 14,21,34816
Mv 36,132,160
buffer_load_dwordx4 v11, s[4:7], s38 offen offset:3072 lds
Mv 40,136,160
Rt 16,22,32768
Mv 72,136,164
s_add_u32 s38, s38, 0x10000
Mv 104,136,168
Rt 18,22,34816
s_add_u32 m0, s36, 4096
Mv 136,136,172
buffer_load_dwordx4 v8, s[4:7], s38 offen offset:0 lds
Mv 168,136,176
Rt 20,23,32768
Mv 200,136,180
Mv 232,136,184
Rt 22,23,34816
Ma 200,136,188
buffer_load_dwordx4 v9, s[4:7], s38 offen offset:1024 lds
)" R"(
Ma 204,140,188
Rt 24,24,32768
Mv 236,140,184
Mv 204,140,180
Rt 26,24,34816
Mv 172,140,176
buffer_load_dwordx4 v10, s[4:7], s38 offen offset:2048 lds
Mv 140,140,172
Rt 28,25,32768
Mv 108,140,168
Mv 76,140,164
Rt 30,25,34816
Mv 44,140,160
buffer_load_dwordx4 v11, s[4:7], s38 offen offset:3072 lds
Mv 48,144,160
Rb 32,16,32768
Mv 80,144,164
Mv 112,144,168
Rb 36,16,34816
Mv 144,144,172
Mv 176,144,176
Rb 40,16,40960
Mv 208,144,180
buffer_load_dword v27, v26, s[24:27], s90 offen
Mv 240,144,184
Rb 44,16,43008
Ma 208,144,188
s_mov_b32 s24, s78
s_mov_b32 s25, s79
Ma 212,148,188
Rb 48,16,49152
s_mov_b32 s26, 0x188000
Mv 244,148,184
s_mov_b32 s27, 0x20000
Mv 212,148,180
Rb 52,16,51200
Mv 180,148,176
Mv 148,148,172
Rb 56,16,57344
Mv 116,148,168
Mv 84,148,164
Rb 60,16,59392
Mv 52,148,160
Mv 56,152,160
Mv 88,152,164
Mv 120,152,168
Mv 152,152,172
Mv 184,152,176
Mv 216,152,180
buffer_load_dword v27, v26, s[24:27], s90 offen
Mv 248,152,184
Ma 216,152,188
s_mov_b32 s24, s70
s_mov_b32 s25, s71
s_mov_b32 s26, 0x188000
Ma 220,156,188
s_mov_b32 s27, 0x20000
Mv 252,156,184
Mv 220,156,180
Mv 188,156,176
Mv 156,156,172
Mv 124,156,168
Mv 92,156,164
Mv 60,156,160
)" R"(
s_waitcnt vmcnt(10) lgkmcnt(0)
s_barrier
Mv 32,0,32
Rt 128,18,49152
Mv 64,0,36
Mv 96,0,40
Rt 130,18,51200
v_lshrrev_b32 v5, 2, v0
Mv 128,0,44
s_add_u32 s39, s39, 0x40000
v_and_b32 v5, 8, v5
Mv 160,0,48
Rt 132,19,49152
s_add_u32 m0, s37, 16384
Mv 192,0,52
buffer_load_dwordx4 v12, s[8:11], s39 offen offset:0 lds
Mv 224,0,56
Rt 134,19,51200
v_and_or_b32 v5, v0, 7, v5
Ma 192,0,60
v_lshlrev_b32 v5, 8, v5
v_and_or_b32 v5, v0, 24, v5
Ma 196,4,60
Rt 136,20,49152
v_lshrrev_b32 v5, 1, v5
Mv 228,4,56
Mv 196,4,52
Rt 138,20,51200
Mv 164,4,48
Mv 132,4,44
Rt 140,21,49152
Mv 100,4,40
buffer_load_dwordx4 v13, s[8:11], s39 offen offset:1024 lds
Mv 68,4,36
Rt 142,21,51200
Mv 36,4,32
Mv 40,8,32
Rt 144,22,49152
Mv 72,8,36
Mv 104,8,40
Rt 146,22,51200
Mv 136,8,44
Mv 168,8,48
Rt 148,23,49152
Mv 200,8,52
buffer_load_dwordx4 v14, s[8:11], s39 offen offset:2048 lds
Mv 232,8,56
Rt 150,23,51200
Ma 200,8,60
Ma 204,12,60
Rt 152,24,49152
Mv 236,12,56
Mv 204,12,52
Rt 154,24,51200
Mv 172,12,48
Mv 140,12,44
Rt 156,25,49152
Mv 108,12,40
buffer_load_dwordx4 v15, s[8:11], s39 offen offset:3072 lds
Mv 76,12,36
Rt 158,25,51200
Mv 44,12,32
Mv 48,16,32
Rb 160,17,32768
Mv 80,16,36
)" R"(
Mv 112,16,40
Rb 164,17,34816
Mv 144,16,44
s_add_u32 s39, s39, 0x40000
Mv 176,16,48
Rb 168,17,40960
s_add_u32 m0, s37, 32768
Mv 208,16,52
buffer_load_dwordx4 v12, s[8:11], s39 offen offset:0 lds
Mv 240,16,56
Rb 172,17,43008
Ma 208,16,60
Ma 212,20,60
Rb 176,17,49152
Mv 244,20,56
Mv 212,20,52
Rb 180,17,51200
Mv 180,20,48
Mv 148,20,44
Rb 184,17,57344
Mv 116,20,40
buffer_load_dwordx4 v13, s[8:11], s39 offen offset:1024 lds
Mv 84,20,36
Rb 188,17,59392
Mv 52,20,32
Mv 56,24,32
Rt 224,29,0
Mv 88,24,36
Rt 226,29,8192
Mv 120,24,40
Rt 228,29,128
Mv 152,24,44
Rt 230,29,8320
Mv 184,24,48
Rt 232,29,256
Mv 216,24,52
buffer_load_dwordx4 v14, s[8:11], s39 offen offset:2048 lds
Rt 234,29,8448
Mv 248,24,56
Rt 236,29,384
Ma 216,24,60
Rt 238,29,8576
Ma 220,28,60
Mv 252,28,56
Mv 220,28,52
Mv 188,28,48
Mv 156,28,44
Mv 124,28,40
buffer_load_dwordx4 v15, s[8:11], s39 offen offset:3072 lds
Mv 92,28,36
Mv 60,28,32
s_waitcnt vmcnt(10) lgkmcnt(0)
s_barrier
Mv 32,128,160
Rt 0,18,0
Mv 64,128,164
s_add_u32 s38, s38, 0xffff0080
Mv 96,128,168
Rt 2,18,2048
s_add_u32 m0, s36, 32768
Mv 128,128,172
buffer_load_dwordx4 v8, s[4:7], s38 offen offset:0 lds
Mv 160,128,176
Rt 4,19,0
Mv 192,128,180
)" R"(
Mv 224,128,184
Rt 6,19,2048
Ma 192,128,188
buffer_load_dwordx4 v9, s[4:7], s38 offen offset:1024 lds
Ma 196,132,188
Rt 8,20,0
Mv 228,132,184
Mv 196,132,180
Rt 10,20,2048
Mv 164,132,176
buffer_load_dwordx4 v10, s[4:7], s38 offen offset:2048 lds
Mv 132,132,172
Rt 12,21,0
Mv 100,132,168
Mv 68,132,164
Rt 14,21,2048
Mv 36,132,160
buffer_load_dwordx4 v11, s[4:7], s38 offen offset:3072 lds
Mv 40,136,160
Rt 16,22,0
Mv 72,136,164
s_add_u32 s38, s38, 0x10000
Mv 104,136,168
Rt 18,22,2048
s_add_u32 m0, s36, 36864
Mv 136,136,172
buffer_load_dwordx4 v8, s[4:7], s38 offen offset:0 lds
Mv 168,136,176
Rt 20,23,0
Mv 200,136,180
Mv 232,136,184
Rt 22,23,2048
Ma 200,136,188
buffer_load_dwordx4 v9, s[4:7], s38 offen offset:1024 lds
Ma 204,140,188
Rt 24,24,0
Mv 236,140,184
Mv 204,140,180
Rt 26,24,2048
Mv 172,140,176
buffer_load_dwordx4 v10, s[4:7], s38 offen offset:2048 lds
Mv 140,140,172
Rt 28,25,0
s_mul_i32 s29, s94, 147
Mv 108,140,168
s_lshr_b32 s29, s29, 10
s_mul_i32 s29, s29, 9
Mv 76,140,164
Rt 30,25,2048
s_add_u32 s29, s29, s94
Mv 44,140,160
buffer_load_dwordx4 v11, s[4:7], s38 offen offset:3072 lds
Mv 48,144,160
Rb 32,16,0
s_lshl_b32 s29, s29, 10
Mv 80,144,164
s_add_u32 s29, s29, s82
s_add_u32 s29, s29, s84
Mv 112,144,168
Rb 36,16,2048
s_add_u32 s94, s94, s92
Mv 144,144,172
buffer_store_dwordx4 a[224:227], v1, s[20:23], s29 offen sc0 sc1
Mv 176,144,176
)" R"(
Rb 40,16,8192
s_mul_i32 s29, s94, 147
Mv 208,144,180
s_lshr_b32 s29, s29, 10
s_mul_i32 s29, s29, 9
Mv 240,144,184
Rb 44,16,10240
s_add_u32 s29, s29, s94
buffer_load_dword v27, v26, s[24:27], s90 offen
Ma 208,144,188
s_lshl_b32 s29, s29, 10
s_add_u32 s29, s29, s82
Ma 212,148,188
Rb 48,16,16384
s_add_u32 s29, s29, s84
Mv 244,148,184
s_add_u32 s94, s94, s92
Mv 212,148,180
Rb 52,16,18432
Mv 180,148,176
buffer_store_dwordx4 a[228:231], v1, s[20:23], s29 offen sc0 sc1
Mv 148,148,172
Rb 56,16,24576
s_mul_i32 s29, s94, 147
Mv 116,148,168
s_lshr_b32 s29, s29, 10
s_mul_i32 s29, s29, 9
Mv 84,148,164
Rb 60,16,26624
s_add_u32 s29, s29, s94
Mv 52,148,160
s_lshl_b32 s29, s29, 10
s_add_u32 s29, s29, s82
Mv 56,152,160
s_add_u32 s29, s29, s84
s_add_u32 s94, s94, s92
Mv 88,152,164
buffer_store_dwordx4 a[232:235], v1, s[20:23], s29 offen sc0 sc1
Mv 120,152,168
Mv 152,152,172
Mv 184,152,176
s_mul_i32 s29, s94, 147
s_lshr_b32 s29, s29, 10
Mv 216,152,180
s_mul_i32 s29, s29, 9
s_add_u32 s29, s29, s94
Mv 248,152,184
s_lshl_b32 s29, s29, 10
s_add_u32 s29, s29, s82
Ma 216,152,188
s_add_u32 s29, s29, s84
Ma 220,156,188
s_cmp_gt_u32 s33, 1
s_cmov_b32 s29, 0x40000000
Mv 252,156,184
buffer_store_dwordx4 a[236:239], v1, s[20:23], s29 offen sc0 sc1
Mv 220,156,180
Mv 188,156,176
Mv 156,156,172
Mv 124,156,168
Mv 92,156,164
Mv 60,156,160
s_waitcnt vmcnt(17) lgkmcnt(0)
s_barrier
Mv 32,0,32
)" R"(
Rt 128,18,16384
Mv 64,0,36
Mv 96,0,40
Rt 130,18,18432
Mv 128,0,44
s_add_u32 s39, s39, 0x40000
Mv 160,0,48
Rt 132,19,16384
s_add_u32 m0, s37, 49152
Mv 192,0,52
buffer_load_dwordx4 v12, s[8:11], s39 offen offset:0 lds
Mv 224,0,56
Rt 134,19,18432
Ma 192,0,60
Ma 196,4,60
Rt 136,20,16384
Mv 228,4,56
Mv 196,4,52
Rt 138,20,18432
Mv 164,4,48
Mv 132,4,44
Rt 140,21,16384
Mv 100,4,40
buffer_load_dwordx4 v13, s[8:11], s39 offen offset:1024 lds
Mv 68,4,36
Rt 142,21,18432
Mv 36,4,32
Mv 40,8,32
Rt 144,22,16384
Mv 72,8,36
Mv 104,8,40
Rt 146,22,18432
Mv 136,8,44
Mv 168,8,48
Rt 148,23,16384
Mv 200,8,52
buffer_load_dwordx4 v14, s[8:11], s39 offen offset:2048 lds
Mv 232,8,56
Rt 150,23,18432
Ma 200,8,60
Ma 204,12,60
Rt 152,24,16384
Mv 236,12,56
Mv 204,12,52
Rt 154,24,18432
Mv 172,12,48
Mv 140,12,44
Rt 156,25,16384
Mv 108,12,40
buffer_load_dwordx4 v15, s[8:11], s39 offen offset:3072 lds
Mv 76,12,36
Rt 158,25,18432
Mv 44,12,32
Mv 48,16,32
Rb 160,17,0
Mv 80,16,36
buffer_load_dword v4, v5, s[20:23], s50 offen offset:0 sc0 sc1
Mv 112,16,40
Rb 164,17,2048
Mv 144,16,44
Mv 176,16,48
Rb 168,17,8192
Mv 208,16,52
Mv 240,16,56
)" R"(
Rb 172,17,10240
Ma 208,16,60
Ma 212,20,60
Rb 176,17,16384
Mv 244,20,56
Mv 212,20,52
Rb 180,17,18432
Mv 180,20,48
Mv 148,20,44
Rb 184,17,24576
Mv 116,20,40
Mv 84,20,36
Rb 188,17,26624
Mv 52,20,32
Mv 56,24,32
Mv 88,24,36
Mv 120,24,40
Mv 152,24,44
Mv 184,24,48
Mv 216,24,52
Mv 248,24,56
Ma 216,24,60
Ma 220,28,60
Mv 252,28,56
Mv 220,28,52
Mv 188,28,48
Mv 156,28,44
Mv 124,28,40
Mv 92,28,36
Mv 60,28,32
s_waitcnt vmcnt(0) lgkmcnt(0)
s_barrier
Mv 32,128,160
Rt 0,18,32768
v_cmp_eq_u32_e32 vcc, s3, v4
s_andn2_b64 s[28:29], s[60:61], vcc
s_cbranch_scc1 .Lzsp12
.Lzok12:
Mv 64,128,164
Mv 96,128,168
Rt 2,18,34816
Mv 128,128,172
Mv 160,128,176
Rt 4,19,32768
s_add_u32 m0, s36, 0
Mv 192,128,180
buffer_load_dwordx4 v2, s[20:23], s55 offen offset:0 sc1 lds
Mv 224,128,184
Rt 6,19,34816
Ma 192,128,188
Ma 196,132,188
Rt 8,20,32768
Mv 228,132,184
Mv 196,132,180
Rt 10,20,34816
Mv 164,132,176
buffer_load_dwordx4 v2, s[20:23], s55 offen offset:1024 sc1 lds
Mv 132,132,172
Rt 12,21,32768
Mv 100,132,168
Mv 68,132,164
Rt 14,21,34816
Mv 36,132,160
Mv 40,136,160
Rt 16,22,32768
)" R"(
Mv 72,136,164
buffer_load_dwordx4 v2, s[20:23], s55 offen offset:2048 sc1 lds
Mv 104,136,168
Rt 18,22,34816
Mv 136,136,172
Mv 168,136,176
Rt 20,23,32768
Mv 200,136,180
Mv 232,136,184
Rt 22,23,34816
Ma 200,136,188
buffer_load_dwordx4 v2, s[20:23], s55 offen offset:3072 sc1 lds
Ma 204,140,188
Rt 24,24,32768
Mv 236,140,184
Mv 204,140,180
Rt 26,24,34816
Mv 172,140,176
Mv 140,140,172
Rt 28,25,32768
s_add_u32 m0, s36, 4096
Mv 108,140,168
buffer_load_dwordx4 v3, s[20:23], s55 offen offset:0 sc1 lds
Mv 76,140,164
Rt 30,25,34816
Mv 44,140,160
Mv 48,144,160
Rb 32,16,32768
Mv 80,144,164
Mv 112,144,168
Rb 36,16,34816
Mv 144,144,172
buffer_load_dwordx4 v3, s[20:23], s55 offen offset:1024 sc1 lds
Mv 176,144,176
Rb 40,16,40960
Mv 208,144,180
Mv 240,144,184
Rb 44,16,43008
Ma 208,144,188
Ma 212,148,188
Rb 48,16,49152
Mv 244,148,184
buffer_load_dwordx4 v3, s[20:23], s55 offen offset:2048 sc1 lds
Mv 212,148,180
Rb 52,16,51200
Mv 180,148,176
Mv 148,148,172
Rb 56,16,57344
Mv 116,148,168
Mv 84,148,164
Rb 60,16,59392
Mv 52,148,160
buffer_load_dwordx4 v3, s[20:23], s55 offen offset:3072 sc1 lds
Mv 56,152,160
Mv 88,152,164
Mv 120,152,168
Mv 152,152,172
Mv 184,152,176
Mv 216,152,180
Mv 248,152,184
Ma 216,152,188
Ma 220,156,188
s_add_u32 s55, s55, 0x10000
Mv 252,156,184
)" R"(
Mv 220,156,180
Mv 188,156,176
Mv 156,156,172
Mv 124,156,168
Mv 92,156,164
Mv 60,156,160
s_waitcnt lgkmcnt(4)
s_barrier
Mv 32,0,32
Rt 128,18,49152
Mv 64,0,36
Rt 130,18,51200
Mv 96,0,40
Rt 132,19,49152
Mv 128,0,44
Rt 134,19,51200
Mv 132,4,44
Rt 136,20,49152
Mv 100,4,40
Rt 138,20,51200
Mv 68,4,36
Rt 140,21,49152
Mv 36,4,32
Rt 142,21,51200
Mv 40,8,32
Rt 144,22,49152
Mv 72,8,36
Rt 146,22,51200
Mv 104,8,40
Rt 148,23,49152
Mv 136,8,44
Rt 150,23,51200
Mv 140,12,44
Rt 152,24,49152
Mv 108,12,40
Rt 154,24,51200
Mv 76,12,36
Rt 156,25,49152
Mv 44,12,32
Rt 158,25,51200
Mv 48,16,32
Rb 160,17,32768
Mv 80,16,36
Rb 164,17,34816
Mv 112,16,40
Rb 168,17,40960
Mv 144,16,44
Rb 172,17,43008
Mv 148,20,44
Rb 176,17,49152
Mv 116,20,40
Rb 180,17,51200
Mv 84,20,36
Rb 184,17,57344
Mv 52,20,32
Rb 188,17,59392
Mv 56,24,32
v_add_u32 v8, 0x10000, v1
v_add_u32 v10, 0x20000, v1
Mv 88,24,36
s_mov_b32 s93, 2
s_mov_b32 s94, 0
Mv 120,24,40
Mv 152,24,44
Mv 156,28,44
)" R"(
Mv 124,28,40
Mv 92,28,36
Mv 60,28,32
s_waitcnt lgkmcnt(0)
s_barrier
Mv 32,128,160
Mv 36,132,160
Mv 40,136,160
s_add_u32 m0, s36, 32768
Mv 44,140,160
buffer_load_dwordx4 v2, s[20:23], s55 offen offset:0 sc1 lds
Mv 48,144,160
Mv 52,148,160
s_nop 0
Cv 32,32,33
Cv 33,34,35
Mv 56,152,160
Cv 34,36,37
Cv 35,38,39
Mv 60,156,160
buffer_load_dwordx4 v2, s[20:23], s55 offen offset:1024 sc1 lds
Mv 64,128,164
Cv 40,40,41
Cv 41,42,43
Mv 68,132,164
Cv 42,44,45
Cv 43,46,47
Mv 72,136,164
Cv 48,48,49
Cv 49,50,51
Mv 76,140,164
buffer_load_dwordx4 v2, s[20:23], s55 offen offset:2048 sc1 lds
Mv 80,144,164
Cv 50,52,53
Cv 51,54,55
Mv 84,148,164
Cv 56,56,57
Cv 57,58,59
Mv 88,152,164
Cv 58,60,61
Cv 59,62,63
Mv 92,156,164
buffer_load_dwordx4 v2, s[20:23], s55 offen offset:3072 sc1 lds
Mv 96,128,168
Cv 64,64,65
Cv 65,66,67
Mv 100,132,168
Cv 66,68,69
Cv 67,70,71
Mv 104,136,168
s_add_u32 m0, s36, 36864
Cv 72,72,73
Mv 108,140,168
buffer_load_dwordx4 v3, s[20:23], s55 offen offset:0 sc1 lds
Mv 112,144,168
Cv 73,74,75
Cv 74,76,77
Mv 116,148,168
Cv 75,78,79
Cv 80,80,81
Mv 120,152,168
Cv 81,82,83
Cv 82,84,85
Mv 124,156,168
buffer_store_dwordx4 v[32:35], v2, s[20:23], s45 offen offset:0 sc0 sc1
)" R"(
Mv 128,128,172
Cv 83,86,87
Cv 88,88,89
Mv 132,132,172
Cv 89,90,91
Cv 90,92,93
Mv 136,136,172
Cv 91,94,95
Mv 140,140,172
buffer_load_dwordx4 v3, s[20:23], s55 offen offset:1024 sc1 lds
Mv 144,144,172
Mv 148,148,172
Cv 128,128,129
Cv 129,130,131
Mv 152,152,172
Cv 130,132,133
Cv 131,134,135
Mv 156,156,172
buffer_store_dwordx4 v[40:43], v2, s[20:23], s45 offen offset:1024 sc0 sc1
Mv 160,0,48
Cv 136,136,137
Cv 137,138,139
Mv 160,128,176
Cv 138,140,141
Cv 139,142,143
Mv 164,4,48
Cv 144,144,145
Cv 145,146,147
Mv 164,132,176
buffer_load_dwordx4 v3, s[20:23], s55 offen offset:2048 sc1 lds
Mv 168,8,48
Cv 146,148,149
Cv 147,150,151
Mv 168,136,176
Cv 152,152,153
Cv 153,154,155
Mv 172,12,48
Cv 154,156,157
Cv 155,158,159
Mv 172,140,176
buffer_store_dwordx4 v[48:51], v2, s[20:23], s45 offen offset:2048 sc0 sc1
Mv 176,16,48
Cv 132,96,97
Cv 133,98,99
Mv 176,144,176
Cv 134,100,101
Cv 135,102,103
Mv 180,20,48
Cv 140,104,105
Cv 141,106,107
Mv 180,148,176
buffer_load_dwordx4 v3, s[20:23], s55 offen offset:3072 sc1 lds
Mv 184,24,48
Cv 142,108,109
Cv 143,110,111
Mv 184,152,176
Cv 148,112,113
Cv 149,114,115
Mv 188,28,48
Cv 150,116,117
Cv 151,118,119
Mv 188,156,176
buffer_store_dwordx4 v[56:59], v2, s[20:23], s45 offen offset:3072 sc0 sc1
Mv 192,0,52
)" R"(
Cv 156,120,121
Cv 157,122,123
Mv 192,128,180
Cv 158,124,125
Cv 159,126,127
Mv 196,4,52
s_add_u32 m0, s36, 98304
Cv 160,160,161
Mv 196,132,180
buffer_load_dwordx4 v2, s[20:23], s56 offen offset:0 sc1 lds
Mv 200,8,52
Cv 161,162,163
Cv 162,164,165
Mv 200,136,180
Cv 163,166,167
v_accvgpr_write_b32 a224, v160
Mv 204,12,52
v_accvgpr_write_b32 a225, v161
v_accvgpr_write_b32 a226, v162
Mv 204,140,180
buffer_store_dwordx4 v[64:67], v3, s[20:23], s45 offen offset:0 sc0 sc1
Mv 208,16,52
v_accvgpr_write_b32 a227, v163
Cv 168,168,169
Mv 208,144,180
Cv 169,170,171
Cv 170,172,173
Mv 212,20,52
Cv 171,174,175
v_accvgpr_write_b32 a228, v168
Mv 212,148,180
buffer_load_dwordx4 v2, s[20:23], s56 offen offset:1024 sc1 lds
Mv 216,24,52
v_accvgpr_write_b32 a229, v169
v_accvgpr_write_b32 a230, v170
Mv 216,152,180
v_accvgpr_write_b32 a231, v171
Cv 176,176,177
Mv 220,28,52
Cv 177,178,179
Cv 178,180,181
Mv 220,156,180
buffer_store_dwordx4 v[72:75], v3, s[20:23], s45 offen offset:1024 sc0 sc1
Mv 224,0,56
Cv 179,182,183
v_accvgpr_write_b32 a232, v176
Ma 192,0,60
v_accvgpr_write_b32 a233, v177
v_accvgpr_write_b32 a234, v178
Ma 196,4,60
v_accvgpr_write_b32 a235, v179
Cv 184,184,185
Mv 228,4,56
buffer_load_dwordx4 v2, s[20:23], s56 offen offset:2048 sc1 lds
Mv 232,8,56
Cv 185,186,187
Cv 186,188,189
Ma 200,8,60
Cv 187,190,191
v_accvgpr_write_b32 a236, v184
Ma 204,12,60
v_accvgpr_write_b32 a237, v185
v_accvgpr_write_b32 a238, v186
Mv 236,12,56
)" R"(
s_waitcnt vmcnt(17)
s_barrier
Mv 240,16,56
buffer_store_dwordx4 v[80:83], v3, s[20:23], s45 offen offset:2048 sc0 sc1
Ma 208,16,60
v_accvgpr_write_b32 a239, v187
Ma 212,20,60
Mv 244,20,56
buffer_load_dwordx4 v2, s[20:23], s56 offen offset:3072 sc1 lds
Mv 248,24,56
Ma 216,24,60
Ma 220,28,60
Mv 252,28,56
buffer_store_dwordx4 v[88:91], v3, s[20:23], s45 offen offset:3072 sc0 sc1
Mv 224,128,184
Rb 0,1,0
Mv 228,132,184
Rb 4,1,1024
Mv 232,136,184
s_add_u32 m0, s36, 102400
Rb 8,1,2048
Mv 236,140,184
buffer_load_dwordx4 v3, s[20:23], s56 offen offset:0 sc1 lds
Rb 12,1,3072
Mv 240,144,184
Rb 16,1,8192
Mv 244,148,184
Rb 20,1,9216
Mv 248,152,184
Rb 24,1,10240
Mv 252,156,184
Rb 28,1,11264
Ma 192,128,188
Rb 32,1,4096
Ma 196,132,188
Rb 36,1,5120
Ma 200,136,188
Rb 40,1,6144
Ma 204,140,188
buffer_load_dwordx4 v3, s[20:23], s56 offen offset:1024 sc1 lds
Rb 44,1,7168
Ma 208,144,188
Ma 212,148,188
buffer_load_dwordx4 v3, s[20:23], s56 offen offset:2048 sc1 lds
Ma 216,152,188
Ma 220,156,188
buffer_load_dwordx4 v3, s[20:23], s56 offen offset:3072 sc1 lds
s_add_u32 s56, s56, 0x10000
s_branch .LzFB
.LzPBVX:
Mv 32,32,0
Rt 128,18,16384
Mv 36,36,0
s_mov_b32 s24, s80
s_mov_b32 s25, s81
Mv 96,40,0
Rt 130,18,18432
s_mov_b32 s26, 0x380000
Mv 100,44,0
s_add_u32 s39, s39, 0x40000
s_mov_b32 s27, 0x20000
Mv 160,48,0
Rt 132,19,16384
s_add_u32 m0, s37, 49152
Mv 164,52,0
)" R"(
buffer_load_dwordx4 v12, s[8:11], s39 offen offset:0 lds
Mv 224,56,0
Rt 134,19,18432
s_bfe_u32 s28, s2, 0x30005
Mv 228,60,0
s_lshl3_add_u32 s29, s34, s28
s_lshl2_add_u32 s29, s29, s33
Mv 236,60,4
Rt 136,20,16384
s_mul_i32 s83, s29, 7168
Mv 232,56,4
s_mul_i32 s29, s91, 448
s_add_u32 s83, s83, s29
Mv 172,52,4
Rt 138,20,18432
s_lshl3_add_u32 s29, s34, s91
Mv 168,48,4
s_lshl_b32 s82, s29, 18
s_add_u32 s82, s82, 0x6000000
Mv 108,44,4
Rt 140,21,16384
s_mul_i32 s29, s28, 14
Mv 104,40,4
buffer_load_dwordx4 v13, s[8:11], s39 offen offset:1024 lds
Mv 44,36,4
Rt 142,21,18432
s_add_u32 s29, s29, s33
Mv 40,32,4
s_lshl3_add_u32 s28, s33, s28
Mv 48,32,8
Rt 144,22,16384
Mv 52,36,8
s_cmp_eq_u32 s91, 0
s_cselect_b32 s94, s29, s28
Mv 112,40,8
Rt 146,22,18432
Mv 116,44,8
s_cmp_eq_u32 s91, 0
s_cselect_b32 s92, 4, 32
Mv 176,48,8
Rt 148,23,16384
v_lshrrev_b32 v4, 5, v0
Mv 180,52,8
buffer_load_dwordx4 v14, s[8:11], s39 offen offset:2048 lds
Mv 240,56,8
Rt 150,23,18432
v_mul_u32_u24 v4, 384, v4
Mv 244,60,8
v_add_u32 v28, v4, v1
v_and_b32 v4, 31, v0
Mv 252,60,12
Rt 152,24,16384
v_cmp_lt_u32_e32 vcc, 27, v4
Mv 248,56,12
v_mov_b32 v4, 0x40000000
v_lshrrev_b32 v29, 2, v0
Mv 188,52,12
Rt 154,24,18432
v_cndmask_b32_e32 v28, v28, v4, vcc
Mv 184,48,12
v_and_b32 v4, 48, v1
v_lshl_or_b32 v29, v29, 9, v4
Mv 124,44,12
Rt 156,25,16384
)" R"(
s_lshr_b32 s28, s33, 1
Mv 120,40,12
buffer_load_dwordx4 v15, s[8:11], s39 offen offset:3072 lds
Mv 60,36,12
Rt 158,25,18432
s_lshl_b32 s28, s28, 6
Mv 56,32,12
s_and_b32 s29, s33, 1
s_lshl3_add_u32 s28, s29, s28
Mv 64,32,16
Rb 160,17,0
s_add_u32 s28, s28, 0x20000
Mv 68,36,16
v_add_u32 v29, s28, v29
Mv 128,40,16
Rb 164,17,2048
Mv 132,44,16
s_add_u32 s39, s39, 0x40000
Mv 192,48,16
Rb 168,17,8192
s_add_u32 m0, s37, 0
Mv 196,52,16
buffer_load_dwordx4 v12, s[8:11], s39 offen offset:0 lds
Ma 192,56,16
Rb 172,17,10240
Ma 196,60,16
Ma 204,60,20
Rb 176,17,16384
Ma 200,56,20
Mv 204,52,20
Rb 180,17,18432
Mv 200,48,20
Mv 140,44,20
Rb 184,17,24576
Mv 136,40,20
buffer_load_dwordx4 v13, s[8:11], s39 offen offset:1024 lds
Mv 76,36,20
Rb 188,17,26624
Mv 72,32,20
Mv 80,32,24
Mv 84,36,24
Mv 144,40,24
Mv 148,44,24
Mv 208,48,24
Mv 212,52,24
buffer_load_dwordx4 v14, s[8:11], s39 offen offset:2048 lds
Ma 208,56,24
Ma 212,60,24
Ma 220,60,28
Ma 216,56,28
Mv 220,52,28
Mv 216,48,28
Mv 156,44,28
Mv 152,40,28
buffer_load_dwordx4 v15, s[8:11], s39 offen offset:3072 lds
Mv 92,36,28
s_add_u32 m0, s37, 0x10000
s_add_u32 s29, s83, 0
buffer_load_dwordx4 v28, s[24:27], s29 offen offset:0 lds
s_add_u32 s29, s83, 768
Mv 88,32,28
buffer_load_dwordx4 v28, s[24:27], s29 offen offset:1024 lds
s_waitcnt vmcnt(10) lgkmcnt(0)
s_barrier
Mv 32,160,128
)" R"(
Rt 0,18,32768
s_add_u32 s29, s83, 1536
buffer_load_dwordx4 v28, s[24:27], s29 offen offset:2048 lds
Mv 36,164,128
s_add_u32 s38, s38, 0xffff0080
s_add_u32 s29, s83, 2304
buffer_load_dwordx4 v28, s[24:27], s29 offen offset:3072 lds
Mv 96,168,128
Rt 2,18,34816
s_add_u32 m0, s36, 0
Mv 100,172,128
buffer_load_dwordx4 v8, s[4:7], s38 offen offset:0 lds
Mv 160,176,128
Rt 4,19,32768
Mv 164,180,128
Mv 224,184,128
Rt 6,19,34816
s_mov_b32 s24, s76
Mv 228,188,128
buffer_load_dwordx4 v9, s[4:7], s38 offen offset:1024 lds
Mv 236,188,132
Rt 8,20,32768
s_mov_b32 s25, s77
Mv 232,184,132
s_mov_b32 s26, 0x188000
s_mov_b32 s27, 0x20000
Mv 172,180,132
Rt 10,20,34816
Mv 168,176,132
buffer_load_dwordx4 v10, s[4:7], s38 offen offset:2048 lds
Mv 108,172,132
Rt 12,21,32768
Mv 104,168,132
Mv 44,164,132
Rt 14,21,34816
Mv 40,160,132
buffer_load_dwordx4 v11, s[4:7], s38 offen offset:3072 lds
Mv 48,160,136
Rt 16,22,32768
Mv 52,164,136
s_add_u32 s38, s38, 0x10000
Mv 112,168,136
Rt 18,22,34816
s_add_u32 m0, s36, 4096
Mv 116,172,136
buffer_load_dwordx4 v8, s[4:7], s38 offen offset:0 lds
Mv 176,176,136
Rt 20,23,32768
Mv 180,180,136
Mv 240,184,136
Rt 22,23,34816
Mv 244,188,136
buffer_load_dwordx4 v9, s[4:7], s38 offen offset:1024 lds
Mv 252,188,140
Rt 24,24,32768
Mv 248,184,140
Mv 188,180,140
Rt 26,24,34816
Mv 184,176,140
buffer_load_dwordx4 v10, s[4:7], s38 offen offset:2048 lds
Mv 124,172,140
Rt 28,25,32768
Mv 120,168,140
Mv 60,164,140
)" R"(
Rt 30,25,34816
Mv 56,160,140
buffer_load_dwordx4 v11, s[4:7], s38 offen offset:3072 lds
Mv 64,160,144
Rb 32,16,32768
Mv 68,164,144
Mv 128,168,144
Rb 36,16,34816
Mv 132,172,144
Mv 192,176,144
Rb 40,16,40960
Mv 196,180,144
buffer_load_dword v27, v26, s[24:27], s90 offen
Ma 192,184,144
Rb 44,16,43008
Ma 196,188,144
s_mov_b32 s24, s78
s_mov_b32 s25, s79
Ma 204,188,148
Rb 48,16,49152
s_mov_b32 s26, 0x188000
Ma 200,184,148
s_mov_b32 s27, 0x20000
Mv 204,180,148
Rb 52,16,51200
Mv 200,176,148
Mv 140,172,148
Rb 56,16,57344
Mv 136,168,148
Mv 76,164,148
Rb 60,16,59392
Mv 72,160,148
Mv 80,160,152
Mv 84,164,152
Mv 144,168,152
Mv 148,172,152
Mv 208,176,152
Mv 212,180,152
buffer_load_dword v27, v26, s[24:27], s90 offen
Ma 208,184,152
Ma 212,188,152
s_mov_b32 s24, s70
s_mov_b32 s25, s71
s_mov_b32 s26, 0x188000
Ma 220,188,156
s_mov_b32 s27, 0x20000
Ma 216,184,156
Mv 220,180,156
Mv 216,176,156
Mv 156,172,156
Mv 152,168,156
Mv 92,164,156
Mv 88,160,156
s_waitcnt vmcnt(10) lgkmcnt(0)
s_barrier
Mv 32,32,0
Rt 128,18,49152
Mv 36,36,0
Mv 96,40,0
Rt 130,18,51200
v_lshrrev_b32 v5, 2, v0
Mv 100,44,0
s_add_u32 s39, s39, 0x40000
v_and_b32 v5, 8, v5
Mv 160,48,0
)" R"(
Rt 132,19,49152
s_add_u32 m0, s37, 16384
Mv 164,52,0
buffer_load_dwordx4 v12, s[8:11], s39 offen offset:0 lds
Mv 224,56,0
Rt 134,19,51200
v_and_or_b32 v5, v0, 7, v5
Mv 228,60,0
v_lshlrev_b32 v5, 8, v5
v_and_or_b32 v5, v0, 24, v5
Mv 236,60,4
Rt 136,20,49152
v_lshrrev_b32 v5, 1, v5
Mv 232,56,4
Mv 172,52,4
Rt 138,20,51200
Mv 168,48,4
Mv 108,44,4
Rt 140,21,49152
Mv 104,40,4
buffer_load_dwordx4 v13, s[8:11], s39 offen offset:1024 lds
Mv 44,36,4
Rt 142,21,51200
Mv 40,32,4
Mv 48,32,8
Rt 144,22,49152
Mv 52,36,8
Mv 112,40,8
Rt 146,22,51200
Mv 116,44,8
Mv 176,48,8
Rt 148,23,49152
Mv 180,52,8
buffer_load_dwordx4 v14, s[8:11], s39 offen offset:2048 lds
Mv 240,56,8
Rt 150,23,51200
Mv 244,60,8
Mv 252,60,12
Rt 152,24,49152
Mv 248,56,12
Mv 188,52,12
Rt 154,24,51200
Mv 184,48,12
Mv 124,44,12
Rt 156,25,49152
Mv 120,40,12
buffer_load_dwordx4 v15, s[8:11], s39 offen offset:3072 lds
Mv 60,36,12
Rt 158,25,51200
Mv 56,32,12
Mv 64,32,16
Rb 160,17,32768
Mv 68,36,16
Mv 128,40,16
Rb 164,17,34816
Mv 132,44,16
s_add_u32 s39, s39, 0x40000
Mv 192,48,16
Rb 168,17,40960
s_add_u32 m0, s37, 32768
Mv 196,52,16
buffer_load_dwordx4 v12, s[8:11], s39 offen offset:0 lds
Ma 192,56,16
Rb 172,17,43008
)" R"(
Ma 196,60,16
Ma 204,60,20
Rb 176,17,49152
Ma 200,56,20
Mv 204,52,20
Rb 180,17,51200
Mv 200,48,20
Mv 140,44,20
Rb 184,17,57344
Mv 136,40,20
buffer_load_dwordx4 v13, s[8:11], s39 offen offset:1024 lds
Mv 76,36,20
Rb 188,17,59392
Mv 72,32,20
Mv 80,32,24
Rt 224,29,0
Mv 84,36,24
Rt 226,29,8192
Mv 144,40,24
Rt 228,29,128
Mv 148,44,24
Rt 230,29,8320
Mv 208,48,24
Rt 232,29,256
Mv 212,52,24
buffer_load_dwordx4 v14, s[8:11], s39 offen offset:2048 lds
Rt 234,29,8448
Ma 208,56,24
Rt 236,29,384
Ma 212,60,24
Rt 238,29,8576
Ma 220,60,28
Ma 216,56,28
Mv 220,52,28
Mv 216,48,28
Mv 156,44,28
Mv 152,40,28
buffer_load_dwordx4 v15, s[8:11], s39 offen offset:3072 lds
Mv 92,36,28
Mv 88,32,28
s_waitcnt vmcnt(10) lgkmcnt(0)
s_barrier
Mv 32,160,128
Rt 0,18,0
Mv 36,164,128
s_add_u32 s38, s38, 0xffff0080
Mv 96,168,128
Rt 2,18,2048
s_add_u32 m0, s36, 32768
Mv 100,172,128
buffer_load_dwordx4 v8, s[4:7], s38 offen offset:0 lds
Mv 160,176,128
Rt 4,19,0
Mv 164,180,128
Mv 224,184,128
Rt 6,19,2048
Mv 228,188,128
buffer_load_dwordx4 v9, s[4:7], s38 offen offset:1024 lds
Mv 236,188,132
Rt 8,20,0
Mv 232,184,132
Mv 172,180,132
Rt 10,20,2048
Mv 168,176,132
buffer_load_dwordx4 v10, s[4:7], s38 offen offset:2048 lds
)" R"(
Mv 108,172,132
Rt 12,21,0
Mv 104,168,132
Mv 44,164,132
Rt 14,21,2048
Mv 40,160,132
buffer_load_dwordx4 v11, s[4:7], s38 offen offset:3072 lds
Mv 48,160,136
Rt 16,22,0
Mv 52,164,136
s_add_u32 s38, s38, 0x10000
Mv 112,168,136
Rt 18,22,2048
s_add_u32 m0, s36, 36864
Mv 116,172,136
buffer_load_dwordx4 v8, s[4:7], s38 offen offset:0 lds
Mv 176,176,136
Rt 20,23,0
Mv 180,180,136
Mv 240,184,136
Rt 22,23,2048
Mv 244,188,136
buffer_load_dwordx4 v9, s[4:7], s38 offen offset:1024 lds
Mv 252,188,140
Rt 24,24,0
Mv 248,184,140
Mv 188,180,140
Rt 26,24,2048
Mv 184,176,140
buffer_load_dwordx4 v10, s[4:7], s38 offen offset:2048 lds
Mv 124,172,140
Rt 28,25,0
s_mul_i32 s29, s94, 147
Mv 120,168,140
s_lshr_b32 s29, s29, 10
s_mul_i32 s29, s29, 9
Mv 60,164,140
Rt 30,25,2048
s_add_u32 s29, s29, s94
Mv 56,160,140
buffer_load_dwordx4 v11, s[4:7], s38 offen offset:3072 lds
Mv 64,160,144
Rb 32,16,0
s_lshl_b32 s29, s29, 10
Mv 68,164,144
s_add_u32 s29, s29, s82
s_add_u32 s29, s29, s84
Mv 128,168,144
Rb 36,16,2048
s_add_u32 s94, s94, s92
Mv 132,172,144
buffer_store_dwordx4 a[224:227], v1, s[20:23], s29 offen sc0 sc1
Mv 192,176,144
Rb 40,16,8192
s_mul_i32 s29, s94, 147
Mv 196,180,144
s_lshr_b32 s29, s29, 10
s_mul_i32 s29, s29, 9
Ma 192,184,144
Rb 44,16,10240
s_add_u32 s29, s29, s94
buffer_load_dword v27, v26, s[24:27], s90 offen
Ma 196,188,144
s_lshl_b32 s29, s29, 10
)" R"(
s_add_u32 s29, s29, s82
Ma 204,188,148
Rb 48,16,16384
s_add_u32 s29, s29, s84
Ma 200,184,148
s_add_u32 s94, s94, s92
Mv 204,180,148
Rb 52,16,18432
Mv 200,176,148
buffer_store_dwordx4 a[228:231], v1, s[20:23], s29 offen sc0 sc1
Mv 140,172,148
Rb 56,16,24576
s_mul_i32 s29, s94, 147
Mv 136,168,148
s_lshr_b32 s29, s29, 10
s_mul_i32 s29, s29, 9
Mv 76,164,148
Rb 60,16,26624
s_add_u32 s29, s29, s94
Mv 72,160,148
s_lshl_b32 s29, s29, 10
s_add_u32 s29, s29, s82
Mv 80,160,152
s_add_u32 s29, s29, s84
s_add_u32 s94, s94, s92
Mv 84,164,152
buffer_store_dwordx4 a[232:235], v1, s[20:23], s29 offen sc0 sc1
Mv 144,168,152
Mv 148,172,152
Mv 208,176,152
s_mul_i32 s29, s94, 147
s_lshr_b32 s29, s29, 10
Mv 212,180,152
s_mul_i32 s29, s29, 9
s_add_u32 s29, s29, s94
Ma 208,184,152
s_lshl_b32 s29, s29, 10
s_add_u32 s29, s29, s82
Ma 212,188,152
s_add_u32 s29, s29, s84
Ma 220,188,156
s_cmp_gt_u32 s33, 1
s_cmov_b32 s29, 0x40000000
Ma 216,184,156
buffer_store_dwordx4 a[236:239], v1, s[20:23], s29 offen sc0 sc1
Mv 220,180,156
Mv 216,176,156
Mv 156,172,156
Mv 152,168,156
Mv 92,164,156
Mv 88,160,156
s_waitcnt vmcnt(17) lgkmcnt(0)
s_barrier
Mv 32,32,0
Rt 128,18,16384
Mv 36,36,0
Mv 96,40,0
Rt 130,18,18432
Mv 100,44,0
s_add_u32 s39, s39, 0x40000
Mv 160,48,0
Rt 132,19,16384
s_add_u32 m0, s37, 49152
Mv 164,52,0
buffer_load_dwordx4 v12, s[8:11], s39 offen offset:0 lds
)" R"(
Mv 224,56,0
Rt 134,19,18432
Mv 228,60,0
Mv 236,60,4
Rt 136,20,16384
Mv 232,56,4
Mv 172,52,4
Rt 138,20,18432
Mv 168,48,4
Mv 108,44,4
Rt 140,21,16384
Mv 104,40,4
buffer_load_dwordx4 v13, s[8:11], s39 offen offset:1024 lds
Mv 44,36,4
Rt 142,21,18432
Mv 40,32,4
Mv 48,32,8
Rt 144,22,16384
Mv 52,36,8
Mv 112,40,8
Rt 146,22,18432
Mv 116,44,8
Mv 176,48,8
Rt 148,23,16384
Mv 180,52,8
buffer_load_dwordx4 v14, s[8:11], s39 offen offset:2048 lds
Mv 240,56,8
Rt 150,23,18432
Mv 244,60,8
Mv 252,60,12
Rt 152,24,16384
Mv 248,56,12
Mv 188,52,12
Rt 154,24,18432
Mv 184,48,12
Mv 124,44,12
Rt 156,25,16384
Mv 120,40,12
buffer_load_dwordx4 v15, s[8:11], s39 offen offset:3072 lds
Mv 60,36,12
Rt 158,25,18432
Mv 56,32,12
Mv 64,32,16
Rb 160,17,0
Mv 68,36,16
buffer_load_dword v4, v5, s[20:23], s50 offen offset:0 sc0 sc1
Mv 128,40,16
Rb 164,17,2048
Mv 132,44,16
Mv 192,48,16
Rb 168,17,8192
Mv 196,52,16
Ma 192,56,16
Rb 172,17,10240
Ma 196,60,16
Ma 204,60,20
Rb 176,17,16384
Ma 200,56,20
Mv 204,52,20
Rb 180,17,18432
Mv 200,48,20
Mv 140,44,20
Rb 184,17,24576
Mv 136,40,20
)" R"(
Mv 76,36,20
Rb 188,17,26624
Mv 72,32,20
Mv 80,32,24
Mv 84,36,24
Mv 144,40,24
Mv 148,44,24
Mv 208,48,24
Mv 212,52,24
Ma 208,56,24
Ma 212,60,24
Ma 220,60,28
Ma 216,56,28
Mv 220,52,28
Mv 216,48,28
Mv 156,44,28
Mv 152,40,28
Mv 92,36,28
Mv 88,32,28
s_waitcnt vmcnt(0) lgkmcnt(0)
s_barrier
Mv 32,160,128
Rt 0,18,32768
v_cmp_eq_u32_e32 vcc, s3, v4
s_andn2_b64 s[28:29], s[60:61], vcc
s_cbranch_scc1 .Lzsp13
.Lzok13:
Mv 36,164,128
Mv 96,168,128
Rt 2,18,34816
Mv 100,172,128
Mv 160,176,128
Rt 4,19,32768
s_add_u32 m0, s36, 0
Mv 164,180,128
buffer_load_dwordx4 v2, s[20:23], s55 offen offset:0 sc1 lds
Mv 224,184,128
Rt 6,19,34816
Mv 228,188,128
Mv 236,188,132
Rt 8,20,32768
Mv 232,184,132
Mv 172,180,132
Rt 10,20,34816
Mv 168,176,132
buffer_load_dwordx4 v2, s[20:23], s55 offen offset:1024 sc1 lds
Mv 108,172,132
Rt 12,21,32768
Mv 104,168,132
Mv 44,164,132
Rt 14,21,34816
Mv 40,160,132
Mv 48,160,136
Rt 16,22,32768
Mv 52,164,136
buffer_load_dwordx4 v2, s[20:23], s55 offen offset:2048 sc1 lds
Mv 112,168,136
Rt 18,22,34816
Mv 116,172,136
Mv 176,176,136
Rt 20,23,32768
Mv 180,180,136
Mv 240,184,136
Rt 22,23,34816
Mv 244,188,136
)" R"(
buffer_load_dwordx4 v2, s[20:23], s55 offen offset:3072 sc1 lds
Mv 252,188,140
Rt 24,24,32768
Mv 248,184,140
Mv 188,180,140
Rt 26,24,34816
Mv 184,176,140
Mv 124,172,140
Rt 28,25,32768
s_add_u32 m0, s36, 4096
Mv 120,168,140
buffer_load_dwordx4 v3, s[20:23], s55 offen offset:0 sc1 lds
Mv 60,164,140
Rt 30,25,34816
Mv 56,160,140
Mv 64,160,144
Rb 32,16,32768
Mv 68,164,144
Mv 128,168,144
Rb 36,16,34816
Mv 132,172,144
buffer_load_dwordx4 v3, s[20:23], s55 offen offset:1024 sc1 lds
Mv 192,176,144
Rb 40,16,40960
Mv 196,180,144
Ma 192,184,144
Rb 44,16,43008
Ma 196,188,144
Ma 204,188,148
Rb 48,16,49152
Ma 200,184,148
buffer_load_dwordx4 v3, s[20:23], s55 offen offset:2048 sc1 lds
Mv 204,180,148
Rb 52,16,51200
Mv 200,176,148
Mv 140,172,148
Rb 56,16,57344
Mv 136,168,148
Mv 76,164,148
Rb 60,16,59392
Mv 72,160,148
buffer_load_dwordx4 v3, s[20:23], s55 offen offset:3072 sc1 lds
Mv 80,160,152
Mv 84,164,152
Mv 144,168,152
Mv 148,172,152
Mv 208,176,152
Mv 212,180,152
Ma 208,184,152
Ma 212,188,152
Ma 220,188,156
s_add_u32 s55, s55, 0x10000
Ma 216,184,156
Mv 220,180,156
Mv 216,176,156
Mv 156,172,156
Mv 152,168,156
Mv 92,164,156
Mv 88,160,156
s_waitcnt lgkmcnt(4)
s_barrier
Mv 32,32,0
Rt 128,18,49152
Mv 36,36,0
Rt 130,18,51200
)" R"(
Mv 96,40,0
Rt 132,19,49152
Mv 100,44,0
Rt 134,19,51200
Mv 108,44,4
Rt 136,20,49152
Mv 104,40,4
Rt 138,20,51200
Mv 44,36,4
Rt 140,21,49152
Mv 40,32,4
Rt 142,21,51200
Mv 48,32,8
Rt 144,22,49152
Mv 52,36,8
Rt 146,22,51200
Mv 112,40,8
Rt 148,23,49152
Mv 116,44,8
Rt 150,23,51200
Mv 124,44,12
Rt 152,24,49152
Mv 120,40,12
Rt 154,24,51200
Mv 60,36,12
Rt 156,25,49152
Mv 56,32,12
Rt 158,25,51200
Mv 64,32,16
Rb 160,17,32768
Mv 68,36,16
Rb 164,17,34816
Mv 128,40,16
Rb 168,17,40960
Mv 132,44,16
Rb 172,17,43008
Mv 140,44,20
Rb 176,17,49152
Mv 136,40,20
Rb 180,17,51200
Mv 76,36,20
Rb 184,17,57344
Mv 72,32,20
Rb 188,17,59392
Mv 80,32,24
v_add_u32 v8, 0x10000, v1
v_add_u32 v10, 0x20000, v1
Mv 84,36,24
s_mov_b32 s93, 2
s_mov_b32 s94, 0
Mv 144,40,24
Mv 148,44,24
Mv 156,44,28
Mv 152,40,28
Mv 92,36,28
Mv 88,32,28
s_waitcnt lgkmcnt(0)
s_barrier
Mv 32,160,128
Mv 36,164,128
Mv 40,160,132
s_add_u32 m0, s36, 32768
Mv 44,164,132
buffer_load_dwordx4 v2, s[20:23], s55 offen offset:0 sc1 lds
Mv 48,160,136
)" R"(
Mv 52,164,136
s_nop 0
Cv 32,32,33
Cv 33,34,35
Mv 56,160,140
Cv 34,36,37
Cv 35,38,39
Mv 60,164,140
buffer_load_dwordx4 v2, s[20:23], s55 offen offset:1024 sc1 lds
Mv 64,160,144
Cv 40,40,41
Cv 41,42,43
Mv 68,164,144
Cv 42,44,45
Cv 43,46,47
Mv 72,160,148
Cv 48,48,49
Cv 49,50,51
Mv 76,164,148
buffer_load_dwordx4 v2, s[20:23], s55 offen offset:2048 sc1 lds
Mv 80,160,152
Cv 50,52,53
Cv 51,54,55
Mv 84,164,152
Cv 56,56,57
Cv 57,58,59
Mv 88,160,156
Cv 58,60,61
Cv 59,62,63
Mv 92,164,156
buffer_load_dwordx4 v2, s[20:23], s55 offen offset:3072 sc1 lds
Mv 96,168,128
Cv 64,64,65
Cv 65,66,67
Mv 100,172,128
Cv 66,68,69
Cv 67,70,71
Mv 104,168,132
s_add_u32 m0, s36, 36864
Cv 72,72,73
Mv 108,172,132
buffer_load_dwordx4 v3, s[20:23], s55 offen offset:0 sc1 lds
Mv 112,168,136
Cv 73,74,75
Cv 74,76,77
Mv 116,172,136
Cv 75,78,79
Cv 80,80,81
Mv 120,168,140
Cv 81,82,83
Cv 82,84,85
Mv 124,172,140
buffer_store_dwordx4 v[32:35], v2, s[20:23], s45 offen offset:0 sc0 sc1
Mv 128,168,144
Cv 83,86,87
Cv 88,88,89
Mv 132,172,144
Cv 89,90,91
Cv 90,92,93
Mv 136,168,148
Cv 91,94,95
Mv 140,172,148
buffer_load_dwordx4 v3, s[20:23], s55 offen offset:1024 sc1 lds
Mv 144,168,152
)" R"(
Mv 148,172,152
Cv 128,128,129
Cv 129,130,131
Mv 152,168,156
Cv 130,132,133
Cv 131,134,135
Mv 156,172,156
buffer_store_dwordx4 v[40:43], v2, s[20:23], s45 offen offset:1024 sc0 sc1
Mv 160,48,0
Cv 136,136,137
Cv 137,138,139
Mv 160,176,128
Cv 138,140,141
Cv 139,142,143
Mv 164,52,0
Cv 144,144,145
Cv 145,146,147
Mv 164,180,128
buffer_load_dwordx4 v3, s[20:23], s55 offen offset:2048 sc1 lds
Mv 168,48,4
Cv 146,148,149
Cv 147,150,151
Mv 168,176,132
Cv 152,152,153
Cv 153,154,155
Mv 172,52,4
Cv 154,156,157
Cv 155,158,159
Mv 172,180,132
buffer_store_dwordx4 v[48:51], v2, s[20:23], s45 offen offset:2048 sc0 sc1
Mv 176,48,8
Cv 132,96,97
Cv 133,98,99
Mv 176,176,136
Cv 134,100,101
Cv 135,102,103
Mv 180,52,8
Cv 140,104,105
Cv 141,106,107
Mv 180,180,136
buffer_load_dwordx4 v3, s[20:23], s55 offen offset:3072 sc1 lds
Mv 184,48,12
Cv 142,108,109
Cv 143,110,111
Mv 184,176,140
Cv 148,112,113
Cv 149,114,115
Mv 188,52,12
Cv 150,116,117
Cv 151,118,119
Mv 188,180,140
buffer_store_dwordx4 v[56:59], v2, s[20:23], s45 offen offset:3072 sc0 sc1
Mv 192,48,16
Cv 156,120,121
Cv 157,122,123
Mv 192,176,144
Cv 158,124,125
Cv 159,126,127
Mv 196,52,16
s_add_u32 m0, s36, 98304
Cv 160,160,161
Mv 196,180,144
buffer_load_dwordx4 v2, s[20:23], s56 offen offset:0 sc1 lds
Mv 200,48,20
)" R"(
Cv 161,162,163
Cv 162,164,165
Mv 200,176,148
Cv 163,166,167
v_accvgpr_write_b32 a224, v160
Mv 204,52,20
v_accvgpr_write_b32 a225, v161
v_accvgpr_write_b32 a226, v162
Mv 204,180,148
buffer_store_dwordx4 v[64:67], v3, s[20:23], s45 offen offset:0 sc0 sc1
Mv 208,48,24
v_accvgpr_write_b32 a227, v163
Cv 168,168,169
Mv 208,176,152
Cv 169,170,171
Cv 170,172,173
Mv 212,52,24
Cv 171,174,175
v_accvgpr_write_b32 a228, v168
Mv 212,180,152
buffer_load_dwordx4 v2, s[20:23], s56 offen offset:1024 sc1 lds
Mv 216,48,28
v_accvgpr_write_b32 a229, v169
v_accvgpr_write_b32 a230, v170
Mv 216,176,156
v_accvgpr_write_b32 a231, v171
Cv 176,176,177
Mv 220,52,28
Cv 177,178,179
Cv 178,180,181
Mv 220,180,156
buffer_store_dwordx4 v[72:75], v3, s[20:23], s45 offen offset:1024 sc0 sc1
Mv 224,56,0
Cv 179,182,183
v_accvgpr_write_b32 a232, v176
Mv 228,60,0
v_accvgpr_write_b32 a233, v177
v_accvgpr_write_b32 a234, v178
Mv 236,60,4
v_accvgpr_write_b32 a235, v179
Cv 184,184,185
Mv 232,56,4
buffer_load_dwordx4 v2, s[20:23], s56 offen offset:2048 sc1 lds
Mv 240,56,8
Cv 185,186,187
Cv 186,188,189
Mv 244,60,8
Cv 187,190,191
v_accvgpr_write_b32 a236, v184
Mv 252,60,12
v_accvgpr_write_b32 a237, v185
v_accvgpr_write_b32 a238, v186
Mv 248,56,12
s_waitcnt vmcnt(17)
s_barrier
Ma 192,56,16
buffer_store_dwordx4 v[80:83], v3, s[20:23], s45 offen offset:2048 sc0 sc1
Ma 196,60,16
v_accvgpr_write_b32 a239, v187
Ma 204,60,20
Ma 200,56,20
buffer_load_dwordx4 v2, s[20:23], s56 offen offset:3072 sc1 lds
Ma 208,56,24
Ma 212,60,24
Ma 220,60,28
)" R"(
Ma 216,56,28
buffer_store_dwordx4 v[88:91], v3, s[20:23], s45 offen offset:3072 sc0 sc1
Mv 224,184,128
Rb 0,1,0
Mv 228,188,128
Rb 4,1,1024
Mv 232,184,132
s_add_u32 m0, s36, 102400
Rb 8,1,2048
Mv 236,188,132
buffer_load_dwordx4 v3, s[20:23], s56 offen offset:0 sc1 lds
Rb 12,1,3072
Mv 240,184,136
Rb 16,1,8192
Mv 244,188,136
Rb 20,1,9216
Mv 248,184,140
Rb 24,1,10240
Mv 252,188,140
Rb 28,1,11264
Ma 192,184,144
Rb 32,1,4096
Ma 196,188,144
Rb 36,1,5120
Ma 200,184,148
Rb 40,1,6144
Ma 204,188,148
buffer_load_dwordx4 v3, s[20:23], s56 offen offset:1024 sc1 lds
Rb 44,1,7168
Ma 208,184,152
Ma 212,188,152
buffer_load_dwordx4 v3, s[20:23], s56 offen offset:2048 sc1 lds
Ma 216,184,156
Ma 220,188,156
buffer_load_dwordx4 v3, s[20:23], s56 offen offset:3072 sc1 lds
s_add_u32 s56, s56, 0x10000
.LzFB:
s_waitcnt lgkmcnt(8)
Mz 64,0,64
Rb 48,1,12288
buffer_load_dword v4, v5, s[20:23], s50 offen offset:0 sc0 sc1
Mz 68,0,68
s_lshl_b64 s[60:61], s[60:61], 8
s_or_b32 s60, s60, 0xff
Mv 68,4,76
Rb 52,1,13312
s_add_u32 m0, s36, 65536
Mv 64,4,72
buffer_load_dwordx4 v2, s[20:23], s56 offen offset:0 sc1 lds
Mv 64,8,80
Rb 56,1,14336
s_add_u32 s55, s55, 0x10000
Cv 192,192,193
Mv 68,8,84
buffer_store_dwordx4 v[132:135], v2, s[20:23], s46 offen offset:0 sc0 sc1
s_cmp_eq_u32 s57, 2
s_cselect_b32 s55, s59, s55
Mv 68,12,92
Rb 60,1,15360
s_sub_u32 s57, s57, 1
s_waitcnt lgkmcnt(8)
Mv 64,12,88
Cv 193,194,195
Cv 194,196,197
)" R"(
Cv 195,198,199
v_accvgpr_write_b32 a240, v192
Mv 64,16,96
Rb 0,1,16384
v_accvgpr_write_b32 a241, v193
v_accvgpr_write_b32 a242, v194
Mv 68,16,100
v_accvgpr_write_b32 a243, v195
Cv 200,200,201
Cv 201,202,203
Cv 202,204,205
Mv 68,20,108
Rb 4,1,17408
Cv 203,206,207
v_accvgpr_write_b32 a244, v200
Mv 64,20,104
buffer_load_dwordx4 v2, s[20:23], s56 offen offset:1024 sc1 lds
v_accvgpr_write_b32 a245, v201
v_accvgpr_write_b32 a246, v202
Mv 64,24,112
Rb 8,1,18432
v_accvgpr_write_b32 a247, v203
Cv 208,208,209
Cv 209,210,211
Mv 68,24,116
Cv 210,212,213
Cv 211,214,215
v_accvgpr_write_b32 a248, v208
Mv 68,28,124
Rb 12,1,19456
v_accvgpr_write_b32 a249, v209
v_accvgpr_write_b32 a250, v210
s_waitcnt lgkmcnt(8)
Mv 64,28,120
v_accvgpr_write_b32 a251, v211
Cv 216,216,217
Cv 217,218,219
Mz 72,32,64
Rb 16,1,24576
Cv 218,220,221
Cv 219,222,223
v_accvgpr_write_b32 a252, v216
Mz 76,32,68
v_accvgpr_write_b32 a253, v217
v_accvgpr_write_b32 a254, v218
v_accvgpr_write_b32 a255, v219
Mv 76,36,76
Rb 20,1,25600
Cv 224,224,225
Cv 225,226,227
Cv 226,228,229
Mv 72,36,72
buffer_load_dwordx4 v2, s[20:23], s56 offen offset:2048 sc1 lds
Cv 227,230,231
v_accvgpr_write_b32 a128, v224
Mv 72,40,80
Rb 24,1,26624
v_accvgpr_write_b32 a129, v225
v_accvgpr_write_b32 a130, v226
Mv 76,40,84
buffer_store_dwordx4 v[140:143], v2, s[20:23], s46 offen offset:1024 sc0 sc1
v_accvgpr_write_b32 a131, v227
Cv 232,232,233
Mv 76,44,92
)" R"(
Rb 28,1,27648
Cv 233,234,235
s_waitcnt lgkmcnt(8)
Mv 72,44,88
Cv 234,236,237
Cv 235,238,239
v_accvgpr_write_b32 a132, v232
v_accvgpr_write_b32 a133, v233
Mv 72,48,96
Rb 32,1,20480
v_accvgpr_write_b32 a134, v234
v_accvgpr_write_b32 a135, v235
Mv 76,48,100
Cv 240,240,241
Cv 241,242,243
Cv 242,244,245
Cv 243,246,247
Mv 76,52,108
Rb 36,1,21504
v_accvgpr_write_b32 a136, v240
v_accvgpr_write_b32 a137, v241
Mv 72,52,104
buffer_load_dwordx4 v2, s[20:23], s56 offen offset:3072 sc1 lds
v_accvgpr_write_b32 a138, v242
v_accvgpr_write_b32 a139, v243
Mv 72,56,112
Rb 40,1,22528
Cv 248,248,249
Cv 249,250,251
Cv 250,252,253
Mv 76,56,116
Cv 251,254,255
v_accvgpr_write_b32 a140, v248
v_accvgpr_write_b32 a141, v249
Mv 76,60,124
Rb 44,1,23552
v_accvgpr_write_b32 a142, v250
v_accvgpr_write_b32 a143, v251
s_waitcnt lgkmcnt(8)
Mv 72,60,120
v_accvgpr_read_b32 v160, a192
v_accvgpr_read_b32 v28, a193
Cv 160,160,28
Mz 80,0,64
Rb 48,1,28672
v_accvgpr_read_b32 v161, a194
v_accvgpr_read_b32 v29, a195
Cv 161,161,29
Mz 84,0,68
v_accvgpr_read_b32 v162, a196
v_accvgpr_read_b32 v28, a197
Cv 162,162,28
Mv 84,4,76
Rb 52,1,29696
s_add_u32 m0, s36, 69632
v_accvgpr_read_b32 v163, a198
v_accvgpr_read_b32 v29, a199
Mv 80,4,72
buffer_load_dwordx4 v3, s[20:23], s56 offen offset:0 sc1 lds
Cv 163,163,29
v_accvgpr_write_b32 a144, v160
Mv 80,8,80
Rb 56,1,30720
v_accvgpr_write_b32 a145, v161
)" R"(
v_accvgpr_write_b32 a146, v162
Mv 84,8,84
buffer_store_dwordx4 v[148:151], v2, s[20:23], s46 offen offset:2048 sc0 sc1
v_accvgpr_write_b32 a147, v163
v_accvgpr_read_b32 v168, a200
Mv 84,12,92
Rb 60,1,31744
v_accvgpr_read_b32 v28, a201
s_waitcnt lgkmcnt(8)
Mv 80,12,88
Cv 168,168,28
v_accvgpr_read_b32 v169, a202
v_accvgpr_read_b32 v29, a203
Cv 169,169,29
Mv 80,16,96
v_accvgpr_read_b32 v170, a204
v_accvgpr_read_b32 v28, a205
Cv 170,170,28
Mv 84,16,100
v_accvgpr_read_b32 v171, a206
v_accvgpr_read_b32 v29, a207
Cv 171,171,29
v_accvgpr_write_b32 a148, v168
Mv 84,20,108
v_accvgpr_write_b32 a149, v169
v_accvgpr_write_b32 a150, v170
v_accvgpr_write_b32 a151, v171
Mv 80,20,104
buffer_load_dwordx4 v3, s[20:23], s56 offen offset:1024 sc1 lds
v_accvgpr_read_b32 v176, a208
v_accvgpr_read_b32 v28, a209
Mv 80,24,112
Cv 176,176,28
v_accvgpr_read_b32 v177, a210
v_accvgpr_read_b32 v29, a211
Cv 177,177,29
Mv 84,24,116
v_accvgpr_read_b32 v178, a212
v_accvgpr_read_b32 v28, a213
Cv 178,178,28
Mv 84,28,124
v_accvgpr_read_b32 v179, a214
v_accvgpr_read_b32 v29, a215
Cv 179,179,29
v_accvgpr_write_b32 a152, v176
Mv 80,28,120
s_waitcnt vmcnt(9) lgkmcnt(0)
s_barrier
buffer_store_dword v6, v7, s[20:23], s49 offen offset:0 sc0 sc1
Mz 88,32,64
Rb 0,1,32768
v_accvgpr_write_b32 a153, v177
v_accvgpr_write_b32 a154, v178
v_accvgpr_write_b32 a155, v179
Mz 92,32,68
Rb 4,1,33792
v_accvgpr_read_b32 v184, a216
v_accvgpr_read_b32 v28, a217
Mv 92,36,76
Rb 8,1,34816
Cv 184,184,28
v_accvgpr_read_b32 v185, a218
v_accvgpr_read_b32 v29, a219
Mv 88,36,72
Rb 12,1,35840
)" R"(
buffer_load_dwordx4 v3, s[20:23], s56 offen offset:2048 sc1 lds
Cv 185,185,29
Mv 88,40,80
Rb 16,1,40960
v_accvgpr_read_b32 v186, a220
v_accvgpr_read_b32 v28, a221
Mv 92,40,84
Rb 20,1,41984
buffer_store_dwordx4 v[156:159], v2, s[20:23], s46 offen offset:3072 sc0 sc1
Cv 186,186,28
Mv 92,44,92
Rb 24,1,43008
v_accvgpr_read_b32 v187, a222
v_accvgpr_read_b32 v29, a223
Mv 88,44,88
Rb 28,1,44032
Cv 187,187,29
v_accvgpr_write_b32 a156, v184
v_accvgpr_write_b32 a157, v185
Mv 88,48,96
Rb 32,1,36864
v_accvgpr_write_b32 a158, v186
v_accvgpr_write_b32 a159, v187
Mv 92,48,100
Rb 36,1,37888
v_mov_b32 v16, 0
v_mov_b32 v17, 0
v_mov_b32 v18, 0
Mv 92,52,108
Rb 40,1,38912
v_mov_b32 v19, 0
v_mov_b32 v20, 0
Mv 88,52,104
Rb 44,1,39936
buffer_load_dwordx4 v3, s[20:23], s56 offen offset:3072 sc1 lds
v_mov_b32 v21, 0
Mv 88,56,112
v_mov_b32 v22, 0
v_cmp_eq_u32_e32 vcc, s3, v4
s_andn2_b64 s[28:29], s[60:61], vcc
s_cbranch_scc1 .Lzsp14
.Lzok14:
Mv 92,56,116
s_add_u32 s56, s56, 0x10000
v_mov_b32 v23, 0
Mv 92,60,124
s_waitcnt lgkmcnt(8)
Mv 88,60,120
v_exp_f32_e32 v32, v64
Mz 96,0,64
Rb 48,1,45056
buffer_load_dword v4, v5, s[20:23], s50 offen offset:0 sc0 sc1
Mz 100,0,68
s_lshl_b64 s[60:61], s[60:61], 8
s_or_b32 s60, s60, 0xff
Mv 100,4,76
Rb 52,1,46080
s_add_u32 m0, s36, 0
Mv 96,4,72
buffer_load_dwordx4 v2, s[20:23], s55 offen offset:0 sc1 lds
Mv 96,8,80
Rb 56,1,47104
v_exp_f32_e32 v24, v65
Mv 100,8,84
)" R"(
buffer_store_dwordx4 v[128:131], v3, s[20:23], s46 offen offset:0 sc0 sc1
v_add_f32_e32 v14, v32, v24
Mv 100,12,92
Rb 60,1,48128
Cv 32,32,24
s_waitcnt lgkmcnt(8)
Mv 96,12,88
v_exp_f32_e32 v33, v66
Mv 96,16,96
Rb 0,1,49152
v_exp_f32_e32 v25, v67
Mv 100,16,100
v_exp_f32_e32 v34, v72
v_exp_f32_e32 v26, v73
Mv 100,20,108
Rb 4,1,50176
v_exp_f32_e32 v35, v74
Mv 96,20,104
buffer_load_dwordx4 v2, s[20:23], s55 offen offset:1024 sc1 lds
v_add_f32_e32 v14, v14, v33
Mv 96,24,112
Rb 8,1,51200
v_exp_f32_e32 v27, v75
Mv 100,24,116
v_exp_f32_e32 v36, v68
v_add_f32_e32 v14, v14, v25
Mv 100,28,124
Rb 12,1,52224
v_exp_f32_e32 v28, v69
s_waitcnt lgkmcnt(8)
Mv 96,28,120
v_exp_f32_e32 v37, v70
Cv 33,33,25
Mz 104,32,64
Rb 16,1,57344
v_exp_f32_e32 v29, v71
Mz 108,32,68
v_exp_f32_e32 v38, v76
v_add_f32_e32 v14, v14, v34
Mv 108,36,76
Rb 20,1,58368
v_exp_f32_e32 v24, v77
Mv 104,36,72
buffer_load_dwordx4 v2, s[20:23], s55 offen offset:2048 sc1 lds
v_add_f32_e32 v14, v14, v26
Mv 104,40,80
Rb 24,1,59392
v_exp_f32_e32 v39, v78
Cv 34,34,26
Mv 108,40,84
buffer_store_dwordx4 v[136:139], v3, s[20:23], s46 offen offset:1024 sc0 sc1
v_add_f32_e32 v14, v14, v35
Mv 108,44,92
Rb 28,1,60416
v_add_f32_e32 v14, v14, v27
s_waitcnt lgkmcnt(8)
Mv 104,44,88
v_exp_f32_e32 v25, v79
Cv 35,35,27
Mv 104,48,96
Rb 32,1,53248
v_exp_f32_e32 v40, v80
Mv 108,48,100
v_exp_f32_e32 v26, v81
)" R"(
v_exp_f32_e32 v41, v82
Mv 108,52,108
Rb 36,1,54272
v_exp_f32_e32 v27, v83
Mv 104,52,104
buffer_load_dwordx4 v2, s[20:23], s55 offen offset:3072 sc1 lds
v_add_f32_e32 v15, v36, v28
Mv 104,56,112
Rb 40,1,55296
Cv 36,36,28
v_add_f32_e32 v15, v15, v37
Mv 108,56,116
v_exp_f32_e32 v42, v88
v_add_f32_e32 v15, v15, v29
Mv 108,60,124
Rb 44,1,56320
v_exp_f32_e32 v28, v89
s_waitcnt lgkmcnt(8)
Mv 104,60,120
Cv 37,37,29
v_exp_f32_e32 v43, v90
Mz 112,0,64
Rb 48,1,61440
v_add_f32_e32 v15, v15, v38
v_add_f32_e32 v15, v15, v24
Mz 116,0,68
v_exp_f32_e32 v29, v91
Cv 38,38,24
Mv 116,4,76
Rb 52,1,62464
s_add_u32 m0, s36, 4096
v_add_f32_e32 v15, v15, v39
Mv 112,4,72
buffer_load_dwordx4 v3, s[20:23], s55 offen offset:0 sc1 lds
v_add_f32_e32 v15, v15, v25
Mv 112,8,80
Rb 56,1,63488
v_exp_f32_e32 v44, v84
Cv 39,39,25
Mv 116,8,84
buffer_store_dwordx4 v[144:147], v3, s[20:23], s46 offen offset:2048 sc0 sc1
v_add_f32_e32 v14, v14, v40
Mv 116,12,92
Rb 60,1,64512
v_add_f32_e32 v14, v14, v26
s_waitcnt lgkmcnt(8)
Mv 112,12,88
v_exp_f32_e32 v24, v85
Cv 40,40,26
Mv 112,16,96
v_exp_f32_e32 v45, v86
v_add_f32_e32 v14, v14, v41
Mv 116,16,100
v_exp_f32_e32 v25, v87
v_exp_f32_e32 v46, v92
Mv 116,20,108
v_exp_f32_e32 v26, v93
v_add_f32_e32 v14, v14, v27
Mv 112,20,104
buffer_load_dwordx4 v3, s[20:23], s55 offen offset:1024 sc1 lds
Cv 41,41,27
Mv 112,24,112
v_exp_f32_e32 v47, v94
v_add_f32_e32 v14, v14, v42
)" R"(
Mv 116,24,116
v_exp_f32_e32 v27, v95
v_add_f32_e32 v14, v14, v28
Mv 116,28,124
Cv 42,42,28
v_add_f32_e32 v14, v14, v43
v_add_f32_e32 v14, v14, v29
Cv 43,43,29
Mv 112,28,120
v_add_f32_e32 v15, v15, v44
s_waitcnt lgkmcnt(0)
s_barrier
Mz 120,32,64
Rb 0,8,32768
v_add_f32_e32 v15, v15, v24
Cv 44,44,24
Mz 124,32,68
Rb 4,8,33792
v_add_f32_e32 v15, v15, v45
v_add_f32_e32 v15, v15, v25
Mv 124,36,76
Rb 8,8,34816
Cv 45,45,25
v_add_f32_e32 v15, v15, v46
Mv 120,36,72
Rb 12,8,35840
buffer_load_dwordx4 v3, s[20:23], s55 offen offset:2048 sc1 lds
Mv 120,40,80
Rb 16,8,36864
v_add_f32_e32 v15, v15, v26
Cv 46,46,26
v_add_f32_e32 v15, v15, v47
Mv 124,40,84
Rb 20,8,37888
buffer_store_dwordx4 v[152:155], v3, s[20:23], s46 offen offset:3072 sc0 sc1
Mv 124,44,92
Rb 24,8,38912
v_add_f32_e32 v15, v15, v27
Cv 47,47,27
Mv 120,44,88
Rb 28,8,39936
v_max_f32_e32 v11, v14, v15
v_mov_b32 v24, v14
Mv 120,48,96
Rb 32,8,40960
v_mov_b32 v25, v15
Mv 124,48,100
Rb 36,8,41984
v_permlane32_swap_b32_e32 v24, v25
v_add_f32_e32 v24, v24, v25
v_mov_b32 v25, v24
Mv 124,52,108
Rb 40,8,43008
v_permlane16_swap_b32_e32 v24, v25
v_add_f32_e32 v24, v24, v25
Mv 120,52,104
Rb 44,8,44032
buffer_load_dwordx4 v3, s[20:23], s55 offen offset:3072 sc1 lds
Mv 120,56,112
v_rcp_f32_e32 v24, v24
s_add_u32 s55, s55, 0x10000
Mv 124,56,116
s_cmp_eq_u32 s57, 2
s_cselect_b32 s55, s59, s55
v_max_f32_e32 v11, v11, v24
)" R"(
Mv 124,60,124
v_cmp_nge_f32_e32 vcc, s62, v11
s_waitcnt lgkmcnt(8)
s_cbranch_vccnz .Lzfx6
.Lzfr6:
Mv 120,60,120
v_mov_b32 v12, v14
v_mov_b32 v13, v15
v_mfma_f32_16x16x32_bf16 v[128:131], a[0:3], v[32:35], 0
Rb 48,8,45056
v_mfma_f32_16x16x32_bf16 v[136:139], a[0:3], v[36:39], 0
v_mfma_f32_16x16x32_bf16 v[140:143], a[4:7], v[36:39], 0
Rb 52,8,46080
s_add_u32 m0, s36, 32768
v_mfma_f32_16x16x32_bf16 v[132:135], a[4:7], v[32:35], 0
buffer_load_dwordx4 v2, s[20:23], s55 offen offset:0 sc1 lds
v_mfma_f32_16x16x32_bf16 v[144:147], a[8:11], v[32:35], 0
Rb 56,8,47104
v_mfma_f32_16x16x32_bf16 v[152:155], a[8:11], v[36:39], 0
v_exp_f32_e32 v48, v96
v_mfma_f32_16x16x32_bf16 v[156:159], a[12:15], v[36:39], 0
Rb 60,8,48128
s_waitcnt lgkmcnt(8)
v_mfma_f32_16x16x32_bf16 v[148:151], a[12:15], v[32:35], 0
v_exp_f32_e32 v28, v97
v_mfma_f32_16x16x32_bf16 v[160:163], a[16:19], v[32:35], 0
Rb 0,8,49152
v_add_f32_e32 v14, v48, v28
v_mfma_f32_16x16x32_bf16 v[168:171], a[16:19], v[36:39], 0
v_exp_f32_e32 v49, v98
v_mfma_f32_16x16x32_bf16 v[172:175], a[20:23], v[36:39], 0
Rb 4,8,50176
Cv 48,48,28
v_mfma_f32_16x16x32_bf16 v[164:167], a[20:23], v[32:35], 0
buffer_load_dwordx4 v2, s[20:23], s55 offen offset:1024 sc1 lds
v_mfma_f32_16x16x32_bf16 v[176:179], a[24:27], v[32:35], 0
Rb 8,8,51200
v_add_f32_e32 v14, v14, v49
v_mfma_f32_16x16x32_bf16 v[184:187], a[24:27], v[36:39], 0
v_exp_f32_e32 v29, v99
v_mfma_f32_16x16x32_bf16 v[188:191], a[28:31], v[36:39], 0
Rb 12,8,52224
s_waitcnt lgkmcnt(8)
v_mfma_f32_16x16x32_bf16 v[180:183], a[28:31], v[32:35], 0
v_exp_f32_e32 v50, v104
v_mfma_f32_16x16x32_bf16 v[192:195], a[32:35], v[32:35], 0
Rb 16,8,53248
v_add_f32_e32 v14, v14, v29
v_mfma_f32_16x16x32_bf16 v[200:203], a[32:35], v[36:39], 0
v_exp_f32_e32 v24, v105
v_mfma_f32_16x16x32_bf16 v[204:207], a[36:39], v[36:39], 0
Rb 20,8,54272
Cv 49,49,29
v_mfma_f32_16x16x32_bf16 v[196:199], a[36:39], v[32:35], 0
buffer_load_dwordx4 v2, s[20:23], s55 offen offset:2048 sc1 lds
v_mfma_f32_16x16x32_bf16 v[208:211], a[40:43], v[32:35], 0
Rb 24,8,55296
v_add_f32_e32 v14, v14, v50
v_mfma_f32_16x16x32_bf16 v[216:219], a[40:43], v[36:39], 0
v_exp_f32_e32 v51, v106
v_mfma_f32_16x16x32_bf16 v[220:223], a[44:47], v[36:39], 0
Rb 28,8,56320
s_waitcnt lgkmcnt(8)
v_mfma_f32_16x16x32_bf16 v[212:215], a[44:47], v[32:35], 0
)" R"(
v_exp_f32_e32 v25, v107
v_mfma_f32_16x16x32_bf16 v[224:227], a[48:51], v[32:35], 0
Rb 32,8,57344
v_add_f32_e32 v14, v14, v24
v_mfma_f32_16x16x32_bf16 v[232:235], a[48:51], v[36:39], 0
v_exp_f32_e32 v52, v100
v_mfma_f32_16x16x32_bf16 v[236:239], a[52:55], v[36:39], 0
Rb 36,8,58368
Cv 50,50,24
v_mfma_f32_16x16x32_bf16 v[228:231], a[52:55], v[32:35], 0
buffer_load_dwordx4 v2, s[20:23], s55 offen offset:3072 sc1 lds
v_mfma_f32_16x16x32_bf16 v[240:243], a[56:59], v[32:35], 0
Rb 40,8,59392
v_add_f32_e32 v14, v14, v51
v_mfma_f32_16x16x32_bf16 v[248:251], a[56:59], v[36:39], 0
v_exp_f32_e32 v26, v101
v_mfma_f32_16x16x32_bf16 v[252:255], a[60:63], v[36:39], 0
Rb 44,8,60416
s_waitcnt lgkmcnt(8)
v_mfma_f32_16x16x32_bf16 v[244:247], a[60:63], v[32:35], 0
v_exp_f32_e32 v53, v102
Mp 128,0,40
Rb 48,8,61440
v_add_f32_e32 v14, v14, v25
Mp 136,0,44
v_exp_f32_e32 v27, v103
Mp 140,4,44
Rb 52,8,62464
s_add_u32 m0, s36, 36864
Mp 132,4,40
buffer_load_dwordx4 v3, s[20:23], s55 offen offset:0 sc1 lds
Mp 144,8,40
Rb 56,8,63488
Cv 51,51,25
Mp 152,8,44
v_exp_f32_e32 v54, v108
Mp 156,12,44
Rb 60,8,64512
s_waitcnt lgkmcnt(8)
Mp 148,12,40
v_exp_f32_e32 v28, v109
Mp 160,16,40
v_exp_f32_e32 v55, v110
Mp 168,16,44
v_exp_f32_e32 v29, v111
Mp 172,20,44
v_add_f32_e32 v15, v52, v26
Cv 52,52,26
Mp 164,20,40
buffer_load_dwordx4 v3, s[20:23], s55 offen offset:1024 sc1 lds
Mp 176,24,40
v_add_f32_e32 v15, v15, v53
v_add_f32_e32 v15, v15, v27
Mp 184,24,44
Cv 53,53,27
v_add_f32_e32 v15, v15, v54
Mp 188,28,44
v_add_f32_e32 v15, v15, v28
Cv 54,54,28
Mp 180,28,40
s_waitcnt vmcnt(6) lgkmcnt(0)
s_barrier
buffer_store_dword v6, v7, s[20:23], s49 offen offset:4 sc0 sc1
Mp 192,32,40
Rb 0,1,0
)" R"(
v_add_f32_e32 v15, v15, v55
Mp 200,32,44
Rb 4,1,1024
v_add_f32_e32 v15, v15, v29
Mp 204,36,44
Rb 8,1,2048
Cv 55,55,29
Mp 196,36,40
Rb 12,1,3072
buffer_load_dwordx4 v3, s[20:23], s55 offen offset:2048 sc1 lds
Mp 208,40,40
Rb 16,1,8192
s_sub_u32 s57, s57, 1
Mp 216,40,44
Rb 20,1,9216
Mp 220,44,44
Rb 24,1,10240
Mp 212,44,40
Rb 28,1,11264
Mp 224,48,40
Rb 32,1,4096
Mp 232,48,44
Rb 36,1,5120
Mp 236,52,44
Rb 40,1,6144
Mp 228,52,40
Rb 44,1,7168
buffer_load_dwordx4 v3, s[20:23], s55 offen offset:3072 sc1 lds
Mp 240,56,40
v_cmp_eq_u32_e32 vcc, s3, v4
s_andn2_b64 s[28:29], s[60:61], vcc
s_cbranch_scc1 .Lzsp15
.Lzok15:
Mp 248,56,44
Mp 252,60,44
s_waitcnt lgkmcnt(8)
Mp 244,60,40
v_mfma_f32_16x16x32_bf16 v[64:67], a[0:3], a[64:67], v[16:19]
Rb 48,1,12288
v_mfma_f32_16x16x32_bf16 v[68:71], a[0:3], a[68:71], v[20:23]
v_exp_f32_e32 v56, v112
Mv 68,4,76
Rb 52,1,13312
s_add_u32 m0, s36, 98304
Mv 64,4,72
buffer_load_dwordx4 v2, s[20:23], s56 offen offset:0 sc1 lds
Mv 64,8,80
Rb 56,1,14336
v_add_f32_e32 v14, v14, v56
Mv 68,8,84
buffer_store_dwordx4 a[224:227], v2, s[20:23], s47 offen offset:0 sc0 sc1
Mv 68,12,92
Rb 60,1,15360
s_waitcnt lgkmcnt(8)
Mv 64,12,88
v_exp_f32_e32 v24, v113
Mv 64,16,96
Rb 0,1,16384
v_add_f32_e32 v14, v14, v24
Mv 68,16,100
v_exp_f32_e32 v57, v114
Mv 68,20,108
Rb 4,1,17408
Cv 56,56,24
)" R"(
Mv 64,20,104
buffer_load_dwordx4 v2, s[20:23], s56 offen offset:1024 sc1 lds
Mv 64,24,112
Rb 8,1,18432
v_add_f32_e32 v14, v14, v57
Mv 68,24,116
v_exp_f32_e32 v25, v115
Mv 68,28,124
Rb 12,1,19456
s_waitcnt lgkmcnt(8)
Mv 64,28,120
v_exp_f32_e32 v58, v120
v_mfma_f32_16x16x32_bf16 v[72:75], a[32:35], a[64:67], v[16:19]
Rb 16,1,24576
v_add_f32_e32 v14, v14, v25
v_mfma_f32_16x16x32_bf16 v[76:79], a[32:35], a[68:71], v[20:23]
v_exp_f32_e32 v26, v121
Mv 76,36,76
Rb 20,1,25600
Cv 57,57,25
Mv 72,36,72
buffer_load_dwordx4 v2, s[20:23], s56 offen offset:2048 sc1 lds
Mv 72,40,80
Rb 24,1,26624
v_add_f32_e32 v14, v14, v58
Mv 76,40,84
v_exp_f32_e32 v59, v122
Mv 76,44,92
Rb 28,1,27648
s_waitcnt lgkmcnt(8)
Mv 72,44,88
v_exp_f32_e32 v27, v123
Mv 72,48,96
Rb 32,1,20480
v_add_f32_e32 v14, v14, v26
Mv 76,48,100
v_exp_f32_e32 v60, v116
Mv 76,52,108
Rb 36,1,21504
Cv 58,58,26
Mv 72,52,104
buffer_load_dwordx4 v2, s[20:23], s56 offen offset:3072 sc1 lds
Mv 72,56,112
Rb 40,1,22528
v_add_f32_e32 v14, v14, v59
Mv 76,56,116
buffer_store_dwordx4 a[228:231], v2, s[20:23], s47 offen offset:1024 sc0 sc1
Mv 76,60,124
Rb 44,1,23552
s_waitcnt lgkmcnt(8)
Mv 72,60,120
v_exp_f32_e32 v28, v117
v_mfma_f32_16x16x32_bf16 v[80:83], a[0:3], a[64:67], v[16:19]
Rb 48,1,28672
v_add_f32_e32 v14, v14, v27
v_mfma_f32_16x16x32_bf16 v[84:87], a[0:3], a[68:71], v[20:23]
v_exp_f32_e32 v61, v118
Mv 84,4,76
Rb 52,1,29696
s_add_u32 m0, s36, 102400
Mv 80,4,72
buffer_load_dwordx4 v3, s[20:23], s56 offen offset:0 sc1 lds
Mv 80,8,80
Rb 56,1,30720
)" R"(
Cv 59,59,27
Mv 84,8,84
v_exp_f32_e32 v29, v119
Mv 84,12,92
Rb 60,1,31744
s_waitcnt lgkmcnt(8)
Mv 80,12,88
v_exp_f32_e32 v62, v124
Mv 80,16,96
v_exp_f32_e32 v24, v125
Mv 84,16,100
v_exp_f32_e32 v63, v126
Mv 84,20,108
v_exp_f32_e32 v25, v127
Mv 80,20,104
buffer_load_dwordx4 v3, s[20:23], s56 offen offset:1024 sc1 lds
Mv 80,24,112
v_add_f32_e32 v15, v15, v60
v_add_f32_e32 v15, v15, v28
Mv 84,24,116
Cv 60,60,28
v_add_f32_e32 v15, v15, v61
Mv 84,28,124
v_add_f32_e32 v15, v15, v29
Cv 61,61,29
Mv 80,28,120
s_waitcnt lgkmcnt(0)
s_barrier
v_mfma_f32_16x16x32_bf16 v[88:91], a[32:35], a[64:67], v[16:19]
Rb 0,8,0
v_add_f32_e32 v15, v15, v62
v_mfma_f32_16x16x32_bf16 v[92:95], a[32:35], a[68:71], v[20:23]
Rb 4,8,1024
v_add_f32_e32 v15, v15, v24
Mv 92,36,76
Rb 8,8,2048
Cv 62,62,24
Mv 88,36,72
Rb 12,8,3072
buffer_load_dwordx4 v3, s[20:23], s56 offen offset:2048 sc1 lds
Mv 88,40,80
Rb 16,8,4096
v_add_f32_e32 v15, v15, v63
Mv 92,40,84
Rb 20,8,5120
buffer_store_dwordx4 a[232:235], v2, s[20:23], s47 offen offset:2048 sc0 sc1
Mv 92,44,92
Rb 24,8,6144
v_add_f32_e32 v15, v15, v25
Mv 88,44,88
Rb 28,8,7168
Cv 63,63,25
Mv 88,48,96
Rb 32,8,8192
v_max_f32_e32 v11, v14, v15
Mv 92,48,100
Rb 36,8,9216
v_cmp_nge_f32_e32 vcc, s62, v11
Mv 92,52,108
Rb 40,8,10240
s_add_u32 s55, s55, 0x10000
Mv 88,52,104
Rb 44,8,11264
buffer_load_dwordx4 v3, s[20:23], s56 offen offset:3072 sc1 lds
Mv 88,56,112
)" R"(
s_cmp_eq_u32 s57, 2
s_cselect_b32 s55, s59, s55
Mv 92,56,116
s_add_u32 s56, s56, 0x10000
Mv 92,60,124
s_waitcnt lgkmcnt(8)
s_cbranch_vccnz .Lzfx7
.Lzfr7:
Mv 88,60,120
v_add_f32_e32 v12, v12, v14
v_add_f32_e32 v13, v13, v15
Mp 128,0,48
Rb 48,8,12288
Mp 136,0,52
s_cmp_le_i32 s57, s58
s_cbranch_scc1 .Lzmk6
.Lzmr6:
Mp 140,4,52
Rb 52,8,13312
s_add_u32 m0, s36, 0
Mp 132,4,48
buffer_load_dwordx4 v2, s[20:23], s55 offen offset:0 sc1 lds
Mp 144,8,48
Rb 56,8,14336
Mp 152,8,52
v_exp_f32_e32 v32, v64
Mp 156,12,52
Rb 60,8,15360
s_waitcnt lgkmcnt(8)
Mp 148,12,48
v_exp_f32_e32 v26, v65
Mp 160,16,48
Rb 0,8,16384
v_add_f32_e32 v14, v32, v26
Mp 168,16,52
v_exp_f32_e32 v33, v66
Mp 172,20,52
Rb 4,8,17408
Cv 32,32,26
Mp 164,20,48
buffer_load_dwordx4 v2, s[20:23], s55 offen offset:1024 sc1 lds
Mp 176,24,48
Rb 8,8,18432
v_add_f32_e32 v14, v14, v33
Mp 184,24,52
buffer_store_dwordx4 a[236:239], v2, s[20:23], s47 offen offset:3072 sc0 sc1
Mp 188,28,52
Rb 12,8,19456
s_waitcnt lgkmcnt(8)
Mp 180,28,48
v_exp_f32_e32 v27, v67
Mp 192,32,48
Rb 16,8,20480
v_add_f32_e32 v14, v14, v27
Mp 200,32,52
v_exp_f32_e32 v34, v72
Mp 204,36,52
Rb 20,8,21504
Cv 33,33,27
Mp 196,36,48
buffer_load_dwordx4 v2, s[20:23], s55 offen offset:2048 sc1 lds
Mp 208,40,48
Rb 24,8,22528
v_add_f32_e32 v14, v14, v34
)" R"(
Mp 216,40,52
v_exp_f32_e32 v28, v73
Mp 220,44,52
Rb 28,8,23552
s_waitcnt lgkmcnt(8)
Mp 212,44,48
v_exp_f32_e32 v35, v74
Mp 224,48,48
Rb 32,8,24576
v_add_f32_e32 v14, v14, v28
Mp 232,48,52
v_exp_f32_e32 v29, v75
Mp 236,52,52
Rb 36,8,25600
Cv 34,34,28
Mp 228,52,48
buffer_load_dwordx4 v2, s[20:23], s55 offen offset:3072 sc1 lds
Mp 240,56,48
Rb 40,8,26624
v_add_f32_e32 v14, v14, v35
Mp 248,56,52
v_exp_f32_e32 v36, v68
Mp 252,60,52
Rb 44,8,27648
s_waitcnt lgkmcnt(8)
Mp 244,60,48
v_exp_f32_e32 v24, v69
Mp 128,0,56
Rb 48,8,28672
v_add_f32_e32 v14, v14, v29
Mp 136,0,60
v_exp_f32_e32 v37, v70
Mp 140,4,60
Rb 52,8,29696
s_add_u32 m0, s36, 4096
Mp 132,4,56
buffer_load_dwordx4 v3, s[20:23], s55 offen offset:0 sc1 lds
Mp 144,8,56
Rb 56,8,30720
Cv 35,35,29
Mp 152,8,60
buffer_store_dwordx4 a[240:243], v3, s[20:23], s47 offen offset:0 sc0 sc1
Mp 156,12,60
Rb 60,8,31744
s_waitcnt lgkmcnt(8)
Mp 148,12,56
v_exp_f32_e32 v25, v71
Mp 160,16,56
v_exp_f32_e32 v38, v76
Mp 168,16,60
v_exp_f32_e32 v26, v77
Mp 172,20,60
v_exp_f32_e32 v39, v78
Mp 164,20,56
buffer_load_dwordx4 v3, s[20:23], s55 offen offset:1024 sc1 lds
Mp 176,24,56
v_exp_f32_e32 v27, v79
Mp 184,24,60
v_add_f32_e32 v15, v36, v24
Cv 36,36,24
Mp 188,28,60
v_add_f32_e32 v15, v15, v37
v_add_f32_e32 v15, v15, v25
Mp 180,28,56
)" R"(
s_waitcnt vmcnt(19) lgkmcnt(0)
s_barrier
Mp 192,32,56
Rb 0,1,32768
Cv 37,37,25
Mp 200,32,60
Rb 4,1,33792
v_add_f32_e32 v15, v15, v38
Mp 204,36,60
Rb 8,1,34816
v_add_f32_e32 v15, v15, v26
Mp 196,36,56
Rb 12,1,35840
buffer_load_dwordx4 v3, s[20:23], s55 offen offset:2048 sc1 lds
Mp 208,40,56
Rb 16,1,40960
Cv 38,38,26
Mp 216,40,60
Rb 20,1,41984
v_add_f32_e32 v15, v15, v39
Mp 220,44,60
Rb 24,1,43008
v_add_f32_e32 v15, v15, v27
Mp 212,44,56
Rb 28,1,44032
Cv 39,39,27
Mp 224,48,56
Rb 32,1,36864
s_sub_u32 s57, s57, 1
Mp 232,48,60
Rb 36,1,37888
Mp 236,52,60
Rb 40,1,38912
Mp 228,52,56
Rb 44,1,39936
buffer_load_dwordx4 v3, s[20:23], s55 offen offset:3072 sc1 lds
Mp 240,56,56
Mp 248,56,60
buffer_store_dwordx4 a[244:247], v3, s[20:23], s47 offen offset:1024 sc0 sc1
Mp 252,60,60
s_waitcnt lgkmcnt(8)
Mp 244,60,56
v_mfma_f32_16x16x32_bf16 v[96:99], a[0:3], a[64:67], v[16:19]
Rb 48,1,45056
buffer_load_dword v4, v5, s[20:23], s50 offen offset:0 sc0 sc1
v_mfma_f32_16x16x32_bf16 v[100:103], a[0:3], a[68:71], v[20:23]
s_lshl_b64 s[60:61], s[60:61], 8
s_or_b32 s60, s60, 0xff
Mv 100,4,76
Rb 52,1,46080
s_add_u32 m0, s36, 65536
Mv 96,4,72
buffer_load_dwordx4 v2, s[20:23], s56 offen offset:0 sc1 lds
Mv 96,8,80
Rb 56,1,47104
Mv 100,8,84
v_exp_f32_e32 v40, v80
Mv 100,12,92
Rb 60,1,48128
s_waitcnt lgkmcnt(8)
Mv 96,12,88
v_exp_f32_e32 v28, v81
Mv 96,16,96
Rb 0,1,49152
v_add_f32_e32 v14, v14, v40
)" R"(
Mv 100,16,100
v_exp_f32_e32 v41, v82
Mv 100,20,108
Rb 4,1,50176
v_add_f32_e32 v14, v14, v28
Mv 96,20,104
buffer_load_dwordx4 v2, s[20:23], s56 offen offset:1024 sc1 lds
Mv 96,24,112
Rb 8,1,51200
Cv 40,40,28
Mv 100,24,116
v_exp_f32_e32 v29, v83
Mv 100,28,124
Rb 12,1,52224
s_waitcnt lgkmcnt(8)
Mv 96,28,120
v_exp_f32_e32 v42, v88
v_add_f32_e32 v14, v14, v41
v_mfma_f32_16x16x32_bf16 v[104:107], a[32:35], a[64:67], v[16:19]
Rb 16,1,57344
v_add_f32_e32 v14, v14, v29
v_mfma_f32_16x16x32_bf16 v[108:111], a[32:35], a[68:71], v[20:23]
v_exp_f32_e32 v24, v89
Mv 108,36,76
Rb 20,1,58368
Cv 41,41,29
Mv 104,36,72
buffer_load_dwordx4 v2, s[20:23], s56 offen offset:2048 sc1 lds
Mv 104,40,80
Rb 24,1,59392
v_add_f32_e32 v14, v14, v42
Mv 108,40,84
buffer_store_dwordx4 a[248:251], v3, s[20:23], s47 offen offset:2048 sc0 sc1
Mv 108,44,92
Rb 28,1,60416
s_waitcnt lgkmcnt(8)
Mv 104,44,88
v_exp_f32_e32 v43, v90
Mv 104,48,96
Rb 32,1,53248
v_add_f32_e32 v14, v14, v24
Mv 108,48,100
v_exp_f32_e32 v25, v91
Mv 108,52,108
Rb 36,1,54272
Cv 42,42,24
Mv 104,52,104
buffer_load_dwordx4 v2, s[20:23], s56 offen offset:3072 sc1 lds
Mv 104,56,112
Rb 40,1,55296
v_add_f32_e32 v14, v14, v43
Mv 108,56,116
v_exp_f32_e32 v44, v84
Mv 108,60,124
Rb 44,1,56320
s_waitcnt lgkmcnt(8)
Mv 104,60,120
v_exp_f32_e32 v26, v85
v_add_f32_e32 v14, v14, v25
v_mfma_f32_16x16x32_bf16 v[112:115], a[0:3], a[64:67], v[16:19]
Rb 48,1,61440
Cv 43,43,25
v_mfma_f32_16x16x32_bf16 v[116:119], a[0:3], a[68:71], v[20:23]
v_exp_f32_e32 v45, v86
)" R"(
Mv 116,4,76
Rb 52,1,62464
s_add_u32 m0, s36, 69632
Mv 112,4,72
buffer_load_dwordx4 v3, s[20:23], s56 offen offset:0 sc1 lds
Mv 112,8,80
Rb 56,1,63488
v_add_f32_e32 v15, v15, v44
Mv 116,8,84
v_exp_f32_e32 v27, v87
Mv 116,12,92
Rb 60,1,64512
s_waitcnt lgkmcnt(8)
Mv 112,12,88
v_exp_f32_e32 v46, v92
Mv 112,16,96
v_exp_f32_e32 v28, v93
Mv 116,16,100
v_exp_f32_e32 v47, v94
Mv 116,20,108
v_exp_f32_e32 v29, v95
Mv 112,20,104
buffer_load_dwordx4 v3, s[20:23], s56 offen offset:1024 sc1 lds
Mv 112,24,112
v_add_f32_e32 v15, v15, v26
Cv 44,44,26
Mv 116,24,116
buffer_store_dwordx4 a[252:255], v3, s[20:23], s47 offen offset:3072 sc0 sc1
Mv 116,28,124
v_add_f32_e32 v15, v15, v45
v_add_f32_e32 v15, v15, v27
Mv 112,28,120
Cv 45,45,27
s_waitcnt vmcnt(20) lgkmcnt(0)
s_barrier
v_mfma_f32_16x16x32_bf16 v[120:123], a[32:35], a[64:67], v[16:19]
Rb 0,8,32768
v_add_f32_e32 v15, v15, v46
v_mfma_f32_16x16x32_bf16 v[124:127], a[32:35], a[68:71], v[20:23]
Rb 4,8,33792
v_add_f32_e32 v15, v15, v28
Mv 124,36,76
Rb 8,8,34816
Cv 46,46,28
Mv 120,36,72
Rb 12,8,35840
buffer_load_dwordx4 v3, s[20:23], s56 offen offset:2048 sc1 lds
Mv 120,40,80
Rb 16,8,36864
v_add_f32_e32 v15, v15, v47
Mv 124,40,84
Rb 20,8,37888
v_add_f32_e32 v15, v15, v29
Mv 124,44,92
Rb 24,8,38912
Cv 47,47,29
Mv 120,44,88
Rb 28,8,39936
v_max_f32_e32 v11, v14, v15
Mv 120,48,96
Rb 32,8,40960
v_cmp_nge_f32_e32 vcc, s62, v11
Mv 124,48,100
Rb 36,8,41984
s_add_u32 s55, s55, 0x10000
)" R"(
Mv 124,52,108
Rb 40,8,43008
Mv 120,52,104
Rb 44,8,44032
buffer_load_dwordx4 v3, s[20:23], s56 offen offset:3072 sc1 lds
Mv 120,56,112
s_cmp_eq_u32 s57, 2
s_cselect_b32 s55, s59, s55
Mv 124,56,116
s_add_u32 s56, s56, 0x10000
Mv 124,60,124
s_waitcnt lgkmcnt(8)
s_cbranch_vccnz .Lzfx8
.Lzfr8:
Mv 120,60,120
v_add_f32_e32 v12, v12, v14
v_add_f32_e32 v13, v13, v15
Mp 128,0,32
Rb 48,8,45056
Mp 136,0,36
s_cmp_le_i32 s57, s58
s_cbranch_scc1 .Lzmk7
.Lzmr7:
Mp 140,4,36
Rb 52,8,46080
s_add_u32 m0, s36, 32768
Mp 132,4,32
buffer_load_dwordx4 v2, s[20:23], s55 offen offset:0 sc1 lds
Mp 144,8,32
Rb 56,8,47104
Mp 152,8,36
v_exp_f32_e32 v48, v96
Mp 156,12,36
Rb 60,8,48128
s_waitcnt lgkmcnt(8)
Mp 148,12,32
v_exp_f32_e32 v24, v97
Mp 160,16,32
Rb 0,8,49152
v_add_f32_e32 v14, v48, v24
Mp 168,16,36
v_exp_f32_e32 v49, v98
Mp 172,20,36
Rb 4,8,50176
Cv 48,48,24
Mp 164,20,32
buffer_load_dwordx4 v2, s[20:23], s55 offen offset:1024 sc1 lds
Mp 176,24,32
Rb 8,8,51200
v_add_f32_e32 v14, v14, v49
Mp 184,24,36
v_exp_f32_e32 v25, v99
Mp 188,28,36
Rb 12,8,52224
s_waitcnt lgkmcnt(8)
Mp 180,28,32
v_exp_f32_e32 v50, v104
Mp 192,32,32
Rb 16,8,53248
v_add_f32_e32 v14, v14, v25
Mp 200,32,36
v_exp_f32_e32 v26, v105
Mp 204,36,36
Rb 20,8,54272
)" R"(
Cv 49,49,25
Mp 196,36,32
buffer_load_dwordx4 v2, s[20:23], s55 offen offset:2048 sc1 lds
Mp 208,40,32
Rb 24,8,55296
v_add_f32_e32 v14, v14, v50
Mp 216,40,36
v_exp_f32_e32 v51, v106
Mp 220,44,36
Rb 28,8,56320
s_waitcnt lgkmcnt(8)
Mp 212,44,32
v_exp_f32_e32 v27, v107
Mp 224,48,32
Rb 32,8,57344
v_add_f32_e32 v14, v14, v26
Mp 232,48,36
v_exp_f32_e32 v52, v100
Mp 236,52,36
Rb 36,8,58368
Cv 50,50,26
Mp 228,52,32
buffer_load_dwordx4 v2, s[20:23], s55 offen offset:3072 sc1 lds
Mp 240,56,32
Rb 40,8,59392
v_add_f32_e32 v14, v14, v51
Mp 248,56,36
v_exp_f32_e32 v28, v101
Mp 252,60,36
Rb 44,8,60416
s_waitcnt lgkmcnt(8)
Mp 244,60,32
v_exp_f32_e32 v53, v102
Mp 128,0,40
Rb 48,8,61440
v_add_f32_e32 v14, v14, v27
Mp 136,0,44
v_exp_f32_e32 v29, v103
Mp 140,4,44
Rb 52,8,62464
s_add_u32 m0, s36, 36864
Mp 132,4,40
buffer_load_dwordx4 v3, s[20:23], s55 offen offset:0 sc1 lds
Mp 144,8,40
Rb 56,8,63488
Cv 51,51,27
Mp 152,8,44
v_exp_f32_e32 v54, v108
Mp 156,12,44
Rb 60,8,64512
s_waitcnt lgkmcnt(8)
Mp 148,12,40
v_exp_f32_e32 v24, v109
Mp 160,16,40
v_exp_f32_e32 v55, v110
Mp 168,16,44
v_exp_f32_e32 v25, v111
Mp 172,20,44
v_add_f32_e32 v15, v52, v28
Cv 52,52,28
Mp 164,20,40
buffer_load_dwordx4 v3, s[20:23], s55 offen offset:1024 sc1 lds
Mp 176,24,40
v_add_f32_e32 v15, v15, v53
)" R"(
v_add_f32_e32 v15, v15, v29
Mp 184,24,44
Cv 53,53,29
v_add_f32_e32 v15, v15, v54
Mp 188,28,44
v_add_f32_e32 v15, v15, v24
Cv 54,54,24
Mp 180,28,40
s_waitcnt vmcnt(8) lgkmcnt(0)
s_barrier
buffer_store_dword v6, v7, s[20:23], s49 offen offset:8 sc0 sc1
Mp 192,32,40
Rb 0,1,0
v_add_f32_e32 v15, v15, v55
Mp 200,32,44
Rb 4,1,1024
v_add_f32_e32 v15, v15, v25
Mp 204,36,44
Rb 8,1,2048
Cv 55,55,25
Mp 196,36,40
Rb 12,1,3072
buffer_load_dwordx4 v3, s[20:23], s55 offen offset:2048 sc1 lds
Mp 208,40,40
Rb 16,1,8192
s_sub_u32 s57, s57, 1
Mp 216,40,44
Rb 20,1,9216
Mp 220,44,44
Rb 24,1,10240
Mp 212,44,40
Rb 28,1,11264
Mp 224,48,40
Rb 32,1,4096
Mp 232,48,44
Rb 36,1,5120
Mp 236,52,44
Rb 40,1,6144
Mp 228,52,40
Rb 44,1,7168
buffer_load_dwordx4 v3, s[20:23], s55 offen offset:3072 sc1 lds
Mp 240,56,40
v_cmp_eq_u32_e32 vcc, s3, v4
s_andn2_b64 s[28:29], s[60:61], vcc
s_cbranch_scc1 .Lzsp16
.Lzok16:
Mp 248,56,44
Mp 252,60,44
s_waitcnt lgkmcnt(8)
Mp 244,60,40
v_mfma_f32_16x16x32_bf16 v[64:67], a[0:3], a[64:67], v[16:19]
Rb 48,1,12288
v_mfma_f32_16x16x32_bf16 v[68:71], a[0:3], a[68:71], v[20:23]
v_exp_f32_e32 v56, v112
Mv 68,4,76
Rb 52,1,13312
s_add_u32 m0, s36, 98304
Mv 64,4,72
buffer_load_dwordx4 v2, s[20:23], s56 offen offset:0 sc1 lds
Mv 64,8,80
Rb 56,1,14336
v_add_f32_e32 v14, v14, v56
Mv 68,8,84
buffer_store_dwordx4 a[128:131], v2, s[20:23], s48 offen offset:0 sc0 sc1
Mv 68,12,92
)" R"(
Rb 60,1,15360
s_waitcnt lgkmcnt(8)
Mv 64,12,88
v_exp_f32_e32 v26, v113
Mv 64,16,96
Rb 0,1,16384
v_add_f32_e32 v14, v14, v26
Mv 68,16,100
v_exp_f32_e32 v57, v114
Mv 68,20,108
Rb 4,1,17408
Cv 56,56,26
Mv 64,20,104
buffer_load_dwordx4 v2, s[20:23], s56 offen offset:1024 sc1 lds
Mv 64,24,112
Rb 8,1,18432
v_add_f32_e32 v14, v14, v57
Mv 68,24,116
v_exp_f32_e32 v27, v115
Mv 68,28,124
Rb 12,1,19456
s_waitcnt lgkmcnt(8)
Mv 64,28,120
v_exp_f32_e32 v58, v120
v_mfma_f32_16x16x32_bf16 v[72:75], a[32:35], a[64:67], v[16:19]
Rb 16,1,24576
v_add_f32_e32 v14, v14, v27
v_mfma_f32_16x16x32_bf16 v[76:79], a[32:35], a[68:71], v[20:23]
v_exp_f32_e32 v28, v121
Mv 76,36,76
Rb 20,1,25600
Cv 57,57,27
Mv 72,36,72
buffer_load_dwordx4 v2, s[20:23], s56 offen offset:2048 sc1 lds
Mv 72,40,80
Rb 24,1,26624
v_add_f32_e32 v14, v14, v58
Mv 76,40,84
v_exp_f32_e32 v59, v122
Mv 76,44,92
Rb 28,1,27648
s_waitcnt lgkmcnt(8)
Mv 72,44,88
v_exp_f32_e32 v29, v123
Mv 72,48,96
Rb 32,1,20480
v_add_f32_e32 v14, v14, v28
Mv 76,48,100
v_exp_f32_e32 v60, v116
Mv 76,52,108
Rb 36,1,21504
Cv 58,58,28
Mv 72,52,104
buffer_load_dwordx4 v2, s[20:23], s56 offen offset:3072 sc1 lds
Mv 72,56,112
Rb 40,1,22528
v_add_f32_e32 v14, v14, v59
Mv 76,56,116
buffer_store_dwordx4 a[132:135], v2, s[20:23], s48 offen offset:1024 sc0 sc1
Mv 76,60,124
Rb 44,1,23552
s_waitcnt lgkmcnt(8)
Mv 72,60,120
v_exp_f32_e32 v24, v117
)" R"(
v_mfma_f32_16x16x32_bf16 v[80:83], a[0:3], a[64:67], v[16:19]
Rb 48,1,28672
v_add_f32_e32 v14, v14, v29
v_mfma_f32_16x16x32_bf16 v[84:87], a[0:3], a[68:71], v[20:23]
v_exp_f32_e32 v61, v118
Mv 84,4,76
Rb 52,1,29696
s_add_u32 m0, s36, 102400
Mv 80,4,72
buffer_load_dwordx4 v3, s[20:23], s56 offen offset:0 sc1 lds
Mv 80,8,80
Rb 56,1,30720
Cv 59,59,29
Mv 84,8,84
v_exp_f32_e32 v25, v119
Mv 84,12,92
Rb 60,1,31744
s_waitcnt lgkmcnt(8)
Mv 80,12,88
v_exp_f32_e32 v62, v124
Mv 80,16,96
v_exp_f32_e32 v26, v125
Mv 84,16,100
v_exp_f32_e32 v63, v126
Mv 84,20,108
v_exp_f32_e32 v27, v127
Mv 80,20,104
buffer_load_dwordx4 v3, s[20:23], s56 offen offset:1024 sc1 lds
Mv 80,24,112
v_add_f32_e32 v15, v15, v60
v_add_f32_e32 v15, v15, v24
Mv 84,24,116
Cv 60,60,24
v_add_f32_e32 v15, v15, v61
Mv 84,28,124
v_add_f32_e32 v15, v15, v25
Cv 61,61,25
Mv 80,28,120
s_waitcnt vmcnt(17) lgkmcnt(0)
s_barrier
v_mfma_f32_16x16x32_bf16 v[88:91], a[32:35], a[64:67], v[16:19]
Rb 0,8,0
v_add_f32_e32 v15, v15, v62
v_mfma_f32_16x16x32_bf16 v[92:95], a[32:35], a[68:71], v[20:23]
Rb 4,8,1024
v_add_f32_e32 v15, v15, v26
Mv 92,36,76
Rb 8,8,2048
Cv 62,62,26
Mv 88,36,72
Rb 12,8,3072
buffer_load_dwordx4 v3, s[20:23], s56 offen offset:2048 sc1 lds
Mv 88,40,80
Rb 16,8,4096
v_add_f32_e32 v15, v15, v63
Mv 92,40,84
Rb 20,8,5120
buffer_store_dwordx4 a[136:139], v2, s[20:23], s48 offen offset:2048 sc0 sc1
Mv 92,44,92
Rb 24,8,6144
v_add_f32_e32 v15, v15, v27
Mv 88,44,88
Rb 28,8,7168
Cv 63,63,27
Mv 88,48,96
)" R"(
Rb 32,8,8192
v_max_f32_e32 v11, v14, v15
Mv 92,48,100
Rb 36,8,9216
v_cmp_nge_f32_e32 vcc, s62, v11
Mv 92,52,108
Rb 40,8,10240
s_add_u32 s55, s55, 0x10000
Mv 88,52,104
Rb 44,8,11264
buffer_load_dwordx4 v3, s[20:23], s56 offen offset:3072 sc1 lds
Mv 88,56,112
s_cmp_eq_u32 s57, 2
s_cselect_b32 s55, s59, s55
Mv 92,56,116
s_add_u32 s56, s56, 0x10000
Mv 92,60,124
s_waitcnt lgkmcnt(8)
s_cbranch_vccnz .Lzfx9
.Lzfr9:
Mv 88,60,120
v_add_f32_e32 v12, v12, v14
v_add_f32_e32 v13, v13, v15
Mp 128,0,48
Rb 48,8,12288
Mp 136,0,52
s_cmp_le_i32 s57, s58
s_cbranch_scc1 .Lzmk8
.Lzmr8:
Mp 140,4,52
Rb 52,8,13312
s_add_u32 m0, s36, 0
Mp 132,4,48
buffer_load_dwordx4 v2, s[20:23], s55 offen offset:0 sc1 lds
Mp 144,8,48
Rb 56,8,14336
Mp 152,8,52
v_exp_f32_e32 v32, v64
Mp 156,12,52
Rb 60,8,15360
s_waitcnt lgkmcnt(8)
Mp 148,12,48
v_exp_f32_e32 v28, v65
Mp 160,16,48
Rb 0,8,16384
v_add_f32_e32 v14, v32, v28
Mp 168,16,52
v_exp_f32_e32 v33, v66
Mp 172,20,52
Rb 4,8,17408
Cv 32,32,28
Mp 164,20,48
buffer_load_dwordx4 v2, s[20:23], s55 offen offset:1024 sc1 lds
Mp 176,24,48
Rb 8,8,18432
v_add_f32_e32 v14, v14, v33
Mp 184,24,52
buffer_store_dwordx4 a[140:143], v2, s[20:23], s48 offen offset:3072 sc0 sc1
Mp 188,28,52
Rb 12,8,19456
s_waitcnt lgkmcnt(8)
Mp 180,28,48
v_exp_f32_e32 v29, v67
Mp 192,32,48
)" R"(
Rb 16,8,20480
v_add_f32_e32 v14, v14, v29
Mp 200,32,52
v_exp_f32_e32 v34, v72
Mp 204,36,52
Rb 20,8,21504
Cv 33,33,29
Mp 196,36,48
buffer_load_dwordx4 v2, s[20:23], s55 offen offset:2048 sc1 lds
Mp 208,40,48
Rb 24,8,22528
v_add_f32_e32 v14, v14, v34
Mp 216,40,52
v_exp_f32_e32 v24, v73
Mp 220,44,52
Rb 28,8,23552
s_waitcnt lgkmcnt(8)
Mp 212,44,48
v_exp_f32_e32 v35, v74
Mp 224,48,48
Rb 32,8,24576
v_add_f32_e32 v14, v14, v24
Mp 232,48,52
v_exp_f32_e32 v25, v75
Mp 236,52,52
Rb 36,8,25600
Cv 34,34,24
Mp 228,52,48
buffer_load_dwordx4 v2, s[20:23], s55 offen offset:3072 sc1 lds
Mp 240,56,48
Rb 40,8,26624
v_add_f32_e32 v14, v14, v35
Mp 248,56,52
v_exp_f32_e32 v36, v68
Mp 252,60,52
Rb 44,8,27648
s_waitcnt lgkmcnt(8)
Mp 244,60,48
v_exp_f32_e32 v26, v69
Mp 128,0,56
Rb 48,8,28672
v_add_f32_e32 v14, v14, v25
Mp 136,0,60
v_exp_f32_e32 v37, v70
Mp 140,4,60
Rb 52,8,29696
s_add_u32 m0, s36, 4096
Mp 132,4,56
buffer_load_dwordx4 v3, s[20:23], s55 offen offset:0 sc1 lds
Mp 144,8,56
Rb 56,8,30720
Cv 35,35,25
Mp 152,8,60
buffer_store_dwordx4 a[144:147], v3, s[20:23], s48 offen offset:0 sc0 sc1
Mp 156,12,60
Rb 60,8,31744
s_waitcnt lgkmcnt(8)
Mp 148,12,56
v_exp_f32_e32 v27, v71
Mp 160,16,56
v_exp_f32_e32 v38, v76
Mp 168,16,60
v_exp_f32_e32 v28, v77
Mp 172,20,60
)" R"(
v_exp_f32_e32 v39, v78
Mp 164,20,56
buffer_load_dwordx4 v3, s[20:23], s55 offen offset:1024 sc1 lds
Mp 176,24,56
v_exp_f32_e32 v29, v79
Mp 184,24,60
v_add_f32_e32 v15, v36, v26
Cv 36,36,26
Mp 188,28,60
v_add_f32_e32 v15, v15, v37
v_add_f32_e32 v15, v15, v27
Mp 180,28,56
s_waitcnt vmcnt(19) lgkmcnt(0)
s_barrier
Mp 192,32,56
Rb 0,1,32768
Cv 37,37,27
Mp 200,32,60
Rb 4,1,33792
v_add_f32_e32 v15, v15, v38
Mp 204,36,60
Rb 8,1,34816
v_add_f32_e32 v15, v15, v28
Mp 196,36,56
Rb 12,1,35840
buffer_load_dwordx4 v3, s[20:23], s55 offen offset:2048 sc1 lds
Mp 208,40,56
Rb 16,1,40960
Cv 38,38,28
Mp 216,40,60
Rb 20,1,41984
v_add_f32_e32 v15, v15, v39
Mp 220,44,60
Rb 24,1,43008
v_add_f32_e32 v15, v15, v29
Mp 212,44,56
Rb 28,1,44032
Cv 39,39,29
Mp 224,48,56
Rb 32,1,36864
s_sub_u32 s57, s57, 1
Mp 232,48,60
Rb 36,1,37888
Mp 236,52,60
Rb 40,1,38912
Mp 228,52,56
Rb 44,1,39936
buffer_load_dwordx4 v3, s[20:23], s55 offen offset:3072 sc1 lds
Mp 240,56,56
Mp 248,56,60
buffer_store_dwordx4 a[148:151], v3, s[20:23], s48 offen offset:1024 sc0 sc1
Mp 252,60,60
s_waitcnt lgkmcnt(8)
Mp 244,60,56
v_mfma_f32_16x16x32_bf16 v[96:99], a[0:3], a[64:67], v[16:19]
Rb 48,1,45056
buffer_load_dword v4, v5, s[20:23], s50 offen offset:0 sc0 sc1
v_mfma_f32_16x16x32_bf16 v[100:103], a[0:3], a[68:71], v[20:23]
s_lshl_b64 s[60:61], s[60:61], 8
s_or_b32 s60, s60, 0xff
Mv 100,4,76
Rb 52,1,46080
s_add_u32 m0, s36, 65536
Mv 96,4,72
buffer_load_dwordx4 v2, s[20:23], s56 offen offset:0 sc1 lds
)" R"(
Mv 96,8,80
Rb 56,1,47104
Mv 100,8,84
v_exp_f32_e32 v40, v80
Mv 100,12,92
Rb 60,1,48128
s_waitcnt lgkmcnt(8)
Mv 96,12,88
v_exp_f32_e32 v24, v81
Mv 96,16,96
Rb 0,1,49152
v_add_f32_e32 v14, v14, v40
Mv 100,16,100
v_exp_f32_e32 v41, v82
Mv 100,20,108
Rb 4,1,50176
v_add_f32_e32 v14, v14, v24
Mv 96,20,104
buffer_load_dwordx4 v2, s[20:23], s56 offen offset:1024 sc1 lds
Mv 96,24,112
Rb 8,1,51200
Cv 40,40,24
Mv 100,24,116
v_exp_f32_e32 v25, v83
Mv 100,28,124
Rb 12,1,52224
s_waitcnt lgkmcnt(8)
Mv 96,28,120
v_exp_f32_e32 v42, v88
v_add_f32_e32 v14, v14, v41
v_mfma_f32_16x16x32_bf16 v[104:107], a[32:35], a[64:67], v[16:19]
Rb 16,1,57344
v_add_f32_e32 v14, v14, v25
v_mfma_f32_16x16x32_bf16 v[108:111], a[32:35], a[68:71], v[20:23]
v_exp_f32_e32 v26, v89
Mv 108,36,76
Rb 20,1,58368
Cv 41,41,25
Mv 104,36,72
buffer_load_dwordx4 v2, s[20:23], s56 offen offset:2048 sc1 lds
Mv 104,40,80
Rb 24,1,59392
v_add_f32_e32 v14, v14, v42
Mv 108,40,84
buffer_store_dwordx4 a[152:155], v3, s[20:23], s48 offen offset:2048 sc0 sc1
Mv 108,44,92
Rb 28,1,60416
s_waitcnt lgkmcnt(8)
Mv 104,44,88
v_exp_f32_e32 v43, v90
Mv 104,48,96
Rb 32,1,53248
v_add_f32_e32 v14, v14, v26
Mv 108,48,100
v_exp_f32_e32 v27, v91
Mv 108,52,108
Rb 36,1,54272
Cv 42,42,26
Mv 104,52,104
buffer_load_dwordx4 v2, s[20:23], s56 offen offset:3072 sc1 lds
Mv 104,56,112
Rb 40,1,55296
v_add_f32_e32 v14, v14, v43
Mv 108,56,116
)" R"(
v_exp_f32_e32 v44, v84
Mv 108,60,124
Rb 44,1,56320
s_waitcnt lgkmcnt(8)
Mv 104,60,120
v_exp_f32_e32 v28, v85
v_add_f32_e32 v14, v14, v27
v_mfma_f32_16x16x32_bf16 v[112:115], a[0:3], a[64:67], v[16:19]
Rb 48,1,61440
Cv 43,43,27
v_mfma_f32_16x16x32_bf16 v[116:119], a[0:3], a[68:71], v[20:23]
v_exp_f32_e32 v45, v86
Mv 116,4,76
Rb 52,1,62464
s_add_u32 m0, s36, 69632
Mv 112,4,72
buffer_load_dwordx4 v3, s[20:23], s56 offen offset:0 sc1 lds
Mv 112,8,80
Rb 56,1,63488
v_add_f32_e32 v15, v15, v44
Mv 116,8,84
v_exp_f32_e32 v29, v87
Mv 116,12,92
Rb 60,1,64512
s_waitcnt lgkmcnt(8)
Mv 112,12,88
v_exp_f32_e32 v46, v92
Mv 112,16,96
v_exp_f32_e32 v24, v93
Mv 116,16,100
v_exp_f32_e32 v47, v94
Mv 116,20,108
v_exp_f32_e32 v25, v95
Mv 112,20,104
buffer_load_dwordx4 v3, s[20:23], s56 offen offset:1024 sc1 lds
Mv 112,24,112
v_add_f32_e32 v15, v15, v28
Cv 44,44,28
Mv 116,24,116
buffer_store_dwordx4 a[156:159], v3, s[20:23], s48 offen offset:3072 sc0 sc1
Mv 116,28,124
v_add_f32_e32 v15, v15, v45
v_add_f32_e32 v15, v15, v29
Mv 112,28,120
Cv 45,45,29
s_waitcnt vmcnt(20) lgkmcnt(0)
s_barrier
v_mfma_f32_16x16x32_bf16 v[120:123], a[32:35], a[64:67], v[16:19]
Rb 0,8,32768
v_add_f32_e32 v15, v15, v46
v_mfma_f32_16x16x32_bf16 v[124:127], a[32:35], a[68:71], v[20:23]
Rb 4,8,33792
v_add_f32_e32 v15, v15, v24
Mv 124,36,76
Rb 8,8,34816
Cv 46,46,24
Mv 120,36,72
Rb 12,8,35840
buffer_load_dwordx4 v3, s[20:23], s56 offen offset:2048 sc1 lds
Mv 120,40,80
Rb 16,8,36864
v_add_f32_e32 v15, v15, v47
Mv 124,40,84
Rb 20,8,37888
v_add_f32_e32 v15, v15, v25
)" R"(
Mv 124,44,92
Rb 24,8,38912
Cv 47,47,25
Mv 120,44,88
Rb 28,8,39936
v_max_f32_e32 v11, v14, v15
Mv 120,48,96
Rb 32,8,40960
v_cmp_nge_f32_e32 vcc, s62, v11
Mv 124,48,100
Rb 36,8,41984
s_add_u32 s55, s55, 0x10000
Mv 124,52,108
Rb 40,8,43008
Mv 120,52,104
Rb 44,8,44032
buffer_load_dwordx4 v3, s[20:23], s56 offen offset:3072 sc1 lds
Mv 120,56,112
s_cmp_eq_u32 s57, 2
s_cselect_b32 s55, s59, s55
Mv 124,56,116
s_add_u32 s56, s56, 0x10000
Mv 124,60,124
s_waitcnt lgkmcnt(8)
s_cbranch_vccnz .Lzfx10
.Lzfr10:
Mv 120,60,120
v_add_f32_e32 v12, v12, v14
v_add_f32_e32 v13, v13, v15
Mp 128,0,32
Rb 48,8,45056
Mp 136,0,36
s_cmp_le_i32 s57, s58
s_cbranch_scc1 .Lzmk9
.Lzmr9:
Mp 140,4,36
Rb 52,8,46080
s_add_u32 m0, s36, 32768
Mp 132,4,32
buffer_load_dwordx4 v2, s[20:23], s55 offen offset:0 sc1 lds
Mp 144,8,32
Rb 56,8,47104
Mp 152,8,36
v_exp_f32_e32 v48, v96
Mp 156,12,36
Rb 60,8,48128
s_waitcnt lgkmcnt(8)
Mp 148,12,32
v_exp_f32_e32 v26, v97
Mp 160,16,32
Rb 0,8,49152
v_add_f32_e32 v14, v48, v26
Mp 168,16,36
v_exp_f32_e32 v49, v98
Mp 172,20,36
Rb 4,8,50176
Cv 48,48,26
Mp 164,20,32
buffer_load_dwordx4 v2, s[20:23], s55 offen offset:1024 sc1 lds
Mp 176,24,32
Rb 8,8,51200
v_add_f32_e32 v14, v14, v49
Mp 184,24,36
v_exp_f32_e32 v27, v99
)" R"(
Mp 188,28,36
Rb 12,8,52224
s_waitcnt lgkmcnt(8)
Mp 180,28,32
v_exp_f32_e32 v50, v104
Mp 192,32,32
Rb 16,8,53248
v_add_f32_e32 v14, v14, v27
Mp 200,32,36
v_exp_f32_e32 v28, v105
Mp 204,36,36
Rb 20,8,54272
Cv 49,49,27
Mp 196,36,32
buffer_load_dwordx4 v2, s[20:23], s55 offen offset:2048 sc1 lds
Mp 208,40,32
Rb 24,8,55296
v_add_f32_e32 v14, v14, v50
Mp 216,40,36
v_exp_f32_e32 v51, v106
Mp 220,44,36
Rb 28,8,56320
s_waitcnt lgkmcnt(8)
Mp 212,44,32
v_exp_f32_e32 v29, v107
Mp 224,48,32
Rb 32,8,57344
v_add_f32_e32 v14, v14, v28
Mp 232,48,36
v_exp_f32_e32 v52, v100
Mp 236,52,36
Rb 36,8,58368
Cv 50,50,28
Mp 228,52,32
buffer_load_dwordx4 v2, s[20:23], s55 offen offset:3072 sc1 lds
Mp 240,56,32
Rb 40,8,59392
v_add_f32_e32 v14, v14, v51
Mp 248,56,36
v_exp_f32_e32 v24, v101
Mp 252,60,36
Rb 44,8,60416
s_waitcnt lgkmcnt(8)
Mp 244,60,32
v_exp_f32_e32 v53, v102
Mp 128,0,40
Rb 48,8,61440
v_add_f32_e32 v14, v14, v29
Mp 136,0,44
v_exp_f32_e32 v25, v103
Mp 140,4,44
Rb 52,8,62464
s_add_u32 m0, s36, 36864
Mp 132,4,40
buffer_load_dwordx4 v3, s[20:23], s55 offen offset:0 sc1 lds
Mp 144,8,40
Rb 56,8,63488
Cv 51,51,29
Mp 152,8,44
v_exp_f32_e32 v54, v108
Mp 156,12,44
Rb 60,8,64512
s_waitcnt lgkmcnt(8)
Mp 148,12,40
)" R"(
v_exp_f32_e32 v26, v109
Mp 160,16,40
v_exp_f32_e32 v55, v110
Mp 168,16,44
v_exp_f32_e32 v27, v111
Mp 172,20,44
v_add_f32_e32 v15, v52, v24
Cv 52,52,24
Mp 164,20,40
buffer_load_dwordx4 v3, s[20:23], s55 offen offset:1024 sc1 lds
Mp 176,24,40
v_add_f32_e32 v15, v15, v53
v_add_f32_e32 v15, v15, v25
Mp 184,24,44
Cv 53,53,25
v_add_f32_e32 v15, v15, v54
Mp 188,28,44
v_add_f32_e32 v15, v15, v26
Cv 54,54,26
Mp 180,28,40
s_waitcnt vmcnt(8) lgkmcnt(0)
s_barrier
buffer_store_dword v6, v7, s[20:23], s49 offen offset:12 sc0 sc1
Mp 192,32,40
Rb 0,1,0
v_add_f32_e32 v15, v15, v55
Mp 200,32,44
Rb 4,1,1024
v_add_f32_e32 v15, v15, v27
Mp 204,36,44
Rb 8,1,2048
Cv 55,55,27
Mp 196,36,40
Rb 12,1,3072
buffer_load_dwordx4 v3, s[20:23], s55 offen offset:2048 sc1 lds
Mp 208,40,40
Rb 16,1,8192
s_sub_u32 s57, s57, 1
Mp 216,40,44
Rb 20,1,9216
Mp 220,44,44
Rb 24,1,10240
Mp 212,44,40
Rb 28,1,11264
Mp 224,48,40
Rb 32,1,4096
Mp 232,48,44
Rb 36,1,5120
Mp 236,52,44
Rb 40,1,6144
Mp 228,52,40
Rb 44,1,7168
buffer_load_dwordx4 v3, s[20:23], s55 offen offset:3072 sc1 lds
Mp 240,56,40
v_cmp_eq_u32_e32 vcc, s3, v4
s_andn2_b64 s[28:29], s[60:61], vcc
s_cbranch_scc1 .Lzsp17
.Lzok17:
Mp 248,56,44
Mp 252,60,44
s_waitcnt lgkmcnt(8)
Mp 244,60,40
s_branch .LzL
.LzEB:
v_mfma_f32_16x16x32_bf16 v[64:67], a[0:3], a[64:67], v[16:19]
)" R"(
Rb 48,1,12288
v_mfma_f32_16x16x32_bf16 v[68:71], a[0:3], a[68:71], v[20:23]
s_cmp_lg_u32 s35, 7
s_cselect_b64 s[60:61], 1, 0
Mv 68,4,76
Rb 52,1,13312
s_add_u32 m0, s36, 98304
Mv 64,4,72
buffer_load_dwordx4 v2, s[20:23], s56 offen offset:0 sc1 lds
Mv 64,8,80
Rb 56,1,14336
Mv 68,8,84
v_exp_f32_e32 v56, v112
Mv 68,12,92
Rb 60,1,15360
s_waitcnt lgkmcnt(8)
Mv 64,12,88
v_exp_f32_e32 v28, v113
Mv 64,16,96
Rb 0,1,16384
v_add_f32_e32 v14, v14, v56
Mv 68,16,100
v_exp_f32_e32 v57, v114
Mv 68,20,108
Rb 4,1,17408
v_add_f32_e32 v14, v14, v28
Mv 64,20,104
buffer_load_dwordx4 v2, s[20:23], s56 offen offset:1024 sc1 lds
Mv 64,24,112
Rb 8,1,18432
Cv 56,56,28
Mv 68,24,116
v_exp_f32_e32 v29, v115
Mv 68,28,124
Rb 12,1,19456
s_waitcnt lgkmcnt(8)
Mv 64,28,120
v_exp_f32_e32 v58, v120
v_mfma_f32_16x16x32_bf16 v[72:75], a[32:35], a[64:67], v[16:19]
Rb 16,1,24576
v_add_f32_e32 v14, v14, v57
v_mfma_f32_16x16x32_bf16 v[76:79], a[32:35], a[68:71], v[20:23]
v_exp_f32_e32 v24, v121
Mv 76,36,76
Rb 20,1,25600
v_add_f32_e32 v14, v14, v29
Mv 72,36,72
buffer_load_dwordx4 v2, s[20:23], s56 offen offset:2048 sc1 lds
Mv 72,40,80
Rb 24,1,26624
Cv 57,57,29
Mv 76,40,84
v_exp_f32_e32 v59, v122
Mv 76,44,92
Rb 28,1,27648
s_waitcnt lgkmcnt(8)
Mv 72,44,88
v_exp_f32_e32 v25, v123
Mv 72,48,96
Rb 32,1,20480
v_add_f32_e32 v14, v14, v58
Mv 76,48,100
v_exp_f32_e32 v60, v116
Mv 76,52,108
)" R"(
Rb 36,1,21504
v_add_f32_e32 v14, v14, v24
Mv 72,52,104
buffer_load_dwordx4 v2, s[20:23], s56 offen offset:3072 sc1 lds
Mv 72,56,112
Rb 40,1,22528
Cv 58,58,24
Mv 76,56,116
v_exp_f32_e32 v26, v117
Mv 76,60,124
Rb 44,1,23552
s_waitcnt lgkmcnt(8)
Mv 72,60,120
v_exp_f32_e32 v61, v118
v_mfma_f32_16x16x32_bf16 v[80:83], a[0:3], a[64:67], v[16:19]
Rb 48,1,28672
v_add_f32_e32 v14, v14, v59
v_mfma_f32_16x16x32_bf16 v[84:87], a[0:3], a[68:71], v[20:23]
v_exp_f32_e32 v27, v119
Mv 84,4,76
Rb 52,1,29696
s_add_u32 m0, s36, 102400
Mv 80,4,72
buffer_load_dwordx4 v3, s[20:23], s56 offen offset:0 sc1 lds
Mv 80,8,80
Rb 56,1,30720
v_add_f32_e32 v14, v14, v25
Mv 84,8,84
v_exp_f32_e32 v62, v124
Mv 84,12,92
Rb 60,1,31744
s_waitcnt lgkmcnt(8)
Mv 80,12,88
v_exp_f32_e32 v28, v125
Mv 80,16,96
v_exp_f32_e32 v63, v126
Mv 84,16,100
v_exp_f32_e32 v29, v127
Mv 84,20,108
Cv 59,59,25
v_add_f32_e32 v15, v15, v60
Mv 80,20,104
buffer_load_dwordx4 v3, s[20:23], s56 offen offset:1024 sc1 lds
Mv 80,24,112
v_add_f32_e32 v15, v15, v26
Cv 60,60,26
Mv 84,24,116
v_add_f32_e32 v15, v15, v61
v_add_f32_e32 v15, v15, v27
Mv 84,28,124
Cv 61,61,27
v_add_f32_e32 v15, v15, v62
Mv 80,28,120
s_waitcnt vmcnt(14) lgkmcnt(0)
s_barrier
v_mfma_f32_16x16x32_bf16 v[88:91], a[32:35], a[64:67], v[16:19]
Rb 0,8,0
v_add_f32_e32 v15, v15, v28
v_mfma_f32_16x16x32_bf16 v[92:95], a[32:35], a[68:71], v[20:23]
Rb 4,8,1024
Cv 62,62,28
Mv 92,36,76
Rb 8,8,2048
v_add_f32_e32 v15, v15, v63
Mv 88,36,72
)" R"(
Rb 12,8,3072
buffer_load_dwordx4 v3, s[20:23], s56 offen offset:2048 sc1 lds
Mv 88,40,80
Rb 16,8,4096
v_add_f32_e32 v15, v15, v29
Mv 92,40,84
Rb 20,8,5120
Cv 63,63,29
Mv 92,44,92
Rb 24,8,6144
v_max_f32_e32 v11, v14, v15
Mv 88,44,88
Rb 28,8,7168
v_cmp_nge_f32_e32 vcc, s62, v11
Mv 88,48,96
Rb 32,8,8192
s_add_u32 s55, s55, 0x10000
Mv 92,48,100
Rb 36,8,9216
Mv 92,52,108
Rb 40,8,10240
Mv 88,52,104
Rb 44,8,11264
buffer_load_dwordx4 v3, s[20:23], s56 offen offset:3072 sc1 lds
Mv 88,56,112
s_cmp_eq_u32 s57, 2
s_cselect_b32 s55, s59, s55
Mv 92,56,116
s_add_u32 s56, s56, 0x10000
Mv 92,60,124
s_waitcnt lgkmcnt(8)
s_cbranch_vccnz .Lzfx11
.Lzfr11:
Mv 88,60,120
v_add_f32_e32 v12, v12, v14
v_add_f32_e32 v13, v13, v15
Mp 128,0,48
Rb 48,8,12288
Mp 136,0,52
s_cmp_le_i32 s57, s58
s_cbranch_scc1 .Lzmk10
.Lzmr10:
Mp 140,4,52
Rb 52,8,13312
s_add_u32 m0, s36, 0
Mp 132,4,48
buffer_load_dwordx4 v2, s[20:23], s55 offen offset:0 sc1 lds
Mp 144,8,48
Rb 56,8,14336
Mp 152,8,52
v_exp_f32_e32 v32, v64
Mp 156,12,52
Rb 60,8,15360
s_waitcnt lgkmcnt(8)
Mp 148,12,48
v_exp_f32_e32 v24, v65
Mp 160,16,48
Rb 0,8,16384
v_add_f32_e32 v14, v32, v24
Mp 168,16,52
v_exp_f32_e32 v33, v66
Cv 32,32,24
Mp 172,20,52
Rb 4,8,17408
)" R"(
v_add_f32_e32 v14, v14, v33
Mp 164,20,48
buffer_load_dwordx4 v2, s[20:23], s55 offen offset:1024 sc1 lds
Mp 176,24,48
Rb 8,8,18432
Mp 184,24,52
v_exp_f32_e32 v25, v67
Mp 188,28,52
Rb 12,8,19456
s_waitcnt lgkmcnt(8)
Mp 180,28,48
v_exp_f32_e32 v34, v72
Mp 192,32,48
Rb 16,8,20480
v_add_f32_e32 v14, v14, v25
Mp 200,32,52
v_exp_f32_e32 v26, v73
Cv 33,33,25
Mp 204,36,52
Rb 20,8,21504
v_add_f32_e32 v14, v14, v34
Mp 196,36,48
buffer_load_dwordx4 v2, s[20:23], s55 offen offset:2048 sc1 lds
Mp 208,40,48
Rb 24,8,22528
v_add_f32_e32 v14, v14, v26
Mp 216,40,52
v_exp_f32_e32 v35, v74
Mp 220,44,52
Rb 28,8,23552
s_waitcnt lgkmcnt(8)
Mp 212,44,48
v_exp_f32_e32 v27, v75
Mp 224,48,48
Rb 32,8,24576
Cv 34,34,26
Mp 232,48,52
v_exp_f32_e32 v36, v68
v_add_f32_e32 v14, v14, v35
Mp 236,52,52
Rb 36,8,25600
v_add_f32_e32 v14, v14, v27
Mp 228,52,48
buffer_load_dwordx4 v2, s[20:23], s55 offen offset:3072 sc1 lds
Mp 240,56,48
Rb 40,8,26624
Cv 35,35,27
Mp 248,56,52
v_exp_f32_e32 v28, v69
Mp 252,60,52
Rb 44,8,27648
s_waitcnt lgkmcnt(8)
Mp 244,60,48
v_exp_f32_e32 v37, v70
Mp 128,0,56
Rb 48,8,28672
v_add_f32_e32 v15, v36, v28
Mp 136,0,60
v_exp_f32_e32 v29, v71
Cv 36,36,28
Mp 140,4,60
Rb 52,8,29696
s_add_u32 m0, s36, 4096
Mp 132,4,56
)" R"(
buffer_load_dwordx4 v3, s[20:23], s55 offen offset:0 sc1 lds
Mp 144,8,56
Rb 56,8,30720
v_add_f32_e32 v15, v15, v37
Mp 152,8,60
v_exp_f32_e32 v38, v76
Mp 156,12,60
Rb 60,8,31744
s_waitcnt lgkmcnt(8)
Mp 148,12,56
v_exp_f32_e32 v24, v77
Mp 160,16,56
v_exp_f32_e32 v39, v78
Mp 168,16,60
v_exp_f32_e32 v25, v79
v_add_f32_e32 v15, v15, v29
Mp 172,20,60
Cv 37,37,29
v_add_f32_e32 v15, v15, v38
Mp 164,20,56
buffer_load_dwordx4 v3, s[20:23], s55 offen offset:1024 sc1 lds
Mp 176,24,56
v_add_f32_e32 v15, v15, v24
Cv 38,38,24
Mp 184,24,60
v_add_f32_e32 v15, v15, v39
v_add_f32_e32 v15, v15, v25
Mp 188,28,60
Cv 39,39,25
s_sub_u32 s57, s57, 1
Mp 180,28,56
s_waitcnt vmcnt(14) lgkmcnt(0)
s_barrier
Mp 192,32,56
Rb 0,1,32768
v_bfe_u32 v11, v0, 2, 2
Mp 200,32,60
Rb 4,1,33792
v_lshrrev_b32 v4, 4, v0
v_xor_b32 v11, v11, v4
Mp 204,36,60
Rb 8,1,34816
v_bfe_u32 v4, v0, 1, 1
Mp 196,36,56
Rb 12,1,35840
buffer_load_dwordx4 v3, s[20:23], s55 offen offset:2048 sc1 lds
Mp 208,40,56
Rb 16,1,40960
v_lshl_or_b32 v11, v11, 1, v4
Mp 216,40,60
Rb 20,1,41984
v_and_b32 v4, 1, v0
Mp 220,44,60
Rb 24,1,43008
v_mul_u32_u24 v4, 0xffff, v4
Mp 212,44,56
Rb 28,1,44032
v_add_u32 v4, 1, v4
Mp 224,48,56
Rb 32,1,36864
v_mul_u32_u24 v4, 0x3f80, v4
Mp 232,48,60
Rb 36,1,37888
v_cmp_eq_u32_e32 vcc, 0, v11
v_cmp_eq_u32_e64 s[28:29], 1, v11
)" R"(
Mp 236,52,60
Rb 40,1,38912
v_accvgpr_write_b32 a192, 0
Mp 228,52,56
Rb 44,1,39936
buffer_load_dwordx4 v3, s[20:23], s55 offen offset:3072 sc1 lds
Mp 240,56,56
v_accvgpr_write_b32 a193, 0
v_accvgpr_write_b32 a196, 0
Mp 248,56,60
v_accvgpr_write_b32 a197, 0
v_cndmask_b32_e32 v11, 0, v4, vcc
Mp 252,60,60
v_cndmask_b32_e64 v4, 0, v4, s[28:29]
s_waitcnt lgkmcnt(8)
Mp 244,60,56
v_accvgpr_write_b32 a194, v11
v_accvgpr_write_b32 a195, v4
v_mfma_f32_16x16x32_bf16 v[96:99], a[0:3], a[64:67], v[16:19]
Rb 48,1,45056
v_mfma_f32_16x16x32_bf16 v[100:103], a[0:3], a[68:71], v[20:23]
v_exp_f32_e32 v40, v80
Mv 100,4,76
Rb 52,1,46080
s_add_u32 m0, s36, 65536
Mv 96,4,72
buffer_load_dwordx4 v2, s[20:23], s56 offen offset:0 sc1 lds
Mv 96,8,80
Rb 56,1,47104
v_add_f32_e32 v14, v14, v40
Mv 100,8,84
v_exp_f32_e32 v26, v81
Mv 100,12,92
Rb 60,1,48128
s_waitcnt lgkmcnt(8)
Mv 96,12,88
v_exp_f32_e32 v41, v82
Mv 96,16,96
Rb 0,1,49152
v_add_f32_e32 v14, v14, v26
Mv 100,16,100
v_exp_f32_e32 v27, v83
Mv 100,20,108
Rb 4,1,50176
Cv 40,40,26
Mv 96,20,104
buffer_load_dwordx4 v2, s[20:23], s56 offen offset:1024 sc1 lds
Mv 96,24,112
Rb 8,1,51200
v_add_f32_e32 v14, v14, v41
Mv 100,24,116
v_exp_f32_e32 v42, v88
Mv 100,28,124
Rb 12,1,52224
s_waitcnt lgkmcnt(8)
Mv 96,28,120
v_exp_f32_e32 v28, v89
v_mfma_f32_16x16x32_bf16 v[104:107], a[32:35], a[64:67], v[16:19]
Rb 16,1,57344
v_add_f32_e32 v14, v14, v27
v_mfma_f32_16x16x32_bf16 v[108:111], a[32:35], a[68:71], v[20:23]
v_exp_f32_e32 v43, v90
Mv 108,36,76
Rb 20,1,58368
)" R"(
Cv 41,41,27
Mv 104,36,72
buffer_load_dwordx4 v2, s[20:23], s56 offen offset:2048 sc1 lds
Mv 104,40,80
Rb 24,1,59392
buffer_load_dword v4, v7, s[20:23], s65 offen offset:0 sc0 sc1
Mv 108,40,84
v_exp_f32_e32 v29, v91
Mv 108,44,92
Rb 28,1,60416
s_waitcnt lgkmcnt(8)
Mv 104,44,88
v_exp_f32_e32 v44, v84
Mv 104,48,96
Rb 32,1,53248
v_add_f32_e32 v14, v14, v42
Mv 108,48,100
v_exp_f32_e32 v24, v85
Mv 108,52,108
Rb 36,1,54272
v_add_f32_e32 v14, v14, v28
Mv 104,52,104
buffer_load_dwordx4 v2, s[20:23], s56 offen offset:3072 sc1 lds
Mv 104,56,112
Rb 40,1,55296
Cv 42,42,28
Mv 108,56,116
v_exp_f32_e32 v45, v86
Mv 108,60,124
Rb 44,1,56320
s_waitcnt lgkmcnt(8)
Mv 104,60,120
v_exp_f32_e32 v25, v87
v_mfma_f32_16x16x32_bf16 v[112:115], a[0:3], a[64:67], v[16:19]
Rb 48,1,61440
v_add_f32_e32 v14, v14, v43
v_mfma_f32_16x16x32_bf16 v[116:119], a[0:3], a[68:71], v[20:23]
v_exp_f32_e32 v46, v92
Mv 116,4,76
Rb 52,1,62464
s_add_u32 m0, s36, 69632
Mv 112,4,72
buffer_load_dwordx4 v3, s[20:23], s56 offen offset:0 sc1 lds
Mv 112,8,80
Rb 56,1,63488
v_add_f32_e32 v14, v14, v29
Mv 116,8,84
v_exp_f32_e32 v26, v93
Mv 116,12,92
Rb 60,1,64512
s_waitcnt lgkmcnt(8)
Mv 112,12,88
v_exp_f32_e32 v47, v94
Mv 112,16,96
v_exp_f32_e32 v27, v95
Mv 116,16,100
Cv 43,43,29
v_add_f32_e32 v15, v15, v44
Mv 116,20,108
v_add_f32_e32 v15, v15, v24
Cv 44,44,24
Mv 112,20,104
buffer_load_dwordx4 v3, s[20:23], s56 offen offset:1024 sc1 lds
Mv 112,24,112
)" R"(
v_add_f32_e32 v15, v15, v45
v_add_f32_e32 v15, v15, v25
Mv 116,24,116
Cv 45,45,25
v_add_f32_e32 v15, v15, v46
Mv 116,28,124
v_add_f32_e32 v15, v15, v26
Cv 46,46,26
Mv 112,28,120
s_waitcnt vmcnt(3) lgkmcnt(0)
s_barrier
v_mfma_f32_16x16x32_bf16 v[120:123], a[32:35], a[64:67], v[16:19]
Rb 0,8,32768
v_add_f32_e32 v15, v15, v47
v_mfma_f32_16x16x32_bf16 v[124:127], a[32:35], a[68:71], v[20:23]
Rb 4,8,33792
v_add_f32_e32 v15, v15, v27
Mv 124,36,76
Rb 8,8,34816
Cv 47,47,27
Mv 120,36,72
Rb 12,8,35840
buffer_load_dwordx4 v3, s[20:23], s56 offen offset:2048 sc1 lds
Mv 120,40,80
Rb 16,8,36864
v_max_f32_e32 v11, v14, v15
Mv 124,40,84
Rb 20,8,37888
v_cmp_nge_f32_e32 vcc, s62, v11
Mv 124,44,92
Rb 24,8,38912
s_add_u32 s55, s55, 0x10000
Mv 120,44,88
Rb 28,8,39936
Mv 120,48,96
Rb 32,8,40960
Mv 124,48,100
Rb 36,8,41984
Mv 124,52,108
Rb 40,8,43008
Mv 120,52,104
Rb 44,8,44032
buffer_load_dwordx4 v3, s[20:23], s56 offen offset:3072 sc1 lds
Mv 120,56,112
s_cmp_eq_u32 s57, 2
s_cselect_b32 s55, s59, s55
Mv 124,56,116
s_add_u32 s56, s56, 0x10000
Mv 124,60,124
s_waitcnt lgkmcnt(8)
s_cbranch_vccnz .Lzfx12
.Lzfr12:
Mv 120,60,120
v_cmp_eq_u32_e32 vcc, s3, v4
s_andn2_b64 s[28:29], s[60:61], vcc
s_cbranch_scc1 .Lzsp18
.Lzok18:
Mp 128,0,32
Rb 48,8,45056
v_add_f32_e32 v12, v12, v14
Mp 136,0,36
v_add_f32_e32 v13, v13, v15
s_cmp_le_i32 s57, s58
s_cbranch_scc1 .Lzmk11
.Lzmr11:
)" R"(
Mp 140,4,36
Rb 52,8,46080
s_add_u32 m0, s36, 32768
buffer_load_dwordx4 v[64:67], v1, s[20:23], s66 offen sc0 sc1
Mp 132,4,32
buffer_load_dwordx4 v2, s[20:23], s55 offen offset:0 sc1 lds
Mp 144,8,32
Rb 56,8,47104
v_exp_f32_e32 v48, v96
Mp 152,8,36
buffer_load_dwordx4 a[128:131], v2, s[20:23], s64 offen offset:0 sc0 sc1
Mp 156,12,36
Rb 60,8,48128
s_waitcnt lgkmcnt(8)
Mp 148,12,32
v_exp_f32_e32 v28, v97
Mp 160,16,32
Rb 0,8,49152
buffer_load_dwordx4 a[132:135], v2, s[20:23], s64 offen offset:1024 sc0 sc1
Mp 168,16,36
v_exp_f32_e32 v49, v98
v_add_f32_e32 v14, v48, v28
Mp 172,20,36
Rb 4,8,50176
Cv 48,48,28
Mp 164,20,32
buffer_load_dwordx4 v2, s[20:23], s55 offen offset:1024 sc1 lds
Mp 176,24,32
Rb 8,8,51200
v_exp_f32_e32 v29, v99
Mp 184,24,36
buffer_load_dwordx4 a[136:139], v2, s[20:23], s64 offen offset:2048 sc0 sc1
Mp 188,28,36
Rb 12,8,52224
s_waitcnt lgkmcnt(8)
Mp 180,28,32
v_exp_f32_e32 v50, v104
v_add_f32_e32 v14, v14, v49
Mp 192,32,32
Rb 16,8,53248
buffer_load_dwordx4 a[140:143], v2, s[20:23], s64 offen offset:3072 sc0 sc1
Mp 200,32,36
v_exp_f32_e32 v24, v105
v_add_f32_e32 v14, v14, v29
Mp 204,36,36
Rb 20,8,54272
Cv 49,49,29
Mp 196,36,32
buffer_load_dwordx4 v2, s[20:23], s55 offen offset:2048 sc1 lds
Mp 208,40,32
Rb 24,8,55296
v_exp_f32_e32 v51, v106
Mp 216,40,36
buffer_load_dwordx4 a[144:147], v3, s[20:23], s64 offen offset:0 sc0 sc1
Mp 220,44,36
Rb 28,8,56320
s_waitcnt lgkmcnt(8)
Mp 212,44,32
v_exp_f32_e32 v25, v107
v_add_f32_e32 v14, v14, v50
Mp 224,48,32
Rb 32,8,57344
buffer_load_dwordx4 a[148:151], v3, s[20:23], s64 offen offset:1024 sc0 sc1
Mp 232,48,36
)" R"(
v_exp_f32_e32 v52, v100
v_add_f32_e32 v14, v14, v24
Mp 236,52,36
Rb 36,8,58368
Cv 50,50,24
Mp 228,52,32
buffer_load_dwordx4 v2, s[20:23], s55 offen offset:3072 sc1 lds
Mp 240,56,32
Rb 40,8,59392
v_exp_f32_e32 v26, v101
Mp 248,56,36
buffer_load_dwordx4 a[152:155], v3, s[20:23], s64 offen offset:2048 sc0 sc1
Mp 252,60,36
Rb 44,8,60416
s_waitcnt lgkmcnt(8)
Mp 244,60,32
v_exp_f32_e32 v53, v102
v_add_f32_e32 v14, v14, v51
Mp 128,0,40
Rb 48,8,61440
buffer_load_dwordx4 a[156:159], v3, s[20:23], s64 offen offset:3072 sc0 sc1
Mp 136,0,44
v_exp_f32_e32 v27, v103
v_add_f32_e32 v14, v14, v25
Mp 140,4,44
Rb 52,8,62464
s_add_u32 m0, s36, 36864
Mp 132,4,40
buffer_load_dwordx4 v3, s[20:23], s55 offen offset:0 sc1 lds
Mp 144,8,40
Rb 56,8,63488
v_exp_f32_e32 v54, v108
Mp 152,8,44
s_add_u32 s64, s64, 0x10000
buffer_load_dwordx4 a[160:163], v2, s[20:23], s64 offen offset:0 sc0 sc1
Mp 156,12,44
Rb 60,8,64512
s_waitcnt lgkmcnt(8)
Mp 148,12,40
v_exp_f32_e32 v28, v109
Cv 51,51,25
Mp 160,16,40
buffer_load_dwordx4 a[164:167], v2, s[20:23], s64 offen offset:1024 sc0 sc1
Mp 168,16,44
v_exp_f32_e32 v55, v110
v_add_f32_e32 v15, v52, v26
Mp 172,20,44
v_exp_f32_e32 v29, v111
Mp 164,20,40
buffer_load_dwordx4 v3, s[20:23], s55 offen offset:1024 sc1 lds
Mp 176,24,40
Cv 52,52,26
v_add_f32_e32 v15, v15, v53
v_add_f32_e32 v15, v15, v27
Mp 184,24,44
buffer_load_dwordx4 a[168:171], v2, s[20:23], s64 offen offset:2048 sc0 sc1
Mp 188,28,44
Cv 53,53,27
v_add_f32_e32 v15, v15, v54
Mp 180,28,40
v_add_f32_e32 v15, v15, v28
s_waitcnt vmcnt(18) lgkmcnt(0)
s_barrier
Mp 192,32,40
Rb 0,8,0
)" R"(
buffer_load_dwordx4 a[172:175], v2, s[20:23], s64 offen offset:3072 sc0 sc1
Mp 200,32,44
Rb 4,8,16384
Cv 54,54,28
v_add_f32_e32 v15, v15, v55
Mp 204,36,44
Rb 8,8,1024
v_add_f32_e32 v15, v15, v29
Mp 196,36,40
Rb 12,8,17408
buffer_load_dwordx4 v3, s[20:23], s55 offen offset:2048 sc1 lds
Mp 208,40,40
Rb 16,8,2048
Cv 55,55,29
s_sub_u32 s57, s57, 1
Mp 216,40,44
Rb 20,8,18432
buffer_load_dwordx4 a[176:179], v3, s[20:23], s64 offen offset:0 sc0 sc1
Mp 220,44,44
Rb 24,8,3072
Mp 212,44,40
Rb 28,8,19456
Mp 224,48,40
Rb 32,8,4096
buffer_load_dwordx4 a[180:183], v3, s[20:23], s64 offen offset:1024 sc0 sc1
Mp 232,48,44
Rb 36,8,20480
Mp 236,52,44
Rb 40,8,5120
Mp 228,52,40
Rb 44,8,21504
buffer_load_dwordx4 v3, s[20:23], s55 offen offset:3072 sc1 lds
Mp 240,56,40
buffer_load_dwordx4 a[188:191], v3, s[20:23], s64 offen offset:3072 sc0 sc1
Mp 248,56,44
buffer_load_dwordx4 a[184:187], v3, s[20:23], s64 offen offset:2048 sc0 sc1
Mp 252,60,44
Mp 244,60,40
s_waitcnt vmcnt(18)
v_cmp_neq_f32_e32 vcc, v16, v66
v_cmp_neq_f32_e64 s[28:29], v20, v67
s_or_b64 vcc, vcc, s[28:29]
s_cbranch_scc1 .LzMRG
.LzMRR:
Mv 128,194,128
v_exp_f32_e32 v56, v112
Mv 136,194,132
v_exp_f32_e32 v26, v113
v_add_f32_e32 v12, v12, v64
Mv 132,192,128
s_add_u32 m0, s36, 131072
s_add_u32 s40, s67, 0x20000
v_add_f32_e32 v13, v13, v65
Mv 140,192,132
buffer_load_dwordx4 v2, s[20:23], s40 offen offset:0 sc1 lds
Mv 144,194,136
v_exp_f32_e32 v57, v114
v_add_f32_e32 v14, v14, v56
Mv 152,194,140
v_exp_f32_e32 v27, v115
v_add_f32_e32 v14, v14, v26
Mv 148,192,136
Cv 56,56,26
s_waitcnt vmcnt(13)
)" R"(
Mv 156,192,140
buffer_load_dwordx4 v2, s[20:23], s40 offen offset:1024 sc1 lds
v_add_f32_e32 v14, v14, v57
Mv 160,194,144
v_exp_f32_e32 v58, v120
v_add_f32_e32 v14, v14, v27
Mv 168,194,148
v_exp_f32_e32 v28, v121
Mv 164,192,144
v_exp_f32_e32 v59, v122
Cv 57,57,27
Mv 172,192,148
buffer_load_dwordx4 v2, s[20:23], s40 offen offset:2048 sc1 lds
v_add_f32_e32 v14, v14, v58
Mv 176,194,152
v_exp_f32_e32 v29, v123
Mv 184,194,156
v_exp_f32_e32 v60, v116
v_add_f32_e32 v14, v14, v28
Mv 180,192,152
v_exp_f32_e32 v24, v117
s_waitcnt vmcnt(9)
Mv 188,192,156
buffer_load_dwordx4 v2, s[20:23], s40 offen offset:3072 sc1 lds
Cv 58,58,28
Mv 192,194,160
v_exp_f32_e32 v61, v118
Mv 200,194,164
v_exp_f32_e32 v25, v119
v_add_f32_e32 v14, v14, v59
Mv 196,192,160
s_add_u32 m0, s36, 135168
v_exp_f32_e32 v62, v124
Mv 204,192,164
buffer_load_dwordx4 v3, s[20:23], s40 offen offset:0 sc1 lds
Mv 208,194,168
v_exp_f32_e32 v26, v125
v_add_f32_e32 v14, v14, v29
Mv 216,194,172
v_exp_f32_e32 v63, v126
Cv 59,59,29
Mv 212,192,168
v_add_f32_e32 v15, v15, v60
s_waitcnt vmcnt(5)
Mv 220,192,172
buffer_load_dwordx4 v3, s[20:23], s40 offen offset:1024 sc1 lds
v_add_f32_e32 v15, v15, v24
Mv 224,194,176
v_exp_f32_e32 v27, v127
Cv 60,60,24
Mv 232,194,180
v_add_f32_e32 v15, v15, v61
v_add_f32_e32 v15, v15, v25
Mv 228,192,176
Cv 61,61,25
v_add_f32_e32 v15, v15, v62
v_add_f32_e32 v15, v15, v26
Mv 236,192,180
buffer_load_dwordx4 v3, s[20:23], s40 offen offset:2048 sc1 lds
Cv 62,62,26
Mv 240,194,184
v_add_f32_e32 v15, v15, v63
v_add_f32_e32 v15, v15, v27
Mv 248,194,188
)" R"(
s_add_u32 s40, s67, 0x30000
Cv 63,63,27
v_max_f32_e32 v11, v14, v15
Mv 244,192,184
s_add_u32 m0, s36, 98304
v_cmp_nge_f32_e32 vcc, s62, v11
s_waitcnt lgkmcnt(8)
Mv 252,192,188
buffer_load_dwordx4 v2, s[20:23], s40 offen offset:0 sc1 lds
s_cbranch_vccnz .Lzfx13
.Lzfr13:
Mp 128,0,48
s_mov_b32 s56, 0xc108000
buffer_load_dword v4, v7, s[20:23], s56 offen offset:4 sc0 sc1
Rb 48,8,6144
v_add_f32_e32 v12, v12, v14
Mp 136,0,52
v_add_f32_e32 v13, v13, v15
v_mov_b32 v24, v12
Mp 136,4,60
Rb 52,8,22528
v_permlane32_swap_b32_e32 v12, v24
Mp 128,4,56
buffer_load_dwordx4 v2, s[20:23], s40 offen offset:1024 sc1 lds
Mp 132,8,48
Rb 56,8,7168
v_add_f32_e32 v12, v12, v24
Mp 140,8,52
v_mov_b32 v24, v12
v_mov_b32 v25, v13
Mp 140,12,60
Rb 60,8,23552
s_waitcnt lgkmcnt(8)
Mp 132,12,56
buffer_load_dwordx4 v2, s[20:23], s40 offen offset:2048 sc1 lds
Mp 144,16,48
Rb 0,8,8192
v_permlane16_swap_b32_e32 v12, v24
Mp 152,16,52
v_add_f32_e32 v12, v12, v24
v_permlane32_swap_b32_e32 v13, v25
Mp 152,20,60
Rb 4,8,24576
v_add_f32_e32 v13, v13, v25
Mp 144,20,56
buffer_load_dwordx4 v2, s[20:23], s40 offen offset:3072 sc1 lds
Mp 148,24,48
Rb 8,8,9216
v_mov_b32 v25, v13
Mp 156,24,52
s_add_u32 m0, s36, 102400
v_permlane16_swap_b32_e32 v13, v25
Mp 156,28,60
Rb 12,8,25600
s_waitcnt lgkmcnt(8)
Mp 148,28,56
buffer_load_dwordx4 v3, s[20:23], s40 offen offset:0 sc1 lds
Mp 160,32,48
Rb 16,8,10240
v_add_f32_e32 v13, v13, v25
Mp 168,32,52
v_rcp_f32_e32 v12, v12
Mp 168,36,60
Rb 20,8,26624
)" R"(
v_mul_f32_e32 v128, v12, v128
Mp 160,36,56
buffer_load_dwordx4 v3, s[20:23], s40 offen offset:1024 sc1 lds
Mp 164,40,48
Rb 24,8,11264
v_mul_f32_e32 v129, v12, v129
Mp 172,40,52
v_rcp_f32_e32 v13, v13
Mp 172,44,60
Rb 28,8,27648
s_waitcnt lgkmcnt(8)
Mp 164,44,56
buffer_load_dwordx4 v3, s[20:23], s40 offen offset:2048 sc1 lds
Mp 176,48,48
Rb 32,8,12288
Cv 128,128,129
Mp 184,48,52
v_mul_f32_e32 v130, v12, v130
v_mul_f32_e32 v131, v12, v131
Mp 184,52,60
Rb 36,8,28672
Cv 129,130,131
Mp 176,52,56
v_mul_f32_e32 v132, v12, v132
v_mul_f32_e32 v133, v12, v133
Mp 180,56,48
Rb 40,8,13312
Cv 130,132,133
Mp 188,56,52
v_mul_f32_e32 v134, v12, v134
v_mul_f32_e32 v135, v12, v135
Mp 188,60,60
Rb 44,8,29696
s_waitcnt lgkmcnt(8)
Mp 180,60,56
Cv 131,134,135
v_mul_f32_e32 v136, v13, v136
Mp 192,0,48
Rb 48,8,14336
v_mul_f32_e32 v137, v13, v137
Mp 200,0,52
Cv 136,136,137
v_mul_f32_e32 v138, v13, v138
Mp 200,4,60
Rb 52,8,30720
v_mul_f32_e32 v139, v13, v139
Mp 192,4,56
Cv 137,138,139
v_mul_f32_e32 v140, v13, v140
Mp 196,8,48
Rb 56,8,15360
v_mul_f32_e32 v141, v13, v141
Mp 204,8,52
Cv 138,140,141
v_mul_f32_e32 v142, v13, v142
Mp 204,12,60
Rb 60,8,31744
s_waitcnt lgkmcnt(8)
Mp 196,12,56
v_mul_f32_e32 v143, v13, v143
Cv 139,142,143
Mp 208,16,48
Rb 0,1,0
v_mul_f32_e32 v144, v12, v144
)" R"(
Mp 216,16,52
v_mul_f32_e32 v145, v12, v145
Cv 144,144,145
Mp 216,20,60
Rb 4,1,1024
v_mul_f32_e32 v146, v12, v146
Mp 208,20,56
v_mul_f32_e32 v147, v12, v147
Cv 145,146,147
Mp 212,24,48
Rb 8,1,2048
v_mul_f32_e32 v148, v12, v148
Mp 220,24,52
v_mul_f32_e32 v149, v12, v149
Cv 146,148,149
Mp 220,28,60
Rb 12,1,3072
s_waitcnt vmcnt(6) lgkmcnt(4)
s_barrier
Mp 212,28,56
s_add_u32 s40, s67, 0x40000
v_mul_f32_e32 v150, v12, v150
v_cmp_eq_u32_e32 vcc, s3, v4
s_andn2_b64 s[28:29], 1, vcc
s_cbranch_scc1 .Lzsp19
.Lzok19:
Mp 224,32,48
Rb 16,1,4096
s_add_u32 m0, s36, 65536
buffer_load_dword v4, v7, s[20:23], s56 offen sc0 sc1
Mp 232,32,52
buffer_load_dwordx4 v2, s[20:23], s40 offen offset:0 sc1 lds
Mp 232,36,60
Rb 20,1,5120
v_mul_f32_e32 v151, v12, v151
Mp 224,36,56
Cv 147,150,151
v_mul_f32_e32 v152, v13, v152
Mp 228,40,48
Rb 24,1,6144
v_mul_f32_e32 v153, v13, v153
Mp 236,40,52
Cv 152,152,153
v_mul_f32_e32 v154, v13, v154
Mp 236,44,60
Rb 28,1,8192
Mp 228,44,56
buffer_load_dwordx4 v2, s[20:23], s40 offen offset:1024 sc1 lds
Mp 240,48,48
Rb 32,1,9216
v_mul_f32_e32 v155, v13, v155
Mp 248,48,52
Cv 153,154,155
v_mul_f32_e32 v156, v13, v156
Mp 248,52,60
Rb 36,1,10240
v_mul_f32_e32 v157, v13, v157
Mp 240,52,56
Cv 154,156,157
v_mul_f32_e32 v158, v13, v158
Mp 244,56,48
Rb 40,1,11264
v_mul_f32_e32 v159, v13, v159
Mp 252,56,52
buffer_load_dwordx4 v2, s[20:23], s40 offen offset:2048 sc1 lds
)" R"(
Mp 252,60,60
Rb 44,1,12288
s_waitcnt lgkmcnt(8)
Mp 244,60,56
Cv 155,158,159
v_mul_f32_e32 v160, v12, v160
v_mfma_f32_16x16x32_bf16 a[64:67], a[0:3], v[128:131], 0
Rb 48,1,13312
v_mul_f32_e32 v161, v12, v161
v_mfma_f32_16x16x32_bf16 a[72:75], a[0:3], v[136:139], 0
Cv 160,160,161
v_mul_f32_e32 v162, v12, v162
v_mfma_f32_16x16x32_bf16 a[76:79], a[4:7], v[136:139], 0
Rb 52,1,14336
v_mul_f32_e32 v163, v12, v163
v_mfma_f32_16x16x32_bf16 a[68:71], a[4:7], v[128:131], 0
buffer_load_dwordx4 v2, s[20:23], s40 offen offset:3072 sc1 lds
v_mfma_f32_16x16x32_bf16 a[80:83], a[8:11], v[128:131], 0
Rb 56,1,16384
Cv 161,162,163
v_mfma_f32_16x16x32_bf16 a[88:91], a[8:11], v[136:139], 0
v_mul_f32_e32 v164, v12, v164
v_mul_f32_e32 v165, v12, v165
v_mfma_f32_16x16x32_bf16 a[92:95], a[12:15], v[136:139], 0
Rb 60,1,17408
s_waitcnt lgkmcnt(8)
v_mfma_f32_16x16x32_bf16 a[84:87], a[12:15], v[128:131], 0
Cv 162,164,165
v_mul_f32_e32 v166, v12, v166
v_mfma_f32_16x16x32_bf16 a[96:99], a[16:19], v[128:131], 0
Rb 0,1,18432
s_add_u32 m0, s36, 69632
v_mfma_f32_16x16x32_bf16 a[104:107], a[16:19], v[136:139], 0
buffer_load_dwordx4 v3, s[20:23], s40 offen offset:0 sc1 lds
v_mfma_f32_16x16x32_bf16 a[108:111], a[20:23], v[136:139], 0
Rb 4,1,19456
v_mul_f32_e32 v167, v12, v167
v_mfma_f32_16x16x32_bf16 a[100:103], a[20:23], v[128:131], 0
Cv 163,166,167
v_mul_f32_e32 v168, v13, v168
v_mfma_f32_16x16x32_bf16 a[112:115], a[24:27], v[128:131], 0
Rb 8,1,20480
v_mul_f32_e32 v169, v13, v169
v_mfma_f32_16x16x32_bf16 a[120:123], a[24:27], v[136:139], 0
Cv 168,168,169
v_mul_f32_e32 v170, v13, v170
v_mfma_f32_16x16x32_bf16 a[124:127], a[28:31], v[136:139], 0
Rb 12,1,21504
s_waitcnt lgkmcnt(8)
v_mfma_f32_16x16x32_bf16 a[116:119], a[28:31], v[128:131], 0
buffer_load_dwordx4 v3, s[20:23], s40 offen offset:1024 sc1 lds
v_mfma_f32_16x16x32_bf16 a[128:131], a[32:35], v[128:131], 0
Rb 16,1,22528
v_mul_f32_e32 v171, v13, v171
v_mfma_f32_16x16x32_bf16 a[136:139], a[32:35], v[136:139], 0
Cv 169,170,171
v_mul_f32_e32 v172, v13, v172
v_mfma_f32_16x16x32_bf16 a[140:143], a[36:39], v[136:139], 0
Rb 20,1,24576
v_mul_f32_e32 v173, v13, v173
v_mfma_f32_16x16x32_bf16 a[132:135], a[36:39], v[128:131], 0
Cv 170,172,173
v_mul_f32_e32 v174, v13, v174
v_mfma_f32_16x16x32_bf16 a[144:147], a[40:43], v[128:131], 0
)" R"(
Rb 24,1,25600
v_mul_f32_e32 v175, v13, v175
v_mfma_f32_16x16x32_bf16 a[152:155], a[40:43], v[136:139], 0
buffer_load_dwordx4 v3, s[20:23], s40 offen offset:2048 sc1 lds
v_mfma_f32_16x16x32_bf16 a[156:159], a[44:47], v[136:139], 0
Rb 28,1,26624
s_waitcnt lgkmcnt(8)
v_mfma_f32_16x16x32_bf16 a[148:151], a[44:47], v[128:131], 0
Cv 171,174,175
v_mul_f32_e32 v176, v12, v176
v_mfma_f32_16x16x32_bf16 a[160:163], a[48:51], v[128:131], 0
Rb 32,1,27648
v_mul_f32_e32 v177, v12, v177
v_mfma_f32_16x16x32_bf16 a[168:171], a[48:51], v[136:139], 0
Cv 176,176,177
v_mul_f32_e32 v178, v12, v178
v_mfma_f32_16x16x32_bf16 a[172:175], a[52:55], v[136:139], 0
Rb 36,1,28672
v_mul_f32_e32 v179, v12, v179
v_mfma_f32_16x16x32_bf16 a[164:167], a[52:55], v[128:131], 0
Cv 177,178,179
v_mul_f32_e32 v180, v12, v180
v_mfma_f32_16x16x32_bf16 a[64:67], a[56:59], v[144:147], a[64:67]
Rb 40,1,29696
v_mul_f32_e32 v181, v12, v181
v_mfma_f32_16x16x32_bf16 a[72:75], a[56:59], v[152:155], a[72:75]
Cv 178,180,181
v_mul_f32_e32 v182, v12, v182
v_mfma_f32_16x16x32_bf16 a[76:79], a[60:63], v[152:155], a[76:79]
Rb 44,1,30720
s_waitcnt lgkmcnt(8)
v_mfma_f32_16x16x32_bf16 a[68:71], a[60:63], v[144:147], a[68:71]
v_mul_f32_e32 v183, v12, v183
Cv 179,182,183
v_mfma_f32_16x16x32_bf16 a[80:83], a[0:3], v[144:147], a[80:83]
Rb 48,1,32768
v_mul_f32_e32 v184, v13, v184
v_mfma_f32_16x16x32_bf16 a[88:91], a[0:3], v[152:155], a[88:91]
v_mul_f32_e32 v185, v13, v185
Cv 184,184,185
v_mfma_f32_16x16x32_bf16 a[92:95], a[4:7], v[152:155], a[92:95]
Rb 52,1,33792
v_mul_f32_e32 v186, v13, v186
v_mfma_f32_16x16x32_bf16 a[84:87], a[4:7], v[144:147], a[84:87]
v_mul_f32_e32 v187, v13, v187
Cv 185,186,187
v_mfma_f32_16x16x32_bf16 a[96:99], a[8:11], v[144:147], a[96:99]
Rb 56,1,34816
v_mul_f32_e32 v188, v13, v188
v_mfma_f32_16x16x32_bf16 a[104:107], a[8:11], v[152:155], a[104:107]
v_mul_f32_e32 v189, v13, v189
Cv 186,188,189
v_mfma_f32_16x16x32_bf16 a[108:111], a[12:15], v[152:155], a[108:111]
Rb 60,1,35840
s_waitcnt lgkmcnt(4)
s_barrier
v_mfma_f32_16x16x32_bf16 a[100:103], a[12:15], v[144:147], a[100:103]
s_add_u32 s40, s67, 0x50000
v_mul_f32_e32 v190, v13, v190
v_mfma_f32_16x16x32_bf16 a[112:115], a[16:19], v[144:147], a[112:115]
Rb 0,1,36864
s_add_u32 m0, s36, 0
v_mfma_f32_16x16x32_bf16 a[120:123], a[16:19], v[152:155], a[120:123]
buffer_load_dwordx4 v2, s[20:23], s40 offen offset:0 sc1 lds
v_mfma_f32_16x16x32_bf16 a[124:127], a[20:23], v[152:155], a[124:127]
)" R"(
Rb 4,1,37888
v_mul_f32_e32 v191, v13, v191
v_mfma_f32_16x16x32_bf16 a[116:119], a[20:23], v[144:147], a[116:119]
Cv 187,190,191
v_mul_f32_e32 v192, v12, v192
v_mfma_f32_16x16x32_bf16 a[128:131], a[24:27], v[144:147], a[128:131]
Rb 8,1,38912
v_mul_f32_e32 v193, v12, v193
v_mfma_f32_16x16x32_bf16 a[136:139], a[24:27], v[152:155], a[136:139]
Cv 192,192,193
v_mul_f32_e32 v194, v12, v194
v_mfma_f32_16x16x32_bf16 a[140:143], a[28:31], v[152:155], a[140:143]
Rb 12,1,40960
v_mfma_f32_16x16x32_bf16 a[132:135], a[28:31], v[144:147], a[132:135]
buffer_load_dwordx4 v2, s[20:23], s40 offen offset:1024 sc1 lds
v_mfma_f32_16x16x32_bf16 a[144:147], a[32:35], v[144:147], a[144:147]
Rb 16,1,41984
v_mul_f32_e32 v195, v12, v195
v_mfma_f32_16x16x32_bf16 a[152:155], a[32:35], v[152:155], a[152:155]
Cv 193,194,195
v_mul_f32_e32 v196, v12, v196
v_mfma_f32_16x16x32_bf16 a[156:159], a[36:39], v[152:155], a[156:159]
Rb 20,1,43008
v_mul_f32_e32 v197, v12, v197
v_mfma_f32_16x16x32_bf16 a[148:151], a[36:39], v[144:147], a[148:151]
Cv 194,196,197
v_mul_f32_e32 v198, v12, v198
v_mfma_f32_16x16x32_bf16 a[160:163], a[40:43], v[144:147], a[160:163]
Rb 24,1,44032
v_mul_f32_e32 v199, v12, v199
v_mfma_f32_16x16x32_bf16 a[168:171], a[40:43], v[152:155], a[168:171]
buffer_load_dwordx4 v2, s[20:23], s40 offen offset:2048 sc1 lds
v_mfma_f32_16x16x32_bf16 a[172:175], a[44:47], v[152:155], a[172:175]
Rb 28,1,45056
s_waitcnt lgkmcnt(8)
v_mfma_f32_16x16x32_bf16 a[164:167], a[44:47], v[144:147], a[164:167]
Cv 195,198,199
v_mul_f32_e32 v200, v13, v200
v_mfma_f32_16x16x32_bf16 a[64:67], a[48:51], v[160:163], a[64:67]
Rb 32,1,46080
v_mul_f32_e32 v201, v13, v201
v_mfma_f32_16x16x32_bf16 a[72:75], a[48:51], v[168:171], a[72:75]
Cv 200,200,201
v_mul_f32_e32 v202, v13, v202
v_mfma_f32_16x16x32_bf16 a[76:79], a[52:55], v[168:171], a[76:79]
Rb 36,1,47104
v_mul_f32_e32 v203, v13, v203
v_mfma_f32_16x16x32_bf16 a[68:71], a[52:55], v[160:163], a[68:71]
buffer_load_dwordx4 v2, s[20:23], s40 offen offset:3072 sc1 lds
v_mfma_f32_16x16x32_bf16 a[80:83], a[56:59], v[160:163], a[80:83]
Rb 40,1,49152
Cv 201,202,203
v_mfma_f32_16x16x32_bf16 a[88:91], a[56:59], v[168:171], a[88:91]
v_mul_f32_e32 v204, v13, v204
v_mul_f32_e32 v205, v13, v205
v_mfma_f32_16x16x32_bf16 a[92:95], a[60:63], v[168:171], a[92:95]
Rb 44,1,50176
s_waitcnt lgkmcnt(8)
v_mfma_f32_16x16x32_bf16 a[84:87], a[60:63], v[160:163], a[84:87]
Cv 202,204,205
v_mul_f32_e32 v206, v13, v206
v_mfma_f32_16x16x32_bf16 a[96:99], a[0:3], v[160:163], a[96:99]
Rb 48,1,51200
s_add_u32 m0, s36, 4096
)" R"(
v_mfma_f32_16x16x32_bf16 a[104:107], a[0:3], v[168:171], a[104:107]
buffer_load_dwordx4 v3, s[20:23], s40 offen offset:0 sc1 lds
v_mfma_f32_16x16x32_bf16 a[108:111], a[4:7], v[168:171], a[108:111]
Rb 52,1,52224
v_mul_f32_e32 v207, v13, v207
v_mfma_f32_16x16x32_bf16 a[100:103], a[4:7], v[160:163], a[100:103]
Cv 203,206,207
v_mul_f32_e32 v208, v12, v208
v_mfma_f32_16x16x32_bf16 a[112:115], a[8:11], v[160:163], a[112:115]
Rb 56,1,53248
v_mul_f32_e32 v209, v12, v209
v_mfma_f32_16x16x32_bf16 a[120:123], a[8:11], v[168:171], a[120:123]
Cv 208,208,209
v_mul_f32_e32 v210, v12, v210
v_mfma_f32_16x16x32_bf16 a[124:127], a[12:15], v[168:171], a[124:127]
Rb 60,1,54272
s_waitcnt lgkmcnt(8)
v_mfma_f32_16x16x32_bf16 a[116:119], a[12:15], v[160:163], a[116:119]
buffer_load_dwordx4 v3, s[20:23], s40 offen offset:1024 sc1 lds
v_mfma_f32_16x16x32_bf16 a[128:131], a[16:19], v[160:163], a[128:131]
Rb 0,1,55296
v_mul_f32_e32 v211, v12, v211
v_mfma_f32_16x16x32_bf16 a[136:139], a[16:19], v[168:171], a[136:139]
Cv 209,210,211
v_mul_f32_e32 v212, v12, v212
v_mfma_f32_16x16x32_bf16 a[140:143], a[20:23], v[168:171], a[140:143]
Rb 4,1,57344
v_mul_f32_e32 v213, v12, v213
v_mfma_f32_16x16x32_bf16 a[132:135], a[20:23], v[160:163], a[132:135]
Cv 210,212,213
v_mul_f32_e32 v214, v12, v214
v_mfma_f32_16x16x32_bf16 a[144:147], a[24:27], v[160:163], a[144:147]
Rb 8,1,58368
v_mul_f32_e32 v215, v12, v215
v_mfma_f32_16x16x32_bf16 a[152:155], a[24:27], v[168:171], a[152:155]
buffer_load_dwordx4 v3, s[20:23], s40 offen offset:2048 sc1 lds
v_mfma_f32_16x16x32_bf16 a[156:159], a[28:31], v[168:171], a[156:159]
Rb 12,1,59392
s_waitcnt lgkmcnt(8)
v_mfma_f32_16x16x32_bf16 a[148:151], a[28:31], v[160:163], a[148:151]
Cv 211,214,215
v_mul_f32_e32 v216, v13, v216
v_mfma_f32_16x16x32_bf16 a[160:163], a[32:35], v[160:163], a[160:163]
Rb 16,1,60416
v_mul_f32_e32 v217, v13, v217
v_mfma_f32_16x16x32_bf16 a[168:171], a[32:35], v[168:171], a[168:171]
Cv 216,216,217
v_mul_f32_e32 v218, v13, v218
v_mfma_f32_16x16x32_bf16 a[172:175], a[36:39], v[168:171], a[172:175]
Rb 20,1,61440
v_mul_f32_e32 v219, v13, v219
v_mfma_f32_16x16x32_bf16 a[164:167], a[36:39], v[160:163], a[164:167]
Cv 217,218,219
v_mul_f32_e32 v220, v13, v220
v_mfma_f32_16x16x32_bf16 a[64:67], a[40:43], v[176:179], a[64:67]
Rb 24,1,62464
v_mul_f32_e32 v221, v13, v221
v_mfma_f32_16x16x32_bf16 a[72:75], a[40:43], v[184:187], a[72:75]
Cv 218,220,221
v_mul_f32_e32 v222, v13, v222
v_mfma_f32_16x16x32_bf16 a[76:79], a[44:47], v[184:187], a[76:79]
Rb 28,1,63488
s_waitcnt lgkmcnt(8)
v_mfma_f32_16x16x32_bf16 a[68:71], a[44:47], v[176:179], a[68:71]
)" R"(
v_mul_f32_e32 v223, v13, v223
Cv 219,222,223
v_mfma_f32_16x16x32_bf16 a[80:83], a[48:51], v[176:179], a[80:83]
Rb 32,10,0
v_mul_f32_e32 v224, v12, v224
v_mfma_f32_16x16x32_bf16 a[88:91], a[48:51], v[184:187], a[88:91]
v_mul_f32_e32 v225, v12, v225
Cv 224,224,225
v_mfma_f32_16x16x32_bf16 a[92:95], a[52:55], v[184:187], a[92:95]
Rb 36,10,1024
v_mul_f32_e32 v226, v12, v226
v_mfma_f32_16x16x32_bf16 a[84:87], a[52:55], v[176:179], a[84:87]
v_mul_f32_e32 v227, v12, v227
Cv 225,226,227
v_mfma_f32_16x16x32_bf16 a[96:99], a[56:59], v[176:179], a[96:99]
Rb 40,10,2048
v_mul_f32_e32 v228, v12, v228
v_mfma_f32_16x16x32_bf16 a[104:107], a[56:59], v[184:187], a[104:107]
v_mul_f32_e32 v229, v12, v229
Cv 226,228,229
v_mfma_f32_16x16x32_bf16 a[108:111], a[60:63], v[184:187], a[108:111]
Rb 44,10,3072
s_waitcnt vmcnt(14) lgkmcnt(4)
s_barrier
v_mfma_f32_16x16x32_bf16 a[100:103], a[60:63], v[176:179], a[100:103]
s_add_u32 s40, s67, 0x60000
v_mul_f32_e32 v230, v12, v230
v_readfirstlane_b32 s63, v4
v_mfma_f32_16x16x32_bf16 a[112:115], a[0:3], v[176:179], a[112:115]
Rb 48,10,4096
s_add_u32 m0, s36, 32768
v_mfma_f32_16x16x32_bf16 a[120:123], a[0:3], v[184:187], a[120:123]
buffer_load_dwordx4 v2, s[20:23], s40 offen offset:0 sc1 lds
v_mfma_f32_16x16x32_bf16 a[124:127], a[4:7], v[184:187], a[124:127]
Rb 52,10,5120
v_mul_f32_e32 v231, v12, v231
v_mfma_f32_16x16x32_bf16 a[116:119], a[4:7], v[176:179], a[116:119]
Cv 227,230,231
v_mul_f32_e32 v232, v13, v232
v_mfma_f32_16x16x32_bf16 a[128:131], a[8:11], v[176:179], a[128:131]
Rb 56,10,6144
v_mul_f32_e32 v233, v13, v233
v_mfma_f32_16x16x32_bf16 a[136:139], a[8:11], v[184:187], a[136:139]
Cv 232,232,233
v_mul_f32_e32 v234, v13, v234
v_mfma_f32_16x16x32_bf16 a[140:143], a[12:15], v[184:187], a[140:143]
Rb 60,10,8192
v_mfma_f32_16x16x32_bf16 a[132:135], a[12:15], v[176:179], a[132:135]
buffer_load_dwordx4 v2, s[20:23], s40 offen offset:1024 sc1 lds
v_mfma_f32_16x16x32_bf16 a[144:147], a[16:19], v[176:179], a[144:147]
Rb 0,10,9216
v_mul_f32_e32 v235, v13, v235
v_mfma_f32_16x16x32_bf16 a[152:155], a[16:19], v[184:187], a[152:155]
Cv 233,234,235
v_mul_f32_e32 v236, v13, v236
v_mfma_f32_16x16x32_bf16 a[156:159], a[20:23], v[184:187], a[156:159]
Rb 4,10,10240
v_mul_f32_e32 v237, v13, v237
v_mfma_f32_16x16x32_bf16 a[148:151], a[20:23], v[176:179], a[148:151]
Cv 234,236,237
v_mul_f32_e32 v238, v13, v238
v_mfma_f32_16x16x32_bf16 a[160:163], a[24:27], v[176:179], a[160:163]
Rb 8,10,11264
v_mul_f32_e32 v239, v13, v239
v_mfma_f32_16x16x32_bf16 a[168:171], a[24:27], v[184:187], a[168:171]
)" R"(
buffer_load_dwordx4 v2, s[20:23], s40 offen offset:2048 sc1 lds
v_mfma_f32_16x16x32_bf16 a[172:175], a[28:31], v[184:187], a[172:175]
Rb 12,10,12288
s_waitcnt lgkmcnt(8)
v_mfma_f32_16x16x32_bf16 a[164:167], a[28:31], v[176:179], a[164:167]
Cv 235,238,239
v_mul_f32_e32 v240, v12, v240
v_mfma_f32_16x16x32_bf16 a[64:67], a[32:35], v[192:195], a[64:67]
Rb 16,10,13312
v_mul_f32_e32 v241, v12, v241
v_mfma_f32_16x16x32_bf16 a[72:75], a[32:35], v[200:203], a[72:75]
Cv 240,240,241
v_mul_f32_e32 v242, v12, v242
v_mfma_f32_16x16x32_bf16 a[76:79], a[36:39], v[200:203], a[76:79]
Rb 20,10,14336
v_mul_f32_e32 v243, v12, v243
v_mfma_f32_16x16x32_bf16 a[68:71], a[36:39], v[192:195], a[68:71]
buffer_load_dwordx4 v2, s[20:23], s40 offen offset:3072 sc1 lds
v_mfma_f32_16x16x32_bf16 a[80:83], a[40:43], v[192:195], a[80:83]
Rb 24,10,16384
Cv 241,242,243
v_mfma_f32_16x16x32_bf16 a[88:91], a[40:43], v[200:203], a[88:91]
v_mul_f32_e32 v244, v12, v244
v_mul_f32_e32 v245, v12, v245
v_mfma_f32_16x16x32_bf16 a[92:95], a[44:47], v[200:203], a[92:95]
Rb 28,10,17408
s_waitcnt lgkmcnt(8)
v_mfma_f32_16x16x32_bf16 a[84:87], a[44:47], v[192:195], a[84:87]
Cv 242,244,245
v_mul_f32_e32 v246, v12, v246
v_mfma_f32_16x16x32_bf16 a[96:99], a[48:51], v[192:195], a[96:99]
Rb 32,10,18432
s_add_u32 m0, s36, 36864
v_mfma_f32_16x16x32_bf16 a[104:107], a[48:51], v[200:203], a[104:107]
buffer_load_dwordx4 v3, s[20:23], s40 offen offset:0 sc1 lds
v_mfma_f32_16x16x32_bf16 a[108:111], a[52:55], v[200:203], a[108:111]
Rb 36,10,19456
v_mul_f32_e32 v247, v12, v247
v_mfma_f32_16x16x32_bf16 a[100:103], a[52:55], v[192:195], a[100:103]
Cv 243,246,247
v_mul_f32_e32 v248, v13, v248
v_mfma_f32_16x16x32_bf16 a[112:115], a[56:59], v[192:195], a[112:115]
Rb 40,10,20480
v_mul_f32_e32 v249, v13, v249
v_mfma_f32_16x16x32_bf16 a[120:123], a[56:59], v[200:203], a[120:123]
Cv 248,248,249
v_mul_f32_e32 v250, v13, v250
v_mfma_f32_16x16x32_bf16 a[124:127], a[60:63], v[200:203], a[124:127]
Rb 44,10,21504
s_waitcnt lgkmcnt(8)
v_mfma_f32_16x16x32_bf16 a[116:119], a[60:63], v[192:195], a[116:119]
buffer_load_dwordx4 v3, s[20:23], s40 offen offset:1024 sc1 lds
v_mfma_f32_16x16x32_bf16 a[128:131], a[0:3], v[192:195], a[128:131]
Rb 48,10,22528
v_mul_f32_e32 v251, v13, v251
v_mfma_f32_16x16x32_bf16 a[136:139], a[0:3], v[200:203], a[136:139]
Cv 249,250,251
v_mul_f32_e32 v252, v13, v252
v_mfma_f32_16x16x32_bf16 a[140:143], a[4:7], v[200:203], a[140:143]
Rb 52,10,24576
v_mul_f32_e32 v253, v13, v253
v_mfma_f32_16x16x32_bf16 a[132:135], a[4:7], v[192:195], a[132:135]
Cv 250,252,253
v_mul_f32_e32 v254, v13, v254
)" R"(
v_mfma_f32_16x16x32_bf16 a[144:147], a[8:11], v[192:195], a[144:147]
Rb 56,10,25600
v_mul_f32_e32 v255, v13, v255
v_mfma_f32_16x16x32_bf16 a[152:155], a[8:11], v[200:203], a[152:155]
buffer_load_dwordx4 v3, s[20:23], s40 offen offset:2048 sc1 lds
v_mfma_f32_16x16x32_bf16 a[156:159], a[12:15], v[200:203], a[156:159]
Rb 60,10,26624
s_waitcnt lgkmcnt(8)
v_mfma_f32_16x16x32_bf16 a[148:151], a[12:15], v[192:195], a[148:151]
Cv 251,254,255
v_mfma_f32_16x16x32_bf16 a[160:163], a[16:19], v[192:195], a[160:163]
Rb 0,10,27648
v_mfma_f32_16x16x32_bf16 a[168:171], a[16:19], v[200:203], a[168:171]
v_mfma_f32_16x16x32_bf16 a[172:175], a[20:23], v[200:203], a[172:175]
Rb 4,10,28672
v_mfma_f32_16x16x32_bf16 a[164:167], a[20:23], v[192:195], a[164:167]
v_mfma_f32_16x16x32_bf16 a[64:67], a[24:27], v[208:211], a[64:67]
Rb 8,10,29696
v_mfma_f32_16x16x32_bf16 a[72:75], a[24:27], v[216:219], a[72:75]
v_mfma_f32_16x16x32_bf16 a[76:79], a[28:31], v[216:219], a[76:79]
Rb 12,10,30720
s_waitcnt lgkmcnt(8)
v_mfma_f32_16x16x32_bf16 a[68:71], a[28:31], v[208:211], a[68:71]
v_mfma_f32_16x16x32_bf16 a[80:83], a[32:35], v[208:211], a[80:83]
Rb 16,8,32768
v_mfma_f32_16x16x32_bf16 a[88:91], a[32:35], v[216:219], a[88:91]
v_mfma_f32_16x16x32_bf16 a[92:95], a[36:39], v[216:219], a[92:95]
Rb 20,8,33792
v_mfma_f32_16x16x32_bf16 a[84:87], a[36:39], v[208:211], a[84:87]
v_mfma_f32_16x16x32_bf16 a[96:99], a[40:43], v[208:211], a[96:99]
Rb 24,8,34816
v_mfma_f32_16x16x32_bf16 a[104:107], a[40:43], v[216:219], a[104:107]
v_mfma_f32_16x16x32_bf16 a[108:111], a[44:47], v[216:219], a[108:111]
Rb 28,8,35840
s_waitcnt vmcnt(14) lgkmcnt(4)
s_barrier
v_mfma_f32_16x16x32_bf16 a[100:103], a[44:47], v[208:211], a[100:103]
s_add_u32 s40, s67, 0x70000
v_mfma_f32_16x16x32_bf16 a[112:115], a[48:51], v[208:211], a[112:115]
Rb 32,8,36864
s_add_u32 m0, s36, 131072
v_mfma_f32_16x16x32_bf16 a[120:123], a[48:51], v[216:219], a[120:123]
buffer_load_dwordx4 v2, s[20:23], s40 offen offset:0 sc1 lds
v_mfma_f32_16x16x32_bf16 a[124:127], a[52:55], v[216:219], a[124:127]
Rb 36,8,37888
v_mfma_f32_16x16x32_bf16 a[116:119], a[52:55], v[208:211], a[116:119]
v_mfma_f32_16x16x32_bf16 a[128:131], a[56:59], v[208:211], a[128:131]
Rb 40,8,38912
v_mfma_f32_16x16x32_bf16 a[136:139], a[56:59], v[216:219], a[136:139]
v_mfma_f32_16x16x32_bf16 a[140:143], a[60:63], v[216:219], a[140:143]
Rb 44,8,40960
v_mfma_f32_16x16x32_bf16 a[132:135], a[60:63], v[208:211], a[132:135]
buffer_load_dwordx4 v2, s[20:23], s40 offen offset:1024 sc1 lds
v_mfma_f32_16x16x32_bf16 a[144:147], a[0:3], v[208:211], a[144:147]
Rb 48,8,41984
v_mfma_f32_16x16x32_bf16 a[152:155], a[0:3], v[216:219], a[152:155]
v_mfma_f32_16x16x32_bf16 a[156:159], a[4:7], v[216:219], a[156:159]
Rb 52,8,43008
v_mfma_f32_16x16x32_bf16 a[148:151], a[4:7], v[208:211], a[148:151]
v_mfma_f32_16x16x32_bf16 a[160:163], a[8:11], v[208:211], a[160:163]
Rb 56,8,44032
v_mfma_f32_16x16x32_bf16 a[168:171], a[8:11], v[216:219], a[168:171]
buffer_load_dwordx4 v2, s[20:23], s40 offen offset:2048 sc1 lds
v_mfma_f32_16x16x32_bf16 a[172:175], a[12:15], v[216:219], a[172:175]
Rb 60,8,45056
)" R"(
s_waitcnt lgkmcnt(8)
v_mfma_f32_16x16x32_bf16 a[164:167], a[12:15], v[208:211], a[164:167]
v_mfma_f32_16x16x32_bf16 a[64:67], a[16:19], v[224:227], a[64:67]
Rb 0,8,46080
v_mfma_f32_16x16x32_bf16 a[72:75], a[16:19], v[232:235], a[72:75]
v_mfma_f32_16x16x32_bf16 a[76:79], a[20:23], v[232:235], a[76:79]
Rb 4,8,47104
v_mfma_f32_16x16x32_bf16 a[68:71], a[20:23], v[224:227], a[68:71]
buffer_load_dwordx4 v2, s[20:23], s40 offen offset:3072 sc1 lds
v_mfma_f32_16x16x32_bf16 a[80:83], a[24:27], v[224:227], a[80:83]
Rb 8,8,49152
v_mfma_f32_16x16x32_bf16 a[88:91], a[24:27], v[232:235], a[88:91]
v_mfma_f32_16x16x32_bf16 a[92:95], a[28:31], v[232:235], a[92:95]
Rb 12,8,50176
s_waitcnt lgkmcnt(8)
v_mfma_f32_16x16x32_bf16 a[84:87], a[28:31], v[224:227], a[84:87]
v_mfma_f32_16x16x32_bf16 a[96:99], a[32:35], v[224:227], a[96:99]
Rb 16,8,51200
s_add_u32 m0, s36, 135168
v_mfma_f32_16x16x32_bf16 a[104:107], a[32:35], v[232:235], a[104:107]
buffer_load_dwordx4 v3, s[20:23], s40 offen offset:0 sc1 lds
v_mfma_f32_16x16x32_bf16 a[108:111], a[36:39], v[232:235], a[108:111]
Rb 20,8,52224
v_mfma_f32_16x16x32_bf16 a[100:103], a[36:39], v[224:227], a[100:103]
v_mfma_f32_16x16x32_bf16 a[112:115], a[40:43], v[224:227], a[112:115]
Rb 24,8,53248
v_mfma_f32_16x16x32_bf16 a[120:123], a[40:43], v[232:235], a[120:123]
v_mfma_f32_16x16x32_bf16 a[124:127], a[44:47], v[232:235], a[124:127]
Rb 28,8,54272
s_waitcnt lgkmcnt(8)
v_mfma_f32_16x16x32_bf16 a[116:119], a[44:47], v[224:227], a[116:119]
buffer_load_dwordx4 v3, s[20:23], s40 offen offset:1024 sc1 lds
v_mfma_f32_16x16x32_bf16 a[128:131], a[48:51], v[224:227], a[128:131]
Rb 32,8,55296
v_mfma_f32_16x16x32_bf16 a[136:139], a[48:51], v[232:235], a[136:139]
v_mfma_f32_16x16x32_bf16 a[140:143], a[52:55], v[232:235], a[140:143]
Rb 36,8,57344
v_mfma_f32_16x16x32_bf16 a[132:135], a[52:55], v[224:227], a[132:135]
v_mfma_f32_16x16x32_bf16 a[144:147], a[56:59], v[224:227], a[144:147]
Rb 40,8,58368
v_mfma_f32_16x16x32_bf16 a[152:155], a[56:59], v[232:235], a[152:155]
buffer_load_dwordx4 v3, s[20:23], s40 offen offset:2048 sc1 lds
v_mfma_f32_16x16x32_bf16 a[156:159], a[60:63], v[232:235], a[156:159]
Rb 44,8,59392
s_waitcnt lgkmcnt(8)
v_mfma_f32_16x16x32_bf16 a[148:151], a[60:63], v[224:227], a[148:151]
v_mfma_f32_16x16x32_bf16 a[160:163], a[0:3], v[224:227], a[160:163]
Rb 48,8,60416
v_mfma_f32_16x16x32_bf16 a[168:171], a[0:3], v[232:235], a[168:171]
v_mfma_f32_16x16x32_bf16 a[172:175], a[4:7], v[232:235], a[172:175]
Rb 52,8,61440
v_mfma_f32_16x16x32_bf16 a[164:167], a[4:7], v[224:227], a[164:167]
v_mfma_f32_16x16x32_bf16 a[64:67], a[8:11], v[240:243], a[64:67]
Rb 56,8,62464
v_mfma_f32_16x16x32_bf16 a[72:75], a[8:11], v[248:251], a[72:75]
v_mfma_f32_16x16x32_bf16 a[76:79], a[12:15], v[248:251], a[76:79]
Rb 60,8,63488
s_waitcnt lgkmcnt(8)
v_mfma_f32_16x16x32_bf16 a[68:71], a[12:15], v[240:243], a[68:71]
v_mfma_f32_16x16x32_bf16 a[80:83], a[16:19], v[240:243], a[80:83]
Rb 0,8,0
v_mfma_f32_16x16x32_bf16 a[88:91], a[16:19], v[248:251], a[88:91]
v_mfma_f32_16x16x32_bf16 a[92:95], a[20:23], v[248:251], a[92:95]
Rb 4,8,1024
)" R"(
v_mfma_f32_16x16x32_bf16 a[84:87], a[20:23], v[240:243], a[84:87]
v_mfma_f32_16x16x32_bf16 a[96:99], a[24:27], v[240:243], a[96:99]
Rb 8,8,2048
v_mfma_f32_16x16x32_bf16 a[104:107], a[24:27], v[248:251], a[104:107]
v_mfma_f32_16x16x32_bf16 a[108:111], a[28:31], v[248:251], a[108:111]
Rb 12,8,3072
s_waitcnt vmcnt(14) lgkmcnt(8)
s_barrier
v_mfma_f32_16x16x32_bf16 a[100:103], a[28:31], v[240:243], a[100:103]
v_mfma_f32_16x16x32_bf16 a[112:115], a[32:35], v[240:243], a[112:115]
Rb 16,8,4096
v_mfma_f32_16x16x32_bf16 a[120:123], a[32:35], v[248:251], a[120:123]
v_mfma_f32_16x16x32_bf16 a[124:127], a[36:39], v[248:251], a[124:127]
Rb 20,8,5120
v_mfma_f32_16x16x32_bf16 a[116:119], a[36:39], v[240:243], a[116:119]
v_mfma_f32_16x16x32_bf16 a[128:131], a[40:43], v[240:243], a[128:131]
Rb 24,8,6144
v_mfma_f32_16x16x32_bf16 a[136:139], a[40:43], v[248:251], a[136:139]
v_mfma_f32_16x16x32_bf16 a[140:143], a[44:47], v[248:251], a[140:143]
Rb 28,8,8192
s_waitcnt lgkmcnt(8)
v_mfma_f32_16x16x32_bf16 a[132:135], a[44:47], v[240:243], a[132:135]
v_mfma_f32_16x16x32_bf16 a[144:147], a[48:51], v[240:243], a[144:147]
Rb 32,8,9216
v_mfma_f32_16x16x32_bf16 a[152:155], a[48:51], v[248:251], a[152:155]
v_mfma_f32_16x16x32_bf16 a[156:159], a[52:55], v[248:251], a[156:159]
Rb 36,8,10240
v_mfma_f32_16x16x32_bf16 a[148:151], a[52:55], v[240:243], a[148:151]
v_mfma_f32_16x16x32_bf16 a[160:163], a[56:59], v[240:243], a[160:163]
Rb 40,8,11264
v_mfma_f32_16x16x32_bf16 a[168:171], a[56:59], v[248:251], a[168:171]
v_mfma_f32_16x16x32_bf16 a[172:175], a[60:63], v[248:251], a[172:175]
Rb 44,8,12288
s_waitcnt lgkmcnt(8)
v_mfma_f32_16x16x32_bf16 a[164:167], a[60:63], v[240:243], a[164:167]
v_mfma_f32_16x16x32_bf16 v[32:35], a[0:3], v[128:131], 0
Rb 48,8,13312
v_mfma_f32_16x16x32_bf16 v[40:43], a[0:3], v[136:139], 0
Mp 40,4,152
Rb 52,8,14336
v_accvgpr_read_b32 v112, a64
Mp 32,4,144
v_accvgpr_read_b32 v24, a65
Cv 112,112,24
Mp 32,8,160
Rb 56,8,16384
v_accvgpr_read_b32 v113, a66
Mp 40,8,168
v_accvgpr_read_b32 v25, a67
Cv 113,113,25
Mp 40,12,184
Rb 60,8,17408
s_waitcnt lgkmcnt(8)
Mp 32,12,176
v_accvgpr_read_b32 v114, a68
v_accvgpr_read_b32 v26, a69
Mp 32,16,192
Rb 0,8,18432
Cv 114,114,26
Mp 40,16,200
v_accvgpr_read_b32 v115, a70
v_accvgpr_read_b32 v27, a71
Mp 40,20,216
Rb 4,8,19456
Cv 115,115,27
)" R"(
Mp 32,20,208
s_add_u32 s29, s73, 0x0
buffer_store_dwordx4 v[112:115], v1, s[20:23], s29 offen sc0 sc1
Mp 32,24,224
Rb 8,8,20480
v_accvgpr_read_b32 v116, a72
Mp 40,24,232
v_accvgpr_read_b32 v24, a73
Cv 116,116,24
Mp 40,28,248
Rb 12,8,21504
s_waitcnt lgkmcnt(8)
Mp 32,28,240
v_accvgpr_read_b32 v117, a74
v_accvgpr_read_b32 v25, a75
v_mfma_f32_16x16x32_bf16 v[36:39], a[32:35], v[128:131], 0
Rb 16,8,22528
Cv 117,117,25
v_mfma_f32_16x16x32_bf16 v[44:47], a[32:35], v[136:139], 0
v_accvgpr_read_b32 v118, a76
v_accvgpr_read_b32 v26, a77
Cv 118,118,26
Mp 44,36,152
Rb 20,8,24576
v_accvgpr_read_b32 v119, a78
Mp 36,36,144
v_accvgpr_read_b32 v27, a79
Cv 119,119,27
Mp 36,40,160
Rb 24,8,25600
v_accvgpr_read_b32 v120, a80
Mp 44,40,168
s_add_u32 s29, s73, 0x400
buffer_store_dwordx4 v[116:119], v1, s[20:23], s29 offen sc0 sc1
Mp 44,44,184
Rb 28,8,26624
s_waitcnt lgkmcnt(8)
Mp 36,44,176
v_accvgpr_read_b32 v24, a81
Cv 120,120,24
Mp 36,48,192
Rb 32,8,27648
v_accvgpr_read_b32 v121, a82
Mp 44,48,200
v_accvgpr_read_b32 v25, a83
Cv 121,121,25
Mp 44,52,216
Rb 36,8,28672
v_accvgpr_read_b32 v122, a84
Mp 36,52,208
v_accvgpr_read_b32 v26, a85
Cv 122,122,26
Mp 36,56,224
Rb 40,8,29696
v_accvgpr_read_b32 v123, a86
Mp 44,56,232
v_accvgpr_read_b32 v27, a87
Cv 123,123,27
Mp 44,60,248
Rb 44,8,30720
s_waitcnt lgkmcnt(8)
Mp 36,60,240
s_add_u32 s29, s73, 0x800
buffer_store_dwordx4 v[120:123], v1, s[20:23], s29 offen sc0 sc1
)" R"(
v_mfma_f32_16x16x32_bf16 v[48:51], a[0:3], v[128:131], 0
Rb 48,1,0
v_accvgpr_read_b32 v124, a88
v_mfma_f32_16x16x32_bf16 v[56:59], a[0:3], v[136:139], 0
v_accvgpr_read_b32 v24, a89
Cv 124,124,24
v_accvgpr_read_b32 v125, a90
Mp 56,4,152
Rb 52,1,1024
v_accvgpr_read_b32 v25, a91
Mp 48,4,144
Cv 125,125,25
v_accvgpr_read_b32 v126, a92
Mp 48,8,160
Rb 56,1,2048
Cv 32,32,33
Mp 56,8,168
Cv 33,34,35
Cv 34,36,37
Mp 56,12,184
Rb 60,1,3072
s_waitcnt vmcnt(10) lgkmcnt(8)
s_barrier
Mp 48,12,176
Cv 35,38,39
v_accvgpr_read_b32 v26, a93
Mp 48,16,192
Rb 0,1,4096
Cv 126,126,26
Mp 56,16,200
s_add_u32 s29, s73, 0x8000
buffer_store_dwordx4 v[32:35], v1, s[20:23], s29 offen sc0 sc1
Mp 56,20,216
Rb 4,1,5120
v_accvgpr_read_b32 v127, a94
Mp 48,20,208
v_accvgpr_read_b32 v27, a95
Cv 127,127,27
Mp 48,24,224
Rb 8,1,6144
v_accvgpr_read_b32 v112, a96
Mp 56,24,232
s_add_u32 s29, s73, 0xc00
buffer_store_dwordx4 v[124:127], v1, s[20:23], s29 offen sc0 sc1
Mp 56,28,248
Rb 12,1,8192
s_waitcnt lgkmcnt(8)
Mp 48,28,240
v_accvgpr_read_b32 v24, a97
Cv 112,112,24
v_mfma_f32_16x16x32_bf16 v[52:55], a[32:35], v[128:131], 0
Rb 16,1,9216
Cv 40,40,41
v_mfma_f32_16x16x32_bf16 v[60:63], a[32:35], v[136:139], 0
Cv 41,42,43
Cv 42,44,45
v_accvgpr_read_b32 v113, a98
Mp 60,36,152
Rb 20,1,10240
Cv 43,46,47
Mp 52,36,144
s_add_u32 s29, s73, 0x8400
buffer_store_dwordx4 v[40:43], v1, s[20:23], s29 offen sc0 sc1
Mp 52,40,160
Rb 24,1,11264
)" R"(
v_accvgpr_read_b32 v25, a99
Mp 60,40,168
Cv 113,113,25
v_accvgpr_read_b32 v114, a100
Mp 60,44,184
Rb 28,1,12288
s_waitcnt lgkmcnt(8)
Mp 52,44,176
v_accvgpr_read_b32 v26, a101
Cv 114,114,26
Mp 52,48,192
Rb 32,1,13312
v_accvgpr_read_b32 v115, a102
Mp 60,48,200
v_accvgpr_read_b32 v27, a103
Cv 115,115,27
Mp 60,52,216
Rb 36,1,14336
v_accvgpr_read_b32 v116, a104
Mp 52,52,208
s_add_u32 s29, s73, 0x1000
buffer_store_dwordx4 v[112:115], v1, s[20:23], s29 offen sc0 sc1
Mp 52,56,224
Rb 40,1,16384
v_accvgpr_read_b32 v24, a105
Mp 60,56,232
Cv 116,116,24
v_accvgpr_read_b32 v117, a106
Mp 60,60,248
Rb 44,1,17408
s_waitcnt lgkmcnt(8)
Mp 52,60,240
v_accvgpr_read_b32 v25, a107
Cv 117,117,25
v_mfma_f32_16x16x32_bf16 v[64:67], a[0:3], v[128:131], 0
Rb 48,1,18432
v_accvgpr_read_b32 v118, a108
v_mfma_f32_16x16x32_bf16 v[72:75], a[0:3], v[136:139], 0
v_accvgpr_read_b32 v26, a109
Cv 118,118,26
v_accvgpr_read_b32 v119, a110
Mp 72,4,152
Rb 52,1,19456
v_accvgpr_read_b32 v27, a111
Mp 64,4,144
Cv 119,119,27
v_accvgpr_read_b32 v120, a112
Mp 64,8,160
Rb 56,1,20480
Cv 48,48,49
Mp 72,8,168
Cv 49,50,51
Cv 50,52,53
Mp 72,12,184
Rb 60,1,21504
s_waitcnt lgkmcnt(8)
Mp 64,12,176
Cv 51,54,55
v_accvgpr_read_b32 v24, a113
Mp 64,16,192
Rb 0,1,22528
Cv 120,120,24
Mp 72,16,200
s_add_u32 s29, s73, 0x8800
)" R"(
buffer_store_dwordx4 v[48:51], v1, s[20:23], s29 offen sc0 sc1
Mp 72,20,216
Rb 4,1,24576
v_accvgpr_read_b32 v121, a114
Mp 64,20,208
s_add_u32 s29, s73, 0x1400
buffer_store_dwordx4 v[116:119], v1, s[20:23], s29 offen sc0 sc1
Mp 64,24,224
Rb 8,1,25600
v_accvgpr_read_b32 v25, a115
Mp 72,24,232
Cv 121,121,25
v_accvgpr_read_b32 v122, a116
Mp 72,28,248
Rb 12,1,26624
s_waitcnt lgkmcnt(8)
Mp 64,28,240
v_accvgpr_read_b32 v26, a117
Cv 122,122,26
v_mfma_f32_16x16x32_bf16 v[68:71], a[32:35], v[128:131], 0
Rb 16,1,27648
Cv 56,56,57
v_mfma_f32_16x16x32_bf16 v[76:79], a[32:35], v[136:139], 0
Cv 57,58,59
Cv 58,60,61
v_accvgpr_read_b32 v123, a118
Mp 76,36,152
Rb 20,1,28672
Cv 59,62,63
Mp 68,36,144
s_add_u32 s29, s73, 0x8c00
buffer_store_dwordx4 v[56:59], v1, s[20:23], s29 offen sc0 sc1
Mp 68,40,160
Rb 24,1,29696
v_accvgpr_read_b32 v27, a119
Mp 76,40,168
Cv 123,123,27
v_accvgpr_read_b32 v124, a120
Mp 76,44,184
Rb 28,1,30720
s_waitcnt lgkmcnt(8)
Mp 68,44,176
s_add_u32 s29, s73, 0x1800
buffer_store_dwordx4 v[120:123], v1, s[20:23], s29 offen sc0 sc1
Mp 68,48,192
Rb 32,1,32768
v_accvgpr_read_b32 v24, a121
Mp 76,48,200
Cv 124,124,24
v_accvgpr_read_b32 v125, a122
Mp 76,52,216
Rb 36,1,33792
v_accvgpr_read_b32 v25, a123
Mp 68,52,208
Cv 125,125,25
v_accvgpr_read_b32 v126, a124
Mp 68,56,224
Rb 40,1,34816
v_accvgpr_read_b32 v26, a125
Mp 76,56,232
Cv 126,126,26
v_accvgpr_read_b32 v127, a126
Mp 76,60,248
Rb 44,1,35840
)" R"(
s_waitcnt vmcnt(11) lgkmcnt(8)
s_barrier
Mp 68,60,240
v_accvgpr_read_b32 v27, a127
Cv 127,127,27
v_mfma_f32_16x16x32_bf16 v[80:83], a[0:3], v[128:131], 0
Rb 48,1,36864
v_accvgpr_read_b32 v112, a128
v_mfma_f32_16x16x32_bf16 v[88:91], a[0:3], v[136:139], 0
s_add_u32 s29, s73, 0x4000
buffer_store_dwordx4 v[124:127], v1, s[20:23], s29 offen sc0 sc1
Mp 88,4,152
Rb 52,1,37888
v_accvgpr_read_b32 v24, a129
Mp 80,4,144
Cv 112,112,24
v_accvgpr_read_b32 v113, a130
Mp 80,8,160
Rb 56,1,38912
Cv 64,64,65
Mp 88,8,168
Cv 65,66,67
Cv 66,68,69
Mp 88,12,184
Rb 60,1,40960
s_waitcnt lgkmcnt(8)
Mp 80,12,176
Cv 67,70,71
v_accvgpr_read_b32 v25, a131
Mp 80,16,192
Rb 0,1,41984
Cv 113,113,25
Mp 88,16,200
s_add_u32 s29, s73, 0x9000
buffer_store_dwordx4 v[64:67], v1, s[20:23], s29 offen sc0 sc1
Mp 88,20,216
Rb 4,1,43008
v_accvgpr_read_b32 v114, a132
Mp 80,20,208
v_accvgpr_read_b32 v26, a133
Cv 114,114,26
Mp 80,24,224
Rb 8,1,44032
v_accvgpr_read_b32 v115, a134
Mp 88,24,232
v_accvgpr_read_b32 v27, a135
Cv 115,115,27
Mp 88,28,248
Rb 12,1,45056
s_waitcnt lgkmcnt(8)
Mp 80,28,240
s_add_u32 s29, s73, 0x4400
buffer_store_dwordx4 v[112:115], v1, s[20:23], s29 offen sc0 sc1
v_mfma_f32_16x16x32_bf16 v[84:87], a[32:35], v[128:131], 0
Rb 16,1,46080
Cv 72,72,73
v_mfma_f32_16x16x32_bf16 v[92:95], a[32:35], v[136:139], 0
Cv 73,74,75
Cv 74,76,77
v_accvgpr_read_b32 v116, a136
Mp 92,36,152
Rb 20,1,47104
Cv 75,78,79
Mp 84,36,144
s_add_u32 s29, s73, 0x9400
)" R"(
buffer_store_dwordx4 v[72:75], v1, s[20:23], s29 offen sc0 sc1
Mp 84,40,160
Rb 24,1,49152
v_accvgpr_read_b32 v24, a137
Mp 92,40,168
Cv 116,116,24
v_accvgpr_read_b32 v117, a138
Mp 92,44,184
Rb 28,1,50176
s_waitcnt lgkmcnt(8)
Mp 84,44,176
v_accvgpr_read_b32 v25, a139
Cv 117,117,25
Mp 84,48,192
Rb 32,1,51200
v_accvgpr_read_b32 v118, a140
Mp 92,48,200
v_accvgpr_read_b32 v26, a141
Cv 118,118,26
Mp 92,52,216
Rb 36,1,52224
v_accvgpr_read_b32 v119, a142
Mp 84,52,208
v_accvgpr_read_b32 v27, a143
Cv 119,119,27
Mp 84,56,224
Rb 40,1,53248
v_accvgpr_read_b32 v120, a144
Mp 92,56,232
s_add_u32 s29, s73, 0x4800
buffer_store_dwordx4 v[116:119], v1, s[20:23], s29 offen sc0 sc1
Mp 92,60,248
Rb 44,1,54272
s_waitcnt lgkmcnt(8)
Mp 84,60,240
v_accvgpr_read_b32 v24, a145
Cv 120,120,24
v_mfma_f32_16x16x32_bf16 v[96:99], a[0:3], v[128:131], 0
Rb 48,1,55296
v_accvgpr_read_b32 v121, a146
v_mfma_f32_16x16x32_bf16 v[104:107], a[0:3], v[136:139], 0
v_accvgpr_read_b32 v25, a147
Cv 121,121,25
v_accvgpr_read_b32 v122, a148
Mp 104,4,152
Rb 52,1,57344
v_accvgpr_read_b32 v26, a149
Mp 96,4,144
Cv 122,122,26
v_accvgpr_read_b32 v123, a150
Mp 96,8,160
Rb 56,1,58368
Cv 80,80,81
Mp 104,8,168
Cv 81,82,83
Cv 82,84,85
Mp 104,12,184
Rb 60,1,59392
s_waitcnt lgkmcnt(8)
Mp 96,12,176
Cv 83,86,87
v_accvgpr_read_b32 v27, a151
Mp 96,16,192
Rb 0,1,60416
)" R"(
Cv 123,123,27
Mp 104,16,200
s_add_u32 s29, s73, 0x9800
buffer_store_dwordx4 v[80:83], v1, s[20:23], s29 offen sc0 sc1
Mp 104,20,216
Rb 4,1,61440
v_accvgpr_read_b32 v124, a152
Mp 96,20,208
s_add_u32 s29, s73, 0x4c00
buffer_store_dwordx4 v[120:123], v1, s[20:23], s29 offen sc0 sc1
Mp 96,24,224
Rb 8,1,62464
v_accvgpr_read_b32 v24, a153
Mp 104,24,232
Cv 124,124,24
v_accvgpr_read_b32 v125, a154
Mp 104,28,248
Rb 12,1,63488
s_waitcnt lgkmcnt(8)
Mp 96,28,240
v_accvgpr_read_b32 v25, a155
Cv 125,125,25
v_mfma_f32_16x16x32_bf16 v[100:103], a[32:35], v[128:131], 0
Rb 16,10,0
Cv 88,88,89
v_mfma_f32_16x16x32_bf16 v[108:111], a[32:35], v[136:139], 0
Cv 89,90,91
Cv 90,92,93
v_accvgpr_read_b32 v126, a156
Mp 108,36,152
Rb 20,10,1024
Cv 91,94,95
Mp 100,36,144
s_add_u32 s29, s73, 0xc000
buffer_store_dwordx4 v[88:91], v1, s[20:23], s29 offen sc0 sc1
Mp 100,40,160
Rb 24,10,2048
v_accvgpr_read_b32 v26, a157
Mp 108,40,168
Cv 126,126,26
v_accvgpr_read_b32 v127, a158
Mp 108,44,184
Rb 28,10,3072
s_waitcnt lgkmcnt(8)
Mp 100,44,176
v_accvgpr_read_b32 v27, a159
Cv 127,127,27
Mp 100,48,192
Rb 32,10,4096
v_accvgpr_read_b32 v112, a160
Mp 108,48,200
s_add_u32 s29, s73, 0x5000
buffer_store_dwordx4 v[124:127], v1, s[20:23], s29 offen sc0 sc1
Mp 108,52,216
Rb 36,10,5120
v_accvgpr_read_b32 v24, a161
Mp 100,52,208
Cv 112,112,24
v_accvgpr_read_b32 v113, a162
Mp 100,56,224
Rb 40,10,6144
v_accvgpr_read_b32 v25, a163
Mp 108,56,232
Cv 113,113,25
)" R"(
v_accvgpr_read_b32 v114, a164
Mp 108,60,248
Rb 44,10,8192
s_waitcnt lgkmcnt(8)
Mp 100,60,240
v_accvgpr_read_b32 v26, a165
Cv 114,114,26
v_mfma_f32_16x16x32_bf16 v[132:135], a[0:3], v[128:131], 0
Rb 48,10,9216
v_accvgpr_read_b32 v115, a166
v_mfma_f32_16x16x32_bf16 v[148:151], a[0:3], v[136:139], 0
v_accvgpr_read_b32 v27, a167
Cv 115,115,27
v_accvgpr_read_b32 v116, a168
Mp 148,4,152
Rb 52,10,10240
v_accvgpr_read_b32 v24, a169
Mp 132,4,144
s_add_u32 s29, s73, 0x5400
buffer_store_dwordx4 v[112:115], v1, s[20:23], s29 offen sc0 sc1
Mp 132,8,160
Rb 56,10,11264
Cv 96,96,97
Mp 148,8,168
Cv 97,98,99
Cv 98,100,101
Mp 148,12,184
Rb 60,10,12288
s_waitcnt lgkmcnt(8)
Mp 132,12,176
Cv 99,102,103
Cv 116,116,24
Mp 132,16,192
Rb 0,10,13312
v_accvgpr_read_b32 v117, a170
Mp 148,16,200
s_add_u32 s29, s73, 0xc400
buffer_store_dwordx4 v[96:99], v1, s[20:23], s29 offen sc0 sc1
Mp 148,20,216
Rb 4,10,14336
v_accvgpr_read_b32 v25, a171
Mp 132,20,208
Cv 117,117,25
v_accvgpr_read_b32 v118, a172
Mp 132,24,224
Rb 8,10,16384
v_accvgpr_read_b32 v26, a173
Mp 148,24,232
Cv 118,118,26
v_accvgpr_read_b32 v119, a174
Mp 148,28,248
Rb 12,10,17408
s_waitcnt lgkmcnt(8)
Mp 132,28,240
v_accvgpr_read_b32 v27, a175
Cv 119,119,27
v_mfma_f32_16x16x32_bf16 v[140:143], a[32:35], v[128:131], 0
Rb 16,10,18432
Cv 104,104,105
v_mfma_f32_16x16x32_bf16 v[156:159], a[32:35], v[136:139], 0
Cv 105,106,107
Cv 106,108,109
Mp 156,36,152
Rb 20,10,19456
)" R"(
Cv 107,110,111
Mp 140,36,144
s_add_u32 s29, s73, 0xc800
buffer_store_dwordx4 v[104:107], v1, s[20:23], s29 offen sc0 sc1
Mp 140,40,160
Rb 24,10,20480
Mp 156,40,168
s_add_u32 s29, s73, 0x5800
buffer_store_dwordx4 v[116:119], v1, s[20:23], s29 offen sc0 sc1
Mp 156,44,184
Rb 28,10,21504
s_waitcnt lgkmcnt(8)
Mp 140,44,176
Mp 140,48,192
Rb 32,10,22528
Mp 156,48,200
Mp 156,52,216
Rb 36,10,24576
Mp 140,52,208
Mp 140,56,224
Rb 40,10,25600
Mp 156,56,232
Mp 156,60,248
Rb 44,10,26624
s_waitcnt lgkmcnt(8)
Mp 140,60,240
v_mfma_f32_16x16x32_bf16 v[164:167], a[0:3], v[128:131], 0
Rb 48,10,27648
v_mfma_f32_16x16x32_bf16 v[180:183], a[0:3], v[136:139], 0
Mp 180,4,152
Rb 52,10,28672
Mp 164,4,144
Mp 164,8,160
Rb 56,10,29696
Cv 132,132,133
Mp 180,8,168
Cv 133,134,135
Cv 134,140,141
Mp 180,12,184
Rb 60,10,30720
s_waitcnt lgkmcnt(8)
Mp 164,12,176
Cv 135,142,143
Mp 164,16,192
s_add_u32 s29, s73, 0xcc00
buffer_store_dwordx4 v[132:135], v1, s[20:23], s29 offen sc0 sc1
Mp 180,16,200
Mp 180,20,216
Mp 164,20,208
Mp 164,24,224
Mp 180,24,232
Mp 180,28,248
s_waitcnt lgkmcnt(4)
Mp 164,28,240
v_mfma_f32_16x16x32_bf16 v[172:175], a[32:35], v[128:131], 0
Cv 148,148,149
Cv 149,150,151
v_mfma_f32_16x16x32_bf16 v[188:191], a[32:35], v[136:139], 0
Cv 150,156,157
Cv 151,158,159
Mp 188,36,152
s_add_u32 s29, s73, 0xd000
buffer_store_dwordx4 v[148:151], v1, s[20:23], s29 offen sc0 sc1
Mp 172,36,144
)" R"(
Mp 172,40,160
Mp 188,40,168
Mp 188,44,184
s_waitcnt lgkmcnt(0)
Mp 172,44,176
Mp 172,48,192
Mp 188,48,200
Mp 188,52,216
Mp 172,52,208
Mp 172,56,224
Mp 188,56,232
Mp 188,60,248
s_waitcnt vmcnt(2)
buffer_store_dword v6, v7, s[20:23], s68 offen offset:0 sc0 sc1
Mp 172,60,240
s_nop 7
Cv 164,164,165
Cv 165,166,167
Cv 166,172,173
Cv 167,174,175
s_add_u32 s29, s73, 0xd400
buffer_store_dwordx4 v[164:167], v1, s[20:23], s29 offen sc0 sc1
Cv 180,180,181
Cv 181,182,183
Cv 182,188,189
Cv 183,190,191
s_add_u32 s29, s73, 0xd800
buffer_store_dwordx4 v[180:183], v1, s[20:23], s29 offen sc0 sc1
.LzEND:
v_lshl_or_b32 v0, s33, 6, v0
s_add_u32 s4, s68, 4
s_mov_b32 s5, s63
s_mov_b32 s6, s84
s_mov_b32 s8, s74
s_mov_b32 s9, s75
s_mov_b32 s10, 0x1c0000
s_mov_b32 s11, 0x20000
)" : "+{s[0:1]}"(ka), "+{s2}"(bid), "+{v0}"(tid), "={s3}"(tag), "={s4}"(sfw), "={s5}"(idx), "={s6}"(tk), "={s[12:15]}"(rX), "={s[20:23]}"(rO), "={s[8:11]}"(rOut) :
   : "memory", "scc", "vcc", "v1", "v2", "v3", "v4", "v5", "v6", "v7", "v8", "v9", R256("v"), ACL,
     "s7", "s16", "s17", "s18", "s19", "s24", "s25", "s26", "s27", "s28", "s29", "s30", "s31", "s33", "s34", "s35", "s36", "s37", "s38", "s39",
     V10("s4"), V10("s5"), V10("s6"), V10("s7"), V10("s8"), V10("s9"));
  bid = rfl(bid); tag = rfl(tag); sfw = rfl(sfw); rX = rfl4(rX); rO = rfl4(rO); rOut = rfl4(rOut);
  const u32 lane = tid & 63, wid = rfl(tid >> 6), r = lane & 15, g = lane >> 4, w = ((bid >> 3) & 15u) + 16u * wid;
  const bool B = bid >= 128u;
  idx = rfl(idx);
  Task k[NS]; u32 vf = OOB, rt = 0u, pend = 0u;
#pragma unroll
  for (int s = 0; s < NS; ++s) {
    const u32 i = B ? (s ? 168u + w : 104u + w) : w + 64u * s, cls = i / 56u, e = i - cls * 56u;
    const bool on = i < 224u && (!B || i >= 128u);
    const u32 blk = (cls >> 1) * 32u + (e / 14u) * 8u + (bid & 7u), G = e % 14u + (cls & 1u) * 14u, r0 = blk * 32u + (G & 1u) * 16u;
    k[s].sy = rfl((u32)WS_Y + blk * 65536u + (G + (G / 7u) * 9u) * 1024u + ((rfl(tk) ^ (bid << 10) ^ (cls << 12)) & 8192u));
    k[s].vx = on ? (r0 + r) * (M_ * 2) + (G >> 1) * 64u + g * 8u : OOB;
    if (g == s) vf = (blk * 16u + r) * 128u + (cls & 1u) * 4u;
    if (on) pend |= 0x1111u << (16 * s);
    if (on) rt |= (((idx & 0xffffu) >> 4 == r0 >> 4 ? 1u : 0u) | (idx >> 20 == r0 >> 4 ? 2u : 0u)) << (2 * s);
    asm volatile("buffer_load_dwordx2 %0, %3, %4, 0 offen\n\tbuffer_load_dwordx2 %1, %3, %4, 0 offen offset:32\n\tbuffer_load_dword %2, %5, %6, %7 offen sc0 sc1"
                 : "=&v"(k[s].x[0]), "=&v"(k[s].x[1]), "=&v"(k[s].g) : "v"(k[s].vx), "s"(rX), "v"((r0 + r) * 16u), "s"(rO), "s"((u32)WS_KEY + 8u) : "memory");
  }
  u32x4 sel[2];
#pragma unroll
  for (int b = 0; b < 2; ++b)
#pragma unroll
    for (int q = 0; q < 4; ++q) sel[b][q] = (r >> 1) == 4u * b + q ? ((r & 1u) ? 0x3f800000u : 0x3f80u) : 0u;
  asm volatile("s_waitcnt vmcnt(6)" : "+v"(sel[0]), "+v"(sel[1]) :: "memory");
  asm volatile("buffer_store_dword %0, %1, %2, %3 offen sc0 sc1" :: "v"(tag), "v"(lane ? OOB : 0u), "s"(rO), "s"(sfw) : "memory");
  asm volatile("s_waitcnt vmcnt(1)" : "+v"(k[0].x[0]), "+v"(k[0].x[1]), "+v"(k[0].g), "+v"(k[1].x[0]), "+v"(k[1].x[1]), "+v"(k[1].g) :: "memory");
  const u32 vy = g * 4194304u + r * 16u;
  rt = rfl(rt); pend = rfl(pend);
  if (rt) {
    await(lane * 128u, rO, (u32)WS_FW, tag, ~0ull); await(lane * 128u + 8192u, rO, (u32)WS_FW, tag, 0xffffffffffffull);
    for (u32 q = 0; q < 4u; ++q) {
      if (!((rt >> q) & 1u)) continue;
      const u32 s = q >> 1, c = q & 1u, ir = (c ? idx >> 16 : idx) & 2047u, vx = s ? k[1].vx : k[0].vx;
      const u32 sf = (u32)WS_YF + (c * 28u + (rfl(vx) % (M_ * 2)) / 32u) * 7168u;
      f32x4 v[14]; float rv;
#pragma unroll
      for (int e = 0; e < 14; ++e) asm volatile("buffer_load_dwordx4 %0, %1, %2, %3 offen offset:%4 sc0 sc1" : "=v"(v[e]) : "v"(lane * 16u), "s"(rO), "s"(sf + (e >> 2) * 4096), "i"((e & 3) * 1024) : "memory");
      asm volatile("buffer_load_dword %0, %15, %16, %17 offen sc0 sc1\n\ts_waitcnt vmcnt(0)"
                   : "=&v"(rv), "+v"(v[0]), "+v"(v[1]), "+v"(v[2]), "+v"(v[3]), "+v"(v[4]), "+v"(v[5]), "+v"(v[6]), "+v"(v[7]), "+v"(v[8]), "+v"(v[9]), "+v"(v[10]), "+v"(v[11]), "+v"(v[12]), "+v"(v[13])
                   : "v"(0u), "s"(rO), "s"((u32)WS_KEY + 12u + ir * 16u) : "memory");
      const float sg = rbf(1.f / (1.f + __expf(-rv)));
#pragma unroll
      for (int t = 0; t < 2; ++t) {
        const f32x4 fs = ((v[7 * t] + v[7 * t + 1]) + (v[7 * t + 2] + v[7 * t + 3])) + ((v[7 * t + 4] + v[7 * t + 5]) + v[7 * t + 6]);
        const u32x2 xv = s ? k[1].x[t] : k[0].x[t];
        float o[4];
#pragma unroll
        for (int e = 0; e < 4; ++e) {
          unsigned u = __float_as_uint(fs[e]);
          const auto s32 = __builtin_amdgcn_permlane32_swap(u, u, false, false);
          u = __float_as_uint(__uint_as_float(s32[0]) + __uint_as_float(s32[1]));
          const auto s16 = __builtin_amdgcn_permlane16_swap(u, u, false, false);
          const float y = __shfl(dpp_add<0x124>(dpp_add<0x128>(__uint_as_float(s16[0]) + __uint_as_float(s16[1]))), g);
          o[e] = ((e & 1) ? BF_HI(xv[e >> 1]) : BF_LO(xv[e >> 1])) + rbf(rbf(y) * sg);
        }
        const u32x2 ov = {cvtpk(o[0], o[1]), cvtpk(o[2], o[3])};
        if (r == (ir & 15u)) { if (s) k[1].x[t] = ov; else k[0].x[t] = ov; }
      }
    }
  }
#pragma unroll
  for (int s = 0; s < NS; ++s) {
    const float omg = rbf(1.f - k[s].g);
#pragma unroll
    for (int e = 0; e < 4; ++e) { k[s].b[2 * e] = rbf(omg * BF_LO(k[s].x[e >> 1][e & 1])); k[s].b[2 * e + 1] = rbf(omg * BF_HI(k[s].x[e >> 1][e & 1])); }
  }
  for (u32 todo = pend, n = 1u << 17; todo; --n) {
    u32 f; unsigned long long m;
    asm volatile("buffer_load_dword %0, %2, %3, %4 offen sc0 sc1\n\ts_waitcnt vmcnt(0)\n\tv_cmp_ne_u32_e64 %1, %5, %0"
                 : "=&v"(f), "=s"(m) : "v"((pend >> (lane & 28u)) & 1u ? vf : OOB), "s"(rO), "s"((u32)WS_YW), "s"(tag) : "memory");
    u32 x = rfl((u32)m); x |= x >> 1; x |= x >> 2;
    const u32 rdy = n ? pend & ~x : pend, had = pend;
    y_load<0>(vy, rO, k[0].sy, rdy);
    y_load<1>(vy, rO, k[1].sy, rdy >> 16);
    pend &= ~rdy;
    if (!rdy && had) __builtin_amdgcn_s_sleep(SLP);
    if ((todo & 0xffffu) && !(had & 0xffffu)) { y_done<0>(k[0], sel, rOut); todo &= 0xffff0000u; }
    if ((todo >> 16) && !(had >> 16)) { y_done<1>(k[1], sel, rOut); todo &= 0xffffu; }
  }
  if (tid == 0u && bid == 0u) asm volatile("buffer_store_dword %0, %1, %2, %3 offen sc0 sc1" :: "v"(tag), "v"(lane ? OOB : 0u), "s"(rO), "s"((u32)WS_EP) : "memory");
  asm volatile(".LzCEND:");
}

extern "C" void kernel_launch(void* const* d_in, const int* in_sizes, int n_in, void* d_out, int out_size, void* d_ws, size_t ws_size, hipStream_t stream) {
  (void)hipFuncSetAttribute((const void*)k_main, hipFuncAttributeMaxDynamicSharedMemorySize, 163840);
  k_main<<<256, 256, 163840, stream>>>((const u16*)d_in[0], (const u16*)d_in[1], (const u16*)d_in[2], (const u16*)d_in[3], (const u16*)d_in[4],
      (char*)d_ws, (const u16*)d_in[11], (const u16*)d_in[7], (const u16*)d_in[5], (const u16*)d_in[8], (const u16*)d_in[9], (const u16*)d_in[10], (u16*)d_out);
}
```

```cpp
#include <hip/hip_runtime.h>
#include <stdint.h>

typedef __attribute__((ext_vector_type(4))) float f32x4;
typedef __attribute__((ext_vector_type(2))) float f32x2;
typedef __attribute__((ext_vector_type(4))) unsigned u32x4;
typedef __attribute__((ext_vector_type(2))) unsigned u32x2;
typedef __attribute__((ext_vector_type(2))) __bf16 bfx2;
typedef unsigned short u16;
typedef unsigned u32;
#define DI __device__ __forceinline__
constexpr int M_ = 448, F_ = 1792, FFN_N = 112;
constexpr size_t WS_K = 0u;
constexpr size_t WS_V = 33554432u;
constexpr size_t WS_Q = 67108864u;
constexpr size_t WS_PB = 68157440u;
constexpr size_t WS_WO = 100663296u;
constexpr size_t WS_Y = 134217728u;
constexpr size_t WS_ML = 201326592u;
constexpr size_t WS_KEY = 202375168u;
constexpr size_t WS_IDX = 202407936u;
constexpr size_t WS_EP = 202408960u;
constexpr size_t WS_PAR = 202409984u;
constexpr size_t WS_BB = 202412032u;
constexpr size_t WS_FW = 202424320u;
constexpr size_t WS_KVF = 202440704u;
constexpr size_t WS_QF = 202473472u;
constexpr size_t WS_PF = 202506240u;
constexpr size_t WS_YW = 202571776u;
constexpr size_t WS_YF = 202702848u;
constexpr size_t WS_END = 203104256u;
DI unsigned cvtpk(float lo, float hi) { return __builtin_bit_cast(unsigned, __builtin_convertvector((f32x2){lo, hi}, bfx2)); }
DI float rbf(float f) { return __uint_as_float(cvtpk(f, f) << 16); }
#define BF_LO(u) __uint_as_float((u) << 16)
#define BF_HI(u) __uint_as_float((u) & 0xffff0000u)
typedef u32x4 srd_t;
DI u32 rfl(u32 x) { return __builtin_amdgcn_readfirstlane(x); }
DI srd_t rfl4(srd_t r) { return (srd_t){rfl(r[0]), rfl(r[1]), rfl(r[2]), rfl(r[3])}; }
constexpr u32 OOB = 0x40000000u;
template <int CTRL> DI float dpp_add(float v) { return v + __uint_as_float(__builtin_amdgcn_update_dpp(0, __float_as_uint(v), CTRL, 0xf, 0xf, false)); }

#define V10(p) p "0", p "1", p "2", p "3", p "4", p "5", p "6", p "7", p "8", p "9"
#define V100(p) V10(p "0"), V10(p "1"), V10(p "2"), V10(p "3"), V10(p "4"), V10(p "5"), V10(p "6"), V10(p "7"), V10(p "8"), V10(p "9")
#define R256(p) V10(p "1"), V10(p "2"), V10(p "3"), V10(p "4"), V10(p "5"), V10(p "6"), V10(p "7"), V10(p "8"), V10(p "9"), V100(p "1"), \
                V10(p "20"), V10(p "21"), V10(p "22"), V10(p "23"), V10(p "24"), p "250", p "251", p "252", p "253", p "254", p "255"

#define ACL V10("a"), R256("a")
#define YL(k, c) "buffer_load_dwordx4 a[%c4+" #k "*16+" #c "*4:%c4+" #k "*16+" #c "*4+3], %1, %2, %0 offen offset:" #c "*256 sc0 sc1\n\t"
#define YK(k) "s_bitcmp0_b32 %5, " #k "*4\n\ts_cbranch_scc1 .Ly" #k "_%=\n\ts_add_u32 %0, %3, " #k "*16777216\n\t" YL(k, 0) YL(k, 1) YL(k, 2) YL(k, 3) ".Ly" #k "_%=:\n\t"
template <int S> DI void y_load(u32 vy, srd_t r, u32 so, u32 rdy) {
  u32 t;
  asm volatile(YK(0) YK(1) YK(2) YK(3) : "=&s"(t) : "v"(vy), "s"(r), "s"(so), "i"(64 * S), "s"(rdy) : "memory", "scc", ACL);
}
#define YM(k, c, d, s, z) "v_mfma_f32_16x16x32_bf16 %" #d ", %" #s ", a[%c4+" #k "*16+" #c "*4:%c4+" #k "*16+" #c "*4+3], " z "\n\t"
#define YS(k) YM(k, 0, 0, 2, "%0") YM(k, 2, 1, 2, "%1") YM(k, 1, 0, 3, "%0") YM(k, 3, 1, 3, "%1")
constexpr int NS = 2, SLP = 8;
struct Task { u32x2 x[2]; float g, b[8]; u32 vx, vg, sy; };
template <int S> DI void y_done(Task& k, const u32x4 (&sel)[2], srd_t rOut) {
  f32x4 c0, c1;
  asm volatile(YM(0, 0, 0, 2, "0") YM(0, 2, 1, 2, "0") YM(0, 1, 0, 3, "%0") YM(0, 3, 1, 3, "%1") YS(1) YS(2) YS(3) "s_nop 7"
               : "=&v"(c0), "=&v"(c1) : "v"(sel[0]), "v"(sel[1]), "i"(64 * S) : "memory");
  float r[8];
#pragma unroll
  for (int e = 0; e < 8; ++e) r[e] = rbf(k.g * rbf(e < 4 ? c0[e & 3] : c1[e & 3])) + k.b[e];
  const u32x2 o0 = {cvtpk(r[0], r[1]), cvtpk(r[2], r[3])}, o1 = {cvtpk(r[4], r[5]), cvtpk(r[6], r[7])};
  asm volatile("buffer_store_dwordx2 %0, %2, %3, 0 offen\n\tbuffer_store_dwordx2 %1, %2, %3, 0 offen offset:32" :: "v"(o0), "v"(o1), "v"(k.vx), "s"(rOut) : "memory");
}

extern "C" __global__ __launch_bounds__(256, 1)
void k_main(const u16* x_, const u16* ckv, const u16* wq, const u16* wuk, const u16* wuv, char* ws_,
            const u16* gate_w, const u16* w_r, const u16* wo, const u16* wg_, const u16* wu_, const u16* wd_, u16* out_) {
  typedef const __attribute__((address_space(4))) char* karg_t;
  karg_t ka = (karg_t)__builtin_amdgcn_kernarg_segment_ptr();
  u32 bid = blockIdx.x, tid = threadIdx.x, tag, sfw, idx, tk;
  srd_t rX, rO, rOut;
  asm volatile(R"(
.macro Mv d,a,b
v_mfma_f32_16x16x32_bf16 v[\d:\d+3], a[\a:\a+3], a[\b:\b+3], v[\d:\d+3]
.endm
.macro Mp d,a,b
v_mfma_f32_16x16x32_bf16 v[\d:\d+3], a[\a:\a+3], v[\b:\b+3], v[\d:\d+3]
.endm
.macro Ma d,a,b
v_mfma_f32_16x16x32_bf16 a[\d:\d+3], a[\a:\a+3], a[\b:\b+3], a[\d:\d+3]
.endm
.macro Mz d,a,b
v_mfma_f32_16x16x32_bf16 v[\d:\d+3], a[\a:\a+3], a[\b:\b+3], 0
.endm
.macro Rb d,v,o
ds_read_b128 a[\d:\d+3], v\v offset:\o
.endm
.macro Rt d,v,o
ds_read_b64_tr_b16 a[\d:\d+1], v\v offset:\o
.endm
.macro Cv d,a,b
v_cvt_pk_bf16_f32 v\d, v\a, v\b
.endm
.LzSTART:
s_load_dwordx16 s[64:79], s[0:1], 0
s_load_dwordx2 s[80:81], s[0:1], 64
v_lshrrev_b32 v1, 6, v0
v_and_b32 v0, 63, v0
v_readfirstlane_b32 s33, v1
v_lshlrev_b32 v1, 4, v0
s_getpc_b64 s[24:25]
.Lzpc0:
s_sub_u32 s24, s24, .Lzpc0-.LzSTART
s_subb_u32 s25, s25, 0
s_mov_b32 s26, .LzEND-.LzSTART+8192
s_mov_b32 s27, 0x20000
s_lshr_b32 s28, s2, 3
s_lshl2_add_u32 s28, s28, s33
s_lshl_b32 s28, s28, 11
v_lshrrev_b32 v27, 4, v0
v_min_u32 v27, 1, v27
v_lshlrev_b32 v27, 27, v27
v_add_lshl_u32 v27, v27, v1, 3
buffer_load_dword v27, v27, s[24:27], s28 offen
s_and_b32 s28, s2, 7
s_bfe_u32 s29, s2, 0x10003
s_lshl1_add_u32 s34, s28, s29
s_bfe_u32 s35, s2, 0x30004
s_bfe_u32 s91, s2, 0x10004
s_bfe_u32 s93, s2, 0x20005
s_lshl_b32 s36, s33, 13
s_mov_b32 s95, 40
s_bitcmp1_b32 s2, 7
s_cbranch_scc1 .LzB0
s_branch .LzA0
.LzPRB:
s_lshl_b32 s24, s34, 21
s_mul_i32 s25, s33, 2304
s_add_u32 s24, s24, s25
s_add_u32 s24, s24, 0x6100000
s_and_b32 s25, s36, 0x2000
s_xor_b32 s24, s24, s25
s_xor_b32 s25, s24, 0x2000
buffer_store_dword v0, v7, s[20:23], s24 offen sc0 sc1
buffer_store_dword v0, v7, s[20:23], s25 offen sc0 sc1
s_mov_b32 s26, -1
s_mov_b32 s27, -1
s_waitcnt vmcnt(0)
s_memtime s[28:29]
s_waitcnt lgkmcnt(0)
buffer_store_dword v0, v7, s[20:23], s24 offen sc0 sc1
s_waitcnt vmcnt(0)
s_memtime s[30:31]
s_waitcnt lgkmcnt(0)
s_sub_u32 s30, s30, s28
s_min_u32 s26, s26, s30
s_memtime s[28:29]
s_waitcnt lgkmcnt(0)
buffer_store_dword v0, v7, s[20:23], s25 offen sc0 sc1
s_waitcnt vmcnt(0)
s_memtime s[30:31]
s_waitcnt lgkmcnt(0)
s_sub_u32 s30, s30, s28
s_min_u32 s27, s27, s30
s_memtime s[28:29]
s_waitcnt lgkmcnt(0)
)" R"(
buffer_store_dword v0, v7, s[20:23], s24 offen sc0 sc1
s_waitcnt vmcnt(0)
s_memtime s[30:31]
s_waitcnt lgkmcnt(0)
s_sub_u32 s30, s30, s28
s_min_u32 s26, s26, s30
s_memtime s[28:29]
s_waitcnt lgkmcnt(0)
buffer_store_dword v0, v7, s[20:23], s25 offen sc0 sc1
s_waitcnt vmcnt(0)
s_memtime s[30:31]
s_waitcnt lgkmcnt(0)
s_sub_u32 s30, s30, s28
s_min_u32 s27, s27, s30
s_bitcmp1_b32 s33, 0
s_cselect_b32 s28, s27, s26
s_cselect_b32 s29, s26, s27
v_mov_b32 v32, s28
v_mov_b32 v33, s29
s_lshl_b32 s24, s33, 3
v_mov_b32 v34, s24
v_mov_b32 v35, 0
ds_write_b64 v34, v[32:33]
s_waitcnt lgkmcnt(0)
s_barrier
ds_read_b128 v[36:39], v35
ds_read_b128 v[40:43], v35 offset:16
s_mul_i32 s24, s3, 1664525
s_add_u32 s24, s24, 1013904223
s_and_b32 s24, s24, -2
s_lshl_b32 s25, s34, 6
s_add_u32 s25, s25, 0xc108800
s_cmp_lg_u32 s33, 0
s_cmov_b32 s25, 0x40000000
s_waitcnt lgkmcnt(0)
v_min3_u32 v32, v36, v38, v40
v_min3_u32 v33, v37, v39, v41
v_min_u32 v32, v32, v42
v_min_u32 v33, v33, v43
v_cmp_gt_u32_e32 vcc, v32, v33
v_mov_b32 v34, s24
s_nop 0
v_addc_co_u32_e32 v34, vcc, 0, v34, vcc
buffer_store_dword v34, v7, s[20:23], s25 offen sc0 sc1
s_barrier
s_branch .LzPRR
.LzA0:
s_mov_b32 s63, 0x6040200
s_mov_b32 s28, 0x7050312
s_bitcmp1_b32 s2, 5
s_cmov_b32 s63, 0x18161401
s_cmov_b32 s28, 0x19171513
s_bitcmp1_b32 s2, 6
s_cmov_b32 s63, s28
s_lshl_b32 s28, s33, 3
s_lshr_b32 s28, s63, s28
s_and_b32 s28, s28, 255
s_lshl_b32 s38, s28, 17
s_add_u32 s38, s38, 0xff80
s_lshl_b32 s39, s33, 16
s_lshl_b32 s28, s34, 9
s_add_u32 s39, s39, s28
s_sub_u32 s39, s39, 0x40000
s_lshl_b32 s37, s33, 12
)" R"(
s_add_u32 s37, s37, 0x10000
v_lshrrev_b32 v28, 4, v0
v_and_b32 v29, 7, v0
v_xor_b32 v28, v28, v29
v_lshlrev_b32 v28, 4, v28
v_lshrrev_b32 v29, 3, v0
v_lshl_or_b32 v8, v29, 11, v28
v_xor_b32 v9, 64, v8
v_add_u32 v9, 15360, v9
v_add_u32 v10, 30720, v8
v_add_u32 v11, 30720, v9
s_and_b32 s28, s33, 1
s_lshl_b32 s28, s28, 7
v_lshrrev_b32 v29, 5, v0
v_mul_u32_u24 v29, 0x2220, v29
v_xor_b32 v29, v29, v1
v_xor_b32 v12, s28, v29
v_xor_b32 v13, 64, v12
v_add_u32 v13, 15360, v13
v_add_u32 v14, 30720, v12
v_add_u32 v15, 30720, v13
v_min_u32 v7, 1, v0
v_lshlrev_b32 v7, 30, v7
s_waitcnt lgkmcnt(0)
s_mov_b32 s28, 0xc108400
s_load_dword s3, s[74:75], s28
s_mov_b32 s20, s74
s_mov_b32 s21, s75
s_mov_b32 s22, 0xc1b2000
s_mov_b32 s23, 0x20000
s_mov_b32 s4, s66
s_mov_b32 s5, s67
s_mov_b32 s6, 0x400000
s_mov_b32 s7, 0x20000
s_cmp_eq_u32 s91, 1
s_cselect_b32 s8, s72, s70
s_cselect_b32 s9, s73, s71
s_mov_b32 s10, 0x800000
s_mov_b32 s11, 0x20000
s_lshr_b32 s28, s2, 3
s_cmp_eq_u32 s28, 14
s_cbranch_scc1 .LzPRB
.LzPRR:
s_add_u32 m0, s37, 0
s_add_u32 s39, s39, 0x40000
buffer_load_dwordx4 v12, s[8:11], s39 offen offset:0 lds
buffer_load_dwordx4 v13, s[8:11], s39 offen offset:1024 lds
buffer_load_dwordx4 v14, s[8:11], s39 offen offset:2048 lds
buffer_load_dwordx4 v15, s[8:11], s39 offen offset:3072 lds
s_add_u32 m0, s36, 0
s_add_u32 s38, s38, 0xffff0080
buffer_load_dwordx4 v8, s[4:7], s38 offen offset:0 lds
buffer_load_dwordx4 v9, s[4:7], s38 offen offset:1024 lds
buffer_load_dwordx4 v10, s[4:7], s38 offen offset:2048 lds
buffer_load_dwordx4 v11, s[4:7], s38 offen offset:3072 lds
s_add_u32 m0, s36, 4096
s_add_u32 s38, s38, 0x10000
buffer_load_dwordx4 v8, s[4:7], s38 offen offset:0 lds
buffer_load_dwordx4 v9, s[4:7], s38 offen offset:1024 lds
buffer_load_dwordx4 v10, s[4:7], s38 offen offset:2048 lds
buffer_load_dwordx4 v11, s[4:7], s38 offen offset:3072 lds
s_add_u32 m0, s37, 16384
s_add_u32 s39, s39, 0x40000
buffer_load_dwordx4 v12, s[8:11], s39 offen offset:0 lds
)" R"(
buffer_load_dwordx4 v13, s[8:11], s39 offen offset:1024 lds
buffer_load_dwordx4 v14, s[8:11], s39 offen offset:2048 lds
buffer_load_dwordx4 v15, s[8:11], s39 offen offset:3072 lds
s_add_u32 m0, s36, 32768
s_add_u32 s38, s38, 0xffff0080
buffer_load_dwordx4 v8, s[4:7], s38 offen offset:0 lds
buffer_load_dwordx4 v9, s[4:7], s38 offen offset:1024 lds
buffer_load_dwordx4 v10, s[4:7], s38 offen offset:2048 lds
buffer_load_dwordx4 v11, s[4:7], s38 offen offset:3072 lds
s_add_u32 m0, s36, 36864
s_add_u32 s38, s38, 0x10000
buffer_load_dwordx4 v8, s[4:7], s38 offen offset:0 lds
buffer_load_dwordx4 v9, s[4:7], s38 offen offset:1024 lds
buffer_load_dwordx4 v10, s[4:7], s38 offen offset:2048 lds
buffer_load_dwordx4 v11, s[4:7], s38 offen offset:3072 lds
s_add_u32 m0, s37, 32768
s_add_u32 s39, s39, 0x40000
buffer_load_dwordx4 v12, s[8:11], s39 offen offset:0 lds
buffer_load_dwordx4 v13, s[8:11], s39 offen offset:1024 lds
buffer_load_dwordx4 v14, s[8:11], s39 offen offset:2048 lds
buffer_load_dwordx4 v15, s[8:11], s39 offen offset:3072 lds
s_mov_b32 s12, s64
s_mov_b32 s13, s65
s_mov_b32 s14, 0x1c0000
s_mov_b32 s15, 0x20000
s_mov_b32 s16, s68
s_mov_b32 s17, s69
s_mov_b32 s18, 0x380000
s_mov_b32 s19, 0x20000
s_load_dwordx4 s[76:79], s[0:1], 72
s_load_dwordx2 s[70:71], s[0:1], 88
s_load_dwordx2 s[74:75], s[0:1], 96
v_bfe_u32 v28, v0, 1, 3
v_lshrrev_b32 v29, 4, v0
v_xor_b32 v28, v28, v29
v_lshlrev_b32 v28, 4, v28
v_and_b32 v29, 15, v0
v_lshl_or_b32 v28, v29, 7, v28
s_lshr_b32 s28, s33, 1
s_lshl_b32 s28, s28, 12
v_or_b32 v16, s28, v28
v_xor_b32 v17, 64, v16
v_bfe_u32 v28, v0, 2, 3
v_lshlrev_b32 v28, 5, v28
v_and_b32 v29, 3, v0
v_lshl_or_b32 v28, v29, 3, v28
v_bfe_u32 v29, v0, 2, 2
v_lshl_or_b32 v28, v29, 9, v28
v_lshrrev_b32 v29, 4, v0
v_lshl_or_b32 v28, v29, 12, v28
s_and_b32 s28, s33, 1
s_lshl_b32 s28, s28, 8
s_bitset1_b32 s28, 16
v_or_b32 v18, s28, v28
v_xor_b32 v19, 32, v18
v_xor_b32 v20, 64, v18
v_xor_b32 v21, 96, v18
v_xor_b32 v22, 128, v18
v_xor_b32 v23, 160, v18
v_xor_b32 v24, 192, v18
v_xor_b32 v25, 224, v18
s_mov_b32 s24, s80
s_mov_b32 s25, s81
s_mov_b32 s26, 0x380000
)" R"(
s_mov_b32 s27, 0x20000
s_bfe_u32 s28, s2, 0x30005
s_lshl3_add_u32 s29, s34, s28
s_lshl2_add_u32 s29, s29, s33
s_mul_i32 s83, s29, 7168
s_mul_i32 s29, s91, 448
s_add_u32 s83, s83, s29
s_lshl_b32 s82, s34, 21
s_add_u32 s82, s82, 0x6000000
s_mul_i32 s94, s91, 14
s_add_u32 s94, s94, s33
s_mov_b32 s92, s28
v_lshrrev_b32 v4, 5, v0
v_mul_u32_u24 v4, 384, v4
v_add_u32 v28, v4, v1
v_and_b32 v4, 31, v0
v_cmp_lt_u32_e32 vcc, 27, v4
v_mov_b32 v4, 0x40000000
v_lshrrev_b32 v29, 2, v0
v_cndmask_b32_e32 v28, v28, v4, vcc
v_and_b32 v4, 48, v1
v_lshl_or_b32 v29, v29, 9, v4
s_lshr_b32 s28, s33, 1
s_lshl_b32 s28, s28, 6
s_and_b32 s29, s33, 1
s_lshl3_add_u32 s28, s29, s28
s_add_u32 s28, s28, 0x20000
v_add_u32 v29, s28, v29
s_add_u32 m0, s37, 0x10000
s_add_u32 s29, s83, 0
buffer_load_dwordx4 v28, s[24:27], s29 offen offset:0 lds
s_add_u32 s29, s83, 768
buffer_load_dwordx4 v28, s[24:27], s29 offen offset:1024 lds
s_add_u32 s29, s83, 1536
buffer_load_dwordx4 v28, s[24:27], s29 offen offset:2048 lds
s_add_u32 s29, s83, 2304
buffer_load_dwordx4 v28, s[24:27], s29 offen offset:3072 lds
s_lshl_b32 s28, s34, 21
s_lshl_b32 s29, s91, 25
s_add_u32 s28, s28, s29
s_bfe_u32 s29, s63, 0x80000
s_lshl_b32 s29, s29, 16
s_add_u32 s45, s28, s29
s_bfe_u32 s29, s63, 0x80008
s_lshl_b32 s29, s29, 16
s_add_u32 s46, s28, s29
s_bfe_u32 s29, s63, 0x80010
s_lshl_b32 s29, s29, 16
s_add_u32 s47, s28, s29
s_bfe_u32 s29, s63, 0x80018
s_lshl_b32 s29, s29, 16
s_add_u32 s48, s28, s29
s_lshl_b32 s50, s34, 11
s_add_u32 s50, s50, 0xc110000
s_lshl2_add_u32 s28, s91, s93
s_lshl_b32 s28, s28, 7
s_add_u32 s49, s28, s50
s_lshl3_add_u32 s28, s34, s35
s_lshl_b32 s52, s28, 7
s_add_u32 s52, s52, 0xc118000
s_lshl4_add_u32 s28, s34, s35
s_lshl_b32 s51, s28, 17
s_add_u32 s51, s51, 0x4000000
s_cmp_lg_u32 s33, 0
)" R"(
s_cmov_b32 s49, 0x40000000
s_mov_b32 s53, 0x40000
s_mov_b32 s54, 0x3db8aa3b
s_mul_i32 s41, s33, 7168
s_mul_i32 s42, s33, 0x70000
s_lshl_b32 s28, s34, 9
s_add_u32 s42, s42, s28
s_lshl_b32 s89, s34, 21
s_mov_b64 s[60:61], 0xff
s_mov_b32 s62, 0x4e800000
s_add_u32 s67, s89, 0x6000000
s_lshl3_add_u32 s28, s34, s35
s_add_u32 s64, s51, 0x100000
s_lshl2_add_u32 s28, s28, s33
s_lshl_b32 s65, s28, 7
s_add_u32 s65, s65, 0xc120000
s_lshl_b32 s66, s28, 10
s_add_u32 s66, s66, 0xc000000
s_mov_b32 s28, s35
s_lshl2_add_u32 s28, s28, s33
s_lshl_b32 s29, s34, 6
s_add_u32 s29, s29, s28
s_lshl_b32 s73, s29, 16
s_add_u32 s73, s73, 0x8000000
s_lshl4_add_u32 s28, s28, s34
s_lshl_b32 s68, s28, 7
s_add_u32 s68, s68, 0xc130000
s_add_u32 s55, s89, 0x120000
s_lshl_b32 s28, s35, 1
s_sub_u32 s57, 14, s28
s_mov_b32 s59, s89
s_mov_b32 s58, 2
s_cmp_eq_u32 s35, 7
s_cmov_b32 s55, s89
s_cmp_eq_u32 s35, 7
s_cmov_b32 s57, 16
s_cmp_eq_u32 s35, 7
s_cmov_b32 s59, s67
s_add_u32 s56, s55, 0x2000000
s_waitcnt vmcnt(20) lgkmcnt(0)
s_barrier
Rt 0,18,0
Rt 2,18,2048
Rt 4,19,0
Rt 6,19,2048
Rt 8,20,0
Rt 10,20,2048
Rt 12,21,0
Rt 14,21,2048
Rt 16,22,0
Rt 18,22,2048
Rt 20,23,0
Rt 22,23,2048
Rt 24,24,0
Rt 26,24,2048
Rt 28,25,0
Rt 30,25,2048
Rb 32,16,0
Rb 36,16,2048
Rb 40,16,8192
Rb 44,16,10240
Rb 48,16,16384
Rb 52,16,18432
Rb 56,16,24576
Rb 60,16,26624
)" R"(
s_mul_i32 s3, s3, 1664525
s_add_u32 s3, s3, 1013904223
s_cmp_eq_u32 s91, 1
s_waitcnt vmcnt(16) lgkmcnt(0)
s_barrier
s_cbranch_scc1 .LzAV
Mz 32,0,32
Rt 64,18,16384
Mz 64,0,36
Mz 96,0,40
Rt 66,18,18432
Mz 128,0,44
s_add_u32 s39, s39, 0x40000
Mz 160,0,48
Rt 68,19,16384
s_add_u32 m0, s37, 49152
Mz 192,0,52
buffer_load_dwordx4 v12, s[8:11], s39 offen offset:0 lds
Mz 224,0,56
Rt 70,19,18432
v_mfma_f32_16x16x32_bf16 a[224:227], a[0:3], a[60:63], 0
v_mfma_f32_16x16x32_bf16 a[228:231], a[4:7], a[60:63], 0
Rt 72,20,16384
Mz 228,4,56
Mz 196,4,52
Rt 74,20,18432
Mz 164,4,48
Mz 132,4,44
Rt 76,21,16384
Mz 100,4,40
buffer_load_dwordx4 v13, s[8:11], s39 offen offset:1024 lds
Mz 68,4,36
Rt 78,21,18432
Mz 36,4,32
Mz 40,8,32
Rt 80,22,16384
Mz 72,8,36
Mz 104,8,40
Rt 82,22,18432
Mz 136,8,44
Mz 168,8,48
Rt 84,23,16384
Mz 200,8,52
buffer_load_dwordx4 v14, s[8:11], s39 offen offset:2048 lds
Mz 232,8,56
Rt 86,23,18432
v_mfma_f32_16x16x32_bf16 a[232:235], a[8:11], a[60:63], 0
v_mfma_f32_16x16x32_bf16 a[236:239], a[12:15], a[60:63], 0
Rt 88,24,16384
Mz 236,12,56
Mz 204,12,52
Rt 90,24,18432
Mz 172,12,48
Mz 140,12,44
Rt 92,25,16384
Mz 108,12,40
buffer_load_dwordx4 v15, s[8:11], s39 offen offset:3072 lds
Mz 76,12,36
Rt 94,25,18432
Mz 44,12,32
Mz 48,16,32
Rb 96,17,0
Mz 80,16,36
Mz 112,16,40
Rb 100,17,2048
)" R"(
Mz 144,16,44
s_add_u32 s39, s39, 0x40000
Mz 176,16,48
Rb 104,17,8192
s_add_u32 m0, s37, 0
Mz 208,16,52
buffer_load_dwordx4 v12, s[8:11], s39 offen offset:0 lds
Mz 240,16,56
Rb 108,17,10240
v_mfma_f32_16x16x32_bf16 a[240:243], a[16:19], a[60:63], 0
v_mfma_f32_16x16x32_bf16 a[244:247], a[20:23], a[60:63], 0
Rb 112,17,16384
Mz 244,20,56
Mz 212,20,52
Rb 116,17,18432
Mz 180,20,48
Mz 148,20,44
Rb 120,17,24576
Mz 116,20,40
buffer_load_dwordx4 v13, s[8:11], s39 offen offset:1024 lds
Mz 84,20,36
Rb 124,17,26624
Mz 52,20,32
Mz 56,24,32
Mz 88,24,36
Mz 120,24,40
Mz 152,24,44
Mz 184,24,48
Mz 216,24,52
buffer_load_dwordx4 v14, s[8:11], s39 offen offset:2048 lds
Mz 248,24,56
v_mfma_f32_16x16x32_bf16 a[248:251], a[24:27], a[60:63], 0
v_mfma_f32_16x16x32_bf16 a[252:255], a[28:31], a[60:63], 0
Mz 252,28,56
Mz 220,28,52
Mz 188,28,48
Mz 156,28,44
Mz 124,28,40
buffer_load_dwordx4 v15, s[8:11], s39 offen offset:3072 lds
Mz 92,28,36
Mz 60,28,32
s_waitcnt vmcnt(12) lgkmcnt(0)
s_barrier
Mv 32,64,96
Rt 0,18,32768
Mv 64,64,100
s_add_u32 s38, s38, 0xffff0080
Mv 96,64,104
Rt 2,18,34816
s_add_u32 m0, s36, 0
Mv 128,64,108
buffer_load_dwordx4 v8, s[4:7], s38 offen offset:0 lds
Mv 160,64,112
Rt 4,19,32768
Mv 192,64,116
Mv 224,64,120
Rt 6,19,34816
Ma 224,64,124
buffer_load_dwordx4 v9, s[4:7], s38 offen offset:1024 lds
Ma 228,68,124
Rt 8,20,32768
Mv 228,68,120
Mv 196,68,116
Rt 10,20,34816
Mv 164,68,112
)" R"(
buffer_load_dwordx4 v10, s[4:7], s38 offen offset:2048 lds
Mv 132,68,108
Rt 12,21,32768
Mv 100,68,104
Mv 68,68,100
Rt 14,21,34816
Mv 36,68,96
buffer_load_dwordx4 v11, s[4:7], s38 offen offset:3072 lds
Mv 40,72,96
Rt 16,22,32768
Mv 72,72,100
s_add_u32 s38, s38, 0x10000
Mv 104,72,104
Rt 18,22,34816
s_add_u32 m0, s36, 4096
Mv 136,72,108
buffer_load_dwordx4 v8, s[4:7], s38 offen offset:0 lds
Mv 168,72,112
Rt 20,23,32768
Mv 200,72,116
Mv 232,72,120
Rt 22,23,34816
Ma 232,72,124
buffer_load_dwordx4 v9, s[4:7], s38 offen offset:1024 lds
Ma 236,76,124
Rt 24,24,32768
Mv 236,76,120
Mv 204,76,116
Rt 26,24,34816
Mv 172,76,112
buffer_load_dwordx4 v10, s[4:7], s38 offen offset:2048 lds
Mv 140,76,108
Rt 28,25,32768
Mv 108,76,104
Mv 76,76,100
Rt 30,25,34816
Mv 44,76,96
buffer_load_dwordx4 v11, s[4:7], s38 offen offset:3072 lds
Mv 48,80,96
Rb 32,16,32768
Mv 80,80,100
Mv 112,80,104
Rb 36,16,34816
Mv 144,80,108
Mv 176,80,112
Rb 40,16,40960
Mv 208,80,116
Mv 240,80,120
Rb 44,16,43008
Ma 240,80,124
Ma 244,84,124
Rb 48,16,49152
Mv 244,84,120
Mv 212,84,116
Rb 52,16,51200
Mv 180,84,112
Mv 148,84,108
Rb 56,16,57344
Mv 116,84,104
Mv 84,84,100
Rb 60,16,59392
Mv 52,84,96
Mv 56,88,96
Mv 88,88,100
)" R"(
Mv 120,88,104
Mv 152,88,108
Mv 184,88,112
Mv 216,88,116
Mv 248,88,120
Ma 248,88,124
Ma 252,92,124
Mv 252,92,120
Mv 220,92,116
Mv 188,92,112
Mv 156,92,108
Mv 124,92,104
Mv 92,92,100
Mv 60,92,96
s_waitcnt vmcnt(12) lgkmcnt(0)
s_barrier
Mv 32,0,32
Rt 64,18,49152
s_mov_b32 s85, 0xc108800
v_and_b32 v2, 7, v0
Mv 64,0,36
v_lshlrev_b32 v2, 7, v2
Mv 96,0,40
Rt 66,18,51200
buffer_load_dword v4, v2, s[20:23], s85 offen sc0 sc1
Mv 128,0,44
s_add_u32 s39, s39, 0x40000
Mv 160,0,48
Rt 68,19,49152
s_add_u32 m0, s37, 16384
Mv 192,0,52
buffer_load_dwordx4 v12, s[8:11], s39 offen offset:0 lds
Mv 224,0,56
Rt 70,19,51200
Ma 224,0,60
Ma 228,4,60
Rt 72,20,49152
Mv 228,4,56
Mv 196,4,52
Rt 74,20,51200
Mv 164,4,48
Mv 132,4,44
Rt 76,21,49152
Mv 100,4,40
buffer_load_dwordx4 v13, s[8:11], s39 offen offset:1024 lds
Mv 68,4,36
Rt 78,21,51200
Mv 36,4,32
Mv 40,8,32
Rt 80,22,49152
Mv 72,8,36
Mv 104,8,40
Rt 82,22,51200
Mv 136,8,44
Mv 168,8,48
Rt 84,23,49152
Mv 200,8,52
buffer_load_dwordx4 v14, s[8:11], s39 offen offset:2048 lds
Mv 232,8,56
Rt 86,23,51200
Ma 232,8,60
Ma 236,12,60
Rt 88,24,49152
Mv 236,12,56
Mv 204,12,52
)" R"(
Rt 90,24,51200
Mv 172,12,48
Mv 140,12,44
Rt 92,25,49152
Mv 108,12,40
buffer_load_dwordx4 v15, s[8:11], s39 offen offset:3072 lds
Mv 76,12,36
Rt 94,25,51200
Mv 44,12,32
Mv 48,16,32
Rb 96,17,32768
Mv 80,16,36
Mv 112,16,40
Rb 100,17,34816
Mv 144,16,44
s_add_u32 s39, s39, 0x40000
Mv 176,16,48
Rb 104,17,40960
s_add_u32 m0, s37, 32768
Mv 208,16,52
buffer_load_dwordx4 v12, s[8:11], s39 offen offset:0 lds
Mv 240,16,56
Rb 108,17,43008
Ma 240,16,60
Ma 244,20,60
Rb 112,17,49152
Mv 244,20,56
Mv 212,20,52
Rb 116,17,51200
Mv 180,20,48
Mv 148,20,44
Rb 120,17,57344
Mv 116,20,40
buffer_load_dwordx4 v13, s[8:11], s39 offen offset:1024 lds
Mv 84,20,36
Rb 124,17,59392
Mv 52,20,32
Mv 56,24,32
Rt 176,29,0
Mv 88,24,36
Rt 178,29,8192
Mv 120,24,40
Rt 180,29,128
Mv 152,24,44
Rt 182,29,8320
Mv 184,24,48
Rt 184,29,256
Mv 216,24,52
buffer_load_dwordx4 v14, s[8:11], s39 offen offset:2048 lds
Rt 186,29,8448
Mv 248,24,56
Rt 188,29,384
Ma 248,24,60
Rt 190,29,8576
Ma 252,28,60
Mv 252,28,56
Mv 220,28,52
Mv 188,28,48
Mv 156,28,44
Mv 124,28,40
buffer_load_dwordx4 v15, s[8:11], s39 offen offset:3072 lds
Mv 92,28,36
Mv 60,28,32
s_waitcnt vmcnt(8) lgkmcnt(0)
s_barrier
)" R"(
Mv 32,64,96
Rt 0,18,0
v_xor_b32 v4, s3, v4
v_cmp_gt_u32_e32 vcc, 2, v4
s_andn2_b32 s28, 0xff, vcc_lo
s_cbranch_scc1 .Lzsp1
.Lzok1:
Mv 64,64,100
s_add_u32 s38, s38, 0xffff0080
v_xor_b32 v4, s3, v4
Mv 96,64,104
Rt 2,18,2048
s_add_u32 m0, s36, 32768
Mv 128,64,108
buffer_load_dwordx4 v8, s[4:7], s38 offen offset:0 lds
Mv 160,64,112
Rt 4,19,0
v_and_b32 v4, 1, v4
Mv 192,64,116
v_cmp_eq_u32_e32 vcc, 1, v4
s_mov_b32 s85, vcc_lo
Mv 224,64,120
Rt 6,19,2048
s_and_b32 s28, s2, 7
Ma 224,64,124
buffer_load_dwordx4 v9, s[4:7], s38 offen offset:1024 lds
Ma 228,68,124
Rt 8,20,0
s_lshr_b32 s28, s85, s28
Mv 228,68,120
s_xor_b32 s28, s28, s34
s_and_b32 s28, s28, 1
Mv 196,68,116
Rt 10,20,2048
s_lshl_b32 s84, s28, 13
Mv 164,68,112
buffer_load_dwordx4 v10, s[4:7], s38 offen offset:2048 lds
Mv 132,68,108
Rt 12,21,0
s_lshl1_add_u32 s28, s36, s84
Mv 100,68,104
v_add_u32 v2, s28, v1
v_add_u32 v3, 0x1000, v2
Mv 68,68,100
Rt 14,21,2048
s_bfe_u32 s28, s73, 0x30010
Mv 36,68,96
buffer_load_dwordx4 v11, s[4:7], s38 offen offset:3072 lds
Mv 40,72,96
Rt 16,22,0
s_lshr_b32 s28, s85, s28
Mv 72,72,100
s_add_u32 s38, s38, 0x10000
s_and_b32 s28, s28, 1
Mv 104,72,104
Rt 18,22,2048
s_add_u32 m0, s36, 36864
Mv 136,72,108
buffer_load_dwordx4 v8, s[4:7], s38 offen offset:0 lds
Mv 168,72,112
Rt 20,23,0
s_lshl_b32 s28, s28, 13
Mv 200,72,116
s_add_u32 s73, s73, s28
)" R"(
Mv 232,72,120
Rt 22,23,2048
Ma 232,72,124
buffer_load_dwordx4 v9, s[4:7], s38 offen offset:1024 lds
Ma 236,76,124
Rt 24,24,0
Mv 236,76,120
Mv 204,76,116
Rt 26,24,2048
Mv 172,76,112
buffer_load_dwordx4 v10, s[4:7], s38 offen offset:2048 lds
Mv 140,76,108
Rt 28,25,0
s_lshl3_add_u32 s28, s94, s92
Mv 108,76,104
s_lshl3_add_u32 s29, s92, s94
s_cmp_lt_u32 s94, 8
s_cmov_b32 s28, s29
Mv 76,76,100
Rt 30,25,2048
s_mul_i32 s29, s28, 293
s_lshr_b32 s29, s29, 11
Mv 44,76,96
buffer_load_dwordx4 v11, s[4:7], s38 offen offset:3072 lds
Mv 48,80,96
Rb 32,16,0
s_mul_i32 s29, s29, 9
s_add_u32 s29, s29, s28
Mv 80,80,100
s_lshl_b32 s29, s29, 10
s_add_u32 s29, s29, s82
s_add_u32 s29, s29, s84
Mv 112,80,104
Rb 36,16,2048
s_add_u32 s94, s94, 4
Mv 144,80,108
buffer_store_dwordx4 a[176:179], v1, s[20:23], s29 offen sc0 sc1
Mv 176,80,112
Rb 40,16,8192
s_lshl3_add_u32 s28, s94, s92
Mv 208,80,116
s_lshl3_add_u32 s29, s92, s94
Mv 240,80,120
Rb 44,16,10240
s_cmp_lt_u32 s94, 8
s_cmov_b32 s28, s29
Ma 240,80,124
s_mul_i32 s29, s28, 293
s_lshr_b32 s29, s29, 11
Ma 244,84,124
Rb 48,16,16384
s_mul_i32 s29, s29, 9
s_add_u32 s29, s29, s28
Mv 244,84,120
s_lshl_b32 s29, s29, 10
s_add_u32 s29, s29, s82
Mv 212,84,116
Rb 52,16,18432
s_add_u32 s29, s29, s84
Mv 180,84,112
s_add_u32 s94, s94, 4
buffer_store_dwordx4 a[180:183], v1, s[20:23], s29 offen sc0 sc1
Mv 148,84,108
Rb 56,16,24576
)" R"(
s_lshl3_add_u32 s28, s94, s92
Mv 116,84,104
s_lshl3_add_u32 s29, s92, s94
Mv 84,84,100
Rb 60,16,26624
s_cmp_lt_u32 s94, 8
s_cmov_b32 s28, s29
Mv 52,84,96
s_mul_i32 s29, s28, 293
s_lshr_b32 s29, s29, 11
Mv 56,88,96
s_mul_i32 s29, s29, 9
s_add_u32 s29, s29, s28
Mv 88,88,100
s_lshl_b32 s29, s29, 10
s_add_u32 s29, s29, s82
Mv 120,88,104
s_add_u32 s29, s29, s84
s_add_u32 s94, s94, 4
Mv 152,88,108
buffer_store_dwordx4 a[184:187], v1, s[20:23], s29 offen sc0 sc1
Mv 184,88,112
s_lshl3_add_u32 s28, s94, s92
s_lshl3_add_u32 s29, s92, s94
Mv 216,88,116
s_cmp_lt_u32 s94, 8
s_cmov_b32 s28, s29
Mv 248,88,120
s_mul_i32 s29, s28, 293
s_lshr_b32 s29, s29, 11
Ma 248,88,124
s_mul_i32 s29, s29, 9
s_add_u32 s29, s29, s28
Ma 252,92,124
s_lshl_b32 s29, s29, 10
s_add_u32 s29, s29, s82
Mv 252,92,120
s_add_u32 s29, s29, s84
s_mov_b32 s40, 5
Mv 220,92,116
s_cmp_gt_u32 s33, 1
s_cmov_b32 s29, 0x40000000
Mv 188,92,112
buffer_store_dwordx4 a[188:191], v1, s[20:23], s29 offen sc0 sc1
Mv 156,92,108
Mv 124,92,104
Mv 92,92,100
Mv 60,92,96
s_waitcnt vmcnt(16) lgkmcnt(0)
s_barrier
s_mov_b32 s28, s35
s_lshl2_add_u32 s28, s28, s33
s_mul_i32 s43, s28, 28672
v_and_b32 v26, 0x70, v1
v_and_b32 v4, 48, v0
v_xor_b32 v26, v26, v4
v_lshrrev_b32 v4, 3, v0
v_mul_u32_u24 v4, 896, v4
v_add_u32 v26, v26, v4
v_xor_b32 v27, 64, v26
v_add_u32 v27, 6144, v27
v_bfe_u32 v28, v0, 1, 3
v_lshrrev_b32 v4, 4, v0
v_xor_b32 v28, v28, v4
v_lshlrev_b32 v28, 4, v28
)" R"(
v_and_b32 v4, 15, v0
v_lshl_or_b32 v28, v4, 7, v28
s_add_u32 s28, s36, 0x18000
v_add_u32 v28, s28, v28
v_xor_b32 v29, 64, v28
s_add_u32 m0, s36, 131072
s_add_u32 s44, s43, 0
buffer_load_dwordx4 v26, s[12:15], s44 offen offset:0 lds
buffer_load_dwordx4 v27, s[12:15], s44 offen offset:1024 lds
s_add_u32 s44, s43, 12288
buffer_load_dwordx4 v26, s[12:15], s44 offen offset:2048 lds
buffer_load_dwordx4 v27, s[12:15], s44 offen offset:3072 lds
s_add_u32 m0, s36, 135168
s_add_u32 s44, s43, 128
buffer_load_dwordx4 v26, s[12:15], s44 offen offset:0 lds
buffer_load_dwordx4 v27, s[12:15], s44 offen offset:1024 lds
s_add_u32 s44, s43, 12416
buffer_load_dwordx4 v26, s[12:15], s44 offen offset:2048 lds
buffer_load_dwordx4 v27, s[12:15], s44 offen offset:3072 lds
.LzPAK:
Mv 32,0,32
Rt 64,18,16384
Mv 64,0,36
Mv 96,0,40
Rt 66,18,18432
Mv 128,0,44
s_add_u32 s39, s39, 0x40000
Mv 160,0,48
Rt 68,19,16384
s_add_u32 m0, s37, 49152
Mv 192,0,52
buffer_load_dwordx4 v12, s[8:11], s39 offen offset:0 lds
Mv 224,0,56
Rt 70,19,18432
Ma 224,0,60
Ma 228,4,60
Rt 72,20,16384
Mv 228,4,56
Mv 196,4,52
Rt 74,20,18432
Mv 164,4,48
Mv 132,4,44
Rt 76,21,16384
Mv 100,4,40
buffer_load_dwordx4 v13, s[8:11], s39 offen offset:1024 lds
Mv 68,4,36
Rt 78,21,18432
Mv 36,4,32
Mv 40,8,32
Rt 80,22,16384
Mv 72,8,36
Mv 104,8,40
Rt 82,22,18432
Mv 136,8,44
Mv 168,8,48
Rt 84,23,16384
Mv 200,8,52
buffer_load_dwordx4 v14, s[8:11], s39 offen offset:2048 lds
Mv 232,8,56
Rt 86,23,18432
Ma 232,8,60
Ma 236,12,60
Rt 88,24,16384
Mv 236,12,56
)" R"(
Mv 204,12,52
Rt 90,24,18432
Mv 172,12,48
Mv 140,12,44
Rt 92,25,16384
Mv 108,12,40
buffer_load_dwordx4 v15, s[8:11], s39 offen offset:3072 lds
Mv 76,12,36
Rt 94,25,18432
Mv 44,12,32
Mv 48,16,32
Rb 96,17,0
Mv 80,16,36
Mv 112,16,40
Rb 100,17,2048
Mv 144,16,44
s_add_u32 s39, s39, 0x40000
Mv 176,16,48
Rb 104,17,8192
s_add_u32 m0, s37, 0
Mv 208,16,52
buffer_load_dwordx4 v12, s[8:11], s39 offen offset:0 lds
Mv 240,16,56
Rb 108,17,10240
Ma 240,16,60
Ma 244,20,60
Rb 112,17,16384
Mv 244,20,56
Mv 212,20,52
Rb 116,17,18432
Mv 180,20,48
Mv 148,20,44
Rb 120,17,24576
Mv 116,20,40
buffer_load_dwordx4 v13, s[8:11], s39 offen offset:1024 lds
Mv 84,20,36
Rb 124,17,26624
Mv 52,20,32
Mv 56,24,32
Mv 88,24,36
Mv 120,24,40
Mv 152,24,44
Mv 184,24,48
Mv 216,24,52
buffer_load_dwordx4 v14, s[8:11], s39 offen offset:2048 lds
Mv 248,24,56
Ma 248,24,60
Ma 252,28,60
Mv 252,28,56
Mv 220,28,52
Mv 188,28,48
Mv 156,28,44
Mv 124,28,40
buffer_load_dwordx4 v15, s[8:11], s39 offen offset:3072 lds
Mv 92,28,36
Mv 60,28,32
s_waitcnt vmcnt(8) lgkmcnt(0)
s_barrier
Mv 32,64,96
Rt 0,18,32768
Mv 64,64,100
s_add_u32 s38, s38, 0xffff0080
Mv 96,64,104
Rt 2,18,34816
s_add_u32 m0, s36, 0
)" R"(
Mv 128,64,108
buffer_load_dwordx4 v8, s[4:7], s38 offen offset:0 lds
Mv 160,64,112
Rt 4,19,32768
Mv 192,64,116
Mv 224,64,120
Rt 6,19,34816
Ma 224,64,124
buffer_load_dwordx4 v9, s[4:7], s38 offen offset:1024 lds
Ma 228,68,124
Rt 8,20,32768
Mv 228,68,120
Mv 196,68,116
Rt 10,20,34816
Mv 164,68,112
buffer_load_dwordx4 v10, s[4:7], s38 offen offset:2048 lds
Mv 132,68,108
Rt 12,21,32768
Mv 100,68,104
Mv 68,68,100
Rt 14,21,34816
Mv 36,68,96
buffer_load_dwordx4 v11, s[4:7], s38 offen offset:3072 lds
Mv 40,72,96
Rt 16,22,32768
Mv 72,72,100
s_add_u32 s38, s38, 0x10000
Mv 104,72,104
Rt 18,22,34816
s_add_u32 m0, s36, 4096
Mv 136,72,108
buffer_load_dwordx4 v8, s[4:7], s38 offen offset:0 lds
Mv 168,72,112
Rt 20,23,32768
Mv 200,72,116
Mv 232,72,120
Rt 22,23,34816
Ma 232,72,124
buffer_load_dwordx4 v9, s[4:7], s38 offen offset:1024 lds
Ma 236,76,124
Rt 24,24,32768
Mv 236,76,120
Mv 204,76,116
Rt 26,24,34816
Mv 172,76,112
buffer_load_dwordx4 v10, s[4:7], s38 offen offset:2048 lds
Mv 140,76,108
Rt 28,25,32768
Mv 108,76,104
Mv 76,76,100
Rt 30,25,34816
Mv 44,76,96
buffer_load_dwordx4 v11, s[4:7], s38 offen offset:3072 lds
Mv 48,80,96
Rb 32,16,32768
Mv 80,80,100
Mv 112,80,104
Rb 36,16,34816
Mv 144,80,108
Mv 176,80,112
Rb 40,16,40960
Mv 208,80,116
Mv 240,80,120
Rb 44,16,43008
)" R"(
Ma 240,80,124
Ma 244,84,124
Rb 48,16,49152
Mv 244,84,120
Mv 212,84,116
Rb 52,16,51200
Mv 180,84,112
Mv 148,84,108
Rb 56,16,57344
Mv 116,84,104
Mv 84,84,100
Rb 60,16,59392
Mv 52,84,96
Mv 56,88,96
Mv 88,88,100
Mv 120,88,104
Mv 152,88,108
Mv 184,88,112
Mv 216,88,116
Mv 248,88,120
Ma 248,88,124
Ma 252,92,124
Mv 252,92,120
Mv 220,92,116
Mv 188,92,112
Mv 156,92,108
Mv 124,92,104
Mv 92,92,100
Mv 60,92,96
s_waitcnt vmcnt(12) lgkmcnt(0)
s_barrier
Mv 32,0,32
Rt 64,18,49152
Mv 64,0,36
Mv 96,0,40
Rt 66,18,51200
Mv 128,0,44
s_add_u32 s39, s39, 0x40000
Mv 160,0,48
Rt 68,19,49152
s_add_u32 m0, s37, 16384
Mv 192,0,52
buffer_load_dwordx4 v12, s[8:11], s39 offen offset:0 lds
Mv 224,0,56
Rt 70,19,51200
Ma 224,0,60
Ma 228,4,60
Rt 72,20,49152
Mv 228,4,56
Mv 196,4,52
Rt 74,20,51200
Mv 164,4,48
Mv 132,4,44
Rt 76,21,49152
Mv 100,4,40
buffer_load_dwordx4 v13, s[8:11], s39 offen offset:1024 lds
Mv 68,4,36
Rt 78,21,51200
Mv 36,4,32
Mv 40,8,32
Rt 80,22,49152
Mv 72,8,36
Mv 104,8,40
Rt 82,22,51200
Mv 136,8,44
)" R"(
Mv 168,8,48
Rt 84,23,49152
Mv 200,8,52
buffer_load_dwordx4 v14, s[8:11], s39 offen offset:2048 lds
Mv 232,8,56
Rt 86,23,51200
Ma 232,8,60
Ma 236,12,60
Rt 88,24,49152
Mv 236,12,56
Mv 204,12,52
Rt 90,24,51200
Mv 172,12,48
Mv 140,12,44
Rt 92,25,49152
Mv 108,12,40
buffer_load_dwordx4 v15, s[8:11], s39 offen offset:3072 lds
Mv 76,12,36
Rt 94,25,51200
Mv 44,12,32
Mv 48,16,32
Rb 96,17,32768
Mv 80,16,36
Mv 112,16,40
Rb 100,17,34816
Mv 144,16,44
s_add_u32 s39, s39, 0x40000
Mv 176,16,48
Rb 104,17,40960
s_add_u32 m0, s37, 32768
Mv 208,16,52
buffer_load_dwordx4 v12, s[8:11], s39 offen offset:0 lds
Mv 240,16,56
Rb 108,17,43008
Ma 240,16,60
Ma 244,20,60
Rb 112,17,49152
Mv 244,20,56
Mv 212,20,52
Rb 116,17,51200
Mv 180,20,48
Mv 148,20,44
Rb 120,17,57344
Mv 116,20,40
buffer_load_dwordx4 v13, s[8:11], s39 offen offset:1024 lds
Mv 84,20,36
Rb 124,17,59392
Mv 52,20,32
Mv 56,24,32
Mv 88,24,36
Mv 120,24,40
Mv 152,24,44
Mv 184,24,48
Mv 216,24,52
buffer_load_dwordx4 v14, s[8:11], s39 offen offset:2048 lds
Mv 248,24,56
Ma 248,24,60
Ma 252,28,60
Mv 252,28,56
Mv 220,28,52
Mv 188,28,48
Mv 156,28,44
Mv 124,28,40
buffer_load_dwordx4 v15, s[8:11], s39 offen offset:3072 lds
)" R"(
Mv 92,28,36
Mv 60,28,32
s_waitcnt vmcnt(8) lgkmcnt(0)
s_barrier
Mv 32,64,96
Rt 0,18,0
Mv 64,64,100
s_add_u32 s38, s38, 0xffff0080
Mv 96,64,104
Rt 2,18,2048
s_add_u32 m0, s36, 32768
Mv 128,64,108
buffer_load_dwordx4 v8, s[4:7], s38 offen offset:0 lds
Mv 160,64,112
Rt 4,19,0
Mv 192,64,116
Mv 224,64,120
Rt 6,19,2048
Ma 224,64,124
buffer_load_dwordx4 v9, s[4:7], s38 offen offset:1024 lds
Ma 228,68,124
Rt 8,20,0
Mv 228,68,120
Mv 196,68,116
Rt 10,20,2048
Mv 164,68,112
buffer_load_dwordx4 v10, s[4:7], s38 offen offset:2048 lds
Mv 132,68,108
Rt 12,21,0
Mv 100,68,104
Mv 68,68,100
Rt 14,21,2048
Mv 36,68,96
buffer_load_dwordx4 v11, s[4:7], s38 offen offset:3072 lds
Mv 40,72,96
Rt 16,22,0
Mv 72,72,100
s_add_u32 s38, s38, 0x10000
Mv 104,72,104
Rt 18,22,2048
s_add_u32 m0, s36, 36864
Mv 136,72,108
buffer_load_dwordx4 v8, s[4:7], s38 offen offset:0 lds
Mv 168,72,112
Rt 20,23,0
Mv 200,72,116
Mv 232,72,120
Rt 22,23,2048
Ma 232,72,124
buffer_load_dwordx4 v9, s[4:7], s38 offen offset:1024 lds
Ma 236,76,124
Rt 24,24,0
Mv 236,76,120
Mv 204,76,116
Rt 26,24,2048
Mv 172,76,112
buffer_load_dwordx4 v10, s[4:7], s38 offen offset:2048 lds
Mv 140,76,108
Rt 28,25,0
Mv 108,76,104
Mv 76,76,100
Rt 30,25,2048
Mv 44,76,96
buffer_load_dwordx4 v11, s[4:7], s38 offen offset:3072 lds
Mv 48,80,96
)" R"(
Rb 32,16,0
Mv 80,80,100
Mv 112,80,104
Rb 36,16,2048
Mv 144,80,108
Mv 176,80,112
Rb 40,16,8192
Mv 208,80,116
Mv 240,80,120
Rb 44,16,10240
Ma 240,80,124
Ma 244,84,124
Rb 48,16,16384
Mv 244,84,120
Mv 212,84,116
Rb 52,16,18432
Mv 180,84,112
Mv 148,84,108
Rb 56,16,24576
Mv 116,84,104
Mv 84,84,100
Rb 60,16,26624
Mv 52,84,96
Mv 56,88,96
Mv 88,88,100
Mv 120,88,104
Mv 152,88,108
Mv 184,88,112
Mv 216,88,116
Mv 248,88,120
Ma 248,88,124
Ma 252,92,124
Mv 252,92,120
Mv 220,92,116
s_sub_u32 s40, s40, 1
Mv 188,92,112
Mv 156,92,108
Mv 124,92,104
Mv 92,92,100
Mv 60,92,96
s_cmp_lg_u32 s40, 0
s_waitcnt vmcnt(12) lgkmcnt(0)
s_barrier
s_cbranch_scc1 .LzPAK
s_branch .LzPAKX
.LzAV:
Mz 32,32,0
Rt 64,18,16384
Mz 36,36,0
Mz 96,40,0
Rt 66,18,18432
Mz 100,44,0
s_add_u32 s39, s39, 0x40000
Mz 160,48,0
Rt 68,19,16384
s_add_u32 m0, s37, 49152
Mz 164,52,0
buffer_load_dwordx4 v12, s[8:11], s39 offen offset:0 lds
Mz 224,56,0
Rt 70,19,18432
Mz 228,60,0
Mz 236,60,4
Rt 72,20,16384
Mz 232,56,4
Mz 172,52,4
)" R"(
Rt 74,20,18432
Mz 168,48,4
Mz 108,44,4
Rt 76,21,16384
Mz 104,40,4
buffer_load_dwordx4 v13, s[8:11], s39 offen offset:1024 lds
Mz 44,36,4
Rt 78,21,18432
Mz 40,32,4
Mz 48,32,8
Rt 80,22,16384
Mz 52,36,8
Mz 112,40,8
Rt 82,22,18432
Mz 116,44,8
Mz 176,48,8
Rt 84,23,16384
Mz 180,52,8
buffer_load_dwordx4 v14, s[8:11], s39 offen offset:2048 lds
Mz 240,56,8
Rt 86,23,18432
Mz 244,60,8
Mz 252,60,12
Rt 88,24,16384
Mz 248,56,12
Mz 188,52,12
Rt 90,24,18432
Mz 184,48,12
Mz 124,44,12
Rt 92,25,16384
Mz 120,40,12
buffer_load_dwordx4 v15, s[8:11], s39 offen offset:3072 lds
Mz 60,36,12
Rt 94,25,18432
Mz 56,32,12
Mz 64,32,16
Rb 96,17,0
Mz 68,36,16
Mz 128,40,16
Rb 100,17,2048
Mz 132,44,16
s_add_u32 s39, s39, 0x40000
Mz 192,48,16
Rb 104,17,8192
s_add_u32 m0, s37, 0
Mz 196,52,16
buffer_load_dwordx4 v12, s[8:11], s39 offen offset:0 lds
v_mfma_f32_16x16x32_bf16 a[224:227], a[56:59], a[16:19], 0
Rb 108,17,10240
v_mfma_f32_16x16x32_bf16 a[228:231], a[60:63], a[16:19], 0
v_mfma_f32_16x16x32_bf16 a[236:239], a[60:63], a[20:23], 0
Rb 112,17,16384
v_mfma_f32_16x16x32_bf16 a[232:235], a[56:59], a[20:23], 0
Mz 204,52,20
Rb 116,17,18432
Mz 200,48,20
Mz 140,44,20
Rb 120,17,24576
Mz 136,40,20
buffer_load_dwordx4 v13, s[8:11], s39 offen offset:1024 lds
Mz 76,36,20
Rb 124,17,26624
Mz 72,32,20
Mz 80,32,24
)" R"(
Mz 84,36,24
Mz 144,40,24
Mz 148,44,24
Mz 208,48,24
Mz 212,52,24
buffer_load_dwordx4 v14, s[8:11], s39 offen offset:2048 lds
v_mfma_f32_16x16x32_bf16 a[240:243], a[56:59], a[24:27], 0
v_mfma_f32_16x16x32_bf16 a[244:247], a[60:63], a[24:27], 0
v_mfma_f32_16x16x32_bf16 a[252:255], a[60:63], a[28:31], 0
v_mfma_f32_16x16x32_bf16 a[248:251], a[56:59], a[28:31], 0
Mz 220,52,28
Mz 216,48,28
Mz 156,44,28
Mz 152,40,28
buffer_load_dwordx4 v15, s[8:11], s39 offen offset:3072 lds
Mz 92,36,28
Mz 88,32,28
s_waitcnt vmcnt(12) lgkmcnt(0)
s_barrier
Mv 32,96,64
Rt 0,18,32768
Mv 36,100,64
s_add_u32 s38, s38, 0xffff0080
Mv 96,104,64
Rt 2,18,34816
s_add_u32 m0, s36, 0
Mv 100,108,64
buffer_load_dwordx4 v8, s[4:7], s38 offen offset:0 lds
Mv 160,112,64
Rt 4,19,32768
Mv 164,116,64
Mv 224,120,64
Rt 6,19,34816
Mv 228,124,64
buffer_load_dwordx4 v9, s[4:7], s38 offen offset:1024 lds
Mv 236,124,68
Rt 8,20,32768
Mv 232,120,68
Mv 172,116,68
Rt 10,20,34816
Mv 168,112,68
buffer_load_dwordx4 v10, s[4:7], s38 offen offset:2048 lds
Mv 108,108,68
Rt 12,21,32768
Mv 104,104,68
Mv 44,100,68
Rt 14,21,34816
Mv 40,96,68
buffer_load_dwordx4 v11, s[4:7], s38 offen offset:3072 lds
Mv 48,96,72
Rt 16,22,32768
Mv 52,100,72
s_add_u32 s38, s38, 0x10000
Mv 112,104,72
Rt 18,22,34816
s_add_u32 m0, s36, 4096
Mv 116,108,72
buffer_load_dwordx4 v8, s[4:7], s38 offen offset:0 lds
Mv 176,112,72
Rt 20,23,32768
Mv 180,116,72
Mv 240,120,72
Rt 22,23,34816
Mv 244,124,72
buffer_load_dwordx4 v9, s[4:7], s38 offen offset:1024 lds
)" R"(
Mv 252,124,76
Rt 24,24,32768
Mv 248,120,76
Mv 188,116,76
Rt 26,24,34816
Mv 184,112,76
buffer_load_dwordx4 v10, s[4:7], s38 offen offset:2048 lds
Mv 124,108,76
Rt 28,25,32768
Mv 120,104,76
Mv 60,100,76
Rt 30,25,34816
Mv 56,96,76
buffer_load_dwordx4 v11, s[4:7], s38 offen offset:3072 lds
Mv 64,96,80
Rb 32,16,32768
Mv 68,100,80
Mv 128,104,80
Rb 36,16,34816
Mv 132,108,80
Mv 192,112,80
Rb 40,16,40960
Mv 196,116,80
Ma 224,120,80
Rb 44,16,43008
Ma 228,124,80
Ma 236,124,84
Rb 48,16,49152
Ma 232,120,84
Mv 204,116,84
Rb 52,16,51200
Mv 200,112,84
Mv 140,108,84
Rb 56,16,57344
Mv 136,104,84
Mv 76,100,84
Rb 60,16,59392
Mv 72,96,84
Mv 80,96,88
Mv 84,100,88
Mv 144,104,88
Mv 148,108,88
Mv 208,112,88
Mv 212,116,88
Ma 240,120,88
Ma 244,124,88
Ma 252,124,92
Ma 248,120,92
Mv 220,116,92
Mv 216,112,92
Mv 156,108,92
Mv 152,104,92
Mv 92,100,92
Mv 88,96,92
s_waitcnt vmcnt(12) lgkmcnt(0)
s_barrier
Mv 32,32,0
Rt 64,18,49152
s_mov_b32 s85, 0xc108800
v_and_b32 v2, 7, v0
Mv 36,36,0
v_lshlrev_b32 v2, 7, v2
Mv 96,40,0
Rt 66,18,51200
buffer_load_dword v4, v2, s[20:23], s85 offen sc0 sc1
)" R"(
Mv 100,44,0
s_add_u32 s39, s39, 0x40000
Mv 160,48,0
Rt 68,19,49152
s_add_u32 m0, s37, 16384
Mv 164,52,0
buffer_load_dwordx4 v12, s[8:11], s39 offen offset:0 lds
Mv 224,56,0
Rt 70,19,51200
Mv 228,60,0
Mv 236,60,4
Rt 72,20,49152
Mv 232,56,4
Mv 172,52,4
Rt 74,20,51200
Mv 168,48,4
Mv 108,44,4
Rt 76,21,49152
Mv 104,40,4
buffer_load_dwordx4 v13, s[8:11], s39 offen offset:1024 lds
Mv 44,36,4
Rt 78,21,51200
Mv 40,32,4
Mv 48,32,8
Rt 80,22,49152
Mv 52,36,8
Mv 112,40,8
Rt 82,22,51200
Mv 116,44,8
Mv 176,48,8
Rt 84,23,49152
Mv 180,52,8
buffer_load_dwordx4 v14, s[8:11], s39 offen offset:2048 lds
Mv 240,56,8
Rt 86,23,51200
Mv 244,60,8
Mv 252,60,12
Rt 88,24,49152
Mv 248,56,12
Mv 188,52,12
Rt 90,24,51200
Mv 184,48,12
Mv 124,44,12
Rt 92,25,49152
Mv 120,40,12
buffer_load_dwordx4 v15, s[8:11], s39 offen offset:3072 lds
Mv 60,36,12
Rt 94,25,51200
Mv 56,32,12
Mv 64,32,16
Rb 96,17,32768
Mv 68,36,16
Mv 128,40,16
Rb 100,17,34816
Mv 132,44,16
s_add_u32 s39, s39, 0x40000
Mv 192,48,16
Rb 104,17,40960
s_add_u32 m0, s37, 32768
Mv 196,52,16
buffer_load_dwordx4 v12, s[8:11], s39 offen offset:0 lds
Ma 224,56,16
Rb 108,17,43008
Ma 228,60,16
)" R"(
Ma 236,60,20
Rb 112,17,49152
Ma 232,56,20
Mv 204,52,20
Rb 116,17,51200
Mv 200,48,20
Mv 140,44,20
Rb 120,17,57344
Mv 136,40,20
buffer_load_dwordx4 v13, s[8:11], s39 offen offset:1024 lds
Mv 76,36,20
Rb 124,17,59392
Mv 72,32,20
Mv 80,32,24
Rt 176,29,0
Mv 84,36,24
Rt 178,29,8192
Mv 144,40,24
Rt 180,29,128
Mv 148,44,24
Rt 182,29,8320
Mv 208,48,24
Rt 184,29,256
Mv 212,52,24
buffer_load_dwordx4 v14, s[8:11], s39 offen offset:2048 lds
Rt 186,29,8448
Ma 240,56,24
Rt 188,29,384
Ma 244,60,24
Rt 190,29,8576
Ma 252,60,28
Ma 248,56,28
Mv 220,52,28
Mv 216,48,28
Mv 156,44,28
Mv 152,40,28
buffer_load_dwordx4 v15, s[8:11], s39 offen offset:3072 lds
Mv 92,36,28
Mv 88,32,28
s_waitcnt vmcnt(8) lgkmcnt(0)
s_barrier
Mv 32,96,64
Rt 0,18,0
v_xor_b32 v4, s3, v4
v_cmp_gt_u32_e32 vcc, 2, v4
s_andn2_b32 s28, 0xff, vcc_lo
s_cbranch_scc1 .Lzsp2
.Lzok2:
Mv 36,100,64
s_add_u32 s38, s38, 0xffff0080
v_xor_b32 v4, s3, v4
Mv 96,104,64
Rt 2,18,2048
s_add_u32 m0, s36, 32768
Mv 100,108,64
buffer_load_dwordx4 v8, s[4:7], s38 offen offset:0 lds
Mv 160,112,64
Rt 4,19,0
v_and_b32 v4, 1, v4
Mv 164,116,64
v_cmp_eq_u32_e32 vcc, 1, v4
s_mov_b32 s85, vcc_lo
Mv 224,120,64
Rt 6,19,2048
s_and_b32 s28, s2, 7
)" R"(
Mv 228,124,64
buffer_load_dwordx4 v9, s[4:7], s38 offen offset:1024 lds
Mv 236,124,68
Rt 8,20,0
s_lshr_b32 s28, s85, s28
Mv 232,120,68
s_xor_b32 s28, s28, s34
s_and_b32 s28, s28, 1
Mv 172,116,68
Rt 10,20,2048
s_lshl_b32 s84, s28, 13
Mv 168,112,68
buffer_load_dwordx4 v10, s[4:7], s38 offen offset:2048 lds
Mv 108,108,68
Rt 12,21,0
s_lshl1_add_u32 s28, s36, s84
Mv 104,104,68
v_add_u32 v2, s28, v1
v_add_u32 v3, 0x1000, v2
Mv 44,100,68
Rt 14,21,2048
s_bfe_u32 s28, s73, 0x30010
Mv 40,96,68
buffer_load_dwordx4 v11, s[4:7], s38 offen offset:3072 lds
Mv 48,96,72
Rt 16,22,0
s_lshr_b32 s28, s85, s28
Mv 52,100,72
s_add_u32 s38, s38, 0x10000
s_and_b32 s28, s28, 1
Mv 112,104,72
Rt 18,22,2048
s_add_u32 m0, s36, 36864
Mv 116,108,72
buffer_load_dwordx4 v8, s[4:7], s38 offen offset:0 lds
Mv 176,112,72
Rt 20,23,0
s_lshl_b32 s28, s28, 13
Mv 180,116,72
s_add_u32 s73, s73, s28
Mv 240,120,72
Rt 22,23,2048
Mv 244,124,72
buffer_load_dwordx4 v9, s[4:7], s38 offen offset:1024 lds
Mv 252,124,76
Rt 24,24,0
Mv 248,120,76
Mv 188,116,76
Rt 26,24,2048
Mv 184,112,76
buffer_load_dwordx4 v10, s[4:7], s38 offen offset:2048 lds
Mv 124,108,76
Rt 28,25,0
s_lshl3_add_u32 s28, s94, s92
Mv 120,104,76
s_lshl3_add_u32 s29, s92, s94
s_cmp_lt_u32 s94, 8
s_cmov_b32 s28, s29
Mv 60,100,76
Rt 30,25,2048
s_mul_i32 s29, s28, 293
s_lshr_b32 s29, s29, 11
Mv 56,96,76
buffer_load_dwordx4 v11, s[4:7], s38 offen offset:3072 lds
)" R"(
Mv 64,96,80
Rb 32,16,0
s_mul_i32 s29, s29, 9
s_add_u32 s29, s29, s28
Mv 68,100,80
s_lshl_b32 s29, s29, 10
s_add_u32 s29, s29, s82
s_add_u32 s29, s29, s84
Mv 128,104,80
Rb 36,16,2048
s_add_u32 s94, s94, 4
Mv 132,108,80
buffer_store_dwordx4 a[176:179], v1, s[20:23], s29 offen sc0 sc1
Mv 192,112,80
Rb 40,16,8192
s_lshl3_add_u32 s28, s94, s92
Mv 196,116,80
s_lshl3_add_u32 s29, s92, s94
Ma 224,120,80
Rb 44,16,10240
s_cmp_lt_u32 s94, 8
s_cmov_b32 s28, s29
Ma 228,124,80
s_mul_i32 s29, s28, 293
s_lshr_b32 s29, s29, 11
Ma 236,124,84
Rb 48,16,16384
s_mul_i32 s29, s29, 9
s_add_u32 s29, s29, s28
Ma 232,120,84
s_lshl_b32 s29, s29, 10
s_add_u32 s29, s29, s82
Mv 204,116,84
Rb 52,16,18432
s_add_u32 s29, s29, s84
Mv 200,112,84
s_add_u32 s94, s94, 4
buffer_store_dwordx4 a[180:183], v1, s[20:23], s29 offen sc0 sc1
Mv 140,108,84
Rb 56,16,24576
s_lshl3_add_u32 s28, s94, s92
Mv 136,104,84
s_lshl3_add_u32 s29, s92, s94
Mv 76,100,84
Rb 60,16,26624
s_cmp_lt_u32 s94, 8
s_cmov_b32 s28, s29
Mv 72,96,84
s_mul_i32 s29, s28, 293
s_lshr_b32 s29, s29, 11
Mv 80,96,88
s_mul_i32 s29, s29, 9
s_add_u32 s29, s29, s28
Mv 84,100,88
s_lshl_b32 s29, s29, 10
s_add_u32 s29, s29, s82
Mv 144,104,88
s_add_u32 s29, s29, s84
s_add_u32 s94, s94, 4
Mv 148,108,88
buffer_store_dwordx4 a[184:187], v1, s[20:23], s29 offen sc0 sc1
Mv 208,112,88
s_lshl3_add_u32 s28, s94, s92
s_lshl3_add_u32 s29, s92, s94
)" R"(
Mv 212,116,88
s_cmp_lt_u32 s94, 8
s_cmov_b32 s28, s29
Ma 240,120,88
s_mul_i32 s29, s28, 293
s_lshr_b32 s29, s29, 11
Ma 244,124,88
s_mul_i32 s29, s29, 9
s_add_u32 s29, s29, s28
Ma 252,124,92
s_lshl_b32 s29, s29, 10
s_add_u32 s29, s29, s82
Ma 248,120,92
s_add_u32 s29, s29, s84
s_mov_b32 s40, 5
Mv 220,116,92
s_cmp_gt_u32 s33, 1
s_cmov_b32 s29, 0x40000000
Mv 216,112,92
buffer_store_dwordx4 a[188:191], v1, s[20:23], s29 offen sc0 sc1
Mv 156,108,92
Mv 152,104,92
Mv 92,100,92
Mv 88,96,92
s_waitcnt vmcnt(16) lgkmcnt(0)
s_barrier
s_mov_b32 s28, s35
s_lshl2_add_u32 s28, s28, s33
s_mul_i32 s43, s28, 28672
v_and_b32 v26, 0x70, v1
v_and_b32 v4, 48, v0
v_xor_b32 v26, v26, v4
v_lshrrev_b32 v4, 3, v0
v_mul_u32_u24 v4, 896, v4
v_add_u32 v26, v26, v4
v_xor_b32 v27, 64, v26
v_add_u32 v27, 6144, v27
v_bfe_u32 v28, v0, 1, 3
v_lshrrev_b32 v4, 4, v0
v_xor_b32 v28, v28, v4
v_lshlrev_b32 v28, 4, v28
v_and_b32 v4, 15, v0
v_lshl_or_b32 v28, v4, 7, v28
s_add_u32 s28, s36, 0x18000
v_add_u32 v28, s28, v28
v_xor_b32 v29, 64, v28
s_add_u32 m0, s36, 131072
s_add_u32 s44, s43, 0
buffer_load_dwordx4 v26, s[12:15], s44 offen offset:0 lds
buffer_load_dwordx4 v27, s[12:15], s44 offen offset:1024 lds
s_add_u32 s44, s43, 12288
buffer_load_dwordx4 v26, s[12:15], s44 offen offset:2048 lds
buffer_load_dwordx4 v27, s[12:15], s44 offen offset:3072 lds
s_add_u32 m0, s36, 135168
s_add_u32 s44, s43, 128
buffer_load_dwordx4 v26, s[12:15], s44 offen offset:0 lds
buffer_load_dwordx4 v27, s[12:15], s44 offen offset:1024 lds
s_add_u32 s44, s43, 12416
buffer_load_dwordx4 v26, s[12:15], s44 offen offset:2048 lds
buffer_load_dwordx4 v27, s[12:15], s44 offen offset:3072 lds
.LzPAV:
Mv 32,32,0
Rt 64,18,16384
Mv 36,36,0
Mv 96,40,0
)" R"(
Rt 66,18,18432
Mv 100,44,0
s_add_u32 s39, s39, 0x40000
Mv 160,48,0
Rt 68,19,16384
s_add_u32 m0, s37, 49152
Mv 164,52,0
buffer_load_dwordx4 v12, s[8:11], s39 offen offset:0 lds
Mv 224,56,0
Rt 70,19,18432
Mv 228,60,0
Mv 236,60,4
Rt 72,20,16384
Mv 232,56,4
Mv 172,52,4
Rt 74,20,18432
Mv 168,48,4
Mv 108,44,4
Rt 76,21,16384
Mv 104,40,4
buffer_load_dwordx4 v13, s[8:11], s39 offen offset:1024 lds
Mv 44,36,4
Rt 78,21,18432
Mv 40,32,4
Mv 48,32,8
Rt 80,22,16384
Mv 52,36,8
Mv 112,40,8
Rt 82,22,18432
Mv 116,44,8
Mv 176,48,8
Rt 84,23,16384
Mv 180,52,8
buffer_load_dwordx4 v14, s[8:11], s39 offen offset:2048 lds
Mv 240,56,8
Rt 86,23,18432
Mv 244,60,8
Mv 252,60,12
Rt 88,24,16384
Mv 248,56,12
Mv 188,52,12
Rt 90,24,18432
Mv 184,48,12
Mv 124,44,12
Rt 92,25,16384
Mv 120,40,12
buffer_load_dwordx4 v15, s[8:11], s39 offen offset:3072 lds
Mv 60,36,12
Rt 94,25,18432
Mv 56,32,12
Mv 64,32,16
Rb 96,17,0
Mv 68,36,16
Mv 128,40,16
Rb 100,17,2048
Mv 132,44,16
s_add_u32 s39, s39, 0x40000
Mv 192,48,16
Rb 104,17,8192
s_add_u32 m0, s37, 0
Mv 196,52,16
buffer_load_dwordx4 v12, s[8:11], s39 offen offset:0 lds
Ma 224,56,16
Rb 108,17,10240
)" R"(
Ma 228,60,16
Ma 236,60,20
Rb 112,17,16384
Ma 232,56,20
Mv 204,52,20
Rb 116,17,18432
Mv 200,48,20
Mv 140,44,20
Rb 120,17,24576
Mv 136,40,20
buffer_load_dwordx4 v13, s[8:11], s39 offen offset:1024 lds
Mv 76,36,20
Rb 124,17,26624
Mv 72,32,20
Mv 80,32,24
Mv 84,36,24
Mv 144,40,24
Mv 148,44,24
Mv 208,48,24
Mv 212,52,24
buffer_load_dwordx4 v14, s[8:11], s39 offen offset:2048 lds
Ma 240,56,24
Ma 244,60,24
Ma 252,60,28
Ma 248,56,28
Mv 220,52,28
Mv 216,48,28
Mv 156,44,28
Mv 152,40,28
buffer_load_dwordx4 v15, s[8:11], s39 offen offset:3072 lds
Mv 92,36,28
Mv 88,32,28
s_waitcnt vmcnt(8) lgkmcnt(0)
s_barrier
Mv 32,96,64
Rt 0,18,32768
Mv 36,100,64
s_add_u32 s38, s38, 0xffff0080
Mv 96,104,64
Rt 2,18,34816
s_add_u32 m0, s36, 0
Mv 100,108,64
buffer_load_dwordx4 v8, s[4:7], s38 offen offset:0 lds
Mv 160,112,64
Rt 4,19,32768
Mv 164,116,64
Mv 224,120,64
Rt 6,19,34816
Mv 228,124,64
buffer_load_dwordx4 v9, s[4:7], s38 offen offset:1024 lds
Mv 236,124,68
Rt 8,20,32768
Mv 232,120,68
Mv 172,116,68
Rt 10,20,34816
Mv 168,112,68
buffer_load_dwordx4 v10, s[4:7], s38 offen offset:2048 lds
Mv 108,108,68
Rt 12,21,32768
Mv 104,104,68
Mv 44,100,68
Rt 14,21,34816
Mv 40,96,68
buffer_load_dwordx4 v11, s[4:7], s38 offen offset:3072 lds
Mv 48,96,72
)" R"(
Rt 16,22,32768
Mv 52,100,72
s_add_u32 s38, s38, 0x10000
Mv 112,104,72
Rt 18,22,34816
s_add_u32 m0, s36, 4096
Mv 116,108,72
buffer_load_dwordx4 v8, s[4:7], s38 offen offset:0 lds
Mv 176,112,72
Rt 20,23,32768
Mv 180,116,72
Mv 240,120,72
Rt 22,23,34816
Mv 244,124,72
buffer_load_dwordx4 v9, s[4:7], s38 offen offset:1024 lds
Mv 252,124,76
Rt 24,24,32768
Mv 248,120,76
Mv 188,116,76
Rt 26,24,34816
Mv 184,112,76
buffer_load_dwordx4 v10, s[4:7], s38 offen offset:2048 lds
Mv 124,108,76
Rt 28,25,32768
Mv 120,104,76
Mv 60,100,76
Rt 30,25,34816
Mv 56,96,76
buffer_load_dwordx4 v11, s[4:7], s38 offen offset:3072 lds
Mv 64,96,80
Rb 32,16,32768
Mv 68,100,80
Mv 128,104,80
Rb 36,16,34816
Mv 132,108,80
Mv 192,112,80
Rb 40,16,40960
Mv 196,116,80
Ma 224,120,80
Rb 44,16,43008
Ma 228,124,80
Ma 236,124,84
Rb 48,16,49152
Ma 232,120,84
Mv 204,116,84
Rb 52,16,51200
Mv 200,112,84
Mv 140,108,84
Rb 56,16,57344
Mv 136,104,84
Mv 76,100,84
Rb 60,16,59392
Mv 72,96,84
Mv 80,96,88
Mv 84,100,88
Mv 144,104,88
Mv 148,108,88
Mv 208,112,88
Mv 212,116,88
Ma 240,120,88
Ma 244,124,88
Ma 252,124,92
Ma 248,120,92
Mv 220,116,92
)" R"(
Mv 216,112,92
Mv 156,108,92
Mv 152,104,92
Mv 92,100,92
Mv 88,96,92
s_waitcnt vmcnt(12) lgkmcnt(0)
s_barrier
Mv 32,32,0
Rt 64,18,49152
Mv 36,36,0
Mv 96,40,0
Rt 66,18,51200
Mv 100,44,0
s_add_u32 s39, s39, 0x40000
Mv 160,48,0
Rt 68,19,49152
s_add_u32 m0, s37, 16384
Mv 164,52,0
buffer_load_dwordx4 v12, s[8:11], s39 offen offset:0 lds
Mv 224,56,0
Rt 70,19,51200
Mv 228,60,0
Mv 236,60,4
Rt 72,20,49152
Mv 232,56,4
Mv 172,52,4
Rt 74,20,51200
Mv 168,48,4
Mv 108,44,4
Rt 76,21,49152
Mv 104,40,4
buffer_load_dwordx4 v13, s[8:11], s39 offen offset:1024 lds
Mv 44,36,4
Rt 78,21,51200
Mv 40,32,4
Mv 48,32,8
Rt 80,22,49152
Mv 52,36,8
Mv 112,40,8
Rt 82,22,51200
Mv 116,44,8
Mv 176,48,8
Rt 84,23,49152
Mv 180,52,8
buffer_load_dwordx4 v14, s[8:11], s39 offen offset:2048 lds
Mv 240,56,8
Rt 86,23,51200
Mv 244,60,8
Mv 252,60,12
Rt 88,24,49152
Mv 248,56,12
Mv 188,52,12
Rt 90,24,51200
Mv 184,48,12
Mv 124,44,12
Rt 92,25,49152
Mv 120,40,12
buffer_load_dwordx4 v15, s[8:11], s39 offen offset:3072 lds
Mv 60,36,12
Rt 94,25,51200
Mv 56,32,12
Mv 64,32,16
Rb 96,17,32768
Mv 68,36,16
Mv 128,40,16
)" R"(
Rb 100,17,34816
Mv 132,44,16
s_add_u32 s39, s39, 0x40000
Mv 192,48,16
Rb 104,17,40960
s_add_u32 m0, s37, 32768
Mv 196,52,16
buffer_load_dwordx4 v12, s[8:11], s39 offen offset:0 lds
Ma 224,56,16
Rb 108,17,43008
Ma 228,60,16
Ma 236,60,20
Rb 112,17,49152
Ma 232,56,20
Mv 204,52,20
Rb 116,17,51200
Mv 200,48,20
Mv 140,44,20
Rb 120,17,57344
Mv 136,40,20
buffer_load_dwordx4 v13, s[8:11], s39 offen offset:1024 lds
Mv 76,36,20
Rb 124,17,59392
Mv 72,32,20
Mv 80,32,24
Mv 84,36,24
Mv 144,40,24
Mv 148,44,24
Mv 208,48,24
Mv 212,52,24
buffer_load_dwordx4 v14, s[8:11], s39 offen offset:2048 lds
Ma 240,56,24
Ma 244,60,24
Ma 252,60,28
Ma 248,56,28
Mv 220,52,28
Mv 216,48,28
Mv 156,44,28
Mv 152,40,28
buffer_load_dwordx4 v15, s[8:11], s39 offen offset:3072 lds
Mv 92,36,28
Mv 88,32,28
s_waitcnt vmcnt(8) lgkmcnt(0)
s_barrier
Mv 32,96,64
Rt 0,18,0
Mv 36,100,64
s_add_u32 s38, s38, 0xffff0080
Mv 96,104,64
Rt 2,18,2048
s_add_u32 m0, s36, 32768
Mv 100,108,64
buffer_load_dwordx4 v8, s[4:7], s38 offen offset:0 lds
Mv 160,112,64
Rt 4,19,0
Mv 164,116,64
Mv 224,120,64
Rt 6,19,2048
Mv 228,124,64
buffer_load_dwordx4 v9, s[4:7], s38 offen offset:1024 lds
Mv 236,124,68
Rt 8,20,0
Mv 232,120,68
Mv 172,116,68
Rt 10,20,2048
)" R"(
Mv 168,112,68
buffer_load_dwordx4 v10, s[4:7], s38 offen offset:2048 lds
Mv 108,108,68
Rt 12,21,0
Mv 104,104,68
Mv 44,100,68
Rt 14,21,2048
Mv 40,96,68
buffer_load_dwordx4 v11, s[4:7], s38 offen offset:3072 lds
Mv 48,96,72
Rt 16,22,0
Mv 52,100,72
s_add_u32 s38, s38, 0x10000
Mv 112,104,72
Rt 18,22,2048
s_add_u32 m0, s36, 36864
Mv 116,108,72
buffer_load_dwordx4 v8, s[4:7], s38 offen offset:0 lds
Mv 176,112,72
Rt 20,23,0
Mv 180,116,72
Mv 240,120,72
Rt 22,23,2048
Mv 244,124,72
buffer_load_dwordx4 v9, s[4:7], s38 offen offset:1024 lds
Mv 252,124,76
Rt 24,24,0
Mv 248,120,76
Mv 188,116,76
Rt 26,24,2048
Mv 184,112,76
buffer_load_dwordx4 v10, s[4:7], s38 offen offset:2048 lds
Mv 124,108,76
Rt 28,25,0
Mv 120,104,76
Mv 60,100,76
Rt 30,25,2048
Mv 56,96,76
buffer_load_dwordx4 v11, s[4:7], s38 offen offset:3072 lds
Mv 64,96,80
Rb 32,16,0
Mv 68,100,80
Mv 128,104,80
Rb 36,16,2048
Mv 132,108,80
Mv 192,112,80
Rb 40,16,8192
Mv 196,116,80
Ma 224,120,80
Rb 44,16,10240
Ma 228,124,80
Ma 236,124,84
Rb 48,16,16384
Ma 232,120,84
Mv 204,116,84
Rb 52,16,18432
Mv 200,112,84
Mv 140,108,84
Rb 56,16,24576
Mv 136,104,84
Mv 76,100,84
Rb 60,16,26624
Mv 72,96,84
Mv 80,96,88
)" R"(
Mv 84,100,88
Mv 144,104,88
Mv 148,108,88
Mv 208,112,88
Mv 212,116,88
Ma 240,120,88
Ma 244,124,88
Ma 252,124,92
Ma 248,120,92
Mv 220,116,92
s_sub_u32 s40, s40, 1
Mv 216,112,92
Mv 156,108,92
Mv 152,104,92
Mv 92,100,92
Mv 88,96,92
s_cmp_lg_u32 s40, 0
s_waitcnt vmcnt(12) lgkmcnt(0)
s_barrier
s_cbranch_scc1 .LzPAV
s_branch .LzPAVX
.LzB0:
s_mul_i32 s41, s33, 7168
s_mul_i32 s42, s33, 0x70000
s_lshl_b32 s28, s34, 9
s_add_u32 s42, s42, s28
v_lshrrev_b32 v28, 3, v0
v_and_b32 v29, 0x70, v1
v_lshl_or_b32 v28, v28, 13, v29
v_lshlrev_b32 v29, 1, v0
v_and_b32 v29, 32, v29
v_xor_b32 v28, v28, v29
s_and_b32 s28, s33, 1
s_lshl_b32 s28, s28, 6
v_xor_b32 v240, s28, v28
v_xor_b32 v241, 64, v240
v_add_u32 v241, 0xfc00, v241
v_add_u32 v242, 0x1f800, v240
v_add_u32 v243, 0x1f800, v241
s_sub_u32 s28, 15, s35
s_lshl2_add_u32 s28, s28, s33
s_mul_i32 s43, s28, 28672
v_and_b32 v2, 0x70, v1
v_and_b32 v28, 48, v0
v_xor_b32 v2, v2, v28
v_lshrrev_b32 v28, 3, v0
v_mul_u32_u24 v28, 896, v28
v_add_u32 v2, v2, v28
v_xor_b32 v3, 64, v2
v_add_u32 v3, 6144, v3
v_bfe_u32 v4, v0, 1, 3
v_lshrrev_b32 v28, 4, v0
v_xor_b32 v4, v4, v28
v_lshlrev_b32 v4, 4, v4
v_and_b32 v28, 15, v0
v_lshl_or_b32 v4, v28, 7, v4
s_add_u32 s28, s36, 0x0
v_add_u32 v4, s28, v4
v_xor_b32 v5, 64, v4
s_waitcnt lgkmcnt(0)
s_mov_b32 s16, s68
s_mov_b32 s17, s69
s_mov_b32 s18, 0x380000
s_mov_b32 s19, 0x20000
s_mov_b32 s12, s64
)" R"(
s_mov_b32 s13, s65
s_mov_b32 s14, 0x1c0000
s_mov_b32 s15, 0x20000
s_add_u32 m0, s36, 0
s_add_u32 s44, s43, 0
buffer_load_dwordx4 v2, s[12:15], s44 offen offset:0 lds
buffer_load_dwordx4 v3, s[12:15], s44 offen offset:1024 lds
s_add_u32 s44, s43, 12288
buffer_load_dwordx4 v2, s[12:15], s44 offen offset:2048 lds
buffer_load_dwordx4 v3, s[12:15], s44 offen offset:3072 lds
s_add_u32 m0, s41, 65536
s_add_u32 s92, s42, 0x0
buffer_load_dwordx4 v240, s[16:19], s92 offen offset:0 lds
buffer_load_dwordx4 v241, s[16:19], s92 offen offset:1024 lds
buffer_load_dwordx4 v242, s[16:19], s92 offen offset:2048 lds
buffer_load_dwordx4 v243, s[16:19], s92 offen offset:3072 lds
s_add_u32 m0, s41, 69632
s_add_u32 s92, s42, 0x40000
buffer_load_dwordx4 v240, s[16:19], s92 offen offset:0 lds
buffer_load_dwordx4 v241, s[16:19], s92 offen offset:1024 lds
buffer_load_dwordx4 v242, s[16:19], s92 offen offset:2048 lds
s_add_u32 m0, s36, 4096
s_add_u32 s44, s43, 128
buffer_load_dwordx4 v2, s[12:15], s44 offen offset:0 lds
buffer_load_dwordx4 v3, s[12:15], s44 offen offset:1024 lds
s_add_u32 s44, s43, 12416
buffer_load_dwordx4 v2, s[12:15], s44 offen offset:2048 lds
buffer_load_dwordx4 v3, s[12:15], s44 offen offset:3072 lds
s_add_u32 m0, s36, 32768
s_add_u32 s44, s43, 256
buffer_load_dwordx4 v2, s[12:15], s44 offen offset:0 lds
buffer_load_dwordx4 v3, s[12:15], s44 offen offset:1024 lds
s_add_u32 s44, s43, 12544
buffer_load_dwordx4 v2, s[12:15], s44 offen offset:2048 lds
buffer_load_dwordx4 v3, s[12:15], s44 offen offset:3072 lds
s_add_u32 m0, s36, 36864
s_add_u32 s44, s43, 384
buffer_load_dwordx4 v2, s[12:15], s44 offen offset:0 lds
buffer_load_dwordx4 v3, s[12:15], s44 offen offset:1024 lds
s_add_u32 s44, s43, 12672
buffer_load_dwordx4 v2, s[12:15], s44 offen offset:2048 lds
buffer_load_dwordx4 v3, s[12:15], s44 offen offset:3072 lds
s_add_u32 m0, s41, 98304
s_add_u32 s92, s42, 0x80
buffer_load_dwordx4 v240, s[16:19], s92 offen offset:0 lds
buffer_load_dwordx4 v241, s[16:19], s92 offen offset:1024 lds
buffer_load_dwordx4 v242, s[16:19], s92 offen offset:2048 lds
buffer_load_dwordx4 v243, s[16:19], s92 offen offset:3072 lds
s_add_u32 m0, s41, 102400
s_add_u32 s92, s42, 0x40080
buffer_load_dwordx4 v240, s[16:19], s92 offen offset:0 lds
buffer_load_dwordx4 v241, s[16:19], s92 offen offset:1024 lds
buffer_load_dwordx4 v242, s[16:19], s92 offen offset:2048 lds
s_sub_u32 s90, s2, 128
s_lshl2_add_u32 s90, s90, s33
s_lshl_b32 s28, s90, 13
v_lshlrev_b32 v26, 7, v0
s_lshl_b32 s90, s90, 12
v_lshrrev_b32 v28, 5, v0
v_lshl_or_b32 v26, v28, 30, v26
s_mov_b32 s28, 0xc108400
s_load_dword s3, s[74:75], s28
s_sub_u32 s94, s2, 128
s_lshl2_add_u32 s94, s94, s33
)" R"(
s_lshl_b32 s94, s94, 2
s_mul_i32 s29, s94, 896
v_mov_b32 v120, 0x40000000
v_cmp_gt_u32_e32 vcc, 56, v0
s_nop 1
v_cndmask_b32_e32 v120, v120, v1, vcc
buffer_load_dwordx4 v[96:99], v120, s[12:15], s29 offen offset:0
buffer_load_dwordx4 v[100:103], v120, s[12:15], s29 offen offset:896
buffer_load_dwordx4 v[104:107], v120, s[12:15], s29 offen offset:1792
buffer_load_dwordx4 v[108:111], v120, s[12:15], s29 offen offset:2688
s_mov_b32 s24, s76
s_mov_b32 s25, s77
s_mov_b32 s26, 0x380
s_mov_b32 s27, 0x20000
buffer_load_dwordx4 v[112:115], v120, s[24:27], 0 offen
s_mov_b32 s24, s78
s_mov_b32 s25, s79
s_mov_b32 s26, 0x380
s_mov_b32 s27, 0x20000
buffer_load_dwordx4 v[116:119], v120, s[24:27], 0 offen
s_mov_b32 s20, s74
s_mov_b32 s21, s75
s_mov_b32 s22, 0xc1b2000
s_mov_b32 s23, 0x20000
s_mov_b32 s4, s66
s_mov_b32 s5, s67
s_mov_b32 s6, 0x400000
s_mov_b32 s7, 0x20000
s_cmp_eq_u32 s91, 1
s_cselect_b32 s8, s72, s70
s_cselect_b32 s9, s73, s71
s_mov_b32 s10, 0x800000
s_mov_b32 s11, 0x20000
s_load_dwordx2 s[74:75], s[0:1], 96
s_load_dwordx4 s[76:79], s[0:1], 72
s_load_dwordx2 s[70:71], s[0:1], 88
v_bfe_u32 v244, v0, 3, 2
v_lshlrev_b32 v244, 5, v244
v_and_b32 v28, 3, v0
v_lshl_or_b32 v244, v28, 3, v244
v_bfe_u32 v28, v0, 2, 2
v_lshl_or_b32 v244, v28, 7, v244
v_lshrrev_b32 v28, 4, v0
v_lshl_or_b32 v244, v28, 10, v244
v_xor_b32 v245, 32, v244
v_xor_b32 v246, 64, v244
v_xor_b32 v247, 96, v244
v_add_u32 v252, 0x20000, v244
v_add_u32 v253, 0x20000, v245
v_add_u32 v254, 0x20000, v246
v_add_u32 v255, 0x20000, v247
v_add_u32 v248, 0x10000, v244
v_add_u32 v249, 0x10000, v245
v_add_u32 v250, 0x10000, v246
v_add_u32 v251, 0x10000, v247
s_mov_b32 s63, 0xe0c0a08
s_mov_b32 s28, 0x100d0b09
s_bitcmp1_b32 s2, 5
s_cmov_b32 s63, 0xf1e1c1a
s_cmov_b32 s28, 0x111f1d1b
s_bitcmp1_b32 s2, 6
s_cmov_b32 s63, s28
s_lshl_b32 s28, s33, 3
s_lshr_b32 s28, s63, s28
)" R"(
s_and_b32 s28, s28, 255
s_lshl_b32 s38, s28, 17
s_add_u32 s38, s38, 0xff80
s_lshl_b32 s39, s33, 16
s_lshl_b32 s28, s34, 9
s_add_u32 s39, s39, s28
s_sub_u32 s39, s39, 0x40000
s_lshl_b32 s37, s33, 12
s_add_u32 s37, s37, 0x10000
v_lshrrev_b32 v28, 4, v0
v_and_b32 v29, 7, v0
v_xor_b32 v28, v28, v29
v_lshlrev_b32 v28, 4, v28
v_lshrrev_b32 v29, 3, v0
v_lshl_or_b32 v8, v29, 11, v28
v_xor_b32 v9, 64, v8
v_add_u32 v9, 15360, v9
v_add_u32 v10, 30720, v8
v_add_u32 v11, 30720, v9
s_and_b32 s28, s33, 1
s_lshl_b32 s28, s28, 7
v_lshrrev_b32 v29, 5, v0
v_mul_u32_u24 v29, 0x2220, v29
v_xor_b32 v29, v29, v1
v_xor_b32 v12, s28, v29
v_xor_b32 v13, 64, v12
v_add_u32 v13, 15360, v13
v_add_u32 v14, 30720, v12
v_add_u32 v15, 30720, v13
v_bfe_u32 v28, v0, 1, 3
v_lshrrev_b32 v29, 4, v0
v_xor_b32 v28, v28, v29
v_lshlrev_b32 v28, 4, v28
v_and_b32 v29, 15, v0
v_lshl_or_b32 v28, v29, 7, v28
s_lshr_b32 s28, s33, 1
s_lshl_b32 s28, s28, 12
v_or_b32 v16, s28, v28
v_xor_b32 v17, 64, v16
v_bfe_u32 v28, v0, 2, 3
v_lshlrev_b32 v28, 5, v28
v_and_b32 v29, 3, v0
v_lshl_or_b32 v28, v29, 3, v28
v_bfe_u32 v29, v0, 2, 2
v_lshl_or_b32 v28, v29, 9, v28
v_lshrrev_b32 v29, 4, v0
v_lshl_or_b32 v28, v29, 12, v28
s_and_b32 s28, s33, 1
s_lshl_b32 s28, s28, 8
s_bitset1_b32 s28, 16
v_or_b32 v18, s28, v28
v_xor_b32 v19, 32, v18
v_xor_b32 v20, 64, v18
v_xor_b32 v21, 96, v18
v_xor_b32 v22, 128, v18
v_xor_b32 v23, 160, v18
v_xor_b32 v24, 192, v18
v_xor_b32 v25, 224, v18
s_lshl_b32 s28, s34, 21
s_lshl_b32 s29, s91, 25
s_add_u32 s28, s28, s29
s_bfe_u32 s29, s63, 0x80000
s_lshl_b32 s29, s29, 16
s_add_u32 s45, s28, s29
)" R"(
s_bfe_u32 s29, s63, 0x80008
s_lshl_b32 s29, s29, 16
s_add_u32 s46, s28, s29
s_bfe_u32 s29, s63, 0x80010
s_lshl_b32 s29, s29, 16
s_add_u32 s47, s28, s29
s_bfe_u32 s29, s63, 0x80018
s_lshl_b32 s29, s29, 16
s_add_u32 s48, s28, s29
s_lshl_b32 s50, s34, 11
s_add_u32 s50, s50, 0xc110000
s_lshl2_add_u32 s28, s91, s93
s_lshl_b32 s28, s28, 7
s_add_u32 s49, s28, s50
s_add_u32 s49, s49, 1024
s_lshl3_add_u32 s28, s34, s35
s_lshl_b32 s52, s28, 7
s_add_u32 s52, s52, 0xc118000
s_lshl4_add_u32 s28, s34, s35
s_lshl_b32 s51, s28, 17
s_add_u32 s51, s51, 0x4000000
s_cmp_lg_u32 s33, 0
s_cmov_b32 s49, 0x40000000
s_cmov_b32 s52, 0x40000000
s_mov_b32 s53, 0x40000
s_mov_b32 s54, 0x3db8aa3b
s_lshl_b32 s89, s34, 21
s_mov_b64 s[60:61], 0xff
s_mov_b32 s62, 0x4e800000
s_add_u32 s67, s89, 0x6000000
s_lshl3_add_u32 s28, s34, s35
s_add_u32 s64, s51, 0x100000
s_lshl2_add_u32 s28, s28, s33
s_lshl_b32 s65, s28, 7
s_add_u32 s65, s65, 0xc120000
s_lshl_b32 s66, s28, 10
s_add_u32 s66, s66, 0xc000000
s_sub_u32 s28, 15, s35
s_lshl2_add_u32 s28, s28, s33
s_lshl_b32 s29, s34, 6
s_add_u32 s29, s29, s28
s_lshl_b32 s73, s29, 16
s_add_u32 s73, s73, 0x8000000
s_lshl4_add_u32 s28, s28, s34
s_lshl_b32 s68, s28, 7
s_add_u32 s68, s68, 0xc130000
s_mov_b32 s55, s89
s_mov_b32 s57, 18
s_mov_b32 s59, s67
s_mov_b32 s58, 0
s_cmp_eq_u32 s35, 7
s_cmov_b32 s58, 2
s_cmp_eq_u32 s35, 7
s_cmov_b32 s64, 0x40000000
s_cmp_eq_u32 s35, 7
s_cmov_b32 s66, 0x40000000
s_add_u32 s56, s55, 0x2000000
s_waitcnt vmcnt(21) lgkmcnt(0)
s_barrier
ds_read_b128 v[128:131], v4 offset:0
ds_read_b128 v[132:135], v4 offset:2048
ds_read_b128 v[136:139], v5 offset:0
ds_read_b128 v[140:143], v5 offset:2048
ds_read_b128 v[144:147], v4 offset:4096
ds_read_b128 v[148:151], v4 offset:6144
)" R"(
ds_read_b128 v[152:155], v5 offset:4096
ds_read_b128 v[156:159], v5 offset:6144
Rt 128,248,0
Rt 130,248,512
Rt 132,249,0
Rt 134,249,512
Rt 136,250,0
Rt 138,250,512
Rt 140,251,0
Rt 142,251,512
Rt 144,248,4096
Rt 146,248,4608
Rt 148,249,4096
Rt 150,249,4608
Rt 152,250,4096
Rt 154,250,4608
Rt 156,251,4096
Rt 158,251,4608
s_mul_i32 s3, s3, 1664525
s_add_u32 s3, s3, 1013904223
v_mov_b32 v6, s3
v_min_u32 v7, 1, v0
v_lshlrev_b32 v7, 30, v7
s_waitcnt lgkmcnt(8)
s_barrier
v_mfma_f32_16x16x32_bf16 v[32:35], a[128:131], v[128:131], 0
Rt 160,248,8192
s_add_u32 m0, s41, 131072
s_add_u32 s92, s42, 0x1c0000
v_mfma_f32_16x16x32_bf16 v[40:43], a[128:131], v[132:135], 0
Rt 162,248,8704
buffer_load_dwordx4 v240, s[16:19], s92 offen offset:0 lds
v_mfma_f32_16x16x32_bf16 v[44:47], a[132:135], v[132:135], 0
Rt 164,249,8192
v_mfma_f32_16x16x32_bf16 v[36:39], a[132:135], v[128:131], 0
Rt 166,249,8704
v_mfma_f32_16x16x32_bf16 v[48:51], a[136:139], v[128:131], 0
Rt 168,250,8192
v_mfma_f32_16x16x32_bf16 v[56:59], a[136:139], v[132:135], 0
Rt 170,250,8704
buffer_load_dwordx4 v241, s[16:19], s92 offen offset:1024 lds
v_mfma_f32_16x16x32_bf16 v[60:63], a[140:143], v[132:135], 0
Rt 172,251,8192
v_mfma_f32_16x16x32_bf16 v[52:55], a[140:143], v[128:131], 0
Rt 174,251,8704
s_waitcnt vmcnt(19) lgkmcnt(8)
Mp 32,144,136
Rt 128,248,12288
ds_read_b128 v[160:163], v4 offset:32768
Mp 40,144,140
Rt 130,248,12800
buffer_load_dwordx4 v242, s[16:19], s92 offen offset:2048 lds
Mp 44,148,140
Rt 132,249,12288
ds_read_b128 v[164:167], v4 offset:34816
Mp 36,148,136
Rt 134,249,12800
Mp 48,152,136
Rt 136,250,12288
ds_read_b128 v[168:171], v5 offset:32768
Mp 56,152,140
Rt 138,250,12800
buffer_load_dwordx4 v243, s[16:19], s92 offen offset:3072 lds
Mp 60,156,140
Rt 140,251,12288
)" R"(
ds_read_b128 v[172:175], v5 offset:34816
Mp 52,156,136
Rt 142,251,12800
s_waitcnt vmcnt(17) lgkmcnt(12)
Mp 32,160,144
Rt 144,248,16384
ds_read_b128 v[176:179], v4 offset:36864
s_add_u32 m0, s41, 135168
s_add_u32 s92, s42, 0x200000
Mp 40,160,148
Rt 146,248,16896
buffer_load_dwordx4 v240, s[16:19], s92 offen offset:0 lds
Mp 44,164,148
Rt 148,249,16384
ds_read_b128 v[180:183], v4 offset:38912
Mp 36,164,144
Rt 150,249,16896
Mp 48,168,144
Rt 152,250,16384
ds_read_b128 v[184:187], v5 offset:36864
Mp 56,168,148
Rt 154,250,16896
buffer_load_dwordx4 v241, s[16:19], s92 offen offset:1024 lds
Mp 60,172,148
Rt 156,251,16384
ds_read_b128 v[188:191], v5 offset:38912
Mp 52,172,144
Rt 158,251,16896
s_waitcnt lgkmcnt(12)
Mp 32,128,152
Rt 160,248,20480
Mp 40,128,156
Rt 162,248,20992
buffer_load_dwordx4 v242, s[16:19], s92 offen offset:2048 lds
Mp 44,132,156
Rt 164,249,20480
Mp 36,132,152
Rt 166,249,20992
Mp 48,136,152
Rt 168,250,20480
Mp 56,136,156
Rt 170,250,20992
Mp 60,140,156
Rt 172,251,20480
s_add_u32 s44, s43, 512
Mp 52,140,152
Rt 174,251,20992
s_waitcnt lgkmcnt(8)
Mp 32,144,160
Rt 128,248,24576
s_add_u32 m0, s36, 32768
Mp 40,144,164
Rt 130,248,25088
buffer_load_dwordx4 v2, s[12:15], s44 offen offset:0 lds
Mp 44,148,164
Rt 132,249,24576
Mp 36,148,160
Rt 134,249,25088
Mp 48,152,160
Rt 136,250,24576
Mp 56,152,164
Rt 138,250,25088
buffer_load_dwordx4 v3, s[12:15], s44 offen offset:1024 lds
Mp 60,156,164
)" R"(
Rt 140,251,24576
Mp 52,156,160
Rt 142,251,25088
s_waitcnt vmcnt(15) lgkmcnt(8)
s_barrier
Mp 32,160,168
Rt 144,248,32768
Mp 40,160,172
Rt 146,248,33280
s_add_u32 s44, s43, 12800
buffer_load_dwordx4 v2, s[12:15], s44 offen offset:2048 lds
Mp 44,164,172
Rt 148,249,32768
Mp 36,164,168
Rt 150,249,33280
Mp 48,168,168
Rt 152,250,32768
Mp 56,168,172
Rt 154,250,33280
buffer_load_dwordx4 v3, s[12:15], s44 offen offset:3072 lds
Mp 60,172,172
Rt 156,251,32768
s_add_u32 s44, s43, 640
Mp 52,172,168
Rt 158,251,33280
s_waitcnt vmcnt(11) lgkmcnt(8)
s_barrier
Mp 32,128,176
Rt 160,248,36864
s_add_u32 m0, s36, 36864
Mp 40,128,180
Rt 162,248,37376
buffer_load_dwordx4 v2, s[12:15], s44 offen offset:0 lds
Mp 44,132,180
Rt 164,249,36864
v_dot2_f32_bf16 v120, v96, v112, 0
Mp 36,132,176
Rt 166,249,37376
v_dot2_f32_bf16 v121, v100, v112, 0
v_dot2_f32_bf16 v122, v104, v112, 0
Mp 48,136,176
Rt 168,250,36864
v_dot2_f32_bf16 v123, v108, v112, 0
Mp 56,136,180
Rt 170,250,37376
buffer_load_dwordx4 v3, s[12:15], s44 offen offset:1024 lds
Mp 60,140,180
Rt 172,251,36864
v_dot2_f32_bf16 v124, v96, v116, 0
v_dot2_f32_bf16 v125, v100, v116, 0
Mp 52,140,176
Rt 174,251,37376
s_waitcnt lgkmcnt(8)
v_mfma_f32_16x16x32_bf16 v[64:67], a[144:147], v[128:131], 0
Rt 128,248,40960
v_dot2_f32_bf16 v126, v104, v116, 0
v_mfma_f32_16x16x32_bf16 v[72:75], a[144:147], v[132:135], 0
Rt 130,248,41472
s_add_u32 s44, s43, 12928
buffer_load_dwordx4 v2, s[12:15], s44 offen offset:2048 lds
v_mfma_f32_16x16x32_bf16 v[76:79], a[148:151], v[132:135], 0
Rt 132,249,40960
v_dot2_f32_bf16 v127, v108, v116, 0
v_mfma_f32_16x16x32_bf16 v[68:71], a[148:151], v[128:131], 0
Rt 134,249,41472
v_dot2_f32_bf16 v120, v97, v113, v120
)" R"(
v_mfma_f32_16x16x32_bf16 v[80:83], a[152:155], v[128:131], 0
Rt 136,250,40960
v_dot2_f32_bf16 v121, v101, v113, v121
v_dot2_f32_bf16 v122, v105, v113, v122
v_mfma_f32_16x16x32_bf16 v[88:91], a[152:155], v[132:135], 0
Rt 138,250,41472
buffer_load_dwordx4 v3, s[12:15], s44 offen offset:3072 lds
v_mfma_f32_16x16x32_bf16 v[92:95], a[156:159], v[132:135], 0
Rt 140,251,40960
s_add_u32 s92, s42, 0x1c0080
v_mfma_f32_16x16x32_bf16 v[84:87], a[156:159], v[128:131], 0
Rt 142,251,41472
v_dot2_f32_bf16 v123, v109, v113, v123
s_waitcnt lgkmcnt(8)
Mp 64,160,136
Rt 144,248,45056
s_add_u32 m0, s41, 0
Mp 72,160,140
Rt 146,248,45568
buffer_load_dwordx4 v240, s[16:19], s92 offen offset:0 lds
Mp 76,164,140
Rt 148,249,45056
v_dot2_f32_bf16 v124, v97, v117, v124
Mp 68,164,136
Rt 150,249,45568
v_dot2_f32_bf16 v125, v101, v117, v125
v_dot2_f32_bf16 v126, v105, v117, v126
Mp 80,168,136
Rt 152,250,45056
v_dot2_f32_bf16 v127, v109, v117, v127
Mp 88,168,140
Rt 154,250,45568
buffer_load_dwordx4 v241, s[16:19], s92 offen offset:1024 lds
Mp 92,172,140
Rt 156,251,45056
v_dot2_f32_bf16 v120, v98, v114, v120
v_dot2_f32_bf16 v121, v102, v114, v121
Mp 84,172,136
Rt 158,251,45568
s_waitcnt lgkmcnt(8)
Mp 64,128,144
Rt 160,248,49152
v_dot2_f32_bf16 v122, v106, v114, v122
Mp 72,128,148
Rt 162,248,49664
buffer_load_dwordx4 v242, s[16:19], s92 offen offset:2048 lds
Mp 76,132,148
Rt 164,249,49152
v_dot2_f32_bf16 v123, v110, v114, v123
Mp 68,132,144
Rt 166,249,49664
v_dot2_f32_bf16 v124, v98, v118, v124
Mp 80,136,144
Rt 168,250,49152
v_dot2_f32_bf16 v125, v102, v118, v125
v_dot2_f32_bf16 v126, v106, v118, v126
Mp 88,136,148
Rt 170,250,49664
buffer_load_dwordx4 v243, s[16:19], s92 offen offset:3072 lds
Mp 92,140,148
Rt 172,251,49152
s_add_u32 s92, s42, 0x200080
Mp 84,140,144
Rt 174,251,49664
)" R"(
v_dot2_f32_bf16 v127, v110, v118, v127
s_waitcnt lgkmcnt(8)
Mp 64,144,152
Rt 128,248,53248
s_add_u32 m0, s41, 4096
Mp 72,144,156
Rt 130,248,53760
buffer_load_dwordx4 v240, s[16:19], s92 offen offset:0 lds
Mp 76,148,156
Rt 132,249,53248
v_dot2_f32_bf16 v120, v99, v115, v120
Mp 68,148,152
Rt 134,249,53760
v_dot2_f32_bf16 v121, v103, v115, v121
v_dot2_f32_bf16 v122, v107, v115, v122
Mp 80,152,152
Rt 136,250,53248
v_dot2_f32_bf16 v123, v111, v115, v123
Mp 88,152,156
Rt 138,250,53760
buffer_load_dwordx4 v241, s[16:19], s92 offen offset:1024 lds
Mp 92,156,156
Rt 140,251,53248
v_dot2_f32_bf16 v124, v99, v119, v124
v_dot2_f32_bf16 v125, v103, v119, v125
Mp 84,156,152
Rt 142,251,53760
s_waitcnt vmcnt(10) lgkmcnt(8)
Mp 64,160,160
Rt 144,248,57344
ds_read_b128 v[192:195], v4 offset:32768
Mp 72,160,164
Rt 146,248,57856
buffer_load_dwordx4 v242, s[16:19], s92 offen offset:2048 lds
Mp 76,164,164
Rt 148,249,57344
ds_read_b128 v[196:199], v4 offset:34816
Mp 68,164,160
Rt 150,249,57856
v_dot2_f32_bf16 v126, v107, v119, v126
Mp 80,168,160
Rt 152,250,57344
ds_read_b128 v[200:203], v5 offset:32768
v_dot2_f32_bf16 v127, v111, v119, v127
Mp 88,168,164
Rt 154,250,57856
v_permlane32_swap_b32_e32 v120, v121
Mp 92,172,164
Rt 156,251,57344
ds_read_b128 v[204:207], v5 offset:34816
Mp 84,172,160
Rt 158,251,57856
s_waitcnt vmcnt(7) lgkmcnt(12)
s_barrier
Mp 64,128,168
Rt 160,252,0
ds_read_b128 v[208:211], v4 offset:36864
Mp 72,128,172
Rt 162,252,512
v_add_f32_e32 v120, v120, v121
Mp 76,132,172
Rt 164,253,0
ds_read_b128 v[212:215], v4 offset:38912
Mp 68,132,168
Rt 166,253,512
)" R"(
v_permlane32_swap_b32_e32 v122, v123
v_add_f32_e32 v122, v122, v123
Mp 80,136,168
Rt 168,254,0
ds_read_b128 v[216:219], v5 offset:36864
Mp 88,136,172
Rt 170,254,512
s_add_u32 s44, s43, 768
Mp 92,140,172
Rt 172,255,0
ds_read_b128 v[220:223], v5 offset:38912
v_permlane32_swap_b32_e32 v124, v125
Mp 84,140,168
Rt 174,255,512
s_waitcnt lgkmcnt(12)
s_barrier
Mp 64,144,176
Rt 128,252,4096
s_add_u32 m0, s36, 32768
Mp 72,144,180
Rt 130,252,4608
buffer_load_dwordx4 v2, s[12:15], s44 offen offset:0 lds
Mp 76,148,180
Rt 132,253,4096
v_add_f32_e32 v124, v124, v125
Mp 68,148,176
Rt 134,253,4608
v_permlane32_swap_b32_e32 v126, v127
Mp 80,152,176
Rt 136,254,4096
buffer_load_dwordx4 v3, s[12:15], s44 offen offset:1024 lds
Mp 88,152,180
Rt 138,254,4608
v_add_f32_e32 v126, v126, v127
Mp 92,156,180
Rt 140,255,4096
v_permlane16_swap_b32_e32 v120, v122
Mp 84,156,176
Rt 142,255,4608
s_add_u32 s44, s43, 13056
buffer_load_dwordx4 v2, s[12:15], s44 offen offset:2048 lds
s_waitcnt lgkmcnt(8)
Mp 32,160,184
Rt 144,252,8192
v_add_f32_e32 v120, v120, v122
Mp 40,160,188
Rt 146,252,8704
v_permlane16_swap_b32_e32 v124, v126
Mp 44,164,188
Rt 148,253,8192
buffer_load_dwordx4 v3, s[12:15], s44 offen offset:3072 lds
Mp 36,164,184
Rt 150,253,8704
v_add_f32_e32 v124, v124, v126
v_add_f32_dpp v120, v120, v120 row_ror:8 row_mask:0xf bank_mask:0xf
Mp 48,168,184
Rt 152,254,8192
v_add_f32_dpp v124, v124, v124 row_ror:8 row_mask:0xf bank_mask:0xf
Mp 56,168,188
Rt 154,254,8704
s_add_u32 s92, s42, 0x100
Mp 60,172,188
Rt 156,255,8192
s_add_u32 m0, s41, 65536
v_add_f32_dpp v120, v120, v120 row_ror:4 row_mask:0xf bank_mask:0xf
)" R"(
Mp 52,172,184
Rt 158,255,8704
buffer_load_dwordx4 v240, s[16:19], s92 offen offset:0 lds
s_waitcnt lgkmcnt(8)
Mp 32,128,192
Rt 160,252,12288
v_add_f32_dpp v124, v124, v124 row_ror:4 row_mask:0xf bank_mask:0xf
Mp 40,128,196
Rt 162,252,12800
v_add_f32_dpp v120, v120, v120 row_ror:2 row_mask:0xf bank_mask:0xf
v_add_f32_dpp v124, v124, v124 row_ror:2 row_mask:0xf bank_mask:0xf
Mp 44,132,196
Rt 164,253,12288
v_add_f32_dpp v120, v120, v120 row_ror:1 row_mask:0xf bank_mask:0xf
Mp 36,132,192
Rt 166,253,12800
buffer_load_dwordx4 v241, s[16:19], s92 offen offset:1024 lds
Mp 48,136,192
Rt 168,254,12288
v_add_f32_dpp v124, v124, v124 row_ror:1 row_mask:0xf bank_mask:0xf
Cv 120,120,120
Mp 56,136,196
Rt 170,254,12800
v_lshlrev_b32 v120, 16, v120
Mp 60,140,196
Rt 172,255,12288
Cv 124,124,124
Mp 52,140,192
Rt 174,255,12800
buffer_load_dwordx4 v242, s[16:19], s92 offen offset:2048 lds
s_waitcnt lgkmcnt(8)
Mp 32,144,200
Rt 128,252,16384
v_lshlrev_b32 v124, 16, v124
Mp 40,144,204
Rt 130,252,16896
v_mul_f32_e32 v120, 0xbfb8aa3b, v120
Mp 44,148,204
Rt 132,253,16384
v_lshrrev_b32 v121, 5, v0
Mp 36,148,200
Rt 134,253,16896
buffer_load_dwordx4 v243, s[16:19], s92 offen offset:3072 lds
Mp 48,152,200
Rt 136,254,16384
v_bfe_u32 v122, v0, 4, 1
Mp 56,152,204
Rt 138,254,16896
s_add_u32 s92, s42, 0x40100
Mp 60,156,204
Rt 140,255,16384
s_add_u32 m0, s41, 69632
v_lshl_or_b32 v121, v122, 1, v121
Mp 52,156,200
Rt 142,255,16896
buffer_load_dwordx4 v240, s[16:19], s92 offen offset:0 lds
s_waitcnt vmcnt(5) lgkmcnt(8)
Mp 32,160,208
Rt 144,252,20480
ds_read_b128 v[224:227], v4 offset:32768
Mp 40,160,212
Rt 146,252,20992
v_exp_f32_e32 v120, v120
Mp 44,164,212
)" R"(
Rt 148,253,20480
ds_read_b128 v[228:231], v4 offset:34816
Mp 36,164,208
Rt 150,253,20992
buffer_load_dwordx4 v241, s[16:19], s92 offen offset:1024 lds
Mp 48,168,208
Rt 152,254,20480
ds_read_b128 v[232:235], v5 offset:32768
v_add_f32_e32 v120, 1.0, v120
Mp 56,168,212
Rt 154,254,20992
v_add_u32 v121, s94, v121
Mp 60,172,212
Rt 156,255,20480
ds_read_b128 v[236:239], v5 offset:34816
Mp 52,172,208
Rt 158,255,20992
buffer_load_dwordx4 v242, s[16:19], s92 offen offset:2048 lds
s_waitcnt lgkmcnt(12)
Mp 32,128,216
Rt 160,252,24576
v_add_f32_e32 v125, 0, v124
Mp 40,128,220
Rt 162,252,25088
s_add_u32 s92, s42, 0x180
Mp 44,132,220
Rt 164,253,24576
s_add_u32 m0, s41, 98304
Mp 36,132,216
Rt 166,253,25088
buffer_load_dwordx4 v240, s[16:19], s92 offen offset:0 lds
Mp 48,136,216
Rt 168,254,24576
v_ashrrev_i32 v126, 31, v125
Mp 56,136,220
Rt 170,254,25088
v_or_b32 v126, 0x80000000, v126
Mp 60,140,220
Rt 172,255,24576
v_rcp_f32_e32 v120, v120
Mp 52,140,216
Rt 174,255,25088
buffer_load_dwordx4 v241, s[16:19], s92 offen offset:1024 lds
s_waitcnt lgkmcnt(8)
s_barrier
Mp 32,144,224
Rt 128,244,0
Cv 120,120,120
Mp 40,144,228
Rt 130,244,512
v_lshlrev_b32 v120, 16, v120
v_xor_b32 v125, v125, v126
Mp 44,148,228
Rt 132,245,0
v_and_b32 v125, 0xffff0000, v125
Mp 36,148,224
Rt 134,245,512
buffer_load_dwordx4 v242, s[16:19], s92 offen offset:2048 lds
Mp 48,152,224
Rt 136,246,0
v_sub_u32 v126, 0xffff, v121
v_or_b32 v116, v125, v126
Mp 56,152,228
Rt 138,246,512
v_mov_b32 v117, s3
)" R"(
Mp 60,156,228
Rt 140,247,0
v_mov_b32 v118, v120
Mp 52,156,224
Rt 142,247,512
buffer_load_dwordx4 v243, s[16:19], s92 offen offset:3072 lds
s_waitcnt lgkmcnt(8)
s_barrier
Mp 32,160,232
Rt 144,244,4096
v_mov_b32 v119, v124
Mp 40,160,236
Rt 146,244,4608
s_add_u32 s92, s42, 0x40180
Mp 44,164,236
Rt 148,245,4096
s_add_u32 m0, s41, 102400
Mp 36,164,232
Rt 150,245,4608
buffer_load_dwordx4 v240, s[16:19], s92 offen offset:0 lds
Mp 48,168,232
Rt 152,246,4096
v_and_b32 v122, 15, v0
Mp 56,168,236
Rt 154,246,4608
v_min_u32 v122, 1, v122
Mp 60,172,236
Rt 156,247,4096
v_lshlrev_b32 v122, 30, v122
v_lshl_or_b32 v122, v121, 4, v122
Mp 52,172,232
Rt 158,247,4608
buffer_load_dwordx4 v241, s[16:19], s92 offen offset:1024 lds
s_waitcnt lgkmcnt(8)
Mp 64,128,184
Rt 160,244,8192
s_mov_b32 s94, 0xc100000
Mp 72,128,188
Rt 162,244,8704
buffer_store_dwordx4 v[116:119], v122, s[20:23], s94 offen sc0 sc1
Mp 76,132,188
Rt 164,245,8192
Mp 68,132,184
Rt 166,245,8704
buffer_load_dwordx4 v242, s[16:19], s92 offen offset:2048 lds
Mp 80,136,184
Rt 168,246,8192
v_mul_f32_e32 v32, s54, v32
Mp 88,136,188
Rt 170,246,8704
s_add_u32 s92, s42, 0x1c0100
Mp 92,140,188
Rt 172,247,8192
s_add_u32 m0, s41, 32768
Mp 84,140,184
Rt 174,247,8704
buffer_load_dwordx4 v240, s[16:19], s92 offen offset:0 lds
s_waitcnt lgkmcnt(8)
Mp 64,144,192
Rt 128,244,12288
v_mul_f32_e32 v33, s54, v33
Mp 72,144,196
Rt 130,244,12800
Cv 32,32,33
Mp 76,148,196
)" R"(
Rt 132,245,12288
v_accvgpr_write_b32 a64, v32
v_mul_f32_e32 v34, s54, v34
Mp 68,148,192
Rt 134,245,12800
buffer_load_dwordx4 v241, s[16:19], s92 offen offset:1024 lds
Mp 80,152,192
Rt 136,246,12288
v_mul_f32_e32 v35, s54, v35
Mp 88,152,196
Rt 138,246,12800
Cv 34,34,35
v_accvgpr_write_b32 a65, v34
Mp 92,156,196
Rt 140,247,12288
v_mul_f32_e32 v36, s54, v36
Mp 84,156,192
Rt 142,247,12800
buffer_load_dwordx4 v242, s[16:19], s92 offen offset:2048 lds
s_waitcnt lgkmcnt(8)
Mp 64,160,200
Rt 144,244,16384
v_mul_f32_e32 v37, s54, v37
Cv 36,36,37
Mp 72,160,204
Rt 146,244,16896
v_accvgpr_write_b32 a66, v36
Mp 76,164,204
Rt 148,245,16384
v_mul_f32_e32 v38, s54, v38
Mp 68,164,200
Rt 150,245,16896
buffer_load_dwordx4 v243, s[16:19], s92 offen offset:3072 lds
Mp 80,168,200
Rt 152,246,16384
v_mul_f32_e32 v39, s54, v39
Mp 88,168,204
Rt 154,246,16896
s_add_u32 s92, s42, 0x200100
Mp 92,172,204
Rt 156,247,16384
s_add_u32 m0, s41, 36864
Mp 84,172,200
Rt 158,247,16896
buffer_load_dwordx4 v240, s[16:19], s92 offen offset:0 lds
s_waitcnt lgkmcnt(8)
Mp 64,128,208
Rt 160,244,20480
Cv 38,38,39
Mp 72,128,212
Rt 162,244,20992
v_accvgpr_write_b32 a67, v38
Mp 76,132,212
Rt 164,245,20480
v_mul_f32_e32 v40, s54, v40
v_mul_f32_e32 v41, s54, v41
Mp 68,132,208
Rt 166,245,20992
buffer_load_dwordx4 v241, s[16:19], s92 offen offset:1024 lds
Mp 80,136,208
Rt 168,246,20480
Cv 40,40,41
Mp 88,136,212
Rt 170,246,20992
)" R"(
v_accvgpr_write_b32 a68, v40
v_mul_f32_e32 v42, s54, v42
Mp 92,140,212
Rt 172,247,20480
v_mul_f32_e32 v43, s54, v43
Mp 84,140,208
Rt 174,247,20992
buffer_load_dwordx4 v242, s[16:19], s92 offen offset:2048 lds
s_waitcnt lgkmcnt(8)
Mp 64,144,216
Rt 128,244,24576
Cv 42,42,43
v_accvgpr_write_b32 a69, v42
Mp 72,144,220
Rt 130,244,25088
v_mul_f32_e32 v44, s54, v44
Mp 76,148,220
Rt 132,245,24576
v_mul_f32_e32 v45, s54, v45
Mp 68,148,216
Rt 134,245,25088
s_add_u32 s92, s42, 0x1c0180
Cv 44,44,45
Mp 80,152,216
Rt 136,246,24576
s_add_u32 m0, s41, 131072
Mp 88,152,220
Rt 138,246,25088
buffer_load_dwordx4 v240, s[16:19], s92 offen offset:0 lds
Mp 92,156,220
Rt 140,247,24576
v_accvgpr_write_b32 a70, v44
Mp 84,156,216
Rt 142,247,25088
s_waitcnt vmcnt(16) lgkmcnt(8)
s_barrier
Mp 64,160,224
Rt 144,248,0
v_mul_f32_e32 v46, s54, v46
Mp 72,160,228
Rt 146,248,512
buffer_load_dwordx4 v241, s[16:19], s92 offen offset:1024 lds
Mp 76,164,228
Rt 148,249,0
v_mul_f32_e32 v47, s54, v47
Cv 46,46,47
Mp 68,164,224
Rt 150,249,512
v_accvgpr_write_b32 a71, v46
Mp 80,168,224
Rt 152,250,0
v_mul_f32_e32 v48, s54, v48
Mp 88,168,228
Rt 154,250,512
buffer_load_dwordx4 v242, s[16:19], s92 offen offset:2048 lds
Mp 92,172,228
Rt 156,251,0
v_mul_f32_e32 v49, s54, v49
Mp 84,172,224
Rt 158,251,512
s_waitcnt vmcnt(3) lgkmcnt(8)
s_barrier
Mp 64,128,232
Rt 160,248,4096
Cv 48,48,49
v_accvgpr_write_b32 a72, v48
)" R"(
Mp 72,128,236
Rt 162,248,4608
buffer_load_dwordx4 v243, s[16:19], s92 offen offset:3072 lds
Mp 76,132,236
Rt 164,249,4096
v_mul_f32_e32 v50, s54, v50
Mp 68,132,232
Rt 166,249,4608
s_add_u32 s92, s42, 0x200180
v_mul_f32_e32 v51, s54, v51
Mp 80,136,232
Rt 168,250,4096
s_add_u32 m0, s41, 135168
Mp 88,136,236
Rt 170,250,4608
buffer_load_dwordx4 v240, s[16:19], s92 offen offset:0 lds
Mp 92,140,236
Rt 172,251,4096
Cv 50,50,51
Mp 84,140,232
Rt 174,251,4608
v_accvgpr_write_b32 a73, v50
s_waitcnt lgkmcnt(8)
v_mfma_f32_16x16x32_bf16 v[96:99], a[144:147], v[128:131], 0
Rt 128,248,8192
v_mul_f32_e32 v52, s54, v52
v_mfma_f32_16x16x32_bf16 v[104:107], a[144:147], v[132:135], 0
Rt 130,248,8704
buffer_load_dwordx4 v241, s[16:19], s92 offen offset:1024 lds
v_mfma_f32_16x16x32_bf16 v[108:111], a[148:151], v[132:135], 0
Rt 132,249,8192
v_mul_f32_e32 v53, s54, v53
Cv 52,52,53
v_mfma_f32_16x16x32_bf16 v[100:103], a[148:151], v[128:131], 0
Rt 134,249,8704
v_accvgpr_write_b32 a74, v52
v_mfma_f32_16x16x32_bf16 v[112:115], a[152:155], v[128:131], 0
Rt 136,250,8192
v_mul_f32_e32 v54, s54, v54
v_mfma_f32_16x16x32_bf16 v[120:123], a[152:155], v[132:135], 0
Rt 138,250,8704
buffer_load_dwordx4 v242, s[16:19], s92 offen offset:2048 lds
v_mfma_f32_16x16x32_bf16 v[124:127], a[156:159], v[132:135], 0
Rt 140,251,8192
v_mul_f32_e32 v55, s54, v55
v_mfma_f32_16x16x32_bf16 v[116:119], a[156:159], v[128:131], 0
Rt 142,251,8704
s_waitcnt lgkmcnt(8)
Mp 96,160,136
Rt 144,248,12288
Cv 54,54,55
v_accvgpr_write_b32 a75, v54
Mp 104,160,140
Rt 146,248,12800
s_add_u32 s38, s38, 0xffff0080
Mp 108,164,140
Rt 148,249,12288
s_add_u32 m0, s36, 0
Mp 100,164,136
Rt 150,249,12800
buffer_load_dwordx4 v8, s[4:7], s38 offen offset:0 lds
Mp 112,168,136
Rt 152,250,12288
v_mul_f32_e32 v56, s54, v56
)" R"(
Mp 120,168,140
Rt 154,250,12800
v_mul_f32_e32 v57, s54, v57
Mp 124,172,140
Rt 156,251,12288
Cv 56,56,57
Mp 116,172,136
Rt 158,251,12800
buffer_load_dwordx4 v9, s[4:7], s38 offen offset:1024 lds
s_waitcnt lgkmcnt(8)
Mp 96,128,144
Rt 160,248,16384
v_accvgpr_write_b32 a76, v56
Mp 104,128,148
Rt 162,248,16896
v_mul_f32_e32 v58, s54, v58
Mp 108,132,148
Rt 164,249,16384
v_mul_f32_e32 v59, s54, v59
Cv 58,58,59
Mp 100,132,144
Rt 166,249,16896
buffer_load_dwordx4 v10, s[4:7], s38 offen offset:2048 lds
Mp 112,136,144
Rt 168,250,16384
v_accvgpr_write_b32 a77, v58
Mp 120,136,148
Rt 170,250,16896
v_mul_f32_e32 v60, s54, v60
v_mul_f32_e32 v61, s54, v61
Mp 124,140,148
Rt 172,251,16384
Cv 60,60,61
Mp 116,140,144
Rt 174,251,16896
buffer_load_dwordx4 v11, s[4:7], s38 offen offset:3072 lds
s_waitcnt lgkmcnt(8)
Mp 96,144,152
Rt 128,248,20480
v_accvgpr_write_b32 a78, v60
v_mul_f32_e32 v62, s54, v62
Mp 104,144,156
Rt 130,248,20992
s_add_u32 s38, s38, 0x10000
Mp 108,148,156
Rt 132,249,20480
s_add_u32 m0, s36, 4096
Mp 100,148,152
Rt 134,249,20992
buffer_load_dwordx4 v8, s[4:7], s38 offen offset:0 lds
Mp 112,152,152
Rt 136,250,20480
v_mul_f32_e32 v63, s54, v63
Mp 120,152,156
Rt 138,250,20992
Cv 62,62,63
Mp 124,156,156
Rt 140,251,20480
v_accvgpr_write_b32 a79, v62
Mp 116,156,152
Rt 142,251,20992
buffer_load_dwordx4 v9, s[4:7], s38 offen offset:1024 lds
s_waitcnt lgkmcnt(8)
Mp 96,160,160
)" R"(
Rt 144,248,24576
v_mul_f32_e32 v64, s54, v64
Mp 104,160,164
Rt 146,248,25088
v_mul_f32_e32 v65, s54, v65
Mp 108,164,164
Rt 148,249,24576
Cv 64,64,65
v_accvgpr_write_b32 a80, v64
Mp 100,164,160
Rt 150,249,25088
buffer_load_dwordx4 v10, s[4:7], s38 offen offset:2048 lds
Mp 112,168,160
Rt 152,250,24576
v_mul_f32_e32 v66, s54, v66
Mp 120,168,164
Rt 154,250,25088
v_mul_f32_e32 v67, s54, v67
Cv 66,66,67
Mp 124,172,164
Rt 156,251,24576
v_accvgpr_write_b32 a81, v66
Mp 116,172,160
Rt 158,251,25088
buffer_load_dwordx4 v11, s[4:7], s38 offen offset:3072 lds
s_waitcnt lgkmcnt(8)
Mp 96,128,168
Rt 160,244,32768
v_mul_f32_e32 v68, s54, v68
v_mul_f32_e32 v69, s54, v69
Mp 104,128,172
Rt 162,244,33280
Cv 68,68,69
s_mov_b32 s85, 0xc108800
Mp 108,132,172
Rt 164,245,32768
v_accvgpr_write_b32 a82, v68
v_and_b32 v2, 7, v0
Mp 100,132,168
Rt 166,245,33280
v_mul_f32_e32 v70, s54, v70
v_mul_f32_e32 v71, s54, v71
v_lshlrev_b32 v2, 7, v2
Mp 112,136,168
Rt 168,246,32768
Cv 70,70,71
Mp 120,136,172
Rt 170,246,33280
v_accvgpr_write_b32 a83, v70
buffer_load_dword v4, v2, s[20:23], s85 offen sc0 sc1
Mp 124,140,172
Rt 172,247,32768
v_mul_f32_e32 v72, s54, v72
Mp 116,140,168
Rt 174,247,33280
s_waitcnt lgkmcnt(8)
s_barrier
Mp 96,144,176
Rt 128,244,36864
v_mul_f32_e32 v73, s54, v73
Mp 104,144,180
Rt 130,244,37376
s_add_u32 s39, s39, 0x40000
Mp 108,148,180
Rt 132,245,36864
)" R"(
s_add_u32 m0, s37, 0
Cv 72,72,73
Mp 100,148,176
Rt 134,245,37376
buffer_load_dwordx4 v12, s[8:11], s39 offen offset:0 lds
Mp 112,152,176
Rt 136,246,36864
v_accvgpr_write_b32 a84, v72
Mp 120,152,180
Rt 138,246,37376
v_mul_f32_e32 v74, s54, v74
v_mul_f32_e32 v75, s54, v75
Mp 124,156,180
Rt 140,247,36864
Cv 74,74,75
Mp 116,156,176
Rt 142,247,37376
s_waitcnt lgkmcnt(8)
Mp 96,160,184
Rt 144,244,40960
v_accvgpr_write_b32 a85, v74
v_mul_f32_e32 v76, s54, v76
Mp 104,160,188
Rt 146,244,41472
buffer_load_dwordx4 v13, s[8:11], s39 offen offset:1024 lds
Mp 108,164,188
Rt 148,245,40960
v_mul_f32_e32 v77, s54, v77
Mp 100,164,184
Rt 150,245,41472
Cv 76,76,77
v_accvgpr_write_b32 a86, v76
Mp 112,168,184
Rt 152,246,40960
v_mul_f32_e32 v78, s54, v78
Mp 120,168,188
Rt 154,246,41472
v_mul_f32_e32 v79, s54, v79
Mp 124,172,188
Rt 156,247,40960
Cv 78,78,79
Mp 116,172,184
Rt 158,247,41472
buffer_load_dwordx4 v14, s[8:11], s39 offen offset:2048 lds
s_waitcnt lgkmcnt(8)
Mp 96,128,192
Rt 160,244,45056
v_accvgpr_write_b32 a87, v78
Mp 104,128,196
Rt 162,244,45568
v_mul_f32_e32 v80, s54, v80
Mp 108,132,196
Rt 164,245,45056
v_mul_f32_e32 v81, s54, v81
Cv 80,80,81
Mp 100,132,192
Rt 166,245,45568
v_accvgpr_write_b32 a88, v80
Mp 112,136,192
Rt 168,246,45056
v_mul_f32_e32 v82, s54, v82
Mp 120,136,196
Rt 170,246,45568
buffer_load_dwordx4 v15, s[8:11], s39 offen offset:3072 lds
)" R"(
Mp 124,140,196
Rt 172,247,45056
v_mul_f32_e32 v83, s54, v83
Mp 116,140,192
Rt 174,247,45568
s_waitcnt lgkmcnt(8)
Mp 96,144,200
Rt 128,244,49152
Cv 82,82,83
v_accvgpr_write_b32 a89, v82
Mp 104,144,204
Rt 130,244,49664
s_add_u32 s39, s39, 0x40000
Mp 108,148,204
Rt 132,245,49152
s_add_u32 m0, s37, 16384
Mp 100,148,200
Rt 134,245,49664
buffer_load_dwordx4 v12, s[8:11], s39 offen offset:0 lds
Mp 112,152,200
Rt 136,246,49152
v_mul_f32_e32 v84, s54, v84
Mp 120,152,204
Rt 138,246,49664
v_mul_f32_e32 v85, s54, v85
Mp 124,156,204
Rt 140,247,49152
Cv 84,84,85
Mp 116,156,200
Rt 142,247,49664
v_accvgpr_write_b32 a90, v84
s_waitcnt lgkmcnt(8)
Mp 96,160,208
Rt 144,244,53248
v_mul_f32_e32 v86, s54, v86
Mp 104,160,212
Rt 146,244,53760
buffer_load_dwordx4 v13, s[8:11], s39 offen offset:1024 lds
Mp 108,164,212
Rt 148,245,53248
v_mul_f32_e32 v87, s54, v87
Cv 86,86,87
Mp 100,164,208
Rt 150,245,53760
v_accvgpr_write_b32 a91, v86
Mp 112,168,208
Rt 152,246,53248
v_mul_f32_e32 v88, s54, v88
Mp 120,168,212
Rt 154,246,53760
v_mul_f32_e32 v89, s54, v89
Cv 88,88,89
Mp 124,172,212
Rt 156,247,53248
v_accvgpr_write_b32 a92, v88
Mp 116,172,208
Rt 158,247,53760
buffer_load_dwordx4 v14, s[8:11], s39 offen offset:2048 lds
s_waitcnt lgkmcnt(8)
Mp 96,128,216
Rt 160,244,57344
v_mul_f32_e32 v90, s54, v90
v_mul_f32_e32 v91, s54, v91
Mp 104,128,220
)" R"(
Rt 162,244,57856
Cv 90,90,91
Mp 108,132,220
Rt 164,245,57344
v_accvgpr_write_b32 a93, v90
Mp 100,132,216
Rt 166,245,57856
v_mul_f32_e32 v92, s54, v92
v_mul_f32_e32 v93, s54, v93
Mp 112,136,216
Rt 168,246,57344
Cv 92,92,93
Mp 120,136,220
Rt 170,246,57856
buffer_load_dwordx4 v15, s[8:11], s39 offen offset:3072 lds
Mp 124,140,220
Rt 172,247,57344
v_accvgpr_write_b32 a94, v92
Mp 116,140,216
Rt 174,247,57856
v_mul_f32_e32 v94, s54, v94
s_waitcnt lgkmcnt(8)
Mp 96,144,224
Rt 128,248,32768
v_mul_f32_e32 v95, s54, v95
Mp 104,144,228
Rt 130,248,33280
Cv 94,94,95
Mp 108,148,228
Rt 132,249,32768
v_accvgpr_write_b32 a95, v94
Mp 100,148,224
Rt 134,249,33280
Mp 112,152,224
Rt 136,250,32768
Mp 120,152,228
Rt 138,250,33280
Mp 124,156,228
Rt 140,251,32768
Mp 116,156,224
Rt 142,251,33280
s_waitcnt vmcnt(8) lgkmcnt(8)
s_barrier
Mp 96,160,232
Rt 144,248,36864
v_xor_b32 v4, s3, v4
v_cmp_gt_u32_e32 vcc, 2, v4
s_andn2_b32 s28, 0xff, vcc_lo
s_cbranch_scc1 .Lzsp10
.Lzok10:
Mp 104,160,236
Rt 146,248,37376
s_add_u32 s38, s38, 0xffff0080
v_xor_b32 v4, s3, v4
Mp 108,164,236
Rt 148,249,36864
s_add_u32 m0, s36, 32768
Mp 100,164,232
Rt 150,249,37376
buffer_load_dwordx4 v8, s[4:7], s38 offen offset:0 lds
Mp 112,168,232
Rt 152,250,36864
v_and_b32 v4, 1, v4
Mp 120,168,236
Rt 154,250,37376
)" R"(
Mp 124,172,236
Rt 156,251,36864
v_cmp_eq_u32_e32 vcc, 1, v4
s_mov_b32 s85, vcc_lo
Mp 116,172,232
Rt 158,251,37376
s_and_b32 s28, s2, 7
s_waitcnt lgkmcnt(8)
v_mfma_f32_16x16x32_bf16 v[64:67], a[128:131], v[128:131], 0
Rt 160,248,40960
s_lshr_b32 s28, s85, s28
v_mfma_f32_16x16x32_bf16 v[72:75], a[128:131], v[132:135], 0
Rt 162,248,41472
buffer_load_dwordx4 v9, s[4:7], s38 offen offset:1024 lds
v_mfma_f32_16x16x32_bf16 v[76:79], a[132:135], v[132:135], 0
Rt 164,249,40960
s_xor_b32 s28, s28, s34
v_mfma_f32_16x16x32_bf16 v[68:71], a[132:135], v[128:131], 0
Rt 166,249,41472
s_and_b32 s28, s28, 1
v_mfma_f32_16x16x32_bf16 v[80:83], a[136:139], v[128:131], 0
Rt 168,250,40960
v_mul_f32_e32 v96, s54, v96
s_lshl_b32 s84, s28, 13
v_mfma_f32_16x16x32_bf16 v[88:91], a[136:139], v[132:135], 0
Rt 170,250,41472
v_mul_f32_e32 v97, s54, v97
v_mfma_f32_16x16x32_bf16 v[92:95], a[140:143], v[132:135], 0
Rt 172,251,40960
Cv 96,96,97
s_lshl1_add_u32 s28, s36, s84
v_mfma_f32_16x16x32_bf16 v[84:87], a[140:143], v[128:131], 0
Rt 174,251,41472
buffer_load_dwordx4 v10, s[4:7], s38 offen offset:2048 lds
s_waitcnt lgkmcnt(8)
Mp 64,144,136
Rt 128,248,45056
v_accvgpr_write_b32 a96, v96
Mp 72,144,140
Rt 130,248,45568
v_mul_f32_e32 v98, s54, v98
v_add_u32 v2, s28, v1
Mp 76,148,140
Rt 132,249,45056
v_mul_f32_e32 v99, s54, v99
Mp 68,148,136
Rt 134,249,45568
Cv 98,98,99
v_add_u32 v3, 0x1000, v2
Mp 80,152,136
Rt 136,250,45056
v_accvgpr_write_b32 a97, v98
s_bfe_u32 s28, s73, 0x30010
Mp 88,152,140
Rt 138,250,45568
buffer_load_dwordx4 v11, s[4:7], s38 offen offset:3072 lds
Mp 92,156,140
Rt 140,251,45056
v_mul_f32_e32 v100, s54, v100
s_lshr_b32 s28, s85, s28
Mp 84,156,136
Rt 142,251,45568
s_waitcnt lgkmcnt(8)
Mp 64,160,144
)" R"(
Rt 144,248,49152
v_mul_f32_e32 v101, s54, v101
Mp 72,160,148
Rt 146,248,49664
s_add_u32 s38, s38, 0x10000
s_not_b32 s28, s28
Mp 76,164,148
Rt 148,249,49152
s_add_u32 m0, s36, 36864
Mp 68,164,144
Rt 150,249,49664
buffer_load_dwordx4 v8, s[4:7], s38 offen offset:0 lds
Mp 80,168,144
Rt 152,250,49152
Cv 100,100,101
s_and_b32 s28, s28, 1
Mp 88,168,148
Rt 154,250,49664
v_accvgpr_write_b32 a98, v100
Mp 92,172,148
Rt 156,251,49152
v_mul_f32_e32 v102, s54, v102
s_lshl_b32 s28, s28, 13
Mp 84,172,144
Rt 158,251,49664
s_waitcnt lgkmcnt(8)
Mp 64,128,152
Rt 160,248,53248
v_mul_f32_e32 v103, s54, v103
s_add_u32 s73, s73, s28
Mp 72,128,156
Rt 162,248,53760
buffer_load_dwordx4 v9, s[4:7], s38 offen offset:1024 lds
Mp 76,132,156
Rt 164,249,53248
Cv 102,102,103
Mp 68,132,152
Rt 166,249,53760
v_accvgpr_write_b32 a99, v102
Mp 80,136,152
Rt 168,250,53248
v_mul_f32_e32 v104, s54, v104
Mp 88,136,156
Rt 170,250,53760
v_mul_f32_e32 v105, s54, v105
Mp 92,140,156
Rt 172,251,53248
Cv 104,104,105
Mp 84,140,152
Rt 174,251,53760
buffer_load_dwordx4 v10, s[4:7], s38 offen offset:2048 lds
s_waitcnt lgkmcnt(8)
Mp 64,144,160
Rt 128,248,57344
v_accvgpr_write_b32 a100, v104
Mp 72,144,164
Rt 130,248,57856
v_mul_f32_e32 v106, s54, v106
Mp 76,148,164
Rt 132,249,57344
v_mul_f32_e32 v107, s54, v107
Mp 68,148,160
Rt 134,249,57856
Cv 106,106,107
)" R"(
Mp 80,152,160
Rt 136,250,57344
v_accvgpr_write_b32 a101, v106
Mp 88,152,164
Rt 138,250,57856
buffer_load_dwordx4 v11, s[4:7], s38 offen offset:3072 lds
Mp 92,156,164
Rt 140,251,57344
v_mul_f32_e32 v108, s54, v108
Mp 84,156,160
Rt 142,251,57856
s_waitcnt lgkmcnt(8)
Mp 64,160,168
Rt 144,252,0
v_mul_f32_e32 v109, s54, v109
Mp 72,160,172
Rt 146,252,512
Cv 108,108,109
Mp 76,164,172
Rt 148,253,0
v_accvgpr_write_b32 a102, v108
Mp 68,164,168
Rt 150,253,512
v_mul_f32_e32 v110, s54, v110
Mp 80,168,168
Rt 152,254,0
v_mul_f32_e32 v111, s54, v111
Mp 88,168,172
Rt 154,254,512
Cv 110,110,111
Mp 92,172,172
Rt 156,255,0
v_accvgpr_write_b32 a103, v110
Mp 84,172,168
Rt 158,255,512
s_waitcnt vmcnt(12) lgkmcnt(8)
s_barrier
Mp 64,128,176
Rt 160,252,4096
v_mul_f32_e32 v112, s54, v112
Mp 72,128,180
Rt 162,252,4608
s_add_u32 s39, s39, 0x40000
Mp 76,132,180
Rt 164,253,4096
s_add_u32 m0, s37, 32768
Mp 68,132,176
Rt 166,253,4608
buffer_load_dwordx4 v12, s[8:11], s39 offen offset:0 lds
Mp 80,136,176
Rt 168,254,4096
v_mul_f32_e32 v113, s54, v113
Mp 88,136,180
Rt 170,254,4608
Cv 112,112,113
Mp 92,140,180
Rt 172,255,4096
v_accvgpr_write_b32 a104, v112
Mp 84,140,176
Rt 174,255,4608
s_waitcnt lgkmcnt(8)
Mp 64,144,184
Rt 128,252,8192
v_mul_f32_e32 v114, s54, v114
Mp 72,144,188
)" R"(
Rt 130,252,8704
buffer_load_dwordx4 v13, s[8:11], s39 offen offset:1024 lds
Mp 76,148,188
Rt 132,253,8192
v_mul_f32_e32 v115, s54, v115
Mp 68,148,184
Rt 134,253,8704
Cv 114,114,115
Mp 80,152,184
Rt 136,254,8192
v_accvgpr_write_b32 a105, v114
Mp 88,152,188
Rt 138,254,8704
v_mul_f32_e32 v116, s54, v116
Mp 92,156,188
Rt 140,255,8192
v_mul_f32_e32 v117, s54, v117
Mp 84,156,184
Rt 142,255,8704
buffer_load_dwordx4 v14, s[8:11], s39 offen offset:2048 lds
s_waitcnt lgkmcnt(8)
Mp 64,160,192
Rt 144,252,12288
Cv 116,116,117
Mp 72,160,196
Rt 146,252,12800
v_accvgpr_write_b32 a106, v116
Mp 76,164,196
Rt 148,253,12288
v_mul_f32_e32 v118, s54, v118
Mp 68,164,192
Rt 150,253,12800
v_mul_f32_e32 v119, s54, v119
Mp 80,168,192
Rt 152,254,12288
Cv 118,118,119
Mp 88,168,196
Rt 154,254,12800
buffer_load_dwordx4 v15, s[8:11], s39 offen offset:3072 lds
Mp 92,172,196
Rt 156,255,12288
v_accvgpr_write_b32 a107, v118
Mp 84,172,192
Rt 158,255,12800
s_waitcnt lgkmcnt(8)
Mp 64,128,200
Rt 160,252,16384
v_mul_f32_e32 v120, s54, v120
Mp 72,128,204
Rt 162,252,16896
v_mul_f32_e32 v121, s54, v121
Mp 76,132,204
Rt 164,253,16384
Cv 120,120,121
Mp 68,132,200
Rt 166,253,16896
v_accvgpr_write_b32 a108, v120
Mp 80,136,200
Rt 168,254,16384
v_mul_f32_e32 v122, s54, v122
Mp 88,136,204
Rt 170,254,16896
v_mul_f32_e32 v123, s54, v123
Mp 92,140,204
)" R"(
Rt 172,255,16384
Cv 122,122,123
Mp 84,140,200
Rt 174,255,16896
s_waitcnt lgkmcnt(8)
Mp 64,144,208
Rt 128,252,20480
Rt 0,18,0
Mp 72,144,212
Rt 130,252,20992
Rt 2,18,2048
Mp 76,148,212
Rt 132,253,20480
Rt 4,19,0
Mp 68,148,208
Rt 134,253,20992
Rt 6,19,2048
Mp 80,152,208
Rt 136,254,20480
Rt 8,20,0
Mp 88,152,212
Rt 138,254,20992
Rt 10,20,2048
Mp 92,156,212
Rt 140,255,20480
Rt 12,21,0
Mp 84,156,208
Rt 142,255,20992
Rt 14,21,2048
Mp 64,160,216
Rt 144,252,24576
Rt 16,22,0
Mp 72,160,220
Rt 146,252,25088
Rt 18,22,2048
Mp 76,164,220
Rt 148,253,24576
Rt 20,23,0
Mp 68,164,216
Rt 150,253,25088
Rt 22,23,2048
Mp 80,168,216
Rt 152,254,24576
Rt 24,24,0
Mp 88,168,220
Rt 154,254,25088
Rt 26,24,2048
Mp 92,172,220
Rt 156,255,24576
Rt 28,25,0
Mp 84,172,216
Rt 158,255,25088
Rt 30,25,2048
Mp 64,128,224
Rb 32,16,0
v_accvgpr_write_b32 a109, v122
Mp 72,128,228
Rb 36,16,2048
v_mul_f32_e32 v124, s54, v124
Mp 76,132,228
Rb 40,16,8192
v_mul_f32_e32 v125, s54, v125
Mp 68,132,224
Rb 44,16,10240
)" R"(
Cv 124,124,125
Mp 80,136,224
Rb 48,16,16384
v_accvgpr_write_b32 a110, v124
Mp 88,136,228
Rb 52,16,18432
v_mul_f32_e32 v126, s54, v126
Mp 92,140,228
Rb 56,16,24576
v_mul_f32_e32 v127, s54, v127
Mp 84,140,224
Rb 60,16,26624
s_waitcnt lgkmcnt(9)
Mp 64,144,232
Cv 126,126,127
v_accvgpr_write_b32 a111, v126
Mp 72,144,236
Mp 76,148,236
Mp 68,148,232
Mp 80,152,232
Mp 88,152,236
Mp 92,156,236
Mp 84,156,232
s_cmp_eq_u32 s91, 1
s_waitcnt vmcnt(12) lgkmcnt(0)
s_barrier
s_cbranch_scc1 .LzBV
Mz 32,0,32
Rt 128,18,16384
v_mul_f32_e32 v64, s54, v64
Mz 96,0,40
v_mul_f32_e32 v65, s54, v65
Cv 64,64,65
Mz 128,0,44
Rt 130,18,18432
v_accvgpr_write_b32 a112, v64
Mz 160,0,48
s_add_u32 s39, s39, 0x40000
v_mul_f32_e32 v66, s54, v66
Mz 192,0,52
Rt 132,19,16384
s_add_u32 m0, s37, 49152
Mz 224,0,56
buffer_load_dwordx4 v12, s[8:11], s39 offen offset:0 lds
v_mfma_f32_16x16x32_bf16 a[192:195], a[0:3], a[60:63], 0
Rt 134,19,18432
v_mul_f32_e32 v67, s54, v67
v_mfma_f32_16x16x32_bf16 a[196:199], a[4:7], a[60:63], 0
Cv 66,66,67
v_accvgpr_write_b32 a113, v66
Mz 228,4,56
Rt 136,20,16384
v_mul_f32_e32 v68, s54, v68
Mz 196,4,52
v_mul_f32_e32 v69, s54, v69
Cv 68,68,69
buffer_store_dwordx4 a[64:67], v2, s[20:23], s51 offen offset:0 sc0 sc1
Mz 164,4,48
Rt 138,20,18432
v_accvgpr_write_b32 a114, v68
Mz 132,4,44
v_mul_f32_e32 v70, s54, v70
v_mul_f32_e32 v71, s54, v71
Mz 100,4,40
Rt 140,21,16384
)" R"(
Cv 70,70,71
Mz 36,4,32
buffer_load_dwordx4 v13, s[8:11], s39 offen offset:1024 lds
Mz 40,8,32
Rt 142,21,18432
v_accvgpr_write_b32 a115, v70
Mz 104,8,40
v_mul_f32_e32 v72, s54, v72
v_mul_f32_e32 v73, s54, v73
Mz 136,8,44
Rt 144,22,16384
Cv 72,72,73
Mz 168,8,48
v_accvgpr_write_b32 a116, v72
v_mul_f32_e32 v74, s54, v74
buffer_store_dwordx4 a[68:71], v2, s[20:23], s51 offen offset:1024 sc0 sc1
Mz 200,8,52
Rt 146,22,18432
v_mul_f32_e32 v75, s54, v75
Mz 232,8,56
Cv 74,74,75
v_accvgpr_write_b32 a117, v74
v_mfma_f32_16x16x32_bf16 a[200:203], a[8:11], a[60:63], 0
Rt 148,23,16384
v_mul_f32_e32 v76, s54, v76
v_mfma_f32_16x16x32_bf16 a[204:207], a[12:15], a[60:63], 0
buffer_load_dwordx4 v14, s[8:11], s39 offen offset:2048 lds
Mz 236,12,56
Rt 150,23,18432
v_mul_f32_e32 v77, s54, v77
Mz 204,12,52
Cv 76,76,77
v_accvgpr_write_b32 a118, v76
Mz 172,12,48
Rt 152,24,16384
v_mul_f32_e32 v78, s54, v78
Mz 140,12,44
v_mul_f32_e32 v79, s54, v79
Cv 78,78,79
buffer_store_dwordx4 a[72:75], v2, s[20:23], s51 offen offset:2048 sc0 sc1
Mz 108,12,40
Rt 154,24,18432
v_accvgpr_write_b32 a119, v78
Mz 44,12,32
v_mul_f32_e32 v80, s54, v80
v_mul_f32_e32 v81, s54, v81
Mz 48,16,32
Rt 156,25,16384
Cv 80,80,81
Mz 112,16,40
buffer_load_dwordx4 v15, s[8:11], s39 offen offset:3072 lds
Mz 144,16,44
Rt 158,25,18432
v_accvgpr_write_b32 a120, v80
Mz 176,16,48
v_mul_f32_e32 v82, s54, v82
v_mul_f32_e32 v83, s54, v83
Mz 208,16,52
Rb 160,17,0
Cv 82,82,83
Mz 240,16,56
v_accvgpr_write_b32 a121, v82
v_mul_f32_e32 v84, s54, v84
buffer_store_dwordx4 a[76:79], v2, s[20:23], s51 offen offset:3072 sc0 sc1
)" R"(
v_mfma_f32_16x16x32_bf16 a[208:211], a[16:19], a[60:63], 0
Rb 164,17,2048
v_mul_f32_e32 v85, s54, v85
v_mfma_f32_16x16x32_bf16 a[212:215], a[20:23], a[60:63], 0
s_add_u32 s39, s39, 0x40000
Cv 84,84,85
Mz 244,20,56
Rb 168,17,8192
s_add_u32 m0, s37, 0
Mz 212,20,52
buffer_load_dwordx4 v12, s[8:11], s39 offen offset:0 lds
Mz 180,20,48
Rb 172,17,10240
v_accvgpr_write_b32 a122, v84
Mz 148,20,44
v_mul_f32_e32 v86, s54, v86
v_mul_f32_e32 v87, s54, v87
Mz 116,20,40
Rb 176,17,16384
Cv 86,86,87
Mz 52,20,32
v_accvgpr_write_b32 a123, v86
v_mul_f32_e32 v88, s54, v88
buffer_store_dwordx4 a[80:83], v3, s[20:23], s51 offen offset:0 sc0 sc1
Mz 56,24,32
Rb 180,17,18432
v_mul_f32_e32 v89, s54, v89
Mz 120,24,40
Cv 88,88,89
v_accvgpr_write_b32 a124, v88
Mz 152,24,44
Rb 184,17,24576
v_mul_f32_e32 v90, s54, v90
Mz 184,24,48
buffer_load_dwordx4 v13, s[8:11], s39 offen offset:1024 lds
Mz 216,24,52
Rb 188,17,26624
v_mul_f32_e32 v91, s54, v91
Mz 248,24,56
Cv 90,90,91
v_accvgpr_write_b32 a125, v90
v_mfma_f32_16x16x32_bf16 a[216:219], a[24:27], a[60:63], 0
v_mul_f32_e32 v92, s54, v92
v_mul_f32_e32 v93, s54, v93
v_mfma_f32_16x16x32_bf16 a[220:223], a[28:31], a[60:63], 0
Cv 92,92,93
v_accvgpr_write_b32 a126, v92
buffer_store_dwordx4 a[84:87], v3, s[20:23], s51 offen offset:1024 sc0 sc1
Mz 252,28,56
v_mul_f32_e32 v94, s54, v94
v_mul_f32_e32 v95, s54, v95
Mz 220,28,52
Cv 94,94,95
v_accvgpr_write_b32 a127, v94
Mz 188,28,48
Mz 156,28,44
buffer_load_dwordx4 v14, s[8:11], s39 offen offset:2048 lds
Mz 124,28,40
Mz 60,28,32
Mz 64,0,36
Mz 68,4,36
buffer_store_dwordx4 a[88:91], v3, s[20:23], s51 offen offset:2048 sc0 sc1
Mz 72,8,36
Mz 76,12,36
)" R"(
Mz 80,16,36
Mz 84,20,36
buffer_load_dwordx4 v15, s[8:11], s39 offen offset:3072 lds
Mz 88,24,36
Mz 92,28,36
s_waitcnt vmcnt(15) lgkmcnt(0)
s_barrier
Mv 32,128,160
Rt 0,18,32768
Mv 64,128,164
s_add_u32 s38, s38, 0xffff0080
Mv 96,128,168
Rt 2,18,34816
s_add_u32 m0, s36, 0
Mv 128,128,172
buffer_load_dwordx4 v8, s[4:7], s38 offen offset:0 lds
Mv 160,128,176
Rt 4,19,32768
Mv 192,128,180
Mv 224,128,184
Rt 6,19,34816
Ma 192,128,188
buffer_load_dwordx4 v9, s[4:7], s38 offen offset:1024 lds
Ma 196,132,188
Rt 8,20,32768
Mv 228,132,184
Mv 196,132,180
Rt 10,20,34816
Mv 164,132,176
buffer_load_dwordx4 v10, s[4:7], s38 offen offset:2048 lds
Mv 132,132,172
Rt 12,21,32768
Mv 100,132,168
Mv 68,132,164
Rt 14,21,34816
Mv 36,132,160
buffer_load_dwordx4 v11, s[4:7], s38 offen offset:3072 lds
Mv 40,136,160
Rt 16,22,32768
Mv 72,136,164
s_add_u32 s38, s38, 0x10000
Mv 104,136,168
Rt 18,22,34816
s_add_u32 m0, s36, 4096
Mv 136,136,172
buffer_load_dwordx4 v8, s[4:7], s38 offen offset:0 lds
Mv 168,136,176
Rt 20,23,32768
Mv 200,136,180
Mv 232,136,184
Rt 22,23,34816
Ma 200,136,188
buffer_load_dwordx4 v9, s[4:7], s38 offen offset:1024 lds
Ma 204,140,188
Rt 24,24,32768
Mv 236,140,184
Mv 204,140,180
Rt 26,24,34816
Mv 172,140,176
buffer_load_dwordx4 v10, s[4:7], s38 offen offset:2048 lds
Mv 140,140,172
Rt 28,25,32768
Mv 108,140,168
Mv 76,140,164
Rt 30,25,34816
)" R"(
Mv 44,140,160
buffer_load_dwordx4 v11, s[4:7], s38 offen offset:3072 lds
Mv 48,144,160
Rb 32,16,32768
Mv 80,144,164
Mv 112,144,168
Rb 36,16,34816
Mv 144,144,172
Mv 176,144,176
Rb 40,16,40960
Mv 208,144,180
Mv 240,144,184
Rb 44,16,43008
Ma 208,144,188
Ma 212,148,188
Rb 48,16,49152
Mv 244,148,184
buffer_store_dwordx4 a[92:95], v3, s[20:23], s51 offen offset:3072 sc0 sc1
Mv 212,148,180
Rb 52,16,51200
Mv 180,148,176
Mv 148,148,172
Rb 56,16,57344
Mv 116,148,168
Mv 84,148,164
Rb 60,16,59392
Mv 52,148,160
Mv 56,152,160
Mv 88,152,164
s_add_u32 s51, s51, 0x10000
buffer_store_dwordx4 a[96:99], v2, s[20:23], s51 offen offset:0 sc0 sc1
Mv 120,152,168
Mv 152,152,172
Mv 184,152,176
Mv 216,152,180
Mv 248,152,184
Ma 216,152,188
Ma 220,156,188
Mv 252,156,184
buffer_store_dwordx4 a[100:103], v2, s[20:23], s51 offen offset:1024 sc0 sc1
Mv 220,156,180
Mv 188,156,176
Mv 156,156,172
Mv 124,156,168
Mv 92,156,164
Mv 60,156,160
s_waitcnt vmcnt(19) lgkmcnt(0)
s_barrier
Mv 32,0,32
Rt 128,18,49152
Mv 64,0,36
Mv 96,0,40
Rt 130,18,51200
s_mov_b32 s24, s80
Mv 128,0,44
s_add_u32 s39, s39, 0x40000
s_mov_b32 s25, s81
Mv 160,0,48
Rt 132,19,49152
s_add_u32 m0, s37, 16384
Mv 192,0,52
buffer_load_dwordx4 v12, s[8:11], s39 offen offset:0 lds
Mv 224,0,56
Rt 134,19,51200
s_mov_b32 s26, 0x380000
)" R"(
Ma 192,0,60
s_mov_b32 s27, 0x20000
s_bfe_u32 s28, s2, 0x30005
Ma 196,4,60
Rt 136,20,49152
s_lshl3_add_u32 s29, s34, s28
Mv 228,4,56
buffer_store_dwordx4 a[104:107], v2, s[20:23], s51 offen offset:2048 sc0 sc1
Mv 196,4,52
Rt 138,20,51200
s_lshl2_add_u32 s29, s29, s33
Mv 164,4,48
s_mul_i32 s83, s29, 7168
s_mul_i32 s29, s91, 448
Mv 132,4,44
Rt 140,21,49152
s_add_u32 s83, s83, s29
Mv 100,4,40
buffer_load_dwordx4 v13, s[8:11], s39 offen offset:1024 lds
Mv 68,4,36
Rt 142,21,51200
s_lshl_b32 s82, s34, 21
Mv 36,4,32
s_add_u32 s82, s82, 0x6000000
s_mul_i32 s94, s91, 14
Mv 40,8,32
Rt 144,22,49152
s_add_u32 s94, s94, s33
Mv 72,8,36
buffer_store_dwordx4 a[108:111], v2, s[20:23], s51 offen offset:3072 sc0 sc1
Mv 104,8,40
Rt 146,22,51200
s_mov_b32 s92, s28
Mv 136,8,44
v_lshrrev_b32 v4, 5, v0
v_mul_u32_u24 v4, 384, v4
Mv 168,8,48
Rt 148,23,49152
v_add_u32 v28, v4, v1
Mv 200,8,52
buffer_load_dwordx4 v14, s[8:11], s39 offen offset:2048 lds
Mv 232,8,56
Rt 150,23,51200
v_and_b32 v4, 31, v0
Ma 200,8,60
v_cmp_lt_u32_e32 vcc, 27, v4
v_mov_b32 v4, 0x40000000
Ma 204,12,60
Rt 152,24,49152
v_lshrrev_b32 v29, 2, v0
Mv 236,12,56
buffer_store_dwordx4 a[112:115], v3, s[20:23], s51 offen offset:0 sc0 sc1
Mv 204,12,52
Rt 154,24,51200
v_cndmask_b32_e32 v28, v28, v4, vcc
Mv 172,12,48
v_and_b32 v4, 48, v1
v_lshl_or_b32 v29, v29, 9, v4
Mv 140,12,44
Rt 156,25,49152
s_lshr_b32 s28, s33, 1
Mv 108,12,40
buffer_load_dwordx4 v15, s[8:11], s39 offen offset:3072 lds
Mv 76,12,36
)" R"(
Rt 158,25,51200
s_lshl_b32 s28, s28, 6
Mv 44,12,32
s_and_b32 s29, s33, 1
s_lshl3_add_u32 s28, s29, s28
Mv 48,16,32
Rb 160,17,32768
s_add_u32 s28, s28, 0x20000
Mv 80,16,36
buffer_store_dwordx4 a[116:119], v3, s[20:23], s51 offen offset:1024 sc0 sc1
Mv 112,16,40
Rb 164,17,34816
v_add_u32 v29, s28, v29
Mv 144,16,44
s_add_u32 s39, s39, 0x40000
Mv 176,16,48
Rb 168,17,40960
s_add_u32 m0, s37, 32768
Mv 208,16,52
buffer_load_dwordx4 v12, s[8:11], s39 offen offset:0 lds
Mv 240,16,56
Rb 172,17,43008
Ma 208,16,60
Ma 212,20,60
Rb 176,17,49152
Mv 244,20,56
buffer_store_dwordx4 a[120:123], v3, s[20:23], s51 offen offset:2048 sc0 sc1
Mv 212,20,52
Rb 180,17,51200
Mv 180,20,48
Mv 148,20,44
Rb 184,17,57344
Mv 116,20,40
buffer_load_dwordx4 v13, s[8:11], s39 offen offset:1024 lds
Mv 84,20,36
Rb 188,17,59392
Mv 52,20,32
Mv 56,24,32
Mv 88,24,36
buffer_store_dwordx4 a[124:127], v3, s[20:23], s51 offen offset:3072 sc0 sc1
Mv 120,24,40
Mv 152,24,44
Mv 184,24,48
Mv 216,24,52
buffer_load_dwordx4 v14, s[8:11], s39 offen offset:2048 lds
Mv 248,24,56
Ma 216,24,60
Ma 220,28,60
Mv 252,28,56
Mv 220,28,52
Mv 188,28,48
Mv 156,28,44
Mv 124,28,40
buffer_load_dwordx4 v15, s[8:11], s39 offen offset:3072 lds
Mv 92,28,36
Mv 60,28,32
s_waitcnt vmcnt(17) lgkmcnt(0)
s_barrier
Mv 32,128,160
Rt 0,18,0
Mv 64,128,164
s_add_u32 s38, s38, 0xffff0080
Mv 96,128,168
Rt 2,18,2048
s_add_u32 m0, s36, 32768
)" R"(
Mv 128,128,172
buffer_load_dwordx4 v8, s[4:7], s38 offen offset:0 lds
Mv 160,128,176
Rt 4,19,0
Mv 192,128,180
Mv 224,128,184
Rt 6,19,2048
Ma 192,128,188
buffer_load_dwordx4 v9, s[4:7], s38 offen offset:1024 lds
Ma 196,132,188
Rt 8,20,0
s_mov_b32 s40, 5
Mv 228,132,184
Mv 196,132,180
Rt 10,20,2048
Mv 164,132,176
buffer_load_dwordx4 v10, s[4:7], s38 offen offset:2048 lds
Mv 132,132,172
Rt 12,21,0
Mv 100,132,168
Mv 68,132,164
Rt 14,21,2048
Mv 36,132,160
buffer_load_dwordx4 v11, s[4:7], s38 offen offset:3072 lds
Mv 40,136,160
Rt 16,22,0
Mv 72,136,164
s_add_u32 s38, s38, 0x10000
Mv 104,136,168
Rt 18,22,2048
s_add_u32 m0, s36, 36864
Mv 136,136,172
buffer_load_dwordx4 v8, s[4:7], s38 offen offset:0 lds
Mv 168,136,176
Rt 20,23,0
Mv 200,136,180
Mv 232,136,184
Rt 22,23,2048
Ma 200,136,188
buffer_load_dwordx4 v9, s[4:7], s38 offen offset:1024 lds
Ma 204,140,188
Rt 24,24,0
Mv 236,140,184
Mv 204,140,180
Rt 26,24,2048
Mv 172,140,176
buffer_load_dwordx4 v10, s[4:7], s38 offen offset:2048 lds
Mv 140,140,172
Rt 28,25,0
Mv 108,140,168
Mv 76,140,164
Rt 30,25,2048
Mv 44,140,160
buffer_load_dwordx4 v11, s[4:7], s38 offen offset:3072 lds
Mv 48,144,160
Rb 32,16,0
Mv 80,144,164
Mv 112,144,168
Rb 36,16,2048
Mv 144,144,172
Mv 176,144,176
Rb 40,16,8192
s_add_u32 m0, s37, 0x10000
Mv 208,144,180
)" R"(
s_add_u32 s29, s83, 0
Mv 240,144,184
Rb 44,16,10240
Ma 208,144,188
buffer_load_dwordx4 v28, s[24:27], s29 offen offset:0 lds
Ma 212,148,188
Rb 48,16,16384
s_add_u32 s29, s83, 768
Mv 244,148,184
buffer_load_dwordx4 v28, s[24:27], s29 offen offset:1024 lds
Mv 212,148,180
Rb 52,16,18432
s_add_u32 s29, s83, 1536
Mv 180,148,176
buffer_load_dwordx4 v28, s[24:27], s29 offen offset:2048 lds
Mv 148,148,172
Rb 56,16,24576
s_add_u32 s29, s83, 2304
Mv 116,148,168
buffer_load_dwordx4 v28, s[24:27], s29 offen offset:3072 lds
Mv 84,148,164
Rb 60,16,26624
Mv 52,148,160
Mv 56,152,160
Mv 88,152,164
Mv 120,152,168
Mv 152,152,172
Mv 184,152,176
Mv 216,152,180
Mv 248,152,184
Ma 216,152,188
Ma 220,156,188
Mv 252,156,184
Mv 220,156,180
Mv 188,156,176
Mv 156,156,172
Mv 124,156,168
Mv 92,156,164
Mv 60,156,160
s_waitcnt vmcnt(14) lgkmcnt(0)
s_barrier
buffer_store_dword v6, v7, s[20:23], s52 offen offset:0 sc0 sc1
.LzPBK:
Mv 32,0,32
Rt 128,18,16384
Mv 64,0,36
Mv 96,0,40
Rt 130,18,18432
Mv 128,0,44
s_add_u32 s39, s39, 0x40000
Mv 160,0,48
Rt 132,19,16384
s_add_u32 m0, s37, 49152
Mv 192,0,52
buffer_load_dwordx4 v12, s[8:11], s39 offen offset:0 lds
Mv 224,0,56
Rt 134,19,18432
Ma 192,0,60
Ma 196,4,60
Rt 136,20,16384
Mv 228,4,56
Mv 196,4,52
Rt 138,20,18432
Mv 164,4,48
Mv 132,4,44
)" R"(
Rt 140,21,16384
Mv 100,4,40
buffer_load_dwordx4 v13, s[8:11], s39 offen offset:1024 lds
Mv 68,4,36
Rt 142,21,18432
Mv 36,4,32
Mv 40,8,32
Rt 144,22,16384
Mv 72,8,36
Mv 104,8,40
Rt 146,22,18432
Mv 136,8,44
Mv 168,8,48
Rt 148,23,16384
Mv 200,8,52
buffer_load_dwordx4 v14, s[8:11], s39 offen offset:2048 lds
Mv 232,8,56
Rt 150,23,18432
Ma 200,8,60
Ma 204,12,60
Rt 152,24,16384
Mv 236,12,56
Mv 204,12,52
Rt 154,24,18432
Mv 172,12,48
Mv 140,12,44
Rt 156,25,16384
Mv 108,12,40
buffer_load_dwordx4 v15, s[8:11], s39 offen offset:3072 lds
Mv 76,12,36
Rt 158,25,18432
Mv 44,12,32
Mv 48,16,32
Rb 160,17,0
Mv 80,16,36
Mv 112,16,40
Rb 164,17,2048
Mv 144,16,44
s_add_u32 s39, s39, 0x40000
Mv 176,16,48
Rb 168,17,8192
s_add_u32 m0, s37, 0
Mv 208,16,52
buffer_load_dwordx4 v12, s[8:11], s39 offen offset:0 lds
Mv 240,16,56
Rb 172,17,10240
Ma 208,16,60
Ma 212,20,60
Rb 176,17,16384
Mv 244,20,56
Mv 212,20,52
Rb 180,17,18432
Mv 180,20,48
Mv 148,20,44
Rb 184,17,24576
Mv 116,20,40
buffer_load_dwordx4 v13, s[8:11], s39 offen offset:1024 lds
Mv 84,20,36
Rb 188,17,26624
Mv 52,20,32
Mv 56,24,32
Mv 88,24,36
Mv 120,24,40
Mv 152,24,44
)" R"(
Mv 184,24,48
Mv 216,24,52
buffer_load_dwordx4 v14, s[8:11], s39 offen offset:2048 lds
Mv 248,24,56
Ma 216,24,60
Ma 220,28,60
Mv 252,28,56
Mv 220,28,52
Mv 188,28,48
Mv 156,28,44
Mv 124,28,40
buffer_load_dwordx4 v15, s[8:11], s39 offen offset:3072 lds
Mv 92,28,36
Mv 60,28,32
s_waitcnt vmcnt(8) lgkmcnt(0)
s_barrier
Mv 32,128,160
Rt 0,18,32768
Mv 64,128,164
s_add_u32 s38, s38, 0xffff0080
Mv 96,128,168
Rt 2,18,34816
s_add_u32 m0, s36, 0
Mv 128,128,172
buffer_load_dwordx4 v8, s[4:7], s38 offen offset:0 lds
Mv 160,128,176
Rt 4,19,32768
Mv 192,128,180
Mv 224,128,184
Rt 6,19,34816
Ma 192,128,188
buffer_load_dwordx4 v9, s[4:7], s38 offen offset:1024 lds
Ma 196,132,188
Rt 8,20,32768
Mv 228,132,184
Mv 196,132,180
Rt 10,20,34816
Mv 164,132,176
buffer_load_dwordx4 v10, s[4:7], s38 offen offset:2048 lds
Mv 132,132,172
Rt 12,21,32768
Mv 100,132,168
Mv 68,132,164
Rt 14,21,34816
Mv 36,132,160
buffer_load_dwordx4 v11, s[4:7], s38 offen offset:3072 lds
Mv 40,136,160
Rt 16,22,32768
Mv 72,136,164
s_add_u32 s38, s38, 0x10000
Mv 104,136,168
Rt 18,22,34816
s_add_u32 m0, s36, 4096
Mv 136,136,172
buffer_load_dwordx4 v8, s[4:7], s38 offen offset:0 lds
Mv 168,136,176
Rt 20,23,32768
Mv 200,136,180
Mv 232,136,184
Rt 22,23,34816
Ma 200,136,188
buffer_load_dwordx4 v9, s[4:7], s38 offen offset:1024 lds
Ma 204,140,188
Rt 24,24,32768
Mv 236,140,184
)" R"(
Mv 204,140,180
Rt 26,24,34816
Mv 172,140,176
buffer_load_dwordx4 v10, s[4:7], s38 offen offset:2048 lds
Mv 140,140,172
Rt 28,25,32768
Mv 108,140,168
Mv 76,140,164
Rt 30,25,34816
Mv 44,140,160
buffer_load_dwordx4 v11, s[4:7], s38 offen offset:3072 lds
Mv 48,144,160
Rb 32,16,32768
Mv 80,144,164
Mv 112,144,168
Rb 36,16,34816
Mv 144,144,172
Mv 176,144,176
Rb 40,16,40960
Mv 208,144,180
Mv 240,144,184
Rb 44,16,43008
Ma 208,144,188
Ma 212,148,188
Rb 48,16,49152
Mv 244,148,184
Mv 212,148,180
Rb 52,16,51200
Mv 180,148,176
Mv 148,148,172
Rb 56,16,57344
Mv 116,148,168
Mv 84,148,164
Rb 60,16,59392
Mv 52,148,160
Mv 56,152,160
Mv 88,152,164
Mv 120,152,168
Mv 152,152,172
Mv 184,152,176
Mv 216,152,180
Mv 248,152,184
Ma 216,152,188
Ma 220,156,188
Mv 252,156,184
Mv 220,156,180
Mv 188,156,176
Mv 156,156,172
Mv 124,156,168
Mv 92,156,164
Mv 60,156,160
s_waitcnt vmcnt(12) lgkmcnt(0)
s_barrier
Mv 32,0,32
Rt 128,18,49152
Mv 64,0,36
Mv 96,0,40
Rt 130,18,51200
Mv 128,0,44
s_add_u32 s39, s39, 0x40000
Mv 160,0,48
Rt 132,19,49152
s_add_u32 m0, s37, 16384
Mv 192,0,52
buffer_load_dwordx4 v12, s[8:11], s39 offen offset:0 lds
)" R"(
Mv 224,0,56
Rt 134,19,51200
Ma 192,0,60
Ma 196,4,60
Rt 136,20,49152
Mv 228,4,56
Mv 196,4,52
Rt 138,20,51200
Mv 164,4,48
Mv 132,4,44
Rt 140,21,49152
Mv 100,4,40
buffer_load_dwordx4 v13, s[8:11], s39 offen offset:1024 lds
Mv 68,4,36
Rt 142,21,51200
Mv 36,4,32
Mv 40,8,32
Rt 144,22,49152
Mv 72,8,36
Mv 104,8,40
Rt 146,22,51200
Mv 136,8,44
Mv 168,8,48
Rt 148,23,49152
Mv 200,8,52
buffer_load_dwordx4 v14, s[8:11], s39 offen offset:2048 lds
Mv 232,8,56
Rt 150,23,51200
Ma 200,8,60
Ma 204,12,60
Rt 152,24,49152
Mv 236,12,56
Mv 204,12,52
Rt 154,24,51200
Mv 172,12,48
Mv 140,12,44
Rt 156,25,49152
Mv 108,12,40
buffer_load_dwordx4 v15, s[8:11], s39 offen offset:3072 lds
Mv 76,12,36
Rt 158,25,51200
Mv 44,12,32
Mv 48,16,32
Rb 160,17,32768
Mv 80,16,36
Mv 112,16,40
Rb 164,17,34816
Mv 144,16,44
s_add_u32 s39, s39, 0x40000
Mv 176,16,48
Rb 168,17,40960
s_add_u32 m0, s37, 32768
Mv 208,16,52
buffer_load_dwordx4 v12, s[8:11], s39 offen offset:0 lds
Mv 240,16,56
Rb 172,17,43008
Ma 208,16,60
Ma 212,20,60
Rb 176,17,49152
Mv 244,20,56
Mv 212,20,52
Rb 180,17,51200
Mv 180,20,48
Mv 148,20,44
)" R"(
Rb 184,17,57344
Mv 116,20,40
buffer_load_dwordx4 v13, s[8:11], s39 offen offset:1024 lds
Mv 84,20,36
Rb 188,17,59392
Mv 52,20,32
Mv 56,24,32
Mv 88,24,36
Mv 120,24,40
Mv 152,24,44
Mv 184,24,48
Mv 216,24,52
buffer_load_dwordx4 v14, s[8:11], s39 offen offset:2048 lds
Mv 248,24,56
Ma 216,24,60
Ma 220,28,60
Mv 252,28,56
Mv 220,28,52
Mv 188,28,48
Mv 156,28,44
Mv 124,28,40
buffer_load_dwordx4 v15, s[8:11], s39 offen offset:3072 lds
Mv 92,28,36
Mv 60,28,32
s_waitcnt vmcnt(8) lgkmcnt(0)
s_barrier
Mv 32,128,160
Rt 0,18,0
Mv 64,128,164
s_add_u32 s38, s38, 0xffff0080
Mv 96,128,168
Rt 2,18,2048
s_add_u32 m0, s36, 32768
Mv 128,128,172
buffer_load_dwordx4 v8, s[4:7], s38 offen offset:0 lds
Mv 160,128,176
Rt 4,19,0
Mv 192,128,180
Mv 224,128,184
Rt 6,19,2048
Ma 192,128,188
buffer_load_dwordx4 v9, s[4:7], s38 offen offset:1024 lds
Ma 196,132,188
Rt 8,20,0
Mv 228,132,184
Mv 196,132,180
Rt 10,20,2048
Mv 164,132,176
buffer_load_dwordx4 v10, s[4:7], s38 offen offset:2048 lds
Mv 132,132,172
Rt 12,21,0
Mv 100,132,168
Mv 68,132,164
Rt 14,21,2048
Mv 36,132,160
buffer_load_dwordx4 v11, s[4:7], s38 offen offset:3072 lds
Mv 40,136,160
Rt 16,22,0
Mv 72,136,164
s_add_u32 s38, s38, 0x10000
Mv 104,136,168
Rt 18,22,2048
s_add_u32 m0, s36, 36864
Mv 136,136,172
buffer_load_dwordx4 v8, s[4:7], s38 offen offset:0 lds
)" R"(
Mv 168,136,176
Rt 20,23,0
Mv 200,136,180
Mv 232,136,184
Rt 22,23,2048
Ma 200,136,188
buffer_load_dwordx4 v9, s[4:7], s38 offen offset:1024 lds
Ma 204,140,188
Rt 24,24,0
Mv 236,140,184
Mv 204,140,180
Rt 26,24,2048
Mv 172,140,176
buffer_load_dwordx4 v10, s[4:7], s38 offen offset:2048 lds
Mv 140,140,172
Rt 28,25,0
Mv 108,140,168
Mv 76,140,164
Rt 30,25,2048
Mv 44,140,160
buffer_load_dwordx4 v11, s[4:7], s38 offen offset:3072 lds
Mv 48,144,160
Rb 32,16,0
Mv 80,144,164
Mv 112,144,168
Rb 36,16,2048
Mv 144,144,172
Mv 176,144,176
Rb 40,16,8192
Mv 208,144,180
Mv 240,144,184
Rb 44,16,10240
Ma 208,144,188
Ma 212,148,188
Rb 48,16,16384
Mv 244,148,184
Mv 212,148,180
Rb 52,16,18432
Mv 180,148,176
Mv 148,148,172
Rb 56,16,24576
Mv 116,148,168
Mv 84,148,164
Rb 60,16,26624
Mv 52,148,160
Mv 56,152,160
Mv 88,152,164
Mv 120,152,168
Mv 152,152,172
Mv 184,152,176
Mv 216,152,180
Mv 248,152,184
Ma 216,152,188
Ma 220,156,188
Mv 252,156,184
Mv 220,156,180
s_sub_u32 s40, s40, 1
Mv 188,156,176
Mv 156,156,172
Mv 124,156,168
Mv 92,156,164
Mv 60,156,160
s_cmp_lg_u32 s40, 0
s_waitcnt vmcnt(12) lgkmcnt(0)
s_barrier
)" R"(
s_cbranch_scc1 .LzPBK
s_branch .LzPBKX
.LzBV:
Mz 32,32,0
Rt 128,18,16384
v_mul_f32_e32 v64, s54, v64
Mz 36,36,0
v_mul_f32_e32 v65, s54, v65
Cv 64,64,65
Mz 96,40,0
Rt 130,18,18432
v_accvgpr_write_b32 a112, v64
Mz 100,44,0
s_add_u32 s39, s39, 0x40000
v_mul_f32_e32 v66, s54, v66
Mz 160,48,0
Rt 132,19,16384
s_add_u32 m0, s37, 49152
Mz 164,52,0
buffer_load_dwordx4 v12, s[8:11], s39 offen offset:0 lds
Mz 224,56,0
Rt 134,19,18432
v_mul_f32_e32 v67, s54, v67
Mz 228,60,0
Cv 66,66,67
v_accvgpr_write_b32 a113, v66
Mz 236,60,4
Rt 136,20,16384
v_mul_f32_e32 v68, s54, v68
Mz 232,56,4
v_mul_f32_e32 v69, s54, v69
Cv 68,68,69
buffer_store_dwordx4 a[64:67], v2, s[20:23], s51 offen offset:0 sc0 sc1
Mz 172,52,4
Rt 138,20,18432
v_accvgpr_write_b32 a114, v68
Mz 168,48,4
v_mul_f32_e32 v70, s54, v70
v_mul_f32_e32 v71, s54, v71
Mz 108,44,4
Rt 140,21,16384
Cv 70,70,71
Mz 104,40,4
buffer_load_dwordx4 v13, s[8:11], s39 offen offset:1024 lds
Mz 44,36,4
Rt 142,21,18432
v_accvgpr_write_b32 a115, v70
Mz 40,32,4
v_mul_f32_e32 v72, s54, v72
v_mul_f32_e32 v73, s54, v73
Mz 48,32,8
Rt 144,22,16384
Cv 72,72,73
Mz 52,36,8
v_accvgpr_write_b32 a116, v72
v_mul_f32_e32 v74, s54, v74
buffer_store_dwordx4 a[68:71], v2, s[20:23], s51 offen offset:1024 sc0 sc1
Mz 112,40,8
Rt 146,22,18432
v_mul_f32_e32 v75, s54, v75
Mz 116,44,8
Cv 74,74,75
v_accvgpr_write_b32 a117, v74
Mz 176,48,8
)" R"(
Rt 148,23,16384
v_mul_f32_e32 v76, s54, v76
Mz 180,52,8
buffer_load_dwordx4 v14, s[8:11], s39 offen offset:2048 lds
Mz 240,56,8
Rt 150,23,18432
v_mul_f32_e32 v77, s54, v77
Mz 244,60,8
Cv 76,76,77
v_accvgpr_write_b32 a118, v76
Mz 252,60,12
Rt 152,24,16384
v_mul_f32_e32 v78, s54, v78
Mz 248,56,12
v_mul_f32_e32 v79, s54, v79
Cv 78,78,79
buffer_store_dwordx4 a[72:75], v2, s[20:23], s51 offen offset:2048 sc0 sc1
Mz 188,52,12
Rt 154,24,18432
v_accvgpr_write_b32 a119, v78
Mz 184,48,12
v_mul_f32_e32 v80, s54, v80
v_mul_f32_e32 v81, s54, v81
Mz 124,44,12
Rt 156,25,16384
Cv 80,80,81
Mz 120,40,12
buffer_load_dwordx4 v15, s[8:11], s39 offen offset:3072 lds
Mz 60,36,12
Rt 158,25,18432
v_accvgpr_write_b32 a120, v80
Mz 56,32,12
v_mul_f32_e32 v82, s54, v82
v_mul_f32_e32 v83, s54, v83
Mz 128,40,16
Rb 160,17,0
Cv 82,82,83
Mz 132,44,16
v_accvgpr_write_b32 a121, v82
v_mul_f32_e32 v84, s54, v84
buffer_store_dwordx4 a[76:79], v2, s[20:23], s51 offen offset:3072 sc0 sc1
Mz 192,48,16
Rb 164,17,2048
v_mul_f32_e32 v85, s54, v85
Mz 196,52,16
s_add_u32 s39, s39, 0x40000
Cv 84,84,85
v_mfma_f32_16x16x32_bf16 a[192:195], a[56:59], a[16:19], 0
Rb 168,17,8192
s_add_u32 m0, s37, 0
v_mfma_f32_16x16x32_bf16 a[196:199], a[60:63], a[16:19], 0
buffer_load_dwordx4 v12, s[8:11], s39 offen offset:0 lds
v_mfma_f32_16x16x32_bf16 a[204:207], a[60:63], a[20:23], 0
Rb 172,17,10240
v_accvgpr_write_b32 a122, v84
v_mfma_f32_16x16x32_bf16 a[200:203], a[56:59], a[20:23], 0
v_mul_f32_e32 v86, s54, v86
v_mul_f32_e32 v87, s54, v87
Mz 204,52,20
Rb 176,17,16384
Cv 86,86,87
Mz 200,48,20
v_accvgpr_write_b32 a123, v86
v_mul_f32_e32 v88, s54, v88
)" R"(
buffer_store_dwordx4 a[80:83], v3, s[20:23], s51 offen offset:0 sc0 sc1
Mz 140,44,20
Rb 180,17,18432
v_mul_f32_e32 v89, s54, v89
Mz 136,40,20
Cv 88,88,89
v_accvgpr_write_b32 a124, v88
Mz 144,40,24
Rb 184,17,24576
v_mul_f32_e32 v90, s54, v90
Mz 148,44,24
buffer_load_dwordx4 v13, s[8:11], s39 offen offset:1024 lds
Mz 208,48,24
Rb 188,17,26624
v_mul_f32_e32 v91, s54, v91
Mz 212,52,24
Cv 90,90,91
v_accvgpr_write_b32 a125, v90
v_mfma_f32_16x16x32_bf16 a[208:211], a[56:59], a[24:27], 0
v_mul_f32_e32 v92, s54, v92
v_mul_f32_e32 v93, s54, v93
v_mfma_f32_16x16x32_bf16 a[212:215], a[60:63], a[24:27], 0
Cv 92,92,93
v_accvgpr_write_b32 a126, v92
buffer_store_dwordx4 a[84:87], v3, s[20:23], s51 offen offset:1024 sc0 sc1
v_mfma_f32_16x16x32_bf16 a[220:223], a[60:63], a[28:31], 0
v_mul_f32_e32 v94, s54, v94
v_mul_f32_e32 v95, s54, v95
v_mfma_f32_16x16x32_bf16 a[216:219], a[56:59], a[28:31], 0
Cv 94,94,95
v_accvgpr_write_b32 a127, v94
Mz 220,52,28
Mz 216,48,28
buffer_load_dwordx4 v14, s[8:11], s39 offen offset:2048 lds
Mz 156,44,28
Mz 152,40,28
Mz 64,32,16
Mz 68,36,16
buffer_store_dwordx4 a[88:91], v3, s[20:23], s51 offen offset:2048 sc0 sc1
Mz 72,32,20
Mz 76,36,20
Mz 80,32,24
Mz 84,36,24
buffer_load_dwordx4 v15, s[8:11], s39 offen offset:3072 lds
Mz 88,32,28
Mz 92,36,28
s_waitcnt vmcnt(15) lgkmcnt(0)
s_barrier
Mv 32,160,128
Rt 0,18,32768
Mv 36,164,128
s_add_u32 s38, s38, 0xffff0080
Mv 96,168,128
Rt 2,18,34816
s_add_u32 m0, s36, 0
Mv 100,172,128
buffer_load_dwordx4 v8, s[4:7], s38 offen offset:0 lds
Mv 160,176,128
Rt 4,19,32768
Mv 164,180,128
Mv 224,184,128
Rt 6,19,34816
Mv 228,188,128
buffer_load_dwordx4 v9, s[4:7], s38 offen offset:1024 lds
Mv 236,188,132
)" R"(
Rt 8,20,32768
Mv 232,184,132
Mv 172,180,132
Rt 10,20,34816
Mv 168,176,132
buffer_load_dwordx4 v10, s[4:7], s38 offen offset:2048 lds
Mv 108,172,132
Rt 12,21,32768
Mv 104,168,132
Mv 44,164,132
Rt 14,21,34816
Mv 40,160,132
buffer_load_dwordx4 v11, s[4:7], s38 offen offset:3072 lds
Mv 48,160,136
Rt 16,22,32768
Mv 52,164,136
s_add_u32 s38, s38, 0x10000
Mv 112,168,136
Rt 18,22,34816
s_add_u32 m0, s36, 4096
Mv 116,172,136
buffer_load_dwordx4 v8, s[4:7], s38 offen offset:0 lds
Mv 176,176,136
Rt 20,23,32768
Mv 180,180,136
Mv 240,184,136
Rt 22,23,34816
Mv 244,188,136
buffer_load_dwordx4 v9, s[4:7], s38 offen offset:1024 lds
Mv 252,188,140
Rt 24,24,32768
Mv 248,184,140
Mv 188,180,140
Rt 26,24,34816
Mv 184,176,140
buffer_load_dwordx4 v10, s[4:7], s38 offen offset:2048 lds
Mv 124,172,140
Rt 28,25,32768
Mv 120,168,140
Mv 60,164,140
Rt 30,25,34816
Mv 56,160,140
buffer_load_dwordx4 v11, s[4:7], s38 offen offset:3072 lds
Mv 64,160,144
Rb 32,16,32768
Mv 68,164,144
Mv 128,168,144
Rb 36,16,34816
Mv 132,172,144
Mv 192,176,144
Rb 40,16,40960
Mv 196,180,144
Ma 192,184,144
Rb 44,16,43008
Ma 196,188,144
Ma 204,188,148
Rb 48,16,49152
Ma 200,184,148
buffer_store_dwordx4 a[92:95], v3, s[20:23], s51 offen offset:3072 sc0 sc1
Mv 204,180,148
Rb 52,16,51200
Mv 200,176,148
Mv 140,172,148
Rb 56,16,57344
)" R"(
Mv 136,168,148
Mv 76,164,148
Rb 60,16,59392
Mv 72,160,148
Mv 80,160,152
Mv 84,164,152
s_add_u32 s51, s51, 0x10000
buffer_store_dwordx4 a[96:99], v2, s[20:23], s51 offen offset:0 sc0 sc1
Mv 144,168,152
Mv 148,172,152
Mv 208,176,152
Mv 212,180,152
Ma 208,184,152
Ma 212,188,152
Ma 220,188,156
Ma 216,184,156
buffer_store_dwordx4 a[100:103], v2, s[20:23], s51 offen offset:1024 sc0 sc1
Mv 220,180,156
Mv 216,176,156
Mv 156,172,156
Mv 152,168,156
Mv 92,164,156
Mv 88,160,156
s_waitcnt vmcnt(19) lgkmcnt(0)
s_barrier
Mv 32,32,0
Rt 128,18,49152
Mv 36,36,0
Mv 96,40,0
Rt 130,18,51200
s_mov_b32 s24, s80
Mv 100,44,0
s_add_u32 s39, s39, 0x40000
s_mov_b32 s25, s81
Mv 160,48,0
Rt 132,19,49152
s_add_u32 m0, s37, 16384
Mv 164,52,0
buffer_load_dwordx4 v12, s[8:11], s39 offen offset:0 lds
Mv 224,56,0
Rt 134,19,51200
s_mov_b32 s26, 0x380000
Mv 228,60,0
s_mov_b32 s27, 0x20000
s_bfe_u32 s28, s2, 0x30005
Mv 236,60,4
Rt 136,20,49152
s_lshl3_add_u32 s29, s34, s28
Mv 232,56,4
buffer_store_dwordx4 a[104:107], v2, s[20:23], s51 offen offset:2048 sc0 sc1
Mv 172,52,4
Rt 138,20,51200
s_lshl2_add_u32 s29, s29, s33
Mv 168,48,4
s_mul_i32 s83, s29, 7168
s_mul_i32 s29, s91, 448
Mv 108,44,4
Rt 140,21,49152
s_add_u32 s83, s83, s29
Mv 104,40,4
buffer_load_dwordx4 v13, s[8:11], s39 offen offset:1024 lds
Mv 44,36,4
Rt 142,21,51200
s_lshl_b32 s82, s34, 21
Mv 40,32,4
)" R"(
s_add_u32 s82, s82, 0x6000000
s_mul_i32 s94, s91, 14
Mv 48,32,8
Rt 144,22,49152
s_add_u32 s94, s94, s33
Mv 52,36,8
buffer_store_dwordx4 a[108:111], v2, s[20:23], s51 offen offset:3072 sc0 sc1
Mv 112,40,8
Rt 146,22,51200
s_mov_b32 s92, s28
Mv 116,44,8
v_lshrrev_b32 v4, 5, v0
v_mul_u32_u24 v4, 384, v4
Mv 176,48,8
Rt 148,23,49152
v_add_u32 v28, v4, v1
Mv 180,52,8
buffer_load_dwordx4 v14, s[8:11], s39 offen offset:2048 lds
Mv 240,56,8
Rt 150,23,51200
v_and_b32 v4, 31, v0
Mv 244,60,8
v_cmp_lt_u32_e32 vcc, 27, v4
v_mov_b32 v4, 0x40000000
Mv 252,60,12
Rt 152,24,49152
v_lshrrev_b32 v29, 2, v0
Mv 248,56,12
buffer_store_dwordx4 a[112:115], v3, s[20:23], s51 offen offset:0 sc0 sc1
Mv 188,52,12
Rt 154,24,51200
v_cndmask_b32_e32 v28, v28, v4, vcc
Mv 184,48,12
v_and_b32 v4, 48, v1
v_lshl_or_b32 v29, v29, 9, v4
Mv 124,44,12
Rt 156,25,49152
s_lshr_b32 s28, s33, 1
Mv 120,40,12
buffer_load_dwordx4 v15, s[8:11], s39 offen offset:3072 lds
Mv 60,36,12
Rt 158,25,51200
s_lshl_b32 s28, s28, 6
Mv 56,32,12
s_and_b32 s29, s33, 1
s_lshl3_add_u32 s28, s29, s28
Mv 64,32,16
Rb 160,17,32768
s_add_u32 s28, s28, 0x20000
Mv 68,36,16
buffer_store_dwordx4 a[116:119], v3, s[20:23], s51 offen offset:1024 sc0 sc1
Mv 128,40,16
Rb 164,17,34816
v_add_u32 v29, s28, v29
Mv 132,44,16
s_add_u32 s39, s39, 0x40000
Mv 192,48,16
Rb 168,17,40960
s_add_u32 m0, s37, 32768
Mv 196,52,16
buffer_load_dwordx4 v12, s[8:11], s39 offen offset:0 lds
Ma 192,56,16
Rb 172,17,43008
Ma 196,60,16
)" R"(
Ma 204,60,20
Rb 176,17,49152
Ma 200,56,20
buffer_store_dwordx4 a[120:123], v3, s[20:23], s51 offen offset:2048 sc0 sc1
Mv 204,52,20
Rb 180,17,51200
Mv 200,48,20
Mv 140,44,20
Rb 184,17,57344
Mv 136,40,20
buffer_load_dwordx4 v13, s[8:11], s39 offen offset:1024 lds
Mv 76,36,20
Rb 188,17,59392
Mv 72,32,20
Mv 80,32,24
Mv 84,36,24
buffer_store_dwordx4 a[124:127], v3, s[20:23], s51 offen offset:3072 sc0 sc1
Mv 144,40,24
Mv 148,44,24
Mv 208,48,24
Mv 212,52,24
buffer_load_dwordx4 v14, s[8:11], s39 offen offset:2048 lds
Ma 208,56,24
Ma 212,60,24
Ma 220,60,28
Ma 216,56,28
Mv 220,52,28
Mv 216,48,28
Mv 156,44,28
Mv 152,40,28
buffer_load_dwordx4 v15, s[8:11], s39 offen offset:3072 lds
Mv 92,36,28
Mv 88,32,28
s_waitcnt vmcnt(17) lgkmcnt(0)
s_barrier
Mv 32,160,128
Rt 0,18,0
Mv 36,164,128
s_add_u32 s38, s38, 0xffff0080
Mv 96,168,128
Rt 2,18,2048
s_add_u32 m0, s36, 32768
Mv 100,172,128
buffer_load_dwordx4 v8, s[4:7], s38 offen offset:0 lds
Mv 160,176,128
Rt 4,19,0
Mv 164,180,128
Mv 224,184,128
Rt 6,19,2048
Mv 228,188,128
buffer_load_dwordx4 v9, s[4:7], s38 offen offset:1024 lds
Mv 236,188,132
Rt 8,20,0
s_mov_b32 s40, 5
Mv 232,184,132
Mv 172,180,132
Rt 10,20,2048
Mv 168,176,132
buffer_load_dwordx4 v10, s[4:7], s38 offen offset:2048 lds
Mv 108,172,132
Rt 12,21,0
Mv 104,168,132
Mv 44,164,132
Rt 14,21,2048
Mv 40,160,132
)" R"(
buffer_load_dwordx4 v11, s[4:7], s38 offen offset:3072 lds
Mv 48,160,136
Rt 16,22,0
Mv 52,164,136
s_add_u32 s38, s38, 0x10000
Mv 112,168,136
Rt 18,22,2048
s_add_u32 m0, s36, 36864
Mv 116,172,136
buffer_load_dwordx4 v8, s[4:7], s38 offen offset:0 lds
Mv 176,176,136
Rt 20,23,0
Mv 180,180,136
Mv 240,184,136
Rt 22,23,2048
Mv 244,188,136
buffer_load_dwordx4 v9, s[4:7], s38 offen offset:1024 lds
Mv 252,188,140
Rt 24,24,0
Mv 248,184,140
Mv 188,180,140
Rt 26,24,2048
Mv 184,176,140
buffer_load_dwordx4 v10, s[4:7], s38 offen offset:2048 lds
Mv 124,172,140
Rt 28,25,0
Mv 120,168,140
Mv 60,164,140
Rt 30,25,2048
Mv 56,160,140
buffer_load_dwordx4 v11, s[4:7], s38 offen offset:3072 lds
Mv 64,160,144
Rb 32,16,0
Mv 68,164,144
Mv 128,168,144
Rb 36,16,2048
Mv 132,172,144
Mv 192,176,144
Rb 40,16,8192
s_add_u32 m0, s37, 0x10000
Mv 196,180,144
s_add_u32 s29, s83, 0
Ma 192,184,144
Rb 44,16,10240
Ma 196,188,144
buffer_load_dwordx4 v28, s[24:27], s29 offen offset:0 lds
Ma 204,188,148
Rb 48,16,16384
s_add_u32 s29, s83, 768
Ma 200,184,148
buffer_load_dwordx4 v28, s[24:27], s29 offen offset:1024 lds
Mv 204,180,148
Rb 52,16,18432
s_add_u32 s29, s83, 1536
Mv 200,176,148
buffer_load_dwordx4 v28, s[24:27], s29 offen offset:2048 lds
Mv 140,172,148
Rb 56,16,24576
s_add_u32 s29, s83, 2304
Mv 136,168,148
buffer_load_dwordx4 v28, s[24:27], s29 offen offset:3072 lds
Mv 76,164,148
Rb 60,16,26624
Mv 72,160,148
)" R"(
Mv 80,160,152
Mv 84,164,152
Mv 144,168,152
Mv 148,172,152
Mv 208,176,152
Mv 212,180,152
Ma 208,184,152
Ma 212,188,152
Ma 220,188,156
Ma 216,184,156
Mv 220,180,156
Mv 216,176,156
Mv 156,172,156
Mv 152,168,156
Mv 92,164,156
Mv 88,160,156
s_waitcnt vmcnt(14) lgkmcnt(0)
s_barrier
buffer_store_dword v6, v7, s[20:23], s52 offen offset:0 sc0 sc1
.LzPBV:
Mv 32,32,0
Rt 128,18,16384
Mv 36,36,0
Mv 96,40,0
Rt 130,18,18432
Mv 100,44,0
s_add_u32 s39, s39, 0x40000
Mv 160,48,0
Rt 132,19,16384
s_add_u32 m0, s37, 49152
Mv 164,52,0
buffer_load_dwordx4 v12, s[8:11], s39 offen offset:0 lds
Mv 224,56,0
Rt 134,19,18432
Mv 228,60,0
Mv 236,60,4
Rt 136,20,16384
Mv 232,56,4
Mv 172,52,4
Rt 138,20,18432
Mv 168,48,4
Mv 108,44,4
Rt 140,21,16384
Mv 104,40,4
buffer_load_dwordx4 v13, s[8:11], s39 offen offset:1024 lds
Mv 44,36,4
Rt 142,21,18432
Mv 40,32,4
Mv 48,32,8
Rt 144,22,16384
Mv 52,36,8
Mv 112,40,8
Rt 146,22,18432
Mv 116,44,8
Mv 176,48,8
Rt 148,23,16384
Mv 180,52,8
buffer_load_dwordx4 v14, s[8:11], s39 offen offset:2048 lds
Mv 240,56,8
Rt 150,23,18432
Mv 244,60,8
Mv 252,60,12
Rt 152,24,16384
Mv 248,56,12
Mv 188,52,12
)" R"(
Rt 154,24,18432
Mv 184,48,12
Mv 124,44,12
Rt 156,25,16384
Mv 120,40,12
buffer_load_dwordx4 v15, s[8:11], s39 offen offset:3072 lds
Mv 60,36,12
Rt 158,25,18432
Mv 56,32,12
Mv 64,32,16
Rb 160,17,0
Mv 68,36,16
Mv 128,40,16
Rb 164,17,2048
Mv 132,44,16
s_add_u32 s39, s39, 0x40000
Mv 192,48,16
Rb 168,17,8192
s_add_u32 m0, s37, 0
Mv 196,52,16
buffer_load_dwordx4 v12, s[8:11], s39 offen offset:0 lds
Ma 192,56,16
Rb 172,17,10240
Ma 196,60,16
Ma 204,60,20
Rb 176,17,16384
Ma 200,56,20
Mv 204,52,20
Rb 180,17,18432
Mv 200,48,20
Mv 140,44,20
Rb 184,17,24576
Mv 136,40,20
buffer_load_dwordx4 v13, s[8:11], s39 offen offset:1024 lds
Mv 76,36,20
Rb 188,17,26624
Mv 72,32,20
Mv 80,32,24
Mv 84,36,24
Mv 144,40,24
Mv 148,44,24
Mv 208,48,24
Mv 212,52,24
buffer_load_dwordx4 v14, s[8:11], s39 offen offset:2048 lds
Ma 208,56,24
Ma 212,60,24
Ma 220,60,28
Ma 216,56,28
Mv 220,52,28
Mv 216,48,28
Mv 156,44,28
Mv 152,40,28
buffer_load_dwordx4 v15, s[8:11], s39 offen offset:3072 lds
Mv 92,36,28
Mv 88,32,28
s_waitcnt vmcnt(8) lgkmcnt(0)
s_barrier
Mv 32,160,128
Rt 0,18,32768
Mv 36,164,128
s_add_u32 s38, s38, 0xffff0080
Mv 96,168,128
Rt 2,18,34816
s_add_u32 m0, s36, 0
Mv 100,172,128
)" R"(
buffer_load_dwordx4 v8, s[4:7], s38 offen offset:0 lds
Mv 160,176,128
Rt 4,19,32768
Mv 164,180,128
Mv 224,184,128
Rt 6,19,34816
Mv 228,188,128
buffer_load_dwordx4 v9, s[4:7], s38 offen offset:1024 lds
Mv 236,188,132
Rt 8,20,32768
Mv 232,184,132
Mv 172,180,132
Rt 10,20,34816
Mv 168,176,132
buffer_load_dwordx4 v10, s[4:7], s38 offen offset:2048 lds
Mv 108,172,132
Rt 12,21,32768
Mv 104,168,132
Mv 44,164,132
Rt 14,21,34816
Mv 40,160,132
buffer_load_dwordx4 v11, s[4:7], s38 offen offset:3072 lds
Mv 48,160,136
Rt 16,22,32768
Mv 52,164,136
s_add_u32 s38, s38, 0x10000
Mv 112,168,136
Rt 18,22,34816
s_add_u32 m0, s36, 4096
Mv 116,172,136
buffer_load_dwordx4 v8, s[4:7], s38 offen offset:0 lds
Mv 176,176,136
Rt 20,23,32768
Mv 180,180,136
Mv 240,184,136
Rt 22,23,34816
Mv 244,188,136
buffer_load_dwordx4 v9, s[4:7], s38 offen offset:1024 lds
Mv 252,188,140
Rt 24,24,32768
Mv 248,184,140
Mv 188,180,140
Rt 26,24,34816
Mv 184,176,140
buffer_load_dwordx4 v10, s[4:7], s38 offen offset:2048 lds
Mv 124,172,140
Rt 28,25,32768
Mv 120,168,140
Mv 60,164,140
Rt 30,25,34816
Mv 56,160,140
buffer_load_dwordx4 v11, s[4:7], s38 offen offset:3072 lds
Mv 64,160,144
Rb 32,16,32768
Mv 68,164,144
Mv 128,168,144
Rb 36,16,34816
Mv 132,172,144
Mv 192,176,144
Rb 40,16,40960
Mv 196,180,144
Ma 192,184,144
Rb 44,16,43008
Ma 196,188,144
)" R"(
Ma 204,188,148
Rb 48,16,49152
Ma 200,184,148
Mv 204,180,148
Rb 52,16,51200
Mv 200,176,148
Mv 140,172,148
Rb 56,16,57344
Mv 136,168,148
Mv 76,164,148
Rb 60,16,59392
Mv 72,160,148
Mv 80,160,152
Mv 84,164,152
Mv 144,168,152
Mv 148,172,152
Mv 208,176,152
Mv 212,180,152
Ma 208,184,152
Ma 212,188,152
Ma 220,188,156
Ma 216,184,156
Mv 220,180,156
Mv 216,176,156
Mv 156,172,156
Mv 152,168,156
Mv 92,164,156
Mv 88,160,156
s_waitcnt vmcnt(12) lgkmcnt(0)
s_barrier
Mv 32,32,0
Rt 128,18,49152
Mv 36,36,0
Mv 96,40,0
Rt 130,18,51200
Mv 100,44,0
s_add_u32 s39, s39, 0x40000
Mv 160,48,0
Rt 132,19,49152
s_add_u32 m0, s37, 16384
Mv 164,52,0
buffer_load_dwordx4 v12, s[8:11], s39 offen offset:0 lds
Mv 224,56,0
Rt 134,19,51200
Mv 228,60,0
Mv 236,60,4
Rt 136,20,49152
Mv 232,56,4
Mv 172,52,4
Rt 138,20,51200
Mv 168,48,4
Mv 108,44,4
Rt 140,21,49152
Mv 104,40,4
buffer_load_dwordx4 v13, s[8:11], s39 offen offset:1024 lds
Mv 44,36,4
Rt 142,21,51200
Mv 40,32,4
Mv 48,32,8
Rt 144,22,49152
Mv 52,36,8
Mv 112,40,8
Rt 146,22,51200
Mv 116,44,8
Mv 176,48,8
)" R"(
Rt 148,23,49152
Mv 180,52,8
buffer_load_dwordx4 v14, s[8:11], s39 offen offset:2048 lds
Mv 240,56,8
Rt 150,23,51200
Mv 244,60,8
Mv 252,60,12
Rt 152,24,49152
Mv 248,56,12
Mv 188,52,12
Rt 154,24,51200
Mv 184,48,12
Mv 124,44,12
Rt 156,25,49152
Mv 120,40,12
buffer_load_dwordx4 v15, s[8:11], s39 offen offset:3072 lds
Mv 60,36,12
Rt 158,25,51200
Mv 56,32,12
Mv 64,32,16
Rb 160,17,32768
Mv 68,36,16
Mv 128,40,16
Rb 164,17,34816
Mv 132,44,16
s_add_u32 s39, s39, 0x40000
Mv 192,48,16
Rb 168,17,40960
s_add_u32 m0, s37, 32768
Mv 196,52,16
buffer_load_dwordx4 v12, s[8:11], s39 offen offset:0 lds
Ma 192,56,16
Rb 172,17,43008
Ma 196,60,16
Ma 204,60,20
Rb 176,17,49152
Ma 200,56,20
Mv 204,52,20
Rb 180,17,51200
Mv 200,48,20
Mv 140,44,20
Rb 184,17,57344
Mv 136,40,20
buffer_load_dwordx4 v13, s[8:11], s39 offen offset:1024 lds
Mv 76,36,20
Rb 188,17,59392
Mv 72,32,20
Mv 80,32,24
Mv 84,36,24
Mv 144,40,24
Mv 148,44,24
Mv 208,48,24
Mv 212,52,24
buffer_load_dwordx4 v14, s[8:11], s39 offen offset:2048 lds
Ma 208,56,24
Ma 212,60,24
Ma 220,60,28
Ma 216,56,28
Mv 220,52,28
Mv 216,48,28
Mv 156,44,28
Mv 152,40,28
buffer_load_dwordx4 v15, s[8:11], s39 offen offset:3072 lds
Mv 92,36,28
)" R"(
Mv 88,32,28
s_waitcnt vmcnt(8) lgkmcnt(0)
s_barrier
Mv 32,160,128
Rt 0,18,0
Mv 36,164,128
s_add_u32 s38, s38, 0xffff0080
Mv 96,168,128
Rt 2,18,2048
s_add_u32 m0, s36, 32768
Mv 100,172,128
buffer_load_dwordx4 v8, s[4:7], s38 offen offset:0 lds
Mv 160,176,128
Rt 4,19,0
Mv 164,180,128
Mv 224,184,128
Rt 6,19,2048
Mv 228,188,128
buffer_load_dwordx4 v9, s[4:7], s38 offen offset:1024 lds
Mv 236,188,132
Rt 8,20,0
Mv 232,184,132
Mv 172,180,132
Rt 10,20,2048
Mv 168,176,132
buffer_load_dwordx4 v10, s[4:7], s38 offen offset:2048 lds
Mv 108,172,132
Rt 12,21,0
Mv 104,168,132
Mv 44,164,132
Rt 14,21,2048
Mv 40,160,132
buffer_load_dwordx4 v11, s[4:7], s38 offen offset:3072 lds
Mv 48,160,136
Rt 16,22,0
Mv 52,164,136
s_add_u32 s38, s38, 0x10000
Mv 112,168,136
Rt 18,22,2048
s_add_u32 m0, s36, 36864
Mv 116,172,136
buffer_load_dwordx4 v8, s[4:7], s38 offen offset:0 lds
Mv 176,176,136
Rt 20,23,0
Mv 180,180,136
Mv 240,184,136
Rt 22,23,2048
Mv 244,188,136
buffer_load_dwordx4 v9, s[4:7], s38 offen offset:1024 lds
Mv 252,188,140
Rt 24,24,0
Mv 248,184,140
Mv 188,180,140
Rt 26,24,2048
Mv 184,176,140
buffer_load_dwordx4 v10, s[4:7], s38 offen offset:2048 lds
Mv 124,172,140
Rt 28,25,0
Mv 120,168,140
Mv 60,164,140
Rt 30,25,2048
Mv 56,160,140
buffer_load_dwordx4 v11, s[4:7], s38 offen offset:3072 lds
Mv 64,160,144
Rb 32,16,0
)" R"(
Mv 68,164,144
Mv 128,168,144
Rb 36,16,2048
Mv 132,172,144
Mv 192,176,144
Rb 40,16,8192
Mv 196,180,144
Ma 192,184,144
Rb 44,16,10240
Ma 196,188,144
Ma 204,188,148
Rb 48,16,16384
Ma 200,184,148
Mv 204,180,148
Rb 52,16,18432
Mv 200,176,148
Mv 140,172,148
Rb 56,16,24576
Mv 136,168,148
Mv 76,164,148
Rb 60,16,26624
Mv 72,160,148
Mv 80,160,152
Mv 84,164,152
Mv 144,168,152
Mv 148,172,152
Mv 208,176,152
Mv 212,180,152
Ma 208,184,152
Ma 212,188,152
Ma 220,188,156
Ma 216,184,156
Mv 220,180,156
s_sub_u32 s40, s40, 1
Mv 216,176,156
Mv 156,172,156
Mv 152,168,156
Mv 92,164,156
Mv 88,160,156
s_cmp_lg_u32 s40, 0
s_waitcnt vmcnt(12) lgkmcnt(0)
s_barrier
s_cbranch_scc1 .LzPBV
s_branch .LzPBVX
.LzWIN:
.LzPAKX:
Mv 32,0,32
Rt 64,18,16384
Mv 64,0,36
Rb 192,28,32768
Mv 96,0,40
Rt 66,18,18432
Mv 128,0,44
s_add_u32 s39, s39, 0x40000
Rb 196,28,34816
Mv 160,0,48
Rt 68,19,16384
s_add_u32 m0, s37, 49152
Mv 192,0,52
buffer_load_dwordx4 v12, s[8:11], s39 offen offset:0 lds
Mv 224,0,56
Rt 70,19,18432
Ma 224,0,60
Rb 200,29,32768
Ma 228,4,60
)" R"(
Rt 72,20,16384
Mv 228,4,56
Rb 204,29,34816
Mv 196,4,52
Rt 74,20,18432
Mv 164,4,48
Rb 208,28,36864
Mv 132,4,44
Rt 76,21,16384
Mv 100,4,40
buffer_load_dwordx4 v13, s[8:11], s39 offen offset:1024 lds
Mv 68,4,36
Rt 78,21,18432
Mv 36,4,32
Rb 212,28,38912
Mv 40,8,32
Rt 80,22,16384
Mv 72,8,36
Rb 216,29,36864
Mv 104,8,40
Rt 82,22,18432
Mv 136,8,44
Rb 220,29,38912
Mv 168,8,48
Rt 84,23,16384
Mv 200,8,52
buffer_load_dwordx4 v14, s[8:11], s39 offen offset:2048 lds
Mv 232,8,56
Rt 86,23,18432
Ma 232,8,60
Ma 236,12,60
Rt 88,24,16384
Mv 236,12,56
Mv 204,12,52
Rt 90,24,18432
Mv 172,12,48
Mv 140,12,44
Rt 92,25,16384
Mv 108,12,40
buffer_load_dwordx4 v15, s[8:11], s39 offen offset:3072 lds
Mv 76,12,36
Rt 94,25,18432
Mv 44,12,32
Mv 48,16,32
Rb 96,17,0
Mv 80,16,36
Mv 112,16,40
Rb 100,17,2048
Mv 144,16,44
s_add_u32 s39, s39, 0x40000
Mv 176,16,48
Rb 104,17,8192
s_add_u32 m0, s37, 0
Mv 208,16,52
buffer_load_dwordx4 v12, s[8:11], s39 offen offset:0 lds
Mv 240,16,56
Rb 108,17,10240
Ma 240,16,60
Ma 244,20,60
Rb 112,17,16384
Mv 244,20,56
Mv 212,20,52
Rb 116,17,18432
Mv 180,20,48
)" R"(
Mv 148,20,44
Rb 120,17,24576
Mv 116,20,40
buffer_load_dwordx4 v13, s[8:11], s39 offen offset:1024 lds
Mv 84,20,36
Rb 124,17,26624
Mv 52,20,32
Mv 56,24,32
Mv 88,24,36
Mv 120,24,40
Mv 152,24,44
Mv 184,24,48
Mv 216,24,52
buffer_load_dwordx4 v14, s[8:11], s39 offen offset:2048 lds
Mv 248,24,56
Ma 248,24,60
Ma 252,28,60
Mv 252,28,56
Mv 220,28,52
Mv 188,28,48
Mv 156,28,44
Mv 124,28,40
buffer_load_dwordx4 v15, s[8:11], s39 offen offset:3072 lds
Mv 92,28,36
Mv 60,28,32
s_waitcnt vmcnt(8) lgkmcnt(0)
s_barrier
s_add_u32 m0, s36, 131072
s_add_u32 s44, s43, 256
buffer_load_dwordx4 v26, s[12:15], s44 offen offset:0 lds
buffer_load_dwordx4 v27, s[12:15], s44 offen offset:1024 lds
s_add_u32 s44, s43, 12544
buffer_load_dwordx4 v26, s[12:15], s44 offen offset:2048 lds
buffer_load_dwordx4 v27, s[12:15], s44 offen offset:3072 lds
Mv 32,64,96
Rt 0,18,32768
Mv 64,64,100
s_add_u32 s38, s38, 0xffff0080
Mv 96,64,104
Rt 2,18,34816
s_add_u32 m0, s36, 0
Mv 128,64,108
buffer_load_dwordx4 v8, s[4:7], s38 offen offset:0 lds
Mv 160,64,112
Rt 4,19,32768
Mv 192,64,116
Mv 224,64,120
Rt 6,19,34816
Ma 224,64,124
buffer_load_dwordx4 v9, s[4:7], s38 offen offset:1024 lds
Ma 228,68,124
Rt 8,20,32768
Mv 228,68,120
Mv 196,68,116
Rt 10,20,34816
Mv 164,68,112
buffer_load_dwordx4 v10, s[4:7], s38 offen offset:2048 lds
Mv 132,68,108
Rt 12,21,32768
Mv 100,68,104
Mv 68,68,100
Rt 14,21,34816
Mv 36,68,96
buffer_load_dwordx4 v11, s[4:7], s38 offen offset:3072 lds
Mv 40,72,96
)" R"(
Rt 16,22,32768
Mv 72,72,100
s_add_u32 s38, s38, 0x10000
Mv 104,72,104
Rt 18,22,34816
s_add_u32 m0, s36, 4096
Mv 136,72,108
buffer_load_dwordx4 v8, s[4:7], s38 offen offset:0 lds
Mv 168,72,112
Rt 20,23,32768
Mv 200,72,116
Mv 232,72,120
Rt 22,23,34816
Ma 232,72,124
buffer_load_dwordx4 v9, s[4:7], s38 offen offset:1024 lds
Ma 236,76,124
Rt 24,24,32768
Mv 236,76,120
Mv 204,76,116
Rt 26,24,34816
Mv 172,76,112
buffer_load_dwordx4 v10, s[4:7], s38 offen offset:2048 lds
Mv 140,76,108
Rt 28,25,32768
Mv 108,76,104
Mv 76,76,100
Rt 30,25,34816
Mv 44,76,96
buffer_load_dwordx4 v11, s[4:7], s38 offen offset:3072 lds
Mv 48,80,96
Rb 32,16,32768
Mv 80,80,100
Mv 112,80,104
Rb 36,16,34816
Mv 144,80,108
s_add_u32 s44, s43, 384
Mv 176,80,112
Rb 40,16,40960
s_add_u32 m0, s36, 135168
Mv 208,80,116
buffer_load_dwordx4 v26, s[12:15], s44 offen offset:0 lds
Mv 240,80,120
Rb 44,16,43008
Ma 240,80,124
Ma 244,84,124
Rb 48,16,49152
Mv 244,84,120
buffer_load_dwordx4 v27, s[12:15], s44 offen offset:1024 lds
Mv 212,84,116
Rb 52,16,51200
Mv 180,84,112
Mv 148,84,108
Rb 56,16,57344
Mv 116,84,104
s_add_u32 s44, s43, 12672
buffer_load_dwordx4 v26, s[12:15], s44 offen offset:2048 lds
Mv 84,84,100
Rb 60,16,59392
Mv 52,84,96
Mv 56,88,96
Mv 88,88,100
buffer_load_dwordx4 v27, s[12:15], s44 offen offset:3072 lds
Mv 120,88,104
Mv 152,88,108
)" R"(
Mv 184,88,112
Mv 216,88,116
Mv 248,88,120
Ma 248,88,124
Ma 252,92,124
Mv 252,92,120
Mv 220,92,116
Mv 188,92,112
Mv 156,92,108
Mv 124,92,104
Mv 92,92,100
Mv 60,92,96
s_waitcnt vmcnt(0) lgkmcnt(0)
s_barrier
Mv 32,0,32
Rt 64,18,49152
Mv 64,0,36
Rb 128,28,32768
Mv 96,0,40
Rt 66,18,51200
Mv 128,0,44
s_add_u32 s39, s39, 0x40000
Rb 132,28,34816
Mv 160,0,48
Rt 68,19,49152
s_add_u32 m0, s37, 16384
Mv 192,0,52
buffer_load_dwordx4 v12, s[8:11], s39 offen offset:0 lds
Mv 224,0,56
Rt 70,19,51200
Ma 224,0,60
Rb 136,29,32768
Ma 228,4,60
Rt 72,20,49152
Mv 228,4,56
Rb 140,29,34816
Mv 196,4,52
Rt 74,20,51200
Mv 164,4,48
Rb 144,28,36864
Mv 132,4,44
Rt 76,21,49152
Mv 100,4,40
buffer_load_dwordx4 v13, s[8:11], s39 offen offset:1024 lds
Mv 68,4,36
Rt 78,21,51200
Mv 36,4,32
Rb 148,28,38912
Mv 40,8,32
Rt 80,22,49152
Mv 72,8,36
Rb 152,29,36864
Mv 104,8,40
Rt 82,22,51200
Mv 136,8,44
Rb 156,29,38912
Mv 168,8,48
Rt 84,23,49152
Mv 200,8,52
buffer_load_dwordx4 v14, s[8:11], s39 offen offset:2048 lds
Mv 232,8,56
Rt 86,23,51200
Ma 232,8,60
Ma 236,12,60
Rt 88,24,49152
)" R"(
Mv 236,12,56
Mv 204,12,52
Rt 90,24,51200
Mv 172,12,48
Mv 140,12,44
Rt 92,25,49152
Mv 108,12,40
buffer_load_dwordx4 v15, s[8:11], s39 offen offset:3072 lds
Mv 76,12,36
Rt 94,25,51200
Mv 44,12,32
Mv 48,16,32
Rb 96,17,32768
Mv 80,16,36
Mv 112,16,40
Rb 100,17,34816
Mv 144,16,44
s_add_u32 s39, s39, 0x40000
Mv 176,16,48
Rb 104,17,40960
s_add_u32 m0, s37, 32768
Mv 208,16,52
buffer_load_dwordx4 v12, s[8:11], s39 offen offset:0 lds
Mv 240,16,56
Rb 108,17,43008
Ma 240,16,60
Ma 244,20,60
Rb 112,17,49152
Mv 244,20,56
Mv 212,20,52
Rb 116,17,51200
Mv 180,20,48
Mv 148,20,44
Rb 120,17,57344
Mv 116,20,40
buffer_load_dwordx4 v13, s[8:11], s39 offen offset:1024 lds
Mv 84,20,36
Rb 124,17,59392
Mv 52,20,32
Mv 56,24,32
Mv 88,24,36
Mv 120,24,40
Mv 152,24,44
Mv 184,24,48
Mv 216,24,52
buffer_load_dwordx4 v14, s[8:11], s39 offen offset:2048 lds
Mv 248,24,56
Ma 248,24,60
Ma 252,28,60
Mv 252,28,56
Mv 220,28,52
Mv 188,28,48
Mv 156,28,44
Mv 124,28,40
buffer_load_dwordx4 v15, s[8:11], s39 offen offset:3072 lds
Mv 92,28,36
Mv 60,28,32
s_waitcnt lgkmcnt(0)
s_barrier
s_add_u32 m0, s36, 131072
s_add_u32 s44, s43, 512
buffer_load_dwordx4 v26, s[12:15], s44 offen offset:0 lds
buffer_load_dwordx4 v27, s[12:15], s44 offen offset:1024 lds
s_add_u32 s44, s43, 12800
buffer_load_dwordx4 v26, s[12:15], s44 offen offset:2048 lds
)" R"(
buffer_load_dwordx4 v27, s[12:15], s44 offen offset:3072 lds
Mv 32,64,96
Rt 0,18,0
Mv 64,64,100
s_add_u32 s38, s38, 0xffff0080
Mv 96,64,104
Rt 2,18,2048
s_add_u32 m0, s36, 32768
Mv 128,64,108
buffer_load_dwordx4 v8, s[4:7], s38 offen offset:0 lds
Mv 160,64,112
Rt 4,19,0
Mv 192,64,116
Mv 224,64,120
Rt 6,19,2048
Ma 224,64,124
buffer_load_dwordx4 v9, s[4:7], s38 offen offset:1024 lds
Ma 228,68,124
Rt 8,20,0
Mv 228,68,120
Mv 196,68,116
Rt 10,20,2048
Mv 164,68,112
buffer_load_dwordx4 v10, s[4:7], s38 offen offset:2048 lds
Mv 132,68,108
Rt 12,21,0
Mv 100,68,104
Mv 68,68,100
Rt 14,21,2048
Mv 36,68,96
buffer_load_dwordx4 v11, s[4:7], s38 offen offset:3072 lds
Mv 40,72,96
Rt 16,22,0
Mv 72,72,100
s_add_u32 s38, s38, 0x10000
Mv 104,72,104
Rt 18,22,2048
s_add_u32 m0, s36, 36864
Mv 136,72,108
buffer_load_dwordx4 v8, s[4:7], s38 offen offset:0 lds
Mv 168,72,112
Rt 20,23,0
Mv 200,72,116
Mv 232,72,120
Rt 22,23,2048
Ma 232,72,124
buffer_load_dwordx4 v9, s[4:7], s38 offen offset:1024 lds
Ma 236,76,124
Rt 24,24,0
Mv 236,76,120
Mv 204,76,116
Rt 26,24,2048
Mv 172,76,112
buffer_load_dwordx4 v10, s[4:7], s38 offen offset:2048 lds
Mv 140,76,108
Rt 28,25,0
Mv 108,76,104
Mv 76,76,100
Rt 30,25,2048
Mv 44,76,96
buffer_load_dwordx4 v11, s[4:7], s38 offen offset:3072 lds
Mv 48,80,96
Rb 32,16,0
Mv 80,80,100
)" R"(
Mv 112,80,104
Rb 36,16,2048
Mv 144,80,108
s_add_u32 s44, s43, 640
Mv 176,80,112
Rb 40,16,8192
s_add_u32 m0, s36, 135168
Mv 208,80,116
buffer_load_dwordx4 v26, s[12:15], s44 offen offset:0 lds
Mv 240,80,120
Rb 44,16,10240
Ma 240,80,124
Ma 244,84,124
Rb 48,16,16384
Mv 244,84,120
buffer_load_dwordx4 v27, s[12:15], s44 offen offset:1024 lds
Mv 212,84,116
Rb 52,16,18432
Mv 180,84,112
Mv 148,84,108
Rb 56,16,24576
Mv 116,84,104
s_add_u32 s44, s43, 12928
buffer_load_dwordx4 v26, s[12:15], s44 offen offset:2048 lds
Mv 84,84,100
Rb 60,16,26624
Mv 52,84,96
Mv 56,88,96
Mv 88,88,100
buffer_load_dwordx4 v27, s[12:15], s44 offen offset:3072 lds
Mv 120,88,104
Mv 152,88,108
Mv 184,88,112
Mv 216,88,116
Mv 248,88,120
Ma 248,88,124
Ma 252,92,124
Mv 252,92,120
Mv 220,92,116
Mv 188,92,112
Mv 156,92,108
Mv 124,92,104
Mv 92,92,100
Mv 60,92,96
s_waitcnt vmcnt(0) lgkmcnt(0)
s_barrier
Mv 32,0,32
Rt 64,18,16384
Mv 64,0,36
Rb 160,28,32768
v_lshrrev_b32 v5, 2, v0
Mv 96,0,40
Rt 66,18,18432
v_and_b32 v5, 8, v5
Mv 128,0,44
s_add_u32 s39, s39, 0x40000
Rb 164,28,34816
Mv 160,0,48
Rt 68,19,16384
s_add_u32 m0, s37, 49152
Mv 192,0,52
buffer_load_dwordx4 v12, s[8:11], s39 offen offset:0 lds
Mv 224,0,56
Rt 70,19,18432
v_and_or_b32 v5, v0, 7, v5
)" R"(
Ma 224,0,60
Rb 168,29,32768
v_lshlrev_b32 v5, 8, v5
Ma 228,4,60
Rt 72,20,16384
v_and_or_b32 v5, v0, 24, v5
Mv 228,4,56
Rb 172,29,34816
v_lshrrev_b32 v5, 1, v5
Mv 196,4,52
Rt 74,20,18432
v_mov_b32 v6, s3
Mv 164,4,48
Rb 176,28,36864
Mv 132,4,44
Rt 76,21,16384
Mv 100,4,40
buffer_load_dwordx4 v13, s[8:11], s39 offen offset:1024 lds
Mv 68,4,36
Rt 78,21,18432
Mv 36,4,32
Rb 180,28,38912
Mv 40,8,32
Rt 80,22,16384
Mv 72,8,36
Rb 184,29,36864
Mv 104,8,40
Rt 82,22,18432
Mv 136,8,44
Rb 188,29,38912
Mv 168,8,48
Rt 84,23,16384
Mv 200,8,52
buffer_load_dwordx4 v14, s[8:11], s39 offen offset:2048 lds
Mv 232,8,56
Rt 86,23,18432
Ma 232,8,60
Ma 236,12,60
Rt 88,24,16384
Mv 236,12,56
Mv 204,12,52
Rt 90,24,18432
Mv 172,12,48
Mv 140,12,44
Rt 92,25,16384
Mv 108,12,40
buffer_load_dwordx4 v15, s[8:11], s39 offen offset:3072 lds
Mv 76,12,36
Rt 94,25,18432
v_lshrrev_b32 v10, 3, v0
Mv 44,12,32
v_and_b32 v11, 0x70, v1
v_lshl_or_b32 v10, v10, 13, v11
Mv 48,16,32
Rb 96,17,0
v_lshlrev_b32 v11, 1, v0
Mv 80,16,36
v_and_b32 v11, 32, v11
v_xor_b32 v10, v10, v11
Mv 112,16,40
Rb 100,17,2048
s_and_b32 s28, s33, 1
Mv 144,16,44
s_lshl_b32 s28, s28, 6
)" R"(
v_xor_b32 v8, s28, v10
Mv 176,16,48
Rb 104,17,8192
v_xor_b32 v9, 64, v8
Mv 208,16,52
v_add_u32 v9, 0xfc00, v9
v_add_u32 v10, 0x1f800, v8
Mv 240,16,56
Rb 108,17,10240
v_add_u32 v11, 0x1f800, v9
Ma 240,16,60
Ma 244,20,60
Rb 112,17,16384
Mv 244,20,56
Mv 212,20,52
Rb 116,17,18432
Mv 180,20,48
Mv 148,20,44
Rb 120,17,24576
Mv 116,20,40
Mv 84,20,36
Rb 124,17,26624
Mv 52,20,32
Mv 56,24,32
Mv 88,24,36
Mv 120,24,40
Mv 152,24,44
Mv 184,24,48
Mv 216,24,52
Mv 248,24,56
Ma 248,24,60
Ma 252,28,60
Mv 252,28,56
Mv 220,28,52
Mv 188,28,48
Mv 156,28,44
Mv 124,28,40
Mv 92,28,36
Mv 60,28,32
s_waitcnt lgkmcnt(0)
s_barrier
Mv 32,64,96
Rt 0,18,32768
Mv 64,64,100
s_add_u32 s92, s42, 0x0
Mv 96,64,104
Rt 2,18,34816
s_add_u32 m0, s41, 131072
Mv 128,64,108
buffer_load_dwordx4 v8, s[16:19], s92 offen offset:0 lds
Mv 160,64,112
Rt 4,19,32768
Mv 192,64,116
Mv 224,64,120
Rt 6,19,34816
Ma 224,64,124
buffer_load_dwordx4 v9, s[16:19], s92 offen offset:1024 lds
Ma 228,68,124
Rt 8,20,32768
Mv 228,68,120
Mv 196,68,116
Rt 10,20,34816
Mv 164,68,112
buffer_load_dwordx4 v10, s[16:19], s92 offen offset:2048 lds
Mv 132,68,108
)" R"(
Rt 12,21,32768
Mv 100,68,104
Mv 68,68,100
Rt 14,21,34816
Mv 36,68,96
buffer_load_dwordx4 v11, s[16:19], s92 offen offset:3072 lds
Mv 40,72,96
Rt 16,22,32768
Mv 72,72,100
s_add_u32 s92, s42, 0x40000
Mv 104,72,104
Rt 18,22,34816
s_add_u32 m0, s41, 135168
Mv 136,72,108
buffer_load_dwordx4 v8, s[16:19], s92 offen offset:0 lds
Mv 168,72,112
Rt 20,23,32768
Mv 200,72,116
Mv 232,72,120
Rt 22,23,34816
Ma 232,72,124
buffer_load_dwordx4 v9, s[16:19], s92 offen offset:1024 lds
Ma 236,76,124
Rt 24,24,32768
Mv 236,76,120
Mv 204,76,116
Rt 26,24,34816
Mv 172,76,112
buffer_load_dwordx4 v10, s[16:19], s92 offen offset:2048 lds
Mv 140,76,108
Rt 28,25,32768
Mv 108,76,104
s_add_u32 s92, s42, 0x1c0000
Mv 76,76,100
Rt 30,25,34816
s_add_u32 m0, s41, 0
Mv 44,76,96
buffer_load_dwordx4 v8, s[16:19], s92 offen offset:0 lds
Mv 48,80,96
Rb 32,16,32768
Mv 80,80,100
Mv 112,80,104
Rb 36,16,34816
Mv 144,80,108
buffer_load_dwordx4 v9, s[16:19], s92 offen offset:1024 lds
Mv 176,80,112
Rb 40,16,40960
Mv 208,80,116
Mv 240,80,120
Rb 44,16,43008
Ma 240,80,124
buffer_load_dwordx4 v10, s[16:19], s92 offen offset:2048 lds
Ma 244,84,124
Rb 48,16,49152
Mv 244,84,120
Mv 212,84,116
Rb 52,16,51200
Mv 180,84,112
buffer_load_dwordx4 v11, s[16:19], s92 offen offset:3072 lds
Mv 148,84,108
Rb 56,16,57344
Mv 116,84,104
s_add_u32 s92, s42, 0x200000
Mv 84,84,100
)" R"(
Rb 60,16,59392
s_add_u32 m0, s41, 4096
Mv 52,84,96
buffer_load_dwordx4 v8, s[16:19], s92 offen offset:0 lds
Mv 56,88,96
Mv 88,88,100
Mv 120,88,104
Mv 152,88,108
buffer_load_dwordx4 v9, s[16:19], s92 offen offset:1024 lds
Mv 184,88,112
Mv 216,88,116
Mv 248,88,120
Ma 248,88,124
buffer_load_dwordx4 v10, s[16:19], s92 offen offset:2048 lds
Ma 252,92,124
Mv 252,92,120
Mv 220,92,116
Mv 188,92,112
Mv 156,92,108
Mv 124,92,104
Mv 92,92,100
Mv 60,92,96
s_waitcnt vmcnt(14) lgkmcnt(4)
s_barrier
Mv 32,0,32
Rt 64,18,49152
Mv 64,0,36
Rt 66,18,51200
s_add_u32 s92, s42, 0x80
Mv 96,0,40
Rt 68,19,49152
s_add_u32 m0, s41, 65536
Mv 128,0,44
Rt 70,19,51200
buffer_load_dwordx4 v8, s[16:19], s92 offen offset:0 lds
Mv 132,4,44
Rt 72,20,49152
Mv 100,4,40
Rt 74,20,51200
Mv 68,4,36
Rt 76,21,49152
Mv 36,4,32
Rt 78,21,51200
buffer_load_dwordx4 v9, s[16:19], s92 offen offset:1024 lds
Mv 40,8,32
Rt 80,22,49152
Mv 72,8,36
Rt 82,22,51200
Mv 104,8,40
Rt 84,23,49152
Mv 136,8,44
Rt 86,23,51200
buffer_load_dwordx4 v10, s[16:19], s92 offen offset:2048 lds
Mv 140,12,44
Rt 88,24,49152
Mv 108,12,40
Rt 90,24,51200
Mv 76,12,36
Rt 92,25,49152
Mv 44,12,32
Rt 94,25,51200
buffer_load_dwordx4 v11, s[16:19], s92 offen offset:3072 lds
Mv 48,16,32
Rb 96,17,32768
Mv 80,16,36
)" R"(
Rb 100,17,34816
s_add_u32 s92, s42, 0x40080
Mv 112,16,40
Rb 104,17,40960
s_add_u32 m0, s41, 69632
Mv 144,16,44
Rb 108,17,43008
buffer_load_dwordx4 v8, s[16:19], s92 offen offset:0 lds
Mv 148,20,44
Rb 112,17,49152
Mv 116,20,40
Rb 116,17,51200
Mv 84,20,36
Rb 120,17,57344
Mv 52,20,32
Rb 124,17,59392
buffer_load_dwordx4 v9, s[16:19], s92 offen offset:1024 lds
Mv 56,24,32
Mv 88,24,36
Mv 120,24,40
Mv 152,24,44
buffer_load_dwordx4 v10, s[16:19], s92 offen offset:2048 lds
Mv 156,28,44
Mv 124,28,40
Mv 92,28,36
Mv 60,28,32
s_waitcnt lgkmcnt(0)
s_barrier
Mv 32,64,96
v_bfe_u32 v12, v0, 3, 2
v_lshlrev_b32 v12, 5, v12
Mv 36,68,96
v_and_b32 v4, 3, v0
v_lshl_or_b32 v12, v4, 3, v12
Mv 40,72,96
s_add_u32 m0, s36, 98304
s_add_u32 s44, s43, 768
Mv 44,76,96
buffer_load_dwordx4 v26, s[12:15], s44 offen offset:0 lds
Mv 48,80,96
v_bfe_u32 v4, v0, 2, 2
v_lshl_or_b32 v12, v4, 7, v12
Mv 52,84,96
v_lshrrev_b32 v4, 4, v0
v_lshl_or_b32 v12, v4, 10, v12
Mv 56,88,96
v_xor_b32 v13, 32, v12
v_xor_b32 v14, 64, v12
Mv 60,92,96
buffer_load_dwordx4 v27, s[12:15], s44 offen offset:1024 lds
Mv 64,64,100
v_xor_b32 v15, 96, v12
v_add_u32 v20, 0x20000, v12
Mv 68,68,100
v_add_u32 v21, 0x20000, v13
v_add_u32 v22, 0x20000, v14
Mv 72,72,100
v_add_u32 v23, 0x20000, v15
v_add_u32 v16, 0x10000, v12
Mv 76,76,100
s_add_u32 s44, s43, 13056
buffer_load_dwordx4 v26, s[12:15], s44 offen offset:2048 lds
Mv 80,80,100
v_add_u32 v17, 0x10000, v13
v_add_u32 v18, 0x10000, v14
)" R"(
Mv 84,84,100
v_add_u32 v19, 0x10000, v15
Cv 32,32,33
Mv 88,88,100
Cv 33,34,35
Cv 34,36,37
Mv 92,92,100
buffer_load_dwordx4 v27, s[12:15], s44 offen offset:3072 lds
Mv 96,64,104
Cv 35,38,39
Cv 40,40,41
Mv 100,68,104
Cv 41,42,43
Cv 42,44,45
Mv 104,72,104
s_add_u32 m0, s41, 32768
s_add_u32 s92, s42, 0x1c0080
Mv 108,76,104
buffer_load_dwordx4 v8, s[16:19], s92 offen offset:0 lds
Mv 112,80,104
Cv 43,46,47
Cv 48,48,49
Mv 116,84,104
Cv 49,50,51
Cv 50,52,53
Mv 120,88,104
Cv 51,54,55
Cv 56,56,57
Mv 124,92,104
buffer_store_dwordx4 v[32:35], v2, s[20:23], s45 offen offset:0 sc0 sc1
Mv 128,64,108
Cv 57,58,59
Cv 58,60,61
Mv 132,68,108
Cv 59,62,63
Cv 64,64,65
Mv 136,72,108
Cv 65,66,67
Cv 66,68,69
Mv 140,76,108
buffer_load_dwordx4 v9, s[16:19], s92 offen offset:1024 lds
Mv 144,80,108
Cv 67,70,71
Cv 72,72,73
Mv 148,84,108
Cv 73,74,75
Cv 74,76,77
Mv 152,88,108
Cv 75,78,79
Cv 80,80,81
Mv 156,92,108
buffer_store_dwordx4 v[40:43], v2, s[20:23], s45 offen offset:1024 sc0 sc1
Mv 160,0,48
Cv 81,82,83
Cv 82,84,85
Mv 160,64,112
Cv 83,86,87
Cv 88,88,89
Mv 164,4,48
Cv 89,90,91
Cv 90,92,93
Mv 164,68,112
buffer_load_dwordx4 v10, s[16:19], s92 offen offset:2048 lds
Mv 168,8,48
)" R"(
Cv 91,94,95
Cv 96,96,97
Mv 168,72,112
Cv 97,98,99
Cv 98,100,101
Mv 172,12,48
Cv 99,102,103
Cv 104,104,105
Mv 172,76,112
buffer_store_dwordx4 v[48:51], v2, s[20:23], s45 offen offset:2048 sc0 sc1
Mv 176,16,48
Cv 105,106,107
Cv 106,108,109
Mv 176,80,112
Cv 107,110,111
Cv 112,112,113
Mv 180,20,48
Cv 113,114,115
Cv 114,116,117
Mv 180,84,112
buffer_load_dwordx4 v11, s[16:19], s92 offen offset:3072 lds
Mv 184,24,48
Cv 115,118,119
Cv 120,120,121
Mv 184,88,112
Cv 121,122,123
Cv 122,124,125
Mv 188,28,48
Cv 123,126,127
Cv 128,128,129
Mv 188,92,112
buffer_store_dwordx4 v[56:59], v2, s[20:23], s45 offen offset:3072 sc0 sc1
Mv 192,0,52
Cv 129,130,131
Cv 130,132,133
Mv 192,64,116
Cv 131,134,135
Cv 136,136,137
Mv 196,4,52
s_add_u32 m0, s41, 36864
s_add_u32 s92, s42, 0x200080
Mv 196,68,116
buffer_load_dwordx4 v8, s[16:19], s92 offen offset:0 lds
Mv 200,8,52
Cv 137,138,139
Cv 138,140,141
Mv 200,72,116
Cv 139,142,143
Cv 144,144,145
Mv 204,12,52
Cv 145,146,147
Cv 146,148,149
Mv 204,76,116
buffer_store_dwordx4 v[64:67], v3, s[20:23], s45 offen offset:0 sc0 sc1
Mv 208,16,52
Cv 147,150,151
Cv 152,152,153
Mv 208,80,116
Cv 153,154,155
Cv 154,156,157
Mv 212,20,52
Cv 155,158,159
Mv 212,84,116
buffer_load_dwordx4 v9, s[16:19], s92 offen offset:1024 lds
)" R"(
Mv 216,24,52
Mv 216,88,116
Mv 220,28,52
Mv 220,92,116
buffer_store_dwordx4 v[72:75], v3, s[20:23], s45 offen offset:1024 sc0 sc1
Mv 224,0,56
Ma 224,0,60
Ma 228,4,60
Mv 228,4,56
buffer_load_dwordx4 v10, s[16:19], s92 offen offset:2048 lds
Mv 232,8,56
Ma 232,8,60
Ma 236,12,60
Mv 236,12,56
s_waitcnt vmcnt(24)
s_barrier
Mv 240,16,56
buffer_store_dwordx4 v[80:83], v3, s[20:23], s45 offen offset:2048 sc0 sc1
Ma 240,16,60
Ma 244,20,60
Mv 244,20,56
Mv 248,24,56
Ma 248,24,60
Ma 252,28,60
Mv 252,28,56
buffer_store_dwordx4 v[88:91], v3, s[20:23], s45 offen offset:3072 sc0 sc1
Mv 224,64,120
Rt 0,20,0
Mv 228,68,120
Rt 2,20,512
Mv 232,72,120
Rt 4,21,0
Mv 236,76,120
Rt 6,21,512
Mv 240,80,120
Rt 8,22,0
Mv 244,84,120
Rt 10,22,512
Mv 248,88,120
Rt 12,23,0
Mv 252,92,120
Rt 14,23,512
Ma 224,64,124
Rt 16,20,4096
Ma 228,68,124
Rt 18,20,4608
Ma 232,72,124
Rt 20,21,4096
Ma 236,76,124
Rt 22,21,4608
Ma 240,80,124
Rt 24,22,4096
Ma 244,84,124
Rt 26,22,4608
Ma 248,88,124
Rt 28,23,4096
Ma 252,92,124
Rt 30,23,4608
s_waitcnt vmcnt(15) lgkmcnt(8)
s_branch .LzQA
.LzPAVX:
Mv 32,32,0
Rt 64,18,16384
Mv 36,36,0
Rb 192,28,32768
)" R"(
Mv 96,40,0
Rt 66,18,18432
Mv 100,44,0
s_add_u32 s39, s39, 0x40000
Rb 196,28,34816
Mv 160,48,0
Rt 68,19,16384
s_add_u32 m0, s37, 49152
Mv 164,52,0
buffer_load_dwordx4 v12, s[8:11], s39 offen offset:0 lds
Mv 224,56,0
Rt 70,19,18432
Mv 228,60,0
Rb 200,29,32768
Mv 236,60,4
Rt 72,20,16384
Mv 232,56,4
Rb 204,29,34816
Mv 172,52,4
Rt 74,20,18432
Mv 168,48,4
Rb 208,28,36864
Mv 108,44,4
Rt 76,21,16384
Mv 104,40,4
buffer_load_dwordx4 v13, s[8:11], s39 offen offset:1024 lds
Mv 44,36,4
Rt 78,21,18432
Mv 40,32,4
Rb 212,28,38912
Mv 48,32,8
Rt 80,22,16384
Mv 52,36,8
Rb 216,29,36864
Mv 112,40,8
Rt 82,22,18432
Mv 116,44,8
Rb 220,29,38912
Mv 176,48,8
Rt 84,23,16384
Mv 180,52,8
buffer_load_dwordx4 v14, s[8:11], s39 offen offset:2048 lds
Mv 240,56,8
Rt 86,23,18432
Mv 244,60,8
Mv 252,60,12
Rt 88,24,16384
Mv 248,56,12
Mv 188,52,12
Rt 90,24,18432
Mv 184,48,12
Mv 124,44,12
Rt 92,25,16384
Mv 120,40,12
buffer_load_dwordx4 v15, s[8:11], s39 offen offset:3072 lds
Mv 60,36,12
Rt 94,25,18432
Mv 56,32,12
Mv 64,32,16
Rb 96,17,0
Mv 68,36,16
Mv 128,40,16
Rb 100,17,2048
Mv 132,44,16
)" R"(
s_add_u32 s39, s39, 0x40000
Mv 192,48,16
Rb 104,17,8192
s_add_u32 m0, s37, 0
Mv 196,52,16
buffer_load_dwordx4 v12, s[8:11], s39 offen offset:0 lds
Ma 224,56,16
Rb 108,17,10240
Ma 228,60,16
Ma 236,60,20
Rb 112,17,16384
Ma 232,56,20
Mv 204,52,20
Rb 116,17,18432
Mv 200,48,20
Mv 140,44,20
Rb 120,17,24576
Mv 136,40,20
buffer_load_dwordx4 v13, s[8:11], s39 offen offset:1024 lds
Mv 76,36,20
Rb 124,17,26624
Mv 72,32,20
Mv 80,32,24
Mv 84,36,24
Mv 144,40,24
Mv 148,44,24
Mv 208,48,24
Mv 212,52,24
buffer_load_dwordx4 v14, s[8:11], s39 offen offset:2048 lds
Ma 240,56,24
Ma 244,60,24
Ma 252,60,28
Ma 248,56,28
Mv 220,52,28
Mv 216,48,28
Mv 156,44,28
Mv 152,40,28
buffer_load_dwordx4 v15, s[8:11], s39 offen offset:3072 lds
Mv 92,36,28
Mv 88,32,28
s_waitcnt vmcnt(8) lgkmcnt(0)
s_barrier
s_add_u32 m0, s36, 131072
s_add_u32 s44, s43, 256
buffer_load_dwordx4 v26, s[12:15], s44 offen offset:0 lds
buffer_load_dwordx4 v27, s[12:15], s44 offen offset:1024 lds
s_add_u32 s44, s43, 12544
buffer_load_dwordx4 v26, s[12:15], s44 offen offset:2048 lds
buffer_load_dwordx4 v27, s[12:15], s44 offen offset:3072 lds
Mv 32,96,64
Rt 0,18,32768
Mv 36,100,64
s_add_u32 s38, s38, 0xffff0080
Mv 96,104,64
Rt 2,18,34816
s_add_u32 m0, s36, 0
Mv 100,108,64
buffer_load_dwordx4 v8, s[4:7], s38 offen offset:0 lds
Mv 160,112,64
Rt 4,19,32768
Mv 164,116,64
Mv 224,120,64
Rt 6,19,34816
Mv 228,124,64
buffer_load_dwordx4 v9, s[4:7], s38 offen offset:1024 lds
)" R"(
Mv 236,124,68
Rt 8,20,32768
Mv 232,120,68
Mv 172,116,68
Rt 10,20,34816
Mv 168,112,68
buffer_load_dwordx4 v10, s[4:7], s38 offen offset:2048 lds
Mv 108,108,68
Rt 12,21,32768
Mv 104,104,68
Mv 44,100,68
Rt 14,21,34816
Mv 40,96,68
buffer_load_dwordx4 v11, s[4:7], s38 offen offset:3072 lds
Mv 48,96,72
Rt 16,22,32768
Mv 52,100,72
s_add_u32 s38, s38, 0x10000
Mv 112,104,72
Rt 18,22,34816
s_add_u32 m0, s36, 4096
Mv 116,108,72
buffer_load_dwordx4 v8, s[4:7], s38 offen offset:0 lds
Mv 176,112,72
Rt 20,23,32768
Mv 180,116,72
Mv 240,120,72
Rt 22,23,34816
Mv 244,124,72
buffer_load_dwordx4 v9, s[4:7], s38 offen offset:1024 lds
Mv 252,124,76
Rt 24,24,32768
Mv 248,120,76
Mv 188,116,76
Rt 26,24,34816
Mv 184,112,76
buffer_load_dwordx4 v10, s[4:7], s38 offen offset:2048 lds
Mv 124,108,76
Rt 28,25,32768
Mv 120,104,76
Mv 60,100,76
Rt 30,25,34816
Mv 56,96,76
buffer_load_dwordx4 v11, s[4:7], s38 offen offset:3072 lds
Mv 64,96,80
Rb 32,16,32768
Mv 68,100,80
Mv 128,104,80
Rb 36,16,34816
Mv 132,108,80
s_add_u32 s44, s43, 384
Mv 192,112,80
Rb 40,16,40960
s_add_u32 m0, s36, 135168
Mv 196,116,80
buffer_load_dwordx4 v26, s[12:15], s44 offen offset:0 lds
Ma 224,120,80
Rb 44,16,43008
Ma 228,124,80
Ma 236,124,84
Rb 48,16,49152
Ma 232,120,84
buffer_load_dwordx4 v27, s[12:15], s44 offen offset:1024 lds
Mv 204,116,84
)" R"(
Rb 52,16,51200
Mv 200,112,84
Mv 140,108,84
Rb 56,16,57344
Mv 136,104,84
s_add_u32 s44, s43, 12672
buffer_load_dwordx4 v26, s[12:15], s44 offen offset:2048 lds
Mv 76,100,84
Rb 60,16,59392
Mv 72,96,84
Mv 80,96,88
Mv 84,100,88
buffer_load_dwordx4 v27, s[12:15], s44 offen offset:3072 lds
Mv 144,104,88
Mv 148,108,88
Mv 208,112,88
Mv 212,116,88
Ma 240,120,88
Ma 244,124,88
Ma 252,124,92
Ma 248,120,92
Mv 220,116,92
Mv 216,112,92
Mv 156,108,92
Mv 152,104,92
Mv 92,100,92
Mv 88,96,92
s_waitcnt vmcnt(0) lgkmcnt(0)
s_barrier
Mv 32,32,0
Rt 64,18,49152
Mv 36,36,0
Rb 128,28,32768
Mv 96,40,0
Rt 66,18,51200
Mv 100,44,0
s_add_u32 s39, s39, 0x40000
Rb 132,28,34816
Mv 160,48,0
Rt 68,19,49152
s_add_u32 m0, s37, 16384
Mv 164,52,0
buffer_load_dwordx4 v12, s[8:11], s39 offen offset:0 lds
Mv 224,56,0
Rt 70,19,51200
Mv 228,60,0
Rb 136,29,32768
Mv 236,60,4
Rt 72,20,49152
Mv 232,56,4
Rb 140,29,34816
Mv 172,52,4
Rt 74,20,51200
Mv 168,48,4
Rb 144,28,36864
Mv 108,44,4
Rt 76,21,49152
Mv 104,40,4
buffer_load_dwordx4 v13, s[8:11], s39 offen offset:1024 lds
Mv 44,36,4
Rt 78,21,51200
Mv 40,32,4
Rb 148,28,38912
Mv 48,32,8
Rt 80,22,49152
)" R"(
Mv 52,36,8
Rb 152,29,36864
Mv 112,40,8
Rt 82,22,51200
Mv 116,44,8
Rb 156,29,38912
Mv 176,48,8
Rt 84,23,49152
Mv 180,52,8
buffer_load_dwordx4 v14, s[8:11], s39 offen offset:2048 lds
Mv 240,56,8
Rt 86,23,51200
Mv 244,60,8
Mv 252,60,12
Rt 88,24,49152
Mv 248,56,12
Mv 188,52,12
Rt 90,24,51200
Mv 184,48,12
Mv 124,44,12
Rt 92,25,49152
Mv 120,40,12
buffer_load_dwordx4 v15, s[8:11], s39 offen offset:3072 lds
Mv 60,36,12
Rt 94,25,51200
Mv 56,32,12
Mv 64,32,16
Rb 96,17,32768
Mv 68,36,16
Mv 128,40,16
Rb 100,17,34816
Mv 132,44,16
s_add_u32 s39, s39, 0x40000
Mv 192,48,16
Rb 104,17,40960
s_add_u32 m0, s37, 32768
Mv 196,52,16
buffer_load_dwordx4 v12, s[8:11], s39 offen offset:0 lds
Ma 224,56,16
Rb 108,17,43008
Ma 228,60,16
Ma 236,60,20
Rb 112,17,49152
Ma 232,56,20
Mv 204,52,20
Rb 116,17,51200
Mv 200,48,20
Mv 140,44,20
Rb 120,17,57344
Mv 136,40,20
buffer_load_dwordx4 v13, s[8:11], s39 offen offset:1024 lds
Mv 76,36,20
Rb 124,17,59392
Mv 72,32,20
Mv 80,32,24
Mv 84,36,24
Mv 144,40,24
Mv 148,44,24
Mv 208,48,24
Mv 212,52,24
buffer_load_dwordx4 v14, s[8:11], s39 offen offset:2048 lds
Ma 240,56,24
Ma 244,60,24
Ma 252,60,28
)" R"(
Ma 248,56,28
Mv 220,52,28
Mv 216,48,28
Mv 156,44,28
Mv 152,40,28
buffer_load_dwordx4 v15, s[8:11], s39 offen offset:3072 lds
Mv 92,36,28
Mv 88,32,28
s_waitcnt lgkmcnt(0)
s_barrier
s_add_u32 m0, s36, 131072
s_add_u32 s44, s43, 512
buffer_load_dwordx4 v26, s[12:15], s44 offen offset:0 lds
buffer_load_dwordx4 v27, s[12:15], s44 offen offset:1024 lds
s_add_u32 s44, s43, 12800
buffer_load_dwordx4 v26, s[12:15], s44 offen offset:2048 lds
buffer_load_dwordx4 v27, s[12:15], s44 offen offset:3072 lds
Mv 32,96,64
Rt 0,18,0
Mv 36,100,64
s_add_u32 s38, s38, 0xffff0080
Mv 96,104,64
Rt 2,18,2048
s_add_u32 m0, s36, 32768
Mv 100,108,64
buffer_load_dwordx4 v8, s[4:7], s38 offen offset:0 lds
Mv 160,112,64
Rt 4,19,0
Mv 164,116,64
Mv 224,120,64
Rt 6,19,2048
Mv 228,124,64
buffer_load_dwordx4 v9, s[4:7], s38 offen offset:1024 lds
Mv 236,124,68
Rt 8,20,0
Mv 232,120,68
Mv 172,116,68
Rt 10,20,2048
Mv 168,112,68
buffer_load_dwordx4 v10, s[4:7], s38 offen offset:2048 lds
Mv 108,108,68
Rt 12,21,0
Mv 104,104,68
Mv 44,100,68
Rt 14,21,2048
Mv 40,96,68
buffer_load_dwordx4 v11, s[4:7], s38 offen offset:3072 lds
Mv 48,96,72
Rt 16,22,0
Mv 52,100,72
s_add_u32 s38, s38, 0x10000
Mv 112,104,72
Rt 18,22,2048
s_add_u32 m0, s36, 36864
Mv 116,108,72
buffer_load_dwordx4 v8, s[4:7], s38 offen offset:0 lds
Mv 176,112,72
Rt 20,23,0
Mv 180,116,72
Mv 240,120,72
Rt 22,23,2048
Mv 244,124,72
buffer_load_dwordx4 v9, s[4:7], s38 offen offset:1024 lds
Mv 252,124,76
Rt 24,24,0
)" R"(
Mv 248,120,76
Mv 188,116,76
Rt 26,24,2048
Mv 184,112,76
buffer_load_dwordx4 v10, s[4:7], s38 offen offset:2048 lds
Mv 124,108,76
Rt 28,25,0
Mv 120,104,76
Mv 60,100,76
Rt 30,25,2048
Mv 56,96,76
buffer_load_dwordx4 v11, s[4:7], s38 offen offset:3072 lds
Mv 64,96,80
Rb 32,16,0
Mv 68,100,80
Mv 128,104,80
Rb 36,16,2048
Mv 132,108,80
s_add_u32 s44, s43, 640
Mv 192,112,80
Rb 40,16,8192
s_add_u32 m0, s36, 135168
Mv 196,116,80
buffer_load_dwordx4 v26, s[12:15], s44 offen offset:0 lds
Ma 224,120,80
Rb 44,16,10240
Ma 228,124,80
Ma 236,124,84
Rb 48,16,16384
Ma 232,120,84
buffer_load_dwordx4 v27, s[12:15], s44 offen offset:1024 lds
Mv 204,116,84
Rb 52,16,18432
Mv 200,112,84
Mv 140,108,84
Rb 56,16,24576
Mv 136,104,84
s_add_u32 s44, s43, 12928
buffer_load_dwordx4 v26, s[12:15], s44 offen offset:2048 lds
Mv 76,100,84
Rb 60,16,26624
Mv 72,96,84
Mv 80,96,88
Mv 84,100,88
buffer_load_dwordx4 v27, s[12:15], s44 offen offset:3072 lds
Mv 144,104,88
Mv 148,108,88
Mv 208,112,88
Mv 212,116,88
Ma 240,120,88
Ma 244,124,88
Ma 252,124,92
Ma 248,120,92
Mv 220,116,92
Mv 216,112,92
Mv 156,108,92
Mv 152,104,92
Mv 92,100,92
Mv 88,96,92
s_waitcnt vmcnt(0) lgkmcnt(0)
s_barrier
Mv 32,32,0
Rt 64,18,16384
Mv 36,36,0
Rb 160,28,32768
)" R"(
v_lshrrev_b32 v5, 2, v0
Mv 96,40,0
Rt 66,18,18432
v_and_b32 v5, 8, v5
Mv 100,44,0
s_add_u32 s39, s39, 0x40000
Rb 164,28,34816
Mv 160,48,0
Rt 68,19,16384
s_add_u32 m0, s37, 49152
Mv 164,52,0
buffer_load_dwordx4 v12, s[8:11], s39 offen offset:0 lds
Mv 224,56,0
Rt 70,19,18432
v_and_or_b32 v5, v0, 7, v5
Mv 228,60,0
Rb 168,29,32768
v_lshlrev_b32 v5, 8, v5
Mv 236,60,4
Rt 72,20,16384
v_and_or_b32 v5, v0, 24, v5
Mv 232,56,4
Rb 172,29,34816
v_lshrrev_b32 v5, 1, v5
Mv 172,52,4
Rt 74,20,18432
v_mov_b32 v6, s3
Mv 168,48,4
Rb 176,28,36864
Mv 108,44,4
Rt 76,21,16384
Mv 104,40,4
buffer_load_dwordx4 v13, s[8:11], s39 offen offset:1024 lds
Mv 44,36,4
Rt 78,21,18432
Mv 40,32,4
Rb 180,28,38912
Mv 48,32,8
Rt 80,22,16384
Mv 52,36,8
Rb 184,29,36864
Mv 112,40,8
Rt 82,22,18432
Mv 116,44,8
Rb 188,29,38912
Mv 176,48,8
Rt 84,23,16384
Mv 180,52,8
buffer_load_dwordx4 v14, s[8:11], s39 offen offset:2048 lds
Mv 240,56,8
Rt 86,23,18432
Mv 244,60,8
Mv 252,60,12
Rt 88,24,16384
Mv 248,56,12
Mv 188,52,12
Rt 90,24,18432
Mv 184,48,12
Mv 124,44,12
Rt 92,25,16384
Mv 120,40,12
buffer_load_dwordx4 v15, s[8:11], s39 offen offset:3072 lds
Mv 60,36,12
Rt 94,25,18432
)" R"(
v_lshrrev_b32 v10, 3, v0
Mv 56,32,12
v_and_b32 v11, 0x70, v1
v_lshl_or_b32 v10, v10, 13, v11
Mv 64,32,16
Rb 96,17,0
v_lshlrev_b32 v11, 1, v0
Mv 68,36,16
v_and_b32 v11, 32, v11
v_xor_b32 v10, v10, v11
Mv 128,40,16
Rb 100,17,2048
s_and_b32 s28, s33, 1
Mv 132,44,16
s_lshl_b32 s28, s28, 6
v_xor_b32 v8, s28, v10
Mv 192,48,16
Rb 104,17,8192
v_xor_b32 v9, 64, v8
Mv 196,52,16
v_add_u32 v9, 0xfc00, v9
v_add_u32 v10, 0x1f800, v8
Ma 224,56,16
Rb 108,17,10240
v_add_u32 v11, 0x1f800, v9
Ma 228,60,16
Ma 236,60,20
Rb 112,17,16384
Ma 232,56,20
Mv 204,52,20
Rb 116,17,18432
Mv 200,48,20
Mv 140,44,20
Rb 120,17,24576
Mv 136,40,20
Mv 76,36,20
Rb 124,17,26624
Mv 72,32,20
Mv 80,32,24
Mv 84,36,24
Mv 144,40,24
Mv 148,44,24
Mv 208,48,24
Mv 212,52,24
Ma 240,56,24
Ma 244,60,24
Ma 252,60,28
Ma 248,56,28
Mv 220,52,28
Mv 216,48,28
Mv 156,44,28
Mv 152,40,28
Mv 92,36,28
Mv 88,32,28
s_waitcnt lgkmcnt(0)
s_barrier
Mv 32,96,64
Rt 0,18,32768
Mv 36,100,64
s_add_u32 s92, s42, 0x0
Mv 96,104,64
Rt 2,18,34816
s_add_u32 m0, s41, 131072
Mv 100,108,64
buffer_load_dwordx4 v8, s[16:19], s92 offen offset:0 lds
)" R"(
Mv 160,112,64
Rt 4,19,32768
Mv 164,116,64
Mv 224,120,64
Rt 6,19,34816
Mv 228,124,64
buffer_load_dwordx4 v9, s[16:19], s92 offen offset:1024 lds
Mv 236,124,68
Rt 8,20,32768
Mv 232,120,68
Mv 172,116,68
Rt 10,20,34816
Mv 168,112,68
buffer_load_dwordx4 v10, s[16:19], s92 offen offset:2048 lds
Mv 108,108,68
Rt 12,21,32768
Mv 104,104,68
Mv 44,100,68
Rt 14,21,34816
Mv 40,96,68
buffer_load_dwordx4 v11, s[16:19], s92 offen offset:3072 lds
Mv 48,96,72
Rt 16,22,32768
Mv 52,100,72
s_add_u32 s92, s42, 0x40000
Mv 112,104,72
Rt 18,22,34816
s_add_u32 m0, s41, 135168
Mv 116,108,72
buffer_load_dwordx4 v8, s[16:19], s92 offen offset:0 lds
Mv 176,112,72
Rt 20,23,32768
Mv 180,116,72
Mv 240,120,72
Rt 22,23,34816
Mv 244,124,72
buffer_load_dwordx4 v9, s[16:19], s92 offen offset:1024 lds
Mv 252,124,76
Rt 24,24,32768
Mv 248,120,76
Mv 188,116,76
Rt 26,24,34816
Mv 184,112,76
buffer_load_dwordx4 v10, s[16:19], s92 offen offset:2048 lds
Mv 124,108,76
Rt 28,25,32768
Mv 120,104,76
s_add_u32 s92, s42, 0x1c0000
Mv 60,100,76
Rt 30,25,34816
s_add_u32 m0, s41, 0
Mv 56,96,76
buffer_load_dwordx4 v8, s[16:19], s92 offen offset:0 lds
Mv 64,96,80
Rb 32,16,32768
Mv 68,100,80
Mv 128,104,80
Rb 36,16,34816
Mv 132,108,80
buffer_load_dwordx4 v9, s[16:19], s92 offen offset:1024 lds
Mv 192,112,80
Rb 40,16,40960
Mv 196,116,80
Ma 224,120,80
)" R"(
Rb 44,16,43008
Ma 228,124,80
buffer_load_dwordx4 v10, s[16:19], s92 offen offset:2048 lds
Ma 236,124,84
Rb 48,16,49152
Ma 232,120,84
Mv 204,116,84
Rb 52,16,51200
Mv 200,112,84
buffer_load_dwordx4 v11, s[16:19], s92 offen offset:3072 lds
Mv 140,108,84
Rb 56,16,57344
Mv 136,104,84
s_add_u32 s92, s42, 0x200000
Mv 76,100,84
Rb 60,16,59392
s_add_u32 m0, s41, 4096
Mv 72,96,84
buffer_load_dwordx4 v8, s[16:19], s92 offen offset:0 lds
Mv 80,96,88
Mv 84,100,88
Mv 144,104,88
Mv 148,108,88
buffer_load_dwordx4 v9, s[16:19], s92 offen offset:1024 lds
Mv 208,112,88
Mv 212,116,88
Ma 240,120,88
Ma 244,124,88
buffer_load_dwordx4 v10, s[16:19], s92 offen offset:2048 lds
Ma 252,124,92
Ma 248,120,92
Mv 220,116,92
Mv 216,112,92
Mv 156,108,92
Mv 152,104,92
Mv 92,100,92
Mv 88,96,92
s_waitcnt vmcnt(14) lgkmcnt(4)
s_barrier
Mv 32,32,0
Rt 64,18,49152
Mv 36,36,0
Rt 66,18,51200
s_add_u32 s92, s42, 0x80
Mv 96,40,0
Rt 68,19,49152
s_add_u32 m0, s41, 65536
Mv 100,44,0
Rt 70,19,51200
buffer_load_dwordx4 v8, s[16:19], s92 offen offset:0 lds
Mv 108,44,4
Rt 72,20,49152
Mv 104,40,4
Rt 74,20,51200
Mv 44,36,4
Rt 76,21,49152
Mv 40,32,4
Rt 78,21,51200
buffer_load_dwordx4 v9, s[16:19], s92 offen offset:1024 lds
Mv 48,32,8
Rt 80,22,49152
Mv 52,36,8
Rt 82,22,51200
Mv 112,40,8
Rt 84,23,49152
)" R"(
Mv 116,44,8
Rt 86,23,51200
buffer_load_dwordx4 v10, s[16:19], s92 offen offset:2048 lds
Mv 124,44,12
Rt 88,24,49152
Mv 120,40,12
Rt 90,24,51200
Mv 60,36,12
Rt 92,25,49152
Mv 56,32,12
Rt 94,25,51200
buffer_load_dwordx4 v11, s[16:19], s92 offen offset:3072 lds
Mv 64,32,16
Rb 96,17,32768
Mv 68,36,16
Rb 100,17,34816
s_add_u32 s92, s42, 0x40080
Mv 128,40,16
Rb 104,17,40960
s_add_u32 m0, s41, 69632
Mv 132,44,16
Rb 108,17,43008
buffer_load_dwordx4 v8, s[16:19], s92 offen offset:0 lds
Mv 140,44,20
Rb 112,17,49152
Mv 136,40,20
Rb 116,17,51200
Mv 76,36,20
Rb 120,17,57344
Mv 72,32,20
Rb 124,17,59392
buffer_load_dwordx4 v9, s[16:19], s92 offen offset:1024 lds
Mv 80,32,24
Mv 84,36,24
Mv 144,40,24
Mv 148,44,24
buffer_load_dwordx4 v10, s[16:19], s92 offen offset:2048 lds
Mv 156,44,28
Mv 152,40,28
Mv 92,36,28
Mv 88,32,28
s_waitcnt lgkmcnt(0)
s_barrier
Mv 32,96,64
v_bfe_u32 v12, v0, 3, 2
v_lshlrev_b32 v12, 5, v12
Mv 36,100,64
v_and_b32 v4, 3, v0
v_lshl_or_b32 v12, v4, 3, v12
Mv 40,96,68
s_add_u32 m0, s36, 98304
s_add_u32 s44, s43, 768
Mv 44,100,68
buffer_load_dwordx4 v26, s[12:15], s44 offen offset:0 lds
Mv 48,96,72
v_bfe_u32 v4, v0, 2, 2
v_lshl_or_b32 v12, v4, 7, v12
Mv 52,100,72
v_lshrrev_b32 v4, 4, v0
v_lshl_or_b32 v12, v4, 10, v12
Mv 56,96,76
v_xor_b32 v13, 32, v12
v_xor_b32 v14, 64, v12
Mv 60,100,76
buffer_load_dwordx4 v27, s[12:15], s44 offen offset:1024 lds
)" R"(
Mv 64,96,80
v_xor_b32 v15, 96, v12
v_add_u32 v20, 0x20000, v12
Mv 68,100,80
v_add_u32 v21, 0x20000, v13
v_add_u32 v22, 0x20000, v14
Mv 72,96,84
v_add_u32 v23, 0x20000, v15
v_add_u32 v16, 0x10000, v12
Mv 76,100,84
s_add_u32 s44, s43, 13056
buffer_load_dwordx4 v26, s[12:15], s44 offen offset:2048 lds
Mv 80,96,88
v_add_u32 v17, 0x10000, v13
v_add_u32 v18, 0x10000, v14
Mv 84,100,88
v_add_u32 v19, 0x10000, v15
Cv 32,32,33
Mv 88,96,92
Cv 33,34,35
Cv 34,36,37
Mv 92,100,92
buffer_load_dwordx4 v27, s[12:15], s44 offen offset:3072 lds
Mv 96,104,64
Cv 35,38,39
Cv 40,40,41
Mv 100,108,64
Cv 41,42,43
Cv 42,44,45
Mv 104,104,68
s_add_u32 m0, s41, 32768
s_add_u32 s92, s42, 0x1c0080
Mv 108,108,68
buffer_load_dwordx4 v8, s[16:19], s92 offen offset:0 lds
Mv 112,104,72
Cv 43,46,47
Cv 48,48,49
Mv 116,108,72
Cv 49,50,51
Cv 50,52,53
Mv 120,104,76
Cv 51,54,55
Cv 56,56,57
Mv 124,108,76
buffer_store_dwordx4 v[32:35], v2, s[20:23], s45 offen offset:0 sc0 sc1
Mv 128,104,80
Cv 57,58,59
Cv 58,60,61
Mv 132,108,80
Cv 59,62,63
Cv 64,64,65
Mv 136,104,84
Cv 65,66,67
Cv 66,68,69
Mv 140,108,84
buffer_load_dwordx4 v9, s[16:19], s92 offen offset:1024 lds
Mv 144,104,88
Cv 67,70,71
Cv 72,72,73
Mv 148,108,88
Cv 73,74,75
Cv 74,76,77
Mv 152,104,92
Cv 75,78,79
)" R"(
Cv 80,80,81
Mv 156,108,92
buffer_store_dwordx4 v[40:43], v2, s[20:23], s45 offen offset:1024 sc0 sc1
Mv 160,48,0
Cv 81,82,83
Cv 82,84,85
Mv 160,112,64
Cv 83,86,87
Cv 88,88,89
Mv 164,52,0
Cv 89,90,91
Cv 90,92,93
Mv 164,116,64
buffer_load_dwordx4 v10, s[16:19], s92 offen offset:2048 lds
Mv 168,48,4
Cv 91,94,95
Cv 96,96,97
Mv 168,112,68
Cv 97,98,99
Cv 98,100,101
Mv 172,52,4
Cv 99,102,103
Cv 104,104,105
Mv 172,116,68
buffer_store_dwordx4 v[48:51], v2, s[20:23], s45 offen offset:2048 sc0 sc1
Mv 176,48,8
Cv 105,106,107
Cv 106,108,109
Mv 176,112,72
Cv 107,110,111
Cv 112,112,113
Mv 180,52,8
Cv 113,114,115
Cv 114,116,117
Mv 180,116,72
buffer_load_dwordx4 v11, s[16:19], s92 offen offset:3072 lds
Mv 184,48,12
Cv 115,118,119
Cv 120,120,121
Mv 184,112,76
Cv 121,122,123
Cv 122,124,125
Mv 188,52,12
Cv 123,126,127
Cv 128,128,129
Mv 188,116,76
buffer_store_dwordx4 v[56:59], v2, s[20:23], s45 offen offset:3072 sc0 sc1
Mv 192,48,16
Cv 129,130,131
Cv 130,132,133
Mv 192,112,80
Cv 131,134,135
Cv 136,136,137
Mv 196,52,16
s_add_u32 m0, s41, 36864
s_add_u32 s92, s42, 0x200080
Mv 196,116,80
buffer_load_dwordx4 v8, s[16:19], s92 offen offset:0 lds
Mv 200,48,20
Cv 137,138,139
Cv 138,140,141
Mv 200,112,84
Cv 139,142,143
Cv 144,144,145
)" R"(
Mv 204,52,20
Cv 145,146,147
Cv 146,148,149
Mv 204,116,84
buffer_store_dwordx4 v[64:67], v3, s[20:23], s45 offen offset:0 sc0 sc1
Mv 208,48,24
Cv 147,150,151
Cv 152,152,153
Mv 208,112,88
Cv 153,154,155
Cv 154,156,157
Mv 212,52,24
Cv 155,158,159
Mv 212,116,88
buffer_load_dwordx4 v9, s[16:19], s92 offen offset:1024 lds
Mv 216,48,28
Mv 216,112,92
Mv 220,52,28
Mv 220,116,92
buffer_store_dwordx4 v[72:75], v3, s[20:23], s45 offen offset:1024 sc0 sc1
Mv 224,56,0
Mv 228,60,0
Mv 236,60,4
Mv 232,56,4
buffer_load_dwordx4 v10, s[16:19], s92 offen offset:2048 lds
Mv 240,56,8
Mv 244,60,8
Mv 252,60,12
Mv 248,56,12
s_waitcnt vmcnt(24)
s_barrier
Ma 224,56,16
buffer_store_dwordx4 v[80:83], v3, s[20:23], s45 offen offset:2048 sc0 sc1
Ma 228,60,16
Ma 236,60,20
Ma 232,56,20
Ma 240,56,24
Ma 244,60,24
Ma 252,60,28
Ma 248,56,28
buffer_store_dwordx4 v[88:91], v3, s[20:23], s45 offen offset:3072 sc0 sc1
Mv 224,120,64
Rt 0,20,0
Mv 228,124,64
Rt 2,20,512
Mv 232,120,68
Rt 4,21,0
Mv 236,124,68
Rt 6,21,512
Mv 240,120,72
Rt 8,22,0
Mv 244,124,72
Rt 10,22,512
Mv 248,120,76
Rt 12,23,0
Mv 252,124,76
Rt 14,23,512
Ma 224,120,80
Rt 16,20,4096
Ma 228,124,80
Rt 18,20,4608
Ma 232,120,84
Rt 20,21,4096
Ma 236,124,84
Rt 22,21,4608
)" R"(
Ma 240,120,88
Rt 24,22,4096
Ma 244,124,88
Rt 26,22,4608
Ma 248,120,92
Rt 28,23,4096
Ma 252,124,92
Rt 30,23,4608
s_waitcnt vmcnt(15) lgkmcnt(8)
.LzQA:
s_cmp_eq_u32 s2, 112
s_cbranch_scc1 .LzT2S
.LzT2E:
Mz 32,0,192
Rt 32,20,8192
Cv 224,224,225
Mz 40,0,196
Rt 34,20,8704
Rb 48,28,0
Mz 44,4,196
Rt 36,21,8192
buffer_store_dwordx4 v[96:99], v2, s[20:23], s46 offen offset:0 sc0 sc1
Mz 36,4,192
Rt 38,21,8704
Rb 52,28,2048
Cv 225,226,227
Mz 48,8,192
Rt 40,22,8192
Cv 226,228,229
Mz 56,8,196
Rt 42,22,8704
Rb 56,29,0
Mz 60,12,196
Rt 44,23,8192
Cv 227,230,231
Cv 232,232,233
Mz 52,12,192
Rt 46,23,8704
Rb 60,29,2048
s_waitcnt lgkmcnt(12)
Mv 32,16,200
Rt 0,20,12288
Cv 233,234,235
Mv 40,16,204
Rt 2,20,12800
Cv 234,236,237
Cv 235,238,239
Mv 44,20,204
Rt 4,21,12288
Cv 240,240,241
Mv 36,20,200
Rt 6,21,12800
Cv 241,242,243
Mv 48,24,200
Rt 8,22,12288
buffer_store_dwordx4 v[104:107], v2, s[20:23], s46 offen offset:1024 sc0 sc1
Mv 56,24,204
Rt 10,22,12800
Cv 242,244,245
Mv 60,28,204
Rt 12,23,12288
Cv 243,246,247
Mv 52,28,200
Rt 14,23,12800
)" R"(
Cv 248,248,249
s_waitcnt lgkmcnt(9)
Mv 32,32,208
Rt 16,20,16384
Cv 249,250,251
Mv 40,32,212
Rt 18,20,16896
Cv 250,252,253
Mv 44,36,212
Rt 20,21,16384
Cv 251,254,255
Mv 36,36,208
Rt 22,21,16896
v_accvgpr_read_b32 v228, a224
v_accvgpr_read_b32 v24, a225
Mv 48,40,208
Rt 24,22,16384
Cv 228,228,24
Mv 56,40,212
Rt 26,22,16896
v_accvgpr_read_b32 v229, a226
Mv 60,44,212
Rt 28,23,16384
buffer_store_dwordx4 v[112:115], v2, s[20:23], s46 offen offset:2048 sc0 sc1
Mv 52,44,208
Rt 30,23,16896
s_waitcnt lgkmcnt(8)
Mv 32,0,216
Rt 32,20,20480
v_accvgpr_read_b32 v25, a227
Mv 40,0,220
Rt 34,20,20992
Cv 229,229,25
v_accvgpr_read_b32 v230, a228
Mv 44,4,220
Rt 36,21,20480
v_accvgpr_read_b32 v24, a229
Mv 36,4,216
Rt 38,21,20992
Cv 230,230,24
Mv 48,8,216
Rt 40,22,20480
v_accvgpr_read_b32 v231, a230
v_accvgpr_read_b32 v25, a231
Mv 56,8,220
Rt 42,22,20992
Cv 231,231,25
Mv 60,12,220
Rt 44,23,20480
v_accvgpr_read_b32 v236, a232
Mv 52,12,216
Rt 46,23,20992
v_accvgpr_read_b32 v24, a233
s_waitcnt lgkmcnt(8)
Mv 32,16,128
Rt 0,20,24576
buffer_store_dwordx4 v[120:123], v2, s[20:23], s46 offen offset:3072 sc0 sc1
Mv 40,16,132
Rt 2,20,25088
Cv 236,236,24
Mv 44,20,132
Rt 4,21,24576
v_accvgpr_read_b32 v237, a234
Mv 36,20,128
)" R"(
Rt 6,21,25088
v_accvgpr_read_b32 v25, a235
Cv 237,237,25
Mv 48,24,128
Rt 8,22,24576
v_accvgpr_read_b32 v238, a236
Mv 56,24,132
Rt 10,22,25088
v_accvgpr_read_b32 v24, a237
Mv 60,28,132
Rt 12,23,24576
Cv 238,238,24
v_accvgpr_read_b32 v239, a238
Mv 52,28,128
Rt 14,23,25088
s_waitcnt lgkmcnt(8)
Mv 32,32,136
Rt 16,12,0
v_accvgpr_read_b32 v25, a239
Mv 40,32,140
Rt 18,12,512
Cv 239,239,25
v_accvgpr_read_b32 v244, a240
Mv 44,36,140
Rt 20,13,0
buffer_store_dwordx4 v[128:131], v3, s[20:23], s46 offen offset:0 sc0 sc1
Mv 36,36,136
Rt 22,13,512
v_accvgpr_read_b32 v24, a241
Mv 48,40,136
Rt 24,14,0
Cv 244,244,24
v_accvgpr_read_b32 v245, a242
Mv 56,40,140
Rt 26,14,512
v_accvgpr_read_b32 v25, a243
Mv 60,44,140
Rt 28,15,0
Cv 245,245,25
Mv 52,44,136
Rt 30,15,512
s_waitcnt vmcnt(5) lgkmcnt(8)
s_barrier
buffer_store_dword v6, v7, s[20:23], s49 offen offset:0 sc0 sc1
Mv 32,0,144
Rt 32,12,4096
v_accvgpr_read_b32 v246, a244
Mv 40,0,148
Rt 34,12,4608
s_add_u32 s92, s42, 0x100
Mv 44,4,148
Rt 36,13,4096
s_add_u32 m0, s41, 98304
Mv 36,4,144
Rt 38,13,4608
buffer_load_dwordx4 v8, s[16:19], s92 offen offset:0 lds
Mv 48,8,144
Rt 40,14,4096
buffer_store_dwordx4 v[136:139], v3, s[20:23], s46 offen offset:1024 sc0 sc1
Mv 56,8,148
Rt 42,14,4608
v_accvgpr_read_b32 v24, a245
Mv 60,12,148
Rt 44,15,4096
Cv 246,246,24
)" R"(
v_accvgpr_read_b32 v247, a246
Mv 52,12,144
Rt 46,15,4608
buffer_load_dwordx4 v9, s[16:19], s92 offen offset:1024 lds
s_waitcnt lgkmcnt(8)
Mv 32,16,152
Rt 0,12,8192
v_accvgpr_read_b32 v25, a247
Mv 40,16,156
Rt 2,12,8704
Cv 247,247,25
v_accvgpr_read_b32 v252, a248
Mv 44,20,156
Rt 4,13,8192
v_accvgpr_read_b32 v24, a249
Mv 36,20,152
Rt 6,13,8704
buffer_load_dwordx4 v10, s[16:19], s92 offen offset:2048 lds
Mv 48,24,152
Rt 8,14,8192
Cv 252,252,24
v_accvgpr_read_b32 v253, a250
Mv 56,24,156
Rt 10,14,8704
v_accvgpr_read_b32 v25, a251
Mv 60,28,156
Rt 12,15,8192
buffer_store_dwordx4 v[144:147], v3, s[20:23], s46 offen offset:2048 sc0 sc1
Mv 52,28,152
Rt 14,15,8704
buffer_load_dwordx4 v11, s[16:19], s92 offen offset:3072 lds
s_waitcnt lgkmcnt(8)
Mv 32,32,160
Rt 16,12,12288
Cv 253,253,25
Mv 40,32,164
Rt 18,12,12800
s_add_u32 s92, s42, 0x40100
Mv 44,36,164
Rt 20,13,12288
s_add_u32 m0, s41, 102400
Mv 36,36,160
Rt 22,13,12800
buffer_load_dwordx4 v8, s[16:19], s92 offen offset:0 lds
Mv 48,40,160
Rt 24,14,12288
v_accvgpr_read_b32 v254, a252
Mv 56,40,164
Rt 26,14,12800
v_accvgpr_read_b32 v24, a253
Mv 60,44,164
Rt 28,15,12288
Cv 254,254,24
v_accvgpr_read_b32 v255, a254
Mv 52,44,160
Rt 30,15,12800
buffer_load_dwordx4 v9, s[16:19], s92 offen offset:1024 lds
s_waitcnt lgkmcnt(8)
Mv 32,0,168
Rt 32,12,16384
buffer_store_dwordx4 v[152:155], v3, s[20:23], s46 offen offset:3072 sc0 sc1
Mv 40,0,172
Rt 34,12,16896
v_accvgpr_read_b32 v25, a255
)" R"(
Cv 255,255,25
Mv 44,4,172
Rt 36,13,16384
Cv 160,160,161
Mv 36,4,168
Rt 38,13,16896
buffer_load_dwordx4 v10, s[16:19], s92 offen offset:2048 lds
Mv 48,8,168
Rt 40,14,16384
Cv 161,162,163
Cv 162,164,165
Mv 56,8,172
Rt 42,14,16896
s_add_u32 s92, s42, 0x1c0100
Mv 60,12,172
Rt 44,15,16384
s_add_u32 m0, s41, 131072
Mv 52,12,168
Rt 46,15,16896
buffer_load_dwordx4 v8, s[16:19], s92 offen offset:0 lds
s_waitcnt lgkmcnt(8)
Mv 32,16,176
Rt 0,12,20480
Cv 163,166,167
Mv 40,16,180
Rt 2,12,20992
Cv 168,168,169
Mv 44,20,180
Rt 4,13,20480
buffer_store_dwordx4 v[160:163], v2, s[20:23], s47 offen offset:0 sc0 sc1
Mv 36,20,176
Rt 6,13,20992
buffer_load_dwordx4 v9, s[16:19], s92 offen offset:1024 lds
Mv 48,24,176
Rt 8,14,20480
Cv 169,170,171
Mv 56,24,180
Rt 10,14,20992
Cv 170,172,173
Mv 60,28,180
Rt 12,15,20480
Cv 171,174,175
Cv 176,176,177
Mv 52,28,176
Rt 14,15,20992
buffer_load_dwordx4 v10, s[16:19], s92 offen offset:2048 lds
s_waitcnt lgkmcnt(8)
Mv 32,32,184
Rt 16,12,24576
Cv 177,178,179
Mv 40,32,188
Rt 18,12,25088
Cv 178,180,181
Cv 179,182,183
Mv 44,36,188
Rt 20,13,24576
Cv 184,184,185
Mv 36,36,184
Rt 22,13,25088
buffer_load_dwordx4 v11, s[16:19], s92 offen offset:3072 lds
Mv 48,40,184
Rt 24,14,24576
Cv 185,186,187
Cv 186,188,189
)" R"(
Mv 56,40,188
Rt 26,14,25088
s_add_u32 s92, s42, 0x200100
Mv 60,44,188
Rt 28,15,24576
s_add_u32 m0, s41, 135168
Mv 52,44,184
Rt 30,15,25088
buffer_load_dwordx4 v8, s[16:19], s92 offen offset:0 lds
s_waitcnt lgkmcnt(8)
Mv 32,0,48
Rt 32,16,0
Cv 187,190,191
Mv 40,0,52
Rt 34,16,512
Cv 192,192,193
Mv 44,4,52
Rt 36,17,0
buffer_store_dwordx4 v[168:171], v2, s[20:23], s47 offen offset:1024 sc0 sc1
Mv 36,4,48
Rt 38,17,512
buffer_load_dwordx4 v9, s[16:19], s92 offen offset:1024 lds
Mv 48,8,48
Rt 40,18,0
Cv 193,194,195
Mv 56,8,52
Rt 42,18,512
Cv 194,196,197
Mv 60,12,52
Rt 44,19,0
Cv 195,198,199
Cv 200,200,201
Mv 52,12,48
Rt 46,19,512
buffer_load_dwordx4 v10, s[16:19], s92 offen offset:2048 lds
s_waitcnt vmcnt(10) lgkmcnt(8)
s_barrier
buffer_store_dword v6, v7, s[20:23], s49 offen offset:4 sc0 sc1
Mv 32,16,56
Rt 0,16,4096
Cv 201,202,203
Mv 40,16,60
Rt 2,16,4608
Cv 202,204,205
Cv 203,206,207
Mv 44,20,60
Rt 4,17,4096
Cv 208,208,209
Mv 36,20,56
Rt 6,17,4608
s_add_u32 s92, s42, 0x180
Mv 48,24,56
Rt 8,18,4096
s_add_u32 m0, s41, 0
Cv 209,210,211
Mv 56,24,60
Rt 10,18,4608
buffer_load_dwordx4 v8, s[16:19], s92 offen offset:0 lds
Mv 60,28,60
Rt 12,19,4096
Cv 210,212,213
Mv 52,28,56
Rt 14,19,4608
Cv 211,214,215
s_waitcnt lgkmcnt(8)
)" R"(
Mz 64,32,192
Rt 16,16,8192
Cv 216,216,217
Mz 72,32,196
Rt 18,16,8704
Cv 217,218,219
Mz 76,36,196
Rt 20,17,8192
buffer_store_dwordx4 v[176:179], v2, s[20:23], s47 offen offset:2048 sc0 sc1
Mz 68,36,192
Rt 22,17,8704
Cv 218,220,221
Cv 219,222,223
Mz 80,40,192
Rt 24,18,8192
v_mul_f32_e32 v32, s54, v32
Mz 88,40,196
Rt 26,18,8704
buffer_load_dwordx4 v9, s[16:19], s92 offen offset:1024 lds
Mz 92,44,196
Rt 28,19,8192
v_mul_f32_e32 v33, s54, v33
Mz 84,44,192
Rt 30,19,8704
s_waitcnt lgkmcnt(8)
Mv 64,0,200
Rt 32,16,12288
Cv 32,32,33
Mv 72,0,204
Rt 34,16,12800
v_accvgpr_write_b32 a224, v32
Mv 76,4,204
Rt 36,17,12288
v_mul_f32_e32 v34, s54, v34
Mv 68,4,200
Rt 38,17,12800
v_mul_f32_e32 v35, s54, v35
Mv 80,8,200
Rt 40,18,12288
Cv 34,34,35
Mv 88,8,204
Rt 42,18,12800
buffer_load_dwordx4 v10, s[16:19], s92 offen offset:2048 lds
Mv 92,12,204
Rt 44,19,12288
v_accvgpr_write_b32 a225, v34
Mv 84,12,200
Rt 46,19,12800
s_waitcnt lgkmcnt(8)
Mv 64,16,208
Rt 0,16,16384
v_mul_f32_e32 v36, s54, v36
Mv 72,16,212
Rt 2,16,16896
v_mul_f32_e32 v37, s54, v37
Mv 76,20,212
Rt 4,17,16384
buffer_store_dwordx4 v[184:187], v2, s[20:23], s47 offen offset:3072 sc0 sc1
Mv 68,20,208
Rt 6,17,16896
Cv 36,36,37
Mv 80,24,208
Rt 8,18,16384
v_accvgpr_write_b32 a226, v36
)" R"(
Mv 88,24,212
Rt 10,18,16896
buffer_load_dwordx4 v11, s[16:19], s92 offen offset:3072 lds
Mv 92,28,212
Rt 12,19,16384
v_mul_f32_e32 v38, s54, v38
Mv 84,28,208
Rt 14,19,16896
s_waitcnt lgkmcnt(8)
Mv 64,32,216
Rt 16,16,20480
v_mul_f32_e32 v39, s54, v39
Mv 72,32,220
Rt 18,16,20992
Cv 38,38,39
Mv 76,36,220
Rt 20,17,20480
v_accvgpr_write_b32 a227, v38
Mv 68,36,216
Rt 22,17,20992
s_add_u32 s92, s42, 0x40180
Mv 80,40,216
Rt 24,18,20480
s_add_u32 m0, s41, 4096
Mv 88,40,220
Rt 26,18,20992
buffer_load_dwordx4 v8, s[16:19], s92 offen offset:0 lds
Mv 92,44,220
Rt 28,19,20480
v_mul_f32_e32 v40, s54, v40
Mv 84,44,216
Rt 30,19,20992
s_waitcnt lgkmcnt(8)
Mv 64,0,128
Rt 32,16,24576
v_mul_f32_e32 v41, s54, v41
Mv 72,0,132
Rt 34,16,25088
Cv 40,40,41
Mv 76,4,132
Rt 36,17,24576
buffer_store_dwordx4 v[192:195], v3, s[20:23], s47 offen offset:0 sc0 sc1
Mv 68,4,128
Rt 38,17,25088
v_accvgpr_write_b32 a228, v40
Mv 80,8,128
Rt 40,18,24576
v_mul_f32_e32 v42, s54, v42
Mv 88,8,132
Rt 42,18,25088
buffer_load_dwordx4 v9, s[16:19], s92 offen offset:1024 lds
Mv 92,12,132
Rt 44,19,24576
v_mul_f32_e32 v43, s54, v43
Mv 84,12,128
Rt 46,19,25088
s_waitcnt lgkmcnt(8)
Mv 64,16,136
Rt 0,12,32768
Cv 42,42,43
Mv 72,16,140
Rt 2,12,33280
v_accvgpr_write_b32 a229, v42
Mv 76,20,140
)" R"(
Rt 4,13,32768
v_mul_f32_e32 v44, s54, v44
Mv 68,20,136
Rt 6,13,33280
v_mul_f32_e32 v45, s54, v45
Mv 80,24,136
Rt 8,14,32768
Cv 44,44,45
Mv 88,24,140
Rt 10,14,33280
buffer_load_dwordx4 v10, s[16:19], s92 offen offset:2048 lds
Mv 92,28,140
Rt 12,15,32768
v_accvgpr_write_b32 a230, v44
Mv 84,28,136
Rt 14,15,33280
s_waitcnt vmcnt(20) lgkmcnt(8)
s_barrier
Mv 64,32,144
Rt 16,12,36864
v_mul_f32_e32 v46, s54, v46
Mv 72,32,148
Rt 18,12,37376
v_mul_f32_e32 v47, s54, v47
Mv 76,36,148
Rt 20,13,36864
buffer_store_dwordx4 v[200:203], v3, s[20:23], s47 offen offset:1024 sc0 sc1
Mv 68,36,144
Rt 22,13,37376
s_add_u32 s92, s42, 0x1c0180
Mv 80,40,144
Rt 24,14,36864
s_add_u32 m0, s41, 65536
Mv 88,40,148
Rt 26,14,37376
buffer_load_dwordx4 v8, s[16:19], s92 offen offset:0 lds
Mv 92,44,148
Rt 28,15,36864
Cv 46,46,47
Mv 84,44,144
Rt 30,15,37376
s_waitcnt lgkmcnt(8)
Mv 64,0,152
Rt 32,12,40960
v_accvgpr_write_b32 a231, v46
Mv 72,0,156
Rt 34,12,41472
v_mul_f32_e32 v48, s54, v48
Mv 76,4,156
Rt 36,13,40960
v_mul_f32_e32 v49, s54, v49
Mv 68,4,152
Rt 38,13,41472
Cv 48,48,49
Mv 80,8,152
Rt 40,14,40960
v_accvgpr_write_b32 a232, v48
Mv 88,8,156
Rt 42,14,41472
buffer_load_dwordx4 v9, s[16:19], s92 offen offset:1024 lds
Mv 92,12,156
Rt 44,15,40960
v_mul_f32_e32 v50, s54, v50
Mv 84,12,152
Rt 46,15,41472
)" R"(
s_waitcnt lgkmcnt(8)
Mv 64,16,160
Rt 0,12,45056
v_mul_f32_e32 v51, s54, v51
Mv 72,16,164
Rt 2,12,45568
Cv 50,50,51
Mv 76,20,164
Rt 4,13,45056
buffer_store_dwordx4 v[208:211], v3, s[20:23], s47 offen offset:2048 sc0 sc1
Mv 68,20,160
Rt 6,13,45568
v_accvgpr_write_b32 a233, v50
Mv 80,24,160
Rt 8,14,45056
v_mul_f32_e32 v52, s54, v52
Mv 88,24,164
Rt 10,14,45568
buffer_load_dwordx4 v10, s[16:19], s92 offen offset:2048 lds
Mv 92,28,164
Rt 12,15,45056
v_mul_f32_e32 v53, s54, v53
Mv 84,28,160
Rt 14,15,45568
s_waitcnt lgkmcnt(8)
Mv 64,32,168
Rt 16,12,49152
Cv 52,52,53
Mv 72,32,172
Rt 18,12,49664
v_accvgpr_write_b32 a234, v52
Mv 76,36,172
Rt 20,13,49152
v_mul_f32_e32 v54, s54, v54
Mv 68,36,168
Rt 22,13,49664
v_mul_f32_e32 v55, s54, v55
Mv 80,40,168
Rt 24,14,49152
buffer_load_dword v4, v5, s[20:23], s50 offen offset:0 sc0 sc1
Mv 88,40,172
Rt 26,14,49664
buffer_load_dwordx4 v11, s[16:19], s92 offen offset:3072 lds
Mv 92,44,172
Rt 28,15,49152
Cv 54,54,55
Mv 84,44,168
Rt 30,15,49664
s_waitcnt lgkmcnt(8)
Mv 64,0,176
Rt 32,12,53248
v_accvgpr_write_b32 a235, v54
Mv 72,0,180
Rt 34,12,53760
v_mul_f32_e32 v56, s54, v56
Mv 76,4,180
Rt 36,13,53248
buffer_store_dwordx4 v[216:219], v3, s[20:23], s47 offen offset:3072 sc0 sc1
Mv 68,4,176
Rt 38,13,53760
s_add_u32 s92, s42, 0x200180
Mv 80,8,176
Rt 40,14,53248
s_add_u32 m0, s41, 69632
)" R"(
Mv 88,8,180
Rt 42,14,53760
buffer_load_dwordx4 v8, s[16:19], s92 offen offset:0 lds
Mv 92,12,180
Rt 44,15,53248
v_mul_f32_e32 v57, s54, v57
Mv 84,12,176
Rt 46,15,53760
s_waitcnt lgkmcnt(8)
Mv 64,16,184
Rt 0,12,57344
Cv 56,56,57
Mv 72,16,188
Rt 2,12,57856
v_accvgpr_write_b32 a236, v56
Mv 76,20,188
Rt 4,13,57344
v_mul_f32_e32 v58, s54, v58
Mv 68,20,184
Rt 6,13,57856
v_mul_f32_e32 v59, s54, v59
Mv 80,24,184
Rt 8,14,57344
Cv 58,58,59
Mv 88,24,188
Rt 10,14,57856
buffer_load_dwordx4 v9, s[16:19], s92 offen offset:1024 lds
Mv 92,28,188
Rt 12,15,57344
v_accvgpr_write_b32 a237, v58
Mv 84,28,184
Rt 14,15,57856
s_waitcnt lgkmcnt(8)
Mv 64,32,48
Rt 16,16,32768
v_mul_f32_e32 v60, s54, v60
Mv 72,32,52
Rt 18,16,33280
v_mul_f32_e32 v61, s54, v61
Mv 76,36,52
Rt 20,17,32768
buffer_store_dwordx4 v[224:227], v2, s[20:23], s48 offen offset:0 sc0 sc1
Mv 68,36,48
Rt 22,17,33280
Cv 60,60,61
Mv 80,40,48
Rt 24,18,32768
v_accvgpr_write_b32 a238, v60
Mv 88,40,52
Rt 26,18,33280
buffer_load_dwordx4 v10, s[16:19], s92 offen offset:2048 lds
Mv 92,44,52
Rt 28,19,32768
v_mul_f32_e32 v62, s54, v62
Mv 84,44,48
Rt 30,19,33280
s_waitcnt vmcnt(6) lgkmcnt(8)
s_barrier
Mv 64,0,56
Rt 32,16,36864
v_cmp_eq_u32_e32 vcc, s3, v4
s_andn2_b64 s[28:29], s[60:61], vcc
s_cbranch_scc1 .Lzsp3
.Lzok3:
Mv 72,0,60
)" R"(
Rt 34,16,37376
s_add_u32 s40, s55, 0x10000
Mv 76,4,60
Rt 36,17,36864
v_mul_f32_e32 v63, s54, v63
Mv 68,4,56
Rt 38,17,37376
Cv 62,62,63
Mv 80,8,56
Rt 40,18,36864
s_add_u32 m0, s36, 32768
Mv 88,8,60
Rt 42,18,37376
buffer_load_dwordx4 v2, s[20:23], s40 offen offset:0 sc1 lds
Mv 92,12,60
Rt 44,19,36864
v_accvgpr_write_b32 a239, v62
Mv 84,12,56
Rt 46,19,37376
s_waitcnt lgkmcnt(8)
Mz 32,16,192
Rt 0,16,40960
Mz 40,16,196
Rt 2,16,41472
Mz 44,20,196
Rt 4,17,40960
buffer_store_dwordx4 v[232:235], v2, s[20:23], s48 offen offset:1024 sc0 sc1
Mz 36,20,192
Rt 6,17,41472
buffer_load_dwordx4 v2, s[20:23], s40 offen offset:1024 sc1 lds
Mz 48,24,192
Rt 8,18,40960
v_mul_f32_e32 v64, s54, v64
Mz 56,24,196
Rt 10,18,41472
v_mul_f32_e32 v65, s54, v65
Mz 60,28,196
Rt 12,19,40960
Cv 64,64,65
Mz 52,28,192
Rt 14,19,41472
s_waitcnt lgkmcnt(8)
Mv 32,32,200
Rt 16,16,45056
v_accvgpr_write_b32 a240, v64
Mv 40,32,204
Rt 18,16,45568
buffer_load_dwordx4 v2, s[20:23], s40 offen offset:2048 sc1 lds
Mv 44,36,204
Rt 20,17,45056
v_mul_f32_e32 v66, s54, v66
Mv 36,36,200
Rt 22,17,45568
v_mul_f32_e32 v67, s54, v67
Mv 48,40,200
Rt 24,18,45056
Cv 66,66,67
Mv 56,40,204
Rt 26,18,45568
v_accvgpr_write_b32 a241, v66
Mv 60,44,204
Rt 28,19,45056
v_mul_f32_e32 v68, s54, v68
Mv 52,44,200
)" R"(
Rt 30,19,45568
buffer_load_dwordx4 v2, s[20:23], s40 offen offset:3072 sc1 lds
s_waitcnt lgkmcnt(8)
Mv 32,0,208
Rt 32,16,49152
v_mul_f32_e32 v69, s54, v69
Mv 40,0,212
Rt 34,16,49664
Cv 68,68,69
Mv 44,4,212
Rt 36,17,49152
buffer_store_dwordx4 v[240:243], v2, s[20:23], s48 offen offset:2048 sc0 sc1
Mv 36,4,208
Rt 38,17,49664
v_accvgpr_write_b32 a242, v68
v_mul_f32_e32 v70, s54, v70
Mv 48,8,208
Rt 40,18,49152
s_add_u32 m0, s36, 36864
buffer_load_dword v4, v7, s[20:23], s52 offen offset:0 sc0 sc1
Mv 56,8,212
Rt 42,18,49664
buffer_load_dwordx4 v3, s[20:23], s40 offen offset:0 sc1 lds
Mv 60,12,212
Rt 44,19,49152
v_mul_f32_e32 v71, s54, v71
Mv 52,12,208
Rt 46,19,49664
s_waitcnt lgkmcnt(8)
Mv 32,16,216
Rt 0,16,53248
Cv 70,70,71
Mv 40,16,220
Rt 2,16,53760
v_accvgpr_write_b32 a243, v70
Mv 44,20,220
Rt 4,17,53248
v_mul_f32_e32 v72, s54, v72
Mv 36,20,216
Rt 6,17,53760
buffer_load_dwordx4 v3, s[20:23], s40 offen offset:1024 sc1 lds
Mv 48,24,216
Rt 8,18,53248
v_mul_f32_e32 v73, s54, v73
Mv 56,24,220
Rt 10,18,53760
Cv 72,72,73
Mv 60,28,220
Rt 12,19,53248
v_accvgpr_write_b32 a244, v72
Mv 52,28,216
Rt 14,19,53760
s_waitcnt lgkmcnt(8)
Mv 32,32,128
Rt 16,16,57344
v_mul_f32_e32 v74, s54, v74
Mv 40,32,132
Rt 18,16,57856
buffer_load_dwordx4 v3, s[20:23], s40 offen offset:2048 sc1 lds
Mv 44,36,132
Rt 20,17,57344
buffer_store_dwordx4 v[248:251], v2, s[20:23], s48 offen offset:3072 sc0 sc1
Mv 36,36,128
Rt 22,17,57856
)" R"(
v_mul_f32_e32 v75, s54, v75
Cv 74,74,75
Mv 48,40,128
Rt 24,18,57344
v_accvgpr_write_b32 a245, v74
Mv 56,40,132
Rt 26,18,57856
v_mul_f32_e32 v76, s54, v76
Mv 60,44,132
Rt 28,19,57344
v_mul_f32_e32 v77, s54, v77
Mv 52,44,128
Rt 30,19,57856
buffer_load_dwordx4 v3, s[20:23], s40 offen offset:3072 sc1 lds
s_waitcnt lgkmcnt(8)
Mv 32,0,136
Rt 32,20,0
Cv 76,76,77
Mv 40,0,140
Rt 34,20,512
v_accvgpr_write_b32 a246, v76
Mv 44,4,140
Rt 36,21,0
v_mul_f32_e32 v78, s54, v78
Mv 36,4,136
Rt 38,21,512
v_mul_f32_e32 v79, s54, v79
Mv 48,8,136
Rt 40,22,0
Cv 78,78,79
Mv 56,8,140
Rt 42,22,512
v_accvgpr_write_b32 a247, v78
Mv 60,12,140
Rt 44,23,0
v_mul_f32_e32 v80, s54, v80
Mv 52,12,136
Rt 46,23,512
s_waitcnt vmcnt(5) lgkmcnt(8)
s_barrier
buffer_store_dword v6, v7, s[20:23], s49 offen offset:8 sc0 sc1
Mv 32,16,144
Rt 0,20,4096
v_cmp_eq_u32_e32 vcc, s3, v4
s_andn2_b64 s[28:29], 1, vcc
s_cbranch_scc1 .Lzsp4
.Lzok4:
Mv 40,16,148
Rt 2,20,4608
v_mul_f32_e32 v81, s54, v81
Mv 44,20,148
Rt 4,21,4096
s_add_u32 m0, s36, 98304
buffer_store_dwordx4 v[228:231], v3, s[20:23], s48 offen offset:0 sc0 sc1
Mv 36,20,144
Rt 6,21,4608
buffer_load_dwordx4 v2, s[20:23], s55 offen offset:0 sc1 lds
Mv 48,24,144
Rt 8,22,4096
Cv 80,80,81
Mv 56,24,148
Rt 10,22,4608
v_accvgpr_write_b32 a248, v80
Mv 60,28,148
Rt 12,23,4096
)" R"(
buffer_load_dwordx4 a[64:67], v2, s[20:23], s51 offen offset:0 sc0 sc1
Mv 52,28,144
Rt 14,23,4608
s_waitcnt lgkmcnt(8)
Mv 32,32,152
Rt 16,20,8192
v_mul_f32_e32 v82, s54, v82
Mv 40,32,156
Rt 18,20,8704
buffer_load_dwordx4 v2, s[20:23], s55 offen offset:1024 sc1 lds
Mv 44,36,156
Rt 20,21,8192
v_mul_f32_e32 v83, s54, v83
Mv 36,36,152
Rt 22,21,8704
Cv 82,82,83
Mv 48,40,152
Rt 24,22,8192
buffer_load_dwordx4 a[68:71], v2, s[20:23], s51 offen offset:1024 sc0 sc1
Mv 56,40,156
Rt 26,22,8704
v_accvgpr_write_b32 a249, v82
Mv 60,44,156
Rt 28,23,8192
v_mul_f32_e32 v84, s54, v84
Mv 52,44,152
Rt 30,23,8704
buffer_load_dwordx4 v2, s[20:23], s55 offen offset:2048 sc1 lds
s_waitcnt lgkmcnt(8)
Mv 32,0,160
Rt 32,20,12288
v_mul_f32_e32 v85, s54, v85
Mv 40,0,164
Rt 34,20,12800
Cv 84,84,85
Mv 44,4,164
Rt 36,21,12288
buffer_load_dwordx4 a[72:75], v2, s[20:23], s51 offen offset:2048 sc0 sc1
buffer_store_dwordx4 v[236:239], v3, s[20:23], s48 offen offset:1024 sc0 sc1
Mv 36,4,160
Rt 38,21,12800
v_accvgpr_write_b32 a250, v84
v_mul_f32_e32 v86, s54, v86
Mv 48,8,160
Rt 40,22,12288
v_mul_f32_e32 v87, s54, v87
Mv 56,8,164
Rt 42,22,12800
buffer_load_dwordx4 v2, s[20:23], s55 offen offset:3072 sc1 lds
Mv 60,12,164
Rt 44,23,12288
Cv 86,86,87
Mv 52,12,160
Rt 46,23,12800
s_waitcnt lgkmcnt(8)
Mv 32,16,168
Rt 0,20,16384
buffer_load_dwordx4 a[76:79], v2, s[20:23], s51 offen offset:3072 sc0 sc1
Mv 40,16,172
Rt 2,20,16896
v_accvgpr_write_b32 a251, v86
Mv 44,20,172
Rt 4,21,16384
s_add_u32 m0, s36, 102400
)" R"(
Mv 36,20,168
Rt 6,21,16896
buffer_load_dwordx4 v3, s[20:23], s55 offen offset:0 sc1 lds
Mv 48,24,168
Rt 8,22,16384
v_mul_f32_e32 v88, s54, v88
Mv 56,24,172
Rt 10,22,16896
v_mul_f32_e32 v89, s54, v89
Mv 60,28,172
Rt 12,23,16384
buffer_load_dwordx4 a[80:83], v3, s[20:23], s51 offen offset:0 sc0 sc1
Mv 52,28,168
Rt 14,23,16896
s_waitcnt lgkmcnt(8)
Mv 32,32,176
Rt 16,20,20480
Cv 88,88,89
Mv 40,32,180
Rt 18,20,20992
buffer_load_dwordx4 v3, s[20:23], s55 offen offset:1024 sc1 lds
Mv 44,36,180
Rt 20,21,20480
buffer_store_dwordx4 v[244:247], v3, s[20:23], s48 offen offset:2048 sc0 sc1
Mv 36,36,176
Rt 22,21,20992
v_accvgpr_write_b32 a252, v88
v_mul_f32_e32 v90, s54, v90
Mv 48,40,176
Rt 24,22,20480
buffer_load_dwordx4 a[84:87], v3, s[20:23], s51 offen offset:1024 sc0 sc1
Mv 56,40,180
Rt 26,22,20992
v_mul_f32_e32 v91, s54, v91
Mv 60,44,180
Rt 28,23,20480
Cv 90,90,91
Mv 52,44,176
Rt 30,23,20992
buffer_load_dwordx4 v3, s[20:23], s55 offen offset:2048 sc1 lds
s_waitcnt lgkmcnt(8)
Mv 32,0,184
Rt 32,20,24576
v_accvgpr_write_b32 a253, v90
Mv 40,0,188
Rt 34,20,25088
v_mul_f32_e32 v92, s54, v92
Mv 44,4,188
Rt 36,21,24576
buffer_load_dwordx4 a[88:91], v3, s[20:23], s51 offen offset:2048 sc0 sc1
Mv 36,4,184
Rt 38,21,25088
v_mul_f32_e32 v93, s54, v93
Mv 48,8,184
Rt 40,22,24576
Cv 92,92,93
Mv 56,8,188
Rt 42,22,25088
buffer_load_dwordx4 v3, s[20:23], s55 offen offset:3072 sc1 lds
Mv 60,12,188
Rt 44,23,24576
v_accvgpr_write_b32 a254, v92
Mv 52,12,184
Rt 46,23,25088
)" R"(
s_waitcnt lgkmcnt(8)
Mv 32,16,48
Rt 0,12,0
buffer_load_dwordx4 a[92:95], v3, s[20:23], s51 offen offset:3072 sc0 sc1
Mv 40,16,52
Rt 2,12,512
v_mul_f32_e32 v94, s54, v94
Mv 44,20,52
Rt 4,13,0
buffer_store_dwordx4 v[252:255], v3, s[20:23], s48 offen offset:3072 sc0 sc1
Mv 36,20,48
Rt 6,13,512
v_mul_f32_e32 v95, s54, v95
Cv 94,94,95
Mv 48,24,48
Rt 8,14,0
v_accvgpr_write_b32 a255, v94
Mv 56,24,52
Rt 10,14,512
Mv 60,28,52
Rt 12,15,0
s_add_u32 s51, s51, 0x10000
buffer_load_dwordx4 a[96:99], v2, s[20:23], s51 offen offset:0 sc0 sc1
Mv 52,28,48
Rt 14,15,512
s_waitcnt lgkmcnt(8)
s_barrier
Mv 32,32,56
Rt 16,12,4096
Mv 40,32,60
Rt 18,12,4608
s_mov_b32 s83, 0xc108000
buffer_load_dword v4, v7, s[20:23], s83 offen offset:4 sc0 sc1
Mv 44,36,60
Rt 20,13,4096
s_add_u32 m0, s36, 131072
Mv 36,36,56
Rt 22,13,4608
buffer_load_dwordx4 v2, s[20:23], s56 offen offset:0 sc1 lds
Mv 48,40,56
Rt 24,14,4096
buffer_load_dwordx4 a[100:103], v2, s[20:23], s51 offen offset:1024 sc0 sc1
Mv 56,40,60
Rt 26,14,4608
Mv 60,44,60
Rt 28,15,4096
Mv 52,44,56
Rt 30,15,4608
s_waitcnt lgkmcnt(8)
Mz 96,0,192
Rt 32,12,8192
Mz 104,0,196
Rt 34,12,8704
buffer_load_dwordx4 v2, s[20:23], s56 offen offset:1024 sc1 lds
Mz 108,4,196
Rt 36,13,8192
buffer_load_dwordx4 a[104:107], v2, s[20:23], s51 offen offset:2048 sc0 sc1
Mz 100,4,192
Rt 38,13,8704
Mz 112,8,192
Rt 40,14,8192
v_mul_f32_e32 v32, s54, v32
Mz 120,8,196
Rt 42,14,8704
v_mul_f32_e32 v33, s54, v33
)" R"(
Mz 124,12,196
Rt 44,15,8192
Cv 32,32,33
Mz 116,12,192
Rt 46,15,8704
buffer_load_dwordx4 v2, s[20:23], s56 offen offset:2048 sc1 lds
s_waitcnt lgkmcnt(8)
Mv 96,16,200
Rt 0,12,12288
buffer_load_dwordx4 a[108:111], v2, s[20:23], s51 offen offset:3072 sc0 sc1
Mv 104,16,204
Rt 2,12,12800
v_accvgpr_write_b32 a192, v32
Mv 108,20,204
Rt 4,13,12288
v_mul_f32_e32 v34, s54, v34
Mv 100,20,200
Rt 6,13,12800
v_mul_f32_e32 v35, s54, v35
Mv 112,24,200
Rt 8,14,12288
Cv 34,34,35
Mv 120,24,204
Rt 10,14,12800
buffer_load_dwordx4 v2, s[20:23], s56 offen offset:3072 sc1 lds
Mv 124,28,204
Rt 12,15,12288
buffer_load_dwordx4 a[112:115], v3, s[20:23], s51 offen offset:0 sc0 sc1
Mv 116,28,200
Rt 14,15,12800
s_waitcnt lgkmcnt(8)
Mv 96,32,208
Rt 16,12,16384
v_accvgpr_write_b32 a193, v34
Mv 104,32,212
Rt 18,12,16896
v_mul_f32_e32 v36, s54, v36
Mv 108,36,212
Rt 20,13,16384
s_add_u32 m0, s36, 135168
Mv 100,36,208
Rt 22,13,16896
buffer_load_dwordx4 v3, s[20:23], s56 offen offset:0 sc1 lds
Mv 112,40,208
Rt 24,14,16384
buffer_load_dwordx4 a[116:119], v3, s[20:23], s51 offen offset:1024 sc0 sc1
Mv 120,40,212
Rt 26,14,16896
v_mul_f32_e32 v37, s54, v37
Mv 124,44,212
Rt 28,15,16384
Cv 36,36,37
Mv 116,44,208
Rt 30,15,16896
s_waitcnt lgkmcnt(8)
Mv 96,0,216
Rt 32,12,20480
v_accvgpr_write_b32 a194, v36
Mv 104,0,220
Rt 34,12,20992
buffer_load_dwordx4 v3, s[20:23], s56 offen offset:1024 sc1 lds
Mv 108,4,220
Rt 36,13,20480
buffer_load_dwordx4 a[120:123], v3, s[20:23], s51 offen offset:2048 sc0 sc1
)" R"(
Mv 100,4,216
Rt 38,13,20992
v_mul_f32_e32 v38, s54, v38
Mv 112,8,216
Rt 40,14,20480
v_mul_f32_e32 v39, s54, v39
Mv 120,8,220
Rt 42,14,20992
Cv 38,38,39
Mv 124,12,220
Rt 44,15,20480
v_accvgpr_write_b32 a195, v38
Mv 116,12,216
Rt 46,15,20992
buffer_load_dwordx4 v3, s[20:23], s56 offen offset:2048 sc1 lds
s_waitcnt lgkmcnt(8)
Mv 96,16,128
Rt 0,12,24576
buffer_load_dwordx4 a[124:127], v3, s[20:23], s51 offen offset:3072 sc0 sc1
Mv 104,16,132
Rt 2,12,25088
v_mul_f32_e32 v40, s54, v40
Mv 108,20,132
Rt 4,13,24576
v_mul_f32_e32 v41, s54, v41
Mv 100,20,128
Rt 6,13,25088
Cv 40,40,41
Mv 112,24,128
Rt 8,14,24576
v_accvgpr_write_b32 a196, v40
Mv 120,24,132
Rt 10,14,25088
buffer_load_dwordx4 v3, s[20:23], s56 offen offset:3072 sc1 lds
Mv 124,28,132
Rt 12,15,24576
v_mul_f32_e32 v42, s54, v42
Mv 116,28,128
Rt 14,15,25088
s_waitcnt lgkmcnt(8)
Mv 96,32,136
Rt 16,16,0
v_mul_f32_e32 v43, s54, v43
Mv 104,32,140
Rt 18,16,512
Cv 42,42,43
Mv 108,36,140
Rt 20,17,0
v_accvgpr_write_b32 a197, v42
Mv 100,36,136
Rt 22,17,512
v_mul_f32_e32 v44, s54, v44
Mv 112,40,136
Rt 24,18,0
v_mul_f32_e32 v45, s54, v45
Mv 120,40,140
Rt 26,18,512
Cv 44,44,45
Mv 124,44,140
Rt 28,19,0
v_accvgpr_write_b32 a198, v44
Mv 116,44,136
Rt 30,19,512
s_waitcnt vmcnt(15) lgkmcnt(8)
s_barrier
)" R"(
buffer_store_dword v6, v7, s[20:23], s49 offen offset:12 sc0 sc1
Mv 96,0,144
Rt 32,16,4096
v_cmp_eq_u32_e32 vcc, s3, v4
s_andn2_b64 s[28:29], 1, vcc
s_cbranch_scc1 .Lzsp6
.Lzok6:
Mv 104,0,148
Rt 34,16,4608
buffer_load_dword v4, v5, s[20:23], s50 offen offset:0 sc0 sc1
Mv 108,4,148
Rt 36,17,4096
s_lshl_b64 s[60:61], s[60:61], 8
v_add_u32 v8, 0x10000, v1
Mv 100,4,144
Rt 38,17,4608
s_or_b32 s60, s60, 0xff
v_add_u32 v10, 0x20000, v1
Mv 112,8,144
Rt 40,18,4096
v_mul_f32_e32 v46, s54, v46
s_add_u32 s56, s56, 0x10000
Mv 120,8,148
Rt 42,18,4608
v_mul_f32_e32 v47, s54, v47
s_add_u32 s55, s55, 0x10000
Mv 124,12,148
Rt 44,19,4096
Cv 46,46,47
s_add_u32 s55, s55, 0x10000
Mv 116,12,144
Rt 46,19,4608
s_waitcnt lgkmcnt(8)
Mv 96,16,152
Rt 0,16,8192
v_accvgpr_write_b32 a199, v46
Mv 104,16,156
Rt 2,16,8704
v_mul_f32_e32 v48, s54, v48
Mv 108,20,156
Rt 4,17,8192
v_mul_f32_e32 v49, s54, v49
Mv 100,20,152
Rt 6,17,8704
Cv 48,48,49
s_cmp_eq_u32 s57, 2
s_cselect_b32 s55, s59, s55
Mv 112,24,152
Rt 8,18,8192
v_accvgpr_write_b32 a200, v48
s_mov_b32 s93, 0
Mv 120,24,156
Rt 10,18,8704
v_mul_f32_e32 v50, s54, v50
s_mov_b32 s94, 1
Mv 124,28,156
Rt 12,19,8192
v_mul_f32_e32 v51, s54, v51
Mv 116,28,152
Rt 14,19,8704
s_waitcnt lgkmcnt(8)
Mv 96,32,160
Rt 16,16,12288
Cv 50,50,51
)" R"(
Mv 104,32,164
Rt 18,16,12800
v_accvgpr_write_b32 a201, v50
Mv 108,36,164
Rt 20,17,12288
v_mul_f32_e32 v52, s54, v52
Mv 100,36,160
Rt 22,17,12800
v_mul_f32_e32 v53, s54, v53
Mv 112,40,160
Rt 24,18,12288
Cv 52,52,53
Mv 120,40,164
Rt 26,18,12800
v_accvgpr_write_b32 a202, v52
Mv 124,44,164
Rt 28,19,12288
v_mul_f32_e32 v54, s54, v54
Mv 116,44,160
Rt 30,19,12800
s_waitcnt lgkmcnt(8)
Mv 96,0,168
Rt 32,16,16384
v_mul_f32_e32 v55, s54, v55
Mv 104,0,172
Rt 34,16,16896
Cv 54,54,55
Mv 108,4,172
Rt 36,17,16384
v_accvgpr_write_b32 a203, v54
Mv 100,4,168
Rt 38,17,16896
v_mul_f32_e32 v56, s54, v56
Mv 112,8,168
Rt 40,18,16384
v_mul_f32_e32 v57, s54, v57
Mv 120,8,172
Rt 42,18,16896
Cv 56,56,57
Mv 124,12,172
Rt 44,19,16384
v_accvgpr_write_b32 a204, v56
Mv 116,12,168
Rt 46,19,16896
s_waitcnt lgkmcnt(8)
Mv 96,16,176
Rt 0,16,20480
v_mul_f32_e32 v58, s54, v58
Mv 104,16,180
Rt 2,16,20992
v_mul_f32_e32 v59, s54, v59
Mv 108,20,180
Rt 4,17,20480
Cv 58,58,59
Mv 100,20,176
Rt 6,17,20992
v_accvgpr_write_b32 a205, v58
Mv 112,24,176
Rt 8,18,20480
v_mul_f32_e32 v60, s54, v60
Mv 120,24,180
Rt 10,18,20992
v_mul_f32_e32 v61, s54, v61
Mv 124,28,180
)" R"(
Rt 12,19,20480
Cv 60,60,61
Mv 116,28,176
Rt 14,19,20992
s_waitcnt vmcnt(0) lgkmcnt(8)
s_barrier
Mv 96,32,184
Rt 16,16,24576
v_cmp_eq_u32_e32 vcc, s3, v4
s_andn2_b64 s[28:29], s[60:61], vcc
s_cbranch_scc1 .Lzsp5
.Lzok5:
Mv 104,32,188
Rt 18,16,25088
v_mov_b32 v24, 0
buffer_load_dword v4, v24, s[20:23], s83 offen sc0 sc1
Mv 108,36,188
Rt 20,17,24576
v_accvgpr_write_b32 a206, v60
Mv 100,36,184
Rt 22,17,25088
v_mul_f32_e32 v62, s54, v62
Mv 112,40,184
Rt 24,18,24576
v_mul_f32_e32 v63, s54, v63
Mv 120,40,188
Rt 26,18,25088
Cv 62,62,63
Mv 124,44,188
Rt 28,19,24576
v_accvgpr_write_b32 a207, v62
Mv 116,44,184
Rt 30,19,25088
s_waitcnt lgkmcnt(8)
Mv 96,0,48
Mv 104,0,52
Rb 0,8,32768
Mv 108,4,52
Mv 100,4,48
Rb 4,8,33792
Mv 112,8,48
Mv 120,8,52
Rb 8,8,34816
Mv 124,12,52
Mv 116,12,48
Rb 12,8,35840
s_waitcnt lgkmcnt(4)
Mv 96,16,56
Mv 104,16,60
Rb 16,8,40960
Mv 108,20,60
Mv 100,20,56
Rb 20,8,41984
Mv 112,24,56
Mv 120,24,60
Rb 24,8,43008
Mv 124,28,60
Mv 116,28,56
Rb 28,8,44032
Rb 32,8,36864
Rb 36,8,37888
Rb 40,8,38912
Rb 44,8,39936
s_cmp_eq_u32 s35, 7
s_cbranch_scc1 .LzA7
)" R"(
.LzA7R:
s_waitcnt lgkmcnt(8)
Mz 64,0,64
Rb 48,8,45056
v_mul_f32_e32 v96, s54, v96
Mz 68,0,68
v_mul_f32_e32 v97, s54, v97
Cv 96,96,97
v_accvgpr_write_b32 a208, v96
Mv 68,4,76
Rb 52,8,46080
s_add_u32 m0, s36, 0
Mv 64,4,72
buffer_load_dwordx4 v2, s[20:23], s55 offen offset:0 sc1 lds
v_mul_f32_e32 v98, s54, v98
Mv 64,8,80
Rb 56,8,47104
v_mul_f32_e32 v99, s54, v99
Mv 68,8,84
Cv 98,98,99
v_accvgpr_write_b32 a209, v98
v_mul_f32_e32 v100, s54, v100
Mv 68,12,92
Rb 60,8,48128
s_waitcnt lgkmcnt(8)
Mv 64,12,88
v_mul_f32_e32 v101, s54, v101
Cv 100,100,101
v_accvgpr_write_b32 a210, v100
Mv 64,16,96
Rb 0,8,49152
v_mul_f32_e32 v102, s54, v102
v_mul_f32_e32 v103, s54, v103
Mv 68,16,100
Cv 102,102,103
v_accvgpr_write_b32 a211, v102
Mv 68,20,108
Rb 4,8,50176
v_mul_f32_e32 v104, s54, v104
v_mul_f32_e32 v105, s54, v105
Mv 64,20,104
buffer_load_dwordx4 v2, s[20:23], s55 offen offset:1024 sc1 lds
Mv 64,24,112
Rb 8,8,51200
Cv 104,104,105
v_accvgpr_write_b32 a212, v104
Mv 68,24,116
v_mul_f32_e32 v106, s54, v106
v_mul_f32_e32 v107, s54, v107
Mv 68,28,124
Rb 12,8,52224
Cv 106,106,107
s_waitcnt lgkmcnt(8)
Mv 64,28,120
v_accvgpr_write_b32 a213, v106
v_mul_f32_e32 v108, s54, v108
v_mul_f32_e32 v109, s54, v109
Mz 72,32,64
Rb 16,8,57344
Cv 108,108,109
Mz 76,32,68
v_accvgpr_write_b32 a214, v108
v_mul_f32_e32 v110, s54, v110
v_mul_f32_e32 v111, s54, v111
)" R"(
Mv 76,36,76
Rb 20,8,58368
Cv 110,110,111
Mv 72,36,72
buffer_load_dwordx4 v2, s[20:23], s55 offen offset:2048 sc1 lds
v_accvgpr_write_b32 a215, v110
Mv 72,40,80
Rb 24,8,59392
v_mul_f32_e32 v112, s54, v112
Mv 76,40,84
v_mul_f32_e32 v113, s54, v113
Cv 112,112,113
v_accvgpr_write_b32 a216, v112
Mv 76,44,92
Rb 28,8,60416
s_waitcnt vmcnt(3) lgkmcnt(8)
Mv 72,44,88
v_mul_f32_e32 v114, s54, v114
v_mul_f32_e32 v115, s54, v115
Cv 114,114,115
Mv 72,48,96
Rb 32,8,53248
v_accvgpr_write_b32 a217, v114
v_mul_f32_e32 v116, s54, v116
Mv 76,48,100
v_mul_f32_e32 v117, s54, v117
Cv 116,116,117
Mv 76,52,108
Rb 36,8,54272
v_accvgpr_write_b32 a218, v116
v_mul_f32_e32 v118, s54, v118
Mv 72,52,104
buffer_load_dwordx4 v2, s[20:23], s55 offen offset:3072 sc1 lds
Mv 72,56,112
Rb 40,8,55296
v_mul_f32_e32 v119, s54, v119
Cv 118,118,119
Mv 76,56,116
v_accvgpr_write_b32 a219, v118
v_mul_f32_e32 v120, s54, v120
Mv 76,60,124
Rb 44,8,56320
v_mul_f32_e32 v121, s54, v121
s_waitcnt lgkmcnt(8)
Mv 72,60,120
Cv 120,120,121
v_accvgpr_write_b32 a220, v120
v_mul_f32_e32 v122, s54, v122
Mz 80,0,64
Rb 48,8,61440
v_mul_f32_e32 v123, s54, v123
Mz 84,0,68
Cv 122,122,123
v_accvgpr_write_b32 a221, v122
v_mul_f32_e32 v124, s54, v124
Mv 84,4,76
Rb 52,8,62464
s_add_u32 m0, s36, 4096
Mv 80,4,72
buffer_load_dwordx4 v3, s[20:23], s55 offen offset:0 sc1 lds
v_mul_f32_e32 v125, s54, v125
Mv 80,8,80
Rb 56,8,63488
Cv 124,124,125
)" R"(
Mv 84,8,84
v_accvgpr_write_b32 a222, v124
v_mul_f32_e32 v126, s54, v126
v_mul_f32_e32 v127, s54, v127
Mv 84,12,92
Rb 60,8,64512
s_waitcnt lgkmcnt(8)
Mv 80,12,88
Cv 126,126,127
v_accvgpr_write_b32 a223, v126
v_mov_b32 v16, 0
Mv 80,16,96
v_mov_b32 v17, 0
v_mov_b32 v18, 0
v_mov_b32 v19, 0
Mv 84,16,100
v_mov_b32 v20, 0
v_mov_b32 v21, 0
Mv 84,20,108
v_mov_b32 v22, 0
v_mov_b32 v23, 0
v_readfirstlane_b32 s63, v4
Mv 80,20,104
buffer_load_dwordx4 v3, s[20:23], s55 offen offset:1024 sc1 lds
Mv 80,24,112
v_and_b32 v24, 16, v1
v_lshrrev_b32 v24, v24, v4
v_and_b32 v24, 0x7ff, v24
Mv 84,24,116
v_mul_u32_u24 v24, 896, v24
v_and_b32 v25, 14, v0
Mv 84,28,124
v_min_u32 v25, 1, v25
v_lshlrev_b32 v25, 30, v25
v_and_or_b32 v25, v0, 48, v25
Mv 80,28,120
v_add_u32 v24, v24, v25
s_waitcnt lgkmcnt(0)
s_barrier
Mz 88,32,64
Rb 0,1,32768
s_and_b32 s83, s33, 1
Mz 92,32,68
Rb 4,1,33792
s_mul_i32 s82, s83, 448
Mv 92,36,76
Rb 8,1,34816
Mv 88,36,72
Rb 12,1,35840
buffer_load_dwordx4 v3, s[20:23], s55 offen offset:2048 sc1 lds
Mv 88,40,80
Rb 16,1,40960
Mv 92,40,84
Rb 20,1,41984
buffer_load_dwordx4 a[128:131], v24, s[12:15], s82 offen offset:0
Mv 92,44,92
Rb 24,1,43008
Mv 88,44,88
Rb 28,1,44032
buffer_load_dwordx4 a[132:135], v24, s[12:15], s82 offen offset:64
Mv 88,48,96
Rb 32,1,36864
buffer_load_dwordx4 a[136:139], v24, s[12:15], s82 offen offset:128
Mv 92,48,100
Rb 36,1,37888
)" R"(
Mv 92,52,108
Rb 40,1,38912
buffer_load_dwordx4 a[140:143], v24, s[12:15], s82 offen offset:192
Mv 88,52,104
Rb 44,1,39936
buffer_load_dwordx4 v3, s[20:23], s55 offen offset:3072 sc1 lds
Mv 88,56,112
buffer_load_dwordx4 a[144:147], v24, s[12:15], s82 offen offset:256
Mv 92,56,116
buffer_load_dwordx4 a[148:151], v24, s[12:15], s82 offen offset:320
Mv 92,60,124
buffer_load_dwordx4 a[152:155], v24, s[12:15], s82 offen offset:384
s_waitcnt lgkmcnt(8)
Mv 88,60,120
Mz 96,0,64
Rb 48,1,45056
buffer_load_dword v4, v5, s[20:23], s50 offen offset:0 sc0 sc1
Mz 100,0,68
s_lshl_b64 s[60:61], s[60:61], 8
s_or_b32 s60, s60, 0xff
s_cmp_le_i32 s57, s58
s_cbranch_scc1 .Lzmk0
.Lzmr0:
Mv 100,4,76
Rb 52,1,46080
s_add_u32 m0, s36, 65536
s_sub_u32 s57, s57, 1
Mv 96,4,72
buffer_load_dwordx4 v2, s[20:23], s56 offen offset:0 sc1 lds
s_add_u32 s55, s55, 0x10000
Mv 96,8,80
Rb 56,1,47104
s_cmp_eq_u32 s57, 2
s_cselect_b32 s55, s59, s55
Mv 100,8,84
v_exp_f32_e32 v32, v64
Mv 100,12,92
Rb 60,1,48128
s_waitcnt lgkmcnt(8)
Mv 96,12,88
v_exp_f32_e32 v24, v65
Mv 96,16,96
Rb 0,1,49152
v_exp_f32_e32 v33, v66
Mv 100,16,100
v_exp_f32_e32 v25, v67
v_exp_f32_e32 v34, v72
Mv 100,20,108
Rb 4,1,50176
v_exp_f32_e32 v26, v73
Mv 96,20,104
buffer_load_dwordx4 v2, s[20:23], s56 offen offset:1024 sc1 lds
v_add_f32_e32 v14, v32, v24
Mv 96,24,112
Rb 8,1,51200
v_exp_f32_e32 v35, v74
Mv 100,24,116
v_exp_f32_e32 v27, v75
Cv 32,32,24
Mv 100,28,124
Rb 12,1,52224
v_add_f32_e32 v14, v14, v33
s_waitcnt lgkmcnt(8)
Mv 96,28,120
)" R"(
v_exp_f32_e32 v36, v68
v_add_f32_e32 v14, v14, v25
Mz 104,32,64
Rb 16,1,57344
v_exp_f32_e32 v28, v69
Mz 108,32,68
v_exp_f32_e32 v37, v70
v_exp_f32_e32 v29, v71
Mv 108,36,76
Rb 20,1,58368
v_exp_f32_e32 v38, v76
Mv 104,36,72
buffer_load_dwordx4 v2, s[20:23], s56 offen offset:2048 sc1 lds
Cv 33,33,25
Mv 104,40,80
Rb 24,1,59392
v_exp_f32_e32 v24, v77
Mv 108,40,84
v_exp_f32_e32 v39, v78
v_add_f32_e32 v14, v14, v34
Mv 108,44,92
Rb 28,1,60416
v_add_f32_e32 v14, v14, v26
s_waitcnt lgkmcnt(8)
Mv 104,44,88
v_exp_f32_e32 v25, v79
Cv 34,34,26
Mv 104,48,96
Rb 32,1,53248
v_exp_f32_e32 v40, v80
Mv 108,48,100
v_exp_f32_e32 v26, v81
v_add_f32_e32 v14, v14, v35
v_add_f32_e32 v14, v14, v27
Mv 108,52,108
Rb 36,1,54272
Cv 35,35,27
v_add_f32_e32 v15, v36, v28
Mv 104,52,104
buffer_load_dwordx4 v2, s[20:23], s56 offen offset:3072 sc1 lds
Cv 36,36,28
Mv 104,56,112
Rb 40,1,55296
v_exp_f32_e32 v41, v82
Mv 108,56,116
v_exp_f32_e32 v27, v83
v_add_f32_e32 v15, v15, v37
Mv 108,60,124
Rb 44,1,56320
v_add_f32_e32 v15, v15, v29
s_waitcnt lgkmcnt(8)
Mv 104,60,120
v_exp_f32_e32 v42, v88
Cv 37,37,29
Mz 112,0,64
Rb 48,1,61440
v_exp_f32_e32 v28, v89
Mz 116,0,68
v_exp_f32_e32 v43, v90
v_add_f32_e32 v15, v15, v38
v_add_f32_e32 v15, v15, v24
Mv 116,4,76
Rb 52,1,62464
s_add_u32 m0, s36, 69632
)" R"(
Cv 38,38,24
Mv 112,4,72
buffer_load_dwordx4 v3, s[20:23], s56 offen offset:0 sc1 lds
v_add_f32_e32 v15, v15, v39
Mv 112,8,80
Rb 56,1,63488
v_exp_f32_e32 v29, v91
Mv 116,8,84
v_exp_f32_e32 v44, v84
v_add_f32_e32 v15, v15, v25
Mv 116,12,92
Rb 60,1,64512
Cv 39,39,25
s_waitcnt lgkmcnt(8)
Mv 112,12,88
v_exp_f32_e32 v24, v85
v_add_f32_e32 v14, v14, v40
Mv 112,16,96
v_exp_f32_e32 v45, v86
v_add_f32_e32 v14, v14, v26
Mv 116,16,100
v_exp_f32_e32 v25, v87
Cv 40,40,26
v_add_f32_e32 v14, v14, v41
Mv 116,20,108
v_exp_f32_e32 v46, v92
v_add_f32_e32 v14, v14, v27
Mv 112,20,104
buffer_load_dwordx4 v3, s[20:23], s56 offen offset:1024 sc1 lds
Cv 41,41,27
Mv 112,24,112
v_exp_f32_e32 v26, v93
v_add_f32_e32 v14, v14, v42
Mv 116,24,116
v_exp_f32_e32 v47, v94
v_add_f32_e32 v14, v14, v28
Mv 116,28,124
v_exp_f32_e32 v27, v95
Cv 42,42,28
Mv 112,28,120
v_add_f32_e32 v14, v14, v43
s_waitcnt lgkmcnt(0)
s_barrier
Mz 120,32,64
Rb 0,10,0
v_add_f32_e32 v14, v14, v29
Cv 43,43,29
Mz 124,32,68
Rb 4,10,1024
v_add_f32_e32 v15, v15, v44
v_add_f32_e32 v15, v15, v24
Cv 44,44,24
Mv 124,36,76
Rb 8,10,2048
v_add_f32_e32 v15, v15, v45
v_add_f32_e32 v15, v15, v25
Mv 120,36,72
Rb 12,10,3072
buffer_load_dwordx4 v3, s[20:23], s56 offen offset:2048 sc1 lds
Mv 120,40,80
Rb 16,10,4096
Cv 45,45,25
v_add_f32_e32 v15, v15, v46
Mv 124,40,84
Rb 20,10,5120
)" R"(
v_add_f32_e32 v15, v15, v26
Cv 46,46,26
Mv 124,44,92
Rb 24,10,6144
v_add_f32_e32 v15, v15, v47
v_add_f32_e32 v15, v15, v27
Mv 120,44,88
Rb 28,10,7168
Cv 47,47,27
v_max_f32_e32 v11, v14, v15
Mv 120,48,96
Rb 32,10,8192
v_mov_b32 v24, v14
v_mov_b32 v25, v15
Mv 124,48,100
Rb 36,10,9216
v_permlane32_swap_b32_e32 v24, v25
v_add_f32_e32 v24, v24, v25
v_mov_b32 v25, v24
Mv 124,52,108
Rb 40,10,10240
v_permlane16_swap_b32_e32 v24, v25
v_add_f32_e32 v24, v24, v25
Mv 120,52,104
Rb 44,10,11264
buffer_load_dwordx4 v3, s[20:23], s56 offen offset:3072 sc1 lds
Mv 120,56,112
v_rcp_f32_e32 v24, v24
Mv 124,56,116
s_add_u32 s56, s56, 0x10000
v_max_f32_e32 v11, v11, v24
v_cmp_nge_f32_e32 vcc, s62, v11
Mv 124,60,124
s_waitcnt lgkmcnt(8)
s_cbranch_vccnz .Lzfx0
.Lzfr0:
Mv 120,60,120
v_mov_b32 v12, v14
v_mov_b32 v13, v15
v_mfma_f32_16x16x32_bf16 v[128:131], a[0:3], v[32:35], 0
Rb 48,10,12288
v_mfma_f32_16x16x32_bf16 v[136:139], a[0:3], v[36:39], 0
s_cmp_le_i32 s57, s58
s_cbranch_scc1 .Lzmk1
.Lzmr1:
v_mfma_f32_16x16x32_bf16 v[140:143], a[4:7], v[36:39], 0
Rb 52,10,13312
s_add_u32 m0, s36, 32768
v_mfma_f32_16x16x32_bf16 v[132:135], a[4:7], v[32:35], 0
buffer_load_dwordx4 v2, s[20:23], s55 offen offset:0 sc1 lds
v_mfma_f32_16x16x32_bf16 v[144:147], a[8:11], v[32:35], 0
Rb 56,10,14336
s_and_b32 s92, s2, 7
v_mfma_f32_16x16x32_bf16 v[152:155], a[8:11], v[36:39], 0
s_mul_i32 s92, s92, 14
s_lshr_b32 s82, s2, 3
v_mfma_f32_16x16x32_bf16 v[156:159], a[12:15], v[36:39], 0
Rb 60,10,15360
s_waitcnt lgkmcnt(8)
v_mfma_f32_16x16x32_bf16 v[148:151], a[12:15], v[32:35], 0
buffer_load_dwordx4 v2, s[20:23], s55 offen offset:1024 sc1 lds
v_mfma_f32_16x16x32_bf16 v[160:163], a[16:19], v[32:35], 0
Rb 0,10,16384
s_add_u32 s82, s82, s92
)" R"(
v_mfma_f32_16x16x32_bf16 v[168:171], a[16:19], v[36:39], 0
v_exp_f32_e32 v48, v96
v_mfma_f32_16x16x32_bf16 v[172:175], a[20:23], v[36:39], 0
Rb 4,10,17408
v_mfma_f32_16x16x32_bf16 v[164:167], a[20:23], v[32:35], 0
buffer_load_dwordx4 v2, s[20:23], s55 offen offset:2048 sc1 lds
v_mfma_f32_16x16x32_bf16 v[176:179], a[24:27], v[32:35], 0
Rb 8,10,18432
v_exp_f32_e32 v28, v97
v_mfma_f32_16x16x32_bf16 v[184:187], a[24:27], v[36:39], 0
v_exp_f32_e32 v49, v98
v_mfma_f32_16x16x32_bf16 v[188:191], a[28:31], v[36:39], 0
Rb 12,10,19456
s_waitcnt lgkmcnt(8)
v_mfma_f32_16x16x32_bf16 v[180:183], a[28:31], v[32:35], 0
buffer_load_dwordx4 v2, s[20:23], s55 offen offset:3072 sc1 lds
v_add_f32_e32 v14, v48, v28
v_mfma_f32_16x16x32_bf16 v[192:195], a[32:35], v[32:35], 0
Rb 16,10,20480
Cv 48,48,28
v_mfma_f32_16x16x32_bf16 v[200:203], a[32:35], v[36:39], 0
v_exp_f32_e32 v29, v99
v_add_f32_e32 v14, v14, v49
v_mfma_f32_16x16x32_bf16 v[204:207], a[36:39], v[36:39], 0
Rb 20,10,21504
s_add_u32 m0, s36, 36864
v_mfma_f32_16x16x32_bf16 v[196:199], a[36:39], v[32:35], 0
buffer_load_dwordx4 v3, s[20:23], s55 offen offset:0 sc1 lds
v_mfma_f32_16x16x32_bf16 v[208:211], a[40:43], v[32:35], 0
Rb 24,10,22528
v_exp_f32_e32 v50, v104
v_mfma_f32_16x16x32_bf16 v[216:219], a[40:43], v[36:39], 0
s_cmp_gt_u32 s33, 1
s_cselect_b32 s24, s78, s76
s_cselect_b32 s25, s79, s77
v_mfma_f32_16x16x32_bf16 v[220:223], a[44:47], v[36:39], 0
Rb 28,10,23552
s_waitcnt lgkmcnt(8)
v_mfma_f32_16x16x32_bf16 v[212:215], a[44:47], v[32:35], 0
buffer_load_dwordx4 v3, s[20:23], s55 offen offset:1024 sc1 lds
v_add_f32_e32 v14, v14, v29
v_mfma_f32_16x16x32_bf16 v[224:227], a[48:51], v[32:35], 0
Rb 32,10,24576
s_mov_b32 s27, 0x20000
v_mfma_f32_16x16x32_bf16 v[232:235], a[48:51], v[36:39], 0
s_cmp_lt_u32 s35, 7
s_cselect_b32 s26, 0x188000, 0
Cv 49,49,29
v_mfma_f32_16x16x32_bf16 v[236:239], a[52:55], v[36:39], 0
Rb 36,10,25600
s_mul_i32 s83, s83, 802816
v_mfma_f32_16x16x32_bf16 v[228:231], a[52:55], v[32:35], 0
buffer_load_dwordx4 v3, s[20:23], s55 offen offset:2048 sc1 lds
v_mfma_f32_16x16x32_bf16 v[240:243], a[56:59], v[32:35], 0
Rb 40,10,26624
s_lshl_b32 s92, s82, 5
v_add_f32_e32 v14, v14, v50
v_mfma_f32_16x16x32_bf16 v[248:251], a[56:59], v[36:39], 0
s_add_u32 s83, s83, s92
v_and_b32 v11, 62, v0
v_mul_u32_u24 v11, 0x6f0, v11
v_mfma_f32_16x16x32_bf16 v[252:255], a[60:63], v[36:39], 0
Rb 44,10,27648
s_waitcnt lgkmcnt(8)
)" R"(
v_mfma_f32_16x16x32_bf16 v[244:247], a[60:63], v[32:35], 0
buffer_load_dwordx4 v3, s[20:23], s55 offen offset:3072 sc1 lds
Mp 128,0,40
Rb 48,10,28672
v_add_u32 v11, v11, v1
Mp 136,0,44
v_exp_f32_e32 v24, v105
Mp 140,4,44
Rb 52,10,29696
v_add_f32_e32 v14, v14, v24
Mp 132,4,40
v_exp_f32_e32 v51, v106
Mp 144,8,40
Rb 56,10,30720
v_exp_f32_e32 v25, v107
Mp 152,8,44
v_exp_f32_e32 v52, v100
Mp 156,12,44
Rb 60,10,31744
s_waitcnt lgkmcnt(8)
Mp 148,12,40
v_exp_f32_e32 v26, v101
Cv 50,50,24
Mp 160,16,40
v_exp_f32_e32 v53, v102
Mp 168,16,44
v_exp_f32_e32 v27, v103
v_add_f32_e32 v14, v14, v51
Mp 172,20,44
v_exp_f32_e32 v54, v108
Mp 164,20,40
v_exp_f32_e32 v28, v109
Mp 176,24,40
v_exp_f32_e32 v55, v110
v_add_f32_e32 v14, v14, v25
Mp 184,24,44
v_exp_f32_e32 v29, v111
Mp 188,28,44
Cv 51,51,25
v_add_f32_e32 v15, v52, v26
Mp 180,28,40
Cv 52,52,26
s_waitcnt vmcnt(16) lgkmcnt(0)
s_barrier
Mp 192,32,40
Rb 0,1,0
s_add_u32 m0, s36, 131072
s_nop 0
buffer_load_dwordx4 v11, s[24:27], s83 offen offset:0 lds
Mp 200,32,44
Rb 4,1,1024
v_add_f32_e32 v15, v15, v53
v_add_f32_e32 v15, v15, v27
Mp 204,36,44
Rb 8,1,2048
s_add_u32 s83, s83, 0x1bc00
buffer_load_dwordx4 v11, s[24:27], s83 offen offset:1024 lds
Mp 196,36,40
Rb 12,1,3072
Cv 53,53,27
Mp 208,40,40
Rb 16,1,8192
s_add_u32 s83, s83, 0x1bc00
buffer_load_dwordx4 v11, s[24:27], s83 offen offset:2048 lds
Mp 216,40,44
)" R"(
Rb 20,1,9216
v_add_f32_e32 v15, v15, v54
Mp 220,44,44
Rb 24,1,10240
s_add_u32 s83, s83, 0x1bc00
buffer_load_dwordx4 v11, s[24:27], s83 offen offset:3072 lds
Mp 212,44,40
Rb 28,1,11264
v_add_f32_e32 v15, v15, v28
Cv 54,54,28
Mp 224,48,40
Rb 32,1,4096
s_add_u32 m0, s36, 135168
s_add_u32 s83, s83, 0x1cc00
buffer_load_dwordx4 v11, s[24:27], s83 offen offset:0 lds
Mp 232,48,44
Rb 36,1,5120
v_add_f32_e32 v15, v15, v55
v_add_f32_e32 v15, v15, v29
Mp 236,52,44
Rb 40,1,6144
s_add_u32 s83, s83, 0x1bc00
buffer_load_dwordx4 v11, s[24:27], s83 offen offset:1024 lds
Mp 228,52,40
Rb 44,1,7168
Cv 55,55,29
Mp 240,56,40
s_add_u32 s83, s83, 0x1bc00
buffer_load_dwordx4 v11, s[24:27], s83 offen offset:2048 lds
v_cmp_eq_u32_e32 vcc, s3, v4
s_andn2_b64 s[28:29], s[60:61], vcc
s_cbranch_scc1 .Lzsp7
.Lzok7:
Mp 248,56,44
s_sub_u32 s57, s57, 1
Mp 252,60,44
s_waitcnt lgkmcnt(8)
Mp 244,60,40
s_cmp_gt_i32 s57, s93
s_cbranch_scc0 .LzLX
.LzL:
v_mfma_f32_16x16x32_bf16 v[64:67], a[0:3], a[64:67], v[16:19]
Rb 48,1,12288
v_mfma_f32_16x16x32_bf16 v[68:71], a[0:3], a[68:71], v[20:23]
v_exp_f32_e32 v56, v112
Mv 68,4,76
Rb 52,1,13312
s_add_u32 m0, s36, 98304
Mv 64,4,72
buffer_load_dwordx4 v2, s[20:23], s56 offen offset:0 sc1 lds
Mv 64,8,80
Rb 56,1,14336
v_add_f32_e32 v14, v14, v56
Mv 68,8,84
v_exp_f32_e32 v24, v113
Mv 68,12,92
Rb 60,1,15360
s_waitcnt lgkmcnt(8)
Mv 64,12,88
v_exp_f32_e32 v57, v114
Mv 64,16,96
Rb 0,1,16384
v_add_f32_e32 v14, v14, v24
Mv 68,16,100
)" R"(
v_exp_f32_e32 v25, v115
Mv 68,20,108
Rb 4,1,17408
Cv 56,56,24
Mv 64,20,104
buffer_load_dwordx4 v2, s[20:23], s56 offen offset:1024 sc1 lds
Mv 64,24,112
Rb 8,1,18432
v_add_f32_e32 v14, v14, v57
Mv 68,24,116
v_exp_f32_e32 v58, v120
Mv 68,28,124
Rb 12,1,19456
s_waitcnt lgkmcnt(8)
Mv 64,28,120
v_exp_f32_e32 v26, v121
v_mfma_f32_16x16x32_bf16 v[72:75], a[32:35], a[64:67], v[16:19]
Rb 16,1,24576
v_add_f32_e32 v14, v14, v25
v_mfma_f32_16x16x32_bf16 v[76:79], a[32:35], a[68:71], v[20:23]
v_exp_f32_e32 v59, v122
Mv 76,36,76
Rb 20,1,25600
Cv 57,57,25
Mv 72,36,72
buffer_load_dwordx4 v2, s[20:23], s56 offen offset:2048 sc1 lds
Mv 72,40,80
Rb 24,1,26624
v_add_f32_e32 v14, v14, v58
Mv 76,40,84
v_exp_f32_e32 v27, v123
Mv 76,44,92
Rb 28,1,27648
s_waitcnt lgkmcnt(8)
Mv 72,44,88
v_exp_f32_e32 v60, v116
Mv 72,48,96
Rb 32,1,20480
v_add_f32_e32 v14, v14, v26
Mv 76,48,100
v_exp_f32_e32 v28, v117
Mv 76,52,108
Rb 36,1,21504
Cv 58,58,26
Mv 72,52,104
buffer_load_dwordx4 v2, s[20:23], s56 offen offset:3072 sc1 lds
Mv 72,56,112
Rb 40,1,22528
v_add_f32_e32 v14, v14, v59
Mv 76,56,116
v_exp_f32_e32 v61, v118
Mv 76,60,124
Rb 44,1,23552
s_waitcnt lgkmcnt(8)
Mv 72,60,120
v_exp_f32_e32 v29, v119
v_mfma_f32_16x16x32_bf16 v[80:83], a[0:3], a[64:67], v[16:19]
Rb 48,1,28672
v_add_f32_e32 v14, v14, v27
v_mfma_f32_16x16x32_bf16 v[84:87], a[0:3], a[68:71], v[20:23]
v_exp_f32_e32 v62, v124
Mv 84,4,76
Rb 52,1,29696
s_add_u32 m0, s36, 102400
)" R"(
Mv 80,4,72
buffer_load_dwordx4 v3, s[20:23], s56 offen offset:0 sc1 lds
Mv 80,8,80
Rb 56,1,30720
Cv 59,59,27
Mv 84,8,84
v_exp_f32_e32 v24, v125
Mv 84,12,92
Rb 60,1,31744
s_waitcnt lgkmcnt(8)
Mv 80,12,88
v_exp_f32_e32 v63, v126
Mv 80,16,96
v_exp_f32_e32 v25, v127
Mv 84,16,100
v_add_f32_e32 v15, v15, v60
v_add_f32_e32 v15, v15, v28
Mv 84,20,108
Cv 60,60,28
v_add_f32_e32 v15, v15, v61
Mv 80,20,104
buffer_load_dwordx4 v3, s[20:23], s56 offen offset:1024 sc1 lds
Mv 80,24,112
v_add_f32_e32 v15, v15, v29
Cv 61,61,29
Mv 84,24,116
v_add_f32_e32 v15, v15, v62
v_add_f32_e32 v15, v15, v24
Mv 84,28,124
Cv 62,62,24
v_add_f32_e32 v15, v15, v63
Mv 80,28,120
s_waitcnt vmcnt(14) lgkmcnt(0)
s_barrier
v_mfma_f32_16x16x32_bf16 v[88:91], a[32:35], a[64:67], v[16:19]
Rb 0,8,0
v_add_f32_e32 v15, v15, v25
v_mfma_f32_16x16x32_bf16 v[92:95], a[32:35], a[68:71], v[20:23]
Rb 4,8,1024
Cv 63,63,25
Mv 92,36,76
Rb 8,8,2048
v_max_f32_e32 v11, v14, v15
Mv 88,36,72
Rb 12,8,3072
buffer_load_dwordx4 v3, s[20:23], s56 offen offset:2048 sc1 lds
Mv 88,40,80
Rb 16,8,4096
v_cmp_nge_f32_e32 vcc, s62, v11
Mv 92,40,84
Rb 20,8,5120
s_add_u32 s55, s55, 0x10000
Mv 92,44,92
Rb 24,8,6144
Mv 88,44,88
Rb 28,8,7168
Mv 88,48,96
Rb 32,8,8192
Mv 92,48,100
Rb 36,8,9216
Mv 92,52,108
Rb 40,8,10240
Mv 88,52,104
Rb 44,8,11264
buffer_load_dwordx4 v3, s[20:23], s56 offen offset:3072 sc1 lds
)" R"(
Mv 88,56,112
s_cmp_eq_u32 s57, 2
s_cselect_b32 s55, s59, s55
Mv 92,56,116
s_add_u32 s56, s56, 0x10000
Mv 92,60,124
s_waitcnt lgkmcnt(8)
s_cbranch_vccnz .Lzfx1
.Lzfr1:
Mv 88,60,120
v_add_f32_e32 v12, v12, v14
v_add_f32_e32 v13, v13, v15
Mp 128,0,48
Rb 48,8,12288
Mp 136,0,52
s_cmp_le_i32 s57, s58
s_cbranch_scc1 .Lzmk2
.Lzmr2:
Mp 140,4,52
Rb 52,8,13312
s_add_u32 m0, s36, 0
Mp 132,4,48
buffer_load_dwordx4 v2, s[20:23], s55 offen offset:0 sc1 lds
Mp 144,8,48
Rb 56,8,14336
Mp 152,8,52
v_exp_f32_e32 v32, v64
Mp 156,12,52
Rb 60,8,15360
s_waitcnt lgkmcnt(8)
Mp 148,12,48
v_exp_f32_e32 v26, v65
Mp 160,16,48
Rb 0,8,16384
v_add_f32_e32 v14, v32, v26
Mp 168,16,52
v_exp_f32_e32 v33, v66
Mp 172,20,52
Rb 4,8,17408
Cv 32,32,26
Mp 164,20,48
buffer_load_dwordx4 v2, s[20:23], s55 offen offset:1024 sc1 lds
Mp 176,24,48
Rb 8,8,18432
v_add_f32_e32 v14, v14, v33
Mp 184,24,52
v_exp_f32_e32 v27, v67
Mp 188,28,52
Rb 12,8,19456
s_waitcnt lgkmcnt(8)
Mp 180,28,48
v_exp_f32_e32 v34, v72
Mp 192,32,48
Rb 16,8,20480
v_add_f32_e32 v14, v14, v27
Mp 200,32,52
v_exp_f32_e32 v28, v73
Mp 204,36,52
Rb 20,8,21504
Cv 33,33,27
Mp 196,36,48
buffer_load_dwordx4 v2, s[20:23], s55 offen offset:2048 sc1 lds
Mp 208,40,48
Rb 24,8,22528
)" R"(
v_add_f32_e32 v14, v14, v34
Mp 216,40,52
v_exp_f32_e32 v35, v74
Mp 220,44,52
Rb 28,8,23552
s_waitcnt lgkmcnt(8)
Mp 212,44,48
v_exp_f32_e32 v29, v75
Mp 224,48,48
Rb 32,8,24576
v_add_f32_e32 v14, v14, v28
Mp 232,48,52
v_exp_f32_e32 v36, v68
Mp 236,52,52
Rb 36,8,25600
Cv 34,34,28
Mp 228,52,48
buffer_load_dwordx4 v2, s[20:23], s55 offen offset:3072 sc1 lds
Mp 240,56,48
Rb 40,8,26624
v_add_f32_e32 v14, v14, v35
Mp 248,56,52
v_exp_f32_e32 v24, v69
Mp 252,60,52
Rb 44,8,27648
s_waitcnt lgkmcnt(8)
Mp 244,60,48
v_exp_f32_e32 v37, v70
Mp 128,0,56
Rb 48,8,28672
v_add_f32_e32 v14, v14, v29
Mp 136,0,60
v_exp_f32_e32 v25, v71
Mp 140,4,60
Rb 52,8,29696
s_add_u32 m0, s36, 4096
Mp 132,4,56
buffer_load_dwordx4 v3, s[20:23], s55 offen offset:0 sc1 lds
Mp 144,8,56
Rb 56,8,30720
Cv 35,35,29
Mp 152,8,60
v_exp_f32_e32 v38, v76
Mp 156,12,60
Rb 60,8,31744
s_waitcnt lgkmcnt(8)
Mp 148,12,56
v_exp_f32_e32 v26, v77
Mp 160,16,56
v_exp_f32_e32 v39, v78
Mp 168,16,60
v_exp_f32_e32 v27, v79
Mp 172,20,60
v_add_f32_e32 v15, v36, v24
Cv 36,36,24
Mp 164,20,56
buffer_load_dwordx4 v3, s[20:23], s55 offen offset:1024 sc1 lds
Mp 176,24,56
v_add_f32_e32 v15, v15, v37
v_add_f32_e32 v15, v15, v25
Mp 184,24,60
Cv 37,37,25
v_add_f32_e32 v15, v15, v38
Mp 188,28,60
)" R"(
v_add_f32_e32 v15, v15, v26
Cv 38,38,26
Mp 180,28,56
s_waitcnt vmcnt(14) lgkmcnt(0)
s_barrier
Mp 192,32,56
Rb 0,1,32768
v_add_f32_e32 v15, v15, v39
Mp 200,32,60
Rb 4,1,33792
v_add_f32_e32 v15, v15, v27
Mp 204,36,60
Rb 8,1,34816
Cv 39,39,27
Mp 196,36,56
Rb 12,1,35840
buffer_load_dwordx4 v3, s[20:23], s55 offen offset:2048 sc1 lds
Mp 208,40,56
Rb 16,1,40960
s_sub_u32 s57, s57, 1
Mp 216,40,60
Rb 20,1,41984
Mp 220,44,60
Rb 24,1,43008
Mp 212,44,56
Rb 28,1,44032
Mp 224,48,56
Rb 32,1,36864
Mp 232,48,60
Rb 36,1,37888
Mp 236,52,60
Rb 40,1,38912
Mp 228,52,56
Rb 44,1,39936
buffer_load_dwordx4 v3, s[20:23], s55 offen offset:3072 sc1 lds
Mp 240,56,56
Mp 248,56,60
Mp 252,60,60
s_waitcnt lgkmcnt(8)
Mp 244,60,56
v_mfma_f32_16x16x32_bf16 v[96:99], a[0:3], a[64:67], v[16:19]
Rb 48,1,45056
buffer_load_dword v4, v5, s[20:23], s50 offen offset:0 sc0 sc1
v_mfma_f32_16x16x32_bf16 v[100:103], a[0:3], a[68:71], v[20:23]
s_lshl_b64 s[60:61], s[60:61], 8
s_or_b32 s60, s60, 0xff
Mv 100,4,76
Rb 52,1,46080
s_add_u32 m0, s36, 65536
Mv 96,4,72
buffer_load_dwordx4 v2, s[20:23], s56 offen offset:0 sc1 lds
Mv 96,8,80
Rb 56,1,47104
Mv 100,8,84
v_exp_f32_e32 v40, v80
Mv 100,12,92
Rb 60,1,48128
s_waitcnt lgkmcnt(8)
Mv 96,12,88
v_exp_f32_e32 v28, v81
Mv 96,16,96
Rb 0,1,49152
v_add_f32_e32 v14, v14, v40
Mv 100,16,100
v_exp_f32_e32 v41, v82
)" R"(
Mv 100,20,108
Rb 4,1,50176
v_add_f32_e32 v14, v14, v28
Mv 96,20,104
buffer_load_dwordx4 v2, s[20:23], s56 offen offset:1024 sc1 lds
Mv 96,24,112
Rb 8,1,51200
Cv 40,40,28
Mv 100,24,116
v_exp_f32_e32 v29, v83
Mv 100,28,124
Rb 12,1,52224
s_waitcnt lgkmcnt(8)
Mv 96,28,120
v_exp_f32_e32 v42, v88
v_mfma_f32_16x16x32_bf16 v[104:107], a[32:35], a[64:67], v[16:19]
Rb 16,1,57344
v_add_f32_e32 v14, v14, v41
v_mfma_f32_16x16x32_bf16 v[108:111], a[32:35], a[68:71], v[20:23]
v_exp_f32_e32 v24, v89
Mv 108,36,76
Rb 20,1,58368
v_add_f32_e32 v14, v14, v29
Mv 104,36,72
buffer_load_dwordx4 v2, s[20:23], s56 offen offset:2048 sc1 lds
Mv 104,40,80
Rb 24,1,59392
Cv 41,41,29
Mv 108,40,84
v_exp_f32_e32 v43, v90
Mv 108,44,92
Rb 28,1,60416
s_waitcnt lgkmcnt(8)
Mv 104,44,88
v_exp_f32_e32 v25, v91
Mv 104,48,96
Rb 32,1,53248
v_add_f32_e32 v14, v14, v42
Mv 108,48,100
v_exp_f32_e32 v44, v84
Mv 108,52,108
Rb 36,1,54272
v_add_f32_e32 v14, v14, v24
Mv 104,52,104
buffer_load_dwordx4 v2, s[20:23], s56 offen offset:3072 sc1 lds
Mv 104,56,112
Rb 40,1,55296
Cv 42,42,24
Mv 108,56,116
v_exp_f32_e32 v26, v85
Mv 108,60,124
Rb 44,1,56320
s_waitcnt lgkmcnt(8)
Mv 104,60,120
v_exp_f32_e32 v45, v86
v_mfma_f32_16x16x32_bf16 v[112:115], a[0:3], a[64:67], v[16:19]
Rb 48,1,61440
v_add_f32_e32 v14, v14, v43
v_mfma_f32_16x16x32_bf16 v[116:119], a[0:3], a[68:71], v[20:23]
v_exp_f32_e32 v27, v87
Mv 116,4,76
Rb 52,1,62464
s_add_u32 m0, s36, 69632
Mv 112,4,72
)" R"(
buffer_load_dwordx4 v3, s[20:23], s56 offen offset:0 sc1 lds
Mv 112,8,80
Rb 56,1,63488
v_add_f32_e32 v14, v14, v25
Mv 116,8,84
v_exp_f32_e32 v46, v92
Mv 116,12,92
Rb 60,1,64512
s_waitcnt lgkmcnt(8)
Mv 112,12,88
v_exp_f32_e32 v28, v93
Mv 112,16,96
v_exp_f32_e32 v47, v94
Mv 116,16,100
v_exp_f32_e32 v29, v95
Mv 116,20,108
Cv 43,43,25
v_add_f32_e32 v15, v15, v44
Mv 112,20,104
buffer_load_dwordx4 v3, s[20:23], s56 offen offset:1024 sc1 lds
Mv 112,24,112
v_add_f32_e32 v15, v15, v26
Cv 44,44,26
Mv 116,24,116
v_add_f32_e32 v15, v15, v45
v_add_f32_e32 v15, v15, v27
Mv 116,28,124
Cv 45,45,27
v_add_f32_e32 v15, v15, v46
Mv 112,28,120
s_waitcnt vmcnt(15) lgkmcnt(0)
s_barrier
v_mfma_f32_16x16x32_bf16 v[120:123], a[32:35], a[64:67], v[16:19]
Rb 0,8,32768
v_add_f32_e32 v15, v15, v28
v_mfma_f32_16x16x32_bf16 v[124:127], a[32:35], a[68:71], v[20:23]
Rb 4,8,33792
Cv 46,46,28
Mv 124,36,76
Rb 8,8,34816
v_add_f32_e32 v15, v15, v47
Mv 120,36,72
Rb 12,8,35840
buffer_load_dwordx4 v3, s[20:23], s56 offen offset:2048 sc1 lds
Mv 120,40,80
Rb 16,8,36864
v_add_f32_e32 v15, v15, v29
Mv 124,40,84
Rb 20,8,37888
Cv 47,47,29
Mv 124,44,92
Rb 24,8,38912
v_max_f32_e32 v11, v14, v15
Mv 120,44,88
Rb 28,8,39936
v_cmp_nge_f32_e32 vcc, s62, v11
Mv 120,48,96
Rb 32,8,40960
s_add_u32 s55, s55, 0x10000
Mv 124,48,100
Rb 36,8,41984
Mv 124,52,108
Rb 40,8,43008
Mv 120,52,104
Rb 44,8,44032
)" R"(
buffer_load_dwordx4 v3, s[20:23], s56 offen offset:3072 sc1 lds
Mv 120,56,112
s_cmp_eq_u32 s57, 2
s_cselect_b32 s55, s59, s55
Mv 124,56,116
s_add_u32 s56, s56, 0x10000
Mv 124,60,124
s_waitcnt lgkmcnt(8)
s_cbranch_vccnz .Lzfx2
.Lzfr2:
Mv 120,60,120
v_add_f32_e32 v12, v12, v14
v_add_f32_e32 v13, v13, v15
Mp 128,0,32
Rb 48,8,45056
Mp 136,0,36
s_cmp_le_i32 s57, s58
s_cbranch_scc1 .Lzmk3
.Lzmr3:
Mp 140,4,36
Rb 52,8,46080
s_add_u32 m0, s36, 32768
Mp 132,4,32
buffer_load_dwordx4 v2, s[20:23], s55 offen offset:0 sc1 lds
Mp 144,8,32
Rb 56,8,47104
Mp 152,8,36
v_exp_f32_e32 v48, v96
Mp 156,12,36
Rb 60,8,48128
s_waitcnt lgkmcnt(8)
Mp 148,12,32
v_exp_f32_e32 v24, v97
Mp 160,16,32
Rb 0,8,49152
v_add_f32_e32 v14, v48, v24
Mp 168,16,36
v_exp_f32_e32 v49, v98
Mp 172,20,36
Rb 4,8,50176
Cv 48,48,24
Mp 164,20,32
buffer_load_dwordx4 v2, s[20:23], s55 offen offset:1024 sc1 lds
Mp 176,24,32
Rb 8,8,51200
v_add_f32_e32 v14, v14, v49
Mp 184,24,36
v_exp_f32_e32 v25, v99
Mp 188,28,36
Rb 12,8,52224
s_waitcnt lgkmcnt(8)
Mp 180,28,32
v_exp_f32_e32 v50, v104
Mp 192,32,32
Rb 16,8,53248
v_add_f32_e32 v14, v14, v25
Mp 200,32,36
v_exp_f32_e32 v26, v105
Mp 204,36,36
Rb 20,8,54272
Cv 49,49,25
Mp 196,36,32
buffer_load_dwordx4 v2, s[20:23], s55 offen offset:2048 sc1 lds
Mp 208,40,32
)" R"(
Rb 24,8,55296
v_add_f32_e32 v14, v14, v50
Mp 216,40,36
v_exp_f32_e32 v51, v106
Mp 220,44,36
Rb 28,8,56320
s_waitcnt lgkmcnt(8)
Mp 212,44,32
v_exp_f32_e32 v27, v107
Mp 224,48,32
Rb 32,8,57344
v_add_f32_e32 v14, v14, v26
Mp 232,48,36
v_exp_f32_e32 v52, v100
Mp 236,52,36
Rb 36,8,58368
Cv 50,50,26
Mp 228,52,32
buffer_load_dwordx4 v2, s[20:23], s55 offen offset:3072 sc1 lds
Mp 240,56,32
Rb 40,8,59392
v_add_f32_e32 v14, v14, v51
Mp 248,56,36
v_exp_f32_e32 v28, v101
Mp 252,60,36
Rb 44,8,60416
s_waitcnt lgkmcnt(8)
Mp 244,60,32
v_exp_f32_e32 v53, v102
Mp 128,0,40
Rb 48,8,61440
v_add_f32_e32 v14, v14, v27
Mp 136,0,44
v_exp_f32_e32 v29, v103
Mp 140,4,44
Rb 52,8,62464
s_add_u32 m0, s36, 36864
Mp 132,4,40
buffer_load_dwordx4 v3, s[20:23], s55 offen offset:0 sc1 lds
Mp 144,8,40
Rb 56,8,63488
Cv 51,51,27
Mp 152,8,44
v_exp_f32_e32 v54, v108
Mp 156,12,44
Rb 60,8,64512
s_waitcnt lgkmcnt(8)
Mp 148,12,40
v_exp_f32_e32 v24, v109
Mp 160,16,40
v_exp_f32_e32 v55, v110
Mp 168,16,44
v_exp_f32_e32 v25, v111
Mp 172,20,44
v_add_f32_e32 v15, v52, v28
Cv 52,52,28
Mp 164,20,40
buffer_load_dwordx4 v3, s[20:23], s55 offen offset:1024 sc1 lds
Mp 176,24,40
v_add_f32_e32 v15, v15, v53
v_add_f32_e32 v15, v15, v29
Mp 184,24,44
Cv 53,53,29
v_add_f32_e32 v15, v15, v54
)" R"(
Mp 188,28,44
v_add_f32_e32 v15, v15, v24
Cv 54,54,24
Mp 180,28,40
s_waitcnt vmcnt(14) lgkmcnt(0)
s_barrier
Mp 192,32,40
Rb 0,1,0
v_add_f32_e32 v15, v15, v55
Mp 200,32,44
Rb 4,1,1024
v_add_f32_e32 v15, v15, v25
Mp 204,36,44
Rb 8,1,2048
Cv 55,55,25
Mp 196,36,40
Rb 12,1,3072
buffer_load_dwordx4 v3, s[20:23], s55 offen offset:2048 sc1 lds
Mp 208,40,40
Rb 16,1,8192
s_sub_u32 s57, s57, 1
Mp 216,40,44
Rb 20,1,9216
Mp 220,44,44
Rb 24,1,10240
Mp 212,44,40
Rb 28,1,11264
Mp 224,48,40
Rb 32,1,4096
Mp 232,48,44
Rb 36,1,5120
Mp 236,52,44
Rb 40,1,6144
Mp 228,52,40
Rb 44,1,7168
buffer_load_dwordx4 v3, s[20:23], s55 offen offset:3072 sc1 lds
Mp 240,56,40
v_cmp_eq_u32_e32 vcc, s3, v4
s_andn2_b64 s[28:29], s[60:61], vcc
s_cbranch_scc1 .Lzsp8
.Lzok8:
Mp 248,56,44
Mp 252,60,44
s_waitcnt lgkmcnt(8)
Mp 244,60,40
s_cmp_gt_i32 s57, s93
s_cbranch_scc1 .LzL
.LzLX:
s_cmp_eq_u32 s94, 0
s_cbranch_scc1 .LzEB
s_cmp_eq_u32 s94, 2
s_cbranch_scc1 .LzDA
s_lshl1_add_u32 s57, s35, 2
s_mov_b32 s59, s67
s_add_u32 s56, s89, 0x2000000
s_mov_b32 s94, 2
Mz 64,0,224
Rb 48,1,12288
v_lshrrev_b32 v4, 4, v0
v_accvgpr_mov_b32 a64, a224
Mz 68,0,228
v_lshlrev_b32 v4, 4, v4
v_add_lshl_u32 v4, v4, v0, 3
v_accvgpr_mov_b32 a65, a225
Mv 68,4,236
)" R"(
Rb 52,1,13312
s_add_u32 m0, s36, 98304
v_accvgpr_mov_b32 a66, a226
Mv 64,4,232
buffer_load_dwordx4 v2, s[20:23], s56 offen offset:0 sc1 lds
v_accvgpr_mov_b32 a67, a227
Mv 64,8,240
Rb 56,1,14336
s_add_u32 s92, s36, 131072
v_accvgpr_mov_b32 a68, a228
Mv 68,8,244
v_add_u32 v4, s92, v4
v_accvgpr_mov_b32 a69, a229
s_waitcnt vmcnt(1)
Mv 68,12,252
Rb 60,1,15360
v_accvgpr_mov_b32 a70, a230
s_waitcnt lgkmcnt(8)
Mv 64,12,248
buffer_load_dwordx4 v2, s[20:23], s56 offen offset:1024 sc1 lds
Rt 156,4,0
Mv 64,16,192
Rb 0,1,16384
Rt 158,4,128
v_accvgpr_mov_b32 a71, a231
Mv 68,16,196
Rt 160,4,1024
v_exp_f32_e32 v56, v112
Mv 68,20,204
Rb 4,1,17408
Rt 162,4,1152
v_add_f32_e32 v14, v14, v56
Mv 64,20,200
buffer_load_dwordx4 v2, s[20:23], s56 offen offset:2048 sc1 lds
Rt 164,4,2048
Mv 64,24,208
Rb 8,1,18432
Rt 166,4,2176
v_accvgpr_mov_b32 a72, a232
Mv 68,24,212
Rt 168,4,3072
v_exp_f32_e32 v26, v113
Mv 68,28,220
Rb 12,1,19456
Rt 170,4,3200
Mv 64,28,216
buffer_load_dwordx4 v2, s[20:23], s56 offen offset:3072 sc1 lds
Rt 172,4,4096
Mz 72,32,224
Rb 16,1,24576
Rt 174,4,4224
v_accvgpr_mov_b32 a73, a233
Mz 76,32,228
Rt 176,4,5120
v_exp_f32_e32 v57, v114
Mv 76,36,236
Rb 20,1,25600
s_add_u32 m0, s36, 102400
Rt 178,4,5248
Mv 72,36,232
buffer_load_dwordx4 v3, s[20:23], s56 offen offset:0 sc1 lds
Rt 180,4,6144
Mv 72,40,240
Rb 24,1,26624
)" R"(
Rt 182,4,6272
v_add_f32_e32 v14, v14, v26
Mv 76,40,244
v_exp_f32_e32 v27, v115
Cv 56,56,26
Mv 76,44,252
Rb 28,1,27648
v_add_f32_e32 v14, v14, v57
Mv 72,44,248
buffer_load_dwordx4 v3, s[20:23], s56 offen offset:1024 sc1 lds
v_add_f32_e32 v14, v14, v27
Mv 72,48,192
Rb 32,1,20480
v_and_b32 v4, 14, v0
v_accvgpr_mov_b32 a74, a234
Mv 76,48,196
v_min_u32 v4, 1, v4
v_lshlrev_b32 v4, 8, v4
Cv 57,57,27
Mv 76,52,204
Rb 36,1,21504
v_and_or_b32 v4, v0, 48, v4
v_accvgpr_mov_b32 a75, a235
Mv 72,52,200
buffer_load_dwordx4 v3, s[20:23], s56 offen offset:2048 sc1 lds
v_accvgpr_mov_b32 a76, a236
Mv 72,56,208
Rb 40,1,22528
v_lshlrev_b32 v4, 1, v4
v_accvgpr_mov_b32 a77, a237
Mv 76,56,212
v_and_or_b32 v4, v1, 16, v4
s_lshl_b32 s92, s33, 7
v_accvgpr_mov_b32 a78, a238
Mv 76,60,220
Rb 44,1,23552
v_accvgpr_mov_b32 a79, a239
s_waitcnt lgkmcnt(5)
Mv 72,60,216
v_mfma_f32_16x16x32_bf16 a[184:187], a[156:159], a[128:131], 0
buffer_load_dwordx4 v3, s[20:23], s56 offen offset:3072 sc1 lds
v_accvgpr_mov_b32 a80, a240
Mz 80,0,224
Ma 184,160,132
Rb 48,1,28672
s_add_u32 s92, s92, 162816
v_accvgpr_mov_b32 a81, a241
Mz 84,0,228
Ma 184,164,136
v_add_u32 v4, s92, v4
v_exp_f32_e32 v58, v120
Mv 84,4,236
Ma 184,168,140
Rb 52,1,29696
v_exp_f32_e32 v28, v121
Mv 80,4,232
Ma 184,172,144
v_exp_f32_e32 v59, v122
v_add_f32_e32 v14, v14, v58
Mv 80,8,240
Ma 184,176,148
Rb 56,1,30720
s_mov_b32 s24, s70
s_mov_b32 s25, s71
)" R"(
Mv 84,8,244
Ma 184,180,152
s_mul_i32 s83, s82, 14336
s_lshl_b32 s92, s33, 12
v_add_f32_e32 v14, v14, v28
Mv 84,12,252
Rb 60,1,31744
Cv 58,58,28
s_waitcnt lgkmcnt(8)
Mv 80,12,248
s_add_u32 s83, s83, s92
s_add_u32 m0, s92, 131072
v_add_f32_e32 v14, v14, v59
Mv 80,16,192
v_exp_f32_e32 v29, v123
v_accvgpr_mov_b32 a82, a242
Mv 84,16,196
v_exp_f32_e32 v60, v116
v_add_f32_e32 v14, v14, v29
Mv 84,20,204
ds_write_b128 v4, a[184:187]
v_and_b32 v4, 0x70, v4
Cv 59,59,29
Mv 80,20,200
v_add_u32 v4, 0x27c00, v4
v_exp_f32_e32 v24, v117
Mv 80,24,208
v_exp_f32_e32 v61, v118
v_add_f32_e32 v15, v15, v60
Mv 84,24,212
v_exp_f32_e32 v25, v119
v_add_f32_e32 v15, v15, v24
Mv 84,28,220
v_exp_f32_e32 v62, v124
Cv 60,60,24
Mv 80,28,216
v_add_f32_e32 v15, v15, v61
s_waitcnt lgkmcnt(0)
s_barrier
Mz 88,32,224
Rb 0,8,0
v_add_f32_e32 v15, v15, v25
v_accvgpr_mov_b32 a83, a243
Mz 92,32,228
Rb 4,8,16384
buffer_load_dwordx4 v1, s[24:27], s83 offen offset:0 lds
Mv 92,36,236
Rb 8,8,1024
v_exp_f32_e32 v26, v125
Mv 88,36,232
Rb 12,8,17408
v_exp_f32_e32 v63, v126
Mv 88,40,240
Rb 16,8,2048
buffer_load_dwordx4 v1, s[24:27], s83 offen offset:1024 lds
Mv 92,40,244
Rb 20,8,18432
v_exp_f32_e32 v27, v127
Mv 92,44,252
Rb 24,8,3072
Cv 61,61,25
v_add_f32_e32 v15, v15, v62
Mv 88,44,248
Rb 28,8,19456
v_add_f32_e32 v15, v15, v26
)" R"(
Cv 62,62,26
Mv 88,48,192
Rb 32,8,4096
v_add_f32_e32 v15, v15, v63
v_accvgpr_mov_b32 a84, a244
Mv 92,48,196
Rb 36,8,20480
buffer_load_dwordx4 v1, s[24:27], s83 offen offset:2048 lds
Mv 92,52,204
Rb 40,8,5120
v_add_f32_e32 v15, v15, v27
Cv 63,63,27
Mv 88,52,200
Rb 44,8,21504
v_max_f32_e32 v11, v14, v15
v_cmp_nge_f32_e32 vcc, s62, v11
Mv 88,56,208
buffer_load_dwordx4 v1, s[24:27], s83 offen offset:3072 lds
s_add_u32 s55, s55, 0x10000
Mv 92,56,212
s_add_u32 s56, s56, 0x10000
s_cmp_eq_u32 s57, 2
s_cselect_b32 s55, s59, s55
Mv 92,60,220
v_accvgpr_mov_b32 a85, a245
s_waitcnt lgkmcnt(8)
s_cbranch_vccnz .Lzfx3
.Lzfr3:
Mv 88,60,216
v_add_f32_e32 v12, v12, v14
v_add_f32_e32 v13, v13, v15
v_mov_b32 v14, v16
Mp 128,0,48
Rb 48,8,6144
v_mov_b32 v15, v20
v_accvgpr_mov_b32 a86, a246
Mp 136,0,52
buffer_store_dwordx4 v[12:15], v1, s[20:23], s66 offen sc0 sc1
s_cmp_le_i32 s57, s58
s_cbranch_scc1 .Lzmk4
.Lzmr4:
Mp 136,4,60
Rb 52,8,22528
s_add_u32 m0, s36, 0
v_accvgpr_mov_b32 a87, a247
Mp 128,4,56
buffer_load_dwordx4 v2, s[20:23], s55 offen offset:0 sc1 lds
v_accvgpr_mov_b32 a88, a248
Mp 132,8,48
Rb 56,8,7168
v_exp_f32_e32 v32, v64
Mp 140,8,52
v_exp_f32_e32 v28, v65
v_accvgpr_mov_b32 a89, a249
Mp 140,12,60
Rb 60,8,23552
v_exp_f32_e32 v33, v66
s_waitcnt lgkmcnt(8)
Mp 132,12,56
v_exp_f32_e32 v29, v67
v_add_f32_e32 v14, v32, v28
Mp 144,16,48
Rb 0,8,8192
v_exp_f32_e32 v34, v72
)" R"(
Mp 152,16,52
v_exp_f32_e32 v24, v73
v_exp_f32_e32 v35, v74
Mp 152,20,60
Rb 4,8,24576
v_exp_f32_e32 v25, v75
Mp 144,20,56
buffer_load_dwordx4 v2, s[20:23], s55 offen offset:1024 sc1 lds
Cv 128,128,129
Mp 148,24,48
Rb 8,8,9216
Cv 129,130,131
Cv 130,132,133
Cv 32,32,28
Mp 156,24,52
Cv 131,134,135
Cv 136,136,137
v_add_f32_e32 v14, v14, v33
Mp 156,28,60
Rb 12,8,25600
Cv 137,138,139
s_waitcnt lgkmcnt(8)
Mp 148,28,56
Cv 138,140,141
Cv 139,142,143
v_exp_f32_e32 v36, v68
Mp 160,32,48
Rb 16,8,10240
v_exp_f32_e32 v26, v69
Mp 168,32,52
v_exp_f32_e32 v37, v70
v_add_f32_e32 v14, v14, v29
Mp 168,36,60
Rb 20,8,26624
v_exp_f32_e32 v27, v71
Cv 33,33,29
Mp 160,36,56
buffer_load_dwordx4 v2, s[20:23], s55 offen offset:2048 sc1 lds
Cv 144,144,145
Mp 164,40,48
Rb 24,8,11264
Cv 145,146,147
Cv 146,148,149
Mp 172,40,52
buffer_store_dwordx4 v[128:131], v2, s[20:23], s64 offen offset:0 sc0 sc1
v_add_f32_e32 v14, v14, v34
Mp 172,44,60
Rb 28,8,27648
Cv 147,150,151
v_add_f32_e32 v14, v14, v24
s_waitcnt lgkmcnt(8)
Mp 164,44,56
Cv 152,152,153
Cv 153,154,155
Cv 154,156,157
Mp 176,48,48
Rb 32,8,12288
Cv 155,158,159
Cv 34,34,24
Mp 184,48,52
v_exp_f32_e32 v38, v76
v_exp_f32_e32 v28, v77
Mp 184,52,60
Rb 36,8,28672
)" R"(
v_exp_f32_e32 v39, v78
Mp 176,52,56
buffer_load_dwordx4 v2, s[20:23], s55 offen offset:3072 sc1 lds
Cv 160,160,161
Mp 180,56,48
Rb 40,8,13312
Cv 161,162,163
Cv 162,164,165
v_add_f32_e32 v14, v14, v35
Mp 188,56,52
buffer_store_dwordx4 v[136:139], v2, s[20:23], s64 offen offset:1024 sc0 sc1
v_add_f32_e32 v14, v14, v25
Mp 188,60,60
Rb 44,8,29696
Cv 163,166,167
s_waitcnt lgkmcnt(8)
Mp 180,60,56
Cv 168,168,169
Cv 169,170,171
Cv 170,172,173
Cv 35,35,25
Mp 192,0,48
Rb 48,8,14336
Cv 171,174,175
v_add_f32_e32 v15, v36, v26
Mp 200,0,52
buffer_store_dwordx4 v[144:147], v2, s[20:23], s64 offen offset:2048 sc0 sc1
Cv 36,36,26
Mp 200,4,60
Rb 52,8,30720
s_add_u32 m0, s36, 4096
v_exp_f32_e32 v29, v79
Mp 192,4,56
buffer_load_dwordx4 v3, s[20:23], s55 offen offset:0 sc1 lds
Cv 176,176,177
Mp 196,8,48
Rb 56,8,15360
Cv 177,178,179
Cv 178,180,181
Mp 204,8,52
Cv 179,182,183
Cv 184,184,185
v_add_f32_e32 v15, v15, v37
Mp 204,12,60
Rb 60,8,31744
Cv 185,186,187
v_add_f32_e32 v15, v15, v27
s_waitcnt lgkmcnt(8)
Mp 196,12,56
buffer_store_dwordx4 v[152:155], v2, s[20:23], s64 offen offset:3072 sc0 sc1
Cv 186,188,189
Mp 208,16,48
ds_read_b128 v[132:135], v4 offset:0
Cv 187,190,191
Cv 37,37,27
Mp 216,16,52
ds_read_b128 v[140:143], v4 offset:128
v_add_f32_e32 v15, v15, v38
v_add_f32_e32 v15, v15, v28
Cv 38,38,28
Mp 216,20,60
ds_read_b128 v[148:151], v4 offset:256
v_add_f32_e32 v15, v15, v39
v_add_f32_e32 v15, v15, v29
)" R"(
Mp 208,20,56
buffer_load_dwordx4 v3, s[20:23], s55 offen offset:1024 sc1 lds
ds_read_b128 v[156:159], v4 offset:384
Mp 212,24,48
Cv 192,192,193
Cv 193,194,195
Cv 194,196,197
Cv 39,39,29
Mp 220,24,52
Cv 195,198,199
Cv 200,200,201
s_sub_u32 s57, s57, 1
Mp 220,28,60
buffer_store_dwordx4 v[160:163], v3, s[20:23], s64 offen offset:0 sc0 sc1
Cv 201,202,203
Mp 212,28,56
Cv 202,204,205
v_mov_b32 v16, 0
s_waitcnt lgkmcnt(0)
s_barrier
Mp 224,32,48
Rb 0,1,32768
v_add_f32_e32 v132, v132, v140
v_mov_b32 v17, 0
Mp 232,32,52
Rb 4,1,33792
v_add_f32_e32 v133, v133, v141
Cv 203,206,207
Mp 232,36,60
Rb 8,1,34816
v_add_f32_e32 v134, v134, v142
v_mov_b32 v18, 0
v_mov_b32 v19, 0
Mp 224,36,56
Rb 12,1,35840
buffer_load_dwordx4 v3, s[20:23], s55 offen offset:2048 sc1 lds
Mp 228,40,48
Rb 16,1,40960
v_add_f32_e32 v135, v135, v143
buffer_store_dwordx4 v[168:171], v3, s[20:23], s64 offen offset:1024 sc0 sc1
Mp 236,40,52
Rb 20,1,41984
v_add_f32_e32 v148, v148, v156
v_mov_b32 v20, 0
Mp 236,44,60
Rb 24,1,43008
v_add_f32_e32 v149, v149, v157
Cv 208,208,209
v_mov_b32 v21, 0
Mp 228,44,56
Rb 28,1,44032
v_add_f32_e32 v150, v150, v158
v_add_f32_e32 v151, v151, v159
Mp 240,48,48
Rb 32,1,36864
Cv 132,132,132
v_mov_b32 v22, 0
Mp 248,48,52
Rb 36,1,37888
v_lshlrev_b32 v132, 16, v132
Cv 209,210,211
v_mov_b32 v23, 0
Mp 248,52,60
Rb 40,1,38912
Cv 148,148,148
)" R"(
buffer_store_dwordx4 v[176:179], v3, s[20:23], s64 offen offset:2048 sc0 sc1
Mp 240,52,56
Rb 44,1,39936
buffer_load_dwordx4 v3, s[20:23], s55 offen offset:3072 sc1 lds
Mp 244,56,48
v_lshlrev_b32 v148, 16, v148
Cv 133,133,133
Cv 210,212,213
Mp 252,56,52
v_lshlrev_b32 v133, 16, v133
Cv 149,149,149
v_accvgpr_mov_b32 a90, a250
Mp 252,60,60
v_lshlrev_b32 v149, 16, v149
Cv 211,214,215
s_waitcnt lgkmcnt(8)
Mp 244,60,56
Cv 134,134,134
v_lshlrev_b32 v134, 16, v134
Cv 150,150,150
Mz 96,0,224
Rb 48,1,45056
buffer_load_dword v4, v5, s[20:23], s50 offen offset:0 sc0 sc1
buffer_store_dwordx4 v[184:187], v3, s[20:23], s64 offen offset:3072 sc0 sc1
Mz 100,0,228
s_lshl_b64 s[60:61], s[60:61], 8
s_or_b32 s60, s60, 0xff
Cv 216,216,217
Mv 100,4,236
Rb 52,1,46080
s_add_u32 m0, s36, 65536
v_exp_f32_e32 v40, v80
Mv 96,4,232
buffer_load_dwordx4 v2, s[20:23], s56 offen offset:0 sc1 lds
Cv 217,218,219
v_add_f32_e32 v14, v14, v40
Mv 96,8,240
Rb 56,1,47104
v_lshlrev_b32 v150, 16, v150
Cv 218,220,221
Mv 100,8,244
Cv 135,135,135
v_lshlrev_b32 v135, 16, v135
v_exp_f32_e32 v24, v81
Mv 100,12,252
Rb 60,1,48128
s_add_u32 s64, s64, 0x10000
buffer_store_dwordx4 v[192:195], v2, s[20:23], s64 offen offset:0 sc0 sc1
s_waitcnt lgkmcnt(8)
Mv 96,12,248
Cv 151,151,151
v_lshlrev_b32 v151, 16, v151
v_mul_f32_e32 v140, 0xbfb8aa3b, v132
v_add_f32_e32 v14, v14, v24
Mv 96,16,192
Rb 0,1,49152
v_mul_f32_e32 v141, 0xbfb8aa3b, v133
v_exp_f32_e32 v41, v82
Mv 100,16,196
v_exp_f32_e32 v140, v140
Cv 219,222,223
Cv 40,40,24
Mv 100,20,204
Rb 4,1,50176
)" R"(
v_mul_f32_e32 v142, 0xbfb8aa3b, v134
v_exp_f32_e32 v25, v83
Mv 96,20,200
buffer_load_dwordx4 v2, s[20:23], s56 offen offset:1024 sc1 lds
Cv 224,224,225
v_add_f32_e32 v14, v14, v41
Mv 96,24,208
Rb 8,1,51200
v_mul_f32_e32 v143, 0xbfb8aa3b, v135
buffer_store_dwordx4 v[200:203], v2, s[20:23], s64 offen offset:1024 sc0 sc1
Mv 100,24,212
v_exp_f32_e32 v141, v141
v_exp_f32_e32 v42, v88
Mv 100,28,220
Rb 12,1,52224
Cv 225,226,227
v_add_f32_e32 v14, v14, v25
s_waitcnt lgkmcnt(8)
Mv 96,28,216
v_exp_f32_e32 v142, v142
v_add_f32_e32 v140, 1.0, v140
Cv 41,41,25
Mz 104,32,224
Rb 16,1,57344
v_add_f32_e32 v141, 1.0, v141
v_add_f32_e32 v14, v14, v42
Mz 108,32,228
v_exp_f32_e32 v143, v143
Cv 226,228,229
Mv 108,36,236
Rb 20,1,58368
v_add_f32_e32 v142, 1.0, v142
buffer_store_dwordx4 v[208:211], v2, s[20:23], s64 offen offset:2048 sc0 sc1
Mv 104,36,232
buffer_load_dwordx4 v2, s[20:23], s56 offen offset:2048 sc1 lds
Cv 227,230,231
Mv 104,40,240
Rb 24,1,59392
v_add_f32_e32 v143, 1.0, v143
Cv 232,232,233
Mv 108,40,244
v_rcp_f32_e32 v140, v140
v_exp_f32_e32 v26, v89
Mv 108,44,252
Rb 28,1,60416
Cv 233,234,235
v_add_f32_e32 v14, v14, v26
s_waitcnt lgkmcnt(8)
Mv 104,44,248
v_rcp_f32_e32 v141, v141
v_mul_f32_e32 v132, v132, v140
Cv 42,42,26
Mv 104,48,192
Rb 32,1,53248
v_mul_f32_e32 v133, v133, v141
buffer_store_dwordx4 v[216:219], v2, s[20:23], s64 offen offset:3072 sc0 sc1
Mv 108,48,196
v_rcp_f32_e32 v142, v142
Cv 234,236,237
Mv 108,52,204
Rb 36,1,54272
v_mul_f32_e32 v134, v134, v142
v_exp_f32_e32 v43, v90
Mv 104,52,200
)" R"(
buffer_load_dwordx4 v2, s[20:23], s56 offen offset:3072 sc1 lds
Cv 235,238,239
v_add_f32_e32 v14, v14, v43
Mv 104,56,208
Rb 40,1,55296
Cv 132,132,132
Cv 240,240,241
Mv 108,56,212
v_rcp_f32_e32 v143, v143
v_exp_f32_e32 v27, v91
Mv 108,60,220
Rb 44,1,56320
buffer_store_dwordx4 v[224:227], v3, s[20:23], s64 offen offset:0 sc0 sc1
s_waitcnt lgkmcnt(8)
Mv 104,60,216
v_mul_f32_e32 v135, v135, v143
v_lshlrev_b32 v132, 16, v132
Cv 133,133,133
v_add_f32_e32 v14, v14, v27
Mz 112,0,224
Rb 48,1,61440
v_lshlrev_b32 v133, 16, v133
Cv 43,43,27
Mz 116,0,228
Cv 134,134,134
v_lshlrev_b32 v134, 16, v134
Cv 241,242,243
Mv 116,4,236
Rb 52,1,62464
s_add_u32 m0, s36, 69632
v_exp_f32_e32 v44, v84
Mv 112,4,232
buffer_load_dwordx4 v3, s[20:23], s56 offen offset:0 sc1 lds
Cv 242,244,245
v_add_f32_e32 v15, v15, v44
Mv 112,8,240
Rb 56,1,63488
Cv 135,135,135
buffer_store_dwordx4 v[232:235], v3, s[20:23], s64 offen offset:1024 sc0 sc1
Mv 116,8,244
v_lshlrev_b32 v135, 16, v135
v_mul_f32_e32 v132, v132, v148
v_exp_f32_e32 v28, v85
Mv 116,12,252
Rb 60,1,64512
Cv 243,246,247
v_add_f32_e32 v15, v15, v28
s_waitcnt lgkmcnt(8)
Mv 112,12,248
v_mul_f32_e32 v133, v133, v149
v_mul_f32_e32 v134, v134, v150
v_mul_f32_e32 v135, v135, v151
Cv 44,44,28
Mv 112,16,192
Cv 140,132,133
Cv 141,134,135
v_exp_f32_e32 v45, v86
Mv 116,16,196
v_accvgpr_write_b32 a188, v140
v_accvgpr_write_b32 a189, v141
Cv 248,248,249
v_add_f32_e32 v15, v15, v45
Mv 116,20,204
v_lshrrev_b32 v156, 2, v0
)" R"(
v_mul_u32_u24 v156, 864, v156
buffer_store_dwordx4 v[240:243], v3, s[20:23], s64 offen offset:2048 sc0 sc1
Mv 112,20,200
buffer_load_dwordx4 v3, s[20:23], s56 offen offset:1024 sc1 lds
Cv 249,250,251
Mv 112,24,208
v_lshl_add_u32 v156, v0, 3, v156
s_mul_i32 s92, s33, 224
Cv 250,252,253
Mv 116,24,212
s_add_u32 s92, s92, 131072
v_add_u32 v156, s92, v156
v_exp_f32_e32 v29, v87
Mv 116,28,220
Cv 251,254,255
v_exp_f32_e32 v46, v92
v_add_f32_e32 v15, v15, v29
Mv 112,28,216
v_exp_f32_e32 v24, v93
s_waitcnt vmcnt(31) lgkmcnt(0)
s_barrier
Mz 120,32,224
Rb 0,8,32768
Rt 156,156,0
buffer_store_dwordx4 v[248:251], v3, s[20:23], s64 offen offset:3072 sc0 sc1
Mz 124,32,228
Rb 4,8,33792
Rt 158,156,32
v_exp_f32_e32 v47, v94
Mv 124,36,236
Rb 8,8,34816
Rt 160,156,64
v_exp_f32_e32 v25, v95
Mv 120,36,232
Rb 12,8,35840
buffer_load_dwordx4 v3, s[20:23], s56 offen offset:2048 sc1 lds
Rt 162,156,96
Mv 120,40,240
Rb 16,8,36864
Rt 164,156,128
Cv 45,45,29
v_add_f32_e32 v15, v15, v46
Mv 124,40,244
Rb 20,8,37888
Rt 166,156,160
v_add_f32_e32 v15, v15, v24
Cv 46,46,24
Mv 124,44,252
Rb 24,8,38912
Rt 168,156,192
v_add_f32_e32 v15, v15, v47
v_add_f32_e32 v15, v15, v25
Mv 120,44,248
Rb 28,8,39936
Cv 47,47,25
v_max_f32_e32 v11, v14, v15
v_mov_b32 v24, v14
Mv 120,48,192
Rb 32,8,40960
v_mov_b32 v25, v15
s_add_u32 s55, s55, 0x10000
Mv 124,48,196
Rb 36,8,41984
s_cmp_eq_u32 s57, 2
s_cselect_b32 s55, s59, s55
)" R"(
v_permlane32_swap_b32_e32 v24, v25
Mv 124,52,204
Rb 40,8,43008
v_add_f32_e32 v24, v24, v25
v_mov_b32 v25, v24
Mv 120,52,200
Rb 44,8,44032
buffer_load_dwordx4 v3, s[20:23], s56 offen offset:3072 sc1 lds
v_permlane16_swap_b32_e32 v24, v25
Mv 120,56,208
v_add_f32_e32 v24, v24, v25
v_rcp_f32_e32 v24, v24
Mv 124,56,212
s_add_u32 s56, s56, 0x10000
v_max_f32_e32 v11, v11, v24
v_cmp_nge_f32_e32 vcc, s62, v11
Mv 124,60,220
v_accvgpr_mov_b32 a91, a251
s_waitcnt lgkmcnt(12)
s_cbranch_vccnz .Lzfx4
.Lzfr4:
Mv 120,60,216
v_mov_b32 v12, v14
v_mov_b32 v13, v15
v_accvgpr_mov_b32 a92, a252
v_mfma_f32_16x16x32_bf16 v[128:131], a[0:3], v[32:35], 0
Rb 48,8,45056
v_accvgpr_mov_b32 a93, a253
v_accvgpr_mov_b32 a94, a254
v_mfma_f32_16x16x32_bf16 v[136:139], a[0:3], v[36:39], 0
v_accvgpr_mov_b32 a95, a255
s_cmp_le_i32 s57, s58
s_cbranch_scc1 .Lzmk5
.Lzmr5:
v_mfma_f32_16x16x32_bf16 v[140:143], a[4:7], v[36:39], 0
Rb 52,8,46080
s_add_u32 m0, s36, 32768
v_accvgpr_mov_b32 a96, a192
v_mfma_f32_16x16x32_bf16 v[132:135], a[4:7], v[32:35], 0
buffer_load_dwordx4 v2, s[20:23], s55 offen offset:0 sc1 lds
v_accvgpr_mov_b32 a97, a193
v_mfma_f32_16x16x32_bf16 v[144:147], a[8:11], v[32:35], 0
Rb 56,8,47104
v_and_b32 v11, 14, v0
v_accvgpr_mov_b32 a98, a194
v_mfma_f32_16x16x32_bf16 v[152:155], a[8:11], v[36:39], 0
v_min_u32 v11, 1, v11
v_lshlrev_b32 v11, 13, v11
v_accvgpr_mov_b32 a99, a195
v_mfma_f32_16x16x32_bf16 v[156:159], a[12:15], v[36:39], 0
Rb 60,8,48128
v_accvgpr_mov_b32 a100, a196
s_waitcnt lgkmcnt(8)
v_mfma_f32_16x16x32_bf16 v[148:151], a[12:15], v[32:35], 0
v_mfma_f32_16x16x16_bf16 a[128:131], a[156:157], a[188:189], 0
v_and_or_b32 v11, v0, 1, v11
v_mul_u32_u24 v11, 0x31000, v11
v_accvgpr_mov_b32 a101, a197
v_mfma_f32_16x16x32_bf16 v[160:163], a[16:19], v[32:35], 0
v_mfma_f32_16x16x16_bf16 a[132:135], a[158:159], a[188:189], 0
Rb 0,8,49152
v_and_or_b32 v11, v0, 48, v11
v_accvgpr_mov_b32 a102, a198
v_mfma_f32_16x16x32_bf16 v[168:171], a[16:19], v[36:39], 0
)" R"(
v_mfma_f32_16x16x16_bf16 a[136:139], a[160:161], a[188:189], 0
s_mul_i32 s83, s33, 50176
s_lshl_b32 s92, s82, 6
v_accvgpr_mov_b32 a103, a199
v_mfma_f32_16x16x32_bf16 v[172:175], a[20:23], v[36:39], 0
v_mfma_f32_16x16x16_bf16 a[140:143], a[162:163], a[188:189], 0
Rb 4,8,50176
s_add_u32 s83, s83, s92
v_accvgpr_mov_b32 a104, a200
v_mfma_f32_16x16x32_bf16 v[164:167], a[20:23], v[32:35], 0
v_mfma_f32_16x16x16_bf16 a[144:147], a[164:165], a[188:189], 0
buffer_load_dwordx4 v2, s[20:23], s55 offen offset:1024 sc1 lds
v_accvgpr_mov_b32 a105, a201
v_mfma_f32_16x16x32_bf16 v[176:179], a[24:27], v[32:35], 0
v_mfma_f32_16x16x16_bf16 a[148:151], a[166:167], a[188:189], 0
Rb 8,8,51200
s_add_u32 s83, s83, 0xc150000
v_accvgpr_mov_b32 a106, a202
v_mfma_f32_16x16x32_bf16 v[184:187], a[24:27], v[36:39], 0
v_mfma_f32_16x16x16_bf16 a[152:155], a[168:169], a[188:189], 0
v_exp_f32_e32 v48, v96
v_accvgpr_mov_b32 a107, a203
v_mfma_f32_16x16x32_bf16 v[188:191], a[28:31], v[36:39], 0
Rb 12,8,52224
v_accvgpr_mov_b32 a108, a204
s_waitcnt lgkmcnt(8)
v_mfma_f32_16x16x32_bf16 v[180:183], a[28:31], v[32:35], 0
v_exp_f32_e32 v26, v97
v_accvgpr_mov_b32 a109, a205
v_mfma_f32_16x16x32_bf16 v[192:195], a[32:35], v[32:35], 0
Rb 16,8,53248
v_add_f32_e32 v14, v48, v26
v_accvgpr_mov_b32 a110, a206
v_mfma_f32_16x16x32_bf16 v[200:203], a[32:35], v[36:39], 0
v_exp_f32_e32 v49, v98
Cv 48,48,26
v_mfma_f32_16x16x32_bf16 v[204:207], a[36:39], v[36:39], 0
Rb 20,8,54272
v_add_f32_e32 v14, v14, v49
v_accvgpr_mov_b32 a111, a207
v_mfma_f32_16x16x32_bf16 v[196:199], a[36:39], v[32:35], 0
buffer_load_dwordx4 v2, s[20:23], s55 offen offset:2048 sc1 lds
v_accvgpr_mov_b32 a112, a208
v_mfma_f32_16x16x32_bf16 v[208:211], a[40:43], v[32:35], 0
Rb 24,8,55296
v_exp_f32_e32 v27, v99
v_mfma_f32_16x16x32_bf16 v[216:219], a[40:43], v[36:39], 0
buffer_store_dwordx4 a[128:131], v11, s[20:23], s83 offen sc0 sc1
v_accvgpr_mov_b32 a113, a209
v_mfma_f32_16x16x32_bf16 v[220:223], a[44:47], v[36:39], 0
Rb 28,8,56320
v_accvgpr_mov_b32 a114, a210
s_waitcnt lgkmcnt(8)
v_mfma_f32_16x16x32_bf16 v[212:215], a[44:47], v[32:35], 0
s_add_u32 s83, s83, 7168
v_exp_f32_e32 v50, v104
v_mfma_f32_16x16x32_bf16 v[224:227], a[48:51], v[32:35], 0
Rb 32,8,57344
v_add_f32_e32 v14, v14, v27
v_accvgpr_mov_b32 a115, a211
v_mfma_f32_16x16x32_bf16 v[232:235], a[48:51], v[36:39], 0
v_exp_f32_e32 v28, v105
Cv 49,49,27
v_mfma_f32_16x16x32_bf16 v[236:239], a[52:55], v[36:39], 0
)" R"(
Rb 36,8,58368
v_add_f32_e32 v14, v14, v50
v_accvgpr_mov_b32 a116, a212
v_mfma_f32_16x16x32_bf16 v[228:231], a[52:55], v[32:35], 0
buffer_load_dwordx4 v2, s[20:23], s55 offen offset:3072 sc1 lds
v_accvgpr_mov_b32 a117, a213
v_mfma_f32_16x16x32_bf16 v[240:243], a[56:59], v[32:35], 0
Rb 40,8,59392
v_exp_f32_e32 v51, v106
v_mfma_f32_16x16x32_bf16 v[248:251], a[56:59], v[36:39], 0
buffer_store_dwordx4 a[132:135], v11, s[20:23], s83 offen sc0 sc1
v_accvgpr_mov_b32 a118, a214
v_mfma_f32_16x16x32_bf16 v[252:255], a[60:63], v[36:39], 0
Rb 44,8,60416
v_accvgpr_mov_b32 a119, a215
s_waitcnt lgkmcnt(8)
v_mfma_f32_16x16x32_bf16 v[244:247], a[60:63], v[32:35], 0
s_add_u32 s83, s83, 7168
v_exp_f32_e32 v29, v107
Mp 128,0,40
Rb 48,8,61440
v_add_f32_e32 v14, v14, v28
v_accvgpr_mov_b32 a120, a216
Mp 136,0,44
buffer_store_dwordx4 a[136:139], v11, s[20:23], s83 offen sc0 sc1
s_add_u32 s83, s83, 7168
Mp 140,4,44
Rb 52,8,62464
s_add_u32 m0, s36, 36864
v_accvgpr_mov_b32 a121, a217
Mp 132,4,40
buffer_load_dwordx4 v3, s[20:23], s55 offen offset:0 sc1 lds
v_accvgpr_mov_b32 a122, a218
Mp 144,8,40
Rb 56,8,63488
v_exp_f32_e32 v52, v100
Mp 152,8,44
v_exp_f32_e32 v24, v101
v_accvgpr_mov_b32 a123, a219
Mp 156,12,44
Rb 60,8,64512
v_accvgpr_mov_b32 a124, a220
s_waitcnt lgkmcnt(8)
Mp 148,12,40
buffer_store_dwordx4 a[140:143], v11, s[20:23], s83 offen sc0 sc1
Cv 50,50,28
Mp 160,16,40
s_add_u32 s83, s83, 7168
v_add_f32_e32 v14, v14, v51
v_accvgpr_mov_b32 a125, a221
Mp 168,16,44
v_exp_f32_e32 v53, v102
v_add_f32_e32 v14, v14, v29
Mp 172,20,44
v_exp_f32_e32 v25, v103
v_accvgpr_mov_b32 a126, a222
Mp 164,20,40
buffer_load_dwordx4 v3, s[20:23], s55 offen offset:1024 sc1 lds
v_accvgpr_mov_b32 a127, a223
Mp 176,24,40
v_exp_f32_e32 v54, v108
Cv 51,51,29
Mp 184,24,44
buffer_store_dwordx4 a[144:147], v11, s[20:23], s83 offen sc0 sc1
)" R"(
Mp 188,28,44
s_add_u32 s83, s83, 7168
v_add_f32_e32 v15, v52, v24
Mp 180,28,40
Cv 52,52,24
s_waitcnt vmcnt(13) lgkmcnt(0)
s_barrier
buffer_store_dword v6, v7, s[20:23], s65 offen offset:0 sc0 sc1
Mp 192,32,40
Rb 0,1,0
v_add_f32_e32 v15, v15, v53
Mp 200,32,44
Rb 4,1,1024
v_exp_f32_e32 v26, v109
Mp 204,36,44
Rb 8,1,2048
v_add_f32_e32 v15, v15, v25
Mp 196,36,40
Rb 12,1,3072
buffer_load_dwordx4 v3, s[20:23], s55 offen offset:2048 sc1 lds
Mp 208,40,40
Rb 16,1,8192
v_exp_f32_e32 v55, v110
Mp 216,40,44
Rb 20,1,9216
Cv 53,53,25
Mp 220,44,44
Rb 24,1,10240
buffer_store_dwordx4 a[148:151], v11, s[20:23], s83 offen sc0 sc1
Mp 212,44,40
Rb 28,1,11264
s_add_u32 s83, s83, 7168
v_add_f32_e32 v15, v15, v54
Mp 224,48,40
Rb 32,1,4096
v_add_f32_e32 v15, v15, v26
Mp 232,48,44
Rb 36,1,5120
v_exp_f32_e32 v27, v111
Mp 236,52,44
Rb 40,1,6144
Cv 54,54,26
Mp 228,52,40
Rb 44,1,7168
buffer_load_dwordx4 v3, s[20:23], s55 offen offset:3072 sc1 lds
Mp 240,56,40
v_cmp_eq_u32_e32 vcc, s3, v4
s_andn2_b64 s[28:29], s[60:61], vcc
s_cbranch_scc1 .Lzsp9
.Lzok9:
Mp 248,56,44
buffer_store_dwordx4 a[152:155], v11, s[20:23], s83 offen sc0 sc1
Mp 252,60,44
v_add_f32_e32 v15, v15, v55
s_waitcnt lgkmcnt(8)
Mp 244,60,40
v_add_f32_e32 v15, v15, v27
Cv 55,55,27
s_sub_u32 s57, s57, 1
s_cmp_gt_i32 s57, s93
s_cbranch_scc1 .LzL
.LzDA:
s_waitcnt vmcnt(8) lgkmcnt(0)
s_barrier
Rb 0,8,0
Rb 4,8,16384
)" R"(
Rb 8,8,1024
Rb 12,8,17408
Rb 16,8,2048
Rb 20,8,18432
Rb 24,8,3072
Rb 28,8,19456
Rb 32,8,4096
Rb 36,8,20480
Rb 40,8,5120
Rb 44,8,21504
v_exp_f32_e32 v56, v112
v_exp_f32_e32 v28, v113
v_add_f32_e32 v14, v14, v56
v_add_f32_e32 v14, v14, v28
Cv 56,56,28
v_exp_f32_e32 v57, v114
v_exp_f32_e32 v29, v115
v_add_f32_e32 v14, v14, v57
v_add_f32_e32 v14, v14, v29
Cv 57,57,29
v_exp_f32_e32 v58, v120
v_exp_f32_e32 v24, v121
v_add_f32_e32 v14, v14, v58
v_add_f32_e32 v14, v14, v24
Cv 58,58,24
v_exp_f32_e32 v59, v122
v_exp_f32_e32 v25, v123
v_add_f32_e32 v14, v14, v59
v_add_f32_e32 v14, v14, v25
Cv 59,59,25
v_exp_f32_e32 v60, v116
v_exp_f32_e32 v26, v117
v_add_f32_e32 v15, v15, v60
v_add_f32_e32 v15, v15, v26
Cv 60,60,26
v_exp_f32_e32 v61, v118
v_exp_f32_e32 v27, v119
v_add_f32_e32 v15, v15, v61
v_add_f32_e32 v15, v15, v27
Cv 61,61,27
v_exp_f32_e32 v62, v124
v_exp_f32_e32 v28, v125
v_add_f32_e32 v15, v15, v62
v_add_f32_e32 v15, v15, v28
Cv 62,62,28
v_exp_f32_e32 v63, v126
v_exp_f32_e32 v29, v127
v_add_f32_e32 v15, v15, v63
v_add_f32_e32 v15, v15, v29
Cv 63,63,29
v_max_f32_e32 v11, v14, v15
v_cmp_nge_f32_e32 vcc, s62, v11
s_cbranch_vccnz .Lzfx5
.Lzfr5:
s_waitcnt lgkmcnt(8)
Mp 128,0,48
Rb 48,8,6144
s_add_u32 m0, s36, 131072
s_add_u32 s40, s67, 0x20000
s_lshl_b32 s82, s82, 7
Mp 136,0,52
buffer_load_dwordx4 v2, s[20:23], s40 offen offset:0 sc1 lds
s_add_u32 s82, s82, 0xc10c000
Mp 136,4,60
)" R"(
Rb 52,8,22528
v_add_f32_e32 v12, v12, v14
Mp 128,4,56
buffer_load_dwordx4 v2, s[20:23], s40 offen offset:1024 sc1 lds
v_add_f32_e32 v13, v13, v15
Mp 132,8,48
Rb 56,8,7168
v_mov_b32 v24, v12
Mp 140,8,52
s_nop 0
v_permlane32_swap_b32_e32 v12, v24
v_add_f32_e32 v12, v12, v24
s_cmp_lg_u32 s33, 0
s_cmov_b32 s82, 0x40000000
Mp 140,12,60
Rb 60,8,23552
v_mov_b32 v24, v12
s_waitcnt lgkmcnt(8)
Mp 132,12,56
buffer_load_dwordx4 v2, s[20:23], s40 offen offset:2048 sc1 lds
Mp 144,16,48
Rb 0,8,8192
v_permlane16_swap_b32_e32 v12, v24
Mp 152,16,52
buffer_load_dwordx4 v2, s[20:23], s40 offen offset:3072 sc1 lds
v_add_f32_e32 v12, v12, v24
Mp 152,20,60
Rb 4,8,24576
v_mov_b32 v25, v13
Mp 144,20,56
v_rcp_f32_e32 v12, v12
Mp 148,24,48
Rb 8,8,9216
s_add_u32 m0, s36, 135168
v_permlane32_swap_b32_e32 v13, v25
Mp 156,24,52
buffer_load_dwordx4 v3, s[20:23], s40 offen offset:0 sc1 lds
s_cmp_eq_u32 s35, 7
s_cmov_b32 s82, 0x40000000
Mp 156,28,60
Rb 12,8,25600
s_waitcnt lgkmcnt(8)
Mp 148,28,56
buffer_load_dwordx4 v3, s[20:23], s40 offen offset:1024 sc1 lds
v_add_f32_e32 v13, v13, v25
Mp 160,32,48
Rb 16,8,10240
v_mov_b32 v25, v13
Mp 168,32,52
s_nop 0
v_permlane16_swap_b32_e32 v13, v25
v_add_f32_e32 v13, v13, v25
Mp 168,36,60
Rb 20,8,26624
v_mul_f32_e32 v128, v12, v128
Mp 160,36,56
buffer_load_dwordx4 v3, s[20:23], s40 offen offset:2048 sc1 lds
v_mul_f32_e32 v129, v12, v129
Mp 164,40,48
Rb 24,8,11264
s_add_u32 m0, s36, 98304
s_add_u32 s40, s67, 0x30000
Mp 172,40,52
buffer_load_dwordx4 v2, s[20:23], s40 offen offset:0 sc1 lds
)" R"(
Mp 172,44,60
Rb 28,8,27648
Cv 128,128,129
s_waitcnt lgkmcnt(8)
Mp 164,44,56
v_rcp_f32_e32 v13, v13
Mp 176,48,48
Rb 32,8,12288
v_mul_f32_e32 v130, v12, v130
Mp 184,48,52
buffer_load_dwordx4 v2, s[20:23], s40 offen offset:1024 sc1 lds
v_mul_f32_e32 v131, v12, v131
Mp 184,52,60
Rb 36,8,28672
Cv 129,130,131
Mp 176,52,56
buffer_load_dwordx4 v2, s[20:23], s40 offen offset:2048 sc1 lds
Mp 180,56,48
Rb 40,8,13312
v_mul_f32_e32 v132, v12, v132
v_mul_f32_e32 v133, v12, v133
Mp 188,56,52
Cv 130,132,133
v_mul_f32_e32 v134, v12, v134
Mp 188,60,60
Rb 44,8,29696
s_waitcnt lgkmcnt(8)
Mp 180,60,56
buffer_load_dwordx4 v2, s[20:23], s40 offen offset:3072 sc1 lds
v_mul_f32_e32 v135, v12, v135
Mp 192,0,48
Rb 48,8,14336
s_add_u32 m0, s36, 102400
Mp 200,0,52
buffer_load_dwordx4 v3, s[20:23], s40 offen offset:0 sc1 lds
Mp 200,4,60
Rb 52,8,30720
Cv 131,134,135
Mp 192,4,56
v_mul_f32_e32 v136, v13, v136
v_mul_f32_e32 v137, v13, v137
Cv 136,136,137
Mp 196,8,48
Rb 56,8,15360
v_mul_f32_e32 v138, v13, v138
Mp 204,8,52
buffer_load_dwordx4 v3, s[20:23], s40 offen offset:1024 sc1 lds
Mp 204,12,60
Rb 60,8,31744
v_mul_f32_e32 v139, v13, v139
s_waitcnt lgkmcnt(8)
Mp 196,12,56
buffer_load_dwordx4 v3, s[20:23], s40 offen offset:2048 sc1 lds
Mp 208,16,48
Rb 0,1,0
Cv 137,138,139
Mp 216,16,52
v_mul_f32_e32 v140, v13, v140
v_mul_f32_e32 v141, v13, v141
Cv 138,140,141
Mp 216,20,60
Rb 4,1,1024
v_mul_f32_e32 v142, v13, v142
Mp 208,20,56
)" R"(
v_mul_f32_e32 v143, v13, v143
Cv 139,142,143
Mp 212,24,48
Rb 8,1,2048
v_mul_f32_e32 v144, v12, v144
v_mul_f32_e32 v145, v12, v145
Mp 220,24,52
Cv 144,144,145
v_mul_f32_e32 v146, v12, v146
Mp 220,28,60
Rb 12,1,3072
s_waitcnt vmcnt(14) lgkmcnt(4)
s_barrier
Mp 212,28,56
s_add_u32 s40, s67, 0x40000
v_mul_f32_e32 v147, v12, v147
Cv 145,146,147
Mp 224,32,48
Rb 16,1,4096
s_add_u32 m0, s36, 65536
Mp 232,32,52
buffer_load_dwordx4 v2, s[20:23], s40 offen offset:0 sc1 lds
Mp 232,36,60
Rb 20,1,5120
v_mul_f32_e32 v148, v12, v148
Mp 224,36,56
v_mul_f32_e32 v149, v12, v149
Cv 146,148,149
v_mul_f32_e32 v150, v12, v150
Mp 228,40,48
Rb 24,1,6144
v_mul_f32_e32 v151, v12, v151
Mp 236,40,52
Cv 147,150,151
v_mul_f32_e32 v152, v13, v152
Mp 236,44,60
Rb 28,1,8192
v_mul_f32_e32 v153, v13, v153
Mp 228,44,56
buffer_load_dwordx4 v2, s[20:23], s40 offen offset:1024 sc1 lds
Mp 240,48,48
Rb 32,1,9216
Cv 152,152,153
Mp 248,48,52
v_mul_f32_e32 v154, v13, v154
v_mul_f32_e32 v155, v13, v155
Cv 153,154,155
Mp 248,52,60
Rb 36,1,10240
v_mul_f32_e32 v156, v13, v156
Mp 240,52,56
v_mul_f32_e32 v157, v13, v157
Cv 154,156,157
Mp 244,56,48
Rb 40,1,11264
v_mul_f32_e32 v158, v13, v158
v_mul_f32_e32 v159, v13, v159
Mp 252,56,52
buffer_load_dwordx4 v2, s[20:23], s40 offen offset:2048 sc1 lds
Mp 252,60,60
Rb 44,1,12288
s_waitcnt lgkmcnt(8)
Mp 244,60,56
Cv 155,158,159
v_mul_f32_e32 v160, v12, v160
)" R"(
v_mul_f32_e32 v161, v12, v161
v_mfma_f32_16x16x32_bf16 v[32:35], a[0:3], v[128:131], 0
Rb 48,1,13312
Cv 160,160,161
v_mfma_f32_16x16x32_bf16 v[40:43], a[0:3], v[136:139], 0
v_mul_f32_e32 v162, v12, v162
v_mul_f32_e32 v163, v12, v163
v_mfma_f32_16x16x32_bf16 v[44:47], a[4:7], v[136:139], 0
Rb 52,1,14336
Cv 161,162,163
v_mfma_f32_16x16x32_bf16 v[36:39], a[4:7], v[128:131], 0
buffer_load_dwordx4 v2, s[20:23], s40 offen offset:3072 sc1 lds
v_mul_f32_e32 v164, v12, v164
v_mfma_f32_16x16x32_bf16 v[48:51], a[8:11], v[128:131], 0
Rb 56,1,16384
v_mul_f32_e32 v165, v12, v165
v_mfma_f32_16x16x32_bf16 v[56:59], a[8:11], v[136:139], 0
Cv 162,164,165
v_mul_f32_e32 v166, v12, v166
v_mfma_f32_16x16x32_bf16 v[60:63], a[12:15], v[136:139], 0
Rb 60,1,17408
v_mul_f32_e32 v167, v12, v167
s_waitcnt lgkmcnt(8)
v_mfma_f32_16x16x32_bf16 v[52:55], a[12:15], v[128:131], 0
Cv 163,166,167
v_mul_f32_e32 v168, v13, v168
v_mfma_f32_16x16x32_bf16 v[64:67], a[16:19], v[128:131], 0
Rb 0,1,18432
s_add_u32 m0, s36, 69632
v_mfma_f32_16x16x32_bf16 v[72:75], a[16:19], v[136:139], 0
buffer_load_dwordx4 v3, s[20:23], s40 offen offset:0 sc1 lds
v_mul_f32_e32 v169, v13, v169
v_mfma_f32_16x16x32_bf16 v[76:79], a[20:23], v[136:139], 0
Rb 4,1,19456
Cv 168,168,169
v_mfma_f32_16x16x32_bf16 v[68:71], a[20:23], v[128:131], 0
v_mul_f32_e32 v170, v13, v170
v_mul_f32_e32 v171, v13, v171
v_mfma_f32_16x16x32_bf16 v[80:83], a[24:27], v[128:131], 0
Rb 8,1,20480
Cv 169,170,171
v_mul_f32_e32 v172, v13, v172
v_mfma_f32_16x16x32_bf16 v[88:91], a[24:27], v[136:139], 0
v_mul_f32_e32 v173, v13, v173
Cv 170,172,173
v_mfma_f32_16x16x32_bf16 v[92:95], a[28:31], v[136:139], 0
Rb 12,1,21504
s_waitcnt lgkmcnt(8)
v_mfma_f32_16x16x32_bf16 v[84:87], a[28:31], v[128:131], 0
buffer_load_dwordx4 v3, s[20:23], s40 offen offset:1024 sc1 lds
v_mul_f32_e32 v174, v13, v174
Mp 32,32,144
Rb 16,1,22528
v_mul_f32_e32 v175, v13, v175
Mp 40,32,152
Cv 171,174,175
v_mul_f32_e32 v176, v12, v176
Mp 44,36,152
Rb 20,1,24576
v_mul_f32_e32 v177, v12, v177
Mp 36,36,144
Cv 176,176,177
v_mul_f32_e32 v178, v12, v178
v_mul_f32_e32 v179, v12, v179
)" R"(
Mp 48,40,144
Rb 24,1,25600
Cv 177,178,179
Mp 56,40,152
buffer_load_dwordx4 v3, s[20:23], s40 offen offset:2048 sc1 lds
Mp 60,44,152
Rb 28,1,26624
v_mul_f32_e32 v180, v12, v180
s_waitcnt lgkmcnt(8)
Mp 52,44,144
v_mul_f32_e32 v181, v12, v181
Cv 178,180,181
Mp 64,48,144
Rb 32,1,27648
v_mul_f32_e32 v182, v12, v182
Mp 72,48,152
v_mul_f32_e32 v183, v12, v183
Cv 179,182,183
v_mul_f32_e32 v184, v13, v184
Mp 76,52,152
Rb 36,1,28672
v_mul_f32_e32 v185, v13, v185
Mp 68,52,144
Cv 184,184,185
v_mul_f32_e32 v186, v13, v186
Mp 80,56,144
Rb 40,1,29696
v_mul_f32_e32 v187, v13, v187
Cv 185,186,187
Mp 88,56,152
v_mul_f32_e32 v188, v13, v188
v_mul_f32_e32 v189, v13, v189
Mp 92,60,152
Rb 44,1,30720
s_waitcnt lgkmcnt(8)
Mp 84,60,144
Cv 186,188,189
v_mul_f32_e32 v190, v13, v190
v_mul_f32_e32 v191, v13, v191
Mp 32,0,160
Rb 48,1,32768
Cv 187,190,191
Mp 40,0,168
v_mul_f32_e32 v192, v12, v192
v_mul_f32_e32 v193, v12, v193
Mp 44,4,168
Rb 52,1,33792
Cv 192,192,193
Mp 36,4,160
v_mul_f32_e32 v194, v12, v194
v_mul_f32_e32 v195, v12, v195
Cv 193,194,195
Mp 48,8,160
Rb 56,1,34816
v_mul_f32_e32 v196, v12, v196
Mp 56,8,168
v_mul_f32_e32 v197, v12, v197
Cv 194,196,197
Mp 60,12,168
Rb 60,1,35840
s_waitcnt vmcnt(14) lgkmcnt(4)
s_barrier
buffer_store_dword v6, v7, s[20:23], s82 offen offset:0 sc0 sc1
Mp 52,12,160
s_add_u32 s40, s67, 0x50000
)" R"(
v_mul_f32_e32 v198, v12, v198
Mp 64,16,160
Rb 0,1,36864
s_add_u32 m0, s36, 0
Mp 72,16,168
buffer_load_dwordx4 v2, s[20:23], s40 offen offset:0 sc1 lds
v_mul_f32_e32 v199, v12, v199
Mp 76,20,168
Rb 4,1,37888
Cv 195,198,199
Mp 68,20,160
v_mul_f32_e32 v200, v13, v200
v_mul_f32_e32 v201, v13, v201
Mp 80,24,160
Rb 8,1,38912
Cv 200,200,201
v_mul_f32_e32 v202, v13, v202
Mp 88,24,168
v_mul_f32_e32 v203, v13, v203
Cv 201,202,203
Mp 92,28,168
Rb 12,1,40960
Mp 84,28,160
buffer_load_dwordx4 v2, s[20:23], s40 offen offset:1024 sc1 lds
v_mul_f32_e32 v204, v13, v204
Mp 32,32,176
Rb 16,1,41984
v_mul_f32_e32 v205, v13, v205
Mp 40,32,184
Cv 202,204,205
v_mul_f32_e32 v206, v13, v206
Mp 44,36,184
Rb 20,1,43008
v_mul_f32_e32 v207, v13, v207
Mp 36,36,176
Cv 203,206,207
v_mul_f32_e32 v208, v12, v208
v_mul_f32_e32 v209, v12, v209
Mp 48,40,176
Rb 24,1,44032
Cv 208,208,209
Mp 56,40,184
buffer_load_dwordx4 v2, s[20:23], s40 offen offset:2048 sc1 lds
Mp 60,44,184
Rb 28,1,45056
v_mul_f32_e32 v210, v12, v210
s_waitcnt lgkmcnt(8)
Mp 52,44,176
v_mul_f32_e32 v211, v12, v211
Cv 209,210,211
Mp 64,48,176
Rb 32,1,46080
v_mul_f32_e32 v212, v12, v212
Mp 72,48,184
v_mul_f32_e32 v213, v12, v213
Cv 210,212,213
v_mul_f32_e32 v214, v12, v214
Mp 76,52,184
Rb 36,1,47104
v_mul_f32_e32 v215, v12, v215
Mp 68,52,176
buffer_load_dwordx4 v2, s[20:23], s40 offen offset:3072 sc1 lds
Mp 80,56,176
Rb 40,1,49152
)" R"(
Cv 211,214,215
v_mul_f32_e32 v216, v13, v216
Mp 88,56,184
v_mul_f32_e32 v217, v13, v217
Cv 216,216,217
Mp 92,60,184
Rb 44,1,50176
s_waitcnt lgkmcnt(8)
Mp 84,60,176
v_mul_f32_e32 v218, v13, v218
v_mul_f32_e32 v219, v13, v219
Cv 217,218,219
Mp 32,0,192
Rb 48,1,51200
s_add_u32 m0, s36, 4096
Mp 40,0,200
buffer_load_dwordx4 v3, s[20:23], s40 offen offset:0 sc1 lds
Mp 44,4,200
Rb 52,1,52224
v_mul_f32_e32 v220, v13, v220
Mp 36,4,192
v_mul_f32_e32 v221, v13, v221
Cv 218,220,221
v_mul_f32_e32 v222, v13, v222
Mp 48,8,192
Rb 56,1,53248
v_mul_f32_e32 v223, v13, v223
Mp 56,8,200
Cv 219,222,223
v_mul_f32_e32 v224, v12, v224
Mp 60,12,200
Rb 60,1,54272
v_mul_f32_e32 v225, v12, v225
s_waitcnt lgkmcnt(8)
Mp 52,12,192
buffer_load_dwordx4 v3, s[20:23], s40 offen offset:1024 sc1 lds
Mp 64,16,192
Rb 0,1,55296
Cv 224,224,225
Mp 72,16,200
v_mul_f32_e32 v226, v12, v226
v_mul_f32_e32 v227, v12, v227
Cv 225,226,227
Mp 76,20,200
Rb 4,1,57344
v_mul_f32_e32 v228, v12, v228
Mp 68,20,192
v_mul_f32_e32 v229, v12, v229
Cv 226,228,229
Mp 80,24,192
Rb 8,1,58368
v_mul_f32_e32 v230, v12, v230
v_mul_f32_e32 v231, v12, v231
Mp 88,24,200
buffer_load_dwordx4 v3, s[20:23], s40 offen offset:2048 sc1 lds
Mp 92,28,200
Rb 12,1,59392
s_waitcnt lgkmcnt(8)
Mp 84,28,192
Cv 227,230,231
v_mul_f32_e32 v232, v13, v232
v_mul_f32_e32 v233, v13, v233
Mp 32,32,208
Rb 16,1,60416
)" R"(
Cv 232,232,233
Mp 40,32,216
v_mul_f32_e32 v234, v13, v234
v_mul_f32_e32 v235, v13, v235
Mp 44,36,216
Rb 20,1,61440
Cv 233,234,235
Mp 36,36,208
v_mul_f32_e32 v236, v13, v236
v_mul_f32_e32 v237, v13, v237
Cv 234,236,237
Mp 48,40,208
Rb 24,1,62464
v_mul_f32_e32 v238, v13, v238
Mp 56,40,216
v_mul_f32_e32 v239, v13, v239
Cv 235,238,239
Mp 60,44,216
Rb 28,1,63488
v_mul_f32_e32 v240, v12, v240
s_waitcnt lgkmcnt(8)
Mp 52,44,208
v_mul_f32_e32 v241, v12, v241
Cv 240,240,241
Mp 64,48,208
Rb 32,10,0
v_mul_f32_e32 v242, v12, v242
Mp 72,48,216
v_mul_f32_e32 v243, v12, v243
Cv 241,242,243
v_mul_f32_e32 v244, v12, v244
Mp 76,52,216
Rb 36,10,1024
v_mul_f32_e32 v245, v12, v245
Mp 68,52,208
Cv 242,244,245
v_mul_f32_e32 v246, v12, v246
Mp 80,56,208
Rb 40,10,2048
v_mul_f32_e32 v247, v12, v247
Cv 243,246,247
Mp 88,56,216
v_mul_f32_e32 v248, v13, v248
v_mul_f32_e32 v249, v13, v249
Mp 92,60,216
Rb 44,10,3072
s_waitcnt vmcnt(15) lgkmcnt(4)
s_barrier
Mp 84,60,208
s_add_u32 s40, s67, 0x60000
Cv 248,248,249
v_mul_f32_e32 v250, v13, v250
Mp 32,0,224
Rb 48,10,4096
s_add_u32 m0, s36, 32768
Mp 40,0,232
buffer_load_dwordx4 v2, s[20:23], s40 offen offset:0 sc1 lds
Mp 44,4,232
Rb 52,10,5120
v_mul_f32_e32 v251, v13, v251
Mp 36,4,224
Cv 249,250,251
v_mul_f32_e32 v252, v13, v252
v_mul_f32_e32 v253, v13, v253
Mp 48,8,224
)" R"(
Rb 56,10,6144
Cv 250,252,253
Mp 56,8,232
v_mul_f32_e32 v254, v13, v254
v_mul_f32_e32 v255, v13, v255
Mp 60,12,232
Rb 60,10,8192
Cv 251,254,255
Mp 52,12,224
buffer_load_dwordx4 v2, s[20:23], s40 offen offset:1024 sc1 lds
Mp 64,16,224
Rb 0,10,9216
Mp 72,16,232
Mp 76,20,232
Rb 4,10,10240
Mp 68,20,224
Mp 80,24,224
Rb 8,10,11264
Mp 88,24,232
buffer_load_dwordx4 v2, s[20:23], s40 offen offset:2048 sc1 lds
Mp 92,28,232
Rb 12,10,12288
s_waitcnt lgkmcnt(8)
Mp 84,28,224
Mp 32,32,240
Rb 16,10,13312
Mp 40,32,248
Mp 44,36,248
Rb 20,10,14336
Mp 36,36,240
buffer_load_dwordx4 v2, s[20:23], s40 offen offset:3072 sc1 lds
Mp 48,40,240
Rb 24,10,16384
Mp 56,40,248
Mp 60,44,248
Rb 28,10,17408
s_waitcnt lgkmcnt(8)
Mp 52,44,240
Mp 64,48,240
Rb 32,10,18432
s_add_u32 m0, s36, 36864
Mp 72,48,248
buffer_load_dwordx4 v3, s[20:23], s40 offen offset:0 sc1 lds
Mp 76,52,248
Rb 36,10,19456
Mp 68,52,240
Mp 80,56,240
Rb 40,10,20480
Mp 88,56,248
Mp 92,60,248
Rb 44,10,21504
s_waitcnt lgkmcnt(8)
Mp 84,60,240
buffer_load_dwordx4 v3, s[20:23], s40 offen offset:1024 sc1 lds
v_mfma_f32_16x16x32_bf16 v[96:99], a[0:3], v[128:131], 0
Rb 48,10,22528
v_mfma_f32_16x16x32_bf16 v[104:107], a[0:3], v[136:139], 0
Mp 104,4,152
Rb 52,10,24576
Mp 96,4,144
Mp 96,8,160
Rb 56,10,25600
Cv 32,32,33
Mp 104,8,168
)" R"(
buffer_load_dwordx4 v3, s[20:23], s40 offen offset:2048 sc1 lds
Mp 104,12,184
Rb 60,10,26624
s_waitcnt lgkmcnt(8)
Mp 96,12,176
Cv 33,34,35
Cv 34,36,37
Mp 96,16,192
Rb 0,10,27648
Cv 35,38,39
Mp 104,16,200
s_add_u32 s29, s73, 0x0
buffer_store_dwordx4 v[32:35], v1, s[20:23], s29 offen sc0 sc1
Mp 104,20,216
Rb 4,10,28672
Mp 96,20,208
Mp 96,24,224
Rb 8,10,29696
Cv 40,40,41
Mp 104,24,232
Cv 41,42,43
Cv 42,44,45
Mp 104,28,248
Rb 12,10,30720
s_waitcnt lgkmcnt(8)
Mp 96,28,240
Cv 43,46,47
v_mfma_f32_16x16x32_bf16 v[100:103], a[32:35], v[128:131], 0
Rb 16,8,32768
v_mfma_f32_16x16x32_bf16 v[108:111], a[32:35], v[136:139], 0
s_add_u32 s29, s73, 0x400
buffer_store_dwordx4 v[40:43], v1, s[20:23], s29 offen sc0 sc1
Mp 108,36,152
Rb 20,8,33792
Mp 100,36,144
Mp 100,40,160
Rb 24,8,34816
Cv 48,48,49
Mp 108,40,168
Cv 49,50,51
Cv 50,52,53
Mp 108,44,184
Rb 28,8,35840
s_waitcnt vmcnt(17) lgkmcnt(4)
s_barrier
Mp 100,44,176
s_add_u32 s40, s67, 0x70000
Cv 51,54,55
Mp 100,48,192
Rb 32,8,36864
s_add_u32 m0, s36, 131072
Mp 108,48,200
buffer_load_dwordx4 v2, s[20:23], s40 offen offset:0 sc1 lds
Mp 108,52,216
Rb 36,8,37888
Mp 100,52,208
s_add_u32 s29, s73, 0x800
buffer_store_dwordx4 v[48:51], v1, s[20:23], s29 offen sc0 sc1
Mp 100,56,224
Rb 40,8,38912
Cv 56,56,57
Mp 108,56,232
Cv 57,58,59
Cv 58,60,61
Mp 108,60,248
)" R"(
Rb 44,8,40960
Mp 100,60,240
buffer_load_dwordx4 v2, s[20:23], s40 offen offset:1024 sc1 lds
v_mfma_f32_16x16x32_bf16 v[112:115], a[0:3], v[128:131], 0
Rb 48,8,41984
Cv 59,62,63
v_mfma_f32_16x16x32_bf16 v[120:123], a[0:3], v[136:139], 0
s_add_u32 s29, s73, 0xc00
buffer_store_dwordx4 v[56:59], v1, s[20:23], s29 offen sc0 sc1
Mp 120,4,152
Rb 52,8,43008
Mp 112,4,144
Mp 112,8,160
Rb 56,8,44032
Cv 96,96,97
Mp 120,8,168
buffer_load_dwordx4 v2, s[20:23], s40 offen offset:2048 sc1 lds
Mp 120,12,184
Rb 60,8,45056
s_waitcnt lgkmcnt(8)
Mp 112,12,176
Cv 97,98,99
Cv 98,100,101
Mp 112,16,192
Rb 0,8,46080
Cv 99,102,103
Mp 120,16,200
s_add_u32 s29, s73, 0x4400
buffer_store_dwordx4 v[96:99], v1, s[20:23], s29 offen sc0 sc1
Mp 120,20,216
Rb 4,8,47104
Mp 112,20,208
buffer_load_dwordx4 v2, s[20:23], s40 offen offset:3072 sc1 lds
Mp 112,24,224
Rb 8,8,49152
Cv 64,64,65
Mp 120,24,232
Cv 65,66,67
Cv 66,68,69
Mp 120,28,248
Rb 12,8,50176
s_waitcnt lgkmcnt(8)
Mp 112,28,240
Cv 67,70,71
v_mfma_f32_16x16x32_bf16 v[116:119], a[32:35], v[128:131], 0
Rb 16,8,51200
s_add_u32 m0, s36, 135168
v_mfma_f32_16x16x32_bf16 v[124:127], a[32:35], v[136:139], 0
buffer_load_dwordx4 v3, s[20:23], s40 offen offset:0 sc1 lds
Mp 124,36,152
Rb 20,8,52224
Mp 116,36,144
s_add_u32 s29, s73, 0x1000
buffer_store_dwordx4 v[64:67], v1, s[20:23], s29 offen sc0 sc1
Mp 116,40,160
Rb 24,8,53248
Cv 104,104,105
Mp 124,40,168
Cv 105,106,107
Cv 106,108,109
Mp 124,44,184
Rb 28,8,54272
s_waitcnt lgkmcnt(8)
Mp 116,44,176
)" R"(
buffer_load_dwordx4 v3, s[20:23], s40 offen offset:1024 sc1 lds
Mp 116,48,192
Rb 32,8,55296
Cv 107,110,111
Mp 124,48,200
s_add_u32 s29, s73, 0x4800
buffer_store_dwordx4 v[104:107], v1, s[20:23], s29 offen sc0 sc1
Mp 124,52,216
Rb 36,8,57344
Mp 116,52,208
Mp 116,56,224
Rb 40,8,58368
Cv 72,72,73
Mp 124,56,232
buffer_load_dwordx4 v3, s[20:23], s40 offen offset:2048 sc1 lds
Mp 124,60,248
Rb 44,8,59392
s_waitcnt lgkmcnt(8)
Mp 116,60,240
Cv 73,74,75
Cv 74,76,77
v_mfma_f32_16x16x32_bf16 v[96:99], a[0:3], v[128:131], 0
Rb 48,8,60416
Cv 75,78,79
v_mfma_f32_16x16x32_bf16 v[104:107], a[0:3], v[136:139], 0
s_add_u32 s29, s73, 0x1400
buffer_store_dwordx4 v[72:75], v1, s[20:23], s29 offen sc0 sc1
Mp 104,4,152
Rb 52,8,61440
Mp 96,4,144
Mp 96,8,160
Rb 56,8,62464
Cv 112,112,113
Mp 104,8,168
Cv 113,114,115
Cv 114,116,117
Mp 104,12,184
Rb 60,8,63488
s_waitcnt lgkmcnt(8)
Mp 96,12,176
Cv 115,118,119
Mp 96,16,192
Rb 0,8,0
Mp 104,16,200
s_add_u32 s29, s73, 0x4c00
buffer_store_dwordx4 v[112:115], v1, s[20:23], s29 offen sc0 sc1
Mp 104,20,216
Rb 4,8,1024
Mp 96,20,208
Mp 96,24,224
Rb 8,8,2048
Cv 80,80,81
Mp 104,24,232
Cv 81,82,83
Cv 82,84,85
Mp 104,28,248
Rb 12,8,3072
s_waitcnt vmcnt(23) lgkmcnt(8)
s_barrier
Mp 96,28,240
Cv 83,86,87
v_mfma_f32_16x16x32_bf16 v[100:103], a[32:35], v[128:131], 0
Rb 16,8,4096
v_mfma_f32_16x16x32_bf16 v[108:111], a[32:35], v[136:139], 0
s_add_u32 s29, s73, 0x1800
)" R"(
buffer_store_dwordx4 v[80:83], v1, s[20:23], s29 offen sc0 sc1
Mp 108,36,152
Rb 20,8,5120
Mp 100,36,144
Mp 100,40,160
Rb 24,8,6144
Cv 120,120,121
Mp 108,40,168
Cv 121,122,123
Cv 122,124,125
Mp 108,44,184
Rb 28,8,8192
s_waitcnt lgkmcnt(8)
Mp 100,44,176
Cv 123,126,127
Mp 100,48,192
Rb 32,8,9216
Mp 108,48,200
s_add_u32 s29, s73, 0x5000
buffer_store_dwordx4 v[120:123], v1, s[20:23], s29 offen sc0 sc1
Mp 108,52,216
Rb 36,8,10240
Mp 100,52,208
Mp 100,56,224
Rb 40,8,11264
Cv 88,88,89
Mp 108,56,232
Cv 89,90,91
Cv 90,92,93
Mp 108,60,248
Rb 44,8,12288
s_waitcnt lgkmcnt(8)
Mp 100,60,240
Cv 91,94,95
v_mfma_f32_16x16x32_bf16 v[112:115], a[0:3], v[128:131], 0
Rb 48,8,13312
v_mfma_f32_16x16x32_bf16 v[120:123], a[0:3], v[136:139], 0
s_add_u32 s29, s73, 0x4000
buffer_store_dwordx4 v[88:91], v1, s[20:23], s29 offen sc0 sc1
Mp 120,4,152
Rb 52,8,14336
Mp 112,4,144
Mp 112,8,160
Rb 56,8,16384
Cv 96,96,97
Mp 120,8,168
Cv 97,98,99
Cv 98,100,101
Mp 120,12,184
Rb 60,8,17408
s_waitcnt lgkmcnt(8)
Mp 112,12,176
Cv 99,102,103
Mp 112,16,192
Rb 0,8,18432
Mp 120,16,200
s_add_u32 s29, s73, 0x5400
buffer_store_dwordx4 v[96:99], v1, s[20:23], s29 offen sc0 sc1
Mp 120,20,216
Rb 4,8,19456
Mp 112,20,208
Mp 112,24,224
Rb 8,8,20480
Mp 120,24,232
)" R"(
Mp 120,28,248
Rb 12,8,21504
s_waitcnt lgkmcnt(8)
Mp 112,28,240
v_mfma_f32_16x16x32_bf16 v[116:119], a[32:35], v[128:131], 0
Rb 16,8,22528
v_mfma_f32_16x16x32_bf16 v[124:127], a[32:35], v[136:139], 0
Mp 124,36,152
Rb 20,8,24576
Mp 116,36,144
Mp 116,40,160
Rb 24,8,25600
Cv 104,104,105
Mp 124,40,168
Cv 105,106,107
Cv 106,108,109
Mp 124,44,184
Rb 28,8,26624
s_waitcnt lgkmcnt(8)
Mp 116,44,176
Cv 107,110,111
Mp 116,48,192
Rb 32,8,27648
Mp 124,48,200
s_add_u32 s29, s73, 0x5800
buffer_store_dwordx4 v[104:107], v1, s[20:23], s29 offen sc0 sc1
Mp 124,52,216
Rb 36,8,28672
Mp 116,52,208
Mp 116,56,224
Rb 40,8,29696
Mp 124,56,232
Mp 124,60,248
Rb 44,8,30720
s_waitcnt lgkmcnt(8)
Mp 116,60,240
v_mfma_f32_16x16x32_bf16 v[96:99], a[0:3], v[128:131], 0
Rb 48,1,0
v_mfma_f32_16x16x32_bf16 v[104:107], a[0:3], v[136:139], 0
Mp 104,4,152
Rb 52,1,1024
Mp 96,4,144
Mp 96,8,160
Rb 56,1,2048
Cv 112,112,113
Mp 104,8,168
Cv 113,114,115
Cv 114,116,117
Mp 104,12,184
Rb 60,1,3072
s_waitcnt vmcnt(21) lgkmcnt(8)
s_barrier
Mp 96,12,176
Cv 115,118,119
Mp 96,16,192
Rb 0,1,4096
Mp 104,16,200
s_add_u32 s29, s73, 0x8000
buffer_store_dwordx4 v[112:115], v1, s[20:23], s29 offen sc0 sc1
Mp 104,20,216
Rb 4,1,5120
Mp 96,20,208
Mp 96,24,224
Rb 8,1,6144
Mp 104,24,232
)" R"(
Mp 104,28,248
Rb 12,1,8192
s_waitcnt lgkmcnt(8)
Mp 96,28,240
v_mfma_f32_16x16x32_bf16 v[100:103], a[32:35], v[128:131], 0
Rb 16,1,9216
v_mfma_f32_16x16x32_bf16 v[108:111], a[32:35], v[136:139], 0
Mp 108,36,152
Rb 20,1,10240
Mp 100,36,144
Mp 100,40,160
Rb 24,1,11264
Cv 120,120,121
Mp 108,40,168
Cv 121,122,123
Cv 122,124,125
Mp 108,44,184
Rb 28,1,12288
s_waitcnt lgkmcnt(8)
Mp 100,44,176
Cv 123,126,127
Mp 100,48,192
Rb 32,1,13312
Mp 108,48,200
s_add_u32 s29, s73, 0x8400
buffer_store_dwordx4 v[120:123], v1, s[20:23], s29 offen sc0 sc1
Mp 108,52,216
Rb 36,1,14336
Mp 100,52,208
Mp 100,56,224
Rb 40,1,16384
Mp 108,56,232
Mp 108,60,248
Rb 44,1,17408
s_waitcnt lgkmcnt(8)
Mp 100,60,240
v_mfma_f32_16x16x32_bf16 v[112:115], a[0:3], v[128:131], 0
Rb 48,1,18432
v_mfma_f32_16x16x32_bf16 v[120:123], a[0:3], v[136:139], 0
Mp 120,4,152
Rb 52,1,19456
Mp 112,4,144
Mp 112,8,160
Rb 56,1,20480
Cv 96,96,97
Mp 120,8,168
Cv 97,98,99
Cv 98,100,101
Mp 120,12,184
Rb 60,1,21504
s_waitcnt vmcnt(2) lgkmcnt(8)
buffer_store_dword v6, v7, s[20:23], s68 offen offset:0 sc0 sc1
Mp 112,12,176
Cv 99,102,103
Mp 112,16,192
Rb 0,1,22528
Mp 120,16,200
s_add_u32 s29, s73, 0x8800
buffer_store_dwordx4 v[96:99], v1, s[20:23], s29 offen sc0 sc1
Mp 120,20,216
Rb 4,1,24576
Mp 112,20,208
Mp 112,24,224
Rb 8,1,25600
)" R"(
Mp 120,24,232
Mp 120,28,248
Rb 12,1,26624
s_waitcnt lgkmcnt(8)
Mp 112,28,240
v_mfma_f32_16x16x32_bf16 v[116:119], a[32:35], v[128:131], 0
Rb 16,1,27648
v_mfma_f32_16x16x32_bf16 v[124:127], a[32:35], v[136:139], 0
Mp 124,36,152
Rb 20,1,28672
Mp 116,36,144
Mp 116,40,160
Rb 24,1,29696
Cv 104,104,105
Mp 124,40,168
Cv 105,106,107
Cv 106,108,109
Mp 124,44,184
Rb 28,1,30720
s_waitcnt lgkmcnt(8)
Mp 116,44,176
Cv 107,110,111
Mp 116,48,192
Rb 32,1,32768
Mp 124,48,200
s_add_u32 s29, s73, 0x8c00
buffer_store_dwordx4 v[104:107], v1, s[20:23], s29 offen sc0 sc1
Mp 124,52,216
Rb 36,1,33792
Mp 116,52,208
Mp 116,56,224
Rb 40,1,34816
Mp 124,56,232
Mp 124,60,248
Rb 44,1,35840
s_waitcnt lgkmcnt(8)
s_barrier
Mp 116,60,240
v_mfma_f32_16x16x32_bf16 v[96:99], a[0:3], v[128:131], 0
Rb 48,1,36864
v_mfma_f32_16x16x32_bf16 v[104:107], a[0:3], v[136:139], 0
Mp 104,4,152
Rb 52,1,37888
Mp 96,4,144
Mp 96,8,160
Rb 56,1,38912
Cv 112,112,113
Mp 104,8,168
Cv 113,114,115
Cv 114,116,117
Mp 104,12,184
Rb 60,1,40960
s_waitcnt lgkmcnt(8)
Mp 96,12,176
Cv 115,118,119
Mp 96,16,192
Rb 0,1,41984
Mp 104,16,200
s_add_u32 s29, s73, 0x9000
buffer_store_dwordx4 v[112:115], v1, s[20:23], s29 offen sc0 sc1
Mp 104,20,216
Rb 4,1,43008
Mp 96,20,208
Mp 96,24,224
Rb 8,1,44032
)" R"(
Mp 104,24,232
Mp 104,28,248
Rb 12,1,45056
s_waitcnt lgkmcnt(8)
Mp 96,28,240
v_mfma_f32_16x16x32_bf16 v[100:103], a[32:35], v[128:131], 0
Rb 16,1,46080
v_mfma_f32_16x16x32_bf16 v[108:111], a[32:35], v[136:139], 0
Mp 108,36,152
Rb 20,1,47104
Mp 100,36,144
Mp 100,40,160
Rb 24,1,49152
Cv 120,120,121
Mp 108,40,168
Cv 121,122,123
Cv 122,124,125
Mp 108,44,184
Rb 28,1,50176
s_waitcnt lgkmcnt(8)
Mp 100,44,176
Cv 123,126,127
Mp 100,48,192
Rb 32,1,51200
Mp 108,48,200
s_add_u32 s29, s73, 0x9400
buffer_store_dwordx4 v[120:123], v1, s[20:23], s29 offen sc0 sc1
Mp 108,52,216
Rb 36,1,52224
Mp 100,52,208
Mp 100,56,224
Rb 40,1,53248
Mp 108,56,232
Mp 108,60,248
Rb 44,1,54272
s_waitcnt lgkmcnt(8)
Mp 100,60,240
v_mfma_f32_16x16x32_bf16 v[112:115], a[0:3], v[128:131], 0
Rb 48,1,55296
v_mfma_f32_16x16x32_bf16 v[120:123], a[0:3], v[136:139], 0
Mp 120,4,152
Rb 52,1,57344
Mp 112,4,144
Mp 112,8,160
Rb 56,1,58368
Cv 96,96,97
Mp 120,8,168
Cv 97,98,99
Cv 98,100,101
Mp 120,12,184
Rb 60,1,59392
s_waitcnt lgkmcnt(8)
Mp 112,12,176
Cv 99,102,103
Mp 112,16,192
Rb 0,1,60416
Mp 120,16,200
s_add_u32 s29, s73, 0x9800
buffer_store_dwordx4 v[96:99], v1, s[20:23], s29 offen sc0 sc1
Mp 120,20,216
Rb 4,1,61440
Mp 112,20,208
Mp 112,24,224
Rb 8,1,62464
)" R"(
Mp 120,24,232
Mp 120,28,248
Rb 12,1,63488
s_waitcnt lgkmcnt(8)
Mp 112,28,240
v_mfma_f32_16x16x32_bf16 v[116:119], a[32:35], v[128:131], 0
Rb 16,10,0
v_mfma_f32_16x16x32_bf16 v[124:127], a[32:35], v[136:139], 0
Mp 124,36,152
Rb 20,10,1024
Mp 116,36,144
Mp 116,40,160
Rb 24,10,2048
Cv 104,104,105
Mp 124,40,168
Cv 105,106,107
Cv 106,108,109
Mp 124,44,184
Rb 28,10,3072
s_waitcnt lgkmcnt(8)
Mp 116,44,176
Cv 107,110,111
Mp 116,48,192
Rb 32,10,4096
Mp 124,48,200
s_add_u32 s29, s73, 0xc000
buffer_store_dwordx4 v[104:107], v1, s[20:23], s29 offen sc0 sc1
Mp 124,52,216
Rb 36,10,5120
Mp 116,52,208
Mp 116,56,224
Rb 40,10,6144
Mp 124,56,232
Mp 124,60,248
Rb 44,10,8192
s_waitcnt lgkmcnt(8)
Mp 116,60,240
v_mfma_f32_16x16x32_bf16 v[96:99], a[0:3], v[128:131], 0
Rb 48,10,9216
v_mfma_f32_16x16x32_bf16 v[104:107], a[0:3], v[136:139], 0
Mp 104,4,152
Rb 52,10,10240
Mp 96,4,144
Mp 96,8,160
Rb 56,10,11264
Cv 112,112,113
Mp 104,8,168
Cv 113,114,115
Cv 114,116,117
Mp 104,12,184
Rb 60,10,12288
s_waitcnt lgkmcnt(8)
Mp 96,12,176
Cv 115,118,119
Mp 96,16,192
Rb 0,10,13312
Mp 104,16,200
s_add_u32 s29, s73, 0xc400
buffer_store_dwordx4 v[112:115], v1, s[20:23], s29 offen sc0 sc1
Mp 104,20,216
Rb 4,10,14336
Mp 96,20,208
Mp 96,24,224
Rb 8,10,16384
)" R"(
Mp 104,24,232
Mp 104,28,248
Rb 12,10,17408
s_waitcnt lgkmcnt(8)
Mp 96,28,240
v_mfma_f32_16x16x32_bf16 v[100:103], a[32:35], v[128:131], 0
Rb 16,10,18432
v_mfma_f32_16x16x32_bf16 v[108:111], a[32:35], v[136:139], 0
Mp 108,36,152
Rb 20,10,19456
Mp 100,36,144
Mp 100,40,160
Rb 24,10,20480
Cv 120,120,121
Mp 108,40,168
Cv 121,122,123
Cv 122,124,125
Mp 108,44,184
Rb 28,10,21504
s_waitcnt lgkmcnt(8)
Mp 100,44,176
Cv 123,126,127
Mp 100,48,192
Rb 32,10,22528
Mp 108,48,200
s_add_u32 s29, s73, 0xc800
buffer_store_dwordx4 v[120:123], v1, s[20:23], s29 offen sc0 sc1
Mp 108,52,216
Rb 36,10,24576
Mp 100,52,208
Mp 100,56,224
Rb 40,10,25600
Mp 108,56,232
Mp 108,60,248
Rb 44,10,26624
s_waitcnt lgkmcnt(8)
Mp 100,60,240
v_mfma_f32_16x16x32_bf16 v[112:115], a[0:3], v[128:131], 0
Rb 48,10,27648
v_mfma_f32_16x16x32_bf16 v[120:123], a[0:3], v[136:139], 0
Mp 120,4,152
Rb 52,10,28672
Mp 112,4,144
Mp 112,8,160
Rb 56,10,29696
Cv 96,96,97
Mp 120,8,168
Cv 97,98,99
Cv 98,100,101
Mp 120,12,184
Rb 60,10,30720
s_waitcnt lgkmcnt(8)
Mp 112,12,176
Cv 99,102,103
Mp 112,16,192
s_add_u32 s29, s73, 0xcc00
buffer_store_dwordx4 v[96:99], v1, s[20:23], s29 offen sc0 sc1
Mp 120,16,200
Mp 120,20,216
Mp 112,20,208
Mp 112,24,224
Mp 120,24,232
Mp 120,28,248
s_waitcnt lgkmcnt(4)
)" R"(
Mp 112,28,240
v_mfma_f32_16x16x32_bf16 v[116:119], a[32:35], v[128:131], 0
v_mfma_f32_16x16x32_bf16 v[124:127], a[32:35], v[136:139], 0
Mp 124,36,152
Mp 116,36,144
Mp 116,40,160
Cv 104,104,105
Cv 105,106,107
Mp 124,40,168
Cv 106,108,109
Cv 107,110,111
Mp 124,44,184
s_waitcnt lgkmcnt(0)
Mp 116,44,176
s_add_u32 s29, s73, 0xd000
buffer_store_dwordx4 v[104:107], v1, s[20:23], s29 offen sc0 sc1
Mp 116,48,192
Mp 124,48,200
Mp 124,52,216
Mp 116,52,208
Mp 116,56,224
Mp 124,56,232
Mp 124,60,248
Mp 116,60,240
s_nop 7
Cv 112,112,113
Cv 113,114,115
Cv 114,116,117
Cv 115,118,119
s_add_u32 s29, s73, 0xd400
buffer_store_dwordx4 v[112:115], v1, s[20:23], s29 offen sc0 sc1
Cv 120,120,121
Cv 121,122,123
Cv 122,124,125
Cv 123,126,127
s_add_u32 s29, s73, 0xd800
buffer_store_dwordx4 v[120:123], v1, s[20:23], s29 offen sc0 sc1
s_branch .LzEND
.LzMASK0:
s_lshl1_add_u32 s28, s57, s33
s_mov_b32 s29, 0xff800000
s_cmp_gt_u32 s28, 5
s_cbranch_scc1 .LzMASKE0
s_cmp_eq_u32 s28, 5
s_cbranch_scc1 .LzMASKB0
v_mov_b32 v72, s29
v_mov_b32 v73, s29
v_mov_b32 v74, s29
v_mov_b32 v75, s29
v_mov_b32 v80, s29
v_mov_b32 v81, s29
v_mov_b32 v82, s29
v_mov_b32 v83, s29
v_mov_b32 v84, s29
v_mov_b32 v85, s29
v_mov_b32 v86, s29
v_mov_b32 v87, s29
v_mov_b32 v88, s29
v_mov_b32 v89, s29
v_mov_b32 v90, s29
v_mov_b32 v91, s29
v_mov_b32 v92, s29
v_mov_b32 v93, s29
v_mov_b32 v94, s29
)" R"(
v_mov_b32 v95, s29
s_cmp_eq_u32 s28, 4
s_cbranch_scc1 .LzMASKA0
v_mov_b32 v64, s29
v_mov_b32 v65, s29
v_mov_b32 v66, s29
v_mov_b32 v67, s29
v_mov_b32 v68, s29
v_mov_b32 v69, s29
v_mov_b32 v70, s29
v_mov_b32 v71, s29
v_mov_b32 v76, s29
v_mov_b32 v77, s29
v_mov_b32 v78, s29
v_mov_b32 v79, s29
.LzMASKE0:
s_setpc_b64 s[30:31]
.LzMASKA0:
s_mov_b32 exec_lo, 0xf0000
s_mov_b32 exec_hi, 0xfff00ff
v_mov_b32 v64, s29
v_mov_b32 v76, s29
s_mov_b32 exec_lo, 0x1f0001
s_mov_b32 exec_hi, 0x1fff01ff
v_mov_b32 v65, s29
v_mov_b32 v77, s29
s_mov_b32 exec_lo, 0x3f0003
s_mov_b32 exec_hi, 0x3fff03ff
v_mov_b32 v66, s29
v_mov_b32 v78, s29
s_mov_b32 exec_lo, 0x7f0007
s_mov_b32 exec_hi, 0x7fff07ff
v_mov_b32 v67, s29
v_mov_b32 v79, s29
s_mov_b64 exec, -1
s_setpc_b64 s[30:31]
.LzMASKB0:
s_nop 1
v_mov_b32 v88, s29
v_mov_b32 v89, s29
v_mov_b32 v90, s29
v_mov_b32 v91, s29
s_mov_b32 exec_lo, 0xf0000
s_mov_b32 exec_hi, 0xfff00ff
v_mov_b32 v80, s29
v_mov_b32 v92, s29
s_mov_b32 exec_lo, 0x1f0001
s_mov_b32 exec_hi, 0x1fff01ff
v_mov_b32 v81, s29
v_mov_b32 v93, s29
s_mov_b32 exec_lo, 0x3f0003
s_mov_b32 exec_hi, 0x3fff03ff
v_mov_b32 v82, s29
v_mov_b32 v94, s29
s_mov_b32 exec_lo, 0x7f0007
s_mov_b32 exec_hi, 0x7fff07ff
v_mov_b32 v83, s29
v_mov_b32 v95, s29
s_mov_b64 exec, -1
s_setpc_b64 s[30:31]
.LzFIX0:
s_nop 7
v_max_f32_e32 v28, v64, v65
v_max3_f32 v28, v28, v66, v67
)" R"(
v_max3_f32 v28, v28, v72, v73
v_max3_f32 v28, v28, v74, v75
v_max3_f32 v28, v28, v80, v81
v_max3_f32 v28, v28, v82, v83
v_max3_f32 v28, v28, v88, v89
v_max3_f32 v28, v28, v90, v91
v_mov_b32 v24, v28
s_nop 1
v_permlane32_swap_b32_e32 v28, v24
v_max_f32_e32 v28, v28, v24
v_mov_b32 v24, v28
s_nop 1
v_permlane16_swap_b32_e32 v28, v24
v_max_f32_e32 v28, v28, v24
v_max_f32_e32 v29, v68, v69
v_max3_f32 v29, v29, v70, v71
v_max3_f32 v29, v29, v76, v77
v_max3_f32 v29, v29, v78, v79
v_max3_f32 v29, v29, v84, v85
v_max3_f32 v29, v29, v86, v87
v_max3_f32 v29, v29, v92, v93
v_max3_f32 v29, v29, v94, v95
v_mov_b32 v24, v29
s_nop 1
v_permlane32_swap_b32_e32 v29, v24
v_max_f32_e32 v29, v29, v24
v_mov_b32 v24, v29
s_nop 1
v_permlane16_swap_b32_e32 v29, v24
v_max_f32_e32 v29, v29, v24
s_bitcmp1_b32 s88, 0
s_cbranch_scc1 .LzFIXF0
v_max_f32_e32 v28, 0, v28
v_sub_f32_e32 v24, 0, v28
v_exp_f32_e32 v24, v24
s_nop 0
v_mul_f32_e32 v12, v24, v12
v_mul_f32_e32 v128, v24, v128
v_mul_f32_e32 v129, v24, v129
v_mul_f32_e32 v130, v24, v130
v_mul_f32_e32 v131, v24, v131
v_mul_f32_e32 v132, v24, v132
v_mul_f32_e32 v133, v24, v133
v_mul_f32_e32 v134, v24, v134
v_mul_f32_e32 v135, v24, v135
v_mul_f32_e32 v144, v24, v144
v_mul_f32_e32 v145, v24, v145
v_mul_f32_e32 v146, v24, v146
v_mul_f32_e32 v147, v24, v147
v_mul_f32_e32 v148, v24, v148
v_mul_f32_e32 v149, v24, v149
v_mul_f32_e32 v150, v24, v150
v_mul_f32_e32 v151, v24, v151
v_mul_f32_e32 v160, v24, v160
v_mul_f32_e32 v161, v24, v161
v_mul_f32_e32 v162, v24, v162
v_mul_f32_e32 v163, v24, v163
v_mul_f32_e32 v164, v24, v164
v_mul_f32_e32 v165, v24, v165
v_mul_f32_e32 v166, v24, v166
v_mul_f32_e32 v167, v24, v167
v_mul_f32_e32 v176, v24, v176
v_mul_f32_e32 v177, v24, v177
v_mul_f32_e32 v178, v24, v178
)" R"(
v_mul_f32_e32 v179, v24, v179
v_mul_f32_e32 v180, v24, v180
v_mul_f32_e32 v181, v24, v181
v_mul_f32_e32 v182, v24, v182
v_mul_f32_e32 v183, v24, v183
v_mul_f32_e32 v192, v24, v192
v_mul_f32_e32 v193, v24, v193
v_mul_f32_e32 v194, v24, v194
v_mul_f32_e32 v195, v24, v195
v_mul_f32_e32 v196, v24, v196
v_mul_f32_e32 v197, v24, v197
v_mul_f32_e32 v198, v24, v198
v_mul_f32_e32 v199, v24, v199
v_mul_f32_e32 v208, v24, v208
v_mul_f32_e32 v209, v24, v209
v_mul_f32_e32 v210, v24, v210
v_mul_f32_e32 v211, v24, v211
v_mul_f32_e32 v212, v24, v212
v_mul_f32_e32 v213, v24, v213
v_mul_f32_e32 v214, v24, v214
v_mul_f32_e32 v215, v24, v215
v_mul_f32_e32 v224, v24, v224
v_mul_f32_e32 v225, v24, v225
v_mul_f32_e32 v226, v24, v226
v_mul_f32_e32 v227, v24, v227
v_mul_f32_e32 v228, v24, v228
v_mul_f32_e32 v229, v24, v229
v_mul_f32_e32 v230, v24, v230
v_mul_f32_e32 v231, v24, v231
v_mul_f32_e32 v240, v24, v240
v_mul_f32_e32 v241, v24, v241
v_mul_f32_e32 v242, v24, v242
v_mul_f32_e32 v243, v24, v243
v_mul_f32_e32 v244, v24, v244
v_mul_f32_e32 v245, v24, v245
v_mul_f32_e32 v246, v24, v246
v_mul_f32_e32 v247, v24, v247
v_max_f32_e32 v29, 0, v29
v_sub_f32_e32 v24, 0, v29
v_exp_f32_e32 v24, v24
s_nop 0
v_mul_f32_e32 v13, v24, v13
v_mul_f32_e32 v136, v24, v136
v_mul_f32_e32 v137, v24, v137
v_mul_f32_e32 v138, v24, v138
v_mul_f32_e32 v139, v24, v139
v_mul_f32_e32 v140, v24, v140
v_mul_f32_e32 v141, v24, v141
v_mul_f32_e32 v142, v24, v142
v_mul_f32_e32 v143, v24, v143
v_mul_f32_e32 v152, v24, v152
v_mul_f32_e32 v153, v24, v153
v_mul_f32_e32 v154, v24, v154
v_mul_f32_e32 v155, v24, v155
v_mul_f32_e32 v156, v24, v156
v_mul_f32_e32 v157, v24, v157
v_mul_f32_e32 v158, v24, v158
v_mul_f32_e32 v159, v24, v159
v_mul_f32_e32 v168, v24, v168
v_mul_f32_e32 v169, v24, v169
v_mul_f32_e32 v170, v24, v170
v_mul_f32_e32 v171, v24, v171
v_mul_f32_e32 v172, v24, v172
v_mul_f32_e32 v173, v24, v173
)" R"(
v_mul_f32_e32 v174, v24, v174
v_mul_f32_e32 v175, v24, v175
v_mul_f32_e32 v184, v24, v184
v_mul_f32_e32 v185, v24, v185
v_mul_f32_e32 v186, v24, v186
v_mul_f32_e32 v187, v24, v187
v_mul_f32_e32 v188, v24, v188
v_mul_f32_e32 v189, v24, v189
v_mul_f32_e32 v190, v24, v190
v_mul_f32_e32 v191, v24, v191
v_mul_f32_e32 v200, v24, v200
v_mul_f32_e32 v201, v24, v201
v_mul_f32_e32 v202, v24, v202
v_mul_f32_e32 v203, v24, v203
v_mul_f32_e32 v204, v24, v204
v_mul_f32_e32 v205, v24, v205
v_mul_f32_e32 v206, v24, v206
v_mul_f32_e32 v207, v24, v207
v_mul_f32_e32 v216, v24, v216
v_mul_f32_e32 v217, v24, v217
v_mul_f32_e32 v218, v24, v218
v_mul_f32_e32 v219, v24, v219
v_mul_f32_e32 v220, v24, v220
v_mul_f32_e32 v221, v24, v221
v_mul_f32_e32 v222, v24, v222
v_mul_f32_e32 v223, v24, v223
v_mul_f32_e32 v232, v24, v232
v_mul_f32_e32 v233, v24, v233
v_mul_f32_e32 v234, v24, v234
v_mul_f32_e32 v235, v24, v235
v_mul_f32_e32 v236, v24, v236
v_mul_f32_e32 v237, v24, v237
v_mul_f32_e32 v238, v24, v238
v_mul_f32_e32 v239, v24, v239
v_mul_f32_e32 v248, v24, v248
v_mul_f32_e32 v249, v24, v249
v_mul_f32_e32 v250, v24, v250
v_mul_f32_e32 v251, v24, v251
v_mul_f32_e32 v252, v24, v252
v_mul_f32_e32 v253, v24, v253
v_mul_f32_e32 v254, v24, v254
v_mul_f32_e32 v255, v24, v255
s_branch .LzFIXG0
.LzFIXF0:
v_cmp_lt_f32_e32 vcc, 0xff7fffff, v28
s_nop 1
v_cndmask_b32_e32 v28, 0, v28, vcc
v_cmp_lt_f32_e32 vcc, 0xff7fffff, v29
s_nop 1
v_cndmask_b32_e32 v29, 0, v29, vcc
.LzFIXG0:
v_sub_f32_e32 v16, v16, v28
v_sub_f32_e32 v17, v17, v28
v_sub_f32_e32 v18, v18, v28
v_sub_f32_e32 v19, v19, v28
v_sub_f32_e32 v64, v64, v28
v_sub_f32_e32 v65, v65, v28
v_sub_f32_e32 v66, v66, v28
v_sub_f32_e32 v67, v67, v28
v_sub_f32_e32 v72, v72, v28
v_sub_f32_e32 v73, v73, v28
v_sub_f32_e32 v74, v74, v28
v_sub_f32_e32 v75, v75, v28
v_sub_f32_e32 v80, v80, v28
)" R"(
v_sub_f32_e32 v81, v81, v28
v_sub_f32_e32 v82, v82, v28
v_sub_f32_e32 v83, v83, v28
v_sub_f32_e32 v88, v88, v28
v_sub_f32_e32 v89, v89, v28
v_sub_f32_e32 v90, v90, v28
v_sub_f32_e32 v91, v91, v28
v_sub_f32_e32 v20, v20, v29
v_sub_f32_e32 v21, v21, v29
v_sub_f32_e32 v22, v22, v29
v_sub_f32_e32 v23, v23, v29
v_sub_f32_e32 v68, v68, v29
v_sub_f32_e32 v69, v69, v29
v_sub_f32_e32 v70, v70, v29
v_sub_f32_e32 v71, v71, v29
v_sub_f32_e32 v76, v76, v29
v_sub_f32_e32 v77, v77, v29
v_sub_f32_e32 v78, v78, v29
v_sub_f32_e32 v79, v79, v29
v_sub_f32_e32 v84, v84, v29
v_sub_f32_e32 v85, v85, v29
v_sub_f32_e32 v86, v86, v29
v_sub_f32_e32 v87, v87, v29
v_sub_f32_e32 v92, v92, v29
v_sub_f32_e32 v93, v93, v29
v_sub_f32_e32 v94, v94, v29
v_sub_f32_e32 v95, v95, v29
s_bitcmp0_b32 s88, 1
s_cbranch_scc1 .LzFIXH0
v_sub_f32_e32 v96, v96, v28
v_sub_f32_e32 v97, v97, v28
v_sub_f32_e32 v98, v98, v28
v_sub_f32_e32 v99, v99, v28
v_sub_f32_e32 v104, v104, v28
v_sub_f32_e32 v105, v105, v28
v_sub_f32_e32 v106, v106, v28
v_sub_f32_e32 v107, v107, v28
v_sub_f32_e32 v112, v112, v28
v_sub_f32_e32 v113, v113, v28
v_sub_f32_e32 v114, v114, v28
v_sub_f32_e32 v115, v115, v28
v_sub_f32_e32 v120, v120, v28
v_sub_f32_e32 v121, v121, v28
v_sub_f32_e32 v122, v122, v28
v_sub_f32_e32 v123, v123, v28
v_sub_f32_e32 v100, v100, v29
v_sub_f32_e32 v101, v101, v29
v_sub_f32_e32 v102, v102, v29
v_sub_f32_e32 v103, v103, v29
v_sub_f32_e32 v108, v108, v29
v_sub_f32_e32 v109, v109, v29
v_sub_f32_e32 v110, v110, v29
v_sub_f32_e32 v111, v111, v29
v_sub_f32_e32 v116, v116, v29
v_sub_f32_e32 v117, v117, v29
v_sub_f32_e32 v118, v118, v29
v_sub_f32_e32 v119, v119, v29
v_sub_f32_e32 v124, v124, v29
v_sub_f32_e32 v125, v125, v29
v_sub_f32_e32 v126, v126, v29
v_sub_f32_e32 v127, v127, v29
.LzFIXH0:
v_exp_f32_e32 v32, v64
v_exp_f32_e32 v28, v65
)" R"(
s_nop 0
v_add_f32_e32 v14, v32, v28
Cv 32,32,28
v_exp_f32_e32 v33, v66
v_exp_f32_e32 v29, v67
v_add_f32_e32 v14, v14, v33
v_add_f32_e32 v14, v14, v29
Cv 33,33,29
v_exp_f32_e32 v34, v72
v_exp_f32_e32 v24, v73
v_add_f32_e32 v14, v14, v34
v_add_f32_e32 v14, v14, v24
Cv 34,34,24
v_exp_f32_e32 v35, v74
v_exp_f32_e32 v25, v75
v_add_f32_e32 v14, v14, v35
v_add_f32_e32 v14, v14, v25
Cv 35,35,25
v_exp_f32_e32 v36, v68
v_exp_f32_e32 v26, v69
s_nop 0
v_add_f32_e32 v15, v36, v26
Cv 36,36,26
v_exp_f32_e32 v37, v70
v_exp_f32_e32 v27, v71
v_add_f32_e32 v15, v15, v37
v_add_f32_e32 v15, v15, v27
Cv 37,37,27
v_exp_f32_e32 v38, v76
v_exp_f32_e32 v28, v77
v_add_f32_e32 v15, v15, v38
v_add_f32_e32 v15, v15, v28
Cv 38,38,28
v_exp_f32_e32 v39, v78
v_exp_f32_e32 v29, v79
v_add_f32_e32 v15, v15, v39
v_add_f32_e32 v15, v15, v29
Cv 39,39,29
v_exp_f32_e32 v40, v80
v_exp_f32_e32 v24, v81
v_add_f32_e32 v14, v14, v40
v_add_f32_e32 v14, v14, v24
Cv 40,40,24
v_exp_f32_e32 v41, v82
v_exp_f32_e32 v25, v83
v_add_f32_e32 v14, v14, v41
v_add_f32_e32 v14, v14, v25
Cv 41,41,25
v_exp_f32_e32 v42, v88
v_exp_f32_e32 v26, v89
v_add_f32_e32 v14, v14, v42
v_add_f32_e32 v14, v14, v26
Cv 42,42,26
v_exp_f32_e32 v43, v90
v_exp_f32_e32 v27, v91
v_add_f32_e32 v14, v14, v43
v_add_f32_e32 v14, v14, v27
Cv 43,43,27
v_exp_f32_e32 v44, v84
v_exp_f32_e32 v28, v85
v_add_f32_e32 v15, v15, v44
v_add_f32_e32 v15, v15, v28
Cv 44,44,28
v_exp_f32_e32 v45, v86
)" R"(
v_exp_f32_e32 v29, v87
v_add_f32_e32 v15, v15, v45
v_add_f32_e32 v15, v15, v29
Cv 45,45,29
v_exp_f32_e32 v46, v92
v_exp_f32_e32 v24, v93
v_add_f32_e32 v15, v15, v46
v_add_f32_e32 v15, v15, v24
Cv 46,46,24
v_exp_f32_e32 v47, v94
v_exp_f32_e32 v25, v95
v_add_f32_e32 v15, v15, v47
v_add_f32_e32 v15, v15, v25
Cv 47,47,25
s_nop 1
s_setpc_b64 s[30:31]
.LzMASK1:
s_lshl1_add_u32 s28, s57, s33
s_mov_b32 s29, 0xff800000
s_cmp_gt_u32 s28, 5
s_cbranch_scc1 .LzMASKE1
s_cmp_eq_u32 s28, 5
s_cbranch_scc1 .LzMASKB1
v_mov_b32 v104, s29
v_mov_b32 v105, s29
v_mov_b32 v106, s29
v_mov_b32 v107, s29
v_mov_b32 v112, s29
v_mov_b32 v113, s29
v_mov_b32 v114, s29
v_mov_b32 v115, s29
v_mov_b32 v116, s29
v_mov_b32 v117, s29
v_mov_b32 v118, s29
v_mov_b32 v119, s29
v_mov_b32 v120, s29
v_mov_b32 v121, s29
v_mov_b32 v122, s29
v_mov_b32 v123, s29
v_mov_b32 v124, s29
v_mov_b32 v125, s29
v_mov_b32 v126, s29
v_mov_b32 v127, s29
s_cmp_eq_u32 s28, 4
s_cbranch_scc1 .LzMASKA1
v_mov_b32 v96, s29
v_mov_b32 v97, s29
v_mov_b32 v98, s29
v_mov_b32 v99, s29
v_mov_b32 v100, s29
v_mov_b32 v101, s29
v_mov_b32 v102, s29
v_mov_b32 v103, s29
v_mov_b32 v108, s29
v_mov_b32 v109, s29
v_mov_b32 v110, s29
v_mov_b32 v111, s29
.LzMASKE1:
s_setpc_b64 s[30:31]
.LzMASKA1:
s_mov_b32 exec_lo, 0xf0000
s_mov_b32 exec_hi, 0xfff00ff
v_mov_b32 v96, s29
v_mov_b32 v108, s29
)" R"(
s_mov_b32 exec_lo, 0x1f0001
s_mov_b32 exec_hi, 0x1fff01ff
v_mov_b32 v97, s29
v_mov_b32 v109, s29
s_mov_b32 exec_lo, 0x3f0003
s_mov_b32 exec_hi, 0x3fff03ff
v_mov_b32 v98, s29
v_mov_b32 v110, s29
s_mov_b32 exec_lo, 0x7f0007
s_mov_b32 exec_hi, 0x7fff07ff
v_mov_b32 v99, s29
v_mov_b32 v111, s29
s_mov_b64 exec, -1
s_setpc_b64 s[30:31]
.LzMASKB1:
s_nop 1
v_mov_b32 v120, s29
v_mov_b32 v121, s29
v_mov_b32 v122, s29
v_mov_b32 v123, s29
s_mov_b32 exec_lo, 0xf0000
s_mov_b32 exec_hi, 0xfff00ff
v_mov_b32 v112, s29
v_mov_b32 v124, s29
s_mov_b32 exec_lo, 0x1f0001
s_mov_b32 exec_hi, 0x1fff01ff
v_mov_b32 v113, s29
v_mov_b32 v125, s29
s_mov_b32 exec_lo, 0x3f0003
s_mov_b32 exec_hi, 0x3fff03ff
v_mov_b32 v114, s29
v_mov_b32 v126, s29
s_mov_b32 exec_lo, 0x7f0007
s_mov_b32 exec_hi, 0x7fff07ff
v_mov_b32 v115, s29
v_mov_b32 v127, s29
s_mov_b64 exec, -1
s_setpc_b64 s[30:31]
.LzFIX1:
s_nop 7
v_max_f32_e32 v28, v96, v97
v_max3_f32 v28, v28, v98, v99
v_max3_f32 v28, v28, v104, v105
v_max3_f32 v28, v28, v106, v107
v_max3_f32 v28, v28, v112, v113
v_max3_f32 v28, v28, v114, v115
v_max3_f32 v28, v28, v120, v121
v_max3_f32 v28, v28, v122, v123
v_mov_b32 v24, v28
s_nop 1
v_permlane32_swap_b32_e32 v28, v24
v_max_f32_e32 v28, v28, v24
v_mov_b32 v24, v28
s_nop 1
v_permlane16_swap_b32_e32 v28, v24
v_max_f32_e32 v28, v28, v24
v_max_f32_e32 v29, v100, v101
v_max3_f32 v29, v29, v102, v103
v_max3_f32 v29, v29, v108, v109
v_max3_f32 v29, v29, v110, v111
v_max3_f32 v29, v29, v116, v117
v_max3_f32 v29, v29, v118, v119
v_max3_f32 v29, v29, v124, v125
v_max3_f32 v29, v29, v126, v127
)" R"(
v_mov_b32 v24, v29
s_nop 1
v_permlane32_swap_b32_e32 v29, v24
v_max_f32_e32 v29, v29, v24
v_mov_b32 v24, v29
s_nop 1
v_permlane16_swap_b32_e32 v29, v24
v_max_f32_e32 v29, v29, v24
s_bitcmp1_b32 s88, 0
s_cbranch_scc1 .LzFIXF1
v_max_f32_e32 v28, 0, v28
v_sub_f32_e32 v24, 0, v28
v_exp_f32_e32 v24, v24
s_nop 0
v_mul_f32_e32 v12, v24, v12
v_mul_f32_e32 v128, v24, v128
v_mul_f32_e32 v129, v24, v129
v_mul_f32_e32 v130, v24, v130
v_mul_f32_e32 v131, v24, v131
v_mul_f32_e32 v132, v24, v132
v_mul_f32_e32 v133, v24, v133
v_mul_f32_e32 v134, v24, v134
v_mul_f32_e32 v135, v24, v135
v_mul_f32_e32 v144, v24, v144
v_mul_f32_e32 v145, v24, v145
v_mul_f32_e32 v146, v24, v146
v_mul_f32_e32 v147, v24, v147
v_mul_f32_e32 v148, v24, v148
v_mul_f32_e32 v149, v24, v149
v_mul_f32_e32 v150, v24, v150
v_mul_f32_e32 v151, v24, v151
v_mul_f32_e32 v160, v24, v160
v_mul_f32_e32 v161, v24, v161
v_mul_f32_e32 v162, v24, v162
v_mul_f32_e32 v163, v24, v163
v_mul_f32_e32 v164, v24, v164
v_mul_f32_e32 v165, v24, v165
v_mul_f32_e32 v166, v24, v166
v_mul_f32_e32 v167, v24, v167
v_mul_f32_e32 v176, v24, v176
v_mul_f32_e32 v177, v24, v177
v_mul_f32_e32 v178, v24, v178
v_mul_f32_e32 v179, v24, v179
v_mul_f32_e32 v180, v24, v180
v_mul_f32_e32 v181, v24, v181
v_mul_f32_e32 v182, v24, v182
v_mul_f32_e32 v183, v24, v183
v_mul_f32_e32 v192, v24, v192
v_mul_f32_e32 v193, v24, v193
v_mul_f32_e32 v194, v24, v194
v_mul_f32_e32 v195, v24, v195
v_mul_f32_e32 v196, v24, v196
v_mul_f32_e32 v197, v24, v197
v_mul_f32_e32 v198, v24, v198
v_mul_f32_e32 v199, v24, v199
v_mul_f32_e32 v208, v24, v208
v_mul_f32_e32 v209, v24, v209
v_mul_f32_e32 v210, v24, v210
v_mul_f32_e32 v211, v24, v211
v_mul_f32_e32 v212, v24, v212
v_mul_f32_e32 v213, v24, v213
v_mul_f32_e32 v214, v24, v214
v_mul_f32_e32 v215, v24, v215
v_mul_f32_e32 v224, v24, v224
)" R"(
v_mul_f32_e32 v225, v24, v225
v_mul_f32_e32 v226, v24, v226
v_mul_f32_e32 v227, v24, v227
v_mul_f32_e32 v228, v24, v228
v_mul_f32_e32 v229, v24, v229
v_mul_f32_e32 v230, v24, v230
v_mul_f32_e32 v231, v24, v231
v_mul_f32_e32 v240, v24, v240
v_mul_f32_e32 v241, v24, v241
v_mul_f32_e32 v242, v24, v242
v_mul_f32_e32 v243, v24, v243
v_mul_f32_e32 v244, v24, v244
v_mul_f32_e32 v245, v24, v245
v_mul_f32_e32 v246, v24, v246
v_mul_f32_e32 v247, v24, v247
v_max_f32_e32 v29, 0, v29
v_sub_f32_e32 v24, 0, v29
v_exp_f32_e32 v24, v24
s_nop 0
v_mul_f32_e32 v13, v24, v13
v_mul_f32_e32 v136, v24, v136
v_mul_f32_e32 v137, v24, v137
v_mul_f32_e32 v138, v24, v138
v_mul_f32_e32 v139, v24, v139
v_mul_f32_e32 v140, v24, v140
v_mul_f32_e32 v141, v24, v141
v_mul_f32_e32 v142, v24, v142
v_mul_f32_e32 v143, v24, v143
v_mul_f32_e32 v152, v24, v152
v_mul_f32_e32 v153, v24, v153
v_mul_f32_e32 v154, v24, v154
v_mul_f32_e32 v155, v24, v155
v_mul_f32_e32 v156, v24, v156
v_mul_f32_e32 v157, v24, v157
v_mul_f32_e32 v158, v24, v158
v_mul_f32_e32 v159, v24, v159
v_mul_f32_e32 v168, v24, v168
v_mul_f32_e32 v169, v24, v169
v_mul_f32_e32 v170, v24, v170
v_mul_f32_e32 v171, v24, v171
v_mul_f32_e32 v172, v24, v172
v_mul_f32_e32 v173, v24, v173
v_mul_f32_e32 v174, v24, v174
v_mul_f32_e32 v175, v24, v175
v_mul_f32_e32 v184, v24, v184
v_mul_f32_e32 v185, v24, v185
v_mul_f32_e32 v186, v24, v186
v_mul_f32_e32 v187, v24, v187
v_mul_f32_e32 v188, v24, v188
v_mul_f32_e32 v189, v24, v189
v_mul_f32_e32 v190, v24, v190
v_mul_f32_e32 v191, v24, v191
v_mul_f32_e32 v200, v24, v200
v_mul_f32_e32 v201, v24, v201
v_mul_f32_e32 v202, v24, v202
v_mul_f32_e32 v203, v24, v203
v_mul_f32_e32 v204, v24, v204
v_mul_f32_e32 v205, v24, v205
v_mul_f32_e32 v206, v24, v206
v_mul_f32_e32 v207, v24, v207
v_mul_f32_e32 v216, v24, v216
v_mul_f32_e32 v217, v24, v217
v_mul_f32_e32 v218, v24, v218
v_mul_f32_e32 v219, v24, v219
)" R"(
v_mul_f32_e32 v220, v24, v220
v_mul_f32_e32 v221, v24, v221
v_mul_f32_e32 v222, v24, v222
v_mul_f32_e32 v223, v24, v223
v_mul_f32_e32 v232, v24, v232
v_mul_f32_e32 v233, v24, v233
v_mul_f32_e32 v234, v24, v234
v_mul_f32_e32 v235, v24, v235
v_mul_f32_e32 v236, v24, v236
v_mul_f32_e32 v237, v24, v237
v_mul_f32_e32 v238, v24, v238
v_mul_f32_e32 v239, v24, v239
v_mul_f32_e32 v248, v24, v248
v_mul_f32_e32 v249, v24, v249
v_mul_f32_e32 v250, v24, v250
v_mul_f32_e32 v251, v24, v251
v_mul_f32_e32 v252, v24, v252
v_mul_f32_e32 v253, v24, v253
v_mul_f32_e32 v254, v24, v254
v_mul_f32_e32 v255, v24, v255
s_branch .LzFIXG1
.LzFIXF1:
v_cmp_lt_f32_e32 vcc, 0xff7fffff, v28
s_nop 1
v_cndmask_b32_e32 v28, 0, v28, vcc
v_cmp_lt_f32_e32 vcc, 0xff7fffff, v29
s_nop 1
v_cndmask_b32_e32 v29, 0, v29, vcc
.LzFIXG1:
v_sub_f32_e32 v16, v16, v28
v_sub_f32_e32 v17, v17, v28
v_sub_f32_e32 v18, v18, v28
v_sub_f32_e32 v19, v19, v28
v_sub_f32_e32 v96, v96, v28
v_sub_f32_e32 v97, v97, v28
v_sub_f32_e32 v98, v98, v28
v_sub_f32_e32 v99, v99, v28
v_sub_f32_e32 v104, v104, v28
v_sub_f32_e32 v105, v105, v28
v_sub_f32_e32 v106, v106, v28
v_sub_f32_e32 v107, v107, v28
v_sub_f32_e32 v112, v112, v28
v_sub_f32_e32 v113, v113, v28
v_sub_f32_e32 v114, v114, v28
v_sub_f32_e32 v115, v115, v28
v_sub_f32_e32 v120, v120, v28
v_sub_f32_e32 v121, v121, v28
v_sub_f32_e32 v122, v122, v28
v_sub_f32_e32 v123, v123, v28
v_sub_f32_e32 v20, v20, v29
v_sub_f32_e32 v21, v21, v29
v_sub_f32_e32 v22, v22, v29
v_sub_f32_e32 v23, v23, v29
v_sub_f32_e32 v100, v100, v29
v_sub_f32_e32 v101, v101, v29
v_sub_f32_e32 v102, v102, v29
v_sub_f32_e32 v103, v103, v29
v_sub_f32_e32 v108, v108, v29
v_sub_f32_e32 v109, v109, v29
v_sub_f32_e32 v110, v110, v29
v_sub_f32_e32 v111, v111, v29
v_sub_f32_e32 v116, v116, v29
v_sub_f32_e32 v117, v117, v29
v_sub_f32_e32 v118, v118, v29
)" R"(
v_sub_f32_e32 v119, v119, v29
v_sub_f32_e32 v124, v124, v29
v_sub_f32_e32 v125, v125, v29
v_sub_f32_e32 v126, v126, v29
v_sub_f32_e32 v127, v127, v29
s_bitcmp0_b32 s88, 1
s_cbranch_scc1 .LzFIXH1
v_sub_f32_e32 v64, v64, v28
v_sub_f32_e32 v65, v65, v28
v_sub_f32_e32 v66, v66, v28
v_sub_f32_e32 v67, v67, v28
v_sub_f32_e32 v72, v72, v28
v_sub_f32_e32 v73, v73, v28
v_sub_f32_e32 v74, v74, v28
v_sub_f32_e32 v75, v75, v28
v_sub_f32_e32 v80, v80, v28
v_sub_f32_e32 v81, v81, v28
v_sub_f32_e32 v82, v82, v28
v_sub_f32_e32 v83, v83, v28
v_sub_f32_e32 v88, v88, v28
v_sub_f32_e32 v89, v89, v28
v_sub_f32_e32 v90, v90, v28
v_sub_f32_e32 v91, v91, v28
v_sub_f32_e32 v68, v68, v29
v_sub_f32_e32 v69, v69, v29
v_sub_f32_e32 v70, v70, v29
v_sub_f32_e32 v71, v71, v29
v_sub_f32_e32 v76, v76, v29
v_sub_f32_e32 v77, v77, v29
v_sub_f32_e32 v78, v78, v29
v_sub_f32_e32 v79, v79, v29
v_sub_f32_e32 v84, v84, v29
v_sub_f32_e32 v85, v85, v29
v_sub_f32_e32 v86, v86, v29
v_sub_f32_e32 v87, v87, v29
v_sub_f32_e32 v92, v92, v29
v_sub_f32_e32 v93, v93, v29
v_sub_f32_e32 v94, v94, v29
v_sub_f32_e32 v95, v95, v29
.LzFIXH1:
v_exp_f32_e32 v48, v96
v_exp_f32_e32 v26, v97
s_nop 0
v_add_f32_e32 v14, v48, v26
Cv 48,48,26
v_exp_f32_e32 v49, v98
v_exp_f32_e32 v27, v99
v_add_f32_e32 v14, v14, v49
v_add_f32_e32 v14, v14, v27
Cv 49,49,27
v_exp_f32_e32 v50, v104
v_exp_f32_e32 v28, v105
v_add_f32_e32 v14, v14, v50
v_add_f32_e32 v14, v14, v28
Cv 50,50,28
v_exp_f32_e32 v51, v106
v_exp_f32_e32 v29, v107
v_add_f32_e32 v14, v14, v51
v_add_f32_e32 v14, v14, v29
Cv 51,51,29
v_exp_f32_e32 v52, v100
v_exp_f32_e32 v24, v101
s_nop 0
v_add_f32_e32 v15, v52, v24
)" R"(
Cv 52,52,24
v_exp_f32_e32 v53, v102
v_exp_f32_e32 v25, v103
v_add_f32_e32 v15, v15, v53
v_add_f32_e32 v15, v15, v25
Cv 53,53,25
v_exp_f32_e32 v54, v108
v_exp_f32_e32 v26, v109
v_add_f32_e32 v15, v15, v54
v_add_f32_e32 v15, v15, v26
Cv 54,54,26
v_exp_f32_e32 v55, v110
v_exp_f32_e32 v27, v111
v_add_f32_e32 v15, v15, v55
v_add_f32_e32 v15, v15, v27
Cv 55,55,27
v_exp_f32_e32 v56, v112
v_exp_f32_e32 v28, v113
v_add_f32_e32 v14, v14, v56
v_add_f32_e32 v14, v14, v28
Cv 56,56,28
v_exp_f32_e32 v57, v114
v_exp_f32_e32 v29, v115
v_add_f32_e32 v14, v14, v57
v_add_f32_e32 v14, v14, v29
Cv 57,57,29
v_exp_f32_e32 v58, v120
v_exp_f32_e32 v24, v121
v_add_f32_e32 v14, v14, v58
v_add_f32_e32 v14, v14, v24
Cv 58,58,24
v_exp_f32_e32 v59, v122
v_exp_f32_e32 v25, v123
v_add_f32_e32 v14, v14, v59
v_add_f32_e32 v14, v14, v25
Cv 59,59,25
v_exp_f32_e32 v60, v116
v_exp_f32_e32 v26, v117
v_add_f32_e32 v15, v15, v60
v_add_f32_e32 v15, v15, v26
Cv 60,60,26
v_exp_f32_e32 v61, v118
v_exp_f32_e32 v27, v119
v_add_f32_e32 v15, v15, v61
v_add_f32_e32 v15, v15, v27
Cv 61,61,27
v_exp_f32_e32 v62, v124
v_exp_f32_e32 v28, v125
v_add_f32_e32 v15, v15, v62
v_add_f32_e32 v15, v15, v28
Cv 62,62,28
v_exp_f32_e32 v63, v126
v_exp_f32_e32 v29, v127
v_add_f32_e32 v15, v15, v63
v_add_f32_e32 v15, v15, v29
Cv 63,63,29
s_nop 1
s_setpc_b64 s[30:31]
.Lzsp1:
s_sub_u32 s53, s53, 1
s_cbranch_scc1 .Lzsq1
s_sleep 1
buffer_load_dword v4, v2, s[20:23], s85 offen sc0 sc1
s_waitcnt vmcnt(0)
)" R"(
v_xor_b32 v4, s3, v4
v_cmp_gt_u32_e32 vcc, 2, v4
s_andn2_b32 s28, 0xff, vcc_lo
s_cbranch_scc1 .Lzsp1
s_branch .Lzok1
.Lzsq1:
s_mov_b32 s53, 0
s_branch .Lzok1
.Lzsp2:
s_sub_u32 s53, s53, 1
s_cbranch_scc1 .Lzsq2
s_sleep 1
buffer_load_dword v4, v2, s[20:23], s85 offen sc0 sc1
s_waitcnt vmcnt(0)
v_xor_b32 v4, s3, v4
v_cmp_gt_u32_e32 vcc, 2, v4
s_andn2_b32 s28, 0xff, vcc_lo
s_cbranch_scc1 .Lzsp2
s_branch .Lzok2
.Lzsq2:
s_mov_b32 s53, 0
s_branch .Lzok2
.Lzsp3:
s_sub_u32 s53, s53, 1
s_cbranch_scc1 .Lzsq3
s_sleep 1
buffer_load_dword v4, v5, s[20:23], s50 offen offset:0 sc0 sc1
s_waitcnt vmcnt(0)
v_cmp_eq_u32_e32 vcc, s3, v4
s_andn2_b64 s[28:29], s[60:61], vcc
s_cbranch_scc1 .Lzsp3
s_branch .Lzok3
.Lzsq3:
s_mov_b32 s53, 0
s_branch .Lzok3
.Lzsp4:
s_sub_u32 s53, s53, 1
s_cbranch_scc1 .Lzsq4
s_sleep 1
buffer_load_dword v4, v7, s[20:23], s52 offen offset:0 sc0 sc1
s_waitcnt vmcnt(0)
v_cmp_eq_u32_e32 vcc, s3, v4
s_andn2_b64 s[28:29], 1, vcc
s_cbranch_scc1 .Lzsp4
s_branch .Lzok4
.Lzsq4:
s_mov_b32 s53, 0
s_branch .Lzok4
.Lzsp5:
s_sub_u32 s53, s53, 1
s_cbranch_scc1 .Lzsq5
s_sleep 1
buffer_load_dword v4, v5, s[20:23], s50 offen offset:0 sc0 sc1
s_waitcnt vmcnt(0)
v_cmp_eq_u32_e32 vcc, s3, v4
s_andn2_b64 s[28:29], s[60:61], vcc
s_cbranch_scc1 .Lzsp5
s_branch .Lzok5
.Lzsq5:
s_mov_b32 s53, 0
s_branch .Lzok5
.Lzsp6:
s_sub_u32 s53, s53, 1
s_cbranch_scc1 .Lzsq6
)" R"(
s_sleep 1
buffer_load_dword v4, v7, s[20:23], s83 offen offset:4 sc0 sc1
s_waitcnt vmcnt(0)
v_cmp_eq_u32_e32 vcc, s3, v4
s_andn2_b64 s[28:29], 1, vcc
s_cbranch_scc1 .Lzsp6
s_branch .Lzok6
.Lzsq6:
s_mov_b32 s53, 0
s_branch .Lzok6
.LzT2S:
s_cmp_lg_u32 s33, 0
s_cbranch_scc1 .LzT2E
.LzT2:
s_mov_b32 s28, 0xc100000
buffer_load_dwordx2 v[32:33], v1, s[20:23], s28 offen offset:0 sc0 sc1
buffer_load_dwordx2 v[34:35], v1, s[20:23], s28 offen offset:1024 sc0 sc1
buffer_load_dwordx2 v[36:37], v1, s[20:23], s28 offen offset:2048 sc0 sc1
buffer_load_dwordx2 v[38:39], v1, s[20:23], s28 offen offset:3072 sc0 sc1
s_mov_b32 s28, 0xc101000
buffer_load_dwordx2 v[40:41], v1, s[20:23], s28 offen offset:0 sc0 sc1
buffer_load_dwordx2 v[42:43], v1, s[20:23], s28 offen offset:1024 sc0 sc1
buffer_load_dwordx2 v[44:45], v1, s[20:23], s28 offen offset:2048 sc0 sc1
buffer_load_dwordx2 v[46:47], v1, s[20:23], s28 offen offset:3072 sc0 sc1
s_mov_b32 s28, 0xc102000
buffer_load_dwordx2 v[48:49], v1, s[20:23], s28 offen offset:0 sc0 sc1
buffer_load_dwordx2 v[50:51], v1, s[20:23], s28 offen offset:1024 sc0 sc1
buffer_load_dwordx2 v[52:53], v1, s[20:23], s28 offen offset:2048 sc0 sc1
buffer_load_dwordx2 v[54:55], v1, s[20:23], s28 offen offset:3072 sc0 sc1
s_mov_b32 s28, 0xc103000
buffer_load_dwordx2 v[56:57], v1, s[20:23], s28 offen offset:0 sc0 sc1
buffer_load_dwordx2 v[58:59], v1, s[20:23], s28 offen offset:1024 sc0 sc1
buffer_load_dwordx2 v[60:61], v1, s[20:23], s28 offen offset:2048 sc0 sc1
buffer_load_dwordx2 v[62:63], v1, s[20:23], s28 offen offset:3072 sc0 sc1
s_mov_b32 s28, 0xc104000
buffer_load_dwordx2 v[64:65], v1, s[20:23], s28 offen offset:0 sc0 sc1
buffer_load_dwordx2 v[66:67], v1, s[20:23], s28 offen offset:1024 sc0 sc1
buffer_load_dwordx2 v[68:69], v1, s[20:23], s28 offen offset:2048 sc0 sc1
buffer_load_dwordx2 v[70:71], v1, s[20:23], s28 offen offset:3072 sc0 sc1
s_mov_b32 s28, 0xc105000
buffer_load_dwordx2 v[72:73], v1, s[20:23], s28 offen offset:0 sc0 sc1
buffer_load_dwordx2 v[74:75], v1, s[20:23], s28 offen offset:1024 sc0 sc1
buffer_load_dwordx2 v[76:77], v1, s[20:23], s28 offen offset:2048 sc0 sc1
buffer_load_dwordx2 v[78:79], v1, s[20:23], s28 offen offset:3072 sc0 sc1
s_mov_b32 s28, 0xc106000
buffer_load_dwordx2 v[80:81], v1, s[20:23], s28 offen offset:0 sc0 sc1
buffer_load_dwordx2 v[82:83], v1, s[20:23], s28 offen offset:1024 sc0 sc1
buffer_load_dwordx2 v[84:85], v1, s[20:23], s28 offen offset:2048 sc0 sc1
buffer_load_dwordx2 v[86:87], v1, s[20:23], s28 offen offset:3072 sc0 sc1
s_mov_b32 s28, 0xc107000
buffer_load_dwordx2 v[88:89], v1, s[20:23], s28 offen offset:0 sc0 sc1
buffer_load_dwordx2 v[90:91], v1, s[20:23], s28 offen offset:1024 sc0 sc1
buffer_load_dwordx2 v[92:93], v1, s[20:23], s28 offen offset:2048 sc0 sc1
buffer_load_dwordx2 v[94:95], v1, s[20:23], s28 offen offset:3072 sc0 sc1
v_mov_b32 v26, 0
v_mov_b32 v27, -1
s_waitcnt vmcnt(0)
v_max3_u32 v26, v26, v33, v35
v_min3_u32 v27, v27, v33, v35
v_max3_u32 v26, v26, v37, v39
v_min3_u32 v27, v27, v37, v39
v_max3_u32 v26, v26, v41, v43
v_min3_u32 v27, v27, v41, v43
v_max3_u32 v26, v26, v45, v47
)" R"(
v_min3_u32 v27, v27, v45, v47
v_max3_u32 v26, v26, v49, v51
v_min3_u32 v27, v27, v49, v51
v_max3_u32 v26, v26, v53, v55
v_min3_u32 v27, v27, v53, v55
v_max3_u32 v26, v26, v57, v59
v_min3_u32 v27, v27, v57, v59
v_max3_u32 v26, v26, v61, v63
v_min3_u32 v27, v27, v61, v63
v_max3_u32 v26, v26, v65, v67
v_min3_u32 v27, v27, v65, v67
v_max3_u32 v26, v26, v69, v71
v_min3_u32 v27, v27, v69, v71
v_max3_u32 v26, v26, v73, v75
v_min3_u32 v27, v27, v73, v75
v_max3_u32 v26, v26, v77, v79
v_min3_u32 v27, v27, v77, v79
v_max3_u32 v26, v26, v81, v83
v_min3_u32 v27, v27, v81, v83
v_max3_u32 v26, v26, v85, v87
v_min3_u32 v27, v27, v85, v87
v_max3_u32 v26, v26, v89, v91
v_min3_u32 v27, v27, v89, v91
v_max3_u32 v26, v26, v93, v95
v_min3_u32 v27, v27, v93, v95
v_cmp_ne_u32_e32 vcc, s3, v26
v_cmp_ne_u32_e64 s[28:29], s3, v27
s_or_b64 vcc, vcc, s[28:29]
s_cbranch_scc0 .LzT2G
s_sub_u32 s53, s53, 1
s_cbranch_scc0 .LzT2
s_mov_b32 s53, 0
.LzT2G:
v_max_u32 v26, v32, v34
v_min_u32 v27, v32, v34
v_med3_u32 v27, v26, v27, v36
v_max_u32 v26, v26, v36
v_med3_u32 v27, v26, v27, v38
v_max_u32 v26, v26, v38
v_med3_u32 v27, v26, v27, v40
v_max_u32 v26, v26, v40
v_med3_u32 v27, v26, v27, v42
v_max_u32 v26, v26, v42
v_med3_u32 v27, v26, v27, v44
v_max_u32 v26, v26, v44
v_med3_u32 v27, v26, v27, v46
v_max_u32 v26, v26, v46
v_med3_u32 v27, v26, v27, v48
v_max_u32 v26, v26, v48
v_med3_u32 v27, v26, v27, v50
v_max_u32 v26, v26, v50
v_med3_u32 v27, v26, v27, v52
v_max_u32 v26, v26, v52
v_med3_u32 v27, v26, v27, v54
v_max_u32 v26, v26, v54
v_med3_u32 v27, v26, v27, v56
v_max_u32 v26, v26, v56
v_med3_u32 v27, v26, v27, v58
v_max_u32 v26, v26, v58
v_med3_u32 v27, v26, v27, v60
v_max_u32 v26, v26, v60
v_med3_u32 v27, v26, v27, v62
v_max_u32 v26, v26, v62
v_med3_u32 v27, v26, v27, v64
)" R"(
v_max_u32 v26, v26, v64
v_med3_u32 v27, v26, v27, v66
v_max_u32 v26, v26, v66
v_med3_u32 v27, v26, v27, v68
v_max_u32 v26, v26, v68
v_med3_u32 v27, v26, v27, v70
v_max_u32 v26, v26, v70
v_med3_u32 v27, v26, v27, v72
v_max_u32 v26, v26, v72
v_med3_u32 v27, v26, v27, v74
v_max_u32 v26, v26, v74
v_med3_u32 v27, v26, v27, v76
v_max_u32 v26, v26, v76
v_med3_u32 v27, v26, v27, v78
v_max_u32 v26, v26, v78
v_med3_u32 v27, v26, v27, v80
v_max_u32 v26, v26, v80
v_med3_u32 v27, v26, v27, v82
v_max_u32 v26, v26, v82
v_med3_u32 v27, v26, v27, v84
v_max_u32 v26, v26, v84
v_med3_u32 v27, v26, v27, v86
v_max_u32 v26, v26, v86
v_med3_u32 v27, v26, v27, v88
v_max_u32 v26, v26, v88
v_med3_u32 v27, v26, v27, v90
v_max_u32 v26, v26, v90
v_med3_u32 v27, v26, v27, v92
v_max_u32 v26, v26, v92
v_med3_u32 v27, v26, v27, v94
v_max_u32 v26, v26, v94
v_mov_b32 v24, v26
v_mov_b32 v25, v27
s_nop 1
v_permlane32_swap_b32_e32 v26, v24
v_permlane32_swap_b32_e32 v27, v25
v_max_u32 v27, v27, v25
v_med3_u32 v27, v26, v27, v24
v_max_u32 v26, v26, v24
v_mov_b32 v24, v26
v_mov_b32 v25, v27
s_nop 1
v_permlane16_swap_b32_e32 v26, v24
v_permlane16_swap_b32_e32 v27, v25
v_max_u32 v27, v27, v25
v_med3_u32 v27, v26, v27, v24
v_max_u32 v26, v26, v24
s_nop 1
v_mov_b32_dpp v24, v26 row_ror:8 row_mask:0xf bank_mask:0xf
v_mov_b32_dpp v25, v27 row_ror:8 row_mask:0xf bank_mask:0xf
v_max_u32 v27, v27, v25
v_med3_u32 v27, v26, v27, v24
v_max_u32 v26, v26, v24
s_nop 1
v_mov_b32_dpp v24, v26 row_ror:4 row_mask:0xf bank_mask:0xf
v_mov_b32_dpp v25, v27 row_ror:4 row_mask:0xf bank_mask:0xf
v_max_u32 v27, v27, v25
v_med3_u32 v27, v26, v27, v24
v_max_u32 v26, v26, v24
s_nop 1
v_mov_b32_dpp v24, v26 row_ror:2 row_mask:0xf bank_mask:0xf
v_mov_b32_dpp v25, v27 row_ror:2 row_mask:0xf bank_mask:0xf
v_max_u32 v27, v27, v25
v_med3_u32 v27, v26, v27, v24
)" R"(
v_max_u32 v26, v26, v24
s_nop 1
v_mov_b32_dpp v24, v26 row_ror:1 row_mask:0xf bank_mask:0xf
v_mov_b32_dpp v25, v27 row_ror:1 row_mask:0xf bank_mask:0xf
v_max_u32 v27, v27, v25
v_med3_u32 v27, v26, v27, v24
v_max_u32 v26, v26, v24
v_and_b32 v26, 0xffff, v26
v_lshl_or_b32 v26, v27, 16, v26
v_not_b32 v24, v26
v_mov_b32 v25, s3
s_mov_b32 s28, 0xc108000
buffer_store_dwordx2 v[24:25], v7, s[20:23], s28 offen sc0 sc1
s_branch .LzT2E
.LzA7:
s_waitcnt vmcnt(0)
s_nop 7
v_mul_f32_e32 v96, s54, v96
v_mul_f32_e32 v97, s54, v97
Cv 96,96,97
v_accvgpr_write_b32 a112, v96
v_mul_f32_e32 v98, s54, v98
v_mul_f32_e32 v99, s54, v99
Cv 98,98,99
v_accvgpr_write_b32 a113, v98
v_mul_f32_e32 v100, s54, v100
v_mul_f32_e32 v101, s54, v101
Cv 100,100,101
v_accvgpr_write_b32 a114, v100
v_mul_f32_e32 v102, s54, v102
v_mul_f32_e32 v103, s54, v103
Cv 102,102,103
v_accvgpr_write_b32 a115, v102
v_mul_f32_e32 v104, s54, v104
v_mul_f32_e32 v105, s54, v105
Cv 104,104,105
v_accvgpr_write_b32 a116, v104
v_mul_f32_e32 v106, s54, v106
v_mul_f32_e32 v107, s54, v107
Cv 106,106,107
v_accvgpr_write_b32 a117, v106
v_mul_f32_e32 v108, s54, v108
v_mul_f32_e32 v109, s54, v109
Cv 108,108,109
v_accvgpr_write_b32 a118, v108
v_mul_f32_e32 v110, s54, v110
v_mul_f32_e32 v111, s54, v111
Cv 110,110,111
v_accvgpr_write_b32 a119, v110
v_mul_f32_e32 v112, s54, v112
v_mul_f32_e32 v113, s54, v113
Cv 112,112,113
v_accvgpr_write_b32 a120, v112
v_mul_f32_e32 v114, s54, v114
v_mul_f32_e32 v115, s54, v115
Cv 114,114,115
v_accvgpr_write_b32 a121, v114
v_mul_f32_e32 v116, s54, v116
v_mul_f32_e32 v117, s54, v117
Cv 116,116,117
v_accvgpr_write_b32 a122, v116
v_mul_f32_e32 v118, s54, v118
v_mul_f32_e32 v119, s54, v119
Cv 118,118,119
)" R"(
v_accvgpr_write_b32 a123, v118
v_mul_f32_e32 v120, s54, v120
v_mul_f32_e32 v121, s54, v121
Cv 120,120,121
v_accvgpr_write_b32 a124, v120
v_mul_f32_e32 v122, s54, v122
v_mul_f32_e32 v123, s54, v123
Cv 122,122,123
v_accvgpr_write_b32 a125, v122
v_mul_f32_e32 v124, s54, v124
v_mul_f32_e32 v125, s54, v125
Cv 124,124,125
v_accvgpr_write_b32 a126, v124
v_mul_f32_e32 v126, s54, v126
v_mul_f32_e32 v127, s54, v127
Cv 126,126,127
v_accvgpr_write_b32 a127, v126
v_accvgpr_mov_b32 a64, a224
v_accvgpr_mov_b32 a65, a225
v_accvgpr_mov_b32 a66, a226
v_accvgpr_mov_b32 a67, a227
v_accvgpr_mov_b32 a68, a228
v_accvgpr_mov_b32 a69, a229
v_accvgpr_mov_b32 a70, a230
v_accvgpr_mov_b32 a71, a231
v_accvgpr_mov_b32 a72, a232
v_accvgpr_mov_b32 a73, a233
v_accvgpr_mov_b32 a74, a234
v_accvgpr_mov_b32 a75, a235
v_accvgpr_mov_b32 a76, a236
v_accvgpr_mov_b32 a77, a237
v_accvgpr_mov_b32 a78, a238
v_accvgpr_mov_b32 a79, a239
v_accvgpr_mov_b32 a80, a240
v_accvgpr_mov_b32 a81, a241
v_accvgpr_mov_b32 a82, a242
v_accvgpr_mov_b32 a83, a243
v_accvgpr_mov_b32 a84, a244
v_accvgpr_mov_b32 a85, a245
v_accvgpr_mov_b32 a86, a246
v_accvgpr_mov_b32 a87, a247
v_accvgpr_mov_b32 a88, a248
v_accvgpr_mov_b32 a89, a249
v_accvgpr_mov_b32 a90, a250
v_accvgpr_mov_b32 a91, a251
v_accvgpr_mov_b32 a92, a252
v_accvgpr_mov_b32 a93, a253
v_accvgpr_mov_b32 a94, a254
v_accvgpr_mov_b32 a95, a255
v_accvgpr_mov_b32 a96, a192
v_accvgpr_mov_b32 a97, a193
v_accvgpr_mov_b32 a98, a194
v_accvgpr_mov_b32 a99, a195
v_accvgpr_mov_b32 a100, a196
v_accvgpr_mov_b32 a101, a197
v_accvgpr_mov_b32 a102, a198
v_accvgpr_mov_b32 a103, a199
v_accvgpr_mov_b32 a104, a200
v_accvgpr_mov_b32 a105, a201
v_accvgpr_mov_b32 a106, a202
v_accvgpr_mov_b32 a107, a203
v_accvgpr_mov_b32 a108, a204
v_accvgpr_mov_b32 a109, a205
v_accvgpr_mov_b32 a110, a206
)" R"(
v_accvgpr_mov_b32 a111, a207
s_mov_b32 s94, 2
s_nop 1
s_branch .LzA7R
.Lzsp7:
s_sub_u32 s53, s53, 1
s_cbranch_scc1 .Lzsq7
s_sleep 1
buffer_load_dword v4, v5, s[20:23], s50 offen offset:0 sc0 sc1
s_waitcnt vmcnt(0)
v_cmp_eq_u32_e32 vcc, s3, v4
s_andn2_b64 s[28:29], s[60:61], vcc
s_cbranch_scc1 .Lzsp7
s_branch .Lzok7
.Lzsq7:
s_mov_b32 s53, 0
s_branch .Lzok7
.Lzmk0:
s_call_b64 s[30:31], .LzMASK0
s_branch .Lzmr0
.Lzfx0:
s_mov_b32 s88, 3
s_call_b64 s[30:31], .LzFIX0
s_branch .Lzfr0
.Lzmk1:
s_call_b64 s[30:31], .LzMASK1
s_branch .Lzmr1
.Lzsp8:
s_sub_u32 s53, s53, 1
s_cbranch_scc1 .Lzsq8
s_sleep 1
buffer_load_dword v4, v5, s[20:23], s50 offen offset:0 sc0 sc1
s_waitcnt vmcnt(0)
v_cmp_eq_u32_e32 vcc, s3, v4
s_andn2_b64 s[28:29], s[60:61], vcc
s_cbranch_scc1 .Lzsp8
s_branch .Lzok8
.Lzsq8:
s_mov_b32 s53, 0
s_branch .Lzok8
.Lzfx1:
s_mov_b32 s88, 2
s_call_b64 s[30:31], .LzFIX1
s_branch .Lzfr1
.Lzmk2:
s_call_b64 s[30:31], .LzMASK0
s_branch .Lzmr2
.Lzfx2:
s_mov_b32 s88, 2
s_call_b64 s[30:31], .LzFIX0
s_branch .Lzfr2
.Lzmk3:
s_call_b64 s[30:31], .LzMASK1
s_branch .Lzmr3
.Lzsp9:
s_sub_u32 s53, s53, 1
s_cbranch_scc1 .Lzsq9
s_sleep 1
buffer_load_dword v4, v5, s[20:23], s50 offen offset:0 sc0 sc1
s_waitcnt vmcnt(0)
v_cmp_eq_u32_e32 vcc, s3, v4
s_andn2_b64 s[28:29], s[60:61], vcc
s_cbranch_scc1 .Lzsp9
s_branch .Lzok9
)" R"(
.Lzsq9:
s_mov_b32 s53, 0
s_branch .Lzok9
.Lzfx3:
s_mov_b32 s88, 0
s_call_b64 s[30:31], .LzFIX1
s_branch .Lzfr3
.Lzmk4:
s_call_b64 s[30:31], .LzMASK0
s_branch .Lzmr4
.Lzfx4:
s_mov_b32 s88, 3
s_call_b64 s[30:31], .LzFIX0
s_branch .Lzfr4
.Lzmk5:
s_call_b64 s[30:31], .LzMASK1
s_branch .Lzmr5
.Lzfx5:
s_mov_b32 s88, 0
s_call_b64 s[30:31], .LzFIX1
s_branch .Lzfr5
.Lzsp10:
s_sub_u32 s53, s53, 1
s_cbranch_scc1 .Lzsq10
s_sleep 1
buffer_load_dword v4, v2, s[20:23], s85 offen sc0 sc1
s_waitcnt vmcnt(0)
v_xor_b32 v4, s3, v4
v_cmp_gt_u32_e32 vcc, 2, v4
s_andn2_b32 s28, 0xff, vcc_lo
s_cbranch_scc1 .Lzsp10
s_branch .Lzok10
.Lzsq10:
s_mov_b32 s53, 0
s_branch .Lzok10
.Lzsp11:
s_sub_u32 s53, s53, 1
s_cbranch_scc1 .Lzsq11
s_sleep 1
buffer_load_dword v4, v5, s[20:23], s50 offen offset:0 sc0 sc1
s_waitcnt vmcnt(0)
v_cmp_eq_u32_e32 vcc, s3, v4
s_andn2_b64 s[28:29], s[60:61], vcc
s_cbranch_scc1 .Lzsp11
s_branch .Lzok11
.Lzsq11:
s_mov_b32 s53, 0
s_branch .Lzok11
.Lzsp12:
s_sub_u32 s53, s53, 1
s_cbranch_scc1 .Lzsq12
s_sleep 1
buffer_load_dword v4, v5, s[20:23], s50 offen offset:0 sc0 sc1
s_waitcnt vmcnt(0)
v_cmp_eq_u32_e32 vcc, s3, v4
s_andn2_b64 s[28:29], s[60:61], vcc
s_cbranch_scc1 .Lzsp12
s_branch .Lzok12
.Lzsq12:
s_mov_b32 s53, 0
s_branch .Lzok12
.Lzsp13:
s_sub_u32 s53, s53, 1
s_cbranch_scc1 .Lzsq13
)" R"(
s_sleep 1
buffer_load_dword v4, v5, s[20:23], s50 offen offset:0 sc0 sc1
s_waitcnt vmcnt(0)
v_cmp_eq_u32_e32 vcc, s3, v4
s_andn2_b64 s[28:29], s[60:61], vcc
s_cbranch_scc1 .Lzsp13
s_branch .Lzok13
.Lzsq13:
s_mov_b32 s53, 0
s_branch .Lzok13
.Lzsp14:
s_sub_u32 s53, s53, 1
s_cbranch_scc1 .Lzsq14
s_sleep 1
buffer_load_dword v4, v5, s[20:23], s50 offen offset:0 sc0 sc1
s_waitcnt vmcnt(0)
v_cmp_eq_u32_e32 vcc, s3, v4
s_andn2_b64 s[28:29], s[60:61], vcc
s_cbranch_scc1 .Lzsp14
s_branch .Lzok14
.Lzsq14:
s_mov_b32 s53, 0
s_branch .Lzok14
.Lzfx6:
s_mov_b32 s88, 3
s_call_b64 s[30:31], .LzFIX0
s_branch .Lzfr6
.Lzsp15:
s_sub_u32 s53, s53, 1
s_cbranch_scc1 .Lzsq15
s_sleep 1
buffer_load_dword v4, v5, s[20:23], s50 offen offset:0 sc0 sc1
s_waitcnt vmcnt(0)
v_cmp_eq_u32_e32 vcc, s3, v4
s_andn2_b64 s[28:29], s[60:61], vcc
s_cbranch_scc1 .Lzsp15
s_branch .Lzok15
.Lzsq15:
s_mov_b32 s53, 0
s_branch .Lzok15
.Lzfx7:
s_mov_b32 s88, 2
s_call_b64 s[30:31], .LzFIX1
s_branch .Lzfr7
.Lzmk6:
s_call_b64 s[30:31], .LzMASK0
s_branch .Lzmr6
.Lzfx8:
s_mov_b32 s88, 2
s_call_b64 s[30:31], .LzFIX0
s_branch .Lzfr8
.Lzmk7:
s_call_b64 s[30:31], .LzMASK1
s_branch .Lzmr7
.Lzsp16:
s_sub_u32 s53, s53, 1
s_cbranch_scc1 .Lzsq16
s_sleep 1
buffer_load_dword v4, v5, s[20:23], s50 offen offset:0 sc0 sc1
s_waitcnt vmcnt(0)
v_cmp_eq_u32_e32 vcc, s3, v4
s_andn2_b64 s[28:29], s[60:61], vcc
s_cbranch_scc1 .Lzsp16
s_branch .Lzok16
)" R"(
.Lzsq16:
s_mov_b32 s53, 0
s_branch .Lzok16
.Lzfx9:
s_mov_b32 s88, 2
s_call_b64 s[30:31], .LzFIX1
s_branch .Lzfr9
.Lzmk8:
s_call_b64 s[30:31], .LzMASK0
s_branch .Lzmr8
.Lzfx10:
s_mov_b32 s88, 2
s_call_b64 s[30:31], .LzFIX0
s_branch .Lzfr10
.Lzmk9:
s_call_b64 s[30:31], .LzMASK1
s_branch .Lzmr9
.Lzsp17:
s_sub_u32 s53, s53, 1
s_cbranch_scc1 .Lzsq17
s_sleep 1
buffer_load_dword v4, v7, s[20:23], s65 offen offset:0 sc0 sc1
s_waitcnt vmcnt(0)
v_cmp_eq_u32_e32 vcc, s3, v4
s_andn2_b64 s[28:29], s[60:61], vcc
s_cbranch_scc1 .Lzsp17
s_branch .Lzok17
.Lzsq17:
s_mov_b32 s53, 0
s_branch .Lzok17
.Lzfx11:
s_mov_b32 s88, 2
s_call_b64 s[30:31], .LzFIX1
s_branch .Lzfr11
.Lzmk10:
s_call_b64 s[30:31], .LzMASK0
s_branch .Lzmr10
.Lzfx12:
s_mov_b32 s88, 2
s_call_b64 s[30:31], .LzFIX0
s_branch .Lzfr12
.Lzmk11:
s_call_b64 s[30:31], .LzMASK1
s_branch .Lzmr11
.Lzfx13:
s_mov_b32 s88, 0
s_call_b64 s[30:31], .LzFIX1
s_branch .Lzfr13
.LzMRG:
s_waitcnt vmcnt(0)
s_nop 7
v_mov_b32 v25, v64
v_mov_b32 v24, v25
s_nop 1
v_permlane32_swap_b32_e32 v25, v24
v_add_f32_e32 v25, v25, v24
v_mov_b32 v24, v25
s_nop 1
v_permlane16_swap_b32_e32 v25, v24
v_add_f32_e32 v25, v25, v24
v_cmp_lt_f32_e32 vcc, 0, v25
s_nop 1
v_cndmask_b32_e32 v66, v16, v66, vcc
v_sub_f32_e32 v28, v16, v66
)" R"(
v_min_f32_e32 v26, 0, v28
v_exp_f32_e32 v26, v26
v_max_f32_e32 v28, 0, v28
v_sub_f32_e32 v24, 0, v28
v_exp_f32_e32 v24, v24
v_mul_f32_e32 v64, v26, v64
v_mul_f32_e32 v12, v24, v12
v_mul_f32_e32 v128, v24, v128
v_mul_f32_e32 v129, v24, v129
v_mul_f32_e32 v130, v24, v130
v_mul_f32_e32 v131, v24, v131
v_mul_f32_e32 v132, v24, v132
v_mul_f32_e32 v133, v24, v133
v_mul_f32_e32 v134, v24, v134
v_mul_f32_e32 v135, v24, v135
v_mul_f32_e32 v144, v24, v144
v_mul_f32_e32 v145, v24, v145
v_mul_f32_e32 v146, v24, v146
v_mul_f32_e32 v147, v24, v147
v_mul_f32_e32 v148, v24, v148
v_mul_f32_e32 v149, v24, v149
v_mul_f32_e32 v150, v24, v150
v_mul_f32_e32 v151, v24, v151
v_mul_f32_e32 v160, v24, v160
v_mul_f32_e32 v161, v24, v161
v_mul_f32_e32 v162, v24, v162
v_mul_f32_e32 v163, v24, v163
v_mul_f32_e32 v164, v24, v164
v_mul_f32_e32 v165, v24, v165
v_mul_f32_e32 v166, v24, v166
v_mul_f32_e32 v167, v24, v167
v_mul_f32_e32 v176, v24, v176
v_mul_f32_e32 v177, v24, v177
v_mul_f32_e32 v178, v24, v178
v_mul_f32_e32 v179, v24, v179
v_mul_f32_e32 v180, v24, v180
v_mul_f32_e32 v181, v24, v181
v_mul_f32_e32 v182, v24, v182
v_mul_f32_e32 v183, v24, v183
v_mul_f32_e32 v192, v24, v192
v_mul_f32_e32 v193, v24, v193
v_mul_f32_e32 v194, v24, v194
v_mul_f32_e32 v195, v24, v195
v_mul_f32_e32 v196, v24, v196
v_mul_f32_e32 v197, v24, v197
v_mul_f32_e32 v198, v24, v198
v_mul_f32_e32 v199, v24, v199
v_mul_f32_e32 v208, v24, v208
v_mul_f32_e32 v209, v24, v209
v_mul_f32_e32 v210, v24, v210
v_mul_f32_e32 v211, v24, v211
v_mul_f32_e32 v212, v24, v212
v_mul_f32_e32 v213, v24, v213
v_mul_f32_e32 v214, v24, v214
v_mul_f32_e32 v215, v24, v215
v_mul_f32_e32 v224, v24, v224
v_mul_f32_e32 v225, v24, v225
v_mul_f32_e32 v226, v24, v226
v_mul_f32_e32 v227, v24, v227
v_mul_f32_e32 v228, v24, v228
v_mul_f32_e32 v229, v24, v229
v_mul_f32_e32 v230, v24, v230
v_mul_f32_e32 v231, v24, v231
v_mul_f32_e32 v240, v24, v240
)" R"(
v_mul_f32_e32 v241, v24, v241
v_mul_f32_e32 v242, v24, v242
v_mul_f32_e32 v243, v24, v243
v_mul_f32_e32 v244, v24, v244
v_mul_f32_e32 v245, v24, v245
v_mul_f32_e32 v246, v24, v246
v_mul_f32_e32 v247, v24, v247
v_sub_f32_e32 v16, v16, v28
v_sub_f32_e32 v17, v17, v28
v_sub_f32_e32 v18, v18, v28
v_sub_f32_e32 v19, v19, v28
v_sub_f32_e32 v96, v96, v28
v_sub_f32_e32 v97, v97, v28
v_sub_f32_e32 v98, v98, v28
v_sub_f32_e32 v99, v99, v28
v_sub_f32_e32 v104, v104, v28
v_sub_f32_e32 v105, v105, v28
v_sub_f32_e32 v106, v106, v28
v_sub_f32_e32 v107, v107, v28
v_sub_f32_e32 v112, v112, v28
v_sub_f32_e32 v113, v113, v28
v_sub_f32_e32 v114, v114, v28
v_sub_f32_e32 v115, v115, v28
v_sub_f32_e32 v120, v120, v28
v_sub_f32_e32 v121, v121, v28
v_sub_f32_e32 v122, v122, v28
v_sub_f32_e32 v123, v123, v28
v_accvgpr_read_b32 v25, a128
v_lshlrev_b32 v24, 16, v25
v_and_b32 v25, 0xffff0000, v25
v_mul_f32_e32 v24, v26, v24
v_mul_f32_e32 v25, v26, v25
Cv 24,24,25
v_accvgpr_write_b32 a128, v24
v_accvgpr_read_b32 v25, a129
v_lshlrev_b32 v24, 16, v25
v_and_b32 v25, 0xffff0000, v25
v_mul_f32_e32 v24, v26, v24
v_mul_f32_e32 v25, v26, v25
Cv 24,24,25
v_accvgpr_write_b32 a129, v24
v_accvgpr_read_b32 v25, a130
v_lshlrev_b32 v24, 16, v25
v_and_b32 v25, 0xffff0000, v25
v_mul_f32_e32 v24, v26, v24
v_mul_f32_e32 v25, v26, v25
Cv 24,24,25
v_accvgpr_write_b32 a130, v24
v_accvgpr_read_b32 v25, a131
v_lshlrev_b32 v24, 16, v25
v_and_b32 v25, 0xffff0000, v25
v_mul_f32_e32 v24, v26, v24
v_mul_f32_e32 v25, v26, v25
Cv 24,24,25
v_accvgpr_write_b32 a131, v24
v_accvgpr_read_b32 v25, a136
v_lshlrev_b32 v24, 16, v25
v_and_b32 v25, 0xffff0000, v25
v_mul_f32_e32 v24, v26, v24
v_mul_f32_e32 v25, v26, v25
Cv 24,24,25
v_accvgpr_write_b32 a136, v24
v_accvgpr_read_b32 v25, a137
v_lshlrev_b32 v24, 16, v25
)" R"(
v_and_b32 v25, 0xffff0000, v25
v_mul_f32_e32 v24, v26, v24
v_mul_f32_e32 v25, v26, v25
Cv 24,24,25
v_accvgpr_write_b32 a137, v24
v_accvgpr_read_b32 v25, a138
v_lshlrev_b32 v24, 16, v25
v_and_b32 v25, 0xffff0000, v25
v_mul_f32_e32 v24, v26, v24
v_mul_f32_e32 v25, v26, v25
Cv 24,24,25
v_accvgpr_write_b32 a138, v24
v_accvgpr_read_b32 v25, a139
v_lshlrev_b32 v24, 16, v25
v_and_b32 v25, 0xffff0000, v25
v_mul_f32_e32 v24, v26, v24
v_mul_f32_e32 v25, v26, v25
Cv 24,24,25
v_accvgpr_write_b32 a139, v24
v_accvgpr_read_b32 v25, a144
v_lshlrev_b32 v24, 16, v25
v_and_b32 v25, 0xffff0000, v25
v_mul_f32_e32 v24, v26, v24
v_mul_f32_e32 v25, v26, v25
Cv 24,24,25
v_accvgpr_write_b32 a144, v24
v_accvgpr_read_b32 v25, a145
v_lshlrev_b32 v24, 16, v25
v_and_b32 v25, 0xffff0000, v25
v_mul_f32_e32 v24, v26, v24
v_mul_f32_e32 v25, v26, v25
Cv 24,24,25
v_accvgpr_write_b32 a145, v24
v_accvgpr_read_b32 v25, a146
v_lshlrev_b32 v24, 16, v25
v_and_b32 v25, 0xffff0000, v25
v_mul_f32_e32 v24, v26, v24
v_mul_f32_e32 v25, v26, v25
Cv 24,24,25
v_accvgpr_write_b32 a146, v24
v_accvgpr_read_b32 v25, a147
v_lshlrev_b32 v24, 16, v25
v_and_b32 v25, 0xffff0000, v25
v_mul_f32_e32 v24, v26, v24
v_mul_f32_e32 v25, v26, v25
Cv 24,24,25
v_accvgpr_write_b32 a147, v24
v_accvgpr_read_b32 v25, a152
v_lshlrev_b32 v24, 16, v25
v_and_b32 v25, 0xffff0000, v25
v_mul_f32_e32 v24, v26, v24
v_mul_f32_e32 v25, v26, v25
Cv 24,24,25
v_accvgpr_write_b32 a152, v24
v_accvgpr_read_b32 v25, a153
v_lshlrev_b32 v24, 16, v25
v_and_b32 v25, 0xffff0000, v25
v_mul_f32_e32 v24, v26, v24
v_mul_f32_e32 v25, v26, v25
Cv 24,24,25
v_accvgpr_write_b32 a153, v24
v_accvgpr_read_b32 v25, a154
v_lshlrev_b32 v24, 16, v25
v_and_b32 v25, 0xffff0000, v25
)" R"(
v_mul_f32_e32 v24, v26, v24
v_mul_f32_e32 v25, v26, v25
Cv 24,24,25
v_accvgpr_write_b32 a154, v24
v_accvgpr_read_b32 v25, a155
v_lshlrev_b32 v24, 16, v25
v_and_b32 v25, 0xffff0000, v25
v_mul_f32_e32 v24, v26, v24
v_mul_f32_e32 v25, v26, v25
Cv 24,24,25
v_accvgpr_write_b32 a155, v24
v_accvgpr_read_b32 v25, a160
v_lshlrev_b32 v24, 16, v25
v_and_b32 v25, 0xffff0000, v25
v_mul_f32_e32 v24, v26, v24
v_mul_f32_e32 v25, v26, v25
Cv 24,24,25
v_accvgpr_write_b32 a160, v24
v_accvgpr_read_b32 v25, a161
v_lshlrev_b32 v24, 16, v25
v_and_b32 v25, 0xffff0000, v25
v_mul_f32_e32 v24, v26, v24
v_mul_f32_e32 v25, v26, v25
Cv 24,24,25
v_accvgpr_write_b32 a161, v24
v_accvgpr_read_b32 v25, a162
v_lshlrev_b32 v24, 16, v25
v_and_b32 v25, 0xffff0000, v25
v_mul_f32_e32 v24, v26, v24
v_mul_f32_e32 v25, v26, v25
Cv 24,24,25
v_accvgpr_write_b32 a162, v24
v_accvgpr_read_b32 v25, a163
v_lshlrev_b32 v24, 16, v25
v_and_b32 v25, 0xffff0000, v25
v_mul_f32_e32 v24, v26, v24
v_mul_f32_e32 v25, v26, v25
Cv 24,24,25
v_accvgpr_write_b32 a163, v24
v_accvgpr_read_b32 v25, a168
v_lshlrev_b32 v24, 16, v25
v_and_b32 v25, 0xffff0000, v25
v_mul_f32_e32 v24, v26, v24
v_mul_f32_e32 v25, v26, v25
Cv 24,24,25
v_accvgpr_write_b32 a168, v24
v_accvgpr_read_b32 v25, a169
v_lshlrev_b32 v24, 16, v25
v_and_b32 v25, 0xffff0000, v25
v_mul_f32_e32 v24, v26, v24
v_mul_f32_e32 v25, v26, v25
Cv 24,24,25
v_accvgpr_write_b32 a169, v24
v_accvgpr_read_b32 v25, a170
v_lshlrev_b32 v24, 16, v25
v_and_b32 v25, 0xffff0000, v25
v_mul_f32_e32 v24, v26, v24
v_mul_f32_e32 v25, v26, v25
Cv 24,24,25
v_accvgpr_write_b32 a170, v24
v_accvgpr_read_b32 v25, a171
v_lshlrev_b32 v24, 16, v25
v_and_b32 v25, 0xffff0000, v25
v_mul_f32_e32 v24, v26, v24
)" R"(
v_mul_f32_e32 v25, v26, v25
Cv 24,24,25
v_accvgpr_write_b32 a171, v24
v_accvgpr_read_b32 v25, a176
v_lshlrev_b32 v24, 16, v25
v_and_b32 v25, 0xffff0000, v25
v_mul_f32_e32 v24, v26, v24
v_mul_f32_e32 v25, v26, v25
Cv 24,24,25
v_accvgpr_write_b32 a176, v24
v_accvgpr_read_b32 v25, a177
v_lshlrev_b32 v24, 16, v25
v_and_b32 v25, 0xffff0000, v25
v_mul_f32_e32 v24, v26, v24
v_mul_f32_e32 v25, v26, v25
Cv 24,24,25
v_accvgpr_write_b32 a177, v24
v_accvgpr_read_b32 v25, a178
v_lshlrev_b32 v24, 16, v25
v_and_b32 v25, 0xffff0000, v25
v_mul_f32_e32 v24, v26, v24
v_mul_f32_e32 v25, v26, v25
Cv 24,24,25
v_accvgpr_write_b32 a178, v24
v_accvgpr_read_b32 v25, a179
v_lshlrev_b32 v24, 16, v25
v_and_b32 v25, 0xffff0000, v25
v_mul_f32_e32 v24, v26, v24
v_mul_f32_e32 v25, v26, v25
Cv 24,24,25
v_accvgpr_write_b32 a179, v24
v_accvgpr_read_b32 v25, a184
v_lshlrev_b32 v24, 16, v25
v_and_b32 v25, 0xffff0000, v25
v_mul_f32_e32 v24, v26, v24
v_mul_f32_e32 v25, v26, v25
Cv 24,24,25
v_accvgpr_write_b32 a184, v24
v_accvgpr_read_b32 v25, a185
v_lshlrev_b32 v24, 16, v25
v_and_b32 v25, 0xffff0000, v25
v_mul_f32_e32 v24, v26, v24
v_mul_f32_e32 v25, v26, v25
Cv 24,24,25
v_accvgpr_write_b32 a185, v24
v_accvgpr_read_b32 v25, a186
v_lshlrev_b32 v24, 16, v25
v_and_b32 v25, 0xffff0000, v25
v_mul_f32_e32 v24, v26, v24
v_mul_f32_e32 v25, v26, v25
Cv 24,24,25
v_accvgpr_write_b32 a186, v24
v_accvgpr_read_b32 v25, a187
v_lshlrev_b32 v24, 16, v25
v_and_b32 v25, 0xffff0000, v25
v_mul_f32_e32 v24, v26, v24
v_mul_f32_e32 v25, v26, v25
Cv 24,24,25
v_accvgpr_write_b32 a187, v24
v_mov_b32 v25, v65
v_mov_b32 v24, v25
s_nop 1
v_permlane32_swap_b32_e32 v25, v24
v_add_f32_e32 v25, v25, v24
)" R"(
v_mov_b32 v24, v25
s_nop 1
v_permlane16_swap_b32_e32 v25, v24
v_add_f32_e32 v25, v25, v24
v_cmp_lt_f32_e32 vcc, 0, v25
s_nop 1
v_cndmask_b32_e32 v67, v20, v67, vcc
v_sub_f32_e32 v29, v20, v67
v_min_f32_e32 v27, 0, v29
v_exp_f32_e32 v27, v27
v_max_f32_e32 v29, 0, v29
v_sub_f32_e32 v24, 0, v29
v_exp_f32_e32 v24, v24
v_mul_f32_e32 v65, v27, v65
v_mul_f32_e32 v13, v24, v13
v_mul_f32_e32 v136, v24, v136
v_mul_f32_e32 v137, v24, v137
v_mul_f32_e32 v138, v24, v138
v_mul_f32_e32 v139, v24, v139
v_mul_f32_e32 v140, v24, v140
v_mul_f32_e32 v141, v24, v141
v_mul_f32_e32 v142, v24, v142
v_mul_f32_e32 v143, v24, v143
v_mul_f32_e32 v152, v24, v152
v_mul_f32_e32 v153, v24, v153
v_mul_f32_e32 v154, v24, v154
v_mul_f32_e32 v155, v24, v155
v_mul_f32_e32 v156, v24, v156
v_mul_f32_e32 v157, v24, v157
v_mul_f32_e32 v158, v24, v158
v_mul_f32_e32 v159, v24, v159
v_mul_f32_e32 v168, v24, v168
v_mul_f32_e32 v169, v24, v169
v_mul_f32_e32 v170, v24, v170
v_mul_f32_e32 v171, v24, v171
v_mul_f32_e32 v172, v24, v172
v_mul_f32_e32 v173, v24, v173
v_mul_f32_e32 v174, v24, v174
v_mul_f32_e32 v175, v24, v175
v_mul_f32_e32 v184, v24, v184
v_mul_f32_e32 v185, v24, v185
v_mul_f32_e32 v186, v24, v186
v_mul_f32_e32 v187, v24, v187
v_mul_f32_e32 v188, v24, v188
v_mul_f32_e32 v189, v24, v189
v_mul_f32_e32 v190, v24, v190
v_mul_f32_e32 v191, v24, v191
v_mul_f32_e32 v200, v24, v200
v_mul_f32_e32 v201, v24, v201
v_mul_f32_e32 v202, v24, v202
v_mul_f32_e32 v203, v24, v203
v_mul_f32_e32 v204, v24, v204
v_mul_f32_e32 v205, v24, v205
v_mul_f32_e32 v206, v24, v206
v_mul_f32_e32 v207, v24, v207
v_mul_f32_e32 v216, v24, v216
v_mul_f32_e32 v217, v24, v217
v_mul_f32_e32 v218, v24, v218
v_mul_f32_e32 v219, v24, v219
v_mul_f32_e32 v220, v24, v220
v_mul_f32_e32 v221, v24, v221
v_mul_f32_e32 v222, v24, v222
v_mul_f32_e32 v223, v24, v223
v_mul_f32_e32 v232, v24, v232
)" R"(
v_mul_f32_e32 v233, v24, v233
v_mul_f32_e32 v234, v24, v234
v_mul_f32_e32 v235, v24, v235
v_mul_f32_e32 v236, v24, v236
v_mul_f32_e32 v237, v24, v237
v_mul_f32_e32 v238, v24, v238
v_mul_f32_e32 v239, v24, v239
v_mul_f32_e32 v248, v24, v248
v_mul_f32_e32 v249, v24, v249
v_mul_f32_e32 v250, v24, v250
v_mul_f32_e32 v251, v24, v251
v_mul_f32_e32 v252, v24, v252
v_mul_f32_e32 v253, v24, v253
v_mul_f32_e32 v254, v24, v254
v_mul_f32_e32 v255, v24, v255
v_sub_f32_e32 v20, v20, v29
v_sub_f32_e32 v21, v21, v29
v_sub_f32_e32 v22, v22, v29
v_sub_f32_e32 v23, v23, v29
v_sub_f32_e32 v100, v100, v29
v_sub_f32_e32 v101, v101, v29
v_sub_f32_e32 v102, v102, v29
v_sub_f32_e32 v103, v103, v29
v_sub_f32_e32 v108, v108, v29
v_sub_f32_e32 v109, v109, v29
v_sub_f32_e32 v110, v110, v29
v_sub_f32_e32 v111, v111, v29
v_sub_f32_e32 v116, v116, v29
v_sub_f32_e32 v117, v117, v29
v_sub_f32_e32 v118, v118, v29
v_sub_f32_e32 v119, v119, v29
v_sub_f32_e32 v124, v124, v29
v_sub_f32_e32 v125, v125, v29
v_sub_f32_e32 v126, v126, v29
v_sub_f32_e32 v127, v127, v29
v_accvgpr_read_b32 v25, a132
v_lshlrev_b32 v24, 16, v25
v_and_b32 v25, 0xffff0000, v25
v_mul_f32_e32 v24, v27, v24
v_mul_f32_e32 v25, v27, v25
Cv 24,24,25
v_accvgpr_write_b32 a132, v24
v_accvgpr_read_b32 v25, a133
v_lshlrev_b32 v24, 16, v25
v_and_b32 v25, 0xffff0000, v25
v_mul_f32_e32 v24, v27, v24
v_mul_f32_e32 v25, v27, v25
Cv 24,24,25
v_accvgpr_write_b32 a133, v24
v_accvgpr_read_b32 v25, a134
v_lshlrev_b32 v24, 16, v25
v_and_b32 v25, 0xffff0000, v25
v_mul_f32_e32 v24, v27, v24
v_mul_f32_e32 v25, v27, v25
Cv 24,24,25
v_accvgpr_write_b32 a134, v24
v_accvgpr_read_b32 v25, a135
v_lshlrev_b32 v24, 16, v25
v_and_b32 v25, 0xffff0000, v25
v_mul_f32_e32 v24, v27, v24
v_mul_f32_e32 v25, v27, v25
Cv 24,24,25
v_accvgpr_write_b32 a135, v24
v_accvgpr_read_b32 v25, a140
)" R"(
v_lshlrev_b32 v24, 16, v25
v_and_b32 v25, 0xffff0000, v25
v_mul_f32_e32 v24, v27, v24
v_mul_f32_e32 v25, v27, v25
Cv 24,24,25
v_accvgpr_write_b32 a140, v24
v_accvgpr_read_b32 v25, a141
v_lshlrev_b32 v24, 16, v25
v_and_b32 v25, 0xffff0000, v25
v_mul_f32_e32 v24, v27, v24
v_mul_f32_e32 v25, v27, v25
Cv 24,24,25
v_accvgpr_write_b32 a141, v24
v_accvgpr_read_b32 v25, a142
v_lshlrev_b32 v24, 16, v25
v_and_b32 v25, 0xffff0000, v25
v_mul_f32_e32 v24, v27, v24
v_mul_f32_e32 v25, v27, v25
Cv 24,24,25
v_accvgpr_write_b32 a142, v24
v_accvgpr_read_b32 v25, a143
v_lshlrev_b32 v24, 16, v25
v_and_b32 v25, 0xffff0000, v25
v_mul_f32_e32 v24, v27, v24
v_mul_f32_e32 v25, v27, v25
Cv 24,24,25
v_accvgpr_write_b32 a143, v24
v_accvgpr_read_b32 v25, a148
v_lshlrev_b32 v24, 16, v25
v_and_b32 v25, 0xffff0000, v25
v_mul_f32_e32 v24, v27, v24
v_mul_f32_e32 v25, v27, v25
Cv 24,24,25
v_accvgpr_write_b32 a148, v24
v_accvgpr_read_b32 v25, a149
v_lshlrev_b32 v24, 16, v25
v_and_b32 v25, 0xffff0000, v25
v_mul_f32_e32 v24, v27, v24
v_mul_f32_e32 v25, v27, v25
Cv 24,24,25
v_accvgpr_write_b32 a149, v24
v_accvgpr_read_b32 v25, a150
v_lshlrev_b32 v24, 16, v25
v_and_b32 v25, 0xffff0000, v25
v_mul_f32_e32 v24, v27, v24
v_mul_f32_e32 v25, v27, v25
Cv 24,24,25
v_accvgpr_write_b32 a150, v24
v_accvgpr_read_b32 v25, a151
v_lshlrev_b32 v24, 16, v25
v_and_b32 v25, 0xffff0000, v25
v_mul_f32_e32 v24, v27, v24
v_mul_f32_e32 v25, v27, v25
Cv 24,24,25
v_accvgpr_write_b32 a151, v24
v_accvgpr_read_b32 v25, a156
v_lshlrev_b32 v24, 16, v25
v_and_b32 v25, 0xffff0000, v25
v_mul_f32_e32 v24, v27, v24
v_mul_f32_e32 v25, v27, v25
Cv 24,24,25
v_accvgpr_write_b32 a156, v24
v_accvgpr_read_b32 v25, a157
v_lshlrev_b32 v24, 16, v25
)" R"(
v_and_b32 v25, 0xffff0000, v25
v_mul_f32_e32 v24, v27, v24
v_mul_f32_e32 v25, v27, v25
Cv 24,24,25
v_accvgpr_write_b32 a157, v24
v_accvgpr_read_b32 v25, a158
v_lshlrev_b32 v24, 16, v25
v_and_b32 v25, 0xffff0000, v25
v_mul_f32_e32 v24, v27, v24
v_mul_f32_e32 v25, v27, v25
Cv 24,24,25
v_accvgpr_write_b32 a158, v24
v_accvgpr_read_b32 v25, a159
v_lshlrev_b32 v24, 16, v25
v_and_b32 v25, 0xffff0000, v25
v_mul_f32_e32 v24, v27, v24
v_mul_f32_e32 v25, v27, v25
Cv 24,24,25
v_accvgpr_write_b32 a159, v24
v_accvgpr_read_b32 v25, a164
v_lshlrev_b32 v24, 16, v25
v_and_b32 v25, 0xffff0000, v25
v_mul_f32_e32 v24, v27, v24
v_mul_f32_e32 v25, v27, v25
Cv 24,24,25
v_accvgpr_write_b32 a164, v24
v_accvgpr_read_b32 v25, a165
v_lshlrev_b32 v24, 16, v25
v_and_b32 v25, 0xffff0000, v25
v_mul_f32_e32 v24, v27, v24
v_mul_f32_e32 v25, v27, v25
Cv 24,24,25
v_accvgpr_write_b32 a165, v24
v_accvgpr_read_b32 v25, a166
v_lshlrev_b32 v24, 16, v25
v_and_b32 v25, 0xffff0000, v25
v_mul_f32_e32 v24, v27, v24
v_mul_f32_e32 v25, v27, v25
Cv 24,24,25
v_accvgpr_write_b32 a166, v24
v_accvgpr_read_b32 v25, a167
v_lshlrev_b32 v24, 16, v25
v_and_b32 v25, 0xffff0000, v25
v_mul_f32_e32 v24, v27, v24
v_mul_f32_e32 v25, v27, v25
Cv 24,24,25
v_accvgpr_write_b32 a167, v24
v_accvgpr_read_b32 v25, a172
v_lshlrev_b32 v24, 16, v25
v_and_b32 v25, 0xffff0000, v25
v_mul_f32_e32 v24, v27, v24
v_mul_f32_e32 v25, v27, v25
Cv 24,24,25
v_accvgpr_write_b32 a172, v24
v_accvgpr_read_b32 v25, a173
v_lshlrev_b32 v24, 16, v25
v_and_b32 v25, 0xffff0000, v25
v_mul_f32_e32 v24, v27, v24
v_mul_f32_e32 v25, v27, v25
Cv 24,24,25
v_accvgpr_write_b32 a173, v24
v_accvgpr_read_b32 v25, a174
v_lshlrev_b32 v24, 16, v25
v_and_b32 v25, 0xffff0000, v25
)" R"(
v_mul_f32_e32 v24, v27, v24
v_mul_f32_e32 v25, v27, v25
Cv 24,24,25
v_accvgpr_write_b32 a174, v24
v_accvgpr_read_b32 v25, a175
v_lshlrev_b32 v24, 16, v25
v_and_b32 v25, 0xffff0000, v25
v_mul_f32_e32 v24, v27, v24
v_mul_f32_e32 v25, v27, v25
Cv 24,24,25
v_accvgpr_write_b32 a175, v24
v_accvgpr_read_b32 v25, a180
v_lshlrev_b32 v24, 16, v25
v_and_b32 v25, 0xffff0000, v25
v_mul_f32_e32 v24, v27, v24
v_mul_f32_e32 v25, v27, v25
Cv 24,24,25
v_accvgpr_write_b32 a180, v24
v_accvgpr_read_b32 v25, a181
v_lshlrev_b32 v24, 16, v25
v_and_b32 v25, 0xffff0000, v25
v_mul_f32_e32 v24, v27, v24
v_mul_f32_e32 v25, v27, v25
Cv 24,24,25
v_accvgpr_write_b32 a181, v24
v_accvgpr_read_b32 v25, a182
v_lshlrev_b32 v24, 16, v25
v_and_b32 v25, 0xffff0000, v25
v_mul_f32_e32 v24, v27, v24
v_mul_f32_e32 v25, v27, v25
Cv 24,24,25
v_accvgpr_write_b32 a182, v24
v_accvgpr_read_b32 v25, a183
v_lshlrev_b32 v24, 16, v25
v_and_b32 v25, 0xffff0000, v25
v_mul_f32_e32 v24, v27, v24
v_mul_f32_e32 v25, v27, v25
Cv 24,24,25
v_accvgpr_write_b32 a183, v24
v_accvgpr_read_b32 v25, a188
v_lshlrev_b32 v24, 16, v25
v_and_b32 v25, 0xffff0000, v25
v_mul_f32_e32 v24, v27, v24
v_mul_f32_e32 v25, v27, v25
Cv 24,24,25
v_accvgpr_write_b32 a188, v24
v_accvgpr_read_b32 v25, a189
v_lshlrev_b32 v24, 16, v25
v_and_b32 v25, 0xffff0000, v25
v_mul_f32_e32 v24, v27, v24
v_mul_f32_e32 v25, v27, v25
Cv 24,24,25
v_accvgpr_write_b32 a189, v24
v_accvgpr_read_b32 v25, a190
v_lshlrev_b32 v24, 16, v25
v_and_b32 v25, 0xffff0000, v25
v_mul_f32_e32 v24, v27, v24
v_mul_f32_e32 v25, v27, v25
Cv 24,24,25
v_accvgpr_write_b32 a190, v24
v_accvgpr_read_b32 v25, a191
v_lshlrev_b32 v24, 16, v25
v_and_b32 v25, 0xffff0000, v25
v_mul_f32_e32 v24, v27, v24
)" R"(
v_mul_f32_e32 v25, v27, v25
Cv 24,24,25
v_accvgpr_write_b32 a191, v24
v_exp_f32_e32 v48, v96
v_exp_f32_e32 v24, v97
s_nop 0
v_add_f32_e32 v14, v48, v24
Cv 48,48,24
v_exp_f32_e32 v49, v98
v_exp_f32_e32 v25, v99
v_add_f32_e32 v14, v14, v49
v_add_f32_e32 v14, v14, v25
Cv 49,49,25
v_exp_f32_e32 v50, v104
v_exp_f32_e32 v26, v105
v_add_f32_e32 v14, v14, v50
v_add_f32_e32 v14, v14, v26
Cv 50,50,26
v_exp_f32_e32 v51, v106
v_exp_f32_e32 v27, v107
v_add_f32_e32 v14, v14, v51
v_add_f32_e32 v14, v14, v27
Cv 51,51,27
v_exp_f32_e32 v52, v100
v_exp_f32_e32 v28, v101
s_nop 0
v_add_f32_e32 v15, v52, v28
Cv 52,52,28
v_exp_f32_e32 v53, v102
v_exp_f32_e32 v29, v103
v_add_f32_e32 v15, v15, v53
v_add_f32_e32 v15, v15, v29
Cv 53,53,29
v_exp_f32_e32 v54, v108
v_exp_f32_e32 v24, v109
v_add_f32_e32 v15, v15, v54
v_add_f32_e32 v15, v15, v24
Cv 54,54,24
v_exp_f32_e32 v55, v110
v_exp_f32_e32 v25, v111
v_add_f32_e32 v15, v15, v55
v_add_f32_e32 v15, v15, v25
Cv 55,55,25
s_nop 1
s_branch .LzMRR
.Lzsp18:
s_sub_u32 s53, s53, 1
s_cbranch_scc1 .Lzsq18
s_sleep 1
buffer_load_dword v4, v7, s[20:23], s56 offen offset:4 sc0 sc1
s_waitcnt vmcnt(0)
v_cmp_eq_u32_e32 vcc, s3, v4
s_andn2_b64 s[28:29], 1, vcc
s_cbranch_scc1 .Lzsp18
s_branch .Lzok18
.Lzsq18:
s_mov_b32 s53, 0
s_branch .Lzok18
.LzPBKX:
Mv 32,0,32
Rt 128,18,16384
Mv 64,0,36
Mv 96,0,40
Rt 130,18,18432
)" R"(
Mv 128,0,44
s_add_u32 s39, s39, 0x40000
Mv 160,0,48
Rt 132,19,16384
s_add_u32 m0, s37, 49152
Mv 192,0,52
buffer_load_dwordx4 v12, s[8:11], s39 offen offset:0 lds
Mv 224,0,56
Rt 134,19,18432
Ma 192,0,60
Ma 196,4,60
Rt 136,20,16384
Mv 228,4,56
Mv 196,4,52
Rt 138,20,18432
Mv 164,4,48
Mv 132,4,44
Rt 140,21,16384
Mv 100,4,40
buffer_load_dwordx4 v13, s[8:11], s39 offen offset:1024 lds
Mv 68,4,36
Rt 142,21,18432
Mv 36,4,32
Mv 40,8,32
Rt 144,22,16384
Mv 72,8,36
Mv 104,8,40
Rt 146,22,18432
Mv 136,8,44
Mv 168,8,48
Rt 148,23,16384
Mv 200,8,52
buffer_load_dwordx4 v14, s[8:11], s39 offen offset:2048 lds
Mv 232,8,56
Rt 150,23,18432
Ma 200,8,60
Ma 204,12,60
Rt 152,24,16384
Mv 236,12,56
Mv 204,12,52
Rt 154,24,18432
Mv 172,12,48
Mv 140,12,44
Rt 156,25,16384
Mv 108,12,40
buffer_load_dwordx4 v15, s[8:11], s39 offen offset:3072 lds
Mv 76,12,36
Rt 158,25,18432
Mv 44,12,32
Mv 48,16,32
Rb 160,17,0
Mv 80,16,36
Mv 112,16,40
Rb 164,17,2048
Mv 144,16,44
s_add_u32 s39, s39, 0x40000
Mv 176,16,48
Rb 168,17,8192
s_add_u32 m0, s37, 0
Mv 208,16,52
buffer_load_dwordx4 v12, s[8:11], s39 offen offset:0 lds
Mv 240,16,56
Rb 172,17,10240
Ma 208,16,60
)" R"(
Ma 212,20,60
Rb 176,17,16384
Mv 244,20,56
Mv 212,20,52
Rb 180,17,18432
Mv 180,20,48
Mv 148,20,44
Rb 184,17,24576
Mv 116,20,40
buffer_load_dwordx4 v13, s[8:11], s39 offen offset:1024 lds
Mv 84,20,36
Rb 188,17,26624
Mv 52,20,32
Mv 56,24,32
Mv 88,24,36
Mv 120,24,40
Mv 152,24,44
Mv 184,24,48
Mv 216,24,52
buffer_load_dwordx4 v14, s[8:11], s39 offen offset:2048 lds
Mv 248,24,56
Ma 216,24,60
Ma 220,28,60
Mv 252,28,56
Mv 220,28,52
Mv 188,28,48
Mv 156,28,44
Mv 124,28,40
buffer_load_dwordx4 v15, s[8:11], s39 offen offset:3072 lds
Mv 92,28,36
Mv 60,28,32
s_waitcnt vmcnt(8) lgkmcnt(0)
s_barrier
Mv 32,128,160
Rt 0,18,32768
Mv 64,128,164
s_add_u32 s38, s38, 0xffff0080
Mv 96,128,168
Rt 2,18,34816
s_add_u32 m0, s36, 0
Mv 128,128,172
buffer_load_dwordx4 v8, s[4:7], s38 offen offset:0 lds
Mv 160,128,176
Rt 4,19,32768
Mv 192,128,180
Mv 224,128,184
Rt 6,19,34816
Ma 192,128,188
buffer_load_dwordx4 v9, s[4:7], s38 offen offset:1024 lds
Ma 196,132,188
Rt 8,20,32768
Mv 228,132,184
Mv 196,132,180
Rt 10,20,34816
Mv 164,132,176
buffer_load_dwordx4 v10, s[4:7], s38 offen offset:2048 lds
Mv 132,132,172
Rt 12,21,32768
Mv 100,132,168
Mv 68,132,164
Rt 14,21,34816
Mv 36,132,160
buffer_load_dwordx4 v11, s[4:7], s38 offen offset:3072 lds
Mv 40,136,160
Rt 16,22,32768
)" R"(
Mv 72,136,164
s_add_u32 s38, s38, 0x10000
Mv 104,136,168
Rt 18,22,34816
s_add_u32 m0, s36, 4096
Mv 136,136,172
buffer_load_dwordx4 v8, s[4:7], s38 offen offset:0 lds
Mv 168,136,176
Rt 20,23,32768
Mv 200,136,180
Mv 232,136,184
Rt 22,23,34816
Ma 200,136,188
buffer_load_dwordx4 v9, s[4:7], s38 offen offset:1024 lds
Ma 204,140,188
Rt 24,24,32768
Mv 236,140,184
Mv 204,140,180
Rt 26,24,34816
Mv 172,140,176
buffer_load_dwordx4 v10, s[4:7], s38 offen offset:2048 lds
Mv 140,140,172
Rt 28,25,32768
Mv 108,140,168
Mv 76,140,164
Rt 30,25,34816
Mv 44,140,160
buffer_load_dwordx4 v11, s[4:7], s38 offen offset:3072 lds
Mv 48,144,160
Rb 32,16,32768
Mv 80,144,164
Mv 112,144,168
Rb 36,16,34816
Mv 144,144,172
Mv 176,144,176
Rb 40,16,40960
Mv 208,144,180
Mv 240,144,184
Rb 44,16,43008
Ma 208,144,188
Ma 212,148,188
Rb 48,16,49152
Mv 244,148,184
Mv 212,148,180
Rb 52,16,51200
Mv 180,148,176
Mv 148,148,172
Rb 56,16,57344
Mv 116,148,168
Mv 84,148,164
Rb 60,16,59392
Mv 52,148,160
Mv 56,152,160
Mv 88,152,164
Mv 120,152,168
Mv 152,152,172
Mv 184,152,176
Mv 216,152,180
Mv 248,152,184
Ma 216,152,188
Ma 220,156,188
Mv 252,156,184
Mv 220,156,180
Mv 188,156,176
)" R"(
Mv 156,156,172
Mv 124,156,168
Mv 92,156,164
Mv 60,156,160
s_waitcnt vmcnt(12) lgkmcnt(0)
s_barrier
Mv 32,0,32
Rt 128,18,49152
Mv 64,0,36
Mv 96,0,40
Rt 130,18,51200
v_lshrrev_b32 v5, 2, v0
Mv 128,0,44
s_add_u32 s39, s39, 0x40000
v_and_b32 v5, 8, v5
Mv 160,0,48
Rt 132,19,49152
s_add_u32 m0, s37, 16384
Mv 192,0,52
buffer_load_dwordx4 v12, s[8:11], s39 offen offset:0 lds
Mv 224,0,56
Rt 134,19,51200
v_and_or_b32 v5, v0, 7, v5
Ma 192,0,60
v_lshlrev_b32 v5, 8, v5
v_and_or_b32 v5, v0, 24, v5
Ma 196,4,60
Rt 136,20,49152
v_lshrrev_b32 v5, 1, v5
Mv 228,4,56
Mv 196,4,52
Rt 138,20,51200
Mv 164,4,48
Mv 132,4,44
Rt 140,21,49152
Mv 100,4,40
buffer_load_dwordx4 v13, s[8:11], s39 offen offset:1024 lds
Mv 68,4,36
Rt 142,21,51200
Mv 36,4,32
Mv 40,8,32
Rt 144,22,49152
Mv 72,8,36
Mv 104,8,40
Rt 146,22,51200
Mv 136,8,44
Mv 168,8,48
Rt 148,23,49152
Mv 200,8,52
buffer_load_dwordx4 v14, s[8:11], s39 offen offset:2048 lds
Mv 232,8,56
Rt 150,23,51200
Ma 200,8,60
Ma 204,12,60
Rt 152,24,49152
Mv 236,12,56
Mv 204,12,52
Rt 154,24,51200
Mv 172,12,48
Mv 140,12,44
Rt 156,25,49152
Mv 108,12,40
buffer_load_dwordx4 v15, s[8:11], s39 offen offset:3072 lds
Mv 76,12,36
Rt 158,25,51200
)" R"(
Mv 44,12,32
Mv 48,16,32
Rb 160,17,32768
Mv 80,16,36
Mv 112,16,40
Rb 164,17,34816
Mv 144,16,44
s_add_u32 s39, s39, 0x40000
Mv 176,16,48
Rb 168,17,40960
s_add_u32 m0, s37, 32768
Mv 208,16,52
buffer_load_dwordx4 v12, s[8:11], s39 offen offset:0 lds
Mv 240,16,56
Rb 172,17,43008
Ma 208,16,60
Ma 212,20,60
Rb 176,17,49152
Mv 244,20,56
Mv 212,20,52
Rb 180,17,51200
Mv 180,20,48
Mv 148,20,44
Rb 184,17,57344
Mv 116,20,40
buffer_load_dwordx4 v13, s[8:11], s39 offen offset:1024 lds
Mv 84,20,36
Rb 188,17,59392
Mv 52,20,32
Mv 56,24,32
Rt 224,29,0
Mv 88,24,36
Rt 226,29,8192
Mv 120,24,40
Rt 228,29,128
Mv 152,24,44
Rt 230,29,8320
Mv 184,24,48
Rt 232,29,256
Mv 216,24,52
buffer_load_dwordx4 v14, s[8:11], s39 offen offset:2048 lds
Rt 234,29,8448
Mv 248,24,56
Rt 236,29,384
Ma 216,24,60
Rt 238,29,8576
Ma 220,28,60
Mv 252,28,56
Mv 220,28,52
Mv 188,28,48
Mv 156,28,44
Mv 124,28,40
buffer_load_dwordx4 v15, s[8:11], s39 offen offset:3072 lds
Mv 92,28,36
Mv 60,28,32
s_waitcnt vmcnt(8) lgkmcnt(0)
s_barrier
Mv 32,128,160
Rt 0,18,0
Mv 64,128,164
s_add_u32 s38, s38, 0xffff0080
Mv 96,128,168
Rt 2,18,2048
s_add_u32 m0, s36, 32768
Mv 128,128,172
)" R"(
buffer_load_dwordx4 v8, s[4:7], s38 offen offset:0 lds
Mv 160,128,176
Rt 4,19,0
Mv 192,128,180
Mv 224,128,184
Rt 6,19,2048
Ma 192,128,188
buffer_load_dwordx4 v9, s[4:7], s38 offen offset:1024 lds
Ma 196,132,188
Rt 8,20,0
Mv 228,132,184
Mv 196,132,180
Rt 10,20,2048
Mv 164,132,176
buffer_load_dwordx4 v10, s[4:7], s38 offen offset:2048 lds
Mv 132,132,172
Rt 12,21,0
Mv 100,132,168
Mv 68,132,164
Rt 14,21,2048
Mv 36,132,160
buffer_load_dwordx4 v11, s[4:7], s38 offen offset:3072 lds
Mv 40,136,160
Rt 16,22,0
Mv 72,136,164
s_add_u32 s38, s38, 0x10000
Mv 104,136,168
Rt 18,22,2048
s_add_u32 m0, s36, 36864
Mv 136,136,172
buffer_load_dwordx4 v8, s[4:7], s38 offen offset:0 lds
Mv 168,136,176
Rt 20,23,0
Mv 200,136,180
Mv 232,136,184
Rt 22,23,2048
Ma 200,136,188
buffer_load_dwordx4 v9, s[4:7], s38 offen offset:1024 lds
Ma 204,140,188
Rt 24,24,0
Mv 236,140,184
Mv 204,140,180
Rt 26,24,2048
Mv 172,140,176
buffer_load_dwordx4 v10, s[4:7], s38 offen offset:2048 lds
Mv 140,140,172
Rt 28,25,0
s_lshl3_add_u32 s28, s94, s92
Mv 108,140,168
s_lshl3_add_u32 s29, s92, s94
s_cmp_lt_u32 s94, 8
s_cmov_b32 s28, s29
Mv 76,140,164
Rt 30,25,2048
s_mul_i32 s29, s28, 293
s_lshr_b32 s29, s29, 11
Mv 44,140,160
buffer_load_dwordx4 v11, s[4:7], s38 offen offset:3072 lds
Mv 48,144,160
Rb 32,16,0
s_mul_i32 s29, s29, 9
s_add_u32 s29, s29, s28
Mv 80,144,164
s_lshl_b32 s29, s29, 10
)" R"(
s_add_u32 s29, s29, s82
s_add_u32 s29, s29, s84
Mv 112,144,168
Rb 36,16,2048
s_add_u32 s94, s94, 4
Mv 144,144,172
buffer_store_dwordx4 a[224:227], v1, s[20:23], s29 offen sc0 sc1
Mv 176,144,176
Rb 40,16,8192
s_lshl3_add_u32 s28, s94, s92
Mv 208,144,180
s_lshl3_add_u32 s29, s92, s94
Mv 240,144,184
Rb 44,16,10240
s_cmp_lt_u32 s94, 8
s_cmov_b32 s28, s29
Ma 208,144,188
s_mul_i32 s29, s28, 293
s_lshr_b32 s29, s29, 11
Ma 212,148,188
Rb 48,16,16384
s_mul_i32 s29, s29, 9
s_add_u32 s29, s29, s28
Mv 244,148,184
s_lshl_b32 s29, s29, 10
s_add_u32 s29, s29, s82
Mv 212,148,180
Rb 52,16,18432
s_add_u32 s29, s29, s84
Mv 180,148,176
s_add_u32 s94, s94, 4
buffer_store_dwordx4 a[228:231], v1, s[20:23], s29 offen sc0 sc1
Mv 148,148,172
Rb 56,16,24576
s_lshl3_add_u32 s28, s94, s92
Mv 116,148,168
s_lshl3_add_u32 s29, s92, s94
Mv 84,148,164
Rb 60,16,26624
s_cmp_lt_u32 s94, 8
s_cmov_b32 s28, s29
Mv 52,148,160
s_mul_i32 s29, s28, 293
s_lshr_b32 s29, s29, 11
Mv 56,152,160
s_mul_i32 s29, s29, 9
s_add_u32 s29, s29, s28
Mv 88,152,164
s_lshl_b32 s29, s29, 10
s_add_u32 s29, s29, s82
Mv 120,152,168
s_add_u32 s29, s29, s84
s_add_u32 s94, s94, 4
Mv 152,152,172
buffer_store_dwordx4 a[232:235], v1, s[20:23], s29 offen sc0 sc1
Mv 184,152,176
s_lshl3_add_u32 s28, s94, s92
s_lshl3_add_u32 s29, s92, s94
Mv 216,152,180
s_cmp_lt_u32 s94, 8
s_cmov_b32 s28, s29
Mv 248,152,184
s_mul_i32 s29, s28, 293
s_lshr_b32 s29, s29, 11
)" R"(
Ma 216,152,188
s_mul_i32 s29, s29, 9
s_add_u32 s29, s29, s28
Ma 220,156,188
s_lshl_b32 s29, s29, 10
s_add_u32 s29, s29, s82
Mv 252,156,184
s_add_u32 s29, s29, s84
Mv 220,156,180
s_cmp_gt_u32 s33, 1
s_cmov_b32 s29, 0x40000000
Mv 188,156,176
buffer_store_dwordx4 a[236:239], v1, s[20:23], s29 offen sc0 sc1
Mv 156,156,172
Mv 124,156,168
Mv 92,156,164
Mv 60,156,160
s_waitcnt vmcnt(16) lgkmcnt(0)
s_barrier
Mv 32,0,32
Rt 128,18,16384
Mv 64,0,36
Mv 96,0,40
Rt 130,18,18432
Mv 128,0,44
s_add_u32 s39, s39, 0x40000
Mv 160,0,48
Rt 132,19,16384
s_add_u32 m0, s37, 49152
Mv 192,0,52
buffer_load_dwordx4 v12, s[8:11], s39 offen offset:0 lds
Mv 224,0,56
Rt 134,19,18432
Ma 192,0,60
Ma 196,4,60
Rt 136,20,16384
Mv 228,4,56
Mv 196,4,52
Rt 138,20,18432
Mv 164,4,48
Mv 132,4,44
Rt 140,21,16384
Mv 100,4,40
buffer_load_dwordx4 v13, s[8:11], s39 offen offset:1024 lds
Mv 68,4,36
Rt 142,21,18432
Mv 36,4,32
Mv 40,8,32
Rt 144,22,16384
Mv 72,8,36
Mv 104,8,40
Rt 146,22,18432
Mv 136,8,44
Mv 168,8,48
Rt 148,23,16384
Mv 200,8,52
buffer_load_dwordx4 v14, s[8:11], s39 offen offset:2048 lds
Mv 232,8,56
Rt 150,23,18432
Ma 200,8,60
Ma 204,12,60
Rt 152,24,16384
Mv 236,12,56
Mv 204,12,52
Rt 154,24,18432
)" R"(
Mv 172,12,48
Mv 140,12,44
Rt 156,25,16384
Mv 108,12,40
buffer_load_dwordx4 v15, s[8:11], s39 offen offset:3072 lds
Mv 76,12,36
Rt 158,25,18432
Mv 44,12,32
Mv 48,16,32
Rb 160,17,0
Mv 80,16,36
buffer_load_dword v4, v5, s[20:23], s50 offen offset:0 sc0 sc1
Mv 112,16,40
Rb 164,17,2048
Mv 144,16,44
Mv 176,16,48
Rb 168,17,8192
Mv 208,16,52
Mv 240,16,56
Rb 172,17,10240
Ma 208,16,60
Ma 212,20,60
Rb 176,17,16384
Mv 244,20,56
Mv 212,20,52
Rb 180,17,18432
Mv 180,20,48
Mv 148,20,44
Rb 184,17,24576
Mv 116,20,40
Mv 84,20,36
Rb 188,17,26624
Mv 52,20,32
Mv 56,24,32
Mv 88,24,36
Mv 120,24,40
Mv 152,24,44
Mv 184,24,48
Mv 216,24,52
Mv 248,24,56
Ma 216,24,60
Ma 220,28,60
Mv 252,28,56
Mv 220,28,52
Mv 188,28,48
Mv 156,28,44
Mv 124,28,40
Mv 92,28,36
Mv 60,28,32
s_waitcnt vmcnt(0) lgkmcnt(0)
s_barrier
Mv 32,128,160
Rt 0,18,32768
v_cmp_eq_u32_e32 vcc, s3, v4
s_andn2_b64 s[28:29], s[60:61], vcc
s_cbranch_scc1 .Lzsp11
.Lzok11:
Mv 64,128,164
Mv 96,128,168
Rt 2,18,34816
Mv 128,128,172
Mv 160,128,176
Rt 4,19,32768
s_add_u32 m0, s36, 0
Mv 192,128,180
)" R"(
buffer_load_dwordx4 v2, s[20:23], s55 offen offset:0 sc1 lds
Mv 224,128,184
Rt 6,19,34816
Ma 192,128,188
Ma 196,132,188
Rt 8,20,32768
Mv 228,132,184
Mv 196,132,180
Rt 10,20,34816
Mv 164,132,176
buffer_load_dwordx4 v2, s[20:23], s55 offen offset:1024 sc1 lds
Mv 132,132,172
Rt 12,21,32768
Mv 100,132,168
Mv 68,132,164
Rt 14,21,34816
Mv 36,132,160
Mv 40,136,160
Rt 16,22,32768
Mv 72,136,164
buffer_load_dwordx4 v2, s[20:23], s55 offen offset:2048 sc1 lds
Mv 104,136,168
Rt 18,22,34816
Mv 136,136,172
Mv 168,136,176
Rt 20,23,32768
Mv 200,136,180
Mv 232,136,184
Rt 22,23,34816
Ma 200,136,188
buffer_load_dwordx4 v2, s[20:23], s55 offen offset:3072 sc1 lds
Ma 204,140,188
Rt 24,24,32768
Mv 236,140,184
Mv 204,140,180
Rt 26,24,34816
Mv 172,140,176
Mv 140,140,172
Rt 28,25,32768
s_add_u32 m0, s36, 4096
Mv 108,140,168
buffer_load_dwordx4 v3, s[20:23], s55 offen offset:0 sc1 lds
Mv 76,140,164
Rt 30,25,34816
Mv 44,140,160
Mv 48,144,160
Rb 32,16,32768
Mv 80,144,164
Mv 112,144,168
Rb 36,16,34816
Mv 144,144,172
buffer_load_dwordx4 v3, s[20:23], s55 offen offset:1024 sc1 lds
Mv 176,144,176
Rb 40,16,40960
Mv 208,144,180
Mv 240,144,184
Rb 44,16,43008
Ma 208,144,188
Ma 212,148,188
Rb 48,16,49152
Mv 244,148,184
buffer_load_dwordx4 v3, s[20:23], s55 offen offset:2048 sc1 lds
Mv 212,148,180
Rb 52,16,51200
)" R"(
Mv 180,148,176
Mv 148,148,172
Rb 56,16,57344
Mv 116,148,168
Mv 84,148,164
Rb 60,16,59392
Mv 52,148,160
buffer_load_dwordx4 v3, s[20:23], s55 offen offset:3072 sc1 lds
Mv 56,152,160
Mv 88,152,164
Mv 120,152,168
Mv 152,152,172
Mv 184,152,176
Mv 216,152,180
Mv 248,152,184
Ma 216,152,188
Ma 220,156,188
s_add_u32 s55, s55, 0x10000
Mv 252,156,184
Mv 220,156,180
Mv 188,156,176
Mv 156,156,172
Mv 124,156,168
Mv 92,156,164
Mv 60,156,160
s_waitcnt lgkmcnt(4)
s_barrier
Mv 32,0,32
Rt 128,18,49152
Mv 64,0,36
Rt 130,18,51200
Mv 96,0,40
Rt 132,19,49152
Mv 128,0,44
Rt 134,19,51200
Mv 132,4,44
Rt 136,20,49152
Mv 100,4,40
Rt 138,20,51200
Mv 68,4,36
Rt 140,21,49152
Mv 36,4,32
Rt 142,21,51200
Mv 40,8,32
Rt 144,22,49152
Mv 72,8,36
Rt 146,22,51200
Mv 104,8,40
Rt 148,23,49152
Mv 136,8,44
Rt 150,23,51200
Mv 140,12,44
Rt 152,24,49152
Mv 108,12,40
Rt 154,24,51200
Mv 76,12,36
Rt 156,25,49152
Mv 44,12,32
Rt 158,25,51200
Mv 48,16,32
Rb 160,17,32768
Mv 80,16,36
Rb 164,17,34816
Mv 112,16,40
Rb 168,17,40960
)" R"(
Mv 144,16,44
Rb 172,17,43008
Mv 148,20,44
Rb 176,17,49152
Mv 116,20,40
Rb 180,17,51200
Mv 84,20,36
Rb 184,17,57344
Mv 52,20,32
Rb 188,17,59392
Mv 56,24,32
v_add_u32 v8, 0x10000, v1
v_add_u32 v10, 0x20000, v1
Mv 88,24,36
s_mov_b32 s93, 2
s_mov_b32 s94, 0
Mv 120,24,40
Mv 152,24,44
Mv 156,28,44
Mv 124,28,40
Mv 92,28,36
Mv 60,28,32
s_waitcnt lgkmcnt(0)
s_barrier
Mv 32,128,160
Mv 36,132,160
Mv 40,136,160
s_add_u32 m0, s36, 32768
Mv 44,140,160
buffer_load_dwordx4 v2, s[20:23], s55 offen offset:0 sc1 lds
Mv 48,144,160
Mv 52,148,160
s_nop 0
Cv 32,32,33
Cv 33,34,35
Mv 56,152,160
Cv 34,36,37
Cv 35,38,39
Mv 60,156,160
buffer_load_dwordx4 v2, s[20:23], s55 offen offset:1024 sc1 lds
Mv 64,128,164
Cv 40,40,41
Cv 41,42,43
Mv 68,132,164
Cv 42,44,45
Cv 43,46,47
Mv 72,136,164
Cv 48,48,49
Cv 49,50,51
Mv 76,140,164
buffer_load_dwordx4 v2, s[20:23], s55 offen offset:2048 sc1 lds
Mv 80,144,164
Cv 50,52,53
Cv 51,54,55
Mv 84,148,164
Cv 56,56,57
Cv 57,58,59
Mv 88,152,164
Cv 58,60,61
Cv 59,62,63
Mv 92,156,164
buffer_load_dwordx4 v2, s[20:23], s55 offen offset:3072 sc1 lds
Mv 96,128,168
Cv 64,64,65
Cv 65,66,67
)" R"(
Mv 100,132,168
Cv 66,68,69
Cv 67,70,71
Mv 104,136,168
s_add_u32 m0, s36, 36864
Cv 72,72,73
Mv 108,140,168
buffer_load_dwordx4 v3, s[20:23], s55 offen offset:0 sc1 lds
Mv 112,144,168
Cv 73,74,75
Cv 74,76,77
Mv 116,148,168
Cv 75,78,79
Cv 80,80,81
Mv 120,152,168
Cv 81,82,83
Cv 82,84,85
Mv 124,156,168
buffer_store_dwordx4 v[32:35], v2, s[20:23], s45 offen offset:0 sc0 sc1
Mv 128,128,172
Cv 83,86,87
Cv 88,88,89
Mv 132,132,172
Cv 89,90,91
Cv 90,92,93
Mv 136,136,172
Cv 91,94,95
Mv 140,140,172
buffer_load_dwordx4 v3, s[20:23], s55 offen offset:1024 sc1 lds
Mv 144,144,172
Mv 148,148,172
Cv 128,128,129
Cv 129,130,131
Mv 152,152,172
Cv 130,132,133
Cv 131,134,135
Mv 156,156,172
buffer_store_dwordx4 v[40:43], v2, s[20:23], s45 offen offset:1024 sc0 sc1
Mv 160,0,48
Cv 136,136,137
Cv 137,138,139
Mv 160,128,176
Cv 138,140,141
Cv 139,142,143
Mv 164,4,48
Cv 144,144,145
Cv 145,146,147
Mv 164,132,176
buffer_load_dwordx4 v3, s[20:23], s55 offen offset:2048 sc1 lds
Mv 168,8,48
Cv 146,148,149
Cv 147,150,151
Mv 168,136,176
Cv 152,152,153
Cv 153,154,155
Mv 172,12,48
Cv 154,156,157
Cv 155,158,159
Mv 172,140,176
buffer_store_dwordx4 v[48:51], v2, s[20:23], s45 offen offset:2048 sc0 sc1
Mv 176,16,48
Cv 132,96,97
Cv 133,98,99
Mv 176,144,176
)" R"(
Cv 134,100,101
Cv 135,102,103
Mv 180,20,48
Cv 140,104,105
Cv 141,106,107
Mv 180,148,176
buffer_load_dwordx4 v3, s[20:23], s55 offen offset:3072 sc1 lds
Mv 184,24,48
Cv 142,108,109
Cv 143,110,111
Mv 184,152,176
Cv 148,112,113
Cv 149,114,115
Mv 188,28,48
Cv 150,116,117
Cv 151,118,119
Mv 188,156,176
buffer_store_dwordx4 v[56:59], v2, s[20:23], s45 offen offset:3072 sc0 sc1
Mv 192,0,52
Cv 156,120,121
Cv 157,122,123
Mv 192,128,180
Cv 158,124,125
Cv 159,126,127
Mv 196,4,52
s_add_u32 m0, s36, 98304
Cv 160,160,161
Mv 196,132,180
buffer_load_dwordx4 v2, s[20:23], s56 offen offset:0 sc1 lds
Mv 200,8,52
Cv 161,162,163
Cv 162,164,165
Mv 200,136,180
Cv 163,166,167
v_accvgpr_write_b32 a224, v160
Mv 204,12,52
v_accvgpr_write_b32 a225, v161
v_accvgpr_write_b32 a226, v162
Mv 204,140,180
buffer_store_dwordx4 v[64:67], v3, s[20:23], s45 offen offset:0 sc0 sc1
Mv 208,16,52
v_accvgpr_write_b32 a227, v163
Cv 168,168,169
Mv 208,144,180
Cv 169,170,171
Cv 170,172,173
Mv 212,20,52
Cv 171,174,175
v_accvgpr_write_b32 a228, v168
Mv 212,148,180
buffer_load_dwordx4 v2, s[20:23], s56 offen offset:1024 sc1 lds
Mv 216,24,52
v_accvgpr_write_b32 a229, v169
v_accvgpr_write_b32 a230, v170
Mv 216,152,180
v_accvgpr_write_b32 a231, v171
Cv 176,176,177
Mv 220,28,52
Cv 177,178,179
Cv 178,180,181
Mv 220,156,180
buffer_store_dwordx4 v[72:75], v3, s[20:23], s45 offen offset:1024 sc0 sc1
Mv 224,0,56
Cv 179,182,183
)" R"(
v_accvgpr_write_b32 a232, v176
Ma 192,0,60
v_accvgpr_write_b32 a233, v177
v_accvgpr_write_b32 a234, v178
Ma 196,4,60
v_accvgpr_write_b32 a235, v179
Cv 184,184,185
Mv 228,4,56
buffer_load_dwordx4 v2, s[20:23], s56 offen offset:2048 sc1 lds
Mv 232,8,56
Cv 185,186,187
Cv 186,188,189
Ma 200,8,60
Cv 187,190,191
v_accvgpr_write_b32 a236, v184
Ma 204,12,60
v_accvgpr_write_b32 a237, v185
v_accvgpr_write_b32 a238, v186
Mv 236,12,56
s_waitcnt vmcnt(17)
s_barrier
Mv 240,16,56
buffer_store_dwordx4 v[80:83], v3, s[20:23], s45 offen offset:2048 sc0 sc1
Ma 208,16,60
v_accvgpr_write_b32 a239, v187
Ma 212,20,60
Mv 244,20,56
buffer_load_dwordx4 v2, s[20:23], s56 offen offset:3072 sc1 lds
Mv 248,24,56
Ma 216,24,60
Ma 220,28,60
Mv 252,28,56
buffer_store_dwordx4 v[88:91], v3, s[20:23], s45 offen offset:3072 sc0 sc1
Mv 224,128,184
Rb 0,1,0
Mv 228,132,184
Rb 4,1,1024
Mv 232,136,184
s_add_u32 m0, s36, 102400
Rb 8,1,2048
Mv 236,140,184
buffer_load_dwordx4 v3, s[20:23], s56 offen offset:0 sc1 lds
Rb 12,1,3072
Mv 240,144,184
Rb 16,1,8192
Mv 244,148,184
Rb 20,1,9216
Mv 248,152,184
Rb 24,1,10240
Mv 252,156,184
Rb 28,1,11264
Ma 192,128,188
Rb 32,1,4096
Ma 196,132,188
Rb 36,1,5120
Ma 200,136,188
Rb 40,1,6144
Ma 204,140,188
buffer_load_dwordx4 v3, s[20:23], s56 offen offset:1024 sc1 lds
Rb 44,1,7168
Ma 208,144,188
Ma 212,148,188
buffer_load_dwordx4 v3, s[20:23], s56 offen offset:2048 sc1 lds
Ma 216,152,188
Ma 220,156,188
)" R"(
buffer_load_dwordx4 v3, s[20:23], s56 offen offset:3072 sc1 lds
s_add_u32 s56, s56, 0x10000
s_branch .LzFB
.LzPBVX:
Mv 32,32,0
Rt 128,18,16384
Mv 36,36,0
Mv 96,40,0
Rt 130,18,18432
Mv 100,44,0
s_add_u32 s39, s39, 0x40000
Mv 160,48,0
Rt 132,19,16384
s_add_u32 m0, s37, 49152
Mv 164,52,0
buffer_load_dwordx4 v12, s[8:11], s39 offen offset:0 lds
Mv 224,56,0
Rt 134,19,18432
Mv 228,60,0
Mv 236,60,4
Rt 136,20,16384
Mv 232,56,4
Mv 172,52,4
Rt 138,20,18432
Mv 168,48,4
Mv 108,44,4
Rt 140,21,16384
Mv 104,40,4
buffer_load_dwordx4 v13, s[8:11], s39 offen offset:1024 lds
Mv 44,36,4
Rt 142,21,18432
Mv 40,32,4
Mv 48,32,8
Rt 144,22,16384
Mv 52,36,8
Mv 112,40,8
Rt 146,22,18432
Mv 116,44,8
Mv 176,48,8
Rt 148,23,16384
Mv 180,52,8
buffer_load_dwordx4 v14, s[8:11], s39 offen offset:2048 lds
Mv 240,56,8
Rt 150,23,18432
Mv 244,60,8
Mv 252,60,12
Rt 152,24,16384
Mv 248,56,12
Mv 188,52,12
Rt 154,24,18432
Mv 184,48,12
Mv 124,44,12
Rt 156,25,16384
Mv 120,40,12
buffer_load_dwordx4 v15, s[8:11], s39 offen offset:3072 lds
Mv 60,36,12
Rt 158,25,18432
Mv 56,32,12
Mv 64,32,16
Rb 160,17,0
Mv 68,36,16
Mv 128,40,16
Rb 164,17,2048
Mv 132,44,16
)" R"(
s_add_u32 s39, s39, 0x40000
Mv 192,48,16
Rb 168,17,8192
s_add_u32 m0, s37, 0
Mv 196,52,16
buffer_load_dwordx4 v12, s[8:11], s39 offen offset:0 lds
Ma 192,56,16
Rb 172,17,10240
Ma 196,60,16
Ma 204,60,20
Rb 176,17,16384
Ma 200,56,20
Mv 204,52,20
Rb 180,17,18432
Mv 200,48,20
Mv 140,44,20
Rb 184,17,24576
Mv 136,40,20
buffer_load_dwordx4 v13, s[8:11], s39 offen offset:1024 lds
Mv 76,36,20
Rb 188,17,26624
Mv 72,32,20
Mv 80,32,24
Mv 84,36,24
Mv 144,40,24
Mv 148,44,24
Mv 208,48,24
Mv 212,52,24
buffer_load_dwordx4 v14, s[8:11], s39 offen offset:2048 lds
Ma 208,56,24
Ma 212,60,24
Ma 220,60,28
Ma 216,56,28
Mv 220,52,28
Mv 216,48,28
Mv 156,44,28
Mv 152,40,28
buffer_load_dwordx4 v15, s[8:11], s39 offen offset:3072 lds
Mv 92,36,28
Mv 88,32,28
s_waitcnt vmcnt(8) lgkmcnt(0)
s_barrier
Mv 32,160,128
Rt 0,18,32768
Mv 36,164,128
s_add_u32 s38, s38, 0xffff0080
Mv 96,168,128
Rt 2,18,34816
s_add_u32 m0, s36, 0
Mv 100,172,128
buffer_load_dwordx4 v8, s[4:7], s38 offen offset:0 lds
Mv 160,176,128
Rt 4,19,32768
Mv 164,180,128
Mv 224,184,128
Rt 6,19,34816
Mv 228,188,128
buffer_load_dwordx4 v9, s[4:7], s38 offen offset:1024 lds
Mv 236,188,132
Rt 8,20,32768
Mv 232,184,132
Mv 172,180,132
Rt 10,20,34816
Mv 168,176,132
buffer_load_dwordx4 v10, s[4:7], s38 offen offset:2048 lds
)" R"(
Mv 108,172,132
Rt 12,21,32768
Mv 104,168,132
Mv 44,164,132
Rt 14,21,34816
Mv 40,160,132
buffer_load_dwordx4 v11, s[4:7], s38 offen offset:3072 lds
Mv 48,160,136
Rt 16,22,32768
Mv 52,164,136
s_add_u32 s38, s38, 0x10000
Mv 112,168,136
Rt 18,22,34816
s_add_u32 m0, s36, 4096
Mv 116,172,136
buffer_load_dwordx4 v8, s[4:7], s38 offen offset:0 lds
Mv 176,176,136
Rt 20,23,32768
Mv 180,180,136
Mv 240,184,136
Rt 22,23,34816
Mv 244,188,136
buffer_load_dwordx4 v9, s[4:7], s38 offen offset:1024 lds
Mv 252,188,140
Rt 24,24,32768
Mv 248,184,140
Mv 188,180,140
Rt 26,24,34816
Mv 184,176,140
buffer_load_dwordx4 v10, s[4:7], s38 offen offset:2048 lds
Mv 124,172,140
Rt 28,25,32768
Mv 120,168,140
Mv 60,164,140
Rt 30,25,34816
Mv 56,160,140
buffer_load_dwordx4 v11, s[4:7], s38 offen offset:3072 lds
Mv 64,160,144
Rb 32,16,32768
Mv 68,164,144
Mv 128,168,144
Rb 36,16,34816
Mv 132,172,144
Mv 192,176,144
Rb 40,16,40960
Mv 196,180,144
Ma 192,184,144
Rb 44,16,43008
Ma 196,188,144
Ma 204,188,148
Rb 48,16,49152
Ma 200,184,148
Mv 204,180,148
Rb 52,16,51200
Mv 200,176,148
Mv 140,172,148
Rb 56,16,57344
Mv 136,168,148
Mv 76,164,148
Rb 60,16,59392
Mv 72,160,148
Mv 80,160,152
Mv 84,164,152
Mv 144,168,152
)" R"(
Mv 148,172,152
Mv 208,176,152
Mv 212,180,152
Ma 208,184,152
Ma 212,188,152
Ma 220,188,156
Ma 216,184,156
Mv 220,180,156
Mv 216,176,156
Mv 156,172,156
Mv 152,168,156
Mv 92,164,156
Mv 88,160,156
s_waitcnt vmcnt(12) lgkmcnt(0)
s_barrier
Mv 32,32,0
Rt 128,18,49152
Mv 36,36,0
Mv 96,40,0
Rt 130,18,51200
v_lshrrev_b32 v5, 2, v0
Mv 100,44,0
s_add_u32 s39, s39, 0x40000
v_and_b32 v5, 8, v5
Mv 160,48,0
Rt 132,19,49152
s_add_u32 m0, s37, 16384
Mv 164,52,0
buffer_load_dwordx4 v12, s[8:11], s39 offen offset:0 lds
Mv 224,56,0
Rt 134,19,51200
v_and_or_b32 v5, v0, 7, v5
Mv 228,60,0
v_lshlrev_b32 v5, 8, v5
v_and_or_b32 v5, v0, 24, v5
Mv 236,60,4
Rt 136,20,49152
v_lshrrev_b32 v5, 1, v5
Mv 232,56,4
Mv 172,52,4
Rt 138,20,51200
Mv 168,48,4
Mv 108,44,4
Rt 140,21,49152
Mv 104,40,4
buffer_load_dwordx4 v13, s[8:11], s39 offen offset:1024 lds
Mv 44,36,4
Rt 142,21,51200
Mv 40,32,4
Mv 48,32,8
Rt 144,22,49152
Mv 52,36,8
Mv 112,40,8
Rt 146,22,51200
Mv 116,44,8
Mv 176,48,8
Rt 148,23,49152
Mv 180,52,8
buffer_load_dwordx4 v14, s[8:11], s39 offen offset:2048 lds
Mv 240,56,8
Rt 150,23,51200
Mv 244,60,8
Mv 252,60,12
Rt 152,24,49152
Mv 248,56,12
)" R"(
Mv 188,52,12
Rt 154,24,51200
Mv 184,48,12
Mv 124,44,12
Rt 156,25,49152
Mv 120,40,12
buffer_load_dwordx4 v15, s[8:11], s39 offen offset:3072 lds
Mv 60,36,12
Rt 158,25,51200
Mv 56,32,12
Mv 64,32,16
Rb 160,17,32768
Mv 68,36,16
Mv 128,40,16
Rb 164,17,34816
Mv 132,44,16
s_add_u32 s39, s39, 0x40000
Mv 192,48,16
Rb 168,17,40960
s_add_u32 m0, s37, 32768
Mv 196,52,16
buffer_load_dwordx4 v12, s[8:11], s39 offen offset:0 lds
Ma 192,56,16
Rb 172,17,43008
Ma 196,60,16
Ma 204,60,20
Rb 176,17,49152
Ma 200,56,20
Mv 204,52,20
Rb 180,17,51200
Mv 200,48,20
Mv 140,44,20
Rb 184,17,57344
Mv 136,40,20
buffer_load_dwordx4 v13, s[8:11], s39 offen offset:1024 lds
Mv 76,36,20
Rb 188,17,59392
Mv 72,32,20
Mv 80,32,24
Rt 224,29,0
Mv 84,36,24
Rt 226,29,8192
Mv 144,40,24
Rt 228,29,128
Mv 148,44,24
Rt 230,29,8320
Mv 208,48,24
Rt 232,29,256
Mv 212,52,24
buffer_load_dwordx4 v14, s[8:11], s39 offen offset:2048 lds
Rt 234,29,8448
Ma 208,56,24
Rt 236,29,384
Ma 212,60,24
Rt 238,29,8576
Ma 220,60,28
Ma 216,56,28
Mv 220,52,28
Mv 216,48,28
Mv 156,44,28
Mv 152,40,28
buffer_load_dwordx4 v15, s[8:11], s39 offen offset:3072 lds
Mv 92,36,28
Mv 88,32,28
)" R"(
s_waitcnt vmcnt(8) lgkmcnt(0)
s_barrier
Mv 32,160,128
Rt 0,18,0
Mv 36,164,128
s_add_u32 s38, s38, 0xffff0080
Mv 96,168,128
Rt 2,18,2048
s_add_u32 m0, s36, 32768
Mv 100,172,128
buffer_load_dwordx4 v8, s[4:7], s38 offen offset:0 lds
Mv 160,176,128
Rt 4,19,0
Mv 164,180,128
Mv 224,184,128
Rt 6,19,2048
Mv 228,188,128
buffer_load_dwordx4 v9, s[4:7], s38 offen offset:1024 lds
Mv 236,188,132
Rt 8,20,0
Mv 232,184,132
Mv 172,180,132
Rt 10,20,2048
Mv 168,176,132
buffer_load_dwordx4 v10, s[4:7], s38 offen offset:2048 lds
Mv 108,172,132
Rt 12,21,0
Mv 104,168,132
Mv 44,164,132
Rt 14,21,2048
Mv 40,160,132
buffer_load_dwordx4 v11, s[4:7], s38 offen offset:3072 lds
Mv 48,160,136
Rt 16,22,0
Mv 52,164,136
s_add_u32 s38, s38, 0x10000
Mv 112,168,136
Rt 18,22,2048
s_add_u32 m0, s36, 36864
Mv 116,172,136
buffer_load_dwordx4 v8, s[4:7], s38 offen offset:0 lds
Mv 176,176,136
Rt 20,23,0
Mv 180,180,136
Mv 240,184,136
Rt 22,23,2048
Mv 244,188,136
buffer_load_dwordx4 v9, s[4:7], s38 offen offset:1024 lds
Mv 252,188,140
Rt 24,24,0
Mv 248,184,140
Mv 188,180,140
Rt 26,24,2048
Mv 184,176,140
buffer_load_dwordx4 v10, s[4:7], s38 offen offset:2048 lds
Mv 124,172,140
Rt 28,25,0
s_lshl3_add_u32 s28, s94, s92
Mv 120,168,140
s_lshl3_add_u32 s29, s92, s94
s_cmp_lt_u32 s94, 8
s_cmov_b32 s28, s29
Mv 60,164,140
Rt 30,25,2048
s_mul_i32 s29, s28, 293
)" R"(
s_lshr_b32 s29, s29, 11
Mv 56,160,140
buffer_load_dwordx4 v11, s[4:7], s38 offen offset:3072 lds
Mv 64,160,144
Rb 32,16,0
s_mul_i32 s29, s29, 9
s_add_u32 s29, s29, s28
Mv 68,164,144
s_lshl_b32 s29, s29, 10
s_add_u32 s29, s29, s82
s_add_u32 s29, s29, s84
Mv 128,168,144
Rb 36,16,2048
s_add_u32 s94, s94, 4
Mv 132,172,144
buffer_store_dwordx4 a[224:227], v1, s[20:23], s29 offen sc0 sc1
Mv 192,176,144
Rb 40,16,8192
s_lshl3_add_u32 s28, s94, s92
Mv 196,180,144
s_lshl3_add_u32 s29, s92, s94
Ma 192,184,144
Rb 44,16,10240
s_cmp_lt_u32 s94, 8
s_cmov_b32 s28, s29
Ma 196,188,144
s_mul_i32 s29, s28, 293
s_lshr_b32 s29, s29, 11
Ma 204,188,148
Rb 48,16,16384
s_mul_i32 s29, s29, 9
s_add_u32 s29, s29, s28
Ma 200,184,148
s_lshl_b32 s29, s29, 10
s_add_u32 s29, s29, s82
Mv 204,180,148
Rb 52,16,18432
s_add_u32 s29, s29, s84
Mv 200,176,148
s_add_u32 s94, s94, 4
buffer_store_dwordx4 a[228:231], v1, s[20:23], s29 offen sc0 sc1
Mv 140,172,148
Rb 56,16,24576
s_lshl3_add_u32 s28, s94, s92
Mv 136,168,148
s_lshl3_add_u32 s29, s92, s94
Mv 76,164,148
Rb 60,16,26624
s_cmp_lt_u32 s94, 8
s_cmov_b32 s28, s29
Mv 72,160,148
s_mul_i32 s29, s28, 293
s_lshr_b32 s29, s29, 11
Mv 80,160,152
s_mul_i32 s29, s29, 9
s_add_u32 s29, s29, s28
Mv 84,164,152
s_lshl_b32 s29, s29, 10
s_add_u32 s29, s29, s82
Mv 144,168,152
s_add_u32 s29, s29, s84
s_add_u32 s94, s94, 4
Mv 148,172,152
buffer_store_dwordx4 a[232:235], v1, s[20:23], s29 offen sc0 sc1
)" R"(
Mv 208,176,152
s_lshl3_add_u32 s28, s94, s92
s_lshl3_add_u32 s29, s92, s94
Mv 212,180,152
s_cmp_lt_u32 s94, 8
s_cmov_b32 s28, s29
Ma 208,184,152
s_mul_i32 s29, s28, 293
s_lshr_b32 s29, s29, 11
Ma 212,188,152
s_mul_i32 s29, s29, 9
s_add_u32 s29, s29, s28
Ma 220,188,156
s_lshl_b32 s29, s29, 10
s_add_u32 s29, s29, s82
Ma 216,184,156
s_add_u32 s29, s29, s84
Mv 220,180,156
s_cmp_gt_u32 s33, 1
s_cmov_b32 s29, 0x40000000
Mv 216,176,156
buffer_store_dwordx4 a[236:239], v1, s[20:23], s29 offen sc0 sc1
Mv 156,172,156
Mv 152,168,156
Mv 92,164,156
Mv 88,160,156
s_waitcnt vmcnt(16) lgkmcnt(0)
s_barrier
Mv 32,32,0
Rt 128,18,16384
Mv 36,36,0
Mv 96,40,0
Rt 130,18,18432
Mv 100,44,0
s_add_u32 s39, s39, 0x40000
Mv 160,48,0
Rt 132,19,16384
s_add_u32 m0, s37, 49152
Mv 164,52,0
buffer_load_dwordx4 v12, s[8:11], s39 offen offset:0 lds
Mv 224,56,0
Rt 134,19,18432
Mv 228,60,0
Mv 236,60,4
Rt 136,20,16384
Mv 232,56,4
Mv 172,52,4
Rt 138,20,18432
Mv 168,48,4
Mv 108,44,4
Rt 140,21,16384
Mv 104,40,4
buffer_load_dwordx4 v13, s[8:11], s39 offen offset:1024 lds
Mv 44,36,4
Rt 142,21,18432
Mv 40,32,4
Mv 48,32,8
Rt 144,22,16384
Mv 52,36,8
Mv 112,40,8
Rt 146,22,18432
Mv 116,44,8
Mv 176,48,8
Rt 148,23,16384
Mv 180,52,8
)" R"(
buffer_load_dwordx4 v14, s[8:11], s39 offen offset:2048 lds
Mv 240,56,8
Rt 150,23,18432
Mv 244,60,8
Mv 252,60,12
Rt 152,24,16384
Mv 248,56,12
Mv 188,52,12
Rt 154,24,18432
Mv 184,48,12
Mv 124,44,12
Rt 156,25,16384
Mv 120,40,12
buffer_load_dwordx4 v15, s[8:11], s39 offen offset:3072 lds
Mv 60,36,12
Rt 158,25,18432
Mv 56,32,12
Mv 64,32,16
Rb 160,17,0
Mv 68,36,16
buffer_load_dword v4, v5, s[20:23], s50 offen offset:0 sc0 sc1
Mv 128,40,16
Rb 164,17,2048
Mv 132,44,16
Mv 192,48,16
Rb 168,17,8192
Mv 196,52,16
Ma 192,56,16
Rb 172,17,10240
Ma 196,60,16
Ma 204,60,20
Rb 176,17,16384
Ma 200,56,20
Mv 204,52,20
Rb 180,17,18432
Mv 200,48,20
Mv 140,44,20
Rb 184,17,24576
Mv 136,40,20
Mv 76,36,20
Rb 188,17,26624
Mv 72,32,20
Mv 80,32,24
Mv 84,36,24
Mv 144,40,24
Mv 148,44,24
Mv 208,48,24
Mv 212,52,24
Ma 208,56,24
Ma 212,60,24
Ma 220,60,28
Ma 216,56,28
Mv 220,52,28
Mv 216,48,28
Mv 156,44,28
Mv 152,40,28
Mv 92,36,28
Mv 88,32,28
s_waitcnt vmcnt(0) lgkmcnt(0)
s_barrier
Mv 32,160,128
Rt 0,18,32768
v_cmp_eq_u32_e32 vcc, s3, v4
s_andn2_b64 s[28:29], s[60:61], vcc
s_cbranch_scc1 .Lzsp12
)" R"(
.Lzok12:
Mv 36,164,128
Mv 96,168,128
Rt 2,18,34816
Mv 100,172,128
Mv 160,176,128
Rt 4,19,32768
s_add_u32 m0, s36, 0
Mv 164,180,128
buffer_load_dwordx4 v2, s[20:23], s55 offen offset:0 sc1 lds
Mv 224,184,128
Rt 6,19,34816
Mv 228,188,128
Mv 236,188,132
Rt 8,20,32768
Mv 232,184,132
Mv 172,180,132
Rt 10,20,34816
Mv 168,176,132
buffer_load_dwordx4 v2, s[20:23], s55 offen offset:1024 sc1 lds
Mv 108,172,132
Rt 12,21,32768
Mv 104,168,132
Mv 44,164,132
Rt 14,21,34816
Mv 40,160,132
Mv 48,160,136
Rt 16,22,32768
Mv 52,164,136
buffer_load_dwordx4 v2, s[20:23], s55 offen offset:2048 sc1 lds
Mv 112,168,136
Rt 18,22,34816
Mv 116,172,136
Mv 176,176,136
Rt 20,23,32768
Mv 180,180,136
Mv 240,184,136
Rt 22,23,34816
Mv 244,188,136
buffer_load_dwordx4 v2, s[20:23], s55 offen offset:3072 sc1 lds
Mv 252,188,140
Rt 24,24,32768
Mv 248,184,140
Mv 188,180,140
Rt 26,24,34816
Mv 184,176,140
Mv 124,172,140
Rt 28,25,32768
s_add_u32 m0, s36, 4096
Mv 120,168,140
buffer_load_dwordx4 v3, s[20:23], s55 offen offset:0 sc1 lds
Mv 60,164,140
Rt 30,25,34816
Mv 56,160,140
Mv 64,160,144
Rb 32,16,32768
Mv 68,164,144
Mv 128,168,144
Rb 36,16,34816
Mv 132,172,144
buffer_load_dwordx4 v3, s[20:23], s55 offen offset:1024 sc1 lds
Mv 192,176,144
Rb 40,16,40960
Mv 196,180,144
)" R"(
Ma 192,184,144
Rb 44,16,43008
Ma 196,188,144
Ma 204,188,148
Rb 48,16,49152
Ma 200,184,148
buffer_load_dwordx4 v3, s[20:23], s55 offen offset:2048 sc1 lds
Mv 204,180,148
Rb 52,16,51200
Mv 200,176,148
Mv 140,172,148
Rb 56,16,57344
Mv 136,168,148
Mv 76,164,148
Rb 60,16,59392
Mv 72,160,148
buffer_load_dwordx4 v3, s[20:23], s55 offen offset:3072 sc1 lds
Mv 80,160,152
Mv 84,164,152
Mv 144,168,152
Mv 148,172,152
Mv 208,176,152
Mv 212,180,152
Ma 208,184,152
Ma 212,188,152
Ma 220,188,156
s_add_u32 s55, s55, 0x10000
Ma 216,184,156
Mv 220,180,156
Mv 216,176,156
Mv 156,172,156
Mv 152,168,156
Mv 92,164,156
Mv 88,160,156
s_waitcnt lgkmcnt(4)
s_barrier
Mv 32,32,0
Rt 128,18,49152
Mv 36,36,0
Rt 130,18,51200
Mv 96,40,0
Rt 132,19,49152
Mv 100,44,0
Rt 134,19,51200
Mv 108,44,4
Rt 136,20,49152
Mv 104,40,4
Rt 138,20,51200
Mv 44,36,4
Rt 140,21,49152
Mv 40,32,4
Rt 142,21,51200
Mv 48,32,8
Rt 144,22,49152
Mv 52,36,8
Rt 146,22,51200
Mv 112,40,8
Rt 148,23,49152
Mv 116,44,8
Rt 150,23,51200
Mv 124,44,12
Rt 152,24,49152
Mv 120,40,12
Rt 154,24,51200
Mv 60,36,12
)" R"(
Rt 156,25,49152
Mv 56,32,12
Rt 158,25,51200
Mv 64,32,16
Rb 160,17,32768
Mv 68,36,16
Rb 164,17,34816
Mv 128,40,16
Rb 168,17,40960
Mv 132,44,16
Rb 172,17,43008
Mv 140,44,20
Rb 176,17,49152
Mv 136,40,20
Rb 180,17,51200
Mv 76,36,20
Rb 184,17,57344
Mv 72,32,20
Rb 188,17,59392
Mv 80,32,24
v_add_u32 v8, 0x10000, v1
v_add_u32 v10, 0x20000, v1
Mv 84,36,24
s_mov_b32 s93, 2
s_mov_b32 s94, 0
Mv 144,40,24
Mv 148,44,24
Mv 156,44,28
Mv 152,40,28
Mv 92,36,28
Mv 88,32,28
s_waitcnt lgkmcnt(0)
s_barrier
Mv 32,160,128
Mv 36,164,128
Mv 40,160,132
s_add_u32 m0, s36, 32768
Mv 44,164,132
buffer_load_dwordx4 v2, s[20:23], s55 offen offset:0 sc1 lds
Mv 48,160,136
Mv 52,164,136
s_nop 0
Cv 32,32,33
Cv 33,34,35
Mv 56,160,140
Cv 34,36,37
Cv 35,38,39
Mv 60,164,140
buffer_load_dwordx4 v2, s[20:23], s55 offen offset:1024 sc1 lds
Mv 64,160,144
Cv 40,40,41
Cv 41,42,43
Mv 68,164,144
Cv 42,44,45
Cv 43,46,47
Mv 72,160,148
Cv 48,48,49
Cv 49,50,51
Mv 76,164,148
buffer_load_dwordx4 v2, s[20:23], s55 offen offset:2048 sc1 lds
Mv 80,160,152
Cv 50,52,53
Cv 51,54,55
Mv 84,164,152
Cv 56,56,57
)" R"(
Cv 57,58,59
Mv 88,160,156
Cv 58,60,61
Cv 59,62,63
Mv 92,164,156
buffer_load_dwordx4 v2, s[20:23], s55 offen offset:3072 sc1 lds
Mv 96,168,128
Cv 64,64,65
Cv 65,66,67
Mv 100,172,128
Cv 66,68,69
Cv 67,70,71
Mv 104,168,132
s_add_u32 m0, s36, 36864
Cv 72,72,73
Mv 108,172,132
buffer_load_dwordx4 v3, s[20:23], s55 offen offset:0 sc1 lds
Mv 112,168,136
Cv 73,74,75
Cv 74,76,77
Mv 116,172,136
Cv 75,78,79
Cv 80,80,81
Mv 120,168,140
Cv 81,82,83
Cv 82,84,85
Mv 124,172,140
buffer_store_dwordx4 v[32:35], v2, s[20:23], s45 offen offset:0 sc0 sc1
Mv 128,168,144
Cv 83,86,87
Cv 88,88,89
Mv 132,172,144
Cv 89,90,91
Cv 90,92,93
Mv 136,168,148
Cv 91,94,95
Mv 140,172,148
buffer_load_dwordx4 v3, s[20:23], s55 offen offset:1024 sc1 lds
Mv 144,168,152
Mv 148,172,152
Cv 128,128,129
Cv 129,130,131
Mv 152,168,156
Cv 130,132,133
Cv 131,134,135
Mv 156,172,156
buffer_store_dwordx4 v[40:43], v2, s[20:23], s45 offen offset:1024 sc0 sc1
Mv 160,48,0
Cv 136,136,137
Cv 137,138,139
Mv 160,176,128
Cv 138,140,141
Cv 139,142,143
Mv 164,52,0
Cv 144,144,145
Cv 145,146,147
Mv 164,180,128
buffer_load_dwordx4 v3, s[20:23], s55 offen offset:2048 sc1 lds
Mv 168,48,4
Cv 146,148,149
Cv 147,150,151
Mv 168,176,132
Cv 152,152,153
Cv 153,154,155
)" R"(
Mv 172,52,4
Cv 154,156,157
Cv 155,158,159
Mv 172,180,132
buffer_store_dwordx4 v[48:51], v2, s[20:23], s45 offen offset:2048 sc0 sc1
Mv 176,48,8
Cv 132,96,97
Cv 133,98,99
Mv 176,176,136
Cv 134,100,101
Cv 135,102,103
Mv 180,52,8
Cv 140,104,105
Cv 141,106,107
Mv 180,180,136
buffer_load_dwordx4 v3, s[20:23], s55 offen offset:3072 sc1 lds
Mv 184,48,12
Cv 142,108,109
Cv 143,110,111
Mv 184,176,140
Cv 148,112,113
Cv 149,114,115
Mv 188,52,12
Cv 150,116,117
Cv 151,118,119
Mv 188,180,140
buffer_store_dwordx4 v[56:59], v2, s[20:23], s45 offen offset:3072 sc0 sc1
Mv 192,48,16
Cv 156,120,121
Cv 157,122,123
Mv 192,176,144
Cv 158,124,125
Cv 159,126,127
Mv 196,52,16
s_add_u32 m0, s36, 98304
Cv 160,160,161
Mv 196,180,144
buffer_load_dwordx4 v2, s[20:23], s56 offen offset:0 sc1 lds
Mv 200,48,20
Cv 161,162,163
Cv 162,164,165
Mv 200,176,148
Cv 163,166,167
v_accvgpr_write_b32 a224, v160
Mv 204,52,20
v_accvgpr_write_b32 a225, v161
v_accvgpr_write_b32 a226, v162
Mv 204,180,148
buffer_store_dwordx4 v[64:67], v3, s[20:23], s45 offen offset:0 sc0 sc1
Mv 208,48,24
v_accvgpr_write_b32 a227, v163
Cv 168,168,169
Mv 208,176,152
Cv 169,170,171
Cv 170,172,173
Mv 212,52,24
Cv 171,174,175
v_accvgpr_write_b32 a228, v168
Mv 212,180,152
buffer_load_dwordx4 v2, s[20:23], s56 offen offset:1024 sc1 lds
Mv 216,48,28
v_accvgpr_write_b32 a229, v169
v_accvgpr_write_b32 a230, v170
Mv 216,176,156
)" R"(
v_accvgpr_write_b32 a231, v171
Cv 176,176,177
Mv 220,52,28
Cv 177,178,179
Cv 178,180,181
Mv 220,180,156
buffer_store_dwordx4 v[72:75], v3, s[20:23], s45 offen offset:1024 sc0 sc1
Mv 224,56,0
Cv 179,182,183
v_accvgpr_write_b32 a232, v176
Mv 228,60,0
v_accvgpr_write_b32 a233, v177
v_accvgpr_write_b32 a234, v178
Mv 236,60,4
v_accvgpr_write_b32 a235, v179
Cv 184,184,185
Mv 232,56,4
buffer_load_dwordx4 v2, s[20:23], s56 offen offset:2048 sc1 lds
Mv 240,56,8
Cv 185,186,187
Cv 186,188,189
Mv 244,60,8
Cv 187,190,191
v_accvgpr_write_b32 a236, v184
Mv 252,60,12
v_accvgpr_write_b32 a237, v185
v_accvgpr_write_b32 a238, v186
Mv 248,56,12
s_waitcnt vmcnt(17)
s_barrier
Ma 192,56,16
buffer_store_dwordx4 v[80:83], v3, s[20:23], s45 offen offset:2048 sc0 sc1
Ma 196,60,16
v_accvgpr_write_b32 a239, v187
Ma 204,60,20
Ma 200,56,20
buffer_load_dwordx4 v2, s[20:23], s56 offen offset:3072 sc1 lds
Ma 208,56,24
Ma 212,60,24
Ma 220,60,28
Ma 216,56,28
buffer_store_dwordx4 v[88:91], v3, s[20:23], s45 offen offset:3072 sc0 sc1
Mv 224,184,128
Rb 0,1,0
Mv 228,188,128
Rb 4,1,1024
Mv 232,184,132
s_add_u32 m0, s36, 102400
Rb 8,1,2048
Mv 236,188,132
buffer_load_dwordx4 v3, s[20:23], s56 offen offset:0 sc1 lds
Rb 12,1,3072
Mv 240,184,136
Rb 16,1,8192
Mv 244,188,136
Rb 20,1,9216
Mv 248,184,140
Rb 24,1,10240
Mv 252,188,140
Rb 28,1,11264
Ma 192,184,144
Rb 32,1,4096
Ma 196,188,144
Rb 36,1,5120
Ma 200,184,148
)" R"(
Rb 40,1,6144
Ma 204,188,148
buffer_load_dwordx4 v3, s[20:23], s56 offen offset:1024 sc1 lds
Rb 44,1,7168
Ma 208,184,152
Ma 212,188,152
buffer_load_dwordx4 v3, s[20:23], s56 offen offset:2048 sc1 lds
Ma 216,184,156
Ma 220,188,156
buffer_load_dwordx4 v3, s[20:23], s56 offen offset:3072 sc1 lds
s_add_u32 s56, s56, 0x10000
.LzFB:
s_waitcnt lgkmcnt(8)
Mz 64,0,64
Rb 48,1,12288
buffer_load_dword v4, v5, s[20:23], s50 offen offset:0 sc0 sc1
Mz 68,0,68
s_lshl_b64 s[60:61], s[60:61], 8
s_or_b32 s60, s60, 0xff
Mv 68,4,76
Rb 52,1,13312
s_add_u32 m0, s36, 65536
Mv 64,4,72
buffer_load_dwordx4 v2, s[20:23], s56 offen offset:0 sc1 lds
Mv 64,8,80
Rb 56,1,14336
s_add_u32 s55, s55, 0x10000
Cv 192,192,193
Mv 68,8,84
buffer_store_dwordx4 v[132:135], v2, s[20:23], s46 offen offset:0 sc0 sc1
s_cmp_eq_u32 s57, 2
s_cselect_b32 s55, s59, s55
Mv 68,12,92
Rb 60,1,15360
s_sub_u32 s57, s57, 1
s_waitcnt lgkmcnt(8)
Mv 64,12,88
Cv 193,194,195
Cv 194,196,197
Cv 195,198,199
v_accvgpr_write_b32 a240, v192
Mv 64,16,96
Rb 0,1,16384
v_accvgpr_write_b32 a241, v193
v_accvgpr_write_b32 a242, v194
Mv 68,16,100
v_accvgpr_write_b32 a243, v195
Cv 200,200,201
Cv 201,202,203
Cv 202,204,205
Mv 68,20,108
Rb 4,1,17408
Cv 203,206,207
v_accvgpr_write_b32 a244, v200
Mv 64,20,104
buffer_load_dwordx4 v2, s[20:23], s56 offen offset:1024 sc1 lds
v_accvgpr_write_b32 a245, v201
v_accvgpr_write_b32 a246, v202
Mv 64,24,112
Rb 8,1,18432
v_accvgpr_write_b32 a247, v203
Cv 208,208,209
Cv 209,210,211
Mv 68,24,116
)" R"(
Cv 210,212,213
Cv 211,214,215
v_accvgpr_write_b32 a248, v208
Mv 68,28,124
Rb 12,1,19456
v_accvgpr_write_b32 a249, v209
v_accvgpr_write_b32 a250, v210
s_waitcnt lgkmcnt(8)
Mv 64,28,120
v_accvgpr_write_b32 a251, v211
Cv 216,216,217
Cv 217,218,219
Mz 72,32,64
Rb 16,1,24576
Cv 218,220,221
Cv 219,222,223
v_accvgpr_write_b32 a252, v216
Mz 76,32,68
v_accvgpr_write_b32 a253, v217
v_accvgpr_write_b32 a254, v218
v_accvgpr_write_b32 a255, v219
Mv 76,36,76
Rb 20,1,25600
Cv 224,224,225
Cv 225,226,227
Cv 226,228,229
Mv 72,36,72
buffer_load_dwordx4 v2, s[20:23], s56 offen offset:2048 sc1 lds
Cv 227,230,231
v_accvgpr_write_b32 a128, v224
Mv 72,40,80
Rb 24,1,26624
v_accvgpr_write_b32 a129, v225
v_accvgpr_write_b32 a130, v226
Mv 76,40,84
buffer_store_dwordx4 v[140:143], v2, s[20:23], s46 offen offset:1024 sc0 sc1
v_accvgpr_write_b32 a131, v227
Cv 232,232,233
Mv 76,44,92
Rb 28,1,27648
Cv 233,234,235
s_waitcnt lgkmcnt(8)
Mv 72,44,88
Cv 234,236,237
Cv 235,238,239
v_accvgpr_write_b32 a132, v232
v_accvgpr_write_b32 a133, v233
Mv 72,48,96
Rb 32,1,20480
v_accvgpr_write_b32 a134, v234
v_accvgpr_write_b32 a135, v235
Mv 76,48,100
Cv 240,240,241
Cv 241,242,243
Cv 242,244,245
Cv 243,246,247
Mv 76,52,108
Rb 36,1,21504
v_accvgpr_write_b32 a136, v240
v_accvgpr_write_b32 a137, v241
Mv 72,52,104
buffer_load_dwordx4 v2, s[20:23], s56 offen offset:3072 sc1 lds
v_accvgpr_write_b32 a138, v242
v_accvgpr_write_b32 a139, v243
)" R"(
Mv 72,56,112
Rb 40,1,22528
Cv 248,248,249
Cv 249,250,251
Cv 250,252,253
Mv 76,56,116
Cv 251,254,255
v_accvgpr_write_b32 a140, v248
v_accvgpr_write_b32 a141, v249
Mv 76,60,124
Rb 44,1,23552
v_accvgpr_write_b32 a142, v250
v_accvgpr_write_b32 a143, v251
s_waitcnt lgkmcnt(8)
Mv 72,60,120
v_accvgpr_read_b32 v160, a192
v_accvgpr_read_b32 v28, a193
Cv 160,160,28
Mz 80,0,64
Rb 48,1,28672
v_accvgpr_read_b32 v161, a194
v_accvgpr_read_b32 v29, a195
Cv 161,161,29
Mz 84,0,68
v_accvgpr_read_b32 v162, a196
v_accvgpr_read_b32 v28, a197
Cv 162,162,28
Mv 84,4,76
Rb 52,1,29696
s_add_u32 m0, s36, 69632
v_accvgpr_read_b32 v163, a198
v_accvgpr_read_b32 v29, a199
Mv 80,4,72
buffer_load_dwordx4 v3, s[20:23], s56 offen offset:0 sc1 lds
Cv 163,163,29
v_accvgpr_write_b32 a144, v160
Mv 80,8,80
Rb 56,1,30720
v_accvgpr_write_b32 a145, v161
v_accvgpr_write_b32 a146, v162
Mv 84,8,84
buffer_store_dwordx4 v[148:151], v2, s[20:23], s46 offen offset:2048 sc0 sc1
v_accvgpr_write_b32 a147, v163
v_accvgpr_read_b32 v168, a200
Mv 84,12,92
Rb 60,1,31744
v_accvgpr_read_b32 v28, a201
s_waitcnt lgkmcnt(8)
Mv 80,12,88
Cv 168,168,28
v_accvgpr_read_b32 v169, a202
v_accvgpr_read_b32 v29, a203
Cv 169,169,29
Mv 80,16,96
v_accvgpr_read_b32 v170, a204
v_accvgpr_read_b32 v28, a205
Cv 170,170,28
Mv 84,16,100
v_accvgpr_read_b32 v171, a206
v_accvgpr_read_b32 v29, a207
Cv 171,171,29
v_accvgpr_write_b32 a148, v168
Mv 84,20,108
v_accvgpr_write_b32 a149, v169
)" R"(
v_accvgpr_write_b32 a150, v170
v_accvgpr_write_b32 a151, v171
Mv 80,20,104
buffer_load_dwordx4 v3, s[20:23], s56 offen offset:1024 sc1 lds
v_accvgpr_read_b32 v176, a208
v_accvgpr_read_b32 v28, a209
Mv 80,24,112
Cv 176,176,28
v_accvgpr_read_b32 v177, a210
v_accvgpr_read_b32 v29, a211
Cv 177,177,29
Mv 84,24,116
v_accvgpr_read_b32 v178, a212
v_accvgpr_read_b32 v28, a213
Cv 178,178,28
Mv 84,28,124
v_accvgpr_read_b32 v179, a214
v_accvgpr_read_b32 v29, a215
Cv 179,179,29
v_accvgpr_write_b32 a152, v176
Mv 80,28,120
s_waitcnt vmcnt(9) lgkmcnt(0)
s_barrier
buffer_store_dword v6, v7, s[20:23], s49 offen offset:0 sc0 sc1
Mz 88,32,64
Rb 0,1,32768
v_accvgpr_write_b32 a153, v177
v_accvgpr_write_b32 a154, v178
v_accvgpr_write_b32 a155, v179
Mz 92,32,68
Rb 4,1,33792
v_accvgpr_read_b32 v184, a216
v_accvgpr_read_b32 v28, a217
Mv 92,36,76
Rb 8,1,34816
Cv 184,184,28
v_accvgpr_read_b32 v185, a218
v_accvgpr_read_b32 v29, a219
Mv 88,36,72
Rb 12,1,35840
buffer_load_dwordx4 v3, s[20:23], s56 offen offset:2048 sc1 lds
Cv 185,185,29
Mv 88,40,80
Rb 16,1,40960
v_accvgpr_read_b32 v186, a220
v_accvgpr_read_b32 v28, a221
Mv 92,40,84
Rb 20,1,41984
buffer_store_dwordx4 v[156:159], v2, s[20:23], s46 offen offset:3072 sc0 sc1
Cv 186,186,28
Mv 92,44,92
Rb 24,1,43008
v_accvgpr_read_b32 v187, a222
v_accvgpr_read_b32 v29, a223
Mv 88,44,88
Rb 28,1,44032
Cv 187,187,29
v_accvgpr_write_b32 a156, v184
v_accvgpr_write_b32 a157, v185
Mv 88,48,96
Rb 32,1,36864
v_accvgpr_write_b32 a158, v186
v_accvgpr_write_b32 a159, v187
Mv 92,48,100
Rb 36,1,37888
)" R"(
v_mov_b32 v16, 0
v_mov_b32 v17, 0
v_mov_b32 v18, 0
Mv 92,52,108
Rb 40,1,38912
v_mov_b32 v19, 0
v_mov_b32 v20, 0
Mv 88,52,104
Rb 44,1,39936
buffer_load_dwordx4 v3, s[20:23], s56 offen offset:3072 sc1 lds
v_mov_b32 v21, 0
Mv 88,56,112
v_mov_b32 v22, 0
v_cmp_eq_u32_e32 vcc, s3, v4
s_andn2_b64 s[28:29], s[60:61], vcc
s_cbranch_scc1 .Lzsp13
.Lzok13:
Mv 92,56,116
s_add_u32 s56, s56, 0x10000
v_mov_b32 v23, 0
Mv 92,60,124
s_waitcnt lgkmcnt(8)
Mv 88,60,120
v_exp_f32_e32 v32, v64
Mz 96,0,64
Rb 48,1,45056
buffer_load_dword v4, v5, s[20:23], s50 offen offset:0 sc0 sc1
Mz 100,0,68
s_lshl_b64 s[60:61], s[60:61], 8
s_or_b32 s60, s60, 0xff
Mv 100,4,76
Rb 52,1,46080
s_add_u32 m0, s36, 0
Mv 96,4,72
buffer_load_dwordx4 v2, s[20:23], s55 offen offset:0 sc1 lds
Mv 96,8,80
Rb 56,1,47104
v_exp_f32_e32 v24, v65
Mv 100,8,84
buffer_store_dwordx4 v[128:131], v3, s[20:23], s46 offen offset:0 sc0 sc1
v_add_f32_e32 v14, v32, v24
Mv 100,12,92
Rb 60,1,48128
Cv 32,32,24
s_waitcnt lgkmcnt(8)
Mv 96,12,88
v_exp_f32_e32 v33, v66
Mv 96,16,96
Rb 0,1,49152
v_exp_f32_e32 v25, v67
Mv 100,16,100
v_exp_f32_e32 v34, v72
v_exp_f32_e32 v26, v73
Mv 100,20,108
Rb 4,1,50176
v_exp_f32_e32 v35, v74
Mv 96,20,104
buffer_load_dwordx4 v2, s[20:23], s55 offen offset:1024 sc1 lds
v_add_f32_e32 v14, v14, v33
Mv 96,24,112
Rb 8,1,51200
v_exp_f32_e32 v27, v75
Mv 100,24,116
v_exp_f32_e32 v36, v68
)" R"(
v_add_f32_e32 v14, v14, v25
Mv 100,28,124
Rb 12,1,52224
v_exp_f32_e32 v28, v69
s_waitcnt lgkmcnt(8)
Mv 96,28,120
v_exp_f32_e32 v37, v70
Cv 33,33,25
Mz 104,32,64
Rb 16,1,57344
v_exp_f32_e32 v29, v71
Mz 108,32,68
v_exp_f32_e32 v38, v76
v_add_f32_e32 v14, v14, v34
Mv 108,36,76
Rb 20,1,58368
v_exp_f32_e32 v24, v77
Mv 104,36,72
buffer_load_dwordx4 v2, s[20:23], s55 offen offset:2048 sc1 lds
v_add_f32_e32 v14, v14, v26
Mv 104,40,80
Rb 24,1,59392
v_exp_f32_e32 v39, v78
Cv 34,34,26
Mv 108,40,84
buffer_store_dwordx4 v[136:139], v3, s[20:23], s46 offen offset:1024 sc0 sc1
v_add_f32_e32 v14, v14, v35
Mv 108,44,92
Rb 28,1,60416
v_add_f32_e32 v14, v14, v27
s_waitcnt lgkmcnt(8)
Mv 104,44,88
v_exp_f32_e32 v25, v79
Cv 35,35,27
Mv 104,48,96
Rb 32,1,53248
v_exp_f32_e32 v40, v80
Mv 108,48,100
v_exp_f32_e32 v26, v81
v_exp_f32_e32 v41, v82
Mv 108,52,108
Rb 36,1,54272
v_exp_f32_e32 v27, v83
Mv 104,52,104
buffer_load_dwordx4 v2, s[20:23], s55 offen offset:3072 sc1 lds
v_add_f32_e32 v15, v36, v28
Mv 104,56,112
Rb 40,1,55296
Cv 36,36,28
v_add_f32_e32 v15, v15, v37
Mv 108,56,116
v_exp_f32_e32 v42, v88
v_add_f32_e32 v15, v15, v29
Mv 108,60,124
Rb 44,1,56320
v_exp_f32_e32 v28, v89
s_waitcnt lgkmcnt(8)
Mv 104,60,120
Cv 37,37,29
v_exp_f32_e32 v43, v90
Mz 112,0,64
Rb 48,1,61440
v_add_f32_e32 v15, v15, v38
v_add_f32_e32 v15, v15, v24
)" R"(
Mz 116,0,68
v_exp_f32_e32 v29, v91
Cv 38,38,24
Mv 116,4,76
Rb 52,1,62464
s_add_u32 m0, s36, 4096
v_add_f32_e32 v15, v15, v39
Mv 112,4,72
buffer_load_dwordx4 v3, s[20:23], s55 offen offset:0 sc1 lds
v_add_f32_e32 v15, v15, v25
Mv 112,8,80
Rb 56,1,63488
v_exp_f32_e32 v44, v84
Cv 39,39,25
Mv 116,8,84
buffer_store_dwordx4 v[144:147], v3, s[20:23], s46 offen offset:2048 sc0 sc1
v_add_f32_e32 v14, v14, v40
Mv 116,12,92
Rb 60,1,64512
v_add_f32_e32 v14, v14, v26
s_waitcnt lgkmcnt(8)
Mv 112,12,88
v_exp_f32_e32 v24, v85
Cv 40,40,26
Mv 112,16,96
v_exp_f32_e32 v45, v86
v_add_f32_e32 v14, v14, v41
Mv 116,16,100
v_exp_f32_e32 v25, v87
v_exp_f32_e32 v46, v92
Mv 116,20,108
v_exp_f32_e32 v26, v93
v_add_f32_e32 v14, v14, v27
Mv 112,20,104
buffer_load_dwordx4 v3, s[20:23], s55 offen offset:1024 sc1 lds
Cv 41,41,27
Mv 112,24,112
v_exp_f32_e32 v47, v94
v_add_f32_e32 v14, v14, v42
Mv 116,24,116
v_exp_f32_e32 v27, v95
v_add_f32_e32 v14, v14, v28
Mv 116,28,124
Cv 42,42,28
v_add_f32_e32 v14, v14, v43
v_add_f32_e32 v14, v14, v29
Cv 43,43,29
Mv 112,28,120
v_add_f32_e32 v15, v15, v44
s_waitcnt lgkmcnt(0)
s_barrier
Mz 120,32,64
Rb 0,8,32768
v_add_f32_e32 v15, v15, v24
Cv 44,44,24
Mz 124,32,68
Rb 4,8,33792
v_add_f32_e32 v15, v15, v45
v_add_f32_e32 v15, v15, v25
Mv 124,36,76
Rb 8,8,34816
Cv 45,45,25
v_add_f32_e32 v15, v15, v46
Mv 120,36,72
Rb 12,8,35840
)" R"(
buffer_load_dwordx4 v3, s[20:23], s55 offen offset:2048 sc1 lds
Mv 120,40,80
Rb 16,8,36864
v_add_f32_e32 v15, v15, v26
Cv 46,46,26
v_add_f32_e32 v15, v15, v47
Mv 124,40,84
Rb 20,8,37888
buffer_store_dwordx4 v[152:155], v3, s[20:23], s46 offen offset:3072 sc0 sc1
Mv 124,44,92
Rb 24,8,38912
v_add_f32_e32 v15, v15, v27
Cv 47,47,27
Mv 120,44,88
Rb 28,8,39936
v_max_f32_e32 v11, v14, v15
v_mov_b32 v24, v14
Mv 120,48,96
Rb 32,8,40960
v_mov_b32 v25, v15
Mv 124,48,100
Rb 36,8,41984
v_permlane32_swap_b32_e32 v24, v25
v_add_f32_e32 v24, v24, v25
v_mov_b32 v25, v24
Mv 124,52,108
Rb 40,8,43008
v_permlane16_swap_b32_e32 v24, v25
v_add_f32_e32 v24, v24, v25
Mv 120,52,104
Rb 44,8,44032
buffer_load_dwordx4 v3, s[20:23], s55 offen offset:3072 sc1 lds
Mv 120,56,112
v_rcp_f32_e32 v24, v24
s_add_u32 s55, s55, 0x10000
Mv 124,56,116
s_cmp_eq_u32 s57, 2
s_cselect_b32 s55, s59, s55
v_max_f32_e32 v11, v11, v24
Mv 124,60,124
v_cmp_nge_f32_e32 vcc, s62, v11
s_waitcnt lgkmcnt(8)
s_cbranch_vccnz .Lzfx6
.Lzfr6:
Mv 120,60,120
v_mov_b32 v12, v14
v_mov_b32 v13, v15
v_mfma_f32_16x16x32_bf16 v[128:131], a[0:3], v[32:35], 0
Rb 48,8,45056
v_mfma_f32_16x16x32_bf16 v[136:139], a[0:3], v[36:39], 0
v_mfma_f32_16x16x32_bf16 v[140:143], a[4:7], v[36:39], 0
Rb 52,8,46080
s_add_u32 m0, s36, 32768
v_mfma_f32_16x16x32_bf16 v[132:135], a[4:7], v[32:35], 0
buffer_load_dwordx4 v2, s[20:23], s55 offen offset:0 sc1 lds
v_mfma_f32_16x16x32_bf16 v[144:147], a[8:11], v[32:35], 0
Rb 56,8,47104
v_mfma_f32_16x16x32_bf16 v[152:155], a[8:11], v[36:39], 0
v_exp_f32_e32 v48, v96
v_mfma_f32_16x16x32_bf16 v[156:159], a[12:15], v[36:39], 0
Rb 60,8,48128
s_waitcnt lgkmcnt(8)
v_mfma_f32_16x16x32_bf16 v[148:151], a[12:15], v[32:35], 0
v_exp_f32_e32 v28, v97
)" R"(
v_mfma_f32_16x16x32_bf16 v[160:163], a[16:19], v[32:35], 0
Rb 0,8,49152
v_add_f32_e32 v14, v48, v28
v_mfma_f32_16x16x32_bf16 v[168:171], a[16:19], v[36:39], 0
v_exp_f32_e32 v49, v98
v_mfma_f32_16x16x32_bf16 v[172:175], a[20:23], v[36:39], 0
Rb 4,8,50176
Cv 48,48,28
v_mfma_f32_16x16x32_bf16 v[164:167], a[20:23], v[32:35], 0
buffer_load_dwordx4 v2, s[20:23], s55 offen offset:1024 sc1 lds
v_mfma_f32_16x16x32_bf16 v[176:179], a[24:27], v[32:35], 0
Rb 8,8,51200
v_add_f32_e32 v14, v14, v49
v_mfma_f32_16x16x32_bf16 v[184:187], a[24:27], v[36:39], 0
v_exp_f32_e32 v29, v99
v_mfma_f32_16x16x32_bf16 v[188:191], a[28:31], v[36:39], 0
Rb 12,8,52224
s_waitcnt lgkmcnt(8)
v_mfma_f32_16x16x32_bf16 v[180:183], a[28:31], v[32:35], 0
v_exp_f32_e32 v50, v104
v_mfma_f32_16x16x32_bf16 v[192:195], a[32:35], v[32:35], 0
Rb 16,8,53248
v_add_f32_e32 v14, v14, v29
v_mfma_f32_16x16x32_bf16 v[200:203], a[32:35], v[36:39], 0
v_exp_f32_e32 v24, v105
v_mfma_f32_16x16x32_bf16 v[204:207], a[36:39], v[36:39], 0
Rb 20,8,54272
Cv 49,49,29
v_mfma_f32_16x16x32_bf16 v[196:199], a[36:39], v[32:35], 0
buffer_load_dwordx4 v2, s[20:23], s55 offen offset:2048 sc1 lds
v_mfma_f32_16x16x32_bf16 v[208:211], a[40:43], v[32:35], 0
Rb 24,8,55296
v_add_f32_e32 v14, v14, v50
v_mfma_f32_16x16x32_bf16 v[216:219], a[40:43], v[36:39], 0
v_exp_f32_e32 v51, v106
v_mfma_f32_16x16x32_bf16 v[220:223], a[44:47], v[36:39], 0
Rb 28,8,56320
s_waitcnt lgkmcnt(8)
v_mfma_f32_16x16x32_bf16 v[212:215], a[44:47], v[32:35], 0
v_exp_f32_e32 v25, v107
v_mfma_f32_16x16x32_bf16 v[224:227], a[48:51], v[32:35], 0
Rb 32,8,57344
v_add_f32_e32 v14, v14, v24
v_mfma_f32_16x16x32_bf16 v[232:235], a[48:51], v[36:39], 0
v_exp_f32_e32 v52, v100
v_mfma_f32_16x16x32_bf16 v[236:239], a[52:55], v[36:39], 0
Rb 36,8,58368
Cv 50,50,24
v_mfma_f32_16x16x32_bf16 v[228:231], a[52:55], v[32:35], 0
buffer_load_dwordx4 v2, s[20:23], s55 offen offset:3072 sc1 lds
v_mfma_f32_16x16x32_bf16 v[240:243], a[56:59], v[32:35], 0
Rb 40,8,59392
v_add_f32_e32 v14, v14, v51
v_mfma_f32_16x16x32_bf16 v[248:251], a[56:59], v[36:39], 0
v_exp_f32_e32 v26, v101
v_mfma_f32_16x16x32_bf16 v[252:255], a[60:63], v[36:39], 0
Rb 44,8,60416
s_waitcnt lgkmcnt(8)
v_mfma_f32_16x16x32_bf16 v[244:247], a[60:63], v[32:35], 0
v_exp_f32_e32 v53, v102
Mp 128,0,40
Rb 48,8,61440
v_add_f32_e32 v14, v14, v25
Mp 136,0,44
)" R"(
v_exp_f32_e32 v27, v103
Mp 140,4,44
Rb 52,8,62464
s_add_u32 m0, s36, 36864
Mp 132,4,40
buffer_load_dwordx4 v3, s[20:23], s55 offen offset:0 sc1 lds
Mp 144,8,40
Rb 56,8,63488
Cv 51,51,25
Mp 152,8,44
v_exp_f32_e32 v54, v108
Mp 156,12,44
Rb 60,8,64512
s_waitcnt lgkmcnt(8)
Mp 148,12,40
v_exp_f32_e32 v28, v109
Mp 160,16,40
v_exp_f32_e32 v55, v110
Mp 168,16,44
v_exp_f32_e32 v29, v111
Mp 172,20,44
v_add_f32_e32 v15, v52, v26
Cv 52,52,26
Mp 164,20,40
buffer_load_dwordx4 v3, s[20:23], s55 offen offset:1024 sc1 lds
Mp 176,24,40
v_add_f32_e32 v15, v15, v53
v_add_f32_e32 v15, v15, v27
Mp 184,24,44
Cv 53,53,27
v_add_f32_e32 v15, v15, v54
Mp 188,28,44
v_add_f32_e32 v15, v15, v28
Cv 54,54,28
Mp 180,28,40
s_waitcnt vmcnt(6) lgkmcnt(0)
s_barrier
buffer_store_dword v6, v7, s[20:23], s49 offen offset:4 sc0 sc1
Mp 192,32,40
Rb 0,1,0
v_add_f32_e32 v15, v15, v55
Mp 200,32,44
Rb 4,1,1024
v_add_f32_e32 v15, v15, v29
Mp 204,36,44
Rb 8,1,2048
Cv 55,55,29
Mp 196,36,40
Rb 12,1,3072
buffer_load_dwordx4 v3, s[20:23], s55 offen offset:2048 sc1 lds
Mp 208,40,40
Rb 16,1,8192
s_sub_u32 s57, s57, 1
Mp 216,40,44
Rb 20,1,9216
Mp 220,44,44
Rb 24,1,10240
Mp 212,44,40
Rb 28,1,11264
Mp 224,48,40
Rb 32,1,4096
Mp 232,48,44
Rb 36,1,5120
Mp 236,52,44
Rb 40,1,6144
)" R"(
Mp 228,52,40
Rb 44,1,7168
buffer_load_dwordx4 v3, s[20:23], s55 offen offset:3072 sc1 lds
Mp 240,56,40
v_cmp_eq_u32_e32 vcc, s3, v4
s_andn2_b64 s[28:29], s[60:61], vcc
s_cbranch_scc1 .Lzsp14
.Lzok14:
Mp 248,56,44
Mp 252,60,44
s_waitcnt lgkmcnt(8)
Mp 244,60,40
v_mfma_f32_16x16x32_bf16 v[64:67], a[0:3], a[64:67], v[16:19]
Rb 48,1,12288
v_mfma_f32_16x16x32_bf16 v[68:71], a[0:3], a[68:71], v[20:23]
v_exp_f32_e32 v56, v112
Mv 68,4,76
Rb 52,1,13312
s_add_u32 m0, s36, 98304
Mv 64,4,72
buffer_load_dwordx4 v2, s[20:23], s56 offen offset:0 sc1 lds
Mv 64,8,80
Rb 56,1,14336
v_add_f32_e32 v14, v14, v56
Mv 68,8,84
buffer_store_dwordx4 a[224:227], v2, s[20:23], s47 offen offset:0 sc0 sc1
Mv 68,12,92
Rb 60,1,15360
s_waitcnt lgkmcnt(8)
Mv 64,12,88
v_exp_f32_e32 v24, v113
Mv 64,16,96
Rb 0,1,16384
v_add_f32_e32 v14, v14, v24
Mv 68,16,100
v_exp_f32_e32 v57, v114
Mv 68,20,108
Rb 4,1,17408
Cv 56,56,24
Mv 64,20,104
buffer_load_dwordx4 v2, s[20:23], s56 offen offset:1024 sc1 lds
Mv 64,24,112
Rb 8,1,18432
v_add_f32_e32 v14, v14, v57
Mv 68,24,116
v_exp_f32_e32 v25, v115
Mv 68,28,124
Rb 12,1,19456
s_waitcnt lgkmcnt(8)
Mv 64,28,120
v_exp_f32_e32 v58, v120
v_mfma_f32_16x16x32_bf16 v[72:75], a[32:35], a[64:67], v[16:19]
Rb 16,1,24576
v_add_f32_e32 v14, v14, v25
v_mfma_f32_16x16x32_bf16 v[76:79], a[32:35], a[68:71], v[20:23]
v_exp_f32_e32 v26, v121
Mv 76,36,76
Rb 20,1,25600
Cv 57,57,25
Mv 72,36,72
buffer_load_dwordx4 v2, s[20:23], s56 offen offset:2048 sc1 lds
Mv 72,40,80
Rb 24,1,26624
v_add_f32_e32 v14, v14, v58
)" R"(
Mv 76,40,84
v_exp_f32_e32 v59, v122
Mv 76,44,92
Rb 28,1,27648
s_waitcnt lgkmcnt(8)
Mv 72,44,88
v_exp_f32_e32 v27, v123
Mv 72,48,96
Rb 32,1,20480
v_add_f32_e32 v14, v14, v26
Mv 76,48,100
v_exp_f32_e32 v60, v116
Mv 76,52,108
Rb 36,1,21504
Cv 58,58,26
Mv 72,52,104
buffer_load_dwordx4 v2, s[20:23], s56 offen offset:3072 sc1 lds
Mv 72,56,112
Rb 40,1,22528
v_add_f32_e32 v14, v14, v59
Mv 76,56,116
buffer_store_dwordx4 a[228:231], v2, s[20:23], s47 offen offset:1024 sc0 sc1
Mv 76,60,124
Rb 44,1,23552
s_waitcnt lgkmcnt(8)
Mv 72,60,120
v_exp_f32_e32 v28, v117
v_mfma_f32_16x16x32_bf16 v[80:83], a[0:3], a[64:67], v[16:19]
Rb 48,1,28672
v_add_f32_e32 v14, v14, v27
v_mfma_f32_16x16x32_bf16 v[84:87], a[0:3], a[68:71], v[20:23]
v_exp_f32_e32 v61, v118
Mv 84,4,76
Rb 52,1,29696
s_add_u32 m0, s36, 102400
Mv 80,4,72
buffer_load_dwordx4 v3, s[20:23], s56 offen offset:0 sc1 lds
Mv 80,8,80
Rb 56,1,30720
Cv 59,59,27
Mv 84,8,84
v_exp_f32_e32 v29, v119
Mv 84,12,92
Rb 60,1,31744
s_waitcnt lgkmcnt(8)
Mv 80,12,88
v_exp_f32_e32 v62, v124
Mv 80,16,96
v_exp_f32_e32 v24, v125
Mv 84,16,100
v_exp_f32_e32 v63, v126
Mv 84,20,108
v_exp_f32_e32 v25, v127
Mv 80,20,104
buffer_load_dwordx4 v3, s[20:23], s56 offen offset:1024 sc1 lds
Mv 80,24,112
v_add_f32_e32 v15, v15, v60
v_add_f32_e32 v15, v15, v28
Mv 84,24,116
Cv 60,60,28
v_add_f32_e32 v15, v15, v61
Mv 84,28,124
v_add_f32_e32 v15, v15, v29
Cv 61,61,29
)" R"(
Mv 80,28,120
s_waitcnt lgkmcnt(0)
s_barrier
v_mfma_f32_16x16x32_bf16 v[88:91], a[32:35], a[64:67], v[16:19]
Rb 0,8,0
v_add_f32_e32 v15, v15, v62
v_mfma_f32_16x16x32_bf16 v[92:95], a[32:35], a[68:71], v[20:23]
Rb 4,8,1024
v_add_f32_e32 v15, v15, v24
Mv 92,36,76
Rb 8,8,2048
Cv 62,62,24
Mv 88,36,72
Rb 12,8,3072
buffer_load_dwordx4 v3, s[20:23], s56 offen offset:2048 sc1 lds
Mv 88,40,80
Rb 16,8,4096
v_add_f32_e32 v15, v15, v63
Mv 92,40,84
Rb 20,8,5120
buffer_store_dwordx4 a[232:235], v2, s[20:23], s47 offen offset:2048 sc0 sc1
Mv 92,44,92
Rb 24,8,6144
v_add_f32_e32 v15, v15, v25
Mv 88,44,88
Rb 28,8,7168
Cv 63,63,25
Mv 88,48,96
Rb 32,8,8192
v_max_f32_e32 v11, v14, v15
Mv 92,48,100
Rb 36,8,9216
v_cmp_nge_f32_e32 vcc, s62, v11
Mv 92,52,108
Rb 40,8,10240
s_add_u32 s55, s55, 0x10000
Mv 88,52,104
Rb 44,8,11264
buffer_load_dwordx4 v3, s[20:23], s56 offen offset:3072 sc1 lds
Mv 88,56,112
s_cmp_eq_u32 s57, 2
s_cselect_b32 s55, s59, s55
Mv 92,56,116
s_add_u32 s56, s56, 0x10000
Mv 92,60,124
s_waitcnt lgkmcnt(8)
s_cbranch_vccnz .Lzfx7
.Lzfr7:
Mv 88,60,120
v_add_f32_e32 v12, v12, v14
v_add_f32_e32 v13, v13, v15
Mp 128,0,48
Rb 48,8,12288
Mp 136,0,52
s_cmp_le_i32 s57, s58
s_cbranch_scc1 .Lzmk6
.Lzmr6:
Mp 140,4,52
Rb 52,8,13312
s_add_u32 m0, s36, 0
Mp 132,4,48
buffer_load_dwordx4 v2, s[20:23], s55 offen offset:0 sc1 lds
Mp 144,8,48
Rb 56,8,14336
Mp 152,8,52
)" R"(
v_exp_f32_e32 v32, v64
Mp 156,12,52
Rb 60,8,15360
s_waitcnt lgkmcnt(8)
Mp 148,12,48
v_exp_f32_e32 v26, v65
Mp 160,16,48
Rb 0,8,16384
v_add_f32_e32 v14, v32, v26
Mp 168,16,52
v_exp_f32_e32 v33, v66
Mp 172,20,52
Rb 4,8,17408
Cv 32,32,26
Mp 164,20,48
buffer_load_dwordx4 v2, s[20:23], s55 offen offset:1024 sc1 lds
Mp 176,24,48
Rb 8,8,18432
v_add_f32_e32 v14, v14, v33
Mp 184,24,52
buffer_store_dwordx4 a[236:239], v2, s[20:23], s47 offen offset:3072 sc0 sc1
Mp 188,28,52
Rb 12,8,19456
s_waitcnt lgkmcnt(8)
Mp 180,28,48
v_exp_f32_e32 v27, v67
Mp 192,32,48
Rb 16,8,20480
v_add_f32_e32 v14, v14, v27
Mp 200,32,52
v_exp_f32_e32 v34, v72
Mp 204,36,52
Rb 20,8,21504
Cv 33,33,27
Mp 196,36,48
buffer_load_dwordx4 v2, s[20:23], s55 offen offset:2048 sc1 lds
Mp 208,40,48
Rb 24,8,22528
v_add_f32_e32 v14, v14, v34
Mp 216,40,52
v_exp_f32_e32 v28, v73
Mp 220,44,52
Rb 28,8,23552
s_waitcnt lgkmcnt(8)
Mp 212,44,48
v_exp_f32_e32 v35, v74
Mp 224,48,48
Rb 32,8,24576
v_add_f32_e32 v14, v14, v28
Mp 232,48,52
v_exp_f32_e32 v29, v75
Mp 236,52,52
Rb 36,8,25600
Cv 34,34,28
Mp 228,52,48
buffer_load_dwordx4 v2, s[20:23], s55 offen offset:3072 sc1 lds
Mp 240,56,48
Rb 40,8,26624
v_add_f32_e32 v14, v14, v35
Mp 248,56,52
v_exp_f32_e32 v36, v68
Mp 252,60,52
Rb 44,8,27648
s_waitcnt lgkmcnt(8)
)" R"(
Mp 244,60,48
v_exp_f32_e32 v24, v69
Mp 128,0,56
Rb 48,8,28672
v_add_f32_e32 v14, v14, v29
Mp 136,0,60
v_exp_f32_e32 v37, v70
Mp 140,4,60
Rb 52,8,29696
s_add_u32 m0, s36, 4096
Mp 132,4,56
buffer_load_dwordx4 v3, s[20:23], s55 offen offset:0 sc1 lds
Mp 144,8,56
Rb 56,8,30720
Cv 35,35,29
Mp 152,8,60
buffer_store_dwordx4 a[240:243], v3, s[20:23], s47 offen offset:0 sc0 sc1
Mp 156,12,60
Rb 60,8,31744
s_waitcnt lgkmcnt(8)
Mp 148,12,56
v_exp_f32_e32 v25, v71
Mp 160,16,56
v_exp_f32_e32 v38, v76
Mp 168,16,60
v_exp_f32_e32 v26, v77
Mp 172,20,60
v_exp_f32_e32 v39, v78
Mp 164,20,56
buffer_load_dwordx4 v3, s[20:23], s55 offen offset:1024 sc1 lds
Mp 176,24,56
v_exp_f32_e32 v27, v79
Mp 184,24,60
v_add_f32_e32 v15, v36, v24
Cv 36,36,24
Mp 188,28,60
v_add_f32_e32 v15, v15, v37
v_add_f32_e32 v15, v15, v25
Mp 180,28,56
s_waitcnt vmcnt(19) lgkmcnt(0)
s_barrier
Mp 192,32,56
Rb 0,1,32768
Cv 37,37,25
Mp 200,32,60
Rb 4,1,33792
v_add_f32_e32 v15, v15, v38
Mp 204,36,60
Rb 8,1,34816
v_add_f32_e32 v15, v15, v26
Mp 196,36,56
Rb 12,1,35840
buffer_load_dwordx4 v3, s[20:23], s55 offen offset:2048 sc1 lds
Mp 208,40,56
Rb 16,1,40960
Cv 38,38,26
Mp 216,40,60
Rb 20,1,41984
v_add_f32_e32 v15, v15, v39
Mp 220,44,60
Rb 24,1,43008
v_add_f32_e32 v15, v15, v27
Mp 212,44,56
Rb 28,1,44032
Cv 39,39,27
)" R"(
Mp 224,48,56
Rb 32,1,36864
s_sub_u32 s57, s57, 1
Mp 232,48,60
Rb 36,1,37888
Mp 236,52,60
Rb 40,1,38912
Mp 228,52,56
Rb 44,1,39936
buffer_load_dwordx4 v3, s[20:23], s55 offen offset:3072 sc1 lds
Mp 240,56,56
Mp 248,56,60
buffer_store_dwordx4 a[244:247], v3, s[20:23], s47 offen offset:1024 sc0 sc1
Mp 252,60,60
s_waitcnt lgkmcnt(8)
Mp 244,60,56
v_mfma_f32_16x16x32_bf16 v[96:99], a[0:3], a[64:67], v[16:19]
Rb 48,1,45056
buffer_load_dword v4, v5, s[20:23], s50 offen offset:0 sc0 sc1
v_mfma_f32_16x16x32_bf16 v[100:103], a[0:3], a[68:71], v[20:23]
s_lshl_b64 s[60:61], s[60:61], 8
s_or_b32 s60, s60, 0xff
Mv 100,4,76
Rb 52,1,46080
s_add_u32 m0, s36, 65536
Mv 96,4,72
buffer_load_dwordx4 v2, s[20:23], s56 offen offset:0 sc1 lds
Mv 96,8,80
Rb 56,1,47104
Mv 100,8,84
v_exp_f32_e32 v40, v80
Mv 100,12,92
Rb 60,1,48128
s_waitcnt lgkmcnt(8)
Mv 96,12,88
v_exp_f32_e32 v28, v81
Mv 96,16,96
Rb 0,1,49152
v_add_f32_e32 v14, v14, v40
Mv 100,16,100
v_exp_f32_e32 v41, v82
Mv 100,20,108
Rb 4,1,50176
v_add_f32_e32 v14, v14, v28
Mv 96,20,104
buffer_load_dwordx4 v2, s[20:23], s56 offen offset:1024 sc1 lds
Mv 96,24,112
Rb 8,1,51200
Cv 40,40,28
Mv 100,24,116
v_exp_f32_e32 v29, v83
Mv 100,28,124
Rb 12,1,52224
s_waitcnt lgkmcnt(8)
Mv 96,28,120
v_exp_f32_e32 v42, v88
v_add_f32_e32 v14, v14, v41
v_mfma_f32_16x16x32_bf16 v[104:107], a[32:35], a[64:67], v[16:19]
Rb 16,1,57344
v_add_f32_e32 v14, v14, v29
v_mfma_f32_16x16x32_bf16 v[108:111], a[32:35], a[68:71], v[20:23]
v_exp_f32_e32 v24, v89
Mv 108,36,76
Rb 20,1,58368
)" R"(
Cv 41,41,29
Mv 104,36,72
buffer_load_dwordx4 v2, s[20:23], s56 offen offset:2048 sc1 lds
Mv 104,40,80
Rb 24,1,59392
v_add_f32_e32 v14, v14, v42
Mv 108,40,84
buffer_store_dwordx4 a[248:251], v3, s[20:23], s47 offen offset:2048 sc0 sc1
Mv 108,44,92
Rb 28,1,60416
s_waitcnt lgkmcnt(8)
Mv 104,44,88
v_exp_f32_e32 v43, v90
Mv 104,48,96
Rb 32,1,53248
v_add_f32_e32 v14, v14, v24
Mv 108,48,100
v_exp_f32_e32 v25, v91
Mv 108,52,108
Rb 36,1,54272
Cv 42,42,24
Mv 104,52,104
buffer_load_dwordx4 v2, s[20:23], s56 offen offset:3072 sc1 lds
Mv 104,56,112
Rb 40,1,55296
v_add_f32_e32 v14, v14, v43
Mv 108,56,116
v_exp_f32_e32 v44, v84
Mv 108,60,124
Rb 44,1,56320
s_waitcnt lgkmcnt(8)
Mv 104,60,120
v_exp_f32_e32 v26, v85
v_add_f32_e32 v14, v14, v25
v_mfma_f32_16x16x32_bf16 v[112:115], a[0:3], a[64:67], v[16:19]
Rb 48,1,61440
Cv 43,43,25
v_mfma_f32_16x16x32_bf16 v[116:119], a[0:3], a[68:71], v[20:23]
v_exp_f32_e32 v45, v86
Mv 116,4,76
Rb 52,1,62464
s_add_u32 m0, s36, 69632
Mv 112,4,72
buffer_load_dwordx4 v3, s[20:23], s56 offen offset:0 sc1 lds
Mv 112,8,80
Rb 56,1,63488
v_add_f32_e32 v15, v15, v44
Mv 116,8,84
v_exp_f32_e32 v27, v87
Mv 116,12,92
Rb 60,1,64512
s_waitcnt lgkmcnt(8)
Mv 112,12,88
v_exp_f32_e32 v46, v92
Mv 112,16,96
v_exp_f32_e32 v28, v93
Mv 116,16,100
v_exp_f32_e32 v47, v94
Mv 116,20,108
v_exp_f32_e32 v29, v95
Mv 112,20,104
buffer_load_dwordx4 v3, s[20:23], s56 offen offset:1024 sc1 lds
Mv 112,24,112
v_add_f32_e32 v15, v15, v26
)" R"(
Cv 44,44,26
Mv 116,24,116
buffer_store_dwordx4 a[252:255], v3, s[20:23], s47 offen offset:3072 sc0 sc1
Mv 116,28,124
v_add_f32_e32 v15, v15, v45
v_add_f32_e32 v15, v15, v27
Mv 112,28,120
Cv 45,45,27
s_waitcnt vmcnt(20) lgkmcnt(0)
s_barrier
v_mfma_f32_16x16x32_bf16 v[120:123], a[32:35], a[64:67], v[16:19]
Rb 0,8,32768
v_add_f32_e32 v15, v15, v46
v_mfma_f32_16x16x32_bf16 v[124:127], a[32:35], a[68:71], v[20:23]
Rb 4,8,33792
v_add_f32_e32 v15, v15, v28
Mv 124,36,76
Rb 8,8,34816
Cv 46,46,28
Mv 120,36,72
Rb 12,8,35840
buffer_load_dwordx4 v3, s[20:23], s56 offen offset:2048 sc1 lds
Mv 120,40,80
Rb 16,8,36864
v_add_f32_e32 v15, v15, v47
Mv 124,40,84
Rb 20,8,37888
v_add_f32_e32 v15, v15, v29
Mv 124,44,92
Rb 24,8,38912
Cv 47,47,29
Mv 120,44,88
Rb 28,8,39936
v_max_f32_e32 v11, v14, v15
Mv 120,48,96
Rb 32,8,40960
v_cmp_nge_f32_e32 vcc, s62, v11
Mv 124,48,100
Rb 36,8,41984
s_add_u32 s55, s55, 0x10000
Mv 124,52,108
Rb 40,8,43008
Mv 120,52,104
Rb 44,8,44032
buffer_load_dwordx4 v3, s[20:23], s56 offen offset:3072 sc1 lds
Mv 120,56,112
s_cmp_eq_u32 s57, 2
s_cselect_b32 s55, s59, s55
Mv 124,56,116
s_add_u32 s56, s56, 0x10000
Mv 124,60,124
s_waitcnt lgkmcnt(8)
s_cbranch_vccnz .Lzfx8
.Lzfr8:
Mv 120,60,120
v_add_f32_e32 v12, v12, v14
v_add_f32_e32 v13, v13, v15
Mp 128,0,32
Rb 48,8,45056
Mp 136,0,36
s_cmp_le_i32 s57, s58
s_cbranch_scc1 .Lzmk7
.Lzmr7:
Mp 140,4,36
Rb 52,8,46080
)" R"(
s_add_u32 m0, s36, 32768
Mp 132,4,32
buffer_load_dwordx4 v2, s[20:23], s55 offen offset:0 sc1 lds
Mp 144,8,32
Rb 56,8,47104
Mp 152,8,36
v_exp_f32_e32 v48, v96
Mp 156,12,36
Rb 60,8,48128
s_waitcnt lgkmcnt(8)
Mp 148,12,32
v_exp_f32_e32 v24, v97
Mp 160,16,32
Rb 0,8,49152
v_add_f32_e32 v14, v48, v24
Mp 168,16,36
v_exp_f32_e32 v49, v98
Mp 172,20,36
Rb 4,8,50176
Cv 48,48,24
Mp 164,20,32
buffer_load_dwordx4 v2, s[20:23], s55 offen offset:1024 sc1 lds
Mp 176,24,32
Rb 8,8,51200
v_add_f32_e32 v14, v14, v49
Mp 184,24,36
v_exp_f32_e32 v25, v99
Mp 188,28,36
Rb 12,8,52224
s_waitcnt lgkmcnt(8)
Mp 180,28,32
v_exp_f32_e32 v50, v104
Mp 192,32,32
Rb 16,8,53248
v_add_f32_e32 v14, v14, v25
Mp 200,32,36
v_exp_f32_e32 v26, v105
Mp 204,36,36
Rb 20,8,54272
Cv 49,49,25
Mp 196,36,32
buffer_load_dwordx4 v2, s[20:23], s55 offen offset:2048 sc1 lds
Mp 208,40,32
Rb 24,8,55296
v_add_f32_e32 v14, v14, v50
Mp 216,40,36
v_exp_f32_e32 v51, v106
Mp 220,44,36
Rb 28,8,56320
s_waitcnt lgkmcnt(8)
Mp 212,44,32
v_exp_f32_e32 v27, v107
Mp 224,48,32
Rb 32,8,57344
v_add_f32_e32 v14, v14, v26
Mp 232,48,36
v_exp_f32_e32 v52, v100
Mp 236,52,36
Rb 36,8,58368
Cv 50,50,26
Mp 228,52,32
buffer_load_dwordx4 v2, s[20:23], s55 offen offset:3072 sc1 lds
Mp 240,56,32
Rb 40,8,59392
)" R"(
v_add_f32_e32 v14, v14, v51
Mp 248,56,36
v_exp_f32_e32 v28, v101
Mp 252,60,36
Rb 44,8,60416
s_waitcnt lgkmcnt(8)
Mp 244,60,32
v_exp_f32_e32 v53, v102
Mp 128,0,40
Rb 48,8,61440
v_add_f32_e32 v14, v14, v27
Mp 136,0,44
v_exp_f32_e32 v29, v103
Mp 140,4,44
Rb 52,8,62464
s_add_u32 m0, s36, 36864
Mp 132,4,40
buffer_load_dwordx4 v3, s[20:23], s55 offen offset:0 sc1 lds
Mp 144,8,40
Rb 56,8,63488
Cv 51,51,27
Mp 152,8,44
v_exp_f32_e32 v54, v108
Mp 156,12,44
Rb 60,8,64512
s_waitcnt lgkmcnt(8)
Mp 148,12,40
v_exp_f32_e32 v24, v109
Mp 160,16,40
v_exp_f32_e32 v55, v110
Mp 168,16,44
v_exp_f32_e32 v25, v111
Mp 172,20,44
v_add_f32_e32 v15, v52, v28
Cv 52,52,28
Mp 164,20,40
buffer_load_dwordx4 v3, s[20:23], s55 offen offset:1024 sc1 lds
Mp 176,24,40
v_add_f32_e32 v15, v15, v53
v_add_f32_e32 v15, v15, v29
Mp 184,24,44
Cv 53,53,29
v_add_f32_e32 v15, v15, v54
Mp 188,28,44
v_add_f32_e32 v15, v15, v24
Cv 54,54,24
Mp 180,28,40
s_waitcnt vmcnt(8) lgkmcnt(0)
s_barrier
buffer_store_dword v6, v7, s[20:23], s49 offen offset:8 sc0 sc1
Mp 192,32,40
Rb 0,1,0
v_add_f32_e32 v15, v15, v55
Mp 200,32,44
Rb 4,1,1024
v_add_f32_e32 v15, v15, v25
Mp 204,36,44
Rb 8,1,2048
Cv 55,55,25
Mp 196,36,40
Rb 12,1,3072
buffer_load_dwordx4 v3, s[20:23], s55 offen offset:2048 sc1 lds
Mp 208,40,40
Rb 16,1,8192
s_sub_u32 s57, s57, 1
)" R"(
Mp 216,40,44
Rb 20,1,9216
Mp 220,44,44
Rb 24,1,10240
Mp 212,44,40
Rb 28,1,11264
Mp 224,48,40
Rb 32,1,4096
Mp 232,48,44
Rb 36,1,5120
Mp 236,52,44
Rb 40,1,6144
Mp 228,52,40
Rb 44,1,7168
buffer_load_dwordx4 v3, s[20:23], s55 offen offset:3072 sc1 lds
Mp 240,56,40
v_cmp_eq_u32_e32 vcc, s3, v4
s_andn2_b64 s[28:29], s[60:61], vcc
s_cbranch_scc1 .Lzsp15
.Lzok15:
Mp 248,56,44
Mp 252,60,44
s_waitcnt lgkmcnt(8)
Mp 244,60,40
v_mfma_f32_16x16x32_bf16 v[64:67], a[0:3], a[64:67], v[16:19]
Rb 48,1,12288
v_mfma_f32_16x16x32_bf16 v[68:71], a[0:3], a[68:71], v[20:23]
v_exp_f32_e32 v56, v112
Mv 68,4,76
Rb 52,1,13312
s_add_u32 m0, s36, 98304
Mv 64,4,72
buffer_load_dwordx4 v2, s[20:23], s56 offen offset:0 sc1 lds
Mv 64,8,80
Rb 56,1,14336
v_add_f32_e32 v14, v14, v56
Mv 68,8,84
buffer_store_dwordx4 a[128:131], v2, s[20:23], s48 offen offset:0 sc0 sc1
Mv 68,12,92
Rb 60,1,15360
s_waitcnt lgkmcnt(8)
Mv 64,12,88
v_exp_f32_e32 v26, v113
Mv 64,16,96
Rb 0,1,16384
v_add_f32_e32 v14, v14, v26
Mv 68,16,100
v_exp_f32_e32 v57, v114
Mv 68,20,108
Rb 4,1,17408
Cv 56,56,26
Mv 64,20,104
buffer_load_dwordx4 v2, s[20:23], s56 offen offset:1024 sc1 lds
Mv 64,24,112
Rb 8,1,18432
v_add_f32_e32 v14, v14, v57
Mv 68,24,116
v_exp_f32_e32 v27, v115
Mv 68,28,124
Rb 12,1,19456
s_waitcnt lgkmcnt(8)
Mv 64,28,120
v_exp_f32_e32 v58, v120
v_mfma_f32_16x16x32_bf16 v[72:75], a[32:35], a[64:67], v[16:19]
)" R"(
Rb 16,1,24576
v_add_f32_e32 v14, v14, v27
v_mfma_f32_16x16x32_bf16 v[76:79], a[32:35], a[68:71], v[20:23]
v_exp_f32_e32 v28, v121
Mv 76,36,76
Rb 20,1,25600
Cv 57,57,27
Mv 72,36,72
buffer_load_dwordx4 v2, s[20:23], s56 offen offset:2048 sc1 lds
Mv 72,40,80
Rb 24,1,26624
v_add_f32_e32 v14, v14, v58
Mv 76,40,84
v_exp_f32_e32 v59, v122
Mv 76,44,92
Rb 28,1,27648
s_waitcnt lgkmcnt(8)
Mv 72,44,88
v_exp_f32_e32 v29, v123
Mv 72,48,96
Rb 32,1,20480
v_add_f32_e32 v14, v14, v28
Mv 76,48,100
v_exp_f32_e32 v60, v116
Mv 76,52,108
Rb 36,1,21504
Cv 58,58,28
Mv 72,52,104
buffer_load_dwordx4 v2, s[20:23], s56 offen offset:3072 sc1 lds
Mv 72,56,112
Rb 40,1,22528
v_add_f32_e32 v14, v14, v59
Mv 76,56,116
buffer_store_dwordx4 a[132:135], v2, s[20:23], s48 offen offset:1024 sc0 sc1
Mv 76,60,124
Rb 44,1,23552
s_waitcnt lgkmcnt(8)
Mv 72,60,120
v_exp_f32_e32 v24, v117
v_mfma_f32_16x16x32_bf16 v[80:83], a[0:3], a[64:67], v[16:19]
Rb 48,1,28672
v_add_f32_e32 v14, v14, v29
v_mfma_f32_16x16x32_bf16 v[84:87], a[0:3], a[68:71], v[20:23]
v_exp_f32_e32 v61, v118
Mv 84,4,76
Rb 52,1,29696
s_add_u32 m0, s36, 102400
Mv 80,4,72
buffer_load_dwordx4 v3, s[20:23], s56 offen offset:0 sc1 lds
Mv 80,8,80
Rb 56,1,30720
Cv 59,59,29
Mv 84,8,84
v_exp_f32_e32 v25, v119
Mv 84,12,92
Rb 60,1,31744
s_waitcnt lgkmcnt(8)
Mv 80,12,88
v_exp_f32_e32 v62, v124
Mv 80,16,96
v_exp_f32_e32 v26, v125
Mv 84,16,100
v_exp_f32_e32 v63, v126
Mv 84,20,108
)" R"(
v_exp_f32_e32 v27, v127
Mv 80,20,104
buffer_load_dwordx4 v3, s[20:23], s56 offen offset:1024 sc1 lds
Mv 80,24,112
v_add_f32_e32 v15, v15, v60
v_add_f32_e32 v15, v15, v24
Mv 84,24,116
Cv 60,60,24
v_add_f32_e32 v15, v15, v61
Mv 84,28,124
v_add_f32_e32 v15, v15, v25
Cv 61,61,25
Mv 80,28,120
s_waitcnt vmcnt(17) lgkmcnt(0)
s_barrier
v_mfma_f32_16x16x32_bf16 v[88:91], a[32:35], a[64:67], v[16:19]
Rb 0,8,0
v_add_f32_e32 v15, v15, v62
v_mfma_f32_16x16x32_bf16 v[92:95], a[32:35], a[68:71], v[20:23]
Rb 4,8,1024
v_add_f32_e32 v15, v15, v26
Mv 92,36,76
Rb 8,8,2048
Cv 62,62,26
Mv 88,36,72
Rb 12,8,3072
buffer_load_dwordx4 v3, s[20:23], s56 offen offset:2048 sc1 lds
Mv 88,40,80
Rb 16,8,4096
v_add_f32_e32 v15, v15, v63
Mv 92,40,84
Rb 20,8,5120
buffer_store_dwordx4 a[136:139], v2, s[20:23], s48 offen offset:2048 sc0 sc1
Mv 92,44,92
Rb 24,8,6144
v_add_f32_e32 v15, v15, v27
Mv 88,44,88
Rb 28,8,7168
Cv 63,63,27
Mv 88,48,96
Rb 32,8,8192
v_max_f32_e32 v11, v14, v15
Mv 92,48,100
Rb 36,8,9216
v_cmp_nge_f32_e32 vcc, s62, v11
Mv 92,52,108
Rb 40,8,10240
s_add_u32 s55, s55, 0x10000
Mv 88,52,104
Rb 44,8,11264
buffer_load_dwordx4 v3, s[20:23], s56 offen offset:3072 sc1 lds
Mv 88,56,112
s_cmp_eq_u32 s57, 2
s_cselect_b32 s55, s59, s55
Mv 92,56,116
s_add_u32 s56, s56, 0x10000
Mv 92,60,124
s_waitcnt lgkmcnt(8)
s_cbranch_vccnz .Lzfx9
.Lzfr9:
Mv 88,60,120
v_add_f32_e32 v12, v12, v14
v_add_f32_e32 v13, v13, v15
Mp 128,0,48
Rb 48,8,12288
)" R"(
Mp 136,0,52
s_cmp_le_i32 s57, s58
s_cbranch_scc1 .Lzmk8
.Lzmr8:
Mp 140,4,52
Rb 52,8,13312
s_add_u32 m0, s36, 0
Mp 132,4,48
buffer_load_dwordx4 v2, s[20:23], s55 offen offset:0 sc1 lds
Mp 144,8,48
Rb 56,8,14336
Mp 152,8,52
v_exp_f32_e32 v32, v64
Mp 156,12,52
Rb 60,8,15360
s_waitcnt lgkmcnt(8)
Mp 148,12,48
v_exp_f32_e32 v28, v65
Mp 160,16,48
Rb 0,8,16384
v_add_f32_e32 v14, v32, v28
Mp 168,16,52
v_exp_f32_e32 v33, v66
Mp 172,20,52
Rb 4,8,17408
Cv 32,32,28
Mp 164,20,48
buffer_load_dwordx4 v2, s[20:23], s55 offen offset:1024 sc1 lds
Mp 176,24,48
Rb 8,8,18432
v_add_f32_e32 v14, v14, v33
Mp 184,24,52
buffer_store_dwordx4 a[140:143], v2, s[20:23], s48 offen offset:3072 sc0 sc1
Mp 188,28,52
Rb 12,8,19456
s_waitcnt lgkmcnt(8)
Mp 180,28,48
v_exp_f32_e32 v29, v67
Mp 192,32,48
Rb 16,8,20480
v_add_f32_e32 v14, v14, v29
Mp 200,32,52
v_exp_f32_e32 v34, v72
Mp 204,36,52
Rb 20,8,21504
Cv 33,33,29
Mp 196,36,48
buffer_load_dwordx4 v2, s[20:23], s55 offen offset:2048 sc1 lds
Mp 208,40,48
Rb 24,8,22528
v_add_f32_e32 v14, v14, v34
Mp 216,40,52
v_exp_f32_e32 v24, v73
Mp 220,44,52
Rb 28,8,23552
s_waitcnt lgkmcnt(8)
Mp 212,44,48
v_exp_f32_e32 v35, v74
Mp 224,48,48
Rb 32,8,24576
v_add_f32_e32 v14, v14, v24
Mp 232,48,52
v_exp_f32_e32 v25, v75
Mp 236,52,52
)" R"(
Rb 36,8,25600
Cv 34,34,24
Mp 228,52,48
buffer_load_dwordx4 v2, s[20:23], s55 offen offset:3072 sc1 lds
Mp 240,56,48
Rb 40,8,26624
v_add_f32_e32 v14, v14, v35
Mp 248,56,52
v_exp_f32_e32 v36, v68
Mp 252,60,52
Rb 44,8,27648
s_waitcnt lgkmcnt(8)
Mp 244,60,48
v_exp_f32_e32 v26, v69
Mp 128,0,56
Rb 48,8,28672
v_add_f32_e32 v14, v14, v25
Mp 136,0,60
v_exp_f32_e32 v37, v70
Mp 140,4,60
Rb 52,8,29696
s_add_u32 m0, s36, 4096
Mp 132,4,56
buffer_load_dwordx4 v3, s[20:23], s55 offen offset:0 sc1 lds
Mp 144,8,56
Rb 56,8,30720
Cv 35,35,25
Mp 152,8,60
buffer_store_dwordx4 a[144:147], v3, s[20:23], s48 offen offset:0 sc0 sc1
Mp 156,12,60
Rb 60,8,31744
s_waitcnt lgkmcnt(8)
Mp 148,12,56
v_exp_f32_e32 v27, v71
Mp 160,16,56
v_exp_f32_e32 v38, v76
Mp 168,16,60
v_exp_f32_e32 v28, v77
Mp 172,20,60
v_exp_f32_e32 v39, v78
Mp 164,20,56
buffer_load_dwordx4 v3, s[20:23], s55 offen offset:1024 sc1 lds
Mp 176,24,56
v_exp_f32_e32 v29, v79
Mp 184,24,60
v_add_f32_e32 v15, v36, v26
Cv 36,36,26
Mp 188,28,60
v_add_f32_e32 v15, v15, v37
v_add_f32_e32 v15, v15, v27
Mp 180,28,56
s_waitcnt vmcnt(19) lgkmcnt(0)
s_barrier
Mp 192,32,56
Rb 0,1,32768
Cv 37,37,27
Mp 200,32,60
Rb 4,1,33792
v_add_f32_e32 v15, v15, v38
Mp 204,36,60
Rb 8,1,34816
v_add_f32_e32 v15, v15, v28
Mp 196,36,56
Rb 12,1,35840
buffer_load_dwordx4 v3, s[20:23], s55 offen offset:2048 sc1 lds
)" R"(
Mp 208,40,56
Rb 16,1,40960
Cv 38,38,28
Mp 216,40,60
Rb 20,1,41984
v_add_f32_e32 v15, v15, v39
Mp 220,44,60
Rb 24,1,43008
v_add_f32_e32 v15, v15, v29
Mp 212,44,56
Rb 28,1,44032
Cv 39,39,29
Mp 224,48,56
Rb 32,1,36864
s_sub_u32 s57, s57, 1
Mp 232,48,60
Rb 36,1,37888
Mp 236,52,60
Rb 40,1,38912
Mp 228,52,56
Rb 44,1,39936
buffer_load_dwordx4 v3, s[20:23], s55 offen offset:3072 sc1 lds
Mp 240,56,56
Mp 248,56,60
buffer_store_dwordx4 a[148:151], v3, s[20:23], s48 offen offset:1024 sc0 sc1
Mp 252,60,60
s_waitcnt lgkmcnt(8)
Mp 244,60,56
v_mfma_f32_16x16x32_bf16 v[96:99], a[0:3], a[64:67], v[16:19]
Rb 48,1,45056
buffer_load_dword v4, v5, s[20:23], s50 offen offset:0 sc0 sc1
v_mfma_f32_16x16x32_bf16 v[100:103], a[0:3], a[68:71], v[20:23]
s_lshl_b64 s[60:61], s[60:61], 8
s_or_b32 s60, s60, 0xff
Mv 100,4,76
Rb 52,1,46080
s_add_u32 m0, s36, 65536
Mv 96,4,72
buffer_load_dwordx4 v2, s[20:23], s56 offen offset:0 sc1 lds
Mv 96,8,80
Rb 56,1,47104
Mv 100,8,84
v_exp_f32_e32 v40, v80
Mv 100,12,92
Rb 60,1,48128
s_waitcnt lgkmcnt(8)
Mv 96,12,88
v_exp_f32_e32 v24, v81
Mv 96,16,96
Rb 0,1,49152
v_add_f32_e32 v14, v14, v40
Mv 100,16,100
v_exp_f32_e32 v41, v82
Mv 100,20,108
Rb 4,1,50176
v_add_f32_e32 v14, v14, v24
Mv 96,20,104
buffer_load_dwordx4 v2, s[20:23], s56 offen offset:1024 sc1 lds
Mv 96,24,112
Rb 8,1,51200
Cv 40,40,24
Mv 100,24,116
v_exp_f32_e32 v25, v83
Mv 100,28,124
)" R"(
Rb 12,1,52224
s_waitcnt lgkmcnt(8)
Mv 96,28,120
v_exp_f32_e32 v42, v88
v_add_f32_e32 v14, v14, v41
v_mfma_f32_16x16x32_bf16 v[104:107], a[32:35], a[64:67], v[16:19]
Rb 16,1,57344
v_add_f32_e32 v14, v14, v25
v_mfma_f32_16x16x32_bf16 v[108:111], a[32:35], a[68:71], v[20:23]
v_exp_f32_e32 v26, v89
Mv 108,36,76
Rb 20,1,58368
Cv 41,41,25
Mv 104,36,72
buffer_load_dwordx4 v2, s[20:23], s56 offen offset:2048 sc1 lds
Mv 104,40,80
Rb 24,1,59392
v_add_f32_e32 v14, v14, v42
Mv 108,40,84
buffer_store_dwordx4 a[152:155], v3, s[20:23], s48 offen offset:2048 sc0 sc1
Mv 108,44,92
Rb 28,1,60416
s_waitcnt lgkmcnt(8)
Mv 104,44,88
v_exp_f32_e32 v43, v90
Mv 104,48,96
Rb 32,1,53248
v_add_f32_e32 v14, v14, v26
Mv 108,48,100
v_exp_f32_e32 v27, v91
Mv 108,52,108
Rb 36,1,54272
Cv 42,42,26
Mv 104,52,104
buffer_load_dwordx4 v2, s[20:23], s56 offen offset:3072 sc1 lds
Mv 104,56,112
Rb 40,1,55296
v_add_f32_e32 v14, v14, v43
Mv 108,56,116
v_exp_f32_e32 v44, v84
Mv 108,60,124
Rb 44,1,56320
s_waitcnt lgkmcnt(8)
Mv 104,60,120
v_exp_f32_e32 v28, v85
v_add_f32_e32 v14, v14, v27
v_mfma_f32_16x16x32_bf16 v[112:115], a[0:3], a[64:67], v[16:19]
Rb 48,1,61440
Cv 43,43,27
v_mfma_f32_16x16x32_bf16 v[116:119], a[0:3], a[68:71], v[20:23]
v_exp_f32_e32 v45, v86
Mv 116,4,76
Rb 52,1,62464
s_add_u32 m0, s36, 69632
Mv 112,4,72
buffer_load_dwordx4 v3, s[20:23], s56 offen offset:0 sc1 lds
Mv 112,8,80
Rb 56,1,63488
v_add_f32_e32 v15, v15, v44
Mv 116,8,84
v_exp_f32_e32 v29, v87
Mv 116,12,92
Rb 60,1,64512
s_waitcnt lgkmcnt(8)
)" R"(
Mv 112,12,88
v_exp_f32_e32 v46, v92
Mv 112,16,96
v_exp_f32_e32 v24, v93
Mv 116,16,100
v_exp_f32_e32 v47, v94
Mv 116,20,108
v_exp_f32_e32 v25, v95
Mv 112,20,104
buffer_load_dwordx4 v3, s[20:23], s56 offen offset:1024 sc1 lds
Mv 112,24,112
v_add_f32_e32 v15, v15, v28
Cv 44,44,28
Mv 116,24,116
buffer_store_dwordx4 a[156:159], v3, s[20:23], s48 offen offset:3072 sc0 sc1
Mv 116,28,124
v_add_f32_e32 v15, v15, v45
v_add_f32_e32 v15, v15, v29
Mv 112,28,120
Cv 45,45,29
s_waitcnt vmcnt(20) lgkmcnt(0)
s_barrier
v_mfma_f32_16x16x32_bf16 v[120:123], a[32:35], a[64:67], v[16:19]
Rb 0,8,32768
v_add_f32_e32 v15, v15, v46
v_mfma_f32_16x16x32_bf16 v[124:127], a[32:35], a[68:71], v[20:23]
Rb 4,8,33792
v_add_f32_e32 v15, v15, v24
Mv 124,36,76
Rb 8,8,34816
Cv 46,46,24
Mv 120,36,72
Rb 12,8,35840
buffer_load_dwordx4 v3, s[20:23], s56 offen offset:2048 sc1 lds
Mv 120,40,80
Rb 16,8,36864
v_add_f32_e32 v15, v15, v47
Mv 124,40,84
Rb 20,8,37888
v_add_f32_e32 v15, v15, v25
Mv 124,44,92
Rb 24,8,38912
Cv 47,47,25
Mv 120,44,88
Rb 28,8,39936
v_max_f32_e32 v11, v14, v15
Mv 120,48,96
Rb 32,8,40960
v_cmp_nge_f32_e32 vcc, s62, v11
Mv 124,48,100
Rb 36,8,41984
s_add_u32 s55, s55, 0x10000
Mv 124,52,108
Rb 40,8,43008
Mv 120,52,104
Rb 44,8,44032
buffer_load_dwordx4 v3, s[20:23], s56 offen offset:3072 sc1 lds
Mv 120,56,112
s_cmp_eq_u32 s57, 2
s_cselect_b32 s55, s59, s55
Mv 124,56,116
s_add_u32 s56, s56, 0x10000
Mv 124,60,124
s_waitcnt lgkmcnt(8)
s_cbranch_vccnz .Lzfx10
)" R"(
.Lzfr10:
Mv 120,60,120
v_add_f32_e32 v12, v12, v14
v_add_f32_e32 v13, v13, v15
Mp 128,0,32
Rb 48,8,45056
Mp 136,0,36
s_cmp_le_i32 s57, s58
s_cbranch_scc1 .Lzmk9
.Lzmr9:
Mp 140,4,36
Rb 52,8,46080
s_add_u32 m0, s36, 32768
Mp 132,4,32
buffer_load_dwordx4 v2, s[20:23], s55 offen offset:0 sc1 lds
Mp 144,8,32
Rb 56,8,47104
Mp 152,8,36
v_exp_f32_e32 v48, v96
Mp 156,12,36
Rb 60,8,48128
s_waitcnt lgkmcnt(8)
Mp 148,12,32
v_exp_f32_e32 v26, v97
Mp 160,16,32
Rb 0,8,49152
v_add_f32_e32 v14, v48, v26
Mp 168,16,36
v_exp_f32_e32 v49, v98
Mp 172,20,36
Rb 4,8,50176
Cv 48,48,26
Mp 164,20,32
buffer_load_dwordx4 v2, s[20:23], s55 offen offset:1024 sc1 lds
Mp 176,24,32
Rb 8,8,51200
v_add_f32_e32 v14, v14, v49
Mp 184,24,36
v_exp_f32_e32 v27, v99
Mp 188,28,36
Rb 12,8,52224
s_waitcnt lgkmcnt(8)
Mp 180,28,32
v_exp_f32_e32 v50, v104
Mp 192,32,32
Rb 16,8,53248
v_add_f32_e32 v14, v14, v27
Mp 200,32,36
v_exp_f32_e32 v28, v105
Mp 204,36,36
Rb 20,8,54272
Cv 49,49,27
Mp 196,36,32
buffer_load_dwordx4 v2, s[20:23], s55 offen offset:2048 sc1 lds
Mp 208,40,32
Rb 24,8,55296
v_add_f32_e32 v14, v14, v50
Mp 216,40,36
v_exp_f32_e32 v51, v106
Mp 220,44,36
Rb 28,8,56320
s_waitcnt lgkmcnt(8)
Mp 212,44,32
v_exp_f32_e32 v29, v107
)" R"(
Mp 224,48,32
Rb 32,8,57344
v_add_f32_e32 v14, v14, v28
Mp 232,48,36
v_exp_f32_e32 v52, v100
Mp 236,52,36
Rb 36,8,58368
Cv 50,50,28
Mp 228,52,32
buffer_load_dwordx4 v2, s[20:23], s55 offen offset:3072 sc1 lds
Mp 240,56,32
Rb 40,8,59392
v_add_f32_e32 v14, v14, v51
Mp 248,56,36
v_exp_f32_e32 v24, v101
Mp 252,60,36
Rb 44,8,60416
s_waitcnt lgkmcnt(8)
Mp 244,60,32
v_exp_f32_e32 v53, v102
Mp 128,0,40
Rb 48,8,61440
v_add_f32_e32 v14, v14, v29
Mp 136,0,44
v_exp_f32_e32 v25, v103
Mp 140,4,44
Rb 52,8,62464
s_add_u32 m0, s36, 36864
Mp 132,4,40
buffer_load_dwordx4 v3, s[20:23], s55 offen offset:0 sc1 lds
Mp 144,8,40
Rb 56,8,63488
Cv 51,51,29
Mp 152,8,44
v_exp_f32_e32 v54, v108
Mp 156,12,44
Rb 60,8,64512
s_waitcnt lgkmcnt(8)
Mp 148,12,40
v_exp_f32_e32 v26, v109
Mp 160,16,40
v_exp_f32_e32 v55, v110
Mp 168,16,44
v_exp_f32_e32 v27, v111
Mp 172,20,44
v_add_f32_e32 v15, v52, v24
Cv 52,52,24
Mp 164,20,40
buffer_load_dwordx4 v3, s[20:23], s55 offen offset:1024 sc1 lds
Mp 176,24,40
v_add_f32_e32 v15, v15, v53
v_add_f32_e32 v15, v15, v25
Mp 184,24,44
Cv 53,53,25
v_add_f32_e32 v15, v15, v54
Mp 188,28,44
v_add_f32_e32 v15, v15, v26
Cv 54,54,26
Mp 180,28,40
s_waitcnt vmcnt(8) lgkmcnt(0)
s_barrier
buffer_store_dword v6, v7, s[20:23], s49 offen offset:12 sc0 sc1
Mp 192,32,40
Rb 0,1,0
v_add_f32_e32 v15, v15, v55
)" R"(
Mp 200,32,44
Rb 4,1,1024
v_add_f32_e32 v15, v15, v27
Mp 204,36,44
Rb 8,1,2048
Cv 55,55,27
Mp 196,36,40
Rb 12,1,3072
buffer_load_dwordx4 v3, s[20:23], s55 offen offset:2048 sc1 lds
Mp 208,40,40
Rb 16,1,8192
s_sub_u32 s57, s57, 1
Mp 216,40,44
Rb 20,1,9216
Mp 220,44,44
Rb 24,1,10240
Mp 212,44,40
Rb 28,1,11264
Mp 224,48,40
Rb 32,1,4096
Mp 232,48,44
Rb 36,1,5120
Mp 236,52,44
Rb 40,1,6144
Mp 228,52,40
Rb 44,1,7168
buffer_load_dwordx4 v3, s[20:23], s55 offen offset:3072 sc1 lds
Mp 240,56,40
v_cmp_eq_u32_e32 vcc, s3, v4
s_andn2_b64 s[28:29], s[60:61], vcc
s_cbranch_scc1 .Lzsp16
.Lzok16:
Mp 248,56,44
Mp 252,60,44
s_waitcnt lgkmcnt(8)
Mp 244,60,40
s_branch .LzL
.LzEB:
v_mfma_f32_16x16x32_bf16 v[64:67], a[0:3], a[64:67], v[16:19]
Rb 48,1,12288
v_mfma_f32_16x16x32_bf16 v[68:71], a[0:3], a[68:71], v[20:23]
s_cmp_lg_u32 s35, 7
s_cselect_b64 s[60:61], 1, 0
Mv 68,4,76
Rb 52,1,13312
s_add_u32 m0, s36, 98304
Mv 64,4,72
buffer_load_dwordx4 v2, s[20:23], s56 offen offset:0 sc1 lds
Mv 64,8,80
Rb 56,1,14336
Mv 68,8,84
v_exp_f32_e32 v56, v112
Mv 68,12,92
Rb 60,1,15360
s_waitcnt lgkmcnt(8)
Mv 64,12,88
v_exp_f32_e32 v28, v113
Mv 64,16,96
Rb 0,1,16384
v_add_f32_e32 v14, v14, v56
Mv 68,16,100
v_exp_f32_e32 v57, v114
Mv 68,20,108
Rb 4,1,17408
)" R"(
v_add_f32_e32 v14, v14, v28
Mv 64,20,104
buffer_load_dwordx4 v2, s[20:23], s56 offen offset:1024 sc1 lds
Mv 64,24,112
Rb 8,1,18432
Cv 56,56,28
Mv 68,24,116
v_exp_f32_e32 v29, v115
Mv 68,28,124
Rb 12,1,19456
s_waitcnt lgkmcnt(8)
Mv 64,28,120
v_exp_f32_e32 v58, v120
v_mfma_f32_16x16x32_bf16 v[72:75], a[32:35], a[64:67], v[16:19]
Rb 16,1,24576
v_add_f32_e32 v14, v14, v57
v_mfma_f32_16x16x32_bf16 v[76:79], a[32:35], a[68:71], v[20:23]
v_exp_f32_e32 v24, v121
Mv 76,36,76
Rb 20,1,25600
v_add_f32_e32 v14, v14, v29
Mv 72,36,72
buffer_load_dwordx4 v2, s[20:23], s56 offen offset:2048 sc1 lds
Mv 72,40,80
Rb 24,1,26624
Cv 57,57,29
Mv 76,40,84
v_exp_f32_e32 v59, v122
Mv 76,44,92
Rb 28,1,27648
s_waitcnt lgkmcnt(8)
Mv 72,44,88
v_exp_f32_e32 v25, v123
Mv 72,48,96
Rb 32,1,20480
v_add_f32_e32 v14, v14, v58
Mv 76,48,100
v_exp_f32_e32 v60, v116
Mv 76,52,108
Rb 36,1,21504
v_add_f32_e32 v14, v14, v24
Mv 72,52,104
buffer_load_dwordx4 v2, s[20:23], s56 offen offset:3072 sc1 lds
Mv 72,56,112
Rb 40,1,22528
Cv 58,58,24
Mv 76,56,116
v_exp_f32_e32 v26, v117
Mv 76,60,124
Rb 44,1,23552
s_waitcnt lgkmcnt(8)
Mv 72,60,120
v_exp_f32_e32 v61, v118
v_mfma_f32_16x16x32_bf16 v[80:83], a[0:3], a[64:67], v[16:19]
Rb 48,1,28672
v_add_f32_e32 v14, v14, v59
v_mfma_f32_16x16x32_bf16 v[84:87], a[0:3], a[68:71], v[20:23]
v_exp_f32_e32 v27, v119
Mv 84,4,76
Rb 52,1,29696
s_add_u32 m0, s36, 102400
Mv 80,4,72
buffer_load_dwordx4 v3, s[20:23], s56 offen offset:0 sc1 lds
Mv 80,8,80
)" R"(
Rb 56,1,30720
v_add_f32_e32 v14, v14, v25
Mv 84,8,84
v_exp_f32_e32 v62, v124
Mv 84,12,92
Rb 60,1,31744
s_waitcnt lgkmcnt(8)
Mv 80,12,88
v_exp_f32_e32 v28, v125
Mv 80,16,96
v_exp_f32_e32 v63, v126
Mv 84,16,100
v_exp_f32_e32 v29, v127
Mv 84,20,108
Cv 59,59,25
v_add_f32_e32 v15, v15, v60
Mv 80,20,104
buffer_load_dwordx4 v3, s[20:23], s56 offen offset:1024 sc1 lds
Mv 80,24,112
v_add_f32_e32 v15, v15, v26
Cv 60,60,26
Mv 84,24,116
v_add_f32_e32 v15, v15, v61
v_add_f32_e32 v15, v15, v27
Mv 84,28,124
Cv 61,61,27
v_add_f32_e32 v15, v15, v62
Mv 80,28,120
s_waitcnt vmcnt(14) lgkmcnt(0)
s_barrier
v_mfma_f32_16x16x32_bf16 v[88:91], a[32:35], a[64:67], v[16:19]
Rb 0,8,0
v_add_f32_e32 v15, v15, v28
v_mfma_f32_16x16x32_bf16 v[92:95], a[32:35], a[68:71], v[20:23]
Rb 4,8,1024
Cv 62,62,28
Mv 92,36,76
Rb 8,8,2048
v_add_f32_e32 v15, v15, v63
Mv 88,36,72
Rb 12,8,3072
buffer_load_dwordx4 v3, s[20:23], s56 offen offset:2048 sc1 lds
Mv 88,40,80
Rb 16,8,4096
v_add_f32_e32 v15, v15, v29
Mv 92,40,84
Rb 20,8,5120
Cv 63,63,29
Mv 92,44,92
Rb 24,8,6144
v_max_f32_e32 v11, v14, v15
Mv 88,44,88
Rb 28,8,7168
v_cmp_nge_f32_e32 vcc, s62, v11
Mv 88,48,96
Rb 32,8,8192
s_add_u32 s55, s55, 0x10000
Mv 92,48,100
Rb 36,8,9216
Mv 92,52,108
Rb 40,8,10240
Mv 88,52,104
Rb 44,8,11264
buffer_load_dwordx4 v3, s[20:23], s56 offen offset:3072 sc1 lds
Mv 88,56,112
)" R"(
s_cmp_eq_u32 s57, 2
s_cselect_b32 s55, s59, s55
Mv 92,56,116
s_add_u32 s56, s56, 0x10000
Mv 92,60,124
s_waitcnt lgkmcnt(8)
s_cbranch_vccnz .Lzfx11
.Lzfr11:
Mv 88,60,120
v_add_f32_e32 v12, v12, v14
v_add_f32_e32 v13, v13, v15
Mp 128,0,48
Rb 48,8,12288
Mp 136,0,52
s_cmp_le_i32 s57, s58
s_cbranch_scc1 .Lzmk10
.Lzmr10:
Mp 140,4,52
Rb 52,8,13312
s_add_u32 m0, s36, 0
Mp 132,4,48
buffer_load_dwordx4 v2, s[20:23], s55 offen offset:0 sc1 lds
Mp 144,8,48
Rb 56,8,14336
Mp 152,8,52
v_exp_f32_e32 v32, v64
Mp 156,12,52
Rb 60,8,15360
s_waitcnt lgkmcnt(8)
Mp 148,12,48
v_exp_f32_e32 v24, v65
Mp 160,16,48
Rb 0,8,16384
v_add_f32_e32 v14, v32, v24
Mp 168,16,52
v_exp_f32_e32 v33, v66
Cv 32,32,24
Mp 172,20,52
Rb 4,8,17408
v_add_f32_e32 v14, v14, v33
Mp 164,20,48
buffer_load_dwordx4 v2, s[20:23], s55 offen offset:1024 sc1 lds
Mp 176,24,48
Rb 8,8,18432
Mp 184,24,52
v_exp_f32_e32 v25, v67
Mp 188,28,52
Rb 12,8,19456
s_waitcnt lgkmcnt(8)
Mp 180,28,48
v_exp_f32_e32 v34, v72
Mp 192,32,48
Rb 16,8,20480
v_add_f32_e32 v14, v14, v25
Mp 200,32,52
v_exp_f32_e32 v26, v73
Cv 33,33,25
Mp 204,36,52
Rb 20,8,21504
v_add_f32_e32 v14, v14, v34
Mp 196,36,48
buffer_load_dwordx4 v2, s[20:23], s55 offen offset:2048 sc1 lds
Mp 208,40,48
Rb 24,8,22528
)" R"(
v_add_f32_e32 v14, v14, v26
Mp 216,40,52
v_exp_f32_e32 v35, v74
Mp 220,44,52
Rb 28,8,23552
s_waitcnt lgkmcnt(8)
Mp 212,44,48
v_exp_f32_e32 v27, v75
Mp 224,48,48
Rb 32,8,24576
Cv 34,34,26
Mp 232,48,52
v_exp_f32_e32 v36, v68
v_add_f32_e32 v14, v14, v35
Mp 236,52,52
Rb 36,8,25600
v_add_f32_e32 v14, v14, v27
Mp 228,52,48
buffer_load_dwordx4 v2, s[20:23], s55 offen offset:3072 sc1 lds
Mp 240,56,48
Rb 40,8,26624
Cv 35,35,27
Mp 248,56,52
v_exp_f32_e32 v28, v69
Mp 252,60,52
Rb 44,8,27648
s_waitcnt lgkmcnt(8)
Mp 244,60,48
v_exp_f32_e32 v37, v70
Mp 128,0,56
Rb 48,8,28672
v_add_f32_e32 v15, v36, v28
Mp 136,0,60
v_exp_f32_e32 v29, v71
Cv 36,36,28
Mp 140,4,60
Rb 52,8,29696
s_add_u32 m0, s36, 4096
Mp 132,4,56
buffer_load_dwordx4 v3, s[20:23], s55 offen offset:0 sc1 lds
Mp 144,8,56
Rb 56,8,30720
v_add_f32_e32 v15, v15, v37
Mp 152,8,60
v_exp_f32_e32 v38, v76
Mp 156,12,60
Rb 60,8,31744
s_waitcnt lgkmcnt(8)
Mp 148,12,56
v_exp_f32_e32 v24, v77
Mp 160,16,56
v_exp_f32_e32 v39, v78
Mp 168,16,60
v_exp_f32_e32 v25, v79
v_add_f32_e32 v15, v15, v29
Mp 172,20,60
Cv 37,37,29
v_add_f32_e32 v15, v15, v38
Mp 164,20,56
buffer_load_dwordx4 v3, s[20:23], s55 offen offset:1024 sc1 lds
Mp 176,24,56
v_add_f32_e32 v15, v15, v24
Cv 38,38,24
Mp 184,24,60
)" R"(
v_add_f32_e32 v15, v15, v39
v_add_f32_e32 v15, v15, v25
Mp 188,28,60
Cv 39,39,25
s_sub_u32 s57, s57, 1
Mp 180,28,56
s_waitcnt vmcnt(14) lgkmcnt(0)
s_barrier
Mp 192,32,56
Rb 0,1,32768
v_bfe_u32 v11, v0, 2, 2
Mp 200,32,60
Rb 4,1,33792
v_lshrrev_b32 v4, 4, v0
v_xor_b32 v11, v11, v4
Mp 204,36,60
Rb 8,1,34816
v_bfe_u32 v4, v0, 1, 1
Mp 196,36,56
Rb 12,1,35840
buffer_load_dwordx4 v3, s[20:23], s55 offen offset:2048 sc1 lds
Mp 208,40,56
Rb 16,1,40960
v_lshl_or_b32 v11, v11, 1, v4
Mp 216,40,60
Rb 20,1,41984
v_and_b32 v4, 1, v0
Mp 220,44,60
Rb 24,1,43008
v_mul_u32_u24 v4, 0xffff, v4
Mp 212,44,56
Rb 28,1,44032
v_add_u32 v4, 1, v4
Mp 224,48,56
Rb 32,1,36864
v_mul_u32_u24 v4, 0x3f80, v4
Mp 232,48,60
Rb 36,1,37888
v_cmp_eq_u32_e32 vcc, 0, v11
v_cmp_eq_u32_e64 s[28:29], 1, v11
Mp 236,52,60
Rb 40,1,38912
v_accvgpr_write_b32 a192, 0
Mp 228,52,56
Rb 44,1,39936
buffer_load_dwordx4 v3, s[20:23], s55 offen offset:3072 sc1 lds
Mp 240,56,56
v_accvgpr_write_b32 a193, 0
v_accvgpr_write_b32 a196, 0
Mp 248,56,60
v_accvgpr_write_b32 a197, 0
v_cndmask_b32_e32 v11, 0, v4, vcc
Mp 252,60,60
v_cndmask_b32_e64 v4, 0, v4, s[28:29]
s_waitcnt lgkmcnt(8)
Mp 244,60,56
v_accvgpr_write_b32 a194, v11
v_accvgpr_write_b32 a195, v4
v_mfma_f32_16x16x32_bf16 v[96:99], a[0:3], a[64:67], v[16:19]
Rb 48,1,45056
v_mfma_f32_16x16x32_bf16 v[100:103], a[0:3], a[68:71], v[20:23]
v_exp_f32_e32 v40, v80
Mv 100,4,76
Rb 52,1,46080
s_add_u32 m0, s36, 65536
)" R"(
Mv 96,4,72
buffer_load_dwordx4 v2, s[20:23], s56 offen offset:0 sc1 lds
Mv 96,8,80
Rb 56,1,47104
v_add_f32_e32 v14, v14, v40
Mv 100,8,84
v_exp_f32_e32 v26, v81
Mv 100,12,92
Rb 60,1,48128
s_waitcnt lgkmcnt(8)
Mv 96,12,88
v_exp_f32_e32 v41, v82
Mv 96,16,96
Rb 0,1,49152
v_add_f32_e32 v14, v14, v26
Mv 100,16,100
v_exp_f32_e32 v27, v83
Mv 100,20,108
Rb 4,1,50176
Cv 40,40,26
Mv 96,20,104
buffer_load_dwordx4 v2, s[20:23], s56 offen offset:1024 sc1 lds
Mv 96,24,112
Rb 8,1,51200
v_add_f32_e32 v14, v14, v41
Mv 100,24,116
v_exp_f32_e32 v42, v88
Mv 100,28,124
Rb 12,1,52224
s_waitcnt lgkmcnt(8)
Mv 96,28,120
v_exp_f32_e32 v28, v89
v_mfma_f32_16x16x32_bf16 v[104:107], a[32:35], a[64:67], v[16:19]
Rb 16,1,57344
v_add_f32_e32 v14, v14, v27
v_mfma_f32_16x16x32_bf16 v[108:111], a[32:35], a[68:71], v[20:23]
v_exp_f32_e32 v43, v90
Mv 108,36,76
Rb 20,1,58368
Cv 41,41,27
Mv 104,36,72
buffer_load_dwordx4 v2, s[20:23], s56 offen offset:2048 sc1 lds
Mv 104,40,80
Rb 24,1,59392
buffer_load_dword v4, v7, s[20:23], s65 offen offset:0 sc0 sc1
Mv 108,40,84
v_exp_f32_e32 v29, v91
Mv 108,44,92
Rb 28,1,60416
s_waitcnt lgkmcnt(8)
Mv 104,44,88
v_exp_f32_e32 v44, v84
Mv 104,48,96
Rb 32,1,53248
v_add_f32_e32 v14, v14, v42
Mv 108,48,100
v_exp_f32_e32 v24, v85
Mv 108,52,108
Rb 36,1,54272
v_add_f32_e32 v14, v14, v28
Mv 104,52,104
buffer_load_dwordx4 v2, s[20:23], s56 offen offset:3072 sc1 lds
Mv 104,56,112
Rb 40,1,55296
)" R"(
Cv 42,42,28
Mv 108,56,116
v_exp_f32_e32 v45, v86
Mv 108,60,124
Rb 44,1,56320
s_waitcnt lgkmcnt(8)
Mv 104,60,120
v_exp_f32_e32 v25, v87
v_mfma_f32_16x16x32_bf16 v[112:115], a[0:3], a[64:67], v[16:19]
Rb 48,1,61440
v_add_f32_e32 v14, v14, v43
v_mfma_f32_16x16x32_bf16 v[116:119], a[0:3], a[68:71], v[20:23]
v_exp_f32_e32 v46, v92
Mv 116,4,76
Rb 52,1,62464
s_add_u32 m0, s36, 69632
Mv 112,4,72
buffer_load_dwordx4 v3, s[20:23], s56 offen offset:0 sc1 lds
Mv 112,8,80
Rb 56,1,63488
v_add_f32_e32 v14, v14, v29
Mv 116,8,84
v_exp_f32_e32 v26, v93
Mv 116,12,92
Rb 60,1,64512
s_waitcnt lgkmcnt(8)
Mv 112,12,88
v_exp_f32_e32 v47, v94
Mv 112,16,96
v_exp_f32_e32 v27, v95
Mv 116,16,100
Cv 43,43,29
v_add_f32_e32 v15, v15, v44
Mv 116,20,108
v_add_f32_e32 v15, v15, v24
Cv 44,44,24
Mv 112,20,104
buffer_load_dwordx4 v3, s[20:23], s56 offen offset:1024 sc1 lds
Mv 112,24,112
v_add_f32_e32 v15, v15, v45
v_add_f32_e32 v15, v15, v25
Mv 116,24,116
Cv 45,45,25
v_add_f32_e32 v15, v15, v46
Mv 116,28,124
v_add_f32_e32 v15, v15, v26
Cv 46,46,26
Mv 112,28,120
s_waitcnt vmcnt(3) lgkmcnt(0)
s_barrier
v_mfma_f32_16x16x32_bf16 v[120:123], a[32:35], a[64:67], v[16:19]
Rb 0,8,32768
v_add_f32_e32 v15, v15, v47
v_mfma_f32_16x16x32_bf16 v[124:127], a[32:35], a[68:71], v[20:23]
Rb 4,8,33792
v_add_f32_e32 v15, v15, v27
Mv 124,36,76
Rb 8,8,34816
Cv 47,47,27
Mv 120,36,72
Rb 12,8,35840
buffer_load_dwordx4 v3, s[20:23], s56 offen offset:2048 sc1 lds
Mv 120,40,80
Rb 16,8,36864
v_max_f32_e32 v11, v14, v15
)" R"(
Mv 124,40,84
Rb 20,8,37888
v_cmp_nge_f32_e32 vcc, s62, v11
Mv 124,44,92
Rb 24,8,38912
s_add_u32 s55, s55, 0x10000
Mv 120,44,88
Rb 28,8,39936
Mv 120,48,96
Rb 32,8,40960
Mv 124,48,100
Rb 36,8,41984
Mv 124,52,108
Rb 40,8,43008
Mv 120,52,104
Rb 44,8,44032
buffer_load_dwordx4 v3, s[20:23], s56 offen offset:3072 sc1 lds
Mv 120,56,112
s_cmp_eq_u32 s57, 2
s_cselect_b32 s55, s59, s55
Mv 124,56,116
s_add_u32 s56, s56, 0x10000
Mv 124,60,124
s_waitcnt lgkmcnt(8)
s_cbranch_vccnz .Lzfx12
.Lzfr12:
Mv 120,60,120
v_cmp_eq_u32_e32 vcc, s3, v4
s_andn2_b64 s[28:29], s[60:61], vcc
s_cbranch_scc1 .Lzsp17
.Lzok17:
Mp 128,0,32
Rb 48,8,45056
v_add_f32_e32 v12, v12, v14
Mp 136,0,36
v_add_f32_e32 v13, v13, v15
s_cmp_le_i32 s57, s58
s_cbranch_scc1 .Lzmk11
.Lzmr11:
Mp 140,4,36
Rb 52,8,46080
s_add_u32 m0, s36, 32768
buffer_load_dwordx4 v[64:67], v1, s[20:23], s66 offen sc0 sc1
Mp 132,4,32
buffer_load_dwordx4 v2, s[20:23], s55 offen offset:0 sc1 lds
Mp 144,8,32
Rb 56,8,47104
v_exp_f32_e32 v48, v96
Mp 152,8,36
buffer_load_dwordx4 a[128:131], v2, s[20:23], s64 offen offset:0 sc0 sc1
Mp 156,12,36
Rb 60,8,48128
s_waitcnt lgkmcnt(8)
Mp 148,12,32
v_exp_f32_e32 v28, v97
Mp 160,16,32
Rb 0,8,49152
buffer_load_dwordx4 a[132:135], v2, s[20:23], s64 offen offset:1024 sc0 sc1
Mp 168,16,36
v_exp_f32_e32 v49, v98
v_add_f32_e32 v14, v48, v28
Mp 172,20,36
Rb 4,8,50176
Cv 48,48,28
)" R"(
Mp 164,20,32
buffer_load_dwordx4 v2, s[20:23], s55 offen offset:1024 sc1 lds
Mp 176,24,32
Rb 8,8,51200
v_exp_f32_e32 v29, v99
Mp 184,24,36
buffer_load_dwordx4 a[136:139], v2, s[20:23], s64 offen offset:2048 sc0 sc1
Mp 188,28,36
Rb 12,8,52224
s_waitcnt lgkmcnt(8)
Mp 180,28,32
v_exp_f32_e32 v50, v104
v_add_f32_e32 v14, v14, v49
Mp 192,32,32
Rb 16,8,53248
buffer_load_dwordx4 a[140:143], v2, s[20:23], s64 offen offset:3072 sc0 sc1
Mp 200,32,36
v_exp_f32_e32 v24, v105
v_add_f32_e32 v14, v14, v29
Mp 204,36,36
Rb 20,8,54272
Cv 49,49,29
Mp 196,36,32
buffer_load_dwordx4 v2, s[20:23], s55 offen offset:2048 sc1 lds
Mp 208,40,32
Rb 24,8,55296
v_exp_f32_e32 v51, v106
Mp 216,40,36
buffer_load_dwordx4 a[144:147], v3, s[20:23], s64 offen offset:0 sc0 sc1
Mp 220,44,36
Rb 28,8,56320
s_waitcnt lgkmcnt(8)
Mp 212,44,32
v_exp_f32_e32 v25, v107
v_add_f32_e32 v14, v14, v50
Mp 224,48,32
Rb 32,8,57344
buffer_load_dwordx4 a[148:151], v3, s[20:23], s64 offen offset:1024 sc0 sc1
Mp 232,48,36
v_exp_f32_e32 v52, v100
v_add_f32_e32 v14, v14, v24
Mp 236,52,36
Rb 36,8,58368
Cv 50,50,24
Mp 228,52,32
buffer_load_dwordx4 v2, s[20:23], s55 offen offset:3072 sc1 lds
Mp 240,56,32
Rb 40,8,59392
v_exp_f32_e32 v26, v101
Mp 248,56,36
buffer_load_dwordx4 a[152:155], v3, s[20:23], s64 offen offset:2048 sc0 sc1
Mp 252,60,36
Rb 44,8,60416
s_waitcnt lgkmcnt(8)
Mp 244,60,32
v_exp_f32_e32 v53, v102
v_add_f32_e32 v14, v14, v51
Mp 128,0,40
Rb 48,8,61440
buffer_load_dwordx4 a[156:159], v3, s[20:23], s64 offen offset:3072 sc0 sc1
Mp 136,0,44
v_exp_f32_e32 v27, v103
v_add_f32_e32 v14, v14, v25
Mp 140,4,44
)" R"(
Rb 52,8,62464
s_add_u32 m0, s36, 36864
Mp 132,4,40
buffer_load_dwordx4 v3, s[20:23], s55 offen offset:0 sc1 lds
Mp 144,8,40
Rb 56,8,63488
v_exp_f32_e32 v54, v108
Mp 152,8,44
s_add_u32 s64, s64, 0x10000
buffer_load_dwordx4 a[160:163], v2, s[20:23], s64 offen offset:0 sc0 sc1
Mp 156,12,44
Rb 60,8,64512
s_waitcnt lgkmcnt(8)
Mp 148,12,40
v_exp_f32_e32 v28, v109
Cv 51,51,25
Mp 160,16,40
buffer_load_dwordx4 a[164:167], v2, s[20:23], s64 offen offset:1024 sc0 sc1
Mp 168,16,44
v_exp_f32_e32 v55, v110
v_add_f32_e32 v15, v52, v26
Mp 172,20,44
v_exp_f32_e32 v29, v111
Mp 164,20,40
buffer_load_dwordx4 v3, s[20:23], s55 offen offset:1024 sc1 lds
Mp 176,24,40
Cv 52,52,26
v_add_f32_e32 v15, v15, v53
v_add_f32_e32 v15, v15, v27
Mp 184,24,44
buffer_load_dwordx4 a[168:171], v2, s[20:23], s64 offen offset:2048 sc0 sc1
Mp 188,28,44
Cv 53,53,27
v_add_f32_e32 v15, v15, v54
Mp 180,28,40
v_add_f32_e32 v15, v15, v28
s_waitcnt vmcnt(18) lgkmcnt(0)
s_barrier
Mp 192,32,40
Rb 0,8,0
buffer_load_dwordx4 a[172:175], v2, s[20:23], s64 offen offset:3072 sc0 sc1
Mp 200,32,44
Rb 4,8,16384
Cv 54,54,28
v_add_f32_e32 v15, v15, v55
Mp 204,36,44
Rb 8,8,1024
v_add_f32_e32 v15, v15, v29
Mp 196,36,40
Rb 12,8,17408
buffer_load_dwordx4 v3, s[20:23], s55 offen offset:2048 sc1 lds
Mp 208,40,40
Rb 16,8,2048
Cv 55,55,29
s_sub_u32 s57, s57, 1
Mp 216,40,44
Rb 20,8,18432
buffer_load_dwordx4 a[176:179], v3, s[20:23], s64 offen offset:0 sc0 sc1
Mp 220,44,44
Rb 24,8,3072
Mp 212,44,40
Rb 28,8,19456
Mp 224,48,40
Rb 32,8,4096
buffer_load_dwordx4 a[180:183], v3, s[20:23], s64 offen offset:1024 sc0 sc1
)" R"(
Mp 232,48,44
Rb 36,8,20480
Mp 236,52,44
Rb 40,8,5120
Mp 228,52,40
Rb 44,8,21504
buffer_load_dwordx4 v3, s[20:23], s55 offen offset:3072 sc1 lds
Mp 240,56,40
buffer_load_dwordx4 a[188:191], v3, s[20:23], s64 offen offset:3072 sc0 sc1
Mp 248,56,44
buffer_load_dwordx4 a[184:187], v3, s[20:23], s64 offen offset:2048 sc0 sc1
Mp 252,60,44
Mp 244,60,40
s_waitcnt vmcnt(18)
v_cmp_neq_f32_e32 vcc, v16, v66
v_cmp_neq_f32_e64 s[28:29], v20, v67
s_or_b64 vcc, vcc, s[28:29]
s_cbranch_scc1 .LzMRG
.LzMRR:
Mv 128,194,128
v_exp_f32_e32 v56, v112
Mv 136,194,132
v_exp_f32_e32 v26, v113
v_add_f32_e32 v12, v12, v64
Mv 132,192,128
s_add_u32 m0, s36, 131072
s_add_u32 s40, s67, 0x20000
v_add_f32_e32 v13, v13, v65
Mv 140,192,132
buffer_load_dwordx4 v2, s[20:23], s40 offen offset:0 sc1 lds
Mv 144,194,136
v_exp_f32_e32 v57, v114
v_add_f32_e32 v14, v14, v56
Mv 152,194,140
v_exp_f32_e32 v27, v115
v_add_f32_e32 v14, v14, v26
Mv 148,192,136
Cv 56,56,26
s_waitcnt vmcnt(13)
Mv 156,192,140
buffer_load_dwordx4 v2, s[20:23], s40 offen offset:1024 sc1 lds
v_add_f32_e32 v14, v14, v57
Mv 160,194,144
v_exp_f32_e32 v58, v120
v_add_f32_e32 v14, v14, v27
Mv 168,194,148
v_exp_f32_e32 v28, v121
Mv 164,192,144
v_exp_f32_e32 v59, v122
Cv 57,57,27
Mv 172,192,148
buffer_load_dwordx4 v2, s[20:23], s40 offen offset:2048 sc1 lds
v_add_f32_e32 v14, v14, v58
Mv 176,194,152
v_exp_f32_e32 v29, v123
Mv 184,194,156
v_exp_f32_e32 v60, v116
v_add_f32_e32 v14, v14, v28
Mv 180,192,152
v_exp_f32_e32 v24, v117
s_waitcnt vmcnt(9)
Mv 188,192,156
buffer_load_dwordx4 v2, s[20:23], s40 offen offset:3072 sc1 lds
Cv 58,58,28
)" R"(
Mv 192,194,160
v_exp_f32_e32 v61, v118
Mv 200,194,164
v_exp_f32_e32 v25, v119
v_add_f32_e32 v14, v14, v59
Mv 196,192,160
s_add_u32 m0, s36, 135168
v_exp_f32_e32 v62, v124
Mv 204,192,164
buffer_load_dwordx4 v3, s[20:23], s40 offen offset:0 sc1 lds
Mv 208,194,168
v_exp_f32_e32 v26, v125
v_add_f32_e32 v14, v14, v29
Mv 216,194,172
v_exp_f32_e32 v63, v126
Cv 59,59,29
Mv 212,192,168
v_add_f32_e32 v15, v15, v60
s_waitcnt vmcnt(5)
Mv 220,192,172
buffer_load_dwordx4 v3, s[20:23], s40 offen offset:1024 sc1 lds
v_add_f32_e32 v15, v15, v24
Mv 224,194,176
v_exp_f32_e32 v27, v127
Cv 60,60,24
Mv 232,194,180
v_add_f32_e32 v15, v15, v61
v_add_f32_e32 v15, v15, v25
Mv 228,192,176
Cv 61,61,25
v_add_f32_e32 v15, v15, v62
v_add_f32_e32 v15, v15, v26
Mv 236,192,180
buffer_load_dwordx4 v3, s[20:23], s40 offen offset:2048 sc1 lds
Cv 62,62,26
Mv 240,194,184
v_add_f32_e32 v15, v15, v63
v_add_f32_e32 v15, v15, v27
Mv 248,194,188
s_add_u32 s40, s67, 0x30000
Cv 63,63,27
v_max_f32_e32 v11, v14, v15
Mv 244,192,184
s_add_u32 m0, s36, 98304
v_cmp_nge_f32_e32 vcc, s62, v11
s_waitcnt lgkmcnt(8)
Mv 252,192,188
buffer_load_dwordx4 v2, s[20:23], s40 offen offset:0 sc1 lds
s_cbranch_vccnz .Lzfx13
.Lzfr13:
Mp 128,0,48
s_mov_b32 s56, 0xc108000
buffer_load_dword v4, v7, s[20:23], s56 offen offset:4 sc0 sc1
Rb 48,8,6144
v_add_f32_e32 v12, v12, v14
Mp 136,0,52
v_add_f32_e32 v13, v13, v15
v_mov_b32 v24, v12
Mp 136,4,60
Rb 52,8,22528
v_permlane32_swap_b32_e32 v12, v24
Mp 128,4,56
buffer_load_dwordx4 v2, s[20:23], s40 offen offset:1024 sc1 lds
Mp 132,8,48
)" R"(
Rb 56,8,7168
v_add_f32_e32 v12, v12, v24
Mp 140,8,52
v_mov_b32 v24, v12
v_mov_b32 v25, v13
Mp 140,12,60
Rb 60,8,23552
s_waitcnt lgkmcnt(8)
Mp 132,12,56
buffer_load_dwordx4 v2, s[20:23], s40 offen offset:2048 sc1 lds
Mp 144,16,48
Rb 0,8,8192
v_permlane16_swap_b32_e32 v12, v24
Mp 152,16,52
v_add_f32_e32 v12, v12, v24
v_permlane32_swap_b32_e32 v13, v25
Mp 152,20,60
Rb 4,8,24576
v_rcp_f32_e32 v12, v12
Mp 144,20,56
buffer_load_dwordx4 v2, s[20:23], s40 offen offset:3072 sc1 lds
Mp 148,24,48
Rb 8,8,9216
v_add_f32_e32 v13, v13, v25
Mp 156,24,52
s_add_u32 m0, s36, 102400
v_mov_b32 v25, v13
Mp 156,28,60
Rb 12,8,25600
s_waitcnt lgkmcnt(8)
Mp 148,28,56
buffer_load_dwordx4 v3, s[20:23], s40 offen offset:0 sc1 lds
v_permlane16_swap_b32_e32 v13, v25
Mp 160,32,48
Rb 16,8,10240
v_add_f32_e32 v13, v13, v25
Mp 168,32,52
v_rcp_f32_e32 v13, v13
Mp 168,36,60
Rb 20,8,26624
v_mul_f32_e32 v128, v12, v128
Mp 160,36,56
buffer_load_dwordx4 v3, s[20:23], s40 offen offset:1024 sc1 lds
Mp 164,40,48
Rb 24,8,11264
v_mul_f32_e32 v129, v12, v129
Mp 172,40,52
Cv 128,128,129
v_mul_f32_e32 v130, v12, v130
v_mul_f32_e32 v131, v12, v131
Mp 172,44,60
Rb 28,8,27648
s_waitcnt lgkmcnt(8)
Mp 164,44,56
buffer_load_dwordx4 v3, s[20:23], s40 offen offset:2048 sc1 lds
Mp 176,48,48
Rb 32,8,12288
Cv 129,130,131
Mp 184,48,52
v_mul_f32_e32 v132, v12, v132
v_mul_f32_e32 v133, v12, v133
Mp 184,52,60
Rb 36,8,28672
Cv 130,132,133
)" R"(
v_mul_f32_e32 v134, v12, v134
Mp 176,52,56
v_mul_f32_e32 v135, v12, v135
Cv 131,134,135
Mp 180,56,48
Rb 40,8,13312
v_mul_f32_e32 v136, v13, v136
Mp 188,56,52
v_mul_f32_e32 v137, v13, v137
Cv 136,136,137
Mp 188,60,60
Rb 44,8,29696
s_waitcnt lgkmcnt(8)
Mp 180,60,56
v_mul_f32_e32 v138, v13, v138
v_mul_f32_e32 v139, v13, v139
Cv 137,138,139
Mp 192,0,48
Rb 48,8,14336
v_mul_f32_e32 v140, v13, v140
Mp 200,0,52
v_mul_f32_e32 v141, v13, v141
Cv 138,140,141
Mp 200,4,60
Rb 52,8,30720
v_mul_f32_e32 v142, v13, v142
Mp 192,4,56
v_mul_f32_e32 v143, v13, v143
Cv 139,142,143
Mp 196,8,48
Rb 56,8,15360
v_mul_f32_e32 v144, v12, v144
Mp 204,8,52
v_mul_f32_e32 v145, v12, v145
Cv 144,144,145
v_mul_f32_e32 v146, v12, v146
Mp 204,12,60
Rb 60,8,31744
s_waitcnt lgkmcnt(8)
Mp 196,12,56
v_mul_f32_e32 v147, v12, v147
Cv 145,146,147
Mp 208,16,48
Rb 0,1,0
v_mul_f32_e32 v148, v12, v148
Mp 216,16,52
v_mul_f32_e32 v149, v12, v149
Cv 146,148,149
Mp 216,20,60
Rb 4,1,1024
v_mul_f32_e32 v150, v12, v150
v_mul_f32_e32 v151, v12, v151
Mp 208,20,56
Cv 147,150,151
v_mul_f32_e32 v152, v13, v152
Mp 212,24,48
Rb 8,1,2048
v_mul_f32_e32 v153, v13, v153
Mp 220,24,52
Cv 152,152,153
v_mul_f32_e32 v154, v13, v154
Mp 220,28,60
Rb 12,1,3072
s_waitcnt vmcnt(6) lgkmcnt(4)
s_barrier
)" R"(
Mp 212,28,56
s_add_u32 s40, s67, 0x40000
v_mul_f32_e32 v155, v13, v155
Cv 153,154,155
v_cmp_eq_u32_e32 vcc, s3, v4
s_andn2_b64 s[28:29], 1, vcc
s_cbranch_scc1 .Lzsp18
.Lzok18:
Mp 224,32,48
Rb 16,1,4096
s_add_u32 m0, s36, 65536
buffer_load_dword v4, v7, s[20:23], s56 offen sc0 sc1
Mp 232,32,52
buffer_load_dwordx4 v2, s[20:23], s40 offen offset:0 sc1 lds
Mp 232,36,60
Rb 20,1,5120
v_mul_f32_e32 v156, v13, v156
Mp 224,36,56
v_mul_f32_e32 v157, v13, v157
Cv 154,156,157
Mp 228,40,48
Rb 24,1,6144
v_mul_f32_e32 v158, v13, v158
Mp 236,40,52
v_mul_f32_e32 v159, v13, v159
Cv 155,158,159
v_mul_f32_e32 v160, v12, v160
Mp 236,44,60
Rb 28,1,8192
Mp 228,44,56
buffer_load_dwordx4 v2, s[20:23], s40 offen offset:1024 sc1 lds
Mp 240,48,48
Rb 32,1,9216
v_mul_f32_e32 v161, v12, v161
Mp 248,48,52
Cv 160,160,161
v_mul_f32_e32 v162, v12, v162
Mp 248,52,60
Rb 36,1,10240
v_mul_f32_e32 v163, v12, v163
Cv 161,162,163
Mp 240,52,56
v_mul_f32_e32 v164, v12, v164
v_mul_f32_e32 v165, v12, v165
Mp 244,56,48
Rb 40,1,11264
Cv 162,164,165
Mp 252,56,52
buffer_load_dwordx4 v2, s[20:23], s40 offen offset:2048 sc1 lds
Mp 252,60,60
Rb 44,1,12288
s_waitcnt lgkmcnt(8)
Mp 244,60,56
v_mul_f32_e32 v166, v12, v166
v_mul_f32_e32 v167, v12, v167
Cv 163,166,167
v_mfma_f32_16x16x32_bf16 v[32:35], a[0:3], v[128:131], 0
Rb 48,1,13312
v_mul_f32_e32 v168, v13, v168
v_mfma_f32_16x16x32_bf16 v[40:43], a[0:3], v[136:139], 0
v_mul_f32_e32 v169, v13, v169
Cv 168,168,169
v_mfma_f32_16x16x32_bf16 v[44:47], a[4:7], v[136:139], 0
Rb 52,1,14336
)" R"(
v_mul_f32_e32 v170, v13, v170
v_mfma_f32_16x16x32_bf16 v[36:39], a[4:7], v[128:131], 0
buffer_load_dwordx4 v2, s[20:23], s40 offen offset:3072 sc1 lds
v_mfma_f32_16x16x32_bf16 v[48:51], a[8:11], v[128:131], 0
Rb 56,1,16384
v_mul_f32_e32 v171, v13, v171
v_mfma_f32_16x16x32_bf16 v[56:59], a[8:11], v[136:139], 0
Cv 169,170,171
v_mul_f32_e32 v172, v13, v172
v_mul_f32_e32 v173, v13, v173
v_mfma_f32_16x16x32_bf16 v[60:63], a[12:15], v[136:139], 0
Rb 60,1,17408
s_waitcnt lgkmcnt(8)
v_mfma_f32_16x16x32_bf16 v[52:55], a[12:15], v[128:131], 0
Cv 170,172,173
v_mul_f32_e32 v174, v13, v174
v_mfma_f32_16x16x32_bf16 v[64:67], a[16:19], v[128:131], 0
Rb 0,1,18432
s_add_u32 m0, s36, 69632
v_mfma_f32_16x16x32_bf16 v[72:75], a[16:19], v[136:139], 0
buffer_load_dwordx4 v3, s[20:23], s40 offen offset:0 sc1 lds
v_mfma_f32_16x16x32_bf16 v[76:79], a[20:23], v[136:139], 0
Rb 4,1,19456
v_mul_f32_e32 v175, v13, v175
Cv 171,174,175
v_mfma_f32_16x16x32_bf16 v[68:71], a[20:23], v[128:131], 0
v_mul_f32_e32 v176, v12, v176
v_mul_f32_e32 v177, v12, v177
v_mfma_f32_16x16x32_bf16 v[80:83], a[24:27], v[128:131], 0
Rb 8,1,20480
Cv 176,176,177
v_mfma_f32_16x16x32_bf16 v[88:91], a[24:27], v[136:139], 0
v_mul_f32_e32 v178, v12, v178
v_mul_f32_e32 v179, v12, v179
v_mfma_f32_16x16x32_bf16 v[92:95], a[28:31], v[136:139], 0
Rb 12,1,21504
s_waitcnt lgkmcnt(8)
v_mfma_f32_16x16x32_bf16 v[84:87], a[28:31], v[128:131], 0
buffer_load_dwordx4 v3, s[20:23], s40 offen offset:1024 sc1 lds
Cv 177,178,179
Mp 32,32,144
Rb 16,1,22528
v_mul_f32_e32 v180, v12, v180
Mp 40,32,152
v_mul_f32_e32 v181, v12, v181
Cv 178,180,181
Mp 44,36,152
Rb 20,1,24576
v_mul_f32_e32 v182, v12, v182
Mp 36,36,144
v_mul_f32_e32 v183, v12, v183
Cv 179,182,183
Mp 48,40,144
Rb 24,1,25600
v_mul_f32_e32 v184, v13, v184
Mp 56,40,152
buffer_load_dwordx4 v3, s[20:23], s40 offen offset:2048 sc1 lds
v_mul_f32_e32 v185, v13, v185
Mp 60,44,152
Rb 28,1,26624
s_waitcnt lgkmcnt(8)
Mp 52,44,144
Cv 184,184,185
v_mul_f32_e32 v186, v13, v186
)" R"(
Mp 64,48,144
Rb 32,1,27648
v_mul_f32_e32 v187, v13, v187
Mp 72,48,152
Cv 185,186,187
v_mul_f32_e32 v188, v13, v188
Mp 76,52,152
Rb 36,1,28672
v_mul_f32_e32 v189, v13, v189
Cv 186,188,189
Mp 68,52,144
v_mul_f32_e32 v190, v13, v190
v_mul_f32_e32 v191, v13, v191
Mp 80,56,144
Rb 40,1,29696
Cv 187,190,191
Mp 88,56,152
v_mul_f32_e32 v192, v12, v192
v_mul_f32_e32 v193, v12, v193
Mp 92,60,152
Rb 44,1,30720
s_waitcnt lgkmcnt(8)
Mp 84,60,144
Cv 192,192,193
v_mul_f32_e32 v194, v12, v194
v_mul_f32_e32 v195, v12, v195
Mp 32,0,160
Rb 48,1,32768
Cv 193,194,195
Mp 40,0,168
v_mul_f32_e32 v196, v12, v196
v_mul_f32_e32 v197, v12, v197
Mp 44,4,168
Rb 52,1,33792
Cv 194,196,197
Mp 36,4,160
v_mul_f32_e32 v198, v12, v198
v_mul_f32_e32 v199, v12, v199
Mp 48,8,160
Rb 56,1,34816
Cv 195,198,199
Mp 56,8,168
v_mul_f32_e32 v200, v13, v200
v_mul_f32_e32 v201, v13, v201
Cv 200,200,201
Mp 60,12,168
Rb 60,1,35840
s_waitcnt lgkmcnt(4)
s_barrier
Mp 52,12,160
s_add_u32 s40, s67, 0x50000
v_mul_f32_e32 v202, v13, v202
Mp 64,16,160
Rb 0,1,36864
s_add_u32 m0, s36, 0
Mp 72,16,168
buffer_load_dwordx4 v2, s[20:23], s40 offen offset:0 sc1 lds
Mp 76,20,168
Rb 4,1,37888
v_mul_f32_e32 v203, v13, v203
Cv 201,202,203
Mp 68,20,160
v_mul_f32_e32 v204, v13, v204
v_mul_f32_e32 v205, v13, v205
Mp 80,24,160
)" R"(
Rb 8,1,38912
Cv 202,204,205
Mp 88,24,168
v_mul_f32_e32 v206, v13, v206
v_mul_f32_e32 v207, v13, v207
Mp 92,28,168
Rb 12,1,40960
Mp 84,28,160
buffer_load_dwordx4 v2, s[20:23], s40 offen offset:1024 sc1 lds
Cv 203,206,207
Mp 32,32,176
Rb 16,1,41984
v_mul_f32_e32 v208, v12, v208
Mp 40,32,184
v_mul_f32_e32 v209, v12, v209
Cv 208,208,209
Mp 44,36,184
Rb 20,1,43008
v_mul_f32_e32 v210, v12, v210
Mp 36,36,176
v_mul_f32_e32 v211, v12, v211
Cv 209,210,211
Mp 48,40,176
Rb 24,1,44032
v_mul_f32_e32 v212, v12, v212
Mp 56,40,184
buffer_load_dwordx4 v2, s[20:23], s40 offen offset:2048 sc1 lds
v_mul_f32_e32 v213, v12, v213
Mp 60,44,184
Rb 28,1,45056
s_waitcnt lgkmcnt(8)
Mp 52,44,176
Cv 210,212,213
v_mul_f32_e32 v214, v12, v214
Mp 64,48,176
Rb 32,1,46080
v_mul_f32_e32 v215, v12, v215
Mp 72,48,184
Cv 211,214,215
v_mul_f32_e32 v216, v13, v216
Mp 76,52,184
Rb 36,1,47104
v_mul_f32_e32 v217, v13, v217
Cv 216,216,217
Mp 68,52,176
buffer_load_dwordx4 v2, s[20:23], s40 offen offset:3072 sc1 lds
Mp 80,56,176
Rb 40,1,49152
v_mul_f32_e32 v218, v13, v218
Mp 88,56,184
v_mul_f32_e32 v219, v13, v219
Cv 217,218,219
Mp 92,60,184
Rb 44,1,50176
s_waitcnt lgkmcnt(8)
Mp 84,60,176
v_mul_f32_e32 v220, v13, v220
v_mul_f32_e32 v221, v13, v221
Cv 218,220,221
Mp 32,0,192
Rb 48,1,51200
s_add_u32 m0, s36, 4096
Mp 40,0,200
buffer_load_dwordx4 v3, s[20:23], s40 offen offset:0 sc1 lds
)" R"(
Mp 44,4,200
Rb 52,1,52224
v_mul_f32_e32 v222, v13, v222
Mp 36,4,192
v_mul_f32_e32 v223, v13, v223
Cv 219,222,223
Mp 48,8,192
Rb 56,1,53248
v_mul_f32_e32 v224, v12, v224
Mp 56,8,200
v_mul_f32_e32 v225, v12, v225
Cv 224,224,225
v_mul_f32_e32 v226, v12, v226
Mp 60,12,200
Rb 60,1,54272
s_waitcnt lgkmcnt(8)
Mp 52,12,192
buffer_load_dwordx4 v3, s[20:23], s40 offen offset:1024 sc1 lds
Mp 64,16,192
Rb 0,1,55296
v_mul_f32_e32 v227, v12, v227
Mp 72,16,200
Cv 225,226,227
v_mul_f32_e32 v228, v12, v228
Mp 76,20,200
Rb 4,1,57344
v_mul_f32_e32 v229, v12, v229
Cv 226,228,229
Mp 68,20,192
v_mul_f32_e32 v230, v12, v230
v_mul_f32_e32 v231, v12, v231
Mp 80,24,192
Rb 8,1,58368
Cv 227,230,231
Mp 88,24,200
buffer_load_dwordx4 v3, s[20:23], s40 offen offset:2048 sc1 lds
Mp 92,28,200
Rb 12,1,59392
s_waitcnt lgkmcnt(8)
Mp 84,28,192
v_mul_f32_e32 v232, v13, v232
v_mul_f32_e32 v233, v13, v233
Cv 232,232,233
Mp 32,32,208
Rb 16,1,60416
v_mul_f32_e32 v234, v13, v234
Mp 40,32,216
v_mul_f32_e32 v235, v13, v235
Cv 233,234,235
Mp 44,36,216
Rb 20,1,61440
v_mul_f32_e32 v236, v13, v236
Mp 36,36,208
v_mul_f32_e32 v237, v13, v237
Cv 234,236,237
Mp 48,40,208
Rb 24,1,62464
v_mul_f32_e32 v238, v13, v238
Mp 56,40,216
v_mul_f32_e32 v239, v13, v239
Cv 235,238,239
v_mul_f32_e32 v240, v12, v240
Mp 60,44,216
Rb 28,1,63488
)" R"(
s_waitcnt lgkmcnt(8)
Mp 52,44,208
v_mul_f32_e32 v241, v12, v241
Cv 240,240,241
Mp 64,48,208
Rb 32,10,0
v_mul_f32_e32 v242, v12, v242
Mp 72,48,216
v_mul_f32_e32 v243, v12, v243
Cv 241,242,243
Mp 76,52,216
Rb 36,10,1024
v_mul_f32_e32 v244, v12, v244
v_mul_f32_e32 v245, v12, v245
Mp 68,52,208
Cv 242,244,245
v_mul_f32_e32 v246, v12, v246
Mp 80,56,208
Rb 40,10,2048
v_mul_f32_e32 v247, v12, v247
Mp 88,56,216
Cv 243,246,247
v_mul_f32_e32 v248, v13, v248
Mp 92,60,216
Rb 44,10,3072
s_waitcnt vmcnt(14) lgkmcnt(4)
s_barrier
Mp 84,60,208
s_add_u32 s40, s67, 0x60000
v_mul_f32_e32 v249, v13, v249
Cv 248,248,249
v_readfirstlane_b32 s63, v4
Mp 32,0,224
Rb 48,10,4096
s_add_u32 m0, s36, 32768
Mp 40,0,232
buffer_load_dwordx4 v2, s[20:23], s40 offen offset:0 sc1 lds
Mp 44,4,232
Rb 52,10,5120
v_mul_f32_e32 v250, v13, v250
Mp 36,4,224
v_mul_f32_e32 v251, v13, v251
Cv 249,250,251
Mp 48,8,224
Rb 56,10,6144
v_mul_f32_e32 v252, v13, v252
Mp 56,8,232
v_mul_f32_e32 v253, v13, v253
Cv 250,252,253
v_mul_f32_e32 v254, v13, v254
Mp 60,12,232
Rb 60,10,8192
Mp 52,12,224
buffer_load_dwordx4 v2, s[20:23], s40 offen offset:1024 sc1 lds
Mp 64,16,224
Rb 0,10,9216
v_mul_f32_e32 v255, v13, v255
Mp 72,16,232
Cv 251,254,255
Mp 76,20,232
Rb 4,10,10240
Mp 68,20,224
Mp 80,24,224
Rb 8,10,11264
Mp 88,24,232
)" R"(
buffer_load_dwordx4 v2, s[20:23], s40 offen offset:2048 sc1 lds
Mp 92,28,232
Rb 12,10,12288
s_waitcnt lgkmcnt(8)
Mp 84,28,224
Mp 32,32,240
Rb 16,10,13312
Mp 40,32,248
Mp 44,36,248
Rb 20,10,14336
Mp 36,36,240
buffer_load_dwordx4 v2, s[20:23], s40 offen offset:3072 sc1 lds
Mp 48,40,240
Rb 24,10,16384
Mp 56,40,248
Mp 60,44,248
Rb 28,10,17408
s_waitcnt lgkmcnt(8)
Mp 52,44,240
Mp 64,48,240
Rb 32,10,18432
s_add_u32 m0, s36, 36864
Mp 72,48,248
buffer_load_dwordx4 v3, s[20:23], s40 offen offset:0 sc1 lds
Mp 76,52,248
Rb 36,10,19456
Mp 68,52,240
Mp 80,56,240
Rb 40,10,20480
Mp 88,56,248
Mp 92,60,248
Rb 44,10,21504
s_waitcnt lgkmcnt(8)
Mp 84,60,240
buffer_load_dwordx4 v3, s[20:23], s40 offen offset:1024 sc1 lds
v_mfma_f32_16x16x32_bf16 v[96:99], a[0:3], v[128:131], 0
Rb 48,10,22528
v_mfma_f32_16x16x32_bf16 v[104:107], a[0:3], v[136:139], 0
Mp 104,4,152
Rb 52,10,24576
Mp 96,4,144
Mp 96,8,160
Rb 56,10,25600
Cv 32,32,33
Mp 104,8,168
buffer_load_dwordx4 v3, s[20:23], s40 offen offset:2048 sc1 lds
Mp 104,12,184
Rb 60,10,26624
s_waitcnt lgkmcnt(8)
Mp 96,12,176
Cv 33,34,35
Cv 34,36,37
Mp 96,16,192
Rb 0,10,27648
Cv 35,38,39
Mp 104,16,200
s_add_u32 s29, s73, 0x0
buffer_store_dwordx4 v[32:35], v1, s[20:23], s29 offen sc0 sc1
Mp 104,20,216
Rb 4,10,28672
Mp 96,20,208
Mp 96,24,224
Rb 8,10,29696
Cv 40,40,41
)" R"(
Mp 104,24,232
Cv 41,42,43
Cv 42,44,45
Mp 104,28,248
Rb 12,10,30720
s_waitcnt lgkmcnt(8)
Mp 96,28,240
Cv 43,46,47
v_mfma_f32_16x16x32_bf16 v[100:103], a[32:35], v[128:131], 0
Rb 16,8,32768
v_mfma_f32_16x16x32_bf16 v[108:111], a[32:35], v[136:139], 0
s_add_u32 s29, s73, 0x400
buffer_store_dwordx4 v[40:43], v1, s[20:23], s29 offen sc0 sc1
Mp 108,36,152
Rb 20,8,33792
Mp 100,36,144
Mp 100,40,160
Rb 24,8,34816
Cv 48,48,49
Mp 108,40,168
Cv 49,50,51
Cv 50,52,53
Mp 108,44,184
Rb 28,8,35840
s_waitcnt vmcnt(16) lgkmcnt(4)
s_barrier
Mp 100,44,176
s_add_u32 s40, s67, 0x70000
Cv 51,54,55
Mp 100,48,192
Rb 32,8,36864
s_add_u32 m0, s36, 131072
Mp 108,48,200
buffer_load_dwordx4 v2, s[20:23], s40 offen offset:0 sc1 lds
Mp 108,52,216
Rb 36,8,37888
Mp 100,52,208
s_add_u32 s29, s73, 0x800
buffer_store_dwordx4 v[48:51], v1, s[20:23], s29 offen sc0 sc1
Mp 100,56,224
Rb 40,8,38912
Cv 56,56,57
Mp 108,56,232
Cv 57,58,59
Cv 58,60,61
Mp 108,60,248
Rb 44,8,40960
Mp 100,60,240
buffer_load_dwordx4 v2, s[20:23], s40 offen offset:1024 sc1 lds
v_mfma_f32_16x16x32_bf16 v[112:115], a[0:3], v[128:131], 0
Rb 48,8,41984
Cv 59,62,63
v_mfma_f32_16x16x32_bf16 v[120:123], a[0:3], v[136:139], 0
s_add_u32 s29, s73, 0xc00
buffer_store_dwordx4 v[56:59], v1, s[20:23], s29 offen sc0 sc1
Mp 120,4,152
Rb 52,8,43008
Mp 112,4,144
Mp 112,8,160
Rb 56,8,44032
Cv 96,96,97
Mp 120,8,168
buffer_load_dwordx4 v2, s[20:23], s40 offen offset:2048 sc1 lds
Mp 120,12,184
Rb 60,8,45056
)" R"(
s_waitcnt lgkmcnt(8)
Mp 112,12,176
Cv 97,98,99
Cv 98,100,101
Mp 112,16,192
Rb 0,8,46080
Cv 99,102,103
Mp 120,16,200
s_add_u32 s29, s73, 0x4400
buffer_store_dwordx4 v[96:99], v1, s[20:23], s29 offen sc0 sc1
Mp 120,20,216
Rb 4,8,47104
Mp 112,20,208
buffer_load_dwordx4 v2, s[20:23], s40 offen offset:3072 sc1 lds
Mp 112,24,224
Rb 8,8,49152
Cv 64,64,65
Mp 120,24,232
Cv 65,66,67
Cv 66,68,69
Mp 120,28,248
Rb 12,8,50176
s_waitcnt lgkmcnt(8)
Mp 112,28,240
Cv 67,70,71
v_mfma_f32_16x16x32_bf16 v[116:119], a[32:35], v[128:131], 0
Rb 16,8,51200
s_add_u32 m0, s36, 135168
v_mfma_f32_16x16x32_bf16 v[124:127], a[32:35], v[136:139], 0
buffer_load_dwordx4 v3, s[20:23], s40 offen offset:0 sc1 lds
Mp 124,36,152
Rb 20,8,52224
Mp 116,36,144
s_add_u32 s29, s73, 0x1000
buffer_store_dwordx4 v[64:67], v1, s[20:23], s29 offen sc0 sc1
Mp 116,40,160
Rb 24,8,53248
Cv 104,104,105
Mp 124,40,168
Cv 105,106,107
Cv 106,108,109
Mp 124,44,184
Rb 28,8,54272
s_waitcnt lgkmcnt(8)
Mp 116,44,176
buffer_load_dwordx4 v3, s[20:23], s40 offen offset:1024 sc1 lds
Mp 116,48,192
Rb 32,8,55296
Cv 107,110,111
Mp 124,48,200
s_add_u32 s29, s73, 0x4800
buffer_store_dwordx4 v[104:107], v1, s[20:23], s29 offen sc0 sc1
Mp 124,52,216
Rb 36,8,57344
Mp 116,52,208
Mp 116,56,224
Rb 40,8,58368
Cv 72,72,73
Mp 124,56,232
buffer_load_dwordx4 v3, s[20:23], s40 offen offset:2048 sc1 lds
Mp 124,60,248
Rb 44,8,59392
s_waitcnt lgkmcnt(8)
Mp 116,60,240
)" R"(
Cv 73,74,75
Cv 74,76,77
v_mfma_f32_16x16x32_bf16 v[96:99], a[0:3], v[128:131], 0
Rb 48,8,60416
Cv 75,78,79
v_mfma_f32_16x16x32_bf16 v[104:107], a[0:3], v[136:139], 0
s_add_u32 s29, s73, 0x1400
buffer_store_dwordx4 v[72:75], v1, s[20:23], s29 offen sc0 sc1
Mp 104,4,152
Rb 52,8,61440
Mp 96,4,144
Mp 96,8,160
Rb 56,8,62464
Cv 112,112,113
Mp 104,8,168
Cv 113,114,115
Cv 114,116,117
Mp 104,12,184
Rb 60,8,63488
s_waitcnt lgkmcnt(8)
Mp 96,12,176
Cv 115,118,119
Mp 96,16,192
Rb 0,8,0
Mp 104,16,200
s_add_u32 s29, s73, 0x4c00
buffer_store_dwordx4 v[112:115], v1, s[20:23], s29 offen sc0 sc1
Mp 104,20,216
Rb 4,8,1024
Mp 96,20,208
Mp 96,24,224
Rb 8,8,2048
Cv 80,80,81
Mp 104,24,232
Cv 81,82,83
Cv 82,84,85
Mp 104,28,248
Rb 12,8,3072
s_waitcnt vmcnt(23) lgkmcnt(8)
s_barrier
Mp 96,28,240
Cv 83,86,87
v_mfma_f32_16x16x32_bf16 v[100:103], a[32:35], v[128:131], 0
Rb 16,8,4096
v_mfma_f32_16x16x32_bf16 v[108:111], a[32:35], v[136:139], 0
s_add_u32 s29, s73, 0x1800
buffer_store_dwordx4 v[80:83], v1, s[20:23], s29 offen sc0 sc1
Mp 108,36,152
Rb 20,8,5120
Mp 100,36,144
Mp 100,40,160
Rb 24,8,6144
Cv 120,120,121
Mp 108,40,168
Cv 121,122,123
Cv 122,124,125
Mp 108,44,184
Rb 28,8,8192
s_waitcnt lgkmcnt(8)
Mp 100,44,176
Cv 123,126,127
Mp 100,48,192
Rb 32,8,9216
Mp 108,48,200
s_add_u32 s29, s73, 0x5000
)" R"(
buffer_store_dwordx4 v[120:123], v1, s[20:23], s29 offen sc0 sc1
Mp 108,52,216
Rb 36,8,10240
Mp 100,52,208
Mp 100,56,224
Rb 40,8,11264
Cv 88,88,89
Mp 108,56,232
Cv 89,90,91
Cv 90,92,93
Mp 108,60,248
Rb 44,8,12288
s_waitcnt lgkmcnt(8)
Mp 100,60,240
Cv 91,94,95
v_mfma_f32_16x16x32_bf16 v[112:115], a[0:3], v[128:131], 0
Rb 48,8,13312
v_mfma_f32_16x16x32_bf16 v[120:123], a[0:3], v[136:139], 0
s_add_u32 s29, s73, 0x4000
buffer_store_dwordx4 v[88:91], v1, s[20:23], s29 offen sc0 sc1
Mp 120,4,152
Rb 52,8,14336
Mp 112,4,144
Mp 112,8,160
Rb 56,8,16384
Cv 96,96,97
Mp 120,8,168
Cv 97,98,99
Cv 98,100,101
Mp 120,12,184
Rb 60,8,17408
s_waitcnt lgkmcnt(8)
Mp 112,12,176
Cv 99,102,103
Mp 112,16,192
Rb 0,8,18432
Mp 120,16,200
s_add_u32 s29, s73, 0x5400
buffer_store_dwordx4 v[96:99], v1, s[20:23], s29 offen sc0 sc1
Mp 120,20,216
Rb 4,8,19456
Mp 112,20,208
Mp 112,24,224
Rb 8,8,20480
Mp 120,24,232
Mp 120,28,248
Rb 12,8,21504
s_waitcnt lgkmcnt(8)
Mp 112,28,240
v_mfma_f32_16x16x32_bf16 v[116:119], a[32:35], v[128:131], 0
Rb 16,8,22528
v_mfma_f32_16x16x32_bf16 v[124:127], a[32:35], v[136:139], 0
Mp 124,36,152
Rb 20,8,24576
Mp 116,36,144
Mp 116,40,160
Rb 24,8,25600
Cv 104,104,105
Mp 124,40,168
Cv 105,106,107
Cv 106,108,109
Mp 124,44,184
Rb 28,8,26624
s_waitcnt lgkmcnt(8)
)" R"(
Mp 116,44,176
Cv 107,110,111
Mp 116,48,192
Rb 32,8,27648
Mp 124,48,200
s_add_u32 s29, s73, 0x5800
buffer_store_dwordx4 v[104:107], v1, s[20:23], s29 offen sc0 sc1
Mp 124,52,216
Rb 36,8,28672
Mp 116,52,208
Mp 116,56,224
Rb 40,8,29696
Mp 124,56,232
Mp 124,60,248
Rb 44,8,30720
s_waitcnt lgkmcnt(8)
Mp 116,60,240
v_mfma_f32_16x16x32_bf16 v[96:99], a[0:3], v[128:131], 0
Rb 48,1,0
v_mfma_f32_16x16x32_bf16 v[104:107], a[0:3], v[136:139], 0
Mp 104,4,152
Rb 52,1,1024
Mp 96,4,144
Mp 96,8,160
Rb 56,1,2048
Cv 112,112,113
Mp 104,8,168
Cv 113,114,115
Cv 114,116,117
Mp 104,12,184
Rb 60,1,3072
s_waitcnt vmcnt(21) lgkmcnt(8)
s_barrier
Mp 96,12,176
Cv 115,118,119
Mp 96,16,192
Rb 0,1,4096
Mp 104,16,200
s_add_u32 s29, s73, 0x8000
buffer_store_dwordx4 v[112:115], v1, s[20:23], s29 offen sc0 sc1
Mp 104,20,216
Rb 4,1,5120
Mp 96,20,208
Mp 96,24,224
Rb 8,1,6144
Mp 104,24,232
Mp 104,28,248
Rb 12,1,8192
s_waitcnt lgkmcnt(8)
Mp 96,28,240
v_mfma_f32_16x16x32_bf16 v[100:103], a[32:35], v[128:131], 0
Rb 16,1,9216
v_mfma_f32_16x16x32_bf16 v[108:111], a[32:35], v[136:139], 0
Mp 108,36,152
Rb 20,1,10240
Mp 100,36,144
Mp 100,40,160
Rb 24,1,11264
Cv 120,120,121
Mp 108,40,168
Cv 121,122,123
Cv 122,124,125
Mp 108,44,184
Rb 28,1,12288
s_waitcnt lgkmcnt(8)
)" R"(
Mp 100,44,176
Cv 123,126,127
Mp 100,48,192
Rb 32,1,13312
Mp 108,48,200
s_add_u32 s29, s73, 0x8400
buffer_store_dwordx4 v[120:123], v1, s[20:23], s29 offen sc0 sc1
Mp 108,52,216
Rb 36,1,14336
Mp 100,52,208
Mp 100,56,224
Rb 40,1,16384
Mp 108,56,232
Mp 108,60,248
Rb 44,1,17408
s_waitcnt lgkmcnt(8)
Mp 100,60,240
v_mfma_f32_16x16x32_bf16 v[112:115], a[0:3], v[128:131], 0
Rb 48,1,18432
v_mfma_f32_16x16x32_bf16 v[120:123], a[0:3], v[136:139], 0
Mp 120,4,152
Rb 52,1,19456
Mp 112,4,144
Mp 112,8,160
Rb 56,1,20480
Cv 96,96,97
Mp 120,8,168
Cv 97,98,99
Cv 98,100,101
Mp 120,12,184
Rb 60,1,21504
s_waitcnt vmcnt(2) lgkmcnt(8)
buffer_store_dword v6, v7, s[20:23], s68 offen offset:0 sc0 sc1
Mp 112,12,176
Cv 99,102,103
Mp 112,16,192
Rb 0,1,22528
Mp 120,16,200
s_add_u32 s29, s73, 0x8800
buffer_store_dwordx4 v[96:99], v1, s[20:23], s29 offen sc0 sc1
Mp 120,20,216
Rb 4,1,24576
Mp 112,20,208
Mp 112,24,224
Rb 8,1,25600
Mp 120,24,232
Mp 120,28,248
Rb 12,1,26624
s_waitcnt lgkmcnt(8)
Mp 112,28,240
v_mfma_f32_16x16x32_bf16 v[116:119], a[32:35], v[128:131], 0
Rb 16,1,27648
v_mfma_f32_16x16x32_bf16 v[124:127], a[32:35], v[136:139], 0
Mp 124,36,152
Rb 20,1,28672
Mp 116,36,144
Mp 116,40,160
Rb 24,1,29696
Cv 104,104,105
Mp 124,40,168
Cv 105,106,107
Cv 106,108,109
Mp 124,44,184
Rb 28,1,30720
)" R"(
s_waitcnt lgkmcnt(8)
Mp 116,44,176
Cv 107,110,111
Mp 116,48,192
Rb 32,1,32768
Mp 124,48,200
s_add_u32 s29, s73, 0x8c00
buffer_store_dwordx4 v[104:107], v1, s[20:23], s29 offen sc0 sc1
Mp 124,52,216
Rb 36,1,33792
Mp 116,52,208
Mp 116,56,224
Rb 40,1,34816
Mp 124,56,232
Mp 124,60,248
Rb 44,1,35840
s_waitcnt lgkmcnt(8)
s_barrier
Mp 116,60,240
v_mfma_f32_16x16x32_bf16 v[96:99], a[0:3], v[128:131], 0
Rb 48,1,36864
v_mfma_f32_16x16x32_bf16 v[104:107], a[0:3], v[136:139], 0
Mp 104,4,152
Rb 52,1,37888
Mp 96,4,144
Mp 96,8,160
Rb 56,1,38912
Cv 112,112,113
Mp 104,8,168
Cv 113,114,115
Cv 114,116,117
Mp 104,12,184
Rb 60,1,40960
s_waitcnt lgkmcnt(8)
Mp 96,12,176
Cv 115,118,119
Mp 96,16,192
Rb 0,1,41984
Mp 104,16,200
s_add_u32 s29, s73, 0x9000
buffer_store_dwordx4 v[112:115], v1, s[20:23], s29 offen sc0 sc1
Mp 104,20,216
Rb 4,1,43008
Mp 96,20,208
Mp 96,24,224
Rb 8,1,44032
Mp 104,24,232
Mp 104,28,248
Rb 12,1,45056
s_waitcnt lgkmcnt(8)
Mp 96,28,240
v_mfma_f32_16x16x32_bf16 v[100:103], a[32:35], v[128:131], 0
Rb 16,1,46080
v_mfma_f32_16x16x32_bf16 v[108:111], a[32:35], v[136:139], 0
Mp 108,36,152
Rb 20,1,47104
Mp 100,36,144
Mp 100,40,160
Rb 24,1,49152
Cv 120,120,121
Mp 108,40,168
Cv 121,122,123
Cv 122,124,125
Mp 108,44,184
Rb 28,1,50176
)" R"(
s_waitcnt lgkmcnt(8)
Mp 100,44,176
Cv 123,126,127
Mp 100,48,192
Rb 32,1,51200
Mp 108,48,200
s_add_u32 s29, s73, 0x9400
buffer_store_dwordx4 v[120:123], v1, s[20:23], s29 offen sc0 sc1
Mp 108,52,216
Rb 36,1,52224
Mp 100,52,208
Mp 100,56,224
Rb 40,1,53248
Mp 108,56,232
Mp 108,60,248
Rb 44,1,54272
s_waitcnt lgkmcnt(8)
Mp 100,60,240
v_mfma_f32_16x16x32_bf16 v[112:115], a[0:3], v[128:131], 0
Rb 48,1,55296
v_mfma_f32_16x16x32_bf16 v[120:123], a[0:3], v[136:139], 0
Mp 120,4,152
Rb 52,1,57344
Mp 112,4,144
Mp 112,8,160
Rb 56,1,58368
Cv 96,96,97
Mp 120,8,168
Cv 97,98,99
Cv 98,100,101
Mp 120,12,184
Rb 60,1,59392
s_waitcnt lgkmcnt(8)
Mp 112,12,176
Cv 99,102,103
Mp 112,16,192
Rb 0,1,60416
Mp 120,16,200
s_add_u32 s29, s73, 0x9800
buffer_store_dwordx4 v[96:99], v1, s[20:23], s29 offen sc0 sc1
Mp 120,20,216
Rb 4,1,61440
Mp 112,20,208
Mp 112,24,224
Rb 8,1,62464
Mp 120,24,232
Mp 120,28,248
Rb 12,1,63488
s_waitcnt lgkmcnt(8)
Mp 112,28,240
v_mfma_f32_16x16x32_bf16 v[116:119], a[32:35], v[128:131], 0
Rb 16,10,0
v_mfma_f32_16x16x32_bf16 v[124:127], a[32:35], v[136:139], 0
Mp 124,36,152
Rb 20,10,1024
Mp 116,36,144
Mp 116,40,160
Rb 24,10,2048
Cv 104,104,105
Mp 124,40,168
Cv 105,106,107
Cv 106,108,109
Mp 124,44,184
Rb 28,10,3072
)" R"(
s_waitcnt lgkmcnt(8)
Mp 116,44,176
Cv 107,110,111
Mp 116,48,192
Rb 32,10,4096
Mp 124,48,200
s_add_u32 s29, s73, 0xc000
buffer_store_dwordx4 v[104:107], v1, s[20:23], s29 offen sc0 sc1
Mp 124,52,216
Rb 36,10,5120
Mp 116,52,208
Mp 116,56,224
Rb 40,10,6144
Mp 124,56,232
Mp 124,60,248
Rb 44,10,8192
s_waitcnt lgkmcnt(8)
Mp 116,60,240
v_mfma_f32_16x16x32_bf16 v[96:99], a[0:3], v[128:131], 0
Rb 48,10,9216
v_mfma_f32_16x16x32_bf16 v[104:107], a[0:3], v[136:139], 0
Mp 104,4,152
Rb 52,10,10240
Mp 96,4,144
Mp 96,8,160
Rb 56,10,11264
Cv 112,112,113
Mp 104,8,168
Cv 113,114,115
Cv 114,116,117
Mp 104,12,184
Rb 60,10,12288
s_waitcnt lgkmcnt(8)
Mp 96,12,176
Cv 115,118,119
Mp 96,16,192
Rb 0,10,13312
Mp 104,16,200
s_add_u32 s29, s73, 0xc400
buffer_store_dwordx4 v[112:115], v1, s[20:23], s29 offen sc0 sc1
Mp 104,20,216
Rb 4,10,14336
Mp 96,20,208
Mp 96,24,224
Rb 8,10,16384
Mp 104,24,232
Mp 104,28,248
Rb 12,10,17408
s_waitcnt lgkmcnt(8)
Mp 96,28,240
v_mfma_f32_16x16x32_bf16 v[100:103], a[32:35], v[128:131], 0
Rb 16,10,18432
v_mfma_f32_16x16x32_bf16 v[108:111], a[32:35], v[136:139], 0
Mp 108,36,152
Rb 20,10,19456
Mp 100,36,144
Mp 100,40,160
Rb 24,10,20480
Cv 120,120,121
Mp 108,40,168
Cv 121,122,123
Cv 122,124,125
Mp 108,44,184
Rb 28,10,21504
)" R"(
s_waitcnt lgkmcnt(8)
Mp 100,44,176
Cv 123,126,127
Mp 100,48,192
Rb 32,10,22528
Mp 108,48,200
s_add_u32 s29, s73, 0xc800
buffer_store_dwordx4 v[120:123], v1, s[20:23], s29 offen sc0 sc1
Mp 108,52,216
Rb 36,10,24576
Mp 100,52,208
Mp 100,56,224
Rb 40,10,25600
Mp 108,56,232
Mp 108,60,248
Rb 44,10,26624
s_waitcnt lgkmcnt(8)
Mp 100,60,240
v_mfma_f32_16x16x32_bf16 v[112:115], a[0:3], v[128:131], 0
Rb 48,10,27648
v_mfma_f32_16x16x32_bf16 v[120:123], a[0:3], v[136:139], 0
Mp 120,4,152
Rb 52,10,28672
Mp 112,4,144
Mp 112,8,160
Rb 56,10,29696
Cv 96,96,97
Mp 120,8,168
Cv 97,98,99
Cv 98,100,101
Mp 120,12,184
Rb 60,10,30720
s_waitcnt lgkmcnt(8)
Mp 112,12,176
Cv 99,102,103
Mp 112,16,192
s_add_u32 s29, s73, 0xcc00
buffer_store_dwordx4 v[96:99], v1, s[20:23], s29 offen sc0 sc1
Mp 120,16,200
Mp 120,20,216
Mp 112,20,208
Mp 112,24,224
Mp 120,24,232
Mp 120,28,248
s_waitcnt lgkmcnt(4)
Mp 112,28,240
v_mfma_f32_16x16x32_bf16 v[116:119], a[32:35], v[128:131], 0
v_mfma_f32_16x16x32_bf16 v[124:127], a[32:35], v[136:139], 0
Mp 124,36,152
Mp 116,36,144
Mp 116,40,160
Cv 104,104,105
Cv 105,106,107
Mp 124,40,168
Cv 106,108,109
Cv 107,110,111
Mp 124,44,184
s_waitcnt lgkmcnt(0)
Mp 116,44,176
s_add_u32 s29, s73, 0xd000
buffer_store_dwordx4 v[104:107], v1, s[20:23], s29 offen sc0 sc1
Mp 116,48,192
Mp 124,48,200
Mp 124,52,216
)" R"(
Mp 116,52,208
Mp 116,56,224
Mp 124,56,232
Mp 124,60,248
Mp 116,60,240
s_nop 7
Cv 112,112,113
Cv 113,114,115
Cv 114,116,117
Cv 115,118,119
s_add_u32 s29, s73, 0xd400
buffer_store_dwordx4 v[112:115], v1, s[20:23], s29 offen sc0 sc1
Cv 120,120,121
Cv 121,122,123
Cv 122,124,125
Cv 123,126,127
s_add_u32 s29, s73, 0xd800
buffer_store_dwordx4 v[120:123], v1, s[20:23], s29 offen sc0 sc1
.LzEND:
v_lshl_or_b32 v0, s33, 6, v0
s_add_u32 s4, s68, 4
s_mov_b32 s5, s63
s_mov_b32 s6, s84
s_mov_b32 s8, s74
s_mov_b32 s9, s75
s_mov_b32 s10, 0x1c0000
s_mov_b32 s11, 0x20000
)" : "+{s[0:1]}"(ka), "+{s2}"(bid), "+{v0}"(tid), "={s3}"(tag), "={s4}"(sfw), "={s5}"(idx), "={s6}"(tk), "={s[12:15]}"(rX), "={s[20:23]}"(rO), "={s[8:11]}"(rOut) :
   : "memory", "scc", "vcc", "v1", "v2", "v3", "v4", "v5", "v6", "v7", "v8", "v9", R256("v"), ACL,
     "s7", "s16", "s17", "s18", "s19", "s24", "s25", "s26", "s27", "s28", "s29", "s30", "s31", "s33", "s34", "s35", "s36", "s37", "s38", "s39",
     V10("s4"), V10("s5"), V10("s6"), V10("s7"), V10("s8"), V10("s9"));
  bid = rfl(bid); tag = rfl(tag); sfw = rfl(sfw); rX = rfl4(rX); rO = rfl4(rO); rOut = rfl4(rOut);
  const u32 lane = tid & 63, wid = rfl(tid >> 6), r = lane & 15, g = lane >> 4, w = ((bid >> 3) & 15u) + 16u * wid;
  const bool B = bid >= 128u;
  idx = rfl(idx);
  Task k[NS]; u32 vf = OOB, rt = 0u, pend = 0u;
#pragma unroll
  for (int s = 0; s < NS; ++s) {
    const u32 i = B ? (s ? 168u + w : 104u + w) : w + 64u * s, cls = i / 56u, e = i - cls * 56u;
    const bool on = i < 224u && (!B || i >= 128u);
    const u32 blk = (cls >> 1) * 32u + (e / 14u) * 8u + (bid & 7u), G = e % 14u + (cls & 1u) * 14u, r0 = blk * 32u + (G & 1u) * 16u;
    k[s].sy = rfl((u32)WS_Y + blk * 65536u + (G + (G / 7u) * 9u) * 1024u + ((rfl(tk) ^ (bid << 10) ^ (cls << 12)) & 8192u));
    k[s].vx = on ? (r0 + r) * (M_ * 2) + (G >> 1) * 64u + g * 8u : OOB;
    if (g == s) vf = (blk * 16u + r) * 128u + (cls & 1u) * 4u;
    if (on) pend |= 0x1111u << (16 * s);
    if (on) rt |= (((idx & 0xffffu) >> 4 == r0 >> 4 ? 1u : 0u) | (idx >> 20 == r0 >> 4 ? 2u : 0u)) << (2 * s);
    k[s].vg = (r0 + r) * 16u;
  }
  u32x4 sel[2];
#pragma unroll
  for (int b = 0; b < 2; ++b)
#pragma unroll
    for (int q = 0; q < 4; ++q) sel[b][q] = (r >> 1) == 4u * b + q ? ((r & 1u) ? 0x3f800000u : 0x3f80u) : 0u;
  asm volatile("" : "+v"(sel[0]), "+v"(sel[1]), "+v"(k[0].vx), "+v"(k[1].vx), "+v"(k[0].vg), "+v"(k[1].vg), "+v"(vf));
  asm volatile("s_waitcnt vmcnt(0)\n\tbuffer_store_dword %0, %1, %2, %3 offen sc0 sc1" :: "v"(tag), "v"(lane ? OOB : 0u), "s"(rO), "s"(sfw) : "memory");
  const u32 vy = g * 4194304u + r * 16u;
  rt = rfl(rt); pend = rfl(pend);
#define LOOK(W) u32 f; unsigned long long m; \
    asm volatile("buffer_load_dword %0, %2, %3, %4 offen sc0 sc1\n\ts_waitcnt vmcnt(0)\n\tv_cmp_ne_u32_e64 %1, %5, %0" \
                 : "=&v"(f), "=s"(m) : "v"((pend >> (lane & 28u)) & 1u ? vf : OOB), "s"(rO), "s"((u32)WS_YW), "s"(tag) : "memory"); \
    u32 x = rfl((u32)m); x |= x >> 1; x |= x >> 2; \
    const u32 rdy = (W) ? pend & ~x : pend, had = pend; \
    y_load<0>(vy, rO, k[0].sy, rdy); y_load<1>(vy, rO, k[1].sy, rdy >> 16); \
    pend &= ~rdy
  u32 todo = pend;
  if (bid - 112u < 14u) {
    const u32 task = (bid - 112u) * 4u + wid, c = task >= 28u, ir = (c ? idx >> 16 : idx) & 2047u, col = (task - 28u * c) * 16u + (lane & 3u) * 4u, sf = (u32)WS_YF + task * 7168u;
    f32x4 v[7]; u32x2 xv; float rv; u32 f0, f1, n = 1u << 17;
    do {
      asm volatile("buffer_load_dword %0, %2, %4, %5 offen sc0 sc1\n\tbuffer_load_dword %1, %3, %4, %5 offen sc0 sc1\n\ts_waitcnt vmcnt(0)"
                   : "=&v"(f0), "=&v"(f1) : "v"(lane * 128u), "v"(lane * 128u + 6144u), "s"(rO), "s"((u32)WS_FW) : "memory");
    } while (!__all(f0 == tag && f1 == tag) && --n);
#pragma unroll
    for (int q = 0; q < 7; ++q) asm volatile("buffer_load_dwordx4 %0, %1, %2, %3 offen offset:%4 sc0 sc1" : "=v"(v[q]) : "v"(lane * 16u), "s"(rO), "s"(sf + (q >> 2) * 4096), "i"((q & 3) * 1024) : "memory");
    asm volatile("buffer_load_dwordx2 %0, %9, %10, %11 offen\n\tbuffer_load_dword %1, %12, %13, %14 offen sc0 sc1\n\ts_waitcnt vmcnt(0)"
                 : "=&v"(xv), "=&v"(rv), "+v"(v[0]), "+v"(v[1]), "+v"(v[2]), "+v"(v[3]), "+v"(v[4]), "+v"(v[5]), "+v"(v[6])
                 : "v"(col * 2u), "s"(rX), "s"(ir * (M_ * 2)), "v"(0u), "s"(rO), "s"((u32)WS_KEY + 12u + ir * 16u) : "memory");
    const f32x4 fs = ((v[0] + v[1]) + (v[2] + v[3])) + ((v[4] + v[5]) + v[6]);
    const float sg = rbf(1.f / (1.f + __expf(-rv))), xs[4] = {BF_LO(xv[0]), BF_HI(xv[0]), BF_LO(xv[1]), BF_HI(xv[1])};
    float o[4];
#pragma unroll
    for (int e = 0; e < 4; ++e) {
      unsigned u = __float_as_uint(fs[e]);
      const auto s32 = __builtin_amdgcn_permlane32_swap(u, u, false, false);
      u = __float_as_uint(__uint_as_float(s32[0]) + __uint_as_float(s32[1]));
      const auto s16 = __builtin_amdgcn_permlane16_swap(u, u, false, false);
      o[e] = xs[e] + rbf(rbf(dpp_add<0x124>(dpp_add<0x128>(__uint_as_float(s16[0]) + __uint_as_float(s16[1])))) * sg);
    }
    const u32x4 ov = {cvtpk(o[0], o[1]), tag, cvtpk(o[2], o[3]), tag};
    asm volatile("buffer_store_dwordx4 %0, %1, %2, %3 offen sc0 sc1\n\ts_nop 1" :: "v"(ov), "v"(lane < 4u ? lane * 16u : OOB), "s"(rO), "s"((u32)WS_BB + task * 128u) : "memory");
  }
#pragma unroll
  for (int s = 0; s < NS; ++s)
    asm volatile("buffer_load_dwordx2 %0, %3, %4, 0 offen\n\tbuffer_load_dwordx2 %1, %3, %4, 0 offen offset:32\n\tbuffer_load_dword %2, %5, %6, %7 offen sc0 sc1"
                 : "=&v"(k[s].x[0]), "=&v"(k[s].x[1]), "=&v"(k[s].g) : "v"(k[s].vx), "s"(rX), "v"(k[s].vg), "s"(rO), "s"((u32)WS_KEY + 8u) : "memory");
  { LOOK(true); (void)had; }
  asm volatile("" : "+v"(k[0].x[0]), "+v"(k[0].x[1]), "+v"(k[0].g), "+v"(k[1].x[0]), "+v"(k[1].x[1]), "+v"(k[1].g));
  if (rt) {
    u32 vo[NS], n = 1u << 17; u32x4 p[2 * NS]; bool ok;
#pragma unroll
    for (int s = 0; s < NS; ++s) {
      const u32 o0 = k[s].vx - (idx & 0xffffu) * (M_ * 2), o1 = k[s].vx - (idx >> 16) * (M_ * 2), o = o0 < M_ * 2 ? o0 : o1 + M_ * 2;
      vo[s] = o0 < M_ * 2 || o1 < M_ * 2 ? (o >> 5) * 128u + g * 16u : OOB;
    }
    do {
      asm volatile("buffer_load_dwordx4 %0, %4, %6, %7 offen sc0 sc1\n\tbuffer_load_dwordx4 %1, %4, %6, %7 offen offset:128 sc0 sc1\n\t"
                   "buffer_load_dwordx4 %2, %5, %6, %7 offen sc0 sc1\n\tbuffer_load_dwordx4 %3, %5, %6, %7 offen offset:128 sc0 sc1\n\ts_waitcnt vmcnt(0)"
                   : "=&v"(p[0]), "=&v"(p[1]), "=&v"(p[2]), "=&v"(p[3]) : "v"(vo[0]), "v"(vo[1]), "s"(rO), "s"((u32)WS_BB) : "memory");
      ok = true;
#pragma unroll
      for (int q = 0; q < 2 * NS; ++q) ok = ok && (vo[q >> 1] == OOB || (p[q][1] == tag && p[q][3] == tag));
    } while (!__all(ok) && --n);
#pragma unroll
    for (int q = 0; q < 2 * NS; ++q) if (vo[q >> 1] != OOB) k[q >> 1].x[q & 1] = (u32x2){p[q][0], p[q][2]};
  }
#pragma unroll
  for (int s = 0; s < NS; ++s) {
    const float omg = rbf(1.f - k[s].g);
#pragma unroll
    for (int e = 0; e < 4; ++e) { k[s].b[2 * e] = rbf(omg * BF_LO(k[s].x[e >> 1][e & 1])); k[s].b[2 * e + 1] = rbf(omg * BF_HI(k[s].x[e >> 1][e & 1])); }
  }
  for (u32 n = 1u << 17; todo; --n) {
    LOOK(n);
    if (!rdy && had) __builtin_amdgcn_s_sleep(SLP);
    if ((todo & 0xffffu) && !(had & 0xffffu)) { y_done<0>(k[0], sel, rOut); todo &= 0xffff0000u; }
    if ((todo >> 16) && !(had >> 16)) { y_done<1>(k[1], sel, rOut); todo &= 0xffffu; }
  }
  if (tid == 0u && bid == 0u) asm volatile("buffer_store_dword %0, %1, %2, %3 offen sc0 sc1" :: "v"(tag), "v"(lane ? OOB : 0u), "s"(rO), "s"((u32)WS_EP) : "memory");
  asm volatile(".LzCEND:");
}

extern "C" void kernel_launch(void* const* d_in, const int* in_sizes, int n_in, void* d_out, int out_size, void* d_ws, size_t ws_size, hipStream_t stream) {
  (void)hipFuncSetAttribute((const void*)k_main, hipFuncAttributeMaxDynamicSharedMemorySize, 163840);
  k_main<<<256, 256, 163840, stream>>>((const u16*)d_in[0], (const u16*)d_in[1], (const u16*)d_in[2], (const u16*)d_in[3], (const u16*)d_in[4],
      (char*)d_ws, (const u16*)d_in[11], (const u16*)d_in[7], (const u16*)d_in[5], (const u16*)d_in[8], (const u16*)d_in[9], (const u16*)d_in[10], (u16*)d_out);
}
```

```cpp
#include <hip/hip_runtime.h>
#include <stdint.h>

typedef __attribute__((ext_vector_type(4))) float f32x4;
typedef __attribute__((ext_vector_type(2))) float f32x2;
typedef __attribute__((ext_vector_type(4))) unsigned u32x4;
typedef __attribute__((ext_vector_type(2))) unsigned u32x2;
typedef __attribute__((ext_vector_type(2))) __bf16 bfx2;
typedef unsigned short u16;
typedef unsigned u32;
#define DI __device__ __forceinline__
constexpr int M_ = 448, F_ = 1792, FFN_N = 112;
constexpr size_t WS_K = 0u;
constexpr size_t WS_V = 33554432u;
constexpr size_t WS_Q = 67108864u;
constexpr size_t WS_PB = 68157440u;
constexpr size_t WS_WO = 100663296u;
constexpr size_t WS_Y = 134217728u;
constexpr size_t WS_ML = 201326592u;
constexpr size_t WS_KEY = 202375168u;
constexpr size_t WS_IDX = 202407936u;
constexpr size_t WS_EP = 202408960u;
constexpr size_t WS_PAR = 202409984u;
constexpr size_t WS_BB = 202412032u;
constexpr size_t WS_FW = 202424320u;
constexpr size_t WS_KVF = 202440704u;
constexpr size_t WS_QF = 202473472u;
constexpr size_t WS_PF = 202506240u;
constexpr size_t WS_YW = 202571776u;
constexpr size_t WS_YF = 202702848u;
constexpr size_t WS_END = 203104256u;
DI unsigned cvtpk(float lo, float hi) { return __builtin_bit_cast(unsigned, __builtin_convertvector((f32x2){lo, hi}, bfx2)); }
DI float rbf(float f) { return __uint_as_float(cvtpk(f, f) << 16); }
#define BF_LO(u) __uint_as_float((u) << 16)
#define BF_HI(u) __uint_as_float((u) & 0xffff0000u)
typedef u32x4 srd_t;
DI u32 rfl(u32 x) { return __builtin_amdgcn_readfirstlane(x); }
DI srd_t rfl4(srd_t r) { return (srd_t){rfl(r[0]), rfl(r[1]), rfl(r[2]), rfl(r[3])}; }
constexpr u32 OOB = 0x40000000u;
template <int CTRL> DI float dpp_add(float v) { return v + __uint_as_float(__builtin_amdgcn_update_dpp(0, __float_as_uint(v), CTRL, 0xf, 0xf, false)); }

#define V10(p) p "0", p "1", p "2", p "3", p "4", p "5", p "6", p "7", p "8", p "9"
#define V100(p) V10(p "0"), V10(p "1"), V10(p "2"), V10(p "3"), V10(p "4"), V10(p "5"), V10(p "6"), V10(p "7"), V10(p "8"), V10(p "9")
#define R256(p) V10(p "1"), V10(p "2"), V10(p "3"), V10(p "4"), V10(p "5"), V10(p "6"), V10(p "7"), V10(p "8"), V10(p "9"), V100(p "1"), \
                V10(p "20"), V10(p "21"), V10(p "22"), V10(p "23"), V10(p "24"), p "250", p "251", p "252", p "253", p "254", p "255"

#define ACL V10("a"), R256("a")
#define YL(k, c) "buffer_load_dwordx4 a[%c4+" #k "*16+" #c "*4:%c4+" #k "*16+" #c "*4+3], %1, %2, %0 offen offset:" #c "*256 sc0 sc1\n\t"
#define YK(k) "s_bitcmp0_b32 %5, " #k "*4\n\ts_cbranch_scc1 .Ly" #k "_%=\n\ts_add_u32 %0, %3, " #k "*16777216\n\t" YL(k, 0) YL(k, 1) YL(k, 2) YL(k, 3) ".Ly" #k "_%=:\n\t"
template <int S> DI void y_load(u32 vy, srd_t r, u32 so, u32 rdy) {
  u32 t;
  asm volatile(YK(0) YK(1) YK(2) YK(3) : "=&s"(t) : "v"(vy), "s"(r), "s"(so), "i"(64 * S), "s"(rdy) : "memory", "scc", ACL);
}
#define YM(k, c, d, s, z) "v_mfma_f32_16x16x32_bf16 %" #d ", %" #s ", a[%c4+" #k "*16+" #c "*4:%c4+" #k "*16+" #c "*4+3], " z "\n\t"
#define YS(k) YM(k, 0, 0, 2, "%0") YM(k, 2, 1, 2, "%1") YM(k, 1, 0, 3, "%0") YM(k, 3, 1, 3, "%1")
constexpr int NS = 2, SLP = 8;
struct Task { u32x2 x[2]; float g, b[8]; u32 vx, vg, sy; };
template <int S> DI void y_done(Task& k, const u32x4 (&sel)[2], srd_t rOut) {
  f32x4 c0, c1;
  asm volatile(YM(0, 0, 0, 2, "0") YM(0, 2, 1, 2, "0") YM(0, 1, 0, 3, "%0") YM(0, 3, 1, 3, "%1") YS(1) YS(2) YS(3) "s_nop 7"
               : "=&v"(c0), "=&v"(c1) : "v"(sel[0]), "v"(sel[1]), "i"(64 * S) : "memory");
  float r[8];
#pragma unroll
  for (int e = 0; e < 8; ++e) r[e] = rbf(k.g * rbf(e < 4 ? c0[e & 3] : c1[e & 3])) + k.b[e];
  const u32x2 o0 = {cvtpk(r[0], r[1]), cvtpk(r[2], r[3])}, o1 = {cvtpk(r[4], r[5]), cvtpk(r[6], r[7])};
  asm volatile("buffer_store_dwordx2 %0, %2, %3, 0 offen\n\tbuffer_store_dwordx2 %1, %2, %3, 0 offen offset:32" :: "v"(o0), "v"(o1), "v"(k.vx), "s"(rOut) : "memory");
}

extern "C" __global__ __launch_bounds__(256, 1)
void k_main(const u16* x_, const u16* ckv, const u16* wq, const u16* wuk, const u16* wuv, char* ws_,
            const u16* gate_w, const u16* w_r, const u16* wo, const u16* wg_, const u16* wu_, const u16* wd_, u16* out_) {
  typedef const __attribute__((address_space(4))) char* karg_t;
  karg_t ka = (karg_t)__builtin_amdgcn_kernarg_segment_ptr();
  u32 bid = blockIdx.x, tid = threadIdx.x, tag, sfw, idx, tk;
  srd_t rX, rO, rOut;
  asm volatile(R"(
.macro Mv d,a,b
v_mfma_f32_16x16x32_bf16 v[\d:\d+3], a[\a:\a+3], a[\b:\b+3], v[\d:\d+3]
.endm
.macro Mp d,a,b
v_mfma_f32_16x16x32_bf16 v[\d:\d+3], a[\a:\a+3], v[\b:\b+3], v[\d:\d+3]
.endm
.macro Ma d,a,b
v_mfma_f32_16x16x32_bf16 a[\d:\d+3], a[\a:\a+3], a[\b:\b+3], a[\d:\d+3]
.endm
.macro Mz d,a,b
v_mfma_f32_16x16x32_bf16 v[\d:\d+3], a[\a:\a+3], a[\b:\b+3], 0
.endm
.macro Rb d,v,o
ds_read_b128 a[\d:\d+3], v\v offset:\o
.endm
.macro Rt d,v,o
ds_read_b64_tr_b16 a[\d:\d+1], v\v offset:\o
.endm
.macro Cv d,a,b
v_cvt_pk_bf16_f32 v\d, v\a, v\b
.endm
.LzSTART:
s_load_dwordx16 s[64:79], s[0:1], 0
s_load_dwordx2 s[80:81], s[0:1], 64
v_lshrrev_b32 v1, 6, v0
v_and_b32 v0, 63, v0
v_readfirstlane_b32 s33, v1
v_lshlrev_b32 v1, 4, v0
s_getpc_b64 s[24:25]
.Lzpc0:
s_sub_u32 s24, s24, .Lzpc0-.LzSTART
s_subb_u32 s25, s25, 0
s_mov_b32 s26, .LzEND-.LzSTART+8192
s_mov_b32 s27, 0x20000
s_lshr_b32 s28, s2, 3
s_lshl2_add_u32 s28, s28, s33
s_lshl_b32 s28, s28, 11
v_lshrrev_b32 v27, 4, v0
v_min_u32 v27, 1, v27
v_lshlrev_b32 v27, 27, v27
v_add_lshl_u32 v27, v27, v1, 3
buffer_load_dword v27, v27, s[24:27], s28 offen
s_and_b32 s28, s2, 7
s_bfe_u32 s29, s2, 0x10003
s_lshl1_add_u32 s34, s28, s29
s_bfe_u32 s35, s2, 0x30004
s_bfe_u32 s91, s2, 0x10004
s_bfe_u32 s93, s2, 0x20005
s_lshl_b32 s36, s33, 13
s_mov_b32 s95, 40
s_bitcmp1_b32 s2, 7
s_cbranch_scc1 .LzB0
s_branch .LzA0
.LzPRB:
s_lshl_b32 s24, s34, 21
s_mul_i32 s25, s33, 2304
s_add_u32 s24, s24, s25
s_add_u32 s24, s24, 0x6100000
s_and_b32 s25, s36, 0x2000
s_xor_b32 s24, s24, s25
s_xor_b32 s25, s24, 0x2000
buffer_store_dword v0, v7, s[20:23], s24 offen sc0 sc1
buffer_store_dword v0, v7, s[20:23], s25 offen sc0 sc1
s_mov_b32 s26, -1
s_mov_b32 s27, -1
s_waitcnt vmcnt(0)
s_memtime s[28:29]
s_waitcnt lgkmcnt(0)
buffer_store_dword v0, v7, s[20:23], s24 offen sc0 sc1
s_waitcnt vmcnt(0)
s_memtime s[30:31]
s_waitcnt lgkmcnt(0)
s_sub_u32 s30, s30, s28
s_min_u32 s26, s26, s30
s_memtime s[28:29]
s_waitcnt lgkmcnt(0)
buffer_store_dword v0, v7, s[20:23], s25 offen sc0 sc1
s_waitcnt vmcnt(0)
s_memtime s[30:31]
s_waitcnt lgkmcnt(0)
s_sub_u32 s30, s30, s28
s_min_u32 s27, s27, s30
s_memtime s[28:29]
s_waitcnt lgkmcnt(0)
)" R"(
buffer_store_dword v0, v7, s[20:23], s24 offen sc0 sc1
s_waitcnt vmcnt(0)
s_memtime s[30:31]
s_waitcnt lgkmcnt(0)
s_sub_u32 s30, s30, s28
s_min_u32 s26, s26, s30
s_memtime s[28:29]
s_waitcnt lgkmcnt(0)
buffer_store_dword v0, v7, s[20:23], s25 offen sc0 sc1
s_waitcnt vmcnt(0)
s_memtime s[30:31]
s_waitcnt lgkmcnt(0)
s_sub_u32 s30, s30, s28
s_min_u32 s27, s27, s30
s_bitcmp1_b32 s33, 0
s_cselect_b32 s28, s27, s26
s_cselect_b32 s29, s26, s27
v_mov_b32 v32, s28
v_mov_b32 v33, s29
s_lshl_b32 s24, s33, 3
v_mov_b32 v34, s24
v_mov_b32 v35, 0
ds_write_b64 v34, v[32:33]
s_waitcnt lgkmcnt(0)
s_barrier
ds_read_b128 v[36:39], v35
ds_read_b128 v[40:43], v35 offset:16
s_mul_i32 s24, s3, 1664525
s_add_u32 s24, s24, 1013904223
s_and_b32 s24, s24, -2
s_lshl_b32 s25, s34, 6
s_add_u32 s25, s25, 0xc108800
s_cmp_lg_u32 s33, 0
s_cmov_b32 s25, 0x40000000
s_waitcnt lgkmcnt(0)
v_min3_u32 v32, v36, v38, v40
v_min3_u32 v33, v37, v39, v41
v_min_u32 v32, v32, v42
v_min_u32 v33, v33, v43
v_cmp_gt_u32_e32 vcc, v32, v33
v_mov_b32 v34, s24
s_nop 0
v_addc_co_u32_e32 v34, vcc, 0, v34, vcc
buffer_store_dword v34, v7, s[20:23], s25 offen sc0 sc1
s_barrier
s_branch .LzPRR
.LzA0:
s_mov_b32 s63, 0x6040200
s_mov_b32 s28, 0x7050312
s_bitcmp1_b32 s2, 5
s_cmov_b32 s63, 0x18161401
s_cmov_b32 s28, 0x19171513
s_bitcmp1_b32 s2, 6
s_cmov_b32 s63, s28
s_lshl_b32 s28, s33, 3
s_lshr_b32 s28, s63, s28
s_and_b32 s28, s28, 255
s_lshl_b32 s38, s28, 17
s_add_u32 s38, s38, 0xff80
s_lshl_b32 s39, s33, 16
s_lshl_b32 s28, s34, 9
s_add_u32 s39, s39, s28
s_sub_u32 s39, s39, 0x40000
s_lshl_b32 s37, s33, 12
)" R"(
s_add_u32 s37, s37, 0x10000
v_lshrrev_b32 v28, 4, v0
v_and_b32 v29, 7, v0
v_xor_b32 v28, v28, v29
v_lshlrev_b32 v28, 4, v28
v_lshrrev_b32 v29, 3, v0
v_lshl_or_b32 v8, v29, 11, v28
v_xor_b32 v9, 64, v8
v_add_u32 v9, 15360, v9
v_add_u32 v10, 30720, v8
v_add_u32 v11, 30720, v9
s_and_b32 s28, s33, 1
s_lshl_b32 s28, s28, 7
v_lshrrev_b32 v29, 5, v0
v_mul_u32_u24 v29, 0x2220, v29
v_xor_b32 v29, v29, v1
v_xor_b32 v12, s28, v29
v_xor_b32 v13, 64, v12
v_add_u32 v13, 15360, v13
v_add_u32 v14, 30720, v12
v_add_u32 v15, 30720, v13
v_min_u32 v7, 1, v0
v_lshlrev_b32 v7, 30, v7
s_waitcnt lgkmcnt(0)
s_mov_b32 s28, 0xc108400
s_load_dword s3, s[74:75], s28
s_mov_b32 s20, s74
s_mov_b32 s21, s75
s_mov_b32 s22, 0xc1b2000
s_mov_b32 s23, 0x20000
s_mov_b32 s4, s66
s_mov_b32 s5, s67
s_mov_b32 s6, 0x400000
s_mov_b32 s7, 0x20000
s_cmp_eq_u32 s91, 1
s_cselect_b32 s8, s72, s70
s_cselect_b32 s9, s73, s71
s_mov_b32 s10, 0x800000
s_mov_b32 s11, 0x20000
s_lshr_b32 s28, s2, 3
s_cmp_eq_u32 s28, 14
s_cbranch_scc1 .LzPRB
.LzPRR:
s_add_u32 m0, s37, 0
s_add_u32 s39, s39, 0x40000
buffer_load_dwordx4 v12, s[8:11], s39 offen offset:0 lds
buffer_load_dwordx4 v13, s[8:11], s39 offen offset:1024 lds
buffer_load_dwordx4 v14, s[8:11], s39 offen offset:2048 lds
buffer_load_dwordx4 v15, s[8:11], s39 offen offset:3072 lds
s_add_u32 m0, s36, 0
s_add_u32 s38, s38, 0xffff0080
buffer_load_dwordx4 v8, s[4:7], s38 offen offset:0 lds
buffer_load_dwordx4 v9, s[4:7], s38 offen offset:1024 lds
buffer_load_dwordx4 v10, s[4:7], s38 offen offset:2048 lds
buffer_load_dwordx4 v11, s[4:7], s38 offen offset:3072 lds
s_add_u32 m0, s36, 4096
s_add_u32 s38, s38, 0x10000
buffer_load_dwordx4 v8, s[4:7], s38 offen offset:0 lds
buffer_load_dwordx4 v9, s[4:7], s38 offen offset:1024 lds
buffer_load_dwordx4 v10, s[4:7], s38 offen offset:2048 lds
buffer_load_dwordx4 v11, s[4:7], s38 offen offset:3072 lds
s_add_u32 m0, s37, 16384
s_add_u32 s39, s39, 0x40000
buffer_load_dwordx4 v12, s[8:11], s39 offen offset:0 lds
)" R"(
buffer_load_dwordx4 v13, s[8:11], s39 offen offset:1024 lds
buffer_load_dwordx4 v14, s[8:11], s39 offen offset:2048 lds
buffer_load_dwordx4 v15, s[8:11], s39 offen offset:3072 lds
s_add_u32 m0, s36, 32768
s_add_u32 s38, s38, 0xffff0080
buffer_load_dwordx4 v8, s[4:7], s38 offen offset:0 lds
buffer_load_dwordx4 v9, s[4:7], s38 offen offset:1024 lds
buffer_load_dwordx4 v10, s[4:7], s38 offen offset:2048 lds
buffer_load_dwordx4 v11, s[4:7], s38 offen offset:3072 lds
s_add_u32 m0, s36, 36864
s_add_u32 s38, s38, 0x10000
buffer_load_dwordx4 v8, s[4:7], s38 offen offset:0 lds
buffer_load_dwordx4 v9, s[4:7], s38 offen offset:1024 lds
buffer_load_dwordx4 v10, s[4:7], s38 offen offset:2048 lds
buffer_load_dwordx4 v11, s[4:7], s38 offen offset:3072 lds
s_add_u32 m0, s37, 32768
s_add_u32 s39, s39, 0x40000
buffer_load_dwordx4 v12, s[8:11], s39 offen offset:0 lds
buffer_load_dwordx4 v13, s[8:11], s39 offen offset:1024 lds
buffer_load_dwordx4 v14, s[8:11], s39 offen offset:2048 lds
buffer_load_dwordx4 v15, s[8:11], s39 offen offset:3072 lds
s_mov_b32 s12, s64
s_mov_b32 s13, s65
s_mov_b32 s14, 0x1c0000
s_mov_b32 s15, 0x20000
s_mov_b32 s16, s68
s_mov_b32 s17, s69
s_mov_b32 s18, 0x380000
s_mov_b32 s19, 0x20000
s_load_dwordx4 s[76:79], s[0:1], 72
s_load_dwordx2 s[70:71], s[0:1], 88
s_load_dwordx2 s[74:75], s[0:1], 96
v_bfe_u32 v28, v0, 1, 3
v_lshrrev_b32 v29, 4, v0
v_xor_b32 v28, v28, v29
v_lshlrev_b32 v28, 4, v28
v_and_b32 v29, 15, v0
v_lshl_or_b32 v28, v29, 7, v28
s_lshr_b32 s28, s33, 1
s_lshl_b32 s28, s28, 12
v_or_b32 v16, s28, v28
v_xor_b32 v17, 64, v16
v_bfe_u32 v28, v0, 2, 3
v_lshlrev_b32 v28, 5, v28
v_and_b32 v29, 3, v0
v_lshl_or_b32 v28, v29, 3, v28
v_bfe_u32 v29, v0, 2, 2
v_lshl_or_b32 v28, v29, 9, v28
v_lshrrev_b32 v29, 4, v0
v_lshl_or_b32 v28, v29, 12, v28
s_and_b32 s28, s33, 1
s_lshl_b32 s28, s28, 8
s_bitset1_b32 s28, 16
v_or_b32 v18, s28, v28
v_xor_b32 v19, 32, v18
v_xor_b32 v20, 64, v18
v_xor_b32 v21, 96, v18
v_xor_b32 v22, 128, v18
v_xor_b32 v23, 160, v18
v_xor_b32 v24, 192, v18
v_xor_b32 v25, 224, v18
s_mov_b32 s24, s80
s_mov_b32 s25, s81
s_mov_b32 s26, 0x380000
)" R"(
s_mov_b32 s27, 0x20000
s_bfe_u32 s28, s2, 0x30005
s_lshl3_add_u32 s29, s34, s28
s_lshl2_add_u32 s29, s29, s33
s_mul_i32 s83, s29, 7168
s_mul_i32 s29, s91, 448
s_add_u32 s83, s83, s29
s_lshl_b32 s82, s34, 21
s_add_u32 s82, s82, 0x6000000
s_mul_i32 s94, s91, 14
s_add_u32 s94, s94, s33
s_mov_b32 s92, s28
v_lshrrev_b32 v4, 5, v0
v_mul_u32_u24 v4, 384, v4
v_add_u32 v28, v4, v1
v_and_b32 v4, 31, v0
v_cmp_lt_u32_e32 vcc, 27, v4
v_mov_b32 v4, 0x40000000
v_lshrrev_b32 v29, 2, v0
v_cndmask_b32_e32 v28, v28, v4, vcc
v_and_b32 v4, 48, v1
v_lshl_or_b32 v29, v29, 9, v4
s_lshr_b32 s28, s33, 1
s_lshl_b32 s28, s28, 6
s_and_b32 s29, s33, 1
s_lshl3_add_u32 s28, s29, s28
s_add_u32 s28, s28, 0x20000
v_add_u32 v29, s28, v29
s_add_u32 m0, s37, 0x10000
s_add_u32 s29, s83, 0
buffer_load_dwordx4 v28, s[24:27], s29 offen offset:0 lds
s_add_u32 s29, s83, 768
buffer_load_dwordx4 v28, s[24:27], s29 offen offset:1024 lds
s_add_u32 s29, s83, 1536
buffer_load_dwordx4 v28, s[24:27], s29 offen offset:2048 lds
s_add_u32 s29, s83, 2304
buffer_load_dwordx4 v28, s[24:27], s29 offen offset:3072 lds
s_lshl_b32 s28, s34, 21
s_lshl_b32 s29, s91, 25
s_add_u32 s28, s28, s29
s_bfe_u32 s29, s63, 0x80000
s_lshl_b32 s29, s29, 16
s_add_u32 s45, s28, s29
s_bfe_u32 s29, s63, 0x80008
s_lshl_b32 s29, s29, 16
s_add_u32 s46, s28, s29
s_bfe_u32 s29, s63, 0x80010
s_lshl_b32 s29, s29, 16
s_add_u32 s47, s28, s29
s_bfe_u32 s29, s63, 0x80018
s_lshl_b32 s29, s29, 16
s_add_u32 s48, s28, s29
s_lshl_b32 s50, s34, 11
s_add_u32 s50, s50, 0xc110000
s_lshl2_add_u32 s28, s91, s93
s_lshl_b32 s28, s28, 7
s_add_u32 s49, s28, s50
s_lshl3_add_u32 s28, s34, s35
s_lshl_b32 s52, s28, 7
s_add_u32 s52, s52, 0xc118000
s_lshl4_add_u32 s28, s34, s35
s_lshl_b32 s51, s28, 17
s_add_u32 s51, s51, 0x4000000
s_cmp_lg_u32 s33, 0
)" R"(
s_cmov_b32 s49, 0x40000000
s_mov_b32 s53, 0x40000
s_mov_b32 s54, 0x3db8aa3b
s_mul_i32 s41, s33, 7168
s_mul_i32 s42, s33, 0x70000
s_lshl_b32 s28, s34, 9
s_add_u32 s42, s42, s28
s_lshl_b32 s89, s34, 21
s_mov_b64 s[60:61], 0xff
s_mov_b32 s62, 0x4e800000
s_add_u32 s67, s89, 0x6000000
s_lshl3_add_u32 s28, s34, s35
s_add_u32 s64, s51, 0x100000
s_lshl2_add_u32 s28, s28, s33
s_lshl_b32 s65, s28, 7
s_add_u32 s65, s65, 0xc120000
s_lshl_b32 s66, s28, 10
s_add_u32 s66, s66, 0xc000000
s_mov_b32 s28, s35
s_lshl2_add_u32 s28, s28, s33
s_lshl_b32 s29, s34, 6
s_add_u32 s29, s29, s28
s_lshl_b32 s73, s29, 16
s_add_u32 s73, s73, 0x8000000
s_lshl4_add_u32 s28, s28, s34
s_lshl_b32 s68, s28, 7
s_add_u32 s68, s68, 0xc130000
s_add_u32 s55, s89, 0x120000
s_lshl_b32 s28, s35, 1
s_sub_u32 s57, 14, s28
s_mov_b32 s59, s89
s_mov_b32 s58, 2
s_cmp_eq_u32 s35, 7
s_cmov_b32 s55, s89
s_cmp_eq_u32 s35, 7
s_cmov_b32 s57, 16
s_cmp_eq_u32 s35, 7
s_cmov_b32 s59, s67
s_add_u32 s56, s55, 0x2000000
s_waitcnt vmcnt(20) lgkmcnt(0)
s_barrier
Rt 0,18,0
Rt 2,18,2048
Rt 4,19,0
Rt 6,19,2048
Rt 8,20,0
Rt 10,20,2048
Rt 12,21,0
Rt 14,21,2048
Rt 16,22,0
Rt 18,22,2048
Rt 20,23,0
Rt 22,23,2048
Rt 24,24,0
Rt 26,24,2048
Rt 28,25,0
Rt 30,25,2048
Rb 32,16,0
Rb 36,16,2048
Rb 40,16,8192
Rb 44,16,10240
Rb 48,16,16384
Rb 52,16,18432
Rb 56,16,24576
Rb 60,16,26624
)" R"(
s_mul_i32 s3, s3, 1664525
s_add_u32 s3, s3, 1013904223
s_cmp_eq_u32 s91, 1
s_waitcnt vmcnt(16) lgkmcnt(0)
s_barrier
s_cbranch_scc1 .LzAV
Mz 32,0,32
Rt 64,18,16384
Mz 64,0,36
Mz 96,0,40
Rt 66,18,18432
Mz 128,0,44
s_add_u32 s39, s39, 0x40000
Mz 160,0,48
Rt 68,19,16384
s_add_u32 m0, s37, 49152
Mz 192,0,52
buffer_load_dwordx4 v12, s[8:11], s39 offen offset:0 lds
Mz 224,0,56
Rt 70,19,18432
v_mfma_f32_16x16x32_bf16 a[224:227], a[0:3], a[60:63], 0
v_mfma_f32_16x16x32_bf16 a[228:231], a[4:7], a[60:63], 0
Rt 72,20,16384
Mz 228,4,56
Mz 196,4,52
Rt 74,20,18432
Mz 164,4,48
Mz 132,4,44
Rt 76,21,16384
Mz 100,4,40
buffer_load_dwordx4 v13, s[8:11], s39 offen offset:1024 lds
Mz 68,4,36
Rt 78,21,18432
Mz 36,4,32
Mz 40,8,32
Rt 80,22,16384
Mz 72,8,36
Mz 104,8,40
Rt 82,22,18432
Mz 136,8,44
Mz 168,8,48
Rt 84,23,16384
Mz 200,8,52
buffer_load_dwordx4 v14, s[8:11], s39 offen offset:2048 lds
Mz 232,8,56
Rt 86,23,18432
v_mfma_f32_16x16x32_bf16 a[232:235], a[8:11], a[60:63], 0
v_mfma_f32_16x16x32_bf16 a[236:239], a[12:15], a[60:63], 0
Rt 88,24,16384
Mz 236,12,56
Mz 204,12,52
Rt 90,24,18432
Mz 172,12,48
Mz 140,12,44
Rt 92,25,16384
Mz 108,12,40
buffer_load_dwordx4 v15, s[8:11], s39 offen offset:3072 lds
Mz 76,12,36
Rt 94,25,18432
Mz 44,12,32
Mz 48,16,32
Rb 96,17,0
Mz 80,16,36
Mz 112,16,40
Rb 100,17,2048
)" R"(
Mz 144,16,44
s_add_u32 s39, s39, 0x40000
Mz 176,16,48
Rb 104,17,8192
s_add_u32 m0, s37, 0
Mz 208,16,52
buffer_load_dwordx4 v12, s[8:11], s39 offen offset:0 lds
Mz 240,16,56
Rb 108,17,10240
v_mfma_f32_16x16x32_bf16 a[240:243], a[16:19], a[60:63], 0
v_mfma_f32_16x16x32_bf16 a[244:247], a[20:23], a[60:63], 0
Rb 112,17,16384
Mz 244,20,56
Mz 212,20,52
Rb 116,17,18432
Mz 180,20,48
Mz 148,20,44
Rb 120,17,24576
Mz 116,20,40
buffer_load_dwordx4 v13, s[8:11], s39 offen offset:1024 lds
Mz 84,20,36
Rb 124,17,26624
Mz 52,20,32
Mz 56,24,32
Mz 88,24,36
Mz 120,24,40
Mz 152,24,44
Mz 184,24,48
Mz 216,24,52
buffer_load_dwordx4 v14, s[8:11], s39 offen offset:2048 lds
Mz 248,24,56
v_mfma_f32_16x16x32_bf16 a[248:251], a[24:27], a[60:63], 0
v_mfma_f32_16x16x32_bf16 a[252:255], a[28:31], a[60:63], 0
Mz 252,28,56
Mz 220,28,52
Mz 188,28,48
Mz 156,28,44
Mz 124,28,40
buffer_load_dwordx4 v15, s[8:11], s39 offen offset:3072 lds
Mz 92,28,36
Mz 60,28,32
s_waitcnt vmcnt(12) lgkmcnt(0)
s_barrier
Mv 32,64,96
Rt 0,18,32768
Mv 64,64,100
s_add_u32 s38, s38, 0xffff0080
Mv 96,64,104
Rt 2,18,34816
s_add_u32 m0, s36, 0
Mv 128,64,108
buffer_load_dwordx4 v8, s[4:7], s38 offen offset:0 lds
Mv 160,64,112
Rt 4,19,32768
Mv 192,64,116
Mv 224,64,120
Rt 6,19,34816
Ma 224,64,124
buffer_load_dwordx4 v9, s[4:7], s38 offen offset:1024 lds
Ma 228,68,124
Rt 8,20,32768
Mv 228,68,120
Mv 196,68,116
Rt 10,20,34816
Mv 164,68,112
)" R"(
buffer_load_dwordx4 v10, s[4:7], s38 offen offset:2048 lds
Mv 132,68,108
Rt 12,21,32768
Mv 100,68,104
Mv 68,68,100
Rt 14,21,34816
Mv 36,68,96
buffer_load_dwordx4 v11, s[4:7], s38 offen offset:3072 lds
Mv 40,72,96
Rt 16,22,32768
Mv 72,72,100
s_add_u32 s38, s38, 0x10000
Mv 104,72,104
Rt 18,22,34816
s_add_u32 m0, s36, 4096
Mv 136,72,108
buffer_load_dwordx4 v8, s[4:7], s38 offen offset:0 lds
Mv 168,72,112
Rt 20,23,32768
Mv 200,72,116
Mv 232,72,120
Rt 22,23,34816
Ma 232,72,124
buffer_load_dwordx4 v9, s[4:7], s38 offen offset:1024 lds
Ma 236,76,124
Rt 24,24,32768
Mv 236,76,120
Mv 204,76,116
Rt 26,24,34816
Mv 172,76,112
buffer_load_dwordx4 v10, s[4:7], s38 offen offset:2048 lds
Mv 140,76,108
Rt 28,25,32768
Mv 108,76,104
Mv 76,76,100
Rt 30,25,34816
Mv 44,76,96
buffer_load_dwordx4 v11, s[4:7], s38 offen offset:3072 lds
Mv 48,80,96
Rb 32,16,32768
Mv 80,80,100
Mv 112,80,104
Rb 36,16,34816
Mv 144,80,108
Mv 176,80,112
Rb 40,16,40960
Mv 208,80,116
Mv 240,80,120
Rb 44,16,43008
Ma 240,80,124
Ma 244,84,124
Rb 48,16,49152
Mv 244,84,120
Mv 212,84,116
Rb 52,16,51200
Mv 180,84,112
Mv 148,84,108
Rb 56,16,57344
Mv 116,84,104
Mv 84,84,100
Rb 60,16,59392
Mv 52,84,96
Mv 56,88,96
Mv 88,88,100
)" R"(
Mv 120,88,104
Mv 152,88,108
Mv 184,88,112
Mv 216,88,116
Mv 248,88,120
Ma 248,88,124
Ma 252,92,124
Mv 252,92,120
Mv 220,92,116
Mv 188,92,112
Mv 156,92,108
Mv 124,92,104
Mv 92,92,100
Mv 60,92,96
s_waitcnt vmcnt(12) lgkmcnt(0)
s_barrier
Mv 32,0,32
Rt 64,18,49152
s_mov_b32 s85, 0xc108800
v_and_b32 v2, 7, v0
Mv 64,0,36
v_lshlrev_b32 v2, 7, v2
Mv 96,0,40
Rt 66,18,51200
buffer_load_dword v4, v2, s[20:23], s85 offen sc0 sc1
Mv 128,0,44
s_add_u32 s39, s39, 0x40000
Mv 160,0,48
Rt 68,19,49152
s_add_u32 m0, s37, 16384
Mv 192,0,52
buffer_load_dwordx4 v12, s[8:11], s39 offen offset:0 lds
Mv 224,0,56
Rt 70,19,51200
Ma 224,0,60
Ma 228,4,60
Rt 72,20,49152
Mv 228,4,56
Mv 196,4,52
Rt 74,20,51200
Mv 164,4,48
Mv 132,4,44
Rt 76,21,49152
Mv 100,4,40
buffer_load_dwordx4 v13, s[8:11], s39 offen offset:1024 lds
Mv 68,4,36
Rt 78,21,51200
Mv 36,4,32
Mv 40,8,32
Rt 80,22,49152
Mv 72,8,36
Mv 104,8,40
Rt 82,22,51200
Mv 136,8,44
Mv 168,8,48
Rt 84,23,49152
Mv 200,8,52
buffer_load_dwordx4 v14, s[8:11], s39 offen offset:2048 lds
Mv 232,8,56
Rt 86,23,51200
Ma 232,8,60
Ma 236,12,60
Rt 88,24,49152
Mv 236,12,56
Mv 204,12,52
)" R"(
Rt 90,24,51200
Mv 172,12,48
Mv 140,12,44
Rt 92,25,49152
Mv 108,12,40
buffer_load_dwordx4 v15, s[8:11], s39 offen offset:3072 lds
Mv 76,12,36
Rt 94,25,51200
Mv 44,12,32
Mv 48,16,32
Rb 96,17,32768
Mv 80,16,36
Mv 112,16,40
Rb 100,17,34816
Mv 144,16,44
s_add_u32 s39, s39, 0x40000
Mv 176,16,48
Rb 104,17,40960
s_add_u32 m0, s37, 32768
Mv 208,16,52
buffer_load_dwordx4 v12, s[8:11], s39 offen offset:0 lds
Mv 240,16,56
Rb 108,17,43008
Ma 240,16,60
Ma 244,20,60
Rb 112,17,49152
Mv 244,20,56
Mv 212,20,52
Rb 116,17,51200
Mv 180,20,48
Mv 148,20,44
Rb 120,17,57344
Mv 116,20,40
buffer_load_dwordx4 v13, s[8:11], s39 offen offset:1024 lds
Mv 84,20,36
Rb 124,17,59392
Mv 52,20,32
Mv 56,24,32
Rt 176,29,0
Mv 88,24,36
Rt 178,29,8192
Mv 120,24,40
Rt 180,29,128
Mv 152,24,44
Rt 182,29,8320
Mv 184,24,48
Rt 184,29,256
Mv 216,24,52
buffer_load_dwordx4 v14, s[8:11], s39 offen offset:2048 lds
Rt 186,29,8448
Mv 248,24,56
Rt 188,29,384
Ma 248,24,60
Rt 190,29,8576
Ma 252,28,60
Mv 252,28,56
Mv 220,28,52
Mv 188,28,48
Mv 156,28,44
Mv 124,28,40
buffer_load_dwordx4 v15, s[8:11], s39 offen offset:3072 lds
Mv 92,28,36
Mv 60,28,32
s_waitcnt vmcnt(8) lgkmcnt(0)
s_barrier
)" R"(
Mv 32,64,96
Rt 0,18,0
v_xor_b32 v4, s3, v4
v_cmp_gt_u32_e32 vcc, 2, v4
s_andn2_b32 s28, 0xff, vcc_lo
s_cbranch_scc1 .Lzsp1
.Lzok1:
Mv 64,64,100
s_add_u32 s38, s38, 0xffff0080
v_xor_b32 v4, s3, v4
Mv 96,64,104
Rt 2,18,2048
s_add_u32 m0, s36, 32768
Mv 128,64,108
buffer_load_dwordx4 v8, s[4:7], s38 offen offset:0 lds
Mv 160,64,112
Rt 4,19,0
v_and_b32 v4, 1, v4
Mv 192,64,116
v_cmp_eq_u32_e32 vcc, 1, v4
s_mov_b32 s85, vcc_lo
Mv 224,64,120
Rt 6,19,2048
s_and_b32 s28, s2, 7
Ma 224,64,124
buffer_load_dwordx4 v9, s[4:7], s38 offen offset:1024 lds
Ma 228,68,124
Rt 8,20,0
s_lshr_b32 s28, s85, s28
Mv 228,68,120
s_xor_b32 s28, s28, s34
s_and_b32 s28, s28, 1
Mv 196,68,116
Rt 10,20,2048
s_lshl_b32 s84, s28, 13
Mv 164,68,112
buffer_load_dwordx4 v10, s[4:7], s38 offen offset:2048 lds
Mv 132,68,108
Rt 12,21,0
s_lshl1_add_u32 s28, s36, s84
Mv 100,68,104
v_add_u32 v2, s28, v1
v_add_u32 v3, 0x1000, v2
Mv 68,68,100
Rt 14,21,2048
s_bfe_u32 s28, s73, 0x30010
Mv 36,68,96
buffer_load_dwordx4 v11, s[4:7], s38 offen offset:3072 lds
Mv 40,72,96
Rt 16,22,0
s_lshr_b32 s28, s85, s28
Mv 72,72,100
s_add_u32 s38, s38, 0x10000
s_and_b32 s28, s28, 1
Mv 104,72,104
Rt 18,22,2048
s_add_u32 m0, s36, 36864
Mv 136,72,108
buffer_load_dwordx4 v8, s[4:7], s38 offen offset:0 lds
Mv 168,72,112
Rt 20,23,0
s_lshl_b32 s28, s28, 13
Mv 200,72,116
s_add_u32 s73, s73, s28
)" R"(
Mv 232,72,120
Rt 22,23,2048
Ma 232,72,124
buffer_load_dwordx4 v9, s[4:7], s38 offen offset:1024 lds
Ma 236,76,124
Rt 24,24,0
Mv 236,76,120
Mv 204,76,116
Rt 26,24,2048
Mv 172,76,112
buffer_load_dwordx4 v10, s[4:7], s38 offen offset:2048 lds
Mv 140,76,108
Rt 28,25,0
s_lshl3_add_u32 s28, s94, s92
Mv 108,76,104
s_lshl3_add_u32 s29, s92, s94
s_cmp_lt_u32 s94, 8
s_cmov_b32 s28, s29
Mv 76,76,100
Rt 30,25,2048
s_mul_i32 s29, s28, 293
s_lshr_b32 s29, s29, 11
Mv 44,76,96
buffer_load_dwordx4 v11, s[4:7], s38 offen offset:3072 lds
Mv 48,80,96
Rb 32,16,0
s_mul_i32 s29, s29, 9
s_add_u32 s29, s29, s28
Mv 80,80,100
s_lshl_b32 s29, s29, 10
s_add_u32 s29, s29, s82
s_add_u32 s29, s29, s84
Mv 112,80,104
Rb 36,16,2048
s_add_u32 s94, s94, 4
Mv 144,80,108
buffer_store_dwordx4 a[176:179], v1, s[20:23], s29 offen sc0 sc1
Mv 176,80,112
Rb 40,16,8192
s_lshl3_add_u32 s28, s94, s92
Mv 208,80,116
s_lshl3_add_u32 s29, s92, s94
Mv 240,80,120
Rb 44,16,10240
s_cmp_lt_u32 s94, 8
s_cmov_b32 s28, s29
Ma 240,80,124
s_mul_i32 s29, s28, 293
s_lshr_b32 s29, s29, 11
Ma 244,84,124
Rb 48,16,16384
s_mul_i32 s29, s29, 9
s_add_u32 s29, s29, s28
Mv 244,84,120
s_lshl_b32 s29, s29, 10
s_add_u32 s29, s29, s82
Mv 212,84,116
Rb 52,16,18432
s_add_u32 s29, s29, s84
Mv 180,84,112
s_add_u32 s94, s94, 4
buffer_store_dwordx4 a[180:183], v1, s[20:23], s29 offen sc0 sc1
Mv 148,84,108
Rb 56,16,24576
)" R"(
s_lshl3_add_u32 s28, s94, s92
Mv 116,84,104
s_lshl3_add_u32 s29, s92, s94
Mv 84,84,100
Rb 60,16,26624
s_cmp_lt_u32 s94, 8
s_cmov_b32 s28, s29
Mv 52,84,96
s_mul_i32 s29, s28, 293
s_lshr_b32 s29, s29, 11
Mv 56,88,96
s_mul_i32 s29, s29, 9
s_add_u32 s29, s29, s28
Mv 88,88,100
s_lshl_b32 s29, s29, 10
s_add_u32 s29, s29, s82
Mv 120,88,104
s_add_u32 s29, s29, s84
s_add_u32 s94, s94, 4
Mv 152,88,108
buffer_store_dwordx4 a[184:187], v1, s[20:23], s29 offen sc0 sc1
Mv 184,88,112
s_lshl3_add_u32 s28, s94, s92
s_lshl3_add_u32 s29, s92, s94
Mv 216,88,116
s_cmp_lt_u32 s94, 8
s_cmov_b32 s28, s29
Mv 248,88,120
s_mul_i32 s29, s28, 293
s_lshr_b32 s29, s29, 11
Ma 248,88,124
s_mul_i32 s29, s29, 9
s_add_u32 s29, s29, s28
Ma 252,92,124
s_lshl_b32 s29, s29, 10
s_add_u32 s29, s29, s82
Mv 252,92,120
s_add_u32 s29, s29, s84
s_mov_b32 s40, 5
Mv 220,92,116
s_cmp_gt_u32 s33, 1
s_cmov_b32 s29, 0x40000000
Mv 188,92,112
buffer_store_dwordx4 a[188:191], v1, s[20:23], s29 offen sc0 sc1
Mv 156,92,108
Mv 124,92,104
Mv 92,92,100
Mv 60,92,96
s_waitcnt vmcnt(16) lgkmcnt(0)
s_barrier
s_mov_b32 s28, s35
s_lshl2_add_u32 s28, s28, s33
s_mul_i32 s43, s28, 28672
v_and_b32 v26, 0x70, v1
v_and_b32 v4, 48, v0
v_xor_b32 v26, v26, v4
v_lshrrev_b32 v4, 3, v0
v_mul_u32_u24 v4, 896, v4
v_add_u32 v26, v26, v4
v_xor_b32 v27, 64, v26
v_add_u32 v27, 6144, v27
v_bfe_u32 v28, v0, 1, 3
v_lshrrev_b32 v4, 4, v0
v_xor_b32 v28, v28, v4
v_lshlrev_b32 v28, 4, v28
)" R"(
v_and_b32 v4, 15, v0
v_lshl_or_b32 v28, v4, 7, v28
s_add_u32 s28, s36, 0x18000
v_add_u32 v28, s28, v28
v_xor_b32 v29, 64, v28
s_add_u32 m0, s36, 131072
s_add_u32 s44, s43, 0
buffer_load_dwordx4 v26, s[12:15], s44 offen offset:0 lds
buffer_load_dwordx4 v27, s[12:15], s44 offen offset:1024 lds
s_add_u32 s44, s43, 12288
buffer_load_dwordx4 v26, s[12:15], s44 offen offset:2048 lds
buffer_load_dwordx4 v27, s[12:15], s44 offen offset:3072 lds
s_add_u32 m0, s36, 135168
s_add_u32 s44, s43, 128
buffer_load_dwordx4 v26, s[12:15], s44 offen offset:0 lds
buffer_load_dwordx4 v27, s[12:15], s44 offen offset:1024 lds
s_add_u32 s44, s43, 12416
buffer_load_dwordx4 v26, s[12:15], s44 offen offset:2048 lds
buffer_load_dwordx4 v27, s[12:15], s44 offen offset:3072 lds
.LzPAK:
Mv 32,0,32
Rt 64,18,16384
Mv 64,0,36
Mv 96,0,40
Rt 66,18,18432
Mv 128,0,44
s_add_u32 s39, s39, 0x40000
Mv 160,0,48
Rt 68,19,16384
s_add_u32 m0, s37, 49152
Mv 192,0,52
buffer_load_dwordx4 v12, s[8:11], s39 offen offset:0 lds
Mv 224,0,56
Rt 70,19,18432
Ma 224,0,60
Ma 228,4,60
Rt 72,20,16384
Mv 228,4,56
Mv 196,4,52
Rt 74,20,18432
Mv 164,4,48
Mv 132,4,44
Rt 76,21,16384
Mv 100,4,40
buffer_load_dwordx4 v13, s[8:11], s39 offen offset:1024 lds
Mv 68,4,36
Rt 78,21,18432
Mv 36,4,32
Mv 40,8,32
Rt 80,22,16384
Mv 72,8,36
Mv 104,8,40
Rt 82,22,18432
Mv 136,8,44
Mv 168,8,48
Rt 84,23,16384
Mv 200,8,52
buffer_load_dwordx4 v14, s[8:11], s39 offen offset:2048 lds
Mv 232,8,56
Rt 86,23,18432
Ma 232,8,60
Ma 236,12,60
Rt 88,24,16384
Mv 236,12,56
)" R"(
Mv 204,12,52
Rt 90,24,18432
Mv 172,12,48
Mv 140,12,44
Rt 92,25,16384
Mv 108,12,40
buffer_load_dwordx4 v15, s[8:11], s39 offen offset:3072 lds
Mv 76,12,36
Rt 94,25,18432
Mv 44,12,32
Mv 48,16,32
Rb 96,17,0
Mv 80,16,36
Mv 112,16,40
Rb 100,17,2048
Mv 144,16,44
s_add_u32 s39, s39, 0x40000
Mv 176,16,48
Rb 104,17,8192
s_add_u32 m0, s37, 0
Mv 208,16,52
buffer_load_dwordx4 v12, s[8:11], s39 offen offset:0 lds
Mv 240,16,56
Rb 108,17,10240
Ma 240,16,60
Ma 244,20,60
Rb 112,17,16384
Mv 244,20,56
Mv 212,20,52
Rb 116,17,18432
Mv 180,20,48
Mv 148,20,44
Rb 120,17,24576
Mv 116,20,40
buffer_load_dwordx4 v13, s[8:11], s39 offen offset:1024 lds
Mv 84,20,36
Rb 124,17,26624
Mv 52,20,32
Mv 56,24,32
Mv 88,24,36
Mv 120,24,40
Mv 152,24,44
Mv 184,24,48
Mv 216,24,52
buffer_load_dwordx4 v14, s[8:11], s39 offen offset:2048 lds
Mv 248,24,56
Ma 248,24,60
Ma 252,28,60
Mv 252,28,56
Mv 220,28,52
Mv 188,28,48
Mv 156,28,44
Mv 124,28,40
buffer_load_dwordx4 v15, s[8:11], s39 offen offset:3072 lds
Mv 92,28,36
Mv 60,28,32
s_waitcnt vmcnt(8) lgkmcnt(0)
s_barrier
Mv 32,64,96
Rt 0,18,32768
Mv 64,64,100
s_add_u32 s38, s38, 0xffff0080
Mv 96,64,104
Rt 2,18,34816
s_add_u32 m0, s36, 0
)" R"(
Mv 128,64,108
buffer_load_dwordx4 v8, s[4:7], s38 offen offset:0 lds
Mv 160,64,112
Rt 4,19,32768
Mv 192,64,116
Mv 224,64,120
Rt 6,19,34816
Ma 224,64,124
buffer_load_dwordx4 v9, s[4:7], s38 offen offset:1024 lds
Ma 228,68,124
Rt 8,20,32768
Mv 228,68,120
Mv 196,68,116
Rt 10,20,34816
Mv 164,68,112
buffer_load_dwordx4 v10, s[4:7], s38 offen offset:2048 lds
Mv 132,68,108
Rt 12,21,32768
Mv 100,68,104
Mv 68,68,100
Rt 14,21,34816
Mv 36,68,96
buffer_load_dwordx4 v11, s[4:7], s38 offen offset:3072 lds
Mv 40,72,96
Rt 16,22,32768
Mv 72,72,100
s_add_u32 s38, s38, 0x10000
Mv 104,72,104
Rt 18,22,34816
s_add_u32 m0, s36, 4096
Mv 136,72,108
buffer_load_dwordx4 v8, s[4:7], s38 offen offset:0 lds
Mv 168,72,112
Rt 20,23,32768
Mv 200,72,116
Mv 232,72,120
Rt 22,23,34816
Ma 232,72,124
buffer_load_dwordx4 v9, s[4:7], s38 offen offset:1024 lds
Ma 236,76,124
Rt 24,24,32768
Mv 236,76,120
Mv 204,76,116
Rt 26,24,34816
Mv 172,76,112
buffer_load_dwordx4 v10, s[4:7], s38 offen offset:2048 lds
Mv 140,76,108
Rt 28,25,32768
Mv 108,76,104
Mv 76,76,100
Rt 30,25,34816
Mv 44,76,96
buffer_load_dwordx4 v11, s[4:7], s38 offen offset:3072 lds
Mv 48,80,96
Rb 32,16,32768
Mv 80,80,100
Mv 112,80,104
Rb 36,16,34816
Mv 144,80,108
Mv 176,80,112
Rb 40,16,40960
Mv 208,80,116
Mv 240,80,120
Rb 44,16,43008
)" R"(
Ma 240,80,124
Ma 244,84,124
Rb 48,16,49152
Mv 244,84,120
Mv 212,84,116
Rb 52,16,51200
Mv 180,84,112
Mv 148,84,108
Rb 56,16,57344
Mv 116,84,104
Mv 84,84,100
Rb 60,16,59392
Mv 52,84,96
Mv 56,88,96
Mv 88,88,100
Mv 120,88,104
Mv 152,88,108
Mv 184,88,112
Mv 216,88,116
Mv 248,88,120
Ma 248,88,124
Ma 252,92,124
Mv 252,92,120
Mv 220,92,116
Mv 188,92,112
Mv 156,92,108
Mv 124,92,104
Mv 92,92,100
Mv 60,92,96
s_waitcnt vmcnt(12) lgkmcnt(0)
s_barrier
Mv 32,0,32
Rt 64,18,49152
Mv 64,0,36
Mv 96,0,40
Rt 66,18,51200
Mv 128,0,44
s_add_u32 s39, s39, 0x40000
Mv 160,0,48
Rt 68,19,49152
s_add_u32 m0, s37, 16384
Mv 192,0,52
buffer_load_dwordx4 v12, s[8:11], s39 offen offset:0 lds
Mv 224,0,56
Rt 70,19,51200
Ma 224,0,60
Ma 228,4,60
Rt 72,20,49152
Mv 228,4,56
Mv 196,4,52
Rt 74,20,51200
Mv 164,4,48
Mv 132,4,44
Rt 76,21,49152
Mv 100,4,40
buffer_load_dwordx4 v13, s[8:11], s39 offen offset:1024 lds
Mv 68,4,36
Rt 78,21,51200
Mv 36,4,32
Mv 40,8,32
Rt 80,22,49152
Mv 72,8,36
Mv 104,8,40
Rt 82,22,51200
Mv 136,8,44
)" R"(
Mv 168,8,48
Rt 84,23,49152
Mv 200,8,52
buffer_load_dwordx4 v14, s[8:11], s39 offen offset:2048 lds
Mv 232,8,56
Rt 86,23,51200
Ma 232,8,60
Ma 236,12,60
Rt 88,24,49152
Mv 236,12,56
Mv 204,12,52
Rt 90,24,51200
Mv 172,12,48
Mv 140,12,44
Rt 92,25,49152
Mv 108,12,40
buffer_load_dwordx4 v15, s[8:11], s39 offen offset:3072 lds
Mv 76,12,36
Rt 94,25,51200
Mv 44,12,32
Mv 48,16,32
Rb 96,17,32768
Mv 80,16,36
Mv 112,16,40
Rb 100,17,34816
Mv 144,16,44
s_add_u32 s39, s39, 0x40000
Mv 176,16,48
Rb 104,17,40960
s_add_u32 m0, s37, 32768
Mv 208,16,52
buffer_load_dwordx4 v12, s[8:11], s39 offen offset:0 lds
Mv 240,16,56
Rb 108,17,43008
Ma 240,16,60
Ma 244,20,60
Rb 112,17,49152
Mv 244,20,56
Mv 212,20,52
Rb 116,17,51200
Mv 180,20,48
Mv 148,20,44
Rb 120,17,57344
Mv 116,20,40
buffer_load_dwordx4 v13, s[8:11], s39 offen offset:1024 lds
Mv 84,20,36
Rb 124,17,59392
Mv 52,20,32
Mv 56,24,32
Mv 88,24,36
Mv 120,24,40
Mv 152,24,44
Mv 184,24,48
Mv 216,24,52
buffer_load_dwordx4 v14, s[8:11], s39 offen offset:2048 lds
Mv 248,24,56
Ma 248,24,60
Ma 252,28,60
Mv 252,28,56
Mv 220,28,52
Mv 188,28,48
Mv 156,28,44
Mv 124,28,40
buffer_load_dwordx4 v15, s[8:11], s39 offen offset:3072 lds
)" R"(
Mv 92,28,36
Mv 60,28,32
s_waitcnt vmcnt(8) lgkmcnt(0)
s_barrier
Mv 32,64,96
Rt 0,18,0
Mv 64,64,100
s_add_u32 s38, s38, 0xffff0080
Mv 96,64,104
Rt 2,18,2048
s_add_u32 m0, s36, 32768
Mv 128,64,108
buffer_load_dwordx4 v8, s[4:7], s38 offen offset:0 lds
Mv 160,64,112
Rt 4,19,0
Mv 192,64,116
Mv 224,64,120
Rt 6,19,2048
Ma 224,64,124
buffer_load_dwordx4 v9, s[4:7], s38 offen offset:1024 lds
Ma 228,68,124
Rt 8,20,0
Mv 228,68,120
Mv 196,68,116
Rt 10,20,2048
Mv 164,68,112
buffer_load_dwordx4 v10, s[4:7], s38 offen offset:2048 lds
Mv 132,68,108
Rt 12,21,0
Mv 100,68,104
Mv 68,68,100
Rt 14,21,2048
Mv 36,68,96
buffer_load_dwordx4 v11, s[4:7], s38 offen offset:3072 lds
Mv 40,72,96
Rt 16,22,0
Mv 72,72,100
s_add_u32 s38, s38, 0x10000
Mv 104,72,104
Rt 18,22,2048
s_add_u32 m0, s36, 36864
Mv 136,72,108
buffer_load_dwordx4 v8, s[4:7], s38 offen offset:0 lds
Mv 168,72,112
Rt 20,23,0
Mv 200,72,116
Mv 232,72,120
Rt 22,23,2048
Ma 232,72,124
buffer_load_dwordx4 v9, s[4:7], s38 offen offset:1024 lds
Ma 236,76,124
Rt 24,24,0
Mv 236,76,120
Mv 204,76,116
Rt 26,24,2048
Mv 172,76,112
buffer_load_dwordx4 v10, s[4:7], s38 offen offset:2048 lds
Mv 140,76,108
Rt 28,25,0
Mv 108,76,104
Mv 76,76,100
Rt 30,25,2048
Mv 44,76,96
buffer_load_dwordx4 v11, s[4:7], s38 offen offset:3072 lds
Mv 48,80,96
)" R"(
Rb 32,16,0
Mv 80,80,100
Mv 112,80,104
Rb 36,16,2048
Mv 144,80,108
Mv 176,80,112
Rb 40,16,8192
Mv 208,80,116
Mv 240,80,120
Rb 44,16,10240
Ma 240,80,124
Ma 244,84,124
Rb 48,16,16384
Mv 244,84,120
Mv 212,84,116
Rb 52,16,18432
Mv 180,84,112
Mv 148,84,108
Rb 56,16,24576
Mv 116,84,104
Mv 84,84,100
Rb 60,16,26624
Mv 52,84,96
Mv 56,88,96
Mv 88,88,100
Mv 120,88,104
Mv 152,88,108
Mv 184,88,112
Mv 216,88,116
Mv 248,88,120
Ma 248,88,124
Ma 252,92,124
Mv 252,92,120
Mv 220,92,116
s_sub_u32 s40, s40, 1
Mv 188,92,112
Mv 156,92,108
Mv 124,92,104
Mv 92,92,100
Mv 60,92,96
s_cmp_lg_u32 s40, 0
s_waitcnt vmcnt(12) lgkmcnt(0)
s_barrier
s_cbranch_scc1 .LzPAK
s_branch .LzPAKX
.LzAV:
Mz 32,32,0
Rt 64,18,16384
Mz 36,36,0
Mz 96,40,0
Rt 66,18,18432
Mz 100,44,0
s_add_u32 s39, s39, 0x40000
Mz 160,48,0
Rt 68,19,16384
s_add_u32 m0, s37, 49152
Mz 164,52,0
buffer_load_dwordx4 v12, s[8:11], s39 offen offset:0 lds
Mz 224,56,0
Rt 70,19,18432
Mz 228,60,0
Mz 236,60,4
Rt 72,20,16384
Mz 232,56,4
Mz 172,52,4
)" R"(
Rt 74,20,18432
Mz 168,48,4
Mz 108,44,4
Rt 76,21,16384
Mz 104,40,4
buffer_load_dwordx4 v13, s[8:11], s39 offen offset:1024 lds
Mz 44,36,4
Rt 78,21,18432
Mz 40,32,4
Mz 48,32,8
Rt 80,22,16384
Mz 52,36,8
Mz 112,40,8
Rt 82,22,18432
Mz 116,44,8
Mz 176,48,8
Rt 84,23,16384
Mz 180,52,8
buffer_load_dwordx4 v14, s[8:11], s39 offen offset:2048 lds
Mz 240,56,8
Rt 86,23,18432
Mz 244,60,8
Mz 252,60,12
Rt 88,24,16384
Mz 248,56,12
Mz 188,52,12
Rt 90,24,18432
Mz 184,48,12
Mz 124,44,12
Rt 92,25,16384
Mz 120,40,12
buffer_load_dwordx4 v15, s[8:11], s39 offen offset:3072 lds
Mz 60,36,12
Rt 94,25,18432
Mz 56,32,12
Mz 64,32,16
Rb 96,17,0
Mz 68,36,16
Mz 128,40,16
Rb 100,17,2048
Mz 132,44,16
s_add_u32 s39, s39, 0x40000
Mz 192,48,16
Rb 104,17,8192
s_add_u32 m0, s37, 0
Mz 196,52,16
buffer_load_dwordx4 v12, s[8:11], s39 offen offset:0 lds
v_mfma_f32_16x16x32_bf16 a[224:227], a[56:59], a[16:19], 0
Rb 108,17,10240
v_mfma_f32_16x16x32_bf16 a[228:231], a[60:63], a[16:19], 0
v_mfma_f32_16x16x32_bf16 a[236:239], a[60:63], a[20:23], 0
Rb 112,17,16384
v_mfma_f32_16x16x32_bf16 a[232:235], a[56:59], a[20:23], 0
Mz 204,52,20
Rb 116,17,18432
Mz 200,48,20
Mz 140,44,20
Rb 120,17,24576
Mz 136,40,20
buffer_load_dwordx4 v13, s[8:11], s39 offen offset:1024 lds
Mz 76,36,20
Rb 124,17,26624
Mz 72,32,20
Mz 80,32,24
)" R"(
Mz 84,36,24
Mz 144,40,24
Mz 148,44,24
Mz 208,48,24
Mz 212,52,24
buffer_load_dwordx4 v14, s[8:11], s39 offen offset:2048 lds
v_mfma_f32_16x16x32_bf16 a[240:243], a[56:59], a[24:27], 0
v_mfma_f32_16x16x32_bf16 a[244:247], a[60:63], a[24:27], 0
v_mfma_f32_16x16x32_bf16 a[252:255], a[60:63], a[28:31], 0
v_mfma_f32_16x16x32_bf16 a[248:251], a[56:59], a[28:31], 0
Mz 220,52,28
Mz 216,48,28
Mz 156,44,28
Mz 152,40,28
buffer_load_dwordx4 v15, s[8:11], s39 offen offset:3072 lds
Mz 92,36,28
Mz 88,32,28
s_waitcnt vmcnt(12) lgkmcnt(0)
s_barrier
Mv 32,96,64
Rt 0,18,32768
Mv 36,100,64
s_add_u32 s38, s38, 0xffff0080
Mv 96,104,64
Rt 2,18,34816
s_add_u32 m0, s36, 0
Mv 100,108,64
buffer_load_dwordx4 v8, s[4:7], s38 offen offset:0 lds
Mv 160,112,64
Rt 4,19,32768
Mv 164,116,64
Mv 224,120,64
Rt 6,19,34816
Mv 228,124,64
buffer_load_dwordx4 v9, s[4:7], s38 offen offset:1024 lds
Mv 236,124,68
Rt 8,20,32768
Mv 232,120,68
Mv 172,116,68
Rt 10,20,34816
Mv 168,112,68
buffer_load_dwordx4 v10, s[4:7], s38 offen offset:2048 lds
Mv 108,108,68
Rt 12,21,32768
Mv 104,104,68
Mv 44,100,68
Rt 14,21,34816
Mv 40,96,68
buffer_load_dwordx4 v11, s[4:7], s38 offen offset:3072 lds
Mv 48,96,72
Rt 16,22,32768
Mv 52,100,72
s_add_u32 s38, s38, 0x10000
Mv 112,104,72
Rt 18,22,34816
s_add_u32 m0, s36, 4096
Mv 116,108,72
buffer_load_dwordx4 v8, s[4:7], s38 offen offset:0 lds
Mv 176,112,72
Rt 20,23,32768
Mv 180,116,72
Mv 240,120,72
Rt 22,23,34816
Mv 244,124,72
buffer_load_dwordx4 v9, s[4:7], s38 offen offset:1024 lds
)" R"(
Mv 252,124,76
Rt 24,24,32768
Mv 248,120,76
Mv 188,116,76
Rt 26,24,34816
Mv 184,112,76
buffer_load_dwordx4 v10, s[4:7], s38 offen offset:2048 lds
Mv 124,108,76
Rt 28,25,32768
Mv 120,104,76
Mv 60,100,76
Rt 30,25,34816
Mv 56,96,76
buffer_load_dwordx4 v11, s[4:7], s38 offen offset:3072 lds
Mv 64,96,80
Rb 32,16,32768
Mv 68,100,80
Mv 128,104,80
Rb 36,16,34816
Mv 132,108,80
Mv 192,112,80
Rb 40,16,40960
Mv 196,116,80
Ma 224,120,80
Rb 44,16,43008
Ma 228,124,80
Ma 236,124,84
Rb 48,16,49152
Ma 232,120,84
Mv 204,116,84
Rb 52,16,51200
Mv 200,112,84
Mv 140,108,84
Rb 56,16,57344
Mv 136,104,84
Mv 76,100,84
Rb 60,16,59392
Mv 72,96,84
Mv 80,96,88
Mv 84,100,88
Mv 144,104,88
Mv 148,108,88
Mv 208,112,88
Mv 212,116,88
Ma 240,120,88
Ma 244,124,88
Ma 252,124,92
Ma 248,120,92
Mv 220,116,92
Mv 216,112,92
Mv 156,108,92
Mv 152,104,92
Mv 92,100,92
Mv 88,96,92
s_waitcnt vmcnt(12) lgkmcnt(0)
s_barrier
Mv 32,32,0
Rt 64,18,49152
s_mov_b32 s85, 0xc108800
v_and_b32 v2, 7, v0
Mv 36,36,0
v_lshlrev_b32 v2, 7, v2
Mv 96,40,0
Rt 66,18,51200
buffer_load_dword v4, v2, s[20:23], s85 offen sc0 sc1
)" R"(
Mv 100,44,0
s_add_u32 s39, s39, 0x40000
Mv 160,48,0
Rt 68,19,49152
s_add_u32 m0, s37, 16384
Mv 164,52,0
buffer_load_dwordx4 v12, s[8:11], s39 offen offset:0 lds
Mv 224,56,0
Rt 70,19,51200
Mv 228,60,0
Mv 236,60,4
Rt 72,20,49152
Mv 232,56,4
Mv 172,52,4
Rt 74,20,51200
Mv 168,48,4
Mv 108,44,4
Rt 76,21,49152
Mv 104,40,4
buffer_load_dwordx4 v13, s[8:11], s39 offen offset:1024 lds
Mv 44,36,4
Rt 78,21,51200
Mv 40,32,4
Mv 48,32,8
Rt 80,22,49152
Mv 52,36,8
Mv 112,40,8
Rt 82,22,51200
Mv 116,44,8
Mv 176,48,8
Rt 84,23,49152
Mv 180,52,8
buffer_load_dwordx4 v14, s[8:11], s39 offen offset:2048 lds
Mv 240,56,8
Rt 86,23,51200
Mv 244,60,8
Mv 252,60,12
Rt 88,24,49152
Mv 248,56,12
Mv 188,52,12
Rt 90,24,51200
Mv 184,48,12
Mv 124,44,12
Rt 92,25,49152
Mv 120,40,12
buffer_load_dwordx4 v15, s[8:11], s39 offen offset:3072 lds
Mv 60,36,12
Rt 94,25,51200
Mv 56,32,12
Mv 64,32,16
Rb 96,17,32768
Mv 68,36,16
Mv 128,40,16
Rb 100,17,34816
Mv 132,44,16
s_add_u32 s39, s39, 0x40000
Mv 192,48,16
Rb 104,17,40960
s_add_u32 m0, s37, 32768
Mv 196,52,16
buffer_load_dwordx4 v12, s[8:11], s39 offen offset:0 lds
Ma 224,56,16
Rb 108,17,43008
Ma 228,60,16
)" R"(
Ma 236,60,20
Rb 112,17,49152
Ma 232,56,20
Mv 204,52,20
Rb 116,17,51200
Mv 200,48,20
Mv 140,44,20
Rb 120,17,57344
Mv 136,40,20
buffer_load_dwordx4 v13, s[8:11], s39 offen offset:1024 lds
Mv 76,36,20
Rb 124,17,59392
Mv 72,32,20
Mv 80,32,24
Rt 176,29,0
Mv 84,36,24
Rt 178,29,8192
Mv 144,40,24
Rt 180,29,128
Mv 148,44,24
Rt 182,29,8320
Mv 208,48,24
Rt 184,29,256
Mv 212,52,24
buffer_load_dwordx4 v14, s[8:11], s39 offen offset:2048 lds
Rt 186,29,8448
Ma 240,56,24
Rt 188,29,384
Ma 244,60,24
Rt 190,29,8576
Ma 252,60,28
Ma 248,56,28
Mv 220,52,28
Mv 216,48,28
Mv 156,44,28
Mv 152,40,28
buffer_load_dwordx4 v15, s[8:11], s39 offen offset:3072 lds
Mv 92,36,28
Mv 88,32,28
s_waitcnt vmcnt(8) lgkmcnt(0)
s_barrier
Mv 32,96,64
Rt 0,18,0
v_xor_b32 v4, s3, v4
v_cmp_gt_u32_e32 vcc, 2, v4
s_andn2_b32 s28, 0xff, vcc_lo
s_cbranch_scc1 .Lzsp2
.Lzok2:
Mv 36,100,64
s_add_u32 s38, s38, 0xffff0080
v_xor_b32 v4, s3, v4
Mv 96,104,64
Rt 2,18,2048
s_add_u32 m0, s36, 32768
Mv 100,108,64
buffer_load_dwordx4 v8, s[4:7], s38 offen offset:0 lds
Mv 160,112,64
Rt 4,19,0
v_and_b32 v4, 1, v4
Mv 164,116,64
v_cmp_eq_u32_e32 vcc, 1, v4
s_mov_b32 s85, vcc_lo
Mv 224,120,64
Rt 6,19,2048
s_and_b32 s28, s2, 7
)" R"(
Mv 228,124,64
buffer_load_dwordx4 v9, s[4:7], s38 offen offset:1024 lds
Mv 236,124,68
Rt 8,20,0
s_lshr_b32 s28, s85, s28
Mv 232,120,68
s_xor_b32 s28, s28, s34
s_and_b32 s28, s28, 1
Mv 172,116,68
Rt 10,20,2048
s_lshl_b32 s84, s28, 13
Mv 168,112,68
buffer_load_dwordx4 v10, s[4:7], s38 offen offset:2048 lds
Mv 108,108,68
Rt 12,21,0
s_lshl1_add_u32 s28, s36, s84
Mv 104,104,68
v_add_u32 v2, s28, v1
v_add_u32 v3, 0x1000, v2
Mv 44,100,68
Rt 14,21,2048
s_bfe_u32 s28, s73, 0x30010
Mv 40,96,68
buffer_load_dwordx4 v11, s[4:7], s38 offen offset:3072 lds
Mv 48,96,72
Rt 16,22,0
s_lshr_b32 s28, s85, s28
Mv 52,100,72
s_add_u32 s38, s38, 0x10000
s_and_b32 s28, s28, 1
Mv 112,104,72
Rt 18,22,2048
s_add_u32 m0, s36, 36864
Mv 116,108,72
buffer_load_dwordx4 v8, s[4:7], s38 offen offset:0 lds
Mv 176,112,72
Rt 20,23,0
s_lshl_b32 s28, s28, 13
Mv 180,116,72
s_add_u32 s73, s73, s28
Mv 240,120,72
Rt 22,23,2048
Mv 244,124,72
buffer_load_dwordx4 v9, s[4:7], s38 offen offset:1024 lds
Mv 252,124,76
Rt 24,24,0
Mv 248,120,76
Mv 188,116,76
Rt 26,24,2048
Mv 184,112,76
buffer_load_dwordx4 v10, s[4:7], s38 offen offset:2048 lds
Mv 124,108,76
Rt 28,25,0
s_lshl3_add_u32 s28, s94, s92
Mv 120,104,76
s_lshl3_add_u32 s29, s92, s94
s_cmp_lt_u32 s94, 8
s_cmov_b32 s28, s29
Mv 60,100,76
Rt 30,25,2048
s_mul_i32 s29, s28, 293
s_lshr_b32 s29, s29, 11
Mv 56,96,76
buffer_load_dwordx4 v11, s[4:7], s38 offen offset:3072 lds
)" R"(
Mv 64,96,80
Rb 32,16,0
s_mul_i32 s29, s29, 9
s_add_u32 s29, s29, s28
Mv 68,100,80
s_lshl_b32 s29, s29, 10
s_add_u32 s29, s29, s82
s_add_u32 s29, s29, s84
Mv 128,104,80
Rb 36,16,2048
s_add_u32 s94, s94, 4
Mv 132,108,80
buffer_store_dwordx4 a[176:179], v1, s[20:23], s29 offen sc0 sc1
Mv 192,112,80
Rb 40,16,8192
s_lshl3_add_u32 s28, s94, s92
Mv 196,116,80
s_lshl3_add_u32 s29, s92, s94
Ma 224,120,80
Rb 44,16,10240
s_cmp_lt_u32 s94, 8
s_cmov_b32 s28, s29
Ma 228,124,80
s_mul_i32 s29, s28, 293
s_lshr_b32 s29, s29, 11
Ma 236,124,84
Rb 48,16,16384
s_mul_i32 s29, s29, 9
s_add_u32 s29, s29, s28
Ma 232,120,84
s_lshl_b32 s29, s29, 10
s_add_u32 s29, s29, s82
Mv 204,116,84
Rb 52,16,18432
s_add_u32 s29, s29, s84
Mv 200,112,84
s_add_u32 s94, s94, 4
buffer_store_dwordx4 a[180:183], v1, s[20:23], s29 offen sc0 sc1
Mv 140,108,84
Rb 56,16,24576
s_lshl3_add_u32 s28, s94, s92
Mv 136,104,84
s_lshl3_add_u32 s29, s92, s94
Mv 76,100,84
Rb 60,16,26624
s_cmp_lt_u32 s94, 8
s_cmov_b32 s28, s29
Mv 72,96,84
s_mul_i32 s29, s28, 293
s_lshr_b32 s29, s29, 11
Mv 80,96,88
s_mul_i32 s29, s29, 9
s_add_u32 s29, s29, s28
Mv 84,100,88
s_lshl_b32 s29, s29, 10
s_add_u32 s29, s29, s82
Mv 144,104,88
s_add_u32 s29, s29, s84
s_add_u32 s94, s94, 4
Mv 148,108,88
buffer_store_dwordx4 a[184:187], v1, s[20:23], s29 offen sc0 sc1
Mv 208,112,88
s_lshl3_add_u32 s28, s94, s92
s_lshl3_add_u32 s29, s92, s94
)" R"(
Mv 212,116,88
s_cmp_lt_u32 s94, 8
s_cmov_b32 s28, s29
Ma 240,120,88
s_mul_i32 s29, s28, 293
s_lshr_b32 s29, s29, 11
Ma 244,124,88
s_mul_i32 s29, s29, 9
s_add_u32 s29, s29, s28
Ma 252,124,92
s_lshl_b32 s29, s29, 10
s_add_u32 s29, s29, s82
Ma 248,120,92
s_add_u32 s29, s29, s84
s_mov_b32 s40, 5
Mv 220,116,92
s_cmp_gt_u32 s33, 1
s_cmov_b32 s29, 0x40000000
Mv 216,112,92
buffer_store_dwordx4 a[188:191], v1, s[20:23], s29 offen sc0 sc1
Mv 156,108,92
Mv 152,104,92
Mv 92,100,92
Mv 88,96,92
s_waitcnt vmcnt(16) lgkmcnt(0)
s_barrier
s_mov_b32 s28, s35
s_lshl2_add_u32 s28, s28, s33
s_mul_i32 s43, s28, 28672
v_and_b32 v26, 0x70, v1
v_and_b32 v4, 48, v0
v_xor_b32 v26, v26, v4
v_lshrrev_b32 v4, 3, v0
v_mul_u32_u24 v4, 896, v4
v_add_u32 v26, v26, v4
v_xor_b32 v27, 64, v26
v_add_u32 v27, 6144, v27
v_bfe_u32 v28, v0, 1, 3
v_lshrrev_b32 v4, 4, v0
v_xor_b32 v28, v28, v4
v_lshlrev_b32 v28, 4, v28
v_and_b32 v4, 15, v0
v_lshl_or_b32 v28, v4, 7, v28
s_add_u32 s28, s36, 0x18000
v_add_u32 v28, s28, v28
v_xor_b32 v29, 64, v28
s_add_u32 m0, s36, 131072
s_add_u32 s44, s43, 0
buffer_load_dwordx4 v26, s[12:15], s44 offen offset:0 lds
buffer_load_dwordx4 v27, s[12:15], s44 offen offset:1024 lds
s_add_u32 s44, s43, 12288
buffer_load_dwordx4 v26, s[12:15], s44 offen offset:2048 lds
buffer_load_dwordx4 v27, s[12:15], s44 offen offset:3072 lds
s_add_u32 m0, s36, 135168
s_add_u32 s44, s43, 128
buffer_load_dwordx4 v26, s[12:15], s44 offen offset:0 lds
buffer_load_dwordx4 v27, s[12:15], s44 offen offset:1024 lds
s_add_u32 s44, s43, 12416
buffer_load_dwordx4 v26, s[12:15], s44 offen offset:2048 lds
buffer_load_dwordx4 v27, s[12:15], s44 offen offset:3072 lds
.LzPAV:
Mv 32,32,0
Rt 64,18,16384
Mv 36,36,0
Mv 96,40,0
)" R"(
Rt 66,18,18432
Mv 100,44,0
s_add_u32 s39, s39, 0x40000
Mv 160,48,0
Rt 68,19,16384
s_add_u32 m0, s37, 49152
Mv 164,52,0
buffer_load_dwordx4 v12, s[8:11], s39 offen offset:0 lds
Mv 224,56,0
Rt 70,19,18432
Mv 228,60,0
Mv 236,60,4
Rt 72,20,16384
Mv 232,56,4
Mv 172,52,4
Rt 74,20,18432
Mv 168,48,4
Mv 108,44,4
Rt 76,21,16384
Mv 104,40,4
buffer_load_dwordx4 v13, s[8:11], s39 offen offset:1024 lds
Mv 44,36,4
Rt 78,21,18432
Mv 40,32,4
Mv 48,32,8
Rt 80,22,16384
Mv 52,36,8
Mv 112,40,8
Rt 82,22,18432
Mv 116,44,8
Mv 176,48,8
Rt 84,23,16384
Mv 180,52,8
buffer_load_dwordx4 v14, s[8:11], s39 offen offset:2048 lds
Mv 240,56,8
Rt 86,23,18432
Mv 244,60,8
Mv 252,60,12
Rt 88,24,16384
Mv 248,56,12
Mv 188,52,12
Rt 90,24,18432
Mv 184,48,12
Mv 124,44,12
Rt 92,25,16384
Mv 120,40,12
buffer_load_dwordx4 v15, s[8:11], s39 offen offset:3072 lds
Mv 60,36,12
Rt 94,25,18432
Mv 56,32,12
Mv 64,32,16
Rb 96,17,0
Mv 68,36,16
Mv 128,40,16
Rb 100,17,2048
Mv 132,44,16
s_add_u32 s39, s39, 0x40000
Mv 192,48,16
Rb 104,17,8192
s_add_u32 m0, s37, 0
Mv 196,52,16
buffer_load_dwordx4 v12, s[8:11], s39 offen offset:0 lds
Ma 224,56,16
Rb 108,17,10240
)" R"(
Ma 228,60,16
Ma 236,60,20
Rb 112,17,16384
Ma 232,56,20
Mv 204,52,20
Rb 116,17,18432
Mv 200,48,20
Mv 140,44,20
Rb 120,17,24576
Mv 136,40,20
buffer_load_dwordx4 v13, s[8:11], s39 offen offset:1024 lds
Mv 76,36,20
Rb 124,17,26624
Mv 72,32,20
Mv 80,32,24
Mv 84,36,24
Mv 144,40,24
Mv 148,44,24
Mv 208,48,24
Mv 212,52,24
buffer_load_dwordx4 v14, s[8:11], s39 offen offset:2048 lds
Ma 240,56,24
Ma 244,60,24
Ma 252,60,28
Ma 248,56,28
Mv 220,52,28
Mv 216,48,28
Mv 156,44,28
Mv 152,40,28
buffer_load_dwordx4 v15, s[8:11], s39 offen offset:3072 lds
Mv 92,36,28
Mv 88,32,28
s_waitcnt vmcnt(8) lgkmcnt(0)
s_barrier
Mv 32,96,64
Rt 0,18,32768
Mv 36,100,64
s_add_u32 s38, s38, 0xffff0080
Mv 96,104,64
Rt 2,18,34816
s_add_u32 m0, s36, 0
Mv 100,108,64
buffer_load_dwordx4 v8, s[4:7], s38 offen offset:0 lds
Mv 160,112,64
Rt 4,19,32768
Mv 164,116,64
Mv 224,120,64
Rt 6,19,34816
Mv 228,124,64
buffer_load_dwordx4 v9, s[4:7], s38 offen offset:1024 lds
Mv 236,124,68
Rt 8,20,32768
Mv 232,120,68
Mv 172,116,68
Rt 10,20,34816
Mv 168,112,68
buffer_load_dwordx4 v10, s[4:7], s38 offen offset:2048 lds
Mv 108,108,68
Rt 12,21,32768
Mv 104,104,68
Mv 44,100,68
Rt 14,21,34816
Mv 40,96,68
buffer_load_dwordx4 v11, s[4:7], s38 offen offset:3072 lds
Mv 48,96,72
)" R"(
Rt 16,22,32768
Mv 52,100,72
s_add_u32 s38, s38, 0x10000
Mv 112,104,72
Rt 18,22,34816
s_add_u32 m0, s36, 4096
Mv 116,108,72
buffer_load_dwordx4 v8, s[4:7], s38 offen offset:0 lds
Mv 176,112,72
Rt 20,23,32768
Mv 180,116,72
Mv 240,120,72
Rt 22,23,34816
Mv 244,124,72
buffer_load_dwordx4 v9, s[4:7], s38 offen offset:1024 lds
Mv 252,124,76
Rt 24,24,32768
Mv 248,120,76
Mv 188,116,76
Rt 26,24,34816
Mv 184,112,76
buffer_load_dwordx4 v10, s[4:7], s38 offen offset:2048 lds
Mv 124,108,76
Rt 28,25,32768
Mv 120,104,76
Mv 60,100,76
Rt 30,25,34816
Mv 56,96,76
buffer_load_dwordx4 v11, s[4:7], s38 offen offset:3072 lds
Mv 64,96,80
Rb 32,16,32768
Mv 68,100,80
Mv 128,104,80
Rb 36,16,34816
Mv 132,108,80
Mv 192,112,80
Rb 40,16,40960
Mv 196,116,80
Ma 224,120,80
Rb 44,16,43008
Ma 228,124,80
Ma 236,124,84
Rb 48,16,49152
Ma 232,120,84
Mv 204,116,84
Rb 52,16,51200
Mv 200,112,84
Mv 140,108,84
Rb 56,16,57344
Mv 136,104,84
Mv 76,100,84
Rb 60,16,59392
Mv 72,96,84
Mv 80,96,88
Mv 84,100,88
Mv 144,104,88
Mv 148,108,88
Mv 208,112,88
Mv 212,116,88
Ma 240,120,88
Ma 244,124,88
Ma 252,124,92
Ma 248,120,92
Mv 220,116,92
)" R"(
Mv 216,112,92
Mv 156,108,92
Mv 152,104,92
Mv 92,100,92
Mv 88,96,92
s_waitcnt vmcnt(12) lgkmcnt(0)
s_barrier
Mv 32,32,0
Rt 64,18,49152
Mv 36,36,0
Mv 96,40,0
Rt 66,18,51200
Mv 100,44,0
s_add_u32 s39, s39, 0x40000
Mv 160,48,0
Rt 68,19,49152
s_add_u32 m0, s37, 16384
Mv 164,52,0
buffer_load_dwordx4 v12, s[8:11], s39 offen offset:0 lds
Mv 224,56,0
Rt 70,19,51200
Mv 228,60,0
Mv 236,60,4
Rt 72,20,49152
Mv 232,56,4
Mv 172,52,4
Rt 74,20,51200
Mv 168,48,4
Mv 108,44,4
Rt 76,21,49152
Mv 104,40,4
buffer_load_dwordx4 v13, s[8:11], s39 offen offset:1024 lds
Mv 44,36,4
Rt 78,21,51200
Mv 40,32,4
Mv 48,32,8
Rt 80,22,49152
Mv 52,36,8
Mv 112,40,8
Rt 82,22,51200
Mv 116,44,8
Mv 176,48,8
Rt 84,23,49152
Mv 180,52,8
buffer_load_dwordx4 v14, s[8:11], s39 offen offset:2048 lds
Mv 240,56,8
Rt 86,23,51200
Mv 244,60,8
Mv 252,60,12
Rt 88,24,49152
Mv 248,56,12
Mv 188,52,12
Rt 90,24,51200
Mv 184,48,12
Mv 124,44,12
Rt 92,25,49152
Mv 120,40,12
buffer_load_dwordx4 v15, s[8:11], s39 offen offset:3072 lds
Mv 60,36,12
Rt 94,25,51200
Mv 56,32,12
Mv 64,32,16
Rb 96,17,32768
Mv 68,36,16
Mv 128,40,16
)" R"(
Rb 100,17,34816
Mv 132,44,16
s_add_u32 s39, s39, 0x40000
Mv 192,48,16
Rb 104,17,40960
s_add_u32 m0, s37, 32768
Mv 196,52,16
buffer_load_dwordx4 v12, s[8:11], s39 offen offset:0 lds
Ma 224,56,16
Rb 108,17,43008
Ma 228,60,16
Ma 236,60,20
Rb 112,17,49152
Ma 232,56,20
Mv 204,52,20
Rb 116,17,51200
Mv 200,48,20
Mv 140,44,20
Rb 120,17,57344
Mv 136,40,20
buffer_load_dwordx4 v13, s[8:11], s39 offen offset:1024 lds
Mv 76,36,20
Rb 124,17,59392
Mv 72,32,20
Mv 80,32,24
Mv 84,36,24
Mv 144,40,24
Mv 148,44,24
Mv 208,48,24
Mv 212,52,24
buffer_load_dwordx4 v14, s[8:11], s39 offen offset:2048 lds
Ma 240,56,24
Ma 244,60,24
Ma 252,60,28
Ma 248,56,28
Mv 220,52,28
Mv 216,48,28
Mv 156,44,28
Mv 152,40,28
buffer_load_dwordx4 v15, s[8:11], s39 offen offset:3072 lds
Mv 92,36,28
Mv 88,32,28
s_waitcnt vmcnt(8) lgkmcnt(0)
s_barrier
Mv 32,96,64
Rt 0,18,0
Mv 36,100,64
s_add_u32 s38, s38, 0xffff0080
Mv 96,104,64
Rt 2,18,2048
s_add_u32 m0, s36, 32768
Mv 100,108,64
buffer_load_dwordx4 v8, s[4:7], s38 offen offset:0 lds
Mv 160,112,64
Rt 4,19,0
Mv 164,116,64
Mv 224,120,64
Rt 6,19,2048
Mv 228,124,64
buffer_load_dwordx4 v9, s[4:7], s38 offen offset:1024 lds
Mv 236,124,68
Rt 8,20,0
Mv 232,120,68
Mv 172,116,68
Rt 10,20,2048
)" R"(
Mv 168,112,68
buffer_load_dwordx4 v10, s[4:7], s38 offen offset:2048 lds
Mv 108,108,68
Rt 12,21,0
Mv 104,104,68
Mv 44,100,68
Rt 14,21,2048
Mv 40,96,68
buffer_load_dwordx4 v11, s[4:7], s38 offen offset:3072 lds
Mv 48,96,72
Rt 16,22,0
Mv 52,100,72
s_add_u32 s38, s38, 0x10000
Mv 112,104,72
Rt 18,22,2048
s_add_u32 m0, s36, 36864
Mv 116,108,72
buffer_load_dwordx4 v8, s[4:7], s38 offen offset:0 lds
Mv 176,112,72
Rt 20,23,0
Mv 180,116,72
Mv 240,120,72
Rt 22,23,2048
Mv 244,124,72
buffer_load_dwordx4 v9, s[4:7], s38 offen offset:1024 lds
Mv 252,124,76
Rt 24,24,0
Mv 248,120,76
Mv 188,116,76
Rt 26,24,2048
Mv 184,112,76
buffer_load_dwordx4 v10, s[4:7], s38 offen offset:2048 lds
Mv 124,108,76
Rt 28,25,0
Mv 120,104,76
Mv 60,100,76
Rt 30,25,2048
Mv 56,96,76
buffer_load_dwordx4 v11, s[4:7], s38 offen offset:3072 lds
Mv 64,96,80
Rb 32,16,0
Mv 68,100,80
Mv 128,104,80
Rb 36,16,2048
Mv 132,108,80
Mv 192,112,80
Rb 40,16,8192
Mv 196,116,80
Ma 224,120,80
Rb 44,16,10240
Ma 228,124,80
Ma 236,124,84
Rb 48,16,16384
Ma 232,120,84
Mv 204,116,84
Rb 52,16,18432
Mv 200,112,84
Mv 140,108,84
Rb 56,16,24576
Mv 136,104,84
Mv 76,100,84
Rb 60,16,26624
Mv 72,96,84
Mv 80,96,88
)" R"(
Mv 84,100,88
Mv 144,104,88
Mv 148,108,88
Mv 208,112,88
Mv 212,116,88
Ma 240,120,88
Ma 244,124,88
Ma 252,124,92
Ma 248,120,92
Mv 220,116,92
s_sub_u32 s40, s40, 1
Mv 216,112,92
Mv 156,108,92
Mv 152,104,92
Mv 92,100,92
Mv 88,96,92
s_cmp_lg_u32 s40, 0
s_waitcnt vmcnt(12) lgkmcnt(0)
s_barrier
s_cbranch_scc1 .LzPAV
s_branch .LzPAVX
.LzB0:
s_mul_i32 s41, s33, 7168
s_mul_i32 s42, s33, 0x70000
s_lshl_b32 s28, s34, 9
s_add_u32 s42, s42, s28
v_lshrrev_b32 v28, 3, v0
v_and_b32 v29, 0x70, v1
v_lshl_or_b32 v28, v28, 13, v29
v_lshlrev_b32 v29, 1, v0
v_and_b32 v29, 32, v29
v_xor_b32 v28, v28, v29
s_and_b32 s28, s33, 1
s_lshl_b32 s28, s28, 6
v_xor_b32 v240, s28, v28
v_xor_b32 v241, 64, v240
v_add_u32 v241, 0xfc00, v241
v_add_u32 v242, 0x1f800, v240
v_add_u32 v243, 0x1f800, v241
s_sub_u32 s28, 15, s35
s_lshl2_add_u32 s28, s28, s33
s_mul_i32 s43, s28, 28672
v_and_b32 v2, 0x70, v1
v_and_b32 v28, 48, v0
v_xor_b32 v2, v2, v28
v_lshrrev_b32 v28, 3, v0
v_mul_u32_u24 v28, 896, v28
v_add_u32 v2, v2, v28
v_xor_b32 v3, 64, v2
v_add_u32 v3, 6144, v3
v_bfe_u32 v4, v0, 1, 3
v_lshrrev_b32 v28, 4, v0
v_xor_b32 v4, v4, v28
v_lshlrev_b32 v4, 4, v4
v_and_b32 v28, 15, v0
v_lshl_or_b32 v4, v28, 7, v4
s_add_u32 s28, s36, 0x0
v_add_u32 v4, s28, v4
v_xor_b32 v5, 64, v4
s_waitcnt lgkmcnt(0)
s_mov_b32 s16, s68
s_mov_b32 s17, s69
s_mov_b32 s18, 0x380000
s_mov_b32 s19, 0x20000
s_mov_b32 s12, s64
)" R"(
s_mov_b32 s13, s65
s_mov_b32 s14, 0x1c0000
s_mov_b32 s15, 0x20000
s_add_u32 m0, s36, 0
s_add_u32 s44, s43, 0
buffer_load_dwordx4 v2, s[12:15], s44 offen offset:0 lds
buffer_load_dwordx4 v3, s[12:15], s44 offen offset:1024 lds
s_add_u32 s44, s43, 12288
buffer_load_dwordx4 v2, s[12:15], s44 offen offset:2048 lds
buffer_load_dwordx4 v3, s[12:15], s44 offen offset:3072 lds
s_add_u32 m0, s41, 65536
s_add_u32 s92, s42, 0x0
buffer_load_dwordx4 v240, s[16:19], s92 offen offset:0 lds
buffer_load_dwordx4 v241, s[16:19], s92 offen offset:1024 lds
buffer_load_dwordx4 v242, s[16:19], s92 offen offset:2048 lds
buffer_load_dwordx4 v243, s[16:19], s92 offen offset:3072 lds
s_add_u32 m0, s41, 69632
s_add_u32 s92, s42, 0x40000
buffer_load_dwordx4 v240, s[16:19], s92 offen offset:0 lds
buffer_load_dwordx4 v241, s[16:19], s92 offen offset:1024 lds
buffer_load_dwordx4 v242, s[16:19], s92 offen offset:2048 lds
s_add_u32 m0, s36, 4096
s_add_u32 s44, s43, 128
buffer_load_dwordx4 v2, s[12:15], s44 offen offset:0 lds
buffer_load_dwordx4 v3, s[12:15], s44 offen offset:1024 lds
s_add_u32 s44, s43, 12416
buffer_load_dwordx4 v2, s[12:15], s44 offen offset:2048 lds
buffer_load_dwordx4 v3, s[12:15], s44 offen offset:3072 lds
s_add_u32 m0, s36, 32768
s_add_u32 s44, s43, 256
buffer_load_dwordx4 v2, s[12:15], s44 offen offset:0 lds
buffer_load_dwordx4 v3, s[12:15], s44 offen offset:1024 lds
s_add_u32 s44, s43, 12544
buffer_load_dwordx4 v2, s[12:15], s44 offen offset:2048 lds
buffer_load_dwordx4 v3, s[12:15], s44 offen offset:3072 lds
s_add_u32 m0, s36, 36864
s_add_u32 s44, s43, 384
buffer_load_dwordx4 v2, s[12:15], s44 offen offset:0 lds
buffer_load_dwordx4 v3, s[12:15], s44 offen offset:1024 lds
s_add_u32 s44, s43, 12672
buffer_load_dwordx4 v2, s[12:15], s44 offen offset:2048 lds
buffer_load_dwordx4 v3, s[12:15], s44 offen offset:3072 lds
s_add_u32 m0, s41, 98304
s_add_u32 s92, s42, 0x80
buffer_load_dwordx4 v240, s[16:19], s92 offen offset:0 lds
buffer_load_dwordx4 v241, s[16:19], s92 offen offset:1024 lds
buffer_load_dwordx4 v242, s[16:19], s92 offen offset:2048 lds
buffer_load_dwordx4 v243, s[16:19], s92 offen offset:3072 lds
s_add_u32 m0, s41, 102400
s_add_u32 s92, s42, 0x40080
buffer_load_dwordx4 v240, s[16:19], s92 offen offset:0 lds
buffer_load_dwordx4 v241, s[16:19], s92 offen offset:1024 lds
buffer_load_dwordx4 v242, s[16:19], s92 offen offset:2048 lds
s_mov_b32 s28, 0xc108400
s_load_dword s3, s[74:75], s28
s_sub_u32 s94, s2, 128
s_lshl2_add_u32 s94, s94, s33
s_lshl_b32 s94, s94, 2
s_mul_i32 s29, s94, 896
v_mov_b32 v120, 0x40000000
v_cmp_gt_u32_e32 vcc, 56, v0
s_nop 1
v_cndmask_b32_e32 v120, v120, v1, vcc
buffer_load_dwordx4 v[96:99], v120, s[12:15], s29 offen offset:0
)" R"(
buffer_load_dwordx4 v[100:103], v120, s[12:15], s29 offen offset:896
buffer_load_dwordx4 v[104:107], v120, s[12:15], s29 offen offset:1792
buffer_load_dwordx4 v[108:111], v120, s[12:15], s29 offen offset:2688
s_mov_b32 s24, s76
s_mov_b32 s25, s77
s_mov_b32 s26, 0x380
s_mov_b32 s27, 0x20000
buffer_load_dwordx4 v[112:115], v120, s[24:27], 0 offen
s_mov_b32 s24, s78
s_mov_b32 s25, s79
s_mov_b32 s26, 0x380
s_mov_b32 s27, 0x20000
buffer_load_dwordx4 v[116:119], v120, s[24:27], 0 offen
s_mov_b32 s20, s74
s_mov_b32 s21, s75
s_mov_b32 s22, 0xc1b2000
s_mov_b32 s23, 0x20000
s_mov_b32 s4, s66
s_mov_b32 s5, s67
s_mov_b32 s6, 0x400000
s_mov_b32 s7, 0x20000
s_cmp_eq_u32 s91, 1
s_cselect_b32 s8, s72, s70
s_cselect_b32 s9, s73, s71
s_mov_b32 s10, 0x800000
s_mov_b32 s11, 0x20000
s_load_dwordx2 s[74:75], s[0:1], 96
s_load_dwordx4 s[76:79], s[0:1], 72
s_load_dwordx2 s[70:71], s[0:1], 88
v_bfe_u32 v244, v0, 3, 2
v_lshlrev_b32 v244, 5, v244
v_and_b32 v28, 3, v0
v_lshl_or_b32 v244, v28, 3, v244
v_bfe_u32 v28, v0, 2, 2
v_lshl_or_b32 v244, v28, 7, v244
v_lshrrev_b32 v28, 4, v0
v_lshl_or_b32 v244, v28, 10, v244
v_xor_b32 v245, 32, v244
v_xor_b32 v246, 64, v244
v_xor_b32 v247, 96, v244
v_add_u32 v252, 0x20000, v244
v_add_u32 v253, 0x20000, v245
v_add_u32 v254, 0x20000, v246
v_add_u32 v255, 0x20000, v247
v_add_u32 v248, 0x10000, v244
v_add_u32 v249, 0x10000, v245
v_add_u32 v250, 0x10000, v246
v_add_u32 v251, 0x10000, v247
s_mov_b32 s63, 0xe0c0a08
s_mov_b32 s28, 0x100d0b09
s_bitcmp1_b32 s2, 5
s_cmov_b32 s63, 0xf1e1c1a
s_cmov_b32 s28, 0x111f1d1b
s_bitcmp1_b32 s2, 6
s_cmov_b32 s63, s28
s_lshl_b32 s28, s33, 3
s_lshr_b32 s28, s63, s28
s_and_b32 s28, s28, 255
s_lshl_b32 s38, s28, 17
s_add_u32 s38, s38, 0xff80
s_lshl_b32 s39, s33, 16
s_lshl_b32 s28, s34, 9
s_add_u32 s39, s39, s28
s_sub_u32 s39, s39, 0x40000
)" R"(
s_lshl_b32 s37, s33, 12
s_add_u32 s37, s37, 0x10000
v_lshrrev_b32 v28, 4, v0
v_and_b32 v29, 7, v0
v_xor_b32 v28, v28, v29
v_lshlrev_b32 v28, 4, v28
v_lshrrev_b32 v29, 3, v0
v_lshl_or_b32 v8, v29, 11, v28
v_xor_b32 v9, 64, v8
v_add_u32 v9, 15360, v9
v_add_u32 v10, 30720, v8
v_add_u32 v11, 30720, v9
s_and_b32 s28, s33, 1
s_lshl_b32 s28, s28, 7
v_lshrrev_b32 v29, 5, v0
v_mul_u32_u24 v29, 0x2220, v29
v_xor_b32 v29, v29, v1
v_xor_b32 v12, s28, v29
v_xor_b32 v13, 64, v12
v_add_u32 v13, 15360, v13
v_add_u32 v14, 30720, v12
v_add_u32 v15, 30720, v13
v_bfe_u32 v28, v0, 1, 3
v_lshrrev_b32 v29, 4, v0
v_xor_b32 v28, v28, v29
v_lshlrev_b32 v28, 4, v28
v_and_b32 v29, 15, v0
v_lshl_or_b32 v28, v29, 7, v28
s_lshr_b32 s28, s33, 1
s_lshl_b32 s28, s28, 12
v_or_b32 v16, s28, v28
v_xor_b32 v17, 64, v16
v_bfe_u32 v28, v0, 2, 3
v_lshlrev_b32 v28, 5, v28
v_and_b32 v29, 3, v0
v_lshl_or_b32 v28, v29, 3, v28
v_bfe_u32 v29, v0, 2, 2
v_lshl_or_b32 v28, v29, 9, v28
v_lshrrev_b32 v29, 4, v0
v_lshl_or_b32 v28, v29, 12, v28
s_and_b32 s28, s33, 1
s_lshl_b32 s28, s28, 8
s_bitset1_b32 s28, 16
v_or_b32 v18, s28, v28
v_xor_b32 v19, 32, v18
v_xor_b32 v20, 64, v18
v_xor_b32 v21, 96, v18
v_xor_b32 v22, 128, v18
v_xor_b32 v23, 160, v18
v_xor_b32 v24, 192, v18
v_xor_b32 v25, 224, v18
s_lshl_b32 s28, s34, 21
s_lshl_b32 s29, s91, 25
s_add_u32 s28, s28, s29
s_bfe_u32 s29, s63, 0x80000
s_lshl_b32 s29, s29, 16
s_add_u32 s45, s28, s29
s_bfe_u32 s29, s63, 0x80008
s_lshl_b32 s29, s29, 16
s_add_u32 s46, s28, s29
s_bfe_u32 s29, s63, 0x80010
s_lshl_b32 s29, s29, 16
s_add_u32 s47, s28, s29
s_bfe_u32 s29, s63, 0x80018
)" R"(
s_lshl_b32 s29, s29, 16
s_add_u32 s48, s28, s29
s_lshl_b32 s50, s34, 11
s_add_u32 s50, s50, 0xc110000
s_lshl2_add_u32 s28, s91, s93
s_lshl_b32 s28, s28, 7
s_add_u32 s49, s28, s50
s_add_u32 s49, s49, 1024
s_lshl3_add_u32 s28, s34, s35
s_lshl_b32 s52, s28, 7
s_add_u32 s52, s52, 0xc118000
s_lshl4_add_u32 s28, s34, s35
s_lshl_b32 s51, s28, 17
s_add_u32 s51, s51, 0x4000000
s_cmp_lg_u32 s33, 0
s_cmov_b32 s49, 0x40000000
s_cmov_b32 s52, 0x40000000
s_mov_b32 s53, 0x40000
s_mov_b32 s54, 0x3db8aa3b
s_lshl_b32 s89, s34, 21
s_mov_b64 s[60:61], 0xff
s_mov_b32 s62, 0x4e800000
s_add_u32 s67, s89, 0x6000000
s_lshl3_add_u32 s28, s34, s35
s_add_u32 s64, s51, 0x100000
s_lshl2_add_u32 s28, s28, s33
s_lshl_b32 s65, s28, 7
s_add_u32 s65, s65, 0xc120000
s_lshl_b32 s66, s28, 10
s_add_u32 s66, s66, 0xc000000
s_sub_u32 s28, 15, s35
s_lshl2_add_u32 s28, s28, s33
s_lshl_b32 s29, s34, 6
s_add_u32 s29, s29, s28
s_lshl_b32 s73, s29, 16
s_add_u32 s73, s73, 0x8000000
s_lshl4_add_u32 s28, s28, s34
s_lshl_b32 s68, s28, 7
s_add_u32 s68, s68, 0xc130000
s_mov_b32 s55, s89
s_mov_b32 s57, 18
s_mov_b32 s59, s67
s_mov_b32 s58, 0
s_cmp_eq_u32 s35, 7
s_cmov_b32 s58, 2
s_cmp_eq_u32 s35, 7
s_cmov_b32 s64, 0x40000000
s_cmp_eq_u32 s35, 7
s_cmov_b32 s66, 0x40000000
s_add_u32 s56, s55, 0x2000000
s_waitcnt vmcnt(21) lgkmcnt(0)
s_barrier
ds_read_b128 v[128:131], v4 offset:0
ds_read_b128 v[132:135], v4 offset:2048
ds_read_b128 v[136:139], v5 offset:0
ds_read_b128 v[140:143], v5 offset:2048
ds_read_b128 v[144:147], v4 offset:4096
ds_read_b128 v[148:151], v4 offset:6144
ds_read_b128 v[152:155], v5 offset:4096
ds_read_b128 v[156:159], v5 offset:6144
Rt 128,248,0
Rt 130,248,512
Rt 132,249,0
Rt 134,249,512
Rt 136,250,0
)" R"(
Rt 138,250,512
Rt 140,251,0
Rt 142,251,512
Rt 144,248,4096
Rt 146,248,4608
Rt 148,249,4096
Rt 150,249,4608
Rt 152,250,4096
Rt 154,250,4608
Rt 156,251,4096
Rt 158,251,4608
s_mul_i32 s3, s3, 1664525
s_add_u32 s3, s3, 1013904223
v_mov_b32 v6, s3
v_min_u32 v7, 1, v0
v_lshlrev_b32 v7, 30, v7
s_waitcnt lgkmcnt(8)
s_barrier
v_mfma_f32_16x16x32_bf16 v[32:35], a[128:131], v[128:131], 0
Rt 160,248,8192
s_add_u32 m0, s41, 131072
s_add_u32 s92, s42, 0x1c0000
v_mfma_f32_16x16x32_bf16 v[40:43], a[128:131], v[132:135], 0
Rt 162,248,8704
buffer_load_dwordx4 v240, s[16:19], s92 offen offset:0 lds
v_mfma_f32_16x16x32_bf16 v[44:47], a[132:135], v[132:135], 0
Rt 164,249,8192
v_mfma_f32_16x16x32_bf16 v[36:39], a[132:135], v[128:131], 0
Rt 166,249,8704
v_mfma_f32_16x16x32_bf16 v[48:51], a[136:139], v[128:131], 0
Rt 168,250,8192
v_mfma_f32_16x16x32_bf16 v[56:59], a[136:139], v[132:135], 0
Rt 170,250,8704
buffer_load_dwordx4 v241, s[16:19], s92 offen offset:1024 lds
v_mfma_f32_16x16x32_bf16 v[60:63], a[140:143], v[132:135], 0
Rt 172,251,8192
v_mfma_f32_16x16x32_bf16 v[52:55], a[140:143], v[128:131], 0
Rt 174,251,8704
s_waitcnt vmcnt(19) lgkmcnt(8)
Mp 32,144,136
Rt 128,248,12288
ds_read_b128 v[160:163], v4 offset:32768
Mp 40,144,140
Rt 130,248,12800
buffer_load_dwordx4 v242, s[16:19], s92 offen offset:2048 lds
Mp 44,148,140
Rt 132,249,12288
ds_read_b128 v[164:167], v4 offset:34816
Mp 36,148,136
Rt 134,249,12800
Mp 48,152,136
Rt 136,250,12288
ds_read_b128 v[168:171], v5 offset:32768
Mp 56,152,140
Rt 138,250,12800
buffer_load_dwordx4 v243, s[16:19], s92 offen offset:3072 lds
Mp 60,156,140
Rt 140,251,12288
ds_read_b128 v[172:175], v5 offset:34816
Mp 52,156,136
Rt 142,251,12800
s_waitcnt vmcnt(17) lgkmcnt(12)
Mp 32,160,144
Rt 144,248,16384
ds_read_b128 v[176:179], v4 offset:36864
)" R"(
s_add_u32 m0, s41, 135168
s_add_u32 s92, s42, 0x200000
Mp 40,160,148
Rt 146,248,16896
buffer_load_dwordx4 v240, s[16:19], s92 offen offset:0 lds
Mp 44,164,148
Rt 148,249,16384
ds_read_b128 v[180:183], v4 offset:38912
Mp 36,164,144
Rt 150,249,16896
Mp 48,168,144
Rt 152,250,16384
ds_read_b128 v[184:187], v5 offset:36864
Mp 56,168,148
Rt 154,250,16896
buffer_load_dwordx4 v241, s[16:19], s92 offen offset:1024 lds
Mp 60,172,148
Rt 156,251,16384
ds_read_b128 v[188:191], v5 offset:38912
Mp 52,172,144
Rt 158,251,16896
s_waitcnt lgkmcnt(12)
Mp 32,128,152
Rt 160,248,20480
Mp 40,128,156
Rt 162,248,20992
buffer_load_dwordx4 v242, s[16:19], s92 offen offset:2048 lds
Mp 44,132,156
Rt 164,249,20480
Mp 36,132,152
Rt 166,249,20992
Mp 48,136,152
Rt 168,250,20480
Mp 56,136,156
Rt 170,250,20992
Mp 60,140,156
Rt 172,251,20480
s_add_u32 s44, s43, 512
Mp 52,140,152
Rt 174,251,20992
s_waitcnt lgkmcnt(8)
Mp 32,144,160
Rt 128,248,24576
s_add_u32 m0, s36, 32768
Mp 40,144,164
Rt 130,248,25088
buffer_load_dwordx4 v2, s[12:15], s44 offen offset:0 lds
Mp 44,148,164
Rt 132,249,24576
Mp 36,148,160
Rt 134,249,25088
Mp 48,152,160
Rt 136,250,24576
Mp 56,152,164
Rt 138,250,25088
buffer_load_dwordx4 v3, s[12:15], s44 offen offset:1024 lds
Mp 60,156,164
Rt 140,251,24576
Mp 52,156,160
Rt 142,251,25088
s_waitcnt vmcnt(15) lgkmcnt(8)
s_barrier
Mp 32,160,168
Rt 144,248,32768
Mp 40,160,172
)" R"(
Rt 146,248,33280
s_add_u32 s44, s43, 12800
buffer_load_dwordx4 v2, s[12:15], s44 offen offset:2048 lds
Mp 44,164,172
Rt 148,249,32768
Mp 36,164,168
Rt 150,249,33280
Mp 48,168,168
Rt 152,250,32768
Mp 56,168,172
Rt 154,250,33280
buffer_load_dwordx4 v3, s[12:15], s44 offen offset:3072 lds
Mp 60,172,172
Rt 156,251,32768
s_add_u32 s44, s43, 640
Mp 52,172,168
Rt 158,251,33280
s_waitcnt vmcnt(11) lgkmcnt(8)
s_barrier
Mp 32,128,176
Rt 160,248,36864
s_add_u32 m0, s36, 36864
Mp 40,128,180
Rt 162,248,37376
buffer_load_dwordx4 v2, s[12:15], s44 offen offset:0 lds
Mp 44,132,180
Rt 164,249,36864
v_dot2_f32_bf16 v120, v96, v112, 0
Mp 36,132,176
Rt 166,249,37376
v_dot2_f32_bf16 v121, v100, v112, 0
v_dot2_f32_bf16 v122, v104, v112, 0
Mp 48,136,176
Rt 168,250,36864
v_dot2_f32_bf16 v123, v108, v112, 0
Mp 56,136,180
Rt 170,250,37376
buffer_load_dwordx4 v3, s[12:15], s44 offen offset:1024 lds
Mp 60,140,180
Rt 172,251,36864
v_dot2_f32_bf16 v124, v96, v116, 0
v_dot2_f32_bf16 v125, v100, v116, 0
Mp 52,140,176
Rt 174,251,37376
s_waitcnt lgkmcnt(8)
v_mfma_f32_16x16x32_bf16 v[64:67], a[144:147], v[128:131], 0
Rt 128,248,40960
v_dot2_f32_bf16 v126, v104, v116, 0
v_mfma_f32_16x16x32_bf16 v[72:75], a[144:147], v[132:135], 0
Rt 130,248,41472
s_add_u32 s44, s43, 12928
buffer_load_dwordx4 v2, s[12:15], s44 offen offset:2048 lds
v_mfma_f32_16x16x32_bf16 v[76:79], a[148:151], v[132:135], 0
Rt 132,249,40960
v_dot2_f32_bf16 v127, v108, v116, 0
v_mfma_f32_16x16x32_bf16 v[68:71], a[148:151], v[128:131], 0
Rt 134,249,41472
v_dot2_f32_bf16 v120, v97, v113, v120
v_mfma_f32_16x16x32_bf16 v[80:83], a[152:155], v[128:131], 0
Rt 136,250,40960
v_dot2_f32_bf16 v121, v101, v113, v121
v_dot2_f32_bf16 v122, v105, v113, v122
v_mfma_f32_16x16x32_bf16 v[88:91], a[152:155], v[132:135], 0
Rt 138,250,41472
buffer_load_dwordx4 v3, s[12:15], s44 offen offset:3072 lds
)" R"(
v_mfma_f32_16x16x32_bf16 v[92:95], a[156:159], v[132:135], 0
Rt 140,251,40960
s_add_u32 s92, s42, 0x1c0080
v_mfma_f32_16x16x32_bf16 v[84:87], a[156:159], v[128:131], 0
Rt 142,251,41472
v_dot2_f32_bf16 v123, v109, v113, v123
s_waitcnt lgkmcnt(8)
Mp 64,160,136
Rt 144,248,45056
s_add_u32 m0, s41, 0
Mp 72,160,140
Rt 146,248,45568
buffer_load_dwordx4 v240, s[16:19], s92 offen offset:0 lds
Mp 76,164,140
Rt 148,249,45056
v_dot2_f32_bf16 v124, v97, v117, v124
Mp 68,164,136
Rt 150,249,45568
v_dot2_f32_bf16 v125, v101, v117, v125
v_dot2_f32_bf16 v126, v105, v117, v126
Mp 80,168,136
Rt 152,250,45056
v_dot2_f32_bf16 v127, v109, v117, v127
Mp 88,168,140
Rt 154,250,45568
buffer_load_dwordx4 v241, s[16:19], s92 offen offset:1024 lds
Mp 92,172,140
Rt 156,251,45056
v_dot2_f32_bf16 v120, v98, v114, v120
v_dot2_f32_bf16 v121, v102, v114, v121
Mp 84,172,136
Rt 158,251,45568
s_waitcnt lgkmcnt(8)
Mp 64,128,144
Rt 160,248,49152
v_dot2_f32_bf16 v122, v106, v114, v122
Mp 72,128,148
Rt 162,248,49664
buffer_load_dwordx4 v242, s[16:19], s92 offen offset:2048 lds
Mp 76,132,148
Rt 164,249,49152
v_dot2_f32_bf16 v123, v110, v114, v123
Mp 68,132,144
Rt 166,249,49664
v_dot2_f32_bf16 v124, v98, v118, v124
Mp 80,136,144
Rt 168,250,49152
v_dot2_f32_bf16 v125, v102, v118, v125
v_dot2_f32_bf16 v126, v106, v118, v126
Mp 88,136,148
Rt 170,250,49664
buffer_load_dwordx4 v243, s[16:19], s92 offen offset:3072 lds
Mp 92,140,148
Rt 172,251,49152
s_add_u32 s92, s42, 0x200080
Mp 84,140,144
Rt 174,251,49664
v_dot2_f32_bf16 v127, v110, v118, v127
s_waitcnt lgkmcnt(8)
Mp 64,144,152
Rt 128,248,53248
s_add_u32 m0, s41, 4096
Mp 72,144,156
Rt 130,248,53760
)" R"(
buffer_load_dwordx4 v240, s[16:19], s92 offen offset:0 lds
Mp 76,148,156
Rt 132,249,53248
v_dot2_f32_bf16 v120, v99, v115, v120
Mp 68,148,152
Rt 134,249,53760
v_dot2_f32_bf16 v121, v103, v115, v121
v_dot2_f32_bf16 v122, v107, v115, v122
Mp 80,152,152
Rt 136,250,53248
v_dot2_f32_bf16 v123, v111, v115, v123
Mp 88,152,156
Rt 138,250,53760
buffer_load_dwordx4 v241, s[16:19], s92 offen offset:1024 lds
Mp 92,156,156
Rt 140,251,53248
v_dot2_f32_bf16 v124, v99, v119, v124
v_dot2_f32_bf16 v125, v103, v119, v125
Mp 84,156,152
Rt 142,251,53760
s_waitcnt vmcnt(10) lgkmcnt(8)
Mp 64,160,160
Rt 144,248,57344
ds_read_b128 v[192:195], v4 offset:32768
Mp 72,160,164
Rt 146,248,57856
buffer_load_dwordx4 v242, s[16:19], s92 offen offset:2048 lds
Mp 76,164,164
Rt 148,249,57344
ds_read_b128 v[196:199], v4 offset:34816
Mp 68,164,160
Rt 150,249,57856
v_dot2_f32_bf16 v126, v107, v119, v126
Mp 80,168,160
Rt 152,250,57344
ds_read_b128 v[200:203], v5 offset:32768
v_dot2_f32_bf16 v127, v111, v119, v127
Mp 88,168,164
Rt 154,250,57856
v_permlane32_swap_b32_e32 v120, v121
Mp 92,172,164
Rt 156,251,57344
ds_read_b128 v[204:207], v5 offset:34816
Mp 84,172,160
Rt 158,251,57856
s_waitcnt vmcnt(7) lgkmcnt(12)
s_barrier
Mp 64,128,168
Rt 160,252,0
ds_read_b128 v[208:211], v4 offset:36864
Mp 72,128,172
Rt 162,252,512
v_add_f32_e32 v120, v120, v121
Mp 76,132,172
Rt 164,253,0
ds_read_b128 v[212:215], v4 offset:38912
Mp 68,132,168
Rt 166,253,512
v_permlane32_swap_b32_e32 v122, v123
v_add_f32_e32 v122, v122, v123
Mp 80,136,168
Rt 168,254,0
ds_read_b128 v[216:219], v5 offset:36864
Mp 88,136,172
Rt 170,254,512
)" R"(
s_add_u32 s44, s43, 768
Mp 92,140,172
Rt 172,255,0
ds_read_b128 v[220:223], v5 offset:38912
v_permlane32_swap_b32_e32 v124, v125
Mp 84,140,168
Rt 174,255,512
s_waitcnt lgkmcnt(12)
s_barrier
Mp 64,144,176
Rt 128,252,4096
s_add_u32 m0, s36, 32768
Mp 72,144,180
Rt 130,252,4608
buffer_load_dwordx4 v2, s[12:15], s44 offen offset:0 lds
Mp 76,148,180
Rt 132,253,4096
v_add_f32_e32 v124, v124, v125
Mp 68,148,176
Rt 134,253,4608
v_permlane32_swap_b32_e32 v126, v127
Mp 80,152,176
Rt 136,254,4096
buffer_load_dwordx4 v3, s[12:15], s44 offen offset:1024 lds
Mp 88,152,180
Rt 138,254,4608
v_add_f32_e32 v126, v126, v127
Mp 92,156,180
Rt 140,255,4096
v_permlane16_swap_b32_e32 v120, v122
Mp 84,156,176
Rt 142,255,4608
s_add_u32 s44, s43, 13056
buffer_load_dwordx4 v2, s[12:15], s44 offen offset:2048 lds
s_waitcnt lgkmcnt(8)
Mp 32,160,184
Rt 144,252,8192
v_add_f32_e32 v120, v120, v122
Mp 40,160,188
Rt 146,252,8704
v_permlane16_swap_b32_e32 v124, v126
Mp 44,164,188
Rt 148,253,8192
buffer_load_dwordx4 v3, s[12:15], s44 offen offset:3072 lds
Mp 36,164,184
Rt 150,253,8704
v_add_f32_e32 v124, v124, v126
v_add_f32_dpp v120, v120, v120 row_ror:8 row_mask:0xf bank_mask:0xf
Mp 48,168,184
Rt 152,254,8192
v_add_f32_dpp v124, v124, v124 row_ror:8 row_mask:0xf bank_mask:0xf
Mp 56,168,188
Rt 154,254,8704
s_add_u32 s92, s42, 0x100
Mp 60,172,188
Rt 156,255,8192
s_add_u32 m0, s41, 65536
v_add_f32_dpp v120, v120, v120 row_ror:4 row_mask:0xf bank_mask:0xf
Mp 52,172,184
Rt 158,255,8704
buffer_load_dwordx4 v240, s[16:19], s92 offen offset:0 lds
s_waitcnt lgkmcnt(8)
Mp 32,128,192
Rt 160,252,12288
v_add_f32_dpp v124, v124, v124 row_ror:4 row_mask:0xf bank_mask:0xf
)" R"(
Mp 40,128,196
Rt 162,252,12800
v_add_f32_dpp v120, v120, v120 row_ror:2 row_mask:0xf bank_mask:0xf
v_add_f32_dpp v124, v124, v124 row_ror:2 row_mask:0xf bank_mask:0xf
Mp 44,132,196
Rt 164,253,12288
v_add_f32_dpp v120, v120, v120 row_ror:1 row_mask:0xf bank_mask:0xf
Mp 36,132,192
Rt 166,253,12800
buffer_load_dwordx4 v241, s[16:19], s92 offen offset:1024 lds
Mp 48,136,192
Rt 168,254,12288
v_add_f32_dpp v124, v124, v124 row_ror:1 row_mask:0xf bank_mask:0xf
Cv 120,120,120
Mp 56,136,196
Rt 170,254,12800
v_lshlrev_b32 v120, 16, v120
Mp 60,140,196
Rt 172,255,12288
Cv 124,124,124
Mp 52,140,192
Rt 174,255,12800
buffer_load_dwordx4 v242, s[16:19], s92 offen offset:2048 lds
s_waitcnt lgkmcnt(8)
Mp 32,144,200
Rt 128,252,16384
v_lshlrev_b32 v124, 16, v124
Mp 40,144,204
Rt 130,252,16896
v_mul_f32_e32 v120, 0xbfb8aa3b, v120
Mp 44,148,204
Rt 132,253,16384
v_lshrrev_b32 v121, 5, v0
Mp 36,148,200
Rt 134,253,16896
buffer_load_dwordx4 v243, s[16:19], s92 offen offset:3072 lds
Mp 48,152,200
Rt 136,254,16384
v_bfe_u32 v122, v0, 4, 1
Mp 56,152,204
Rt 138,254,16896
s_add_u32 s92, s42, 0x40100
Mp 60,156,204
Rt 140,255,16384
s_add_u32 m0, s41, 69632
v_lshl_or_b32 v121, v122, 1, v121
Mp 52,156,200
Rt 142,255,16896
buffer_load_dwordx4 v240, s[16:19], s92 offen offset:0 lds
s_waitcnt vmcnt(5) lgkmcnt(8)
Mp 32,160,208
Rt 144,252,20480
ds_read_b128 v[224:227], v4 offset:32768
Mp 40,160,212
Rt 146,252,20992
v_exp_f32_e32 v120, v120
Mp 44,164,212
Rt 148,253,20480
ds_read_b128 v[228:231], v4 offset:34816
Mp 36,164,208
Rt 150,253,20992
buffer_load_dwordx4 v241, s[16:19], s92 offen offset:1024 lds
Mp 48,168,208
Rt 152,254,20480
)" R"(
ds_read_b128 v[232:235], v5 offset:32768
v_add_f32_e32 v120, 1.0, v120
Mp 56,168,212
Rt 154,254,20992
v_add_u32 v121, s94, v121
Mp 60,172,212
Rt 156,255,20480
ds_read_b128 v[236:239], v5 offset:34816
Mp 52,172,208
Rt 158,255,20992
buffer_load_dwordx4 v242, s[16:19], s92 offen offset:2048 lds
s_waitcnt lgkmcnt(12)
Mp 32,128,216
Rt 160,252,24576
v_add_f32_e32 v125, 0, v124
Mp 40,128,220
Rt 162,252,25088
s_add_u32 s92, s42, 0x180
Mp 44,132,220
Rt 164,253,24576
s_add_u32 m0, s41, 98304
Mp 36,132,216
Rt 166,253,25088
buffer_load_dwordx4 v240, s[16:19], s92 offen offset:0 lds
Mp 48,136,216
Rt 168,254,24576
v_ashrrev_i32 v126, 31, v125
Mp 56,136,220
Rt 170,254,25088
v_or_b32 v126, 0x80000000, v126
Mp 60,140,220
Rt 172,255,24576
v_rcp_f32_e32 v120, v120
Mp 52,140,216
Rt 174,255,25088
buffer_load_dwordx4 v241, s[16:19], s92 offen offset:1024 lds
s_waitcnt lgkmcnt(8)
s_barrier
Mp 32,144,224
Rt 128,244,0
Cv 120,120,120
Mp 40,144,228
Rt 130,244,512
v_lshlrev_b32 v120, 16, v120
v_xor_b32 v125, v125, v126
Mp 44,148,228
Rt 132,245,0
v_and_b32 v125, 0xffff0000, v125
Mp 36,148,224
Rt 134,245,512
buffer_load_dwordx4 v242, s[16:19], s92 offen offset:2048 lds
Mp 48,152,224
Rt 136,246,0
v_sub_u32 v126, 0xffff, v121
v_or_b32 v116, v125, v126
Mp 56,152,228
Rt 138,246,512
v_mov_b32 v117, s3
Mp 60,156,228
Rt 140,247,0
v_mov_b32 v118, v120
Mp 52,156,224
Rt 142,247,512
buffer_load_dwordx4 v243, s[16:19], s92 offen offset:3072 lds
s_waitcnt lgkmcnt(8)
s_barrier
)" R"(
Mp 32,160,232
Rt 144,244,4096
v_mov_b32 v119, v124
Mp 40,160,236
Rt 146,244,4608
s_add_u32 s92, s42, 0x40180
Mp 44,164,236
Rt 148,245,4096
s_add_u32 m0, s41, 102400
Mp 36,164,232
Rt 150,245,4608
buffer_load_dwordx4 v240, s[16:19], s92 offen offset:0 lds
Mp 48,168,232
Rt 152,246,4096
v_and_b32 v122, 15, v0
Mp 56,168,236
Rt 154,246,4608
v_min_u32 v122, 1, v122
Mp 60,172,236
Rt 156,247,4096
v_lshlrev_b32 v122, 30, v122
v_lshl_or_b32 v122, v121, 4, v122
Mp 52,172,232
Rt 158,247,4608
buffer_load_dwordx4 v241, s[16:19], s92 offen offset:1024 lds
s_waitcnt lgkmcnt(8)
Mp 64,128,184
Rt 160,244,8192
s_mov_b32 s94, 0xc100000
Mp 72,128,188
Rt 162,244,8704
buffer_store_dwordx4 v[116:119], v122, s[20:23], s94 offen sc0 sc1
Mp 76,132,188
Rt 164,245,8192
Mp 68,132,184
Rt 166,245,8704
buffer_load_dwordx4 v242, s[16:19], s92 offen offset:2048 lds
Mp 80,136,184
Rt 168,246,8192
v_mul_f32_e32 v32, s54, v32
Mp 88,136,188
Rt 170,246,8704
s_add_u32 s92, s42, 0x1c0100
Mp 92,140,188
Rt 172,247,8192
s_add_u32 m0, s41, 32768
Mp 84,140,184
Rt 174,247,8704
buffer_load_dwordx4 v240, s[16:19], s92 offen offset:0 lds
s_waitcnt lgkmcnt(8)
Mp 64,144,192
Rt 128,244,12288
v_mul_f32_e32 v33, s54, v33
Mp 72,144,196
Rt 130,244,12800
Cv 32,32,33
Mp 76,148,196
Rt 132,245,12288
v_accvgpr_write_b32 a64, v32
v_mul_f32_e32 v34, s54, v34
Mp 68,148,192
Rt 134,245,12800
buffer_load_dwordx4 v241, s[16:19], s92 offen offset:1024 lds
Mp 80,152,192
)" R"(
Rt 136,246,12288
v_mul_f32_e32 v35, s54, v35
Mp 88,152,196
Rt 138,246,12800
Cv 34,34,35
v_accvgpr_write_b32 a65, v34
Mp 92,156,196
Rt 140,247,12288
v_mul_f32_e32 v36, s54, v36
Mp 84,156,192
Rt 142,247,12800
buffer_load_dwordx4 v242, s[16:19], s92 offen offset:2048 lds
s_waitcnt lgkmcnt(8)
Mp 64,160,200
Rt 144,244,16384
v_mul_f32_e32 v37, s54, v37
Cv 36,36,37
Mp 72,160,204
Rt 146,244,16896
v_accvgpr_write_b32 a66, v36
Mp 76,164,204
Rt 148,245,16384
v_mul_f32_e32 v38, s54, v38
Mp 68,164,200
Rt 150,245,16896
buffer_load_dwordx4 v243, s[16:19], s92 offen offset:3072 lds
Mp 80,168,200
Rt 152,246,16384
v_mul_f32_e32 v39, s54, v39
Mp 88,168,204
Rt 154,246,16896
s_add_u32 s92, s42, 0x200100
Mp 92,172,204
Rt 156,247,16384
s_add_u32 m0, s41, 36864
Mp 84,172,200
Rt 158,247,16896
buffer_load_dwordx4 v240, s[16:19], s92 offen offset:0 lds
s_waitcnt lgkmcnt(8)
Mp 64,128,208
Rt 160,244,20480
Cv 38,38,39
Mp 72,128,212
Rt 162,244,20992
v_accvgpr_write_b32 a67, v38
Mp 76,132,212
Rt 164,245,20480
v_mul_f32_e32 v40, s54, v40
v_mul_f32_e32 v41, s54, v41
Mp 68,132,208
Rt 166,245,20992
buffer_load_dwordx4 v241, s[16:19], s92 offen offset:1024 lds
Mp 80,136,208
Rt 168,246,20480
Cv 40,40,41
Mp 88,136,212
Rt 170,246,20992
v_accvgpr_write_b32 a68, v40
v_mul_f32_e32 v42, s54, v42
Mp 92,140,212
Rt 172,247,20480
v_mul_f32_e32 v43, s54, v43
Mp 84,140,208
Rt 174,247,20992
)" R"(
buffer_load_dwordx4 v242, s[16:19], s92 offen offset:2048 lds
s_waitcnt lgkmcnt(8)
Mp 64,144,216
Rt 128,244,24576
Cv 42,42,43
v_accvgpr_write_b32 a69, v42
Mp 72,144,220
Rt 130,244,25088
v_mul_f32_e32 v44, s54, v44
Mp 76,148,220
Rt 132,245,24576
v_mul_f32_e32 v45, s54, v45
Mp 68,148,216
Rt 134,245,25088
s_add_u32 s92, s42, 0x1c0180
Cv 44,44,45
Mp 80,152,216
Rt 136,246,24576
s_add_u32 m0, s41, 131072
Mp 88,152,220
Rt 138,246,25088
buffer_load_dwordx4 v240, s[16:19], s92 offen offset:0 lds
Mp 92,156,220
Rt 140,247,24576
v_accvgpr_write_b32 a70, v44
Mp 84,156,216
Rt 142,247,25088
s_waitcnt vmcnt(16) lgkmcnt(8)
s_barrier
Mp 64,160,224
Rt 144,248,0
v_mul_f32_e32 v46, s54, v46
Mp 72,160,228
Rt 146,248,512
buffer_load_dwordx4 v241, s[16:19], s92 offen offset:1024 lds
Mp 76,164,228
Rt 148,249,0
v_mul_f32_e32 v47, s54, v47
Cv 46,46,47
Mp 68,164,224
Rt 150,249,512
v_accvgpr_write_b32 a71, v46
Mp 80,168,224
Rt 152,250,0
v_mul_f32_e32 v48, s54, v48
Mp 88,168,228
Rt 154,250,512
buffer_load_dwordx4 v242, s[16:19], s92 offen offset:2048 lds
Mp 92,172,228
Rt 156,251,0
v_mul_f32_e32 v49, s54, v49
Mp 84,172,224
Rt 158,251,512
s_waitcnt vmcnt(3) lgkmcnt(8)
s_barrier
Mp 64,128,232
Rt 160,248,4096
Cv 48,48,49
v_accvgpr_write_b32 a72, v48
Mp 72,128,236
Rt 162,248,4608
buffer_load_dwordx4 v243, s[16:19], s92 offen offset:3072 lds
Mp 76,132,236
Rt 164,249,4096
v_mul_f32_e32 v50, s54, v50
Mp 68,132,232
)" R"(
Rt 166,249,4608
s_add_u32 s92, s42, 0x200180
v_mul_f32_e32 v51, s54, v51
Mp 80,136,232
Rt 168,250,4096
s_add_u32 m0, s41, 135168
Mp 88,136,236
Rt 170,250,4608
buffer_load_dwordx4 v240, s[16:19], s92 offen offset:0 lds
Mp 92,140,236
Rt 172,251,4096
Cv 50,50,51
Mp 84,140,232
Rt 174,251,4608
v_accvgpr_write_b32 a73, v50
s_waitcnt lgkmcnt(8)
v_mfma_f32_16x16x32_bf16 v[96:99], a[144:147], v[128:131], 0
Rt 128,248,8192
v_mul_f32_e32 v52, s54, v52
v_mfma_f32_16x16x32_bf16 v[104:107], a[144:147], v[132:135], 0
Rt 130,248,8704
buffer_load_dwordx4 v241, s[16:19], s92 offen offset:1024 lds
v_mfma_f32_16x16x32_bf16 v[108:111], a[148:151], v[132:135], 0
Rt 132,249,8192
v_mul_f32_e32 v53, s54, v53
Cv 52,52,53
v_mfma_f32_16x16x32_bf16 v[100:103], a[148:151], v[128:131], 0
Rt 134,249,8704
v_accvgpr_write_b32 a74, v52
v_mfma_f32_16x16x32_bf16 v[112:115], a[152:155], v[128:131], 0
Rt 136,250,8192
v_mul_f32_e32 v54, s54, v54
v_mfma_f32_16x16x32_bf16 v[120:123], a[152:155], v[132:135], 0
Rt 138,250,8704
buffer_load_dwordx4 v242, s[16:19], s92 offen offset:2048 lds
v_mfma_f32_16x16x32_bf16 v[124:127], a[156:159], v[132:135], 0
Rt 140,251,8192
v_mul_f32_e32 v55, s54, v55
v_mfma_f32_16x16x32_bf16 v[116:119], a[156:159], v[128:131], 0
Rt 142,251,8704
s_waitcnt lgkmcnt(8)
Mp 96,160,136
Rt 144,248,12288
Cv 54,54,55
v_accvgpr_write_b32 a75, v54
Mp 104,160,140
Rt 146,248,12800
s_add_u32 s38, s38, 0xffff0080
Mp 108,164,140
Rt 148,249,12288
s_add_u32 m0, s36, 0
Mp 100,164,136
Rt 150,249,12800
buffer_load_dwordx4 v8, s[4:7], s38 offen offset:0 lds
Mp 112,168,136
Rt 152,250,12288
v_mul_f32_e32 v56, s54, v56
Mp 120,168,140
Rt 154,250,12800
v_mul_f32_e32 v57, s54, v57
Mp 124,172,140
Rt 156,251,12288
Cv 56,56,57
Mp 116,172,136
)" R"(
Rt 158,251,12800
buffer_load_dwordx4 v9, s[4:7], s38 offen offset:1024 lds
s_waitcnt lgkmcnt(8)
Mp 96,128,144
Rt 160,248,16384
v_accvgpr_write_b32 a76, v56
Mp 104,128,148
Rt 162,248,16896
v_mul_f32_e32 v58, s54, v58
Mp 108,132,148
Rt 164,249,16384
v_mul_f32_e32 v59, s54, v59
Cv 58,58,59
Mp 100,132,144
Rt 166,249,16896
buffer_load_dwordx4 v10, s[4:7], s38 offen offset:2048 lds
Mp 112,136,144
Rt 168,250,16384
v_accvgpr_write_b32 a77, v58
Mp 120,136,148
Rt 170,250,16896
v_mul_f32_e32 v60, s54, v60
v_mul_f32_e32 v61, s54, v61
Mp 124,140,148
Rt 172,251,16384
Cv 60,60,61
Mp 116,140,144
Rt 174,251,16896
buffer_load_dwordx4 v11, s[4:7], s38 offen offset:3072 lds
s_waitcnt lgkmcnt(8)
Mp 96,144,152
Rt 128,248,20480
v_accvgpr_write_b32 a78, v60
v_mul_f32_e32 v62, s54, v62
Mp 104,144,156
Rt 130,248,20992
s_add_u32 s38, s38, 0x10000
Mp 108,148,156
Rt 132,249,20480
s_add_u32 m0, s36, 4096
Mp 100,148,152
Rt 134,249,20992
buffer_load_dwordx4 v8, s[4:7], s38 offen offset:0 lds
Mp 112,152,152
Rt 136,250,20480
v_mul_f32_e32 v63, s54, v63
Mp 120,152,156
Rt 138,250,20992
Cv 62,62,63
Mp 124,156,156
Rt 140,251,20480
v_accvgpr_write_b32 a79, v62
Mp 116,156,152
Rt 142,251,20992
buffer_load_dwordx4 v9, s[4:7], s38 offen offset:1024 lds
s_waitcnt lgkmcnt(8)
Mp 96,160,160
Rt 144,248,24576
v_mul_f32_e32 v64, s54, v64
Mp 104,160,164
Rt 146,248,25088
v_mul_f32_e32 v65, s54, v65
Mp 108,164,164
Rt 148,249,24576
)" R"(
Cv 64,64,65
v_accvgpr_write_b32 a80, v64
Mp 100,164,160
Rt 150,249,25088
buffer_load_dwordx4 v10, s[4:7], s38 offen offset:2048 lds
Mp 112,168,160
Rt 152,250,24576
v_mul_f32_e32 v66, s54, v66
Mp 120,168,164
Rt 154,250,25088
v_mul_f32_e32 v67, s54, v67
Cv 66,66,67
Mp 124,172,164
Rt 156,251,24576
v_accvgpr_write_b32 a81, v66
Mp 116,172,160
Rt 158,251,25088
buffer_load_dwordx4 v11, s[4:7], s38 offen offset:3072 lds
s_waitcnt lgkmcnt(8)
Mp 96,128,168
Rt 160,244,32768
v_mul_f32_e32 v68, s54, v68
v_mul_f32_e32 v69, s54, v69
Mp 104,128,172
Rt 162,244,33280
Cv 68,68,69
s_mov_b32 s85, 0xc108800
Mp 108,132,172
Rt 164,245,32768
v_accvgpr_write_b32 a82, v68
v_and_b32 v2, 7, v0
Mp 100,132,168
Rt 166,245,33280
v_mul_f32_e32 v70, s54, v70
v_mul_f32_e32 v71, s54, v71
v_lshlrev_b32 v2, 7, v2
Mp 112,136,168
Rt 168,246,32768
Cv 70,70,71
Mp 120,136,172
Rt 170,246,33280
v_accvgpr_write_b32 a83, v70
buffer_load_dword v4, v2, s[20:23], s85 offen sc0 sc1
Mp 124,140,172
Rt 172,247,32768
v_mul_f32_e32 v72, s54, v72
Mp 116,140,168
Rt 174,247,33280
s_waitcnt lgkmcnt(8)
s_barrier
Mp 96,144,176
Rt 128,244,36864
v_mul_f32_e32 v73, s54, v73
Mp 104,144,180
Rt 130,244,37376
s_add_u32 s39, s39, 0x40000
Mp 108,148,180
Rt 132,245,36864
s_add_u32 m0, s37, 0
Cv 72,72,73
Mp 100,148,176
Rt 134,245,37376
buffer_load_dwordx4 v12, s[8:11], s39 offen offset:0 lds
Mp 112,152,176
Rt 136,246,36864
)" R"(
v_accvgpr_write_b32 a84, v72
Mp 120,152,180
Rt 138,246,37376
v_mul_f32_e32 v74, s54, v74
v_mul_f32_e32 v75, s54, v75
Mp 124,156,180
Rt 140,247,36864
Cv 74,74,75
Mp 116,156,176
Rt 142,247,37376
s_waitcnt lgkmcnt(8)
Mp 96,160,184
Rt 144,244,40960
v_accvgpr_write_b32 a85, v74
v_mul_f32_e32 v76, s54, v76
Mp 104,160,188
Rt 146,244,41472
buffer_load_dwordx4 v13, s[8:11], s39 offen offset:1024 lds
Mp 108,164,188
Rt 148,245,40960
v_mul_f32_e32 v77, s54, v77
Mp 100,164,184
Rt 150,245,41472
Cv 76,76,77
v_accvgpr_write_b32 a86, v76
Mp 112,168,184
Rt 152,246,40960
v_mul_f32_e32 v78, s54, v78
Mp 120,168,188
Rt 154,246,41472
v_mul_f32_e32 v79, s54, v79
Mp 124,172,188
Rt 156,247,40960
Cv 78,78,79
Mp 116,172,184
Rt 158,247,41472
buffer_load_dwordx4 v14, s[8:11], s39 offen offset:2048 lds
s_waitcnt lgkmcnt(8)
Mp 96,128,192
Rt 160,244,45056
v_accvgpr_write_b32 a87, v78
Mp 104,128,196
Rt 162,244,45568
v_mul_f32_e32 v80, s54, v80
Mp 108,132,196
Rt 164,245,45056
v_mul_f32_e32 v81, s54, v81
Cv 80,80,81
Mp 100,132,192
Rt 166,245,45568
v_accvgpr_write_b32 a88, v80
Mp 112,136,192
Rt 168,246,45056
v_mul_f32_e32 v82, s54, v82
Mp 120,136,196
Rt 170,246,45568
buffer_load_dwordx4 v15, s[8:11], s39 offen offset:3072 lds
Mp 124,140,196
Rt 172,247,45056
v_mul_f32_e32 v83, s54, v83
Mp 116,140,192
Rt 174,247,45568
s_waitcnt lgkmcnt(8)
Mp 96,144,200
)" R"(
Rt 128,244,49152
Cv 82,82,83
v_accvgpr_write_b32 a89, v82
Mp 104,144,204
Rt 130,244,49664
s_add_u32 s39, s39, 0x40000
Mp 108,148,204
Rt 132,245,49152
s_add_u32 m0, s37, 16384
Mp 100,148,200
Rt 134,245,49664
buffer_load_dwordx4 v12, s[8:11], s39 offen offset:0 lds
Mp 112,152,200
Rt 136,246,49152
v_mul_f32_e32 v84, s54, v84
Mp 120,152,204
Rt 138,246,49664
v_mul_f32_e32 v85, s54, v85
Mp 124,156,204
Rt 140,247,49152
Cv 84,84,85
Mp 116,156,200
Rt 142,247,49664
v_accvgpr_write_b32 a90, v84
s_waitcnt lgkmcnt(8)
Mp 96,160,208
Rt 144,244,53248
v_mul_f32_e32 v86, s54, v86
Mp 104,160,212
Rt 146,244,53760
buffer_load_dwordx4 v13, s[8:11], s39 offen offset:1024 lds
Mp 108,164,212
Rt 148,245,53248
v_mul_f32_e32 v87, s54, v87
Cv 86,86,87
Mp 100,164,208
Rt 150,245,53760
v_accvgpr_write_b32 a91, v86
Mp 112,168,208
Rt 152,246,53248
v_mul_f32_e32 v88, s54, v88
Mp 120,168,212
Rt 154,246,53760
v_mul_f32_e32 v89, s54, v89
Cv 88,88,89
Mp 124,172,212
Rt 156,247,53248
v_accvgpr_write_b32 a92, v88
Mp 116,172,208
Rt 158,247,53760
buffer_load_dwordx4 v14, s[8:11], s39 offen offset:2048 lds
s_waitcnt lgkmcnt(8)
Mp 96,128,216
Rt 160,244,57344
v_mul_f32_e32 v90, s54, v90
v_mul_f32_e32 v91, s54, v91
Mp 104,128,220
Rt 162,244,57856
Cv 90,90,91
Mp 108,132,220
Rt 164,245,57344
v_accvgpr_write_b32 a93, v90
Mp 100,132,216
Rt 166,245,57856
)" R"(
v_mul_f32_e32 v92, s54, v92
v_mul_f32_e32 v93, s54, v93
Mp 112,136,216
Rt 168,246,57344
Cv 92,92,93
Mp 120,136,220
Rt 170,246,57856
buffer_load_dwordx4 v15, s[8:11], s39 offen offset:3072 lds
Mp 124,140,220
Rt 172,247,57344
v_accvgpr_write_b32 a94, v92
Mp 116,140,216
Rt 174,247,57856
v_mul_f32_e32 v94, s54, v94
s_waitcnt lgkmcnt(8)
Mp 96,144,224
Rt 128,248,32768
v_mul_f32_e32 v95, s54, v95
Mp 104,144,228
Rt 130,248,33280
Cv 94,94,95
Mp 108,148,228
Rt 132,249,32768
v_accvgpr_write_b32 a95, v94
Mp 100,148,224
Rt 134,249,33280
Mp 112,152,224
Rt 136,250,32768
Mp 120,152,228
Rt 138,250,33280
Mp 124,156,228
Rt 140,251,32768
Mp 116,156,224
Rt 142,251,33280
s_waitcnt vmcnt(8) lgkmcnt(8)
s_barrier
Mp 96,160,232
Rt 144,248,36864
v_xor_b32 v4, s3, v4
v_cmp_gt_u32_e32 vcc, 2, v4
s_andn2_b32 s28, 0xff, vcc_lo
s_cbranch_scc1 .Lzsp10
.Lzok10:
Mp 104,160,236
Rt 146,248,37376
s_add_u32 s38, s38, 0xffff0080
v_xor_b32 v4, s3, v4
Mp 108,164,236
Rt 148,249,36864
s_add_u32 m0, s36, 32768
Mp 100,164,232
Rt 150,249,37376
buffer_load_dwordx4 v8, s[4:7], s38 offen offset:0 lds
Mp 112,168,232
Rt 152,250,36864
v_and_b32 v4, 1, v4
Mp 120,168,236
Rt 154,250,37376
Mp 124,172,236
Rt 156,251,36864
v_cmp_eq_u32_e32 vcc, 1, v4
s_mov_b32 s85, vcc_lo
Mp 116,172,232
Rt 158,251,37376
s_and_b32 s28, s2, 7
)" R"(
s_waitcnt lgkmcnt(8)
v_mfma_f32_16x16x32_bf16 v[64:67], a[128:131], v[128:131], 0
Rt 160,248,40960
s_lshr_b32 s28, s85, s28
v_mfma_f32_16x16x32_bf16 v[72:75], a[128:131], v[132:135], 0
Rt 162,248,41472
buffer_load_dwordx4 v9, s[4:7], s38 offen offset:1024 lds
v_mfma_f32_16x16x32_bf16 v[76:79], a[132:135], v[132:135], 0
Rt 164,249,40960
s_xor_b32 s28, s28, s34
v_mfma_f32_16x16x32_bf16 v[68:71], a[132:135], v[128:131], 0
Rt 166,249,41472
s_and_b32 s28, s28, 1
v_mfma_f32_16x16x32_bf16 v[80:83], a[136:139], v[128:131], 0
Rt 168,250,40960
v_mul_f32_e32 v96, s54, v96
s_lshl_b32 s84, s28, 13
v_mfma_f32_16x16x32_bf16 v[88:91], a[136:139], v[132:135], 0
Rt 170,250,41472
v_mul_f32_e32 v97, s54, v97
v_mfma_f32_16x16x32_bf16 v[92:95], a[140:143], v[132:135], 0
Rt 172,251,40960
Cv 96,96,97
s_lshl1_add_u32 s28, s36, s84
v_mfma_f32_16x16x32_bf16 v[84:87], a[140:143], v[128:131], 0
Rt 174,251,41472
buffer_load_dwordx4 v10, s[4:7], s38 offen offset:2048 lds
s_waitcnt lgkmcnt(8)
Mp 64,144,136
Rt 128,248,45056
v_accvgpr_write_b32 a96, v96
Mp 72,144,140
Rt 130,248,45568
v_mul_f32_e32 v98, s54, v98
v_add_u32 v2, s28, v1
Mp 76,148,140
Rt 132,249,45056
v_mul_f32_e32 v99, s54, v99
Mp 68,148,136
Rt 134,249,45568
Cv 98,98,99
v_add_u32 v3, 0x1000, v2
Mp 80,152,136
Rt 136,250,45056
v_accvgpr_write_b32 a97, v98
s_bfe_u32 s28, s73, 0x30010
Mp 88,152,140
Rt 138,250,45568
buffer_load_dwordx4 v11, s[4:7], s38 offen offset:3072 lds
Mp 92,156,140
Rt 140,251,45056
v_mul_f32_e32 v100, s54, v100
s_lshr_b32 s28, s85, s28
Mp 84,156,136
Rt 142,251,45568
s_waitcnt lgkmcnt(8)
Mp 64,160,144
Rt 144,248,49152
v_mul_f32_e32 v101, s54, v101
Mp 72,160,148
Rt 146,248,49664
s_add_u32 s38, s38, 0x10000
s_not_b32 s28, s28
Mp 76,164,148
)" R"(
Rt 148,249,49152
s_add_u32 m0, s36, 36864
Mp 68,164,144
Rt 150,249,49664
buffer_load_dwordx4 v8, s[4:7], s38 offen offset:0 lds
Mp 80,168,144
Rt 152,250,49152
Cv 100,100,101
s_and_b32 s28, s28, 1
Mp 88,168,148
Rt 154,250,49664
v_accvgpr_write_b32 a98, v100
Mp 92,172,148
Rt 156,251,49152
v_mul_f32_e32 v102, s54, v102
s_lshl_b32 s28, s28, 13
Mp 84,172,144
Rt 158,251,49664
s_waitcnt lgkmcnt(8)
Mp 64,128,152
Rt 160,248,53248
v_mul_f32_e32 v103, s54, v103
s_add_u32 s73, s73, s28
Mp 72,128,156
Rt 162,248,53760
buffer_load_dwordx4 v9, s[4:7], s38 offen offset:1024 lds
Mp 76,132,156
Rt 164,249,53248
Cv 102,102,103
Mp 68,132,152
Rt 166,249,53760
v_accvgpr_write_b32 a99, v102
Mp 80,136,152
Rt 168,250,53248
v_mul_f32_e32 v104, s54, v104
Mp 88,136,156
Rt 170,250,53760
v_mul_f32_e32 v105, s54, v105
Mp 92,140,156
Rt 172,251,53248
Cv 104,104,105
Mp 84,140,152
Rt 174,251,53760
buffer_load_dwordx4 v10, s[4:7], s38 offen offset:2048 lds
s_waitcnt lgkmcnt(8)
Mp 64,144,160
Rt 128,248,57344
v_accvgpr_write_b32 a100, v104
Mp 72,144,164
Rt 130,248,57856
v_mul_f32_e32 v106, s54, v106
Mp 76,148,164
Rt 132,249,57344
v_mul_f32_e32 v107, s54, v107
Mp 68,148,160
Rt 134,249,57856
Cv 106,106,107
Mp 80,152,160
Rt 136,250,57344
v_accvgpr_write_b32 a101, v106
Mp 88,152,164
Rt 138,250,57856
buffer_load_dwordx4 v11, s[4:7], s38 offen offset:3072 lds
Mp 92,156,164
)" R"(
Rt 140,251,57344
v_mul_f32_e32 v108, s54, v108
Mp 84,156,160
Rt 142,251,57856
s_waitcnt lgkmcnt(8)
Mp 64,160,168
Rt 144,252,0
v_mul_f32_e32 v109, s54, v109
Mp 72,160,172
Rt 146,252,512
Cv 108,108,109
Mp 76,164,172
Rt 148,253,0
v_accvgpr_write_b32 a102, v108
Mp 68,164,168
Rt 150,253,512
v_mul_f32_e32 v110, s54, v110
Mp 80,168,168
Rt 152,254,0
v_mul_f32_e32 v111, s54, v111
Mp 88,168,172
Rt 154,254,512
Cv 110,110,111
Mp 92,172,172
Rt 156,255,0
v_accvgpr_write_b32 a103, v110
Mp 84,172,168
Rt 158,255,512
s_waitcnt vmcnt(12) lgkmcnt(8)
s_barrier
Mp 64,128,176
Rt 160,252,4096
v_mul_f32_e32 v112, s54, v112
Mp 72,128,180
Rt 162,252,4608
s_add_u32 s39, s39, 0x40000
Mp 76,132,180
Rt 164,253,4096
s_add_u32 m0, s37, 32768
Mp 68,132,176
Rt 166,253,4608
buffer_load_dwordx4 v12, s[8:11], s39 offen offset:0 lds
Mp 80,136,176
Rt 168,254,4096
v_mul_f32_e32 v113, s54, v113
Mp 88,136,180
Rt 170,254,4608
Cv 112,112,113
Mp 92,140,180
Rt 172,255,4096
v_accvgpr_write_b32 a104, v112
Mp 84,140,176
Rt 174,255,4608
s_waitcnt lgkmcnt(8)
Mp 64,144,184
Rt 128,252,8192
v_mul_f32_e32 v114, s54, v114
Mp 72,144,188
Rt 130,252,8704
buffer_load_dwordx4 v13, s[8:11], s39 offen offset:1024 lds
Mp 76,148,188
Rt 132,253,8192
v_mul_f32_e32 v115, s54, v115
Mp 68,148,184
Rt 134,253,8704
)" R"(
Cv 114,114,115
Mp 80,152,184
Rt 136,254,8192
v_accvgpr_write_b32 a105, v114
Mp 88,152,188
Rt 138,254,8704
v_mul_f32_e32 v116, s54, v116
Mp 92,156,188
Rt 140,255,8192
v_mul_f32_e32 v117, s54, v117
Mp 84,156,184
Rt 142,255,8704
buffer_load_dwordx4 v14, s[8:11], s39 offen offset:2048 lds
s_waitcnt lgkmcnt(8)
Mp 64,160,192
Rt 144,252,12288
Cv 116,116,117
Mp 72,160,196
Rt 146,252,12800
v_accvgpr_write_b32 a106, v116
Mp 76,164,196
Rt 148,253,12288
v_mul_f32_e32 v118, s54, v118
Mp 68,164,192
Rt 150,253,12800
v_mul_f32_e32 v119, s54, v119
Mp 80,168,192
Rt 152,254,12288
Cv 118,118,119
Mp 88,168,196
Rt 154,254,12800
buffer_load_dwordx4 v15, s[8:11], s39 offen offset:3072 lds
Mp 92,172,196
Rt 156,255,12288
v_accvgpr_write_b32 a107, v118
Mp 84,172,192
Rt 158,255,12800
s_waitcnt lgkmcnt(8)
Mp 64,128,200
Rt 160,252,16384
v_mul_f32_e32 v120, s54, v120
Mp 72,128,204
Rt 162,252,16896
v_mul_f32_e32 v121, s54, v121
Mp 76,132,204
Rt 164,253,16384
Cv 120,120,121
Mp 68,132,200
Rt 166,253,16896
v_accvgpr_write_b32 a108, v120
Mp 80,136,200
Rt 168,254,16384
v_mul_f32_e32 v122, s54, v122
Mp 88,136,204
Rt 170,254,16896
v_mul_f32_e32 v123, s54, v123
Mp 92,140,204
Rt 172,255,16384
Cv 122,122,123
Mp 84,140,200
Rt 174,255,16896
s_waitcnt lgkmcnt(8)
Mp 64,144,208
Rt 128,252,20480
)" R"(
Rt 0,18,0
Mp 72,144,212
Rt 130,252,20992
Rt 2,18,2048
Mp 76,148,212
Rt 132,253,20480
Rt 4,19,0
Mp 68,148,208
Rt 134,253,20992
Rt 6,19,2048
Mp 80,152,208
Rt 136,254,20480
Rt 8,20,0
Mp 88,152,212
Rt 138,254,20992
Rt 10,20,2048
Mp 92,156,212
Rt 140,255,20480
Rt 12,21,0
Mp 84,156,208
Rt 142,255,20992
Rt 14,21,2048
Mp 64,160,216
Rt 144,252,24576
Rt 16,22,0
Mp 72,160,220
Rt 146,252,25088
Rt 18,22,2048
Mp 76,164,220
Rt 148,253,24576
Rt 20,23,0
Mp 68,164,216
Rt 150,253,25088
Rt 22,23,2048
Mp 80,168,216
Rt 152,254,24576
Rt 24,24,0
Mp 88,168,220
Rt 154,254,25088
Rt 26,24,2048
Mp 92,172,220
Rt 156,255,24576
Rt 28,25,0
Mp 84,172,216
Rt 158,255,25088
Rt 30,25,2048
Mp 64,128,224
Rb 32,16,0
v_accvgpr_write_b32 a109, v122
Mp 72,128,228
Rb 36,16,2048
v_mul_f32_e32 v124, s54, v124
Mp 76,132,228
Rb 40,16,8192
v_mul_f32_e32 v125, s54, v125
Mp 68,132,224
Rb 44,16,10240
Cv 124,124,125
Mp 80,136,224
Rb 48,16,16384
v_accvgpr_write_b32 a110, v124
Mp 88,136,228
Rb 52,16,18432
v_mul_f32_e32 v126, s54, v126
)" R"(
Mp 92,140,228
Rb 56,16,24576
v_mul_f32_e32 v127, s54, v127
Mp 84,140,224
Rb 60,16,26624
s_waitcnt lgkmcnt(9)
Mp 64,144,232
Cv 126,126,127
v_accvgpr_write_b32 a111, v126
Mp 72,144,236
Mp 76,148,236
Mp 68,148,232
Mp 80,152,232
Mp 88,152,236
Mp 92,156,236
Mp 84,156,232
s_cmp_eq_u32 s91, 1
s_waitcnt vmcnt(12) lgkmcnt(0)
s_barrier
s_cbranch_scc1 .LzBV
Mz 32,0,32
Rt 128,18,16384
v_mul_f32_e32 v64, s54, v64
Mz 96,0,40
v_mul_f32_e32 v65, s54, v65
Cv 64,64,65
Mz 128,0,44
Rt 130,18,18432
v_accvgpr_write_b32 a112, v64
Mz 160,0,48
s_add_u32 s39, s39, 0x40000
v_mul_f32_e32 v66, s54, v66
Mz 192,0,52
Rt 132,19,16384
s_add_u32 m0, s37, 49152
Mz 224,0,56
buffer_load_dwordx4 v12, s[8:11], s39 offen offset:0 lds
v_mfma_f32_16x16x32_bf16 a[192:195], a[0:3], a[60:63], 0
Rt 134,19,18432
v_mul_f32_e32 v67, s54, v67
v_mfma_f32_16x16x32_bf16 a[196:199], a[4:7], a[60:63], 0
Cv 66,66,67
v_accvgpr_write_b32 a113, v66
Mz 228,4,56
Rt 136,20,16384
v_mul_f32_e32 v68, s54, v68
Mz 196,4,52
v_mul_f32_e32 v69, s54, v69
Cv 68,68,69
buffer_store_dwordx4 a[64:67], v2, s[20:23], s51 offen offset:0 sc0 sc1
Mz 164,4,48
Rt 138,20,18432
v_accvgpr_write_b32 a114, v68
Mz 132,4,44
v_mul_f32_e32 v70, s54, v70
v_mul_f32_e32 v71, s54, v71
Mz 100,4,40
Rt 140,21,16384
Cv 70,70,71
Mz 36,4,32
buffer_load_dwordx4 v13, s[8:11], s39 offen offset:1024 lds
Mz 40,8,32
Rt 142,21,18432
v_accvgpr_write_b32 a115, v70
Mz 104,8,40
)" R"(
v_mul_f32_e32 v72, s54, v72
v_mul_f32_e32 v73, s54, v73
Mz 136,8,44
Rt 144,22,16384
Cv 72,72,73
Mz 168,8,48
v_accvgpr_write_b32 a116, v72
v_mul_f32_e32 v74, s54, v74
buffer_store_dwordx4 a[68:71], v2, s[20:23], s51 offen offset:1024 sc0 sc1
Mz 200,8,52
Rt 146,22,18432
v_mul_f32_e32 v75, s54, v75
Mz 232,8,56
Cv 74,74,75
v_accvgpr_write_b32 a117, v74
v_mfma_f32_16x16x32_bf16 a[200:203], a[8:11], a[60:63], 0
Rt 148,23,16384
v_mul_f32_e32 v76, s54, v76
v_mfma_f32_16x16x32_bf16 a[204:207], a[12:15], a[60:63], 0
buffer_load_dwordx4 v14, s[8:11], s39 offen offset:2048 lds
Mz 236,12,56
Rt 150,23,18432
v_mul_f32_e32 v77, s54, v77
Mz 204,12,52
Cv 76,76,77
v_accvgpr_write_b32 a118, v76
Mz 172,12,48
Rt 152,24,16384
v_mul_f32_e32 v78, s54, v78
Mz 140,12,44
v_mul_f32_e32 v79, s54, v79
Cv 78,78,79
buffer_store_dwordx4 a[72:75], v2, s[20:23], s51 offen offset:2048 sc0 sc1
Mz 108,12,40
Rt 154,24,18432
v_accvgpr_write_b32 a119, v78
Mz 44,12,32
v_mul_f32_e32 v80, s54, v80
v_mul_f32_e32 v81, s54, v81
Mz 48,16,32
Rt 156,25,16384
Cv 80,80,81
Mz 112,16,40
buffer_load_dwordx4 v15, s[8:11], s39 offen offset:3072 lds
Mz 144,16,44
Rt 158,25,18432
v_accvgpr_write_b32 a120, v80
Mz 176,16,48
v_mul_f32_e32 v82, s54, v82
v_mul_f32_e32 v83, s54, v83
Mz 208,16,52
Rb 160,17,0
Cv 82,82,83
Mz 240,16,56
v_accvgpr_write_b32 a121, v82
v_mul_f32_e32 v84, s54, v84
buffer_store_dwordx4 a[76:79], v2, s[20:23], s51 offen offset:3072 sc0 sc1
v_mfma_f32_16x16x32_bf16 a[208:211], a[16:19], a[60:63], 0
Rb 164,17,2048
v_mul_f32_e32 v85, s54, v85
v_mfma_f32_16x16x32_bf16 a[212:215], a[20:23], a[60:63], 0
s_add_u32 s39, s39, 0x40000
Cv 84,84,85
Mz 244,20,56
)" R"(
Rb 168,17,8192
s_add_u32 m0, s37, 0
Mz 212,20,52
buffer_load_dwordx4 v12, s[8:11], s39 offen offset:0 lds
Mz 180,20,48
Rb 172,17,10240
v_accvgpr_write_b32 a122, v84
Mz 148,20,44
v_mul_f32_e32 v86, s54, v86
v_mul_f32_e32 v87, s54, v87
Mz 116,20,40
Rb 176,17,16384
Cv 86,86,87
Mz 52,20,32
v_accvgpr_write_b32 a123, v86
v_mul_f32_e32 v88, s54, v88
buffer_store_dwordx4 a[80:83], v3, s[20:23], s51 offen offset:0 sc0 sc1
Mz 56,24,32
Rb 180,17,18432
v_mul_f32_e32 v89, s54, v89
Mz 120,24,40
Cv 88,88,89
v_accvgpr_write_b32 a124, v88
Mz 152,24,44
Rb 184,17,24576
v_mul_f32_e32 v90, s54, v90
Mz 184,24,48
buffer_load_dwordx4 v13, s[8:11], s39 offen offset:1024 lds
Mz 216,24,52
Rb 188,17,26624
v_mul_f32_e32 v91, s54, v91
Mz 248,24,56
Cv 90,90,91
v_accvgpr_write_b32 a125, v90
v_mfma_f32_16x16x32_bf16 a[216:219], a[24:27], a[60:63], 0
v_mul_f32_e32 v92, s54, v92
v_mul_f32_e32 v93, s54, v93
v_mfma_f32_16x16x32_bf16 a[220:223], a[28:31], a[60:63], 0
Cv 92,92,93
v_accvgpr_write_b32 a126, v92
buffer_store_dwordx4 a[84:87], v3, s[20:23], s51 offen offset:1024 sc0 sc1
Mz 252,28,56
v_mul_f32_e32 v94, s54, v94
v_mul_f32_e32 v95, s54, v95
Mz 220,28,52
Cv 94,94,95
v_accvgpr_write_b32 a127, v94
Mz 188,28,48
Mz 156,28,44
buffer_load_dwordx4 v14, s[8:11], s39 offen offset:2048 lds
Mz 124,28,40
Mz 60,28,32
Mz 64,0,36
Mz 68,4,36
buffer_store_dwordx4 a[88:91], v3, s[20:23], s51 offen offset:2048 sc0 sc1
Mz 72,8,36
Mz 76,12,36
Mz 80,16,36
Mz 84,20,36
buffer_load_dwordx4 v15, s[8:11], s39 offen offset:3072 lds
Mz 88,24,36
Mz 92,28,36
s_waitcnt vmcnt(15) lgkmcnt(0)
s_barrier
Mv 32,128,160
)" R"(
Rt 0,18,32768
Mv 64,128,164
s_add_u32 s38, s38, 0xffff0080
Mv 96,128,168
Rt 2,18,34816
s_add_u32 m0, s36, 0
Mv 128,128,172
buffer_load_dwordx4 v8, s[4:7], s38 offen offset:0 lds
Mv 160,128,176
Rt 4,19,32768
Mv 192,128,180
Mv 224,128,184
Rt 6,19,34816
Ma 192,128,188
buffer_load_dwordx4 v9, s[4:7], s38 offen offset:1024 lds
Ma 196,132,188
Rt 8,20,32768
Mv 228,132,184
Mv 196,132,180
Rt 10,20,34816
Mv 164,132,176
buffer_load_dwordx4 v10, s[4:7], s38 offen offset:2048 lds
Mv 132,132,172
Rt 12,21,32768
Mv 100,132,168
Mv 68,132,164
Rt 14,21,34816
Mv 36,132,160
buffer_load_dwordx4 v11, s[4:7], s38 offen offset:3072 lds
Mv 40,136,160
Rt 16,22,32768
Mv 72,136,164
s_add_u32 s38, s38, 0x10000
Mv 104,136,168
Rt 18,22,34816
s_add_u32 m0, s36, 4096
Mv 136,136,172
buffer_load_dwordx4 v8, s[4:7], s38 offen offset:0 lds
Mv 168,136,176
Rt 20,23,32768
Mv 200,136,180
Mv 232,136,184
Rt 22,23,34816
Ma 200,136,188
buffer_load_dwordx4 v9, s[4:7], s38 offen offset:1024 lds
Ma 204,140,188
Rt 24,24,32768
Mv 236,140,184
Mv 204,140,180
Rt 26,24,34816
Mv 172,140,176
buffer_load_dwordx4 v10, s[4:7], s38 offen offset:2048 lds
Mv 140,140,172
Rt 28,25,32768
Mv 108,140,168
Mv 76,140,164
Rt 30,25,34816
Mv 44,140,160
buffer_load_dwordx4 v11, s[4:7], s38 offen offset:3072 lds
Mv 48,144,160
Rb 32,16,32768
Mv 80,144,164
Mv 112,144,168
Rb 36,16,34816
)" R"(
Mv 144,144,172
Mv 176,144,176
Rb 40,16,40960
Mv 208,144,180
Mv 240,144,184
Rb 44,16,43008
Ma 208,144,188
Ma 212,148,188
Rb 48,16,49152
Mv 244,148,184
buffer_store_dwordx4 a[92:95], v3, s[20:23], s51 offen offset:3072 sc0 sc1
Mv 212,148,180
Rb 52,16,51200
Mv 180,148,176
Mv 148,148,172
Rb 56,16,57344
Mv 116,148,168
Mv 84,148,164
Rb 60,16,59392
Mv 52,148,160
Mv 56,152,160
Mv 88,152,164
s_add_u32 s51, s51, 0x10000
buffer_store_dwordx4 a[96:99], v2, s[20:23], s51 offen offset:0 sc0 sc1
Mv 120,152,168
Mv 152,152,172
Mv 184,152,176
Mv 216,152,180
Mv 248,152,184
Ma 216,152,188
Ma 220,156,188
Mv 252,156,184
buffer_store_dwordx4 a[100:103], v2, s[20:23], s51 offen offset:1024 sc0 sc1
Mv 220,156,180
Mv 188,156,176
Mv 156,156,172
Mv 124,156,168
Mv 92,156,164
Mv 60,156,160
s_waitcnt vmcnt(19) lgkmcnt(0)
s_barrier
Mv 32,0,32
Rt 128,18,49152
Mv 64,0,36
Mv 96,0,40
Rt 130,18,51200
s_mov_b32 s24, s80
Mv 128,0,44
s_add_u32 s39, s39, 0x40000
s_mov_b32 s25, s81
Mv 160,0,48
Rt 132,19,49152
s_add_u32 m0, s37, 16384
Mv 192,0,52
buffer_load_dwordx4 v12, s[8:11], s39 offen offset:0 lds
Mv 224,0,56
Rt 134,19,51200
s_mov_b32 s26, 0x380000
Ma 192,0,60
s_mov_b32 s27, 0x20000
s_bfe_u32 s28, s2, 0x30005
Ma 196,4,60
Rt 136,20,49152
s_lshl3_add_u32 s29, s34, s28
Mv 228,4,56
)" R"(
buffer_store_dwordx4 a[104:107], v2, s[20:23], s51 offen offset:2048 sc0 sc1
Mv 196,4,52
Rt 138,20,51200
s_lshl2_add_u32 s29, s29, s33
Mv 164,4,48
s_mul_i32 s83, s29, 7168
s_mul_i32 s29, s91, 448
Mv 132,4,44
Rt 140,21,49152
s_add_u32 s83, s83, s29
Mv 100,4,40
buffer_load_dwordx4 v13, s[8:11], s39 offen offset:1024 lds
Mv 68,4,36
Rt 142,21,51200
s_lshl_b32 s82, s34, 21
Mv 36,4,32
s_add_u32 s82, s82, 0x6000000
s_mul_i32 s94, s91, 14
Mv 40,8,32
Rt 144,22,49152
s_add_u32 s94, s94, s33
Mv 72,8,36
buffer_store_dwordx4 a[108:111], v2, s[20:23], s51 offen offset:3072 sc0 sc1
Mv 104,8,40
Rt 146,22,51200
s_mov_b32 s92, s28
Mv 136,8,44
v_lshrrev_b32 v4, 5, v0
v_mul_u32_u24 v4, 384, v4
Mv 168,8,48
Rt 148,23,49152
v_add_u32 v28, v4, v1
Mv 200,8,52
buffer_load_dwordx4 v14, s[8:11], s39 offen offset:2048 lds
Mv 232,8,56
Rt 150,23,51200
v_and_b32 v4, 31, v0
Ma 200,8,60
v_cmp_lt_u32_e32 vcc, 27, v4
v_mov_b32 v4, 0x40000000
Ma 204,12,60
Rt 152,24,49152
v_lshrrev_b32 v29, 2, v0
Mv 236,12,56
buffer_store_dwordx4 a[112:115], v3, s[20:23], s51 offen offset:0 sc0 sc1
Mv 204,12,52
Rt 154,24,51200
v_cndmask_b32_e32 v28, v28, v4, vcc
Mv 172,12,48
v_and_b32 v4, 48, v1
v_lshl_or_b32 v29, v29, 9, v4
Mv 140,12,44
Rt 156,25,49152
s_lshr_b32 s28, s33, 1
Mv 108,12,40
buffer_load_dwordx4 v15, s[8:11], s39 offen offset:3072 lds
Mv 76,12,36
Rt 158,25,51200
s_lshl_b32 s28, s28, 6
Mv 44,12,32
s_and_b32 s29, s33, 1
s_lshl3_add_u32 s28, s29, s28
Mv 48,16,32
Rb 160,17,32768
)" R"(
s_add_u32 s28, s28, 0x20000
Mv 80,16,36
buffer_store_dwordx4 a[116:119], v3, s[20:23], s51 offen offset:1024 sc0 sc1
Mv 112,16,40
Rb 164,17,34816
v_add_u32 v29, s28, v29
Mv 144,16,44
s_add_u32 s39, s39, 0x40000
Mv 176,16,48
Rb 168,17,40960
s_add_u32 m0, s37, 32768
Mv 208,16,52
buffer_load_dwordx4 v12, s[8:11], s39 offen offset:0 lds
Mv 240,16,56
Rb 172,17,43008
Ma 208,16,60
Ma 212,20,60
Rb 176,17,49152
Mv 244,20,56
buffer_store_dwordx4 a[120:123], v3, s[20:23], s51 offen offset:2048 sc0 sc1
Mv 212,20,52
Rb 180,17,51200
Mv 180,20,48
Mv 148,20,44
Rb 184,17,57344
Mv 116,20,40
buffer_load_dwordx4 v13, s[8:11], s39 offen offset:1024 lds
Mv 84,20,36
Rb 188,17,59392
Mv 52,20,32
Mv 56,24,32
Mv 88,24,36
buffer_store_dwordx4 a[124:127], v3, s[20:23], s51 offen offset:3072 sc0 sc1
Mv 120,24,40
Mv 152,24,44
Mv 184,24,48
Mv 216,24,52
buffer_load_dwordx4 v14, s[8:11], s39 offen offset:2048 lds
Mv 248,24,56
Ma 216,24,60
Ma 220,28,60
Mv 252,28,56
Mv 220,28,52
Mv 188,28,48
Mv 156,28,44
Mv 124,28,40
buffer_load_dwordx4 v15, s[8:11], s39 offen offset:3072 lds
Mv 92,28,36
Mv 60,28,32
s_waitcnt vmcnt(17) lgkmcnt(0)
s_barrier
Mv 32,128,160
Rt 0,18,0
Mv 64,128,164
s_add_u32 s38, s38, 0xffff0080
Mv 96,128,168
Rt 2,18,2048
s_add_u32 m0, s36, 32768
Mv 128,128,172
buffer_load_dwordx4 v8, s[4:7], s38 offen offset:0 lds
Mv 160,128,176
Rt 4,19,0
Mv 192,128,180
Mv 224,128,184
Rt 6,19,2048
)" R"(
Ma 192,128,188
buffer_load_dwordx4 v9, s[4:7], s38 offen offset:1024 lds
Ma 196,132,188
Rt 8,20,0
s_mov_b32 s40, 5
Mv 228,132,184
Mv 196,132,180
Rt 10,20,2048
Mv 164,132,176
buffer_load_dwordx4 v10, s[4:7], s38 offen offset:2048 lds
Mv 132,132,172
Rt 12,21,0
Mv 100,132,168
Mv 68,132,164
Rt 14,21,2048
Mv 36,132,160
buffer_load_dwordx4 v11, s[4:7], s38 offen offset:3072 lds
Mv 40,136,160
Rt 16,22,0
Mv 72,136,164
s_add_u32 s38, s38, 0x10000
Mv 104,136,168
Rt 18,22,2048
s_add_u32 m0, s36, 36864
Mv 136,136,172
buffer_load_dwordx4 v8, s[4:7], s38 offen offset:0 lds
Mv 168,136,176
Rt 20,23,0
Mv 200,136,180
Mv 232,136,184
Rt 22,23,2048
Ma 200,136,188
buffer_load_dwordx4 v9, s[4:7], s38 offen offset:1024 lds
Ma 204,140,188
Rt 24,24,0
Mv 236,140,184
Mv 204,140,180
Rt 26,24,2048
Mv 172,140,176
buffer_load_dwordx4 v10, s[4:7], s38 offen offset:2048 lds
Mv 140,140,172
Rt 28,25,0
Mv 108,140,168
Mv 76,140,164
Rt 30,25,2048
Mv 44,140,160
buffer_load_dwordx4 v11, s[4:7], s38 offen offset:3072 lds
Mv 48,144,160
Rb 32,16,0
Mv 80,144,164
Mv 112,144,168
Rb 36,16,2048
Mv 144,144,172
Mv 176,144,176
Rb 40,16,8192
s_add_u32 m0, s37, 0x10000
Mv 208,144,180
s_add_u32 s29, s83, 0
Mv 240,144,184
Rb 44,16,10240
Ma 208,144,188
buffer_load_dwordx4 v28, s[24:27], s29 offen offset:0 lds
Ma 212,148,188
Rb 48,16,16384
)" R"(
s_add_u32 s29, s83, 768
Mv 244,148,184
buffer_load_dwordx4 v28, s[24:27], s29 offen offset:1024 lds
Mv 212,148,180
Rb 52,16,18432
s_add_u32 s29, s83, 1536
Mv 180,148,176
buffer_load_dwordx4 v28, s[24:27], s29 offen offset:2048 lds
Mv 148,148,172
Rb 56,16,24576
s_add_u32 s29, s83, 2304
Mv 116,148,168
buffer_load_dwordx4 v28, s[24:27], s29 offen offset:3072 lds
Mv 84,148,164
Rb 60,16,26624
Mv 52,148,160
Mv 56,152,160
Mv 88,152,164
Mv 120,152,168
Mv 152,152,172
Mv 184,152,176
Mv 216,152,180
Mv 248,152,184
Ma 216,152,188
Ma 220,156,188
Mv 252,156,184
Mv 220,156,180
Mv 188,156,176
Mv 156,156,172
Mv 124,156,168
Mv 92,156,164
Mv 60,156,160
s_waitcnt vmcnt(14) lgkmcnt(0)
s_barrier
buffer_store_dword v6, v7, s[20:23], s52 offen offset:0 sc0 sc1
.LzPBK:
Mv 32,0,32
Rt 128,18,16384
Mv 64,0,36
Mv 96,0,40
Rt 130,18,18432
Mv 128,0,44
s_add_u32 s39, s39, 0x40000
Mv 160,0,48
Rt 132,19,16384
s_add_u32 m0, s37, 49152
Mv 192,0,52
buffer_load_dwordx4 v12, s[8:11], s39 offen offset:0 lds
Mv 224,0,56
Rt 134,19,18432
Ma 192,0,60
Ma 196,4,60
Rt 136,20,16384
Mv 228,4,56
Mv 196,4,52
Rt 138,20,18432
Mv 164,4,48
Mv 132,4,44
Rt 140,21,16384
Mv 100,4,40
buffer_load_dwordx4 v13, s[8:11], s39 offen offset:1024 lds
Mv 68,4,36
Rt 142,21,18432
Mv 36,4,32
Mv 40,8,32
)" R"(
Rt 144,22,16384
Mv 72,8,36
Mv 104,8,40
Rt 146,22,18432
Mv 136,8,44
Mv 168,8,48
Rt 148,23,16384
Mv 200,8,52
buffer_load_dwordx4 v14, s[8:11], s39 offen offset:2048 lds
Mv 232,8,56
Rt 150,23,18432
Ma 200,8,60
Ma 204,12,60
Rt 152,24,16384
Mv 236,12,56
Mv 204,12,52
Rt 154,24,18432
Mv 172,12,48
Mv 140,12,44
Rt 156,25,16384
Mv 108,12,40
buffer_load_dwordx4 v15, s[8:11], s39 offen offset:3072 lds
Mv 76,12,36
Rt 158,25,18432
Mv 44,12,32
Mv 48,16,32
Rb 160,17,0
Mv 80,16,36
Mv 112,16,40
Rb 164,17,2048
Mv 144,16,44
s_add_u32 s39, s39, 0x40000
Mv 176,16,48
Rb 168,17,8192
s_add_u32 m0, s37, 0
Mv 208,16,52
buffer_load_dwordx4 v12, s[8:11], s39 offen offset:0 lds
Mv 240,16,56
Rb 172,17,10240
Ma 208,16,60
Ma 212,20,60
Rb 176,17,16384
Mv 244,20,56
Mv 212,20,52
Rb 180,17,18432
Mv 180,20,48
Mv 148,20,44
Rb 184,17,24576
Mv 116,20,40
buffer_load_dwordx4 v13, s[8:11], s39 offen offset:1024 lds
Mv 84,20,36
Rb 188,17,26624
Mv 52,20,32
Mv 56,24,32
Mv 88,24,36
Mv 120,24,40
Mv 152,24,44
Mv 184,24,48
Mv 216,24,52
buffer_load_dwordx4 v14, s[8:11], s39 offen offset:2048 lds
Mv 248,24,56
Ma 216,24,60
Ma 220,28,60
Mv 252,28,56
)" R"(
Mv 220,28,52
Mv 188,28,48
Mv 156,28,44
Mv 124,28,40
buffer_load_dwordx4 v15, s[8:11], s39 offen offset:3072 lds
Mv 92,28,36
Mv 60,28,32
s_waitcnt vmcnt(8) lgkmcnt(0)
s_barrier
Mv 32,128,160
Rt 0,18,32768
Mv 64,128,164
s_add_u32 s38, s38, 0xffff0080
Mv 96,128,168
Rt 2,18,34816
s_add_u32 m0, s36, 0
Mv 128,128,172
buffer_load_dwordx4 v8, s[4:7], s38 offen offset:0 lds
Mv 160,128,176
Rt 4,19,32768
Mv 192,128,180
Mv 224,128,184
Rt 6,19,34816
Ma 192,128,188
buffer_load_dwordx4 v9, s[4:7], s38 offen offset:1024 lds
Ma 196,132,188
Rt 8,20,32768
Mv 228,132,184
Mv 196,132,180
Rt 10,20,34816
Mv 164,132,176
buffer_load_dwordx4 v10, s[4:7], s38 offen offset:2048 lds
Mv 132,132,172
Rt 12,21,32768
Mv 100,132,168
Mv 68,132,164
Rt 14,21,34816
Mv 36,132,160
buffer_load_dwordx4 v11, s[4:7], s38 offen offset:3072 lds
Mv 40,136,160
Rt 16,22,32768
Mv 72,136,164
s_add_u32 s38, s38, 0x10000
Mv 104,136,168
Rt 18,22,34816
s_add_u32 m0, s36, 4096
Mv 136,136,172
buffer_load_dwordx4 v8, s[4:7], s38 offen offset:0 lds
Mv 168,136,176
Rt 20,23,32768
Mv 200,136,180
Mv 232,136,184
Rt 22,23,34816
Ma 200,136,188
buffer_load_dwordx4 v9, s[4:7], s38 offen offset:1024 lds
Ma 204,140,188
Rt 24,24,32768
Mv 236,140,184
Mv 204,140,180
Rt 26,24,34816
Mv 172,140,176
buffer_load_dwordx4 v10, s[4:7], s38 offen offset:2048 lds
Mv 140,140,172
Rt 28,25,32768
Mv 108,140,168
)" R"(
Mv 76,140,164
Rt 30,25,34816
Mv 44,140,160
buffer_load_dwordx4 v11, s[4:7], s38 offen offset:3072 lds
Mv 48,144,160
Rb 32,16,32768
Mv 80,144,164
Mv 112,144,168
Rb 36,16,34816
Mv 144,144,172
Mv 176,144,176
Rb 40,16,40960
Mv 208,144,180
Mv 240,144,184
Rb 44,16,43008
Ma 208,144,188
Ma 212,148,188
Rb 48,16,49152
Mv 244,148,184
Mv 212,148,180
Rb 52,16,51200
Mv 180,148,176
Mv 148,148,172
Rb 56,16,57344
Mv 116,148,168
Mv 84,148,164
Rb 60,16,59392
Mv 52,148,160
Mv 56,152,160
Mv 88,152,164
Mv 120,152,168
Mv 152,152,172
Mv 184,152,176
Mv 216,152,180
Mv 248,152,184
Ma 216,152,188
Ma 220,156,188
Mv 252,156,184
Mv 220,156,180
Mv 188,156,176
Mv 156,156,172
Mv 124,156,168
Mv 92,156,164
Mv 60,156,160
s_waitcnt vmcnt(12) lgkmcnt(0)
s_barrier
Mv 32,0,32
Rt 128,18,49152
Mv 64,0,36
Mv 96,0,40
Rt 130,18,51200
Mv 128,0,44
s_add_u32 s39, s39, 0x40000
Mv 160,0,48
Rt 132,19,49152
s_add_u32 m0, s37, 16384
Mv 192,0,52
buffer_load_dwordx4 v12, s[8:11], s39 offen offset:0 lds
Mv 224,0,56
Rt 134,19,51200
Ma 192,0,60
Ma 196,4,60
Rt 136,20,49152
Mv 228,4,56
Mv 196,4,52
)" R"(
Rt 138,20,51200
Mv 164,4,48
Mv 132,4,44
Rt 140,21,49152
Mv 100,4,40
buffer_load_dwordx4 v13, s[8:11], s39 offen offset:1024 lds
Mv 68,4,36
Rt 142,21,51200
Mv 36,4,32
Mv 40,8,32
Rt 144,22,49152
Mv 72,8,36
Mv 104,8,40
Rt 146,22,51200
Mv 136,8,44
Mv 168,8,48
Rt 148,23,49152
Mv 200,8,52
buffer_load_dwordx4 v14, s[8:11], s39 offen offset:2048 lds
Mv 232,8,56
Rt 150,23,51200
Ma 200,8,60
Ma 204,12,60
Rt 152,24,49152
Mv 236,12,56
Mv 204,12,52
Rt 154,24,51200
Mv 172,12,48
Mv 140,12,44
Rt 156,25,49152
Mv 108,12,40
buffer_load_dwordx4 v15, s[8:11], s39 offen offset:3072 lds
Mv 76,12,36
Rt 158,25,51200
Mv 44,12,32
Mv 48,16,32
Rb 160,17,32768
Mv 80,16,36
Mv 112,16,40
Rb 164,17,34816
Mv 144,16,44
s_add_u32 s39, s39, 0x40000
Mv 176,16,48
Rb 168,17,40960
s_add_u32 m0, s37, 32768
Mv 208,16,52
buffer_load_dwordx4 v12, s[8:11], s39 offen offset:0 lds
Mv 240,16,56
Rb 172,17,43008
Ma 208,16,60
Ma 212,20,60
Rb 176,17,49152
Mv 244,20,56
Mv 212,20,52
Rb 180,17,51200
Mv 180,20,48
Mv 148,20,44
Rb 184,17,57344
Mv 116,20,40
buffer_load_dwordx4 v13, s[8:11], s39 offen offset:1024 lds
Mv 84,20,36
Rb 188,17,59392
Mv 52,20,32
Mv 56,24,32
)" R"(
Mv 88,24,36
Mv 120,24,40
Mv 152,24,44
Mv 184,24,48
Mv 216,24,52
buffer_load_dwordx4 v14, s[8:11], s39 offen offset:2048 lds
Mv 248,24,56
Ma 216,24,60
Ma 220,28,60
Mv 252,28,56
Mv 220,28,52
Mv 188,28,48
Mv 156,28,44
Mv 124,28,40
buffer_load_dwordx4 v15, s[8:11], s39 offen offset:3072 lds
Mv 92,28,36
Mv 60,28,32
s_waitcnt vmcnt(8) lgkmcnt(0)
s_barrier
Mv 32,128,160
Rt 0,18,0
Mv 64,128,164
s_add_u32 s38, s38, 0xffff0080
Mv 96,128,168
Rt 2,18,2048
s_add_u32 m0, s36, 32768
Mv 128,128,172
buffer_load_dwordx4 v8, s[4:7], s38 offen offset:0 lds
Mv 160,128,176
Rt 4,19,0
Mv 192,128,180
Mv 224,128,184
Rt 6,19,2048
Ma 192,128,188
buffer_load_dwordx4 v9, s[4:7], s38 offen offset:1024 lds
Ma 196,132,188
Rt 8,20,0
Mv 228,132,184
Mv 196,132,180
Rt 10,20,2048
Mv 164,132,176
buffer_load_dwordx4 v10, s[4:7], s38 offen offset:2048 lds
Mv 132,132,172
Rt 12,21,0
Mv 100,132,168
Mv 68,132,164
Rt 14,21,2048
Mv 36,132,160
buffer_load_dwordx4 v11, s[4:7], s38 offen offset:3072 lds
Mv 40,136,160
Rt 16,22,0
Mv 72,136,164
s_add_u32 s38, s38, 0x10000
Mv 104,136,168
Rt 18,22,2048
s_add_u32 m0, s36, 36864
Mv 136,136,172
buffer_load_dwordx4 v8, s[4:7], s38 offen offset:0 lds
Mv 168,136,176
Rt 20,23,0
Mv 200,136,180
Mv 232,136,184
Rt 22,23,2048
Ma 200,136,188
buffer_load_dwordx4 v9, s[4:7], s38 offen offset:1024 lds
)" R"(
Ma 204,140,188
Rt 24,24,0
Mv 236,140,184
Mv 204,140,180
Rt 26,24,2048
Mv 172,140,176
buffer_load_dwordx4 v10, s[4:7], s38 offen offset:2048 lds
Mv 140,140,172
Rt 28,25,0
Mv 108,140,168
Mv 76,140,164
Rt 30,25,2048
Mv 44,140,160
buffer_load_dwordx4 v11, s[4:7], s38 offen offset:3072 lds
Mv 48,144,160
Rb 32,16,0
Mv 80,144,164
Mv 112,144,168
Rb 36,16,2048
Mv 144,144,172
Mv 176,144,176
Rb 40,16,8192
Mv 208,144,180
Mv 240,144,184
Rb 44,16,10240
Ma 208,144,188
Ma 212,148,188
Rb 48,16,16384
Mv 244,148,184
Mv 212,148,180
Rb 52,16,18432
Mv 180,148,176
Mv 148,148,172
Rb 56,16,24576
Mv 116,148,168
Mv 84,148,164
Rb 60,16,26624
Mv 52,148,160
Mv 56,152,160
Mv 88,152,164
Mv 120,152,168
Mv 152,152,172
Mv 184,152,176
Mv 216,152,180
Mv 248,152,184
Ma 216,152,188
Ma 220,156,188
Mv 252,156,184
Mv 220,156,180
s_sub_u32 s40, s40, 1
Mv 188,156,176
Mv 156,156,172
Mv 124,156,168
Mv 92,156,164
Mv 60,156,160
s_cmp_lg_u32 s40, 0
s_waitcnt vmcnt(12) lgkmcnt(0)
s_barrier
s_cbranch_scc1 .LzPBK
s_branch .LzPBKX
.LzBV:
Mz 32,32,0
Rt 128,18,16384
v_mul_f32_e32 v64, s54, v64
Mz 36,36,0
)" R"(
v_mul_f32_e32 v65, s54, v65
Cv 64,64,65
Mz 96,40,0
Rt 130,18,18432
v_accvgpr_write_b32 a112, v64
Mz 100,44,0
s_add_u32 s39, s39, 0x40000
v_mul_f32_e32 v66, s54, v66
Mz 160,48,0
Rt 132,19,16384
s_add_u32 m0, s37, 49152
Mz 164,52,0
buffer_load_dwordx4 v12, s[8:11], s39 offen offset:0 lds
Mz 224,56,0
Rt 134,19,18432
v_mul_f32_e32 v67, s54, v67
Mz 228,60,0
Cv 66,66,67
v_accvgpr_write_b32 a113, v66
Mz 236,60,4
Rt 136,20,16384
v_mul_f32_e32 v68, s54, v68
Mz 232,56,4
v_mul_f32_e32 v69, s54, v69
Cv 68,68,69
buffer_store_dwordx4 a[64:67], v2, s[20:23], s51 offen offset:0 sc0 sc1
Mz 172,52,4
Rt 138,20,18432
v_accvgpr_write_b32 a114, v68
Mz 168,48,4
v_mul_f32_e32 v70, s54, v70
v_mul_f32_e32 v71, s54, v71
Mz 108,44,4
Rt 140,21,16384
Cv 70,70,71
Mz 104,40,4
buffer_load_dwordx4 v13, s[8:11], s39 offen offset:1024 lds
Mz 44,36,4
Rt 142,21,18432
v_accvgpr_write_b32 a115, v70
Mz 40,32,4
v_mul_f32_e32 v72, s54, v72
v_mul_f32_e32 v73, s54, v73
Mz 48,32,8
Rt 144,22,16384
Cv 72,72,73
Mz 52,36,8
v_accvgpr_write_b32 a116, v72
v_mul_f32_e32 v74, s54, v74
buffer_store_dwordx4 a[68:71], v2, s[20:23], s51 offen offset:1024 sc0 sc1
Mz 112,40,8
Rt 146,22,18432
v_mul_f32_e32 v75, s54, v75
Mz 116,44,8
Cv 74,74,75
v_accvgpr_write_b32 a117, v74
Mz 176,48,8
Rt 148,23,16384
v_mul_f32_e32 v76, s54, v76
Mz 180,52,8
buffer_load_dwordx4 v14, s[8:11], s39 offen offset:2048 lds
Mz 240,56,8
Rt 150,23,18432
v_mul_f32_e32 v77, s54, v77
)" R"(
Mz 244,60,8
Cv 76,76,77
v_accvgpr_write_b32 a118, v76
Mz 252,60,12
Rt 152,24,16384
v_mul_f32_e32 v78, s54, v78
Mz 248,56,12
v_mul_f32_e32 v79, s54, v79
Cv 78,78,79
buffer_store_dwordx4 a[72:75], v2, s[20:23], s51 offen offset:2048 sc0 sc1
Mz 188,52,12
Rt 154,24,18432
v_accvgpr_write_b32 a119, v78
Mz 184,48,12
v_mul_f32_e32 v80, s54, v80
v_mul_f32_e32 v81, s54, v81
Mz 124,44,12
Rt 156,25,16384
Cv 80,80,81
Mz 120,40,12
buffer_load_dwordx4 v15, s[8:11], s39 offen offset:3072 lds
Mz 60,36,12
Rt 158,25,18432
v_accvgpr_write_b32 a120, v80
Mz 56,32,12
v_mul_f32_e32 v82, s54, v82
v_mul_f32_e32 v83, s54, v83
Mz 128,40,16
Rb 160,17,0
Cv 82,82,83
Mz 132,44,16
v_accvgpr_write_b32 a121, v82
v_mul_f32_e32 v84, s54, v84
buffer_store_dwordx4 a[76:79], v2, s[20:23], s51 offen offset:3072 sc0 sc1
Mz 192,48,16
Rb 164,17,2048
v_mul_f32_e32 v85, s54, v85
Mz 196,52,16
s_add_u32 s39, s39, 0x40000
Cv 84,84,85
v_mfma_f32_16x16x32_bf16 a[192:195], a[56:59], a[16:19], 0
Rb 168,17,8192
s_add_u32 m0, s37, 0
v_mfma_f32_16x16x32_bf16 a[196:199], a[60:63], a[16:19], 0
buffer_load_dwordx4 v12, s[8:11], s39 offen offset:0 lds
v_mfma_f32_16x16x32_bf16 a[204:207], a[60:63], a[20:23], 0
Rb 172,17,10240
v_accvgpr_write_b32 a122, v84
v_mfma_f32_16x16x32_bf16 a[200:203], a[56:59], a[20:23], 0
v_mul_f32_e32 v86, s54, v86
v_mul_f32_e32 v87, s54, v87
Mz 204,52,20
Rb 176,17,16384
Cv 86,86,87
Mz 200,48,20
v_accvgpr_write_b32 a123, v86
v_mul_f32_e32 v88, s54, v88
buffer_store_dwordx4 a[80:83], v3, s[20:23], s51 offen offset:0 sc0 sc1
Mz 140,44,20
Rb 180,17,18432
v_mul_f32_e32 v89, s54, v89
Mz 136,40,20
Cv 88,88,89
v_accvgpr_write_b32 a124, v88
)" R"(
Mz 144,40,24
Rb 184,17,24576
v_mul_f32_e32 v90, s54, v90
Mz 148,44,24
buffer_load_dwordx4 v13, s[8:11], s39 offen offset:1024 lds
Mz 208,48,24
Rb 188,17,26624
v_mul_f32_e32 v91, s54, v91
Mz 212,52,24
Cv 90,90,91
v_accvgpr_write_b32 a125, v90
v_mfma_f32_16x16x32_bf16 a[208:211], a[56:59], a[24:27], 0
v_mul_f32_e32 v92, s54, v92
v_mul_f32_e32 v93, s54, v93
v_mfma_f32_16x16x32_bf16 a[212:215], a[60:63], a[24:27], 0
Cv 92,92,93
v_accvgpr_write_b32 a126, v92
buffer_store_dwordx4 a[84:87], v3, s[20:23], s51 offen offset:1024 sc0 sc1
v_mfma_f32_16x16x32_bf16 a[220:223], a[60:63], a[28:31], 0
v_mul_f32_e32 v94, s54, v94
v_mul_f32_e32 v95, s54, v95
v_mfma_f32_16x16x32_bf16 a[216:219], a[56:59], a[28:31], 0
Cv 94,94,95
v_accvgpr_write_b32 a127, v94
Mz 220,52,28
Mz 216,48,28
buffer_load_dwordx4 v14, s[8:11], s39 offen offset:2048 lds
Mz 156,44,28
Mz 152,40,28
Mz 64,32,16
Mz 68,36,16
buffer_store_dwordx4 a[88:91], v3, s[20:23], s51 offen offset:2048 sc0 sc1
Mz 72,32,20
Mz 76,36,20
Mz 80,32,24
Mz 84,36,24
buffer_load_dwordx4 v15, s[8:11], s39 offen offset:3072 lds
Mz 88,32,28
Mz 92,36,28
s_waitcnt vmcnt(15) lgkmcnt(0)
s_barrier
Mv 32,160,128
Rt 0,18,32768
Mv 36,164,128
s_add_u32 s38, s38, 0xffff0080
Mv 96,168,128
Rt 2,18,34816
s_add_u32 m0, s36, 0
Mv 100,172,128
buffer_load_dwordx4 v8, s[4:7], s38 offen offset:0 lds
Mv 160,176,128
Rt 4,19,32768
Mv 164,180,128
Mv 224,184,128
Rt 6,19,34816
Mv 228,188,128
buffer_load_dwordx4 v9, s[4:7], s38 offen offset:1024 lds
Mv 236,188,132
Rt 8,20,32768
Mv 232,184,132
Mv 172,180,132
Rt 10,20,34816
Mv 168,176,132
buffer_load_dwordx4 v10, s[4:7], s38 offen offset:2048 lds
Mv 108,172,132
)" R"(
Rt 12,21,32768
Mv 104,168,132
Mv 44,164,132
Rt 14,21,34816
Mv 40,160,132
buffer_load_dwordx4 v11, s[4:7], s38 offen offset:3072 lds
Mv 48,160,136
Rt 16,22,32768
Mv 52,164,136
s_add_u32 s38, s38, 0x10000
Mv 112,168,136
Rt 18,22,34816
s_add_u32 m0, s36, 4096
Mv 116,172,136
buffer_load_dwordx4 v8, s[4:7], s38 offen offset:0 lds
Mv 176,176,136
Rt 20,23,32768
Mv 180,180,136
Mv 240,184,136
Rt 22,23,34816
Mv 244,188,136
buffer_load_dwordx4 v9, s[4:7], s38 offen offset:1024 lds
Mv 252,188,140
Rt 24,24,32768
Mv 248,184,140
Mv 188,180,140
Rt 26,24,34816
Mv 184,176,140
buffer_load_dwordx4 v10, s[4:7], s38 offen offset:2048 lds
Mv 124,172,140
Rt 28,25,32768
Mv 120,168,140
Mv 60,164,140
Rt 30,25,34816
Mv 56,160,140
buffer_load_dwordx4 v11, s[4:7], s38 offen offset:3072 lds
Mv 64,160,144
Rb 32,16,32768
Mv 68,164,144
Mv 128,168,144
Rb 36,16,34816
Mv 132,172,144
Mv 192,176,144
Rb 40,16,40960
Mv 196,180,144
Ma 192,184,144
Rb 44,16,43008
Ma 196,188,144
Ma 204,188,148
Rb 48,16,49152
Ma 200,184,148
buffer_store_dwordx4 a[92:95], v3, s[20:23], s51 offen offset:3072 sc0 sc1
Mv 204,180,148
Rb 52,16,51200
Mv 200,176,148
Mv 140,172,148
Rb 56,16,57344
Mv 136,168,148
Mv 76,164,148
Rb 60,16,59392
Mv 72,160,148
Mv 80,160,152
Mv 84,164,152
s_add_u32 s51, s51, 0x10000
)" R"(
buffer_store_dwordx4 a[96:99], v2, s[20:23], s51 offen offset:0 sc0 sc1
Mv 144,168,152
Mv 148,172,152
Mv 208,176,152
Mv 212,180,152
Ma 208,184,152
Ma 212,188,152
Ma 220,188,156
Ma 216,184,156
buffer_store_dwordx4 a[100:103], v2, s[20:23], s51 offen offset:1024 sc0 sc1
Mv 220,180,156
Mv 216,176,156
Mv 156,172,156
Mv 152,168,156
Mv 92,164,156
Mv 88,160,156
s_waitcnt vmcnt(19) lgkmcnt(0)
s_barrier
Mv 32,32,0
Rt 128,18,49152
Mv 36,36,0
Mv 96,40,0
Rt 130,18,51200
s_mov_b32 s24, s80
Mv 100,44,0
s_add_u32 s39, s39, 0x40000
s_mov_b32 s25, s81
Mv 160,48,0
Rt 132,19,49152
s_add_u32 m0, s37, 16384
Mv 164,52,0
buffer_load_dwordx4 v12, s[8:11], s39 offen offset:0 lds
Mv 224,56,0
Rt 134,19,51200
s_mov_b32 s26, 0x380000
Mv 228,60,0
s_mov_b32 s27, 0x20000
s_bfe_u32 s28, s2, 0x30005
Mv 236,60,4
Rt 136,20,49152
s_lshl3_add_u32 s29, s34, s28
Mv 232,56,4
buffer_store_dwordx4 a[104:107], v2, s[20:23], s51 offen offset:2048 sc0 sc1
Mv 172,52,4
Rt 138,20,51200
s_lshl2_add_u32 s29, s29, s33
Mv 168,48,4
s_mul_i32 s83, s29, 7168
s_mul_i32 s29, s91, 448
Mv 108,44,4
Rt 140,21,49152
s_add_u32 s83, s83, s29
Mv 104,40,4
buffer_load_dwordx4 v13, s[8:11], s39 offen offset:1024 lds
Mv 44,36,4
Rt 142,21,51200
s_lshl_b32 s82, s34, 21
Mv 40,32,4
s_add_u32 s82, s82, 0x6000000
s_mul_i32 s94, s91, 14
Mv 48,32,8
Rt 144,22,49152
s_add_u32 s94, s94, s33
Mv 52,36,8
buffer_store_dwordx4 a[108:111], v2, s[20:23], s51 offen offset:3072 sc0 sc1
)" R"(
Mv 112,40,8
Rt 146,22,51200
s_mov_b32 s92, s28
Mv 116,44,8
v_lshrrev_b32 v4, 5, v0
v_mul_u32_u24 v4, 384, v4
Mv 176,48,8
Rt 148,23,49152
v_add_u32 v28, v4, v1
Mv 180,52,8
buffer_load_dwordx4 v14, s[8:11], s39 offen offset:2048 lds
Mv 240,56,8
Rt 150,23,51200
v_and_b32 v4, 31, v0
Mv 244,60,8
v_cmp_lt_u32_e32 vcc, 27, v4
v_mov_b32 v4, 0x40000000
Mv 252,60,12
Rt 152,24,49152
v_lshrrev_b32 v29, 2, v0
Mv 248,56,12
buffer_store_dwordx4 a[112:115], v3, s[20:23], s51 offen offset:0 sc0 sc1
Mv 188,52,12
Rt 154,24,51200
v_cndmask_b32_e32 v28, v28, v4, vcc
Mv 184,48,12
v_and_b32 v4, 48, v1
v_lshl_or_b32 v29, v29, 9, v4
Mv 124,44,12
Rt 156,25,49152
s_lshr_b32 s28, s33, 1
Mv 120,40,12
buffer_load_dwordx4 v15, s[8:11], s39 offen offset:3072 lds
Mv 60,36,12
Rt 158,25,51200
s_lshl_b32 s28, s28, 6
Mv 56,32,12
s_and_b32 s29, s33, 1
s_lshl3_add_u32 s28, s29, s28
Mv 64,32,16
Rb 160,17,32768
s_add_u32 s28, s28, 0x20000
Mv 68,36,16
buffer_store_dwordx4 a[116:119], v3, s[20:23], s51 offen offset:1024 sc0 sc1
Mv 128,40,16
Rb 164,17,34816
v_add_u32 v29, s28, v29
Mv 132,44,16
s_add_u32 s39, s39, 0x40000
Mv 192,48,16
Rb 168,17,40960
s_add_u32 m0, s37, 32768
Mv 196,52,16
buffer_load_dwordx4 v12, s[8:11], s39 offen offset:0 lds
Ma 192,56,16
Rb 172,17,43008
Ma 196,60,16
Ma 204,60,20
Rb 176,17,49152
Ma 200,56,20
buffer_store_dwordx4 a[120:123], v3, s[20:23], s51 offen offset:2048 sc0 sc1
Mv 204,52,20
Rb 180,17,51200
Mv 200,48,20
)" R"(
Mv 140,44,20
Rb 184,17,57344
Mv 136,40,20
buffer_load_dwordx4 v13, s[8:11], s39 offen offset:1024 lds
Mv 76,36,20
Rb 188,17,59392
Mv 72,32,20
Mv 80,32,24
Mv 84,36,24
buffer_store_dwordx4 a[124:127], v3, s[20:23], s51 offen offset:3072 sc0 sc1
Mv 144,40,24
Mv 148,44,24
Mv 208,48,24
Mv 212,52,24
buffer_load_dwordx4 v14, s[8:11], s39 offen offset:2048 lds
Ma 208,56,24
Ma 212,60,24
Ma 220,60,28
Ma 216,56,28
Mv 220,52,28
Mv 216,48,28
Mv 156,44,28
Mv 152,40,28
buffer_load_dwordx4 v15, s[8:11], s39 offen offset:3072 lds
Mv 92,36,28
Mv 88,32,28
s_waitcnt vmcnt(17) lgkmcnt(0)
s_barrier
Mv 32,160,128
Rt 0,18,0
Mv 36,164,128
s_add_u32 s38, s38, 0xffff0080
Mv 96,168,128
Rt 2,18,2048
s_add_u32 m0, s36, 32768
Mv 100,172,128
buffer_load_dwordx4 v8, s[4:7], s38 offen offset:0 lds
Mv 160,176,128
Rt 4,19,0
Mv 164,180,128
Mv 224,184,128
Rt 6,19,2048
Mv 228,188,128
buffer_load_dwordx4 v9, s[4:7], s38 offen offset:1024 lds
Mv 236,188,132
Rt 8,20,0
s_mov_b32 s40, 5
Mv 232,184,132
Mv 172,180,132
Rt 10,20,2048
Mv 168,176,132
buffer_load_dwordx4 v10, s[4:7], s38 offen offset:2048 lds
Mv 108,172,132
Rt 12,21,0
Mv 104,168,132
Mv 44,164,132
Rt 14,21,2048
Mv 40,160,132
buffer_load_dwordx4 v11, s[4:7], s38 offen offset:3072 lds
Mv 48,160,136
Rt 16,22,0
Mv 52,164,136
s_add_u32 s38, s38, 0x10000
Mv 112,168,136
Rt 18,22,2048
)" R"(
s_add_u32 m0, s36, 36864
Mv 116,172,136
buffer_load_dwordx4 v8, s[4:7], s38 offen offset:0 lds
Mv 176,176,136
Rt 20,23,0
Mv 180,180,136
Mv 240,184,136
Rt 22,23,2048
Mv 244,188,136
buffer_load_dwordx4 v9, s[4:7], s38 offen offset:1024 lds
Mv 252,188,140
Rt 24,24,0
Mv 248,184,140
Mv 188,180,140
Rt 26,24,2048
Mv 184,176,140
buffer_load_dwordx4 v10, s[4:7], s38 offen offset:2048 lds
Mv 124,172,140
Rt 28,25,0
Mv 120,168,140
Mv 60,164,140
Rt 30,25,2048
Mv 56,160,140
buffer_load_dwordx4 v11, s[4:7], s38 offen offset:3072 lds
Mv 64,160,144
Rb 32,16,0
Mv 68,164,144
Mv 128,168,144
Rb 36,16,2048
Mv 132,172,144
Mv 192,176,144
Rb 40,16,8192
s_add_u32 m0, s37, 0x10000
Mv 196,180,144
s_add_u32 s29, s83, 0
Ma 192,184,144
Rb 44,16,10240
Ma 196,188,144
buffer_load_dwordx4 v28, s[24:27], s29 offen offset:0 lds
Ma 204,188,148
Rb 48,16,16384
s_add_u32 s29, s83, 768
Ma 200,184,148
buffer_load_dwordx4 v28, s[24:27], s29 offen offset:1024 lds
Mv 204,180,148
Rb 52,16,18432
s_add_u32 s29, s83, 1536
Mv 200,176,148
buffer_load_dwordx4 v28, s[24:27], s29 offen offset:2048 lds
Mv 140,172,148
Rb 56,16,24576
s_add_u32 s29, s83, 2304
Mv 136,168,148
buffer_load_dwordx4 v28, s[24:27], s29 offen offset:3072 lds
Mv 76,164,148
Rb 60,16,26624
Mv 72,160,148
Mv 80,160,152
Mv 84,164,152
Mv 144,168,152
Mv 148,172,152
Mv 208,176,152
Mv 212,180,152
Ma 208,184,152
)" R"(
Ma 212,188,152
Ma 220,188,156
Ma 216,184,156
Mv 220,180,156
Mv 216,176,156
Mv 156,172,156
Mv 152,168,156
Mv 92,164,156
Mv 88,160,156
s_waitcnt vmcnt(14) lgkmcnt(0)
s_barrier
buffer_store_dword v6, v7, s[20:23], s52 offen offset:0 sc0 sc1
.LzPBV:
Mv 32,32,0
Rt 128,18,16384
Mv 36,36,0
Mv 96,40,0
Rt 130,18,18432
Mv 100,44,0
s_add_u32 s39, s39, 0x40000
Mv 160,48,0
Rt 132,19,16384
s_add_u32 m0, s37, 49152
Mv 164,52,0
buffer_load_dwordx4 v12, s[8:11], s39 offen offset:0 lds
Mv 224,56,0
Rt 134,19,18432
Mv 228,60,0
Mv 236,60,4
Rt 136,20,16384
Mv 232,56,4
Mv 172,52,4
Rt 138,20,18432
Mv 168,48,4
Mv 108,44,4
Rt 140,21,16384
Mv 104,40,4
buffer_load_dwordx4 v13, s[8:11], s39 offen offset:1024 lds
Mv 44,36,4
Rt 142,21,18432
Mv 40,32,4
Mv 48,32,8
Rt 144,22,16384
Mv 52,36,8
Mv 112,40,8
Rt 146,22,18432
Mv 116,44,8
Mv 176,48,8
Rt 148,23,16384
Mv 180,52,8
buffer_load_dwordx4 v14, s[8:11], s39 offen offset:2048 lds
Mv 240,56,8
Rt 150,23,18432
Mv 244,60,8
Mv 252,60,12
Rt 152,24,16384
Mv 248,56,12
Mv 188,52,12
Rt 154,24,18432
Mv 184,48,12
Mv 124,44,12
Rt 156,25,16384
Mv 120,40,12
buffer_load_dwordx4 v15, s[8:11], s39 offen offset:3072 lds
Mv 60,36,12
)" R"(
Rt 158,25,18432
Mv 56,32,12
Mv 64,32,16
Rb 160,17,0
Mv 68,36,16
Mv 128,40,16
Rb 164,17,2048
Mv 132,44,16
s_add_u32 s39, s39, 0x40000
Mv 192,48,16
Rb 168,17,8192
s_add_u32 m0, s37, 0
Mv 196,52,16
buffer_load_dwordx4 v12, s[8:11], s39 offen offset:0 lds
Ma 192,56,16
Rb 172,17,10240
Ma 196,60,16
Ma 204,60,20
Rb 176,17,16384
Ma 200,56,20
Mv 204,52,20
Rb 180,17,18432
Mv 200,48,20
Mv 140,44,20
Rb 184,17,24576
Mv 136,40,20
buffer_load_dwordx4 v13, s[8:11], s39 offen offset:1024 lds
Mv 76,36,20
Rb 188,17,26624
Mv 72,32,20
Mv 80,32,24
Mv 84,36,24
Mv 144,40,24
Mv 148,44,24
Mv 208,48,24
Mv 212,52,24
buffer_load_dwordx4 v14, s[8:11], s39 offen offset:2048 lds
Ma 208,56,24
Ma 212,60,24
Ma 220,60,28
Ma 216,56,28
Mv 220,52,28
Mv 216,48,28
Mv 156,44,28
Mv 152,40,28
buffer_load_dwordx4 v15, s[8:11], s39 offen offset:3072 lds
Mv 92,36,28
Mv 88,32,28
s_waitcnt vmcnt(8) lgkmcnt(0)
s_barrier
Mv 32,160,128
Rt 0,18,32768
Mv 36,164,128
s_add_u32 s38, s38, 0xffff0080
Mv 96,168,128
Rt 2,18,34816
s_add_u32 m0, s36, 0
Mv 100,172,128
buffer_load_dwordx4 v8, s[4:7], s38 offen offset:0 lds
Mv 160,176,128
Rt 4,19,32768
Mv 164,180,128
Mv 224,184,128
Rt 6,19,34816
Mv 228,188,128
)" R"(
buffer_load_dwordx4 v9, s[4:7], s38 offen offset:1024 lds
Mv 236,188,132
Rt 8,20,32768
Mv 232,184,132
Mv 172,180,132
Rt 10,20,34816
Mv 168,176,132
buffer_load_dwordx4 v10, s[4:7], s38 offen offset:2048 lds
Mv 108,172,132
Rt 12,21,32768
Mv 104,168,132
Mv 44,164,132
Rt 14,21,34816
Mv 40,160,132
buffer_load_dwordx4 v11, s[4:7], s38 offen offset:3072 lds
Mv 48,160,136
Rt 16,22,32768
Mv 52,164,136
s_add_u32 s38, s38, 0x10000
Mv 112,168,136
Rt 18,22,34816
s_add_u32 m0, s36, 4096
Mv 116,172,136
buffer_load_dwordx4 v8, s[4:7], s38 offen offset:0 lds
Mv 176,176,136
Rt 20,23,32768
Mv 180,180,136
Mv 240,184,136
Rt 22,23,34816
Mv 244,188,136
buffer_load_dwordx4 v9, s[4:7], s38 offen offset:1024 lds
Mv 252,188,140
Rt 24,24,32768
Mv 248,184,140
Mv 188,180,140
Rt 26,24,34816
Mv 184,176,140
buffer_load_dwordx4 v10, s[4:7], s38 offen offset:2048 lds
Mv 124,172,140
Rt 28,25,32768
Mv 120,168,140
Mv 60,164,140
Rt 30,25,34816
Mv 56,160,140
buffer_load_dwordx4 v11, s[4:7], s38 offen offset:3072 lds
Mv 64,160,144
Rb 32,16,32768
Mv 68,164,144
Mv 128,168,144
Rb 36,16,34816
Mv 132,172,144
Mv 192,176,144
Rb 40,16,40960
Mv 196,180,144
Ma 192,184,144
Rb 44,16,43008
Ma 196,188,144
Ma 204,188,148
Rb 48,16,49152
Ma 200,184,148
Mv 204,180,148
Rb 52,16,51200
Mv 200,176,148
Mv 140,172,148
)" R"(
Rb 56,16,57344
Mv 136,168,148
Mv 76,164,148
Rb 60,16,59392
Mv 72,160,148
Mv 80,160,152
Mv 84,164,152
Mv 144,168,152
Mv 148,172,152
Mv 208,176,152
Mv 212,180,152
Ma 208,184,152
Ma 212,188,152
Ma 220,188,156
Ma 216,184,156
Mv 220,180,156
Mv 216,176,156
Mv 156,172,156
Mv 152,168,156
Mv 92,164,156
Mv 88,160,156
s_waitcnt vmcnt(12) lgkmcnt(0)
s_barrier
Mv 32,32,0
Rt 128,18,49152
Mv 36,36,0
Mv 96,40,0
Rt 130,18,51200
Mv 100,44,0
s_add_u32 s39, s39, 0x40000
Mv 160,48,0
Rt 132,19,49152
s_add_u32 m0, s37, 16384
Mv 164,52,0
buffer_load_dwordx4 v12, s[8:11], s39 offen offset:0 lds
Mv 224,56,0
Rt 134,19,51200
Mv 228,60,0
Mv 236,60,4
Rt 136,20,49152
Mv 232,56,4
Mv 172,52,4
Rt 138,20,51200
Mv 168,48,4
Mv 108,44,4
Rt 140,21,49152
Mv 104,40,4
buffer_load_dwordx4 v13, s[8:11], s39 offen offset:1024 lds
Mv 44,36,4
Rt 142,21,51200
Mv 40,32,4
Mv 48,32,8
Rt 144,22,49152
Mv 52,36,8
Mv 112,40,8
Rt 146,22,51200
Mv 116,44,8
Mv 176,48,8
Rt 148,23,49152
Mv 180,52,8
buffer_load_dwordx4 v14, s[8:11], s39 offen offset:2048 lds
Mv 240,56,8
Rt 150,23,51200
Mv 244,60,8
Mv 252,60,12
)" R"(
Rt 152,24,49152
Mv 248,56,12
Mv 188,52,12
Rt 154,24,51200
Mv 184,48,12
Mv 124,44,12
Rt 156,25,49152
Mv 120,40,12
buffer_load_dwordx4 v15, s[8:11], s39 offen offset:3072 lds
Mv 60,36,12
Rt 158,25,51200
Mv 56,32,12
Mv 64,32,16
Rb 160,17,32768
Mv 68,36,16
Mv 128,40,16
Rb 164,17,34816
Mv 132,44,16
s_add_u32 s39, s39, 0x40000
Mv 192,48,16
Rb 168,17,40960
s_add_u32 m0, s37, 32768
Mv 196,52,16
buffer_load_dwordx4 v12, s[8:11], s39 offen offset:0 lds
Ma 192,56,16
Rb 172,17,43008
Ma 196,60,16
Ma 204,60,20
Rb 176,17,49152
Ma 200,56,20
Mv 204,52,20
Rb 180,17,51200
Mv 200,48,20
Mv 140,44,20
Rb 184,17,57344
Mv 136,40,20
buffer_load_dwordx4 v13, s[8:11], s39 offen offset:1024 lds
Mv 76,36,20
Rb 188,17,59392
Mv 72,32,20
Mv 80,32,24
Mv 84,36,24
Mv 144,40,24
Mv 148,44,24
Mv 208,48,24
Mv 212,52,24
buffer_load_dwordx4 v14, s[8:11], s39 offen offset:2048 lds
Ma 208,56,24
Ma 212,60,24
Ma 220,60,28
Ma 216,56,28
Mv 220,52,28
Mv 216,48,28
Mv 156,44,28
Mv 152,40,28
buffer_load_dwordx4 v15, s[8:11], s39 offen offset:3072 lds
Mv 92,36,28
Mv 88,32,28
s_waitcnt vmcnt(8) lgkmcnt(0)
s_barrier
Mv 32,160,128
Rt 0,18,0
Mv 36,164,128
s_add_u32 s38, s38, 0xffff0080
Mv 96,168,128
)" R"(
Rt 2,18,2048
s_add_u32 m0, s36, 32768
Mv 100,172,128
buffer_load_dwordx4 v8, s[4:7], s38 offen offset:0 lds
Mv 160,176,128
Rt 4,19,0
Mv 164,180,128
Mv 224,184,128
Rt 6,19,2048
Mv 228,188,128
buffer_load_dwordx4 v9, s[4:7], s38 offen offset:1024 lds
Mv 236,188,132
Rt 8,20,0
Mv 232,184,132
Mv 172,180,132
Rt 10,20,2048
Mv 168,176,132
buffer_load_dwordx4 v10, s[4:7], s38 offen offset:2048 lds
Mv 108,172,132
Rt 12,21,0
Mv 104,168,132
Mv 44,164,132
Rt 14,21,2048
Mv 40,160,132
buffer_load_dwordx4 v11, s[4:7], s38 offen offset:3072 lds
Mv 48,160,136
Rt 16,22,0
Mv 52,164,136
s_add_u32 s38, s38, 0x10000
Mv 112,168,136
Rt 18,22,2048
s_add_u32 m0, s36, 36864
Mv 116,172,136
buffer_load_dwordx4 v8, s[4:7], s38 offen offset:0 lds
Mv 176,176,136
Rt 20,23,0
Mv 180,180,136
Mv 240,184,136
Rt 22,23,2048
Mv 244,188,136
buffer_load_dwordx4 v9, s[4:7], s38 offen offset:1024 lds
Mv 252,188,140
Rt 24,24,0
Mv 248,184,140
Mv 188,180,140
Rt 26,24,2048
Mv 184,176,140
buffer_load_dwordx4 v10, s[4:7], s38 offen offset:2048 lds
Mv 124,172,140
Rt 28,25,0
Mv 120,168,140
Mv 60,164,140
Rt 30,25,2048
Mv 56,160,140
buffer_load_dwordx4 v11, s[4:7], s38 offen offset:3072 lds
Mv 64,160,144
Rb 32,16,0
Mv 68,164,144
Mv 128,168,144
Rb 36,16,2048
Mv 132,172,144
Mv 192,176,144
Rb 40,16,8192
Mv 196,180,144
)" R"(
Ma 192,184,144
Rb 44,16,10240
Ma 196,188,144
Ma 204,188,148
Rb 48,16,16384
Ma 200,184,148
Mv 204,180,148
Rb 52,16,18432
Mv 200,176,148
Mv 140,172,148
Rb 56,16,24576
Mv 136,168,148
Mv 76,164,148
Rb 60,16,26624
Mv 72,160,148
Mv 80,160,152
Mv 84,164,152
Mv 144,168,152
Mv 148,172,152
Mv 208,176,152
Mv 212,180,152
Ma 208,184,152
Ma 212,188,152
Ma 220,188,156
Ma 216,184,156
Mv 220,180,156
s_sub_u32 s40, s40, 1
Mv 216,176,156
Mv 156,172,156
Mv 152,168,156
Mv 92,164,156
Mv 88,160,156
s_cmp_lg_u32 s40, 0
s_waitcnt vmcnt(12) lgkmcnt(0)
s_barrier
s_cbranch_scc1 .LzPBV
s_branch .LzPBVX
.LzWIN:
.LzPAKX:
Mv 32,0,32
Rt 64,18,16384
Mv 64,0,36
Rb 192,28,32768
Mv 96,0,40
Rt 66,18,18432
Mv 128,0,44
s_add_u32 s39, s39, 0x40000
Rb 196,28,34816
Mv 160,0,48
Rt 68,19,16384
s_add_u32 m0, s37, 49152
Mv 192,0,52
buffer_load_dwordx4 v12, s[8:11], s39 offen offset:0 lds
Mv 224,0,56
Rt 70,19,18432
Ma 224,0,60
Rb 200,29,32768
Ma 228,4,60
Rt 72,20,16384
Mv 228,4,56
Rb 204,29,34816
Mv 196,4,52
Rt 74,20,18432
Mv 164,4,48
Rb 208,28,36864
)" R"(
Mv 132,4,44
Rt 76,21,16384
Mv 100,4,40
buffer_load_dwordx4 v13, s[8:11], s39 offen offset:1024 lds
Mv 68,4,36
Rt 78,21,18432
Mv 36,4,32
Rb 212,28,38912
Mv 40,8,32
Rt 80,22,16384
Mv 72,8,36
Rb 216,29,36864
Mv 104,8,40
Rt 82,22,18432
Mv 136,8,44
Rb 220,29,38912
Mv 168,8,48
Rt 84,23,16384
Mv 200,8,52
buffer_load_dwordx4 v14, s[8:11], s39 offen offset:2048 lds
Mv 232,8,56
Rt 86,23,18432
Ma 232,8,60
Ma 236,12,60
Rt 88,24,16384
Mv 236,12,56
Mv 204,12,52
Rt 90,24,18432
Mv 172,12,48
Mv 140,12,44
Rt 92,25,16384
Mv 108,12,40
buffer_load_dwordx4 v15, s[8:11], s39 offen offset:3072 lds
Mv 76,12,36
Rt 94,25,18432
Mv 44,12,32
Mv 48,16,32
Rb 96,17,0
Mv 80,16,36
Mv 112,16,40
Rb 100,17,2048
Mv 144,16,44
s_add_u32 s39, s39, 0x40000
Mv 176,16,48
Rb 104,17,8192
s_add_u32 m0, s37, 0
Mv 208,16,52
buffer_load_dwordx4 v12, s[8:11], s39 offen offset:0 lds
Mv 240,16,56
Rb 108,17,10240
Ma 240,16,60
Ma 244,20,60
Rb 112,17,16384
Mv 244,20,56
Mv 212,20,52
Rb 116,17,18432
Mv 180,20,48
Mv 148,20,44
Rb 120,17,24576
Mv 116,20,40
buffer_load_dwordx4 v13, s[8:11], s39 offen offset:1024 lds
Mv 84,20,36
Rb 124,17,26624
Mv 52,20,32
)" R"(
Mv 56,24,32
Mv 88,24,36
Mv 120,24,40
Mv 152,24,44
Mv 184,24,48
Mv 216,24,52
buffer_load_dwordx4 v14, s[8:11], s39 offen offset:2048 lds
Mv 248,24,56
Ma 248,24,60
Ma 252,28,60
Mv 252,28,56
Mv 220,28,52
Mv 188,28,48
Mv 156,28,44
Mv 124,28,40
buffer_load_dwordx4 v15, s[8:11], s39 offen offset:3072 lds
Mv 92,28,36
Mv 60,28,32
s_waitcnt vmcnt(8) lgkmcnt(0)
s_barrier
s_add_u32 m0, s36, 131072
s_add_u32 s44, s43, 256
buffer_load_dwordx4 v26, s[12:15], s44 offen offset:0 lds
buffer_load_dwordx4 v27, s[12:15], s44 offen offset:1024 lds
s_add_u32 s44, s43, 12544
buffer_load_dwordx4 v26, s[12:15], s44 offen offset:2048 lds
buffer_load_dwordx4 v27, s[12:15], s44 offen offset:3072 lds
Mv 32,64,96
Rt 0,18,32768
Mv 64,64,100
s_add_u32 s38, s38, 0xffff0080
Mv 96,64,104
Rt 2,18,34816
s_add_u32 m0, s36, 0
Mv 128,64,108
buffer_load_dwordx4 v8, s[4:7], s38 offen offset:0 lds
Mv 160,64,112
Rt 4,19,32768
Mv 192,64,116
Mv 224,64,120
Rt 6,19,34816
Ma 224,64,124
buffer_load_dwordx4 v9, s[4:7], s38 offen offset:1024 lds
Ma 228,68,124
Rt 8,20,32768
Mv 228,68,120
Mv 196,68,116
Rt 10,20,34816
Mv 164,68,112
buffer_load_dwordx4 v10, s[4:7], s38 offen offset:2048 lds
Mv 132,68,108
Rt 12,21,32768
Mv 100,68,104
Mv 68,68,100
Rt 14,21,34816
Mv 36,68,96
buffer_load_dwordx4 v11, s[4:7], s38 offen offset:3072 lds
Mv 40,72,96
Rt 16,22,32768
Mv 72,72,100
s_add_u32 s38, s38, 0x10000
Mv 104,72,104
Rt 18,22,34816
s_add_u32 m0, s36, 4096
Mv 136,72,108
)" R"(
buffer_load_dwordx4 v8, s[4:7], s38 offen offset:0 lds
Mv 168,72,112
Rt 20,23,32768
Mv 200,72,116
Mv 232,72,120
Rt 22,23,34816
Ma 232,72,124
buffer_load_dwordx4 v9, s[4:7], s38 offen offset:1024 lds
Ma 236,76,124
Rt 24,24,32768
Mv 236,76,120
Mv 204,76,116
Rt 26,24,34816
Mv 172,76,112
buffer_load_dwordx4 v10, s[4:7], s38 offen offset:2048 lds
Mv 140,76,108
Rt 28,25,32768
Mv 108,76,104
Mv 76,76,100
Rt 30,25,34816
Mv 44,76,96
buffer_load_dwordx4 v11, s[4:7], s38 offen offset:3072 lds
Mv 48,80,96
Rb 32,16,32768
Mv 80,80,100
Mv 112,80,104
Rb 36,16,34816
Mv 144,80,108
s_add_u32 s44, s43, 384
Mv 176,80,112
Rb 40,16,40960
s_add_u32 m0, s36, 135168
Mv 208,80,116
buffer_load_dwordx4 v26, s[12:15], s44 offen offset:0 lds
Mv 240,80,120
Rb 44,16,43008
Ma 240,80,124
Ma 244,84,124
Rb 48,16,49152
Mv 244,84,120
buffer_load_dwordx4 v27, s[12:15], s44 offen offset:1024 lds
Mv 212,84,116
Rb 52,16,51200
Mv 180,84,112
Mv 148,84,108
Rb 56,16,57344
Mv 116,84,104
s_add_u32 s44, s43, 12672
buffer_load_dwordx4 v26, s[12:15], s44 offen offset:2048 lds
Mv 84,84,100
Rb 60,16,59392
Mv 52,84,96
Mv 56,88,96
Mv 88,88,100
buffer_load_dwordx4 v27, s[12:15], s44 offen offset:3072 lds
Mv 120,88,104
Mv 152,88,108
Mv 184,88,112
Mv 216,88,116
Mv 248,88,120
Ma 248,88,124
Ma 252,92,124
Mv 252,92,120
Mv 220,92,116
)" R"(
Mv 188,92,112
Mv 156,92,108
Mv 124,92,104
Mv 92,92,100
Mv 60,92,96
s_waitcnt vmcnt(0) lgkmcnt(0)
s_barrier
Mv 32,0,32
Rt 64,18,49152
Mv 64,0,36
Rb 128,28,32768
Mv 96,0,40
Rt 66,18,51200
Mv 128,0,44
s_add_u32 s39, s39, 0x40000
Rb 132,28,34816
Mv 160,0,48
Rt 68,19,49152
s_add_u32 m0, s37, 16384
Mv 192,0,52
buffer_load_dwordx4 v12, s[8:11], s39 offen offset:0 lds
Mv 224,0,56
Rt 70,19,51200
Ma 224,0,60
Rb 136,29,32768
Ma 228,4,60
Rt 72,20,49152
Mv 228,4,56
Rb 140,29,34816
Mv 196,4,52
Rt 74,20,51200
Mv 164,4,48
Rb 144,28,36864
Mv 132,4,44
Rt 76,21,49152
Mv 100,4,40
buffer_load_dwordx4 v13, s[8:11], s39 offen offset:1024 lds
Mv 68,4,36
Rt 78,21,51200
Mv 36,4,32
Rb 148,28,38912
Mv 40,8,32
Rt 80,22,49152
Mv 72,8,36
Rb 152,29,36864
Mv 104,8,40
Rt 82,22,51200
Mv 136,8,44
Rb 156,29,38912
Mv 168,8,48
Rt 84,23,49152
Mv 200,8,52
buffer_load_dwordx4 v14, s[8:11], s39 offen offset:2048 lds
Mv 232,8,56
Rt 86,23,51200
Ma 232,8,60
Ma 236,12,60
Rt 88,24,49152
Mv 236,12,56
Mv 204,12,52
Rt 90,24,51200
Mv 172,12,48
Mv 140,12,44
Rt 92,25,49152
Mv 108,12,40
)" R"(
buffer_load_dwordx4 v15, s[8:11], s39 offen offset:3072 lds
Mv 76,12,36
Rt 94,25,51200
Mv 44,12,32
Mv 48,16,32
Rb 96,17,32768
Mv 80,16,36
Mv 112,16,40
Rb 100,17,34816
Mv 144,16,44
s_add_u32 s39, s39, 0x40000
Mv 176,16,48
Rb 104,17,40960
s_add_u32 m0, s37, 32768
Mv 208,16,52
buffer_load_dwordx4 v12, s[8:11], s39 offen offset:0 lds
Mv 240,16,56
Rb 108,17,43008
Ma 240,16,60
Ma 244,20,60
Rb 112,17,49152
Mv 244,20,56
Mv 212,20,52
Rb 116,17,51200
Mv 180,20,48
Mv 148,20,44
Rb 120,17,57344
Mv 116,20,40
buffer_load_dwordx4 v13, s[8:11], s39 offen offset:1024 lds
Mv 84,20,36
Rb 124,17,59392
Mv 52,20,32
Mv 56,24,32
Mv 88,24,36
Mv 120,24,40
Mv 152,24,44
Mv 184,24,48
Mv 216,24,52
buffer_load_dwordx4 v14, s[8:11], s39 offen offset:2048 lds
Mv 248,24,56
Ma 248,24,60
Ma 252,28,60
Mv 252,28,56
Mv 220,28,52
Mv 188,28,48
Mv 156,28,44
Mv 124,28,40
buffer_load_dwordx4 v15, s[8:11], s39 offen offset:3072 lds
Mv 92,28,36
Mv 60,28,32
s_waitcnt lgkmcnt(0)
s_barrier
s_add_u32 m0, s36, 131072
s_add_u32 s44, s43, 512
buffer_load_dwordx4 v26, s[12:15], s44 offen offset:0 lds
buffer_load_dwordx4 v27, s[12:15], s44 offen offset:1024 lds
s_add_u32 s44, s43, 12800
buffer_load_dwordx4 v26, s[12:15], s44 offen offset:2048 lds
buffer_load_dwordx4 v27, s[12:15], s44 offen offset:3072 lds
Mv 32,64,96
Rt 0,18,0
Mv 64,64,100
s_add_u32 s38, s38, 0xffff0080
Mv 96,64,104
Rt 2,18,2048
)" R"(
s_add_u32 m0, s36, 32768
Mv 128,64,108
buffer_load_dwordx4 v8, s[4:7], s38 offen offset:0 lds
Mv 160,64,112
Rt 4,19,0
Mv 192,64,116
Mv 224,64,120
Rt 6,19,2048
Ma 224,64,124
buffer_load_dwordx4 v9, s[4:7], s38 offen offset:1024 lds
Ma 228,68,124
Rt 8,20,0
Mv 228,68,120
Mv 196,68,116
Rt 10,20,2048
Mv 164,68,112
buffer_load_dwordx4 v10, s[4:7], s38 offen offset:2048 lds
Mv 132,68,108
Rt 12,21,0
Mv 100,68,104
Mv 68,68,100
Rt 14,21,2048
Mv 36,68,96
buffer_load_dwordx4 v11, s[4:7], s38 offen offset:3072 lds
Mv 40,72,96
Rt 16,22,0
Mv 72,72,100
s_add_u32 s38, s38, 0x10000
Mv 104,72,104
Rt 18,22,2048
s_add_u32 m0, s36, 36864
Mv 136,72,108
buffer_load_dwordx4 v8, s[4:7], s38 offen offset:0 lds
Mv 168,72,112
Rt 20,23,0
Mv 200,72,116
Mv 232,72,120
Rt 22,23,2048
Ma 232,72,124
buffer_load_dwordx4 v9, s[4:7], s38 offen offset:1024 lds
Ma 236,76,124
Rt 24,24,0
Mv 236,76,120
Mv 204,76,116
Rt 26,24,2048
Mv 172,76,112
buffer_load_dwordx4 v10, s[4:7], s38 offen offset:2048 lds
Mv 140,76,108
Rt 28,25,0
Mv 108,76,104
Mv 76,76,100
Rt 30,25,2048
Mv 44,76,96
buffer_load_dwordx4 v11, s[4:7], s38 offen offset:3072 lds
Mv 48,80,96
Rb 32,16,0
Mv 80,80,100
Mv 112,80,104
Rb 36,16,2048
Mv 144,80,108
s_add_u32 s44, s43, 640
Mv 176,80,112
Rb 40,16,8192
s_add_u32 m0, s36, 135168
)" R"(
Mv 208,80,116
buffer_load_dwordx4 v26, s[12:15], s44 offen offset:0 lds
Mv 240,80,120
Rb 44,16,10240
Ma 240,80,124
Ma 244,84,124
Rb 48,16,16384
Mv 244,84,120
buffer_load_dwordx4 v27, s[12:15], s44 offen offset:1024 lds
Mv 212,84,116
Rb 52,16,18432
Mv 180,84,112
Mv 148,84,108
Rb 56,16,24576
Mv 116,84,104
s_add_u32 s44, s43, 12928
buffer_load_dwordx4 v26, s[12:15], s44 offen offset:2048 lds
Mv 84,84,100
Rb 60,16,26624
Mv 52,84,96
Mv 56,88,96
Mv 88,88,100
buffer_load_dwordx4 v27, s[12:15], s44 offen offset:3072 lds
Mv 120,88,104
Mv 152,88,108
Mv 184,88,112
Mv 216,88,116
Mv 248,88,120
Ma 248,88,124
Ma 252,92,124
Mv 252,92,120
Mv 220,92,116
Mv 188,92,112
Mv 156,92,108
Mv 124,92,104
Mv 92,92,100
Mv 60,92,96
s_waitcnt vmcnt(0) lgkmcnt(0)
s_barrier
Mv 32,0,32
Rt 64,18,16384
Mv 64,0,36
Rb 160,28,32768
v_lshrrev_b32 v5, 2, v0
Mv 96,0,40
Rt 66,18,18432
v_and_b32 v5, 8, v5
Mv 128,0,44
s_add_u32 s39, s39, 0x40000
Rb 164,28,34816
Mv 160,0,48
Rt 68,19,16384
s_add_u32 m0, s37, 49152
Mv 192,0,52
buffer_load_dwordx4 v12, s[8:11], s39 offen offset:0 lds
Mv 224,0,56
Rt 70,19,18432
v_and_or_b32 v5, v0, 7, v5
Ma 224,0,60
Rb 168,29,32768
v_lshlrev_b32 v5, 8, v5
Ma 228,4,60
Rt 72,20,16384
v_and_or_b32 v5, v0, 24, v5
Mv 228,4,56
)" R"(
Rb 172,29,34816
v_lshrrev_b32 v5, 1, v5
Mv 196,4,52
Rt 74,20,18432
v_mov_b32 v6, s3
Mv 164,4,48
Rb 176,28,36864
Mv 132,4,44
Rt 76,21,16384
Mv 100,4,40
buffer_load_dwordx4 v13, s[8:11], s39 offen offset:1024 lds
Mv 68,4,36
Rt 78,21,18432
Mv 36,4,32
Rb 180,28,38912
Mv 40,8,32
Rt 80,22,16384
Mv 72,8,36
Rb 184,29,36864
Mv 104,8,40
Rt 82,22,18432
Mv 136,8,44
Rb 188,29,38912
Mv 168,8,48
Rt 84,23,16384
Mv 200,8,52
buffer_load_dwordx4 v14, s[8:11], s39 offen offset:2048 lds
Mv 232,8,56
Rt 86,23,18432
Ma 232,8,60
Ma 236,12,60
Rt 88,24,16384
Mv 236,12,56
Mv 204,12,52
Rt 90,24,18432
Mv 172,12,48
Mv 140,12,44
Rt 92,25,16384
Mv 108,12,40
buffer_load_dwordx4 v15, s[8:11], s39 offen offset:3072 lds
Mv 76,12,36
Rt 94,25,18432
v_lshrrev_b32 v10, 3, v0
Mv 44,12,32
v_and_b32 v11, 0x70, v1
v_lshl_or_b32 v10, v10, 13, v11
Mv 48,16,32
Rb 96,17,0
v_lshlrev_b32 v11, 1, v0
Mv 80,16,36
v_and_b32 v11, 32, v11
v_xor_b32 v10, v10, v11
Mv 112,16,40
Rb 100,17,2048
s_and_b32 s28, s33, 1
Mv 144,16,44
s_lshl_b32 s28, s28, 6
v_xor_b32 v8, s28, v10
Mv 176,16,48
Rb 104,17,8192
v_xor_b32 v9, 64, v8
Mv 208,16,52
v_add_u32 v9, 0xfc00, v9
v_add_u32 v10, 0x1f800, v8
)" R"(
Mv 240,16,56
Rb 108,17,10240
v_add_u32 v11, 0x1f800, v9
Ma 240,16,60
Ma 244,20,60
Rb 112,17,16384
Mv 244,20,56
Mv 212,20,52
Rb 116,17,18432
Mv 180,20,48
Mv 148,20,44
Rb 120,17,24576
Mv 116,20,40
Mv 84,20,36
Rb 124,17,26624
Mv 52,20,32
Mv 56,24,32
Mv 88,24,36
Mv 120,24,40
Mv 152,24,44
Mv 184,24,48
Mv 216,24,52
Mv 248,24,56
Ma 248,24,60
Ma 252,28,60
Mv 252,28,56
Mv 220,28,52
Mv 188,28,48
Mv 156,28,44
Mv 124,28,40
Mv 92,28,36
Mv 60,28,32
s_waitcnt lgkmcnt(0)
s_barrier
Mv 32,64,96
Rt 0,18,32768
Mv 64,64,100
s_add_u32 s92, s42, 0x0
Mv 96,64,104
Rt 2,18,34816
s_add_u32 m0, s41, 131072
Mv 128,64,108
buffer_load_dwordx4 v8, s[16:19], s92 offen offset:0 lds
Mv 160,64,112
Rt 4,19,32768
Mv 192,64,116
Mv 224,64,120
Rt 6,19,34816
Ma 224,64,124
buffer_load_dwordx4 v9, s[16:19], s92 offen offset:1024 lds
Ma 228,68,124
Rt 8,20,32768
Mv 228,68,120
Mv 196,68,116
Rt 10,20,34816
Mv 164,68,112
buffer_load_dwordx4 v10, s[16:19], s92 offen offset:2048 lds
Mv 132,68,108
Rt 12,21,32768
Mv 100,68,104
Mv 68,68,100
Rt 14,21,34816
Mv 36,68,96
buffer_load_dwordx4 v11, s[16:19], s92 offen offset:3072 lds
Mv 40,72,96
)" R"(
Rt 16,22,32768
Mv 72,72,100
s_add_u32 s92, s42, 0x40000
Mv 104,72,104
Rt 18,22,34816
s_add_u32 m0, s41, 135168
Mv 136,72,108
buffer_load_dwordx4 v8, s[16:19], s92 offen offset:0 lds
Mv 168,72,112
Rt 20,23,32768
Mv 200,72,116
Mv 232,72,120
Rt 22,23,34816
Ma 232,72,124
buffer_load_dwordx4 v9, s[16:19], s92 offen offset:1024 lds
Ma 236,76,124
Rt 24,24,32768
Mv 236,76,120
Mv 204,76,116
Rt 26,24,34816
Mv 172,76,112
buffer_load_dwordx4 v10, s[16:19], s92 offen offset:2048 lds
Mv 140,76,108
Rt 28,25,32768
Mv 108,76,104
s_add_u32 s92, s42, 0x1c0000
Mv 76,76,100
Rt 30,25,34816
s_add_u32 m0, s41, 0
Mv 44,76,96
buffer_load_dwordx4 v8, s[16:19], s92 offen offset:0 lds
Mv 48,80,96
Rb 32,16,32768
Mv 80,80,100
Mv 112,80,104
Rb 36,16,34816
Mv 144,80,108
buffer_load_dwordx4 v9, s[16:19], s92 offen offset:1024 lds
Mv 176,80,112
Rb 40,16,40960
Mv 208,80,116
Mv 240,80,120
Rb 44,16,43008
Ma 240,80,124
buffer_load_dwordx4 v10, s[16:19], s92 offen offset:2048 lds
Ma 244,84,124
Rb 48,16,49152
Mv 244,84,120
Mv 212,84,116
Rb 52,16,51200
Mv 180,84,112
buffer_load_dwordx4 v11, s[16:19], s92 offen offset:3072 lds
Mv 148,84,108
Rb 56,16,57344
Mv 116,84,104
s_add_u32 s92, s42, 0x200000
Mv 84,84,100
Rb 60,16,59392
s_add_u32 m0, s41, 4096
Mv 52,84,96
buffer_load_dwordx4 v8, s[16:19], s92 offen offset:0 lds
Mv 56,88,96
Mv 88,88,100
Mv 120,88,104
)" R"(
Mv 152,88,108
buffer_load_dwordx4 v9, s[16:19], s92 offen offset:1024 lds
Mv 184,88,112
Mv 216,88,116
Mv 248,88,120
Ma 248,88,124
buffer_load_dwordx4 v10, s[16:19], s92 offen offset:2048 lds
Ma 252,92,124
Mv 252,92,120
Mv 220,92,116
Mv 188,92,112
Mv 156,92,108
Mv 124,92,104
Mv 92,92,100
Mv 60,92,96
s_waitcnt vmcnt(14) lgkmcnt(4)
s_barrier
Mv 32,0,32
Rt 64,18,49152
Mv 64,0,36
Rt 66,18,51200
s_add_u32 s92, s42, 0x80
Mv 96,0,40
Rt 68,19,49152
s_add_u32 m0, s41, 65536
Mv 128,0,44
Rt 70,19,51200
buffer_load_dwordx4 v8, s[16:19], s92 offen offset:0 lds
Mv 132,4,44
Rt 72,20,49152
Mv 100,4,40
Rt 74,20,51200
Mv 68,4,36
Rt 76,21,49152
Mv 36,4,32
Rt 78,21,51200
buffer_load_dwordx4 v9, s[16:19], s92 offen offset:1024 lds
Mv 40,8,32
Rt 80,22,49152
Mv 72,8,36
Rt 82,22,51200
Mv 104,8,40
Rt 84,23,49152
Mv 136,8,44
Rt 86,23,51200
buffer_load_dwordx4 v10, s[16:19], s92 offen offset:2048 lds
Mv 140,12,44
Rt 88,24,49152
Mv 108,12,40
Rt 90,24,51200
Mv 76,12,36
Rt 92,25,49152
Mv 44,12,32
Rt 94,25,51200
buffer_load_dwordx4 v11, s[16:19], s92 offen offset:3072 lds
Mv 48,16,32
Rb 96,17,32768
Mv 80,16,36
Rb 100,17,34816
s_add_u32 s92, s42, 0x40080
Mv 112,16,40
Rb 104,17,40960
s_add_u32 m0, s41, 69632
Mv 144,16,44
Rb 108,17,43008
)" R"(
buffer_load_dwordx4 v8, s[16:19], s92 offen offset:0 lds
Mv 148,20,44
Rb 112,17,49152
Mv 116,20,40
Rb 116,17,51200
Mv 84,20,36
Rb 120,17,57344
Mv 52,20,32
Rb 124,17,59392
buffer_load_dwordx4 v9, s[16:19], s92 offen offset:1024 lds
Mv 56,24,32
Mv 88,24,36
Mv 120,24,40
Mv 152,24,44
buffer_load_dwordx4 v10, s[16:19], s92 offen offset:2048 lds
Mv 156,28,44
Mv 124,28,40
Mv 92,28,36
Mv 60,28,32
s_waitcnt lgkmcnt(0)
s_barrier
Mv 32,64,96
v_bfe_u32 v12, v0, 3, 2
v_lshlrev_b32 v12, 5, v12
Mv 36,68,96
v_and_b32 v4, 3, v0
v_lshl_or_b32 v12, v4, 3, v12
Mv 40,72,96
s_add_u32 m0, s36, 98304
s_add_u32 s44, s43, 768
Mv 44,76,96
buffer_load_dwordx4 v26, s[12:15], s44 offen offset:0 lds
Mv 48,80,96
v_bfe_u32 v4, v0, 2, 2
v_lshl_or_b32 v12, v4, 7, v12
Mv 52,84,96
v_lshrrev_b32 v4, 4, v0
v_lshl_or_b32 v12, v4, 10, v12
Mv 56,88,96
v_xor_b32 v13, 32, v12
v_xor_b32 v14, 64, v12
Mv 60,92,96
buffer_load_dwordx4 v27, s[12:15], s44 offen offset:1024 lds
Mv 64,64,100
v_xor_b32 v15, 96, v12
v_add_u32 v20, 0x20000, v12
Mv 68,68,100
v_add_u32 v21, 0x20000, v13
v_add_u32 v22, 0x20000, v14
Mv 72,72,100
v_add_u32 v23, 0x20000, v15
v_add_u32 v16, 0x10000, v12
Mv 76,76,100
s_add_u32 s44, s43, 13056
buffer_load_dwordx4 v26, s[12:15], s44 offen offset:2048 lds
Mv 80,80,100
v_add_u32 v17, 0x10000, v13
v_add_u32 v18, 0x10000, v14
Mv 84,84,100
v_add_u32 v19, 0x10000, v15
Cv 32,32,33
Mv 88,88,100
Cv 33,34,35
Cv 34,36,37
Mv 92,92,100
)" R"(
buffer_load_dwordx4 v27, s[12:15], s44 offen offset:3072 lds
Mv 96,64,104
Cv 35,38,39
Cv 40,40,41
Mv 100,68,104
Cv 41,42,43
Cv 42,44,45
Mv 104,72,104
s_add_u32 m0, s41, 32768
s_add_u32 s92, s42, 0x1c0080
Mv 108,76,104
buffer_load_dwordx4 v8, s[16:19], s92 offen offset:0 lds
Mv 112,80,104
Cv 43,46,47
Cv 48,48,49
Mv 116,84,104
Cv 49,50,51
Cv 50,52,53
Mv 120,88,104
Cv 51,54,55
Cv 56,56,57
Mv 124,92,104
buffer_store_dwordx4 v[32:35], v2, s[20:23], s45 offen offset:0 sc0 sc1
Mv 128,64,108
Cv 57,58,59
Cv 58,60,61
Mv 132,68,108
Cv 59,62,63
Cv 64,64,65
Mv 136,72,108
Cv 65,66,67
Cv 66,68,69
Mv 140,76,108
buffer_load_dwordx4 v9, s[16:19], s92 offen offset:1024 lds
Mv 144,80,108
Cv 67,70,71
Cv 72,72,73
Mv 148,84,108
Cv 73,74,75
Cv 74,76,77
Mv 152,88,108
Cv 75,78,79
Cv 80,80,81
Mv 156,92,108
buffer_store_dwordx4 v[40:43], v2, s[20:23], s45 offen offset:1024 sc0 sc1
Mv 160,0,48
Cv 81,82,83
Cv 82,84,85
Mv 160,64,112
Cv 83,86,87
Cv 88,88,89
Mv 164,4,48
Cv 89,90,91
Cv 90,92,93
Mv 164,68,112
buffer_load_dwordx4 v10, s[16:19], s92 offen offset:2048 lds
Mv 168,8,48
Cv 91,94,95
Cv 96,96,97
Mv 168,72,112
Cv 97,98,99
Cv 98,100,101
Mv 172,12,48
Cv 99,102,103
)" R"(
Cv 104,104,105
Mv 172,76,112
buffer_store_dwordx4 v[48:51], v2, s[20:23], s45 offen offset:2048 sc0 sc1
Mv 176,16,48
Cv 105,106,107
Cv 106,108,109
Mv 176,80,112
Cv 107,110,111
Cv 112,112,113
Mv 180,20,48
Cv 113,114,115
Cv 114,116,117
Mv 180,84,112
buffer_load_dwordx4 v11, s[16:19], s92 offen offset:3072 lds
Mv 184,24,48
Cv 115,118,119
Cv 120,120,121
Mv 184,88,112
Cv 121,122,123
Cv 122,124,125
Mv 188,28,48
Cv 123,126,127
Cv 128,128,129
Mv 188,92,112
buffer_store_dwordx4 v[56:59], v2, s[20:23], s45 offen offset:3072 sc0 sc1
Mv 192,0,52
Cv 129,130,131
Cv 130,132,133
Mv 192,64,116
Cv 131,134,135
Cv 136,136,137
Mv 196,4,52
s_add_u32 m0, s41, 36864
s_add_u32 s92, s42, 0x200080
Mv 196,68,116
buffer_load_dwordx4 v8, s[16:19], s92 offen offset:0 lds
Mv 200,8,52
Cv 137,138,139
Cv 138,140,141
Mv 200,72,116
Cv 139,142,143
Cv 144,144,145
Mv 204,12,52
Cv 145,146,147
Cv 146,148,149
Mv 204,76,116
buffer_store_dwordx4 v[64:67], v3, s[20:23], s45 offen offset:0 sc0 sc1
Mv 208,16,52
Cv 147,150,151
Cv 152,152,153
Mv 208,80,116
Cv 153,154,155
Cv 154,156,157
Mv 212,20,52
Cv 155,158,159
Mv 212,84,116
buffer_load_dwordx4 v9, s[16:19], s92 offen offset:1024 lds
Mv 216,24,52
Mv 216,88,116
Mv 220,28,52
Mv 220,92,116
buffer_store_dwordx4 v[72:75], v3, s[20:23], s45 offen offset:1024 sc0 sc1
Mv 224,0,56
Ma 224,0,60
)" R"(
Ma 228,4,60
Mv 228,4,56
buffer_load_dwordx4 v10, s[16:19], s92 offen offset:2048 lds
Mv 232,8,56
Ma 232,8,60
Ma 236,12,60
Mv 236,12,56
s_waitcnt vmcnt(24)
s_barrier
Mv 240,16,56
buffer_store_dwordx4 v[80:83], v3, s[20:23], s45 offen offset:2048 sc0 sc1
Ma 240,16,60
Ma 244,20,60
Mv 244,20,56
Mv 248,24,56
Ma 248,24,60
Ma 252,28,60
Mv 252,28,56
buffer_store_dwordx4 v[88:91], v3, s[20:23], s45 offen offset:3072 sc0 sc1
Mv 224,64,120
Rt 0,20,0
Mv 228,68,120
Rt 2,20,512
Mv 232,72,120
Rt 4,21,0
Mv 236,76,120
Rt 6,21,512
Mv 240,80,120
Rt 8,22,0
Mv 244,84,120
Rt 10,22,512
Mv 248,88,120
Rt 12,23,0
Mv 252,92,120
Rt 14,23,512
Ma 224,64,124
Rt 16,20,4096
Ma 228,68,124
Rt 18,20,4608
Ma 232,72,124
Rt 20,21,4096
Ma 236,76,124
Rt 22,21,4608
Ma 240,80,124
Rt 24,22,4096
Ma 244,84,124
Rt 26,22,4608
Ma 248,88,124
Rt 28,23,4096
Ma 252,92,124
Rt 30,23,4608
s_waitcnt vmcnt(15) lgkmcnt(8)
s_branch .LzQA
.LzPAVX:
Mv 32,32,0
Rt 64,18,16384
Mv 36,36,0
Rb 192,28,32768
Mv 96,40,0
Rt 66,18,18432
Mv 100,44,0
s_add_u32 s39, s39, 0x40000
Rb 196,28,34816
Mv 160,48,0
Rt 68,19,16384
)" R"(
s_add_u32 m0, s37, 49152
Mv 164,52,0
buffer_load_dwordx4 v12, s[8:11], s39 offen offset:0 lds
Mv 224,56,0
Rt 70,19,18432
Mv 228,60,0
Rb 200,29,32768
Mv 236,60,4
Rt 72,20,16384
Mv 232,56,4
Rb 204,29,34816
Mv 172,52,4
Rt 74,20,18432
Mv 168,48,4
Rb 208,28,36864
Mv 108,44,4
Rt 76,21,16384
Mv 104,40,4
buffer_load_dwordx4 v13, s[8:11], s39 offen offset:1024 lds
Mv 44,36,4
Rt 78,21,18432
Mv 40,32,4
Rb 212,28,38912
Mv 48,32,8
Rt 80,22,16384
Mv 52,36,8
Rb 216,29,36864
Mv 112,40,8
Rt 82,22,18432
Mv 116,44,8
Rb 220,29,38912
Mv 176,48,8
Rt 84,23,16384
Mv 180,52,8
buffer_load_dwordx4 v14, s[8:11], s39 offen offset:2048 lds
Mv 240,56,8
Rt 86,23,18432
Mv 244,60,8
Mv 252,60,12
Rt 88,24,16384
Mv 248,56,12
Mv 188,52,12
Rt 90,24,18432
Mv 184,48,12
Mv 124,44,12
Rt 92,25,16384
Mv 120,40,12
buffer_load_dwordx4 v15, s[8:11], s39 offen offset:3072 lds
Mv 60,36,12
Rt 94,25,18432
Mv 56,32,12
Mv 64,32,16
Rb 96,17,0
Mv 68,36,16
Mv 128,40,16
Rb 100,17,2048
Mv 132,44,16
s_add_u32 s39, s39, 0x40000
Mv 192,48,16
Rb 104,17,8192
s_add_u32 m0, s37, 0
Mv 196,52,16
buffer_load_dwordx4 v12, s[8:11], s39 offen offset:0 lds
Ma 224,56,16
)" R"(
Rb 108,17,10240
Ma 228,60,16
Ma 236,60,20
Rb 112,17,16384
Ma 232,56,20
Mv 204,52,20
Rb 116,17,18432
Mv 200,48,20
Mv 140,44,20
Rb 120,17,24576
Mv 136,40,20
buffer_load_dwordx4 v13, s[8:11], s39 offen offset:1024 lds
Mv 76,36,20
Rb 124,17,26624
Mv 72,32,20
Mv 80,32,24
Mv 84,36,24
Mv 144,40,24
Mv 148,44,24
Mv 208,48,24
Mv 212,52,24
buffer_load_dwordx4 v14, s[8:11], s39 offen offset:2048 lds
Ma 240,56,24
Ma 244,60,24
Ma 252,60,28
Ma 248,56,28
Mv 220,52,28
Mv 216,48,28
Mv 156,44,28
Mv 152,40,28
buffer_load_dwordx4 v15, s[8:11], s39 offen offset:3072 lds
Mv 92,36,28
Mv 88,32,28
s_waitcnt vmcnt(8) lgkmcnt(0)
s_barrier
s_add_u32 m0, s36, 131072
s_add_u32 s44, s43, 256
buffer_load_dwordx4 v26, s[12:15], s44 offen offset:0 lds
buffer_load_dwordx4 v27, s[12:15], s44 offen offset:1024 lds
s_add_u32 s44, s43, 12544
buffer_load_dwordx4 v26, s[12:15], s44 offen offset:2048 lds
buffer_load_dwordx4 v27, s[12:15], s44 offen offset:3072 lds
Mv 32,96,64
Rt 0,18,32768
Mv 36,100,64
s_add_u32 s38, s38, 0xffff0080
Mv 96,104,64
Rt 2,18,34816
s_add_u32 m0, s36, 0
Mv 100,108,64
buffer_load_dwordx4 v8, s[4:7], s38 offen offset:0 lds
Mv 160,112,64
Rt 4,19,32768
Mv 164,116,64
Mv 224,120,64
Rt 6,19,34816
Mv 228,124,64
buffer_load_dwordx4 v9, s[4:7], s38 offen offset:1024 lds
Mv 236,124,68
Rt 8,20,32768
Mv 232,120,68
Mv 172,116,68
Rt 10,20,34816
Mv 168,112,68
buffer_load_dwordx4 v10, s[4:7], s38 offen offset:2048 lds
)" R"(
Mv 108,108,68
Rt 12,21,32768
Mv 104,104,68
Mv 44,100,68
Rt 14,21,34816
Mv 40,96,68
buffer_load_dwordx4 v11, s[4:7], s38 offen offset:3072 lds
Mv 48,96,72
Rt 16,22,32768
Mv 52,100,72
s_add_u32 s38, s38, 0x10000
Mv 112,104,72
Rt 18,22,34816
s_add_u32 m0, s36, 4096
Mv 116,108,72
buffer_load_dwordx4 v8, s[4:7], s38 offen offset:0 lds
Mv 176,112,72
Rt 20,23,32768
Mv 180,116,72
Mv 240,120,72
Rt 22,23,34816
Mv 244,124,72
buffer_load_dwordx4 v9, s[4:7], s38 offen offset:1024 lds
Mv 252,124,76
Rt 24,24,32768
Mv 248,120,76
Mv 188,116,76
Rt 26,24,34816
Mv 184,112,76
buffer_load_dwordx4 v10, s[4:7], s38 offen offset:2048 lds
Mv 124,108,76
Rt 28,25,32768
Mv 120,104,76
Mv 60,100,76
Rt 30,25,34816
Mv 56,96,76
buffer_load_dwordx4 v11, s[4:7], s38 offen offset:3072 lds
Mv 64,96,80
Rb 32,16,32768
Mv 68,100,80
Mv 128,104,80
Rb 36,16,34816
Mv 132,108,80
s_add_u32 s44, s43, 384
Mv 192,112,80
Rb 40,16,40960
s_add_u32 m0, s36, 135168
Mv 196,116,80
buffer_load_dwordx4 v26, s[12:15], s44 offen offset:0 lds
Ma 224,120,80
Rb 44,16,43008
Ma 228,124,80
Ma 236,124,84
Rb 48,16,49152
Ma 232,120,84
buffer_load_dwordx4 v27, s[12:15], s44 offen offset:1024 lds
Mv 204,116,84
Rb 52,16,51200
Mv 200,112,84
Mv 140,108,84
Rb 56,16,57344
Mv 136,104,84
s_add_u32 s44, s43, 12672
buffer_load_dwordx4 v26, s[12:15], s44 offen offset:2048 lds
)" R"(
Mv 76,100,84
Rb 60,16,59392
Mv 72,96,84
Mv 80,96,88
Mv 84,100,88
buffer_load_dwordx4 v27, s[12:15], s44 offen offset:3072 lds
Mv 144,104,88
Mv 148,108,88
Mv 208,112,88
Mv 212,116,88
Ma 240,120,88
Ma 244,124,88
Ma 252,124,92
Ma 248,120,92
Mv 220,116,92
Mv 216,112,92
Mv 156,108,92
Mv 152,104,92
Mv 92,100,92
Mv 88,96,92
s_waitcnt vmcnt(0) lgkmcnt(0)
s_barrier
Mv 32,32,0
Rt 64,18,49152
Mv 36,36,0
Rb 128,28,32768
Mv 96,40,0
Rt 66,18,51200
Mv 100,44,0
s_add_u32 s39, s39, 0x40000
Rb 132,28,34816
Mv 160,48,0
Rt 68,19,49152
s_add_u32 m0, s37, 16384
Mv 164,52,0
buffer_load_dwordx4 v12, s[8:11], s39 offen offset:0 lds
Mv 224,56,0
Rt 70,19,51200
Mv 228,60,0
Rb 136,29,32768
Mv 236,60,4
Rt 72,20,49152
Mv 232,56,4
Rb 140,29,34816
Mv 172,52,4
Rt 74,20,51200
Mv 168,48,4
Rb 144,28,36864
Mv 108,44,4
Rt 76,21,49152
Mv 104,40,4
buffer_load_dwordx4 v13, s[8:11], s39 offen offset:1024 lds
Mv 44,36,4
Rt 78,21,51200
Mv 40,32,4
Rb 148,28,38912
Mv 48,32,8
Rt 80,22,49152
Mv 52,36,8
Rb 152,29,36864
Mv 112,40,8
Rt 82,22,51200
Mv 116,44,8
Rb 156,29,38912
Mv 176,48,8
)" R"(
Rt 84,23,49152
Mv 180,52,8
buffer_load_dwordx4 v14, s[8:11], s39 offen offset:2048 lds
Mv 240,56,8
Rt 86,23,51200
Mv 244,60,8
Mv 252,60,12
Rt 88,24,49152
Mv 248,56,12
Mv 188,52,12
Rt 90,24,51200
Mv 184,48,12
Mv 124,44,12
Rt 92,25,49152
Mv 120,40,12
buffer_load_dwordx4 v15, s[8:11], s39 offen offset:3072 lds
Mv 60,36,12
Rt 94,25,51200
Mv 56,32,12
Mv 64,32,16
Rb 96,17,32768
Mv 68,36,16
Mv 128,40,16
Rb 100,17,34816
Mv 132,44,16
s_add_u32 s39, s39, 0x40000
Mv 192,48,16
Rb 104,17,40960
s_add_u32 m0, s37, 32768
Mv 196,52,16
buffer_load_dwordx4 v12, s[8:11], s39 offen offset:0 lds
Ma 224,56,16
Rb 108,17,43008
Ma 228,60,16
Ma 236,60,20
Rb 112,17,49152
Ma 232,56,20
Mv 204,52,20
Rb 116,17,51200
Mv 200,48,20
Mv 140,44,20
Rb 120,17,57344
Mv 136,40,20
buffer_load_dwordx4 v13, s[8:11], s39 offen offset:1024 lds
Mv 76,36,20
Rb 124,17,59392
Mv 72,32,20
Mv 80,32,24
Mv 84,36,24
Mv 144,40,24
Mv 148,44,24
Mv 208,48,24
Mv 212,52,24
buffer_load_dwordx4 v14, s[8:11], s39 offen offset:2048 lds
Ma 240,56,24
Ma 244,60,24
Ma 252,60,28
Ma 248,56,28
Mv 220,52,28
Mv 216,48,28
Mv 156,44,28
Mv 152,40,28
buffer_load_dwordx4 v15, s[8:11], s39 offen offset:3072 lds
Mv 92,36,28
)" R"(
Mv 88,32,28
s_waitcnt lgkmcnt(0)
s_barrier
s_add_u32 m0, s36, 131072
s_add_u32 s44, s43, 512
buffer_load_dwordx4 v26, s[12:15], s44 offen offset:0 lds
buffer_load_dwordx4 v27, s[12:15], s44 offen offset:1024 lds
s_add_u32 s44, s43, 12800
buffer_load_dwordx4 v26, s[12:15], s44 offen offset:2048 lds
buffer_load_dwordx4 v27, s[12:15], s44 offen offset:3072 lds
Mv 32,96,64
Rt 0,18,0
Mv 36,100,64
s_add_u32 s38, s38, 0xffff0080
Mv 96,104,64
Rt 2,18,2048
s_add_u32 m0, s36, 32768
Mv 100,108,64
buffer_load_dwordx4 v8, s[4:7], s38 offen offset:0 lds
Mv 160,112,64
Rt 4,19,0
Mv 164,116,64
Mv 224,120,64
Rt 6,19,2048
Mv 228,124,64
buffer_load_dwordx4 v9, s[4:7], s38 offen offset:1024 lds
Mv 236,124,68
Rt 8,20,0
Mv 232,120,68
Mv 172,116,68
Rt 10,20,2048
Mv 168,112,68
buffer_load_dwordx4 v10, s[4:7], s38 offen offset:2048 lds
Mv 108,108,68
Rt 12,21,0
Mv 104,104,68
Mv 44,100,68
Rt 14,21,2048
Mv 40,96,68
buffer_load_dwordx4 v11, s[4:7], s38 offen offset:3072 lds
Mv 48,96,72
Rt 16,22,0
Mv 52,100,72
s_add_u32 s38, s38, 0x10000
Mv 112,104,72
Rt 18,22,2048
s_add_u32 m0, s36, 36864
Mv 116,108,72
buffer_load_dwordx4 v8, s[4:7], s38 offen offset:0 lds
Mv 176,112,72
Rt 20,23,0
Mv 180,116,72
Mv 240,120,72
Rt 22,23,2048
Mv 244,124,72
buffer_load_dwordx4 v9, s[4:7], s38 offen offset:1024 lds
Mv 252,124,76
Rt 24,24,0
Mv 248,120,76
Mv 188,116,76
Rt 26,24,2048
Mv 184,112,76
buffer_load_dwordx4 v10, s[4:7], s38 offen offset:2048 lds
Mv 124,108,76
Rt 28,25,0
)" R"(
Mv 120,104,76
Mv 60,100,76
Rt 30,25,2048
Mv 56,96,76
buffer_load_dwordx4 v11, s[4:7], s38 offen offset:3072 lds
Mv 64,96,80
Rb 32,16,0
Mv 68,100,80
Mv 128,104,80
Rb 36,16,2048
Mv 132,108,80
s_add_u32 s44, s43, 640
Mv 192,112,80
Rb 40,16,8192
s_add_u32 m0, s36, 135168
Mv 196,116,80
buffer_load_dwordx4 v26, s[12:15], s44 offen offset:0 lds
Ma 224,120,80
Rb 44,16,10240
Ma 228,124,80
Ma 236,124,84
Rb 48,16,16384
Ma 232,120,84
buffer_load_dwordx4 v27, s[12:15], s44 offen offset:1024 lds
Mv 204,116,84
Rb 52,16,18432
Mv 200,112,84
Mv 140,108,84
Rb 56,16,24576
Mv 136,104,84
s_add_u32 s44, s43, 12928
buffer_load_dwordx4 v26, s[12:15], s44 offen offset:2048 lds
Mv 76,100,84
Rb 60,16,26624
Mv 72,96,84
Mv 80,96,88
Mv 84,100,88
buffer_load_dwordx4 v27, s[12:15], s44 offen offset:3072 lds
Mv 144,104,88
Mv 148,108,88
Mv 208,112,88
Mv 212,116,88
Ma 240,120,88
Ma 244,124,88
Ma 252,124,92
Ma 248,120,92
Mv 220,116,92
Mv 216,112,92
Mv 156,108,92
Mv 152,104,92
Mv 92,100,92
Mv 88,96,92
s_waitcnt vmcnt(0) lgkmcnt(0)
s_barrier
Mv 32,32,0
Rt 64,18,16384
Mv 36,36,0
Rb 160,28,32768
v_lshrrev_b32 v5, 2, v0
Mv 96,40,0
Rt 66,18,18432
v_and_b32 v5, 8, v5
Mv 100,44,0
s_add_u32 s39, s39, 0x40000
Rb 164,28,34816
)" R"(
Mv 160,48,0
Rt 68,19,16384
s_add_u32 m0, s37, 49152
Mv 164,52,0
buffer_load_dwordx4 v12, s[8:11], s39 offen offset:0 lds
Mv 224,56,0
Rt 70,19,18432
v_and_or_b32 v5, v0, 7, v5
Mv 228,60,0
Rb 168,29,32768
v_lshlrev_b32 v5, 8, v5
Mv 236,60,4
Rt 72,20,16384
v_and_or_b32 v5, v0, 24, v5
Mv 232,56,4
Rb 172,29,34816
v_lshrrev_b32 v5, 1, v5
Mv 172,52,4
Rt 74,20,18432
v_mov_b32 v6, s3
Mv 168,48,4
Rb 176,28,36864
Mv 108,44,4
Rt 76,21,16384
Mv 104,40,4
buffer_load_dwordx4 v13, s[8:11], s39 offen offset:1024 lds
Mv 44,36,4
Rt 78,21,18432
Mv 40,32,4
Rb 180,28,38912
Mv 48,32,8
Rt 80,22,16384
Mv 52,36,8
Rb 184,29,36864
Mv 112,40,8
Rt 82,22,18432
Mv 116,44,8
Rb 188,29,38912
Mv 176,48,8
Rt 84,23,16384
Mv 180,52,8
buffer_load_dwordx4 v14, s[8:11], s39 offen offset:2048 lds
Mv 240,56,8
Rt 86,23,18432
Mv 244,60,8
Mv 252,60,12
Rt 88,24,16384
Mv 248,56,12
Mv 188,52,12
Rt 90,24,18432
Mv 184,48,12
Mv 124,44,12
Rt 92,25,16384
Mv 120,40,12
buffer_load_dwordx4 v15, s[8:11], s39 offen offset:3072 lds
Mv 60,36,12
Rt 94,25,18432
v_lshrrev_b32 v10, 3, v0
Mv 56,32,12
v_and_b32 v11, 0x70, v1
v_lshl_or_b32 v10, v10, 13, v11
Mv 64,32,16
Rb 96,17,0
v_lshlrev_b32 v11, 1, v0
)" R"(
Mv 68,36,16
v_and_b32 v11, 32, v11
v_xor_b32 v10, v10, v11
Mv 128,40,16
Rb 100,17,2048
s_and_b32 s28, s33, 1
Mv 132,44,16
s_lshl_b32 s28, s28, 6
v_xor_b32 v8, s28, v10
Mv 192,48,16
Rb 104,17,8192
v_xor_b32 v9, 64, v8
Mv 196,52,16
v_add_u32 v9, 0xfc00, v9
v_add_u32 v10, 0x1f800, v8
Ma 224,56,16
Rb 108,17,10240
v_add_u32 v11, 0x1f800, v9
Ma 228,60,16
Ma 236,60,20
Rb 112,17,16384
Ma 232,56,20
Mv 204,52,20
Rb 116,17,18432
Mv 200,48,20
Mv 140,44,20
Rb 120,17,24576
Mv 136,40,20
Mv 76,36,20
Rb 124,17,26624
Mv 72,32,20
Mv 80,32,24
Mv 84,36,24
Mv 144,40,24
Mv 148,44,24
Mv 208,48,24
Mv 212,52,24
Ma 240,56,24
Ma 244,60,24
Ma 252,60,28
Ma 248,56,28
Mv 220,52,28
Mv 216,48,28
Mv 156,44,28
Mv 152,40,28
Mv 92,36,28
Mv 88,32,28
s_waitcnt lgkmcnt(0)
s_barrier
Mv 32,96,64
Rt 0,18,32768
Mv 36,100,64
s_add_u32 s92, s42, 0x0
Mv 96,104,64
Rt 2,18,34816
s_add_u32 m0, s41, 131072
Mv 100,108,64
buffer_load_dwordx4 v8, s[16:19], s92 offen offset:0 lds
Mv 160,112,64
Rt 4,19,32768
Mv 164,116,64
Mv 224,120,64
Rt 6,19,34816
Mv 228,124,64
buffer_load_dwordx4 v9, s[16:19], s92 offen offset:1024 lds
)" R"(
Mv 236,124,68
Rt 8,20,32768
Mv 232,120,68
Mv 172,116,68
Rt 10,20,34816
Mv 168,112,68
buffer_load_dwordx4 v10, s[16:19], s92 offen offset:2048 lds
Mv 108,108,68
Rt 12,21,32768
Mv 104,104,68
Mv 44,100,68
Rt 14,21,34816
Mv 40,96,68
buffer_load_dwordx4 v11, s[16:19], s92 offen offset:3072 lds
Mv 48,96,72
Rt 16,22,32768
Mv 52,100,72
s_add_u32 s92, s42, 0x40000
Mv 112,104,72
Rt 18,22,34816
s_add_u32 m0, s41, 135168
Mv 116,108,72
buffer_load_dwordx4 v8, s[16:19], s92 offen offset:0 lds
Mv 176,112,72
Rt 20,23,32768
Mv 180,116,72
Mv 240,120,72
Rt 22,23,34816
Mv 244,124,72
buffer_load_dwordx4 v9, s[16:19], s92 offen offset:1024 lds
Mv 252,124,76
Rt 24,24,32768
Mv 248,120,76
Mv 188,116,76
Rt 26,24,34816
Mv 184,112,76
buffer_load_dwordx4 v10, s[16:19], s92 offen offset:2048 lds
Mv 124,108,76
Rt 28,25,32768
Mv 120,104,76
s_add_u32 s92, s42, 0x1c0000
Mv 60,100,76
Rt 30,25,34816
s_add_u32 m0, s41, 0
Mv 56,96,76
buffer_load_dwordx4 v8, s[16:19], s92 offen offset:0 lds
Mv 64,96,80
Rb 32,16,32768
Mv 68,100,80
Mv 128,104,80
Rb 36,16,34816
Mv 132,108,80
buffer_load_dwordx4 v9, s[16:19], s92 offen offset:1024 lds
Mv 192,112,80
Rb 40,16,40960
Mv 196,116,80
Ma 224,120,80
Rb 44,16,43008
Ma 228,124,80
buffer_load_dwordx4 v10, s[16:19], s92 offen offset:2048 lds
Ma 236,124,84
Rb 48,16,49152
Ma 232,120,84
Mv 204,116,84
)" R"(
Rb 52,16,51200
Mv 200,112,84
buffer_load_dwordx4 v11, s[16:19], s92 offen offset:3072 lds
Mv 140,108,84
Rb 56,16,57344
Mv 136,104,84
s_add_u32 s92, s42, 0x200000
Mv 76,100,84
Rb 60,16,59392
s_add_u32 m0, s41, 4096
Mv 72,96,84
buffer_load_dwordx4 v8, s[16:19], s92 offen offset:0 lds
Mv 80,96,88
Mv 84,100,88
Mv 144,104,88
Mv 148,108,88
buffer_load_dwordx4 v9, s[16:19], s92 offen offset:1024 lds
Mv 208,112,88
Mv 212,116,88
Ma 240,120,88
Ma 244,124,88
buffer_load_dwordx4 v10, s[16:19], s92 offen offset:2048 lds
Ma 252,124,92
Ma 248,120,92
Mv 220,116,92
Mv 216,112,92
Mv 156,108,92
Mv 152,104,92
Mv 92,100,92
Mv 88,96,92
s_waitcnt vmcnt(14) lgkmcnt(4)
s_barrier
Mv 32,32,0
Rt 64,18,49152
Mv 36,36,0
Rt 66,18,51200
s_add_u32 s92, s42, 0x80
Mv 96,40,0
Rt 68,19,49152
s_add_u32 m0, s41, 65536
Mv 100,44,0
Rt 70,19,51200
buffer_load_dwordx4 v8, s[16:19], s92 offen offset:0 lds
Mv 108,44,4
Rt 72,20,49152
Mv 104,40,4
Rt 74,20,51200
Mv 44,36,4
Rt 76,21,49152
Mv 40,32,4
Rt 78,21,51200
buffer_load_dwordx4 v9, s[16:19], s92 offen offset:1024 lds
Mv 48,32,8
Rt 80,22,49152
Mv 52,36,8
Rt 82,22,51200
Mv 112,40,8
Rt 84,23,49152
Mv 116,44,8
Rt 86,23,51200
buffer_load_dwordx4 v10, s[16:19], s92 offen offset:2048 lds
Mv 124,44,12
Rt 88,24,49152
Mv 120,40,12
Rt 90,24,51200
)" R"(
Mv 60,36,12
Rt 92,25,49152
Mv 56,32,12
Rt 94,25,51200
buffer_load_dwordx4 v11, s[16:19], s92 offen offset:3072 lds
Mv 64,32,16
Rb 96,17,32768
Mv 68,36,16
Rb 100,17,34816
s_add_u32 s92, s42, 0x40080
Mv 128,40,16
Rb 104,17,40960
s_add_u32 m0, s41, 69632
Mv 132,44,16
Rb 108,17,43008
buffer_load_dwordx4 v8, s[16:19], s92 offen offset:0 lds
Mv 140,44,20
Rb 112,17,49152
Mv 136,40,20
Rb 116,17,51200
Mv 76,36,20
Rb 120,17,57344
Mv 72,32,20
Rb 124,17,59392
buffer_load_dwordx4 v9, s[16:19], s92 offen offset:1024 lds
Mv 80,32,24
Mv 84,36,24
Mv 144,40,24
Mv 148,44,24
buffer_load_dwordx4 v10, s[16:19], s92 offen offset:2048 lds
Mv 156,44,28
Mv 152,40,28
Mv 92,36,28
Mv 88,32,28
s_waitcnt lgkmcnt(0)
s_barrier
Mv 32,96,64
v_bfe_u32 v12, v0, 3, 2
v_lshlrev_b32 v12, 5, v12
Mv 36,100,64
v_and_b32 v4, 3, v0
v_lshl_or_b32 v12, v4, 3, v12
Mv 40,96,68
s_add_u32 m0, s36, 98304
s_add_u32 s44, s43, 768
Mv 44,100,68
buffer_load_dwordx4 v26, s[12:15], s44 offen offset:0 lds
Mv 48,96,72
v_bfe_u32 v4, v0, 2, 2
v_lshl_or_b32 v12, v4, 7, v12
Mv 52,100,72
v_lshrrev_b32 v4, 4, v0
v_lshl_or_b32 v12, v4, 10, v12
Mv 56,96,76
v_xor_b32 v13, 32, v12
v_xor_b32 v14, 64, v12
Mv 60,100,76
buffer_load_dwordx4 v27, s[12:15], s44 offen offset:1024 lds
Mv 64,96,80
v_xor_b32 v15, 96, v12
v_add_u32 v20, 0x20000, v12
Mv 68,100,80
v_add_u32 v21, 0x20000, v13
v_add_u32 v22, 0x20000, v14
Mv 72,96,84
)" R"(
v_add_u32 v23, 0x20000, v15
v_add_u32 v16, 0x10000, v12
Mv 76,100,84
s_add_u32 s44, s43, 13056
buffer_load_dwordx4 v26, s[12:15], s44 offen offset:2048 lds
Mv 80,96,88
v_add_u32 v17, 0x10000, v13
v_add_u32 v18, 0x10000, v14
Mv 84,100,88
v_add_u32 v19, 0x10000, v15
Cv 32,32,33
Mv 88,96,92
Cv 33,34,35
Cv 34,36,37
Mv 92,100,92
buffer_load_dwordx4 v27, s[12:15], s44 offen offset:3072 lds
Mv 96,104,64
Cv 35,38,39
Cv 40,40,41
Mv 100,108,64
Cv 41,42,43
Cv 42,44,45
Mv 104,104,68
s_add_u32 m0, s41, 32768
s_add_u32 s92, s42, 0x1c0080
Mv 108,108,68
buffer_load_dwordx4 v8, s[16:19], s92 offen offset:0 lds
Mv 112,104,72
Cv 43,46,47
Cv 48,48,49
Mv 116,108,72
Cv 49,50,51
Cv 50,52,53
Mv 120,104,76
Cv 51,54,55
Cv 56,56,57
Mv 124,108,76
buffer_store_dwordx4 v[32:35], v2, s[20:23], s45 offen offset:0 sc0 sc1
Mv 128,104,80
Cv 57,58,59
Cv 58,60,61
Mv 132,108,80
Cv 59,62,63
Cv 64,64,65
Mv 136,104,84
Cv 65,66,67
Cv 66,68,69
Mv 140,108,84
buffer_load_dwordx4 v9, s[16:19], s92 offen offset:1024 lds
Mv 144,104,88
Cv 67,70,71
Cv 72,72,73
Mv 148,108,88
Cv 73,74,75
Cv 74,76,77
Mv 152,104,92
Cv 75,78,79
Cv 80,80,81
Mv 156,108,92
buffer_store_dwordx4 v[40:43], v2, s[20:23], s45 offen offset:1024 sc0 sc1
Mv 160,48,0
Cv 81,82,83
Cv 82,84,85
Mv 160,112,64
)" R"(
Cv 83,86,87
Cv 88,88,89
Mv 164,52,0
Cv 89,90,91
Cv 90,92,93
Mv 164,116,64
buffer_load_dwordx4 v10, s[16:19], s92 offen offset:2048 lds
Mv 168,48,4
Cv 91,94,95
Cv 96,96,97
Mv 168,112,68
Cv 97,98,99
Cv 98,100,101
Mv 172,52,4
Cv 99,102,103
Cv 104,104,105
Mv 172,116,68
buffer_store_dwordx4 v[48:51], v2, s[20:23], s45 offen offset:2048 sc0 sc1
Mv 176,48,8
Cv 105,106,107
Cv 106,108,109
Mv 176,112,72
Cv 107,110,111
Cv 112,112,113
Mv 180,52,8
Cv 113,114,115
Cv 114,116,117
Mv 180,116,72
buffer_load_dwordx4 v11, s[16:19], s92 offen offset:3072 lds
Mv 184,48,12
Cv 115,118,119
Cv 120,120,121
Mv 184,112,76
Cv 121,122,123
Cv 122,124,125
Mv 188,52,12
Cv 123,126,127
Cv 128,128,129
Mv 188,116,76
buffer_store_dwordx4 v[56:59], v2, s[20:23], s45 offen offset:3072 sc0 sc1
Mv 192,48,16
Cv 129,130,131
Cv 130,132,133
Mv 192,112,80
Cv 131,134,135
Cv 136,136,137
Mv 196,52,16
s_add_u32 m0, s41, 36864
s_add_u32 s92, s42, 0x200080
Mv 196,116,80
buffer_load_dwordx4 v8, s[16:19], s92 offen offset:0 lds
Mv 200,48,20
Cv 137,138,139
Cv 138,140,141
Mv 200,112,84
Cv 139,142,143
Cv 144,144,145
Mv 204,52,20
Cv 145,146,147
Cv 146,148,149
Mv 204,116,84
buffer_store_dwordx4 v[64:67], v3, s[20:23], s45 offen offset:0 sc0 sc1
Mv 208,48,24
Cv 147,150,151
)" R"(
Cv 152,152,153
Mv 208,112,88
Cv 153,154,155
Cv 154,156,157
Mv 212,52,24
Cv 155,158,159
Mv 212,116,88
buffer_load_dwordx4 v9, s[16:19], s92 offen offset:1024 lds
Mv 216,48,28
Mv 216,112,92
Mv 220,52,28
Mv 220,116,92
buffer_store_dwordx4 v[72:75], v3, s[20:23], s45 offen offset:1024 sc0 sc1
Mv 224,56,0
Mv 228,60,0
Mv 236,60,4
Mv 232,56,4
buffer_load_dwordx4 v10, s[16:19], s92 offen offset:2048 lds
Mv 240,56,8
Mv 244,60,8
Mv 252,60,12
Mv 248,56,12
s_waitcnt vmcnt(24)
s_barrier
Ma 224,56,16
buffer_store_dwordx4 v[80:83], v3, s[20:23], s45 offen offset:2048 sc0 sc1
Ma 228,60,16
Ma 236,60,20
Ma 232,56,20
Ma 240,56,24
Ma 244,60,24
Ma 252,60,28
Ma 248,56,28
buffer_store_dwordx4 v[88:91], v3, s[20:23], s45 offen offset:3072 sc0 sc1
Mv 224,120,64
Rt 0,20,0
Mv 228,124,64
Rt 2,20,512
Mv 232,120,68
Rt 4,21,0
Mv 236,124,68
Rt 6,21,512
Mv 240,120,72
Rt 8,22,0
Mv 244,124,72
Rt 10,22,512
Mv 248,120,76
Rt 12,23,0
Mv 252,124,76
Rt 14,23,512
Ma 224,120,80
Rt 16,20,4096
Ma 228,124,80
Rt 18,20,4608
Ma 232,120,84
Rt 20,21,4096
Ma 236,124,84
Rt 22,21,4608
Ma 240,120,88
Rt 24,22,4096
Ma 244,124,88
Rt 26,22,4608
Ma 248,120,92
Rt 28,23,4096
Ma 252,124,92
)" R"(
Rt 30,23,4608
s_waitcnt vmcnt(15) lgkmcnt(8)
.LzQA:
s_cmp_eq_u32 s2, 112
s_cbranch_scc1 .LzT2S
.LzT2E:
Mz 32,0,192
Rt 32,20,8192
Cv 224,224,225
Mz 40,0,196
Rt 34,20,8704
Rb 48,28,0
Mz 44,4,196
Rt 36,21,8192
buffer_store_dwordx4 v[96:99], v2, s[20:23], s46 offen offset:0 sc0 sc1
Mz 36,4,192
Rt 38,21,8704
Rb 52,28,2048
Cv 225,226,227
Mz 48,8,192
Rt 40,22,8192
Cv 226,228,229
Mz 56,8,196
Rt 42,22,8704
Rb 56,29,0
Mz 60,12,196
Rt 44,23,8192
Cv 227,230,231
Cv 232,232,233
Mz 52,12,192
Rt 46,23,8704
Rb 60,29,2048
s_waitcnt lgkmcnt(12)
Mv 32,16,200
Rt 0,20,12288
Cv 233,234,235
Mv 40,16,204
Rt 2,20,12800
Cv 234,236,237
Cv 235,238,239
Mv 44,20,204
Rt 4,21,12288
Cv 240,240,241
Mv 36,20,200
Rt 6,21,12800
Cv 241,242,243
Mv 48,24,200
Rt 8,22,12288
buffer_store_dwordx4 v[104:107], v2, s[20:23], s46 offen offset:1024 sc0 sc1
Mv 56,24,204
Rt 10,22,12800
Cv 242,244,245
Mv 60,28,204
Rt 12,23,12288
Cv 243,246,247
Mv 52,28,200
Rt 14,23,12800
Cv 248,248,249
s_waitcnt lgkmcnt(9)
Mv 32,32,208
Rt 16,20,16384
Cv 249,250,251
Mv 40,32,212
Rt 18,20,16896
)" R"(
Cv 250,252,253
Mv 44,36,212
Rt 20,21,16384
Cv 251,254,255
Mv 36,36,208
Rt 22,21,16896
v_accvgpr_read_b32 v228, a224
v_accvgpr_read_b32 v24, a225
Mv 48,40,208
Rt 24,22,16384
Cv 228,228,24
Mv 56,40,212
Rt 26,22,16896
v_accvgpr_read_b32 v229, a226
Mv 60,44,212
Rt 28,23,16384
buffer_store_dwordx4 v[112:115], v2, s[20:23], s46 offen offset:2048 sc0 sc1
Mv 52,44,208
Rt 30,23,16896
s_waitcnt lgkmcnt(8)
Mv 32,0,216
Rt 32,20,20480
v_accvgpr_read_b32 v25, a227
Mv 40,0,220
Rt 34,20,20992
Cv 229,229,25
v_accvgpr_read_b32 v230, a228
Mv 44,4,220
Rt 36,21,20480
v_accvgpr_read_b32 v24, a229
Mv 36,4,216
Rt 38,21,20992
Cv 230,230,24
Mv 48,8,216
Rt 40,22,20480
v_accvgpr_read_b32 v231, a230
v_accvgpr_read_b32 v25, a231
Mv 56,8,220
Rt 42,22,20992
Cv 231,231,25
Mv 60,12,220
Rt 44,23,20480
v_accvgpr_read_b32 v236, a232
Mv 52,12,216
Rt 46,23,20992
v_accvgpr_read_b32 v24, a233
s_waitcnt lgkmcnt(8)
Mv 32,16,128
Rt 0,20,24576
buffer_store_dwordx4 v[120:123], v2, s[20:23], s46 offen offset:3072 sc0 sc1
Mv 40,16,132
Rt 2,20,25088
Cv 236,236,24
Mv 44,20,132
Rt 4,21,24576
v_accvgpr_read_b32 v237, a234
Mv 36,20,128
Rt 6,21,25088
v_accvgpr_read_b32 v25, a235
Cv 237,237,25
Mv 48,24,128
Rt 8,22,24576
v_accvgpr_read_b32 v238, a236
Mv 56,24,132
)" R"(
Rt 10,22,25088
v_accvgpr_read_b32 v24, a237
Mv 60,28,132
Rt 12,23,24576
Cv 238,238,24
v_accvgpr_read_b32 v239, a238
Mv 52,28,128
Rt 14,23,25088
s_waitcnt lgkmcnt(8)
Mv 32,32,136
Rt 16,12,0
v_accvgpr_read_b32 v25, a239
Mv 40,32,140
Rt 18,12,512
Cv 239,239,25
v_accvgpr_read_b32 v244, a240
Mv 44,36,140
Rt 20,13,0
buffer_store_dwordx4 v[128:131], v3, s[20:23], s46 offen offset:0 sc0 sc1
Mv 36,36,136
Rt 22,13,512
v_accvgpr_read_b32 v24, a241
Mv 48,40,136
Rt 24,14,0
Cv 244,244,24
v_accvgpr_read_b32 v245, a242
Mv 56,40,140
Rt 26,14,512
v_accvgpr_read_b32 v25, a243
Mv 60,44,140
Rt 28,15,0
Cv 245,245,25
Mv 52,44,136
Rt 30,15,512
s_waitcnt vmcnt(5) lgkmcnt(8)
s_barrier
buffer_store_dword v6, v7, s[20:23], s49 offen offset:0 sc0 sc1
Mv 32,0,144
Rt 32,12,4096
v_accvgpr_read_b32 v246, a244
Mv 40,0,148
Rt 34,12,4608
s_add_u32 s92, s42, 0x100
Mv 44,4,148
Rt 36,13,4096
s_add_u32 m0, s41, 98304
Mv 36,4,144
Rt 38,13,4608
buffer_load_dwordx4 v8, s[16:19], s92 offen offset:0 lds
Mv 48,8,144
Rt 40,14,4096
buffer_store_dwordx4 v[136:139], v3, s[20:23], s46 offen offset:1024 sc0 sc1
Mv 56,8,148
Rt 42,14,4608
v_accvgpr_read_b32 v24, a245
Mv 60,12,148
Rt 44,15,4096
Cv 246,246,24
v_accvgpr_read_b32 v247, a246
Mv 52,12,144
Rt 46,15,4608
buffer_load_dwordx4 v9, s[16:19], s92 offen offset:1024 lds
s_waitcnt lgkmcnt(8)
Mv 32,16,152
Rt 0,12,8192
)" R"(
v_accvgpr_read_b32 v25, a247
Mv 40,16,156
Rt 2,12,8704
Cv 247,247,25
v_accvgpr_read_b32 v252, a248
Mv 44,20,156
Rt 4,13,8192
v_accvgpr_read_b32 v24, a249
Mv 36,20,152
Rt 6,13,8704
buffer_load_dwordx4 v10, s[16:19], s92 offen offset:2048 lds
Mv 48,24,152
Rt 8,14,8192
Cv 252,252,24
v_accvgpr_read_b32 v253, a250
Mv 56,24,156
Rt 10,14,8704
v_accvgpr_read_b32 v25, a251
Mv 60,28,156
Rt 12,15,8192
buffer_store_dwordx4 v[144:147], v3, s[20:23], s46 offen offset:2048 sc0 sc1
Mv 52,28,152
Rt 14,15,8704
buffer_load_dwordx4 v11, s[16:19], s92 offen offset:3072 lds
s_waitcnt lgkmcnt(8)
Mv 32,32,160
Rt 16,12,12288
Cv 253,253,25
Mv 40,32,164
Rt 18,12,12800
s_add_u32 s92, s42, 0x40100
Mv 44,36,164
Rt 20,13,12288
s_add_u32 m0, s41, 102400
Mv 36,36,160
Rt 22,13,12800
buffer_load_dwordx4 v8, s[16:19], s92 offen offset:0 lds
Mv 48,40,160
Rt 24,14,12288
v_accvgpr_read_b32 v254, a252
Mv 56,40,164
Rt 26,14,12800
v_accvgpr_read_b32 v24, a253
Mv 60,44,164
Rt 28,15,12288
Cv 254,254,24
v_accvgpr_read_b32 v255, a254
Mv 52,44,160
Rt 30,15,12800
buffer_load_dwordx4 v9, s[16:19], s92 offen offset:1024 lds
s_waitcnt lgkmcnt(8)
Mv 32,0,168
Rt 32,12,16384
buffer_store_dwordx4 v[152:155], v3, s[20:23], s46 offen offset:3072 sc0 sc1
Mv 40,0,172
Rt 34,12,16896
v_accvgpr_read_b32 v25, a255
Cv 255,255,25
Mv 44,4,172
Rt 36,13,16384
Cv 160,160,161
Mv 36,4,168
Rt 38,13,16896
buffer_load_dwordx4 v10, s[16:19], s92 offen offset:2048 lds
)" R"(
Mv 48,8,168
Rt 40,14,16384
Cv 161,162,163
Cv 162,164,165
Mv 56,8,172
Rt 42,14,16896
s_add_u32 s92, s42, 0x1c0100
Mv 60,12,172
Rt 44,15,16384
s_add_u32 m0, s41, 131072
Mv 52,12,168
Rt 46,15,16896
buffer_load_dwordx4 v8, s[16:19], s92 offen offset:0 lds
s_waitcnt lgkmcnt(8)
Mv 32,16,176
Rt 0,12,20480
Cv 163,166,167
Mv 40,16,180
Rt 2,12,20992
Cv 168,168,169
Mv 44,20,180
Rt 4,13,20480
buffer_store_dwordx4 v[160:163], v2, s[20:23], s47 offen offset:0 sc0 sc1
Mv 36,20,176
Rt 6,13,20992
buffer_load_dwordx4 v9, s[16:19], s92 offen offset:1024 lds
Mv 48,24,176
Rt 8,14,20480
Cv 169,170,171
Mv 56,24,180
Rt 10,14,20992
Cv 170,172,173
Mv 60,28,180
Rt 12,15,20480
Cv 171,174,175
Cv 176,176,177
Mv 52,28,176
Rt 14,15,20992
buffer_load_dwordx4 v10, s[16:19], s92 offen offset:2048 lds
s_waitcnt lgkmcnt(8)
Mv 32,32,184
Rt 16,12,24576
Cv 177,178,179
Mv 40,32,188
Rt 18,12,25088
Cv 178,180,181
Cv 179,182,183
Mv 44,36,188
Rt 20,13,24576
Cv 184,184,185
Mv 36,36,184
Rt 22,13,25088
buffer_load_dwordx4 v11, s[16:19], s92 offen offset:3072 lds
Mv 48,40,184
Rt 24,14,24576
Cv 185,186,187
Cv 186,188,189
Mv 56,40,188
Rt 26,14,25088
s_add_u32 s92, s42, 0x200100
Mv 60,44,188
Rt 28,15,24576
s_add_u32 m0, s41, 135168
Mv 52,44,184
)" R"(
Rt 30,15,25088
buffer_load_dwordx4 v8, s[16:19], s92 offen offset:0 lds
s_waitcnt lgkmcnt(8)
Mv 32,0,48
Rt 32,16,0
Cv 187,190,191
Mv 40,0,52
Rt 34,16,512
Cv 192,192,193
Mv 44,4,52
Rt 36,17,0
buffer_store_dwordx4 v[168:171], v2, s[20:23], s47 offen offset:1024 sc0 sc1
Mv 36,4,48
Rt 38,17,512
buffer_load_dwordx4 v9, s[16:19], s92 offen offset:1024 lds
Mv 48,8,48
Rt 40,18,0
Cv 193,194,195
Mv 56,8,52
Rt 42,18,512
Cv 194,196,197
Mv 60,12,52
Rt 44,19,0
Cv 195,198,199
Cv 200,200,201
Mv 52,12,48
Rt 46,19,512
buffer_load_dwordx4 v10, s[16:19], s92 offen offset:2048 lds
s_waitcnt vmcnt(10) lgkmcnt(8)
s_barrier
buffer_store_dword v6, v7, s[20:23], s49 offen offset:4 sc0 sc1
Mv 32,16,56
Rt 0,16,4096
Cv 201,202,203
Mv 40,16,60
Rt 2,16,4608
Cv 202,204,205
Cv 203,206,207
Mv 44,20,60
Rt 4,17,4096
Cv 208,208,209
Mv 36,20,56
Rt 6,17,4608
s_add_u32 s92, s42, 0x180
Mv 48,24,56
Rt 8,18,4096
s_add_u32 m0, s41, 0
Cv 209,210,211
Mv 56,24,60
Rt 10,18,4608
buffer_load_dwordx4 v8, s[16:19], s92 offen offset:0 lds
Mv 60,28,60
Rt 12,19,4096
Cv 210,212,213
Mv 52,28,56
Rt 14,19,4608
Cv 211,214,215
s_waitcnt lgkmcnt(8)
Mz 64,32,192
Rt 16,16,8192
Cv 216,216,217
Mz 72,32,196
Rt 18,16,8704
Cv 217,218,219
Mz 76,36,196
)" R"(
Rt 20,17,8192
buffer_store_dwordx4 v[176:179], v2, s[20:23], s47 offen offset:2048 sc0 sc1
Mz 68,36,192
Rt 22,17,8704
Cv 218,220,221
Cv 219,222,223
Mz 80,40,192
Rt 24,18,8192
v_mul_f32_e32 v32, s54, v32
Mz 88,40,196
Rt 26,18,8704
buffer_load_dwordx4 v9, s[16:19], s92 offen offset:1024 lds
Mz 92,44,196
Rt 28,19,8192
v_mul_f32_e32 v33, s54, v33
Mz 84,44,192
Rt 30,19,8704
s_waitcnt lgkmcnt(8)
Mv 64,0,200
Rt 32,16,12288
Cv 32,32,33
Mv 72,0,204
Rt 34,16,12800
v_accvgpr_write_b32 a224, v32
Mv 76,4,204
Rt 36,17,12288
v_mul_f32_e32 v34, s54, v34
Mv 68,4,200
Rt 38,17,12800
v_mul_f32_e32 v35, s54, v35
Mv 80,8,200
Rt 40,18,12288
Cv 34,34,35
Mv 88,8,204
Rt 42,18,12800
buffer_load_dwordx4 v10, s[16:19], s92 offen offset:2048 lds
Mv 92,12,204
Rt 44,19,12288
v_accvgpr_write_b32 a225, v34
Mv 84,12,200
Rt 46,19,12800
s_waitcnt lgkmcnt(8)
Mv 64,16,208
Rt 0,16,16384
v_mul_f32_e32 v36, s54, v36
Mv 72,16,212
Rt 2,16,16896
v_mul_f32_e32 v37, s54, v37
Mv 76,20,212
Rt 4,17,16384
buffer_store_dwordx4 v[184:187], v2, s[20:23], s47 offen offset:3072 sc0 sc1
Mv 68,20,208
Rt 6,17,16896
Cv 36,36,37
Mv 80,24,208
Rt 8,18,16384
v_accvgpr_write_b32 a226, v36
Mv 88,24,212
Rt 10,18,16896
buffer_load_dwordx4 v11, s[16:19], s92 offen offset:3072 lds
Mv 92,28,212
Rt 12,19,16384
v_mul_f32_e32 v38, s54, v38
Mv 84,28,208
)" R"(
Rt 14,19,16896
s_waitcnt lgkmcnt(8)
Mv 64,32,216
Rt 16,16,20480
v_mul_f32_e32 v39, s54, v39
Mv 72,32,220
Rt 18,16,20992
Cv 38,38,39
Mv 76,36,220
Rt 20,17,20480
v_accvgpr_write_b32 a227, v38
Mv 68,36,216
Rt 22,17,20992
s_add_u32 s92, s42, 0x40180
Mv 80,40,216
Rt 24,18,20480
s_add_u32 m0, s41, 4096
Mv 88,40,220
Rt 26,18,20992
buffer_load_dwordx4 v8, s[16:19], s92 offen offset:0 lds
Mv 92,44,220
Rt 28,19,20480
v_mul_f32_e32 v40, s54, v40
Mv 84,44,216
Rt 30,19,20992
s_waitcnt lgkmcnt(8)
Mv 64,0,128
Rt 32,16,24576
v_mul_f32_e32 v41, s54, v41
Mv 72,0,132
Rt 34,16,25088
Cv 40,40,41
Mv 76,4,132
Rt 36,17,24576
buffer_store_dwordx4 v[192:195], v3, s[20:23], s47 offen offset:0 sc0 sc1
Mv 68,4,128
Rt 38,17,25088
v_accvgpr_write_b32 a228, v40
Mv 80,8,128
Rt 40,18,24576
v_mul_f32_e32 v42, s54, v42
Mv 88,8,132
Rt 42,18,25088
buffer_load_dwordx4 v9, s[16:19], s92 offen offset:1024 lds
Mv 92,12,132
Rt 44,19,24576
v_mul_f32_e32 v43, s54, v43
Mv 84,12,128
Rt 46,19,25088
s_waitcnt lgkmcnt(8)
Mv 64,16,136
Rt 0,12,32768
Cv 42,42,43
Mv 72,16,140
Rt 2,12,33280
v_accvgpr_write_b32 a229, v42
Mv 76,20,140
Rt 4,13,32768
v_mul_f32_e32 v44, s54, v44
Mv 68,20,136
Rt 6,13,33280
v_mul_f32_e32 v45, s54, v45
Mv 80,24,136
Rt 8,14,32768
)" R"(
Cv 44,44,45
Mv 88,24,140
Rt 10,14,33280
buffer_load_dwordx4 v10, s[16:19], s92 offen offset:2048 lds
Mv 92,28,140
Rt 12,15,32768
v_accvgpr_write_b32 a230, v44
Mv 84,28,136
Rt 14,15,33280
s_waitcnt vmcnt(20) lgkmcnt(8)
s_barrier
Mv 64,32,144
Rt 16,12,36864
v_mul_f32_e32 v46, s54, v46
Mv 72,32,148
Rt 18,12,37376
v_mul_f32_e32 v47, s54, v47
Mv 76,36,148
Rt 20,13,36864
buffer_store_dwordx4 v[200:203], v3, s[20:23], s47 offen offset:1024 sc0 sc1
Mv 68,36,144
Rt 22,13,37376
s_add_u32 s92, s42, 0x1c0180
Mv 80,40,144
Rt 24,14,36864
s_add_u32 m0, s41, 65536
Mv 88,40,148
Rt 26,14,37376
buffer_load_dwordx4 v8, s[16:19], s92 offen offset:0 lds
Mv 92,44,148
Rt 28,15,36864
Cv 46,46,47
Mv 84,44,144
Rt 30,15,37376
s_waitcnt lgkmcnt(8)
Mv 64,0,152
Rt 32,12,40960
v_accvgpr_write_b32 a231, v46
Mv 72,0,156
Rt 34,12,41472
v_mul_f32_e32 v48, s54, v48
Mv 76,4,156
Rt 36,13,40960
v_mul_f32_e32 v49, s54, v49
Mv 68,4,152
Rt 38,13,41472
Cv 48,48,49
Mv 80,8,152
Rt 40,14,40960
v_accvgpr_write_b32 a232, v48
Mv 88,8,156
Rt 42,14,41472
buffer_load_dwordx4 v9, s[16:19], s92 offen offset:1024 lds
Mv 92,12,156
Rt 44,15,40960
v_mul_f32_e32 v50, s54, v50
Mv 84,12,152
Rt 46,15,41472
s_waitcnt lgkmcnt(8)
Mv 64,16,160
Rt 0,12,45056
v_mul_f32_e32 v51, s54, v51
Mv 72,16,164
Rt 2,12,45568
Cv 50,50,51
)" R"(
Mv 76,20,164
Rt 4,13,45056
buffer_store_dwordx4 v[208:211], v3, s[20:23], s47 offen offset:2048 sc0 sc1
Mv 68,20,160
Rt 6,13,45568
v_accvgpr_write_b32 a233, v50
Mv 80,24,160
Rt 8,14,45056
v_mul_f32_e32 v52, s54, v52
Mv 88,24,164
Rt 10,14,45568
buffer_load_dwordx4 v10, s[16:19], s92 offen offset:2048 lds
Mv 92,28,164
Rt 12,15,45056
v_mul_f32_e32 v53, s54, v53
Mv 84,28,160
Rt 14,15,45568
s_waitcnt lgkmcnt(8)
Mv 64,32,168
Rt 16,12,49152
Cv 52,52,53
Mv 72,32,172
Rt 18,12,49664
v_accvgpr_write_b32 a234, v52
Mv 76,36,172
Rt 20,13,49152
v_mul_f32_e32 v54, s54, v54
Mv 68,36,168
Rt 22,13,49664
v_mul_f32_e32 v55, s54, v55
Mv 80,40,168
Rt 24,14,49152
buffer_load_dword v4, v5, s[20:23], s50 offen offset:0 sc0 sc1
Mv 88,40,172
Rt 26,14,49664
buffer_load_dwordx4 v11, s[16:19], s92 offen offset:3072 lds
Mv 92,44,172
Rt 28,15,49152
Cv 54,54,55
Mv 84,44,168
Rt 30,15,49664
s_waitcnt lgkmcnt(8)
Mv 64,0,176
Rt 32,12,53248
v_accvgpr_write_b32 a235, v54
Mv 72,0,180
Rt 34,12,53760
v_mul_f32_e32 v56, s54, v56
Mv 76,4,180
Rt 36,13,53248
buffer_store_dwordx4 v[216:219], v3, s[20:23], s47 offen offset:3072 sc0 sc1
Mv 68,4,176
Rt 38,13,53760
s_add_u32 s92, s42, 0x200180
Mv 80,8,176
Rt 40,14,53248
s_add_u32 m0, s41, 69632
Mv 88,8,180
Rt 42,14,53760
buffer_load_dwordx4 v8, s[16:19], s92 offen offset:0 lds
Mv 92,12,180
Rt 44,15,53248
v_mul_f32_e32 v57, s54, v57
Mv 84,12,176
)" R"(
Rt 46,15,53760
s_waitcnt lgkmcnt(8)
Mv 64,16,184
Rt 0,12,57344
Cv 56,56,57
Mv 72,16,188
Rt 2,12,57856
v_accvgpr_write_b32 a236, v56
Mv 76,20,188
Rt 4,13,57344
v_mul_f32_e32 v58, s54, v58
Mv 68,20,184
Rt 6,13,57856
v_mul_f32_e32 v59, s54, v59
Mv 80,24,184
Rt 8,14,57344
Cv 58,58,59
Mv 88,24,188
Rt 10,14,57856
buffer_load_dwordx4 v9, s[16:19], s92 offen offset:1024 lds
Mv 92,28,188
Rt 12,15,57344
v_accvgpr_write_b32 a237, v58
Mv 84,28,184
Rt 14,15,57856
s_waitcnt lgkmcnt(8)
Mv 64,32,48
Rt 16,16,32768
v_mul_f32_e32 v60, s54, v60
Mv 72,32,52
Rt 18,16,33280
v_mul_f32_e32 v61, s54, v61
Mv 76,36,52
Rt 20,17,32768
buffer_store_dwordx4 v[224:227], v2, s[20:23], s48 offen offset:0 sc0 sc1
Mv 68,36,48
Rt 22,17,33280
Cv 60,60,61
Mv 80,40,48
Rt 24,18,32768
v_accvgpr_write_b32 a238, v60
Mv 88,40,52
Rt 26,18,33280
buffer_load_dwordx4 v10, s[16:19], s92 offen offset:2048 lds
Mv 92,44,52
Rt 28,19,32768
v_mul_f32_e32 v62, s54, v62
Mv 84,44,48
Rt 30,19,33280
s_waitcnt vmcnt(6) lgkmcnt(8)
s_barrier
Mv 64,0,56
Rt 32,16,36864
v_cmp_eq_u32_e32 vcc, s3, v4
s_andn2_b64 s[28:29], s[60:61], vcc
s_cbranch_scc1 .Lzsp3
.Lzok3:
Mv 72,0,60
Rt 34,16,37376
s_add_u32 s40, s55, 0x10000
Mv 76,4,60
Rt 36,17,36864
v_mul_f32_e32 v63, s54, v63
Mv 68,4,56
Rt 38,17,37376
)" R"(
Cv 62,62,63
Mv 80,8,56
Rt 40,18,36864
s_add_u32 m0, s36, 32768
Mv 88,8,60
Rt 42,18,37376
buffer_load_dwordx4 v2, s[20:23], s40 offen offset:0 sc1 lds
Mv 92,12,60
Rt 44,19,36864
v_accvgpr_write_b32 a239, v62
Mv 84,12,56
Rt 46,19,37376
s_waitcnt lgkmcnt(8)
Mz 32,16,192
Rt 0,16,40960
Mz 40,16,196
Rt 2,16,41472
Mz 44,20,196
Rt 4,17,40960
buffer_store_dwordx4 v[232:235], v2, s[20:23], s48 offen offset:1024 sc0 sc1
Mz 36,20,192
Rt 6,17,41472
buffer_load_dwordx4 v2, s[20:23], s40 offen offset:1024 sc1 lds
Mz 48,24,192
Rt 8,18,40960
v_mul_f32_e32 v64, s54, v64
Mz 56,24,196
Rt 10,18,41472
v_mul_f32_e32 v65, s54, v65
Mz 60,28,196
Rt 12,19,40960
Cv 64,64,65
Mz 52,28,192
Rt 14,19,41472
s_waitcnt lgkmcnt(8)
Mv 32,32,200
Rt 16,16,45056
v_accvgpr_write_b32 a240, v64
Mv 40,32,204
Rt 18,16,45568
buffer_load_dwordx4 v2, s[20:23], s40 offen offset:2048 sc1 lds
Mv 44,36,204
Rt 20,17,45056
v_mul_f32_e32 v66, s54, v66
Mv 36,36,200
Rt 22,17,45568
v_mul_f32_e32 v67, s54, v67
Mv 48,40,200
Rt 24,18,45056
Cv 66,66,67
Mv 56,40,204
Rt 26,18,45568
v_accvgpr_write_b32 a241, v66
Mv 60,44,204
Rt 28,19,45056
v_mul_f32_e32 v68, s54, v68
Mv 52,44,200
Rt 30,19,45568
buffer_load_dwordx4 v2, s[20:23], s40 offen offset:3072 sc1 lds
s_waitcnt lgkmcnt(8)
Mv 32,0,208
Rt 32,16,49152
v_mul_f32_e32 v69, s54, v69
Mv 40,0,212
)" R"(
Rt 34,16,49664
Cv 68,68,69
Mv 44,4,212
Rt 36,17,49152
buffer_store_dwordx4 v[240:243], v2, s[20:23], s48 offen offset:2048 sc0 sc1
Mv 36,4,208
Rt 38,17,49664
v_accvgpr_write_b32 a242, v68
v_mul_f32_e32 v70, s54, v70
Mv 48,8,208
Rt 40,18,49152
s_add_u32 m0, s36, 36864
buffer_load_dword v4, v7, s[20:23], s52 offen offset:0 sc0 sc1
Mv 56,8,212
Rt 42,18,49664
buffer_load_dwordx4 v3, s[20:23], s40 offen offset:0 sc1 lds
Mv 60,12,212
Rt 44,19,49152
v_mul_f32_e32 v71, s54, v71
Mv 52,12,208
Rt 46,19,49664
s_waitcnt lgkmcnt(8)
Mv 32,16,216
Rt 0,16,53248
Cv 70,70,71
Mv 40,16,220
Rt 2,16,53760
v_accvgpr_write_b32 a243, v70
Mv 44,20,220
Rt 4,17,53248
v_mul_f32_e32 v72, s54, v72
Mv 36,20,216
Rt 6,17,53760
buffer_load_dwordx4 v3, s[20:23], s40 offen offset:1024 sc1 lds
Mv 48,24,216
Rt 8,18,53248
v_mul_f32_e32 v73, s54, v73
Mv 56,24,220
Rt 10,18,53760
Cv 72,72,73
Mv 60,28,220
Rt 12,19,53248
v_accvgpr_write_b32 a244, v72
Mv 52,28,216
Rt 14,19,53760
s_waitcnt lgkmcnt(8)
Mv 32,32,128
Rt 16,16,57344
v_mul_f32_e32 v74, s54, v74
Mv 40,32,132
Rt 18,16,57856
buffer_load_dwordx4 v3, s[20:23], s40 offen offset:2048 sc1 lds
Mv 44,36,132
Rt 20,17,57344
buffer_store_dwordx4 v[248:251], v2, s[20:23], s48 offen offset:3072 sc0 sc1
Mv 36,36,128
Rt 22,17,57856
v_mul_f32_e32 v75, s54, v75
Cv 74,74,75
Mv 48,40,128
Rt 24,18,57344
v_accvgpr_write_b32 a245, v74
Mv 56,40,132
Rt 26,18,57856
)" R"(
v_mul_f32_e32 v76, s54, v76
Mv 60,44,132
Rt 28,19,57344
v_mul_f32_e32 v77, s54, v77
Mv 52,44,128
Rt 30,19,57856
buffer_load_dwordx4 v3, s[20:23], s40 offen offset:3072 sc1 lds
s_waitcnt lgkmcnt(8)
Mv 32,0,136
Rt 32,20,0
Cv 76,76,77
Mv 40,0,140
Rt 34,20,512
v_accvgpr_write_b32 a246, v76
Mv 44,4,140
Rt 36,21,0
v_mul_f32_e32 v78, s54, v78
Mv 36,4,136
Rt 38,21,512
v_mul_f32_e32 v79, s54, v79
Mv 48,8,136
Rt 40,22,0
Cv 78,78,79
Mv 56,8,140
Rt 42,22,512
v_accvgpr_write_b32 a247, v78
Mv 60,12,140
Rt 44,23,0
v_mul_f32_e32 v80, s54, v80
Mv 52,12,136
Rt 46,23,512
s_waitcnt vmcnt(5) lgkmcnt(8)
s_barrier
buffer_store_dword v6, v7, s[20:23], s49 offen offset:8 sc0 sc1
Mv 32,16,144
Rt 0,20,4096
v_cmp_eq_u32_e32 vcc, s3, v4
s_andn2_b64 s[28:29], 1, vcc
s_cbranch_scc1 .Lzsp4
.Lzok4:
Mv 40,16,148
Rt 2,20,4608
v_mul_f32_e32 v81, s54, v81
Mv 44,20,148
Rt 4,21,4096
s_add_u32 m0, s36, 98304
buffer_store_dwordx4 v[228:231], v3, s[20:23], s48 offen offset:0 sc0 sc1
Mv 36,20,144
Rt 6,21,4608
buffer_load_dwordx4 v2, s[20:23], s55 offen offset:0 sc1 lds
Mv 48,24,144
Rt 8,22,4096
Cv 80,80,81
Mv 56,24,148
Rt 10,22,4608
v_accvgpr_write_b32 a248, v80
Mv 60,28,148
Rt 12,23,4096
buffer_load_dwordx4 a[64:67], v2, s[20:23], s51 offen offset:0 sc0 sc1
Mv 52,28,144
Rt 14,23,4608
s_waitcnt lgkmcnt(8)
Mv 32,32,152
Rt 16,20,8192
v_mul_f32_e32 v82, s54, v82
)" R"(
Mv 40,32,156
Rt 18,20,8704
buffer_load_dwordx4 v2, s[20:23], s55 offen offset:1024 sc1 lds
Mv 44,36,156
Rt 20,21,8192
v_mul_f32_e32 v83, s54, v83
Mv 36,36,152
Rt 22,21,8704
Cv 82,82,83
Mv 48,40,152
Rt 24,22,8192
buffer_load_dwordx4 a[68:71], v2, s[20:23], s51 offen offset:1024 sc0 sc1
Mv 56,40,156
Rt 26,22,8704
v_accvgpr_write_b32 a249, v82
Mv 60,44,156
Rt 28,23,8192
v_mul_f32_e32 v84, s54, v84
Mv 52,44,152
Rt 30,23,8704
buffer_load_dwordx4 v2, s[20:23], s55 offen offset:2048 sc1 lds
s_waitcnt lgkmcnt(8)
Mv 32,0,160
Rt 32,20,12288
v_mul_f32_e32 v85, s54, v85
Mv 40,0,164
Rt 34,20,12800
Cv 84,84,85
Mv 44,4,164
Rt 36,21,12288
buffer_load_dwordx4 a[72:75], v2, s[20:23], s51 offen offset:2048 sc0 sc1
buffer_store_dwordx4 v[236:239], v3, s[20:23], s48 offen offset:1024 sc0 sc1
Mv 36,4,160
Rt 38,21,12800
v_accvgpr_write_b32 a250, v84
v_mul_f32_e32 v86, s54, v86
Mv 48,8,160
Rt 40,22,12288
v_mul_f32_e32 v87, s54, v87
Mv 56,8,164
Rt 42,22,12800
buffer_load_dwordx4 v2, s[20:23], s55 offen offset:3072 sc1 lds
Mv 60,12,164
Rt 44,23,12288
Cv 86,86,87
Mv 52,12,160
Rt 46,23,12800
s_waitcnt lgkmcnt(8)
Mv 32,16,168
Rt 0,20,16384
buffer_load_dwordx4 a[76:79], v2, s[20:23], s51 offen offset:3072 sc0 sc1
Mv 40,16,172
Rt 2,20,16896
v_accvgpr_write_b32 a251, v86
Mv 44,20,172
Rt 4,21,16384
s_add_u32 m0, s36, 102400
Mv 36,20,168
Rt 6,21,16896
buffer_load_dwordx4 v3, s[20:23], s55 offen offset:0 sc1 lds
Mv 48,24,168
Rt 8,22,16384
v_mul_f32_e32 v88, s54, v88
Mv 56,24,172
)" R"(
Rt 10,22,16896
v_mul_f32_e32 v89, s54, v89
Mv 60,28,172
Rt 12,23,16384
buffer_load_dwordx4 a[80:83], v3, s[20:23], s51 offen offset:0 sc0 sc1
Mv 52,28,168
Rt 14,23,16896
s_waitcnt lgkmcnt(8)
Mv 32,32,176
Rt 16,20,20480
Cv 88,88,89
Mv 40,32,180
Rt 18,20,20992
buffer_load_dwordx4 v3, s[20:23], s55 offen offset:1024 sc1 lds
Mv 44,36,180
Rt 20,21,20480
buffer_store_dwordx4 v[244:247], v3, s[20:23], s48 offen offset:2048 sc0 sc1
Mv 36,36,176
Rt 22,21,20992
v_accvgpr_write_b32 a252, v88
v_mul_f32_e32 v90, s54, v90
Mv 48,40,176
Rt 24,22,20480
buffer_load_dwordx4 a[84:87], v3, s[20:23], s51 offen offset:1024 sc0 sc1
Mv 56,40,180
Rt 26,22,20992
v_mul_f32_e32 v91, s54, v91
Mv 60,44,180
Rt 28,23,20480
Cv 90,90,91
Mv 52,44,176
Rt 30,23,20992
buffer_load_dwordx4 v3, s[20:23], s55 offen offset:2048 sc1 lds
s_waitcnt lgkmcnt(8)
Mv 32,0,184
Rt 32,20,24576
v_accvgpr_write_b32 a253, v90
Mv 40,0,188
Rt 34,20,25088
v_mul_f32_e32 v92, s54, v92
Mv 44,4,188
Rt 36,21,24576
buffer_load_dwordx4 a[88:91], v3, s[20:23], s51 offen offset:2048 sc0 sc1
Mv 36,4,184
Rt 38,21,25088
v_mul_f32_e32 v93, s54, v93
Mv 48,8,184
Rt 40,22,24576
Cv 92,92,93
Mv 56,8,188
Rt 42,22,25088
buffer_load_dwordx4 v3, s[20:23], s55 offen offset:3072 sc1 lds
Mv 60,12,188
Rt 44,23,24576
v_accvgpr_write_b32 a254, v92
Mv 52,12,184
Rt 46,23,25088
s_waitcnt lgkmcnt(8)
Mv 32,16,48
Rt 0,12,0
buffer_load_dwordx4 a[92:95], v3, s[20:23], s51 offen offset:3072 sc0 sc1
Mv 40,16,52
Rt 2,12,512
v_mul_f32_e32 v94, s54, v94
)" R"(
Mv 44,20,52
Rt 4,13,0
buffer_store_dwordx4 v[252:255], v3, s[20:23], s48 offen offset:3072 sc0 sc1
Mv 36,20,48
Rt 6,13,512
v_mul_f32_e32 v95, s54, v95
Cv 94,94,95
Mv 48,24,48
Rt 8,14,0
v_accvgpr_write_b32 a255, v94
Mv 56,24,52
Rt 10,14,512
Mv 60,28,52
Rt 12,15,0
s_add_u32 s51, s51, 0x10000
buffer_load_dwordx4 a[96:99], v2, s[20:23], s51 offen offset:0 sc0 sc1
Mv 52,28,48
Rt 14,15,512
s_waitcnt lgkmcnt(8)
s_barrier
Mv 32,32,56
Rt 16,12,4096
Mv 40,32,60
Rt 18,12,4608
s_mov_b32 s83, 0xc108000
buffer_load_dword v4, v7, s[20:23], s83 offen offset:4 sc0 sc1
Mv 44,36,60
Rt 20,13,4096
s_add_u32 m0, s36, 131072
Mv 36,36,56
Rt 22,13,4608
buffer_load_dwordx4 v2, s[20:23], s56 offen offset:0 sc1 lds
Mv 48,40,56
Rt 24,14,4096
buffer_load_dwordx4 a[100:103], v2, s[20:23], s51 offen offset:1024 sc0 sc1
Mv 56,40,60
Rt 26,14,4608
Mv 60,44,60
Rt 28,15,4096
Mv 52,44,56
Rt 30,15,4608
s_waitcnt lgkmcnt(8)
Mz 96,0,192
Rt 32,12,8192
Mz 104,0,196
Rt 34,12,8704
buffer_load_dwordx4 v2, s[20:23], s56 offen offset:1024 sc1 lds
Mz 108,4,196
Rt 36,13,8192
buffer_load_dwordx4 a[104:107], v2, s[20:23], s51 offen offset:2048 sc0 sc1
Mz 100,4,192
Rt 38,13,8704
Mz 112,8,192
Rt 40,14,8192
v_mul_f32_e32 v32, s54, v32
Mz 120,8,196
Rt 42,14,8704
v_mul_f32_e32 v33, s54, v33
Mz 124,12,196
Rt 44,15,8192
Cv 32,32,33
Mz 116,12,192
Rt 46,15,8704
buffer_load_dwordx4 v2, s[20:23], s56 offen offset:2048 sc1 lds
s_waitcnt lgkmcnt(8)
)" R"(
Mv 96,16,200
Rt 0,12,12288
buffer_load_dwordx4 a[108:111], v2, s[20:23], s51 offen offset:3072 sc0 sc1
Mv 104,16,204
Rt 2,12,12800
v_accvgpr_write_b32 a192, v32
Mv 108,20,204
Rt 4,13,12288
v_mul_f32_e32 v34, s54, v34
Mv 100,20,200
Rt 6,13,12800
v_mul_f32_e32 v35, s54, v35
Mv 112,24,200
Rt 8,14,12288
Cv 34,34,35
Mv 120,24,204
Rt 10,14,12800
buffer_load_dwordx4 v2, s[20:23], s56 offen offset:3072 sc1 lds
Mv 124,28,204
Rt 12,15,12288
buffer_load_dwordx4 a[112:115], v3, s[20:23], s51 offen offset:0 sc0 sc1
Mv 116,28,200
Rt 14,15,12800
s_waitcnt lgkmcnt(8)
Mv 96,32,208
Rt 16,12,16384
v_accvgpr_write_b32 a193, v34
Mv 104,32,212
Rt 18,12,16896
v_mul_f32_e32 v36, s54, v36
Mv 108,36,212
Rt 20,13,16384
s_add_u32 m0, s36, 135168
Mv 100,36,208
Rt 22,13,16896
buffer_load_dwordx4 v3, s[20:23], s56 offen offset:0 sc1 lds
Mv 112,40,208
Rt 24,14,16384
buffer_load_dwordx4 a[116:119], v3, s[20:23], s51 offen offset:1024 sc0 sc1
Mv 120,40,212
Rt 26,14,16896
v_mul_f32_e32 v37, s54, v37
Mv 124,44,212
Rt 28,15,16384
Cv 36,36,37
Mv 116,44,208
Rt 30,15,16896
s_waitcnt lgkmcnt(8)
Mv 96,0,216
Rt 32,12,20480
v_accvgpr_write_b32 a194, v36
Mv 104,0,220
Rt 34,12,20992
buffer_load_dwordx4 v3, s[20:23], s56 offen offset:1024 sc1 lds
Mv 108,4,220
Rt 36,13,20480
buffer_load_dwordx4 a[120:123], v3, s[20:23], s51 offen offset:2048 sc0 sc1
Mv 100,4,216
Rt 38,13,20992
v_mul_f32_e32 v38, s54, v38
Mv 112,8,216
Rt 40,14,20480
v_mul_f32_e32 v39, s54, v39
Mv 120,8,220
)" R"(
Rt 42,14,20992
Cv 38,38,39
Mv 124,12,220
Rt 44,15,20480
v_accvgpr_write_b32 a195, v38
Mv 116,12,216
Rt 46,15,20992
buffer_load_dwordx4 v3, s[20:23], s56 offen offset:2048 sc1 lds
s_waitcnt lgkmcnt(8)
Mv 96,16,128
Rt 0,12,24576
buffer_load_dwordx4 a[124:127], v3, s[20:23], s51 offen offset:3072 sc0 sc1
Mv 104,16,132
Rt 2,12,25088
v_mul_f32_e32 v40, s54, v40
Mv 108,20,132
Rt 4,13,24576
v_mul_f32_e32 v41, s54, v41
Mv 100,20,128
Rt 6,13,25088
Cv 40,40,41
Mv 112,24,128
Rt 8,14,24576
v_accvgpr_write_b32 a196, v40
Mv 120,24,132
Rt 10,14,25088
buffer_load_dwordx4 v3, s[20:23], s56 offen offset:3072 sc1 lds
Mv 124,28,132
Rt 12,15,24576
v_mul_f32_e32 v42, s54, v42
Mv 116,28,128
Rt 14,15,25088
s_waitcnt lgkmcnt(8)
Mv 96,32,136
Rt 16,16,0
v_mul_f32_e32 v43, s54, v43
Mv 104,32,140
Rt 18,16,512
Cv 42,42,43
Mv 108,36,140
Rt 20,17,0
v_accvgpr_write_b32 a197, v42
Mv 100,36,136
Rt 22,17,512
v_mul_f32_e32 v44, s54, v44
Mv 112,40,136
Rt 24,18,0
v_mul_f32_e32 v45, s54, v45
Mv 120,40,140
Rt 26,18,512
Cv 44,44,45
Mv 124,44,140
Rt 28,19,0
v_accvgpr_write_b32 a198, v44
Mv 116,44,136
Rt 30,19,512
s_waitcnt vmcnt(15) lgkmcnt(8)
s_barrier
buffer_store_dword v6, v7, s[20:23], s49 offen offset:12 sc0 sc1
Mv 96,0,144
Rt 32,16,4096
v_cmp_eq_u32_e32 vcc, s3, v4
s_andn2_b64 s[28:29], 1, vcc
s_cbranch_scc1 .Lzsp6
.Lzok6:
)" R"(
Mv 104,0,148
Rt 34,16,4608
buffer_load_dword v4, v5, s[20:23], s50 offen offset:0 sc0 sc1
Mv 108,4,148
Rt 36,17,4096
s_lshl_b64 s[60:61], s[60:61], 8
v_add_u32 v8, 0x10000, v1
Mv 100,4,144
Rt 38,17,4608
s_or_b32 s60, s60, 0xff
v_add_u32 v10, 0x20000, v1
Mv 112,8,144
Rt 40,18,4096
v_mul_f32_e32 v46, s54, v46
s_add_u32 s56, s56, 0x10000
Mv 120,8,148
Rt 42,18,4608
v_mul_f32_e32 v47, s54, v47
s_add_u32 s55, s55, 0x10000
Mv 124,12,148
Rt 44,19,4096
Cv 46,46,47
s_add_u32 s55, s55, 0x10000
Mv 116,12,144
Rt 46,19,4608
s_waitcnt lgkmcnt(8)
Mv 96,16,152
Rt 0,16,8192
v_accvgpr_write_b32 a199, v46
Mv 104,16,156
Rt 2,16,8704
v_mul_f32_e32 v48, s54, v48
Mv 108,20,156
Rt 4,17,8192
v_mul_f32_e32 v49, s54, v49
Mv 100,20,152
Rt 6,17,8704
Cv 48,48,49
s_cmp_eq_u32 s57, 2
s_cselect_b32 s55, s59, s55
Mv 112,24,152
Rt 8,18,8192
v_accvgpr_write_b32 a200, v48
s_mov_b32 s93, 0
Mv 120,24,156
Rt 10,18,8704
v_mul_f32_e32 v50, s54, v50
s_mov_b32 s94, 1
Mv 124,28,156
Rt 12,19,8192
v_mul_f32_e32 v51, s54, v51
Mv 116,28,152
Rt 14,19,8704
s_waitcnt lgkmcnt(8)
Mv 96,32,160
Rt 16,16,12288
Cv 50,50,51
Mv 104,32,164
Rt 18,16,12800
v_accvgpr_write_b32 a201, v50
Mv 108,36,164
Rt 20,17,12288
v_mul_f32_e32 v52, s54, v52
Mv 100,36,160
)" R"(
Rt 22,17,12800
v_mul_f32_e32 v53, s54, v53
Mv 112,40,160
Rt 24,18,12288
Cv 52,52,53
Mv 120,40,164
Rt 26,18,12800
v_accvgpr_write_b32 a202, v52
Mv 124,44,164
Rt 28,19,12288
v_mul_f32_e32 v54, s54, v54
Mv 116,44,160
Rt 30,19,12800
s_waitcnt lgkmcnt(8)
Mv 96,0,168
Rt 32,16,16384
v_mul_f32_e32 v55, s54, v55
Mv 104,0,172
Rt 34,16,16896
Cv 54,54,55
Mv 108,4,172
Rt 36,17,16384
v_accvgpr_write_b32 a203, v54
Mv 100,4,168
Rt 38,17,16896
v_mul_f32_e32 v56, s54, v56
Mv 112,8,168
Rt 40,18,16384
v_mul_f32_e32 v57, s54, v57
Mv 120,8,172
Rt 42,18,16896
Cv 56,56,57
Mv 124,12,172
Rt 44,19,16384
v_accvgpr_write_b32 a204, v56
Mv 116,12,168
Rt 46,19,16896
s_waitcnt lgkmcnt(8)
Mv 96,16,176
Rt 0,16,20480
v_mul_f32_e32 v58, s54, v58
Mv 104,16,180
Rt 2,16,20992
v_mul_f32_e32 v59, s54, v59
Mv 108,20,180
Rt 4,17,20480
Cv 58,58,59
Mv 100,20,176
Rt 6,17,20992
v_accvgpr_write_b32 a205, v58
Mv 112,24,176
Rt 8,18,20480
v_mul_f32_e32 v60, s54, v60
Mv 120,24,180
Rt 10,18,20992
v_mul_f32_e32 v61, s54, v61
Mv 124,28,180
Rt 12,19,20480
Cv 60,60,61
Mv 116,28,176
Rt 14,19,20992
s_waitcnt vmcnt(0) lgkmcnt(8)
s_barrier
Mv 96,32,184
Rt 16,16,24576
)" R"(
v_cmp_eq_u32_e32 vcc, s3, v4
s_andn2_b64 s[28:29], s[60:61], vcc
s_cbranch_scc1 .Lzsp5
.Lzok5:
Mv 104,32,188
Rt 18,16,25088
v_mov_b32 v24, 0
buffer_load_dword v4, v24, s[20:23], s83 offen sc0 sc1
Mv 108,36,188
Rt 20,17,24576
v_accvgpr_write_b32 a206, v60
Mv 100,36,184
Rt 22,17,25088
v_mul_f32_e32 v62, s54, v62
Mv 112,40,184
Rt 24,18,24576
v_mul_f32_e32 v63, s54, v63
Mv 120,40,188
Rt 26,18,25088
Cv 62,62,63
Mv 124,44,188
Rt 28,19,24576
v_accvgpr_write_b32 a207, v62
Mv 116,44,184
Rt 30,19,25088
s_waitcnt lgkmcnt(8)
Mv 96,0,48
Mv 104,0,52
Rb 0,8,32768
Mv 108,4,52
Mv 100,4,48
Rb 4,8,33792
Mv 112,8,48
Mv 120,8,52
Rb 8,8,34816
Mv 124,12,52
Mv 116,12,48
Rb 12,8,35840
s_waitcnt lgkmcnt(4)
Mv 96,16,56
Mv 104,16,60
Rb 16,8,40960
Mv 108,20,60
Mv 100,20,56
Rb 20,8,41984
Mv 112,24,56
Mv 120,24,60
Rb 24,8,43008
Mv 124,28,60
Mv 116,28,56
Rb 28,8,44032
Rb 32,8,36864
Rb 36,8,37888
Rb 40,8,38912
Rb 44,8,39936
s_cmp_eq_u32 s35, 7
s_cbranch_scc1 .LzA7
.LzA7R:
s_waitcnt lgkmcnt(8)
Mz 64,0,64
Rb 48,8,45056
v_mul_f32_e32 v96, s54, v96
Mz 68,0,68
v_mul_f32_e32 v97, s54, v97
)" R"(
Cv 96,96,97
v_accvgpr_write_b32 a208, v96
Mv 68,4,76
Rb 52,8,46080
s_add_u32 m0, s36, 0
Mv 64,4,72
buffer_load_dwordx4 v2, s[20:23], s55 offen offset:0 sc1 lds
v_mul_f32_e32 v98, s54, v98
Mv 64,8,80
Rb 56,8,47104
v_mul_f32_e32 v99, s54, v99
Mv 68,8,84
Cv 98,98,99
v_accvgpr_write_b32 a209, v98
v_mul_f32_e32 v100, s54, v100
Mv 68,12,92
Rb 60,8,48128
s_waitcnt lgkmcnt(8)
Mv 64,12,88
v_mul_f32_e32 v101, s54, v101
Cv 100,100,101
v_accvgpr_write_b32 a210, v100
Mv 64,16,96
Rb 0,8,49152
v_mul_f32_e32 v102, s54, v102
v_mul_f32_e32 v103, s54, v103
Mv 68,16,100
Cv 102,102,103
v_accvgpr_write_b32 a211, v102
Mv 68,20,108
Rb 4,8,50176
v_mul_f32_e32 v104, s54, v104
v_mul_f32_e32 v105, s54, v105
Mv 64,20,104
buffer_load_dwordx4 v2, s[20:23], s55 offen offset:1024 sc1 lds
Mv 64,24,112
Rb 8,8,51200
Cv 104,104,105
v_accvgpr_write_b32 a212, v104
Mv 68,24,116
v_mul_f32_e32 v106, s54, v106
v_mul_f32_e32 v107, s54, v107
Mv 68,28,124
Rb 12,8,52224
Cv 106,106,107
s_waitcnt lgkmcnt(8)
Mv 64,28,120
v_accvgpr_write_b32 a213, v106
v_mul_f32_e32 v108, s54, v108
v_mul_f32_e32 v109, s54, v109
Mz 72,32,64
Rb 16,8,57344
Cv 108,108,109
Mz 76,32,68
v_accvgpr_write_b32 a214, v108
v_mul_f32_e32 v110, s54, v110
v_mul_f32_e32 v111, s54, v111
Mv 76,36,76
Rb 20,8,58368
Cv 110,110,111
Mv 72,36,72
buffer_load_dwordx4 v2, s[20:23], s55 offen offset:2048 sc1 lds
v_accvgpr_write_b32 a215, v110
Mv 72,40,80
)" R"(
Rb 24,8,59392
v_mul_f32_e32 v112, s54, v112
Mv 76,40,84
v_mul_f32_e32 v113, s54, v113
Cv 112,112,113
v_accvgpr_write_b32 a216, v112
Mv 76,44,92
Rb 28,8,60416
s_waitcnt vmcnt(3) lgkmcnt(8)
Mv 72,44,88
v_mul_f32_e32 v114, s54, v114
v_mul_f32_e32 v115, s54, v115
Cv 114,114,115
Mv 72,48,96
Rb 32,8,53248
v_accvgpr_write_b32 a217, v114
v_mul_f32_e32 v116, s54, v116
Mv 76,48,100
v_mul_f32_e32 v117, s54, v117
Cv 116,116,117
Mv 76,52,108
Rb 36,8,54272
v_accvgpr_write_b32 a218, v116
v_mul_f32_e32 v118, s54, v118
Mv 72,52,104
buffer_load_dwordx4 v2, s[20:23], s55 offen offset:3072 sc1 lds
Mv 72,56,112
Rb 40,8,55296
v_mul_f32_e32 v119, s54, v119
Cv 118,118,119
Mv 76,56,116
v_accvgpr_write_b32 a219, v118
v_mul_f32_e32 v120, s54, v120
Mv 76,60,124
Rb 44,8,56320
v_mul_f32_e32 v121, s54, v121
s_waitcnt lgkmcnt(8)
Mv 72,60,120
Cv 120,120,121
v_accvgpr_write_b32 a220, v120
v_mul_f32_e32 v122, s54, v122
Mz 80,0,64
Rb 48,8,61440
v_mul_f32_e32 v123, s54, v123
Mz 84,0,68
Cv 122,122,123
v_accvgpr_write_b32 a221, v122
v_mul_f32_e32 v124, s54, v124
Mv 84,4,76
Rb 52,8,62464
s_add_u32 m0, s36, 4096
Mv 80,4,72
buffer_load_dwordx4 v3, s[20:23], s55 offen offset:0 sc1 lds
v_mul_f32_e32 v125, s54, v125
Mv 80,8,80
Rb 56,8,63488
Cv 124,124,125
Mv 84,8,84
v_accvgpr_write_b32 a222, v124
v_mul_f32_e32 v126, s54, v126
v_mul_f32_e32 v127, s54, v127
Mv 84,12,92
Rb 60,8,64512
s_waitcnt lgkmcnt(8)
)" R"(
Mv 80,12,88
Cv 126,126,127
v_accvgpr_write_b32 a223, v126
v_mov_b32 v16, 0
Mv 80,16,96
v_mov_b32 v17, 0
v_mov_b32 v18, 0
v_mov_b32 v19, 0
Mv 84,16,100
v_mov_b32 v20, 0
v_mov_b32 v21, 0
Mv 84,20,108
v_mov_b32 v22, 0
v_mov_b32 v23, 0
v_readfirstlane_b32 s63, v4
Mv 80,20,104
buffer_load_dwordx4 v3, s[20:23], s55 offen offset:1024 sc1 lds
Mv 80,24,112
v_and_b32 v24, 16, v1
v_lshrrev_b32 v24, v24, v4
v_and_b32 v24, 0x7ff, v24
Mv 84,24,116
v_mul_u32_u24 v24, 896, v24
v_and_b32 v25, 14, v0
Mv 84,28,124
v_min_u32 v25, 1, v25
v_lshlrev_b32 v25, 30, v25
v_and_or_b32 v25, v0, 48, v25
Mv 80,28,120
v_add_u32 v24, v24, v25
s_waitcnt lgkmcnt(0)
s_barrier
Mz 88,32,64
Rb 0,1,32768
s_and_b32 s83, s33, 1
Mz 92,32,68
Rb 4,1,33792
s_mul_i32 s82, s83, 448
Mv 92,36,76
Rb 8,1,34816
Mv 88,36,72
Rb 12,1,35840
buffer_load_dwordx4 v3, s[20:23], s55 offen offset:2048 sc1 lds
Mv 88,40,80
Rb 16,1,40960
Mv 92,40,84
Rb 20,1,41984
buffer_load_dwordx4 a[128:131], v24, s[12:15], s82 offen offset:0
Mv 92,44,92
Rb 24,1,43008
Mv 88,44,88
Rb 28,1,44032
buffer_load_dwordx4 a[132:135], v24, s[12:15], s82 offen offset:64
Mv 88,48,96
Rb 32,1,36864
buffer_load_dwordx4 a[136:139], v24, s[12:15], s82 offen offset:128
Mv 92,48,100
Rb 36,1,37888
Mv 92,52,108
Rb 40,1,38912
buffer_load_dwordx4 a[140:143], v24, s[12:15], s82 offen offset:192
Mv 88,52,104
Rb 44,1,39936
buffer_load_dwordx4 v3, s[20:23], s55 offen offset:3072 sc1 lds
Mv 88,56,112
)" R"(
buffer_load_dwordx4 a[144:147], v24, s[12:15], s82 offen offset:256
Mv 92,56,116
buffer_load_dwordx4 a[148:151], v24, s[12:15], s82 offen offset:320
Mv 92,60,124
buffer_load_dwordx4 a[152:155], v24, s[12:15], s82 offen offset:384
s_waitcnt lgkmcnt(8)
Mv 88,60,120
Mz 96,0,64
Rb 48,1,45056
buffer_load_dword v4, v5, s[20:23], s50 offen offset:0 sc0 sc1
Mz 100,0,68
s_lshl_b64 s[60:61], s[60:61], 8
s_or_b32 s60, s60, 0xff
s_cmp_le_i32 s57, s58
s_cbranch_scc1 .Lzmk0
.Lzmr0:
Mv 100,4,76
Rb 52,1,46080
s_add_u32 m0, s36, 65536
s_sub_u32 s57, s57, 1
Mv 96,4,72
buffer_load_dwordx4 v2, s[20:23], s56 offen offset:0 sc1 lds
s_add_u32 s55, s55, 0x10000
Mv 96,8,80
Rb 56,1,47104
s_cmp_eq_u32 s57, 2
s_cselect_b32 s55, s59, s55
Mv 100,8,84
v_exp_f32_e32 v32, v64
Mv 100,12,92
Rb 60,1,48128
s_waitcnt lgkmcnt(8)
Mv 96,12,88
v_exp_f32_e32 v24, v65
Mv 96,16,96
Rb 0,1,49152
v_exp_f32_e32 v33, v66
Mv 100,16,100
v_exp_f32_e32 v25, v67
v_exp_f32_e32 v34, v72
Mv 100,20,108
Rb 4,1,50176
v_exp_f32_e32 v26, v73
Mv 96,20,104
buffer_load_dwordx4 v2, s[20:23], s56 offen offset:1024 sc1 lds
v_add_f32_e32 v14, v32, v24
Mv 96,24,112
Rb 8,1,51200
v_exp_f32_e32 v35, v74
Mv 100,24,116
v_exp_f32_e32 v27, v75
Cv 32,32,24
Mv 100,28,124
Rb 12,1,52224
v_add_f32_e32 v14, v14, v33
s_waitcnt lgkmcnt(8)
Mv 96,28,120
v_exp_f32_e32 v36, v68
v_add_f32_e32 v14, v14, v25
Mz 104,32,64
Rb 16,1,57344
v_exp_f32_e32 v28, v69
Mz 108,32,68
v_exp_f32_e32 v37, v70
)" R"(
v_exp_f32_e32 v29, v71
Mv 108,36,76
Rb 20,1,58368
v_exp_f32_e32 v38, v76
Mv 104,36,72
buffer_load_dwordx4 v2, s[20:23], s56 offen offset:2048 sc1 lds
Cv 33,33,25
Mv 104,40,80
Rb 24,1,59392
v_exp_f32_e32 v24, v77
Mv 108,40,84
v_exp_f32_e32 v39, v78
v_add_f32_e32 v14, v14, v34
Mv 108,44,92
Rb 28,1,60416
v_add_f32_e32 v14, v14, v26
s_waitcnt lgkmcnt(8)
Mv 104,44,88
v_exp_f32_e32 v25, v79
Cv 34,34,26
Mv 104,48,96
Rb 32,1,53248
v_exp_f32_e32 v40, v80
Mv 108,48,100
v_exp_f32_e32 v26, v81
v_add_f32_e32 v14, v14, v35
v_add_f32_e32 v14, v14, v27
Mv 108,52,108
Rb 36,1,54272
Cv 35,35,27
v_add_f32_e32 v15, v36, v28
Mv 104,52,104
buffer_load_dwordx4 v2, s[20:23], s56 offen offset:3072 sc1 lds
Cv 36,36,28
Mv 104,56,112
Rb 40,1,55296
v_exp_f32_e32 v41, v82
Mv 108,56,116
v_exp_f32_e32 v27, v83
v_add_f32_e32 v15, v15, v37
Mv 108,60,124
Rb 44,1,56320
v_add_f32_e32 v15, v15, v29
s_waitcnt lgkmcnt(8)
Mv 104,60,120
v_exp_f32_e32 v42, v88
Cv 37,37,29
Mz 112,0,64
Rb 48,1,61440
v_exp_f32_e32 v28, v89
Mz 116,0,68
v_exp_f32_e32 v43, v90
v_add_f32_e32 v15, v15, v38
v_add_f32_e32 v15, v15, v24
Mv 116,4,76
Rb 52,1,62464
s_add_u32 m0, s36, 69632
Cv 38,38,24
Mv 112,4,72
buffer_load_dwordx4 v3, s[20:23], s56 offen offset:0 sc1 lds
v_add_f32_e32 v15, v15, v39
Mv 112,8,80
Rb 56,1,63488
v_exp_f32_e32 v29, v91
)" R"(
Mv 116,8,84
v_exp_f32_e32 v44, v84
v_add_f32_e32 v15, v15, v25
Mv 116,12,92
Rb 60,1,64512
Cv 39,39,25
s_waitcnt lgkmcnt(8)
Mv 112,12,88
v_exp_f32_e32 v24, v85
v_add_f32_e32 v14, v14, v40
Mv 112,16,96
v_exp_f32_e32 v45, v86
v_add_f32_e32 v14, v14, v26
Mv 116,16,100
v_exp_f32_e32 v25, v87
Cv 40,40,26
v_add_f32_e32 v14, v14, v41
Mv 116,20,108
v_exp_f32_e32 v46, v92
v_add_f32_e32 v14, v14, v27
Mv 112,20,104
buffer_load_dwordx4 v3, s[20:23], s56 offen offset:1024 sc1 lds
Cv 41,41,27
Mv 112,24,112
v_exp_f32_e32 v26, v93
v_add_f32_e32 v14, v14, v42
Mv 116,24,116
v_exp_f32_e32 v47, v94
v_add_f32_e32 v14, v14, v28
Mv 116,28,124
v_exp_f32_e32 v27, v95
Cv 42,42,28
Mv 112,28,120
v_add_f32_e32 v14, v14, v43
s_waitcnt lgkmcnt(0)
s_barrier
Mz 120,32,64
Rb 0,10,0
v_add_f32_e32 v14, v14, v29
Cv 43,43,29
Mz 124,32,68
Rb 4,10,1024
v_add_f32_e32 v15, v15, v44
v_add_f32_e32 v15, v15, v24
Cv 44,44,24
Mv 124,36,76
Rb 8,10,2048
v_add_f32_e32 v15, v15, v45
v_add_f32_e32 v15, v15, v25
Mv 120,36,72
Rb 12,10,3072
buffer_load_dwordx4 v3, s[20:23], s56 offen offset:2048 sc1 lds
Mv 120,40,80
Rb 16,10,4096
Cv 45,45,25
v_add_f32_e32 v15, v15, v46
Mv 124,40,84
Rb 20,10,5120
v_add_f32_e32 v15, v15, v26
Cv 46,46,26
Mv 124,44,92
Rb 24,10,6144
v_add_f32_e32 v15, v15, v47
v_add_f32_e32 v15, v15, v27
Mv 120,44,88
)" R"(
Rb 28,10,7168
Cv 47,47,27
v_max_f32_e32 v11, v14, v15
Mv 120,48,96
Rb 32,10,8192
v_mov_b32 v24, v14
v_mov_b32 v25, v15
Mv 124,48,100
Rb 36,10,9216
v_permlane32_swap_b32_e32 v24, v25
v_add_f32_e32 v24, v24, v25
v_mov_b32 v25, v24
Mv 124,52,108
Rb 40,10,10240
v_permlane16_swap_b32_e32 v24, v25
v_add_f32_e32 v24, v24, v25
Mv 120,52,104
Rb 44,10,11264
buffer_load_dwordx4 v3, s[20:23], s56 offen offset:3072 sc1 lds
Mv 120,56,112
v_rcp_f32_e32 v24, v24
Mv 124,56,116
s_add_u32 s56, s56, 0x10000
v_max_f32_e32 v11, v11, v24
v_cmp_nge_f32_e32 vcc, s62, v11
Mv 124,60,124
s_waitcnt lgkmcnt(8)
s_cbranch_vccnz .Lzfx0
.Lzfr0:
Mv 120,60,120
v_mov_b32 v12, v14
v_mov_b32 v13, v15
v_mfma_f32_16x16x32_bf16 v[128:131], a[0:3], v[32:35], 0
Rb 48,10,12288
v_mfma_f32_16x16x32_bf16 v[136:139], a[0:3], v[36:39], 0
s_cmp_le_i32 s57, s58
s_cbranch_scc1 .Lzmk1
.Lzmr1:
v_mfma_f32_16x16x32_bf16 v[140:143], a[4:7], v[36:39], 0
Rb 52,10,13312
s_add_u32 m0, s36, 32768
v_mfma_f32_16x16x32_bf16 v[132:135], a[4:7], v[32:35], 0
buffer_load_dwordx4 v2, s[20:23], s55 offen offset:0 sc1 lds
v_mfma_f32_16x16x32_bf16 v[144:147], a[8:11], v[32:35], 0
Rb 56,10,14336
s_and_b32 s92, s2, 7
v_mfma_f32_16x16x32_bf16 v[152:155], a[8:11], v[36:39], 0
s_mul_i32 s92, s92, 14
s_lshr_b32 s82, s2, 3
v_mfma_f32_16x16x32_bf16 v[156:159], a[12:15], v[36:39], 0
Rb 60,10,15360
s_waitcnt lgkmcnt(8)
v_mfma_f32_16x16x32_bf16 v[148:151], a[12:15], v[32:35], 0
buffer_load_dwordx4 v2, s[20:23], s55 offen offset:1024 sc1 lds
v_mfma_f32_16x16x32_bf16 v[160:163], a[16:19], v[32:35], 0
Rb 0,10,16384
s_add_u32 s82, s82, s92
v_mfma_f32_16x16x32_bf16 v[168:171], a[16:19], v[36:39], 0
v_exp_f32_e32 v48, v96
v_mfma_f32_16x16x32_bf16 v[172:175], a[20:23], v[36:39], 0
Rb 4,10,17408
v_mfma_f32_16x16x32_bf16 v[164:167], a[20:23], v[32:35], 0
buffer_load_dwordx4 v2, s[20:23], s55 offen offset:2048 sc1 lds
v_mfma_f32_16x16x32_bf16 v[176:179], a[24:27], v[32:35], 0
)" R"(
Rb 8,10,18432
v_exp_f32_e32 v28, v97
v_mfma_f32_16x16x32_bf16 v[184:187], a[24:27], v[36:39], 0
v_exp_f32_e32 v49, v98
v_mfma_f32_16x16x32_bf16 v[188:191], a[28:31], v[36:39], 0
Rb 12,10,19456
s_waitcnt lgkmcnt(8)
v_mfma_f32_16x16x32_bf16 v[180:183], a[28:31], v[32:35], 0
buffer_load_dwordx4 v2, s[20:23], s55 offen offset:3072 sc1 lds
v_add_f32_e32 v14, v48, v28
v_mfma_f32_16x16x32_bf16 v[192:195], a[32:35], v[32:35], 0
Rb 16,10,20480
Cv 48,48,28
v_mfma_f32_16x16x32_bf16 v[200:203], a[32:35], v[36:39], 0
v_exp_f32_e32 v29, v99
v_add_f32_e32 v14, v14, v49
v_mfma_f32_16x16x32_bf16 v[204:207], a[36:39], v[36:39], 0
Rb 20,10,21504
s_add_u32 m0, s36, 36864
v_mfma_f32_16x16x32_bf16 v[196:199], a[36:39], v[32:35], 0
buffer_load_dwordx4 v3, s[20:23], s55 offen offset:0 sc1 lds
v_mfma_f32_16x16x32_bf16 v[208:211], a[40:43], v[32:35], 0
Rb 24,10,22528
v_exp_f32_e32 v50, v104
v_mfma_f32_16x16x32_bf16 v[216:219], a[40:43], v[36:39], 0
s_cmp_gt_u32 s33, 1
s_cselect_b32 s24, s78, s76
s_cselect_b32 s25, s79, s77
v_mfma_f32_16x16x32_bf16 v[220:223], a[44:47], v[36:39], 0
Rb 28,10,23552
s_waitcnt lgkmcnt(8)
v_mfma_f32_16x16x32_bf16 v[212:215], a[44:47], v[32:35], 0
buffer_load_dwordx4 v3, s[20:23], s55 offen offset:1024 sc1 lds
v_add_f32_e32 v14, v14, v29
v_mfma_f32_16x16x32_bf16 v[224:227], a[48:51], v[32:35], 0
Rb 32,10,24576
s_mov_b32 s27, 0x20000
v_mfma_f32_16x16x32_bf16 v[232:235], a[48:51], v[36:39], 0
s_cmp_lt_u32 s35, 7
s_cselect_b32 s26, 0x188000, 0
Cv 49,49,29
v_mfma_f32_16x16x32_bf16 v[236:239], a[52:55], v[36:39], 0
Rb 36,10,25600
s_mul_i32 s83, s83, 802816
v_mfma_f32_16x16x32_bf16 v[228:231], a[52:55], v[32:35], 0
buffer_load_dwordx4 v3, s[20:23], s55 offen offset:2048 sc1 lds
v_mfma_f32_16x16x32_bf16 v[240:243], a[56:59], v[32:35], 0
Rb 40,10,26624
s_lshl_b32 s92, s82, 5
v_add_f32_e32 v14, v14, v50
v_mfma_f32_16x16x32_bf16 v[248:251], a[56:59], v[36:39], 0
s_add_u32 s83, s83, s92
v_and_b32 v11, 62, v0
v_mul_u32_u24 v11, 0x6f0, v11
v_mfma_f32_16x16x32_bf16 v[252:255], a[60:63], v[36:39], 0
Rb 44,10,27648
s_waitcnt lgkmcnt(8)
v_mfma_f32_16x16x32_bf16 v[244:247], a[60:63], v[32:35], 0
buffer_load_dwordx4 v3, s[20:23], s55 offen offset:3072 sc1 lds
Mp 128,0,40
Rb 48,10,28672
v_add_u32 v11, v11, v1
Mp 136,0,44
v_exp_f32_e32 v24, v105
)" R"(
Mp 140,4,44
Rb 52,10,29696
v_add_f32_e32 v14, v14, v24
Mp 132,4,40
v_exp_f32_e32 v51, v106
Mp 144,8,40
Rb 56,10,30720
v_exp_f32_e32 v25, v107
Mp 152,8,44
v_exp_f32_e32 v52, v100
Mp 156,12,44
Rb 60,10,31744
s_waitcnt lgkmcnt(8)
Mp 148,12,40
v_exp_f32_e32 v26, v101
Cv 50,50,24
Mp 160,16,40
v_exp_f32_e32 v53, v102
Mp 168,16,44
v_exp_f32_e32 v27, v103
v_add_f32_e32 v14, v14, v51
Mp 172,20,44
v_exp_f32_e32 v54, v108
Mp 164,20,40
v_exp_f32_e32 v28, v109
Mp 176,24,40
v_exp_f32_e32 v55, v110
v_add_f32_e32 v14, v14, v25
Mp 184,24,44
v_exp_f32_e32 v29, v111
Mp 188,28,44
Cv 51,51,25
v_add_f32_e32 v15, v52, v26
Mp 180,28,40
Cv 52,52,26
s_waitcnt vmcnt(16) lgkmcnt(0)
s_barrier
Mp 192,32,40
Rb 0,1,0
s_add_u32 m0, s36, 131072
s_nop 0
buffer_load_dwordx4 v11, s[24:27], s83 offen offset:0 lds
Mp 200,32,44
Rb 4,1,1024
v_add_f32_e32 v15, v15, v53
v_add_f32_e32 v15, v15, v27
Mp 204,36,44
Rb 8,1,2048
s_add_u32 s83, s83, 0x1bc00
buffer_load_dwordx4 v11, s[24:27], s83 offen offset:1024 lds
Mp 196,36,40
Rb 12,1,3072
Cv 53,53,27
Mp 208,40,40
Rb 16,1,8192
s_add_u32 s83, s83, 0x1bc00
buffer_load_dwordx4 v11, s[24:27], s83 offen offset:2048 lds
Mp 216,40,44
Rb 20,1,9216
v_add_f32_e32 v15, v15, v54
Mp 220,44,44
Rb 24,1,10240
s_add_u32 s83, s83, 0x1bc00
buffer_load_dwordx4 v11, s[24:27], s83 offen offset:3072 lds
Mp 212,44,40
)" R"(
Rb 28,1,11264
v_add_f32_e32 v15, v15, v28
Cv 54,54,28
Mp 224,48,40
Rb 32,1,4096
s_add_u32 m0, s36, 135168
s_add_u32 s83, s83, 0x1cc00
buffer_load_dwordx4 v11, s[24:27], s83 offen offset:0 lds
Mp 232,48,44
Rb 36,1,5120
v_add_f32_e32 v15, v15, v55
v_add_f32_e32 v15, v15, v29
Mp 236,52,44
Rb 40,1,6144
s_add_u32 s83, s83, 0x1bc00
buffer_load_dwordx4 v11, s[24:27], s83 offen offset:1024 lds
Mp 228,52,40
Rb 44,1,7168
Cv 55,55,29
Mp 240,56,40
s_add_u32 s83, s83, 0x1bc00
buffer_load_dwordx4 v11, s[24:27], s83 offen offset:2048 lds
v_cmp_eq_u32_e32 vcc, s3, v4
s_andn2_b64 s[28:29], s[60:61], vcc
s_cbranch_scc1 .Lzsp7
.Lzok7:
Mp 248,56,44
s_sub_u32 s57, s57, 1
Mp 252,60,44
s_waitcnt lgkmcnt(8)
Mp 244,60,40
s_cmp_gt_i32 s57, s93
s_cbranch_scc0 .LzLX
.LzL:
v_mfma_f32_16x16x32_bf16 v[64:67], a[0:3], a[64:67], v[16:19]
Rb 48,1,12288
v_mfma_f32_16x16x32_bf16 v[68:71], a[0:3], a[68:71], v[20:23]
v_exp_f32_e32 v56, v112
Mv 68,4,76
Rb 52,1,13312
s_add_u32 m0, s36, 98304
Mv 64,4,72
buffer_load_dwordx4 v2, s[20:23], s56 offen offset:0 sc1 lds
Mv 64,8,80
Rb 56,1,14336
v_add_f32_e32 v14, v14, v56
Mv 68,8,84
v_exp_f32_e32 v24, v113
Mv 68,12,92
Rb 60,1,15360
s_waitcnt lgkmcnt(8)
Mv 64,12,88
v_exp_f32_e32 v57, v114
Mv 64,16,96
Rb 0,1,16384
v_add_f32_e32 v14, v14, v24
Mv 68,16,100
v_exp_f32_e32 v25, v115
Mv 68,20,108
Rb 4,1,17408
Cv 56,56,24
Mv 64,20,104
buffer_load_dwordx4 v2, s[20:23], s56 offen offset:1024 sc1 lds
Mv 64,24,112
)" R"(
Rb 8,1,18432
v_add_f32_e32 v14, v14, v57
Mv 68,24,116
v_exp_f32_e32 v58, v120
Mv 68,28,124
Rb 12,1,19456
s_waitcnt lgkmcnt(8)
Mv 64,28,120
v_exp_f32_e32 v26, v121
v_mfma_f32_16x16x32_bf16 v[72:75], a[32:35], a[64:67], v[16:19]
Rb 16,1,24576
v_add_f32_e32 v14, v14, v25
v_mfma_f32_16x16x32_bf16 v[76:79], a[32:35], a[68:71], v[20:23]
v_exp_f32_e32 v59, v122
Mv 76,36,76
Rb 20,1,25600
Cv 57,57,25
Mv 72,36,72
buffer_load_dwordx4 v2, s[20:23], s56 offen offset:2048 sc1 lds
Mv 72,40,80
Rb 24,1,26624
v_add_f32_e32 v14, v14, v58
Mv 76,40,84
v_exp_f32_e32 v27, v123
Mv 76,44,92
Rb 28,1,27648
s_waitcnt lgkmcnt(8)
Mv 72,44,88
v_exp_f32_e32 v60, v116
Mv 72,48,96
Rb 32,1,20480
v_add_f32_e32 v14, v14, v26
Mv 76,48,100
v_exp_f32_e32 v28, v117
Mv 76,52,108
Rb 36,1,21504
Cv 58,58,26
Mv 72,52,104
buffer_load_dwordx4 v2, s[20:23], s56 offen offset:3072 sc1 lds
Mv 72,56,112
Rb 40,1,22528
v_add_f32_e32 v14, v14, v59
Mv 76,56,116
v_exp_f32_e32 v61, v118
Mv 76,60,124
Rb 44,1,23552
s_waitcnt lgkmcnt(8)
Mv 72,60,120
v_exp_f32_e32 v29, v119
v_mfma_f32_16x16x32_bf16 v[80:83], a[0:3], a[64:67], v[16:19]
Rb 48,1,28672
v_add_f32_e32 v14, v14, v27
v_mfma_f32_16x16x32_bf16 v[84:87], a[0:3], a[68:71], v[20:23]
v_exp_f32_e32 v62, v124
Mv 84,4,76
Rb 52,1,29696
s_add_u32 m0, s36, 102400
Mv 80,4,72
buffer_load_dwordx4 v3, s[20:23], s56 offen offset:0 sc1 lds
Mv 80,8,80
Rb 56,1,30720
Cv 59,59,27
Mv 84,8,84
v_exp_f32_e32 v24, v125
)" R"(
Mv 84,12,92
Rb 60,1,31744
s_waitcnt lgkmcnt(8)
Mv 80,12,88
v_exp_f32_e32 v63, v126
Mv 80,16,96
v_exp_f32_e32 v25, v127
Mv 84,16,100
v_add_f32_e32 v15, v15, v60
v_add_f32_e32 v15, v15, v28
Mv 84,20,108
Cv 60,60,28
v_add_f32_e32 v15, v15, v61
Mv 80,20,104
buffer_load_dwordx4 v3, s[20:23], s56 offen offset:1024 sc1 lds
Mv 80,24,112
v_add_f32_e32 v15, v15, v29
Cv 61,61,29
Mv 84,24,116
v_add_f32_e32 v15, v15, v62
v_add_f32_e32 v15, v15, v24
Mv 84,28,124
Cv 62,62,24
v_add_f32_e32 v15, v15, v63
Mv 80,28,120
s_waitcnt vmcnt(14) lgkmcnt(0)
s_barrier
v_mfma_f32_16x16x32_bf16 v[88:91], a[32:35], a[64:67], v[16:19]
Rb 0,8,0
v_add_f32_e32 v15, v15, v25
v_mfma_f32_16x16x32_bf16 v[92:95], a[32:35], a[68:71], v[20:23]
Rb 4,8,1024
Cv 63,63,25
Mv 92,36,76
Rb 8,8,2048
v_max_f32_e32 v11, v14, v15
Mv 88,36,72
Rb 12,8,3072
buffer_load_dwordx4 v3, s[20:23], s56 offen offset:2048 sc1 lds
Mv 88,40,80
Rb 16,8,4096
v_cmp_nge_f32_e32 vcc, s62, v11
Mv 92,40,84
Rb 20,8,5120
s_add_u32 s55, s55, 0x10000
Mv 92,44,92
Rb 24,8,6144
Mv 88,44,88
Rb 28,8,7168
Mv 88,48,96
Rb 32,8,8192
Mv 92,48,100
Rb 36,8,9216
Mv 92,52,108
Rb 40,8,10240
Mv 88,52,104
Rb 44,8,11264
buffer_load_dwordx4 v3, s[20:23], s56 offen offset:3072 sc1 lds
Mv 88,56,112
s_cmp_eq_u32 s57, 2
s_cselect_b32 s55, s59, s55
Mv 92,56,116
s_add_u32 s56, s56, 0x10000
Mv 92,60,124
s_waitcnt lgkmcnt(8)
)" R"(
s_cbranch_vccnz .Lzfx1
.Lzfr1:
Mv 88,60,120
v_add_f32_e32 v12, v12, v14
v_add_f32_e32 v13, v13, v15
Mp 128,0,48
Rb 48,8,12288
Mp 136,0,52
s_cmp_le_i32 s57, s58
s_cbranch_scc1 .Lzmk2
.Lzmr2:
Mp 140,4,52
Rb 52,8,13312
s_add_u32 m0, s36, 0
Mp 132,4,48
buffer_load_dwordx4 v2, s[20:23], s55 offen offset:0 sc1 lds
Mp 144,8,48
Rb 56,8,14336
Mp 152,8,52
v_exp_f32_e32 v32, v64
Mp 156,12,52
Rb 60,8,15360
s_waitcnt lgkmcnt(8)
Mp 148,12,48
v_exp_f32_e32 v26, v65
Mp 160,16,48
Rb 0,8,16384
v_add_f32_e32 v14, v32, v26
Mp 168,16,52
v_exp_f32_e32 v33, v66
Mp 172,20,52
Rb 4,8,17408
Cv 32,32,26
Mp 164,20,48
buffer_load_dwordx4 v2, s[20:23], s55 offen offset:1024 sc1 lds
Mp 176,24,48
Rb 8,8,18432
v_add_f32_e32 v14, v14, v33
Mp 184,24,52
v_exp_f32_e32 v27, v67
Mp 188,28,52
Rb 12,8,19456
s_waitcnt lgkmcnt(8)
Mp 180,28,48
v_exp_f32_e32 v34, v72
Mp 192,32,48
Rb 16,8,20480
v_add_f32_e32 v14, v14, v27
Mp 200,32,52
v_exp_f32_e32 v28, v73
Mp 204,36,52
Rb 20,8,21504
Cv 33,33,27
Mp 196,36,48
buffer_load_dwordx4 v2, s[20:23], s55 offen offset:2048 sc1 lds
Mp 208,40,48
Rb 24,8,22528
v_add_f32_e32 v14, v14, v34
Mp 216,40,52
v_exp_f32_e32 v35, v74
Mp 220,44,52
Rb 28,8,23552
s_waitcnt lgkmcnt(8)
Mp 212,44,48
)" R"(
v_exp_f32_e32 v29, v75
Mp 224,48,48
Rb 32,8,24576
v_add_f32_e32 v14, v14, v28
Mp 232,48,52
v_exp_f32_e32 v36, v68
Mp 236,52,52
Rb 36,8,25600
Cv 34,34,28
Mp 228,52,48
buffer_load_dwordx4 v2, s[20:23], s55 offen offset:3072 sc1 lds
Mp 240,56,48
Rb 40,8,26624
v_add_f32_e32 v14, v14, v35
Mp 248,56,52
v_exp_f32_e32 v24, v69
Mp 252,60,52
Rb 44,8,27648
s_waitcnt lgkmcnt(8)
Mp 244,60,48
v_exp_f32_e32 v37, v70
Mp 128,0,56
Rb 48,8,28672
v_add_f32_e32 v14, v14, v29
Mp 136,0,60
v_exp_f32_e32 v25, v71
Mp 140,4,60
Rb 52,8,29696
s_add_u32 m0, s36, 4096
Mp 132,4,56
buffer_load_dwordx4 v3, s[20:23], s55 offen offset:0 sc1 lds
Mp 144,8,56
Rb 56,8,30720
Cv 35,35,29
Mp 152,8,60
v_exp_f32_e32 v38, v76
Mp 156,12,60
Rb 60,8,31744
s_waitcnt lgkmcnt(8)
Mp 148,12,56
v_exp_f32_e32 v26, v77
Mp 160,16,56
v_exp_f32_e32 v39, v78
Mp 168,16,60
v_exp_f32_e32 v27, v79
Mp 172,20,60
v_add_f32_e32 v15, v36, v24
Cv 36,36,24
Mp 164,20,56
buffer_load_dwordx4 v3, s[20:23], s55 offen offset:1024 sc1 lds
Mp 176,24,56
v_add_f32_e32 v15, v15, v37
v_add_f32_e32 v15, v15, v25
Mp 184,24,60
Cv 37,37,25
v_add_f32_e32 v15, v15, v38
Mp 188,28,60
v_add_f32_e32 v15, v15, v26
Cv 38,38,26
Mp 180,28,56
s_waitcnt vmcnt(14) lgkmcnt(0)
s_barrier
Mp 192,32,56
Rb 0,1,32768
v_add_f32_e32 v15, v15, v39
)" R"(
Mp 200,32,60
Rb 4,1,33792
v_add_f32_e32 v15, v15, v27
Mp 204,36,60
Rb 8,1,34816
Cv 39,39,27
Mp 196,36,56
Rb 12,1,35840
buffer_load_dwordx4 v3, s[20:23], s55 offen offset:2048 sc1 lds
Mp 208,40,56
Rb 16,1,40960
s_sub_u32 s57, s57, 1
Mp 216,40,60
Rb 20,1,41984
Mp 220,44,60
Rb 24,1,43008
Mp 212,44,56
Rb 28,1,44032
Mp 224,48,56
Rb 32,1,36864
Mp 232,48,60
Rb 36,1,37888
Mp 236,52,60
Rb 40,1,38912
Mp 228,52,56
Rb 44,1,39936
buffer_load_dwordx4 v3, s[20:23], s55 offen offset:3072 sc1 lds
Mp 240,56,56
Mp 248,56,60
Mp 252,60,60
s_waitcnt lgkmcnt(8)
Mp 244,60,56
v_mfma_f32_16x16x32_bf16 v[96:99], a[0:3], a[64:67], v[16:19]
Rb 48,1,45056
buffer_load_dword v4, v5, s[20:23], s50 offen offset:0 sc0 sc1
v_mfma_f32_16x16x32_bf16 v[100:103], a[0:3], a[68:71], v[20:23]
s_lshl_b64 s[60:61], s[60:61], 8
s_or_b32 s60, s60, 0xff
Mv 100,4,76
Rb 52,1,46080
s_add_u32 m0, s36, 65536
Mv 96,4,72
buffer_load_dwordx4 v2, s[20:23], s56 offen offset:0 sc1 lds
Mv 96,8,80
Rb 56,1,47104
Mv 100,8,84
v_exp_f32_e32 v40, v80
Mv 100,12,92
Rb 60,1,48128
s_waitcnt lgkmcnt(8)
Mv 96,12,88
v_exp_f32_e32 v28, v81
Mv 96,16,96
Rb 0,1,49152
v_add_f32_e32 v14, v14, v40
Mv 100,16,100
v_exp_f32_e32 v41, v82
Mv 100,20,108
Rb 4,1,50176
v_add_f32_e32 v14, v14, v28
Mv 96,20,104
buffer_load_dwordx4 v2, s[20:23], s56 offen offset:1024 sc1 lds
Mv 96,24,112
Rb 8,1,51200
)" R"(
Cv 40,40,28
Mv 100,24,116
v_exp_f32_e32 v29, v83
Mv 100,28,124
Rb 12,1,52224
s_waitcnt lgkmcnt(8)
Mv 96,28,120
v_exp_f32_e32 v42, v88
v_mfma_f32_16x16x32_bf16 v[104:107], a[32:35], a[64:67], v[16:19]
Rb 16,1,57344
v_add_f32_e32 v14, v14, v41
v_mfma_f32_16x16x32_bf16 v[108:111], a[32:35], a[68:71], v[20:23]
v_exp_f32_e32 v24, v89
Mv 108,36,76
Rb 20,1,58368
v_add_f32_e32 v14, v14, v29
Mv 104,36,72
buffer_load_dwordx4 v2, s[20:23], s56 offen offset:2048 sc1 lds
Mv 104,40,80
Rb 24,1,59392
Cv 41,41,29
Mv 108,40,84
v_exp_f32_e32 v43, v90
Mv 108,44,92
Rb 28,1,60416
s_waitcnt lgkmcnt(8)
Mv 104,44,88
v_exp_f32_e32 v25, v91
Mv 104,48,96
Rb 32,1,53248
v_add_f32_e32 v14, v14, v42
Mv 108,48,100
v_exp_f32_e32 v44, v84
Mv 108,52,108
Rb 36,1,54272
v_add_f32_e32 v14, v14, v24
Mv 104,52,104
buffer_load_dwordx4 v2, s[20:23], s56 offen offset:3072 sc1 lds
Mv 104,56,112
Rb 40,1,55296
Cv 42,42,24
Mv 108,56,116
v_exp_f32_e32 v26, v85
Mv 108,60,124
Rb 44,1,56320
s_waitcnt lgkmcnt(8)
Mv 104,60,120
v_exp_f32_e32 v45, v86
v_mfma_f32_16x16x32_bf16 v[112:115], a[0:3], a[64:67], v[16:19]
Rb 48,1,61440
v_add_f32_e32 v14, v14, v43
v_mfma_f32_16x16x32_bf16 v[116:119], a[0:3], a[68:71], v[20:23]
v_exp_f32_e32 v27, v87
Mv 116,4,76
Rb 52,1,62464
s_add_u32 m0, s36, 69632
Mv 112,4,72
buffer_load_dwordx4 v3, s[20:23], s56 offen offset:0 sc1 lds
Mv 112,8,80
Rb 56,1,63488
v_add_f32_e32 v14, v14, v25
Mv 116,8,84
v_exp_f32_e32 v46, v92
Mv 116,12,92
)" R"(
Rb 60,1,64512
s_waitcnt lgkmcnt(8)
Mv 112,12,88
v_exp_f32_e32 v28, v93
Mv 112,16,96
v_exp_f32_e32 v47, v94
Mv 116,16,100
v_exp_f32_e32 v29, v95
Mv 116,20,108
Cv 43,43,25
v_add_f32_e32 v15, v15, v44
Mv 112,20,104
buffer_load_dwordx4 v3, s[20:23], s56 offen offset:1024 sc1 lds
Mv 112,24,112
v_add_f32_e32 v15, v15, v26
Cv 44,44,26
Mv 116,24,116
v_add_f32_e32 v15, v15, v45
v_add_f32_e32 v15, v15, v27
Mv 116,28,124
Cv 45,45,27
v_add_f32_e32 v15, v15, v46
Mv 112,28,120
s_waitcnt vmcnt(15) lgkmcnt(0)
s_barrier
v_mfma_f32_16x16x32_bf16 v[120:123], a[32:35], a[64:67], v[16:19]
Rb 0,8,32768
v_add_f32_e32 v15, v15, v28
v_mfma_f32_16x16x32_bf16 v[124:127], a[32:35], a[68:71], v[20:23]
Rb 4,8,33792
Cv 46,46,28
Mv 124,36,76
Rb 8,8,34816
v_add_f32_e32 v15, v15, v47
Mv 120,36,72
Rb 12,8,35840
buffer_load_dwordx4 v3, s[20:23], s56 offen offset:2048 sc1 lds
Mv 120,40,80
Rb 16,8,36864
v_add_f32_e32 v15, v15, v29
Mv 124,40,84
Rb 20,8,37888
Cv 47,47,29
Mv 124,44,92
Rb 24,8,38912
v_max_f32_e32 v11, v14, v15
Mv 120,44,88
Rb 28,8,39936
v_cmp_nge_f32_e32 vcc, s62, v11
Mv 120,48,96
Rb 32,8,40960
s_add_u32 s55, s55, 0x10000
Mv 124,48,100
Rb 36,8,41984
Mv 124,52,108
Rb 40,8,43008
Mv 120,52,104
Rb 44,8,44032
buffer_load_dwordx4 v3, s[20:23], s56 offen offset:3072 sc1 lds
Mv 120,56,112
s_cmp_eq_u32 s57, 2
s_cselect_b32 s55, s59, s55
Mv 124,56,116
s_add_u32 s56, s56, 0x10000
Mv 124,60,124
)" R"(
s_waitcnt lgkmcnt(8)
s_cbranch_vccnz .Lzfx2
.Lzfr2:
Mv 120,60,120
v_add_f32_e32 v12, v12, v14
v_add_f32_e32 v13, v13, v15
Mp 128,0,32
Rb 48,8,45056
Mp 136,0,36
s_cmp_le_i32 s57, s58
s_cbranch_scc1 .Lzmk3
.Lzmr3:
Mp 140,4,36
Rb 52,8,46080
s_add_u32 m0, s36, 32768
Mp 132,4,32
buffer_load_dwordx4 v2, s[20:23], s55 offen offset:0 sc1 lds
Mp 144,8,32
Rb 56,8,47104
Mp 152,8,36
v_exp_f32_e32 v48, v96
Mp 156,12,36
Rb 60,8,48128
s_waitcnt lgkmcnt(8)
Mp 148,12,32
v_exp_f32_e32 v24, v97
Mp 160,16,32
Rb 0,8,49152
v_add_f32_e32 v14, v48, v24
Mp 168,16,36
v_exp_f32_e32 v49, v98
Mp 172,20,36
Rb 4,8,50176
Cv 48,48,24
Mp 164,20,32
buffer_load_dwordx4 v2, s[20:23], s55 offen offset:1024 sc1 lds
Mp 176,24,32
Rb 8,8,51200
v_add_f32_e32 v14, v14, v49
Mp 184,24,36
v_exp_f32_e32 v25, v99
Mp 188,28,36
Rb 12,8,52224
s_waitcnt lgkmcnt(8)
Mp 180,28,32
v_exp_f32_e32 v50, v104
Mp 192,32,32
Rb 16,8,53248
v_add_f32_e32 v14, v14, v25
Mp 200,32,36
v_exp_f32_e32 v26, v105
Mp 204,36,36
Rb 20,8,54272
Cv 49,49,25
Mp 196,36,32
buffer_load_dwordx4 v2, s[20:23], s55 offen offset:2048 sc1 lds
Mp 208,40,32
Rb 24,8,55296
v_add_f32_e32 v14, v14, v50
Mp 216,40,36
v_exp_f32_e32 v51, v106
Mp 220,44,36
Rb 28,8,56320
s_waitcnt lgkmcnt(8)
)" R"(
Mp 212,44,32
v_exp_f32_e32 v27, v107
Mp 224,48,32
Rb 32,8,57344
v_add_f32_e32 v14, v14, v26
Mp 232,48,36
v_exp_f32_e32 v52, v100
Mp 236,52,36
Rb 36,8,58368
Cv 50,50,26
Mp 228,52,32
buffer_load_dwordx4 v2, s[20:23], s55 offen offset:3072 sc1 lds
Mp 240,56,32
Rb 40,8,59392
v_add_f32_e32 v14, v14, v51
Mp 248,56,36
v_exp_f32_e32 v28, v101
Mp 252,60,36
Rb 44,8,60416
s_waitcnt lgkmcnt(8)
Mp 244,60,32
v_exp_f32_e32 v53, v102
Mp 128,0,40
Rb 48,8,61440
v_add_f32_e32 v14, v14, v27
Mp 136,0,44
v_exp_f32_e32 v29, v103
Mp 140,4,44
Rb 52,8,62464
s_add_u32 m0, s36, 36864
Mp 132,4,40
buffer_load_dwordx4 v3, s[20:23], s55 offen offset:0 sc1 lds
Mp 144,8,40
Rb 56,8,63488
Cv 51,51,27
Mp 152,8,44
v_exp_f32_e32 v54, v108
Mp 156,12,44
Rb 60,8,64512
s_waitcnt lgkmcnt(8)
Mp 148,12,40
v_exp_f32_e32 v24, v109
Mp 160,16,40
v_exp_f32_e32 v55, v110
Mp 168,16,44
v_exp_f32_e32 v25, v111
Mp 172,20,44
v_add_f32_e32 v15, v52, v28
Cv 52,52,28
Mp 164,20,40
buffer_load_dwordx4 v3, s[20:23], s55 offen offset:1024 sc1 lds
Mp 176,24,40
v_add_f32_e32 v15, v15, v53
v_add_f32_e32 v15, v15, v29
Mp 184,24,44
Cv 53,53,29
v_add_f32_e32 v15, v15, v54
Mp 188,28,44
v_add_f32_e32 v15, v15, v24
Cv 54,54,24
Mp 180,28,40
s_waitcnt vmcnt(14) lgkmcnt(0)
s_barrier
Mp 192,32,40
Rb 0,1,0
)" R"(
v_add_f32_e32 v15, v15, v55
Mp 200,32,44
Rb 4,1,1024
v_add_f32_e32 v15, v15, v25
Mp 204,36,44
Rb 8,1,2048
Cv 55,55,25
Mp 196,36,40
Rb 12,1,3072
buffer_load_dwordx4 v3, s[20:23], s55 offen offset:2048 sc1 lds
Mp 208,40,40
Rb 16,1,8192
s_sub_u32 s57, s57, 1
Mp 216,40,44
Rb 20,1,9216
Mp 220,44,44
Rb 24,1,10240
Mp 212,44,40
Rb 28,1,11264
Mp 224,48,40
Rb 32,1,4096
Mp 232,48,44
Rb 36,1,5120
Mp 236,52,44
Rb 40,1,6144
Mp 228,52,40
Rb 44,1,7168
buffer_load_dwordx4 v3, s[20:23], s55 offen offset:3072 sc1 lds
Mp 240,56,40
v_cmp_eq_u32_e32 vcc, s3, v4
s_andn2_b64 s[28:29], s[60:61], vcc
s_cbranch_scc1 .Lzsp8
.Lzok8:
Mp 248,56,44
Mp 252,60,44
s_waitcnt lgkmcnt(8)
Mp 244,60,40
s_cmp_gt_i32 s57, s93
s_cbranch_scc1 .LzL
.LzLX:
s_cmp_eq_u32 s94, 0
s_cbranch_scc1 .LzEB
s_cmp_eq_u32 s94, 2
s_cbranch_scc1 .LzDA
s_lshl1_add_u32 s57, s35, 2
s_mov_b32 s59, s67
s_add_u32 s56, s89, 0x2000000
s_mov_b32 s94, 2
Mz 64,0,224
Rb 48,1,12288
v_lshrrev_b32 v4, 4, v0
v_accvgpr_mov_b32 a64, a224
Mz 68,0,228
v_lshlrev_b32 v4, 4, v4
v_add_lshl_u32 v4, v4, v0, 3
v_accvgpr_mov_b32 a65, a225
Mv 68,4,236
Rb 52,1,13312
s_add_u32 m0, s36, 98304
v_accvgpr_mov_b32 a66, a226
Mv 64,4,232
buffer_load_dwordx4 v2, s[20:23], s56 offen offset:0 sc1 lds
v_accvgpr_mov_b32 a67, a227
Mv 64,8,240
)" R"(
Rb 56,1,14336
s_add_u32 s92, s36, 131072
v_accvgpr_mov_b32 a68, a228
Mv 68,8,244
v_add_u32 v4, s92, v4
v_accvgpr_mov_b32 a69, a229
s_waitcnt vmcnt(1)
Mv 68,12,252
Rb 60,1,15360
v_accvgpr_mov_b32 a70, a230
s_waitcnt lgkmcnt(8)
Mv 64,12,248
buffer_load_dwordx4 v2, s[20:23], s56 offen offset:1024 sc1 lds
Rt 156,4,0
Mv 64,16,192
Rb 0,1,16384
Rt 158,4,128
v_accvgpr_mov_b32 a71, a231
Mv 68,16,196
Rt 160,4,1024
v_exp_f32_e32 v56, v112
Mv 68,20,204
Rb 4,1,17408
Rt 162,4,1152
v_add_f32_e32 v14, v14, v56
Mv 64,20,200
buffer_load_dwordx4 v2, s[20:23], s56 offen offset:2048 sc1 lds
Rt 164,4,2048
Mv 64,24,208
Rb 8,1,18432
Rt 166,4,2176
v_accvgpr_mov_b32 a72, a232
Mv 68,24,212
Rt 168,4,3072
v_exp_f32_e32 v26, v113
Mv 68,28,220
Rb 12,1,19456
Rt 170,4,3200
Mv 64,28,216
buffer_load_dwordx4 v2, s[20:23], s56 offen offset:3072 sc1 lds
Rt 172,4,4096
Mz 72,32,224
Rb 16,1,24576
Rt 174,4,4224
v_accvgpr_mov_b32 a73, a233
Mz 76,32,228
Rt 176,4,5120
v_exp_f32_e32 v57, v114
Mv 76,36,236
Rb 20,1,25600
s_add_u32 m0, s36, 102400
Rt 178,4,5248
Mv 72,36,232
buffer_load_dwordx4 v3, s[20:23], s56 offen offset:0 sc1 lds
Rt 180,4,6144
Mv 72,40,240
Rb 24,1,26624
Rt 182,4,6272
v_add_f32_e32 v14, v14, v26
Mv 76,40,244
v_exp_f32_e32 v27, v115
Cv 56,56,26
Mv 76,44,252
Rb 28,1,27648
)" R"(
v_add_f32_e32 v14, v14, v57
Mv 72,44,248
buffer_load_dwordx4 v3, s[20:23], s56 offen offset:1024 sc1 lds
v_add_f32_e32 v14, v14, v27
Mv 72,48,192
Rb 32,1,20480
v_and_b32 v4, 14, v0
v_accvgpr_mov_b32 a74, a234
Mv 76,48,196
v_min_u32 v4, 1, v4
v_lshlrev_b32 v4, 8, v4
Cv 57,57,27
Mv 76,52,204
Rb 36,1,21504
v_and_or_b32 v4, v0, 48, v4
v_accvgpr_mov_b32 a75, a235
Mv 72,52,200
buffer_load_dwordx4 v3, s[20:23], s56 offen offset:2048 sc1 lds
v_accvgpr_mov_b32 a76, a236
Mv 72,56,208
Rb 40,1,22528
v_lshlrev_b32 v4, 1, v4
v_accvgpr_mov_b32 a77, a237
Mv 76,56,212
v_and_or_b32 v4, v1, 16, v4
s_lshl_b32 s92, s33, 7
v_accvgpr_mov_b32 a78, a238
Mv 76,60,220
Rb 44,1,23552
v_accvgpr_mov_b32 a79, a239
s_waitcnt lgkmcnt(5)
Mv 72,60,216
v_mfma_f32_16x16x32_bf16 a[184:187], a[156:159], a[128:131], 0
buffer_load_dwordx4 v3, s[20:23], s56 offen offset:3072 sc1 lds
v_accvgpr_mov_b32 a80, a240
Mz 80,0,224
Ma 184,160,132
Rb 48,1,28672
s_add_u32 s92, s92, 162816
v_accvgpr_mov_b32 a81, a241
Mz 84,0,228
Ma 184,164,136
v_add_u32 v4, s92, v4
v_exp_f32_e32 v58, v120
Mv 84,4,236
Ma 184,168,140
Rb 52,1,29696
v_exp_f32_e32 v28, v121
Mv 80,4,232
Ma 184,172,144
v_exp_f32_e32 v59, v122
v_add_f32_e32 v14, v14, v58
Mv 80,8,240
Ma 184,176,148
Rb 56,1,30720
s_mov_b32 s24, s70
s_mov_b32 s25, s71
Mv 84,8,244
Ma 184,180,152
s_mul_i32 s83, s82, 14336
s_lshl_b32 s92, s33, 12
v_add_f32_e32 v14, v14, v28
Mv 84,12,252
Rb 60,1,31744
)" R"(
Cv 58,58,28
s_waitcnt lgkmcnt(8)
Mv 80,12,248
s_add_u32 s83, s83, s92
s_add_u32 m0, s92, 131072
v_add_f32_e32 v14, v14, v59
Mv 80,16,192
v_exp_f32_e32 v29, v123
v_accvgpr_mov_b32 a82, a242
Mv 84,16,196
v_exp_f32_e32 v60, v116
v_add_f32_e32 v14, v14, v29
Mv 84,20,204
ds_write_b128 v4, a[184:187]
v_and_b32 v4, 0x70, v4
Cv 59,59,29
Mv 80,20,200
v_add_u32 v4, 0x27c00, v4
v_exp_f32_e32 v24, v117
Mv 80,24,208
v_exp_f32_e32 v61, v118
v_add_f32_e32 v15, v15, v60
Mv 84,24,212
v_exp_f32_e32 v25, v119
v_add_f32_e32 v15, v15, v24
Mv 84,28,220
v_exp_f32_e32 v62, v124
Cv 60,60,24
Mv 80,28,216
v_add_f32_e32 v15, v15, v61
s_waitcnt lgkmcnt(0)
s_barrier
Mz 88,32,224
Rb 0,8,0
v_add_f32_e32 v15, v15, v25
v_accvgpr_mov_b32 a83, a243
Mz 92,32,228
Rb 4,8,16384
buffer_load_dwordx4 v1, s[24:27], s83 offen offset:0 lds
Mv 92,36,236
Rb 8,8,1024
v_exp_f32_e32 v26, v125
Mv 88,36,232
Rb 12,8,17408
v_exp_f32_e32 v63, v126
Mv 88,40,240
Rb 16,8,2048
buffer_load_dwordx4 v1, s[24:27], s83 offen offset:1024 lds
Mv 92,40,244
Rb 20,8,18432
v_exp_f32_e32 v27, v127
Mv 92,44,252
Rb 24,8,3072
Cv 61,61,25
v_add_f32_e32 v15, v15, v62
Mv 88,44,248
Rb 28,8,19456
v_add_f32_e32 v15, v15, v26
Cv 62,62,26
Mv 88,48,192
Rb 32,8,4096
v_add_f32_e32 v15, v15, v63
v_accvgpr_mov_b32 a84, a244
Mv 92,48,196
Rb 36,8,20480
)" R"(
buffer_load_dwordx4 v1, s[24:27], s83 offen offset:2048 lds
Mv 92,52,204
Rb 40,8,5120
v_add_f32_e32 v15, v15, v27
Cv 63,63,27
Mv 88,52,200
Rb 44,8,21504
v_max_f32_e32 v11, v14, v15
v_cmp_nge_f32_e32 vcc, s62, v11
Mv 88,56,208
buffer_load_dwordx4 v1, s[24:27], s83 offen offset:3072 lds
s_add_u32 s55, s55, 0x10000
Mv 92,56,212
s_add_u32 s56, s56, 0x10000
s_cmp_eq_u32 s57, 2
s_cselect_b32 s55, s59, s55
Mv 92,60,220
v_accvgpr_mov_b32 a85, a245
s_waitcnt lgkmcnt(8)
s_cbranch_vccnz .Lzfx3
.Lzfr3:
Mv 88,60,216
v_add_f32_e32 v12, v12, v14
v_add_f32_e32 v13, v13, v15
v_mov_b32 v14, v16
Mp 128,0,48
Rb 48,8,6144
v_mov_b32 v15, v20
v_accvgpr_mov_b32 a86, a246
Mp 136,0,52
buffer_store_dwordx4 v[12:15], v1, s[20:23], s66 offen sc0 sc1
s_cmp_le_i32 s57, s58
s_cbranch_scc1 .Lzmk4
.Lzmr4:
Mp 136,4,60
Rb 52,8,22528
s_add_u32 m0, s36, 0
v_accvgpr_mov_b32 a87, a247
Mp 128,4,56
buffer_load_dwordx4 v2, s[20:23], s55 offen offset:0 sc1 lds
v_accvgpr_mov_b32 a88, a248
Mp 132,8,48
Rb 56,8,7168
v_exp_f32_e32 v32, v64
Mp 140,8,52
v_exp_f32_e32 v28, v65
v_accvgpr_mov_b32 a89, a249
Mp 140,12,60
Rb 60,8,23552
v_exp_f32_e32 v33, v66
s_waitcnt lgkmcnt(8)
Mp 132,12,56
v_exp_f32_e32 v29, v67
v_add_f32_e32 v14, v32, v28
Mp 144,16,48
Rb 0,8,8192
v_exp_f32_e32 v34, v72
Mp 152,16,52
v_exp_f32_e32 v24, v73
v_exp_f32_e32 v35, v74
Mp 152,20,60
Rb 4,8,24576
v_exp_f32_e32 v25, v75
Mp 144,20,56
)" R"(
buffer_load_dwordx4 v2, s[20:23], s55 offen offset:1024 sc1 lds
Cv 128,128,129
Mp 148,24,48
Rb 8,8,9216
Cv 129,130,131
Cv 130,132,133
Cv 32,32,28
Mp 156,24,52
Cv 131,134,135
Cv 136,136,137
v_add_f32_e32 v14, v14, v33
Mp 156,28,60
Rb 12,8,25600
Cv 137,138,139
s_waitcnt lgkmcnt(8)
Mp 148,28,56
Cv 138,140,141
Cv 139,142,143
v_exp_f32_e32 v36, v68
Mp 160,32,48
Rb 16,8,10240
v_exp_f32_e32 v26, v69
Mp 168,32,52
v_exp_f32_e32 v37, v70
v_add_f32_e32 v14, v14, v29
Mp 168,36,60
Rb 20,8,26624
v_exp_f32_e32 v27, v71
Cv 33,33,29
Mp 160,36,56
buffer_load_dwordx4 v2, s[20:23], s55 offen offset:2048 sc1 lds
Cv 144,144,145
Mp 164,40,48
Rb 24,8,11264
Cv 145,146,147
Cv 146,148,149
Mp 172,40,52
buffer_store_dwordx4 v[128:131], v2, s[20:23], s64 offen offset:0 sc0 sc1
v_add_f32_e32 v14, v14, v34
Mp 172,44,60
Rb 28,8,27648
Cv 147,150,151
v_add_f32_e32 v14, v14, v24
s_waitcnt lgkmcnt(8)
Mp 164,44,56
Cv 152,152,153
Cv 153,154,155
Cv 154,156,157
Mp 176,48,48
Rb 32,8,12288
Cv 155,158,159
Cv 34,34,24
Mp 184,48,52
v_exp_f32_e32 v38, v76
v_exp_f32_e32 v28, v77
Mp 184,52,60
Rb 36,8,28672
v_exp_f32_e32 v39, v78
Mp 176,52,56
buffer_load_dwordx4 v2, s[20:23], s55 offen offset:3072 sc1 lds
Cv 160,160,161
Mp 180,56,48
Rb 40,8,13312
Cv 161,162,163
)" R"(
Cv 162,164,165
v_add_f32_e32 v14, v14, v35
Mp 188,56,52
buffer_store_dwordx4 v[136:139], v2, s[20:23], s64 offen offset:1024 sc0 sc1
v_add_f32_e32 v14, v14, v25
Mp 188,60,60
Rb 44,8,29696
Cv 163,166,167
s_waitcnt lgkmcnt(8)
Mp 180,60,56
Cv 168,168,169
Cv 169,170,171
Cv 170,172,173
Cv 35,35,25
Mp 192,0,48
Rb 48,8,14336
Cv 171,174,175
v_add_f32_e32 v15, v36, v26
Mp 200,0,52
buffer_store_dwordx4 v[144:147], v2, s[20:23], s64 offen offset:2048 sc0 sc1
Cv 36,36,26
Mp 200,4,60
Rb 52,8,30720
s_add_u32 m0, s36, 4096
v_exp_f32_e32 v29, v79
Mp 192,4,56
buffer_load_dwordx4 v3, s[20:23], s55 offen offset:0 sc1 lds
Cv 176,176,177
Mp 196,8,48
Rb 56,8,15360
Cv 177,178,179
Cv 178,180,181
Mp 204,8,52
Cv 179,182,183
Cv 184,184,185
v_add_f32_e32 v15, v15, v37
Mp 204,12,60
Rb 60,8,31744
Cv 185,186,187
v_add_f32_e32 v15, v15, v27
s_waitcnt lgkmcnt(8)
Mp 196,12,56
buffer_store_dwordx4 v[152:155], v2, s[20:23], s64 offen offset:3072 sc0 sc1
Cv 186,188,189
Mp 208,16,48
ds_read_b128 v[132:135], v4 offset:0
Cv 187,190,191
Cv 37,37,27
Mp 216,16,52
ds_read_b128 v[140:143], v4 offset:128
v_add_f32_e32 v15, v15, v38
v_add_f32_e32 v15, v15, v28
Cv 38,38,28
Mp 216,20,60
ds_read_b128 v[148:151], v4 offset:256
v_add_f32_e32 v15, v15, v39
v_add_f32_e32 v15, v15, v29
Mp 208,20,56
buffer_load_dwordx4 v3, s[20:23], s55 offen offset:1024 sc1 lds
ds_read_b128 v[156:159], v4 offset:384
Mp 212,24,48
Cv 192,192,193
Cv 193,194,195
Cv 194,196,197
)" R"(
Cv 39,39,29
Mp 220,24,52
Cv 195,198,199
Cv 200,200,201
s_sub_u32 s57, s57, 1
Mp 220,28,60
buffer_store_dwordx4 v[160:163], v3, s[20:23], s64 offen offset:0 sc0 sc1
Cv 201,202,203
Mp 212,28,56
Cv 202,204,205
v_mov_b32 v16, 0
s_waitcnt lgkmcnt(0)
s_barrier
Mp 224,32,48
Rb 0,1,32768
v_add_f32_e32 v132, v132, v140
v_mov_b32 v17, 0
Mp 232,32,52
Rb 4,1,33792
v_add_f32_e32 v133, v133, v141
Cv 203,206,207
Mp 232,36,60
Rb 8,1,34816
v_add_f32_e32 v134, v134, v142
v_mov_b32 v18, 0
v_mov_b32 v19, 0
Mp 224,36,56
Rb 12,1,35840
buffer_load_dwordx4 v3, s[20:23], s55 offen offset:2048 sc1 lds
Mp 228,40,48
Rb 16,1,40960
v_add_f32_e32 v135, v135, v143
buffer_store_dwordx4 v[168:171], v3, s[20:23], s64 offen offset:1024 sc0 sc1
Mp 236,40,52
Rb 20,1,41984
v_add_f32_e32 v148, v148, v156
v_mov_b32 v20, 0
Mp 236,44,60
Rb 24,1,43008
v_add_f32_e32 v149, v149, v157
Cv 208,208,209
v_mov_b32 v21, 0
Mp 228,44,56
Rb 28,1,44032
v_add_f32_e32 v150, v150, v158
v_add_f32_e32 v151, v151, v159
Mp 240,48,48
Rb 32,1,36864
Cv 132,132,132
v_mov_b32 v22, 0
Mp 248,48,52
Rb 36,1,37888
v_lshlrev_b32 v132, 16, v132
Cv 209,210,211
v_mov_b32 v23, 0
Mp 248,52,60
Rb 40,1,38912
Cv 148,148,148
buffer_store_dwordx4 v[176:179], v3, s[20:23], s64 offen offset:2048 sc0 sc1
Mp 240,52,56
Rb 44,1,39936
buffer_load_dwordx4 v3, s[20:23], s55 offen offset:3072 sc1 lds
Mp 244,56,48
v_lshlrev_b32 v148, 16, v148
Cv 133,133,133
)" R"(
Cv 210,212,213
Mp 252,56,52
v_lshlrev_b32 v133, 16, v133
Cv 149,149,149
v_accvgpr_mov_b32 a90, a250
Mp 252,60,60
v_lshlrev_b32 v149, 16, v149
Cv 211,214,215
s_waitcnt lgkmcnt(8)
Mp 244,60,56
Cv 134,134,134
v_lshlrev_b32 v134, 16, v134
Cv 150,150,150
Mz 96,0,224
Rb 48,1,45056
buffer_load_dword v4, v5, s[20:23], s50 offen offset:0 sc0 sc1
buffer_store_dwordx4 v[184:187], v3, s[20:23], s64 offen offset:3072 sc0 sc1
Mz 100,0,228
s_lshl_b64 s[60:61], s[60:61], 8
s_or_b32 s60, s60, 0xff
Cv 216,216,217
Mv 100,4,236
Rb 52,1,46080
s_add_u32 m0, s36, 65536
v_exp_f32_e32 v40, v80
Mv 96,4,232
buffer_load_dwordx4 v2, s[20:23], s56 offen offset:0 sc1 lds
Cv 217,218,219
v_add_f32_e32 v14, v14, v40
Mv 96,8,240
Rb 56,1,47104
v_lshlrev_b32 v150, 16, v150
Cv 218,220,221
Mv 100,8,244
Cv 135,135,135
v_lshlrev_b32 v135, 16, v135
v_exp_f32_e32 v24, v81
Mv 100,12,252
Rb 60,1,48128
s_add_u32 s64, s64, 0x10000
buffer_store_dwordx4 v[192:195], v2, s[20:23], s64 offen offset:0 sc0 sc1
s_waitcnt lgkmcnt(8)
Mv 96,12,248
Cv 151,151,151
v_lshlrev_b32 v151, 16, v151
v_mul_f32_e32 v140, 0xbfb8aa3b, v132
v_add_f32_e32 v14, v14, v24
Mv 96,16,192
Rb 0,1,49152
v_mul_f32_e32 v141, 0xbfb8aa3b, v133
v_exp_f32_e32 v41, v82
Mv 100,16,196
v_exp_f32_e32 v140, v140
Cv 219,222,223
Cv 40,40,24
Mv 100,20,204
Rb 4,1,50176
v_mul_f32_e32 v142, 0xbfb8aa3b, v134
v_exp_f32_e32 v25, v83
Mv 96,20,200
buffer_load_dwordx4 v2, s[20:23], s56 offen offset:1024 sc1 lds
Cv 224,224,225
v_add_f32_e32 v14, v14, v41
Mv 96,24,208
)" R"(
Rb 8,1,51200
v_mul_f32_e32 v143, 0xbfb8aa3b, v135
buffer_store_dwordx4 v[200:203], v2, s[20:23], s64 offen offset:1024 sc0 sc1
Mv 100,24,212
v_exp_f32_e32 v141, v141
v_exp_f32_e32 v42, v88
Mv 100,28,220
Rb 12,1,52224
Cv 225,226,227
v_add_f32_e32 v14, v14, v25
s_waitcnt lgkmcnt(8)
Mv 96,28,216
v_exp_f32_e32 v142, v142
v_add_f32_e32 v140, 1.0, v140
Cv 41,41,25
Mz 104,32,224
Rb 16,1,57344
v_add_f32_e32 v141, 1.0, v141
v_add_f32_e32 v14, v14, v42
Mz 108,32,228
v_exp_f32_e32 v143, v143
Cv 226,228,229
Mv 108,36,236
Rb 20,1,58368
v_add_f32_e32 v142, 1.0, v142
buffer_store_dwordx4 v[208:211], v2, s[20:23], s64 offen offset:2048 sc0 sc1
Mv 104,36,232
buffer_load_dwordx4 v2, s[20:23], s56 offen offset:2048 sc1 lds
Cv 227,230,231
Mv 104,40,240
Rb 24,1,59392
v_add_f32_e32 v143, 1.0, v143
Cv 232,232,233
Mv 108,40,244
v_rcp_f32_e32 v140, v140
v_exp_f32_e32 v26, v89
Mv 108,44,252
Rb 28,1,60416
Cv 233,234,235
v_add_f32_e32 v14, v14, v26
s_waitcnt lgkmcnt(8)
Mv 104,44,248
v_rcp_f32_e32 v141, v141
v_mul_f32_e32 v132, v132, v140
Cv 42,42,26
Mv 104,48,192
Rb 32,1,53248
v_mul_f32_e32 v133, v133, v141
buffer_store_dwordx4 v[216:219], v2, s[20:23], s64 offen offset:3072 sc0 sc1
Mv 108,48,196
v_rcp_f32_e32 v142, v142
Cv 234,236,237
Mv 108,52,204
Rb 36,1,54272
v_mul_f32_e32 v134, v134, v142
v_exp_f32_e32 v43, v90
Mv 104,52,200
buffer_load_dwordx4 v2, s[20:23], s56 offen offset:3072 sc1 lds
Cv 235,238,239
v_add_f32_e32 v14, v14, v43
Mv 104,56,208
Rb 40,1,55296
Cv 132,132,132
Cv 240,240,241
)" R"(
Mv 108,56,212
v_rcp_f32_e32 v143, v143
v_exp_f32_e32 v27, v91
Mv 108,60,220
Rb 44,1,56320
buffer_store_dwordx4 v[224:227], v3, s[20:23], s64 offen offset:0 sc0 sc1
s_waitcnt lgkmcnt(8)
Mv 104,60,216
v_mul_f32_e32 v135, v135, v143
v_lshlrev_b32 v132, 16, v132
Cv 133,133,133
v_add_f32_e32 v14, v14, v27
Mz 112,0,224
Rb 48,1,61440
v_lshlrev_b32 v133, 16, v133
Cv 43,43,27
Mz 116,0,228
Cv 134,134,134
v_lshlrev_b32 v134, 16, v134
Cv 241,242,243
Mv 116,4,236
Rb 52,1,62464
s_add_u32 m0, s36, 69632
v_exp_f32_e32 v44, v84
Mv 112,4,232
buffer_load_dwordx4 v3, s[20:23], s56 offen offset:0 sc1 lds
Cv 242,244,245
v_add_f32_e32 v15, v15, v44
Mv 112,8,240
Rb 56,1,63488
Cv 135,135,135
buffer_store_dwordx4 v[232:235], v3, s[20:23], s64 offen offset:1024 sc0 sc1
Mv 116,8,244
v_lshlrev_b32 v135, 16, v135
v_mul_f32_e32 v132, v132, v148
v_exp_f32_e32 v28, v85
Mv 116,12,252
Rb 60,1,64512
Cv 243,246,247
v_add_f32_e32 v15, v15, v28
s_waitcnt lgkmcnt(8)
Mv 112,12,248
v_mul_f32_e32 v133, v133, v149
v_mul_f32_e32 v134, v134, v150
v_mul_f32_e32 v135, v135, v151
Cv 44,44,28
Mv 112,16,192
Cv 140,132,133
Cv 141,134,135
v_exp_f32_e32 v45, v86
Mv 116,16,196
v_accvgpr_write_b32 a188, v140
v_accvgpr_write_b32 a189, v141
Cv 248,248,249
v_add_f32_e32 v15, v15, v45
Mv 116,20,204
v_lshrrev_b32 v156, 2, v0
v_mul_u32_u24 v156, 864, v156
buffer_store_dwordx4 v[240:243], v3, s[20:23], s64 offen offset:2048 sc0 sc1
Mv 112,20,200
buffer_load_dwordx4 v3, s[20:23], s56 offen offset:1024 sc1 lds
Cv 249,250,251
Mv 112,24,208
v_lshl_add_u32 v156, v0, 3, v156
)" R"(
s_mul_i32 s92, s33, 224
Cv 250,252,253
Mv 116,24,212
s_add_u32 s92, s92, 131072
v_add_u32 v156, s92, v156
v_exp_f32_e32 v29, v87
Mv 116,28,220
Cv 251,254,255
v_exp_f32_e32 v46, v92
v_add_f32_e32 v15, v15, v29
Mv 112,28,216
v_exp_f32_e32 v24, v93
s_waitcnt vmcnt(31) lgkmcnt(0)
s_barrier
Mz 120,32,224
Rb 0,8,32768
Rt 156,156,0
buffer_store_dwordx4 v[248:251], v3, s[20:23], s64 offen offset:3072 sc0 sc1
Mz 124,32,228
Rb 4,8,33792
Rt 158,156,32
v_exp_f32_e32 v47, v94
Mv 124,36,236
Rb 8,8,34816
Rt 160,156,64
v_exp_f32_e32 v25, v95
Mv 120,36,232
Rb 12,8,35840
buffer_load_dwordx4 v3, s[20:23], s56 offen offset:2048 sc1 lds
Rt 162,156,96
Mv 120,40,240
Rb 16,8,36864
Rt 164,156,128
Cv 45,45,29
v_add_f32_e32 v15, v15, v46
Mv 124,40,244
Rb 20,8,37888
Rt 166,156,160
v_add_f32_e32 v15, v15, v24
Cv 46,46,24
Mv 124,44,252
Rb 24,8,38912
Rt 168,156,192
v_add_f32_e32 v15, v15, v47
v_add_f32_e32 v15, v15, v25
Mv 120,44,248
Rb 28,8,39936
Cv 47,47,25
v_max_f32_e32 v11, v14, v15
v_mov_b32 v24, v14
Mv 120,48,192
Rb 32,8,40960
v_mov_b32 v25, v15
s_add_u32 s55, s55, 0x10000
Mv 124,48,196
Rb 36,8,41984
s_cmp_eq_u32 s57, 2
s_cselect_b32 s55, s59, s55
v_permlane32_swap_b32_e32 v24, v25
Mv 124,52,204
Rb 40,8,43008
v_add_f32_e32 v24, v24, v25
v_mov_b32 v25, v24
Mv 120,52,200
Rb 44,8,44032
)" R"(
buffer_load_dwordx4 v3, s[20:23], s56 offen offset:3072 sc1 lds
v_permlane16_swap_b32_e32 v24, v25
Mv 120,56,208
v_add_f32_e32 v24, v24, v25
v_rcp_f32_e32 v24, v24
Mv 124,56,212
s_add_u32 s56, s56, 0x10000
v_max_f32_e32 v11, v11, v24
v_cmp_nge_f32_e32 vcc, s62, v11
Mv 124,60,220
v_accvgpr_mov_b32 a91, a251
s_waitcnt lgkmcnt(12)
s_cbranch_vccnz .Lzfx4
.Lzfr4:
Mv 120,60,216
v_mov_b32 v12, v14
v_mov_b32 v13, v15
v_accvgpr_mov_b32 a92, a252
v_mfma_f32_16x16x32_bf16 v[128:131], a[0:3], v[32:35], 0
Rb 48,8,45056
v_accvgpr_mov_b32 a93, a253
v_accvgpr_mov_b32 a94, a254
v_mfma_f32_16x16x32_bf16 v[136:139], a[0:3], v[36:39], 0
v_accvgpr_mov_b32 a95, a255
s_cmp_le_i32 s57, s58
s_cbranch_scc1 .Lzmk5
.Lzmr5:
v_mfma_f32_16x16x32_bf16 v[140:143], a[4:7], v[36:39], 0
Rb 52,8,46080
s_add_u32 m0, s36, 32768
v_accvgpr_mov_b32 a96, a192
v_mfma_f32_16x16x32_bf16 v[132:135], a[4:7], v[32:35], 0
buffer_load_dwordx4 v2, s[20:23], s55 offen offset:0 sc1 lds
v_accvgpr_mov_b32 a97, a193
v_mfma_f32_16x16x32_bf16 v[144:147], a[8:11], v[32:35], 0
Rb 56,8,47104
v_and_b32 v11, 14, v0
v_accvgpr_mov_b32 a98, a194
v_mfma_f32_16x16x32_bf16 v[152:155], a[8:11], v[36:39], 0
v_min_u32 v11, 1, v11
v_lshlrev_b32 v11, 13, v11
v_accvgpr_mov_b32 a99, a195
v_mfma_f32_16x16x32_bf16 v[156:159], a[12:15], v[36:39], 0
Rb 60,8,48128
v_accvgpr_mov_b32 a100, a196
s_waitcnt lgkmcnt(8)
v_mfma_f32_16x16x32_bf16 v[148:151], a[12:15], v[32:35], 0
v_mfma_f32_16x16x16_bf16 a[128:131], a[156:157], a[188:189], 0
v_and_or_b32 v11, v0, 1, v11
v_mul_u32_u24 v11, 0x31000, v11
v_accvgpr_mov_b32 a101, a197
v_mfma_f32_16x16x32_bf16 v[160:163], a[16:19], v[32:35], 0
v_mfma_f32_16x16x16_bf16 a[132:135], a[158:159], a[188:189], 0
Rb 0,8,49152
v_and_or_b32 v11, v0, 48, v11
v_accvgpr_mov_b32 a102, a198
v_mfma_f32_16x16x32_bf16 v[168:171], a[16:19], v[36:39], 0
v_mfma_f32_16x16x16_bf16 a[136:139], a[160:161], a[188:189], 0
s_mul_i32 s83, s33, 50176
s_lshl_b32 s92, s82, 6
v_accvgpr_mov_b32 a103, a199
v_mfma_f32_16x16x32_bf16 v[172:175], a[20:23], v[36:39], 0
v_mfma_f32_16x16x16_bf16 a[140:143], a[162:163], a[188:189], 0
Rb 4,8,50176
)" R"(
s_add_u32 s83, s83, s92
v_accvgpr_mov_b32 a104, a200
v_mfma_f32_16x16x32_bf16 v[164:167], a[20:23], v[32:35], 0
v_mfma_f32_16x16x16_bf16 a[144:147], a[164:165], a[188:189], 0
buffer_load_dwordx4 v2, s[20:23], s55 offen offset:1024 sc1 lds
v_accvgpr_mov_b32 a105, a201
v_mfma_f32_16x16x32_bf16 v[176:179], a[24:27], v[32:35], 0
v_mfma_f32_16x16x16_bf16 a[148:151], a[166:167], a[188:189], 0
Rb 8,8,51200
s_add_u32 s83, s83, 0xc150000
v_accvgpr_mov_b32 a106, a202
v_mfma_f32_16x16x32_bf16 v[184:187], a[24:27], v[36:39], 0
v_mfma_f32_16x16x16_bf16 a[152:155], a[168:169], a[188:189], 0
v_exp_f32_e32 v48, v96
v_accvgpr_mov_b32 a107, a203
v_mfma_f32_16x16x32_bf16 v[188:191], a[28:31], v[36:39], 0
Rb 12,8,52224
v_accvgpr_mov_b32 a108, a204
s_waitcnt lgkmcnt(8)
v_mfma_f32_16x16x32_bf16 v[180:183], a[28:31], v[32:35], 0
v_exp_f32_e32 v26, v97
v_accvgpr_mov_b32 a109, a205
v_mfma_f32_16x16x32_bf16 v[192:195], a[32:35], v[32:35], 0
Rb 16,8,53248
v_add_f32_e32 v14, v48, v26
v_accvgpr_mov_b32 a110, a206
v_mfma_f32_16x16x32_bf16 v[200:203], a[32:35], v[36:39], 0
v_exp_f32_e32 v49, v98
Cv 48,48,26
v_mfma_f32_16x16x32_bf16 v[204:207], a[36:39], v[36:39], 0
Rb 20,8,54272
v_add_f32_e32 v14, v14, v49
v_accvgpr_mov_b32 a111, a207
v_mfma_f32_16x16x32_bf16 v[196:199], a[36:39], v[32:35], 0
buffer_load_dwordx4 v2, s[20:23], s55 offen offset:2048 sc1 lds
v_accvgpr_mov_b32 a112, a208
v_mfma_f32_16x16x32_bf16 v[208:211], a[40:43], v[32:35], 0
Rb 24,8,55296
v_exp_f32_e32 v27, v99
v_mfma_f32_16x16x32_bf16 v[216:219], a[40:43], v[36:39], 0
buffer_store_dwordx4 a[128:131], v11, s[20:23], s83 offen sc0 sc1
v_accvgpr_mov_b32 a113, a209
v_mfma_f32_16x16x32_bf16 v[220:223], a[44:47], v[36:39], 0
Rb 28,8,56320
v_accvgpr_mov_b32 a114, a210
s_waitcnt lgkmcnt(8)
v_mfma_f32_16x16x32_bf16 v[212:215], a[44:47], v[32:35], 0
s_add_u32 s83, s83, 7168
v_exp_f32_e32 v50, v104
v_mfma_f32_16x16x32_bf16 v[224:227], a[48:51], v[32:35], 0
Rb 32,8,57344
v_add_f32_e32 v14, v14, v27
v_accvgpr_mov_b32 a115, a211
v_mfma_f32_16x16x32_bf16 v[232:235], a[48:51], v[36:39], 0
v_exp_f32_e32 v28, v105
Cv 49,49,27
v_mfma_f32_16x16x32_bf16 v[236:239], a[52:55], v[36:39], 0
Rb 36,8,58368
v_add_f32_e32 v14, v14, v50
v_accvgpr_mov_b32 a116, a212
v_mfma_f32_16x16x32_bf16 v[228:231], a[52:55], v[32:35], 0
buffer_load_dwordx4 v2, s[20:23], s55 offen offset:3072 sc1 lds
v_accvgpr_mov_b32 a117, a213
v_mfma_f32_16x16x32_bf16 v[240:243], a[56:59], v[32:35], 0
)" R"(
Rb 40,8,59392
v_exp_f32_e32 v51, v106
v_mfma_f32_16x16x32_bf16 v[248:251], a[56:59], v[36:39], 0
buffer_store_dwordx4 a[132:135], v11, s[20:23], s83 offen sc0 sc1
v_accvgpr_mov_b32 a118, a214
v_mfma_f32_16x16x32_bf16 v[252:255], a[60:63], v[36:39], 0
Rb 44,8,60416
v_accvgpr_mov_b32 a119, a215
s_waitcnt lgkmcnt(8)
v_mfma_f32_16x16x32_bf16 v[244:247], a[60:63], v[32:35], 0
s_add_u32 s83, s83, 7168
v_exp_f32_e32 v29, v107
Mp 128,0,40
Rb 48,8,61440
v_add_f32_e32 v14, v14, v28
v_accvgpr_mov_b32 a120, a216
Mp 136,0,44
buffer_store_dwordx4 a[136:139], v11, s[20:23], s83 offen sc0 sc1
s_add_u32 s83, s83, 7168
Mp 140,4,44
Rb 52,8,62464
s_add_u32 m0, s36, 36864
v_accvgpr_mov_b32 a121, a217
Mp 132,4,40
buffer_load_dwordx4 v3, s[20:23], s55 offen offset:0 sc1 lds
v_accvgpr_mov_b32 a122, a218
Mp 144,8,40
Rb 56,8,63488
v_exp_f32_e32 v52, v100
Mp 152,8,44
v_exp_f32_e32 v24, v101
v_accvgpr_mov_b32 a123, a219
Mp 156,12,44
Rb 60,8,64512
v_accvgpr_mov_b32 a124, a220
s_waitcnt lgkmcnt(8)
Mp 148,12,40
buffer_store_dwordx4 a[140:143], v11, s[20:23], s83 offen sc0 sc1
Cv 50,50,28
Mp 160,16,40
s_add_u32 s83, s83, 7168
v_add_f32_e32 v14, v14, v51
v_accvgpr_mov_b32 a125, a221
Mp 168,16,44
v_exp_f32_e32 v53, v102
v_add_f32_e32 v14, v14, v29
Mp 172,20,44
v_exp_f32_e32 v25, v103
v_accvgpr_mov_b32 a126, a222
Mp 164,20,40
buffer_load_dwordx4 v3, s[20:23], s55 offen offset:1024 sc1 lds
v_accvgpr_mov_b32 a127, a223
Mp 176,24,40
v_exp_f32_e32 v54, v108
Cv 51,51,29
Mp 184,24,44
buffer_store_dwordx4 a[144:147], v11, s[20:23], s83 offen sc0 sc1
Mp 188,28,44
s_add_u32 s83, s83, 7168
v_add_f32_e32 v15, v52, v24
Mp 180,28,40
Cv 52,52,24
s_waitcnt vmcnt(13) lgkmcnt(0)
s_barrier
buffer_store_dword v6, v7, s[20:23], s65 offen offset:0 sc0 sc1
)" R"(
Mp 192,32,40
Rb 0,1,0
v_add_f32_e32 v15, v15, v53
Mp 200,32,44
Rb 4,1,1024
v_exp_f32_e32 v26, v109
Mp 204,36,44
Rb 8,1,2048
v_add_f32_e32 v15, v15, v25
Mp 196,36,40
Rb 12,1,3072
buffer_load_dwordx4 v3, s[20:23], s55 offen offset:2048 sc1 lds
Mp 208,40,40
Rb 16,1,8192
v_exp_f32_e32 v55, v110
Mp 216,40,44
Rb 20,1,9216
Cv 53,53,25
Mp 220,44,44
Rb 24,1,10240
buffer_store_dwordx4 a[148:151], v11, s[20:23], s83 offen sc0 sc1
Mp 212,44,40
Rb 28,1,11264
s_add_u32 s83, s83, 7168
v_add_f32_e32 v15, v15, v54
Mp 224,48,40
Rb 32,1,4096
v_add_f32_e32 v15, v15, v26
Mp 232,48,44
Rb 36,1,5120
v_exp_f32_e32 v27, v111
Mp 236,52,44
Rb 40,1,6144
Cv 54,54,26
Mp 228,52,40
Rb 44,1,7168
buffer_load_dwordx4 v3, s[20:23], s55 offen offset:3072 sc1 lds
Mp 240,56,40
v_cmp_eq_u32_e32 vcc, s3, v4
s_andn2_b64 s[28:29], s[60:61], vcc
s_cbranch_scc1 .Lzsp9
.Lzok9:
Mp 248,56,44
buffer_store_dwordx4 a[152:155], v11, s[20:23], s83 offen sc0 sc1
Mp 252,60,44
v_add_f32_e32 v15, v15, v55
s_waitcnt lgkmcnt(8)
Mp 244,60,40
v_add_f32_e32 v15, v15, v27
Cv 55,55,27
s_sub_u32 s57, s57, 1
s_cmp_gt_i32 s57, s93
s_cbranch_scc1 .LzL
.LzDA:
s_waitcnt vmcnt(8) lgkmcnt(0)
s_barrier
Rb 0,8,0
Rb 4,8,16384
Rb 8,8,1024
Rb 12,8,17408
Rb 16,8,2048
Rb 20,8,18432
Rb 24,8,3072
Rb 28,8,19456
Rb 32,8,4096
)" R"(
Rb 36,8,20480
Rb 40,8,5120
Rb 44,8,21504
v_exp_f32_e32 v56, v112
v_exp_f32_e32 v28, v113
v_add_f32_e32 v14, v14, v56
v_add_f32_e32 v14, v14, v28
Cv 56,56,28
v_exp_f32_e32 v57, v114
v_exp_f32_e32 v29, v115
v_add_f32_e32 v14, v14, v57
v_add_f32_e32 v14, v14, v29
Cv 57,57,29
v_exp_f32_e32 v58, v120
v_exp_f32_e32 v24, v121
v_add_f32_e32 v14, v14, v58
v_add_f32_e32 v14, v14, v24
Cv 58,58,24
v_exp_f32_e32 v59, v122
v_exp_f32_e32 v25, v123
v_add_f32_e32 v14, v14, v59
v_add_f32_e32 v14, v14, v25
Cv 59,59,25
v_exp_f32_e32 v60, v116
v_exp_f32_e32 v26, v117
v_add_f32_e32 v15, v15, v60
v_add_f32_e32 v15, v15, v26
Cv 60,60,26
v_exp_f32_e32 v61, v118
v_exp_f32_e32 v27, v119
v_add_f32_e32 v15, v15, v61
v_add_f32_e32 v15, v15, v27
Cv 61,61,27
v_exp_f32_e32 v62, v124
v_exp_f32_e32 v28, v125
v_add_f32_e32 v15, v15, v62
v_add_f32_e32 v15, v15, v28
Cv 62,62,28
v_exp_f32_e32 v63, v126
v_exp_f32_e32 v29, v127
v_add_f32_e32 v15, v15, v63
v_add_f32_e32 v15, v15, v29
Cv 63,63,29
v_max_f32_e32 v11, v14, v15
v_cmp_nge_f32_e32 vcc, s62, v11
s_cbranch_vccnz .Lzfx5
.Lzfr5:
s_waitcnt lgkmcnt(8)
Mp 128,0,48
Rb 48,8,6144
s_add_u32 m0, s36, 131072
s_add_u32 s40, s67, 0x20000
s_lshl_b32 s82, s82, 7
Mp 136,0,52
buffer_load_dwordx4 v2, s[20:23], s40 offen offset:0 sc1 lds
s_add_u32 s82, s82, 0xc10c000
Mp 136,4,60
Rb 52,8,22528
v_add_f32_e32 v12, v12, v14
Mp 128,4,56
buffer_load_dwordx4 v2, s[20:23], s40 offen offset:1024 sc1 lds
v_add_f32_e32 v13, v13, v15
Mp 132,8,48
Rb 56,8,7168
)" R"(
v_mov_b32 v24, v12
Mp 140,8,52
s_nop 0
v_permlane32_swap_b32_e32 v12, v24
v_add_f32_e32 v12, v12, v24
s_cmp_lg_u32 s33, 0
s_cmov_b32 s82, 0x40000000
Mp 140,12,60
Rb 60,8,23552
v_mov_b32 v24, v12
s_waitcnt lgkmcnt(8)
Mp 132,12,56
buffer_load_dwordx4 v2, s[20:23], s40 offen offset:2048 sc1 lds
Mp 144,16,48
Rb 0,8,8192
v_permlane16_swap_b32_e32 v12, v24
Mp 152,16,52
buffer_load_dwordx4 v2, s[20:23], s40 offen offset:3072 sc1 lds
v_add_f32_e32 v12, v12, v24
Mp 152,20,60
Rb 4,8,24576
v_mov_b32 v25, v13
Mp 144,20,56
v_rcp_f32_e32 v12, v12
Mp 148,24,48
Rb 8,8,9216
s_add_u32 m0, s36, 135168
v_permlane32_swap_b32_e32 v13, v25
Mp 156,24,52
buffer_load_dwordx4 v3, s[20:23], s40 offen offset:0 sc1 lds
s_cmp_eq_u32 s35, 7
s_cmov_b32 s82, 0x40000000
Mp 156,28,60
Rb 12,8,25600
s_waitcnt lgkmcnt(8)
Mp 148,28,56
buffer_load_dwordx4 v3, s[20:23], s40 offen offset:1024 sc1 lds
v_add_f32_e32 v13, v13, v25
Mp 160,32,48
Rb 16,8,10240
v_mov_b32 v25, v13
Mp 168,32,52
s_nop 0
v_permlane16_swap_b32_e32 v13, v25
v_add_f32_e32 v13, v13, v25
Mp 168,36,60
Rb 20,8,26624
v_mul_f32_e32 v128, v12, v128
Mp 160,36,56
buffer_load_dwordx4 v3, s[20:23], s40 offen offset:2048 sc1 lds
v_mul_f32_e32 v129, v12, v129
Mp 164,40,48
Rb 24,8,11264
s_add_u32 m0, s36, 98304
s_add_u32 s40, s67, 0x30000
Mp 172,40,52
buffer_load_dwordx4 v2, s[20:23], s40 offen offset:0 sc1 lds
Mp 172,44,60
Rb 28,8,27648
Cv 128,128,129
s_waitcnt lgkmcnt(8)
Mp 164,44,56
v_rcp_f32_e32 v13, v13
Mp 176,48,48
)" R"(
Rb 32,8,12288
v_mul_f32_e32 v130, v12, v130
Mp 184,48,52
buffer_load_dwordx4 v2, s[20:23], s40 offen offset:1024 sc1 lds
v_mul_f32_e32 v131, v12, v131
Mp 184,52,60
Rb 36,8,28672
Cv 129,130,131
Mp 176,52,56
buffer_load_dwordx4 v2, s[20:23], s40 offen offset:2048 sc1 lds
Mp 180,56,48
Rb 40,8,13312
v_mul_f32_e32 v132, v12, v132
v_mul_f32_e32 v133, v12, v133
Mp 188,56,52
Cv 130,132,133
v_mul_f32_e32 v134, v12, v134
Mp 188,60,60
Rb 44,8,29696
s_waitcnt lgkmcnt(8)
Mp 180,60,56
buffer_load_dwordx4 v2, s[20:23], s40 offen offset:3072 sc1 lds
v_mul_f32_e32 v135, v12, v135
Mp 192,0,48
Rb 48,8,14336
s_add_u32 m0, s36, 102400
Mp 200,0,52
buffer_load_dwordx4 v3, s[20:23], s40 offen offset:0 sc1 lds
Mp 200,4,60
Rb 52,8,30720
Cv 131,134,135
Mp 192,4,56
v_mul_f32_e32 v136, v13, v136
v_mul_f32_e32 v137, v13, v137
Cv 136,136,137
Mp 196,8,48
Rb 56,8,15360
v_mul_f32_e32 v138, v13, v138
Mp 204,8,52
buffer_load_dwordx4 v3, s[20:23], s40 offen offset:1024 sc1 lds
Mp 204,12,60
Rb 60,8,31744
v_mul_f32_e32 v139, v13, v139
s_waitcnt lgkmcnt(8)
Mp 196,12,56
buffer_load_dwordx4 v3, s[20:23], s40 offen offset:2048 sc1 lds
Mp 208,16,48
Rb 0,1,0
Cv 137,138,139
Mp 216,16,52
v_mul_f32_e32 v140, v13, v140
v_mul_f32_e32 v141, v13, v141
Cv 138,140,141
Mp 216,20,60
Rb 4,1,1024
v_mul_f32_e32 v142, v13, v142
Mp 208,20,56
v_mul_f32_e32 v143, v13, v143
Cv 139,142,143
Mp 212,24,48
Rb 8,1,2048
v_mul_f32_e32 v144, v12, v144
v_mul_f32_e32 v145, v12, v145
Mp 220,24,52
)" R"(
Cv 144,144,145
v_mul_f32_e32 v146, v12, v146
Mp 220,28,60
Rb 12,1,3072
s_waitcnt vmcnt(14) lgkmcnt(4)
s_barrier
Mp 212,28,56
s_add_u32 s40, s67, 0x40000
v_mul_f32_e32 v147, v12, v147
Cv 145,146,147
Mp 224,32,48
Rb 16,1,4096
s_add_u32 m0, s36, 65536
Mp 232,32,52
buffer_load_dwordx4 v2, s[20:23], s40 offen offset:0 sc1 lds
Mp 232,36,60
Rb 20,1,5120
v_mul_f32_e32 v148, v12, v148
Mp 224,36,56
v_mul_f32_e32 v149, v12, v149
Cv 146,148,149
v_mul_f32_e32 v150, v12, v150
Mp 228,40,48
Rb 24,1,6144
v_mul_f32_e32 v151, v12, v151
Mp 236,40,52
Cv 147,150,151
v_mul_f32_e32 v152, v13, v152
Mp 236,44,60
Rb 28,1,8192
v_mul_f32_e32 v153, v13, v153
Mp 228,44,56
buffer_load_dwordx4 v2, s[20:23], s40 offen offset:1024 sc1 lds
Mp 240,48,48
Rb 32,1,9216
Cv 152,152,153
Mp 248,48,52
v_mul_f32_e32 v154, v13, v154
v_mul_f32_e32 v155, v13, v155
Cv 153,154,155
Mp 248,52,60
Rb 36,1,10240
v_mul_f32_e32 v156, v13, v156
Mp 240,52,56
v_mul_f32_e32 v157, v13, v157
Cv 154,156,157
Mp 244,56,48
Rb 40,1,11264
v_mul_f32_e32 v158, v13, v158
v_mul_f32_e32 v159, v13, v159
Mp 252,56,52
buffer_load_dwordx4 v2, s[20:23], s40 offen offset:2048 sc1 lds
Mp 252,60,60
Rb 44,1,12288
s_waitcnt lgkmcnt(8)
Mp 244,60,56
Cv 155,158,159
v_mul_f32_e32 v160, v12, v160
v_mul_f32_e32 v161, v12, v161
v_mfma_f32_16x16x32_bf16 v[32:35], a[0:3], v[128:131], 0
Rb 48,1,13312
Cv 160,160,161
v_mfma_f32_16x16x32_bf16 v[40:43], a[0:3], v[136:139], 0
v_mul_f32_e32 v162, v12, v162
v_mul_f32_e32 v163, v12, v163
)" R"(
v_mfma_f32_16x16x32_bf16 v[44:47], a[4:7], v[136:139], 0
Rb 52,1,14336
Cv 161,162,163
v_mfma_f32_16x16x32_bf16 v[36:39], a[4:7], v[128:131], 0
buffer_load_dwordx4 v2, s[20:23], s40 offen offset:3072 sc1 lds
v_mul_f32_e32 v164, v12, v164
v_mfma_f32_16x16x32_bf16 v[48:51], a[8:11], v[128:131], 0
Rb 56,1,16384
v_mul_f32_e32 v165, v12, v165
v_mfma_f32_16x16x32_bf16 v[56:59], a[8:11], v[136:139], 0
Cv 162,164,165
v_mul_f32_e32 v166, v12, v166
v_mfma_f32_16x16x32_bf16 v[60:63], a[12:15], v[136:139], 0
Rb 60,1,17408
v_mul_f32_e32 v167, v12, v167
s_waitcnt lgkmcnt(8)
v_mfma_f32_16x16x32_bf16 v[52:55], a[12:15], v[128:131], 0
Cv 163,166,167
v_mul_f32_e32 v168, v13, v168
v_mfma_f32_16x16x32_bf16 v[64:67], a[16:19], v[128:131], 0
Rb 0,1,18432
s_add_u32 m0, s36, 69632
v_mfma_f32_16x16x32_bf16 v[72:75], a[16:19], v[136:139], 0
buffer_load_dwordx4 v3, s[20:23], s40 offen offset:0 sc1 lds
v_mul_f32_e32 v169, v13, v169
v_mfma_f32_16x16x32_bf16 v[76:79], a[20:23], v[136:139], 0
Rb 4,1,19456
Cv 168,168,169
v_mfma_f32_16x16x32_bf16 v[68:71], a[20:23], v[128:131], 0
v_mul_f32_e32 v170, v13, v170
v_mul_f32_e32 v171, v13, v171
v_mfma_f32_16x16x32_bf16 v[80:83], a[24:27], v[128:131], 0
Rb 8,1,20480
Cv 169,170,171
v_mul_f32_e32 v172, v13, v172
v_mfma_f32_16x16x32_bf16 v[88:91], a[24:27], v[136:139], 0
v_mul_f32_e32 v173, v13, v173
Cv 170,172,173
v_mfma_f32_16x16x32_bf16 v[92:95], a[28:31], v[136:139], 0
Rb 12,1,21504
s_waitcnt lgkmcnt(8)
v_mfma_f32_16x16x32_bf16 v[84:87], a[28:31], v[128:131], 0
buffer_load_dwordx4 v3, s[20:23], s40 offen offset:1024 sc1 lds
v_mul_f32_e32 v174, v13, v174
Mp 32,32,144
Rb 16,1,22528
v_mul_f32_e32 v175, v13, v175
Mp 40,32,152
Cv 171,174,175
v_mul_f32_e32 v176, v12, v176
Mp 44,36,152
Rb 20,1,24576
v_mul_f32_e32 v177, v12, v177
Mp 36,36,144
Cv 176,176,177
v_mul_f32_e32 v178, v12, v178
v_mul_f32_e32 v179, v12, v179
Mp 48,40,144
Rb 24,1,25600
Cv 177,178,179
Mp 56,40,152
buffer_load_dwordx4 v3, s[20:23], s40 offen offset:2048 sc1 lds
Mp 60,44,152
Rb 28,1,26624
)" R"(
v_mul_f32_e32 v180, v12, v180
s_waitcnt lgkmcnt(8)
Mp 52,44,144
v_mul_f32_e32 v181, v12, v181
Cv 178,180,181
Mp 64,48,144
Rb 32,1,27648
v_mul_f32_e32 v182, v12, v182
Mp 72,48,152
v_mul_f32_e32 v183, v12, v183
Cv 179,182,183
v_mul_f32_e32 v184, v13, v184
Mp 76,52,152
Rb 36,1,28672
v_mul_f32_e32 v185, v13, v185
Mp 68,52,144
Cv 184,184,185
v_mul_f32_e32 v186, v13, v186
Mp 80,56,144
Rb 40,1,29696
v_mul_f32_e32 v187, v13, v187
Cv 185,186,187
Mp 88,56,152
v_mul_f32_e32 v188, v13, v188
v_mul_f32_e32 v189, v13, v189
Mp 92,60,152
Rb 44,1,30720
s_waitcnt lgkmcnt(8)
Mp 84,60,144
Cv 186,188,189
v_mul_f32_e32 v190, v13, v190
v_mul_f32_e32 v191, v13, v191
Mp 32,0,160
Rb 48,1,32768
Cv 187,190,191
Mp 40,0,168
v_mul_f32_e32 v192, v12, v192
v_mul_f32_e32 v193, v12, v193
Mp 44,4,168
Rb 52,1,33792
Cv 192,192,193
Mp 36,4,160
v_mul_f32_e32 v194, v12, v194
v_mul_f32_e32 v195, v12, v195
Cv 193,194,195
Mp 48,8,160
Rb 56,1,34816
v_mul_f32_e32 v196, v12, v196
Mp 56,8,168
v_mul_f32_e32 v197, v12, v197
Cv 194,196,197
Mp 60,12,168
Rb 60,1,35840
s_waitcnt vmcnt(14) lgkmcnt(4)
s_barrier
buffer_store_dword v6, v7, s[20:23], s82 offen offset:0 sc0 sc1
Mp 52,12,160
s_add_u32 s40, s67, 0x50000
v_mul_f32_e32 v198, v12, v198
Mp 64,16,160
Rb 0,1,36864
s_add_u32 m0, s36, 0
Mp 72,16,168
buffer_load_dwordx4 v2, s[20:23], s40 offen offset:0 sc1 lds
v_mul_f32_e32 v199, v12, v199
)" R"(
Mp 76,20,168
Rb 4,1,37888
Cv 195,198,199
Mp 68,20,160
v_mul_f32_e32 v200, v13, v200
v_mul_f32_e32 v201, v13, v201
Mp 80,24,160
Rb 8,1,38912
Cv 200,200,201
v_mul_f32_e32 v202, v13, v202
Mp 88,24,168
v_mul_f32_e32 v203, v13, v203
Cv 201,202,203
Mp 92,28,168
Rb 12,1,40960
Mp 84,28,160
buffer_load_dwordx4 v2, s[20:23], s40 offen offset:1024 sc1 lds
v_mul_f32_e32 v204, v13, v204
Mp 32,32,176
Rb 16,1,41984
v_mul_f32_e32 v205, v13, v205
Mp 40,32,184
Cv 202,204,205
v_mul_f32_e32 v206, v13, v206
Mp 44,36,184
Rb 20,1,43008
v_mul_f32_e32 v207, v13, v207
Mp 36,36,176
Cv 203,206,207
v_mul_f32_e32 v208, v12, v208
v_mul_f32_e32 v209, v12, v209
Mp 48,40,176
Rb 24,1,44032
Cv 208,208,209
Mp 56,40,184
buffer_load_dwordx4 v2, s[20:23], s40 offen offset:2048 sc1 lds
Mp 60,44,184
Rb 28,1,45056
v_mul_f32_e32 v210, v12, v210
s_waitcnt lgkmcnt(8)
Mp 52,44,176
v_mul_f32_e32 v211, v12, v211
Cv 209,210,211
Mp 64,48,176
Rb 32,1,46080
v_mul_f32_e32 v212, v12, v212
Mp 72,48,184
v_mul_f32_e32 v213, v12, v213
Cv 210,212,213
v_mul_f32_e32 v214, v12, v214
Mp 76,52,184
Rb 36,1,47104
v_mul_f32_e32 v215, v12, v215
Mp 68,52,176
buffer_load_dwordx4 v2, s[20:23], s40 offen offset:3072 sc1 lds
Mp 80,56,176
Rb 40,1,49152
Cv 211,214,215
v_mul_f32_e32 v216, v13, v216
Mp 88,56,184
v_mul_f32_e32 v217, v13, v217
Cv 216,216,217
Mp 92,60,184
Rb 44,1,50176
)" R"(
s_waitcnt lgkmcnt(8)
Mp 84,60,176
v_mul_f32_e32 v218, v13, v218
v_mul_f32_e32 v219, v13, v219
Cv 217,218,219
Mp 32,0,192
Rb 48,1,51200
s_add_u32 m0, s36, 4096
Mp 40,0,200
buffer_load_dwordx4 v3, s[20:23], s40 offen offset:0 sc1 lds
Mp 44,4,200
Rb 52,1,52224
v_mul_f32_e32 v220, v13, v220
Mp 36,4,192
v_mul_f32_e32 v221, v13, v221
Cv 218,220,221
v_mul_f32_e32 v222, v13, v222
Mp 48,8,192
Rb 56,1,53248
v_mul_f32_e32 v223, v13, v223
Mp 56,8,200
Cv 219,222,223
v_mul_f32_e32 v224, v12, v224
Mp 60,12,200
Rb 60,1,54272
v_mul_f32_e32 v225, v12, v225
s_waitcnt lgkmcnt(8)
Mp 52,12,192
buffer_load_dwordx4 v3, s[20:23], s40 offen offset:1024 sc1 lds
Mp 64,16,192
Rb 0,1,55296
Cv 224,224,225
Mp 72,16,200
v_mul_f32_e32 v226, v12, v226
v_mul_f32_e32 v227, v12, v227
Cv 225,226,227
Mp 76,20,200
Rb 4,1,57344
v_mul_f32_e32 v228, v12, v228
Mp 68,20,192
v_mul_f32_e32 v229, v12, v229
Cv 226,228,229
Mp 80,24,192
Rb 8,1,58368
v_mul_f32_e32 v230, v12, v230
v_mul_f32_e32 v231, v12, v231
Mp 88,24,200
buffer_load_dwordx4 v3, s[20:23], s40 offen offset:2048 sc1 lds
Mp 92,28,200
Rb 12,1,59392
s_waitcnt lgkmcnt(8)
Mp 84,28,192
Cv 227,230,231
v_mul_f32_e32 v232, v13, v232
v_mul_f32_e32 v233, v13, v233
Mp 32,32,208
Rb 16,1,60416
Cv 232,232,233
Mp 40,32,216
v_mul_f32_e32 v234, v13, v234
v_mul_f32_e32 v235, v13, v235
Mp 44,36,216
Rb 20,1,61440
Cv 233,234,235
)" R"(
Mp 36,36,208
v_mul_f32_e32 v236, v13, v236
v_mul_f32_e32 v237, v13, v237
Cv 234,236,237
Mp 48,40,208
Rb 24,1,62464
v_mul_f32_e32 v238, v13, v238
Mp 56,40,216
v_mul_f32_e32 v239, v13, v239
Cv 235,238,239
Mp 60,44,216
Rb 28,1,63488
v_mul_f32_e32 v240, v12, v240
s_waitcnt lgkmcnt(8)
Mp 52,44,208
v_mul_f32_e32 v241, v12, v241
Cv 240,240,241
Mp 64,48,208
Rb 32,10,0
v_mul_f32_e32 v242, v12, v242
Mp 72,48,216
v_mul_f32_e32 v243, v12, v243
Cv 241,242,243
v_mul_f32_e32 v244, v12, v244
Mp 76,52,216
Rb 36,10,1024
v_mul_f32_e32 v245, v12, v245
Mp 68,52,208
Cv 242,244,245
v_mul_f32_e32 v246, v12, v246
Mp 80,56,208
Rb 40,10,2048
v_mul_f32_e32 v247, v12, v247
Cv 243,246,247
Mp 88,56,216
v_mul_f32_e32 v248, v13, v248
v_mul_f32_e32 v249, v13, v249
Mp 92,60,216
Rb 44,10,3072
s_waitcnt vmcnt(15) lgkmcnt(4)
s_barrier
Mp 84,60,208
s_add_u32 s40, s67, 0x60000
Cv 248,248,249
v_mul_f32_e32 v250, v13, v250
Mp 32,0,224
Rb 48,10,4096
s_add_u32 m0, s36, 32768
Mp 40,0,232
buffer_load_dwordx4 v2, s[20:23], s40 offen offset:0 sc1 lds
Mp 44,4,232
Rb 52,10,5120
v_mul_f32_e32 v251, v13, v251
Mp 36,4,224
Cv 249,250,251
v_mul_f32_e32 v252, v13, v252
v_mul_f32_e32 v253, v13, v253
Mp 48,8,224
Rb 56,10,6144
Cv 250,252,253
Mp 56,8,232
v_mul_f32_e32 v254, v13, v254
v_mul_f32_e32 v255, v13, v255
Mp 60,12,232
Rb 60,10,8192
)" R"(
Cv 251,254,255
Mp 52,12,224
buffer_load_dwordx4 v2, s[20:23], s40 offen offset:1024 sc1 lds
Mp 64,16,224
Rb 0,10,9216
Mp 72,16,232
Mp 76,20,232
Rb 4,10,10240
Mp 68,20,224
Mp 80,24,224
Rb 8,10,11264
Mp 88,24,232
buffer_load_dwordx4 v2, s[20:23], s40 offen offset:2048 sc1 lds
Mp 92,28,232
Rb 12,10,12288
s_waitcnt lgkmcnt(8)
Mp 84,28,224
Mp 32,32,240
Rb 16,10,13312
Mp 40,32,248
Mp 44,36,248
Rb 20,10,14336
Mp 36,36,240
buffer_load_dwordx4 v2, s[20:23], s40 offen offset:3072 sc1 lds
Mp 48,40,240
Rb 24,10,16384
Mp 56,40,248
Mp 60,44,248
Rb 28,10,17408
s_waitcnt lgkmcnt(8)
Mp 52,44,240
Mp 64,48,240
Rb 32,10,18432
s_add_u32 m0, s36, 36864
Mp 72,48,248
buffer_load_dwordx4 v3, s[20:23], s40 offen offset:0 sc1 lds
Mp 76,52,248
Rb 36,10,19456
Mp 68,52,240
Mp 80,56,240
Rb 40,10,20480
Mp 88,56,248
Mp 92,60,248
Rb 44,10,21504
s_waitcnt lgkmcnt(8)
Mp 84,60,240
buffer_load_dwordx4 v3, s[20:23], s40 offen offset:1024 sc1 lds
v_mfma_f32_16x16x32_bf16 v[96:99], a[0:3], v[128:131], 0
Rb 48,10,22528
v_mfma_f32_16x16x32_bf16 v[104:107], a[0:3], v[136:139], 0
Mp 104,4,152
Rb 52,10,24576
Mp 96,4,144
Mp 96,8,160
Rb 56,10,25600
Cv 32,32,33
Mp 104,8,168
buffer_load_dwordx4 v3, s[20:23], s40 offen offset:2048 sc1 lds
Mp 104,12,184
Rb 60,10,26624
s_waitcnt lgkmcnt(8)
Mp 96,12,176
Cv 33,34,35
Cv 34,36,37
)" R"(
Mp 96,16,192
Rb 0,10,27648
Cv 35,38,39
Mp 104,16,200
s_add_u32 s29, s73, 0x0
buffer_store_dwordx4 v[32:35], v1, s[20:23], s29 offen sc0 sc1
Mp 104,20,216
Rb 4,10,28672
Mp 96,20,208
Mp 96,24,224
Rb 8,10,29696
Cv 40,40,41
Mp 104,24,232
Cv 41,42,43
Cv 42,44,45
Mp 104,28,248
Rb 12,10,30720
s_waitcnt lgkmcnt(8)
Mp 96,28,240
Cv 43,46,47
v_mfma_f32_16x16x32_bf16 v[100:103], a[32:35], v[128:131], 0
Rb 16,8,32768
v_mfma_f32_16x16x32_bf16 v[108:111], a[32:35], v[136:139], 0
s_add_u32 s29, s73, 0x400
buffer_store_dwordx4 v[40:43], v1, s[20:23], s29 offen sc0 sc1
Mp 108,36,152
Rb 20,8,33792
Mp 100,36,144
Mp 100,40,160
Rb 24,8,34816
Cv 48,48,49
Mp 108,40,168
Cv 49,50,51
Cv 50,52,53
Mp 108,44,184
Rb 28,8,35840
s_waitcnt vmcnt(17) lgkmcnt(4)
s_barrier
Mp 100,44,176
s_add_u32 s40, s67, 0x70000
Cv 51,54,55
Mp 100,48,192
Rb 32,8,36864
s_add_u32 m0, s36, 131072
Mp 108,48,200
buffer_load_dwordx4 v2, s[20:23], s40 offen offset:0 sc1 lds
Mp 108,52,216
Rb 36,8,37888
Mp 100,52,208
s_add_u32 s29, s73, 0x800
buffer_store_dwordx4 v[48:51], v1, s[20:23], s29 offen sc0 sc1
Mp 100,56,224
Rb 40,8,38912
Cv 56,56,57
Mp 108,56,232
Cv 57,58,59
Cv 58,60,61
Mp 108,60,248
Rb 44,8,40960
Mp 100,60,240
buffer_load_dwordx4 v2, s[20:23], s40 offen offset:1024 sc1 lds
v_mfma_f32_16x16x32_bf16 v[112:115], a[0:3], v[128:131], 0
Rb 48,8,41984
Cv 59,62,63
v_mfma_f32_16x16x32_bf16 v[120:123], a[0:3], v[136:139], 0
)" R"(
s_add_u32 s29, s73, 0xc00
buffer_store_dwordx4 v[56:59], v1, s[20:23], s29 offen sc0 sc1
Mp 120,4,152
Rb 52,8,43008
Mp 112,4,144
Mp 112,8,160
Rb 56,8,44032
Cv 96,96,97
Mp 120,8,168
buffer_load_dwordx4 v2, s[20:23], s40 offen offset:2048 sc1 lds
Mp 120,12,184
Rb 60,8,45056
s_waitcnt lgkmcnt(8)
Mp 112,12,176
Cv 97,98,99
Cv 98,100,101
Mp 112,16,192
Rb 0,8,46080
Cv 99,102,103
Mp 120,16,200
s_add_u32 s29, s73, 0x4400
buffer_store_dwordx4 v[96:99], v1, s[20:23], s29 offen sc0 sc1
Mp 120,20,216
Rb 4,8,47104
Mp 112,20,208
buffer_load_dwordx4 v2, s[20:23], s40 offen offset:3072 sc1 lds
Mp 112,24,224
Rb 8,8,49152
Cv 64,64,65
Mp 120,24,232
Cv 65,66,67
Cv 66,68,69
Mp 120,28,248
Rb 12,8,50176
s_waitcnt lgkmcnt(8)
Mp 112,28,240
Cv 67,70,71
v_mfma_f32_16x16x32_bf16 v[116:119], a[32:35], v[128:131], 0
Rb 16,8,51200
s_add_u32 m0, s36, 135168
v_mfma_f32_16x16x32_bf16 v[124:127], a[32:35], v[136:139], 0
buffer_load_dwordx4 v3, s[20:23], s40 offen offset:0 sc1 lds
Mp 124,36,152
Rb 20,8,52224
Mp 116,36,144
s_add_u32 s29, s73, 0x1000
buffer_store_dwordx4 v[64:67], v1, s[20:23], s29 offen sc0 sc1
Mp 116,40,160
Rb 24,8,53248
Cv 104,104,105
Mp 124,40,168
Cv 105,106,107
Cv 106,108,109
Mp 124,44,184
Rb 28,8,54272
s_waitcnt lgkmcnt(8)
Mp 116,44,176
buffer_load_dwordx4 v3, s[20:23], s40 offen offset:1024 sc1 lds
Mp 116,48,192
Rb 32,8,55296
Cv 107,110,111
Mp 124,48,200
s_add_u32 s29, s73, 0x4800
buffer_store_dwordx4 v[104:107], v1, s[20:23], s29 offen sc0 sc1
)" R"(
Mp 124,52,216
Rb 36,8,57344
Mp 116,52,208
Mp 116,56,224
Rb 40,8,58368
Cv 72,72,73
Mp 124,56,232
buffer_load_dwordx4 v3, s[20:23], s40 offen offset:2048 sc1 lds
Mp 124,60,248
Rb 44,8,59392
s_waitcnt lgkmcnt(8)
Mp 116,60,240
Cv 73,74,75
Cv 74,76,77
v_mfma_f32_16x16x32_bf16 v[96:99], a[0:3], v[128:131], 0
Rb 48,8,60416
Cv 75,78,79
v_mfma_f32_16x16x32_bf16 v[104:107], a[0:3], v[136:139], 0
s_add_u32 s29, s73, 0x1400
buffer_store_dwordx4 v[72:75], v1, s[20:23], s29 offen sc0 sc1
Mp 104,4,152
Rb 52,8,61440
Mp 96,4,144
Mp 96,8,160
Rb 56,8,62464
Cv 112,112,113
Mp 104,8,168
Cv 113,114,115
Cv 114,116,117
Mp 104,12,184
Rb 60,8,63488
s_waitcnt lgkmcnt(8)
Mp 96,12,176
Cv 115,118,119
Mp 96,16,192
Rb 0,8,0
Mp 104,16,200
s_add_u32 s29, s73, 0x4c00
buffer_store_dwordx4 v[112:115], v1, s[20:23], s29 offen sc0 sc1
Mp 104,20,216
Rb 4,8,1024
Mp 96,20,208
Mp 96,24,224
Rb 8,8,2048
Cv 80,80,81
Mp 104,24,232
Cv 81,82,83
Cv 82,84,85
Mp 104,28,248
Rb 12,8,3072
s_waitcnt vmcnt(23) lgkmcnt(8)
s_barrier
Mp 96,28,240
Cv 83,86,87
v_mfma_f32_16x16x32_bf16 v[100:103], a[32:35], v[128:131], 0
Rb 16,8,4096
v_mfma_f32_16x16x32_bf16 v[108:111], a[32:35], v[136:139], 0
s_add_u32 s29, s73, 0x1800
buffer_store_dwordx4 v[80:83], v1, s[20:23], s29 offen sc0 sc1
Mp 108,36,152
Rb 20,8,5120
Mp 100,36,144
Mp 100,40,160
Rb 24,8,6144
Cv 120,120,121
)" R"(
Mp 108,40,168
Cv 121,122,123
Cv 122,124,125
Mp 108,44,184
Rb 28,8,8192
s_waitcnt lgkmcnt(8)
Mp 100,44,176
Cv 123,126,127
Mp 100,48,192
Rb 32,8,9216
Mp 108,48,200
s_add_u32 s29, s73, 0x5000
buffer_store_dwordx4 v[120:123], v1, s[20:23], s29 offen sc0 sc1
Mp 108,52,216
Rb 36,8,10240
Mp 100,52,208
Mp 100,56,224
Rb 40,8,11264
Cv 88,88,89
Mp 108,56,232
Cv 89,90,91
Cv 90,92,93
Mp 108,60,248
Rb 44,8,12288
s_waitcnt lgkmcnt(8)
Mp 100,60,240
Cv 91,94,95
v_mfma_f32_16x16x32_bf16 v[112:115], a[0:3], v[128:131], 0
Rb 48,8,13312
v_mfma_f32_16x16x32_bf16 v[120:123], a[0:3], v[136:139], 0
s_add_u32 s29, s73, 0x4000
buffer_store_dwordx4 v[88:91], v1, s[20:23], s29 offen sc0 sc1
Mp 120,4,152
Rb 52,8,14336
Mp 112,4,144
Mp 112,8,160
Rb 56,8,16384
Cv 96,96,97
Mp 120,8,168
Cv 97,98,99
Cv 98,100,101
Mp 120,12,184
Rb 60,8,17408
s_waitcnt lgkmcnt(8)
Mp 112,12,176
Cv 99,102,103
Mp 112,16,192
Rb 0,8,18432
Mp 120,16,200
s_add_u32 s29, s73, 0x5400
buffer_store_dwordx4 v[96:99], v1, s[20:23], s29 offen sc0 sc1
Mp 120,20,216
Rb 4,8,19456
Mp 112,20,208
Mp 112,24,224
Rb 8,8,20480
Mp 120,24,232
Mp 120,28,248
Rb 12,8,21504
s_waitcnt lgkmcnt(8)
Mp 112,28,240
v_mfma_f32_16x16x32_bf16 v[116:119], a[32:35], v[128:131], 0
Rb 16,8,22528
v_mfma_f32_16x16x32_bf16 v[124:127], a[32:35], v[136:139], 0
)" R"(
Mp 124,36,152
Rb 20,8,24576
Mp 116,36,144
Mp 116,40,160
Rb 24,8,25600
Cv 104,104,105
Mp 124,40,168
Cv 105,106,107
Cv 106,108,109
Mp 124,44,184
Rb 28,8,26624
s_waitcnt lgkmcnt(8)
Mp 116,44,176
Cv 107,110,111
Mp 116,48,192
Rb 32,8,27648
Mp 124,48,200
s_add_u32 s29, s73, 0x5800
buffer_store_dwordx4 v[104:107], v1, s[20:23], s29 offen sc0 sc1
Mp 124,52,216
Rb 36,8,28672
Mp 116,52,208
Mp 116,56,224
Rb 40,8,29696
Mp 124,56,232
Mp 124,60,248
Rb 44,8,30720
s_waitcnt lgkmcnt(8)
Mp 116,60,240
v_mfma_f32_16x16x32_bf16 v[96:99], a[0:3], v[128:131], 0
Rb 48,1,0
v_mfma_f32_16x16x32_bf16 v[104:107], a[0:3], v[136:139], 0
Mp 104,4,152
Rb 52,1,1024
Mp 96,4,144
Mp 96,8,160
Rb 56,1,2048
Cv 112,112,113
Mp 104,8,168
Cv 113,114,115
Cv 114,116,117
Mp 104,12,184
Rb 60,1,3072
s_waitcnt vmcnt(21) lgkmcnt(8)
s_barrier
Mp 96,12,176
Cv 115,118,119
Mp 96,16,192
Rb 0,1,4096
Mp 104,16,200
s_add_u32 s29, s73, 0x8000
buffer_store_dwordx4 v[112:115], v1, s[20:23], s29 offen sc0 sc1
Mp 104,20,216
Rb 4,1,5120
Mp 96,20,208
Mp 96,24,224
Rb 8,1,6144
Mp 104,24,232
Mp 104,28,248
Rb 12,1,8192
s_waitcnt lgkmcnt(8)
Mp 96,28,240
v_mfma_f32_16x16x32_bf16 v[100:103], a[32:35], v[128:131], 0
Rb 16,1,9216
v_mfma_f32_16x16x32_bf16 v[108:111], a[32:35], v[136:139], 0
)" R"(
Mp 108,36,152
Rb 20,1,10240
Mp 100,36,144
Mp 100,40,160
Rb 24,1,11264
Cv 120,120,121
Mp 108,40,168
Cv 121,122,123
Cv 122,124,125
Mp 108,44,184
Rb 28,1,12288
s_waitcnt lgkmcnt(8)
Mp 100,44,176
Cv 123,126,127
Mp 100,48,192
Rb 32,1,13312
Mp 108,48,200
s_add_u32 s29, s73, 0x8400
buffer_store_dwordx4 v[120:123], v1, s[20:23], s29 offen sc0 sc1
Mp 108,52,216
Rb 36,1,14336
Mp 100,52,208
Mp 100,56,224
Rb 40,1,16384
Mp 108,56,232
Mp 108,60,248
Rb 44,1,17408
s_waitcnt lgkmcnt(8)
Mp 100,60,240
v_mfma_f32_16x16x32_bf16 v[112:115], a[0:3], v[128:131], 0
Rb 48,1,18432
v_mfma_f32_16x16x32_bf16 v[120:123], a[0:3], v[136:139], 0
Mp 120,4,152
Rb 52,1,19456
Mp 112,4,144
Mp 112,8,160
Rb 56,1,20480
Cv 96,96,97
Mp 120,8,168
Cv 97,98,99
Cv 98,100,101
Mp 120,12,184
Rb 60,1,21504
s_waitcnt vmcnt(2) lgkmcnt(8)
buffer_store_dword v6, v7, s[20:23], s68 offen offset:0 sc0 sc1
Mp 112,12,176
Cv 99,102,103
Mp 112,16,192
Rb 0,1,22528
Mp 120,16,200
s_add_u32 s29, s73, 0x8800
buffer_store_dwordx4 v[96:99], v1, s[20:23], s29 offen sc0 sc1
Mp 120,20,216
Rb 4,1,24576
Mp 112,20,208
Mp 112,24,224
Rb 8,1,25600
Mp 120,24,232
Mp 120,28,248
Rb 12,1,26624
s_waitcnt lgkmcnt(8)
Mp 112,28,240
v_mfma_f32_16x16x32_bf16 v[116:119], a[32:35], v[128:131], 0
Rb 16,1,27648
)" R"(
v_mfma_f32_16x16x32_bf16 v[124:127], a[32:35], v[136:139], 0
Mp 124,36,152
Rb 20,1,28672
Mp 116,36,144
Mp 116,40,160
Rb 24,1,29696
Cv 104,104,105
Mp 124,40,168
Cv 105,106,107
Cv 106,108,109
Mp 124,44,184
Rb 28,1,30720
s_waitcnt lgkmcnt(8)
Mp 116,44,176
Cv 107,110,111
Mp 116,48,192
Rb 32,1,32768
Mp 124,48,200
s_add_u32 s29, s73, 0x8c00
buffer_store_dwordx4 v[104:107], v1, s[20:23], s29 offen sc0 sc1
Mp 124,52,216
Rb 36,1,33792
Mp 116,52,208
Mp 116,56,224
Rb 40,1,34816
Mp 124,56,232
Mp 124,60,248
Rb 44,1,35840
s_waitcnt lgkmcnt(8)
s_barrier
Mp 116,60,240
v_mfma_f32_16x16x32_bf16 v[96:99], a[0:3], v[128:131], 0
Rb 48,1,36864
v_mfma_f32_16x16x32_bf16 v[104:107], a[0:3], v[136:139], 0
Mp 104,4,152
Rb 52,1,37888
Mp 96,4,144
Mp 96,8,160
Rb 56,1,38912
Cv 112,112,113
Mp 104,8,168
Cv 113,114,115
Cv 114,116,117
Mp 104,12,184
Rb 60,1,40960
s_waitcnt lgkmcnt(8)
Mp 96,12,176
Cv 115,118,119
Mp 96,16,192
Rb 0,1,41984
Mp 104,16,200
s_add_u32 s29, s73, 0x9000
buffer_store_dwordx4 v[112:115], v1, s[20:23], s29 offen sc0 sc1
Mp 104,20,216
Rb 4,1,43008
Mp 96,20,208
Mp 96,24,224
Rb 8,1,44032
Mp 104,24,232
Mp 104,28,248
Rb 12,1,45056
s_waitcnt lgkmcnt(8)
Mp 96,28,240
v_mfma_f32_16x16x32_bf16 v[100:103], a[32:35], v[128:131], 0
Rb 16,1,46080
)" R"(
v_mfma_f32_16x16x32_bf16 v[108:111], a[32:35], v[136:139], 0
Mp 108,36,152
Rb 20,1,47104
Mp 100,36,144
Mp 100,40,160
Rb 24,1,49152
Cv 120,120,121
Mp 108,40,168
Cv 121,122,123
Cv 122,124,125
Mp 108,44,184
Rb 28,1,50176
s_waitcnt lgkmcnt(8)
Mp 100,44,176
Cv 123,126,127
Mp 100,48,192
Rb 32,1,51200
Mp 108,48,200
s_add_u32 s29, s73, 0x9400
buffer_store_dwordx4 v[120:123], v1, s[20:23], s29 offen sc0 sc1
Mp 108,52,216
Rb 36,1,52224
Mp 100,52,208
Mp 100,56,224
Rb 40,1,53248
Mp 108,56,232
Mp 108,60,248
Rb 44,1,54272
s_waitcnt lgkmcnt(8)
Mp 100,60,240
v_mfma_f32_16x16x32_bf16 v[112:115], a[0:3], v[128:131], 0
Rb 48,1,55296
v_mfma_f32_16x16x32_bf16 v[120:123], a[0:3], v[136:139], 0
Mp 120,4,152
Rb 52,1,57344
Mp 112,4,144
Mp 112,8,160
Rb 56,1,58368
Cv 96,96,97
Mp 120,8,168
Cv 97,98,99
Cv 98,100,101
Mp 120,12,184
Rb 60,1,59392
s_waitcnt lgkmcnt(8)
Mp 112,12,176
Cv 99,102,103
Mp 112,16,192
Rb 0,1,60416
Mp 120,16,200
s_add_u32 s29, s73, 0x9800
buffer_store_dwordx4 v[96:99], v1, s[20:23], s29 offen sc0 sc1
Mp 120,20,216
Rb 4,1,61440
Mp 112,20,208
Mp 112,24,224
Rb 8,1,62464
Mp 120,24,232
Mp 120,28,248
Rb 12,1,63488
s_waitcnt lgkmcnt(8)
Mp 112,28,240
v_mfma_f32_16x16x32_bf16 v[116:119], a[32:35], v[128:131], 0
Rb 16,10,0
)" R"(
v_mfma_f32_16x16x32_bf16 v[124:127], a[32:35], v[136:139], 0
Mp 124,36,152
Rb 20,10,1024
Mp 116,36,144
Mp 116,40,160
Rb 24,10,2048
Cv 104,104,105
Mp 124,40,168
Cv 105,106,107
Cv 106,108,109
Mp 124,44,184
Rb 28,10,3072
s_waitcnt lgkmcnt(8)
Mp 116,44,176
Cv 107,110,111
Mp 116,48,192
Rb 32,10,4096
Mp 124,48,200
s_add_u32 s29, s73, 0xc000
buffer_store_dwordx4 v[104:107], v1, s[20:23], s29 offen sc0 sc1
Mp 124,52,216
Rb 36,10,5120
Mp 116,52,208
Mp 116,56,224
Rb 40,10,6144
Mp 124,56,232
Mp 124,60,248
Rb 44,10,8192
s_waitcnt lgkmcnt(8)
Mp 116,60,240
v_mfma_f32_16x16x32_bf16 v[96:99], a[0:3], v[128:131], 0
Rb 48,10,9216
v_mfma_f32_16x16x32_bf16 v[104:107], a[0:3], v[136:139], 0
Mp 104,4,152
Rb 52,10,10240
Mp 96,4,144
Mp 96,8,160
Rb 56,10,11264
Cv 112,112,113
Mp 104,8,168
Cv 113,114,115
Cv 114,116,117
Mp 104,12,184
Rb 60,10,12288
s_waitcnt lgkmcnt(8)
Mp 96,12,176
Cv 115,118,119
Mp 96,16,192
Rb 0,10,13312
Mp 104,16,200
s_add_u32 s29, s73, 0xc400
buffer_store_dwordx4 v[112:115], v1, s[20:23], s29 offen sc0 sc1
Mp 104,20,216
Rb 4,10,14336
Mp 96,20,208
Mp 96,24,224
Rb 8,10,16384
Mp 104,24,232
Mp 104,28,248
Rb 12,10,17408
s_waitcnt lgkmcnt(8)
Mp 96,28,240
v_mfma_f32_16x16x32_bf16 v[100:103], a[32:35], v[128:131], 0
Rb 16,10,18432
)" R"(
v_mfma_f32_16x16x32_bf16 v[108:111], a[32:35], v[136:139], 0
Mp 108,36,152
Rb 20,10,19456
Mp 100,36,144
Mp 100,40,160
Rb 24,10,20480
Cv 120,120,121
Mp 108,40,168
Cv 121,122,123
Cv 122,124,125
Mp 108,44,184
Rb 28,10,21504
s_waitcnt lgkmcnt(8)
Mp 100,44,176
Cv 123,126,127
Mp 100,48,192
Rb 32,10,22528
Mp 108,48,200
s_add_u32 s29, s73, 0xc800
buffer_store_dwordx4 v[120:123], v1, s[20:23], s29 offen sc0 sc1
Mp 108,52,216
Rb 36,10,24576
Mp 100,52,208
Mp 100,56,224
Rb 40,10,25600
Mp 108,56,232
Mp 108,60,248
Rb 44,10,26624
s_waitcnt lgkmcnt(8)
Mp 100,60,240
v_mfma_f32_16x16x32_bf16 v[112:115], a[0:3], v[128:131], 0
Rb 48,10,27648
v_mfma_f32_16x16x32_bf16 v[120:123], a[0:3], v[136:139], 0
Mp 120,4,152
Rb 52,10,28672
Mp 112,4,144
Mp 112,8,160
Rb 56,10,29696
Cv 96,96,97
Mp 120,8,168
Cv 97,98,99
Cv 98,100,101
Mp 120,12,184
Rb 60,10,30720
s_waitcnt lgkmcnt(8)
Mp 112,12,176
Cv 99,102,103
Mp 112,16,192
s_add_u32 s29, s73, 0xcc00
buffer_store_dwordx4 v[96:99], v1, s[20:23], s29 offen sc0 sc1
Mp 120,16,200
Mp 120,20,216
Mp 112,20,208
Mp 112,24,224
Mp 120,24,232
Mp 120,28,248
s_waitcnt lgkmcnt(4)
Mp 112,28,240
v_mfma_f32_16x16x32_bf16 v[116:119], a[32:35], v[128:131], 0
v_mfma_f32_16x16x32_bf16 v[124:127], a[32:35], v[136:139], 0
Mp 124,36,152
Mp 116,36,144
Mp 116,40,160
Cv 104,104,105
)" R"(
Cv 105,106,107
Mp 124,40,168
Cv 106,108,109
Cv 107,110,111
Mp 124,44,184
s_waitcnt lgkmcnt(0)
Mp 116,44,176
s_add_u32 s29, s73, 0xd000
buffer_store_dwordx4 v[104:107], v1, s[20:23], s29 offen sc0 sc1
Mp 116,48,192
Mp 124,48,200
Mp 124,52,216
Mp 116,52,208
Mp 116,56,224
Mp 124,56,232
Mp 124,60,248
Mp 116,60,240
s_nop 7
Cv 112,112,113
Cv 113,114,115
Cv 114,116,117
Cv 115,118,119
s_add_u32 s29, s73, 0xd400
buffer_store_dwordx4 v[112:115], v1, s[20:23], s29 offen sc0 sc1
Cv 120,120,121
Cv 121,122,123
Cv 122,124,125
Cv 123,126,127
s_add_u32 s29, s73, 0xd800
buffer_store_dwordx4 v[120:123], v1, s[20:23], s29 offen sc0 sc1
s_branch .LzEND
.LzMASK0:
s_lshl1_add_u32 s28, s57, s33
s_mov_b32 s29, 0xff800000
s_cmp_gt_u32 s28, 5
s_cbranch_scc1 .LzMASKE0
s_cmp_eq_u32 s28, 5
s_cbranch_scc1 .LzMASKB0
v_mov_b32 v72, s29
v_mov_b32 v73, s29
v_mov_b32 v74, s29
v_mov_b32 v75, s29
v_mov_b32 v80, s29
v_mov_b32 v81, s29
v_mov_b32 v82, s29
v_mov_b32 v83, s29
v_mov_b32 v84, s29
v_mov_b32 v85, s29
v_mov_b32 v86, s29
v_mov_b32 v87, s29
v_mov_b32 v88, s29
v_mov_b32 v89, s29
v_mov_b32 v90, s29
v_mov_b32 v91, s29
v_mov_b32 v92, s29
v_mov_b32 v93, s29
v_mov_b32 v94, s29
v_mov_b32 v95, s29
s_cmp_eq_u32 s28, 4
s_cbranch_scc1 .LzMASKA0
v_mov_b32 v64, s29
v_mov_b32 v65, s29
v_mov_b32 v66, s29
v_mov_b32 v67, s29
)" R"(
v_mov_b32 v68, s29
v_mov_b32 v69, s29
v_mov_b32 v70, s29
v_mov_b32 v71, s29
v_mov_b32 v76, s29
v_mov_b32 v77, s29
v_mov_b32 v78, s29
v_mov_b32 v79, s29
.LzMASKE0:
s_setpc_b64 s[30:31]
.LzMASKA0:
s_mov_b32 exec_lo, 0xf0000
s_mov_b32 exec_hi, 0xfff00ff
v_mov_b32 v64, s29
v_mov_b32 v76, s29
s_mov_b32 exec_lo, 0x1f0001
s_mov_b32 exec_hi, 0x1fff01ff
v_mov_b32 v65, s29
v_mov_b32 v77, s29
s_mov_b32 exec_lo, 0x3f0003
s_mov_b32 exec_hi, 0x3fff03ff
v_mov_b32 v66, s29
v_mov_b32 v78, s29
s_mov_b32 exec_lo, 0x7f0007
s_mov_b32 exec_hi, 0x7fff07ff
v_mov_b32 v67, s29
v_mov_b32 v79, s29
s_mov_b64 exec, -1
s_setpc_b64 s[30:31]
.LzMASKB0:
s_nop 1
v_mov_b32 v88, s29
v_mov_b32 v89, s29
v_mov_b32 v90, s29
v_mov_b32 v91, s29
s_mov_b32 exec_lo, 0xf0000
s_mov_b32 exec_hi, 0xfff00ff
v_mov_b32 v80, s29
v_mov_b32 v92, s29
s_mov_b32 exec_lo, 0x1f0001
s_mov_b32 exec_hi, 0x1fff01ff
v_mov_b32 v81, s29
v_mov_b32 v93, s29
s_mov_b32 exec_lo, 0x3f0003
s_mov_b32 exec_hi, 0x3fff03ff
v_mov_b32 v82, s29
v_mov_b32 v94, s29
s_mov_b32 exec_lo, 0x7f0007
s_mov_b32 exec_hi, 0x7fff07ff
v_mov_b32 v83, s29
v_mov_b32 v95, s29
s_mov_b64 exec, -1
s_setpc_b64 s[30:31]
.LzFIX0:
s_nop 7
v_max_f32_e32 v28, v64, v65
v_max3_f32 v28, v28, v66, v67
v_max3_f32 v28, v28, v72, v73
v_max3_f32 v28, v28, v74, v75
v_max3_f32 v28, v28, v80, v81
v_max3_f32 v28, v28, v82, v83
v_max3_f32 v28, v28, v88, v89
v_max3_f32 v28, v28, v90, v91
v_mov_b32 v24, v28
)" R"(
s_nop 1
v_permlane32_swap_b32_e32 v28, v24
v_max_f32_e32 v28, v28, v24
v_mov_b32 v24, v28
s_nop 1
v_permlane16_swap_b32_e32 v28, v24
v_max_f32_e32 v28, v28, v24
v_max_f32_e32 v29, v68, v69
v_max3_f32 v29, v29, v70, v71
v_max3_f32 v29, v29, v76, v77
v_max3_f32 v29, v29, v78, v79
v_max3_f32 v29, v29, v84, v85
v_max3_f32 v29, v29, v86, v87
v_max3_f32 v29, v29, v92, v93
v_max3_f32 v29, v29, v94, v95
v_mov_b32 v24, v29
s_nop 1
v_permlane32_swap_b32_e32 v29, v24
v_max_f32_e32 v29, v29, v24
v_mov_b32 v24, v29
s_nop 1
v_permlane16_swap_b32_e32 v29, v24
v_max_f32_e32 v29, v29, v24
s_bitcmp1_b32 s88, 0
s_cbranch_scc1 .LzFIXF0
v_max_f32_e32 v28, 0, v28
v_sub_f32_e32 v24, 0, v28
v_exp_f32_e32 v24, v24
s_nop 0
v_mul_f32_e32 v12, v24, v12
v_mul_f32_e32 v128, v24, v128
v_mul_f32_e32 v129, v24, v129
v_mul_f32_e32 v130, v24, v130
v_mul_f32_e32 v131, v24, v131
v_mul_f32_e32 v132, v24, v132
v_mul_f32_e32 v133, v24, v133
v_mul_f32_e32 v134, v24, v134
v_mul_f32_e32 v135, v24, v135
v_mul_f32_e32 v144, v24, v144
v_mul_f32_e32 v145, v24, v145
v_mul_f32_e32 v146, v24, v146
v_mul_f32_e32 v147, v24, v147
v_mul_f32_e32 v148, v24, v148
v_mul_f32_e32 v149, v24, v149
v_mul_f32_e32 v150, v24, v150
v_mul_f32_e32 v151, v24, v151
v_mul_f32_e32 v160, v24, v160
v_mul_f32_e32 v161, v24, v161
v_mul_f32_e32 v162, v24, v162
v_mul_f32_e32 v163, v24, v163
v_mul_f32_e32 v164, v24, v164
v_mul_f32_e32 v165, v24, v165
v_mul_f32_e32 v166, v24, v166
v_mul_f32_e32 v167, v24, v167
v_mul_f32_e32 v176, v24, v176
v_mul_f32_e32 v177, v24, v177
v_mul_f32_e32 v178, v24, v178
v_mul_f32_e32 v179, v24, v179
v_mul_f32_e32 v180, v24, v180
v_mul_f32_e32 v181, v24, v181
v_mul_f32_e32 v182, v24, v182
v_mul_f32_e32 v183, v24, v183
v_mul_f32_e32 v192, v24, v192
v_mul_f32_e32 v193, v24, v193
)" R"(
v_mul_f32_e32 v194, v24, v194
v_mul_f32_e32 v195, v24, v195
v_mul_f32_e32 v196, v24, v196
v_mul_f32_e32 v197, v24, v197
v_mul_f32_e32 v198, v24, v198
v_mul_f32_e32 v199, v24, v199
v_mul_f32_e32 v208, v24, v208
v_mul_f32_e32 v209, v24, v209
v_mul_f32_e32 v210, v24, v210
v_mul_f32_e32 v211, v24, v211
v_mul_f32_e32 v212, v24, v212
v_mul_f32_e32 v213, v24, v213
v_mul_f32_e32 v214, v24, v214
v_mul_f32_e32 v215, v24, v215
v_mul_f32_e32 v224, v24, v224
v_mul_f32_e32 v225, v24, v225
v_mul_f32_e32 v226, v24, v226
v_mul_f32_e32 v227, v24, v227
v_mul_f32_e32 v228, v24, v228
v_mul_f32_e32 v229, v24, v229
v_mul_f32_e32 v230, v24, v230
v_mul_f32_e32 v231, v24, v231
v_mul_f32_e32 v240, v24, v240
v_mul_f32_e32 v241, v24, v241
v_mul_f32_e32 v242, v24, v242
v_mul_f32_e32 v243, v24, v243
v_mul_f32_e32 v244, v24, v244
v_mul_f32_e32 v245, v24, v245
v_mul_f32_e32 v246, v24, v246
v_mul_f32_e32 v247, v24, v247
v_max_f32_e32 v29, 0, v29
v_sub_f32_e32 v24, 0, v29
v_exp_f32_e32 v24, v24
s_nop 0
v_mul_f32_e32 v13, v24, v13
v_mul_f32_e32 v136, v24, v136
v_mul_f32_e32 v137, v24, v137
v_mul_f32_e32 v138, v24, v138
v_mul_f32_e32 v139, v24, v139
v_mul_f32_e32 v140, v24, v140
v_mul_f32_e32 v141, v24, v141
v_mul_f32_e32 v142, v24, v142
v_mul_f32_e32 v143, v24, v143
v_mul_f32_e32 v152, v24, v152
v_mul_f32_e32 v153, v24, v153
v_mul_f32_e32 v154, v24, v154
v_mul_f32_e32 v155, v24, v155
v_mul_f32_e32 v156, v24, v156
v_mul_f32_e32 v157, v24, v157
v_mul_f32_e32 v158, v24, v158
v_mul_f32_e32 v159, v24, v159
v_mul_f32_e32 v168, v24, v168
v_mul_f32_e32 v169, v24, v169
v_mul_f32_e32 v170, v24, v170
v_mul_f32_e32 v171, v24, v171
v_mul_f32_e32 v172, v24, v172
v_mul_f32_e32 v173, v24, v173
v_mul_f32_e32 v174, v24, v174
v_mul_f32_e32 v175, v24, v175
v_mul_f32_e32 v184, v24, v184
v_mul_f32_e32 v185, v24, v185
v_mul_f32_e32 v186, v24, v186
v_mul_f32_e32 v187, v24, v187
v_mul_f32_e32 v188, v24, v188
)" R"(
v_mul_f32_e32 v189, v24, v189
v_mul_f32_e32 v190, v24, v190
v_mul_f32_e32 v191, v24, v191
v_mul_f32_e32 v200, v24, v200
v_mul_f32_e32 v201, v24, v201
v_mul_f32_e32 v202, v24, v202
v_mul_f32_e32 v203, v24, v203
v_mul_f32_e32 v204, v24, v204
v_mul_f32_e32 v205, v24, v205
v_mul_f32_e32 v206, v24, v206
v_mul_f32_e32 v207, v24, v207
v_mul_f32_e32 v216, v24, v216
v_mul_f32_e32 v217, v24, v217
v_mul_f32_e32 v218, v24, v218
v_mul_f32_e32 v219, v24, v219
v_mul_f32_e32 v220, v24, v220
v_mul_f32_e32 v221, v24, v221
v_mul_f32_e32 v222, v24, v222
v_mul_f32_e32 v223, v24, v223
v_mul_f32_e32 v232, v24, v232
v_mul_f32_e32 v233, v24, v233
v_mul_f32_e32 v234, v24, v234
v_mul_f32_e32 v235, v24, v235
v_mul_f32_e32 v236, v24, v236
v_mul_f32_e32 v237, v24, v237
v_mul_f32_e32 v238, v24, v238
v_mul_f32_e32 v239, v24, v239
v_mul_f32_e32 v248, v24, v248
v_mul_f32_e32 v249, v24, v249
v_mul_f32_e32 v250, v24, v250
v_mul_f32_e32 v251, v24, v251
v_mul_f32_e32 v252, v24, v252
v_mul_f32_e32 v253, v24, v253
v_mul_f32_e32 v254, v24, v254
v_mul_f32_e32 v255, v24, v255
s_branch .LzFIXG0
.LzFIXF0:
v_cmp_lt_f32_e32 vcc, 0xff7fffff, v28
s_nop 1
v_cndmask_b32_e32 v28, 0, v28, vcc
v_cmp_lt_f32_e32 vcc, 0xff7fffff, v29
s_nop 1
v_cndmask_b32_e32 v29, 0, v29, vcc
.LzFIXG0:
v_sub_f32_e32 v16, v16, v28
v_sub_f32_e32 v17, v17, v28
v_sub_f32_e32 v18, v18, v28
v_sub_f32_e32 v19, v19, v28
v_sub_f32_e32 v64, v64, v28
v_sub_f32_e32 v65, v65, v28
v_sub_f32_e32 v66, v66, v28
v_sub_f32_e32 v67, v67, v28
v_sub_f32_e32 v72, v72, v28
v_sub_f32_e32 v73, v73, v28
v_sub_f32_e32 v74, v74, v28
v_sub_f32_e32 v75, v75, v28
v_sub_f32_e32 v80, v80, v28
v_sub_f32_e32 v81, v81, v28
v_sub_f32_e32 v82, v82, v28
v_sub_f32_e32 v83, v83, v28
v_sub_f32_e32 v88, v88, v28
v_sub_f32_e32 v89, v89, v28
v_sub_f32_e32 v90, v90, v28
v_sub_f32_e32 v91, v91, v28
)" R"(
v_sub_f32_e32 v20, v20, v29
v_sub_f32_e32 v21, v21, v29
v_sub_f32_e32 v22, v22, v29
v_sub_f32_e32 v23, v23, v29
v_sub_f32_e32 v68, v68, v29
v_sub_f32_e32 v69, v69, v29
v_sub_f32_e32 v70, v70, v29
v_sub_f32_e32 v71, v71, v29
v_sub_f32_e32 v76, v76, v29
v_sub_f32_e32 v77, v77, v29
v_sub_f32_e32 v78, v78, v29
v_sub_f32_e32 v79, v79, v29
v_sub_f32_e32 v84, v84, v29
v_sub_f32_e32 v85, v85, v29
v_sub_f32_e32 v86, v86, v29
v_sub_f32_e32 v87, v87, v29
v_sub_f32_e32 v92, v92, v29
v_sub_f32_e32 v93, v93, v29
v_sub_f32_e32 v94, v94, v29
v_sub_f32_e32 v95, v95, v29
s_bitcmp0_b32 s88, 1
s_cbranch_scc1 .LzFIXH0
v_sub_f32_e32 v96, v96, v28
v_sub_f32_e32 v97, v97, v28
v_sub_f32_e32 v98, v98, v28
v_sub_f32_e32 v99, v99, v28
v_sub_f32_e32 v104, v104, v28
v_sub_f32_e32 v105, v105, v28
v_sub_f32_e32 v106, v106, v28
v_sub_f32_e32 v107, v107, v28
v_sub_f32_e32 v112, v112, v28
v_sub_f32_e32 v113, v113, v28
v_sub_f32_e32 v114, v114, v28
v_sub_f32_e32 v115, v115, v28
v_sub_f32_e32 v120, v120, v28
v_sub_f32_e32 v121, v121, v28
v_sub_f32_e32 v122, v122, v28
v_sub_f32_e32 v123, v123, v28
v_sub_f32_e32 v100, v100, v29
v_sub_f32_e32 v101, v101, v29
v_sub_f32_e32 v102, v102, v29
v_sub_f32_e32 v103, v103, v29
v_sub_f32_e32 v108, v108, v29
v_sub_f32_e32 v109, v109, v29
v_sub_f32_e32 v110, v110, v29
v_sub_f32_e32 v111, v111, v29
v_sub_f32_e32 v116, v116, v29
v_sub_f32_e32 v117, v117, v29
v_sub_f32_e32 v118, v118, v29
v_sub_f32_e32 v119, v119, v29
v_sub_f32_e32 v124, v124, v29
v_sub_f32_e32 v125, v125, v29
v_sub_f32_e32 v126, v126, v29
v_sub_f32_e32 v127, v127, v29
.LzFIXH0:
v_exp_f32_e32 v32, v64
v_exp_f32_e32 v28, v65
s_nop 0
v_add_f32_e32 v14, v32, v28
Cv 32,32,28
v_exp_f32_e32 v33, v66
v_exp_f32_e32 v29, v67
v_add_f32_e32 v14, v14, v33
v_add_f32_e32 v14, v14, v29
)" R"(
Cv 33,33,29
v_exp_f32_e32 v34, v72
v_exp_f32_e32 v24, v73
v_add_f32_e32 v14, v14, v34
v_add_f32_e32 v14, v14, v24
Cv 34,34,24
v_exp_f32_e32 v35, v74
v_exp_f32_e32 v25, v75
v_add_f32_e32 v14, v14, v35
v_add_f32_e32 v14, v14, v25
Cv 35,35,25
v_exp_f32_e32 v36, v68
v_exp_f32_e32 v26, v69
s_nop 0
v_add_f32_e32 v15, v36, v26
Cv 36,36,26
v_exp_f32_e32 v37, v70
v_exp_f32_e32 v27, v71
v_add_f32_e32 v15, v15, v37
v_add_f32_e32 v15, v15, v27
Cv 37,37,27
v_exp_f32_e32 v38, v76
v_exp_f32_e32 v28, v77
v_add_f32_e32 v15, v15, v38
v_add_f32_e32 v15, v15, v28
Cv 38,38,28
v_exp_f32_e32 v39, v78
v_exp_f32_e32 v29, v79
v_add_f32_e32 v15, v15, v39
v_add_f32_e32 v15, v15, v29
Cv 39,39,29
v_exp_f32_e32 v40, v80
v_exp_f32_e32 v24, v81
v_add_f32_e32 v14, v14, v40
v_add_f32_e32 v14, v14, v24
Cv 40,40,24
v_exp_f32_e32 v41, v82
v_exp_f32_e32 v25, v83
v_add_f32_e32 v14, v14, v41
v_add_f32_e32 v14, v14, v25
Cv 41,41,25
v_exp_f32_e32 v42, v88
v_exp_f32_e32 v26, v89
v_add_f32_e32 v14, v14, v42
v_add_f32_e32 v14, v14, v26
Cv 42,42,26
v_exp_f32_e32 v43, v90
v_exp_f32_e32 v27, v91
v_add_f32_e32 v14, v14, v43
v_add_f32_e32 v14, v14, v27
Cv 43,43,27
v_exp_f32_e32 v44, v84
v_exp_f32_e32 v28, v85
v_add_f32_e32 v15, v15, v44
v_add_f32_e32 v15, v15, v28
Cv 44,44,28
v_exp_f32_e32 v45, v86
v_exp_f32_e32 v29, v87
v_add_f32_e32 v15, v15, v45
v_add_f32_e32 v15, v15, v29
Cv 45,45,29
v_exp_f32_e32 v46, v92
v_exp_f32_e32 v24, v93
v_add_f32_e32 v15, v15, v46
)" R"(
v_add_f32_e32 v15, v15, v24
Cv 46,46,24
v_exp_f32_e32 v47, v94
v_exp_f32_e32 v25, v95
v_add_f32_e32 v15, v15, v47
v_add_f32_e32 v15, v15, v25
Cv 47,47,25
s_nop 1
s_setpc_b64 s[30:31]
.LzMASK1:
s_lshl1_add_u32 s28, s57, s33
s_mov_b32 s29, 0xff800000
s_cmp_gt_u32 s28, 5
s_cbranch_scc1 .LzMASKE1
s_cmp_eq_u32 s28, 5
s_cbranch_scc1 .LzMASKB1
v_mov_b32 v104, s29
v_mov_b32 v105, s29
v_mov_b32 v106, s29
v_mov_b32 v107, s29
v_mov_b32 v112, s29
v_mov_b32 v113, s29
v_mov_b32 v114, s29
v_mov_b32 v115, s29
v_mov_b32 v116, s29
v_mov_b32 v117, s29
v_mov_b32 v118, s29
v_mov_b32 v119, s29
v_mov_b32 v120, s29
v_mov_b32 v121, s29
v_mov_b32 v122, s29
v_mov_b32 v123, s29
v_mov_b32 v124, s29
v_mov_b32 v125, s29
v_mov_b32 v126, s29
v_mov_b32 v127, s29
s_cmp_eq_u32 s28, 4
s_cbranch_scc1 .LzMASKA1
v_mov_b32 v96, s29
v_mov_b32 v97, s29
v_mov_b32 v98, s29
v_mov_b32 v99, s29
v_mov_b32 v100, s29
v_mov_b32 v101, s29
v_mov_b32 v102, s29
v_mov_b32 v103, s29
v_mov_b32 v108, s29
v_mov_b32 v109, s29
v_mov_b32 v110, s29
v_mov_b32 v111, s29
.LzMASKE1:
s_setpc_b64 s[30:31]
.LzMASKA1:
s_mov_b32 exec_lo, 0xf0000
s_mov_b32 exec_hi, 0xfff00ff
v_mov_b32 v96, s29
v_mov_b32 v108, s29
s_mov_b32 exec_lo, 0x1f0001
s_mov_b32 exec_hi, 0x1fff01ff
v_mov_b32 v97, s29
v_mov_b32 v109, s29
s_mov_b32 exec_lo, 0x3f0003
s_mov_b32 exec_hi, 0x3fff03ff
v_mov_b32 v98, s29
)" R"(
v_mov_b32 v110, s29
s_mov_b32 exec_lo, 0x7f0007
s_mov_b32 exec_hi, 0x7fff07ff
v_mov_b32 v99, s29
v_mov_b32 v111, s29
s_mov_b64 exec, -1
s_setpc_b64 s[30:31]
.LzMASKB1:
s_nop 1
v_mov_b32 v120, s29
v_mov_b32 v121, s29
v_mov_b32 v122, s29
v_mov_b32 v123, s29
s_mov_b32 exec_lo, 0xf0000
s_mov_b32 exec_hi, 0xfff00ff
v_mov_b32 v112, s29
v_mov_b32 v124, s29
s_mov_b32 exec_lo, 0x1f0001
s_mov_b32 exec_hi, 0x1fff01ff
v_mov_b32 v113, s29
v_mov_b32 v125, s29
s_mov_b32 exec_lo, 0x3f0003
s_mov_b32 exec_hi, 0x3fff03ff
v_mov_b32 v114, s29
v_mov_b32 v126, s29
s_mov_b32 exec_lo, 0x7f0007
s_mov_b32 exec_hi, 0x7fff07ff
v_mov_b32 v115, s29
v_mov_b32 v127, s29
s_mov_b64 exec, -1
s_setpc_b64 s[30:31]
.LzFIX1:
s_nop 7
v_max_f32_e32 v28, v96, v97
v_max3_f32 v28, v28, v98, v99
v_max3_f32 v28, v28, v104, v105
v_max3_f32 v28, v28, v106, v107
v_max3_f32 v28, v28, v112, v113
v_max3_f32 v28, v28, v114, v115
v_max3_f32 v28, v28, v120, v121
v_max3_f32 v28, v28, v122, v123
v_mov_b32 v24, v28
s_nop 1
v_permlane32_swap_b32_e32 v28, v24
v_max_f32_e32 v28, v28, v24
v_mov_b32 v24, v28
s_nop 1
v_permlane16_swap_b32_e32 v28, v24
v_max_f32_e32 v28, v28, v24
v_max_f32_e32 v29, v100, v101
v_max3_f32 v29, v29, v102, v103
v_max3_f32 v29, v29, v108, v109
v_max3_f32 v29, v29, v110, v111
v_max3_f32 v29, v29, v116, v117
v_max3_f32 v29, v29, v118, v119
v_max3_f32 v29, v29, v124, v125
v_max3_f32 v29, v29, v126, v127
v_mov_b32 v24, v29
s_nop 1
v_permlane32_swap_b32_e32 v29, v24
v_max_f32_e32 v29, v29, v24
v_mov_b32 v24, v29
s_nop 1
v_permlane16_swap_b32_e32 v29, v24
)" R"(
v_max_f32_e32 v29, v29, v24
s_bitcmp1_b32 s88, 0
s_cbranch_scc1 .LzFIXF1
v_max_f32_e32 v28, 0, v28
v_sub_f32_e32 v24, 0, v28
v_exp_f32_e32 v24, v24
s_nop 0
v_mul_f32_e32 v12, v24, v12
v_mul_f32_e32 v128, v24, v128
v_mul_f32_e32 v129, v24, v129
v_mul_f32_e32 v130, v24, v130
v_mul_f32_e32 v131, v24, v131
v_mul_f32_e32 v132, v24, v132
v_mul_f32_e32 v133, v24, v133
v_mul_f32_e32 v134, v24, v134
v_mul_f32_e32 v135, v24, v135
v_mul_f32_e32 v144, v24, v144
v_mul_f32_e32 v145, v24, v145
v_mul_f32_e32 v146, v24, v146
v_mul_f32_e32 v147, v24, v147
v_mul_f32_e32 v148, v24, v148
v_mul_f32_e32 v149, v24, v149
v_mul_f32_e32 v150, v24, v150
v_mul_f32_e32 v151, v24, v151
v_mul_f32_e32 v160, v24, v160
v_mul_f32_e32 v161, v24, v161
v_mul_f32_e32 v162, v24, v162
v_mul_f32_e32 v163, v24, v163
v_mul_f32_e32 v164, v24, v164
v_mul_f32_e32 v165, v24, v165
v_mul_f32_e32 v166, v24, v166
v_mul_f32_e32 v167, v24, v167
v_mul_f32_e32 v176, v24, v176
v_mul_f32_e32 v177, v24, v177
v_mul_f32_e32 v178, v24, v178
v_mul_f32_e32 v179, v24, v179
v_mul_f32_e32 v180, v24, v180
v_mul_f32_e32 v181, v24, v181
v_mul_f32_e32 v182, v24, v182
v_mul_f32_e32 v183, v24, v183
v_mul_f32_e32 v192, v24, v192
v_mul_f32_e32 v193, v24, v193
v_mul_f32_e32 v194, v24, v194
v_mul_f32_e32 v195, v24, v195
v_mul_f32_e32 v196, v24, v196
v_mul_f32_e32 v197, v24, v197
v_mul_f32_e32 v198, v24, v198
v_mul_f32_e32 v199, v24, v199
v_mul_f32_e32 v208, v24, v208
v_mul_f32_e32 v209, v24, v209
v_mul_f32_e32 v210, v24, v210
v_mul_f32_e32 v211, v24, v211
v_mul_f32_e32 v212, v24, v212
v_mul_f32_e32 v213, v24, v213
v_mul_f32_e32 v214, v24, v214
v_mul_f32_e32 v215, v24, v215
v_mul_f32_e32 v224, v24, v224
v_mul_f32_e32 v225, v24, v225
v_mul_f32_e32 v226, v24, v226
v_mul_f32_e32 v227, v24, v227
v_mul_f32_e32 v228, v24, v228
v_mul_f32_e32 v229, v24, v229
v_mul_f32_e32 v230, v24, v230
v_mul_f32_e32 v231, v24, v231
)" R"(
v_mul_f32_e32 v240, v24, v240
v_mul_f32_e32 v241, v24, v241
v_mul_f32_e32 v242, v24, v242
v_mul_f32_e32 v243, v24, v243
v_mul_f32_e32 v244, v24, v244
v_mul_f32_e32 v245, v24, v245
v_mul_f32_e32 v246, v24, v246
v_mul_f32_e32 v247, v24, v247
v_max_f32_e32 v29, 0, v29
v_sub_f32_e32 v24, 0, v29
v_exp_f32_e32 v24, v24
s_nop 0
v_mul_f32_e32 v13, v24, v13
v_mul_f32_e32 v136, v24, v136
v_mul_f32_e32 v137, v24, v137
v_mul_f32_e32 v138, v24, v138
v_mul_f32_e32 v139, v24, v139
v_mul_f32_e32 v140, v24, v140
v_mul_f32_e32 v141, v24, v141
v_mul_f32_e32 v142, v24, v142
v_mul_f32_e32 v143, v24, v143
v_mul_f32_e32 v152, v24, v152
v_mul_f32_e32 v153, v24, v153
v_mul_f32_e32 v154, v24, v154
v_mul_f32_e32 v155, v24, v155
v_mul_f32_e32 v156, v24, v156
v_mul_f32_e32 v157, v24, v157
v_mul_f32_e32 v158, v24, v158
v_mul_f32_e32 v159, v24, v159
v_mul_f32_e32 v168, v24, v168
v_mul_f32_e32 v169, v24, v169
v_mul_f32_e32 v170, v24, v170
v_mul_f32_e32 v171, v24, v171
v_mul_f32_e32 v172, v24, v172
v_mul_f32_e32 v173, v24, v173
v_mul_f32_e32 v174, v24, v174
v_mul_f32_e32 v175, v24, v175
v_mul_f32_e32 v184, v24, v184
v_mul_f32_e32 v185, v24, v185
v_mul_f32_e32 v186, v24, v186
v_mul_f32_e32 v187, v24, v187
v_mul_f32_e32 v188, v24, v188
v_mul_f32_e32 v189, v24, v189
v_mul_f32_e32 v190, v24, v190
v_mul_f32_e32 v191, v24, v191
v_mul_f32_e32 v200, v24, v200
v_mul_f32_e32 v201, v24, v201
v_mul_f32_e32 v202, v24, v202
v_mul_f32_e32 v203, v24, v203
v_mul_f32_e32 v204, v24, v204
v_mul_f32_e32 v205, v24, v205
v_mul_f32_e32 v206, v24, v206
v_mul_f32_e32 v207, v24, v207
v_mul_f32_e32 v216, v24, v216
v_mul_f32_e32 v217, v24, v217
v_mul_f32_e32 v218, v24, v218
v_mul_f32_e32 v219, v24, v219
v_mul_f32_e32 v220, v24, v220
v_mul_f32_e32 v221, v24, v221
v_mul_f32_e32 v222, v24, v222
v_mul_f32_e32 v223, v24, v223
v_mul_f32_e32 v232, v24, v232
v_mul_f32_e32 v233, v24, v233
v_mul_f32_e32 v234, v24, v234
)" R"(
v_mul_f32_e32 v235, v24, v235
v_mul_f32_e32 v236, v24, v236
v_mul_f32_e32 v237, v24, v237
v_mul_f32_e32 v238, v24, v238
v_mul_f32_e32 v239, v24, v239
v_mul_f32_e32 v248, v24, v248
v_mul_f32_e32 v249, v24, v249
v_mul_f32_e32 v250, v24, v250
v_mul_f32_e32 v251, v24, v251
v_mul_f32_e32 v252, v24, v252
v_mul_f32_e32 v253, v24, v253
v_mul_f32_e32 v254, v24, v254
v_mul_f32_e32 v255, v24, v255
s_branch .LzFIXG1
.LzFIXF1:
v_cmp_lt_f32_e32 vcc, 0xff7fffff, v28
s_nop 1
v_cndmask_b32_e32 v28, 0, v28, vcc
v_cmp_lt_f32_e32 vcc, 0xff7fffff, v29
s_nop 1
v_cndmask_b32_e32 v29, 0, v29, vcc
.LzFIXG1:
v_sub_f32_e32 v16, v16, v28
v_sub_f32_e32 v17, v17, v28
v_sub_f32_e32 v18, v18, v28
v_sub_f32_e32 v19, v19, v28
v_sub_f32_e32 v96, v96, v28
v_sub_f32_e32 v97, v97, v28
v_sub_f32_e32 v98, v98, v28
v_sub_f32_e32 v99, v99, v28
v_sub_f32_e32 v104, v104, v28
v_sub_f32_e32 v105, v105, v28
v_sub_f32_e32 v106, v106, v28
v_sub_f32_e32 v107, v107, v28
v_sub_f32_e32 v112, v112, v28
v_sub_f32_e32 v113, v113, v28
v_sub_f32_e32 v114, v114, v28
v_sub_f32_e32 v115, v115, v28
v_sub_f32_e32 v120, v120, v28
v_sub_f32_e32 v121, v121, v28
v_sub_f32_e32 v122, v122, v28
v_sub_f32_e32 v123, v123, v28
v_sub_f32_e32 v20, v20, v29
v_sub_f32_e32 v21, v21, v29
v_sub_f32_e32 v22, v22, v29
v_sub_f32_e32 v23, v23, v29
v_sub_f32_e32 v100, v100, v29
v_sub_f32_e32 v101, v101, v29
v_sub_f32_e32 v102, v102, v29
v_sub_f32_e32 v103, v103, v29
v_sub_f32_e32 v108, v108, v29
v_sub_f32_e32 v109, v109, v29
v_sub_f32_e32 v110, v110, v29
v_sub_f32_e32 v111, v111, v29
v_sub_f32_e32 v116, v116, v29
v_sub_f32_e32 v117, v117, v29
v_sub_f32_e32 v118, v118, v29
v_sub_f32_e32 v119, v119, v29
v_sub_f32_e32 v124, v124, v29
v_sub_f32_e32 v125, v125, v29
v_sub_f32_e32 v126, v126, v29
v_sub_f32_e32 v127, v127, v29
s_bitcmp0_b32 s88, 1
s_cbranch_scc1 .LzFIXH1
)" R"(
v_sub_f32_e32 v64, v64, v28
v_sub_f32_e32 v65, v65, v28
v_sub_f32_e32 v66, v66, v28
v_sub_f32_e32 v67, v67, v28
v_sub_f32_e32 v72, v72, v28
v_sub_f32_e32 v73, v73, v28
v_sub_f32_e32 v74, v74, v28
v_sub_f32_e32 v75, v75, v28
v_sub_f32_e32 v80, v80, v28
v_sub_f32_e32 v81, v81, v28
v_sub_f32_e32 v82, v82, v28
v_sub_f32_e32 v83, v83, v28
v_sub_f32_e32 v88, v88, v28
v_sub_f32_e32 v89, v89, v28
v_sub_f32_e32 v90, v90, v28
v_sub_f32_e32 v91, v91, v28
v_sub_f32_e32 v68, v68, v29
v_sub_f32_e32 v69, v69, v29
v_sub_f32_e32 v70, v70, v29
v_sub_f32_e32 v71, v71, v29
v_sub_f32_e32 v76, v76, v29
v_sub_f32_e32 v77, v77, v29
v_sub_f32_e32 v78, v78, v29
v_sub_f32_e32 v79, v79, v29
v_sub_f32_e32 v84, v84, v29
v_sub_f32_e32 v85, v85, v29
v_sub_f32_e32 v86, v86, v29
v_sub_f32_e32 v87, v87, v29
v_sub_f32_e32 v92, v92, v29
v_sub_f32_e32 v93, v93, v29
v_sub_f32_e32 v94, v94, v29
v_sub_f32_e32 v95, v95, v29
.LzFIXH1:
v_exp_f32_e32 v48, v96
v_exp_f32_e32 v26, v97
s_nop 0
v_add_f32_e32 v14, v48, v26
Cv 48,48,26
v_exp_f32_e32 v49, v98
v_exp_f32_e32 v27, v99
v_add_f32_e32 v14, v14, v49
v_add_f32_e32 v14, v14, v27
Cv 49,49,27
v_exp_f32_e32 v50, v104
v_exp_f32_e32 v28, v105
v_add_f32_e32 v14, v14, v50
v_add_f32_e32 v14, v14, v28
Cv 50,50,28
v_exp_f32_e32 v51, v106
v_exp_f32_e32 v29, v107
v_add_f32_e32 v14, v14, v51
v_add_f32_e32 v14, v14, v29
Cv 51,51,29
v_exp_f32_e32 v52, v100
v_exp_f32_e32 v24, v101
s_nop 0
v_add_f32_e32 v15, v52, v24
Cv 52,52,24
v_exp_f32_e32 v53, v102
v_exp_f32_e32 v25, v103
v_add_f32_e32 v15, v15, v53
v_add_f32_e32 v15, v15, v25
Cv 53,53,25
v_exp_f32_e32 v54, v108
)" R"(
v_exp_f32_e32 v26, v109
v_add_f32_e32 v15, v15, v54
v_add_f32_e32 v15, v15, v26
Cv 54,54,26
v_exp_f32_e32 v55, v110
v_exp_f32_e32 v27, v111
v_add_f32_e32 v15, v15, v55
v_add_f32_e32 v15, v15, v27
Cv 55,55,27
v_exp_f32_e32 v56, v112
v_exp_f32_e32 v28, v113
v_add_f32_e32 v14, v14, v56
v_add_f32_e32 v14, v14, v28
Cv 56,56,28
v_exp_f32_e32 v57, v114
v_exp_f32_e32 v29, v115
v_add_f32_e32 v14, v14, v57
v_add_f32_e32 v14, v14, v29
Cv 57,57,29
v_exp_f32_e32 v58, v120
v_exp_f32_e32 v24, v121
v_add_f32_e32 v14, v14, v58
v_add_f32_e32 v14, v14, v24
Cv 58,58,24
v_exp_f32_e32 v59, v122
v_exp_f32_e32 v25, v123
v_add_f32_e32 v14, v14, v59
v_add_f32_e32 v14, v14, v25
Cv 59,59,25
v_exp_f32_e32 v60, v116
v_exp_f32_e32 v26, v117
v_add_f32_e32 v15, v15, v60
v_add_f32_e32 v15, v15, v26
Cv 60,60,26
v_exp_f32_e32 v61, v118
v_exp_f32_e32 v27, v119
v_add_f32_e32 v15, v15, v61
v_add_f32_e32 v15, v15, v27
Cv 61,61,27
v_exp_f32_e32 v62, v124
v_exp_f32_e32 v28, v125
v_add_f32_e32 v15, v15, v62
v_add_f32_e32 v15, v15, v28
Cv 62,62,28
v_exp_f32_e32 v63, v126
v_exp_f32_e32 v29, v127
v_add_f32_e32 v15, v15, v63
v_add_f32_e32 v15, v15, v29
Cv 63,63,29
s_nop 1
s_setpc_b64 s[30:31]
.Lzsp1:
s_sub_u32 s53, s53, 1
s_cbranch_scc1 .Lzsq1
s_sleep 1
buffer_load_dword v4, v2, s[20:23], s85 offen sc0 sc1
s_waitcnt vmcnt(0)
v_xor_b32 v4, s3, v4
v_cmp_gt_u32_e32 vcc, 2, v4
s_andn2_b32 s28, 0xff, vcc_lo
s_cbranch_scc1 .Lzsp1
s_branch .Lzok1
.Lzsq1:
s_mov_b32 s53, 0
)" R"(
s_branch .Lzok1
.Lzsp2:
s_sub_u32 s53, s53, 1
s_cbranch_scc1 .Lzsq2
s_sleep 1
buffer_load_dword v4, v2, s[20:23], s85 offen sc0 sc1
s_waitcnt vmcnt(0)
v_xor_b32 v4, s3, v4
v_cmp_gt_u32_e32 vcc, 2, v4
s_andn2_b32 s28, 0xff, vcc_lo
s_cbranch_scc1 .Lzsp2
s_branch .Lzok2
.Lzsq2:
s_mov_b32 s53, 0
s_branch .Lzok2
.Lzsp3:
s_sub_u32 s53, s53, 1
s_cbranch_scc1 .Lzsq3
s_sleep 1
buffer_load_dword v4, v5, s[20:23], s50 offen offset:0 sc0 sc1
s_waitcnt vmcnt(0)
v_cmp_eq_u32_e32 vcc, s3, v4
s_andn2_b64 s[28:29], s[60:61], vcc
s_cbranch_scc1 .Lzsp3
s_branch .Lzok3
.Lzsq3:
s_mov_b32 s53, 0
s_branch .Lzok3
.Lzsp4:
s_sub_u32 s53, s53, 1
s_cbranch_scc1 .Lzsq4
s_sleep 1
buffer_load_dword v4, v7, s[20:23], s52 offen offset:0 sc0 sc1
s_waitcnt vmcnt(0)
v_cmp_eq_u32_e32 vcc, s3, v4
s_andn2_b64 s[28:29], 1, vcc
s_cbranch_scc1 .Lzsp4
s_branch .Lzok4
.Lzsq4:
s_mov_b32 s53, 0
s_branch .Lzok4
.Lzsp5:
s_sub_u32 s53, s53, 1
s_cbranch_scc1 .Lzsq5
s_sleep 1
buffer_load_dword v4, v5, s[20:23], s50 offen offset:0 sc0 sc1
s_waitcnt vmcnt(0)
v_cmp_eq_u32_e32 vcc, s3, v4
s_andn2_b64 s[28:29], s[60:61], vcc
s_cbranch_scc1 .Lzsp5
s_branch .Lzok5
.Lzsq5:
s_mov_b32 s53, 0
s_branch .Lzok5
.Lzsp6:
s_sub_u32 s53, s53, 1
s_cbranch_scc1 .Lzsq6
s_sleep 1
buffer_load_dword v4, v7, s[20:23], s83 offen offset:4 sc0 sc1
s_waitcnt vmcnt(0)
v_cmp_eq_u32_e32 vcc, s3, v4
s_andn2_b64 s[28:29], 1, vcc
s_cbranch_scc1 .Lzsp6
s_branch .Lzok6
)" R"(
.Lzsq6:
s_mov_b32 s53, 0
s_branch .Lzok6
.LzT2S:
s_cmp_lg_u32 s33, 0
s_cbranch_scc1 .LzT2E
.LzT2:
s_mov_b32 s28, 0xc100000
buffer_load_dwordx2 v[32:33], v1, s[20:23], s28 offen offset:0 sc0 sc1
buffer_load_dwordx2 v[34:35], v1, s[20:23], s28 offen offset:1024 sc0 sc1
buffer_load_dwordx2 v[36:37], v1, s[20:23], s28 offen offset:2048 sc0 sc1
buffer_load_dwordx2 v[38:39], v1, s[20:23], s28 offen offset:3072 sc0 sc1
s_mov_b32 s28, 0xc101000
buffer_load_dwordx2 v[40:41], v1, s[20:23], s28 offen offset:0 sc0 sc1
buffer_load_dwordx2 v[42:43], v1, s[20:23], s28 offen offset:1024 sc0 sc1
buffer_load_dwordx2 v[44:45], v1, s[20:23], s28 offen offset:2048 sc0 sc1
buffer_load_dwordx2 v[46:47], v1, s[20:23], s28 offen offset:3072 sc0 sc1
s_mov_b32 s28, 0xc102000
buffer_load_dwordx2 v[48:49], v1, s[20:23], s28 offen offset:0 sc0 sc1
buffer_load_dwordx2 v[50:51], v1, s[20:23], s28 offen offset:1024 sc0 sc1
buffer_load_dwordx2 v[52:53], v1, s[20:23], s28 offen offset:2048 sc0 sc1
buffer_load_dwordx2 v[54:55], v1, s[20:23], s28 offen offset:3072 sc0 sc1
s_mov_b32 s28, 0xc103000
buffer_load_dwordx2 v[56:57], v1, s[20:23], s28 offen offset:0 sc0 sc1
buffer_load_dwordx2 v[58:59], v1, s[20:23], s28 offen offset:1024 sc0 sc1
buffer_load_dwordx2 v[60:61], v1, s[20:23], s28 offen offset:2048 sc0 sc1
buffer_load_dwordx2 v[62:63], v1, s[20:23], s28 offen offset:3072 sc0 sc1
s_mov_b32 s28, 0xc104000
buffer_load_dwordx2 v[64:65], v1, s[20:23], s28 offen offset:0 sc0 sc1
buffer_load_dwordx2 v[66:67], v1, s[20:23], s28 offen offset:1024 sc0 sc1
buffer_load_dwordx2 v[68:69], v1, s[20:23], s28 offen offset:2048 sc0 sc1
buffer_load_dwordx2 v[70:71], v1, s[20:23], s28 offen offset:3072 sc0 sc1
s_mov_b32 s28, 0xc105000
buffer_load_dwordx2 v[72:73], v1, s[20:23], s28 offen offset:0 sc0 sc1
buffer_load_dwordx2 v[74:75], v1, s[20:23], s28 offen offset:1024 sc0 sc1
buffer_load_dwordx2 v[76:77], v1, s[20:23], s28 offen offset:2048 sc0 sc1
buffer_load_dwordx2 v[78:79], v1, s[20:23], s28 offen offset:3072 sc0 sc1
s_mov_b32 s28, 0xc106000
buffer_load_dwordx2 v[80:81], v1, s[20:23], s28 offen offset:0 sc0 sc1
buffer_load_dwordx2 v[82:83], v1, s[20:23], s28 offen offset:1024 sc0 sc1
buffer_load_dwordx2 v[84:85], v1, s[20:23], s28 offen offset:2048 sc0 sc1
buffer_load_dwordx2 v[86:87], v1, s[20:23], s28 offen offset:3072 sc0 sc1
s_mov_b32 s28, 0xc107000
buffer_load_dwordx2 v[88:89], v1, s[20:23], s28 offen offset:0 sc0 sc1
buffer_load_dwordx2 v[90:91], v1, s[20:23], s28 offen offset:1024 sc0 sc1
buffer_load_dwordx2 v[92:93], v1, s[20:23], s28 offen offset:2048 sc0 sc1
buffer_load_dwordx2 v[94:95], v1, s[20:23], s28 offen offset:3072 sc0 sc1
v_mov_b32 v26, 0
v_mov_b32 v27, -1
s_waitcnt vmcnt(0)
v_max3_u32 v26, v26, v33, v35
v_min3_u32 v27, v27, v33, v35
v_max3_u32 v26, v26, v37, v39
v_min3_u32 v27, v27, v37, v39
v_max3_u32 v26, v26, v41, v43
v_min3_u32 v27, v27, v41, v43
v_max3_u32 v26, v26, v45, v47
v_min3_u32 v27, v27, v45, v47
v_max3_u32 v26, v26, v49, v51
v_min3_u32 v27, v27, v49, v51
v_max3_u32 v26, v26, v53, v55
v_min3_u32 v27, v27, v53, v55
v_max3_u32 v26, v26, v57, v59
v_min3_u32 v27, v27, v57, v59
)" R"(
v_max3_u32 v26, v26, v61, v63
v_min3_u32 v27, v27, v61, v63
v_max3_u32 v26, v26, v65, v67
v_min3_u32 v27, v27, v65, v67
v_max3_u32 v26, v26, v69, v71
v_min3_u32 v27, v27, v69, v71
v_max3_u32 v26, v26, v73, v75
v_min3_u32 v27, v27, v73, v75
v_max3_u32 v26, v26, v77, v79
v_min3_u32 v27, v27, v77, v79
v_max3_u32 v26, v26, v81, v83
v_min3_u32 v27, v27, v81, v83
v_max3_u32 v26, v26, v85, v87
v_min3_u32 v27, v27, v85, v87
v_max3_u32 v26, v26, v89, v91
v_min3_u32 v27, v27, v89, v91
v_max3_u32 v26, v26, v93, v95
v_min3_u32 v27, v27, v93, v95
v_cmp_ne_u32_e32 vcc, s3, v26
v_cmp_ne_u32_e64 s[28:29], s3, v27
s_or_b64 vcc, vcc, s[28:29]
s_cbranch_scc0 .LzT2G
s_sub_u32 s53, s53, 1
s_cbranch_scc0 .LzT2
s_mov_b32 s53, 0
.LzT2G:
v_max_u32 v26, v32, v34
v_min_u32 v27, v32, v34
v_med3_u32 v27, v26, v27, v36
v_max_u32 v26, v26, v36
v_med3_u32 v27, v26, v27, v38
v_max_u32 v26, v26, v38
v_med3_u32 v27, v26, v27, v40
v_max_u32 v26, v26, v40
v_med3_u32 v27, v26, v27, v42
v_max_u32 v26, v26, v42
v_med3_u32 v27, v26, v27, v44
v_max_u32 v26, v26, v44
v_med3_u32 v27, v26, v27, v46
v_max_u32 v26, v26, v46
v_med3_u32 v27, v26, v27, v48
v_max_u32 v26, v26, v48
v_med3_u32 v27, v26, v27, v50
v_max_u32 v26, v26, v50
v_med3_u32 v27, v26, v27, v52
v_max_u32 v26, v26, v52
v_med3_u32 v27, v26, v27, v54
v_max_u32 v26, v26, v54
v_med3_u32 v27, v26, v27, v56
v_max_u32 v26, v26, v56
v_med3_u32 v27, v26, v27, v58
v_max_u32 v26, v26, v58
v_med3_u32 v27, v26, v27, v60
v_max_u32 v26, v26, v60
v_med3_u32 v27, v26, v27, v62
v_max_u32 v26, v26, v62
v_med3_u32 v27, v26, v27, v64
v_max_u32 v26, v26, v64
v_med3_u32 v27, v26, v27, v66
v_max_u32 v26, v26, v66
v_med3_u32 v27, v26, v27, v68
v_max_u32 v26, v26, v68
v_med3_u32 v27, v26, v27, v70
v_max_u32 v26, v26, v70
)" R"(
v_med3_u32 v27, v26, v27, v72
v_max_u32 v26, v26, v72
v_med3_u32 v27, v26, v27, v74
v_max_u32 v26, v26, v74
v_med3_u32 v27, v26, v27, v76
v_max_u32 v26, v26, v76
v_med3_u32 v27, v26, v27, v78
v_max_u32 v26, v26, v78
v_med3_u32 v27, v26, v27, v80
v_max_u32 v26, v26, v80
v_med3_u32 v27, v26, v27, v82
v_max_u32 v26, v26, v82
v_med3_u32 v27, v26, v27, v84
v_max_u32 v26, v26, v84
v_med3_u32 v27, v26, v27, v86
v_max_u32 v26, v26, v86
v_med3_u32 v27, v26, v27, v88
v_max_u32 v26, v26, v88
v_med3_u32 v27, v26, v27, v90
v_max_u32 v26, v26, v90
v_med3_u32 v27, v26, v27, v92
v_max_u32 v26, v26, v92
v_med3_u32 v27, v26, v27, v94
v_max_u32 v26, v26, v94
v_mov_b32 v24, v26
v_mov_b32 v25, v27
s_nop 1
v_permlane32_swap_b32_e32 v26, v24
v_permlane32_swap_b32_e32 v27, v25
v_max_u32 v27, v27, v25
v_med3_u32 v27, v26, v27, v24
v_max_u32 v26, v26, v24
v_mov_b32 v24, v26
v_mov_b32 v25, v27
s_nop 1
v_permlane16_swap_b32_e32 v26, v24
v_permlane16_swap_b32_e32 v27, v25
v_max_u32 v27, v27, v25
v_med3_u32 v27, v26, v27, v24
v_max_u32 v26, v26, v24
s_nop 1
v_mov_b32_dpp v24, v26 row_ror:8 row_mask:0xf bank_mask:0xf
v_mov_b32_dpp v25, v27 row_ror:8 row_mask:0xf bank_mask:0xf
v_max_u32 v27, v27, v25
v_med3_u32 v27, v26, v27, v24
v_max_u32 v26, v26, v24
s_nop 1
v_mov_b32_dpp v24, v26 row_ror:4 row_mask:0xf bank_mask:0xf
v_mov_b32_dpp v25, v27 row_ror:4 row_mask:0xf bank_mask:0xf
v_max_u32 v27, v27, v25
v_med3_u32 v27, v26, v27, v24
v_max_u32 v26, v26, v24
s_nop 1
v_mov_b32_dpp v24, v26 row_ror:2 row_mask:0xf bank_mask:0xf
v_mov_b32_dpp v25, v27 row_ror:2 row_mask:0xf bank_mask:0xf
v_max_u32 v27, v27, v25
v_med3_u32 v27, v26, v27, v24
v_max_u32 v26, v26, v24
s_nop 1
v_mov_b32_dpp v24, v26 row_ror:1 row_mask:0xf bank_mask:0xf
v_mov_b32_dpp v25, v27 row_ror:1 row_mask:0xf bank_mask:0xf
v_max_u32 v27, v27, v25
v_med3_u32 v27, v26, v27, v24
v_max_u32 v26, v26, v24
)" R"(
v_and_b32 v26, 0xffff, v26
v_lshl_or_b32 v26, v27, 16, v26
v_not_b32 v24, v26
v_mov_b32 v25, s3
s_mov_b32 s28, 0xc108000
buffer_store_dwordx2 v[24:25], v7, s[20:23], s28 offen sc0 sc1
s_branch .LzT2E
.LzA7:
s_waitcnt vmcnt(0)
s_nop 7
v_mul_f32_e32 v96, s54, v96
v_mul_f32_e32 v97, s54, v97
Cv 96,96,97
v_accvgpr_write_b32 a112, v96
v_mul_f32_e32 v98, s54, v98
v_mul_f32_e32 v99, s54, v99
Cv 98,98,99
v_accvgpr_write_b32 a113, v98
v_mul_f32_e32 v100, s54, v100
v_mul_f32_e32 v101, s54, v101
Cv 100,100,101
v_accvgpr_write_b32 a114, v100
v_mul_f32_e32 v102, s54, v102
v_mul_f32_e32 v103, s54, v103
Cv 102,102,103
v_accvgpr_write_b32 a115, v102
v_mul_f32_e32 v104, s54, v104
v_mul_f32_e32 v105, s54, v105
Cv 104,104,105
v_accvgpr_write_b32 a116, v104
v_mul_f32_e32 v106, s54, v106
v_mul_f32_e32 v107, s54, v107
Cv 106,106,107
v_accvgpr_write_b32 a117, v106
v_mul_f32_e32 v108, s54, v108
v_mul_f32_e32 v109, s54, v109
Cv 108,108,109
v_accvgpr_write_b32 a118, v108
v_mul_f32_e32 v110, s54, v110
v_mul_f32_e32 v111, s54, v111
Cv 110,110,111
v_accvgpr_write_b32 a119, v110
v_mul_f32_e32 v112, s54, v112
v_mul_f32_e32 v113, s54, v113
Cv 112,112,113
v_accvgpr_write_b32 a120, v112
v_mul_f32_e32 v114, s54, v114
v_mul_f32_e32 v115, s54, v115
Cv 114,114,115
v_accvgpr_write_b32 a121, v114
v_mul_f32_e32 v116, s54, v116
v_mul_f32_e32 v117, s54, v117
Cv 116,116,117
v_accvgpr_write_b32 a122, v116
v_mul_f32_e32 v118, s54, v118
v_mul_f32_e32 v119, s54, v119
Cv 118,118,119
v_accvgpr_write_b32 a123, v118
v_mul_f32_e32 v120, s54, v120
v_mul_f32_e32 v121, s54, v121
Cv 120,120,121
v_accvgpr_write_b32 a124, v120
v_mul_f32_e32 v122, s54, v122
v_mul_f32_e32 v123, s54, v123
)" R"(
Cv 122,122,123
v_accvgpr_write_b32 a125, v122
v_mul_f32_e32 v124, s54, v124
v_mul_f32_e32 v125, s54, v125
Cv 124,124,125
v_accvgpr_write_b32 a126, v124
v_mul_f32_e32 v126, s54, v126
v_mul_f32_e32 v127, s54, v127
Cv 126,126,127
v_accvgpr_write_b32 a127, v126
v_accvgpr_mov_b32 a64, a224
v_accvgpr_mov_b32 a65, a225
v_accvgpr_mov_b32 a66, a226
v_accvgpr_mov_b32 a67, a227
v_accvgpr_mov_b32 a68, a228
v_accvgpr_mov_b32 a69, a229
v_accvgpr_mov_b32 a70, a230
v_accvgpr_mov_b32 a71, a231
v_accvgpr_mov_b32 a72, a232
v_accvgpr_mov_b32 a73, a233
v_accvgpr_mov_b32 a74, a234
v_accvgpr_mov_b32 a75, a235
v_accvgpr_mov_b32 a76, a236
v_accvgpr_mov_b32 a77, a237
v_accvgpr_mov_b32 a78, a238
v_accvgpr_mov_b32 a79, a239
v_accvgpr_mov_b32 a80, a240
v_accvgpr_mov_b32 a81, a241
v_accvgpr_mov_b32 a82, a242
v_accvgpr_mov_b32 a83, a243
v_accvgpr_mov_b32 a84, a244
v_accvgpr_mov_b32 a85, a245
v_accvgpr_mov_b32 a86, a246
v_accvgpr_mov_b32 a87, a247
v_accvgpr_mov_b32 a88, a248
v_accvgpr_mov_b32 a89, a249
v_accvgpr_mov_b32 a90, a250
v_accvgpr_mov_b32 a91, a251
v_accvgpr_mov_b32 a92, a252
v_accvgpr_mov_b32 a93, a253
v_accvgpr_mov_b32 a94, a254
v_accvgpr_mov_b32 a95, a255
v_accvgpr_mov_b32 a96, a192
v_accvgpr_mov_b32 a97, a193
v_accvgpr_mov_b32 a98, a194
v_accvgpr_mov_b32 a99, a195
v_accvgpr_mov_b32 a100, a196
v_accvgpr_mov_b32 a101, a197
v_accvgpr_mov_b32 a102, a198
v_accvgpr_mov_b32 a103, a199
v_accvgpr_mov_b32 a104, a200
v_accvgpr_mov_b32 a105, a201
v_accvgpr_mov_b32 a106, a202
v_accvgpr_mov_b32 a107, a203
v_accvgpr_mov_b32 a108, a204
v_accvgpr_mov_b32 a109, a205
v_accvgpr_mov_b32 a110, a206
v_accvgpr_mov_b32 a111, a207
s_mov_b32 s94, 2
s_nop 1
s_branch .LzA7R
.Lzsp7:
s_sub_u32 s53, s53, 1
s_cbranch_scc1 .Lzsq7
)" R"(
s_sleep 1
buffer_load_dword v4, v5, s[20:23], s50 offen offset:0 sc0 sc1
s_waitcnt vmcnt(0)
v_cmp_eq_u32_e32 vcc, s3, v4
s_andn2_b64 s[28:29], s[60:61], vcc
s_cbranch_scc1 .Lzsp7
s_branch .Lzok7
.Lzsq7:
s_mov_b32 s53, 0
s_branch .Lzok7
.Lzmk0:
s_call_b64 s[30:31], .LzMASK0
s_branch .Lzmr0
.Lzfx0:
s_mov_b32 s88, 3
s_call_b64 s[30:31], .LzFIX0
s_branch .Lzfr0
.Lzmk1:
s_call_b64 s[30:31], .LzMASK1
s_branch .Lzmr1
.Lzsp8:
s_sub_u32 s53, s53, 1
s_cbranch_scc1 .Lzsq8
s_sleep 1
buffer_load_dword v4, v5, s[20:23], s50 offen offset:0 sc0 sc1
s_waitcnt vmcnt(0)
v_cmp_eq_u32_e32 vcc, s3, v4
s_andn2_b64 s[28:29], s[60:61], vcc
s_cbranch_scc1 .Lzsp8
s_branch .Lzok8
.Lzsq8:
s_mov_b32 s53, 0
s_branch .Lzok8
.Lzfx1:
s_mov_b32 s88, 2
s_call_b64 s[30:31], .LzFIX1
s_branch .Lzfr1
.Lzmk2:
s_call_b64 s[30:31], .LzMASK0
s_branch .Lzmr2
.Lzfx2:
s_mov_b32 s88, 2
s_call_b64 s[30:31], .LzFIX0
s_branch .Lzfr2
.Lzmk3:
s_call_b64 s[30:31], .LzMASK1
s_branch .Lzmr3
.Lzsp9:
s_sub_u32 s53, s53, 1
s_cbranch_scc1 .Lzsq9
s_sleep 1
buffer_load_dword v4, v5, s[20:23], s50 offen offset:0 sc0 sc1
s_waitcnt vmcnt(0)
v_cmp_eq_u32_e32 vcc, s3, v4
s_andn2_b64 s[28:29], s[60:61], vcc
s_cbranch_scc1 .Lzsp9
s_branch .Lzok9
.Lzsq9:
s_mov_b32 s53, 0
s_branch .Lzok9
.Lzfx3:
s_mov_b32 s88, 0
s_call_b64 s[30:31], .LzFIX1
s_branch .Lzfr3
)" R"(
.Lzmk4:
s_call_b64 s[30:31], .LzMASK0
s_branch .Lzmr4
.Lzfx4:
s_mov_b32 s88, 3
s_call_b64 s[30:31], .LzFIX0
s_branch .Lzfr4
.Lzmk5:
s_call_b64 s[30:31], .LzMASK1
s_branch .Lzmr5
.Lzfx5:
s_mov_b32 s88, 0
s_call_b64 s[30:31], .LzFIX1
s_branch .Lzfr5
.Lzsp10:
s_sub_u32 s53, s53, 1
s_cbranch_scc1 .Lzsq10
s_sleep 1
buffer_load_dword v4, v2, s[20:23], s85 offen sc0 sc1
s_waitcnt vmcnt(0)
v_xor_b32 v4, s3, v4
v_cmp_gt_u32_e32 vcc, 2, v4
s_andn2_b32 s28, 0xff, vcc_lo
s_cbranch_scc1 .Lzsp10
s_branch .Lzok10
.Lzsq10:
s_mov_b32 s53, 0
s_branch .Lzok10
.Lzsp11:
s_sub_u32 s53, s53, 1
s_cbranch_scc1 .Lzsq11
s_sleep 1
buffer_load_dword v4, v5, s[20:23], s50 offen offset:0 sc0 sc1
s_waitcnt vmcnt(0)
v_cmp_eq_u32_e32 vcc, s3, v4
s_andn2_b64 s[28:29], s[60:61], vcc
s_cbranch_scc1 .Lzsp11
s_branch .Lzok11
.Lzsq11:
s_mov_b32 s53, 0
s_branch .Lzok11
.Lzsp12:
s_sub_u32 s53, s53, 1
s_cbranch_scc1 .Lzsq12
s_sleep 1
buffer_load_dword v4, v5, s[20:23], s50 offen offset:0 sc0 sc1
s_waitcnt vmcnt(0)
v_cmp_eq_u32_e32 vcc, s3, v4
s_andn2_b64 s[28:29], s[60:61], vcc
s_cbranch_scc1 .Lzsp12
s_branch .Lzok12
.Lzsq12:
s_mov_b32 s53, 0
s_branch .Lzok12
.Lzsp13:
s_sub_u32 s53, s53, 1
s_cbranch_scc1 .Lzsq13
s_sleep 1
buffer_load_dword v4, v5, s[20:23], s50 offen offset:0 sc0 sc1
s_waitcnt vmcnt(0)
v_cmp_eq_u32_e32 vcc, s3, v4
s_andn2_b64 s[28:29], s[60:61], vcc
s_cbranch_scc1 .Lzsp13
s_branch .Lzok13
)" R"(
.Lzsq13:
s_mov_b32 s53, 0
s_branch .Lzok13
.Lzsp14:
s_sub_u32 s53, s53, 1
s_cbranch_scc1 .Lzsq14
s_sleep 1
buffer_load_dword v4, v5, s[20:23], s50 offen offset:0 sc0 sc1
s_waitcnt vmcnt(0)
v_cmp_eq_u32_e32 vcc, s3, v4
s_andn2_b64 s[28:29], s[60:61], vcc
s_cbranch_scc1 .Lzsp14
s_branch .Lzok14
.Lzsq14:
s_mov_b32 s53, 0
s_branch .Lzok14
.Lzfx6:
s_mov_b32 s88, 3
s_call_b64 s[30:31], .LzFIX0
s_branch .Lzfr6
.Lzsp15:
s_sub_u32 s53, s53, 1
s_cbranch_scc1 .Lzsq15
s_sleep 1
buffer_load_dword v4, v5, s[20:23], s50 offen offset:0 sc0 sc1
s_waitcnt vmcnt(0)
v_cmp_eq_u32_e32 vcc, s3, v4
s_andn2_b64 s[28:29], s[60:61], vcc
s_cbranch_scc1 .Lzsp15
s_branch .Lzok15
.Lzsq15:
s_mov_b32 s53, 0
s_branch .Lzok15
.Lzfx7:
s_mov_b32 s88, 2
s_call_b64 s[30:31], .LzFIX1
s_branch .Lzfr7
.Lzmk6:
s_call_b64 s[30:31], .LzMASK0
s_branch .Lzmr6
.Lzfx8:
s_mov_b32 s88, 2
s_call_b64 s[30:31], .LzFIX0
s_branch .Lzfr8
.Lzmk7:
s_call_b64 s[30:31], .LzMASK1
s_branch .Lzmr7
.Lzsp16:
s_sub_u32 s53, s53, 1
s_cbranch_scc1 .Lzsq16
s_sleep 1
buffer_load_dword v4, v5, s[20:23], s50 offen offset:0 sc0 sc1
s_waitcnt vmcnt(0)
v_cmp_eq_u32_e32 vcc, s3, v4
s_andn2_b64 s[28:29], s[60:61], vcc
s_cbranch_scc1 .Lzsp16
s_branch .Lzok16
.Lzsq16:
s_mov_b32 s53, 0
s_branch .Lzok16
.Lzfx9:
s_mov_b32 s88, 2
s_call_b64 s[30:31], .LzFIX1
s_branch .Lzfr9
)" R"(
.Lzmk8:
s_call_b64 s[30:31], .LzMASK0
s_branch .Lzmr8
.Lzfx10:
s_mov_b32 s88, 2
s_call_b64 s[30:31], .LzFIX0
s_branch .Lzfr10
.Lzmk9:
s_call_b64 s[30:31], .LzMASK1
s_branch .Lzmr9
.Lzsp17:
s_sub_u32 s53, s53, 1
s_cbranch_scc1 .Lzsq17
s_sleep 1
buffer_load_dword v4, v7, s[20:23], s65 offen offset:0 sc0 sc1
s_waitcnt vmcnt(0)
v_cmp_eq_u32_e32 vcc, s3, v4
s_andn2_b64 s[28:29], s[60:61], vcc
s_cbranch_scc1 .Lzsp17
s_branch .Lzok17
.Lzsq17:
s_mov_b32 s53, 0
s_branch .Lzok17
.Lzfx11:
s_mov_b32 s88, 2
s_call_b64 s[30:31], .LzFIX1
s_branch .Lzfr11
.Lzmk10:
s_call_b64 s[30:31], .LzMASK0
s_branch .Lzmr10
.Lzfx12:
s_mov_b32 s88, 2
s_call_b64 s[30:31], .LzFIX0
s_branch .Lzfr12
.Lzmk11:
s_call_b64 s[30:31], .LzMASK1
s_branch .Lzmr11
.Lzfx13:
s_mov_b32 s88, 0
s_call_b64 s[30:31], .LzFIX1
s_branch .Lzfr13
.LzMRG:
s_waitcnt vmcnt(0)
s_nop 7
v_mov_b32 v25, v64
v_mov_b32 v24, v25
s_nop 1
v_permlane32_swap_b32_e32 v25, v24
v_add_f32_e32 v25, v25, v24
v_mov_b32 v24, v25
s_nop 1
v_permlane16_swap_b32_e32 v25, v24
v_add_f32_e32 v25, v25, v24
v_cmp_lt_f32_e32 vcc, 0, v25
s_nop 1
v_cndmask_b32_e32 v66, v16, v66, vcc
v_sub_f32_e32 v28, v16, v66
v_min_f32_e32 v26, 0, v28
v_exp_f32_e32 v26, v26
v_max_f32_e32 v28, 0, v28
v_sub_f32_e32 v24, 0, v28
v_exp_f32_e32 v24, v24
v_mul_f32_e32 v64, v26, v64
v_mul_f32_e32 v12, v24, v12
)" R"(
v_mul_f32_e32 v128, v24, v128
v_mul_f32_e32 v129, v24, v129
v_mul_f32_e32 v130, v24, v130
v_mul_f32_e32 v131, v24, v131
v_mul_f32_e32 v132, v24, v132
v_mul_f32_e32 v133, v24, v133
v_mul_f32_e32 v134, v24, v134
v_mul_f32_e32 v135, v24, v135
v_mul_f32_e32 v144, v24, v144
v_mul_f32_e32 v145, v24, v145
v_mul_f32_e32 v146, v24, v146
v_mul_f32_e32 v147, v24, v147
v_mul_f32_e32 v148, v24, v148
v_mul_f32_e32 v149, v24, v149
v_mul_f32_e32 v150, v24, v150
v_mul_f32_e32 v151, v24, v151
v_mul_f32_e32 v160, v24, v160
v_mul_f32_e32 v161, v24, v161
v_mul_f32_e32 v162, v24, v162
v_mul_f32_e32 v163, v24, v163
v_mul_f32_e32 v164, v24, v164
v_mul_f32_e32 v165, v24, v165
v_mul_f32_e32 v166, v24, v166
v_mul_f32_e32 v167, v24, v167
v_mul_f32_e32 v176, v24, v176
v_mul_f32_e32 v177, v24, v177
v_mul_f32_e32 v178, v24, v178
v_mul_f32_e32 v179, v24, v179
v_mul_f32_e32 v180, v24, v180
v_mul_f32_e32 v181, v24, v181
v_mul_f32_e32 v182, v24, v182
v_mul_f32_e32 v183, v24, v183
v_mul_f32_e32 v192, v24, v192
v_mul_f32_e32 v193, v24, v193
v_mul_f32_e32 v194, v24, v194
v_mul_f32_e32 v195, v24, v195
v_mul_f32_e32 v196, v24, v196
v_mul_f32_e32 v197, v24, v197
v_mul_f32_e32 v198, v24, v198
v_mul_f32_e32 v199, v24, v199
v_mul_f32_e32 v208, v24, v208
v_mul_f32_e32 v209, v24, v209
v_mul_f32_e32 v210, v24, v210
v_mul_f32_e32 v211, v24, v211
v_mul_f32_e32 v212, v24, v212
v_mul_f32_e32 v213, v24, v213
v_mul_f32_e32 v214, v24, v214
v_mul_f32_e32 v215, v24, v215
v_mul_f32_e32 v224, v24, v224
v_mul_f32_e32 v225, v24, v225
v_mul_f32_e32 v226, v24, v226
v_mul_f32_e32 v227, v24, v227
v_mul_f32_e32 v228, v24, v228
v_mul_f32_e32 v229, v24, v229
v_mul_f32_e32 v230, v24, v230
v_mul_f32_e32 v231, v24, v231
v_mul_f32_e32 v240, v24, v240
v_mul_f32_e32 v241, v24, v241
v_mul_f32_e32 v242, v24, v242
v_mul_f32_e32 v243, v24, v243
v_mul_f32_e32 v244, v24, v244
v_mul_f32_e32 v245, v24, v245
v_mul_f32_e32 v246, v24, v246
v_mul_f32_e32 v247, v24, v247
)" R"(
v_sub_f32_e32 v16, v16, v28
v_sub_f32_e32 v17, v17, v28
v_sub_f32_e32 v18, v18, v28
v_sub_f32_e32 v19, v19, v28
v_sub_f32_e32 v96, v96, v28
v_sub_f32_e32 v97, v97, v28
v_sub_f32_e32 v98, v98, v28
v_sub_f32_e32 v99, v99, v28
v_sub_f32_e32 v104, v104, v28
v_sub_f32_e32 v105, v105, v28
v_sub_f32_e32 v106, v106, v28
v_sub_f32_e32 v107, v107, v28
v_sub_f32_e32 v112, v112, v28
v_sub_f32_e32 v113, v113, v28
v_sub_f32_e32 v114, v114, v28
v_sub_f32_e32 v115, v115, v28
v_sub_f32_e32 v120, v120, v28
v_sub_f32_e32 v121, v121, v28
v_sub_f32_e32 v122, v122, v28
v_sub_f32_e32 v123, v123, v28
v_accvgpr_read_b32 v25, a128
v_lshlrev_b32 v24, 16, v25
v_and_b32 v25, 0xffff0000, v25
v_mul_f32_e32 v24, v26, v24
v_mul_f32_e32 v25, v26, v25
Cv 24,24,25
v_accvgpr_write_b32 a128, v24
v_accvgpr_read_b32 v25, a129
v_lshlrev_b32 v24, 16, v25
v_and_b32 v25, 0xffff0000, v25
v_mul_f32_e32 v24, v26, v24
v_mul_f32_e32 v25, v26, v25
Cv 24,24,25
v_accvgpr_write_b32 a129, v24
v_accvgpr_read_b32 v25, a130
v_lshlrev_b32 v24, 16, v25
v_and_b32 v25, 0xffff0000, v25
v_mul_f32_e32 v24, v26, v24
v_mul_f32_e32 v25, v26, v25
Cv 24,24,25
v_accvgpr_write_b32 a130, v24
v_accvgpr_read_b32 v25, a131
v_lshlrev_b32 v24, 16, v25
v_and_b32 v25, 0xffff0000, v25
v_mul_f32_e32 v24, v26, v24
v_mul_f32_e32 v25, v26, v25
Cv 24,24,25
v_accvgpr_write_b32 a131, v24
v_accvgpr_read_b32 v25, a136
v_lshlrev_b32 v24, 16, v25
v_and_b32 v25, 0xffff0000, v25
v_mul_f32_e32 v24, v26, v24
v_mul_f32_e32 v25, v26, v25
Cv 24,24,25
v_accvgpr_write_b32 a136, v24
v_accvgpr_read_b32 v25, a137
v_lshlrev_b32 v24, 16, v25
v_and_b32 v25, 0xffff0000, v25
v_mul_f32_e32 v24, v26, v24
v_mul_f32_e32 v25, v26, v25
Cv 24,24,25
v_accvgpr_write_b32 a137, v24
v_accvgpr_read_b32 v25, a138
v_lshlrev_b32 v24, 16, v25
)" R"(
v_and_b32 v25, 0xffff0000, v25
v_mul_f32_e32 v24, v26, v24
v_mul_f32_e32 v25, v26, v25
Cv 24,24,25
v_accvgpr_write_b32 a138, v24
v_accvgpr_read_b32 v25, a139
v_lshlrev_b32 v24, 16, v25
v_and_b32 v25, 0xffff0000, v25
v_mul_f32_e32 v24, v26, v24
v_mul_f32_e32 v25, v26, v25
Cv 24,24,25
v_accvgpr_write_b32 a139, v24
v_accvgpr_read_b32 v25, a144
v_lshlrev_b32 v24, 16, v25
v_and_b32 v25, 0xffff0000, v25
v_mul_f32_e32 v24, v26, v24
v_mul_f32_e32 v25, v26, v25
Cv 24,24,25
v_accvgpr_write_b32 a144, v24
v_accvgpr_read_b32 v25, a145
v_lshlrev_b32 v24, 16, v25
v_and_b32 v25, 0xffff0000, v25
v_mul_f32_e32 v24, v26, v24
v_mul_f32_e32 v25, v26, v25
Cv 24,24,25
v_accvgpr_write_b32 a145, v24
v_accvgpr_read_b32 v25, a146
v_lshlrev_b32 v24, 16, v25
v_and_b32 v25, 0xffff0000, v25
v_mul_f32_e32 v24, v26, v24
v_mul_f32_e32 v25, v26, v25
Cv 24,24,25
v_accvgpr_write_b32 a146, v24
v_accvgpr_read_b32 v25, a147
v_lshlrev_b32 v24, 16, v25
v_and_b32 v25, 0xffff0000, v25
v_mul_f32_e32 v24, v26, v24
v_mul_f32_e32 v25, v26, v25
Cv 24,24,25
v_accvgpr_write_b32 a147, v24
v_accvgpr_read_b32 v25, a152
v_lshlrev_b32 v24, 16, v25
v_and_b32 v25, 0xffff0000, v25
v_mul_f32_e32 v24, v26, v24
v_mul_f32_e32 v25, v26, v25
Cv 24,24,25
v_accvgpr_write_b32 a152, v24
v_accvgpr_read_b32 v25, a153
v_lshlrev_b32 v24, 16, v25
v_and_b32 v25, 0xffff0000, v25
v_mul_f32_e32 v24, v26, v24
v_mul_f32_e32 v25, v26, v25
Cv 24,24,25
v_accvgpr_write_b32 a153, v24
v_accvgpr_read_b32 v25, a154
v_lshlrev_b32 v24, 16, v25
v_and_b32 v25, 0xffff0000, v25
v_mul_f32_e32 v24, v26, v24
v_mul_f32_e32 v25, v26, v25
Cv 24,24,25
v_accvgpr_write_b32 a154, v24
v_accvgpr_read_b32 v25, a155
v_lshlrev_b32 v24, 16, v25
v_and_b32 v25, 0xffff0000, v25
)" R"(
v_mul_f32_e32 v24, v26, v24
v_mul_f32_e32 v25, v26, v25
Cv 24,24,25
v_accvgpr_write_b32 a155, v24
v_accvgpr_read_b32 v25, a160
v_lshlrev_b32 v24, 16, v25
v_and_b32 v25, 0xffff0000, v25
v_mul_f32_e32 v24, v26, v24
v_mul_f32_e32 v25, v26, v25
Cv 24,24,25
v_accvgpr_write_b32 a160, v24
v_accvgpr_read_b32 v25, a161
v_lshlrev_b32 v24, 16, v25
v_and_b32 v25, 0xffff0000, v25
v_mul_f32_e32 v24, v26, v24
v_mul_f32_e32 v25, v26, v25
Cv 24,24,25
v_accvgpr_write_b32 a161, v24
v_accvgpr_read_b32 v25, a162
v_lshlrev_b32 v24, 16, v25
v_and_b32 v25, 0xffff0000, v25
v_mul_f32_e32 v24, v26, v24
v_mul_f32_e32 v25, v26, v25
Cv 24,24,25
v_accvgpr_write_b32 a162, v24
v_accvgpr_read_b32 v25, a163
v_lshlrev_b32 v24, 16, v25
v_and_b32 v25, 0xffff0000, v25
v_mul_f32_e32 v24, v26, v24
v_mul_f32_e32 v25, v26, v25
Cv 24,24,25
v_accvgpr_write_b32 a163, v24
v_accvgpr_read_b32 v25, a168
v_lshlrev_b32 v24, 16, v25
v_and_b32 v25, 0xffff0000, v25
v_mul_f32_e32 v24, v26, v24
v_mul_f32_e32 v25, v26, v25
Cv 24,24,25
v_accvgpr_write_b32 a168, v24
v_accvgpr_read_b32 v25, a169
v_lshlrev_b32 v24, 16, v25
v_and_b32 v25, 0xffff0000, v25
v_mul_f32_e32 v24, v26, v24
v_mul_f32_e32 v25, v26, v25
Cv 24,24,25
v_accvgpr_write_b32 a169, v24
v_accvgpr_read_b32 v25, a170
v_lshlrev_b32 v24, 16, v25
v_and_b32 v25, 0xffff0000, v25
v_mul_f32_e32 v24, v26, v24
v_mul_f32_e32 v25, v26, v25
Cv 24,24,25
v_accvgpr_write_b32 a170, v24
v_accvgpr_read_b32 v25, a171
v_lshlrev_b32 v24, 16, v25
v_and_b32 v25, 0xffff0000, v25
v_mul_f32_e32 v24, v26, v24
v_mul_f32_e32 v25, v26, v25
Cv 24,24,25
v_accvgpr_write_b32 a171, v24
v_accvgpr_read_b32 v25, a176
v_lshlrev_b32 v24, 16, v25
v_and_b32 v25, 0xffff0000, v25
v_mul_f32_e32 v24, v26, v24
)" R"(
v_mul_f32_e32 v25, v26, v25
Cv 24,24,25
v_accvgpr_write_b32 a176, v24
v_accvgpr_read_b32 v25, a177
v_lshlrev_b32 v24, 16, v25
v_and_b32 v25, 0xffff0000, v25
v_mul_f32_e32 v24, v26, v24
v_mul_f32_e32 v25, v26, v25
Cv 24,24,25
v_accvgpr_write_b32 a177, v24
v_accvgpr_read_b32 v25, a178
v_lshlrev_b32 v24, 16, v25
v_and_b32 v25, 0xffff0000, v25
v_mul_f32_e32 v24, v26, v24
v_mul_f32_e32 v25, v26, v25
Cv 24,24,25
v_accvgpr_write_b32 a178, v24
v_accvgpr_read_b32 v25, a179
v_lshlrev_b32 v24, 16, v25
v_and_b32 v25, 0xffff0000, v25
v_mul_f32_e32 v24, v26, v24
v_mul_f32_e32 v25, v26, v25
Cv 24,24,25
v_accvgpr_write_b32 a179, v24
v_accvgpr_read_b32 v25, a184
v_lshlrev_b32 v24, 16, v25
v_and_b32 v25, 0xffff0000, v25
v_mul_f32_e32 v24, v26, v24
v_mul_f32_e32 v25, v26, v25
Cv 24,24,25
v_accvgpr_write_b32 a184, v24
v_accvgpr_read_b32 v25, a185
v_lshlrev_b32 v24, 16, v25
v_and_b32 v25, 0xffff0000, v25
v_mul_f32_e32 v24, v26, v24
v_mul_f32_e32 v25, v26, v25
Cv 24,24,25
v_accvgpr_write_b32 a185, v24
v_accvgpr_read_b32 v25, a186
v_lshlrev_b32 v24, 16, v25
v_and_b32 v25, 0xffff0000, v25
v_mul_f32_e32 v24, v26, v24
v_mul_f32_e32 v25, v26, v25
Cv 24,24,25
v_accvgpr_write_b32 a186, v24
v_accvgpr_read_b32 v25, a187
v_lshlrev_b32 v24, 16, v25
v_and_b32 v25, 0xffff0000, v25
v_mul_f32_e32 v24, v26, v24
v_mul_f32_e32 v25, v26, v25
Cv 24,24,25
v_accvgpr_write_b32 a187, v24
v_mov_b32 v25, v65
v_mov_b32 v24, v25
s_nop 1
v_permlane32_swap_b32_e32 v25, v24
v_add_f32_e32 v25, v25, v24
v_mov_b32 v24, v25
s_nop 1
v_permlane16_swap_b32_e32 v25, v24
v_add_f32_e32 v25, v25, v24
v_cmp_lt_f32_e32 vcc, 0, v25
s_nop 1
v_cndmask_b32_e32 v67, v20, v67, vcc
)" R"(
v_sub_f32_e32 v29, v20, v67
v_min_f32_e32 v27, 0, v29
v_exp_f32_e32 v27, v27
v_max_f32_e32 v29, 0, v29
v_sub_f32_e32 v24, 0, v29
v_exp_f32_e32 v24, v24
v_mul_f32_e32 v65, v27, v65
v_mul_f32_e32 v13, v24, v13
v_mul_f32_e32 v136, v24, v136
v_mul_f32_e32 v137, v24, v137
v_mul_f32_e32 v138, v24, v138
v_mul_f32_e32 v139, v24, v139
v_mul_f32_e32 v140, v24, v140
v_mul_f32_e32 v141, v24, v141
v_mul_f32_e32 v142, v24, v142
v_mul_f32_e32 v143, v24, v143
v_mul_f32_e32 v152, v24, v152
v_mul_f32_e32 v153, v24, v153
v_mul_f32_e32 v154, v24, v154
v_mul_f32_e32 v155, v24, v155
v_mul_f32_e32 v156, v24, v156
v_mul_f32_e32 v157, v24, v157
v_mul_f32_e32 v158, v24, v158
v_mul_f32_e32 v159, v24, v159
v_mul_f32_e32 v168, v24, v168
v_mul_f32_e32 v169, v24, v169
v_mul_f32_e32 v170, v24, v170
v_mul_f32_e32 v171, v24, v171
v_mul_f32_e32 v172, v24, v172
v_mul_f32_e32 v173, v24, v173
v_mul_f32_e32 v174, v24, v174
v_mul_f32_e32 v175, v24, v175
v_mul_f32_e32 v184, v24, v184
v_mul_f32_e32 v185, v24, v185
v_mul_f32_e32 v186, v24, v186
v_mul_f32_e32 v187, v24, v187
v_mul_f32_e32 v188, v24, v188
v_mul_f32_e32 v189, v24, v189
v_mul_f32_e32 v190, v24, v190
v_mul_f32_e32 v191, v24, v191
v_mul_f32_e32 v200, v24, v200
v_mul_f32_e32 v201, v24, v201
v_mul_f32_e32 v202, v24, v202
v_mul_f32_e32 v203, v24, v203
v_mul_f32_e32 v204, v24, v204
v_mul_f32_e32 v205, v24, v205
v_mul_f32_e32 v206, v24, v206
v_mul_f32_e32 v207, v24, v207
v_mul_f32_e32 v216, v24, v216
v_mul_f32_e32 v217, v24, v217
v_mul_f32_e32 v218, v24, v218
v_mul_f32_e32 v219, v24, v219
v_mul_f32_e32 v220, v24, v220
v_mul_f32_e32 v221, v24, v221
v_mul_f32_e32 v222, v24, v222
v_mul_f32_e32 v223, v24, v223
v_mul_f32_e32 v232, v24, v232
v_mul_f32_e32 v233, v24, v233
v_mul_f32_e32 v234, v24, v234
v_mul_f32_e32 v235, v24, v235
v_mul_f32_e32 v236, v24, v236
v_mul_f32_e32 v237, v24, v237
v_mul_f32_e32 v238, v24, v238
v_mul_f32_e32 v239, v24, v239
)" R"(
v_mul_f32_e32 v248, v24, v248
v_mul_f32_e32 v249, v24, v249
v_mul_f32_e32 v250, v24, v250
v_mul_f32_e32 v251, v24, v251
v_mul_f32_e32 v252, v24, v252
v_mul_f32_e32 v253, v24, v253
v_mul_f32_e32 v254, v24, v254
v_mul_f32_e32 v255, v24, v255
v_sub_f32_e32 v20, v20, v29
v_sub_f32_e32 v21, v21, v29
v_sub_f32_e32 v22, v22, v29
v_sub_f32_e32 v23, v23, v29
v_sub_f32_e32 v100, v100, v29
v_sub_f32_e32 v101, v101, v29
v_sub_f32_e32 v102, v102, v29
v_sub_f32_e32 v103, v103, v29
v_sub_f32_e32 v108, v108, v29
v_sub_f32_e32 v109, v109, v29
v_sub_f32_e32 v110, v110, v29
v_sub_f32_e32 v111, v111, v29
v_sub_f32_e32 v116, v116, v29
v_sub_f32_e32 v117, v117, v29
v_sub_f32_e32 v118, v118, v29
v_sub_f32_e32 v119, v119, v29
v_sub_f32_e32 v124, v124, v29
v_sub_f32_e32 v125, v125, v29
v_sub_f32_e32 v126, v126, v29
v_sub_f32_e32 v127, v127, v29
v_accvgpr_read_b32 v25, a132
v_lshlrev_b32 v24, 16, v25
v_and_b32 v25, 0xffff0000, v25
v_mul_f32_e32 v24, v27, v24
v_mul_f32_e32 v25, v27, v25
Cv 24,24,25
v_accvgpr_write_b32 a132, v24
v_accvgpr_read_b32 v25, a133
v_lshlrev_b32 v24, 16, v25
v_and_b32 v25, 0xffff0000, v25
v_mul_f32_e32 v24, v27, v24
v_mul_f32_e32 v25, v27, v25
Cv 24,24,25
v_accvgpr_write_b32 a133, v24
v_accvgpr_read_b32 v25, a134
v_lshlrev_b32 v24, 16, v25
v_and_b32 v25, 0xffff0000, v25
v_mul_f32_e32 v24, v27, v24
v_mul_f32_e32 v25, v27, v25
Cv 24,24,25
v_accvgpr_write_b32 a134, v24
v_accvgpr_read_b32 v25, a135
v_lshlrev_b32 v24, 16, v25
v_and_b32 v25, 0xffff0000, v25
v_mul_f32_e32 v24, v27, v24
v_mul_f32_e32 v25, v27, v25
Cv 24,24,25
v_accvgpr_write_b32 a135, v24
v_accvgpr_read_b32 v25, a140
v_lshlrev_b32 v24, 16, v25
v_and_b32 v25, 0xffff0000, v25
v_mul_f32_e32 v24, v27, v24
v_mul_f32_e32 v25, v27, v25
Cv 24,24,25
v_accvgpr_write_b32 a140, v24
v_accvgpr_read_b32 v25, a141
)" R"(
v_lshlrev_b32 v24, 16, v25
v_and_b32 v25, 0xffff0000, v25
v_mul_f32_e32 v24, v27, v24
v_mul_f32_e32 v25, v27, v25
Cv 24,24,25
v_accvgpr_write_b32 a141, v24
v_accvgpr_read_b32 v25, a142
v_lshlrev_b32 v24, 16, v25
v_and_b32 v25, 0xffff0000, v25
v_mul_f32_e32 v24, v27, v24
v_mul_f32_e32 v25, v27, v25
Cv 24,24,25
v_accvgpr_write_b32 a142, v24
v_accvgpr_read_b32 v25, a143
v_lshlrev_b32 v24, 16, v25
v_and_b32 v25, 0xffff0000, v25
v_mul_f32_e32 v24, v27, v24
v_mul_f32_e32 v25, v27, v25
Cv 24,24,25
v_accvgpr_write_b32 a143, v24
v_accvgpr_read_b32 v25, a148
v_lshlrev_b32 v24, 16, v25
v_and_b32 v25, 0xffff0000, v25
v_mul_f32_e32 v24, v27, v24
v_mul_f32_e32 v25, v27, v25
Cv 24,24,25
v_accvgpr_write_b32 a148, v24
v_accvgpr_read_b32 v25, a149
v_lshlrev_b32 v24, 16, v25
v_and_b32 v25, 0xffff0000, v25
v_mul_f32_e32 v24, v27, v24
v_mul_f32_e32 v25, v27, v25
Cv 24,24,25
v_accvgpr_write_b32 a149, v24
v_accvgpr_read_b32 v25, a150
v_lshlrev_b32 v24, 16, v25
v_and_b32 v25, 0xffff0000, v25
v_mul_f32_e32 v24, v27, v24
v_mul_f32_e32 v25, v27, v25
Cv 24,24,25
v_accvgpr_write_b32 a150, v24
v_accvgpr_read_b32 v25, a151
v_lshlrev_b32 v24, 16, v25
v_and_b32 v25, 0xffff0000, v25
v_mul_f32_e32 v24, v27, v24
v_mul_f32_e32 v25, v27, v25
Cv 24,24,25
v_accvgpr_write_b32 a151, v24
v_accvgpr_read_b32 v25, a156
v_lshlrev_b32 v24, 16, v25
v_and_b32 v25, 0xffff0000, v25
v_mul_f32_e32 v24, v27, v24
v_mul_f32_e32 v25, v27, v25
Cv 24,24,25
v_accvgpr_write_b32 a156, v24
v_accvgpr_read_b32 v25, a157
v_lshlrev_b32 v24, 16, v25
v_and_b32 v25, 0xffff0000, v25
v_mul_f32_e32 v24, v27, v24
v_mul_f32_e32 v25, v27, v25
Cv 24,24,25
v_accvgpr_write_b32 a157, v24
v_accvgpr_read_b32 v25, a158
v_lshlrev_b32 v24, 16, v25
)" R"(
v_and_b32 v25, 0xffff0000, v25
v_mul_f32_e32 v24, v27, v24
v_mul_f32_e32 v25, v27, v25
Cv 24,24,25
v_accvgpr_write_b32 a158, v24
v_accvgpr_read_b32 v25, a159
v_lshlrev_b32 v24, 16, v25
v_and_b32 v25, 0xffff0000, v25
v_mul_f32_e32 v24, v27, v24
v_mul_f32_e32 v25, v27, v25
Cv 24,24,25
v_accvgpr_write_b32 a159, v24
v_accvgpr_read_b32 v25, a164
v_lshlrev_b32 v24, 16, v25
v_and_b32 v25, 0xffff0000, v25
v_mul_f32_e32 v24, v27, v24
v_mul_f32_e32 v25, v27, v25
Cv 24,24,25
v_accvgpr_write_b32 a164, v24
v_accvgpr_read_b32 v25, a165
v_lshlrev_b32 v24, 16, v25
v_and_b32 v25, 0xffff0000, v25
v_mul_f32_e32 v24, v27, v24
v_mul_f32_e32 v25, v27, v25
Cv 24,24,25
v_accvgpr_write_b32 a165, v24
v_accvgpr_read_b32 v25, a166
v_lshlrev_b32 v24, 16, v25
v_and_b32 v25, 0xffff0000, v25
v_mul_f32_e32 v24, v27, v24
v_mul_f32_e32 v25, v27, v25
Cv 24,24,25
v_accvgpr_write_b32 a166, v24
v_accvgpr_read_b32 v25, a167
v_lshlrev_b32 v24, 16, v25
v_and_b32 v25, 0xffff0000, v25
v_mul_f32_e32 v24, v27, v24
v_mul_f32_e32 v25, v27, v25
Cv 24,24,25
v_accvgpr_write_b32 a167, v24
v_accvgpr_read_b32 v25, a172
v_lshlrev_b32 v24, 16, v25
v_and_b32 v25, 0xffff0000, v25
v_mul_f32_e32 v24, v27, v24
v_mul_f32_e32 v25, v27, v25
Cv 24,24,25
v_accvgpr_write_b32 a172, v24
v_accvgpr_read_b32 v25, a173
v_lshlrev_b32 v24, 16, v25
v_and_b32 v25, 0xffff0000, v25
v_mul_f32_e32 v24, v27, v24
v_mul_f32_e32 v25, v27, v25
Cv 24,24,25
v_accvgpr_write_b32 a173, v24
v_accvgpr_read_b32 v25, a174
v_lshlrev_b32 v24, 16, v25
v_and_b32 v25, 0xffff0000, v25
v_mul_f32_e32 v24, v27, v24
v_mul_f32_e32 v25, v27, v25
Cv 24,24,25
v_accvgpr_write_b32 a174, v24
v_accvgpr_read_b32 v25, a175
v_lshlrev_b32 v24, 16, v25
v_and_b32 v25, 0xffff0000, v25
)" R"(
v_mul_f32_e32 v24, v27, v24
v_mul_f32_e32 v25, v27, v25
Cv 24,24,25
v_accvgpr_write_b32 a175, v24
v_accvgpr_read_b32 v25, a180
v_lshlrev_b32 v24, 16, v25
v_and_b32 v25, 0xffff0000, v25
v_mul_f32_e32 v24, v27, v24
v_mul_f32_e32 v25, v27, v25
Cv 24,24,25
v_accvgpr_write_b32 a180, v24
v_accvgpr_read_b32 v25, a181
v_lshlrev_b32 v24, 16, v25
v_and_b32 v25, 0xffff0000, v25
v_mul_f32_e32 v24, v27, v24
v_mul_f32_e32 v25, v27, v25
Cv 24,24,25
v_accvgpr_write_b32 a181, v24
v_accvgpr_read_b32 v25, a182
v_lshlrev_b32 v24, 16, v25
v_and_b32 v25, 0xffff0000, v25
v_mul_f32_e32 v24, v27, v24
v_mul_f32_e32 v25, v27, v25
Cv 24,24,25
v_accvgpr_write_b32 a182, v24
v_accvgpr_read_b32 v25, a183
v_lshlrev_b32 v24, 16, v25
v_and_b32 v25, 0xffff0000, v25
v_mul_f32_e32 v24, v27, v24
v_mul_f32_e32 v25, v27, v25
Cv 24,24,25
v_accvgpr_write_b32 a183, v24
v_accvgpr_read_b32 v25, a188
v_lshlrev_b32 v24, 16, v25
v_and_b32 v25, 0xffff0000, v25
v_mul_f32_e32 v24, v27, v24
v_mul_f32_e32 v25, v27, v25
Cv 24,24,25
v_accvgpr_write_b32 a188, v24
v_accvgpr_read_b32 v25, a189
v_lshlrev_b32 v24, 16, v25
v_and_b32 v25, 0xffff0000, v25
v_mul_f32_e32 v24, v27, v24
v_mul_f32_e32 v25, v27, v25
Cv 24,24,25
v_accvgpr_write_b32 a189, v24
v_accvgpr_read_b32 v25, a190
v_lshlrev_b32 v24, 16, v25
v_and_b32 v25, 0xffff0000, v25
v_mul_f32_e32 v24, v27, v24
v_mul_f32_e32 v25, v27, v25
Cv 24,24,25
v_accvgpr_write_b32 a190, v24
v_accvgpr_read_b32 v25, a191
v_lshlrev_b32 v24, 16, v25
v_and_b32 v25, 0xffff0000, v25
v_mul_f32_e32 v24, v27, v24
v_mul_f32_e32 v25, v27, v25
Cv 24,24,25
v_accvgpr_write_b32 a191, v24
v_exp_f32_e32 v48, v96
v_exp_f32_e32 v24, v97
s_nop 0
v_add_f32_e32 v14, v48, v24
)" R"(
Cv 48,48,24
v_exp_f32_e32 v49, v98
v_exp_f32_e32 v25, v99
v_add_f32_e32 v14, v14, v49
v_add_f32_e32 v14, v14, v25
Cv 49,49,25
v_exp_f32_e32 v50, v104
v_exp_f32_e32 v26, v105
v_add_f32_e32 v14, v14, v50
v_add_f32_e32 v14, v14, v26
Cv 50,50,26
v_exp_f32_e32 v51, v106
v_exp_f32_e32 v27, v107
v_add_f32_e32 v14, v14, v51
v_add_f32_e32 v14, v14, v27
Cv 51,51,27
v_exp_f32_e32 v52, v100
v_exp_f32_e32 v28, v101
s_nop 0
v_add_f32_e32 v15, v52, v28
Cv 52,52,28
v_exp_f32_e32 v53, v102
v_exp_f32_e32 v29, v103
v_add_f32_e32 v15, v15, v53
v_add_f32_e32 v15, v15, v29
Cv 53,53,29
v_exp_f32_e32 v54, v108
v_exp_f32_e32 v24, v109
v_add_f32_e32 v15, v15, v54
v_add_f32_e32 v15, v15, v24
Cv 54,54,24
v_exp_f32_e32 v55, v110
v_exp_f32_e32 v25, v111
v_add_f32_e32 v15, v15, v55
v_add_f32_e32 v15, v15, v25
Cv 55,55,25
s_nop 1
s_branch .LzMRR
.Lzsp18:
s_sub_u32 s53, s53, 1
s_cbranch_scc1 .Lzsq18
s_sleep 1
buffer_load_dword v4, v7, s[20:23], s56 offen offset:4 sc0 sc1
s_waitcnt vmcnt(0)
v_cmp_eq_u32_e32 vcc, s3, v4
s_andn2_b64 s[28:29], 1, vcc
s_cbranch_scc1 .Lzsp18
s_branch .Lzok18
.Lzsq18:
s_mov_b32 s53, 0
s_branch .Lzok18
.LzPBKX:
Mv 32,0,32
Rt 128,18,16384
Mv 64,0,36
Mv 96,0,40
Rt 130,18,18432
Mv 128,0,44
s_add_u32 s39, s39, 0x40000
Mv 160,0,48
Rt 132,19,16384
s_add_u32 m0, s37, 49152
Mv 192,0,52
buffer_load_dwordx4 v12, s[8:11], s39 offen offset:0 lds
)" R"(
Mv 224,0,56
Rt 134,19,18432
Ma 192,0,60
Ma 196,4,60
Rt 136,20,16384
Mv 228,4,56
Mv 196,4,52
Rt 138,20,18432
Mv 164,4,48
Mv 132,4,44
Rt 140,21,16384
Mv 100,4,40
buffer_load_dwordx4 v13, s[8:11], s39 offen offset:1024 lds
Mv 68,4,36
Rt 142,21,18432
Mv 36,4,32
Mv 40,8,32
Rt 144,22,16384
Mv 72,8,36
Mv 104,8,40
Rt 146,22,18432
Mv 136,8,44
Mv 168,8,48
Rt 148,23,16384
Mv 200,8,52
buffer_load_dwordx4 v14, s[8:11], s39 offen offset:2048 lds
Mv 232,8,56
Rt 150,23,18432
Ma 200,8,60
Ma 204,12,60
Rt 152,24,16384
Mv 236,12,56
Mv 204,12,52
Rt 154,24,18432
Mv 172,12,48
Mv 140,12,44
Rt 156,25,16384
Mv 108,12,40
buffer_load_dwordx4 v15, s[8:11], s39 offen offset:3072 lds
Mv 76,12,36
Rt 158,25,18432
Mv 44,12,32
Mv 48,16,32
Rb 160,17,0
Mv 80,16,36
Mv 112,16,40
Rb 164,17,2048
Mv 144,16,44
s_add_u32 s39, s39, 0x40000
Mv 176,16,48
Rb 168,17,8192
s_add_u32 m0, s37, 0
Mv 208,16,52
buffer_load_dwordx4 v12, s[8:11], s39 offen offset:0 lds
Mv 240,16,56
Rb 172,17,10240
Ma 208,16,60
Ma 212,20,60
Rb 176,17,16384
Mv 244,20,56
Mv 212,20,52
Rb 180,17,18432
Mv 180,20,48
Mv 148,20,44
)" R"(
Rb 184,17,24576
Mv 116,20,40
buffer_load_dwordx4 v13, s[8:11], s39 offen offset:1024 lds
Mv 84,20,36
Rb 188,17,26624
Mv 52,20,32
Mv 56,24,32
Mv 88,24,36
Mv 120,24,40
Mv 152,24,44
Mv 184,24,48
Mv 216,24,52
buffer_load_dwordx4 v14, s[8:11], s39 offen offset:2048 lds
Mv 248,24,56
Ma 216,24,60
Ma 220,28,60
Mv 252,28,56
Mv 220,28,52
Mv 188,28,48
Mv 156,28,44
Mv 124,28,40
buffer_load_dwordx4 v15, s[8:11], s39 offen offset:3072 lds
Mv 92,28,36
Mv 60,28,32
s_waitcnt vmcnt(8) lgkmcnt(0)
s_barrier
Mv 32,128,160
Rt 0,18,32768
Mv 64,128,164
s_add_u32 s38, s38, 0xffff0080
Mv 96,128,168
Rt 2,18,34816
s_add_u32 m0, s36, 0
Mv 128,128,172
buffer_load_dwordx4 v8, s[4:7], s38 offen offset:0 lds
Mv 160,128,176
Rt 4,19,32768
Mv 192,128,180
Mv 224,128,184
Rt 6,19,34816
Ma 192,128,188
buffer_load_dwordx4 v9, s[4:7], s38 offen offset:1024 lds
Ma 196,132,188
Rt 8,20,32768
Mv 228,132,184
Mv 196,132,180
Rt 10,20,34816
Mv 164,132,176
buffer_load_dwordx4 v10, s[4:7], s38 offen offset:2048 lds
Mv 132,132,172
Rt 12,21,32768
Mv 100,132,168
Mv 68,132,164
Rt 14,21,34816
Mv 36,132,160
buffer_load_dwordx4 v11, s[4:7], s38 offen offset:3072 lds
Mv 40,136,160
Rt 16,22,32768
Mv 72,136,164
s_add_u32 s38, s38, 0x10000
Mv 104,136,168
Rt 18,22,34816
s_add_u32 m0, s36, 4096
Mv 136,136,172
buffer_load_dwordx4 v8, s[4:7], s38 offen offset:0 lds
)" R"(
Mv 168,136,176
Rt 20,23,32768
Mv 200,136,180
Mv 232,136,184
Rt 22,23,34816
Ma 200,136,188
buffer_load_dwordx4 v9, s[4:7], s38 offen offset:1024 lds
Ma 204,140,188
Rt 24,24,32768
Mv 236,140,184
Mv 204,140,180
Rt 26,24,34816
Mv 172,140,176
buffer_load_dwordx4 v10, s[4:7], s38 offen offset:2048 lds
Mv 140,140,172
Rt 28,25,32768
Mv 108,140,168
Mv 76,140,164
Rt 30,25,34816
Mv 44,140,160
buffer_load_dwordx4 v11, s[4:7], s38 offen offset:3072 lds
Mv 48,144,160
Rb 32,16,32768
Mv 80,144,164
Mv 112,144,168
Rb 36,16,34816
Mv 144,144,172
Mv 176,144,176
Rb 40,16,40960
Mv 208,144,180
Mv 240,144,184
Rb 44,16,43008
Ma 208,144,188
Ma 212,148,188
Rb 48,16,49152
Mv 244,148,184
Mv 212,148,180
Rb 52,16,51200
Mv 180,148,176
Mv 148,148,172
Rb 56,16,57344
Mv 116,148,168
Mv 84,148,164
Rb 60,16,59392
Mv 52,148,160
Mv 56,152,160
Mv 88,152,164
Mv 120,152,168
Mv 152,152,172
Mv 184,152,176
Mv 216,152,180
Mv 248,152,184
Ma 216,152,188
Ma 220,156,188
Mv 252,156,184
Mv 220,156,180
Mv 188,156,176
Mv 156,156,172
Mv 124,156,168
Mv 92,156,164
Mv 60,156,160
s_waitcnt vmcnt(12) lgkmcnt(0)
s_barrier
Mv 32,0,32
Rt 128,18,49152
)" R"(
Mv 64,0,36
Mv 96,0,40
Rt 130,18,51200
v_lshrrev_b32 v5, 2, v0
Mv 128,0,44
s_add_u32 s39, s39, 0x40000
v_and_b32 v5, 8, v5
Mv 160,0,48
Rt 132,19,49152
s_add_u32 m0, s37, 16384
Mv 192,0,52
buffer_load_dwordx4 v12, s[8:11], s39 offen offset:0 lds
Mv 224,0,56
Rt 134,19,51200
v_and_or_b32 v5, v0, 7, v5
Ma 192,0,60
v_lshlrev_b32 v5, 8, v5
v_and_or_b32 v5, v0, 24, v5
Ma 196,4,60
Rt 136,20,49152
v_lshrrev_b32 v5, 1, v5
Mv 228,4,56
Mv 196,4,52
Rt 138,20,51200
Mv 164,4,48
Mv 132,4,44
Rt 140,21,49152
Mv 100,4,40
buffer_load_dwordx4 v13, s[8:11], s39 offen offset:1024 lds
Mv 68,4,36
Rt 142,21,51200
Mv 36,4,32
Mv 40,8,32
Rt 144,22,49152
Mv 72,8,36
Mv 104,8,40
Rt 146,22,51200
Mv 136,8,44
Mv 168,8,48
Rt 148,23,49152
Mv 200,8,52
buffer_load_dwordx4 v14, s[8:11], s39 offen offset:2048 lds
Mv 232,8,56
Rt 150,23,51200
Ma 200,8,60
Ma 204,12,60
Rt 152,24,49152
Mv 236,12,56
Mv 204,12,52
Rt 154,24,51200
Mv 172,12,48
Mv 140,12,44
Rt 156,25,49152
Mv 108,12,40
buffer_load_dwordx4 v15, s[8:11], s39 offen offset:3072 lds
Mv 76,12,36
Rt 158,25,51200
Mv 44,12,32
Mv 48,16,32
Rb 160,17,32768
Mv 80,16,36
Mv 112,16,40
Rb 164,17,34816
Mv 144,16,44
)" R"(
s_add_u32 s39, s39, 0x40000
Mv 176,16,48
Rb 168,17,40960
s_add_u32 m0, s37, 32768
Mv 208,16,52
buffer_load_dwordx4 v12, s[8:11], s39 offen offset:0 lds
Mv 240,16,56
Rb 172,17,43008
Ma 208,16,60
Ma 212,20,60
Rb 176,17,49152
Mv 244,20,56
Mv 212,20,52
Rb 180,17,51200
Mv 180,20,48
Mv 148,20,44
Rb 184,17,57344
Mv 116,20,40
buffer_load_dwordx4 v13, s[8:11], s39 offen offset:1024 lds
Mv 84,20,36
Rb 188,17,59392
Mv 52,20,32
Mv 56,24,32
Rt 224,29,0
Mv 88,24,36
Rt 226,29,8192
Mv 120,24,40
Rt 228,29,128
Mv 152,24,44
Rt 230,29,8320
Mv 184,24,48
Rt 232,29,256
Mv 216,24,52
buffer_load_dwordx4 v14, s[8:11], s39 offen offset:2048 lds
Rt 234,29,8448
Mv 248,24,56
Rt 236,29,384
Ma 216,24,60
Rt 238,29,8576
Ma 220,28,60
Mv 252,28,56
Mv 220,28,52
Mv 188,28,48
Mv 156,28,44
Mv 124,28,40
buffer_load_dwordx4 v15, s[8:11], s39 offen offset:3072 lds
Mv 92,28,36
Mv 60,28,32
s_waitcnt vmcnt(8) lgkmcnt(0)
s_barrier
Mv 32,128,160
Rt 0,18,0
Mv 64,128,164
s_add_u32 s38, s38, 0xffff0080
Mv 96,128,168
Rt 2,18,2048
s_add_u32 m0, s36, 32768
Mv 128,128,172
buffer_load_dwordx4 v8, s[4:7], s38 offen offset:0 lds
Mv 160,128,176
Rt 4,19,0
Mv 192,128,180
Mv 224,128,184
Rt 6,19,2048
Ma 192,128,188
)" R"(
buffer_load_dwordx4 v9, s[4:7], s38 offen offset:1024 lds
Ma 196,132,188
Rt 8,20,0
Mv 228,132,184
Mv 196,132,180
Rt 10,20,2048
Mv 164,132,176
buffer_load_dwordx4 v10, s[4:7], s38 offen offset:2048 lds
Mv 132,132,172
Rt 12,21,0
Mv 100,132,168
Mv 68,132,164
Rt 14,21,2048
Mv 36,132,160
buffer_load_dwordx4 v11, s[4:7], s38 offen offset:3072 lds
Mv 40,136,160
Rt 16,22,0
Mv 72,136,164
s_add_u32 s38, s38, 0x10000
Mv 104,136,168
Rt 18,22,2048
s_add_u32 m0, s36, 36864
Mv 136,136,172
buffer_load_dwordx4 v8, s[4:7], s38 offen offset:0 lds
Mv 168,136,176
Rt 20,23,0
Mv 200,136,180
Mv 232,136,184
Rt 22,23,2048
Ma 200,136,188
buffer_load_dwordx4 v9, s[4:7], s38 offen offset:1024 lds
Ma 204,140,188
Rt 24,24,0
Mv 236,140,184
Mv 204,140,180
Rt 26,24,2048
Mv 172,140,176
buffer_load_dwordx4 v10, s[4:7], s38 offen offset:2048 lds
Mv 140,140,172
Rt 28,25,0
s_lshl3_add_u32 s28, s94, s92
Mv 108,140,168
s_lshl3_add_u32 s29, s92, s94
s_cmp_lt_u32 s94, 8
s_cmov_b32 s28, s29
Mv 76,140,164
Rt 30,25,2048
s_mul_i32 s29, s28, 293
s_lshr_b32 s29, s29, 11
Mv 44,140,160
buffer_load_dwordx4 v11, s[4:7], s38 offen offset:3072 lds
Mv 48,144,160
Rb 32,16,0
s_mul_i32 s29, s29, 9
s_add_u32 s29, s29, s28
Mv 80,144,164
s_lshl_b32 s29, s29, 10
s_add_u32 s29, s29, s82
s_add_u32 s29, s29, s84
Mv 112,144,168
Rb 36,16,2048
s_add_u32 s94, s94, 4
Mv 144,144,172
buffer_store_dwordx4 a[224:227], v1, s[20:23], s29 offen sc0 sc1
)" R"(
Mv 176,144,176
Rb 40,16,8192
s_lshl3_add_u32 s28, s94, s92
Mv 208,144,180
s_lshl3_add_u32 s29, s92, s94
Mv 240,144,184
Rb 44,16,10240
s_cmp_lt_u32 s94, 8
s_cmov_b32 s28, s29
Ma 208,144,188
s_mul_i32 s29, s28, 293
s_lshr_b32 s29, s29, 11
Ma 212,148,188
Rb 48,16,16384
s_mul_i32 s29, s29, 9
s_add_u32 s29, s29, s28
Mv 244,148,184
s_lshl_b32 s29, s29, 10
s_add_u32 s29, s29, s82
Mv 212,148,180
Rb 52,16,18432
s_add_u32 s29, s29, s84
Mv 180,148,176
s_add_u32 s94, s94, 4
buffer_store_dwordx4 a[228:231], v1, s[20:23], s29 offen sc0 sc1
Mv 148,148,172
Rb 56,16,24576
s_lshl3_add_u32 s28, s94, s92
Mv 116,148,168
s_lshl3_add_u32 s29, s92, s94
Mv 84,148,164
Rb 60,16,26624
s_cmp_lt_u32 s94, 8
s_cmov_b32 s28, s29
Mv 52,148,160
s_mul_i32 s29, s28, 293
s_lshr_b32 s29, s29, 11
Mv 56,152,160
s_mul_i32 s29, s29, 9
s_add_u32 s29, s29, s28
Mv 88,152,164
s_lshl_b32 s29, s29, 10
s_add_u32 s29, s29, s82
Mv 120,152,168
s_add_u32 s29, s29, s84
s_add_u32 s94, s94, 4
Mv 152,152,172
buffer_store_dwordx4 a[232:235], v1, s[20:23], s29 offen sc0 sc1
Mv 184,152,176
s_lshl3_add_u32 s28, s94, s92
s_lshl3_add_u32 s29, s92, s94
Mv 216,152,180
s_cmp_lt_u32 s94, 8
s_cmov_b32 s28, s29
Mv 248,152,184
s_mul_i32 s29, s28, 293
s_lshr_b32 s29, s29, 11
Ma 216,152,188
s_mul_i32 s29, s29, 9
s_add_u32 s29, s29, s28
Ma 220,156,188
s_lshl_b32 s29, s29, 10
s_add_u32 s29, s29, s82
Mv 252,156,184
)" R"(
s_add_u32 s29, s29, s84
Mv 220,156,180
s_cmp_gt_u32 s33, 1
s_cmov_b32 s29, 0x40000000
Mv 188,156,176
buffer_store_dwordx4 a[236:239], v1, s[20:23], s29 offen sc0 sc1
Mv 156,156,172
Mv 124,156,168
Mv 92,156,164
Mv 60,156,160
s_waitcnt vmcnt(16) lgkmcnt(0)
s_barrier
Mv 32,0,32
Rt 128,18,16384
Mv 64,0,36
Mv 96,0,40
Rt 130,18,18432
Mv 128,0,44
s_add_u32 s39, s39, 0x40000
Mv 160,0,48
Rt 132,19,16384
s_add_u32 m0, s37, 49152
Mv 192,0,52
buffer_load_dwordx4 v12, s[8:11], s39 offen offset:0 lds
Mv 224,0,56
Rt 134,19,18432
Ma 192,0,60
Ma 196,4,60
Rt 136,20,16384
Mv 228,4,56
Mv 196,4,52
Rt 138,20,18432
Mv 164,4,48
Mv 132,4,44
Rt 140,21,16384
Mv 100,4,40
buffer_load_dwordx4 v13, s[8:11], s39 offen offset:1024 lds
Mv 68,4,36
Rt 142,21,18432
Mv 36,4,32
Mv 40,8,32
Rt 144,22,16384
Mv 72,8,36
Mv 104,8,40
Rt 146,22,18432
Mv 136,8,44
Mv 168,8,48
Rt 148,23,16384
Mv 200,8,52
buffer_load_dwordx4 v14, s[8:11], s39 offen offset:2048 lds
Mv 232,8,56
Rt 150,23,18432
Ma 200,8,60
Ma 204,12,60
Rt 152,24,16384
Mv 236,12,56
Mv 204,12,52
Rt 154,24,18432
Mv 172,12,48
Mv 140,12,44
Rt 156,25,16384
Mv 108,12,40
buffer_load_dwordx4 v15, s[8:11], s39 offen offset:3072 lds
Mv 76,12,36
Rt 158,25,18432
)" R"(
Mv 44,12,32
Mv 48,16,32
Rb 160,17,0
Mv 80,16,36
buffer_load_dword v4, v5, s[20:23], s50 offen offset:0 sc0 sc1
Mv 112,16,40
Rb 164,17,2048
Mv 144,16,44
Mv 176,16,48
Rb 168,17,8192
Mv 208,16,52
Mv 240,16,56
Rb 172,17,10240
Ma 208,16,60
Ma 212,20,60
Rb 176,17,16384
Mv 244,20,56
Mv 212,20,52
Rb 180,17,18432
Mv 180,20,48
Mv 148,20,44
Rb 184,17,24576
Mv 116,20,40
Mv 84,20,36
Rb 188,17,26624
Mv 52,20,32
Mv 56,24,32
Mv 88,24,36
Mv 120,24,40
Mv 152,24,44
Mv 184,24,48
Mv 216,24,52
Mv 248,24,56
Ma 216,24,60
Ma 220,28,60
Mv 252,28,56
Mv 220,28,52
Mv 188,28,48
Mv 156,28,44
Mv 124,28,40
Mv 92,28,36
Mv 60,28,32
s_waitcnt vmcnt(0) lgkmcnt(0)
s_barrier
Mv 32,128,160
Rt 0,18,32768
v_cmp_eq_u32_e32 vcc, s3, v4
s_andn2_b64 s[28:29], s[60:61], vcc
s_cbranch_scc1 .Lzsp11
.Lzok11:
Mv 64,128,164
Mv 96,128,168
Rt 2,18,34816
Mv 128,128,172
Mv 160,128,176
Rt 4,19,32768
s_add_u32 m0, s36, 0
Mv 192,128,180
buffer_load_dwordx4 v2, s[20:23], s55 offen offset:0 sc1 lds
Mv 224,128,184
Rt 6,19,34816
Ma 192,128,188
Ma 196,132,188
Rt 8,20,32768
Mv 228,132,184
)" R"(
Mv 196,132,180
Rt 10,20,34816
Mv 164,132,176
buffer_load_dwordx4 v2, s[20:23], s55 offen offset:1024 sc1 lds
Mv 132,132,172
Rt 12,21,32768
Mv 100,132,168
Mv 68,132,164
Rt 14,21,34816
Mv 36,132,160
Mv 40,136,160
Rt 16,22,32768
Mv 72,136,164
buffer_load_dwordx4 v2, s[20:23], s55 offen offset:2048 sc1 lds
Mv 104,136,168
Rt 18,22,34816
Mv 136,136,172
Mv 168,136,176
Rt 20,23,32768
Mv 200,136,180
Mv 232,136,184
Rt 22,23,34816
Ma 200,136,188
buffer_load_dwordx4 v2, s[20:23], s55 offen offset:3072 sc1 lds
Ma 204,140,188
Rt 24,24,32768
Mv 236,140,184
Mv 204,140,180
Rt 26,24,34816
Mv 172,140,176
Mv 140,140,172
Rt 28,25,32768
s_add_u32 m0, s36, 4096
Mv 108,140,168
buffer_load_dwordx4 v3, s[20:23], s55 offen offset:0 sc1 lds
Mv 76,140,164
Rt 30,25,34816
Mv 44,140,160
Mv 48,144,160
Rb 32,16,32768
Mv 80,144,164
Mv 112,144,168
Rb 36,16,34816
Mv 144,144,172
buffer_load_dwordx4 v3, s[20:23], s55 offen offset:1024 sc1 lds
Mv 176,144,176
Rb 40,16,40960
Mv 208,144,180
Mv 240,144,184
Rb 44,16,43008
Ma 208,144,188
Ma 212,148,188
Rb 48,16,49152
Mv 244,148,184
buffer_load_dwordx4 v3, s[20:23], s55 offen offset:2048 sc1 lds
Mv 212,148,180
Rb 52,16,51200
Mv 180,148,176
Mv 148,148,172
Rb 56,16,57344
Mv 116,148,168
Mv 84,148,164
Rb 60,16,59392
Mv 52,148,160
)" R"(
buffer_load_dwordx4 v3, s[20:23], s55 offen offset:3072 sc1 lds
Mv 56,152,160
Mv 88,152,164
Mv 120,152,168
Mv 152,152,172
Mv 184,152,176
Mv 216,152,180
Mv 248,152,184
Ma 216,152,188
Ma 220,156,188
s_add_u32 s55, s55, 0x10000
Mv 252,156,184
Mv 220,156,180
Mv 188,156,176
Mv 156,156,172
Mv 124,156,168
Mv 92,156,164
Mv 60,156,160
s_waitcnt lgkmcnt(4)
s_barrier
Mv 32,0,32
Rt 128,18,49152
Mv 64,0,36
Rt 130,18,51200
Mv 96,0,40
Rt 132,19,49152
Mv 128,0,44
Rt 134,19,51200
Mv 132,4,44
Rt 136,20,49152
Mv 100,4,40
Rt 138,20,51200
Mv 68,4,36
Rt 140,21,49152
Mv 36,4,32
Rt 142,21,51200
Mv 40,8,32
Rt 144,22,49152
Mv 72,8,36
Rt 146,22,51200
Mv 104,8,40
Rt 148,23,49152
Mv 136,8,44
Rt 150,23,51200
Mv 140,12,44
Rt 152,24,49152
Mv 108,12,40
Rt 154,24,51200
Mv 76,12,36
Rt 156,25,49152
Mv 44,12,32
Rt 158,25,51200
Mv 48,16,32
Rb 160,17,32768
Mv 80,16,36
Rb 164,17,34816
Mv 112,16,40
Rb 168,17,40960
Mv 144,16,44
Rb 172,17,43008
Mv 148,20,44
Rb 176,17,49152
Mv 116,20,40
Rb 180,17,51200
Mv 84,20,36
)" R"(
Rb 184,17,57344
Mv 52,20,32
Rb 188,17,59392
Mv 56,24,32
v_add_u32 v8, 0x10000, v1
v_add_u32 v10, 0x20000, v1
Mv 88,24,36
s_mov_b32 s93, 2
s_mov_b32 s94, 0
Mv 120,24,40
Mv 152,24,44
Mv 156,28,44
Mv 124,28,40
Mv 92,28,36
Mv 60,28,32
s_waitcnt lgkmcnt(0)
s_barrier
Mv 32,128,160
Mv 36,132,160
Mv 40,136,160
s_add_u32 m0, s36, 32768
Mv 44,140,160
buffer_load_dwordx4 v2, s[20:23], s55 offen offset:0 sc1 lds
Mv 48,144,160
Mv 52,148,160
s_nop 0
Cv 32,32,33
Cv 33,34,35
Mv 56,152,160
Cv 34,36,37
Cv 35,38,39
Mv 60,156,160
buffer_load_dwordx4 v2, s[20:23], s55 offen offset:1024 sc1 lds
Mv 64,128,164
Cv 40,40,41
Cv 41,42,43
Mv 68,132,164
Cv 42,44,45
Cv 43,46,47
Mv 72,136,164
Cv 48,48,49
Cv 49,50,51
Mv 76,140,164
buffer_load_dwordx4 v2, s[20:23], s55 offen offset:2048 sc1 lds
Mv 80,144,164
Cv 50,52,53
Cv 51,54,55
Mv 84,148,164
Cv 56,56,57
Cv 57,58,59
Mv 88,152,164
Cv 58,60,61
Cv 59,62,63
Mv 92,156,164
buffer_load_dwordx4 v2, s[20:23], s55 offen offset:3072 sc1 lds
Mv 96,128,168
Cv 64,64,65
Cv 65,66,67
Mv 100,132,168
Cv 66,68,69
Cv 67,70,71
Mv 104,136,168
s_add_u32 m0, s36, 36864
Cv 72,72,73
Mv 108,140,168
)" R"(
buffer_load_dwordx4 v3, s[20:23], s55 offen offset:0 sc1 lds
Mv 112,144,168
Cv 73,74,75
Cv 74,76,77
Mv 116,148,168
Cv 75,78,79
Cv 80,80,81
Mv 120,152,168
Cv 81,82,83
Cv 82,84,85
Mv 124,156,168
buffer_store_dwordx4 v[32:35], v2, s[20:23], s45 offen offset:0 sc0 sc1
Mv 128,128,172
Cv 83,86,87
Cv 88,88,89
Mv 132,132,172
Cv 89,90,91
Cv 90,92,93
Mv 136,136,172
Cv 91,94,95
Mv 140,140,172
buffer_load_dwordx4 v3, s[20:23], s55 offen offset:1024 sc1 lds
Mv 144,144,172
Mv 148,148,172
Cv 128,128,129
Cv 129,130,131
Mv 152,152,172
Cv 130,132,133
Cv 131,134,135
Mv 156,156,172
buffer_store_dwordx4 v[40:43], v2, s[20:23], s45 offen offset:1024 sc0 sc1
Mv 160,0,48
Cv 136,136,137
Cv 137,138,139
Mv 160,128,176
Cv 138,140,141
Cv 139,142,143
Mv 164,4,48
Cv 144,144,145
Cv 145,146,147
Mv 164,132,176
buffer_load_dwordx4 v3, s[20:23], s55 offen offset:2048 sc1 lds
Mv 168,8,48
Cv 146,148,149
Cv 147,150,151
Mv 168,136,176
Cv 152,152,153
Cv 153,154,155
Mv 172,12,48
Cv 154,156,157
Cv 155,158,159
Mv 172,140,176
buffer_store_dwordx4 v[48:51], v2, s[20:23], s45 offen offset:2048 sc0 sc1
Mv 176,16,48
Cv 132,96,97
Cv 133,98,99
Mv 176,144,176
Cv 134,100,101
Cv 135,102,103
Mv 180,20,48
Cv 140,104,105
Cv 141,106,107
Mv 180,148,176
buffer_load_dwordx4 v3, s[20:23], s55 offen offset:3072 sc1 lds
)" R"(
Mv 184,24,48
Cv 142,108,109
Cv 143,110,111
Mv 184,152,176
Cv 148,112,113
Cv 149,114,115
Mv 188,28,48
Cv 150,116,117
Cv 151,118,119
Mv 188,156,176
buffer_store_dwordx4 v[56:59], v2, s[20:23], s45 offen offset:3072 sc0 sc1
Mv 192,0,52
Cv 156,120,121
Cv 157,122,123
Mv 192,128,180
Cv 158,124,125
Cv 159,126,127
Mv 196,4,52
s_add_u32 m0, s36, 98304
Cv 160,160,161
Mv 196,132,180
buffer_load_dwordx4 v2, s[20:23], s56 offen offset:0 sc1 lds
Mv 200,8,52
Cv 161,162,163
Cv 162,164,165
Mv 200,136,180
Cv 163,166,167
v_accvgpr_write_b32 a224, v160
Mv 204,12,52
v_accvgpr_write_b32 a225, v161
v_accvgpr_write_b32 a226, v162
Mv 204,140,180
buffer_store_dwordx4 v[64:67], v3, s[20:23], s45 offen offset:0 sc0 sc1
Mv 208,16,52
v_accvgpr_write_b32 a227, v163
Cv 168,168,169
Mv 208,144,180
Cv 169,170,171
Cv 170,172,173
Mv 212,20,52
Cv 171,174,175
v_accvgpr_write_b32 a228, v168
Mv 212,148,180
buffer_load_dwordx4 v2, s[20:23], s56 offen offset:1024 sc1 lds
Mv 216,24,52
v_accvgpr_write_b32 a229, v169
v_accvgpr_write_b32 a230, v170
Mv 216,152,180
v_accvgpr_write_b32 a231, v171
Cv 176,176,177
Mv 220,28,52
Cv 177,178,179
Cv 178,180,181
Mv 220,156,180
buffer_store_dwordx4 v[72:75], v3, s[20:23], s45 offen offset:1024 sc0 sc1
Mv 224,0,56
Cv 179,182,183
v_accvgpr_write_b32 a232, v176
Ma 192,0,60
v_accvgpr_write_b32 a233, v177
v_accvgpr_write_b32 a234, v178
Ma 196,4,60
v_accvgpr_write_b32 a235, v179
Cv 184,184,185
)" R"(
Mv 228,4,56
buffer_load_dwordx4 v2, s[20:23], s56 offen offset:2048 sc1 lds
Mv 232,8,56
Cv 185,186,187
Cv 186,188,189
Ma 200,8,60
Cv 187,190,191
v_accvgpr_write_b32 a236, v184
Ma 204,12,60
v_accvgpr_write_b32 a237, v185
v_accvgpr_write_b32 a238, v186
Mv 236,12,56
s_waitcnt vmcnt(17)
s_barrier
Mv 240,16,56
buffer_store_dwordx4 v[80:83], v3, s[20:23], s45 offen offset:2048 sc0 sc1
Ma 208,16,60
v_accvgpr_write_b32 a239, v187
Ma 212,20,60
Mv 244,20,56
buffer_load_dwordx4 v2, s[20:23], s56 offen offset:3072 sc1 lds
Mv 248,24,56
Ma 216,24,60
Ma 220,28,60
Mv 252,28,56
buffer_store_dwordx4 v[88:91], v3, s[20:23], s45 offen offset:3072 sc0 sc1
Mv 224,128,184
Rb 0,1,0
Mv 228,132,184
Rb 4,1,1024
Mv 232,136,184
s_add_u32 m0, s36, 102400
Rb 8,1,2048
Mv 236,140,184
buffer_load_dwordx4 v3, s[20:23], s56 offen offset:0 sc1 lds
Rb 12,1,3072
Mv 240,144,184
Rb 16,1,8192
Mv 244,148,184
Rb 20,1,9216
Mv 248,152,184
Rb 24,1,10240
Mv 252,156,184
Rb 28,1,11264
Ma 192,128,188
Rb 32,1,4096
Ma 196,132,188
Rb 36,1,5120
Ma 200,136,188
Rb 40,1,6144
Ma 204,140,188
buffer_load_dwordx4 v3, s[20:23], s56 offen offset:1024 sc1 lds
Rb 44,1,7168
Ma 208,144,188
Ma 212,148,188
buffer_load_dwordx4 v3, s[20:23], s56 offen offset:2048 sc1 lds
Ma 216,152,188
Ma 220,156,188
buffer_load_dwordx4 v3, s[20:23], s56 offen offset:3072 sc1 lds
s_add_u32 s56, s56, 0x10000
s_branch .LzFB
.LzPBVX:
Mv 32,32,0
Rt 128,18,16384
Mv 36,36,0
)" R"(
Mv 96,40,0
Rt 130,18,18432
Mv 100,44,0
s_add_u32 s39, s39, 0x40000
Mv 160,48,0
Rt 132,19,16384
s_add_u32 m0, s37, 49152
Mv 164,52,0
buffer_load_dwordx4 v12, s[8:11], s39 offen offset:0 lds
Mv 224,56,0
Rt 134,19,18432
Mv 228,60,0
Mv 236,60,4
Rt 136,20,16384
Mv 232,56,4
Mv 172,52,4
Rt 138,20,18432
Mv 168,48,4
Mv 108,44,4
Rt 140,21,16384
Mv 104,40,4
buffer_load_dwordx4 v13, s[8:11], s39 offen offset:1024 lds
Mv 44,36,4
Rt 142,21,18432
Mv 40,32,4
Mv 48,32,8
Rt 144,22,16384
Mv 52,36,8
Mv 112,40,8
Rt 146,22,18432
Mv 116,44,8
Mv 176,48,8
Rt 148,23,16384
Mv 180,52,8
buffer_load_dwordx4 v14, s[8:11], s39 offen offset:2048 lds
Mv 240,56,8
Rt 150,23,18432
Mv 244,60,8
Mv 252,60,12
Rt 152,24,16384
Mv 248,56,12
Mv 188,52,12
Rt 154,24,18432
Mv 184,48,12
Mv 124,44,12
Rt 156,25,16384
Mv 120,40,12
buffer_load_dwordx4 v15, s[8:11], s39 offen offset:3072 lds
Mv 60,36,12
Rt 158,25,18432
Mv 56,32,12
Mv 64,32,16
Rb 160,17,0
Mv 68,36,16
Mv 128,40,16
Rb 164,17,2048
Mv 132,44,16
s_add_u32 s39, s39, 0x40000
Mv 192,48,16
Rb 168,17,8192
s_add_u32 m0, s37, 0
Mv 196,52,16
buffer_load_dwordx4 v12, s[8:11], s39 offen offset:0 lds
Ma 192,56,16
)" R"(
Rb 172,17,10240
Ma 196,60,16
Ma 204,60,20
Rb 176,17,16384
Ma 200,56,20
Mv 204,52,20
Rb 180,17,18432
Mv 200,48,20
Mv 140,44,20
Rb 184,17,24576
Mv 136,40,20
buffer_load_dwordx4 v13, s[8:11], s39 offen offset:1024 lds
Mv 76,36,20
Rb 188,17,26624
Mv 72,32,20
Mv 80,32,24
Mv 84,36,24
Mv 144,40,24
Mv 148,44,24
Mv 208,48,24
Mv 212,52,24
buffer_load_dwordx4 v14, s[8:11], s39 offen offset:2048 lds
Ma 208,56,24
Ma 212,60,24
Ma 220,60,28
Ma 216,56,28
Mv 220,52,28
Mv 216,48,28
Mv 156,44,28
Mv 152,40,28
buffer_load_dwordx4 v15, s[8:11], s39 offen offset:3072 lds
Mv 92,36,28
Mv 88,32,28
s_waitcnt vmcnt(8) lgkmcnt(0)
s_barrier
Mv 32,160,128
Rt 0,18,32768
Mv 36,164,128
s_add_u32 s38, s38, 0xffff0080
Mv 96,168,128
Rt 2,18,34816
s_add_u32 m0, s36, 0
Mv 100,172,128
buffer_load_dwordx4 v8, s[4:7], s38 offen offset:0 lds
Mv 160,176,128
Rt 4,19,32768
Mv 164,180,128
Mv 224,184,128
Rt 6,19,34816
Mv 228,188,128
buffer_load_dwordx4 v9, s[4:7], s38 offen offset:1024 lds
Mv 236,188,132
Rt 8,20,32768
Mv 232,184,132
Mv 172,180,132
Rt 10,20,34816
Mv 168,176,132
buffer_load_dwordx4 v10, s[4:7], s38 offen offset:2048 lds
Mv 108,172,132
Rt 12,21,32768
Mv 104,168,132
Mv 44,164,132
Rt 14,21,34816
Mv 40,160,132
buffer_load_dwordx4 v11, s[4:7], s38 offen offset:3072 lds
)" R"(
Mv 48,160,136
Rt 16,22,32768
Mv 52,164,136
s_add_u32 s38, s38, 0x10000
Mv 112,168,136
Rt 18,22,34816
s_add_u32 m0, s36, 4096
Mv 116,172,136
buffer_load_dwordx4 v8, s[4:7], s38 offen offset:0 lds
Mv 176,176,136
Rt 20,23,32768
Mv 180,180,136
Mv 240,184,136
Rt 22,23,34816
Mv 244,188,136
buffer_load_dwordx4 v9, s[4:7], s38 offen offset:1024 lds
Mv 252,188,140
Rt 24,24,32768
Mv 248,184,140
Mv 188,180,140
Rt 26,24,34816
Mv 184,176,140
buffer_load_dwordx4 v10, s[4:7], s38 offen offset:2048 lds
Mv 124,172,140
Rt 28,25,32768
Mv 120,168,140
Mv 60,164,140
Rt 30,25,34816
Mv 56,160,140
buffer_load_dwordx4 v11, s[4:7], s38 offen offset:3072 lds
Mv 64,160,144
Rb 32,16,32768
Mv 68,164,144
Mv 128,168,144
Rb 36,16,34816
Mv 132,172,144
Mv 192,176,144
Rb 40,16,40960
Mv 196,180,144
Ma 192,184,144
Rb 44,16,43008
Ma 196,188,144
Ma 204,188,148
Rb 48,16,49152
Ma 200,184,148
Mv 204,180,148
Rb 52,16,51200
Mv 200,176,148
Mv 140,172,148
Rb 56,16,57344
Mv 136,168,148
Mv 76,164,148
Rb 60,16,59392
Mv 72,160,148
Mv 80,160,152
Mv 84,164,152
Mv 144,168,152
Mv 148,172,152
Mv 208,176,152
Mv 212,180,152
Ma 208,184,152
Ma 212,188,152
Ma 220,188,156
Ma 216,184,156
)" R"(
Mv 220,180,156
Mv 216,176,156
Mv 156,172,156
Mv 152,168,156
Mv 92,164,156
Mv 88,160,156
s_waitcnt vmcnt(12) lgkmcnt(0)
s_barrier
Mv 32,32,0
Rt 128,18,49152
Mv 36,36,0
Mv 96,40,0
Rt 130,18,51200
v_lshrrev_b32 v5, 2, v0
Mv 100,44,0
s_add_u32 s39, s39, 0x40000
v_and_b32 v5, 8, v5
Mv 160,48,0
Rt 132,19,49152
s_add_u32 m0, s37, 16384
Mv 164,52,0
buffer_load_dwordx4 v12, s[8:11], s39 offen offset:0 lds
Mv 224,56,0
Rt 134,19,51200
v_and_or_b32 v5, v0, 7, v5
Mv 228,60,0
v_lshlrev_b32 v5, 8, v5
v_and_or_b32 v5, v0, 24, v5
Mv 236,60,4
Rt 136,20,49152
v_lshrrev_b32 v5, 1, v5
Mv 232,56,4
Mv 172,52,4
Rt 138,20,51200
Mv 168,48,4
Mv 108,44,4
Rt 140,21,49152
Mv 104,40,4
buffer_load_dwordx4 v13, s[8:11], s39 offen offset:1024 lds
Mv 44,36,4
Rt 142,21,51200
Mv 40,32,4
Mv 48,32,8
Rt 144,22,49152
Mv 52,36,8
Mv 112,40,8
Rt 146,22,51200
Mv 116,44,8
Mv 176,48,8
Rt 148,23,49152
Mv 180,52,8
buffer_load_dwordx4 v14, s[8:11], s39 offen offset:2048 lds
Mv 240,56,8
Rt 150,23,51200
Mv 244,60,8
Mv 252,60,12
Rt 152,24,49152
Mv 248,56,12
Mv 188,52,12
Rt 154,24,51200
Mv 184,48,12
Mv 124,44,12
Rt 156,25,49152
Mv 120,40,12
buffer_load_dwordx4 v15, s[8:11], s39 offen offset:3072 lds
)" R"(
Mv 60,36,12
Rt 158,25,51200
Mv 56,32,12
Mv 64,32,16
Rb 160,17,32768
Mv 68,36,16
Mv 128,40,16
Rb 164,17,34816
Mv 132,44,16
s_add_u32 s39, s39, 0x40000
Mv 192,48,16
Rb 168,17,40960
s_add_u32 m0, s37, 32768
Mv 196,52,16
buffer_load_dwordx4 v12, s[8:11], s39 offen offset:0 lds
Ma 192,56,16
Rb 172,17,43008
Ma 196,60,16
Ma 204,60,20
Rb 176,17,49152
Ma 200,56,20
Mv 204,52,20
Rb 180,17,51200
Mv 200,48,20
Mv 140,44,20
Rb 184,17,57344
Mv 136,40,20
buffer_load_dwordx4 v13, s[8:11], s39 offen offset:1024 lds
Mv 76,36,20
Rb 188,17,59392
Mv 72,32,20
Mv 80,32,24
Rt 224,29,0
Mv 84,36,24
Rt 226,29,8192
Mv 144,40,24
Rt 228,29,128
Mv 148,44,24
Rt 230,29,8320
Mv 208,48,24
Rt 232,29,256
Mv 212,52,24
buffer_load_dwordx4 v14, s[8:11], s39 offen offset:2048 lds
Rt 234,29,8448
Ma 208,56,24
Rt 236,29,384
Ma 212,60,24
Rt 238,29,8576
Ma 220,60,28
Ma 216,56,28
Mv 220,52,28
Mv 216,48,28
Mv 156,44,28
Mv 152,40,28
buffer_load_dwordx4 v15, s[8:11], s39 offen offset:3072 lds
Mv 92,36,28
Mv 88,32,28
s_waitcnt vmcnt(8) lgkmcnt(0)
s_barrier
Mv 32,160,128
Rt 0,18,0
Mv 36,164,128
s_add_u32 s38, s38, 0xffff0080
Mv 96,168,128
Rt 2,18,2048
)" R"(
s_add_u32 m0, s36, 32768
Mv 100,172,128
buffer_load_dwordx4 v8, s[4:7], s38 offen offset:0 lds
Mv 160,176,128
Rt 4,19,0
Mv 164,180,128
Mv 224,184,128
Rt 6,19,2048
Mv 228,188,128
buffer_load_dwordx4 v9, s[4:7], s38 offen offset:1024 lds
Mv 236,188,132
Rt 8,20,0
Mv 232,184,132
Mv 172,180,132
Rt 10,20,2048
Mv 168,176,132
buffer_load_dwordx4 v10, s[4:7], s38 offen offset:2048 lds
Mv 108,172,132
Rt 12,21,0
Mv 104,168,132
Mv 44,164,132
Rt 14,21,2048
Mv 40,160,132
buffer_load_dwordx4 v11, s[4:7], s38 offen offset:3072 lds
Mv 48,160,136
Rt 16,22,0
Mv 52,164,136
s_add_u32 s38, s38, 0x10000
Mv 112,168,136
Rt 18,22,2048
s_add_u32 m0, s36, 36864
Mv 116,172,136
buffer_load_dwordx4 v8, s[4:7], s38 offen offset:0 lds
Mv 176,176,136
Rt 20,23,0
Mv 180,180,136
Mv 240,184,136
Rt 22,23,2048
Mv 244,188,136
buffer_load_dwordx4 v9, s[4:7], s38 offen offset:1024 lds
Mv 252,188,140
Rt 24,24,0
Mv 248,184,140
Mv 188,180,140
Rt 26,24,2048
Mv 184,176,140
buffer_load_dwordx4 v10, s[4:7], s38 offen offset:2048 lds
Mv 124,172,140
Rt 28,25,0
s_lshl3_add_u32 s28, s94, s92
Mv 120,168,140
s_lshl3_add_u32 s29, s92, s94
s_cmp_lt_u32 s94, 8
s_cmov_b32 s28, s29
Mv 60,164,140
Rt 30,25,2048
s_mul_i32 s29, s28, 293
s_lshr_b32 s29, s29, 11
Mv 56,160,140
buffer_load_dwordx4 v11, s[4:7], s38 offen offset:3072 lds
Mv 64,160,144
Rb 32,16,0
s_mul_i32 s29, s29, 9
s_add_u32 s29, s29, s28
)" R"(
Mv 68,164,144
s_lshl_b32 s29, s29, 10
s_add_u32 s29, s29, s82
s_add_u32 s29, s29, s84
Mv 128,168,144
Rb 36,16,2048
s_add_u32 s94, s94, 4
Mv 132,172,144
buffer_store_dwordx4 a[224:227], v1, s[20:23], s29 offen sc0 sc1
Mv 192,176,144
Rb 40,16,8192
s_lshl3_add_u32 s28, s94, s92
Mv 196,180,144
s_lshl3_add_u32 s29, s92, s94
Ma 192,184,144
Rb 44,16,10240
s_cmp_lt_u32 s94, 8
s_cmov_b32 s28, s29
Ma 196,188,144
s_mul_i32 s29, s28, 293
s_lshr_b32 s29, s29, 11
Ma 204,188,148
Rb 48,16,16384
s_mul_i32 s29, s29, 9
s_add_u32 s29, s29, s28
Ma 200,184,148
s_lshl_b32 s29, s29, 10
s_add_u32 s29, s29, s82
Mv 204,180,148
Rb 52,16,18432
s_add_u32 s29, s29, s84
Mv 200,176,148
s_add_u32 s94, s94, 4
buffer_store_dwordx4 a[228:231], v1, s[20:23], s29 offen sc0 sc1
Mv 140,172,148
Rb 56,16,24576
s_lshl3_add_u32 s28, s94, s92
Mv 136,168,148
s_lshl3_add_u32 s29, s92, s94
Mv 76,164,148
Rb 60,16,26624
s_cmp_lt_u32 s94, 8
s_cmov_b32 s28, s29
Mv 72,160,148
s_mul_i32 s29, s28, 293
s_lshr_b32 s29, s29, 11
Mv 80,160,152
s_mul_i32 s29, s29, 9
s_add_u32 s29, s29, s28
Mv 84,164,152
s_lshl_b32 s29, s29, 10
s_add_u32 s29, s29, s82
Mv 144,168,152
s_add_u32 s29, s29, s84
s_add_u32 s94, s94, 4
Mv 148,172,152
buffer_store_dwordx4 a[232:235], v1, s[20:23], s29 offen sc0 sc1
Mv 208,176,152
s_lshl3_add_u32 s28, s94, s92
s_lshl3_add_u32 s29, s92, s94
Mv 212,180,152
s_cmp_lt_u32 s94, 8
s_cmov_b32 s28, s29
Ma 208,184,152
)" R"(
s_mul_i32 s29, s28, 293
s_lshr_b32 s29, s29, 11
Ma 212,188,152
s_mul_i32 s29, s29, 9
s_add_u32 s29, s29, s28
Ma 220,188,156
s_lshl_b32 s29, s29, 10
s_add_u32 s29, s29, s82
Ma 216,184,156
s_add_u32 s29, s29, s84
Mv 220,180,156
s_cmp_gt_u32 s33, 1
s_cmov_b32 s29, 0x40000000
Mv 216,176,156
buffer_store_dwordx4 a[236:239], v1, s[20:23], s29 offen sc0 sc1
Mv 156,172,156
Mv 152,168,156
Mv 92,164,156
Mv 88,160,156
s_waitcnt vmcnt(16) lgkmcnt(0)
s_barrier
Mv 32,32,0
Rt 128,18,16384
Mv 36,36,0
Mv 96,40,0
Rt 130,18,18432
Mv 100,44,0
s_add_u32 s39, s39, 0x40000
Mv 160,48,0
Rt 132,19,16384
s_add_u32 m0, s37, 49152
Mv 164,52,0
buffer_load_dwordx4 v12, s[8:11], s39 offen offset:0 lds
Mv 224,56,0
Rt 134,19,18432
Mv 228,60,0
Mv 236,60,4
Rt 136,20,16384
Mv 232,56,4
Mv 172,52,4
Rt 138,20,18432
Mv 168,48,4
Mv 108,44,4
Rt 140,21,16384
Mv 104,40,4
buffer_load_dwordx4 v13, s[8:11], s39 offen offset:1024 lds
Mv 44,36,4
Rt 142,21,18432
Mv 40,32,4
Mv 48,32,8
Rt 144,22,16384
Mv 52,36,8
Mv 112,40,8
Rt 146,22,18432
Mv 116,44,8
Mv 176,48,8
Rt 148,23,16384
Mv 180,52,8
buffer_load_dwordx4 v14, s[8:11], s39 offen offset:2048 lds
Mv 240,56,8
Rt 150,23,18432
Mv 244,60,8
Mv 252,60,12
Rt 152,24,16384
Mv 248,56,12
)" R"(
Mv 188,52,12
Rt 154,24,18432
Mv 184,48,12
Mv 124,44,12
Rt 156,25,16384
Mv 120,40,12
buffer_load_dwordx4 v15, s[8:11], s39 offen offset:3072 lds
Mv 60,36,12
Rt 158,25,18432
Mv 56,32,12
Mv 64,32,16
Rb 160,17,0
Mv 68,36,16
buffer_load_dword v4, v5, s[20:23], s50 offen offset:0 sc0 sc1
Mv 128,40,16
Rb 164,17,2048
Mv 132,44,16
Mv 192,48,16
Rb 168,17,8192
Mv 196,52,16
Ma 192,56,16
Rb 172,17,10240
Ma 196,60,16
Ma 204,60,20
Rb 176,17,16384
Ma 200,56,20
Mv 204,52,20
Rb 180,17,18432
Mv 200,48,20
Mv 140,44,20
Rb 184,17,24576
Mv 136,40,20
Mv 76,36,20
Rb 188,17,26624
Mv 72,32,20
Mv 80,32,24
Mv 84,36,24
Mv 144,40,24
Mv 148,44,24
Mv 208,48,24
Mv 212,52,24
Ma 208,56,24
Ma 212,60,24
Ma 220,60,28
Ma 216,56,28
Mv 220,52,28
Mv 216,48,28
Mv 156,44,28
Mv 152,40,28
Mv 92,36,28
Mv 88,32,28
s_waitcnt vmcnt(0) lgkmcnt(0)
s_barrier
Mv 32,160,128
Rt 0,18,32768
v_cmp_eq_u32_e32 vcc, s3, v4
s_andn2_b64 s[28:29], s[60:61], vcc
s_cbranch_scc1 .Lzsp12
.Lzok12:
Mv 36,164,128
Mv 96,168,128
Rt 2,18,34816
Mv 100,172,128
Mv 160,176,128
Rt 4,19,32768
)" R"(
s_add_u32 m0, s36, 0
Mv 164,180,128
buffer_load_dwordx4 v2, s[20:23], s55 offen offset:0 sc1 lds
Mv 224,184,128
Rt 6,19,34816
Mv 228,188,128
Mv 236,188,132
Rt 8,20,32768
Mv 232,184,132
Mv 172,180,132
Rt 10,20,34816
Mv 168,176,132
buffer_load_dwordx4 v2, s[20:23], s55 offen offset:1024 sc1 lds
Mv 108,172,132
Rt 12,21,32768
Mv 104,168,132
Mv 44,164,132
Rt 14,21,34816
Mv 40,160,132
Mv 48,160,136
Rt 16,22,32768
Mv 52,164,136
buffer_load_dwordx4 v2, s[20:23], s55 offen offset:2048 sc1 lds
Mv 112,168,136
Rt 18,22,34816
Mv 116,172,136
Mv 176,176,136
Rt 20,23,32768
Mv 180,180,136
Mv 240,184,136
Rt 22,23,34816
Mv 244,188,136
buffer_load_dwordx4 v2, s[20:23], s55 offen offset:3072 sc1 lds
Mv 252,188,140
Rt 24,24,32768
Mv 248,184,140
Mv 188,180,140
Rt 26,24,34816
Mv 184,176,140
Mv 124,172,140
Rt 28,25,32768
s_add_u32 m0, s36, 4096
Mv 120,168,140
buffer_load_dwordx4 v3, s[20:23], s55 offen offset:0 sc1 lds
Mv 60,164,140
Rt 30,25,34816
Mv 56,160,140
Mv 64,160,144
Rb 32,16,32768
Mv 68,164,144
Mv 128,168,144
Rb 36,16,34816
Mv 132,172,144
buffer_load_dwordx4 v3, s[20:23], s55 offen offset:1024 sc1 lds
Mv 192,176,144
Rb 40,16,40960
Mv 196,180,144
Ma 192,184,144
Rb 44,16,43008
Ma 196,188,144
Ma 204,188,148
Rb 48,16,49152
Ma 200,184,148
buffer_load_dwordx4 v3, s[20:23], s55 offen offset:2048 sc1 lds
)" R"(
Mv 204,180,148
Rb 52,16,51200
Mv 200,176,148
Mv 140,172,148
Rb 56,16,57344
Mv 136,168,148
Mv 76,164,148
Rb 60,16,59392
Mv 72,160,148
buffer_load_dwordx4 v3, s[20:23], s55 offen offset:3072 sc1 lds
Mv 80,160,152
Mv 84,164,152
Mv 144,168,152
Mv 148,172,152
Mv 208,176,152
Mv 212,180,152
Ma 208,184,152
Ma 212,188,152
Ma 220,188,156
s_add_u32 s55, s55, 0x10000
Ma 216,184,156
Mv 220,180,156
Mv 216,176,156
Mv 156,172,156
Mv 152,168,156
Mv 92,164,156
Mv 88,160,156
s_waitcnt lgkmcnt(4)
s_barrier
Mv 32,32,0
Rt 128,18,49152
Mv 36,36,0
Rt 130,18,51200
Mv 96,40,0
Rt 132,19,49152
Mv 100,44,0
Rt 134,19,51200
Mv 108,44,4
Rt 136,20,49152
Mv 104,40,4
Rt 138,20,51200
Mv 44,36,4
Rt 140,21,49152
Mv 40,32,4
Rt 142,21,51200
Mv 48,32,8
Rt 144,22,49152
Mv 52,36,8
Rt 146,22,51200
Mv 112,40,8
Rt 148,23,49152
Mv 116,44,8
Rt 150,23,51200
Mv 124,44,12
Rt 152,24,49152
Mv 120,40,12
Rt 154,24,51200
Mv 60,36,12
Rt 156,25,49152
Mv 56,32,12
Rt 158,25,51200
Mv 64,32,16
Rb 160,17,32768
Mv 68,36,16
Rb 164,17,34816
)" R"(
Mv 128,40,16
Rb 168,17,40960
Mv 132,44,16
Rb 172,17,43008
Mv 140,44,20
Rb 176,17,49152
Mv 136,40,20
Rb 180,17,51200
Mv 76,36,20
Rb 184,17,57344
Mv 72,32,20
Rb 188,17,59392
Mv 80,32,24
v_add_u32 v8, 0x10000, v1
v_add_u32 v10, 0x20000, v1
Mv 84,36,24
s_mov_b32 s93, 2
s_mov_b32 s94, 0
Mv 144,40,24
Mv 148,44,24
Mv 156,44,28
Mv 152,40,28
Mv 92,36,28
Mv 88,32,28
s_waitcnt lgkmcnt(0)
s_barrier
Mv 32,160,128
Mv 36,164,128
Mv 40,160,132
s_add_u32 m0, s36, 32768
Mv 44,164,132
buffer_load_dwordx4 v2, s[20:23], s55 offen offset:0 sc1 lds
Mv 48,160,136
Mv 52,164,136
s_nop 0
Cv 32,32,33
Cv 33,34,35
Mv 56,160,140
Cv 34,36,37
Cv 35,38,39
Mv 60,164,140
buffer_load_dwordx4 v2, s[20:23], s55 offen offset:1024 sc1 lds
Mv 64,160,144
Cv 40,40,41
Cv 41,42,43
Mv 68,164,144
Cv 42,44,45
Cv 43,46,47
Mv 72,160,148
Cv 48,48,49
Cv 49,50,51
Mv 76,164,148
buffer_load_dwordx4 v2, s[20:23], s55 offen offset:2048 sc1 lds
Mv 80,160,152
Cv 50,52,53
Cv 51,54,55
Mv 84,164,152
Cv 56,56,57
Cv 57,58,59
Mv 88,160,156
Cv 58,60,61
Cv 59,62,63
Mv 92,164,156
buffer_load_dwordx4 v2, s[20:23], s55 offen offset:3072 sc1 lds
Mv 96,168,128
)" R"(
Cv 64,64,65
Cv 65,66,67
Mv 100,172,128
Cv 66,68,69
Cv 67,70,71
Mv 104,168,132
s_add_u32 m0, s36, 36864
Cv 72,72,73
Mv 108,172,132
buffer_load_dwordx4 v3, s[20:23], s55 offen offset:0 sc1 lds
Mv 112,168,136
Cv 73,74,75
Cv 74,76,77
Mv 116,172,136
Cv 75,78,79
Cv 80,80,81
Mv 120,168,140
Cv 81,82,83
Cv 82,84,85
Mv 124,172,140
buffer_store_dwordx4 v[32:35], v2, s[20:23], s45 offen offset:0 sc0 sc1
Mv 128,168,144
Cv 83,86,87
Cv 88,88,89
Mv 132,172,144
Cv 89,90,91
Cv 90,92,93
Mv 136,168,148
Cv 91,94,95
Mv 140,172,148
buffer_load_dwordx4 v3, s[20:23], s55 offen offset:1024 sc1 lds
Mv 144,168,152
Mv 148,172,152
Cv 128,128,129
Cv 129,130,131
Mv 152,168,156
Cv 130,132,133
Cv 131,134,135
Mv 156,172,156
buffer_store_dwordx4 v[40:43], v2, s[20:23], s45 offen offset:1024 sc0 sc1
Mv 160,48,0
Cv 136,136,137
Cv 137,138,139
Mv 160,176,128
Cv 138,140,141
Cv 139,142,143
Mv 164,52,0
Cv 144,144,145
Cv 145,146,147
Mv 164,180,128
buffer_load_dwordx4 v3, s[20:23], s55 offen offset:2048 sc1 lds
Mv 168,48,4
Cv 146,148,149
Cv 147,150,151
Mv 168,176,132
Cv 152,152,153
Cv 153,154,155
Mv 172,52,4
Cv 154,156,157
Cv 155,158,159
Mv 172,180,132
buffer_store_dwordx4 v[48:51], v2, s[20:23], s45 offen offset:2048 sc0 sc1
Mv 176,48,8
Cv 132,96,97
)" R"(
Cv 133,98,99
Mv 176,176,136
Cv 134,100,101
Cv 135,102,103
Mv 180,52,8
Cv 140,104,105
Cv 141,106,107
Mv 180,180,136
buffer_load_dwordx4 v3, s[20:23], s55 offen offset:3072 sc1 lds
Mv 184,48,12
Cv 142,108,109
Cv 143,110,111
Mv 184,176,140
Cv 148,112,113
Cv 149,114,115
Mv 188,52,12
Cv 150,116,117
Cv 151,118,119
Mv 188,180,140
buffer_store_dwordx4 v[56:59], v2, s[20:23], s45 offen offset:3072 sc0 sc1
Mv 192,48,16
Cv 156,120,121
Cv 157,122,123
Mv 192,176,144
Cv 158,124,125
Cv 159,126,127
Mv 196,52,16
s_add_u32 m0, s36, 98304
Cv 160,160,161
Mv 196,180,144
buffer_load_dwordx4 v2, s[20:23], s56 offen offset:0 sc1 lds
Mv 200,48,20
Cv 161,162,163
Cv 162,164,165
Mv 200,176,148
Cv 163,166,167
v_accvgpr_write_b32 a224, v160
Mv 204,52,20
v_accvgpr_write_b32 a225, v161
v_accvgpr_write_b32 a226, v162
Mv 204,180,148
buffer_store_dwordx4 v[64:67], v3, s[20:23], s45 offen offset:0 sc0 sc1
Mv 208,48,24
v_accvgpr_write_b32 a227, v163
Cv 168,168,169
Mv 208,176,152
Cv 169,170,171
Cv 170,172,173
Mv 212,52,24
Cv 171,174,175
v_accvgpr_write_b32 a228, v168
Mv 212,180,152
buffer_load_dwordx4 v2, s[20:23], s56 offen offset:1024 sc1 lds
Mv 216,48,28
v_accvgpr_write_b32 a229, v169
v_accvgpr_write_b32 a230, v170
Mv 216,176,156
v_accvgpr_write_b32 a231, v171
Cv 176,176,177
Mv 220,52,28
Cv 177,178,179
Cv 178,180,181
Mv 220,180,156
buffer_store_dwordx4 v[72:75], v3, s[20:23], s45 offen offset:1024 sc0 sc1
)" R"(
Mv 224,56,0
Cv 179,182,183
v_accvgpr_write_b32 a232, v176
Mv 228,60,0
v_accvgpr_write_b32 a233, v177
v_accvgpr_write_b32 a234, v178
Mv 236,60,4
v_accvgpr_write_b32 a235, v179
Cv 184,184,185
Mv 232,56,4
buffer_load_dwordx4 v2, s[20:23], s56 offen offset:2048 sc1 lds
Mv 240,56,8
Cv 185,186,187
Cv 186,188,189
Mv 244,60,8
Cv 187,190,191
v_accvgpr_write_b32 a236, v184
Mv 252,60,12
v_accvgpr_write_b32 a237, v185
v_accvgpr_write_b32 a238, v186
Mv 248,56,12
s_waitcnt vmcnt(17)
s_barrier
Ma 192,56,16
buffer_store_dwordx4 v[80:83], v3, s[20:23], s45 offen offset:2048 sc0 sc1
Ma 196,60,16
v_accvgpr_write_b32 a239, v187
Ma 204,60,20
Ma 200,56,20
buffer_load_dwordx4 v2, s[20:23], s56 offen offset:3072 sc1 lds
Ma 208,56,24
Ma 212,60,24
Ma 220,60,28
Ma 216,56,28
buffer_store_dwordx4 v[88:91], v3, s[20:23], s45 offen offset:3072 sc0 sc1
Mv 224,184,128
Rb 0,1,0
Mv 228,188,128
Rb 4,1,1024
Mv 232,184,132
s_add_u32 m0, s36, 102400
Rb 8,1,2048
Mv 236,188,132
buffer_load_dwordx4 v3, s[20:23], s56 offen offset:0 sc1 lds
Rb 12,1,3072
Mv 240,184,136
Rb 16,1,8192
Mv 244,188,136
Rb 20,1,9216
Mv 248,184,140
Rb 24,1,10240
Mv 252,188,140
Rb 28,1,11264
Ma 192,184,144
Rb 32,1,4096
Ma 196,188,144
Rb 36,1,5120
Ma 200,184,148
Rb 40,1,6144
Ma 204,188,148
buffer_load_dwordx4 v3, s[20:23], s56 offen offset:1024 sc1 lds
Rb 44,1,7168
Ma 208,184,152
Ma 212,188,152
buffer_load_dwordx4 v3, s[20:23], s56 offen offset:2048 sc1 lds
)" R"(
Ma 216,184,156
Ma 220,188,156
buffer_load_dwordx4 v3, s[20:23], s56 offen offset:3072 sc1 lds
s_add_u32 s56, s56, 0x10000
.LzFB:
s_waitcnt lgkmcnt(8)
Mz 64,0,64
Rb 48,1,12288
buffer_load_dword v4, v5, s[20:23], s50 offen offset:0 sc0 sc1
Mz 68,0,68
s_lshl_b64 s[60:61], s[60:61], 8
s_or_b32 s60, s60, 0xff
Mv 68,4,76
Rb 52,1,13312
s_add_u32 m0, s36, 65536
Mv 64,4,72
buffer_load_dwordx4 v2, s[20:23], s56 offen offset:0 sc1 lds
Mv 64,8,80
Rb 56,1,14336
s_add_u32 s55, s55, 0x10000
Cv 192,192,193
Mv 68,8,84
buffer_store_dwordx4 v[132:135], v2, s[20:23], s46 offen offset:0 sc0 sc1
s_cmp_eq_u32 s57, 2
s_cselect_b32 s55, s59, s55
Mv 68,12,92
Rb 60,1,15360
s_sub_u32 s57, s57, 1
s_waitcnt lgkmcnt(8)
Mv 64,12,88
Cv 193,194,195
Cv 194,196,197
Cv 195,198,199
v_accvgpr_write_b32 a240, v192
Mv 64,16,96
Rb 0,1,16384
v_accvgpr_write_b32 a241, v193
v_accvgpr_write_b32 a242, v194
Mv 68,16,100
v_accvgpr_write_b32 a243, v195
Cv 200,200,201
Cv 201,202,203
Cv 202,204,205
Mv 68,20,108
Rb 4,1,17408
Cv 203,206,207
v_accvgpr_write_b32 a244, v200
Mv 64,20,104
buffer_load_dwordx4 v2, s[20:23], s56 offen offset:1024 sc1 lds
v_accvgpr_write_b32 a245, v201
v_accvgpr_write_b32 a246, v202
Mv 64,24,112
Rb 8,1,18432
v_accvgpr_write_b32 a247, v203
Cv 208,208,209
Cv 209,210,211
Mv 68,24,116
Cv 210,212,213
Cv 211,214,215
v_accvgpr_write_b32 a248, v208
Mv 68,28,124
Rb 12,1,19456
v_accvgpr_write_b32 a249, v209
v_accvgpr_write_b32 a250, v210
)" R"(
s_waitcnt lgkmcnt(8)
Mv 64,28,120
v_accvgpr_write_b32 a251, v211
Cv 216,216,217
Cv 217,218,219
Mz 72,32,64
Rb 16,1,24576
Cv 218,220,221
Cv 219,222,223
v_accvgpr_write_b32 a252, v216
Mz 76,32,68
v_accvgpr_write_b32 a253, v217
v_accvgpr_write_b32 a254, v218
v_accvgpr_write_b32 a255, v219
Mv 76,36,76
Rb 20,1,25600
Cv 224,224,225
Cv 225,226,227
Cv 226,228,229
Mv 72,36,72
buffer_load_dwordx4 v2, s[20:23], s56 offen offset:2048 sc1 lds
Cv 227,230,231
v_accvgpr_write_b32 a128, v224
Mv 72,40,80
Rb 24,1,26624
v_accvgpr_write_b32 a129, v225
v_accvgpr_write_b32 a130, v226
Mv 76,40,84
buffer_store_dwordx4 v[140:143], v2, s[20:23], s46 offen offset:1024 sc0 sc1
v_accvgpr_write_b32 a131, v227
Cv 232,232,233
Mv 76,44,92
Rb 28,1,27648
Cv 233,234,235
s_waitcnt lgkmcnt(8)
Mv 72,44,88
Cv 234,236,237
Cv 235,238,239
v_accvgpr_write_b32 a132, v232
v_accvgpr_write_b32 a133, v233
Mv 72,48,96
Rb 32,1,20480
v_accvgpr_write_b32 a134, v234
v_accvgpr_write_b32 a135, v235
Mv 76,48,100
Cv 240,240,241
Cv 241,242,243
Cv 242,244,245
Cv 243,246,247
Mv 76,52,108
Rb 36,1,21504
v_accvgpr_write_b32 a136, v240
v_accvgpr_write_b32 a137, v241
Mv 72,52,104
buffer_load_dwordx4 v2, s[20:23], s56 offen offset:3072 sc1 lds
v_accvgpr_write_b32 a138, v242
v_accvgpr_write_b32 a139, v243
Mv 72,56,112
Rb 40,1,22528
Cv 248,248,249
Cv 249,250,251
Cv 250,252,253
Mv 76,56,116
Cv 251,254,255
)" R"(
v_accvgpr_write_b32 a140, v248
v_accvgpr_write_b32 a141, v249
Mv 76,60,124
Rb 44,1,23552
v_accvgpr_write_b32 a142, v250
v_accvgpr_write_b32 a143, v251
s_waitcnt lgkmcnt(8)
Mv 72,60,120
v_accvgpr_read_b32 v160, a192
v_accvgpr_read_b32 v28, a193
Cv 160,160,28
Mz 80,0,64
Rb 48,1,28672
v_accvgpr_read_b32 v161, a194
v_accvgpr_read_b32 v29, a195
Cv 161,161,29
Mz 84,0,68
v_accvgpr_read_b32 v162, a196
v_accvgpr_read_b32 v28, a197
Cv 162,162,28
Mv 84,4,76
Rb 52,1,29696
s_add_u32 m0, s36, 69632
v_accvgpr_read_b32 v163, a198
v_accvgpr_read_b32 v29, a199
Mv 80,4,72
buffer_load_dwordx4 v3, s[20:23], s56 offen offset:0 sc1 lds
Cv 163,163,29
v_accvgpr_write_b32 a144, v160
Mv 80,8,80
Rb 56,1,30720
v_accvgpr_write_b32 a145, v161
v_accvgpr_write_b32 a146, v162
Mv 84,8,84
buffer_store_dwordx4 v[148:151], v2, s[20:23], s46 offen offset:2048 sc0 sc1
v_accvgpr_write_b32 a147, v163
v_accvgpr_read_b32 v168, a200
Mv 84,12,92
Rb 60,1,31744
v_accvgpr_read_b32 v28, a201
s_waitcnt lgkmcnt(8)
Mv 80,12,88
Cv 168,168,28
v_accvgpr_read_b32 v169, a202
v_accvgpr_read_b32 v29, a203
Cv 169,169,29
Mv 80,16,96
v_accvgpr_read_b32 v170, a204
v_accvgpr_read_b32 v28, a205
Cv 170,170,28
Mv 84,16,100
v_accvgpr_read_b32 v171, a206
v_accvgpr_read_b32 v29, a207
Cv 171,171,29
v_accvgpr_write_b32 a148, v168
Mv 84,20,108
v_accvgpr_write_b32 a149, v169
v_accvgpr_write_b32 a150, v170
v_accvgpr_write_b32 a151, v171
Mv 80,20,104
buffer_load_dwordx4 v3, s[20:23], s56 offen offset:1024 sc1 lds
v_accvgpr_read_b32 v176, a208
v_accvgpr_read_b32 v28, a209
Mv 80,24,112
)" R"(
Cv 176,176,28
v_accvgpr_read_b32 v177, a210
v_accvgpr_read_b32 v29, a211
Cv 177,177,29
Mv 84,24,116
v_accvgpr_read_b32 v178, a212
v_accvgpr_read_b32 v28, a213
Cv 178,178,28
Mv 84,28,124
v_accvgpr_read_b32 v179, a214
v_accvgpr_read_b32 v29, a215
Cv 179,179,29
v_accvgpr_write_b32 a152, v176
Mv 80,28,120
s_waitcnt vmcnt(9) lgkmcnt(0)
s_barrier
buffer_store_dword v6, v7, s[20:23], s49 offen offset:0 sc0 sc1
Mz 88,32,64
Rb 0,1,32768
v_accvgpr_write_b32 a153, v177
v_accvgpr_write_b32 a154, v178
v_accvgpr_write_b32 a155, v179
Mz 92,32,68
Rb 4,1,33792
v_accvgpr_read_b32 v184, a216
v_accvgpr_read_b32 v28, a217
Mv 92,36,76
Rb 8,1,34816
Cv 184,184,28
v_accvgpr_read_b32 v185, a218
v_accvgpr_read_b32 v29, a219
Mv 88,36,72
Rb 12,1,35840
buffer_load_dwordx4 v3, s[20:23], s56 offen offset:2048 sc1 lds
Cv 185,185,29
Mv 88,40,80
Rb 16,1,40960
v_accvgpr_read_b32 v186, a220
v_accvgpr_read_b32 v28, a221
Mv 92,40,84
Rb 20,1,41984
buffer_store_dwordx4 v[156:159], v2, s[20:23], s46 offen offset:3072 sc0 sc1
Cv 186,186,28
Mv 92,44,92
Rb 24,1,43008
v_accvgpr_read_b32 v187, a222
v_accvgpr_read_b32 v29, a223
Mv 88,44,88
Rb 28,1,44032
Cv 187,187,29
v_accvgpr_write_b32 a156, v184
v_accvgpr_write_b32 a157, v185
Mv 88,48,96
Rb 32,1,36864
v_accvgpr_write_b32 a158, v186
v_accvgpr_write_b32 a159, v187
Mv 92,48,100
Rb 36,1,37888
v_mov_b32 v16, 0
v_mov_b32 v17, 0
v_mov_b32 v18, 0
Mv 92,52,108
Rb 40,1,38912
v_mov_b32 v19, 0
v_mov_b32 v20, 0
)" R"(
Mv 88,52,104
Rb 44,1,39936
buffer_load_dwordx4 v3, s[20:23], s56 offen offset:3072 sc1 lds
v_mov_b32 v21, 0
Mv 88,56,112
v_mov_b32 v22, 0
v_cmp_eq_u32_e32 vcc, s3, v4
s_andn2_b64 s[28:29], s[60:61], vcc
s_cbranch_scc1 .Lzsp13
.Lzok13:
Mv 92,56,116
s_add_u32 s56, s56, 0x10000
v_mov_b32 v23, 0
Mv 92,60,124
s_waitcnt lgkmcnt(8)
Mv 88,60,120
v_exp_f32_e32 v32, v64
Mz 96,0,64
Rb 48,1,45056
buffer_load_dword v4, v5, s[20:23], s50 offen offset:0 sc0 sc1
Mz 100,0,68
s_lshl_b64 s[60:61], s[60:61], 8
s_or_b32 s60, s60, 0xff
Mv 100,4,76
Rb 52,1,46080
s_add_u32 m0, s36, 0
Mv 96,4,72
buffer_load_dwordx4 v2, s[20:23], s55 offen offset:0 sc1 lds
Mv 96,8,80
Rb 56,1,47104
v_exp_f32_e32 v24, v65
Mv 100,8,84
buffer_store_dwordx4 v[128:131], v3, s[20:23], s46 offen offset:0 sc0 sc1
v_add_f32_e32 v14, v32, v24
Mv 100,12,92
Rb 60,1,48128
Cv 32,32,24
s_waitcnt lgkmcnt(8)
Mv 96,12,88
v_exp_f32_e32 v33, v66
Mv 96,16,96
Rb 0,1,49152
v_exp_f32_e32 v25, v67
Mv 100,16,100
v_exp_f32_e32 v34, v72
v_exp_f32_e32 v26, v73
Mv 100,20,108
Rb 4,1,50176
v_exp_f32_e32 v35, v74
Mv 96,20,104
buffer_load_dwordx4 v2, s[20:23], s55 offen offset:1024 sc1 lds
v_add_f32_e32 v14, v14, v33
Mv 96,24,112
Rb 8,1,51200
v_exp_f32_e32 v27, v75
Mv 100,24,116
v_exp_f32_e32 v36, v68
v_add_f32_e32 v14, v14, v25
Mv 100,28,124
Rb 12,1,52224
v_exp_f32_e32 v28, v69
s_waitcnt lgkmcnt(8)
Mv 96,28,120
v_exp_f32_e32 v37, v70
)" R"(
Cv 33,33,25
Mz 104,32,64
Rb 16,1,57344
v_exp_f32_e32 v29, v71
Mz 108,32,68
v_exp_f32_e32 v38, v76
v_add_f32_e32 v14, v14, v34
Mv 108,36,76
Rb 20,1,58368
v_exp_f32_e32 v24, v77
Mv 104,36,72
buffer_load_dwordx4 v2, s[20:23], s55 offen offset:2048 sc1 lds
v_add_f32_e32 v14, v14, v26
Mv 104,40,80
Rb 24,1,59392
v_exp_f32_e32 v39, v78
Cv 34,34,26
Mv 108,40,84
buffer_store_dwordx4 v[136:139], v3, s[20:23], s46 offen offset:1024 sc0 sc1
v_add_f32_e32 v14, v14, v35
Mv 108,44,92
Rb 28,1,60416
v_add_f32_e32 v14, v14, v27
s_waitcnt lgkmcnt(8)
Mv 104,44,88
v_exp_f32_e32 v25, v79
Cv 35,35,27
Mv 104,48,96
Rb 32,1,53248
v_exp_f32_e32 v40, v80
Mv 108,48,100
v_exp_f32_e32 v26, v81
v_exp_f32_e32 v41, v82
Mv 108,52,108
Rb 36,1,54272
v_exp_f32_e32 v27, v83
Mv 104,52,104
buffer_load_dwordx4 v2, s[20:23], s55 offen offset:3072 sc1 lds
v_add_f32_e32 v15, v36, v28
Mv 104,56,112
Rb 40,1,55296
Cv 36,36,28
v_add_f32_e32 v15, v15, v37
Mv 108,56,116
v_exp_f32_e32 v42, v88
v_add_f32_e32 v15, v15, v29
Mv 108,60,124
Rb 44,1,56320
v_exp_f32_e32 v28, v89
s_waitcnt lgkmcnt(8)
Mv 104,60,120
Cv 37,37,29
v_exp_f32_e32 v43, v90
Mz 112,0,64
Rb 48,1,61440
v_add_f32_e32 v15, v15, v38
v_add_f32_e32 v15, v15, v24
Mz 116,0,68
v_exp_f32_e32 v29, v91
Cv 38,38,24
Mv 116,4,76
Rb 52,1,62464
s_add_u32 m0, s36, 4096
v_add_f32_e32 v15, v15, v39
)" R"(
Mv 112,4,72
buffer_load_dwordx4 v3, s[20:23], s55 offen offset:0 sc1 lds
v_add_f32_e32 v15, v15, v25
Mv 112,8,80
Rb 56,1,63488
v_exp_f32_e32 v44, v84
Cv 39,39,25
Mv 116,8,84
buffer_store_dwordx4 v[144:147], v3, s[20:23], s46 offen offset:2048 sc0 sc1
v_add_f32_e32 v14, v14, v40
Mv 116,12,92
Rb 60,1,64512
v_add_f32_e32 v14, v14, v26
s_waitcnt lgkmcnt(8)
Mv 112,12,88
v_exp_f32_e32 v24, v85
Cv 40,40,26
Mv 112,16,96
v_exp_f32_e32 v45, v86
v_add_f32_e32 v14, v14, v41
Mv 116,16,100
v_exp_f32_e32 v25, v87
v_exp_f32_e32 v46, v92
Mv 116,20,108
v_exp_f32_e32 v26, v93
v_add_f32_e32 v14, v14, v27
Mv 112,20,104
buffer_load_dwordx4 v3, s[20:23], s55 offen offset:1024 sc1 lds
Cv 41,41,27
Mv 112,24,112
v_exp_f32_e32 v47, v94
v_add_f32_e32 v14, v14, v42
Mv 116,24,116
v_exp_f32_e32 v27, v95
v_add_f32_e32 v14, v14, v28
Mv 116,28,124
Cv 42,42,28
v_add_f32_e32 v14, v14, v43
v_add_f32_e32 v14, v14, v29
Cv 43,43,29
Mv 112,28,120
v_add_f32_e32 v15, v15, v44
s_waitcnt lgkmcnt(0)
s_barrier
Mz 120,32,64
Rb 0,8,32768
v_add_f32_e32 v15, v15, v24
Cv 44,44,24
Mz 124,32,68
Rb 4,8,33792
v_add_f32_e32 v15, v15, v45
v_add_f32_e32 v15, v15, v25
Mv 124,36,76
Rb 8,8,34816
Cv 45,45,25
v_add_f32_e32 v15, v15, v46
Mv 120,36,72
Rb 12,8,35840
buffer_load_dwordx4 v3, s[20:23], s55 offen offset:2048 sc1 lds
Mv 120,40,80
Rb 16,8,36864
v_add_f32_e32 v15, v15, v26
Cv 46,46,26
v_add_f32_e32 v15, v15, v47
Mv 124,40,84
)" R"(
Rb 20,8,37888
buffer_store_dwordx4 v[152:155], v3, s[20:23], s46 offen offset:3072 sc0 sc1
Mv 124,44,92
Rb 24,8,38912
v_add_f32_e32 v15, v15, v27
Cv 47,47,27
Mv 120,44,88
Rb 28,8,39936
v_max_f32_e32 v11, v14, v15
v_mov_b32 v24, v14
Mv 120,48,96
Rb 32,8,40960
v_mov_b32 v25, v15
Mv 124,48,100
Rb 36,8,41984
v_permlane32_swap_b32_e32 v24, v25
v_add_f32_e32 v24, v24, v25
v_mov_b32 v25, v24
Mv 124,52,108
Rb 40,8,43008
v_permlane16_swap_b32_e32 v24, v25
v_add_f32_e32 v24, v24, v25
Mv 120,52,104
Rb 44,8,44032
buffer_load_dwordx4 v3, s[20:23], s55 offen offset:3072 sc1 lds
Mv 120,56,112
v_rcp_f32_e32 v24, v24
s_add_u32 s55, s55, 0x10000
Mv 124,56,116
s_cmp_eq_u32 s57, 2
s_cselect_b32 s55, s59, s55
v_max_f32_e32 v11, v11, v24
Mv 124,60,124
v_cmp_nge_f32_e32 vcc, s62, v11
s_waitcnt lgkmcnt(8)
s_cbranch_vccnz .Lzfx6
.Lzfr6:
Mv 120,60,120
v_mov_b32 v12, v14
v_mov_b32 v13, v15
v_mfma_f32_16x16x32_bf16 v[128:131], a[0:3], v[32:35], 0
Rb 48,8,45056
v_mfma_f32_16x16x32_bf16 v[136:139], a[0:3], v[36:39], 0
v_mfma_f32_16x16x32_bf16 v[140:143], a[4:7], v[36:39], 0
Rb 52,8,46080
s_add_u32 m0, s36, 32768
v_mfma_f32_16x16x32_bf16 v[132:135], a[4:7], v[32:35], 0
buffer_load_dwordx4 v2, s[20:23], s55 offen offset:0 sc1 lds
v_mfma_f32_16x16x32_bf16 v[144:147], a[8:11], v[32:35], 0
Rb 56,8,47104
v_mfma_f32_16x16x32_bf16 v[152:155], a[8:11], v[36:39], 0
v_exp_f32_e32 v48, v96
v_mfma_f32_16x16x32_bf16 v[156:159], a[12:15], v[36:39], 0
Rb 60,8,48128
s_waitcnt lgkmcnt(8)
v_mfma_f32_16x16x32_bf16 v[148:151], a[12:15], v[32:35], 0
v_exp_f32_e32 v28, v97
v_mfma_f32_16x16x32_bf16 v[160:163], a[16:19], v[32:35], 0
Rb 0,8,49152
v_add_f32_e32 v14, v48, v28
v_mfma_f32_16x16x32_bf16 v[168:171], a[16:19], v[36:39], 0
v_exp_f32_e32 v49, v98
v_mfma_f32_16x16x32_bf16 v[172:175], a[20:23], v[36:39], 0
Rb 4,8,50176
)" R"(
Cv 48,48,28
v_mfma_f32_16x16x32_bf16 v[164:167], a[20:23], v[32:35], 0
buffer_load_dwordx4 v2, s[20:23], s55 offen offset:1024 sc1 lds
v_mfma_f32_16x16x32_bf16 v[176:179], a[24:27], v[32:35], 0
Rb 8,8,51200
v_add_f32_e32 v14, v14, v49
v_mfma_f32_16x16x32_bf16 v[184:187], a[24:27], v[36:39], 0
v_exp_f32_e32 v29, v99
v_mfma_f32_16x16x32_bf16 v[188:191], a[28:31], v[36:39], 0
Rb 12,8,52224
s_waitcnt lgkmcnt(8)
v_mfma_f32_16x16x32_bf16 v[180:183], a[28:31], v[32:35], 0
v_exp_f32_e32 v50, v104
v_mfma_f32_16x16x32_bf16 v[192:195], a[32:35], v[32:35], 0
Rb 16,8,53248
v_add_f32_e32 v14, v14, v29
v_mfma_f32_16x16x32_bf16 v[200:203], a[32:35], v[36:39], 0
v_exp_f32_e32 v24, v105
v_mfma_f32_16x16x32_bf16 v[204:207], a[36:39], v[36:39], 0
Rb 20,8,54272
Cv 49,49,29
v_mfma_f32_16x16x32_bf16 v[196:199], a[36:39], v[32:35], 0
buffer_load_dwordx4 v2, s[20:23], s55 offen offset:2048 sc1 lds
v_mfma_f32_16x16x32_bf16 v[208:211], a[40:43], v[32:35], 0
Rb 24,8,55296
v_add_f32_e32 v14, v14, v50
v_mfma_f32_16x16x32_bf16 v[216:219], a[40:43], v[36:39], 0
v_exp_f32_e32 v51, v106
v_mfma_f32_16x16x32_bf16 v[220:223], a[44:47], v[36:39], 0
Rb 28,8,56320
s_waitcnt lgkmcnt(8)
v_mfma_f32_16x16x32_bf16 v[212:215], a[44:47], v[32:35], 0
v_exp_f32_e32 v25, v107
v_mfma_f32_16x16x32_bf16 v[224:227], a[48:51], v[32:35], 0
Rb 32,8,57344
v_add_f32_e32 v14, v14, v24
v_mfma_f32_16x16x32_bf16 v[232:235], a[48:51], v[36:39], 0
v_exp_f32_e32 v52, v100
v_mfma_f32_16x16x32_bf16 v[236:239], a[52:55], v[36:39], 0
Rb 36,8,58368
Cv 50,50,24
v_mfma_f32_16x16x32_bf16 v[228:231], a[52:55], v[32:35], 0
buffer_load_dwordx4 v2, s[20:23], s55 offen offset:3072 sc1 lds
v_mfma_f32_16x16x32_bf16 v[240:243], a[56:59], v[32:35], 0
Rb 40,8,59392
v_add_f32_e32 v14, v14, v51
v_mfma_f32_16x16x32_bf16 v[248:251], a[56:59], v[36:39], 0
v_exp_f32_e32 v26, v101
v_mfma_f32_16x16x32_bf16 v[252:255], a[60:63], v[36:39], 0
Rb 44,8,60416
s_waitcnt lgkmcnt(8)
v_mfma_f32_16x16x32_bf16 v[244:247], a[60:63], v[32:35], 0
v_exp_f32_e32 v53, v102
Mp 128,0,40
Rb 48,8,61440
v_add_f32_e32 v14, v14, v25
Mp 136,0,44
v_exp_f32_e32 v27, v103
Mp 140,4,44
Rb 52,8,62464
s_add_u32 m0, s36, 36864
Mp 132,4,40
buffer_load_dwordx4 v3, s[20:23], s55 offen offset:0 sc1 lds
Mp 144,8,40
)" R"(
Rb 56,8,63488
Cv 51,51,25
Mp 152,8,44
v_exp_f32_e32 v54, v108
Mp 156,12,44
Rb 60,8,64512
s_waitcnt lgkmcnt(8)
Mp 148,12,40
v_exp_f32_e32 v28, v109
Mp 160,16,40
v_exp_f32_e32 v55, v110
Mp 168,16,44
v_exp_f32_e32 v29, v111
Mp 172,20,44
v_add_f32_e32 v15, v52, v26
Cv 52,52,26
Mp 164,20,40
buffer_load_dwordx4 v3, s[20:23], s55 offen offset:1024 sc1 lds
Mp 176,24,40
v_add_f32_e32 v15, v15, v53
v_add_f32_e32 v15, v15, v27
Mp 184,24,44
Cv 53,53,27
v_add_f32_e32 v15, v15, v54
Mp 188,28,44
v_add_f32_e32 v15, v15, v28
Cv 54,54,28
Mp 180,28,40
s_waitcnt vmcnt(6) lgkmcnt(0)
s_barrier
buffer_store_dword v6, v7, s[20:23], s49 offen offset:4 sc0 sc1
Mp 192,32,40
Rb 0,1,0
v_add_f32_e32 v15, v15, v55
Mp 200,32,44
Rb 4,1,1024
v_add_f32_e32 v15, v15, v29
Mp 204,36,44
Rb 8,1,2048
Cv 55,55,29
Mp 196,36,40
Rb 12,1,3072
buffer_load_dwordx4 v3, s[20:23], s55 offen offset:2048 sc1 lds
Mp 208,40,40
Rb 16,1,8192
s_sub_u32 s57, s57, 1
Mp 216,40,44
Rb 20,1,9216
Mp 220,44,44
Rb 24,1,10240
Mp 212,44,40
Rb 28,1,11264
Mp 224,48,40
Rb 32,1,4096
Mp 232,48,44
Rb 36,1,5120
Mp 236,52,44
Rb 40,1,6144
Mp 228,52,40
Rb 44,1,7168
buffer_load_dwordx4 v3, s[20:23], s55 offen offset:3072 sc1 lds
Mp 240,56,40
v_cmp_eq_u32_e32 vcc, s3, v4
s_andn2_b64 s[28:29], s[60:61], vcc
s_cbranch_scc1 .Lzsp14
)" R"(
.Lzok14:
Mp 248,56,44
Mp 252,60,44
s_waitcnt lgkmcnt(8)
Mp 244,60,40
v_mfma_f32_16x16x32_bf16 v[64:67], a[0:3], a[64:67], v[16:19]
Rb 48,1,12288
v_mfma_f32_16x16x32_bf16 v[68:71], a[0:3], a[68:71], v[20:23]
v_exp_f32_e32 v56, v112
Mv 68,4,76
Rb 52,1,13312
s_add_u32 m0, s36, 98304
Mv 64,4,72
buffer_load_dwordx4 v2, s[20:23], s56 offen offset:0 sc1 lds
Mv 64,8,80
Rb 56,1,14336
v_add_f32_e32 v14, v14, v56
Mv 68,8,84
buffer_store_dwordx4 a[224:227], v2, s[20:23], s47 offen offset:0 sc0 sc1
Mv 68,12,92
Rb 60,1,15360
s_waitcnt lgkmcnt(8)
Mv 64,12,88
v_exp_f32_e32 v24, v113
Mv 64,16,96
Rb 0,1,16384
v_add_f32_e32 v14, v14, v24
Mv 68,16,100
v_exp_f32_e32 v57, v114
Mv 68,20,108
Rb 4,1,17408
Cv 56,56,24
Mv 64,20,104
buffer_load_dwordx4 v2, s[20:23], s56 offen offset:1024 sc1 lds
Mv 64,24,112
Rb 8,1,18432
v_add_f32_e32 v14, v14, v57
Mv 68,24,116
v_exp_f32_e32 v25, v115
Mv 68,28,124
Rb 12,1,19456
s_waitcnt lgkmcnt(8)
Mv 64,28,120
v_exp_f32_e32 v58, v120
v_mfma_f32_16x16x32_bf16 v[72:75], a[32:35], a[64:67], v[16:19]
Rb 16,1,24576
v_add_f32_e32 v14, v14, v25
v_mfma_f32_16x16x32_bf16 v[76:79], a[32:35], a[68:71], v[20:23]
v_exp_f32_e32 v26, v121
Mv 76,36,76
Rb 20,1,25600
Cv 57,57,25
Mv 72,36,72
buffer_load_dwordx4 v2, s[20:23], s56 offen offset:2048 sc1 lds
Mv 72,40,80
Rb 24,1,26624
v_add_f32_e32 v14, v14, v58
Mv 76,40,84
v_exp_f32_e32 v59, v122
Mv 76,44,92
Rb 28,1,27648
s_waitcnt lgkmcnt(8)
Mv 72,44,88
v_exp_f32_e32 v27, v123
)" R"(
Mv 72,48,96
Rb 32,1,20480
v_add_f32_e32 v14, v14, v26
Mv 76,48,100
v_exp_f32_e32 v60, v116
Mv 76,52,108
Rb 36,1,21504
Cv 58,58,26
Mv 72,52,104
buffer_load_dwordx4 v2, s[20:23], s56 offen offset:3072 sc1 lds
Mv 72,56,112
Rb 40,1,22528
v_add_f32_e32 v14, v14, v59
Mv 76,56,116
buffer_store_dwordx4 a[228:231], v2, s[20:23], s47 offen offset:1024 sc0 sc1
Mv 76,60,124
Rb 44,1,23552
s_waitcnt lgkmcnt(8)
Mv 72,60,120
v_exp_f32_e32 v28, v117
v_mfma_f32_16x16x32_bf16 v[80:83], a[0:3], a[64:67], v[16:19]
Rb 48,1,28672
v_add_f32_e32 v14, v14, v27
v_mfma_f32_16x16x32_bf16 v[84:87], a[0:3], a[68:71], v[20:23]
v_exp_f32_e32 v61, v118
Mv 84,4,76
Rb 52,1,29696
s_add_u32 m0, s36, 102400
Mv 80,4,72
buffer_load_dwordx4 v3, s[20:23], s56 offen offset:0 sc1 lds
Mv 80,8,80
Rb 56,1,30720
Cv 59,59,27
Mv 84,8,84
v_exp_f32_e32 v29, v119
Mv 84,12,92
Rb 60,1,31744
s_waitcnt lgkmcnt(8)
Mv 80,12,88
v_exp_f32_e32 v62, v124
Mv 80,16,96
v_exp_f32_e32 v24, v125
Mv 84,16,100
v_exp_f32_e32 v63, v126
Mv 84,20,108
v_exp_f32_e32 v25, v127
Mv 80,20,104
buffer_load_dwordx4 v3, s[20:23], s56 offen offset:1024 sc1 lds
Mv 80,24,112
v_add_f32_e32 v15, v15, v60
v_add_f32_e32 v15, v15, v28
Mv 84,24,116
Cv 60,60,28
v_add_f32_e32 v15, v15, v61
Mv 84,28,124
v_add_f32_e32 v15, v15, v29
Cv 61,61,29
Mv 80,28,120
s_waitcnt lgkmcnt(0)
s_barrier
v_mfma_f32_16x16x32_bf16 v[88:91], a[32:35], a[64:67], v[16:19]
Rb 0,8,0
v_add_f32_e32 v15, v15, v62
v_mfma_f32_16x16x32_bf16 v[92:95], a[32:35], a[68:71], v[20:23]
Rb 4,8,1024
)" R"(
v_add_f32_e32 v15, v15, v24
Mv 92,36,76
Rb 8,8,2048
Cv 62,62,24
Mv 88,36,72
Rb 12,8,3072
buffer_load_dwordx4 v3, s[20:23], s56 offen offset:2048 sc1 lds
Mv 88,40,80
Rb 16,8,4096
v_add_f32_e32 v15, v15, v63
Mv 92,40,84
Rb 20,8,5120
buffer_store_dwordx4 a[232:235], v2, s[20:23], s47 offen offset:2048 sc0 sc1
Mv 92,44,92
Rb 24,8,6144
v_add_f32_e32 v15, v15, v25
Mv 88,44,88
Rb 28,8,7168
Cv 63,63,25
Mv 88,48,96
Rb 32,8,8192
v_max_f32_e32 v11, v14, v15
Mv 92,48,100
Rb 36,8,9216
v_cmp_nge_f32_e32 vcc, s62, v11
Mv 92,52,108
Rb 40,8,10240
s_add_u32 s55, s55, 0x10000
Mv 88,52,104
Rb 44,8,11264
buffer_load_dwordx4 v3, s[20:23], s56 offen offset:3072 sc1 lds
Mv 88,56,112
s_cmp_eq_u32 s57, 2
s_cselect_b32 s55, s59, s55
Mv 92,56,116
s_add_u32 s56, s56, 0x10000
Mv 92,60,124
s_waitcnt lgkmcnt(8)
s_cbranch_vccnz .Lzfx7
.Lzfr7:
Mv 88,60,120
v_add_f32_e32 v12, v12, v14
v_add_f32_e32 v13, v13, v15
Mp 128,0,48
Rb 48,8,12288
Mp 136,0,52
s_cmp_le_i32 s57, s58
s_cbranch_scc1 .Lzmk6
.Lzmr6:
Mp 140,4,52
Rb 52,8,13312
s_add_u32 m0, s36, 0
Mp 132,4,48
buffer_load_dwordx4 v2, s[20:23], s55 offen offset:0 sc1 lds
Mp 144,8,48
Rb 56,8,14336
Mp 152,8,52
v_exp_f32_e32 v32, v64
Mp 156,12,52
Rb 60,8,15360
s_waitcnt lgkmcnt(8)
Mp 148,12,48
v_exp_f32_e32 v26, v65
Mp 160,16,48
)" R"(
Rb 0,8,16384
v_add_f32_e32 v14, v32, v26
Mp 168,16,52
v_exp_f32_e32 v33, v66
Mp 172,20,52
Rb 4,8,17408
Cv 32,32,26
Mp 164,20,48
buffer_load_dwordx4 v2, s[20:23], s55 offen offset:1024 sc1 lds
Mp 176,24,48
Rb 8,8,18432
v_add_f32_e32 v14, v14, v33
Mp 184,24,52
buffer_store_dwordx4 a[236:239], v2, s[20:23], s47 offen offset:3072 sc0 sc1
Mp 188,28,52
Rb 12,8,19456
s_waitcnt lgkmcnt(8)
Mp 180,28,48
v_exp_f32_e32 v27, v67
Mp 192,32,48
Rb 16,8,20480
v_add_f32_e32 v14, v14, v27
Mp 200,32,52
v_exp_f32_e32 v34, v72
Mp 204,36,52
Rb 20,8,21504
Cv 33,33,27
Mp 196,36,48
buffer_load_dwordx4 v2, s[20:23], s55 offen offset:2048 sc1 lds
Mp 208,40,48
Rb 24,8,22528
v_add_f32_e32 v14, v14, v34
Mp 216,40,52
v_exp_f32_e32 v28, v73
Mp 220,44,52
Rb 28,8,23552
s_waitcnt lgkmcnt(8)
Mp 212,44,48
v_exp_f32_e32 v35, v74
Mp 224,48,48
Rb 32,8,24576
v_add_f32_e32 v14, v14, v28
Mp 232,48,52
v_exp_f32_e32 v29, v75
Mp 236,52,52
Rb 36,8,25600
Cv 34,34,28
Mp 228,52,48
buffer_load_dwordx4 v2, s[20:23], s55 offen offset:3072 sc1 lds
Mp 240,56,48
Rb 40,8,26624
v_add_f32_e32 v14, v14, v35
Mp 248,56,52
v_exp_f32_e32 v36, v68
Mp 252,60,52
Rb 44,8,27648
s_waitcnt lgkmcnt(8)
Mp 244,60,48
v_exp_f32_e32 v24, v69
Mp 128,0,56
Rb 48,8,28672
v_add_f32_e32 v14, v14, v29
Mp 136,0,60
v_exp_f32_e32 v37, v70
)" R"(
Mp 140,4,60
Rb 52,8,29696
s_add_u32 m0, s36, 4096
Mp 132,4,56
buffer_load_dwordx4 v3, s[20:23], s55 offen offset:0 sc1 lds
Mp 144,8,56
Rb 56,8,30720
Cv 35,35,29
Mp 152,8,60
buffer_store_dwordx4 a[240:243], v3, s[20:23], s47 offen offset:0 sc0 sc1
Mp 156,12,60
Rb 60,8,31744
s_waitcnt lgkmcnt(8)
Mp 148,12,56
v_exp_f32_e32 v25, v71
Mp 160,16,56
v_exp_f32_e32 v38, v76
Mp 168,16,60
v_exp_f32_e32 v26, v77
Mp 172,20,60
v_exp_f32_e32 v39, v78
Mp 164,20,56
buffer_load_dwordx4 v3, s[20:23], s55 offen offset:1024 sc1 lds
Mp 176,24,56
v_exp_f32_e32 v27, v79
Mp 184,24,60
v_add_f32_e32 v15, v36, v24
Cv 36,36,24
Mp 188,28,60
v_add_f32_e32 v15, v15, v37
v_add_f32_e32 v15, v15, v25
Mp 180,28,56
s_waitcnt vmcnt(19) lgkmcnt(0)
s_barrier
Mp 192,32,56
Rb 0,1,32768
Cv 37,37,25
Mp 200,32,60
Rb 4,1,33792
v_add_f32_e32 v15, v15, v38
Mp 204,36,60
Rb 8,1,34816
v_add_f32_e32 v15, v15, v26
Mp 196,36,56
Rb 12,1,35840
buffer_load_dwordx4 v3, s[20:23], s55 offen offset:2048 sc1 lds
Mp 208,40,56
Rb 16,1,40960
Cv 38,38,26
Mp 216,40,60
Rb 20,1,41984
v_add_f32_e32 v15, v15, v39
Mp 220,44,60
Rb 24,1,43008
v_add_f32_e32 v15, v15, v27
Mp 212,44,56
Rb 28,1,44032
Cv 39,39,27
Mp 224,48,56
Rb 32,1,36864
s_sub_u32 s57, s57, 1
Mp 232,48,60
Rb 36,1,37888
Mp 236,52,60
Rb 40,1,38912
)" R"(
Mp 228,52,56
Rb 44,1,39936
buffer_load_dwordx4 v3, s[20:23], s55 offen offset:3072 sc1 lds
Mp 240,56,56
Mp 248,56,60
buffer_store_dwordx4 a[244:247], v3, s[20:23], s47 offen offset:1024 sc0 sc1
Mp 252,60,60
s_waitcnt lgkmcnt(8)
Mp 244,60,56
v_mfma_f32_16x16x32_bf16 v[96:99], a[0:3], a[64:67], v[16:19]
Rb 48,1,45056
buffer_load_dword v4, v5, s[20:23], s50 offen offset:0 sc0 sc1
v_mfma_f32_16x16x32_bf16 v[100:103], a[0:3], a[68:71], v[20:23]
s_lshl_b64 s[60:61], s[60:61], 8
s_or_b32 s60, s60, 0xff
Mv 100,4,76
Rb 52,1,46080
s_add_u32 m0, s36, 65536
Mv 96,4,72
buffer_load_dwordx4 v2, s[20:23], s56 offen offset:0 sc1 lds
Mv 96,8,80
Rb 56,1,47104
Mv 100,8,84
v_exp_f32_e32 v40, v80
Mv 100,12,92
Rb 60,1,48128
s_waitcnt lgkmcnt(8)
Mv 96,12,88
v_exp_f32_e32 v28, v81
Mv 96,16,96
Rb 0,1,49152
v_add_f32_e32 v14, v14, v40
Mv 100,16,100
v_exp_f32_e32 v41, v82
Mv 100,20,108
Rb 4,1,50176
v_add_f32_e32 v14, v14, v28
Mv 96,20,104
buffer_load_dwordx4 v2, s[20:23], s56 offen offset:1024 sc1 lds
Mv 96,24,112
Rb 8,1,51200
Cv 40,40,28
Mv 100,24,116
v_exp_f32_e32 v29, v83
Mv 100,28,124
Rb 12,1,52224
s_waitcnt lgkmcnt(8)
Mv 96,28,120
v_exp_f32_e32 v42, v88
v_add_f32_e32 v14, v14, v41
v_mfma_f32_16x16x32_bf16 v[104:107], a[32:35], a[64:67], v[16:19]
Rb 16,1,57344
v_add_f32_e32 v14, v14, v29
v_mfma_f32_16x16x32_bf16 v[108:111], a[32:35], a[68:71], v[20:23]
v_exp_f32_e32 v24, v89
Mv 108,36,76
Rb 20,1,58368
Cv 41,41,29
Mv 104,36,72
buffer_load_dwordx4 v2, s[20:23], s56 offen offset:2048 sc1 lds
Mv 104,40,80
Rb 24,1,59392
v_add_f32_e32 v14, v14, v42
Mv 108,40,84
)" R"(
buffer_store_dwordx4 a[248:251], v3, s[20:23], s47 offen offset:2048 sc0 sc1
Mv 108,44,92
Rb 28,1,60416
s_waitcnt lgkmcnt(8)
Mv 104,44,88
v_exp_f32_e32 v43, v90
Mv 104,48,96
Rb 32,1,53248
v_add_f32_e32 v14, v14, v24
Mv 108,48,100
v_exp_f32_e32 v25, v91
Mv 108,52,108
Rb 36,1,54272
Cv 42,42,24
Mv 104,52,104
buffer_load_dwordx4 v2, s[20:23], s56 offen offset:3072 sc1 lds
Mv 104,56,112
Rb 40,1,55296
v_add_f32_e32 v14, v14, v43
Mv 108,56,116
v_exp_f32_e32 v44, v84
Mv 108,60,124
Rb 44,1,56320
s_waitcnt lgkmcnt(8)
Mv 104,60,120
v_exp_f32_e32 v26, v85
v_add_f32_e32 v14, v14, v25
v_mfma_f32_16x16x32_bf16 v[112:115], a[0:3], a[64:67], v[16:19]
Rb 48,1,61440
Cv 43,43,25
v_mfma_f32_16x16x32_bf16 v[116:119], a[0:3], a[68:71], v[20:23]
v_exp_f32_e32 v45, v86
Mv 116,4,76
Rb 52,1,62464
s_add_u32 m0, s36, 69632
Mv 112,4,72
buffer_load_dwordx4 v3, s[20:23], s56 offen offset:0 sc1 lds
Mv 112,8,80
Rb 56,1,63488
v_add_f32_e32 v15, v15, v44
Mv 116,8,84
v_exp_f32_e32 v27, v87
Mv 116,12,92
Rb 60,1,64512
s_waitcnt lgkmcnt(8)
Mv 112,12,88
v_exp_f32_e32 v46, v92
Mv 112,16,96
v_exp_f32_e32 v28, v93
Mv 116,16,100
v_exp_f32_e32 v47, v94
Mv 116,20,108
v_exp_f32_e32 v29, v95
Mv 112,20,104
buffer_load_dwordx4 v3, s[20:23], s56 offen offset:1024 sc1 lds
Mv 112,24,112
v_add_f32_e32 v15, v15, v26
Cv 44,44,26
Mv 116,24,116
buffer_store_dwordx4 a[252:255], v3, s[20:23], s47 offen offset:3072 sc0 sc1
Mv 116,28,124
v_add_f32_e32 v15, v15, v45
v_add_f32_e32 v15, v15, v27
Mv 112,28,120
)" R"(
Cv 45,45,27
s_waitcnt vmcnt(20) lgkmcnt(0)
s_barrier
v_mfma_f32_16x16x32_bf16 v[120:123], a[32:35], a[64:67], v[16:19]
Rb 0,8,32768
v_add_f32_e32 v15, v15, v46
v_mfma_f32_16x16x32_bf16 v[124:127], a[32:35], a[68:71], v[20:23]
Rb 4,8,33792
v_add_f32_e32 v15, v15, v28
Mv 124,36,76
Rb 8,8,34816
Cv 46,46,28
Mv 120,36,72
Rb 12,8,35840
buffer_load_dwordx4 v3, s[20:23], s56 offen offset:2048 sc1 lds
Mv 120,40,80
Rb 16,8,36864
v_add_f32_e32 v15, v15, v47
Mv 124,40,84
Rb 20,8,37888
v_add_f32_e32 v15, v15, v29
Mv 124,44,92
Rb 24,8,38912
Cv 47,47,29
Mv 120,44,88
Rb 28,8,39936
v_max_f32_e32 v11, v14, v15
Mv 120,48,96
Rb 32,8,40960
v_cmp_nge_f32_e32 vcc, s62, v11
Mv 124,48,100
Rb 36,8,41984
s_add_u32 s55, s55, 0x10000
Mv 124,52,108
Rb 40,8,43008
Mv 120,52,104
Rb 44,8,44032
buffer_load_dwordx4 v3, s[20:23], s56 offen offset:3072 sc1 lds
Mv 120,56,112
s_cmp_eq_u32 s57, 2
s_cselect_b32 s55, s59, s55
Mv 124,56,116
s_add_u32 s56, s56, 0x10000
Mv 124,60,124
s_waitcnt lgkmcnt(8)
s_cbranch_vccnz .Lzfx8
.Lzfr8:
Mv 120,60,120
v_add_f32_e32 v12, v12, v14
v_add_f32_e32 v13, v13, v15
Mp 128,0,32
Rb 48,8,45056
Mp 136,0,36
s_cmp_le_i32 s57, s58
s_cbranch_scc1 .Lzmk7
.Lzmr7:
Mp 140,4,36
Rb 52,8,46080
s_add_u32 m0, s36, 32768
Mp 132,4,32
buffer_load_dwordx4 v2, s[20:23], s55 offen offset:0 sc1 lds
Mp 144,8,32
Rb 56,8,47104
Mp 152,8,36
v_exp_f32_e32 v48, v96
)" R"(
Mp 156,12,36
Rb 60,8,48128
s_waitcnt lgkmcnt(8)
Mp 148,12,32
v_exp_f32_e32 v24, v97
Mp 160,16,32
Rb 0,8,49152
v_add_f32_e32 v14, v48, v24
Mp 168,16,36
v_exp_f32_e32 v49, v98
Mp 172,20,36
Rb 4,8,50176
Cv 48,48,24
Mp 164,20,32
buffer_load_dwordx4 v2, s[20:23], s55 offen offset:1024 sc1 lds
Mp 176,24,32
Rb 8,8,51200
v_add_f32_e32 v14, v14, v49
Mp 184,24,36
v_exp_f32_e32 v25, v99
Mp 188,28,36
Rb 12,8,52224
s_waitcnt lgkmcnt(8)
Mp 180,28,32
v_exp_f32_e32 v50, v104
Mp 192,32,32
Rb 16,8,53248
v_add_f32_e32 v14, v14, v25
Mp 200,32,36
v_exp_f32_e32 v26, v105
Mp 204,36,36
Rb 20,8,54272
Cv 49,49,25
Mp 196,36,32
buffer_load_dwordx4 v2, s[20:23], s55 offen offset:2048 sc1 lds
Mp 208,40,32
Rb 24,8,55296
v_add_f32_e32 v14, v14, v50
Mp 216,40,36
v_exp_f32_e32 v51, v106
Mp 220,44,36
Rb 28,8,56320
s_waitcnt lgkmcnt(8)
Mp 212,44,32
v_exp_f32_e32 v27, v107
Mp 224,48,32
Rb 32,8,57344
v_add_f32_e32 v14, v14, v26
Mp 232,48,36
v_exp_f32_e32 v52, v100
Mp 236,52,36
Rb 36,8,58368
Cv 50,50,26
Mp 228,52,32
buffer_load_dwordx4 v2, s[20:23], s55 offen offset:3072 sc1 lds
Mp 240,56,32
Rb 40,8,59392
v_add_f32_e32 v14, v14, v51
Mp 248,56,36
v_exp_f32_e32 v28, v101
Mp 252,60,36
Rb 44,8,60416
s_waitcnt lgkmcnt(8)
Mp 244,60,32
)" R"(
v_exp_f32_e32 v53, v102
Mp 128,0,40
Rb 48,8,61440
v_add_f32_e32 v14, v14, v27
Mp 136,0,44
v_exp_f32_e32 v29, v103
Mp 140,4,44
Rb 52,8,62464
s_add_u32 m0, s36, 36864
Mp 132,4,40
buffer_load_dwordx4 v3, s[20:23], s55 offen offset:0 sc1 lds
Mp 144,8,40
Rb 56,8,63488
Cv 51,51,27
Mp 152,8,44
v_exp_f32_e32 v54, v108
Mp 156,12,44
Rb 60,8,64512
s_waitcnt lgkmcnt(8)
Mp 148,12,40
v_exp_f32_e32 v24, v109
Mp 160,16,40
v_exp_f32_e32 v55, v110
Mp 168,16,44
v_exp_f32_e32 v25, v111
Mp 172,20,44
v_add_f32_e32 v15, v52, v28
Cv 52,52,28
Mp 164,20,40
buffer_load_dwordx4 v3, s[20:23], s55 offen offset:1024 sc1 lds
Mp 176,24,40
v_add_f32_e32 v15, v15, v53
v_add_f32_e32 v15, v15, v29
Mp 184,24,44
Cv 53,53,29
v_add_f32_e32 v15, v15, v54
Mp 188,28,44
v_add_f32_e32 v15, v15, v24
Cv 54,54,24
Mp 180,28,40
s_waitcnt vmcnt(8) lgkmcnt(0)
s_barrier
buffer_store_dword v6, v7, s[20:23], s49 offen offset:8 sc0 sc1
Mp 192,32,40
Rb 0,1,0
v_add_f32_e32 v15, v15, v55
Mp 200,32,44
Rb 4,1,1024
v_add_f32_e32 v15, v15, v25
Mp 204,36,44
Rb 8,1,2048
Cv 55,55,25
Mp 196,36,40
Rb 12,1,3072
buffer_load_dwordx4 v3, s[20:23], s55 offen offset:2048 sc1 lds
Mp 208,40,40
Rb 16,1,8192
s_sub_u32 s57, s57, 1
Mp 216,40,44
Rb 20,1,9216
Mp 220,44,44
Rb 24,1,10240
Mp 212,44,40
Rb 28,1,11264
Mp 224,48,40
)" R"(
Rb 32,1,4096
Mp 232,48,44
Rb 36,1,5120
Mp 236,52,44
Rb 40,1,6144
Mp 228,52,40
Rb 44,1,7168
buffer_load_dwordx4 v3, s[20:23], s55 offen offset:3072 sc1 lds
Mp 240,56,40
v_cmp_eq_u32_e32 vcc, s3, v4
s_andn2_b64 s[28:29], s[60:61], vcc
s_cbranch_scc1 .Lzsp15
.Lzok15:
Mp 248,56,44
Mp 252,60,44
s_waitcnt lgkmcnt(8)
Mp 244,60,40
v_mfma_f32_16x16x32_bf16 v[64:67], a[0:3], a[64:67], v[16:19]
Rb 48,1,12288
v_mfma_f32_16x16x32_bf16 v[68:71], a[0:3], a[68:71], v[20:23]
v_exp_f32_e32 v56, v112
Mv 68,4,76
Rb 52,1,13312
s_add_u32 m0, s36, 98304
Mv 64,4,72
buffer_load_dwordx4 v2, s[20:23], s56 offen offset:0 sc1 lds
Mv 64,8,80
Rb 56,1,14336
v_add_f32_e32 v14, v14, v56
Mv 68,8,84
buffer_store_dwordx4 a[128:131], v2, s[20:23], s48 offen offset:0 sc0 sc1
Mv 68,12,92
Rb 60,1,15360
s_waitcnt lgkmcnt(8)
Mv 64,12,88
v_exp_f32_e32 v26, v113
Mv 64,16,96
Rb 0,1,16384
v_add_f32_e32 v14, v14, v26
Mv 68,16,100
v_exp_f32_e32 v57, v114
Mv 68,20,108
Rb 4,1,17408
Cv 56,56,26
Mv 64,20,104
buffer_load_dwordx4 v2, s[20:23], s56 offen offset:1024 sc1 lds
Mv 64,24,112
Rb 8,1,18432
v_add_f32_e32 v14, v14, v57
Mv 68,24,116
v_exp_f32_e32 v27, v115
Mv 68,28,124
Rb 12,1,19456
s_waitcnt lgkmcnt(8)
Mv 64,28,120
v_exp_f32_e32 v58, v120
v_mfma_f32_16x16x32_bf16 v[72:75], a[32:35], a[64:67], v[16:19]
Rb 16,1,24576
v_add_f32_e32 v14, v14, v27
v_mfma_f32_16x16x32_bf16 v[76:79], a[32:35], a[68:71], v[20:23]
v_exp_f32_e32 v28, v121
Mv 76,36,76
Rb 20,1,25600
Cv 57,57,27
)" R"(
Mv 72,36,72
buffer_load_dwordx4 v2, s[20:23], s56 offen offset:2048 sc1 lds
Mv 72,40,80
Rb 24,1,26624
v_add_f32_e32 v14, v14, v58
Mv 76,40,84
v_exp_f32_e32 v59, v122
Mv 76,44,92
Rb 28,1,27648
s_waitcnt lgkmcnt(8)
Mv 72,44,88
v_exp_f32_e32 v29, v123
Mv 72,48,96
Rb 32,1,20480
v_add_f32_e32 v14, v14, v28
Mv 76,48,100
v_exp_f32_e32 v60, v116
Mv 76,52,108
Rb 36,1,21504
Cv 58,58,28
Mv 72,52,104
buffer_load_dwordx4 v2, s[20:23], s56 offen offset:3072 sc1 lds
Mv 72,56,112
Rb 40,1,22528
v_add_f32_e32 v14, v14, v59
Mv 76,56,116
buffer_store_dwordx4 a[132:135], v2, s[20:23], s48 offen offset:1024 sc0 sc1
Mv 76,60,124
Rb 44,1,23552
s_waitcnt lgkmcnt(8)
Mv 72,60,120
v_exp_f32_e32 v24, v117
v_mfma_f32_16x16x32_bf16 v[80:83], a[0:3], a[64:67], v[16:19]
Rb 48,1,28672
v_add_f32_e32 v14, v14, v29
v_mfma_f32_16x16x32_bf16 v[84:87], a[0:3], a[68:71], v[20:23]
v_exp_f32_e32 v61, v118
Mv 84,4,76
Rb 52,1,29696
s_add_u32 m0, s36, 102400
Mv 80,4,72
buffer_load_dwordx4 v3, s[20:23], s56 offen offset:0 sc1 lds
Mv 80,8,80
Rb 56,1,30720
Cv 59,59,29
Mv 84,8,84
v_exp_f32_e32 v25, v119
Mv 84,12,92
Rb 60,1,31744
s_waitcnt lgkmcnt(8)
Mv 80,12,88
v_exp_f32_e32 v62, v124
Mv 80,16,96
v_exp_f32_e32 v26, v125
Mv 84,16,100
v_exp_f32_e32 v63, v126
Mv 84,20,108
v_exp_f32_e32 v27, v127
Mv 80,20,104
buffer_load_dwordx4 v3, s[20:23], s56 offen offset:1024 sc1 lds
Mv 80,24,112
v_add_f32_e32 v15, v15, v60
v_add_f32_e32 v15, v15, v24
Mv 84,24,116
)" R"(
Cv 60,60,24
v_add_f32_e32 v15, v15, v61
Mv 84,28,124
v_add_f32_e32 v15, v15, v25
Cv 61,61,25
Mv 80,28,120
s_waitcnt vmcnt(17) lgkmcnt(0)
s_barrier
v_mfma_f32_16x16x32_bf16 v[88:91], a[32:35], a[64:67], v[16:19]
Rb 0,8,0
v_add_f32_e32 v15, v15, v62
v_mfma_f32_16x16x32_bf16 v[92:95], a[32:35], a[68:71], v[20:23]
Rb 4,8,1024
v_add_f32_e32 v15, v15, v26
Mv 92,36,76
Rb 8,8,2048
Cv 62,62,26
Mv 88,36,72
Rb 12,8,3072
buffer_load_dwordx4 v3, s[20:23], s56 offen offset:2048 sc1 lds
Mv 88,40,80
Rb 16,8,4096
v_add_f32_e32 v15, v15, v63
Mv 92,40,84
Rb 20,8,5120
buffer_store_dwordx4 a[136:139], v2, s[20:23], s48 offen offset:2048 sc0 sc1
Mv 92,44,92
Rb 24,8,6144
v_add_f32_e32 v15, v15, v27
Mv 88,44,88
Rb 28,8,7168
Cv 63,63,27
Mv 88,48,96
Rb 32,8,8192
v_max_f32_e32 v11, v14, v15
Mv 92,48,100
Rb 36,8,9216
v_cmp_nge_f32_e32 vcc, s62, v11
Mv 92,52,108
Rb 40,8,10240
s_add_u32 s55, s55, 0x10000
Mv 88,52,104
Rb 44,8,11264
buffer_load_dwordx4 v3, s[20:23], s56 offen offset:3072 sc1 lds
Mv 88,56,112
s_cmp_eq_u32 s57, 2
s_cselect_b32 s55, s59, s55
Mv 92,56,116
s_add_u32 s56, s56, 0x10000
Mv 92,60,124
s_waitcnt lgkmcnt(8)
s_cbranch_vccnz .Lzfx9
.Lzfr9:
Mv 88,60,120
v_add_f32_e32 v12, v12, v14
v_add_f32_e32 v13, v13, v15
Mp 128,0,48
Rb 48,8,12288
Mp 136,0,52
s_cmp_le_i32 s57, s58
s_cbranch_scc1 .Lzmk8
.Lzmr8:
Mp 140,4,52
Rb 52,8,13312
s_add_u32 m0, s36, 0
)" R"(
Mp 132,4,48
buffer_load_dwordx4 v2, s[20:23], s55 offen offset:0 sc1 lds
Mp 144,8,48
Rb 56,8,14336
Mp 152,8,52
v_exp_f32_e32 v32, v64
Mp 156,12,52
Rb 60,8,15360
s_waitcnt lgkmcnt(8)
Mp 148,12,48
v_exp_f32_e32 v28, v65
Mp 160,16,48
Rb 0,8,16384
v_add_f32_e32 v14, v32, v28
Mp 168,16,52
v_exp_f32_e32 v33, v66
Mp 172,20,52
Rb 4,8,17408
Cv 32,32,28
Mp 164,20,48
buffer_load_dwordx4 v2, s[20:23], s55 offen offset:1024 sc1 lds
Mp 176,24,48
Rb 8,8,18432
v_add_f32_e32 v14, v14, v33
Mp 184,24,52
buffer_store_dwordx4 a[140:143], v2, s[20:23], s48 offen offset:3072 sc0 sc1
Mp 188,28,52
Rb 12,8,19456
s_waitcnt lgkmcnt(8)
Mp 180,28,48
v_exp_f32_e32 v29, v67
Mp 192,32,48
Rb 16,8,20480
v_add_f32_e32 v14, v14, v29
Mp 200,32,52
v_exp_f32_e32 v34, v72
Mp 204,36,52
Rb 20,8,21504
Cv 33,33,29
Mp 196,36,48
buffer_load_dwordx4 v2, s[20:23], s55 offen offset:2048 sc1 lds
Mp 208,40,48
Rb 24,8,22528
v_add_f32_e32 v14, v14, v34
Mp 216,40,52
v_exp_f32_e32 v24, v73
Mp 220,44,52
Rb 28,8,23552
s_waitcnt lgkmcnt(8)
Mp 212,44,48
v_exp_f32_e32 v35, v74
Mp 224,48,48
Rb 32,8,24576
v_add_f32_e32 v14, v14, v24
Mp 232,48,52
v_exp_f32_e32 v25, v75
Mp 236,52,52
Rb 36,8,25600
Cv 34,34,24
Mp 228,52,48
buffer_load_dwordx4 v2, s[20:23], s55 offen offset:3072 sc1 lds
Mp 240,56,48
Rb 40,8,26624
v_add_f32_e32 v14, v14, v35
)" R"(
Mp 248,56,52
v_exp_f32_e32 v36, v68
Mp 252,60,52
Rb 44,8,27648
s_waitcnt lgkmcnt(8)
Mp 244,60,48
v_exp_f32_e32 v26, v69
Mp 128,0,56
Rb 48,8,28672
v_add_f32_e32 v14, v14, v25
Mp 136,0,60
v_exp_f32_e32 v37, v70
Mp 140,4,60
Rb 52,8,29696
s_add_u32 m0, s36, 4096
Mp 132,4,56
buffer_load_dwordx4 v3, s[20:23], s55 offen offset:0 sc1 lds
Mp 144,8,56
Rb 56,8,30720
Cv 35,35,25
Mp 152,8,60
buffer_store_dwordx4 a[144:147], v3, s[20:23], s48 offen offset:0 sc0 sc1
Mp 156,12,60
Rb 60,8,31744
s_waitcnt lgkmcnt(8)
Mp 148,12,56
v_exp_f32_e32 v27, v71
Mp 160,16,56
v_exp_f32_e32 v38, v76
Mp 168,16,60
v_exp_f32_e32 v28, v77
Mp 172,20,60
v_exp_f32_e32 v39, v78
Mp 164,20,56
buffer_load_dwordx4 v3, s[20:23], s55 offen offset:1024 sc1 lds
Mp 176,24,56
v_exp_f32_e32 v29, v79
Mp 184,24,60
v_add_f32_e32 v15, v36, v26
Cv 36,36,26
Mp 188,28,60
v_add_f32_e32 v15, v15, v37
v_add_f32_e32 v15, v15, v27
Mp 180,28,56
s_waitcnt vmcnt(19) lgkmcnt(0)
s_barrier
Mp 192,32,56
Rb 0,1,32768
Cv 37,37,27
Mp 200,32,60
Rb 4,1,33792
v_add_f32_e32 v15, v15, v38
Mp 204,36,60
Rb 8,1,34816
v_add_f32_e32 v15, v15, v28
Mp 196,36,56
Rb 12,1,35840
buffer_load_dwordx4 v3, s[20:23], s55 offen offset:2048 sc1 lds
Mp 208,40,56
Rb 16,1,40960
Cv 38,38,28
Mp 216,40,60
Rb 20,1,41984
v_add_f32_e32 v15, v15, v39
Mp 220,44,60
)" R"(
Rb 24,1,43008
v_add_f32_e32 v15, v15, v29
Mp 212,44,56
Rb 28,1,44032
Cv 39,39,29
Mp 224,48,56
Rb 32,1,36864
s_sub_u32 s57, s57, 1
Mp 232,48,60
Rb 36,1,37888
Mp 236,52,60
Rb 40,1,38912
Mp 228,52,56
Rb 44,1,39936
buffer_load_dwordx4 v3, s[20:23], s55 offen offset:3072 sc1 lds
Mp 240,56,56
Mp 248,56,60
buffer_store_dwordx4 a[148:151], v3, s[20:23], s48 offen offset:1024 sc0 sc1
Mp 252,60,60
s_waitcnt lgkmcnt(8)
Mp 244,60,56
v_mfma_f32_16x16x32_bf16 v[96:99], a[0:3], a[64:67], v[16:19]
Rb 48,1,45056
buffer_load_dword v4, v5, s[20:23], s50 offen offset:0 sc0 sc1
v_mfma_f32_16x16x32_bf16 v[100:103], a[0:3], a[68:71], v[20:23]
s_lshl_b64 s[60:61], s[60:61], 8
s_or_b32 s60, s60, 0xff
Mv 100,4,76
Rb 52,1,46080
s_add_u32 m0, s36, 65536
Mv 96,4,72
buffer_load_dwordx4 v2, s[20:23], s56 offen offset:0 sc1 lds
Mv 96,8,80
Rb 56,1,47104
Mv 100,8,84
v_exp_f32_e32 v40, v80
Mv 100,12,92
Rb 60,1,48128
s_waitcnt lgkmcnt(8)
Mv 96,12,88
v_exp_f32_e32 v24, v81
Mv 96,16,96
Rb 0,1,49152
v_add_f32_e32 v14, v14, v40
Mv 100,16,100
v_exp_f32_e32 v41, v82
Mv 100,20,108
Rb 4,1,50176
v_add_f32_e32 v14, v14, v24
Mv 96,20,104
buffer_load_dwordx4 v2, s[20:23], s56 offen offset:1024 sc1 lds
Mv 96,24,112
Rb 8,1,51200
Cv 40,40,24
Mv 100,24,116
v_exp_f32_e32 v25, v83
Mv 100,28,124
Rb 12,1,52224
s_waitcnt lgkmcnt(8)
Mv 96,28,120
v_exp_f32_e32 v42, v88
v_add_f32_e32 v14, v14, v41
v_mfma_f32_16x16x32_bf16 v[104:107], a[32:35], a[64:67], v[16:19]
Rb 16,1,57344
)" R"(
v_add_f32_e32 v14, v14, v25
v_mfma_f32_16x16x32_bf16 v[108:111], a[32:35], a[68:71], v[20:23]
v_exp_f32_e32 v26, v89
Mv 108,36,76
Rb 20,1,58368
Cv 41,41,25
Mv 104,36,72
buffer_load_dwordx4 v2, s[20:23], s56 offen offset:2048 sc1 lds
Mv 104,40,80
Rb 24,1,59392
v_add_f32_e32 v14, v14, v42
Mv 108,40,84
buffer_store_dwordx4 a[152:155], v3, s[20:23], s48 offen offset:2048 sc0 sc1
Mv 108,44,92
Rb 28,1,60416
s_waitcnt lgkmcnt(8)
Mv 104,44,88
v_exp_f32_e32 v43, v90
Mv 104,48,96
Rb 32,1,53248
v_add_f32_e32 v14, v14, v26
Mv 108,48,100
v_exp_f32_e32 v27, v91
Mv 108,52,108
Rb 36,1,54272
Cv 42,42,26
Mv 104,52,104
buffer_load_dwordx4 v2, s[20:23], s56 offen offset:3072 sc1 lds
Mv 104,56,112
Rb 40,1,55296
v_add_f32_e32 v14, v14, v43
Mv 108,56,116
v_exp_f32_e32 v44, v84
Mv 108,60,124
Rb 44,1,56320
s_waitcnt lgkmcnt(8)
Mv 104,60,120
v_exp_f32_e32 v28, v85
v_add_f32_e32 v14, v14, v27
v_mfma_f32_16x16x32_bf16 v[112:115], a[0:3], a[64:67], v[16:19]
Rb 48,1,61440
Cv 43,43,27
v_mfma_f32_16x16x32_bf16 v[116:119], a[0:3], a[68:71], v[20:23]
v_exp_f32_e32 v45, v86
Mv 116,4,76
Rb 52,1,62464
s_add_u32 m0, s36, 69632
Mv 112,4,72
buffer_load_dwordx4 v3, s[20:23], s56 offen offset:0 sc1 lds
Mv 112,8,80
Rb 56,1,63488
v_add_f32_e32 v15, v15, v44
Mv 116,8,84
v_exp_f32_e32 v29, v87
Mv 116,12,92
Rb 60,1,64512
s_waitcnt lgkmcnt(8)
Mv 112,12,88
v_exp_f32_e32 v46, v92
Mv 112,16,96
v_exp_f32_e32 v24, v93
Mv 116,16,100
v_exp_f32_e32 v47, v94
Mv 116,20,108
)" R"(
v_exp_f32_e32 v25, v95
Mv 112,20,104
buffer_load_dwordx4 v3, s[20:23], s56 offen offset:1024 sc1 lds
Mv 112,24,112
v_add_f32_e32 v15, v15, v28
Cv 44,44,28
Mv 116,24,116
buffer_store_dwordx4 a[156:159], v3, s[20:23], s48 offen offset:3072 sc0 sc1
Mv 116,28,124
v_add_f32_e32 v15, v15, v45
v_add_f32_e32 v15, v15, v29
Mv 112,28,120
Cv 45,45,29
s_waitcnt vmcnt(20) lgkmcnt(0)
s_barrier
v_mfma_f32_16x16x32_bf16 v[120:123], a[32:35], a[64:67], v[16:19]
Rb 0,8,32768
v_add_f32_e32 v15, v15, v46
v_mfma_f32_16x16x32_bf16 v[124:127], a[32:35], a[68:71], v[20:23]
Rb 4,8,33792
v_add_f32_e32 v15, v15, v24
Mv 124,36,76
Rb 8,8,34816
Cv 46,46,24
Mv 120,36,72
Rb 12,8,35840
buffer_load_dwordx4 v3, s[20:23], s56 offen offset:2048 sc1 lds
Mv 120,40,80
Rb 16,8,36864
v_add_f32_e32 v15, v15, v47
Mv 124,40,84
Rb 20,8,37888
v_add_f32_e32 v15, v15, v25
Mv 124,44,92
Rb 24,8,38912
Cv 47,47,25
Mv 120,44,88
Rb 28,8,39936
v_max_f32_e32 v11, v14, v15
Mv 120,48,96
Rb 32,8,40960
v_cmp_nge_f32_e32 vcc, s62, v11
Mv 124,48,100
Rb 36,8,41984
s_add_u32 s55, s55, 0x10000
Mv 124,52,108
Rb 40,8,43008
Mv 120,52,104
Rb 44,8,44032
buffer_load_dwordx4 v3, s[20:23], s56 offen offset:3072 sc1 lds
Mv 120,56,112
s_cmp_eq_u32 s57, 2
s_cselect_b32 s55, s59, s55
Mv 124,56,116
s_add_u32 s56, s56, 0x10000
Mv 124,60,124
s_waitcnt lgkmcnt(8)
s_cbranch_vccnz .Lzfx10
.Lzfr10:
Mv 120,60,120
v_add_f32_e32 v12, v12, v14
v_add_f32_e32 v13, v13, v15
Mp 128,0,32
Rb 48,8,45056
Mp 136,0,36
)" R"(
s_cmp_le_i32 s57, s58
s_cbranch_scc1 .Lzmk9
.Lzmr9:
Mp 140,4,36
Rb 52,8,46080
s_add_u32 m0, s36, 32768
Mp 132,4,32
buffer_load_dwordx4 v2, s[20:23], s55 offen offset:0 sc1 lds
Mp 144,8,32
Rb 56,8,47104
Mp 152,8,36
v_exp_f32_e32 v48, v96
Mp 156,12,36
Rb 60,8,48128
s_waitcnt lgkmcnt(8)
Mp 148,12,32
v_exp_f32_e32 v26, v97
Mp 160,16,32
Rb 0,8,49152
v_add_f32_e32 v14, v48, v26
Mp 168,16,36
v_exp_f32_e32 v49, v98
Mp 172,20,36
Rb 4,8,50176
Cv 48,48,26
Mp 164,20,32
buffer_load_dwordx4 v2, s[20:23], s55 offen offset:1024 sc1 lds
Mp 176,24,32
Rb 8,8,51200
v_add_f32_e32 v14, v14, v49
Mp 184,24,36
v_exp_f32_e32 v27, v99
Mp 188,28,36
Rb 12,8,52224
s_waitcnt lgkmcnt(8)
Mp 180,28,32
v_exp_f32_e32 v50, v104
Mp 192,32,32
Rb 16,8,53248
v_add_f32_e32 v14, v14, v27
Mp 200,32,36
v_exp_f32_e32 v28, v105
Mp 204,36,36
Rb 20,8,54272
Cv 49,49,27
Mp 196,36,32
buffer_load_dwordx4 v2, s[20:23], s55 offen offset:2048 sc1 lds
Mp 208,40,32
Rb 24,8,55296
v_add_f32_e32 v14, v14, v50
Mp 216,40,36
v_exp_f32_e32 v51, v106
Mp 220,44,36
Rb 28,8,56320
s_waitcnt lgkmcnt(8)
Mp 212,44,32
v_exp_f32_e32 v29, v107
Mp 224,48,32
Rb 32,8,57344
v_add_f32_e32 v14, v14, v28
Mp 232,48,36
v_exp_f32_e32 v52, v100
Mp 236,52,36
Rb 36,8,58368
)" R"(
Cv 50,50,28
Mp 228,52,32
buffer_load_dwordx4 v2, s[20:23], s55 offen offset:3072 sc1 lds
Mp 240,56,32
Rb 40,8,59392
v_add_f32_e32 v14, v14, v51
Mp 248,56,36
v_exp_f32_e32 v24, v101
Mp 252,60,36
Rb 44,8,60416
s_waitcnt lgkmcnt(8)
Mp 244,60,32
v_exp_f32_e32 v53, v102
Mp 128,0,40
Rb 48,8,61440
v_add_f32_e32 v14, v14, v29
Mp 136,0,44
v_exp_f32_e32 v25, v103
Mp 140,4,44
Rb 52,8,62464
s_add_u32 m0, s36, 36864
Mp 132,4,40
buffer_load_dwordx4 v3, s[20:23], s55 offen offset:0 sc1 lds
Mp 144,8,40
Rb 56,8,63488
Cv 51,51,29
Mp 152,8,44
v_exp_f32_e32 v54, v108
Mp 156,12,44
Rb 60,8,64512
s_waitcnt lgkmcnt(8)
Mp 148,12,40
v_exp_f32_e32 v26, v109
Mp 160,16,40
v_exp_f32_e32 v55, v110
Mp 168,16,44
v_exp_f32_e32 v27, v111
Mp 172,20,44
v_add_f32_e32 v15, v52, v24
Cv 52,52,24
Mp 164,20,40
buffer_load_dwordx4 v3, s[20:23], s55 offen offset:1024 sc1 lds
Mp 176,24,40
v_add_f32_e32 v15, v15, v53
v_add_f32_e32 v15, v15, v25
Mp 184,24,44
Cv 53,53,25
v_add_f32_e32 v15, v15, v54
Mp 188,28,44
v_add_f32_e32 v15, v15, v26
Cv 54,54,26
Mp 180,28,40
s_waitcnt vmcnt(8) lgkmcnt(0)
s_barrier
buffer_store_dword v6, v7, s[20:23], s49 offen offset:12 sc0 sc1
Mp 192,32,40
Rb 0,1,0
v_add_f32_e32 v15, v15, v55
Mp 200,32,44
Rb 4,1,1024
v_add_f32_e32 v15, v15, v27
Mp 204,36,44
Rb 8,1,2048
Cv 55,55,27
Mp 196,36,40
)" R"(
Rb 12,1,3072
buffer_load_dwordx4 v3, s[20:23], s55 offen offset:2048 sc1 lds
Mp 208,40,40
Rb 16,1,8192
s_sub_u32 s57, s57, 1
Mp 216,40,44
Rb 20,1,9216
Mp 220,44,44
Rb 24,1,10240
Mp 212,44,40
Rb 28,1,11264
Mp 224,48,40
Rb 32,1,4096
Mp 232,48,44
Rb 36,1,5120
Mp 236,52,44
Rb 40,1,6144
Mp 228,52,40
Rb 44,1,7168
buffer_load_dwordx4 v3, s[20:23], s55 offen offset:3072 sc1 lds
Mp 240,56,40
v_cmp_eq_u32_e32 vcc, s3, v4
s_andn2_b64 s[28:29], s[60:61], vcc
s_cbranch_scc1 .Lzsp16
.Lzok16:
Mp 248,56,44
Mp 252,60,44
s_waitcnt lgkmcnt(8)
Mp 244,60,40
s_branch .LzL
.LzEB:
v_mfma_f32_16x16x32_bf16 v[64:67], a[0:3], a[64:67], v[16:19]
Rb 48,1,12288
v_mfma_f32_16x16x32_bf16 v[68:71], a[0:3], a[68:71], v[20:23]
s_cmp_lg_u32 s35, 7
s_cselect_b64 s[60:61], 1, 0
Mv 68,4,76
Rb 52,1,13312
s_add_u32 m0, s36, 98304
Mv 64,4,72
buffer_load_dwordx4 v2, s[20:23], s56 offen offset:0 sc1 lds
Mv 64,8,80
Rb 56,1,14336
Mv 68,8,84
v_exp_f32_e32 v56, v112
Mv 68,12,92
Rb 60,1,15360
s_waitcnt lgkmcnt(8)
Mv 64,12,88
v_exp_f32_e32 v28, v113
Mv 64,16,96
Rb 0,1,16384
v_add_f32_e32 v14, v14, v56
Mv 68,16,100
v_exp_f32_e32 v57, v114
Mv 68,20,108
Rb 4,1,17408
v_add_f32_e32 v14, v14, v28
Mv 64,20,104
buffer_load_dwordx4 v2, s[20:23], s56 offen offset:1024 sc1 lds
Mv 64,24,112
Rb 8,1,18432
Cv 56,56,28
Mv 68,24,116
)" R"(
v_exp_f32_e32 v29, v115
Mv 68,28,124
Rb 12,1,19456
s_waitcnt lgkmcnt(8)
Mv 64,28,120
v_exp_f32_e32 v58, v120
v_mfma_f32_16x16x32_bf16 v[72:75], a[32:35], a[64:67], v[16:19]
Rb 16,1,24576
v_add_f32_e32 v14, v14, v57
v_mfma_f32_16x16x32_bf16 v[76:79], a[32:35], a[68:71], v[20:23]
v_exp_f32_e32 v24, v121
Mv 76,36,76
Rb 20,1,25600
v_add_f32_e32 v14, v14, v29
Mv 72,36,72
buffer_load_dwordx4 v2, s[20:23], s56 offen offset:2048 sc1 lds
Mv 72,40,80
Rb 24,1,26624
Cv 57,57,29
Mv 76,40,84
v_exp_f32_e32 v59, v122
Mv 76,44,92
Rb 28,1,27648
s_waitcnt lgkmcnt(8)
Mv 72,44,88
v_exp_f32_e32 v25, v123
Mv 72,48,96
Rb 32,1,20480
v_add_f32_e32 v14, v14, v58
Mv 76,48,100
v_exp_f32_e32 v60, v116
Mv 76,52,108
Rb 36,1,21504
v_add_f32_e32 v14, v14, v24
Mv 72,52,104
buffer_load_dwordx4 v2, s[20:23], s56 offen offset:3072 sc1 lds
Mv 72,56,112
Rb 40,1,22528
Cv 58,58,24
Mv 76,56,116
v_exp_f32_e32 v26, v117
Mv 76,60,124
Rb 44,1,23552
s_waitcnt lgkmcnt(8)
Mv 72,60,120
v_exp_f32_e32 v61, v118
v_mfma_f32_16x16x32_bf16 v[80:83], a[0:3], a[64:67], v[16:19]
Rb 48,1,28672
v_add_f32_e32 v14, v14, v59
v_mfma_f32_16x16x32_bf16 v[84:87], a[0:3], a[68:71], v[20:23]
v_exp_f32_e32 v27, v119
Mv 84,4,76
Rb 52,1,29696
s_add_u32 m0, s36, 102400
Mv 80,4,72
buffer_load_dwordx4 v3, s[20:23], s56 offen offset:0 sc1 lds
Mv 80,8,80
Rb 56,1,30720
v_add_f32_e32 v14, v14, v25
Mv 84,8,84
v_exp_f32_e32 v62, v124
Mv 84,12,92
Rb 60,1,31744
s_waitcnt lgkmcnt(8)
)" R"(
Mv 80,12,88
v_exp_f32_e32 v28, v125
Mv 80,16,96
v_exp_f32_e32 v63, v126
Mv 84,16,100
v_exp_f32_e32 v29, v127
Mv 84,20,108
Cv 59,59,25
v_add_f32_e32 v15, v15, v60
Mv 80,20,104
buffer_load_dwordx4 v3, s[20:23], s56 offen offset:1024 sc1 lds
Mv 80,24,112
v_add_f32_e32 v15, v15, v26
Cv 60,60,26
Mv 84,24,116
v_add_f32_e32 v15, v15, v61
v_add_f32_e32 v15, v15, v27
Mv 84,28,124
Cv 61,61,27
v_add_f32_e32 v15, v15, v62
Mv 80,28,120
s_waitcnt vmcnt(14) lgkmcnt(0)
s_barrier
v_mfma_f32_16x16x32_bf16 v[88:91], a[32:35], a[64:67], v[16:19]
Rb 0,8,0
v_add_f32_e32 v15, v15, v28
v_mfma_f32_16x16x32_bf16 v[92:95], a[32:35], a[68:71], v[20:23]
Rb 4,8,1024
Cv 62,62,28
Mv 92,36,76
Rb 8,8,2048
v_add_f32_e32 v15, v15, v63
Mv 88,36,72
Rb 12,8,3072
buffer_load_dwordx4 v3, s[20:23], s56 offen offset:2048 sc1 lds
Mv 88,40,80
Rb 16,8,4096
v_add_f32_e32 v15, v15, v29
Mv 92,40,84
Rb 20,8,5120
Cv 63,63,29
Mv 92,44,92
Rb 24,8,6144
v_max_f32_e32 v11, v14, v15
Mv 88,44,88
Rb 28,8,7168
v_cmp_nge_f32_e32 vcc, s62, v11
Mv 88,48,96
Rb 32,8,8192
s_add_u32 s55, s55, 0x10000
Mv 92,48,100
Rb 36,8,9216
Mv 92,52,108
Rb 40,8,10240
Mv 88,52,104
Rb 44,8,11264
buffer_load_dwordx4 v3, s[20:23], s56 offen offset:3072 sc1 lds
Mv 88,56,112
s_cmp_eq_u32 s57, 2
s_cselect_b32 s55, s59, s55
Mv 92,56,116
s_add_u32 s56, s56, 0x10000
Mv 92,60,124
s_waitcnt lgkmcnt(8)
s_cbranch_vccnz .Lzfx11
)" R"(
.Lzfr11:
Mv 88,60,120
v_add_f32_e32 v12, v12, v14
v_add_f32_e32 v13, v13, v15
Mp 128,0,48
Rb 48,8,12288
Mp 136,0,52
s_cmp_le_i32 s57, s58
s_cbranch_scc1 .Lzmk10
.Lzmr10:
Mp 140,4,52
Rb 52,8,13312
s_add_u32 m0, s36, 0
Mp 132,4,48
buffer_load_dwordx4 v2, s[20:23], s55 offen offset:0 sc1 lds
Mp 144,8,48
Rb 56,8,14336
Mp 152,8,52
v_exp_f32_e32 v32, v64
Mp 156,12,52
Rb 60,8,15360
s_waitcnt lgkmcnt(8)
Mp 148,12,48
v_exp_f32_e32 v24, v65
Mp 160,16,48
Rb 0,8,16384
v_add_f32_e32 v14, v32, v24
Mp 168,16,52
v_exp_f32_e32 v33, v66
Cv 32,32,24
Mp 172,20,52
Rb 4,8,17408
v_add_f32_e32 v14, v14, v33
Mp 164,20,48
buffer_load_dwordx4 v2, s[20:23], s55 offen offset:1024 sc1 lds
Mp 176,24,48
Rb 8,8,18432
Mp 184,24,52
v_exp_f32_e32 v25, v67
Mp 188,28,52
Rb 12,8,19456
s_waitcnt lgkmcnt(8)
Mp 180,28,48
v_exp_f32_e32 v34, v72
Mp 192,32,48
Rb 16,8,20480
v_add_f32_e32 v14, v14, v25
Mp 200,32,52
v_exp_f32_e32 v26, v73
Cv 33,33,25
Mp 204,36,52
Rb 20,8,21504
v_add_f32_e32 v14, v14, v34
Mp 196,36,48
buffer_load_dwordx4 v2, s[20:23], s55 offen offset:2048 sc1 lds
Mp 208,40,48
Rb 24,8,22528
v_add_f32_e32 v14, v14, v26
Mp 216,40,52
v_exp_f32_e32 v35, v74
Mp 220,44,52
Rb 28,8,23552
s_waitcnt lgkmcnt(8)
Mp 212,44,48
)" R"(
v_exp_f32_e32 v27, v75
Mp 224,48,48
Rb 32,8,24576
Cv 34,34,26
Mp 232,48,52
v_exp_f32_e32 v36, v68
v_add_f32_e32 v14, v14, v35
Mp 236,52,52
Rb 36,8,25600
v_add_f32_e32 v14, v14, v27
Mp 228,52,48
buffer_load_dwordx4 v2, s[20:23], s55 offen offset:3072 sc1 lds
Mp 240,56,48
Rb 40,8,26624
Cv 35,35,27
Mp 248,56,52
v_exp_f32_e32 v28, v69
Mp 252,60,52
Rb 44,8,27648
s_waitcnt lgkmcnt(8)
Mp 244,60,48
v_exp_f32_e32 v37, v70
Mp 128,0,56
Rb 48,8,28672
v_add_f32_e32 v15, v36, v28
Mp 136,0,60
v_exp_f32_e32 v29, v71
Cv 36,36,28
Mp 140,4,60
Rb 52,8,29696
s_add_u32 m0, s36, 4096
Mp 132,4,56
buffer_load_dwordx4 v3, s[20:23], s55 offen offset:0 sc1 lds
Mp 144,8,56
Rb 56,8,30720
v_add_f32_e32 v15, v15, v37
Mp 152,8,60
v_exp_f32_e32 v38, v76
Mp 156,12,60
Rb 60,8,31744
s_waitcnt lgkmcnt(8)
Mp 148,12,56
v_exp_f32_e32 v24, v77
Mp 160,16,56
v_exp_f32_e32 v39, v78
Mp 168,16,60
v_exp_f32_e32 v25, v79
v_add_f32_e32 v15, v15, v29
Mp 172,20,60
Cv 37,37,29
v_add_f32_e32 v15, v15, v38
Mp 164,20,56
buffer_load_dwordx4 v3, s[20:23], s55 offen offset:1024 sc1 lds
Mp 176,24,56
v_add_f32_e32 v15, v15, v24
Cv 38,38,24
Mp 184,24,60
v_add_f32_e32 v15, v15, v39
v_add_f32_e32 v15, v15, v25
Mp 188,28,60
Cv 39,39,25
s_sub_u32 s57, s57, 1
Mp 180,28,56
s_waitcnt vmcnt(14) lgkmcnt(0)
s_barrier
)" R"(
Mp 192,32,56
Rb 0,1,32768
v_bfe_u32 v11, v0, 2, 2
Mp 200,32,60
Rb 4,1,33792
v_lshrrev_b32 v4, 4, v0
v_xor_b32 v11, v11, v4
Mp 204,36,60
Rb 8,1,34816
v_bfe_u32 v4, v0, 1, 1
Mp 196,36,56
Rb 12,1,35840
buffer_load_dwordx4 v3, s[20:23], s55 offen offset:2048 sc1 lds
Mp 208,40,56
Rb 16,1,40960
v_lshl_or_b32 v11, v11, 1, v4
Mp 216,40,60
Rb 20,1,41984
v_and_b32 v4, 1, v0
Mp 220,44,60
Rb 24,1,43008
v_mul_u32_u24 v4, 0xffff, v4
Mp 212,44,56
Rb 28,1,44032
v_add_u32 v4, 1, v4
Mp 224,48,56
Rb 32,1,36864
v_mul_u32_u24 v4, 0x3f80, v4
Mp 232,48,60
Rb 36,1,37888
v_cmp_eq_u32_e32 vcc, 0, v11
v_cmp_eq_u32_e64 s[28:29], 1, v11
Mp 236,52,60
Rb 40,1,38912
v_accvgpr_write_b32 a192, 0
Mp 228,52,56
Rb 44,1,39936
buffer_load_dwordx4 v3, s[20:23], s55 offen offset:3072 sc1 lds
Mp 240,56,56
v_accvgpr_write_b32 a193, 0
v_accvgpr_write_b32 a196, 0
Mp 248,56,60
v_accvgpr_write_b32 a197, 0
v_cndmask_b32_e32 v11, 0, v4, vcc
Mp 252,60,60
v_cndmask_b32_e64 v4, 0, v4, s[28:29]
s_waitcnt lgkmcnt(8)
Mp 244,60,56
v_accvgpr_write_b32 a194, v11
v_accvgpr_write_b32 a195, v4
v_mfma_f32_16x16x32_bf16 v[96:99], a[0:3], a[64:67], v[16:19]
Rb 48,1,45056
v_mfma_f32_16x16x32_bf16 v[100:103], a[0:3], a[68:71], v[20:23]
v_exp_f32_e32 v40, v80
Mv 100,4,76
Rb 52,1,46080
s_add_u32 m0, s36, 65536
Mv 96,4,72
buffer_load_dwordx4 v2, s[20:23], s56 offen offset:0 sc1 lds
Mv 96,8,80
Rb 56,1,47104
v_add_f32_e32 v14, v14, v40
Mv 100,8,84
v_exp_f32_e32 v26, v81
)" R"(
Mv 100,12,92
Rb 60,1,48128
s_waitcnt lgkmcnt(8)
Mv 96,12,88
v_exp_f32_e32 v41, v82
Mv 96,16,96
Rb 0,1,49152
v_add_f32_e32 v14, v14, v26
Mv 100,16,100
v_exp_f32_e32 v27, v83
Mv 100,20,108
Rb 4,1,50176
Cv 40,40,26
Mv 96,20,104
buffer_load_dwordx4 v2, s[20:23], s56 offen offset:1024 sc1 lds
Mv 96,24,112
Rb 8,1,51200
v_add_f32_e32 v14, v14, v41
Mv 100,24,116
v_exp_f32_e32 v42, v88
Mv 100,28,124
Rb 12,1,52224
s_waitcnt lgkmcnt(8)
Mv 96,28,120
v_exp_f32_e32 v28, v89
v_mfma_f32_16x16x32_bf16 v[104:107], a[32:35], a[64:67], v[16:19]
Rb 16,1,57344
v_add_f32_e32 v14, v14, v27
v_mfma_f32_16x16x32_bf16 v[108:111], a[32:35], a[68:71], v[20:23]
v_exp_f32_e32 v43, v90
Mv 108,36,76
Rb 20,1,58368
Cv 41,41,27
Mv 104,36,72
buffer_load_dwordx4 v2, s[20:23], s56 offen offset:2048 sc1 lds
Mv 104,40,80
Rb 24,1,59392
buffer_load_dword v4, v7, s[20:23], s65 offen offset:0 sc0 sc1
Mv 108,40,84
v_exp_f32_e32 v29, v91
Mv 108,44,92
Rb 28,1,60416
s_waitcnt lgkmcnt(8)
Mv 104,44,88
v_exp_f32_e32 v44, v84
Mv 104,48,96
Rb 32,1,53248
v_add_f32_e32 v14, v14, v42
Mv 108,48,100
v_exp_f32_e32 v24, v85
Mv 108,52,108
Rb 36,1,54272
v_add_f32_e32 v14, v14, v28
Mv 104,52,104
buffer_load_dwordx4 v2, s[20:23], s56 offen offset:3072 sc1 lds
Mv 104,56,112
Rb 40,1,55296
Cv 42,42,28
Mv 108,56,116
v_exp_f32_e32 v45, v86
Mv 108,60,124
Rb 44,1,56320
s_waitcnt lgkmcnt(8)
Mv 104,60,120
)" R"(
v_exp_f32_e32 v25, v87
v_mfma_f32_16x16x32_bf16 v[112:115], a[0:3], a[64:67], v[16:19]
Rb 48,1,61440
v_add_f32_e32 v14, v14, v43
v_mfma_f32_16x16x32_bf16 v[116:119], a[0:3], a[68:71], v[20:23]
v_exp_f32_e32 v46, v92
Mv 116,4,76
Rb 52,1,62464
s_add_u32 m0, s36, 69632
Mv 112,4,72
buffer_load_dwordx4 v3, s[20:23], s56 offen offset:0 sc1 lds
Mv 112,8,80
Rb 56,1,63488
v_add_f32_e32 v14, v14, v29
Mv 116,8,84
v_exp_f32_e32 v26, v93
Mv 116,12,92
Rb 60,1,64512
s_waitcnt lgkmcnt(8)
Mv 112,12,88
v_exp_f32_e32 v47, v94
Mv 112,16,96
v_exp_f32_e32 v27, v95
Mv 116,16,100
Cv 43,43,29
v_add_f32_e32 v15, v15, v44
Mv 116,20,108
v_add_f32_e32 v15, v15, v24
Cv 44,44,24
Mv 112,20,104
buffer_load_dwordx4 v3, s[20:23], s56 offen offset:1024 sc1 lds
Mv 112,24,112
v_add_f32_e32 v15, v15, v45
v_add_f32_e32 v15, v15, v25
Mv 116,24,116
Cv 45,45,25
v_add_f32_e32 v15, v15, v46
Mv 116,28,124
v_add_f32_e32 v15, v15, v26
Cv 46,46,26
Mv 112,28,120
s_waitcnt vmcnt(3) lgkmcnt(0)
s_barrier
v_mfma_f32_16x16x32_bf16 v[120:123], a[32:35], a[64:67], v[16:19]
Rb 0,8,32768
v_add_f32_e32 v15, v15, v47
v_mfma_f32_16x16x32_bf16 v[124:127], a[32:35], a[68:71], v[20:23]
Rb 4,8,33792
v_add_f32_e32 v15, v15, v27
Mv 124,36,76
Rb 8,8,34816
Cv 47,47,27
Mv 120,36,72
Rb 12,8,35840
buffer_load_dwordx4 v3, s[20:23], s56 offen offset:2048 sc1 lds
Mv 120,40,80
Rb 16,8,36864
v_max_f32_e32 v11, v14, v15
Mv 124,40,84
Rb 20,8,37888
v_cmp_nge_f32_e32 vcc, s62, v11
Mv 124,44,92
Rb 24,8,38912
s_add_u32 s55, s55, 0x10000
Mv 120,44,88
)" R"(
Rb 28,8,39936
Mv 120,48,96
Rb 32,8,40960
Mv 124,48,100
Rb 36,8,41984
Mv 124,52,108
Rb 40,8,43008
Mv 120,52,104
Rb 44,8,44032
buffer_load_dwordx4 v3, s[20:23], s56 offen offset:3072 sc1 lds
Mv 120,56,112
s_cmp_eq_u32 s57, 2
s_cselect_b32 s55, s59, s55
Mv 124,56,116
s_add_u32 s56, s56, 0x10000
Mv 124,60,124
s_waitcnt lgkmcnt(8)
s_cbranch_vccnz .Lzfx12
.Lzfr12:
Mv 120,60,120
v_cmp_eq_u32_e32 vcc, s3, v4
s_andn2_b64 s[28:29], s[60:61], vcc
s_cbranch_scc1 .Lzsp17
.Lzok17:
Mp 128,0,32
Rb 48,8,45056
v_add_f32_e32 v12, v12, v14
Mp 136,0,36
v_add_f32_e32 v13, v13, v15
s_cmp_le_i32 s57, s58
s_cbranch_scc1 .Lzmk11
.Lzmr11:
Mp 140,4,36
Rb 52,8,46080
s_add_u32 m0, s36, 32768
buffer_load_dwordx4 v[64:67], v1, s[20:23], s66 offen sc0 sc1
Mp 132,4,32
buffer_load_dwordx4 v2, s[20:23], s55 offen offset:0 sc1 lds
Mp 144,8,32
Rb 56,8,47104
v_exp_f32_e32 v48, v96
Mp 152,8,36
buffer_load_dwordx4 a[128:131], v2, s[20:23], s64 offen offset:0 sc0 sc1
Mp 156,12,36
Rb 60,8,48128
s_waitcnt lgkmcnt(8)
Mp 148,12,32
v_exp_f32_e32 v28, v97
Mp 160,16,32
Rb 0,8,49152
buffer_load_dwordx4 a[132:135], v2, s[20:23], s64 offen offset:1024 sc0 sc1
Mp 168,16,36
v_exp_f32_e32 v49, v98
v_add_f32_e32 v14, v48, v28
Mp 172,20,36
Rb 4,8,50176
Cv 48,48,28
Mp 164,20,32
buffer_load_dwordx4 v2, s[20:23], s55 offen offset:1024 sc1 lds
Mp 176,24,32
Rb 8,8,51200
v_exp_f32_e32 v29, v99
Mp 184,24,36
buffer_load_dwordx4 a[136:139], v2, s[20:23], s64 offen offset:2048 sc0 sc1
)" R"(
Mp 188,28,36
Rb 12,8,52224
s_waitcnt lgkmcnt(8)
Mp 180,28,32
v_exp_f32_e32 v50, v104
v_add_f32_e32 v14, v14, v49
Mp 192,32,32
Rb 16,8,53248
buffer_load_dwordx4 a[140:143], v2, s[20:23], s64 offen offset:3072 sc0 sc1
Mp 200,32,36
v_exp_f32_e32 v24, v105
v_add_f32_e32 v14, v14, v29
Mp 204,36,36
Rb 20,8,54272
Cv 49,49,29
Mp 196,36,32
buffer_load_dwordx4 v2, s[20:23], s55 offen offset:2048 sc1 lds
Mp 208,40,32
Rb 24,8,55296
v_exp_f32_e32 v51, v106
Mp 216,40,36
buffer_load_dwordx4 a[144:147], v3, s[20:23], s64 offen offset:0 sc0 sc1
Mp 220,44,36
Rb 28,8,56320
s_waitcnt lgkmcnt(8)
Mp 212,44,32
v_exp_f32_e32 v25, v107
v_add_f32_e32 v14, v14, v50
Mp 224,48,32
Rb 32,8,57344
buffer_load_dwordx4 a[148:151], v3, s[20:23], s64 offen offset:1024 sc0 sc1
Mp 232,48,36
v_exp_f32_e32 v52, v100
v_add_f32_e32 v14, v14, v24
Mp 236,52,36
Rb 36,8,58368
Cv 50,50,24
Mp 228,52,32
buffer_load_dwordx4 v2, s[20:23], s55 offen offset:3072 sc1 lds
Mp 240,56,32
Rb 40,8,59392
v_exp_f32_e32 v26, v101
Mp 248,56,36
buffer_load_dwordx4 a[152:155], v3, s[20:23], s64 offen offset:2048 sc0 sc1
Mp 252,60,36
Rb 44,8,60416
s_waitcnt lgkmcnt(8)
Mp 244,60,32
v_exp_f32_e32 v53, v102
v_add_f32_e32 v14, v14, v51
Mp 128,0,40
Rb 48,8,61440
buffer_load_dwordx4 a[156:159], v3, s[20:23], s64 offen offset:3072 sc0 sc1
Mp 136,0,44
v_exp_f32_e32 v27, v103
v_add_f32_e32 v14, v14, v25
Mp 140,4,44
Rb 52,8,62464
s_add_u32 m0, s36, 36864
Mp 132,4,40
buffer_load_dwordx4 v3, s[20:23], s55 offen offset:0 sc1 lds
Mp 144,8,40
Rb 56,8,63488
v_exp_f32_e32 v54, v108
)" R"(
Mp 152,8,44
s_add_u32 s64, s64, 0x10000
buffer_load_dwordx4 a[160:163], v2, s[20:23], s64 offen offset:0 sc0 sc1
Mp 156,12,44
Rb 60,8,64512
s_waitcnt lgkmcnt(8)
Mp 148,12,40
v_exp_f32_e32 v28, v109
Cv 51,51,25
Mp 160,16,40
buffer_load_dwordx4 a[164:167], v2, s[20:23], s64 offen offset:1024 sc0 sc1
Mp 168,16,44
v_exp_f32_e32 v55, v110
v_add_f32_e32 v15, v52, v26
Mp 172,20,44
v_exp_f32_e32 v29, v111
Mp 164,20,40
buffer_load_dwordx4 v3, s[20:23], s55 offen offset:1024 sc1 lds
Mp 176,24,40
Cv 52,52,26
v_add_f32_e32 v15, v15, v53
v_add_f32_e32 v15, v15, v27
Mp 184,24,44
buffer_load_dwordx4 a[168:171], v2, s[20:23], s64 offen offset:2048 sc0 sc1
Mp 188,28,44
Cv 53,53,27
v_add_f32_e32 v15, v15, v54
Mp 180,28,40
v_add_f32_e32 v15, v15, v28
s_waitcnt vmcnt(18) lgkmcnt(0)
s_barrier
Mp 192,32,40
Rb 0,8,0
buffer_load_dwordx4 a[172:175], v2, s[20:23], s64 offen offset:3072 sc0 sc1
Mp 200,32,44
Rb 4,8,16384
Cv 54,54,28
v_add_f32_e32 v15, v15, v55
Mp 204,36,44
Rb 8,8,1024
v_add_f32_e32 v15, v15, v29
Mp 196,36,40
Rb 12,8,17408
buffer_load_dwordx4 v3, s[20:23], s55 offen offset:2048 sc1 lds
Mp 208,40,40
Rb 16,8,2048
Cv 55,55,29
s_sub_u32 s57, s57, 1
Mp 216,40,44
Rb 20,8,18432
buffer_load_dwordx4 a[176:179], v3, s[20:23], s64 offen offset:0 sc0 sc1
Mp 220,44,44
Rb 24,8,3072
Mp 212,44,40
Rb 28,8,19456
Mp 224,48,40
Rb 32,8,4096
buffer_load_dwordx4 a[180:183], v3, s[20:23], s64 offen offset:1024 sc0 sc1
Mp 232,48,44
Rb 36,8,20480
Mp 236,52,44
Rb 40,8,5120
Mp 228,52,40
Rb 44,8,21504
buffer_load_dwordx4 v3, s[20:23], s55 offen offset:3072 sc1 lds
)" R"(
Mp 240,56,40
buffer_load_dwordx4 a[188:191], v3, s[20:23], s64 offen offset:3072 sc0 sc1
Mp 248,56,44
buffer_load_dwordx4 a[184:187], v3, s[20:23], s64 offen offset:2048 sc0 sc1
Mp 252,60,44
Mp 244,60,40
s_waitcnt vmcnt(18)
v_cmp_neq_f32_e32 vcc, v16, v66
v_cmp_neq_f32_e64 s[28:29], v20, v67
s_or_b64 vcc, vcc, s[28:29]
s_cbranch_scc1 .LzMRG
.LzMRR:
Mv 128,194,128
v_exp_f32_e32 v56, v112
Mv 136,194,132
v_exp_f32_e32 v26, v113
v_add_f32_e32 v12, v12, v64
Mv 132,192,128
s_add_u32 m0, s36, 131072
s_add_u32 s40, s67, 0x20000
v_add_f32_e32 v13, v13, v65
Mv 140,192,132
buffer_load_dwordx4 v2, s[20:23], s40 offen offset:0 sc1 lds
Mv 144,194,136
v_exp_f32_e32 v57, v114
v_add_f32_e32 v14, v14, v56
Mv 152,194,140
v_exp_f32_e32 v27, v115
v_add_f32_e32 v14, v14, v26
Mv 148,192,136
Cv 56,56,26
s_waitcnt vmcnt(13)
Mv 156,192,140
buffer_load_dwordx4 v2, s[20:23], s40 offen offset:1024 sc1 lds
v_add_f32_e32 v14, v14, v57
Mv 160,194,144
v_exp_f32_e32 v58, v120
v_add_f32_e32 v14, v14, v27
Mv 168,194,148
v_exp_f32_e32 v28, v121
Mv 164,192,144
v_exp_f32_e32 v59, v122
Cv 57,57,27
Mv 172,192,148
buffer_load_dwordx4 v2, s[20:23], s40 offen offset:2048 sc1 lds
v_add_f32_e32 v14, v14, v58
Mv 176,194,152
v_exp_f32_e32 v29, v123
Mv 184,194,156
v_exp_f32_e32 v60, v116
v_add_f32_e32 v14, v14, v28
Mv 180,192,152
v_exp_f32_e32 v24, v117
s_waitcnt vmcnt(9)
Mv 188,192,156
buffer_load_dwordx4 v2, s[20:23], s40 offen offset:3072 sc1 lds
Cv 58,58,28
Mv 192,194,160
v_exp_f32_e32 v61, v118
Mv 200,194,164
v_exp_f32_e32 v25, v119
v_add_f32_e32 v14, v14, v59
Mv 196,192,160
s_add_u32 m0, s36, 135168
)" R"(
v_exp_f32_e32 v62, v124
Mv 204,192,164
buffer_load_dwordx4 v3, s[20:23], s40 offen offset:0 sc1 lds
Mv 208,194,168
v_exp_f32_e32 v26, v125
v_add_f32_e32 v14, v14, v29
Mv 216,194,172
v_exp_f32_e32 v63, v126
Cv 59,59,29
Mv 212,192,168
v_add_f32_e32 v15, v15, v60
s_waitcnt vmcnt(5)
Mv 220,192,172
buffer_load_dwordx4 v3, s[20:23], s40 offen offset:1024 sc1 lds
v_add_f32_e32 v15, v15, v24
Mv 224,194,176
v_exp_f32_e32 v27, v127
Cv 60,60,24
Mv 232,194,180
v_add_f32_e32 v15, v15, v61
v_add_f32_e32 v15, v15, v25
Mv 228,192,176
Cv 61,61,25
v_add_f32_e32 v15, v15, v62
v_add_f32_e32 v15, v15, v26
Mv 236,192,180
buffer_load_dwordx4 v3, s[20:23], s40 offen offset:2048 sc1 lds
Cv 62,62,26
Mv 240,194,184
v_add_f32_e32 v15, v15, v63
v_add_f32_e32 v15, v15, v27
Mv 248,194,188
s_add_u32 s40, s67, 0x30000
Cv 63,63,27
v_max_f32_e32 v11, v14, v15
Mv 244,192,184
s_add_u32 m0, s36, 98304
v_cmp_nge_f32_e32 vcc, s62, v11
s_waitcnt lgkmcnt(8)
Mv 252,192,188
buffer_load_dwordx4 v2, s[20:23], s40 offen offset:0 sc1 lds
s_cbranch_vccnz .Lzfx13
.Lzfr13:
Mp 128,0,48
s_mov_b32 s56, 0xc108000
buffer_load_dword v4, v7, s[20:23], s56 offen offset:4 sc0 sc1
Rb 48,8,6144
v_add_f32_e32 v12, v12, v14
Mp 136,0,52
v_add_f32_e32 v13, v13, v15
v_mov_b32 v24, v12
Mp 136,4,60
Rb 52,8,22528
v_permlane32_swap_b32_e32 v12, v24
Mp 128,4,56
buffer_load_dwordx4 v2, s[20:23], s40 offen offset:1024 sc1 lds
Mp 132,8,48
Rb 56,8,7168
v_add_f32_e32 v12, v12, v24
Mp 140,8,52
v_mov_b32 v24, v12
v_mov_b32 v25, v13
Mp 140,12,60
Rb 60,8,23552
)" R"(
s_waitcnt lgkmcnt(8)
Mp 132,12,56
buffer_load_dwordx4 v2, s[20:23], s40 offen offset:2048 sc1 lds
Mp 144,16,48
Rb 0,8,8192
v_permlane16_swap_b32_e32 v12, v24
Mp 152,16,52
v_add_f32_e32 v12, v12, v24
v_permlane32_swap_b32_e32 v13, v25
Mp 152,20,60
Rb 4,8,24576
v_rcp_f32_e32 v12, v12
Mp 144,20,56
buffer_load_dwordx4 v2, s[20:23], s40 offen offset:3072 sc1 lds
Mp 148,24,48
Rb 8,8,9216
v_add_f32_e32 v13, v13, v25
Mp 156,24,52
s_add_u32 m0, s36, 102400
v_mov_b32 v25, v13
Mp 156,28,60
Rb 12,8,25600
s_waitcnt lgkmcnt(8)
Mp 148,28,56
buffer_load_dwordx4 v3, s[20:23], s40 offen offset:0 sc1 lds
v_permlane16_swap_b32_e32 v13, v25
Mp 160,32,48
Rb 16,8,10240
v_add_f32_e32 v13, v13, v25
Mp 168,32,52
v_rcp_f32_e32 v13, v13
Mp 168,36,60
Rb 20,8,26624
v_mul_f32_e32 v128, v12, v128
Mp 160,36,56
buffer_load_dwordx4 v3, s[20:23], s40 offen offset:1024 sc1 lds
Mp 164,40,48
Rb 24,8,11264
v_mul_f32_e32 v129, v12, v129
Mp 172,40,52
Cv 128,128,129
v_mul_f32_e32 v130, v12, v130
v_mul_f32_e32 v131, v12, v131
Mp 172,44,60
Rb 28,8,27648
s_waitcnt lgkmcnt(8)
Mp 164,44,56
buffer_load_dwordx4 v3, s[20:23], s40 offen offset:2048 sc1 lds
Mp 176,48,48
Rb 32,8,12288
Cv 129,130,131
Mp 184,48,52
v_mul_f32_e32 v132, v12, v132
v_mul_f32_e32 v133, v12, v133
Mp 184,52,60
Rb 36,8,28672
Cv 130,132,133
v_mul_f32_e32 v134, v12, v134
Mp 176,52,56
v_mul_f32_e32 v135, v12, v135
Cv 131,134,135
Mp 180,56,48
Rb 40,8,13312
v_mul_f32_e32 v136, v13, v136
)" R"(
Mp 188,56,52
v_mul_f32_e32 v137, v13, v137
Cv 136,136,137
Mp 188,60,60
Rb 44,8,29696
s_waitcnt lgkmcnt(8)
Mp 180,60,56
v_mul_f32_e32 v138, v13, v138
v_mul_f32_e32 v139, v13, v139
Cv 137,138,139
Mp 192,0,48
Rb 48,8,14336
v_mul_f32_e32 v140, v13, v140
Mp 200,0,52
v_mul_f32_e32 v141, v13, v141
Cv 138,140,141
Mp 200,4,60
Rb 52,8,30720
v_mul_f32_e32 v142, v13, v142
Mp 192,4,56
v_mul_f32_e32 v143, v13, v143
Cv 139,142,143
Mp 196,8,48
Rb 56,8,15360
v_mul_f32_e32 v144, v12, v144
Mp 204,8,52
v_mul_f32_e32 v145, v12, v145
Cv 144,144,145
v_mul_f32_e32 v146, v12, v146
Mp 204,12,60
Rb 60,8,31744
s_waitcnt lgkmcnt(8)
Mp 196,12,56
v_mul_f32_e32 v147, v12, v147
Cv 145,146,147
Mp 208,16,48
Rb 0,1,0
v_mul_f32_e32 v148, v12, v148
Mp 216,16,52
v_mul_f32_e32 v149, v12, v149
Cv 146,148,149
Mp 216,20,60
Rb 4,1,1024
v_mul_f32_e32 v150, v12, v150
v_mul_f32_e32 v151, v12, v151
Mp 208,20,56
Cv 147,150,151
v_mul_f32_e32 v152, v13, v152
Mp 212,24,48
Rb 8,1,2048
v_mul_f32_e32 v153, v13, v153
Mp 220,24,52
Cv 152,152,153
v_mul_f32_e32 v154, v13, v154
Mp 220,28,60
Rb 12,1,3072
s_waitcnt vmcnt(6) lgkmcnt(4)
s_barrier
Mp 212,28,56
s_add_u32 s40, s67, 0x40000
v_mul_f32_e32 v155, v13, v155
Cv 153,154,155
v_cmp_eq_u32_e32 vcc, s3, v4
s_andn2_b64 s[28:29], 1, vcc
s_cbranch_scc1 .Lzsp18
)" R"(
.Lzok18:
Mp 224,32,48
Rb 16,1,4096
s_add_u32 m0, s36, 65536
buffer_load_dword v4, v7, s[20:23], s56 offen sc0 sc1
Mp 232,32,52
buffer_load_dwordx4 v2, s[20:23], s40 offen offset:0 sc1 lds
Mp 232,36,60
Rb 20,1,5120
v_mul_f32_e32 v156, v13, v156
Mp 224,36,56
v_mul_f32_e32 v157, v13, v157
Cv 154,156,157
Mp 228,40,48
Rb 24,1,6144
v_mul_f32_e32 v158, v13, v158
Mp 236,40,52
v_mul_f32_e32 v159, v13, v159
Cv 155,158,159
v_mul_f32_e32 v160, v12, v160
Mp 236,44,60
Rb 28,1,8192
Mp 228,44,56
buffer_load_dwordx4 v2, s[20:23], s40 offen offset:1024 sc1 lds
Mp 240,48,48
Rb 32,1,9216
v_mul_f32_e32 v161, v12, v161
Mp 248,48,52
Cv 160,160,161
v_mul_f32_e32 v162, v12, v162
Mp 248,52,60
Rb 36,1,10240
v_mul_f32_e32 v163, v12, v163
Cv 161,162,163
Mp 240,52,56
v_mul_f32_e32 v164, v12, v164
v_mul_f32_e32 v165, v12, v165
Mp 244,56,48
Rb 40,1,11264
Cv 162,164,165
Mp 252,56,52
buffer_load_dwordx4 v2, s[20:23], s40 offen offset:2048 sc1 lds
Mp 252,60,60
Rb 44,1,12288
s_waitcnt lgkmcnt(8)
Mp 244,60,56
v_mul_f32_e32 v166, v12, v166
v_mul_f32_e32 v167, v12, v167
Cv 163,166,167
v_mfma_f32_16x16x32_bf16 v[32:35], a[0:3], v[128:131], 0
Rb 48,1,13312
v_mul_f32_e32 v168, v13, v168
v_mfma_f32_16x16x32_bf16 v[40:43], a[0:3], v[136:139], 0
v_mul_f32_e32 v169, v13, v169
Cv 168,168,169
v_mfma_f32_16x16x32_bf16 v[44:47], a[4:7], v[136:139], 0
Rb 52,1,14336
v_mul_f32_e32 v170, v13, v170
v_mfma_f32_16x16x32_bf16 v[36:39], a[4:7], v[128:131], 0
buffer_load_dwordx4 v2, s[20:23], s40 offen offset:3072 sc1 lds
v_mfma_f32_16x16x32_bf16 v[48:51], a[8:11], v[128:131], 0
Rb 56,1,16384
v_mul_f32_e32 v171, v13, v171
v_mfma_f32_16x16x32_bf16 v[56:59], a[8:11], v[136:139], 0
)" R"(
Cv 169,170,171
v_mul_f32_e32 v172, v13, v172
v_mul_f32_e32 v173, v13, v173
v_mfma_f32_16x16x32_bf16 v[60:63], a[12:15], v[136:139], 0
Rb 60,1,17408
s_waitcnt lgkmcnt(8)
v_mfma_f32_16x16x32_bf16 v[52:55], a[12:15], v[128:131], 0
Cv 170,172,173
v_mul_f32_e32 v174, v13, v174
v_mfma_f32_16x16x32_bf16 v[64:67], a[16:19], v[128:131], 0
Rb 0,1,18432
s_add_u32 m0, s36, 69632
v_mfma_f32_16x16x32_bf16 v[72:75], a[16:19], v[136:139], 0
buffer_load_dwordx4 v3, s[20:23], s40 offen offset:0 sc1 lds
v_mfma_f32_16x16x32_bf16 v[76:79], a[20:23], v[136:139], 0
Rb 4,1,19456
v_mul_f32_e32 v175, v13, v175
Cv 171,174,175
v_mfma_f32_16x16x32_bf16 v[68:71], a[20:23], v[128:131], 0
v_mul_f32_e32 v176, v12, v176
v_mul_f32_e32 v177, v12, v177
v_mfma_f32_16x16x32_bf16 v[80:83], a[24:27], v[128:131], 0
Rb 8,1,20480
Cv 176,176,177
v_mfma_f32_16x16x32_bf16 v[88:91], a[24:27], v[136:139], 0
v_mul_f32_e32 v178, v12, v178
v_mul_f32_e32 v179, v12, v179
v_mfma_f32_16x16x32_bf16 v[92:95], a[28:31], v[136:139], 0
Rb 12,1,21504
s_waitcnt lgkmcnt(8)
v_mfma_f32_16x16x32_bf16 v[84:87], a[28:31], v[128:131], 0
buffer_load_dwordx4 v3, s[20:23], s40 offen offset:1024 sc1 lds
Cv 177,178,179
Mp 32,32,144
Rb 16,1,22528
v_mul_f32_e32 v180, v12, v180
Mp 40,32,152
v_mul_f32_e32 v181, v12, v181
Cv 178,180,181
Mp 44,36,152
Rb 20,1,24576
v_mul_f32_e32 v182, v12, v182
Mp 36,36,144
v_mul_f32_e32 v183, v12, v183
Cv 179,182,183
Mp 48,40,144
Rb 24,1,25600
v_mul_f32_e32 v184, v13, v184
Mp 56,40,152
buffer_load_dwordx4 v3, s[20:23], s40 offen offset:2048 sc1 lds
v_mul_f32_e32 v185, v13, v185
Mp 60,44,152
Rb 28,1,26624
s_waitcnt lgkmcnt(8)
Mp 52,44,144
Cv 184,184,185
v_mul_f32_e32 v186, v13, v186
Mp 64,48,144
Rb 32,1,27648
v_mul_f32_e32 v187, v13, v187
Mp 72,48,152
Cv 185,186,187
v_mul_f32_e32 v188, v13, v188
Mp 76,52,152
)" R"(
Rb 36,1,28672
v_mul_f32_e32 v189, v13, v189
Cv 186,188,189
Mp 68,52,144
v_mul_f32_e32 v190, v13, v190
v_mul_f32_e32 v191, v13, v191
Mp 80,56,144
Rb 40,1,29696
Cv 187,190,191
Mp 88,56,152
v_mul_f32_e32 v192, v12, v192
v_mul_f32_e32 v193, v12, v193
Mp 92,60,152
Rb 44,1,30720
s_waitcnt lgkmcnt(8)
Mp 84,60,144
Cv 192,192,193
v_mul_f32_e32 v194, v12, v194
v_mul_f32_e32 v195, v12, v195
Mp 32,0,160
Rb 48,1,32768
Cv 193,194,195
Mp 40,0,168
v_mul_f32_e32 v196, v12, v196
v_mul_f32_e32 v197, v12, v197
Mp 44,4,168
Rb 52,1,33792
Cv 194,196,197
Mp 36,4,160
v_mul_f32_e32 v198, v12, v198
v_mul_f32_e32 v199, v12, v199
Mp 48,8,160
Rb 56,1,34816
Cv 195,198,199
Mp 56,8,168
v_mul_f32_e32 v200, v13, v200
v_mul_f32_e32 v201, v13, v201
Cv 200,200,201
Mp 60,12,168
Rb 60,1,35840
s_waitcnt lgkmcnt(4)
s_barrier
Mp 52,12,160
s_add_u32 s40, s67, 0x50000
v_mul_f32_e32 v202, v13, v202
Mp 64,16,160
Rb 0,1,36864
s_add_u32 m0, s36, 0
Mp 72,16,168
buffer_load_dwordx4 v2, s[20:23], s40 offen offset:0 sc1 lds
Mp 76,20,168
Rb 4,1,37888
v_mul_f32_e32 v203, v13, v203
Cv 201,202,203
Mp 68,20,160
v_mul_f32_e32 v204, v13, v204
v_mul_f32_e32 v205, v13, v205
Mp 80,24,160
Rb 8,1,38912
Cv 202,204,205
Mp 88,24,168
v_mul_f32_e32 v206, v13, v206
v_mul_f32_e32 v207, v13, v207
Mp 92,28,168
Rb 12,1,40960
)" R"(
Mp 84,28,160
buffer_load_dwordx4 v2, s[20:23], s40 offen offset:1024 sc1 lds
Cv 203,206,207
Mp 32,32,176
Rb 16,1,41984
v_mul_f32_e32 v208, v12, v208
Mp 40,32,184
v_mul_f32_e32 v209, v12, v209
Cv 208,208,209
Mp 44,36,184
Rb 20,1,43008
v_mul_f32_e32 v210, v12, v210
Mp 36,36,176
v_mul_f32_e32 v211, v12, v211
Cv 209,210,211
Mp 48,40,176
Rb 24,1,44032
v_mul_f32_e32 v212, v12, v212
Mp 56,40,184
buffer_load_dwordx4 v2, s[20:23], s40 offen offset:2048 sc1 lds
v_mul_f32_e32 v213, v12, v213
Mp 60,44,184
Rb 28,1,45056
s_waitcnt lgkmcnt(8)
Mp 52,44,176
Cv 210,212,213
v_mul_f32_e32 v214, v12, v214
Mp 64,48,176
Rb 32,1,46080
v_mul_f32_e32 v215, v12, v215
Mp 72,48,184
Cv 211,214,215
v_mul_f32_e32 v216, v13, v216
Mp 76,52,184
Rb 36,1,47104
v_mul_f32_e32 v217, v13, v217
Cv 216,216,217
Mp 68,52,176
buffer_load_dwordx4 v2, s[20:23], s40 offen offset:3072 sc1 lds
Mp 80,56,176
Rb 40,1,49152
v_mul_f32_e32 v218, v13, v218
Mp 88,56,184
v_mul_f32_e32 v219, v13, v219
Cv 217,218,219
Mp 92,60,184
Rb 44,1,50176
s_waitcnt lgkmcnt(8)
Mp 84,60,176
v_mul_f32_e32 v220, v13, v220
v_mul_f32_e32 v221, v13, v221
Cv 218,220,221
Mp 32,0,192
Rb 48,1,51200
s_add_u32 m0, s36, 4096
Mp 40,0,200
buffer_load_dwordx4 v3, s[20:23], s40 offen offset:0 sc1 lds
Mp 44,4,200
Rb 52,1,52224
v_mul_f32_e32 v222, v13, v222
Mp 36,4,192
v_mul_f32_e32 v223, v13, v223
Cv 219,222,223
Mp 48,8,192
)" R"(
Rb 56,1,53248
v_mul_f32_e32 v224, v12, v224
Mp 56,8,200
v_mul_f32_e32 v225, v12, v225
Cv 224,224,225
v_mul_f32_e32 v226, v12, v226
Mp 60,12,200
Rb 60,1,54272
s_waitcnt lgkmcnt(8)
Mp 52,12,192
buffer_load_dwordx4 v3, s[20:23], s40 offen offset:1024 sc1 lds
Mp 64,16,192
Rb 0,1,55296
v_mul_f32_e32 v227, v12, v227
Mp 72,16,200
Cv 225,226,227
v_mul_f32_e32 v228, v12, v228
Mp 76,20,200
Rb 4,1,57344
v_mul_f32_e32 v229, v12, v229
Cv 226,228,229
Mp 68,20,192
v_mul_f32_e32 v230, v12, v230
v_mul_f32_e32 v231, v12, v231
Mp 80,24,192
Rb 8,1,58368
Cv 227,230,231
Mp 88,24,200
buffer_load_dwordx4 v3, s[20:23], s40 offen offset:2048 sc1 lds
Mp 92,28,200
Rb 12,1,59392
s_waitcnt lgkmcnt(8)
Mp 84,28,192
v_mul_f32_e32 v232, v13, v232
v_mul_f32_e32 v233, v13, v233
Cv 232,232,233
Mp 32,32,208
Rb 16,1,60416
v_mul_f32_e32 v234, v13, v234
Mp 40,32,216
v_mul_f32_e32 v235, v13, v235
Cv 233,234,235
Mp 44,36,216
Rb 20,1,61440
v_mul_f32_e32 v236, v13, v236
Mp 36,36,208
v_mul_f32_e32 v237, v13, v237
Cv 234,236,237
Mp 48,40,208
Rb 24,1,62464
v_mul_f32_e32 v238, v13, v238
Mp 56,40,216
v_mul_f32_e32 v239, v13, v239
Cv 235,238,239
v_mul_f32_e32 v240, v12, v240
Mp 60,44,216
Rb 28,1,63488
s_waitcnt lgkmcnt(8)
Mp 52,44,208
v_mul_f32_e32 v241, v12, v241
Cv 240,240,241
Mp 64,48,208
Rb 32,10,0
v_mul_f32_e32 v242, v12, v242
)" R"(
Mp 72,48,216
v_mul_f32_e32 v243, v12, v243
Cv 241,242,243
Mp 76,52,216
Rb 36,10,1024
v_mul_f32_e32 v244, v12, v244
v_mul_f32_e32 v245, v12, v245
Mp 68,52,208
Cv 242,244,245
v_mul_f32_e32 v246, v12, v246
Mp 80,56,208
Rb 40,10,2048
v_mul_f32_e32 v247, v12, v247
Mp 88,56,216
Cv 243,246,247
v_mul_f32_e32 v248, v13, v248
Mp 92,60,216
Rb 44,10,3072
s_waitcnt vmcnt(14) lgkmcnt(4)
s_barrier
Mp 84,60,208
s_add_u32 s40, s67, 0x60000
v_mul_f32_e32 v249, v13, v249
Cv 248,248,249
v_readfirstlane_b32 s63, v4
Mp 32,0,224
Rb 48,10,4096
s_add_u32 m0, s36, 32768
Mp 40,0,232
buffer_load_dwordx4 v2, s[20:23], s40 offen offset:0 sc1 lds
Mp 44,4,232
Rb 52,10,5120
v_mul_f32_e32 v250, v13, v250
Mp 36,4,224
v_mul_f32_e32 v251, v13, v251
Cv 249,250,251
Mp 48,8,224
Rb 56,10,6144
v_mul_f32_e32 v252, v13, v252
Mp 56,8,232
v_mul_f32_e32 v253, v13, v253
Cv 250,252,253
v_mul_f32_e32 v254, v13, v254
Mp 60,12,232
Rb 60,10,8192
Mp 52,12,224
buffer_load_dwordx4 v2, s[20:23], s40 offen offset:1024 sc1 lds
Mp 64,16,224
Rb 0,10,9216
v_mul_f32_e32 v255, v13, v255
Mp 72,16,232
Cv 251,254,255
Mp 76,20,232
Rb 4,10,10240
Mp 68,20,224
Mp 80,24,224
Rb 8,10,11264
Mp 88,24,232
buffer_load_dwordx4 v2, s[20:23], s40 offen offset:2048 sc1 lds
Mp 92,28,232
Rb 12,10,12288
s_waitcnt lgkmcnt(8)
Mp 84,28,224
Mp 32,32,240
Rb 16,10,13312
)" R"(
Mp 40,32,248
Mp 44,36,248
Rb 20,10,14336
Mp 36,36,240
buffer_load_dwordx4 v2, s[20:23], s40 offen offset:3072 sc1 lds
Mp 48,40,240
Rb 24,10,16384
Mp 56,40,248
Mp 60,44,248
Rb 28,10,17408
s_waitcnt lgkmcnt(8)
Mp 52,44,240
Mp 64,48,240
Rb 32,10,18432
s_add_u32 m0, s36, 36864
Mp 72,48,248
buffer_load_dwordx4 v3, s[20:23], s40 offen offset:0 sc1 lds
Mp 76,52,248
Rb 36,10,19456
Mp 68,52,240
Mp 80,56,240
Rb 40,10,20480
Mp 88,56,248
Mp 92,60,248
Rb 44,10,21504
s_waitcnt lgkmcnt(8)
Mp 84,60,240
buffer_load_dwordx4 v3, s[20:23], s40 offen offset:1024 sc1 lds
v_mfma_f32_16x16x32_bf16 v[96:99], a[0:3], v[128:131], 0
Rb 48,10,22528
v_mfma_f32_16x16x32_bf16 v[104:107], a[0:3], v[136:139], 0
Mp 104,4,152
Rb 52,10,24576
Mp 96,4,144
Mp 96,8,160
Rb 56,10,25600
Cv 32,32,33
Mp 104,8,168
buffer_load_dwordx4 v3, s[20:23], s40 offen offset:2048 sc1 lds
Mp 104,12,184
Rb 60,10,26624
s_waitcnt lgkmcnt(8)
Mp 96,12,176
Cv 33,34,35
Cv 34,36,37
Mp 96,16,192
Rb 0,10,27648
Cv 35,38,39
Mp 104,16,200
s_add_u32 s29, s73, 0x0
buffer_store_dwordx4 v[32:35], v1, s[20:23], s29 offen sc0 sc1
Mp 104,20,216
Rb 4,10,28672
Mp 96,20,208
Mp 96,24,224
Rb 8,10,29696
Cv 40,40,41
Mp 104,24,232
Cv 41,42,43
Cv 42,44,45
Mp 104,28,248
Rb 12,10,30720
s_waitcnt lgkmcnt(8)
Mp 96,28,240
)" R"(
Cv 43,46,47
v_mfma_f32_16x16x32_bf16 v[100:103], a[32:35], v[128:131], 0
Rb 16,8,32768
v_mfma_f32_16x16x32_bf16 v[108:111], a[32:35], v[136:139], 0
s_add_u32 s29, s73, 0x400
buffer_store_dwordx4 v[40:43], v1, s[20:23], s29 offen sc0 sc1
Mp 108,36,152
Rb 20,8,33792
Mp 100,36,144
Mp 100,40,160
Rb 24,8,34816
Cv 48,48,49
Mp 108,40,168
Cv 49,50,51
Cv 50,52,53
Mp 108,44,184
Rb 28,8,35840
s_waitcnt vmcnt(16) lgkmcnt(4)
s_barrier
Mp 100,44,176
s_add_u32 s40, s67, 0x70000
Cv 51,54,55
Mp 100,48,192
Rb 32,8,36864
s_add_u32 m0, s36, 131072
Mp 108,48,200
buffer_load_dwordx4 v2, s[20:23], s40 offen offset:0 sc1 lds
Mp 108,52,216
Rb 36,8,37888
Mp 100,52,208
s_add_u32 s29, s73, 0x800
buffer_store_dwordx4 v[48:51], v1, s[20:23], s29 offen sc0 sc1
Mp 100,56,224
Rb 40,8,38912
Cv 56,56,57
Mp 108,56,232
Cv 57,58,59
Cv 58,60,61
Mp 108,60,248
Rb 44,8,40960
Mp 100,60,240
buffer_load_dwordx4 v2, s[20:23], s40 offen offset:1024 sc1 lds
v_mfma_f32_16x16x32_bf16 v[112:115], a[0:3], v[128:131], 0
Rb 48,8,41984
Cv 59,62,63
v_mfma_f32_16x16x32_bf16 v[120:123], a[0:3], v[136:139], 0
s_add_u32 s29, s73, 0xc00
buffer_store_dwordx4 v[56:59], v1, s[20:23], s29 offen sc0 sc1
Mp 120,4,152
Rb 52,8,43008
Mp 112,4,144
Mp 112,8,160
Rb 56,8,44032
Cv 96,96,97
Mp 120,8,168
buffer_load_dwordx4 v2, s[20:23], s40 offen offset:2048 sc1 lds
Mp 120,12,184
Rb 60,8,45056
s_waitcnt lgkmcnt(8)
Mp 112,12,176
Cv 97,98,99
Cv 98,100,101
Mp 112,16,192
Rb 0,8,46080
Cv 99,102,103
)" R"(
Mp 120,16,200
s_add_u32 s29, s73, 0x4400
buffer_store_dwordx4 v[96:99], v1, s[20:23], s29 offen sc0 sc1
Mp 120,20,216
Rb 4,8,47104
Mp 112,20,208
buffer_load_dwordx4 v2, s[20:23], s40 offen offset:3072 sc1 lds
Mp 112,24,224
Rb 8,8,49152
Cv 64,64,65
Mp 120,24,232
Cv 65,66,67
Cv 66,68,69
Mp 120,28,248
Rb 12,8,50176
s_waitcnt lgkmcnt(8)
Mp 112,28,240
Cv 67,70,71
v_mfma_f32_16x16x32_bf16 v[116:119], a[32:35], v[128:131], 0
Rb 16,8,51200
s_add_u32 m0, s36, 135168
v_mfma_f32_16x16x32_bf16 v[124:127], a[32:35], v[136:139], 0
buffer_load_dwordx4 v3, s[20:23], s40 offen offset:0 sc1 lds
Mp 124,36,152
Rb 20,8,52224
Mp 116,36,144
s_add_u32 s29, s73, 0x1000
buffer_store_dwordx4 v[64:67], v1, s[20:23], s29 offen sc0 sc1
Mp 116,40,160
Rb 24,8,53248
Cv 104,104,105
Mp 124,40,168
Cv 105,106,107
Cv 106,108,109
Mp 124,44,184
Rb 28,8,54272
s_waitcnt lgkmcnt(8)
Mp 116,44,176
buffer_load_dwordx4 v3, s[20:23], s40 offen offset:1024 sc1 lds
Mp 116,48,192
Rb 32,8,55296
Cv 107,110,111
Mp 124,48,200
s_add_u32 s29, s73, 0x4800
buffer_store_dwordx4 v[104:107], v1, s[20:23], s29 offen sc0 sc1
Mp 124,52,216
Rb 36,8,57344
Mp 116,52,208
Mp 116,56,224
Rb 40,8,58368
Cv 72,72,73
Mp 124,56,232
buffer_load_dwordx4 v3, s[20:23], s40 offen offset:2048 sc1 lds
Mp 124,60,248
Rb 44,8,59392
s_waitcnt lgkmcnt(8)
Mp 116,60,240
Cv 73,74,75
Cv 74,76,77
v_mfma_f32_16x16x32_bf16 v[96:99], a[0:3], v[128:131], 0
Rb 48,8,60416
Cv 75,78,79
v_mfma_f32_16x16x32_bf16 v[104:107], a[0:3], v[136:139], 0
s_add_u32 s29, s73, 0x1400
)" R"(
buffer_store_dwordx4 v[72:75], v1, s[20:23], s29 offen sc0 sc1
Mp 104,4,152
Rb 52,8,61440
Mp 96,4,144
Mp 96,8,160
Rb 56,8,62464
Cv 112,112,113
Mp 104,8,168
Cv 113,114,115
Cv 114,116,117
Mp 104,12,184
Rb 60,8,63488
s_waitcnt lgkmcnt(8)
Mp 96,12,176
Cv 115,118,119
Mp 96,16,192
Rb 0,8,0
Mp 104,16,200
s_add_u32 s29, s73, 0x4c00
buffer_store_dwordx4 v[112:115], v1, s[20:23], s29 offen sc0 sc1
Mp 104,20,216
Rb 4,8,1024
Mp 96,20,208
Mp 96,24,224
Rb 8,8,2048
Cv 80,80,81
Mp 104,24,232
Cv 81,82,83
Cv 82,84,85
Mp 104,28,248
Rb 12,8,3072
s_waitcnt vmcnt(23) lgkmcnt(8)
s_barrier
Mp 96,28,240
Cv 83,86,87
v_mfma_f32_16x16x32_bf16 v[100:103], a[32:35], v[128:131], 0
Rb 16,8,4096
v_mfma_f32_16x16x32_bf16 v[108:111], a[32:35], v[136:139], 0
s_add_u32 s29, s73, 0x1800
buffer_store_dwordx4 v[80:83], v1, s[20:23], s29 offen sc0 sc1
Mp 108,36,152
Rb 20,8,5120
Mp 100,36,144
Mp 100,40,160
Rb 24,8,6144
Cv 120,120,121
Mp 108,40,168
Cv 121,122,123
Cv 122,124,125
Mp 108,44,184
Rb 28,8,8192
s_waitcnt lgkmcnt(8)
Mp 100,44,176
Cv 123,126,127
Mp 100,48,192
Rb 32,8,9216
Mp 108,48,200
s_add_u32 s29, s73, 0x5000
buffer_store_dwordx4 v[120:123], v1, s[20:23], s29 offen sc0 sc1
Mp 108,52,216
Rb 36,8,10240
Mp 100,52,208
Mp 100,56,224
Rb 40,8,11264
Cv 88,88,89
)" R"(
Mp 108,56,232
Cv 89,90,91
Cv 90,92,93
Mp 108,60,248
Rb 44,8,12288
s_waitcnt lgkmcnt(8)
Mp 100,60,240
Cv 91,94,95
v_mfma_f32_16x16x32_bf16 v[112:115], a[0:3], v[128:131], 0
Rb 48,8,13312
v_mfma_f32_16x16x32_bf16 v[120:123], a[0:3], v[136:139], 0
s_add_u32 s29, s73, 0x4000
buffer_store_dwordx4 v[88:91], v1, s[20:23], s29 offen sc0 sc1
Mp 120,4,152
Rb 52,8,14336
Mp 112,4,144
Mp 112,8,160
Rb 56,8,16384
Cv 96,96,97
Mp 120,8,168
Cv 97,98,99
Cv 98,100,101
Mp 120,12,184
Rb 60,8,17408
s_waitcnt lgkmcnt(8)
Mp 112,12,176
Cv 99,102,103
Mp 112,16,192
Rb 0,8,18432
Mp 120,16,200
s_add_u32 s29, s73, 0x5400
buffer_store_dwordx4 v[96:99], v1, s[20:23], s29 offen sc0 sc1
Mp 120,20,216
Rb 4,8,19456
Mp 112,20,208
Mp 112,24,224
Rb 8,8,20480
Mp 120,24,232
Mp 120,28,248
Rb 12,8,21504
s_waitcnt lgkmcnt(8)
Mp 112,28,240
v_mfma_f32_16x16x32_bf16 v[116:119], a[32:35], v[128:131], 0
Rb 16,8,22528
v_mfma_f32_16x16x32_bf16 v[124:127], a[32:35], v[136:139], 0
Mp 124,36,152
Rb 20,8,24576
Mp 116,36,144
Mp 116,40,160
Rb 24,8,25600
Cv 104,104,105
Mp 124,40,168
Cv 105,106,107
Cv 106,108,109
Mp 124,44,184
Rb 28,8,26624
s_waitcnt lgkmcnt(8)
Mp 116,44,176
Cv 107,110,111
Mp 116,48,192
Rb 32,8,27648
Mp 124,48,200
s_add_u32 s29, s73, 0x5800
buffer_store_dwordx4 v[104:107], v1, s[20:23], s29 offen sc0 sc1
)" R"(
Mp 124,52,216
Rb 36,8,28672
Mp 116,52,208
Mp 116,56,224
Rb 40,8,29696
Mp 124,56,232
Mp 124,60,248
Rb 44,8,30720
s_waitcnt lgkmcnt(8)
Mp 116,60,240
v_mfma_f32_16x16x32_bf16 v[96:99], a[0:3], v[128:131], 0
Rb 48,1,0
v_mfma_f32_16x16x32_bf16 v[104:107], a[0:3], v[136:139], 0
Mp 104,4,152
Rb 52,1,1024
Mp 96,4,144
Mp 96,8,160
Rb 56,1,2048
Cv 112,112,113
Mp 104,8,168
Cv 113,114,115
Cv 114,116,117
Mp 104,12,184
Rb 60,1,3072
s_waitcnt vmcnt(21) lgkmcnt(8)
s_barrier
Mp 96,12,176
Cv 115,118,119
Mp 96,16,192
Rb 0,1,4096
Mp 104,16,200
s_add_u32 s29, s73, 0x8000
buffer_store_dwordx4 v[112:115], v1, s[20:23], s29 offen sc0 sc1
Mp 104,20,216
Rb 4,1,5120
Mp 96,20,208
Mp 96,24,224
Rb 8,1,6144
Mp 104,24,232
Mp 104,28,248
Rb 12,1,8192
s_waitcnt lgkmcnt(8)
Mp 96,28,240
v_mfma_f32_16x16x32_bf16 v[100:103], a[32:35], v[128:131], 0
Rb 16,1,9216
v_mfma_f32_16x16x32_bf16 v[108:111], a[32:35], v[136:139], 0
Mp 108,36,152
Rb 20,1,10240
Mp 100,36,144
Mp 100,40,160
Rb 24,1,11264
Cv 120,120,121
Mp 108,40,168
Cv 121,122,123
Cv 122,124,125
Mp 108,44,184
Rb 28,1,12288
s_waitcnt lgkmcnt(8)
Mp 100,44,176
Cv 123,126,127
Mp 100,48,192
Rb 32,1,13312
Mp 108,48,200
s_add_u32 s29, s73, 0x8400
buffer_store_dwordx4 v[120:123], v1, s[20:23], s29 offen sc0 sc1
)" R"(
Mp 108,52,216
Rb 36,1,14336
Mp 100,52,208
Mp 100,56,224
Rb 40,1,16384
Mp 108,56,232
Mp 108,60,248
Rb 44,1,17408
s_waitcnt lgkmcnt(8)
Mp 100,60,240
v_mfma_f32_16x16x32_bf16 v[112:115], a[0:3], v[128:131], 0
Rb 48,1,18432
v_mfma_f32_16x16x32_bf16 v[120:123], a[0:3], v[136:139], 0
Mp 120,4,152
Rb 52,1,19456
Mp 112,4,144
Mp 112,8,160
Rb 56,1,20480
Cv 96,96,97
Mp 120,8,168
Cv 97,98,99
Cv 98,100,101
Mp 120,12,184
Rb 60,1,21504
s_waitcnt vmcnt(2) lgkmcnt(8)
buffer_store_dword v6, v7, s[20:23], s68 offen offset:0 sc0 sc1
Mp 112,12,176
Cv 99,102,103
Mp 112,16,192
Rb 0,1,22528
Mp 120,16,200
s_add_u32 s29, s73, 0x8800
buffer_store_dwordx4 v[96:99], v1, s[20:23], s29 offen sc0 sc1
Mp 120,20,216
Rb 4,1,24576
Mp 112,20,208
Mp 112,24,224
Rb 8,1,25600
Mp 120,24,232
Mp 120,28,248
Rb 12,1,26624
s_waitcnt lgkmcnt(8)
Mp 112,28,240
v_mfma_f32_16x16x32_bf16 v[116:119], a[32:35], v[128:131], 0
Rb 16,1,27648
v_mfma_f32_16x16x32_bf16 v[124:127], a[32:35], v[136:139], 0
Mp 124,36,152
Rb 20,1,28672
Mp 116,36,144
Mp 116,40,160
Rb 24,1,29696
Cv 104,104,105
Mp 124,40,168
Cv 105,106,107
Cv 106,108,109
Mp 124,44,184
Rb 28,1,30720
s_waitcnt lgkmcnt(8)
Mp 116,44,176
Cv 107,110,111
Mp 116,48,192
Rb 32,1,32768
Mp 124,48,200
s_add_u32 s29, s73, 0x8c00
)" R"(
buffer_store_dwordx4 v[104:107], v1, s[20:23], s29 offen sc0 sc1
Mp 124,52,216
Rb 36,1,33792
Mp 116,52,208
Mp 116,56,224
Rb 40,1,34816
Mp 124,56,232
Mp 124,60,248
Rb 44,1,35840
s_waitcnt lgkmcnt(8)
s_barrier
Mp 116,60,240
v_mfma_f32_16x16x32_bf16 v[96:99], a[0:3], v[128:131], 0
Rb 48,1,36864
v_mfma_f32_16x16x32_bf16 v[104:107], a[0:3], v[136:139], 0
Mp 104,4,152
Rb 52,1,37888
Mp 96,4,144
Mp 96,8,160
Rb 56,1,38912
Cv 112,112,113
Mp 104,8,168
Cv 113,114,115
Cv 114,116,117
Mp 104,12,184
Rb 60,1,40960
s_waitcnt lgkmcnt(8)
Mp 96,12,176
Cv 115,118,119
Mp 96,16,192
Rb 0,1,41984
Mp 104,16,200
s_add_u32 s29, s73, 0x9000
buffer_store_dwordx4 v[112:115], v1, s[20:23], s29 offen sc0 sc1
Mp 104,20,216
Rb 4,1,43008
Mp 96,20,208
Mp 96,24,224
Rb 8,1,44032
Mp 104,24,232
Mp 104,28,248
Rb 12,1,45056
s_waitcnt lgkmcnt(8)
Mp 96,28,240
v_mfma_f32_16x16x32_bf16 v[100:103], a[32:35], v[128:131], 0
Rb 16,1,46080
v_mfma_f32_16x16x32_bf16 v[108:111], a[32:35], v[136:139], 0
Mp 108,36,152
Rb 20,1,47104
Mp 100,36,144
Mp 100,40,160
Rb 24,1,49152
Cv 120,120,121
Mp 108,40,168
Cv 121,122,123
Cv 122,124,125
Mp 108,44,184
Rb 28,1,50176
s_waitcnt lgkmcnt(8)
Mp 100,44,176
Cv 123,126,127
Mp 100,48,192
Rb 32,1,51200
Mp 108,48,200
s_add_u32 s29, s73, 0x9400
)" R"(
buffer_store_dwordx4 v[120:123], v1, s[20:23], s29 offen sc0 sc1
Mp 108,52,216
Rb 36,1,52224
Mp 100,52,208
Mp 100,56,224
Rb 40,1,53248
Mp 108,56,232
Mp 108,60,248
Rb 44,1,54272
s_waitcnt lgkmcnt(8)
Mp 100,60,240
v_mfma_f32_16x16x32_bf16 v[112:115], a[0:3], v[128:131], 0
Rb 48,1,55296
v_mfma_f32_16x16x32_bf16 v[120:123], a[0:3], v[136:139], 0
Mp 120,4,152
Rb 52,1,57344
Mp 112,4,144
Mp 112,8,160
Rb 56,1,58368
Cv 96,96,97
Mp 120,8,168
Cv 97,98,99
Cv 98,100,101
Mp 120,12,184
Rb 60,1,59392
s_waitcnt lgkmcnt(8)
Mp 112,12,176
Cv 99,102,103
Mp 112,16,192
Rb 0,1,60416
Mp 120,16,200
s_add_u32 s29, s73, 0x9800
buffer_store_dwordx4 v[96:99], v1, s[20:23], s29 offen sc0 sc1
Mp 120,20,216
Rb 4,1,61440
Mp 112,20,208
Mp 112,24,224
Rb 8,1,62464
Mp 120,24,232
Mp 120,28,248
Rb 12,1,63488
s_waitcnt lgkmcnt(8)
Mp 112,28,240
v_mfma_f32_16x16x32_bf16 v[116:119], a[32:35], v[128:131], 0
Rb 16,10,0
v_mfma_f32_16x16x32_bf16 v[124:127], a[32:35], v[136:139], 0
Mp 124,36,152
Rb 20,10,1024
Mp 116,36,144
Mp 116,40,160
Rb 24,10,2048
Cv 104,104,105
Mp 124,40,168
Cv 105,106,107
Cv 106,108,109
Mp 124,44,184
Rb 28,10,3072
s_waitcnt lgkmcnt(8)
Mp 116,44,176
Cv 107,110,111
Mp 116,48,192
Rb 32,10,4096
Mp 124,48,200
s_add_u32 s29, s73, 0xc000
)" R"(
buffer_store_dwordx4 v[104:107], v1, s[20:23], s29 offen sc0 sc1
Mp 124,52,216
Rb 36,10,5120
Mp 116,52,208
Mp 116,56,224
Rb 40,10,6144
Mp 124,56,232
Mp 124,60,248
Rb 44,10,8192
s_waitcnt lgkmcnt(8)
Mp 116,60,240
v_mfma_f32_16x16x32_bf16 v[96:99], a[0:3], v[128:131], 0
Rb 48,10,9216
v_mfma_f32_16x16x32_bf16 v[104:107], a[0:3], v[136:139], 0
Mp 104,4,152
Rb 52,10,10240
Mp 96,4,144
Mp 96,8,160
Rb 56,10,11264
Cv 112,112,113
Mp 104,8,168
Cv 113,114,115
Cv 114,116,117
Mp 104,12,184
Rb 60,10,12288
s_waitcnt lgkmcnt(8)
Mp 96,12,176
Cv 115,118,119
Mp 96,16,192
Rb 0,10,13312
Mp 104,16,200
s_add_u32 s29, s73, 0xc400
buffer_store_dwordx4 v[112:115], v1, s[20:23], s29 offen sc0 sc1
Mp 104,20,216
Rb 4,10,14336
Mp 96,20,208
Mp 96,24,224
Rb 8,10,16384
Mp 104,24,232
Mp 104,28,248
Rb 12,10,17408
s_waitcnt lgkmcnt(8)
Mp 96,28,240
v_mfma_f32_16x16x32_bf16 v[100:103], a[32:35], v[128:131], 0
Rb 16,10,18432
v_mfma_f32_16x16x32_bf16 v[108:111], a[32:35], v[136:139], 0
Mp 108,36,152
Rb 20,10,19456
Mp 100,36,144
Mp 100,40,160
Rb 24,10,20480
Cv 120,120,121
Mp 108,40,168
Cv 121,122,123
Cv 122,124,125
Mp 108,44,184
Rb 28,10,21504
s_waitcnt lgkmcnt(8)
Mp 100,44,176
Cv 123,126,127
Mp 100,48,192
Rb 32,10,22528
Mp 108,48,200
s_add_u32 s29, s73, 0xc800
)" R"(
buffer_store_dwordx4 v[120:123], v1, s[20:23], s29 offen sc0 sc1
Mp 108,52,216
Rb 36,10,24576
Mp 100,52,208
Mp 100,56,224
Rb 40,10,25600
Mp 108,56,232
Mp 108,60,248
Rb 44,10,26624
s_waitcnt lgkmcnt(8)
Mp 100,60,240
v_mfma_f32_16x16x32_bf16 v[112:115], a[0:3], v[128:131], 0
Rb 48,10,27648
v_mfma_f32_16x16x32_bf16 v[120:123], a[0:3], v[136:139], 0
Mp 120,4,152
Rb 52,10,28672
Mp 112,4,144
Mp 112,8,160
Rb 56,10,29696
Cv 96,96,97
Mp 120,8,168
Cv 97,98,99
Cv 98,100,101
Mp 120,12,184
Rb 60,10,30720
s_waitcnt lgkmcnt(8)
Mp 112,12,176
Cv 99,102,103
Mp 112,16,192
s_add_u32 s29, s73, 0xcc00
buffer_store_dwordx4 v[96:99], v1, s[20:23], s29 offen sc0 sc1
Mp 120,16,200
Mp 120,20,216
Mp 112,20,208
Mp 112,24,224
Mp 120,24,232
Mp 120,28,248
s_waitcnt lgkmcnt(4)
Mp 112,28,240
v_mfma_f32_16x16x32_bf16 v[116:119], a[32:35], v[128:131], 0
v_mfma_f32_16x16x32_bf16 v[124:127], a[32:35], v[136:139], 0
Mp 124,36,152
Mp 116,36,144
Mp 116,40,160
Cv 104,104,105
Cv 105,106,107
Mp 124,40,168
Cv 106,108,109
Cv 107,110,111
Mp 124,44,184
s_waitcnt lgkmcnt(0)
Mp 116,44,176
s_add_u32 s29, s73, 0xd000
buffer_store_dwordx4 v[104:107], v1, s[20:23], s29 offen sc0 sc1
Mp 116,48,192
Mp 124,48,200
Mp 124,52,216
Mp 116,52,208
Mp 116,56,224
Mp 124,56,232
Mp 124,60,248
Mp 116,60,240
s_nop 7
Cv 112,112,113
)" R"(
Cv 113,114,115
Cv 114,116,117
Cv 115,118,119
s_add_u32 s29, s73, 0xd400
buffer_store_dwordx4 v[112:115], v1, s[20:23], s29 offen sc0 sc1
Cv 120,120,121
Cv 121,122,123
Cv 122,124,125
Cv 123,126,127
s_add_u32 s29, s73, 0xd800
buffer_store_dwordx4 v[120:123], v1, s[20:23], s29 offen sc0 sc1
.LzEND:
v_lshl_or_b32 v0, s33, 6, v0
s_add_u32 s4, s68, 4
s_mov_b32 s5, s63
s_mov_b32 s6, s84
s_mov_b32 s8, s74
s_mov_b32 s9, s75
s_mov_b32 s10, 0x1c0000
s_mov_b32 s11, 0x20000
)" : "+{s[0:1]}"(ka), "+{s2}"(bid), "+{v0}"(tid), "={s3}"(tag), "={s4}"(sfw), "={s5}"(idx), "={s6}"(tk), "={s[12:15]}"(rX), "={s[20:23]}"(rO), "={s[8:11]}"(rOut) :
   : "memory", "scc", "vcc", "v1", "v2", "v3", "v4", "v5", "v6", "v7", "v8", "v9", R256("v"), ACL,
     "s7", "s16", "s17", "s18", "s19", "s24", "s25", "s26", "s27", "s28", "s29", "s30", "s31", "s33", "s34", "s35", "s36", "s37", "s38", "s39",
     V10("s4"), V10("s5"), V10("s6"), V10("s7"), V10("s8"), V10("s9"));
  bid = rfl(bid); tag = rfl(tag); sfw = rfl(sfw); rX = rfl4(rX); rO = rfl4(rO); rOut = rfl4(rOut);
  const u32 lane = tid & 63, wid = rfl(tid >> 6), r = lane & 15, g = lane >> 4, w = ((bid >> 3) & 15u) + 16u * wid;
  const bool B = bid >= 128u;
  idx = rfl(idx);
  Task k[NS]; u32 vf = OOB, rt = 0u, pend = 0u;
#pragma unroll
  for (int s = 0; s < NS; ++s) {
    const u32 i = B ? (s ? 168u + w : 104u + w) : w + 64u * s, cls = i / 56u, e = i - cls * 56u;
    const bool on = i < 224u && (!B || i >= 128u);
    const u32 blk = (cls >> 1) * 32u + (e / 14u) * 8u + (bid & 7u), G = e % 14u + (cls & 1u) * 14u, r0 = blk * 32u + (G & 1u) * 16u;
    k[s].sy = rfl((u32)WS_Y + blk * 65536u + (G + (G / 7u) * 9u) * 1024u + ((rfl(tk) ^ (bid << 10) ^ (cls << 12)) & 8192u));
    k[s].vx = on ? (r0 + r) * (M_ * 2) + (G >> 1) * 64u + g * 8u : OOB;
    if (g == s) vf = (blk * 16u + r) * 128u + (cls & 1u) * 4u;
    if (on) pend |= 0x1111u << (16 * s);
    if (on) rt |= (((idx & 0xffffu) >> 4 == r0 >> 4 ? 1u : 0u) | (idx >> 20 == r0 >> 4 ? 2u : 0u)) << (2 * s);
    k[s].vg = (r0 + r) * 16u;
  }
  u32x4 sel[2];
#pragma unroll
  for (int b = 0; b < 2; ++b)
#pragma unroll
    for (int q = 0; q < 4; ++q) sel[b][q] = (r >> 1) == 4u * b + q ? ((r & 1u) ? 0x3f800000u : 0x3f80u) : 0u;
  asm volatile("" : "+v"(sel[0]), "+v"(sel[1]), "+v"(k[0].vx), "+v"(k[1].vx), "+v"(k[0].vg), "+v"(k[1].vg), "+v"(vf));
  asm volatile("s_waitcnt vmcnt(0)\n\tbuffer_store_dword %0, %1, %2, %3 offen sc0 sc1" :: "v"(tag), "v"(lane ? OOB : 0u), "s"(rO), "s"(sfw) : "memory");
  const u32 vy = g * 4194304u + r * 16u;
  rt = rfl(rt); pend = rfl(pend);
#define LOOK(W) u32 f; unsigned long long m; \
    asm volatile("buffer_load_dword %0, %2, %3, %4 offen sc0 sc1\n\ts_waitcnt vmcnt(0)\n\tv_cmp_ne_u32_e64 %1, %5, %0" \
                 : "=&v"(f), "=s"(m) : "v"((pend >> (lane & 28u)) & 1u ? vf : OOB), "s"(rO), "s"((u32)WS_YW), "s"(tag) : "memory"); \
    u32 x = rfl((u32)m); x |= x >> 1; x |= x >> 2; \
    const u32 rdy = (W) ? pend & ~x : pend, had = pend; \
    y_load<0>(vy, rO, k[0].sy, rdy); y_load<1>(vy, rO, k[1].sy, rdy >> 16); \
    pend &= ~rdy
  u32 todo = pend;
  if (bid - 112u < 14u) {
    const u32 task = (bid - 112u) * 4u + wid, c = task >= 28u, ir = (c ? idx >> 16 : idx) & 2047u, col = (task - 28u * c) * 16u + (lane & 3u) * 4u, sf = (u32)WS_YF + task * 7168u;
    f32x4 v[7]; u32x2 xv; float rv; u32 f0, f1, n = 1u << 17;
    do {
      asm volatile("buffer_load_dword %0, %2, %4, %5 offen sc0 sc1\n\tbuffer_load_dword %1, %3, %4, %5 offen sc0 sc1\n\ts_waitcnt vmcnt(0)"
                   : "=&v"(f0), "=&v"(f1) : "v"(lane * 128u), "v"(lane * 128u + 6144u), "s"(rO), "s"((u32)WS_FW) : "memory");
    } while (!__all(f0 == tag && f1 == tag) && --n);
#pragma unroll
    for (int q = 0; q < 7; ++q) asm volatile("buffer_load_dwordx4 %0, %1, %2, %3 offen offset:%4 sc0 sc1" : "=v"(v[q]) : "v"(lane * 16u), "s"(rO), "s"(sf + (q >> 2) * 4096), "i"((q & 3) * 1024) : "memory");
    asm volatile("buffer_load_dwordx2 %0, %9, %10, %11 offen\n\tbuffer_load_dword %1, %12, %13, %14 offen sc0 sc1\n\ts_waitcnt vmcnt(0)"
                 : "=&v"(xv), "=&v"(rv), "+v"(v[0]), "+v"(v[1]), "+v"(v[2]), "+v"(v[3]), "+v"(v[4]), "+v"(v[5]), "+v"(v[6])
                 : "v"(col * 2u), "s"(rX), "s"(ir * (M_ * 2)), "v"(0u), "s"(rO), "s"((u32)WS_KEY + 12u + ir * 16u) : "memory");
    const f32x4 fs = ((v[0] + v[1]) + (v[2] + v[3])) + ((v[4] + v[5]) + v[6]);
    const float sg = rbf(1.f / (1.f + __expf(-rv))), xs[4] = {BF_LO(xv[0]), BF_HI(xv[0]), BF_LO(xv[1]), BF_HI(xv[1])};
    float o[4];
#pragma unroll
    for (int e = 0; e < 4; ++e) {
      unsigned u = __float_as_uint(fs[e]);
      const auto s32 = __builtin_amdgcn_permlane32_swap(u, u, false, false);
      u = __float_as_uint(__uint_as_float(s32[0]) + __uint_as_float(s32[1]));
      const auto s16 = __builtin_amdgcn_permlane16_swap(u, u, false, false);
      o[e] = xs[e] + rbf(rbf(dpp_add<0x124>(dpp_add<0x128>(__uint_as_float(s16[0]) + __uint_as_float(s16[1])))) * sg);
    }
    const u32x4 ov = {cvtpk(o[0], o[1]), tag, cvtpk(o[2], o[3]), tag};
    asm volatile("buffer_store_dwordx4 %0, %1, %2, %3 offen sc0 sc1\n\ts_nop 1" :: "v"(ov), "v"(lane < 4u ? lane * 16u : OOB), "s"(rO), "s"((u32)WS_BB + task * 128u) : "memory");
  }
#pragma unroll
  for (int s = 0; s < NS; ++s)
    asm volatile("buffer_load_dwordx2 %0, %3, %4, 0 offen\n\tbuffer_load_dwordx2 %1, %3, %4, 0 offen offset:32\n\tbuffer_load_dword %2, %5, %6, %7 offen sc0 sc1"
                 : "=&v"(k[s].x[0]), "=&v"(k[s].x[1]), "=&v"(k[s].g) : "v"(k[s].vx), "s"(rX), "v"(k[s].vg), "s"(rO), "s"((u32)WS_KEY + 8u) : "memory");
  { LOOK(true); (void)had; }
  asm volatile("" : "+v"(k[0].x[0]), "+v"(k[0].x[1]), "+v"(k[0].g), "+v"(k[1].x[0]), "+v"(k[1].x[1]), "+v"(k[1].g));
  if (rt) {
    u32 vo[NS], n = 1u << 17; u32x4 p[2 * NS]; bool ok;
#pragma unroll
    for (int s = 0; s < NS; ++s) {
      const u32 o0 = k[s].vx - (idx & 0xffffu) * (M_ * 2), o1 = k[s].vx - (idx >> 16) * (M_ * 2), o = o0 < M_ * 2 ? o0 : o1 + M_ * 2;
      vo[s] = o0 < M_ * 2 || o1 < M_ * 2 ? (o >> 5) * 128u + g * 16u : OOB;
    }
    do {
      asm volatile("buffer_load_dwordx4 %0, %4, %6, %7 offen sc0 sc1\n\tbuffer_load_dwordx4 %1, %4, %6, %7 offen offset:128 sc0 sc1\n\t"
                   "buffer_load_dwordx4 %2, %5, %6, %7 offen sc0 sc1\n\tbuffer_load_dwordx4 %3, %5, %6, %7 offen offset:128 sc0 sc1\n\ts_waitcnt vmcnt(0)"
                   : "=&v"(p[0]), "=&v"(p[1]), "=&v"(p[2]), "=&v"(p[3]) : "v"(vo[0]), "v"(vo[1]), "s"(rO), "s"((u32)WS_BB) : "memory");
      ok = true;
#pragma unroll
      for (int q = 0; q < 2 * NS; ++q) ok = ok && (vo[q >> 1] == OOB || (p[q][1] == tag && p[q][3] == tag));
    } while (!__all(ok) && --n);
#pragma unroll
    for (int q = 0; q < 2 * NS; ++q) if (vo[q >> 1] != OOB) k[q >> 1].x[q & 1] = (u32x2){p[q][0], p[q][2]};
  }
#pragma unroll
  for (int s = 0; s < NS; ++s) {
    const float omg = rbf(1.f - k[s].g);
#pragma unroll
    for (int e = 0; e < 4; ++e) { k[s].b[2 * e] = rbf(omg * BF_LO(k[s].x[e >> 1][e & 1])); k[s].b[2 * e + 1] = rbf(omg * BF_HI(k[s].x[e >> 1][e & 1])); }
  }
  for (u32 n = 1u << 17; todo; --n) {
    LOOK(n);
    if (!rdy && had) __builtin_amdgcn_s_sleep(SLP);
    if ((todo & 0xffffu) && !(had & 0xffffu)) { y_done<0>(k[0], sel, rOut); todo &= 0xffff0000u; }
    if ((todo >> 16) && !(had >> 16)) { y_done<1>(k[1], sel, rOut); todo &= 0xffffu; }
  }
  if (tid == 0u && bid == 0u) asm volatile("buffer_store_dword %0, %1, %2, %3 offen sc0 sc1" :: "v"(tag), "v"(lane ? OOB : 0u), "s"(rO), "s"((u32)WS_EP) : "memory");
  asm volatile(".LzCEND:");
}

extern "C" void kernel_launch(void* const* d_in, const int* in_sizes, int n_in, void* d_out, int out_size, void* d_ws, size_t ws_size, hipStream_t stream) {
  (void)hipFuncSetAttribute((const void*)k_main, hipFuncAttributeMaxDynamicSharedMemorySize, 163840);
  k_main<<<256, 256, 163840, stream>>>((const u16*)d_in[0], (const u16*)d_in[1], (const u16*)d_in[2], (const u16*)d_in[3], (const u16*)d_in[4],
      (char*)d_ws, (const u16*)d_in[11], (const u16*)d_in[7], (const u16*)d_in[5], (const u16*)d_in[8], (const u16*)d_in[9], (const u16*)d_in[10], (u16*)d_out);
}
```

```cpp
#include <hip/hip_runtime.h>

typedef unsigned short u16;
#define V10(p) p "0", p "1", p "2", p "3", p "4", p "5", p "6", p "7", p "8", p "9"
#define V100(p) V10(p "0"), V10(p "1"), V10(p "2"), V10(p "3"), V10(p "4"), V10(p "5"), V10(p "6"), V10(p "7"), V10(p "8"), V10(p "9")
#define R256(p) V10(p "1"), V10(p "2"), V10(p "3"), V10(p "4"), V10(p "5"), V10(p "6"), V10(p "7"), V10(p "8"), V10(p "9"), V100(p "1"), \
                V10(p "20"), V10(p "21"), V10(p "22"), V10(p "23"), V10(p "24"), p "250", p "251", p "252", p "253", p "254", p "255"

extern "C" __global__ __launch_bounds__(256, 1)
void k_main(const u16* x, const u16* ckv, const u16* wq, const u16* wuk, const u16* wuv, char* ws,
            const u16* gate_w, const u16* w_r, const u16* wo, const u16* wg, const u16* wu, const u16* wd, u16* out) {
  typedef const __attribute__((address_space(4))) char* karg_t;
  karg_t ka = (karg_t)__builtin_amdgcn_kernarg_segment_ptr();
  unsigned bid = blockIdx.x, tid = threadIdx.x;
  asm volatile(R"(
.macro Mv d,a,b
v_mfma_f32_16x16x32_bf16 v[\d:\d+3], a[\a:\a+3], a[\b:\b+3], v[\d:\d+3]
.endm
.macro Mp d,a,b
v_mfma_f32_16x16x32_bf16 v[\d:\d+3], a[\a:\a+3], v[\b:\b+3], v[\d:\d+3]
.endm
.macro Ma d,a,b
v_mfma_f32_16x16x32_bf16 a[\d:\d+3], a[\a:\a+3], a[\b:\b+3], a[\d:\d+3]
.endm
.macro Mz d,a,b
v_mfma_f32_16x16x32_bf16 v[\d:\d+3], a[\a:\a+3], a[\b:\b+3], 0
.endm
.macro Rb d,v,o
ds_read_b128 a[\d:\d+3], v\v offset:\o
.endm
.macro Rt d,v,o
ds_read_b64_tr_b16 a[\d:\d+1], v\v offset:\o
.endm
.macro Cv d,a,b
v_cvt_pk_bf16_f32 v\d, v\a, v\b
.endm
.LzSTART:
s_load_dwordx16 s[64:79], s[0:1], 0
s_load_dwordx2 s[80:81], s[0:1], 64
v_lshrrev_b32 v1, 6, v0
v_and_b32 v0, 63, v0
v_readfirstlane_b32 s33, v1
v_lshlrev_b32 v1, 4, v0
s_getpc_b64 s[24:25]
.Lzpc0:
s_sub_u32 s24, s24, .Lzpc0-.LzSTART
s_subb_u32 s25, s25, 0
s_mov_b32 s26, .LzCEND-.LzSTART
s_mov_b32 s27, 0x20000
s_lshr_b32 s28, s2, 3
s_lshl2_add_u32 s28, s28, s33
s_lshl_b32 s28, s28, 11
v_lshrrev_b32 v5, 4, v0
v_min_u32 v5, 1, v5
v_lshlrev_b32 v5, 27, v5
v_add_lshl_u32 v5, v5, v1, 3
buffer_load_dword v5, v5, s[24:27], s28 offen
s_and_b32 s28, s2, 7
s_bfe_u32 s29, s2, 0x10003
s_lshl1_add_u32 s34, s28, s29
s_bfe_u32 s35, s2, 0x30004
s_bfe_u32 s91, s2, 0x10004
s_bfe_u32 s93, s2, 0x20005
s_lshl_b32 s36, s33, 13
s_mov_b32 s95, 40
s_bitcmp1_b32 s2, 7
s_cbranch_scc1 .LzB0
s_branch .LzA0
.LzPRB:
s_lshl_b32 s24, s34, 21
s_mul_i32 s25, s33, 2304
s_add_u32 s24, s24, s25
s_add_u32 s24, s24, 0x6100000
s_and_b32 s25, s36, 0x2000
s_xor_b32 s24, s24, s25
s_xor_b32 s25, s24, 0x2000
buffer_store_dword v0, v7, s[20:23], s24 offen sc0 sc1
buffer_store_dword v0, v7, s[20:23], s25 offen sc0 sc1
s_mov_b32 s26, -1
s_mov_b32 s27, -1
s_waitcnt vmcnt(0)
s_memtime s[28:29]
s_waitcnt lgkmcnt(0)
buffer_store_dword v0, v7, s[20:23], s24 offen sc0 sc1
s_waitcnt vmcnt(0)
s_memtime s[30:31]
s_waitcnt lgkmcnt(0)
s_sub_u32 s30, s30, s28
s_min_u32 s26, s26, s30
s_memtime s[28:29]
s_waitcnt lgkmcnt(0)
buffer_store_dword v0, v7, s[20:23], s25 offen sc0 sc1
s_waitcnt vmcnt(0)
s_memtime s[30:31]
s_waitcnt lgkmcnt(0)
s_sub_u32 s30, s30, s28
s_min_u32 s27, s27, s30
s_memtime s[28:29]
s_waitcnt lgkmcnt(0)
)" R"(
buffer_store_dword v0, v7, s[20:23], s24 offen sc0 sc1
s_waitcnt vmcnt(0)
s_memtime s[30:31]
s_waitcnt lgkmcnt(0)
s_sub_u32 s30, s30, s28
s_min_u32 s26, s26, s30
s_memtime s[28:29]
s_waitcnt lgkmcnt(0)
buffer_store_dword v0, v7, s[20:23], s25 offen sc0 sc1
s_waitcnt vmcnt(0)
s_memtime s[30:31]
s_waitcnt lgkmcnt(0)
s_sub_u32 s30, s30, s28
s_min_u32 s27, s27, s30
s_bitcmp1_b32 s33, 0
s_cselect_b32 s28, s27, s26
s_cselect_b32 s29, s26, s27
v_mov_b32 v32, s28
v_mov_b32 v33, s29
s_lshl_b32 s24, s33, 3
v_mov_b32 v34, s24
v_mov_b32 v35, 0
ds_write_b64 v34, v[32:33]
s_waitcnt lgkmcnt(0)
s_barrier
ds_read_b128 v[36:39], v35
ds_read_b128 v[40:43], v35 offset:16
s_mul_i32 s24, s3, 1664525
s_add_u32 s24, s24, 1013904223
s_and_b32 s24, s24, -2
s_lshl_b32 s25, s34, 6
s_add_u32 s25, s25, 0xc108800
s_cmp_lg_u32 s33, 0
s_cmov_b32 s25, 0x40000000
s_waitcnt lgkmcnt(0)
v_min3_u32 v32, v36, v38, v40
v_min3_u32 v33, v37, v39, v41
v_min_u32 v32, v32, v42
v_min_u32 v33, v33, v43
v_cmp_gt_u32_e32 vcc, v32, v33
v_mov_b32 v34, s24
s_nop 0
v_addc_co_u32_e32 v34, vcc, 0, v34, vcc
buffer_store_dword v34, v7, s[20:23], s25 offen sc0 sc1
s_barrier
s_branch .LzPRR
.LzA0:
s_mov_b32 s63, 0x6040200
s_mov_b32 s28, 0x7050312
s_bitcmp1_b32 s2, 5
s_cmov_b32 s63, 0x18161401
s_cmov_b32 s28, 0x19171513
s_bitcmp1_b32 s2, 6
s_cmov_b32 s63, s28
s_lshl_b32 s28, s33, 3
s_lshr_b32 s28, s63, s28
s_and_b32 s28, s28, 255
s_lshl_b32 s38, s28, 17
s_add_u32 s38, s38, 0xff80
s_lshl_b32 s39, s33, 16
s_lshl_b32 s28, s34, 9
s_add_u32 s39, s39, s28
s_sub_u32 s39, s39, 0x40000
s_lshl_b32 s37, s33, 12
)" R"(
s_add_u32 s37, s37, 0x10000
v_lshrrev_b32 v28, 4, v0
v_and_b32 v29, 7, v0
v_xor_b32 v28, v28, v29
v_lshlrev_b32 v28, 4, v28
v_lshrrev_b32 v29, 3, v0
v_lshl_or_b32 v8, v29, 11, v28
v_xor_b32 v9, 64, v8
v_add_u32 v9, 15360, v9
v_add_u32 v10, 30720, v8
v_add_u32 v11, 30720, v9
s_and_b32 s28, s33, 1
s_lshl_b32 s28, s28, 7
v_lshrrev_b32 v29, 5, v0
v_mul_u32_u24 v29, 0x2220, v29
v_xor_b32 v29, v29, v1
v_xor_b32 v12, s28, v29
v_xor_b32 v13, 64, v12
v_add_u32 v13, 15360, v13
v_add_u32 v14, 30720, v12
v_add_u32 v15, 30720, v13
v_min_u32 v7, 1, v0
v_lshlrev_b32 v7, 30, v7
s_waitcnt lgkmcnt(0)
s_mov_b32 s28, 0xc108400
s_load_dword s3, s[74:75], s28
s_mov_b32 s20, s74
s_mov_b32 s21, s75
s_mov_b32 s22, 0xc510000
s_mov_b32 s23, 0x20000
s_mov_b32 s4, s66
s_mov_b32 s5, s67
s_mov_b32 s6, 0x400000
s_mov_b32 s7, 0x20000
s_cmp_eq_u32 s91, 1
s_cselect_b32 s8, s72, s70
s_cselect_b32 s9, s73, s71
s_mov_b32 s10, 0x800000
s_mov_b32 s11, 0x20000
s_lshr_b32 s28, s2, 3
s_cmp_eq_u32 s28, 14
s_cbranch_scc1 .LzPRB
.LzPRR:
s_add_u32 m0, s37, 0
s_add_u32 s39, s39, 0x40000
buffer_load_dwordx4 v12, s[8:11], s39 offen offset:0 lds
buffer_load_dwordx4 v13, s[8:11], s39 offen offset:1024 lds
buffer_load_dwordx4 v14, s[8:11], s39 offen offset:2048 lds
buffer_load_dwordx4 v15, s[8:11], s39 offen offset:3072 lds
s_add_u32 m0, s36, 0
s_add_u32 s38, s38, 0xffff0080
buffer_load_dwordx4 v8, s[4:7], s38 offen offset:0 lds
buffer_load_dwordx4 v9, s[4:7], s38 offen offset:1024 lds
buffer_load_dwordx4 v10, s[4:7], s38 offen offset:2048 lds
buffer_load_dwordx4 v11, s[4:7], s38 offen offset:3072 lds
s_add_u32 m0, s36, 4096
s_add_u32 s38, s38, 0x10000
buffer_load_dwordx4 v8, s[4:7], s38 offen offset:0 lds
buffer_load_dwordx4 v9, s[4:7], s38 offen offset:1024 lds
buffer_load_dwordx4 v10, s[4:7], s38 offen offset:2048 lds
buffer_load_dwordx4 v11, s[4:7], s38 offen offset:3072 lds
s_add_u32 m0, s37, 16384
s_add_u32 s39, s39, 0x40000
buffer_load_dwordx4 v12, s[8:11], s39 offen offset:0 lds
)" R"(
buffer_load_dwordx4 v13, s[8:11], s39 offen offset:1024 lds
buffer_load_dwordx4 v14, s[8:11], s39 offen offset:2048 lds
buffer_load_dwordx4 v15, s[8:11], s39 offen offset:3072 lds
s_add_u32 m0, s36, 32768
s_add_u32 s38, s38, 0xffff0080
buffer_load_dwordx4 v8, s[4:7], s38 offen offset:0 lds
buffer_load_dwordx4 v9, s[4:7], s38 offen offset:1024 lds
buffer_load_dwordx4 v10, s[4:7], s38 offen offset:2048 lds
buffer_load_dwordx4 v11, s[4:7], s38 offen offset:3072 lds
s_add_u32 m0, s36, 36864
s_add_u32 s38, s38, 0x10000
buffer_load_dwordx4 v8, s[4:7], s38 offen offset:0 lds
buffer_load_dwordx4 v9, s[4:7], s38 offen offset:1024 lds
buffer_load_dwordx4 v10, s[4:7], s38 offen offset:2048 lds
buffer_load_dwordx4 v11, s[4:7], s38 offen offset:3072 lds
s_add_u32 m0, s37, 32768
s_add_u32 s39, s39, 0x40000
buffer_load_dwordx4 v12, s[8:11], s39 offen offset:0 lds
buffer_load_dwordx4 v13, s[8:11], s39 offen offset:1024 lds
buffer_load_dwordx4 v14, s[8:11], s39 offen offset:2048 lds
buffer_load_dwordx4 v15, s[8:11], s39 offen offset:3072 lds
s_add_u32 m0, s37, 49152
s_add_u32 s39, s39, 0x40000
buffer_load_dwordx4 v12, s[8:11], s39 offen offset:0 lds
buffer_load_dwordx4 v13, s[8:11], s39 offen offset:1024 lds
buffer_load_dwordx4 v14, s[8:11], s39 offen offset:2048 lds
buffer_load_dwordx4 v15, s[8:11], s39 offen offset:3072 lds
s_mov_b32 s12, s64
s_mov_b32 s13, s65
s_mov_b32 s14, 0x1c0000
s_mov_b32 s15, 0x20000
s_mov_b32 s16, s68
s_mov_b32 s17, s69
s_mov_b32 s18, 0x380000
s_mov_b32 s19, 0x20000
s_load_dwordx2 s[70:71], s[0:1], 88
s_load_dwordx2 s[74:75], s[0:1], 96
v_bfe_u32 v28, v0, 1, 3
v_lshrrev_b32 v29, 4, v0
v_xor_b32 v28, v28, v29
v_lshlrev_b32 v28, 4, v28
v_and_b32 v29, 15, v0
v_lshl_or_b32 v28, v29, 7, v28
s_lshr_b32 s28, s33, 1
s_lshl_b32 s28, s28, 12
v_or_b32 v16, s28, v28
v_xor_b32 v17, 64, v16
v_bfe_u32 v28, v0, 2, 3
v_lshlrev_b32 v28, 5, v28
v_and_b32 v29, 3, v0
v_lshl_or_b32 v28, v29, 3, v28
v_bfe_u32 v29, v0, 2, 2
v_lshl_or_b32 v28, v29, 9, v28
v_lshrrev_b32 v29, 4, v0
v_lshl_or_b32 v28, v29, 12, v28
s_and_b32 s28, s33, 1
s_lshl_b32 s28, s28, 8
s_bitset1_b32 s28, 16
v_or_b32 v18, s28, v28
v_xor_b32 v19, 32, v18
v_xor_b32 v20, 64, v18
v_xor_b32 v21, 96, v18
v_xor_b32 v22, 128, v18
v_xor_b32 v23, 160, v18
)" R"(
v_xor_b32 v24, 192, v18
v_xor_b32 v25, 224, v18
s_max_u32 s28, s33, 1
s_lshl3_add_u32 s28, s28, 56
s_load_dwordx2 s[84:85], s[0:1], s28
s_mov_b32 s28, s35
s_lshl2_add_u32 s28, s28, s33
s_mul_i32 s43, s28, 28672
v_and_b32 v26, 0x70, v1
v_and_b32 v4, 48, v0
v_xor_b32 v26, v26, v4
v_lshrrev_b32 v4, 3, v0
v_mul_u32_u24 v4, 896, v4
v_add_u32 v26, v26, v4
v_xor_b32 v27, 64, v26
v_add_u32 v27, 6144, v27
v_bfe_u32 v28, v0, 1, 3
v_lshrrev_b32 v4, 4, v0
v_xor_b32 v28, v28, v4
v_lshlrev_b32 v28, 4, v28
v_and_b32 v4, 15, v0
v_lshl_or_b32 v28, v4, 7, v28
s_add_u32 s28, s36, 0x18000
v_add_u32 v28, s28, v28
v_xor_b32 v29, 64, v28
s_lshl_b32 s28, s34, 21
s_lshl_b32 s29, s91, 25
s_add_u32 s28, s28, s29
s_bfe_u32 s29, s63, 0x80000
s_lshl_b32 s29, s29, 16
s_add_u32 s45, s28, s29
s_bfe_u32 s29, s63, 0x80008
s_lshl_b32 s29, s29, 16
s_add_u32 s46, s28, s29
s_bfe_u32 s29, s63, 0x80010
s_lshl_b32 s29, s29, 16
s_add_u32 s47, s28, s29
s_bfe_u32 s29, s63, 0x80018
s_lshl_b32 s29, s29, 16
s_add_u32 s48, s28, s29
s_lshl_b32 s50, s34, 11
s_add_u32 s50, s50, 0xc110000
s_lshl2_add_u32 s28, s91, s93
s_lshl_b32 s28, s28, 7
s_add_u32 s49, s28, s50
s_lshl3_add_u32 s28, s34, s35
s_lshl_b32 s52, s28, 7
s_add_u32 s52, s52, 0xc118000
s_lshl4_add_u32 s28, s34, s35
s_lshl_b32 s51, s28, 17
s_add_u32 s51, s51, 0x4000000
s_cmp_lg_u32 s33, 0
s_cmov_b32 s49, 0x40000000
s_mov_b32 s53, 0x40000
s_mov_b32 s54, 0x3db8aa3b
s_mul_i32 s41, s33, 7168
s_mul_i32 s42, s33, 0x70000
s_lshl_b32 s28, s34, 9
s_add_u32 s42, s42, s28
s_lshl_b32 s89, s34, 21
s_mov_b64 s[60:61], 0xff
s_mov_b32 s62, 0x4e800000
s_add_u32 s67, s89, 0x6000000
s_lshl3_add_u32 s28, s34, s35
)" R"(
s_add_u32 s64, s51, 0x100000
s_lshl2_add_u32 s28, s28, s33
s_lshl_b32 s65, s28, 7
s_add_u32 s65, s65, 0xc120000
s_lshl_b32 s66, s28, 10
s_add_u32 s66, s66, 0xc000000
s_mov_b32 s28, s35
s_lshl2_add_u32 s28, s28, s33
s_lshl_b32 s29, s28, 5
s_add_u32 s29, s29, s34
s_lshl_b32 s73, s29, 15
s_add_u32 s73, s73, 0x8000000
s_lshr_b32 s29, s36, 1
s_xor_b32 s29, s29, s36
s_and_b32 s29, s29, 0x2000
s_add_u32 s73, s73, s29
s_lshl4_add_u32 s28, s28, s34
s_lshl_b32 s68, s28, 7
s_add_u32 s68, s68, 0xc130000
s_add_u32 s55, s89, 0x120000
s_lshl_b32 s28, s35, 1
s_sub_u32 s57, 14, s28
s_mov_b32 s59, s89
s_mov_b32 s58, 2
s_cmp_eq_u32 s35, 7
s_cmov_b32 s55, s89
s_cmp_eq_u32 s35, 7
s_cmov_b32 s57, 16
s_cmp_eq_u32 s35, 7
s_cmov_b32 s59, s67
s_add_u32 s56, s55, 0x2000000
s_waitcnt vmcnt(20) lgkmcnt(0)
s_barrier
Rt 0,18,0
Rt 2,18,2048
Rt 4,19,0
Rt 6,19,2048
Rt 8,20,0
Rt 10,20,2048
Rt 12,21,0
Rt 14,21,2048
Rt 16,22,0
Rt 18,22,2048
Rt 20,23,0
Rt 22,23,2048
Rt 24,24,0
Rt 26,24,2048
Rt 28,25,0
Rt 30,25,2048
Rb 32,16,0
Rb 36,16,2048
Rb 40,16,8192
Rb 44,16,10240
Rb 48,16,16384
Rb 52,16,18432
Rb 56,16,24576
Rb 60,16,26624
s_mul_i32 s3, s3, 1664525
s_add_u32 s3, s3, 1013904223
s_cmp_eq_u32 s91, 1
s_waitcnt vmcnt(16) lgkmcnt(0)
s_barrier
s_cbranch_scc1 .LzAV
Mz 32,0,32
Rt 64,18,16384
Mz 64,0,36
)" R"(
s_lshl2_add_u32 s40, s2, s33
s_lshl_b32 s40, s40, 13
Mz 96,0,40
Rt 66,18,18432
v_lshlrev_b32 v3, 7, v0
Mz 128,0,44
s_add_u32 s39, s39, 0x40000
Mz 160,0,48
Rt 68,19,16384
s_add_u32 m0, s37, 0
Mz 192,0,52
buffer_load_dwordx4 v12, s[8:11], s39 offen offset:0 lds
Mz 224,0,56
Rt 70,19,18432
v_mfma_f32_16x16x32_bf16 a[224:227], a[0:3], a[60:63], 0
v_mfma_f32_16x16x32_bf16 a[228:231], a[4:7], a[60:63], 0
Rt 72,20,16384
Mz 228,4,56
Mz 196,4,52
Rt 74,20,18432
Mz 164,4,48
Mz 132,4,44
Rt 76,21,16384
Mz 100,4,40
buffer_load_dwordx4 v13, s[8:11], s39 offen offset:1024 lds
Mz 68,4,36
Rt 78,21,18432
Mz 36,4,32
Mz 40,8,32
Rt 80,22,16384
Mz 72,8,36
Mz 104,8,40
Rt 82,22,18432
Mz 136,8,44
Mz 168,8,48
Rt 84,23,16384
Mz 200,8,52
buffer_load_dwordx4 v14, s[8:11], s39 offen offset:2048 lds
Mz 232,8,56
Rt 86,23,18432
v_mfma_f32_16x16x32_bf16 a[232:235], a[8:11], a[60:63], 0
v_mfma_f32_16x16x32_bf16 a[236:239], a[12:15], a[60:63], 0
Rt 88,24,16384
Mz 236,12,56
Mz 204,12,52
Rt 90,24,18432
Mz 172,12,48
Mz 140,12,44
Rt 92,25,16384
Mz 108,12,40
buffer_load_dwordx4 v15, s[8:11], s39 offen offset:3072 lds
Mz 76,12,36
Rt 94,25,18432
Mz 44,12,32
Mz 48,16,32
Rb 96,17,0
Mz 80,16,36
buffer_load_dword v6, v3, s[4:7], s40 offen
Mz 112,16,40
Rb 100,17,2048
Mz 144,16,44
Mz 176,16,48
Rb 104,17,8192
Mz 208,16,52
)" R"(
Mz 240,16,56
Rb 108,17,10240
v_mfma_f32_16x16x32_bf16 a[240:243], a[16:19], a[60:63], 0
v_mfma_f32_16x16x32_bf16 a[244:247], a[20:23], a[60:63], 0
Rb 112,17,16384
Mz 244,20,56
Mz 212,20,52
Rb 116,17,18432
Mz 180,20,48
Mz 148,20,44
Rb 120,17,24576
Mz 116,20,40
Mz 84,20,36
Rb 124,17,26624
Mz 52,20,32
Mz 56,24,32
Mz 88,24,36
Mz 120,24,40
Mz 152,24,44
Mz 184,24,48
Mz 216,24,52
Mz 248,24,56
v_mfma_f32_16x16x32_bf16 a[248:251], a[24:27], a[60:63], 0
v_mfma_f32_16x16x32_bf16 a[252:255], a[28:31], a[60:63], 0
Mz 252,28,56
Mz 220,28,52
Mz 188,28,48
Mz 156,28,44
Mz 124,28,40
Mz 92,28,36
Mz 60,28,32
s_waitcnt vmcnt(9) lgkmcnt(0)
s_barrier
Mv 32,64,96
Rt 0,18,32768
s_bfe_u32 s88, s2, 0x20005
Mv 64,64,100
s_add_u32 s38, s38, 0xffff0080
s_and_b32 s28, s2, 15
Mv 96,64,104
Rt 2,18,34816
s_add_u32 m0, s36, 0
Mv 128,64,108
buffer_load_dwordx4 v8, s[4:7], s38 offen offset:0 lds
Mv 160,64,112
Rt 4,19,32768
s_lshl4_add_u32 s88, s88, s28
Mv 192,64,116
s_lshl2_add_u32 s88, s88, s33
s_lshl_b32 s88, s88, 12
Mv 224,64,120
Rt 6,19,34816
s_add_u32 s88, s88, 0x100000
Ma 224,64,124
buffer_load_dwordx4 v9, s[4:7], s38 offen offset:1024 lds
Ma 228,68,124
Rt 8,20,32768
s_mov_b32 s90, 0x100000
Mv 228,68,120
s_mov_b64 s[24:25], s[8:9]
s_mov_b64 s[26:27], s[10:11]
Mv 196,68,116
Rt 10,20,34816
v_min_u32 v5, 31, v0
Mv 164,68,112
)" R"(
buffer_load_dwordx4 v10, s[4:7], s38 offen offset:2048 lds
Mv 132,68,108
Rt 12,21,32768
v_lshlrev_b32 v5, 7, v5
Mv 100,68,104
s_mov_b32 s86, 0x188000
Mv 68,68,100
Rt 14,21,34816
Mv 36,68,96
buffer_load_dwordx4 v11, s[4:7], s38 offen offset:3072 lds
Mv 40,72,96
Rt 16,22,32768
Mv 72,72,100
s_add_u32 s38, s38, 0x10000
Mv 104,72,104
Rt 18,22,34816
s_add_u32 m0, s36, 4096
Mv 136,72,108
buffer_load_dwordx4 v8, s[4:7], s38 offen offset:0 lds
Mv 168,72,112
Rt 20,23,32768
Mv 200,72,116
s_cmp_lt_u32 s33, 2
s_cmov_b32 s86, 0x380000
Mv 232,72,120
Rt 22,23,34816
s_lshl_b32 s87, s2, 12
Ma 232,72,124
buffer_load_dwordx4 v9, s[4:7], s38 offen offset:1024 lds
Ma 236,76,124
Rt 24,24,32768
Mv 236,76,120
s_cmp_eq_u32 s33, 1
s_cselect_b32 s28, 0x200000, 0
Mv 204,76,116
Rt 26,24,34816
s_add_u32 s87, s87, s28
Mv 172,76,112
buffer_load_dwordx4 v10, s[4:7], s38 offen offset:2048 lds
Mv 140,76,108
Rt 28,25,32768
s_sub_u32 s87, s87, 0x80000
Mv 108,76,104
Mv 76,76,100
Rt 30,25,34816
Mv 44,76,96
buffer_load_dwordx4 v11, s[4:7], s38 offen offset:3072 lds
Mv 48,80,96
Rb 32,16,32768
Mv 80,80,100
Mv 112,80,104
Rb 36,16,34816
Mv 144,80,108
s_add_u32 s39, s39, 0x40000
Mv 176,80,112
Rb 40,16,40960
s_add_u32 m0, s37, 16384
Mv 208,80,116
buffer_load_dwordx4 v12, s[8:11], s39 offen offset:0 lds
Mv 240,80,120
Rb 44,16,43008
Ma 240,80,124
Ma 244,84,124
Rb 48,16,49152
)" R"(
Mv 244,84,120
Mv 212,84,116
Rb 52,16,51200
Mv 180,84,112
buffer_load_dwordx4 v13, s[8:11], s39 offen offset:1024 lds
Mv 148,84,108
Rb 56,16,57344
Mv 116,84,104
Mv 84,84,100
Rb 60,16,59392
Mv 52,84,96
Mv 56,88,96
Mv 88,88,100
buffer_load_dwordx4 v14, s[8:11], s39 offen offset:2048 lds
Mv 120,88,104
Mv 152,88,108
Mv 184,88,112
Mv 216,88,116
Mv 248,88,120
Ma 248,88,124
buffer_load_dwordx4 v15, s[8:11], s39 offen offset:3072 lds
Ma 252,92,124
Mv 252,92,120
s_add_u32 s88, s88, s90
Mv 220,92,116
Mv 188,92,112
buffer_load_dword v6, v5, s[24:27], s88 offen
Mv 156,92,108
Mv 124,92,104
Mv 92,92,100
Mv 60,92,96
s_waitcnt vmcnt(18) lgkmcnt(0)
s_barrier
Mv 32,0,32
Rt 64,18,49152
Mv 64,0,36
Mv 96,0,40
Rt 66,18,51200
Mv 128,0,44
s_add_u32 s39, s39, 0x40000
Mv 160,0,48
Rt 68,19,49152
s_add_u32 m0, s37, 32768
Mv 192,0,52
buffer_load_dwordx4 v12, s[8:11], s39 offen offset:0 lds
Mv 224,0,56
Rt 70,19,51200
Ma 224,0,60
Ma 228,4,60
Rt 72,20,49152
Mv 228,4,56
Mv 196,4,52
Rt 74,20,51200
Mv 164,4,48
Mv 132,4,44
Rt 76,21,49152
Mv 100,4,40
buffer_load_dwordx4 v13, s[8:11], s39 offen offset:1024 lds
Mv 68,4,36
Rt 78,21,51200
Mv 36,4,32
Mv 40,8,32
Rt 80,22,49152
Mv 72,8,36
Mv 104,8,40
)" R"(
Rt 82,22,51200
Mv 136,8,44
Mv 168,8,48
Rt 84,23,49152
Mv 200,8,52
buffer_load_dwordx4 v14, s[8:11], s39 offen offset:2048 lds
Mv 232,8,56
Rt 86,23,51200
Ma 232,8,60
Ma 236,12,60
Rt 88,24,49152
Mv 236,12,56
Mv 204,12,52
Rt 90,24,51200
Mv 172,12,48
Mv 140,12,44
Rt 92,25,49152
Mv 108,12,40
buffer_load_dwordx4 v15, s[8:11], s39 offen offset:3072 lds
Mv 76,12,36
Rt 94,25,51200
Mv 44,12,32
s_add_u32 s44, s43, 0
Mv 48,16,32
Rb 96,17,32768
s_add_u32 m0, s36, 131072
Mv 80,16,36
buffer_load_dwordx4 v26, s[12:15], s44 offen offset:0 lds
Mv 112,16,40
Rb 100,17,34816
Mv 144,16,44
Mv 176,16,48
Rb 104,17,40960
Mv 208,16,52
buffer_load_dwordx4 v27, s[12:15], s44 offen offset:1024 lds
Mv 240,16,56
Rb 108,17,43008
Ma 240,16,60
Ma 244,20,60
Rb 112,17,49152
Mv 244,20,56
s_add_u32 s44, s43, 12288
buffer_load_dwordx4 v26, s[12:15], s44 offen offset:2048 lds
Mv 212,20,52
Rb 116,17,51200
Mv 180,20,48
Mv 148,20,44
Rb 120,17,57344
Mv 116,20,40
buffer_load_dwordx4 v27, s[12:15], s44 offen offset:3072 lds
Mv 84,20,36
Rb 124,17,59392
Mv 52,20,32
Mv 56,24,32
s_add_u32 m0, s36, 135168
s_add_u32 s44, s43, 128
Mv 88,24,36
buffer_load_dwordx4 v26, s[12:15], s44 offen offset:0 lds
Mv 120,24,40
Mv 152,24,44
Mv 184,24,48
Mv 216,24,52
buffer_load_dwordx4 v27, s[12:15], s44 offen offset:1024 lds
Mv 248,24,56
)" R"(
Ma 248,24,60
Ma 252,28,60
Mv 252,28,56
s_add_u32 s44, s43, 12416
buffer_load_dwordx4 v26, s[12:15], s44 offen offset:2048 lds
Mv 220,28,52
Mv 188,28,48
Mv 156,28,44
Mv 124,28,40
buffer_load_dwordx4 v27, s[12:15], s44 offen offset:3072 lds
Mv 92,28,36
Mv 60,28,32
s_waitcnt vmcnt(17) lgkmcnt(0)
s_barrier
Mv 32,64,96
Rt 0,18,0
Mv 64,64,100
s_add_u32 s38, s38, 0xffff0080
Mv 96,64,104
Rt 2,18,2048
s_add_u32 m0, s36, 32768
Mv 128,64,108
buffer_load_dwordx4 v8, s[4:7], s38 offen offset:0 lds
Mv 160,64,112
Rt 4,19,0
Mv 192,64,116
Mv 224,64,120
Rt 6,19,2048
Ma 224,64,124
buffer_load_dwordx4 v9, s[4:7], s38 offen offset:1024 lds
Ma 228,68,124
Rt 8,20,0
Mv 228,68,120
Mv 196,68,116
Rt 10,20,2048
Mv 164,68,112
buffer_load_dwordx4 v10, s[4:7], s38 offen offset:2048 lds
Mv 132,68,108
Rt 12,21,0
Mv 100,68,104
Mv 68,68,100
Rt 14,21,2048
Mv 36,68,96
buffer_load_dwordx4 v11, s[4:7], s38 offen offset:3072 lds
Mv 40,72,96
Rt 16,22,0
Mv 72,72,100
s_add_u32 s38, s38, 0x10000
Mv 104,72,104
Rt 18,22,2048
s_add_u32 m0, s36, 36864
Mv 136,72,108
buffer_load_dwordx4 v8, s[4:7], s38 offen offset:0 lds
Mv 168,72,112
Rt 20,23,0
Mv 200,72,116
Mv 232,72,120
Rt 22,23,2048
Ma 232,72,124
buffer_load_dwordx4 v9, s[4:7], s38 offen offset:1024 lds
Ma 236,76,124
Rt 24,24,0
Mv 236,76,120
Mv 204,76,116
Rt 26,24,2048
)" R"(
Mv 172,76,112
buffer_load_dwordx4 v10, s[4:7], s38 offen offset:2048 lds
Mv 140,76,108
Rt 28,25,0
Mv 108,76,104
Mv 76,76,100
Rt 30,25,2048
Mv 44,76,96
buffer_load_dwordx4 v11, s[4:7], s38 offen offset:3072 lds
Mv 48,80,96
Rb 32,16,0
Mv 80,80,100
Mv 112,80,104
Rb 36,16,2048
Mv 144,80,108
s_add_u32 s39, s39, 0x40000
Mv 176,80,112
Rb 40,16,8192
s_add_u32 m0, s37, 49152
Mv 208,80,116
buffer_load_dwordx4 v12, s[8:11], s39 offen offset:0 lds
Mv 240,80,120
Rb 44,16,10240
Ma 240,80,124
Ma 244,84,124
Rb 48,16,16384
Mv 244,84,120
Mv 212,84,116
Rb 52,16,18432
Mv 180,84,112
buffer_load_dwordx4 v13, s[8:11], s39 offen offset:1024 lds
Mv 148,84,108
Rb 56,16,24576
Mv 116,84,104
Mv 84,84,100
Rb 60,16,26624
Mv 52,84,96
Mv 56,88,96
Mv 88,88,100
buffer_load_dwordx4 v14, s[8:11], s39 offen offset:2048 lds
Mv 120,88,104
Mv 152,88,108
Mv 184,88,112
Mv 216,88,116
Mv 248,88,120
Ma 248,88,124
buffer_load_dwordx4 v15, s[8:11], s39 offen offset:3072 lds
Ma 252,92,124
Mv 252,92,120
s_add_u32 s88, s88, s90
s_mov_b32 s40, 5
Mv 220,92,116
Mv 188,92,112
buffer_load_dword v6, v5, s[24:27], s88 offen
Mv 156,92,108
Mv 124,92,104
Mv 92,92,100
Mv 60,92,96
s_waitcnt vmcnt(26) lgkmcnt(0)
s_barrier
.LzPAK:
Mv 32,0,32
Rt 64,18,16384
Mv 64,0,36
Mv 96,0,40
)" R"(
Rt 66,18,18432
Mv 128,0,44
s_add_u32 s39, s39, 0x40000
Mv 160,0,48
Rt 68,19,16384
s_add_u32 m0, s37, 0
Mv 192,0,52
buffer_load_dwordx4 v12, s[8:11], s39 offen offset:0 lds
Mv 224,0,56
Rt 70,19,18432
Ma 224,0,60
Ma 228,4,60
Rt 72,20,16384
Mv 228,4,56
Mv 196,4,52
Rt 74,20,18432
Mv 164,4,48
Mv 132,4,44
Rt 76,21,16384
Mv 100,4,40
buffer_load_dwordx4 v13, s[8:11], s39 offen offset:1024 lds
Mv 68,4,36
Rt 78,21,18432
Mv 36,4,32
Mv 40,8,32
Rt 80,22,16384
Mv 72,8,36
Mv 104,8,40
Rt 82,22,18432
Mv 136,8,44
Mv 168,8,48
Rt 84,23,16384
Mv 200,8,52
buffer_load_dwordx4 v14, s[8:11], s39 offen offset:2048 lds
Mv 232,8,56
Rt 86,23,18432
Ma 232,8,60
Ma 236,12,60
Rt 88,24,16384
Mv 236,12,56
Mv 204,12,52
Rt 90,24,18432
Mv 172,12,48
Mv 140,12,44
Rt 92,25,16384
Mv 108,12,40
buffer_load_dwordx4 v15, s[8:11], s39 offen offset:3072 lds
Mv 76,12,36
Rt 94,25,18432
Mv 44,12,32
Mv 48,16,32
Rb 96,17,0
Mv 80,16,36
s_cmp_eq_u32 s40, 3
s_cmov_b64 s[24:25], s[84:85]
Mv 112,16,40
Rb 100,17,2048
Mv 144,16,44
s_cmp_eq_u32 s40, 3
s_cmov_b32 s26, s86
Mv 176,16,48
Rb 104,17,8192
Mv 208,16,52
s_cmp_eq_u32 s40, 3
)" R"(
s_cmov_b32 s88, s87
Mv 240,16,56
Rb 108,17,10240
Ma 240,16,60
s_cmp_eq_u32 s40, 3
s_cmov_b32 s90, 0x80000
Ma 244,20,60
Rb 112,17,16384
Mv 244,20,56
Mv 212,20,52
Rb 116,17,18432
Mv 180,20,48
Mv 148,20,44
Rb 120,17,24576
Mv 116,20,40
Mv 84,20,36
Rb 124,17,26624
Mv 52,20,32
Mv 56,24,32
Mv 88,24,36
Mv 120,24,40
Mv 152,24,44
Mv 184,24,48
Mv 216,24,52
Mv 248,24,56
Ma 248,24,60
Ma 252,28,60
Mv 252,28,56
Mv 220,28,52
Mv 188,28,48
Mv 156,28,44
Mv 124,28,40
Mv 92,28,36
Mv 60,28,32
s_waitcnt vmcnt(9) lgkmcnt(0)
s_barrier
Mv 32,64,96
Rt 0,18,32768
Mv 64,64,100
s_add_u32 s38, s38, 0xffff0080
Mv 96,64,104
Rt 2,18,34816
s_add_u32 m0, s36, 0
Mv 128,64,108
buffer_load_dwordx4 v8, s[4:7], s38 offen offset:0 lds
Mv 160,64,112
Rt 4,19,32768
Mv 192,64,116
Mv 224,64,120
Rt 6,19,34816
Ma 224,64,124
buffer_load_dwordx4 v9, s[4:7], s38 offen offset:1024 lds
Ma 228,68,124
Rt 8,20,32768
Mv 228,68,120
Mv 196,68,116
Rt 10,20,34816
Mv 164,68,112
buffer_load_dwordx4 v10, s[4:7], s38 offen offset:2048 lds
Mv 132,68,108
Rt 12,21,32768
Mv 100,68,104
Mv 68,68,100
Rt 14,21,34816
Mv 36,68,96
)" R"(
buffer_load_dwordx4 v11, s[4:7], s38 offen offset:3072 lds
Mv 40,72,96
Rt 16,22,32768
Mv 72,72,100
s_add_u32 s38, s38, 0x10000
Mv 104,72,104
Rt 18,22,34816
s_add_u32 m0, s36, 4096
Mv 136,72,108
buffer_load_dwordx4 v8, s[4:7], s38 offen offset:0 lds
Mv 168,72,112
Rt 20,23,32768
Mv 200,72,116
Mv 232,72,120
Rt 22,23,34816
Ma 232,72,124
buffer_load_dwordx4 v9, s[4:7], s38 offen offset:1024 lds
Ma 236,76,124
Rt 24,24,32768
Mv 236,76,120
Mv 204,76,116
Rt 26,24,34816
Mv 172,76,112
buffer_load_dwordx4 v10, s[4:7], s38 offen offset:2048 lds
Mv 140,76,108
Rt 28,25,32768
Mv 108,76,104
Mv 76,76,100
Rt 30,25,34816
Mv 44,76,96
buffer_load_dwordx4 v11, s[4:7], s38 offen offset:3072 lds
Mv 48,80,96
Rb 32,16,32768
Mv 80,80,100
Mv 112,80,104
Rb 36,16,34816
Mv 144,80,108
s_add_u32 s39, s39, 0x40000
Mv 176,80,112
Rb 40,16,40960
s_add_u32 m0, s37, 16384
Mv 208,80,116
buffer_load_dwordx4 v12, s[8:11], s39 offen offset:0 lds
Mv 240,80,120
Rb 44,16,43008
Ma 240,80,124
Ma 244,84,124
Rb 48,16,49152
Mv 244,84,120
Mv 212,84,116
Rb 52,16,51200
Mv 180,84,112
buffer_load_dwordx4 v13, s[8:11], s39 offen offset:1024 lds
Mv 148,84,108
Rb 56,16,57344
Mv 116,84,104
Mv 84,84,100
Rb 60,16,59392
Mv 52,84,96
Mv 56,88,96
Mv 88,88,100
buffer_load_dwordx4 v14, s[8:11], s39 offen offset:2048 lds
Mv 120,88,104
Mv 152,88,108
)" R"(
Mv 184,88,112
Mv 216,88,116
Mv 248,88,120
Ma 248,88,124
buffer_load_dwordx4 v15, s[8:11], s39 offen offset:3072 lds
Ma 252,92,124
Mv 252,92,120
s_add_u32 s88, s88, s90
Mv 220,92,116
Mv 188,92,112
buffer_load_dword v6, v5, s[24:27], s88 offen
Mv 156,92,108
Mv 124,92,104
Mv 92,92,100
Mv 60,92,96
s_waitcnt vmcnt(18) lgkmcnt(0)
s_barrier
Mv 32,0,32
Rt 64,18,49152
Mv 64,0,36
Mv 96,0,40
Rt 66,18,51200
Mv 128,0,44
s_add_u32 s39, s39, 0x40000
Mv 160,0,48
Rt 68,19,49152
s_add_u32 m0, s37, 32768
Mv 192,0,52
buffer_load_dwordx4 v12, s[8:11], s39 offen offset:0 lds
Mv 224,0,56
Rt 70,19,51200
Ma 224,0,60
Ma 228,4,60
Rt 72,20,49152
Mv 228,4,56
Mv 196,4,52
Rt 74,20,51200
Mv 164,4,48
Mv 132,4,44
Rt 76,21,49152
Mv 100,4,40
buffer_load_dwordx4 v13, s[8:11], s39 offen offset:1024 lds
Mv 68,4,36
Rt 78,21,51200
Mv 36,4,32
Mv 40,8,32
Rt 80,22,49152
Mv 72,8,36
Mv 104,8,40
Rt 82,22,51200
Mv 136,8,44
Mv 168,8,48
Rt 84,23,49152
Mv 200,8,52
buffer_load_dwordx4 v14, s[8:11], s39 offen offset:2048 lds
Mv 232,8,56
Rt 86,23,51200
Ma 232,8,60
Ma 236,12,60
Rt 88,24,49152
Mv 236,12,56
Mv 204,12,52
Rt 90,24,51200
Mv 172,12,48
Mv 140,12,44
)" R"(
Rt 92,25,49152
Mv 108,12,40
buffer_load_dwordx4 v15, s[8:11], s39 offen offset:3072 lds
Mv 76,12,36
Rt 94,25,51200
Mv 44,12,32
Mv 48,16,32
Rb 96,17,32768
Mv 80,16,36
Mv 112,16,40
Rb 100,17,34816
Mv 144,16,44
Mv 176,16,48
Rb 104,17,40960
Mv 208,16,52
Mv 240,16,56
Rb 108,17,43008
Ma 240,16,60
Ma 244,20,60
Rb 112,17,49152
Mv 244,20,56
Mv 212,20,52
Rb 116,17,51200
Mv 180,20,48
Mv 148,20,44
Rb 120,17,57344
Mv 116,20,40
Mv 84,20,36
Rb 124,17,59392
Mv 52,20,32
Mv 56,24,32
Mv 88,24,36
Mv 120,24,40
Mv 152,24,44
Mv 184,24,48
Mv 216,24,52
Mv 248,24,56
Ma 248,24,60
Ma 252,28,60
Mv 252,28,56
Mv 220,28,52
Mv 188,28,48
Mv 156,28,44
Mv 124,28,40
Mv 92,28,36
Mv 60,28,32
s_waitcnt vmcnt(9) lgkmcnt(0)
s_barrier
Mv 32,64,96
Rt 0,18,0
Mv 64,64,100
s_add_u32 s38, s38, 0xffff0080
Mv 96,64,104
Rt 2,18,2048
s_add_u32 m0, s36, 32768
Mv 128,64,108
buffer_load_dwordx4 v8, s[4:7], s38 offen offset:0 lds
Mv 160,64,112
Rt 4,19,0
Mv 192,64,116
Mv 224,64,120
Rt 6,19,2048
Ma 224,64,124
buffer_load_dwordx4 v9, s[4:7], s38 offen offset:1024 lds
Ma 228,68,124
)" R"(
Rt 8,20,0
Mv 228,68,120
Mv 196,68,116
Rt 10,20,2048
Mv 164,68,112
buffer_load_dwordx4 v10, s[4:7], s38 offen offset:2048 lds
Mv 132,68,108
Rt 12,21,0
Mv 100,68,104
Mv 68,68,100
Rt 14,21,2048
Mv 36,68,96
buffer_load_dwordx4 v11, s[4:7], s38 offen offset:3072 lds
Mv 40,72,96
Rt 16,22,0
Mv 72,72,100
s_add_u32 s38, s38, 0x10000
Mv 104,72,104
Rt 18,22,2048
s_add_u32 m0, s36, 36864
Mv 136,72,108
buffer_load_dwordx4 v8, s[4:7], s38 offen offset:0 lds
Mv 168,72,112
Rt 20,23,0
Mv 200,72,116
Mv 232,72,120
Rt 22,23,2048
Ma 232,72,124
buffer_load_dwordx4 v9, s[4:7], s38 offen offset:1024 lds
Ma 236,76,124
Rt 24,24,0
Mv 236,76,120
Mv 204,76,116
Rt 26,24,2048
Mv 172,76,112
buffer_load_dwordx4 v10, s[4:7], s38 offen offset:2048 lds
Mv 140,76,108
Rt 28,25,0
Mv 108,76,104
Mv 76,76,100
Rt 30,25,2048
Mv 44,76,96
buffer_load_dwordx4 v11, s[4:7], s38 offen offset:3072 lds
Mv 48,80,96
Rb 32,16,0
Mv 80,80,100
Mv 112,80,104
Rb 36,16,2048
Mv 144,80,108
s_add_u32 s39, s39, 0x40000
Mv 176,80,112
Rb 40,16,8192
s_add_u32 m0, s37, 49152
Mv 208,80,116
buffer_load_dwordx4 v12, s[8:11], s39 offen offset:0 lds
Mv 240,80,120
Rb 44,16,10240
Ma 240,80,124
Ma 244,84,124
Rb 48,16,16384
Mv 244,84,120
Mv 212,84,116
Rb 52,16,18432
Mv 180,84,112
)" R"(
buffer_load_dwordx4 v13, s[8:11], s39 offen offset:1024 lds
Mv 148,84,108
Rb 56,16,24576
Mv 116,84,104
Mv 84,84,100
Rb 60,16,26624
Mv 52,84,96
Mv 56,88,96
Mv 88,88,100
buffer_load_dwordx4 v14, s[8:11], s39 offen offset:2048 lds
Mv 120,88,104
Mv 152,88,108
Mv 184,88,112
Mv 216,88,116
Mv 248,88,120
Ma 248,88,124
buffer_load_dwordx4 v15, s[8:11], s39 offen offset:3072 lds
Ma 252,92,124
Mv 252,92,120
s_add_u32 s88, s88, s90
Mv 220,92,116
s_sub_u32 s40, s40, 1
Mv 188,92,112
buffer_load_dword v6, v5, s[24:27], s88 offen
Mv 156,92,108
Mv 124,92,104
Mv 92,92,100
Mv 60,92,96
s_cmp_lg_u32 s40, 0
s_waitcnt vmcnt(18) lgkmcnt(0)
s_barrier
s_cbranch_scc1 .LzPAK
s_branch .LzPAKX
.LzAV:
Mz 32,32,0
Rt 64,18,16384
Mz 36,36,0
s_lshl2_add_u32 s40, s2, s33
s_lshl_b32 s40, s40, 13
Mz 96,40,0
Rt 66,18,18432
v_lshlrev_b32 v3, 7, v0
Mz 100,44,0
s_add_u32 s39, s39, 0x40000
Mz 160,48,0
Rt 68,19,16384
s_add_u32 m0, s37, 0
Mz 164,52,0
buffer_load_dwordx4 v12, s[8:11], s39 offen offset:0 lds
Mz 224,56,0
Rt 70,19,18432
Mz 228,60,0
Mz 236,60,4
Rt 72,20,16384
Mz 232,56,4
Mz 172,52,4
Rt 74,20,18432
Mz 168,48,4
Mz 108,44,4
Rt 76,21,16384
Mz 104,40,4
buffer_load_dwordx4 v13, s[8:11], s39 offen offset:1024 lds
Mz 44,36,4
Rt 78,21,18432
Mz 40,32,4
)" R"(
Mz 48,32,8
Rt 80,22,16384
Mz 52,36,8
Mz 112,40,8
Rt 82,22,18432
Mz 116,44,8
Mz 176,48,8
Rt 84,23,16384
Mz 180,52,8
buffer_load_dwordx4 v14, s[8:11], s39 offen offset:2048 lds
Mz 240,56,8
Rt 86,23,18432
Mz 244,60,8
Mz 252,60,12
Rt 88,24,16384
Mz 248,56,12
Mz 188,52,12
Rt 90,24,18432
Mz 184,48,12
Mz 124,44,12
Rt 92,25,16384
Mz 120,40,12
buffer_load_dwordx4 v15, s[8:11], s39 offen offset:3072 lds
Mz 60,36,12
Rt 94,25,18432
Mz 56,32,12
Mz 64,32,16
Rb 96,17,0
Mz 68,36,16
buffer_load_dword v6, v3, s[4:7], s40 offen
Mz 128,40,16
Rb 100,17,2048
Mz 132,44,16
Mz 192,48,16
Rb 104,17,8192
Mz 196,52,16
v_mfma_f32_16x16x32_bf16 a[224:227], a[56:59], a[16:19], 0
Rb 108,17,10240
v_mfma_f32_16x16x32_bf16 a[228:231], a[60:63], a[16:19], 0
v_mfma_f32_16x16x32_bf16 a[236:239], a[60:63], a[20:23], 0
Rb 112,17,16384
v_mfma_f32_16x16x32_bf16 a[232:235], a[56:59], a[20:23], 0
Mz 204,52,20
Rb 116,17,18432
Mz 200,48,20
Mz 140,44,20
Rb 120,17,24576
Mz 136,40,20
Mz 76,36,20
Rb 124,17,26624
Mz 72,32,20
Mz 80,32,24
Mz 84,36,24
Mz 144,40,24
Mz 148,44,24
Mz 208,48,24
Mz 212,52,24
v_mfma_f32_16x16x32_bf16 a[240:243], a[56:59], a[24:27], 0
v_mfma_f32_16x16x32_bf16 a[244:247], a[60:63], a[24:27], 0
v_mfma_f32_16x16x32_bf16 a[252:255], a[60:63], a[28:31], 0
v_mfma_f32_16x16x32_bf16 a[248:251], a[56:59], a[28:31], 0
Mz 220,52,28
Mz 216,48,28
Mz 156,44,28
)" R"(
Mz 152,40,28
Mz 92,36,28
Mz 88,32,28
s_waitcnt vmcnt(9) lgkmcnt(0)
s_barrier
Mv 32,96,64
Rt 0,18,32768
s_bfe_u32 s88, s2, 0x20005
Mv 36,100,64
s_add_u32 s38, s38, 0xffff0080
s_and_b32 s28, s2, 15
Mv 96,104,64
Rt 2,18,34816
s_add_u32 m0, s36, 0
Mv 100,108,64
buffer_load_dwordx4 v8, s[4:7], s38 offen offset:0 lds
Mv 160,112,64
Rt 4,19,32768
s_lshl4_add_u32 s88, s88, s28
Mv 164,116,64
s_lshl2_add_u32 s88, s88, s33
s_lshl_b32 s88, s88, 12
Mv 224,120,64
Rt 6,19,34816
s_add_u32 s88, s88, 0x100000
Mv 228,124,64
buffer_load_dwordx4 v9, s[4:7], s38 offen offset:1024 lds
Mv 236,124,68
Rt 8,20,32768
s_mov_b32 s90, 0x100000
Mv 232,120,68
s_mov_b64 s[24:25], s[8:9]
s_mov_b64 s[26:27], s[10:11]
Mv 172,116,68
Rt 10,20,34816
v_min_u32 v5, 31, v0
Mv 168,112,68
buffer_load_dwordx4 v10, s[4:7], s38 offen offset:2048 lds
Mv 108,108,68
Rt 12,21,32768
v_lshlrev_b32 v5, 7, v5
Mv 104,104,68
s_mov_b32 s86, 0x188000
Mv 44,100,68
Rt 14,21,34816
Mv 40,96,68
buffer_load_dwordx4 v11, s[4:7], s38 offen offset:3072 lds
Mv 48,96,72
Rt 16,22,32768
Mv 52,100,72
s_add_u32 s38, s38, 0x10000
Mv 112,104,72
Rt 18,22,34816
s_add_u32 m0, s36, 4096
Mv 116,108,72
buffer_load_dwordx4 v8, s[4:7], s38 offen offset:0 lds
Mv 176,112,72
Rt 20,23,32768
Mv 180,116,72
s_cmp_lt_u32 s33, 2
s_cmov_b32 s86, 0x380000
Mv 240,120,72
Rt 22,23,34816
s_lshl_b32 s87, s2, 12
Mv 244,124,72
)" R"(
buffer_load_dwordx4 v9, s[4:7], s38 offen offset:1024 lds
Mv 252,124,76
Rt 24,24,32768
Mv 248,120,76
s_cmp_eq_u32 s33, 1
s_cselect_b32 s28, 0x200000, 0
Mv 188,116,76
Rt 26,24,34816
s_add_u32 s87, s87, s28
Mv 184,112,76
buffer_load_dwordx4 v10, s[4:7], s38 offen offset:2048 lds
Mv 124,108,76
Rt 28,25,32768
s_sub_u32 s87, s87, 0x80000
Mv 120,104,76
Mv 60,100,76
Rt 30,25,34816
Mv 56,96,76
buffer_load_dwordx4 v11, s[4:7], s38 offen offset:3072 lds
Mv 64,96,80
Rb 32,16,32768
Mv 68,100,80
Mv 128,104,80
Rb 36,16,34816
Mv 132,108,80
s_add_u32 s39, s39, 0x40000
Mv 192,112,80
Rb 40,16,40960
s_add_u32 m0, s37, 16384
Mv 196,116,80
buffer_load_dwordx4 v12, s[8:11], s39 offen offset:0 lds
Ma 224,120,80
Rb 44,16,43008
Ma 228,124,80
Ma 236,124,84
Rb 48,16,49152
Ma 232,120,84
Mv 204,116,84
Rb 52,16,51200
Mv 200,112,84
buffer_load_dwordx4 v13, s[8:11], s39 offen offset:1024 lds
Mv 140,108,84
Rb 56,16,57344
Mv 136,104,84
Mv 76,100,84
Rb 60,16,59392
Mv 72,96,84
Mv 80,96,88
Mv 84,100,88
buffer_load_dwordx4 v14, s[8:11], s39 offen offset:2048 lds
Mv 144,104,88
Mv 148,108,88
Mv 208,112,88
Mv 212,116,88
Ma 240,120,88
Ma 244,124,88
buffer_load_dwordx4 v15, s[8:11], s39 offen offset:3072 lds
Ma 252,124,92
Ma 248,120,92
s_add_u32 s88, s88, s90
Mv 220,116,92
Mv 216,112,92
buffer_load_dword v6, v5, s[24:27], s88 offen
Mv 156,108,92
)" R"(
Mv 152,104,92
Mv 92,100,92
Mv 88,96,92
s_waitcnt vmcnt(18) lgkmcnt(0)
s_barrier
Mv 32,32,0
Rt 64,18,49152
Mv 36,36,0
Mv 96,40,0
Rt 66,18,51200
Mv 100,44,0
s_add_u32 s39, s39, 0x40000
Mv 160,48,0
Rt 68,19,49152
s_add_u32 m0, s37, 32768
Mv 164,52,0
buffer_load_dwordx4 v12, s[8:11], s39 offen offset:0 lds
Mv 224,56,0
Rt 70,19,51200
Mv 228,60,0
Mv 236,60,4
Rt 72,20,49152
Mv 232,56,4
Mv 172,52,4
Rt 74,20,51200
Mv 168,48,4
Mv 108,44,4
Rt 76,21,49152
Mv 104,40,4
buffer_load_dwordx4 v13, s[8:11], s39 offen offset:1024 lds
Mv 44,36,4
Rt 78,21,51200
Mv 40,32,4
Mv 48,32,8
Rt 80,22,49152
Mv 52,36,8
Mv 112,40,8
Rt 82,22,51200
Mv 116,44,8
Mv 176,48,8
Rt 84,23,49152
Mv 180,52,8
buffer_load_dwordx4 v14, s[8:11], s39 offen offset:2048 lds
Mv 240,56,8
Rt 86,23,51200
Mv 244,60,8
Mv 252,60,12
Rt 88,24,49152
Mv 248,56,12
Mv 188,52,12
Rt 90,24,51200
Mv 184,48,12
Mv 124,44,12
Rt 92,25,49152
Mv 120,40,12
buffer_load_dwordx4 v15, s[8:11], s39 offen offset:3072 lds
Mv 60,36,12
Rt 94,25,51200
Mv 56,32,12
s_add_u32 s44, s43, 0
Mv 64,32,16
Rb 96,17,32768
s_add_u32 m0, s36, 131072
Mv 68,36,16
buffer_load_dwordx4 v26, s[12:15], s44 offen offset:0 lds
)" R"(
Mv 128,40,16
Rb 100,17,34816
Mv 132,44,16
Mv 192,48,16
Rb 104,17,40960
Mv 196,52,16
buffer_load_dwordx4 v27, s[12:15], s44 offen offset:1024 lds
Ma 224,56,16
Rb 108,17,43008
Ma 228,60,16
Ma 236,60,20
Rb 112,17,49152
Ma 232,56,20
s_add_u32 s44, s43, 12288
buffer_load_dwordx4 v26, s[12:15], s44 offen offset:2048 lds
Mv 204,52,20
Rb 116,17,51200
Mv 200,48,20
Mv 140,44,20
Rb 120,17,57344
Mv 136,40,20
buffer_load_dwordx4 v27, s[12:15], s44 offen offset:3072 lds
Mv 76,36,20
Rb 124,17,59392
Mv 72,32,20
Mv 80,32,24
s_add_u32 m0, s36, 135168
s_add_u32 s44, s43, 128
Mv 84,36,24
buffer_load_dwordx4 v26, s[12:15], s44 offen offset:0 lds
Mv 144,40,24
Mv 148,44,24
Mv 208,48,24
Mv 212,52,24
buffer_load_dwordx4 v27, s[12:15], s44 offen offset:1024 lds
Ma 240,56,24
Ma 244,60,24
Ma 252,60,28
Ma 248,56,28
s_add_u32 s44, s43, 12416
buffer_load_dwordx4 v26, s[12:15], s44 offen offset:2048 lds
Mv 220,52,28
Mv 216,48,28
Mv 156,44,28
Mv 152,40,28
buffer_load_dwordx4 v27, s[12:15], s44 offen offset:3072 lds
Mv 92,36,28
Mv 88,32,28
s_waitcnt vmcnt(17) lgkmcnt(0)
s_barrier
Mv 32,96,64
Rt 0,18,0
Mv 36,100,64
s_add_u32 s38, s38, 0xffff0080
Mv 96,104,64
Rt 2,18,2048
s_add_u32 m0, s36, 32768
Mv 100,108,64
buffer_load_dwordx4 v8, s[4:7], s38 offen offset:0 lds
Mv 160,112,64
Rt 4,19,0
Mv 164,116,64
Mv 224,120,64
Rt 6,19,2048
Mv 228,124,64
)" R"(
buffer_load_dwordx4 v9, s[4:7], s38 offen offset:1024 lds
Mv 236,124,68
Rt 8,20,0
Mv 232,120,68
Mv 172,116,68
Rt 10,20,2048
Mv 168,112,68
buffer_load_dwordx4 v10, s[4:7], s38 offen offset:2048 lds
Mv 108,108,68
Rt 12,21,0
Mv 104,104,68
Mv 44,100,68
Rt 14,21,2048
Mv 40,96,68
buffer_load_dwordx4 v11, s[4:7], s38 offen offset:3072 lds
Mv 48,96,72
Rt 16,22,0
Mv 52,100,72
s_add_u32 s38, s38, 0x10000
Mv 112,104,72
Rt 18,22,2048
s_add_u32 m0, s36, 36864
Mv 116,108,72
buffer_load_dwordx4 v8, s[4:7], s38 offen offset:0 lds
Mv 176,112,72
Rt 20,23,0
Mv 180,116,72
Mv 240,120,72
Rt 22,23,2048
Mv 244,124,72
buffer_load_dwordx4 v9, s[4:7], s38 offen offset:1024 lds
Mv 252,124,76
Rt 24,24,0
Mv 248,120,76
Mv 188,116,76
Rt 26,24,2048
Mv 184,112,76
buffer_load_dwordx4 v10, s[4:7], s38 offen offset:2048 lds
Mv 124,108,76
Rt 28,25,0
Mv 120,104,76
Mv 60,100,76
Rt 30,25,2048
Mv 56,96,76
buffer_load_dwordx4 v11, s[4:7], s38 offen offset:3072 lds
Mv 64,96,80
Rb 32,16,0
Mv 68,100,80
Mv 128,104,80
Rb 36,16,2048
Mv 132,108,80
s_add_u32 s39, s39, 0x40000
Mv 192,112,80
Rb 40,16,8192
s_add_u32 m0, s37, 49152
Mv 196,116,80
buffer_load_dwordx4 v12, s[8:11], s39 offen offset:0 lds
Ma 224,120,80
Rb 44,16,10240
Ma 228,124,80
Ma 236,124,84
Rb 48,16,16384
Ma 232,120,84
Mv 204,116,84
)" R"(
Rb 52,16,18432
Mv 200,112,84
buffer_load_dwordx4 v13, s[8:11], s39 offen offset:1024 lds
Mv 140,108,84
Rb 56,16,24576
Mv 136,104,84
Mv 76,100,84
Rb 60,16,26624
Mv 72,96,84
Mv 80,96,88
Mv 84,100,88
buffer_load_dwordx4 v14, s[8:11], s39 offen offset:2048 lds
Mv 144,104,88
Mv 148,108,88
Mv 208,112,88
Mv 212,116,88
Ma 240,120,88
Ma 244,124,88
buffer_load_dwordx4 v15, s[8:11], s39 offen offset:3072 lds
Ma 252,124,92
Ma 248,120,92
s_add_u32 s88, s88, s90
s_mov_b32 s40, 5
Mv 220,116,92
Mv 216,112,92
buffer_load_dword v6, v5, s[24:27], s88 offen
Mv 156,108,92
Mv 152,104,92
Mv 92,100,92
Mv 88,96,92
s_waitcnt vmcnt(26) lgkmcnt(0)
s_barrier
.LzPAV:
Mv 32,32,0
Rt 64,18,16384
Mv 36,36,0
Mv 96,40,0
Rt 66,18,18432
Mv 100,44,0
s_add_u32 s39, s39, 0x40000
Mv 160,48,0
Rt 68,19,16384
s_add_u32 m0, s37, 0
Mv 164,52,0
buffer_load_dwordx4 v12, s[8:11], s39 offen offset:0 lds
Mv 224,56,0
Rt 70,19,18432
Mv 228,60,0
Mv 236,60,4
Rt 72,20,16384
Mv 232,56,4
Mv 172,52,4
Rt 74,20,18432
Mv 168,48,4
Mv 108,44,4
Rt 76,21,16384
Mv 104,40,4
buffer_load_dwordx4 v13, s[8:11], s39 offen offset:1024 lds
Mv 44,36,4
Rt 78,21,18432
Mv 40,32,4
Mv 48,32,8
Rt 80,22,16384
Mv 52,36,8
Mv 112,40,8
)" R"(
Rt 82,22,18432
Mv 116,44,8
Mv 176,48,8
Rt 84,23,16384
Mv 180,52,8
buffer_load_dwordx4 v14, s[8:11], s39 offen offset:2048 lds
Mv 240,56,8
Rt 86,23,18432
Mv 244,60,8
Mv 252,60,12
Rt 88,24,16384
Mv 248,56,12
Mv 188,52,12
Rt 90,24,18432
Mv 184,48,12
Mv 124,44,12
Rt 92,25,16384
Mv 120,40,12
buffer_load_dwordx4 v15, s[8:11], s39 offen offset:3072 lds
Mv 60,36,12
Rt 94,25,18432
Mv 56,32,12
Mv 64,32,16
Rb 96,17,0
Mv 68,36,16
s_cmp_eq_u32 s40, 3
s_cmov_b64 s[24:25], s[84:85]
Mv 128,40,16
Rb 100,17,2048
Mv 132,44,16
s_cmp_eq_u32 s40, 3
s_cmov_b32 s26, s86
Mv 192,48,16
Rb 104,17,8192
Mv 196,52,16
s_cmp_eq_u32 s40, 3
s_cmov_b32 s88, s87
Ma 224,56,16
Rb 108,17,10240
Ma 228,60,16
s_cmp_eq_u32 s40, 3
s_cmov_b32 s90, 0x80000
Ma 236,60,20
Rb 112,17,16384
Ma 232,56,20
Mv 204,52,20
Rb 116,17,18432
Mv 200,48,20
Mv 140,44,20
Rb 120,17,24576
Mv 136,40,20
Mv 76,36,20
Rb 124,17,26624
Mv 72,32,20
Mv 80,32,24
Mv 84,36,24
Mv 144,40,24
Mv 148,44,24
Mv 208,48,24
Mv 212,52,24
Ma 240,56,24
Ma 244,60,24
Ma 252,60,28
Ma 248,56,28
)" R"(
Mv 220,52,28
Mv 216,48,28
Mv 156,44,28
Mv 152,40,28
Mv 92,36,28
Mv 88,32,28
s_waitcnt vmcnt(9) lgkmcnt(0)
s_barrier
Mv 32,96,64
Rt 0,18,32768
Mv 36,100,64
s_add_u32 s38, s38, 0xffff0080
Mv 96,104,64
Rt 2,18,34816
s_add_u32 m0, s36, 0
Mv 100,108,64
buffer_load_dwordx4 v8, s[4:7], s38 offen offset:0 lds
Mv 160,112,64
Rt 4,19,32768
Mv 164,116,64
Mv 224,120,64
Rt 6,19,34816
Mv 228,124,64
buffer_load_dwordx4 v9, s[4:7], s38 offen offset:1024 lds
Mv 236,124,68
Rt 8,20,32768
Mv 232,120,68
Mv 172,116,68
Rt 10,20,34816
Mv 168,112,68
buffer_load_dwordx4 v10, s[4:7], s38 offen offset:2048 lds
Mv 108,108,68
Rt 12,21,32768
Mv 104,104,68
Mv 44,100,68
Rt 14,21,34816
Mv 40,96,68
buffer_load_dwordx4 v11, s[4:7], s38 offen offset:3072 lds
Mv 48,96,72
Rt 16,22,32768
Mv 52,100,72
s_add_u32 s38, s38, 0x10000
Mv 112,104,72
Rt 18,22,34816
s_add_u32 m0, s36, 4096
Mv 116,108,72
buffer_load_dwordx4 v8, s[4:7], s38 offen offset:0 lds
Mv 176,112,72
Rt 20,23,32768
Mv 180,116,72
Mv 240,120,72
Rt 22,23,34816
Mv 244,124,72
buffer_load_dwordx4 v9, s[4:7], s38 offen offset:1024 lds
Mv 252,124,76
Rt 24,24,32768
Mv 248,120,76
Mv 188,116,76
Rt 26,24,34816
Mv 184,112,76
buffer_load_dwordx4 v10, s[4:7], s38 offen offset:2048 lds
Mv 124,108,76
Rt 28,25,32768
Mv 120,104,76
Mv 60,100,76
)" R"(
Rt 30,25,34816
Mv 56,96,76
buffer_load_dwordx4 v11, s[4:7], s38 offen offset:3072 lds
Mv 64,96,80
Rb 32,16,32768
Mv 68,100,80
Mv 128,104,80
Rb 36,16,34816
Mv 132,108,80
s_add_u32 s39, s39, 0x40000
Mv 192,112,80
Rb 40,16,40960
s_add_u32 m0, s37, 16384
Mv 196,116,80
buffer_load_dwordx4 v12, s[8:11], s39 offen offset:0 lds
Ma 224,120,80
Rb 44,16,43008
Ma 228,124,80
Ma 236,124,84
Rb 48,16,49152
Ma 232,120,84
Mv 204,116,84
Rb 52,16,51200
Mv 200,112,84
buffer_load_dwordx4 v13, s[8:11], s39 offen offset:1024 lds
Mv 140,108,84
Rb 56,16,57344
Mv 136,104,84
Mv 76,100,84
Rb 60,16,59392
Mv 72,96,84
Mv 80,96,88
Mv 84,100,88
buffer_load_dwordx4 v14, s[8:11], s39 offen offset:2048 lds
Mv 144,104,88
Mv 148,108,88
Mv 208,112,88
Mv 212,116,88
Ma 240,120,88
Ma 244,124,88
buffer_load_dwordx4 v15, s[8:11], s39 offen offset:3072 lds
Ma 252,124,92
Ma 248,120,92
s_add_u32 s88, s88, s90
Mv 220,116,92
Mv 216,112,92
buffer_load_dword v6, v5, s[24:27], s88 offen
Mv 156,108,92
Mv 152,104,92
Mv 92,100,92
Mv 88,96,92
s_waitcnt vmcnt(18) lgkmcnt(0)
s_barrier
Mv 32,32,0
Rt 64,18,49152
Mv 36,36,0
Mv 96,40,0
Rt 66,18,51200
Mv 100,44,0
s_add_u32 s39, s39, 0x40000
Mv 160,48,0
Rt 68,19,49152
s_add_u32 m0, s37, 32768
Mv 164,52,0
buffer_load_dwordx4 v12, s[8:11], s39 offen offset:0 lds
)" R"(
Mv 224,56,0
Rt 70,19,51200
Mv 228,60,0
Mv 236,60,4
Rt 72,20,49152
Mv 232,56,4
Mv 172,52,4
Rt 74,20,51200
Mv 168,48,4
Mv 108,44,4
Rt 76,21,49152
Mv 104,40,4
buffer_load_dwordx4 v13, s[8:11], s39 offen offset:1024 lds
Mv 44,36,4
Rt 78,21,51200
Mv 40,32,4
Mv 48,32,8
Rt 80,22,49152
Mv 52,36,8
Mv 112,40,8
Rt 82,22,51200
Mv 116,44,8
Mv 176,48,8
Rt 84,23,49152
Mv 180,52,8
buffer_load_dwordx4 v14, s[8:11], s39 offen offset:2048 lds
Mv 240,56,8
Rt 86,23,51200
Mv 244,60,8
Mv 252,60,12
Rt 88,24,49152
Mv 248,56,12
Mv 188,52,12
Rt 90,24,51200
Mv 184,48,12
Mv 124,44,12
Rt 92,25,49152
Mv 120,40,12
buffer_load_dwordx4 v15, s[8:11], s39 offen offset:3072 lds
Mv 60,36,12
Rt 94,25,51200
Mv 56,32,12
Mv 64,32,16
Rb 96,17,32768
Mv 68,36,16
Mv 128,40,16
Rb 100,17,34816
Mv 132,44,16
Mv 192,48,16
Rb 104,17,40960
Mv 196,52,16
Ma 224,56,16
Rb 108,17,43008
Ma 228,60,16
Ma 236,60,20
Rb 112,17,49152
Ma 232,56,20
Mv 204,52,20
Rb 116,17,51200
Mv 200,48,20
Mv 140,44,20
Rb 120,17,57344
Mv 136,40,20
Mv 76,36,20
)" R"(
Rb 124,17,59392
Mv 72,32,20
Mv 80,32,24
Mv 84,36,24
Mv 144,40,24
Mv 148,44,24
Mv 208,48,24
Mv 212,52,24
Ma 240,56,24
Ma 244,60,24
Ma 252,60,28
Ma 248,56,28
Mv 220,52,28
Mv 216,48,28
Mv 156,44,28
Mv 152,40,28
Mv 92,36,28
Mv 88,32,28
s_waitcnt vmcnt(9) lgkmcnt(0)
s_barrier
Mv 32,96,64
Rt 0,18,0
Mv 36,100,64
s_add_u32 s38, s38, 0xffff0080
Mv 96,104,64
Rt 2,18,2048
s_add_u32 m0, s36, 32768
Mv 100,108,64
buffer_load_dwordx4 v8, s[4:7], s38 offen offset:0 lds
Mv 160,112,64
Rt 4,19,0
Mv 164,116,64
Mv 224,120,64
Rt 6,19,2048
Mv 228,124,64
buffer_load_dwordx4 v9, s[4:7], s38 offen offset:1024 lds
Mv 236,124,68
Rt 8,20,0
Mv 232,120,68
Mv 172,116,68
Rt 10,20,2048
Mv 168,112,68
buffer_load_dwordx4 v10, s[4:7], s38 offen offset:2048 lds
Mv 108,108,68
Rt 12,21,0
Mv 104,104,68
Mv 44,100,68
Rt 14,21,2048
Mv 40,96,68
buffer_load_dwordx4 v11, s[4:7], s38 offen offset:3072 lds
Mv 48,96,72
Rt 16,22,0
Mv 52,100,72
s_add_u32 s38, s38, 0x10000
Mv 112,104,72
Rt 18,22,2048
s_add_u32 m0, s36, 36864
Mv 116,108,72
buffer_load_dwordx4 v8, s[4:7], s38 offen offset:0 lds
Mv 176,112,72
Rt 20,23,0
Mv 180,116,72
Mv 240,120,72
Rt 22,23,2048
Mv 244,124,72
)" R"(
buffer_load_dwordx4 v9, s[4:7], s38 offen offset:1024 lds
Mv 252,124,76
Rt 24,24,0
Mv 248,120,76
Mv 188,116,76
Rt 26,24,2048
Mv 184,112,76
buffer_load_dwordx4 v10, s[4:7], s38 offen offset:2048 lds
Mv 124,108,76
Rt 28,25,0
Mv 120,104,76
Mv 60,100,76
Rt 30,25,2048
Mv 56,96,76
buffer_load_dwordx4 v11, s[4:7], s38 offen offset:3072 lds
Mv 64,96,80
Rb 32,16,0
Mv 68,100,80
Mv 128,104,80
Rb 36,16,2048
Mv 132,108,80
s_add_u32 s39, s39, 0x40000
Mv 192,112,80
Rb 40,16,8192
s_add_u32 m0, s37, 49152
Mv 196,116,80
buffer_load_dwordx4 v12, s[8:11], s39 offen offset:0 lds
Ma 224,120,80
Rb 44,16,10240
Ma 228,124,80
Ma 236,124,84
Rb 48,16,16384
Ma 232,120,84
Mv 204,116,84
Rb 52,16,18432
Mv 200,112,84
buffer_load_dwordx4 v13, s[8:11], s39 offen offset:1024 lds
Mv 140,108,84
Rb 56,16,24576
Mv 136,104,84
Mv 76,100,84
Rb 60,16,26624
Mv 72,96,84
Mv 80,96,88
Mv 84,100,88
buffer_load_dwordx4 v14, s[8:11], s39 offen offset:2048 lds
Mv 144,104,88
Mv 148,108,88
Mv 208,112,88
Mv 212,116,88
Ma 240,120,88
Ma 244,124,88
buffer_load_dwordx4 v15, s[8:11], s39 offen offset:3072 lds
Ma 252,124,92
Ma 248,120,92
s_add_u32 s88, s88, s90
Mv 220,116,92
s_sub_u32 s40, s40, 1
Mv 216,112,92
buffer_load_dword v6, v5, s[24:27], s88 offen
Mv 156,108,92
Mv 152,104,92
Mv 92,100,92
Mv 88,96,92
)" R"(
s_cmp_lg_u32 s40, 0
s_waitcnt vmcnt(18) lgkmcnt(0)
s_barrier
s_cbranch_scc1 .LzPAV
s_branch .LzPAVX
.LzB0:
s_mul_i32 s41, s33, 7168
s_mul_i32 s42, s33, 0x70000
s_lshl_b32 s28, s34, 9
s_add_u32 s42, s42, s28
v_lshrrev_b32 v28, 3, v0
v_and_b32 v29, 0x70, v1
v_lshl_or_b32 v28, v28, 13, v29
v_lshlrev_b32 v29, 1, v0
v_and_b32 v29, 32, v29
v_xor_b32 v28, v28, v29
s_and_b32 s28, s33, 1
s_lshl_b32 s28, s28, 6
v_xor_b32 v240, s28, v28
v_xor_b32 v241, 64, v240
v_add_u32 v241, 0xfc00, v241
v_add_u32 v242, 0x1f800, v240
v_add_u32 v243, 0x1f800, v241
s_sub_u32 s28, 15, s35
s_lshl2_add_u32 s28, s28, s33
s_mul_i32 s43, s28, 28672
v_and_b32 v2, 0x70, v1
v_and_b32 v28, 48, v0
v_xor_b32 v2, v2, v28
v_lshrrev_b32 v28, 3, v0
v_mul_u32_u24 v28, 896, v28
v_add_u32 v2, v2, v28
v_xor_b32 v3, 64, v2
v_add_u32 v3, 6144, v3
v_bfe_u32 v26, v0, 1, 3
v_lshrrev_b32 v28, 4, v0
v_xor_b32 v26, v26, v28
v_lshlrev_b32 v26, 4, v26
v_and_b32 v28, 15, v0
v_lshl_or_b32 v26, v28, 7, v26
s_add_u32 s28, s36, 0x0
v_add_u32 v26, s28, v26
v_xor_b32 v27, 64, v26
s_waitcnt lgkmcnt(0)
s_mov_b32 s16, s68
s_mov_b32 s17, s69
s_mov_b32 s18, 0x380000
s_mov_b32 s19, 0x20000
s_mov_b32 s12, s64
s_mov_b32 s13, s65
s_mov_b32 s14, 0x1c0000
s_mov_b32 s15, 0x20000
s_add_u32 m0, s36, 0
s_add_u32 s44, s43, 0
buffer_load_dwordx4 v2, s[12:15], s44 offen offset:0 lds
buffer_load_dwordx4 v3, s[12:15], s44 offen offset:1024 lds
s_add_u32 s44, s43, 12288
buffer_load_dwordx4 v2, s[12:15], s44 offen offset:2048 lds
buffer_load_dwordx4 v3, s[12:15], s44 offen offset:3072 lds
s_add_u32 m0, s41, 65536
s_add_u32 s92, s42, 0x0
buffer_load_dwordx4 v240, s[16:19], s92 offen offset:0 lds
buffer_load_dwordx4 v241, s[16:19], s92 offen offset:1024 lds
buffer_load_dwordx4 v242, s[16:19], s92 offen offset:2048 lds
buffer_load_dwordx4 v243, s[16:19], s92 offen offset:3072 lds
)" R"(
s_add_u32 m0, s41, 69632
s_add_u32 s92, s42, 0x40000
buffer_load_dwordx4 v240, s[16:19], s92 offen offset:0 lds
buffer_load_dwordx4 v241, s[16:19], s92 offen offset:1024 lds
buffer_load_dwordx4 v242, s[16:19], s92 offen offset:2048 lds
s_add_u32 m0, s36, 4096
s_add_u32 s44, s43, 128
buffer_load_dwordx4 v2, s[12:15], s44 offen offset:0 lds
buffer_load_dwordx4 v3, s[12:15], s44 offen offset:1024 lds
s_add_u32 s44, s43, 12416
buffer_load_dwordx4 v2, s[12:15], s44 offen offset:2048 lds
buffer_load_dwordx4 v3, s[12:15], s44 offen offset:3072 lds
s_add_u32 m0, s36, 32768
s_add_u32 s44, s43, 256
buffer_load_dwordx4 v2, s[12:15], s44 offen offset:0 lds
buffer_load_dwordx4 v3, s[12:15], s44 offen offset:1024 lds
s_add_u32 s44, s43, 12544
buffer_load_dwordx4 v2, s[12:15], s44 offen offset:2048 lds
buffer_load_dwordx4 v3, s[12:15], s44 offen offset:3072 lds
s_add_u32 m0, s36, 36864
s_add_u32 s44, s43, 384
buffer_load_dwordx4 v2, s[12:15], s44 offen offset:0 lds
buffer_load_dwordx4 v3, s[12:15], s44 offen offset:1024 lds
s_add_u32 s44, s43, 12672
buffer_load_dwordx4 v2, s[12:15], s44 offen offset:2048 lds
buffer_load_dwordx4 v3, s[12:15], s44 offen offset:3072 lds
s_add_u32 m0, s41, 98304
s_add_u32 s92, s42, 0x80
buffer_load_dwordx4 v240, s[16:19], s92 offen offset:0 lds
buffer_load_dwordx4 v241, s[16:19], s92 offen offset:1024 lds
buffer_load_dwordx4 v242, s[16:19], s92 offen offset:2048 lds
buffer_load_dwordx4 v243, s[16:19], s92 offen offset:3072 lds
s_add_u32 m0, s41, 102400
s_add_u32 s92, s42, 0x40080
buffer_load_dwordx4 v240, s[16:19], s92 offen offset:0 lds
buffer_load_dwordx4 v241, s[16:19], s92 offen offset:1024 lds
buffer_load_dwordx4 v242, s[16:19], s92 offen offset:2048 lds
s_mov_b32 s28, 0xc108400
s_load_dword s3, s[74:75], s28
s_sub_u32 s94, s2, 128
s_lshl2_add_u32 s94, s94, s33
s_lshl_b32 s94, s94, 2
s_mul_i32 s29, s94, 896
v_mov_b32 v120, 0x40000000
v_cmp_gt_u32_e32 vcc, 56, v0
s_nop 1
v_cndmask_b32_e32 v120, v120, v1, vcc
buffer_load_dwordx4 v[96:99], v120, s[12:15], s29 offen offset:0
buffer_load_dwordx4 v[100:103], v120, s[12:15], s29 offen offset:896
buffer_load_dwordx4 v[104:107], v120, s[12:15], s29 offen offset:1792
buffer_load_dwordx4 v[108:111], v120, s[12:15], s29 offen offset:2688
s_mov_b32 s24, s76
s_mov_b32 s25, s77
s_mov_b32 s26, 0x380
s_mov_b32 s27, 0x20000
buffer_load_dwordx4 v[112:115], v120, s[24:27], 0 offen
s_mov_b32 s24, s78
s_mov_b32 s25, s79
s_mov_b32 s26, 0x380
s_mov_b32 s27, 0x20000
buffer_load_dwordx4 v[116:119], v120, s[24:27], 0 offen
s_mov_b32 s20, s74
s_mov_b32 s21, s75
s_mov_b32 s22, 0xc510000
)" R"(
s_mov_b32 s23, 0x20000
s_mov_b32 s4, s66
s_mov_b32 s5, s67
s_mov_b32 s6, 0x400000
s_mov_b32 s7, 0x20000
s_cmp_eq_u32 s91, 1
s_cselect_b32 s8, s72, s70
s_cselect_b32 s9, s73, s71
s_mov_b32 s10, 0x800000
s_mov_b32 s11, 0x20000
s_load_dwordx2 s[74:75], s[0:1], 96
s_load_dwordx4 s[76:79], s[0:1], 72
v_bfe_u32 v244, v0, 3, 2
v_lshlrev_b32 v244, 5, v244
v_and_b32 v28, 3, v0
v_lshl_or_b32 v244, v28, 3, v244
v_bfe_u32 v28, v0, 2, 2
v_lshl_or_b32 v244, v28, 7, v244
v_lshrrev_b32 v28, 4, v0
v_lshl_or_b32 v244, v28, 10, v244
v_xor_b32 v245, 32, v244
v_xor_b32 v246, 64, v244
v_xor_b32 v247, 96, v244
v_add_u32 v252, 0x20000, v244
v_add_u32 v253, 0x20000, v245
v_add_u32 v254, 0x20000, v246
v_add_u32 v255, 0x20000, v247
v_add_u32 v248, 0x10000, v244
v_add_u32 v249, 0x10000, v245
v_add_u32 v250, 0x10000, v246
v_add_u32 v251, 0x10000, v247
s_mov_b32 s63, 0xe0c0a08
s_mov_b32 s28, 0x100d0b09
s_bitcmp1_b32 s2, 5
s_cmov_b32 s63, 0xf1e1c1a
s_cmov_b32 s28, 0x111f1d1b
s_bitcmp1_b32 s2, 6
s_cmov_b32 s63, s28
s_lshl_b32 s28, s33, 3
s_lshr_b32 s28, s63, s28
s_and_b32 s28, s28, 255
s_lshl_b32 s38, s28, 17
s_add_u32 s38, s38, 0xff80
s_lshl_b32 s39, s33, 16
s_lshl_b32 s28, s34, 9
s_add_u32 s39, s39, s28
s_sub_u32 s39, s39, 0x40000
s_lshl_b32 s37, s33, 12
s_add_u32 s37, s37, 0x10000
v_lshrrev_b32 v28, 4, v0
v_and_b32 v29, 7, v0
v_xor_b32 v28, v28, v29
v_lshlrev_b32 v28, 4, v28
v_lshrrev_b32 v29, 3, v0
v_lshl_or_b32 v8, v29, 11, v28
v_xor_b32 v9, 64, v8
v_add_u32 v9, 15360, v9
v_add_u32 v10, 30720, v8
v_add_u32 v11, 30720, v9
s_and_b32 s28, s33, 1
s_lshl_b32 s28, s28, 7
v_lshrrev_b32 v29, 5, v0
v_mul_u32_u24 v29, 0x2220, v29
v_xor_b32 v29, v29, v1
)" R"(
v_xor_b32 v12, s28, v29
v_xor_b32 v13, 64, v12
v_add_u32 v13, 15360, v13
v_add_u32 v14, 30720, v12
v_add_u32 v15, 30720, v13
v_bfe_u32 v28, v0, 1, 3
v_lshrrev_b32 v29, 4, v0
v_xor_b32 v28, v28, v29
v_lshlrev_b32 v28, 4, v28
v_and_b32 v29, 15, v0
v_lshl_or_b32 v28, v29, 7, v28
s_lshr_b32 s28, s33, 1
s_lshl_b32 s28, s28, 12
v_or_b32 v16, s28, v28
v_xor_b32 v17, 64, v16
v_bfe_u32 v28, v0, 2, 3
v_lshlrev_b32 v28, 5, v28
v_and_b32 v29, 3, v0
v_lshl_or_b32 v28, v29, 3, v28
v_bfe_u32 v29, v0, 2, 2
v_lshl_or_b32 v28, v29, 9, v28
v_lshrrev_b32 v29, 4, v0
v_lshl_or_b32 v28, v29, 12, v28
s_and_b32 s28, s33, 1
s_lshl_b32 s28, s28, 8
s_bitset1_b32 s28, 16
v_or_b32 v18, s28, v28
v_xor_b32 v19, 32, v18
v_xor_b32 v20, 64, v18
v_xor_b32 v21, 96, v18
v_xor_b32 v22, 128, v18
v_xor_b32 v23, 160, v18
v_xor_b32 v24, 192, v18
v_xor_b32 v25, 224, v18
s_lshl_b32 s28, s34, 21
s_lshl_b32 s29, s91, 25
s_add_u32 s28, s28, s29
s_bfe_u32 s29, s63, 0x80000
s_lshl_b32 s29, s29, 16
s_add_u32 s45, s28, s29
s_bfe_u32 s29, s63, 0x80008
s_lshl_b32 s29, s29, 16
s_add_u32 s46, s28, s29
s_bfe_u32 s29, s63, 0x80010
s_lshl_b32 s29, s29, 16
s_add_u32 s47, s28, s29
s_bfe_u32 s29, s63, 0x80018
s_lshl_b32 s29, s29, 16
s_add_u32 s48, s28, s29
s_lshl_b32 s50, s34, 11
s_add_u32 s50, s50, 0xc110000
s_lshl2_add_u32 s28, s91, s93
s_lshl_b32 s28, s28, 7
s_add_u32 s49, s28, s50
s_add_u32 s49, s49, 1024
s_lshl3_add_u32 s28, s34, s35
s_lshl_b32 s52, s28, 7
s_add_u32 s52, s52, 0xc118000
s_lshl4_add_u32 s28, s34, s35
s_lshl_b32 s51, s28, 17
s_add_u32 s51, s51, 0x4000000
s_cmp_lg_u32 s33, 0
s_cmov_b32 s49, 0x40000000
s_cmov_b32 s52, 0x40000000
)" R"(
s_mov_b32 s53, 0x40000
s_mov_b32 s54, 0x3db8aa3b
s_lshl_b32 s89, s34, 21
s_mov_b64 s[60:61], 0xff
s_mov_b32 s62, 0x4e800000
s_add_u32 s67, s89, 0x6000000
s_lshl3_add_u32 s28, s34, s35
s_add_u32 s64, s51, 0x100000
s_lshl2_add_u32 s28, s28, s33
s_lshl_b32 s65, s28, 7
s_add_u32 s65, s65, 0xc120000
s_lshl_b32 s66, s28, 10
s_add_u32 s66, s66, 0xc000000
s_sub_u32 s28, 15, s35
s_lshl2_add_u32 s28, s28, s33
s_lshl_b32 s29, s28, 5
s_add_u32 s29, s29, s34
s_lshl_b32 s73, s29, 15
s_add_u32 s73, s73, 0x8000000
s_lshr_b32 s29, s36, 1
s_xor_b32 s29, s29, s36
s_and_b32 s29, s29, 0x2000
s_add_u32 s73, s73, s29
s_lshl4_add_u32 s28, s28, s34
s_lshl_b32 s68, s28, 7
s_add_u32 s68, s68, 0xc130000
s_xor_b32 s72, s73, 0x3c04000
s_mov_b32 s55, s89
s_mov_b32 s57, 18
s_mov_b32 s59, s67
s_mov_b32 s58, 0
s_cmp_eq_u32 s35, 7
s_cmov_b32 s58, 2
s_cmp_eq_u32 s35, 7
s_cmov_b32 s64, 0x40000000
s_cmp_eq_u32 s35, 7
s_cmov_b32 s66, 0x40000000
s_add_u32 s56, s55, 0x2000000
s_waitcnt vmcnt(21) lgkmcnt(0)
s_barrier
ds_read_b128 v[128:131], v26 offset:0
ds_read_b128 v[132:135], v26 offset:2048
ds_read_b128 v[136:139], v27 offset:0
ds_read_b128 v[140:143], v27 offset:2048
ds_read_b128 v[144:147], v26 offset:4096
ds_read_b128 v[148:151], v26 offset:6144
ds_read_b128 v[152:155], v27 offset:4096
ds_read_b128 v[156:159], v27 offset:6144
Rt 144,248,0
Rt 146,248,512
Rt 148,249,0
Rt 150,249,512
Rt 152,250,0
Rt 154,250,512
Rt 156,251,0
Rt 158,251,512
Rt 160,248,4096
Rt 162,248,4608
Rt 164,249,4096
Rt 166,249,4608
Rt 168,250,4096
Rt 170,250,4608
Rt 172,251,4096
Rt 174,251,4608
s_mul_i32 s3, s3, 1664525
)" R"(
s_add_u32 s3, s3, 1013904223
v_mov_b32 v6, s3
v_min_u32 v7, 1, v0
v_lshlrev_b32 v7, 30, v7
s_waitcnt lgkmcnt(8)
s_barrier
v_mfma_f32_16x16x32_bf16 v[32:35], a[144:147], v[128:131], 0
Rt 128,248,8192
s_add_u32 m0, s41, 131072
s_add_u32 s92, s42, 0x1c0000
v_mfma_f32_16x16x32_bf16 v[40:43], a[144:147], v[132:135], 0
Rt 130,248,8704
buffer_load_dwordx4 v240, s[16:19], s92 offen offset:0 lds
v_mfma_f32_16x16x32_bf16 v[44:47], a[148:151], v[132:135], 0
Rt 132,249,8192
v_mfma_f32_16x16x32_bf16 v[36:39], a[148:151], v[128:131], 0
Rt 134,249,8704
v_mfma_f32_16x16x32_bf16 v[48:51], a[152:155], v[128:131], 0
Rt 136,250,8192
v_mfma_f32_16x16x32_bf16 v[56:59], a[152:155], v[132:135], 0
Rt 138,250,8704
buffer_load_dwordx4 v241, s[16:19], s92 offen offset:1024 lds
v_mfma_f32_16x16x32_bf16 v[60:63], a[156:159], v[132:135], 0
Rt 140,251,8192
v_mfma_f32_16x16x32_bf16 v[52:55], a[156:159], v[128:131], 0
Rt 142,251,8704
s_waitcnt vmcnt(19) lgkmcnt(8)
Mp 32,160,136
Rt 144,248,12288
ds_read_b128 v[160:163], v26 offset:32768
Mp 40,160,140
Rt 146,248,12800
buffer_load_dwordx4 v242, s[16:19], s92 offen offset:2048 lds
Mp 44,164,140
Rt 148,249,12288
ds_read_b128 v[164:167], v26 offset:34816
Mp 36,164,136
Rt 150,249,12800
Mp 48,168,136
Rt 152,250,12288
ds_read_b128 v[168:171], v27 offset:32768
Mp 56,168,140
Rt 154,250,12800
buffer_load_dwordx4 v243, s[16:19], s92 offen offset:3072 lds
Mp 60,172,140
Rt 156,251,12288
ds_read_b128 v[172:175], v27 offset:34816
Mp 52,172,136
Rt 158,251,12800
s_waitcnt vmcnt(17) lgkmcnt(12)
Mp 32,128,144
Rt 160,248,16384
ds_read_b128 v[176:179], v26 offset:36864
s_add_u32 m0, s41, 135168
s_add_u32 s92, s42, 0x200000
Mp 40,128,148
Rt 162,248,16896
buffer_load_dwordx4 v240, s[16:19], s92 offen offset:0 lds
Mp 44,132,148
Rt 164,249,16384
ds_read_b128 v[180:183], v26 offset:38912
Mp 36,132,144
Rt 166,249,16896
Mp 48,136,144
Rt 168,250,16384
)" R"(
ds_read_b128 v[184:187], v27 offset:36864
Mp 56,136,148
Rt 170,250,16896
buffer_load_dwordx4 v241, s[16:19], s92 offen offset:1024 lds
Mp 60,140,148
Rt 172,251,16384
ds_read_b128 v[188:191], v27 offset:38912
Mp 52,140,144
Rt 174,251,16896
s_waitcnt lgkmcnt(12)
Mp 32,144,152
Rt 128,248,20480
Mp 40,144,156
Rt 130,248,20992
buffer_load_dwordx4 v242, s[16:19], s92 offen offset:2048 lds
Mp 44,148,156
Rt 132,249,20480
Mp 36,148,152
Rt 134,249,20992
Mp 48,152,152
Rt 136,250,20480
Mp 56,152,156
Rt 138,250,20992
Mp 60,156,156
Rt 140,251,20480
s_add_u32 s44, s43, 512
Mp 52,156,152
Rt 142,251,20992
s_waitcnt lgkmcnt(8)
Mp 32,160,160
Rt 144,248,24576
s_add_u32 m0, s36, 32768
Mp 40,160,164
Rt 146,248,25088
buffer_load_dwordx4 v2, s[12:15], s44 offen offset:0 lds
Mp 44,164,164
Rt 148,249,24576
Mp 36,164,160
Rt 150,249,25088
Mp 48,168,160
Rt 152,250,24576
Mp 56,168,164
Rt 154,250,25088
buffer_load_dwordx4 v3, s[12:15], s44 offen offset:1024 lds
Mp 60,172,164
Rt 156,251,24576
Mp 52,172,160
Rt 158,251,25088
s_waitcnt vmcnt(15) lgkmcnt(8)
s_barrier
Mp 32,128,168
Rt 160,248,32768
Mp 40,128,172
Rt 162,248,33280
s_add_u32 s44, s43, 12800
buffer_load_dwordx4 v2, s[12:15], s44 offen offset:2048 lds
Mp 44,132,172
Rt 164,249,32768
Mp 36,132,168
Rt 166,249,33280
Mp 48,136,168
Rt 168,250,32768
Mp 56,136,172
Rt 170,250,33280
buffer_load_dwordx4 v3, s[12:15], s44 offen offset:3072 lds
)" R"(
Mp 60,140,172
Rt 172,251,32768
s_add_u32 s44, s43, 640
Mp 52,140,168
Rt 174,251,33280
s_waitcnt vmcnt(11) lgkmcnt(8)
s_barrier
Mp 32,144,176
Rt 128,248,36864
s_add_u32 m0, s36, 36864
Mp 40,144,180
Rt 130,248,37376
buffer_load_dwordx4 v2, s[12:15], s44 offen offset:0 lds
Mp 44,148,180
Rt 132,249,36864
v_dot2_f32_bf16 v120, v96, v112, 0
Mp 36,148,176
Rt 134,249,37376
v_dot2_f32_bf16 v121, v100, v112, 0
v_dot2_f32_bf16 v122, v104, v112, 0
Mp 48,152,176
Rt 136,250,36864
v_dot2_f32_bf16 v123, v108, v112, 0
Mp 56,152,180
Rt 138,250,37376
buffer_load_dwordx4 v3, s[12:15], s44 offen offset:1024 lds
Mp 60,156,180
Rt 140,251,36864
v_dot2_f32_bf16 v124, v96, v116, 0
v_dot2_f32_bf16 v125, v100, v116, 0
Mp 52,156,176
Rt 142,251,37376
s_waitcnt lgkmcnt(8)
v_mfma_f32_16x16x32_bf16 v[64:67], a[160:163], v[128:131], 0
Rt 144,248,40960
v_dot2_f32_bf16 v126, v104, v116, 0
v_mfma_f32_16x16x32_bf16 v[72:75], a[160:163], v[132:135], 0
Rt 146,248,41472
s_add_u32 s44, s43, 12928
buffer_load_dwordx4 v2, s[12:15], s44 offen offset:2048 lds
v_mfma_f32_16x16x32_bf16 v[76:79], a[164:167], v[132:135], 0
Rt 148,249,40960
v_dot2_f32_bf16 v127, v108, v116, 0
v_mfma_f32_16x16x32_bf16 v[68:71], a[164:167], v[128:131], 0
Rt 150,249,41472
v_dot2_f32_bf16 v120, v97, v113, v120
v_mfma_f32_16x16x32_bf16 v[80:83], a[168:171], v[128:131], 0
Rt 152,250,40960
v_dot2_f32_bf16 v121, v101, v113, v121
v_dot2_f32_bf16 v122, v105, v113, v122
v_mfma_f32_16x16x32_bf16 v[88:91], a[168:171], v[132:135], 0
Rt 154,250,41472
buffer_load_dwordx4 v3, s[12:15], s44 offen offset:3072 lds
v_mfma_f32_16x16x32_bf16 v[92:95], a[172:175], v[132:135], 0
Rt 156,251,40960
s_add_u32 s92, s42, 0x1c0080
v_mfma_f32_16x16x32_bf16 v[84:87], a[172:175], v[128:131], 0
Rt 158,251,41472
v_dot2_f32_bf16 v123, v109, v113, v123
s_waitcnt lgkmcnt(8)
Mp 64,128,136
Rt 160,248,45056
s_add_u32 m0, s41, 0
Mp 72,128,140
Rt 162,248,45568
)" R"(
buffer_load_dwordx4 v240, s[16:19], s92 offen offset:0 lds
Mp 76,132,140
Rt 164,249,45056
v_dot2_f32_bf16 v124, v97, v117, v124
Mp 68,132,136
Rt 166,249,45568
v_dot2_f32_bf16 v125, v101, v117, v125
v_dot2_f32_bf16 v126, v105, v117, v126
Mp 80,136,136
Rt 168,250,45056
v_dot2_f32_bf16 v127, v109, v117, v127
Mp 88,136,140
Rt 170,250,45568
buffer_load_dwordx4 v241, s[16:19], s92 offen offset:1024 lds
Mp 92,140,140
Rt 172,251,45056
v_dot2_f32_bf16 v120, v98, v114, v120
v_dot2_f32_bf16 v121, v102, v114, v121
Mp 84,140,136
Rt 174,251,45568
s_waitcnt lgkmcnt(8)
Mp 64,144,144
Rt 128,248,49152
v_dot2_f32_bf16 v122, v106, v114, v122
Mp 72,144,148
Rt 130,248,49664
buffer_load_dwordx4 v242, s[16:19], s92 offen offset:2048 lds
Mp 76,148,148
Rt 132,249,49152
v_dot2_f32_bf16 v123, v110, v114, v123
Mp 68,148,144
Rt 134,249,49664
v_dot2_f32_bf16 v124, v98, v118, v124
Mp 80,152,144
Rt 136,250,49152
v_dot2_f32_bf16 v125, v102, v118, v125
v_dot2_f32_bf16 v126, v106, v118, v126
Mp 88,152,148
Rt 138,250,49664
buffer_load_dwordx4 v243, s[16:19], s92 offen offset:3072 lds
Mp 92,156,148
Rt 140,251,49152
s_add_u32 s92, s42, 0x200080
Mp 84,156,144
Rt 142,251,49664
v_dot2_f32_bf16 v127, v110, v118, v127
s_waitcnt lgkmcnt(8)
Mp 64,160,152
Rt 144,248,53248
s_add_u32 m0, s41, 4096
Mp 72,160,156
Rt 146,248,53760
buffer_load_dwordx4 v240, s[16:19], s92 offen offset:0 lds
Mp 76,164,156
Rt 148,249,53248
v_dot2_f32_bf16 v120, v99, v115, v120
Mp 68,164,152
Rt 150,249,53760
v_dot2_f32_bf16 v121, v103, v115, v121
v_dot2_f32_bf16 v122, v107, v115, v122
Mp 80,168,152
Rt 152,250,53248
v_dot2_f32_bf16 v123, v111, v115, v123
Mp 88,168,156
)" R"(
Rt 154,250,53760
buffer_load_dwordx4 v241, s[16:19], s92 offen offset:1024 lds
Mp 92,172,156
Rt 156,251,53248
v_dot2_f32_bf16 v124, v99, v119, v124
v_dot2_f32_bf16 v125, v103, v119, v125
Mp 84,172,152
Rt 158,251,53760
s_waitcnt vmcnt(10) lgkmcnt(8)
Mp 64,128,160
Rt 160,248,57344
ds_read_b128 v[192:195], v26 offset:32768
Mp 72,128,164
Rt 162,248,57856
buffer_load_dwordx4 v242, s[16:19], s92 offen offset:2048 lds
Mp 76,132,164
Rt 164,249,57344
ds_read_b128 v[196:199], v26 offset:34816
Mp 68,132,160
Rt 166,249,57856
v_dot2_f32_bf16 v126, v107, v119, v126
Mp 80,136,160
Rt 168,250,57344
ds_read_b128 v[200:203], v27 offset:32768
v_dot2_f32_bf16 v127, v111, v119, v127
Mp 88,136,164
Rt 170,250,57856
v_permlane32_swap_b32_e32 v120, v121
Mp 92,140,164
Rt 172,251,57344
ds_read_b128 v[204:207], v27 offset:34816
Mp 84,140,160
Rt 174,251,57856
s_waitcnt vmcnt(7) lgkmcnt(12)
s_barrier
Mp 64,144,168
Rt 128,252,0
ds_read_b128 v[208:211], v26 offset:36864
Mp 72,144,172
Rt 130,252,512
v_add_f32_e32 v120, v120, v121
Mp 76,148,172
Rt 132,253,0
ds_read_b128 v[212:215], v26 offset:38912
Mp 68,148,168
Rt 134,253,512
v_permlane32_swap_b32_e32 v122, v123
v_add_f32_e32 v122, v122, v123
Mp 80,152,168
Rt 136,254,0
ds_read_b128 v[216:219], v27 offset:36864
Mp 88,152,172
Rt 138,254,512
s_add_u32 s44, s43, 768
Mp 92,156,172
Rt 140,255,0
ds_read_b128 v[220:223], v27 offset:38912
v_permlane32_swap_b32_e32 v124, v125
Mp 84,156,168
Rt 142,255,512
s_waitcnt lgkmcnt(12)
s_barrier
Mp 64,160,176
Rt 144,252,4096
s_add_u32 m0, s36, 32768
Mp 72,160,180
)" R"(
Rt 146,252,4608
buffer_load_dwordx4 v2, s[12:15], s44 offen offset:0 lds
Mp 76,164,180
Rt 148,253,4096
v_add_f32_e32 v124, v124, v125
Mp 68,164,176
Rt 150,253,4608
v_permlane32_swap_b32_e32 v126, v127
Mp 80,168,176
Rt 152,254,4096
buffer_load_dwordx4 v3, s[12:15], s44 offen offset:1024 lds
Mp 88,168,180
Rt 154,254,4608
v_add_f32_e32 v126, v126, v127
Mp 92,172,180
Rt 156,255,4096
v_permlane16_swap_b32_e32 v120, v122
Mp 84,172,176
Rt 158,255,4608
s_add_u32 s44, s43, 13056
buffer_load_dwordx4 v2, s[12:15], s44 offen offset:2048 lds
s_waitcnt lgkmcnt(8)
Mp 32,128,184
Rt 160,252,8192
v_add_f32_e32 v120, v120, v122
Mp 40,128,188
Rt 162,252,8704
v_permlane16_swap_b32_e32 v124, v126
Mp 44,132,188
Rt 164,253,8192
buffer_load_dwordx4 v3, s[12:15], s44 offen offset:3072 lds
Mp 36,132,184
Rt 166,253,8704
v_add_f32_e32 v124, v124, v126
v_add_f32_dpp v120, v120, v120 row_ror:8 row_mask:0xf bank_mask:0xf
Mp 48,136,184
Rt 168,254,8192
v_add_f32_dpp v124, v124, v124 row_ror:8 row_mask:0xf bank_mask:0xf
Mp 56,136,188
Rt 170,254,8704
s_add_u32 s92, s42, 0x100
Mp 60,140,188
Rt 172,255,8192
s_add_u32 m0, s41, 65536
v_add_f32_dpp v120, v120, v120 row_ror:4 row_mask:0xf bank_mask:0xf
Mp 52,140,184
Rt 174,255,8704
buffer_load_dwordx4 v240, s[16:19], s92 offen offset:0 lds
s_waitcnt lgkmcnt(8)
Mp 32,144,192
Rt 128,252,12288
v_add_f32_dpp v124, v124, v124 row_ror:4 row_mask:0xf bank_mask:0xf
Mp 40,144,196
Rt 130,252,12800
v_add_f32_dpp v120, v120, v120 row_ror:2 row_mask:0xf bank_mask:0xf
v_add_f32_dpp v124, v124, v124 row_ror:2 row_mask:0xf bank_mask:0xf
Mp 44,148,196
Rt 132,253,12288
v_add_f32_dpp v120, v120, v120 row_ror:1 row_mask:0xf bank_mask:0xf
Mp 36,148,192
Rt 134,253,12800
buffer_load_dwordx4 v241, s[16:19], s92 offen offset:1024 lds
Mp 48,152,192
Rt 136,254,12288
)" R"(
v_add_f32_dpp v124, v124, v124 row_ror:1 row_mask:0xf bank_mask:0xf
Cv 120,120,120
Mp 56,152,196
Rt 138,254,12800
v_lshlrev_b32 v120, 16, v120
Mp 60,156,196
Rt 140,255,12288
Cv 124,124,124
Mp 52,156,192
Rt 142,255,12800
buffer_load_dwordx4 v242, s[16:19], s92 offen offset:2048 lds
s_waitcnt lgkmcnt(8)
Mp 32,160,200
Rt 144,252,16384
v_lshlrev_b32 v124, 16, v124
Mp 40,160,204
Rt 146,252,16896
v_mul_f32_e32 v120, 0xbfb8aa3b, v120
Mp 44,164,204
Rt 148,253,16384
v_lshrrev_b32 v121, 5, v0
Mp 36,164,200
Rt 150,253,16896
buffer_load_dwordx4 v243, s[16:19], s92 offen offset:3072 lds
Mp 48,168,200
Rt 152,254,16384
v_bfe_u32 v122, v0, 4, 1
Mp 56,168,204
Rt 154,254,16896
s_add_u32 s92, s42, 0x40100
Mp 60,172,204
Rt 156,255,16384
s_add_u32 m0, s41, 69632
v_lshl_or_b32 v121, v122, 1, v121
Mp 52,172,200
Rt 158,255,16896
buffer_load_dwordx4 v240, s[16:19], s92 offen offset:0 lds
s_waitcnt vmcnt(5) lgkmcnt(8)
Mp 32,128,208
Rt 160,252,20480
ds_read_b128 v[224:227], v26 offset:32768
Mp 40,128,212
Rt 162,252,20992
v_exp_f32_e32 v120, v120
Mp 44,132,212
Rt 164,253,20480
ds_read_b128 v[228:231], v26 offset:34816
Mp 36,132,208
Rt 166,253,20992
buffer_load_dwordx4 v241, s[16:19], s92 offen offset:1024 lds
Mp 48,136,208
Rt 168,254,20480
ds_read_b128 v[232:235], v27 offset:32768
v_add_f32_e32 v120, 1.0, v120
Mp 56,136,212
Rt 170,254,20992
v_add_u32 v121, s94, v121
Mp 60,140,212
Rt 172,255,20480
ds_read_b128 v[236:239], v27 offset:34816
Mp 52,140,208
Rt 174,255,20992
buffer_load_dwordx4 v242, s[16:19], s92 offen offset:2048 lds
s_waitcnt lgkmcnt(12)
)" R"(
Mp 32,144,216
Rt 128,252,24576
v_add_f32_e32 v125, 0, v124
Mp 40,144,220
Rt 130,252,25088
s_add_u32 s92, s42, 0x180
Mp 44,148,220
Rt 132,253,24576
s_add_u32 m0, s41, 98304
Mp 36,148,216
Rt 134,253,25088
buffer_load_dwordx4 v240, s[16:19], s92 offen offset:0 lds
Mp 48,152,216
Rt 136,254,24576
v_ashrrev_i32 v126, 31, v125
Mp 56,152,220
Rt 138,254,25088
v_or_b32 v126, 0x80000000, v126
Mp 60,156,220
Rt 140,255,24576
v_rcp_f32_e32 v120, v120
Mp 52,156,216
Rt 142,255,25088
buffer_load_dwordx4 v241, s[16:19], s92 offen offset:1024 lds
s_waitcnt lgkmcnt(8)
s_barrier
Mp 32,160,224
Rt 144,244,0
Cv 120,120,120
Mp 40,160,228
Rt 146,244,512
v_lshlrev_b32 v120, 16, v120
v_xor_b32 v125, v125, v126
Mp 44,164,228
Rt 148,245,0
v_and_b32 v125, 0xffff0000, v125
Mp 36,164,224
Rt 150,245,512
buffer_load_dwordx4 v242, s[16:19], s92 offen offset:2048 lds
Mp 48,168,224
Rt 152,246,0
v_sub_u32 v126, 0xffff, v121
v_or_b32 v116, v125, v126
Mp 56,168,228
Rt 154,246,512
v_mov_b32 v117, s3
Mp 60,172,228
Rt 156,247,0
v_mov_b32 v118, v120
Mp 52,172,224
Rt 158,247,512
buffer_load_dwordx4 v243, s[16:19], s92 offen offset:3072 lds
s_waitcnt lgkmcnt(8)
s_barrier
Mp 32,128,232
Rt 160,244,4096
v_mov_b32 v119, v124
Mp 40,128,236
Rt 162,244,4608
s_add_u32 s92, s42, 0x40180
Mp 44,132,236
Rt 164,245,4096
s_add_u32 m0, s41, 102400
Mp 36,132,232
Rt 166,245,4608
buffer_load_dwordx4 v240, s[16:19], s92 offen offset:0 lds
)" R"(
Mp 48,136,232
Rt 168,246,4096
v_and_b32 v122, 15, v0
Mp 56,136,236
Rt 170,246,4608
v_min_u32 v122, 1, v122
Mp 60,140,236
Rt 172,247,4096
v_lshlrev_b32 v122, 30, v122
v_lshl_or_b32 v122, v121, 4, v122
Mp 52,140,232
Rt 174,247,4608
buffer_load_dwordx4 v241, s[16:19], s92 offen offset:1024 lds
s_waitcnt lgkmcnt(8)
Mp 64,144,184
Rt 128,244,8192
s_mov_b32 s94, 0xc100000
Mp 72,144,188
Rt 130,244,8704
buffer_store_dwordx4 v[116:119], v122, s[20:23], s94 offen sc0 sc1
Mp 76,148,188
Rt 132,245,8192
Mp 68,148,184
Rt 134,245,8704
buffer_load_dwordx4 v242, s[16:19], s92 offen offset:2048 lds
Mp 80,152,184
Rt 136,246,8192
v_mul_f32_e32 v32, s54, v32
Mp 88,152,188
Rt 138,246,8704
s_add_u32 s92, s42, 0x1c0100
Mp 92,156,188
Rt 140,247,8192
s_add_u32 m0, s41, 32768
Mp 84,156,184
Rt 142,247,8704
buffer_load_dwordx4 v240, s[16:19], s92 offen offset:0 lds
s_waitcnt lgkmcnt(8)
Mp 64,160,192
Rt 144,244,12288
v_mul_f32_e32 v33, s54, v33
Mp 72,160,196
Rt 146,244,12800
Cv 32,32,33
Mp 76,164,196
Rt 148,245,12288
v_accvgpr_write_b32 a64, v32
v_mul_f32_e32 v34, s54, v34
Mp 68,164,192
Rt 150,245,12800
buffer_load_dwordx4 v241, s[16:19], s92 offen offset:1024 lds
Mp 80,168,192
Rt 152,246,12288
v_mul_f32_e32 v35, s54, v35
Mp 88,168,196
Rt 154,246,12800
Cv 34,34,35
v_accvgpr_write_b32 a65, v34
Mp 92,172,196
Rt 156,247,12288
v_mul_f32_e32 v36, s54, v36
Mp 84,172,192
Rt 158,247,12800
buffer_load_dwordx4 v242, s[16:19], s92 offen offset:2048 lds
)" R"(
s_waitcnt lgkmcnt(8)
Mp 64,128,200
Rt 160,244,16384
v_mul_f32_e32 v37, s54, v37
Cv 36,36,37
Mp 72,128,204
Rt 162,244,16896
v_accvgpr_write_b32 a66, v36
Mp 76,132,204
Rt 164,245,16384
v_mul_f32_e32 v38, s54, v38
Mp 68,132,200
Rt 166,245,16896
buffer_load_dwordx4 v243, s[16:19], s92 offen offset:3072 lds
Mp 80,136,200
Rt 168,246,16384
v_mul_f32_e32 v39, s54, v39
Mp 88,136,204
Rt 170,246,16896
s_add_u32 s92, s42, 0x200100
Mp 92,140,204
Rt 172,247,16384
s_add_u32 m0, s41, 36864
Mp 84,140,200
Rt 174,247,16896
buffer_load_dwordx4 v240, s[16:19], s92 offen offset:0 lds
s_waitcnt lgkmcnt(8)
Mp 64,144,208
Rt 128,244,20480
Cv 38,38,39
Mp 72,144,212
Rt 130,244,20992
v_accvgpr_write_b32 a67, v38
Mp 76,148,212
Rt 132,245,20480
v_mul_f32_e32 v40, s54, v40
v_mul_f32_e32 v41, s54, v41
Mp 68,148,208
Rt 134,245,20992
buffer_load_dwordx4 v241, s[16:19], s92 offen offset:1024 lds
Mp 80,152,208
Rt 136,246,20480
Cv 40,40,41
Mp 88,152,212
Rt 138,246,20992
v_accvgpr_write_b32 a68, v40
v_mul_f32_e32 v42, s54, v42
Mp 92,156,212
Rt 140,247,20480
v_mul_f32_e32 v43, s54, v43
Mp 84,156,208
Rt 142,247,20992
buffer_load_dwordx4 v242, s[16:19], s92 offen offset:2048 lds
s_waitcnt lgkmcnt(8)
Mp 64,160,216
Rt 144,244,24576
Cv 42,42,43
v_accvgpr_write_b32 a69, v42
Mp 72,160,220
Rt 146,244,25088
v_mul_f32_e32 v44, s54, v44
Mp 76,164,220
Rt 148,245,24576
v_mul_f32_e32 v45, s54, v45
)" R"(
Mp 68,164,216
Rt 150,245,25088
s_add_u32 s92, s42, 0x1c0180
Cv 44,44,45
Mp 80,168,216
Rt 152,246,24576
s_add_u32 m0, s41, 131072
Mp 88,168,220
Rt 154,246,25088
buffer_load_dwordx4 v240, s[16:19], s92 offen offset:0 lds
Mp 92,172,220
Rt 156,247,24576
v_accvgpr_write_b32 a70, v44
Mp 84,172,216
Rt 158,247,25088
s_waitcnt vmcnt(16) lgkmcnt(8)
s_barrier
Mp 64,128,224
Rt 160,248,0
v_mul_f32_e32 v46, s54, v46
Mp 72,128,228
Rt 162,248,512
buffer_load_dwordx4 v241, s[16:19], s92 offen offset:1024 lds
Mp 76,132,228
Rt 164,249,0
v_mul_f32_e32 v47, s54, v47
Cv 46,46,47
Mp 68,132,224
Rt 166,249,512
v_accvgpr_write_b32 a71, v46
Mp 80,136,224
Rt 168,250,0
v_mul_f32_e32 v48, s54, v48
Mp 88,136,228
Rt 170,250,512
buffer_load_dwordx4 v242, s[16:19], s92 offen offset:2048 lds
Mp 92,140,228
Rt 172,251,0
v_mul_f32_e32 v49, s54, v49
Mp 84,140,224
Rt 174,251,512
s_waitcnt vmcnt(3) lgkmcnt(8)
s_barrier
Mp 64,144,232
Rt 128,248,4096
Cv 48,48,49
v_accvgpr_write_b32 a72, v48
Mp 72,144,236
Rt 130,248,4608
buffer_load_dwordx4 v243, s[16:19], s92 offen offset:3072 lds
Mp 76,148,236
Rt 132,249,4096
v_mul_f32_e32 v50, s54, v50
Mp 68,148,232
Rt 134,249,4608
s_add_u32 s92, s42, 0x200180
v_mul_f32_e32 v51, s54, v51
Mp 80,152,232
Rt 136,250,4096
s_add_u32 m0, s41, 135168
Mp 88,152,236
Rt 138,250,4608
buffer_load_dwordx4 v240, s[16:19], s92 offen offset:0 lds
Mp 92,156,236
Rt 140,251,4096
Cv 50,50,51
)" R"(
Mp 84,156,232
Rt 142,251,4608
v_accvgpr_write_b32 a73, v50
s_waitcnt lgkmcnt(8)
v_mfma_f32_16x16x32_bf16 v[96:99], a[160:163], v[128:131], 0
Rt 144,248,8192
v_mul_f32_e32 v52, s54, v52
v_mfma_f32_16x16x32_bf16 v[104:107], a[160:163], v[132:135], 0
Rt 146,248,8704
buffer_load_dwordx4 v241, s[16:19], s92 offen offset:1024 lds
v_mfma_f32_16x16x32_bf16 v[108:111], a[164:167], v[132:135], 0
Rt 148,249,8192
v_mul_f32_e32 v53, s54, v53
Cv 52,52,53
v_mfma_f32_16x16x32_bf16 v[100:103], a[164:167], v[128:131], 0
Rt 150,249,8704
v_accvgpr_write_b32 a74, v52
v_mfma_f32_16x16x32_bf16 v[112:115], a[168:171], v[128:131], 0
Rt 152,250,8192
v_mul_f32_e32 v54, s54, v54
v_mfma_f32_16x16x32_bf16 v[120:123], a[168:171], v[132:135], 0
Rt 154,250,8704
buffer_load_dwordx4 v242, s[16:19], s92 offen offset:2048 lds
v_mfma_f32_16x16x32_bf16 v[124:127], a[172:175], v[132:135], 0
Rt 156,251,8192
v_mul_f32_e32 v55, s54, v55
v_mfma_f32_16x16x32_bf16 v[116:119], a[172:175], v[128:131], 0
Rt 158,251,8704
s_waitcnt lgkmcnt(8)
Mp 96,128,136
Rt 160,248,12288
Cv 54,54,55
v_accvgpr_write_b32 a75, v54
Mp 104,128,140
Rt 162,248,12800
s_add_u32 s38, s38, 0xffff0080
Mp 108,132,140
Rt 164,249,12288
s_add_u32 m0, s36, 0
Mp 100,132,136
Rt 166,249,12800
buffer_load_dwordx4 v8, s[4:7], s38 offen offset:0 lds
Mp 112,136,136
Rt 168,250,12288
v_mul_f32_e32 v56, s54, v56
Mp 120,136,140
Rt 170,250,12800
v_mul_f32_e32 v57, s54, v57
Mp 124,140,140
Rt 172,251,12288
Cv 56,56,57
Mp 116,140,136
Rt 174,251,12800
buffer_load_dwordx4 v9, s[4:7], s38 offen offset:1024 lds
s_waitcnt lgkmcnt(8)
Mp 96,144,144
Rt 128,248,16384
v_accvgpr_write_b32 a76, v56
Mp 104,144,148
Rt 130,248,16896
v_mul_f32_e32 v58, s54, v58
Mp 108,148,148
Rt 132,249,16384
v_mul_f32_e32 v59, s54, v59
)" R"(
Cv 58,58,59
Mp 100,148,144
Rt 134,249,16896
buffer_load_dwordx4 v10, s[4:7], s38 offen offset:2048 lds
Mp 112,152,144
Rt 136,250,16384
v_accvgpr_write_b32 a77, v58
Mp 120,152,148
Rt 138,250,16896
v_mul_f32_e32 v60, s54, v60
v_mul_f32_e32 v61, s54, v61
Mp 124,156,148
Rt 140,251,16384
Cv 60,60,61
Mp 116,156,144
Rt 142,251,16896
buffer_load_dwordx4 v11, s[4:7], s38 offen offset:3072 lds
s_waitcnt lgkmcnt(8)
Mp 96,160,152
Rt 144,248,20480
v_accvgpr_write_b32 a78, v60
v_mul_f32_e32 v62, s54, v62
Mp 104,160,156
Rt 146,248,20992
s_add_u32 s38, s38, 0x10000
Mp 108,164,156
Rt 148,249,20480
s_add_u32 m0, s36, 4096
Mp 100,164,152
Rt 150,249,20992
buffer_load_dwordx4 v8, s[4:7], s38 offen offset:0 lds
Mp 112,168,152
Rt 152,250,20480
v_mul_f32_e32 v63, s54, v63
Mp 120,168,156
Rt 154,250,20992
Cv 62,62,63
Mp 124,172,156
Rt 156,251,20480
v_accvgpr_write_b32 a79, v62
Mp 116,172,152
Rt 158,251,20992
buffer_load_dwordx4 v9, s[4:7], s38 offen offset:1024 lds
s_waitcnt lgkmcnt(8)
Mp 96,128,160
Rt 160,248,24576
v_mul_f32_e32 v64, s54, v64
Mp 104,128,164
Rt 162,248,25088
v_mul_f32_e32 v65, s54, v65
Mp 108,132,164
Rt 164,249,24576
Cv 64,64,65
v_accvgpr_write_b32 a80, v64
Mp 100,132,160
Rt 166,249,25088
buffer_load_dwordx4 v10, s[4:7], s38 offen offset:2048 lds
Mp 112,136,160
Rt 168,250,24576
v_mul_f32_e32 v66, s54, v66
Mp 120,136,164
Rt 170,250,25088
v_mul_f32_e32 v67, s54, v67
Cv 66,66,67
)" R"(
Mp 124,140,164
Rt 172,251,24576
v_accvgpr_write_b32 a81, v66
Mp 116,140,160
Rt 174,251,25088
buffer_load_dwordx4 v11, s[4:7], s38 offen offset:3072 lds
s_waitcnt lgkmcnt(8)
Mp 96,144,168
Rt 128,244,32768
v_mul_f32_e32 v68, s54, v68
v_mul_f32_e32 v69, s54, v69
Mp 104,144,172
Rt 130,244,33280
Cv 68,68,69
s_mov_b32 s85, 0xc108800
Mp 108,148,172
Rt 132,245,32768
v_accvgpr_write_b32 a82, v68
v_and_b32 v2, 7, v0
Mp 100,148,168
Rt 134,245,33280
v_mul_f32_e32 v70, s54, v70
v_mul_f32_e32 v71, s54, v71
v_lshlrev_b32 v2, 7, v2
Mp 112,152,168
Rt 136,246,32768
Cv 70,70,71
Mp 120,152,172
Rt 138,246,33280
v_accvgpr_write_b32 a83, v70
buffer_load_dword v4, v2, s[20:23], s85 offen sc0 sc1
Mp 124,156,172
Rt 140,247,32768
v_mul_f32_e32 v72, s54, v72
Mp 116,156,168
Rt 142,247,33280
s_waitcnt lgkmcnt(8)
s_barrier
Mp 96,160,176
Rt 144,244,36864
v_mul_f32_e32 v73, s54, v73
Mp 104,160,180
Rt 146,244,37376
s_add_u32 s39, s39, 0x40000
Mp 108,164,180
Rt 148,245,36864
s_add_u32 m0, s37, 0
Cv 72,72,73
Mp 100,164,176
Rt 150,245,37376
buffer_load_dwordx4 v12, s[8:11], s39 offen offset:0 lds
Mp 112,168,176
Rt 152,246,36864
v_accvgpr_write_b32 a84, v72
Mp 120,168,180
Rt 154,246,37376
v_mul_f32_e32 v74, s54, v74
v_mul_f32_e32 v75, s54, v75
Mp 124,172,180
Rt 156,247,36864
Cv 74,74,75
Mp 116,172,176
Rt 158,247,37376
s_waitcnt lgkmcnt(8)
Mp 96,128,184
)" R"(
Rt 160,244,40960
v_accvgpr_write_b32 a85, v74
v_mul_f32_e32 v76, s54, v76
Mp 104,128,188
Rt 162,244,41472
buffer_load_dwordx4 v13, s[8:11], s39 offen offset:1024 lds
Mp 108,132,188
Rt 164,245,40960
v_mul_f32_e32 v77, s54, v77
Mp 100,132,184
Rt 166,245,41472
Cv 76,76,77
v_accvgpr_write_b32 a86, v76
Mp 112,136,184
Rt 168,246,40960
v_mul_f32_e32 v78, s54, v78
Mp 120,136,188
Rt 170,246,41472
v_mul_f32_e32 v79, s54, v79
Mp 124,140,188
Rt 172,247,40960
Cv 78,78,79
Mp 116,140,184
Rt 174,247,41472
buffer_load_dwordx4 v14, s[8:11], s39 offen offset:2048 lds
s_waitcnt lgkmcnt(8)
Mp 96,144,192
Rt 128,244,45056
v_accvgpr_write_b32 a87, v78
Mp 104,144,196
Rt 130,244,45568
v_mul_f32_e32 v80, s54, v80
Mp 108,148,196
Rt 132,245,45056
v_mul_f32_e32 v81, s54, v81
Cv 80,80,81
Mp 100,148,192
Rt 134,245,45568
v_accvgpr_write_b32 a88, v80
Mp 112,152,192
Rt 136,246,45056
v_mul_f32_e32 v82, s54, v82
Mp 120,152,196
Rt 138,246,45568
buffer_load_dwordx4 v15, s[8:11], s39 offen offset:3072 lds
Mp 124,156,196
Rt 140,247,45056
v_mul_f32_e32 v83, s54, v83
Mp 116,156,192
Rt 142,247,45568
s_waitcnt lgkmcnt(8)
Mp 96,160,200
Rt 144,244,49152
Cv 82,82,83
v_accvgpr_write_b32 a89, v82
Mp 104,160,204
Rt 146,244,49664
s_add_u32 s39, s39, 0x40000
Mp 108,164,204
Rt 148,245,49152
s_add_u32 m0, s37, 16384
Mp 100,164,200
Rt 150,245,49664
buffer_load_dwordx4 v12, s[8:11], s39 offen offset:0 lds
)" R"(
Mp 112,168,200
Rt 152,246,49152
v_mul_f32_e32 v84, s54, v84
Mp 120,168,204
Rt 154,246,49664
v_mul_f32_e32 v85, s54, v85
Mp 124,172,204
Rt 156,247,49152
Cv 84,84,85
Mp 116,172,200
Rt 158,247,49664
v_accvgpr_write_b32 a90, v84
s_waitcnt lgkmcnt(8)
Mp 96,128,208
Rt 160,244,53248
v_mul_f32_e32 v86, s54, v86
Mp 104,128,212
Rt 162,244,53760
buffer_load_dwordx4 v13, s[8:11], s39 offen offset:1024 lds
Mp 108,132,212
Rt 164,245,53248
v_mul_f32_e32 v87, s54, v87
Cv 86,86,87
Mp 100,132,208
Rt 166,245,53760
v_accvgpr_write_b32 a91, v86
Mp 112,136,208
Rt 168,246,53248
v_mul_f32_e32 v88, s54, v88
Mp 120,136,212
Rt 170,246,53760
v_mul_f32_e32 v89, s54, v89
Cv 88,88,89
Mp 124,140,212
Rt 172,247,53248
v_accvgpr_write_b32 a92, v88
Mp 116,140,208
Rt 174,247,53760
buffer_load_dwordx4 v14, s[8:11], s39 offen offset:2048 lds
s_waitcnt lgkmcnt(8)
Mp 96,144,216
Rt 128,244,57344
v_mul_f32_e32 v90, s54, v90
v_mul_f32_e32 v91, s54, v91
Mp 104,144,220
Rt 130,244,57856
Cv 90,90,91
Mp 108,148,220
Rt 132,245,57344
v_accvgpr_write_b32 a93, v90
Mp 100,148,216
Rt 134,245,57856
v_mul_f32_e32 v92, s54, v92
v_mul_f32_e32 v93, s54, v93
Mp 112,152,216
Rt 136,246,57344
Cv 92,92,93
Mp 120,152,220
Rt 138,246,57856
buffer_load_dwordx4 v15, s[8:11], s39 offen offset:3072 lds
Mp 124,156,220
Rt 140,247,57344
v_accvgpr_write_b32 a94, v92
Mp 116,156,216
)" R"(
Rt 142,247,57856
v_mul_f32_e32 v94, s54, v94
s_waitcnt lgkmcnt(8)
Mp 96,160,224
Rt 144,248,32768
v_mul_f32_e32 v95, s54, v95
Mp 104,160,228
Rt 146,248,33280
Cv 94,94,95
Mp 108,164,228
Rt 148,249,32768
v_accvgpr_write_b32 a95, v94
Mp 100,164,224
Rt 150,249,33280
Mp 112,168,224
Rt 152,250,32768
Mp 120,168,228
Rt 154,250,33280
Mp 124,172,228
Rt 156,251,32768
Mp 116,172,224
Rt 158,251,33280
s_waitcnt vmcnt(8) lgkmcnt(8)
s_barrier
Mp 96,128,232
Rt 160,248,36864
v_xor_b32 v4, s3, v4
v_cmp_gt_u32_e32 vcc, 2, v4
s_andn2_b32 s28, 0xff, vcc_lo
s_cbranch_scc1 .Lzsp14
.Lzok14:
Mp 104,128,236
Rt 162,248,37376
s_add_u32 s38, s38, 0xffff0080
v_xor_b32 v4, s3, v4
Mp 108,132,236
Rt 164,249,36864
s_add_u32 m0, s36, 32768
v_and_b32 v4, 1, v4
Mp 100,132,232
Rt 166,249,37376
buffer_load_dwordx4 v8, s[4:7], s38 offen offset:0 lds
Mp 112,136,232
Rt 168,250,36864
v_cmp_eq_u32_e32 vcc, 1, v4
s_mov_b32 s85, vcc_lo
Mp 120,136,236
Rt 170,250,37376
s_and_b32 s28, s2, 7
s_lshr_b32 s28, s85, s28
Mp 124,140,236
Rt 172,251,36864
s_xor_b32 s28, s28, s34
s_and_b32 s28, s28, 1
Mp 116,140,232
Rt 174,251,37376
s_lshl_b32 s84, s28, 13
s_lshl1_add_u32 s28, s36, s84
s_waitcnt lgkmcnt(8)
v_mfma_f32_16x16x32_bf16 v[64:67], a[144:147], v[128:131], 0
Rt 128,248,40960
v_add_u32 v2, s28, v1
v_add_u32 v3, 0x1000, v2
v_mfma_f32_16x16x32_bf16 v[72:75], a[144:147], v[132:135], 0
Rt 130,248,41472
)" R"(
buffer_load_dwordx4 v9, s[4:7], s38 offen offset:1024 lds
v_mfma_f32_16x16x32_bf16 v[76:79], a[148:151], v[132:135], 0
Rt 132,249,40960
s_and_b32 s85, s85, 0xff
s_bcnt1_i32_b32 s28, s85
v_mfma_f32_16x16x32_bf16 v[68:71], a[148:151], v[128:131], 0
Rt 134,249,41472
s_cmp_lg_u32 s28, 4
s_cmov_b32 s85, 0xf0
v_mfma_f32_16x16x32_bf16 v[80:83], a[152:155], v[128:131], 0
Rt 136,250,40960
v_mul_f32_e32 v96, s54, v96
s_and_b32 s28, s2, 7
s_lshr_b32 s85, s85, s28
v_mfma_f32_16x16x32_bf16 v[88:91], a[152:155], v[132:135], 0
Rt 138,250,41472
v_mul_f32_e32 v97, s54, v97
s_and_b32 s85, s85, 1
v_mfma_f32_16x16x32_bf16 v[92:95], a[156:159], v[132:135], 0
Rt 140,251,40960
Cv 96,96,97
v_mfma_f32_16x16x32_bf16 v[84:87], a[156:159], v[128:131], 0
Rt 142,251,41472
buffer_load_dwordx4 v10, s[4:7], s38 offen offset:2048 lds
s_waitcnt lgkmcnt(8)
Mp 64,160,136
Rt 144,248,45056
v_accvgpr_write_b32 a96, v96
Mp 72,160,140
Rt 146,248,45568
v_mul_f32_e32 v98, s54, v98
Mp 76,164,140
Rt 148,249,45056
v_mul_f32_e32 v99, s54, v99
Mp 68,164,136
Rt 150,249,45568
Cv 98,98,99
Mp 80,168,136
Rt 152,250,45056
v_accvgpr_write_b32 a97, v98
Mp 88,168,140
Rt 154,250,45568
buffer_load_dwordx4 v11, s[4:7], s38 offen offset:3072 lds
Mp 92,172,140
Rt 156,251,45056
v_mul_f32_e32 v100, s54, v100
Mp 84,172,136
Rt 158,251,45568
s_waitcnt lgkmcnt(8)
Mp 64,128,144
Rt 160,248,49152
v_mul_f32_e32 v101, s54, v101
Mp 72,128,148
Rt 162,248,49664
s_add_u32 s38, s38, 0x10000
Mp 76,132,148
Rt 164,249,49152
s_add_u32 m0, s36, 36864
Mp 68,132,144
Rt 166,249,49664
buffer_load_dwordx4 v8, s[4:7], s38 offen offset:0 lds
Mp 80,136,144
Rt 168,250,49152
Cv 100,100,101
)" R"(
Mp 88,136,148
Rt 170,250,49664
v_accvgpr_write_b32 a98, v100
Mp 92,140,148
Rt 172,251,49152
v_mul_f32_e32 v102, s54, v102
Mp 84,140,144
Rt 174,251,49664
s_waitcnt lgkmcnt(8)
Mp 64,144,152
Rt 128,248,53248
v_mul_f32_e32 v103, s54, v103
Mp 72,144,156
Rt 130,248,53760
buffer_load_dwordx4 v9, s[4:7], s38 offen offset:1024 lds
Mp 76,148,156
Rt 132,249,53248
Cv 102,102,103
Mp 68,148,152
Rt 134,249,53760
v_accvgpr_write_b32 a99, v102
Mp 80,152,152
Rt 136,250,53248
v_mul_f32_e32 v104, s54, v104
Mp 88,152,156
Rt 138,250,53760
v_mul_f32_e32 v105, s54, v105
Mp 92,156,156
Rt 140,251,53248
Cv 104,104,105
Mp 84,156,152
Rt 142,251,53760
buffer_load_dwordx4 v10, s[4:7], s38 offen offset:2048 lds
s_waitcnt lgkmcnt(8)
Mp 64,160,160
Rt 144,248,57344
v_accvgpr_write_b32 a100, v104
Mp 72,160,164
Rt 146,248,57856
v_mul_f32_e32 v106, s54, v106
Mp 76,164,164
Rt 148,249,57344
v_mul_f32_e32 v107, s54, v107
Mp 68,164,160
Rt 150,249,57856
Cv 106,106,107
Mp 80,168,160
Rt 152,250,57344
v_accvgpr_write_b32 a101, v106
Mp 88,168,164
Rt 154,250,57856
buffer_load_dwordx4 v11, s[4:7], s38 offen offset:3072 lds
Mp 92,172,164
Rt 156,251,57344
v_mul_f32_e32 v108, s54, v108
Mp 84,172,160
Rt 158,251,57856
s_waitcnt lgkmcnt(8)
Mp 64,128,168
Rt 160,252,0
v_mul_f32_e32 v109, s54, v109
Mp 72,128,172
Rt 162,252,512
Cv 108,108,109
)" R"(
Mp 76,132,172
Rt 164,253,0
v_accvgpr_write_b32 a102, v108
Mp 68,132,168
Rt 166,253,512
v_mul_f32_e32 v110, s54, v110
Mp 80,136,168
Rt 168,254,0
v_mul_f32_e32 v111, s54, v111
Mp 88,136,172
Rt 170,254,512
Cv 110,110,111
Mp 92,140,172
Rt 172,255,0
v_accvgpr_write_b32 a103, v110
Mp 84,140,168
Rt 174,255,512
s_waitcnt vmcnt(12) lgkmcnt(8)
s_barrier
Mp 64,144,176
Rt 128,252,4096
v_mul_f32_e32 v112, s54, v112
Mp 72,144,180
Rt 130,252,4608
s_add_u32 s39, s39, 0x40000
Mp 76,148,180
Rt 132,253,4096
s_add_u32 m0, s37, 32768
Mp 68,148,176
Rt 134,253,4608
buffer_load_dwordx4 v12, s[8:11], s39 offen offset:0 lds
Mp 80,152,176
Rt 136,254,4096
v_mul_f32_e32 v113, s54, v113
Mp 88,152,180
Rt 138,254,4608
Cv 112,112,113
Mp 92,156,180
Rt 140,255,4096
v_accvgpr_write_b32 a104, v112
Mp 84,156,176
Rt 142,255,4608
s_waitcnt lgkmcnt(8)
Mp 64,160,184
Rt 144,252,8192
v_mul_f32_e32 v114, s54, v114
Mp 72,160,188
Rt 146,252,8704
buffer_load_dwordx4 v13, s[8:11], s39 offen offset:1024 lds
Mp 76,164,188
Rt 148,253,8192
v_mul_f32_e32 v115, s54, v115
Mp 68,164,184
Rt 150,253,8704
Cv 114,114,115
Mp 80,168,184
Rt 152,254,8192
v_accvgpr_write_b32 a105, v114
Mp 88,168,188
Rt 154,254,8704
v_mul_f32_e32 v116, s54, v116
Mp 92,172,188
Rt 156,255,8192
v_mul_f32_e32 v117, s54, v117
Mp 84,172,184
)" R"(
Rt 158,255,8704
buffer_load_dwordx4 v14, s[8:11], s39 offen offset:2048 lds
s_waitcnt lgkmcnt(8)
Mp 64,128,192
Rt 160,252,12288
Cv 116,116,117
Mp 72,128,196
Rt 162,252,12800
v_accvgpr_write_b32 a106, v116
Mp 76,132,196
Rt 164,253,12288
v_mul_f32_e32 v118, s54, v118
Mp 68,132,192
Rt 166,253,12800
v_mul_f32_e32 v119, s54, v119
Mp 80,136,192
Rt 168,254,12288
Cv 118,118,119
Mp 88,136,196
Rt 170,254,12800
buffer_load_dwordx4 v15, s[8:11], s39 offen offset:3072 lds
Mp 92,140,196
Rt 172,255,12288
v_accvgpr_write_b32 a107, v118
Mp 84,140,192
Rt 174,255,12800
s_waitcnt lgkmcnt(8)
Mp 64,144,200
Rt 128,252,16384
v_mul_f32_e32 v120, s54, v120
Mp 72,144,204
Rt 130,252,16896
s_add_u32 s39, s39, 0x40000
Mp 76,148,204
Rt 132,253,16384
s_add_u32 m0, s37, 49152
Mp 68,148,200
Rt 134,253,16896
buffer_load_dwordx4 v12, s[8:11], s39 offen offset:0 lds
Mp 80,152,200
Rt 136,254,16384
v_mul_f32_e32 v121, s54, v121
Mp 88,152,204
Rt 138,254,16896
Cv 120,120,121
Mp 92,156,204
Rt 140,255,16384
v_accvgpr_write_b32 a108, v120
Mp 84,156,200
Rt 142,255,16896
s_waitcnt lgkmcnt(8)
Mp 64,160,208
Rt 144,252,20480
Rt 0,18,0
Mp 72,160,212
Rt 146,252,20992
buffer_load_dwordx4 v13, s[8:11], s39 offen offset:1024 lds
Rt 2,18,2048
Mp 76,164,212
Rt 148,253,20480
Rt 4,19,0
Mp 68,164,208
Rt 150,253,20992
Rt 6,19,2048
)" R"(
Mp 80,168,208
Rt 152,254,20480
Rt 8,20,0
Mp 88,168,212
Rt 154,254,20992
Rt 10,20,2048
Mp 92,172,212
Rt 156,255,20480
Rt 12,21,0
Mp 84,172,208
Rt 158,255,20992
buffer_load_dwordx4 v14, s[8:11], s39 offen offset:2048 lds
Rt 14,21,2048
Mp 64,128,216
Rt 160,252,24576
Rt 16,22,0
Mp 72,128,220
Rt 162,252,25088
Rt 18,22,2048
Mp 76,132,220
Rt 164,253,24576
Rt 20,23,0
Mp 68,132,216
Rt 166,253,25088
Rt 22,23,2048
Mp 80,136,216
Rt 168,254,24576
Rt 24,24,0
Mp 88,136,220
Rt 170,254,25088
buffer_load_dwordx4 v15, s[8:11], s39 offen offset:3072 lds
Rt 26,24,2048
Mp 92,140,220
Rt 172,255,24576
Rt 28,25,0
Mp 84,140,216
Rt 174,255,25088
Rt 30,25,2048
Mp 64,144,224
Rb 32,16,0
v_mul_f32_e32 v122, s54, v122
Mp 72,144,228
Rb 36,16,2048
v_mul_f32_e32 v123, s54, v123
Mp 76,148,228
Rb 40,16,8192
Cv 122,122,123
Mp 68,148,224
Rb 44,16,10240
v_accvgpr_write_b32 a109, v122
Mp 80,152,224
Rb 48,16,16384
v_mul_f32_e32 v124, s54, v124
Mp 88,152,228
Rb 52,16,18432
v_mul_f32_e32 v125, s54, v125
Mp 92,156,228
Rb 56,16,24576
Cv 124,124,125
Mp 84,156,224
Rb 60,16,26624
s_waitcnt lgkmcnt(9)
Mp 64,160,232
v_accvgpr_write_b32 a110, v124
)" R"(
v_mul_f32_e32 v126, s54, v126
Mp 72,160,236
v_mul_f32_e32 v127, s54, v127
Cv 126,126,127
Mp 76,164,236
v_accvgpr_write_b32 a111, v126
Mp 68,164,232
Mp 80,168,232
Mp 88,168,236
Mp 92,172,236
Mp 84,172,232
s_cmp_eq_u32 s91, 1
s_waitcnt vmcnt(16) lgkmcnt(0)
s_barrier
s_cbranch_scc1 .LzBV
Mz 32,0,32
Rt 128,18,16384
v_mul_f32_e32 v64, s54, v64
Mz 96,0,40
v_mul_f32_e32 v65, s54, v65
Cv 64,64,65
Mz 128,0,44
Rt 130,18,18432
v_accvgpr_write_b32 a112, v64
Mz 160,0,48
s_add_u32 s39, s39, 0x40000
v_mul_f32_e32 v66, s54, v66
Mz 192,0,52
Rt 132,19,16384
s_add_u32 m0, s37, 0
Mz 224,0,56
buffer_load_dwordx4 v12, s[8:11], s39 offen offset:0 lds
v_mfma_f32_16x16x32_bf16 a[192:195], a[0:3], a[60:63], 0
Rt 134,19,18432
v_mul_f32_e32 v67, s54, v67
v_mfma_f32_16x16x32_bf16 a[196:199], a[4:7], a[60:63], 0
Cv 66,66,67
v_accvgpr_write_b32 a113, v66
Mz 228,4,56
Rt 136,20,16384
v_mul_f32_e32 v68, s54, v68
Mz 196,4,52
v_mul_f32_e32 v69, s54, v69
Cv 68,68,69
buffer_store_dwordx4 a[64:67], v2, s[20:23], s51 offen offset:0 sc0 sc1
Mz 164,4,48
Rt 138,20,18432
v_accvgpr_write_b32 a114, v68
Mz 132,4,44
v_mul_f32_e32 v70, s54, v70
v_mul_f32_e32 v71, s54, v71
Mz 100,4,40
Rt 140,21,16384
Cv 70,70,71
Mz 36,4,32
buffer_load_dwordx4 v13, s[8:11], s39 offen offset:1024 lds
Mz 40,8,32
Rt 142,21,18432
v_accvgpr_write_b32 a115, v70
Mz 104,8,40
v_mul_f32_e32 v72, s54, v72
v_mul_f32_e32 v73, s54, v73
Mz 136,8,44
Rt 144,22,16384
Cv 72,72,73
)" R"(
Mz 168,8,48
v_accvgpr_write_b32 a116, v72
v_mul_f32_e32 v74, s54, v74
buffer_store_dwordx4 a[68:71], v2, s[20:23], s51 offen offset:1024 sc0 sc1
Mz 200,8,52
Rt 146,22,18432
v_mul_f32_e32 v75, s54, v75
Mz 232,8,56
Cv 74,74,75
v_accvgpr_write_b32 a117, v74
v_mfma_f32_16x16x32_bf16 a[200:203], a[8:11], a[60:63], 0
Rt 148,23,16384
v_mul_f32_e32 v76, s54, v76
v_mfma_f32_16x16x32_bf16 a[204:207], a[12:15], a[60:63], 0
buffer_load_dwordx4 v14, s[8:11], s39 offen offset:2048 lds
Mz 236,12,56
Rt 150,23,18432
v_mul_f32_e32 v77, s54, v77
Mz 204,12,52
Cv 76,76,77
v_accvgpr_write_b32 a118, v76
Mz 172,12,48
Rt 152,24,16384
v_mul_f32_e32 v78, s54, v78
Mz 140,12,44
v_mul_f32_e32 v79, s54, v79
Cv 78,78,79
buffer_store_dwordx4 a[72:75], v2, s[20:23], s51 offen offset:2048 sc0 sc1
Mz 108,12,40
Rt 154,24,18432
v_accvgpr_write_b32 a119, v78
Mz 44,12,32
v_mul_f32_e32 v80, s54, v80
v_mul_f32_e32 v81, s54, v81
Mz 48,16,32
Rt 156,25,16384
Cv 80,80,81
Mz 112,16,40
buffer_load_dwordx4 v15, s[8:11], s39 offen offset:3072 lds
Mz 144,16,44
Rt 158,25,18432
v_accvgpr_write_b32 a120, v80
Mz 176,16,48
v_mul_f32_e32 v82, s54, v82
v_mul_f32_e32 v83, s54, v83
Mz 208,16,52
Rb 160,17,0
Cv 82,82,83
Mz 240,16,56
v_accvgpr_write_b32 a121, v82
v_mul_f32_e32 v84, s54, v84
buffer_store_dwordx4 a[76:79], v2, s[20:23], s51 offen offset:3072 sc0 sc1
v_mfma_f32_16x16x32_bf16 a[208:211], a[16:19], a[60:63], 0
Rb 164,17,2048
v_mul_f32_e32 v85, s54, v85
v_mfma_f32_16x16x32_bf16 a[212:215], a[20:23], a[60:63], 0
Cv 84,84,85
v_accvgpr_write_b32 a122, v84
Mz 244,20,56
Rb 168,17,8192
v_mul_f32_e32 v86, s54, v86
Mz 212,20,52
v_mul_f32_e32 v87, s54, v87
Cv 86,86,87
)" R"(
Mz 180,20,48
Rb 172,17,10240
v_accvgpr_write_b32 a123, v86
Mz 148,20,44
v_mul_f32_e32 v88, s54, v88
v_mul_f32_e32 v89, s54, v89
Mz 116,20,40
Rb 176,17,16384
Cv 88,88,89
Mz 52,20,32
v_accvgpr_write_b32 a124, v88
v_mul_f32_e32 v90, s54, v90
buffer_store_dwordx4 a[80:83], v3, s[20:23], s51 offen offset:0 sc0 sc1
Mz 56,24,32
Rb 180,17,18432
v_mul_f32_e32 v91, s54, v91
Mz 120,24,40
Cv 90,90,91
v_accvgpr_write_b32 a125, v90
Mz 152,24,44
Rb 184,17,24576
v_mul_f32_e32 v92, s54, v92
Mz 184,24,48
v_mul_f32_e32 v93, s54, v93
Cv 92,92,93
Mz 216,24,52
Rb 188,17,26624
v_accvgpr_write_b32 a126, v92
Mz 248,24,56
v_mul_f32_e32 v94, s54, v94
v_mul_f32_e32 v95, s54, v95
v_mfma_f32_16x16x32_bf16 a[216:219], a[24:27], a[60:63], 0
Cv 94,94,95
v_accvgpr_write_b32 a127, v94
v_mfma_f32_16x16x32_bf16 a[220:223], a[28:31], a[60:63], 0
buffer_store_dwordx4 a[84:87], v3, s[20:23], s51 offen offset:1024 sc0 sc1
Mz 252,28,56
Mz 220,28,52
Mz 188,28,48
Mz 156,28,44
Mz 124,28,40
Mz 60,28,32
Mz 64,0,36
Mz 68,4,36
buffer_store_dwordx4 a[88:91], v3, s[20:23], s51 offen offset:2048 sc0 sc1
Mz 72,8,36
Mz 76,12,36
Mz 80,16,36
Mz 84,20,36
Mz 88,24,36
Mz 92,28,36
s_waitcnt vmcnt(15) lgkmcnt(0)
s_barrier
Mv 32,128,160
Rt 0,18,32768
Mv 64,128,164
s_add_u32 s38, s38, 0xffff0080
Mv 96,128,168
Rt 2,18,34816
s_add_u32 m0, s36, 0
Mv 128,128,172
buffer_load_dwordx4 v8, s[4:7], s38 offen offset:0 lds
Mv 160,128,176
Rt 4,19,32768
Mv 192,128,180
)" R"(
Mv 224,128,184
Rt 6,19,34816
Ma 192,128,188
buffer_load_dwordx4 v9, s[4:7], s38 offen offset:1024 lds
Ma 196,132,188
Rt 8,20,32768
Mv 228,132,184
Mv 196,132,180
Rt 10,20,34816
Mv 164,132,176
buffer_load_dwordx4 v10, s[4:7], s38 offen offset:2048 lds
Mv 132,132,172
Rt 12,21,32768
Mv 100,132,168
Mv 68,132,164
Rt 14,21,34816
Mv 36,132,160
buffer_load_dwordx4 v11, s[4:7], s38 offen offset:3072 lds
Mv 40,136,160
Rt 16,22,32768
Mv 72,136,164
s_add_u32 s38, s38, 0x10000
Mv 104,136,168
Rt 18,22,34816
s_add_u32 m0, s36, 4096
Mv 136,136,172
buffer_load_dwordx4 v8, s[4:7], s38 offen offset:0 lds
Mv 168,136,176
Rt 20,23,32768
Mv 200,136,180
Mv 232,136,184
Rt 22,23,34816
Ma 200,136,188
buffer_load_dwordx4 v9, s[4:7], s38 offen offset:1024 lds
Ma 204,140,188
Rt 24,24,32768
Mv 236,140,184
Mv 204,140,180
Rt 26,24,34816
Mv 172,140,176
buffer_load_dwordx4 v10, s[4:7], s38 offen offset:2048 lds
Mv 140,140,172
Rt 28,25,32768
Mv 108,140,168
Mv 76,140,164
Rt 30,25,34816
Mv 44,140,160
buffer_load_dwordx4 v11, s[4:7], s38 offen offset:3072 lds
Mv 48,144,160
Rb 32,16,32768
Mv 80,144,164
Mv 112,144,168
Rb 36,16,34816
Mv 144,144,172
s_add_u32 s39, s39, 0x40000
Mv 176,144,176
Rb 40,16,40960
s_add_u32 m0, s37, 16384
Mv 208,144,180
buffer_load_dwordx4 v12, s[8:11], s39 offen offset:0 lds
Mv 240,144,184
Rb 44,16,43008
Ma 208,144,188
Ma 212,148,188
)" R"(
Rb 48,16,49152
Mv 244,148,184
Mv 212,148,180
Rb 52,16,51200
Mv 180,148,176
buffer_load_dwordx4 v13, s[8:11], s39 offen offset:1024 lds
Mv 148,148,172
Rb 56,16,57344
Mv 116,148,168
Mv 84,148,164
Rb 60,16,59392
s_lshl2_add_u32 s28, s2, s33
Mv 52,148,160
s_lshl_b32 s28, s28, 11
s_add_u32 s28, s28, 0xc000000
Mv 56,152,160
v_min_u32 v5, 15, v0
v_lshlrev_b32 v5, 7, v5
Mv 88,152,164
buffer_load_dwordx4 v14, s[8:11], s39 offen offset:2048 lds
Mv 120,152,168
buffer_load_dword v5, v5, s[20:23], s28 offen
Mv 152,152,172
Mv 184,152,176
Mv 216,152,180
Mv 248,152,184
Ma 216,152,188
buffer_load_dwordx4 v15, s[8:11], s39 offen offset:3072 lds
Ma 220,156,188
Mv 252,156,184
Mv 220,156,180
Mv 188,156,176
buffer_store_dwordx4 a[92:95], v3, s[20:23], s51 offen offset:3072 sc0 sc1
Mv 156,156,172
Mv 124,156,168
Mv 92,156,164
Mv 60,156,160
s_waitcnt vmcnt(25) lgkmcnt(0)
s_barrier
Mv 32,0,32
Rt 128,18,49152
Mv 64,0,36
Mv 96,0,40
Rt 130,18,51200
v_accvgpr_write_b32 a224, -1
Mv 128,0,44
s_add_u32 s39, s39, 0x40000
v_accvgpr_write_b32 a225, -1
Mv 160,0,48
Rt 132,19,49152
s_add_u32 m0, s37, 32768
Mv 192,0,52
buffer_load_dwordx4 v12, s[8:11], s39 offen offset:0 lds
Mv 224,0,56
Rt 134,19,51200
v_accvgpr_write_b32 a226, -1
Ma 192,0,60
v_accvgpr_write_b32 a227, -1
s_add_u32 s88, s73, 0x4000
Ma 196,4,60
Rt 136,20,49152
s_mov_b32 s40, 2
Mv 228,4,56
s_add_u32 s51, s51, 0x10000
buffer_store_dwordx4 a[96:99], v2, s[20:23], s51 offen offset:0 sc0 sc1
)" R"(
Mv 196,4,52
Rt 138,20,51200
s_mov_b32 s94, 0
Mv 164,4,48
Mv 132,4,44
Rt 140,21,49152
Mv 100,4,40
buffer_load_dwordx4 v13, s[8:11], s39 offen offset:1024 lds
Mv 68,4,36
Rt 142,21,51200
Mv 36,4,32
Mv 40,8,32
Rt 144,22,49152
Mv 72,8,36
buffer_store_dwordx4 a[100:103], v2, s[20:23], s51 offen offset:1024 sc0 sc1
Mv 104,8,40
Rt 146,22,51200
Mv 136,8,44
Mv 168,8,48
Rt 148,23,49152
Mv 200,8,52
buffer_load_dwordx4 v14, s[8:11], s39 offen offset:2048 lds
Mv 232,8,56
Rt 150,23,51200
Ma 200,8,60
Ma 204,12,60
Rt 152,24,49152
Mv 236,12,56
buffer_store_dwordx4 a[104:107], v2, s[20:23], s51 offen offset:2048 sc0 sc1
Mv 204,12,52
Rt 154,24,51200
Mv 172,12,48
Mv 140,12,44
Rt 156,25,49152
Mv 108,12,40
buffer_load_dwordx4 v15, s[8:11], s39 offen offset:3072 lds
Mv 76,12,36
Rt 158,25,51200
Mv 44,12,32
Mv 48,16,32
Rb 160,17,32768
Mv 80,16,36
buffer_store_dwordx4 a[108:111], v2, s[20:23], s51 offen offset:3072 sc0 sc1
Mv 112,16,40
Rb 164,17,34816
s_lshl3_add_u32 s29, s34, s35
Mv 144,16,44
s_lshl2_add_u32 s29, s29, s33
s_mul_i32 s83, s29, 7168
Mv 176,16,48
Rb 168,17,40960
Mv 208,16,52
s_bitcmp1_b32 s34, 0
s_cselect_b32 s29, 0, 448
Mv 240,16,56
Rb 172,17,43008
s_add_u32 s83, s83, s29
Ma 208,16,60
s_lshl_b32 s82, s34, 21
s_add_u32 s82, s82, 0x6000000
Ma 212,20,60
Rb 176,17,49152
s_bfe_u32 s28, s2, 0x40003
Mv 244,20,56
)" R"(
buffer_store_dwordx4 a[112:115], v3, s[20:23], s51 offen offset:0 sc0 sc1
Mv 212,20,52
Rb 180,17,51200
s_lshl2_add_u32 s90, s28, s33
Mv 180,20,48
s_mul_i32 s90, s90, 28672
s_and_b32 s29, s2, 7
Mv 148,20,44
Rb 184,17,57344
s_mul_i32 s87, s29, 448
Mv 116,20,40
s_add_u32 s90, s90, s87
s_mul_i32 s29, s29, 14
Mv 84,20,36
Rb 188,17,59392
s_add_u32 s29, s29, s33
Mv 52,20,32
s_mul_i32 s29, s29, 28
s_add_u32 s86, s28, s29
Mv 56,24,32
s_lshl_b32 s86, s86, 10
s_add_u32 s86, s86, 0xc200000
Mv 88,24,36
buffer_store_dwordx4 a[116:119], v3, s[20:23], s51 offen offset:1024 sc0 sc1
Mv 120,24,40
s_cmp_gt_u32 s28, 13
s_cmov_b32 s86, 0x40000000
Mv 152,24,44
s_lshl_b32 s87, s2, 7
s_add_u32 s87, s87, 0xc150000
Mv 184,24,48
s_cmp_lg_u32 s33, 0
s_cmov_b32 s87, 0x40000000
Mv 216,24,52
v_lshrrev_b32 v4, 5, v0
v_mul_u32_u24 v27, 2688, v4
Mv 248,24,56
v_mul_u32_u24 v4, 384, v4
v_add_u32 v26, v4, v1
Ma 216,24,60
v_and_b32 v4, 31, v0
v_cmp_lt_u32_e32 vcc, 27, v4
Ma 220,28,60
v_mov_b32 v4, 0x40000000
v_lshrrev_b32 v29, 2, v0
Mv 252,28,56
buffer_store_dwordx4 a[120:123], v3, s[20:23], s51 offen offset:2048 sc0 sc1
Mv 220,28,52
v_cndmask_b32_e32 v26, v26, v4, vcc
v_add_u32 v27, v27, v26
Mv 188,28,48
v_and_b32 v4, 48, v1
v_lshl_or_b32 v29, v29, 9, v4
Mv 156,28,44
s_lshr_b32 s28, s33, 1
s_lshl_b32 s28, s28, 6
Mv 124,28,40
s_and_b32 s29, s33, 1
s_lshl3_add_u32 s28, s29, s28
Mv 92,28,36
s_add_u32 s28, s28, 0x20000
v_add_u32 v29, s28, v29
Mv 60,28,32
s_waitcnt vmcnt(17) lgkmcnt(0)
s_barrier
)" R"(
Mv 32,128,160
Rt 0,18,0
v_lshrrev_b32 v4, 4, v0
Mv 64,128,164
s_add_u32 s38, s38, 0xffff0080
v_bfe_u32 v28, v0, 2, 2
Mv 96,128,168
Rt 2,18,2048
s_add_u32 m0, s36, 32768
Mv 128,128,172
buffer_load_dwordx4 v8, s[4:7], s38 offen offset:0 lds
Mv 160,128,176
Rt 4,19,0
v_lshl_or_b32 v28, v4, 3, v28
Mv 192,128,180
v_and_b32 v4, 3, v0
v_lshlrev_b32 v4, 3, v4
Mv 224,128,184
Rt 6,19,2048
v_lshl_or_b32 v28, v28, 9, v4
Ma 192,128,188
buffer_load_dwordx4 v9, s[4:7], s38 offen offset:1024 lds
Ma 196,132,188
Rt 8,20,0
s_lshl_b32 s29, s33, 5
Mv 228,132,184
s_add_u32 s29, s29, 0x24000
v_add_u32 v28, s29, v28
Mv 196,132,180
Rt 10,20,2048
Mv 164,132,176
buffer_load_dwordx4 v10, s[4:7], s38 offen offset:2048 lds
Mv 132,132,172
Rt 12,21,0
Mv 100,132,168
Mv 68,132,164
Rt 14,21,2048
Mv 36,132,160
buffer_load_dwordx4 v11, s[4:7], s38 offen offset:3072 lds
Mv 40,136,160
Rt 16,22,0
Mv 72,136,164
s_add_u32 s38, s38, 0x10000
Mv 104,136,168
Rt 18,22,2048
s_add_u32 m0, s36, 36864
Mv 136,136,172
buffer_load_dwordx4 v8, s[4:7], s38 offen offset:0 lds
Mv 168,136,176
Rt 20,23,0
Mv 200,136,180
Mv 232,136,184
Rt 22,23,2048
Ma 200,136,188
buffer_load_dwordx4 v9, s[4:7], s38 offen offset:1024 lds
Ma 204,140,188
Rt 24,24,0
Mv 236,140,184
Mv 204,140,180
Rt 26,24,2048
Mv 172,140,176
buffer_load_dwordx4 v10, s[4:7], s38 offen offset:2048 lds
Mv 140,140,172
Rt 28,25,0
)" R"(
Mv 108,140,168
Mv 76,140,164
Rt 30,25,2048
Mv 44,140,160
buffer_load_dwordx4 v11, s[4:7], s38 offen offset:3072 lds
Mv 48,144,160
Rb 32,16,0
Mv 80,144,164
Mv 112,144,168
Rb 36,16,2048
Mv 144,144,172
s_add_u32 s39, s39, 0x40000
Mv 176,144,176
Rb 40,16,8192
s_add_u32 m0, s37, 49152
Mv 208,144,180
buffer_load_dwordx4 v12, s[8:11], s39 offen offset:0 lds
Mv 240,144,184
Rb 44,16,10240
Ma 208,144,188
Ma 212,148,188
Rb 48,16,16384
Mv 244,148,184
Mv 212,148,180
Rb 52,16,18432
Mv 180,148,176
buffer_load_dwordx4 v13, s[8:11], s39 offen offset:1024 lds
Mv 148,148,172
Rb 56,16,24576
Mv 116,148,168
Mv 84,148,164
Rb 60,16,26624
Mv 52,148,160
Mv 56,152,160
Mv 88,152,164
buffer_load_dwordx4 v14, s[8:11], s39 offen offset:2048 lds
Mv 120,152,168
Mv 152,152,172
Mv 184,152,176
Mv 216,152,180
Mv 248,152,184
Ma 216,152,188
buffer_load_dwordx4 v15, s[8:11], s39 offen offset:3072 lds
Ma 220,156,188
Mv 252,156,184
Mv 220,156,180
Mv 188,156,176
buffer_store_dwordx4 a[124:127], v3, s[20:23], s51 offen offset:3072 sc0 sc1
Mv 156,156,172
Mv 124,156,168
Mv 92,156,164
Mv 60,156,160
s_waitcnt vmcnt(25) lgkmcnt(0)
s_barrier
.LzPBK:
Mv 32,0,32
Rt 128,18,16384
Mv 64,0,36
Mv 96,0,40
Rt 130,18,18432
Mv 128,0,44
s_add_u32 s39, s39, 0x40000
Mv 160,0,48
Rt 132,19,16384
s_add_u32 m0, s37, 0
)" R"(
Mv 192,0,52
buffer_load_dwordx4 v12, s[8:11], s39 offen offset:0 lds
Mv 224,0,56
Rt 134,19,18432
Ma 192,0,60
Ma 196,4,60
Rt 136,20,16384
Mv 228,4,56
Mv 196,4,52
Rt 138,20,18432
Mv 164,4,48
Mv 132,4,44
Rt 140,21,16384
Mv 100,4,40
buffer_load_dwordx4 v13, s[8:11], s39 offen offset:1024 lds
Mv 68,4,36
Rt 142,21,18432
Mv 36,4,32
Mv 40,8,32
Rt 144,22,16384
Mv 72,8,36
Mv 104,8,40
Rt 146,22,18432
Mv 136,8,44
Mv 168,8,48
Rt 148,23,16384
Mv 200,8,52
buffer_load_dwordx4 v14, s[8:11], s39 offen offset:2048 lds
Mv 232,8,56
Rt 150,23,18432
Ma 200,8,60
Ma 204,12,60
Rt 152,24,16384
Mv 236,12,56
Mv 204,12,52
Rt 154,24,18432
Mv 172,12,48
Mv 140,12,44
Rt 156,25,16384
Mv 108,12,40
buffer_load_dwordx4 v15, s[8:11], s39 offen offset:3072 lds
Mv 76,12,36
Rt 158,25,18432
Mv 44,12,32
Mv 48,16,32
Rb 160,17,0
Mv 80,16,36
Mv 112,16,40
Rb 164,17,2048
Mv 144,16,44
Mv 176,16,48
Rb 168,17,8192
Mv 208,16,52
buffer_store_dwordx4 a[224:227], v1, s[20:23], s72 offen sc0 sc1
Mv 240,16,56
Rb 172,17,10240
s_add_u32 s72, s72, 1024
Ma 208,16,60
Ma 212,20,60
Rb 176,17,16384
Mv 244,20,56
Mv 212,20,52
Rb 180,17,18432
Mv 180,20,48
)" R"(
Mv 148,20,44
Rb 184,17,24576
Mv 116,20,40
Mv 84,20,36
Rb 188,17,26624
Mv 52,20,32
Mv 56,24,32
Mv 88,24,36
buffer_store_dwordx4 a[224:227], v1, s[20:23], s88 offen sc0 sc1
Mv 120,24,40
s_add_u32 s88, s88, 1024
Mv 152,24,44
Mv 184,24,48
Mv 216,24,52
Mv 248,24,56
Ma 216,24,60
Ma 220,28,60
Mv 252,28,56
Mv 220,28,52
Mv 188,28,48
Mv 156,28,44
Mv 124,28,40
Mv 92,28,36
Mv 60,28,32
s_waitcnt vmcnt(10) lgkmcnt(0)
s_barrier
Mv 32,128,160
Rt 0,18,32768
Mv 64,128,164
s_add_u32 s38, s38, 0xffff0080
Mv 96,128,168
Rt 2,18,34816
s_add_u32 m0, s36, 0
Mv 128,128,172
buffer_load_dwordx4 v8, s[4:7], s38 offen offset:0 lds
Mv 160,128,176
Rt 4,19,32768
Mv 192,128,180
Mv 224,128,184
Rt 6,19,34816
Ma 192,128,188
buffer_load_dwordx4 v9, s[4:7], s38 offen offset:1024 lds
Ma 196,132,188
Rt 8,20,32768
Mv 228,132,184
Mv 196,132,180
Rt 10,20,34816
Mv 164,132,176
buffer_load_dwordx4 v10, s[4:7], s38 offen offset:2048 lds
Mv 132,132,172
Rt 12,21,32768
Mv 100,132,168
Mv 68,132,164
Rt 14,21,34816
Mv 36,132,160
buffer_load_dwordx4 v11, s[4:7], s38 offen offset:3072 lds
Mv 40,136,160
Rt 16,22,32768
Mv 72,136,164
s_add_u32 s38, s38, 0x10000
Mv 104,136,168
Rt 18,22,34816
s_add_u32 m0, s36, 4096
Mv 136,136,172
buffer_load_dwordx4 v8, s[4:7], s38 offen offset:0 lds
)" R"(
Mv 168,136,176
Rt 20,23,32768
Mv 200,136,180
Mv 232,136,184
Rt 22,23,34816
Ma 200,136,188
buffer_load_dwordx4 v9, s[4:7], s38 offen offset:1024 lds
Ma 204,140,188
Rt 24,24,32768
Mv 236,140,184
Mv 204,140,180
Rt 26,24,34816
Mv 172,140,176
buffer_load_dwordx4 v10, s[4:7], s38 offen offset:2048 lds
Mv 140,140,172
Rt 28,25,32768
Mv 108,140,168
Mv 76,140,164
Rt 30,25,34816
Mv 44,140,160
buffer_load_dwordx4 v11, s[4:7], s38 offen offset:3072 lds
Mv 48,144,160
Rb 32,16,32768
Mv 80,144,164
Mv 112,144,168
Rb 36,16,34816
Mv 144,144,172
s_add_u32 s39, s39, 0x40000
Mv 176,144,176
Rb 40,16,40960
s_add_u32 m0, s37, 16384
Mv 208,144,180
buffer_load_dwordx4 v12, s[8:11], s39 offen offset:0 lds
Mv 240,144,184
Rb 44,16,43008
Ma 208,144,188
Ma 212,148,188
Rb 48,16,49152
Mv 244,148,184
Mv 212,148,180
Rb 52,16,51200
Mv 180,148,176
buffer_load_dwordx4 v13, s[8:11], s39 offen offset:1024 lds
Mv 148,148,172
Rb 56,16,57344
Mv 116,148,168
Mv 84,148,164
Rb 60,16,59392
Mv 52,148,160
Mv 56,152,160
Mv 88,152,164
buffer_load_dwordx4 v14, s[8:11], s39 offen offset:2048 lds
Mv 120,152,168
Mv 152,152,172
Mv 184,152,176
Mv 216,152,180
Mv 248,152,184
Ma 216,152,188
buffer_load_dwordx4 v15, s[8:11], s39 offen offset:3072 lds
Ma 220,156,188
Mv 252,156,184
Mv 220,156,180
Mv 188,156,176
Mv 156,156,172
)" R"(
Mv 124,156,168
Mv 92,156,164
Mv 60,156,160
s_waitcnt vmcnt(18) lgkmcnt(0)
s_barrier
buffer_store_dword v6, v7, s[20:23], s52 offen offset:0 sc0 sc1
s_mov_b32 s52, 0x40000000
Mv 32,0,32
Rt 128,18,49152
Mv 64,0,36
Mv 96,0,40
Rt 130,18,51200
Mv 128,0,44
s_add_u32 s39, s39, 0x40000
Mv 160,0,48
Rt 132,19,49152
s_add_u32 m0, s37, 32768
Mv 192,0,52
buffer_load_dwordx4 v12, s[8:11], s39 offen offset:0 lds
Mv 224,0,56
Rt 134,19,51200
Ma 192,0,60
Ma 196,4,60
Rt 136,20,49152
Mv 228,4,56
Mv 196,4,52
Rt 138,20,51200
Mv 164,4,48
Mv 132,4,44
Rt 140,21,49152
Mv 100,4,40
buffer_load_dwordx4 v13, s[8:11], s39 offen offset:1024 lds
Mv 68,4,36
Rt 142,21,51200
Mv 36,4,32
Mv 40,8,32
Rt 144,22,49152
Mv 72,8,36
Mv 104,8,40
Rt 146,22,51200
Mv 136,8,44
Mv 168,8,48
Rt 148,23,49152
Mv 200,8,52
buffer_load_dwordx4 v14, s[8:11], s39 offen offset:2048 lds
Mv 232,8,56
Rt 150,23,51200
Ma 200,8,60
Ma 204,12,60
Rt 152,24,49152
Mv 236,12,56
Mv 204,12,52
Rt 154,24,51200
Mv 172,12,48
Mv 140,12,44
Rt 156,25,49152
Mv 108,12,40
buffer_load_dwordx4 v15, s[8:11], s39 offen offset:3072 lds
Mv 76,12,36
Rt 158,25,51200
Mv 44,12,32
Mv 48,16,32
Rb 160,17,32768
Mv 80,16,36
Mv 112,16,40
)" R"(
Rb 164,17,34816
Mv 144,16,44
Mv 176,16,48
Rb 168,17,40960
Mv 208,16,52
buffer_store_dwordx4 a[224:227], v1, s[20:23], s72 offen sc0 sc1
Mv 240,16,56
Rb 172,17,43008
s_add_u32 s72, s72, 1024
Ma 208,16,60
Ma 212,20,60
Rb 176,17,49152
Mv 244,20,56
Mv 212,20,52
Rb 180,17,51200
Mv 180,20,48
Mv 148,20,44
Rb 184,17,57344
Mv 116,20,40
Mv 84,20,36
Rb 188,17,59392
Mv 52,20,32
Mv 56,24,32
Mv 88,24,36
buffer_store_dwordx4 a[224:227], v1, s[20:23], s88 offen sc0 sc1
Mv 120,24,40
s_add_u32 s88, s88, 1024
Mv 152,24,44
Mv 184,24,48
Mv 216,24,52
Mv 248,24,56
Ma 216,24,60
Ma 220,28,60
Mv 252,28,56
Mv 220,28,52
Mv 188,28,48
Mv 156,28,44
Mv 124,28,40
Mv 92,28,36
Mv 60,28,32
s_waitcnt vmcnt(11) lgkmcnt(0)
s_barrier
Mv 32,128,160
Rt 0,18,0
Mv 64,128,164
s_add_u32 s38, s38, 0xffff0080
Mv 96,128,168
Rt 2,18,2048
s_add_u32 m0, s36, 32768
Mv 128,128,172
buffer_load_dwordx4 v8, s[4:7], s38 offen offset:0 lds
Mv 160,128,176
Rt 4,19,0
Mv 192,128,180
Mv 224,128,184
Rt 6,19,2048
Ma 192,128,188
buffer_load_dwordx4 v9, s[4:7], s38 offen offset:1024 lds
Ma 196,132,188
Rt 8,20,0
Mv 228,132,184
Mv 196,132,180
Rt 10,20,2048
Mv 164,132,176
buffer_load_dwordx4 v10, s[4:7], s38 offen offset:2048 lds
)" R"(
Mv 132,132,172
Rt 12,21,0
Mv 100,132,168
Mv 68,132,164
Rt 14,21,2048
Mv 36,132,160
buffer_load_dwordx4 v11, s[4:7], s38 offen offset:3072 lds
Mv 40,136,160
Rt 16,22,0
Mv 72,136,164
s_add_u32 s38, s38, 0x10000
Mv 104,136,168
Rt 18,22,2048
s_add_u32 m0, s36, 36864
Mv 136,136,172
buffer_load_dwordx4 v8, s[4:7], s38 offen offset:0 lds
Mv 168,136,176
Rt 20,23,0
Mv 200,136,180
Mv 232,136,184
Rt 22,23,2048
Ma 200,136,188
buffer_load_dwordx4 v9, s[4:7], s38 offen offset:1024 lds
Ma 204,140,188
Rt 24,24,0
Mv 236,140,184
Mv 204,140,180
Rt 26,24,2048
Mv 172,140,176
buffer_load_dwordx4 v10, s[4:7], s38 offen offset:2048 lds
Mv 140,140,172
Rt 28,25,0
Mv 108,140,168
Mv 76,140,164
Rt 30,25,2048
Mv 44,140,160
buffer_load_dwordx4 v11, s[4:7], s38 offen offset:3072 lds
Mv 48,144,160
Rb 32,16,0
Mv 80,144,164
Mv 112,144,168
Rb 36,16,2048
Mv 144,144,172
s_add_u32 s39, s39, 0x40000
Mv 176,144,176
Rb 40,16,8192
s_add_u32 m0, s37, 49152
Mv 208,144,180
buffer_load_dwordx4 v12, s[8:11], s39 offen offset:0 lds
Mv 240,144,184
Rb 44,16,10240
Ma 208,144,188
Ma 212,148,188
Rb 48,16,16384
Mv 244,148,184
Mv 212,148,180
Rb 52,16,18432
Mv 180,148,176
buffer_load_dwordx4 v13, s[8:11], s39 offen offset:1024 lds
Mv 148,148,172
Rb 56,16,24576
Mv 116,148,168
Mv 84,148,164
Rb 60,16,26624
)" R"(
Mv 52,148,160
Mv 56,152,160
Mv 88,152,164
buffer_load_dwordx4 v14, s[8:11], s39 offen offset:2048 lds
Mv 120,152,168
Mv 152,152,172
Mv 184,152,176
Mv 216,152,180
Mv 248,152,184
Ma 216,152,188
buffer_load_dwordx4 v15, s[8:11], s39 offen offset:3072 lds
Ma 220,156,188
Mv 252,156,184
Mv 220,156,180
s_sub_u32 s40, s40, 1
Mv 188,156,176
Mv 156,156,172
Mv 124,156,168
Mv 92,156,164
Mv 60,156,160
s_cmp_lg_u32 s40, 0
s_waitcnt vmcnt(19) lgkmcnt(0)
s_barrier
s_cbranch_scc1 .LzPBK
s_cmp_lg_u32 s94, 0
s_cbranch_scc1 .LzPBKX
Mv 32,0,32
Rt 128,18,16384
Mv 64,0,36
Mv 96,0,40
Rt 130,18,18432
Mv 128,0,44
s_add_u32 s39, s39, 0x40000
Mv 160,0,48
Rt 132,19,16384
s_add_u32 m0, s37, 0
Mv 192,0,52
buffer_load_dwordx4 v12, s[8:11], s39 offen offset:0 lds
Mv 224,0,56
Rt 134,19,18432
Ma 192,0,60
Ma 196,4,60
Rt 136,20,16384
Mv 228,4,56
Mv 196,4,52
Rt 138,20,18432
Mv 164,4,48
Mv 132,4,44
Rt 140,21,16384
Mv 100,4,40
buffer_load_dwordx4 v13, s[8:11], s39 offen offset:1024 lds
Mv 68,4,36
Rt 142,21,18432
Mv 36,4,32
Mv 40,8,32
Rt 144,22,16384
Mv 72,8,36
Mv 104,8,40
Rt 146,22,18432
Mv 136,8,44
Mv 168,8,48
Rt 148,23,16384
s_mov_b32 s24, s76
Mv 200,8,52
buffer_load_dwordx4 v14, s[8:11], s39 offen offset:2048 lds
)" R"(
Mv 232,8,56
Rt 150,23,18432
s_mov_b32 s25, s77
Ma 200,8,60
s_mov_b32 s26, 0x188000
s_mov_b32 s27, 0x20000
Ma 204,12,60
Rt 152,24,16384
Mv 236,12,56
Mv 204,12,52
Rt 154,24,18432
Mv 172,12,48
Mv 140,12,44
Rt 156,25,16384
Mv 108,12,40
buffer_load_dwordx4 v15, s[8:11], s39 offen offset:3072 lds
Mv 76,12,36
Rt 158,25,18432
s_add_u32 m0, s37, 0x14000
Mv 44,12,32
s_add_u32 s92, s90, 0
Mv 48,16,32
Rb 160,17,0
Mv 80,16,36
buffer_load_dwordx4 v27, s[24:27], s92 offen offset:0 lds
Mv 112,16,40
Rb 164,17,2048
s_add_u32 s92, s90, 6144
Mv 144,16,44
Mv 176,16,48
Rb 168,17,8192
Mv 208,16,52
buffer_load_dwordx4 v27, s[24:27], s92 offen offset:1024 lds
Mv 240,16,56
Rb 172,17,10240
s_add_u32 s92, s90, 12288
Ma 208,16,60
Ma 212,20,60
Rb 176,17,16384
Mv 244,20,56
buffer_load_dwordx4 v27, s[24:27], s92 offen offset:2048 lds
Mv 212,20,52
Rb 180,17,18432
s_add_u32 s92, s90, 18432
Mv 180,20,48
Mv 148,20,44
Rb 184,17,24576
Mv 116,20,40
buffer_load_dwordx4 v27, s[24:27], s92 offen offset:3072 lds
Mv 84,20,36
Rb 188,17,26624
s_mov_b32 s24, s80
Mv 52,20,32
s_mov_b32 s25, s81
s_mov_b32 s26, 0x380000
Mv 56,24,32
s_mov_b32 s27, 0x20000
s_add_u32 m0, s37, 0x10000
Mv 88,24,36
s_add_u32 s92, s83, 0
Mv 120,24,40
buffer_load_dwordx4 v26, s[24:27], s92 offen offset:0 lds
Mv 152,24,44
s_add_u32 s92, s83, 768
)" R"(
Mv 184,24,48
Mv 216,24,52
buffer_load_dwordx4 v26, s[24:27], s92 offen offset:1024 lds
Mv 248,24,56
s_add_u32 s92, s83, 1536
Ma 216,24,60
Ma 220,28,60
Mv 252,28,56
buffer_load_dwordx4 v26, s[24:27], s92 offen offset:2048 lds
Mv 220,28,52
s_add_u32 s92, s83, 2304
Mv 188,28,48
Mv 156,28,44
Mv 124,28,40
buffer_load_dwordx4 v26, s[24:27], s92 offen offset:3072 lds
Mv 92,28,36
Mv 60,28,32
s_waitcnt vmcnt(16) lgkmcnt(0)
s_barrier
Mv 32,128,160
Rt 0,18,32768
Mv 64,128,164
s_add_u32 s38, s38, 0xffff0080
Mv 96,128,168
Rt 2,18,34816
s_add_u32 m0, s36, 0
Mv 128,128,172
buffer_load_dwordx4 v8, s[4:7], s38 offen offset:0 lds
Mv 160,128,176
Rt 4,19,32768
Mv 192,128,180
Mv 224,128,184
Rt 6,19,34816
Ma 192,128,188
buffer_load_dwordx4 v9, s[4:7], s38 offen offset:1024 lds
Ma 196,132,188
Rt 8,20,32768
Mv 228,132,184
Mv 196,132,180
Rt 10,20,34816
Mv 164,132,176
buffer_load_dwordx4 v10, s[4:7], s38 offen offset:2048 lds
Mv 132,132,172
Rt 12,21,32768
Mv 100,132,168
Mv 68,132,164
Rt 14,21,34816
Mv 36,132,160
buffer_load_dwordx4 v11, s[4:7], s38 offen offset:3072 lds
Mv 40,136,160
Rt 16,22,32768
Mv 72,136,164
s_add_u32 s38, s38, 0x10000
Mv 104,136,168
Rt 18,22,34816
s_add_u32 m0, s36, 4096
Mv 136,136,172
buffer_load_dwordx4 v8, s[4:7], s38 offen offset:0 lds
Mv 168,136,176
Rt 20,23,32768
Mv 200,136,180
Mv 232,136,184
Rt 22,23,34816
Ma 200,136,188
buffer_load_dwordx4 v9, s[4:7], s38 offen offset:1024 lds
)" R"(
Ma 204,140,188
Rt 24,24,32768
Mv 236,140,184
Mv 204,140,180
Rt 26,24,34816
Mv 172,140,176
buffer_load_dwordx4 v10, s[4:7], s38 offen offset:2048 lds
Mv 140,140,172
Rt 28,25,32768
Mv 108,140,168
Mv 76,140,164
Rt 30,25,34816
Mv 44,140,160
buffer_load_dwordx4 v11, s[4:7], s38 offen offset:3072 lds
Mv 48,144,160
Rb 32,16,32768
Mv 80,144,164
Mv 112,144,168
Rb 36,16,34816
Mv 144,144,172
s_add_u32 s39, s39, 0x40000
Mv 176,144,176
Rb 40,16,40960
s_add_u32 m0, s37, 16384
Mv 208,144,180
buffer_load_dwordx4 v12, s[8:11], s39 offen offset:0 lds
Mv 240,144,184
Rb 44,16,43008
Ma 208,144,188
Ma 212,148,188
Rb 48,16,49152
Mv 244,148,184
Mv 212,148,180
Rb 52,16,51200
Mv 180,148,176
buffer_load_dwordx4 v13, s[8:11], s39 offen offset:1024 lds
Mv 148,148,172
Rb 56,16,57344
Mv 116,148,168
Mv 84,148,164
Rb 60,16,59392
Mv 52,148,160
Mv 56,152,160
Mv 88,152,164
buffer_load_dwordx4 v14, s[8:11], s39 offen offset:2048 lds
Mv 120,152,168
Mv 152,152,172
Mv 184,152,176
Mv 216,152,180
Mv 248,152,184
Ma 216,152,188
buffer_load_dwordx4 v15, s[8:11], s39 offen offset:3072 lds
Ma 220,156,188
Mv 252,156,184
Mv 220,156,180
Mv 188,156,176
Mv 156,156,172
Mv 124,156,168
Mv 92,156,164
Mv 60,156,160
s_waitcnt vmcnt(24) lgkmcnt(0)
s_barrier
Mv 32,0,32
Rt 128,18,49152
Mv 64,0,36
)" R"(
Mv 96,0,40
Rt 130,18,51200
Mv 128,0,44
s_add_u32 s39, s39, 0x40000
Mv 160,0,48
Rt 132,19,49152
s_add_u32 m0, s37, 32768
Mv 192,0,52
buffer_load_dwordx4 v12, s[8:11], s39 offen offset:0 lds
Mv 224,0,56
Rt 134,19,51200
Ma 192,0,60
Ma 196,4,60
Rt 136,20,49152
Mv 228,4,56
buffer_store_dwordx4 a[224:227], v1, s[20:23], s72 offen sc0 sc1
Mv 196,4,52
Rt 138,20,51200
s_add_u32 s72, s72, 1024
Mv 164,4,48
Mv 132,4,44
Rt 140,21,49152
Mv 100,4,40
buffer_load_dwordx4 v13, s[8:11], s39 offen offset:1024 lds
Mv 68,4,36
Rt 142,21,51200
Mv 36,4,32
Mv 40,8,32
Rt 144,22,49152
Mv 72,8,36
buffer_store_dwordx4 a[224:227], v1, s[20:23], s88 offen sc0 sc1
Mv 104,8,40
Rt 146,22,51200
s_add_u32 s88, s88, 1024
Mv 136,8,44
Mv 168,8,48
Rt 148,23,49152
Mv 200,8,52
buffer_load_dwordx4 v14, s[8:11], s39 offen offset:2048 lds
Mv 232,8,56
Rt 150,23,51200
Ma 200,8,60
Ma 204,12,60
Rt 152,24,49152
Mv 236,12,56
buffer_store_dwordx4 a[224:227], v1, s[20:23], s72 offen sc0 sc1
Mv 204,12,52
Rt 154,24,51200
s_add_u32 s72, s72, 1024
Mv 172,12,48
Mv 140,12,44
Rt 156,25,49152
Mv 108,12,40
buffer_load_dwordx4 v15, s[8:11], s39 offen offset:3072 lds
Mv 76,12,36
Rt 158,25,51200
Mv 44,12,32
Mv 48,16,32
Rb 160,17,32768
Mv 80,16,36
buffer_store_dwordx4 a[224:227], v1, s[20:23], s88 offen sc0 sc1
Mv 112,16,40
Rb 164,17,34816
s_add_u32 s88, s88, 1024
)" R"(
Mv 144,16,44
Mv 176,16,48
Rb 168,17,40960
Mv 208,16,52
Mv 240,16,56
Rb 172,17,43008
Ma 208,16,60
Ma 212,20,60
Rb 176,17,49152
Mv 244,20,56
buffer_store_dwordx4 a[224:227], v1, s[20:23], s72 offen sc0 sc1
Mv 212,20,52
Rb 180,17,51200
s_add_u32 s72, s72, 1024
Mv 180,20,48
Mv 148,20,44
Rb 184,17,57344
Mv 116,20,40
Mv 84,20,36
Rb 188,17,59392
Mv 52,20,32
Mv 56,24,32
Mv 88,24,36
buffer_store_dwordx4 a[224:227], v1, s[20:23], s88 offen sc0 sc1
Mv 120,24,40
s_add_u32 s88, s88, 1024
Mv 152,24,44
Mv 184,24,48
Mv 216,24,52
Mv 248,24,56
Ma 216,24,60
Ma 220,28,60
Mv 252,28,56
Mv 220,28,52
Mv 188,28,48
Mv 156,28,44
Mv 124,28,40
Mv 92,28,36
Mv 60,28,32
s_waitcnt vmcnt(14) lgkmcnt(0)
s_barrier
Mv 32,128,160
Rt 0,18,0
s_mov_b32 s72, 0x40000000
Mv 64,128,164
s_add_u32 s38, s38, 0xffff0080
s_mov_b32 s88, 0x40000000
Mv 96,128,168
Rt 2,18,2048
s_add_u32 m0, s36, 32768
Mv 128,128,172
buffer_load_dwordx4 v8, s[4:7], s38 offen offset:0 lds
Mv 160,128,176
Rt 4,19,0
v_lshrrev_b32 v5, 2, v0
Mv 192,128,180
v_and_b32 v5, 8, v5
v_and_or_b32 v5, v0, 7, v5
Mv 224,128,184
Rt 6,19,2048
v_lshlrev_b32 v5, 8, v5
Ma 192,128,188
buffer_load_dwordx4 v9, s[4:7], s38 offen offset:1024 lds
Ma 196,132,188
Rt 8,20,0
)" R"(
v_and_or_b32 v5, v0, 24, v5
Mv 228,132,184
v_lshrrev_b32 v5, 1, v5
Mv 196,132,180
Rt 10,20,2048
Mv 164,132,176
buffer_load_dwordx4 v10, s[4:7], s38 offen offset:2048 lds
Mv 132,132,172
Rt 12,21,0
Mv 100,132,168
Mv 68,132,164
Rt 14,21,2048
Mv 36,132,160
buffer_load_dwordx4 v11, s[4:7], s38 offen offset:3072 lds
Mv 40,136,160
Rt 16,22,0
Mv 72,136,164
s_add_u32 s38, s38, 0x10000
Mv 104,136,168
Rt 18,22,2048
s_add_u32 m0, s36, 36864
Mv 136,136,172
buffer_load_dwordx4 v8, s[4:7], s38 offen offset:0 lds
Mv 168,136,176
Rt 20,23,0
Mv 200,136,180
Mv 232,136,184
Rt 22,23,2048
Ma 200,136,188
buffer_load_dwordx4 v9, s[4:7], s38 offen offset:1024 lds
Ma 204,140,188
Rt 24,24,0
Mv 236,140,184
Mv 204,140,180
Rt 26,24,2048
Mv 172,140,176
buffer_load_dwordx4 v10, s[4:7], s38 offen offset:2048 lds
Mv 140,140,172
Rt 28,25,0
Mv 108,140,168
Mv 76,140,164
Rt 30,25,2048
Mv 44,140,160
buffer_load_dwordx4 v11, s[4:7], s38 offen offset:3072 lds
Mv 48,144,160
Rb 32,16,0
Mv 80,144,164
Mv 112,144,168
Rb 36,16,2048
Mv 144,144,172
s_add_u32 s39, s39, 0x40000
Mv 176,144,176
Rb 40,16,8192
s_add_u32 m0, s37, 49152
Mv 208,144,180
buffer_load_dwordx4 v12, s[8:11], s39 offen offset:0 lds
Mv 240,144,184
Rb 44,16,10240
Ma 208,144,188
Ma 212,148,188
Rb 48,16,16384
Mv 244,148,184
Mv 212,148,180
Rb 52,16,18432
)" R"(
Mv 180,148,176
buffer_load_dwordx4 v13, s[8:11], s39 offen offset:1024 lds
Mv 148,148,172
Rb 56,16,24576
Mv 116,148,168
Mv 84,148,164
Rb 60,16,26624
Mv 52,148,160
Mv 56,152,160
Mv 88,152,164
buffer_load_dwordx4 v14, s[8:11], s39 offen offset:2048 lds
Mv 120,152,168
Mv 152,152,172
Mv 184,152,176
Mv 216,152,180
Mv 248,152,184
Ma 216,152,188
buffer_load_dwordx4 v15, s[8:11], s39 offen offset:3072 lds
Ma 220,156,188
Mv 252,156,184
Mv 220,156,180
Mv 188,156,176
Mv 156,156,172
Mv 124,156,168
Mv 92,156,164
Mv 60,156,160
s_waitcnt vmcnt(22) lgkmcnt(0)
s_barrier
Mv 32,0,32
Rt 128,18,16384
Mv 64,0,36
Rt 224,28,0
Mv 96,0,40
Rt 130,18,18432
Mv 128,0,44
s_add_u32 s39, s39, 0x40000
Rt 226,28,2048
Mv 160,0,48
Rt 132,19,16384
s_add_u32 m0, s37, 0
Mv 192,0,52
buffer_load_dwordx4 v12, s[8:11], s39 offen offset:0 lds
Mv 224,0,56
Rt 134,19,18432
Ma 192,0,60
Rt 228,28,128
Ma 196,4,60
Rt 136,20,16384
Mv 228,4,56
Rt 230,28,2176
Mv 196,4,52
Rt 138,20,18432
Mv 164,4,48
Rt 232,28,256
Mv 132,4,44
Rt 140,21,16384
Mv 100,4,40
buffer_load_dwordx4 v13, s[8:11], s39 offen offset:1024 lds
Mv 68,4,36
Rt 142,21,18432
Mv 36,4,32
Rt 234,28,2304
Mv 40,8,32
Rt 144,22,16384
Mv 72,8,36
)" R"(
Rt 236,28,384
Mv 104,8,40
Rt 146,22,18432
Mv 136,8,44
Rt 238,28,2432
Mv 168,8,48
Rt 148,23,16384
Mv 200,8,52
buffer_load_dwordx4 v14, s[8:11], s39 offen offset:2048 lds
Mv 232,8,56
Rt 150,23,18432
Ma 200,8,60
Ma 204,12,60
Rt 152,24,16384
Mv 236,12,56
Mv 204,12,52
Rt 154,24,18432
Mv 172,12,48
Mv 140,12,44
Rt 156,25,16384
Mv 108,12,40
buffer_load_dwordx4 v15, s[8:11], s39 offen offset:3072 lds
Mv 76,12,36
Rt 158,25,18432
Mv 44,12,32
s_waitcnt lgkmcnt(6)
Mv 48,16,32
Rb 160,17,0
Mv 80,16,36
Rt 240,29,0
Mv 112,16,40
Rb 164,17,2048
Mv 144,16,44
s_add_u32 s29, s86, 0
Rt 242,29,8192
Mv 176,16,48
Rb 168,17,8192
Mv 208,16,52
buffer_store_dwordx4 a[224:227], v1, s[20:23], s29 offen sc0 sc1
Mv 240,16,56
Rb 172,17,10240
Ma 208,16,60
Rt 244,29,128
Ma 212,20,60
Rb 176,17,16384
Mv 244,20,56
Rt 246,29,8320
Mv 212,20,52
Rb 180,17,18432
Mv 180,20,48
s_add_u32 s29, s86, 114688
Rt 248,29,256
Mv 148,20,44
Rb 184,17,24576
Mv 116,20,40
buffer_store_dwordx4 a[228:231], v1, s[20:23], s29 offen sc0 sc1
Mv 84,20,36
Rb 188,17,26624
Mv 52,20,32
Rt 250,29,8448
Mv 56,24,32
Mv 88,24,36
Rt 252,29,384
Mv 120,24,40
)" R"(
Mv 152,24,44
s_add_u32 s29, s86, 229376
Rt 254,29,8576
Mv 184,24,48
Mv 216,24,52
buffer_store_dwordx4 a[232:235], v1, s[20:23], s29 offen sc0 sc1
Mv 248,24,56
Ma 216,24,60
Ma 220,28,60
Mv 252,28,56
Mv 220,28,52
Mv 188,28,48
s_add_u32 s29, s86, 344064
Mv 156,28,44
s_cmp_gt_u32 s33, 1
s_cmov_b32 s29, 0x40000000
Mv 124,28,40
buffer_store_dwordx4 a[236:239], v1, s[20:23], s29 offen sc0 sc1
Mv 92,28,36
Mv 60,28,32
s_waitcnt vmcnt(12) lgkmcnt(3)
s_barrier
Mv 32,128,160
Rt 0,18,32768
Mv 64,128,164
s_add_u32 s38, s38, 0xffff0080
Mv 96,128,168
Rt 2,18,34816
s_add_u32 m0, s36, 0
Mv 128,128,172
buffer_load_dwordx4 v8, s[4:7], s38 offen offset:0 lds
Mv 160,128,176
Rt 4,19,32768
Mv 192,128,180
Mv 224,128,184
Rt 6,19,34816
Ma 192,128,188
buffer_load_dwordx4 v9, s[4:7], s38 offen offset:1024 lds
Ma 196,132,188
Rt 8,20,32768
Mv 228,132,184
Mv 196,132,180
Rt 10,20,34816
Mv 164,132,176
buffer_load_dwordx4 v10, s[4:7], s38 offen offset:2048 lds
Mv 132,132,172
Rt 12,21,32768
Mv 100,132,168
Mv 68,132,164
Rt 14,21,34816
Mv 36,132,160
buffer_load_dwordx4 v11, s[4:7], s38 offen offset:3072 lds
Mv 40,136,160
Rt 16,22,32768
Mv 72,136,164
s_add_u32 s38, s38, 0x10000
Mv 104,136,168
Rt 18,22,34816
s_add_u32 m0, s36, 4096
Mv 136,136,172
buffer_load_dwordx4 v8, s[4:7], s38 offen offset:0 lds
Mv 168,136,176
Rt 20,23,32768
Mv 200,136,180
Mv 232,136,184
)" R"(
Rt 22,23,34816
Ma 200,136,188
buffer_load_dwordx4 v9, s[4:7], s38 offen offset:1024 lds
Ma 204,140,188
Rt 24,24,32768
Mv 236,140,184
Mv 204,140,180
Rt 26,24,34816
Mv 172,140,176
buffer_load_dwordx4 v10, s[4:7], s38 offen offset:2048 lds
Mv 140,140,172
Rt 28,25,32768
Mv 108,140,168
Mv 76,140,164
Rt 30,25,34816
Mv 44,140,160
buffer_load_dwordx4 v11, s[4:7], s38 offen offset:3072 lds
Mv 48,144,160
Rb 32,16,32768
Mv 80,144,164
Mv 112,144,168
Rb 36,16,34816
Mv 144,144,172
s_add_u32 s39, s39, 0x40000
Mv 176,144,176
Rb 40,16,40960
s_add_u32 m0, s37, 16384
Mv 208,144,180
buffer_load_dwordx4 v12, s[8:11], s39 offen offset:0 lds
Mv 240,144,184
Rb 44,16,43008
Ma 208,144,188
Ma 212,148,188
Rb 48,16,49152
Mv 244,148,184
Mv 212,148,180
Rb 52,16,51200
Mv 180,148,176
buffer_load_dwordx4 v13, s[8:11], s39 offen offset:1024 lds
Mv 148,148,172
Rb 56,16,57344
Mv 116,148,168
Mv 84,148,164
Rb 60,16,59392
Mv 52,148,160
Mv 56,152,160
Mv 88,152,164
buffer_load_dwordx4 v14, s[8:11], s39 offen offset:2048 lds
Mv 120,152,168
Mv 152,152,172
Mv 184,152,176
Mv 216,152,180
Mv 248,152,184
Ma 216,152,188
buffer_load_dwordx4 v15, s[8:11], s39 offen offset:3072 lds
Ma 220,156,188
Mv 252,156,184
Mv 220,156,180
Mv 188,156,176
Mv 156,156,172
Mv 124,156,168
Mv 92,156,164
Mv 60,156,160
s_waitcnt vmcnt(20) lgkmcnt(0)
s_barrier
)" R"(
Mv 32,0,32
Rt 128,18,49152
Mv 64,0,36
Mv 96,0,40
Rt 130,18,51200
Mv 128,0,44
s_add_u32 s39, s39, 0x40000
Mv 160,0,48
Rt 132,19,49152
s_add_u32 m0, s37, 32768
Mv 192,0,52
buffer_load_dwordx4 v12, s[8:11], s39 offen offset:0 lds
Mv 224,0,56
Rt 134,19,51200
Ma 192,0,60
Ma 196,4,60
Rt 136,20,49152
Mv 228,4,56
Mv 196,4,52
Rt 138,20,51200
Mv 164,4,48
Mv 132,4,44
Rt 140,21,49152
Mv 100,4,40
buffer_load_dwordx4 v13, s[8:11], s39 offen offset:1024 lds
Mv 68,4,36
Rt 142,21,51200
Mv 36,4,32
Mv 40,8,32
Rt 144,22,49152
Mv 72,8,36
Mv 104,8,40
Rt 146,22,51200
Mv 136,8,44
Mv 168,8,48
Rt 148,23,49152
s_mov_b32 s24, s78
Mv 200,8,52
buffer_load_dwordx4 v14, s[8:11], s39 offen offset:2048 lds
Mv 232,8,56
Rt 150,23,51200
s_mov_b32 s25, s79
Ma 200,8,60
s_mov_b32 s26, 0x188000
s_mov_b32 s27, 0x20000
Ma 204,12,60
Rt 152,24,49152
Mv 236,12,56
Mv 204,12,52
Rt 154,24,51200
Mv 172,12,48
Mv 140,12,44
Rt 156,25,49152
Mv 108,12,40
buffer_load_dwordx4 v15, s[8:11], s39 offen offset:3072 lds
Mv 76,12,36
Rt 158,25,51200
s_add_u32 m0, s37, 0x14000
Mv 44,12,32
s_add_u32 s92, s90, 0
Mv 48,16,32
Rb 160,17,32768
Mv 80,16,36
buffer_load_dwordx4 v27, s[24:27], s92 offen offset:0 lds
)" R"(
Mv 112,16,40
Rb 164,17,34816
s_add_u32 s92, s90, 6144
Mv 144,16,44
Mv 176,16,48
Rb 168,17,40960
Mv 208,16,52
buffer_load_dwordx4 v27, s[24:27], s92 offen offset:1024 lds
Mv 240,16,56
Rb 172,17,43008
s_add_u32 s92, s90, 12288
Ma 208,16,60
Ma 212,20,60
Rb 176,17,49152
Mv 244,20,56
buffer_load_dwordx4 v27, s[24:27], s92 offen offset:2048 lds
Mv 212,20,52
Rb 180,17,51200
s_add_u32 s92, s90, 18432
Mv 180,20,48
Mv 148,20,44
Rb 184,17,57344
Mv 116,20,40
buffer_load_dwordx4 v27, s[24:27], s92 offen offset:3072 lds
Mv 84,20,36
Rb 188,17,59392
s_mov_b32 s24, s80
Mv 52,20,32
s_mov_b32 s25, s81
s_mov_b32 s26, 0x380000
Mv 56,24,32
s_mov_b32 s27, 0x20000
s_movk_i32 s92, -448
Mv 88,24,36
s_bitcmp1_b32 s34, 0
s_cmovk_i32 s92, 448
Mv 120,24,40
s_add_u32 s83, s83, s92
s_add_u32 m0, s37, 0x10000
Mv 152,24,44
s_add_u32 s92, s83, 0
Mv 184,24,48
buffer_load_dwordx4 v26, s[24:27], s92 offen offset:0 lds
Mv 216,24,52
s_add_u32 s92, s83, 768
Mv 248,24,56
buffer_load_dwordx4 v26, s[24:27], s92 offen offset:1024 lds
Ma 216,24,60
s_add_u32 s92, s83, 1536
Ma 220,28,60
Mv 252,28,56
buffer_load_dwordx4 v26, s[24:27], s92 offen offset:2048 lds
Mv 220,28,52
s_add_u32 s92, s83, 2304
Mv 188,28,48
Mv 156,28,44
Mv 124,28,40
buffer_load_dwordx4 v26, s[24:27], s92 offen offset:3072 lds
Mv 92,28,36
Mv 60,28,32
s_waitcnt vmcnt(16) lgkmcnt(0)
s_barrier
Mv 32,128,160
Rt 0,18,0
Mv 64,128,164
)" R"(
s_add_u32 s38, s38, 0xffff0080
Mv 96,128,168
Rt 2,18,2048
s_add_u32 m0, s36, 32768
Mv 128,128,172
buffer_load_dwordx4 v8, s[4:7], s38 offen offset:0 lds
Mv 160,128,176
Rt 4,19,0
Mv 192,128,180
Mv 224,128,184
Rt 6,19,2048
Ma 192,128,188
buffer_load_dwordx4 v9, s[4:7], s38 offen offset:1024 lds
Ma 196,132,188
Rt 8,20,0
Mv 228,132,184
Mv 196,132,180
Rt 10,20,2048
Mv 164,132,176
buffer_load_dwordx4 v10, s[4:7], s38 offen offset:2048 lds
Mv 132,132,172
Rt 12,21,0
Mv 100,132,168
Mv 68,132,164
Rt 14,21,2048
Mv 36,132,160
buffer_load_dwordx4 v11, s[4:7], s38 offen offset:3072 lds
Mv 40,136,160
Rt 16,22,0
Mv 72,136,164
s_add_u32 s38, s38, 0x10000
Mv 104,136,168
Rt 18,22,2048
s_add_u32 m0, s36, 36864
Mv 136,136,172
buffer_load_dwordx4 v8, s[4:7], s38 offen offset:0 lds
Mv 168,136,176
Rt 20,23,0
Mv 200,136,180
Mv 232,136,184
Rt 22,23,2048
Ma 200,136,188
buffer_load_dwordx4 v9, s[4:7], s38 offen offset:1024 lds
Ma 204,140,188
Rt 24,24,0
Mv 236,140,184
Mv 204,140,180
Rt 26,24,2048
Mv 172,140,176
buffer_load_dwordx4 v10, s[4:7], s38 offen offset:2048 lds
Mv 140,140,172
Rt 28,25,0
Mv 108,140,168
Mv 76,140,164
Rt 30,25,2048
Mv 44,140,160
buffer_load_dwordx4 v11, s[4:7], s38 offen offset:3072 lds
Mv 48,144,160
Rb 32,16,0
Mv 80,144,164
Mv 112,144,168
Rb 36,16,2048
Mv 144,144,172
s_add_u32 s39, s39, 0x40000
)" R"(
Mv 176,144,176
Rb 40,16,8192
s_add_u32 m0, s37, 49152
Mv 208,144,180
buffer_load_dwordx4 v12, s[8:11], s39 offen offset:0 lds
Mv 240,144,184
Rb 44,16,10240
Ma 208,144,188
Ma 212,148,188
Rb 48,16,16384
Mv 244,148,184
Mv 212,148,180
Rb 52,16,18432
Mv 180,148,176
buffer_load_dwordx4 v13, s[8:11], s39 offen offset:1024 lds
Mv 148,148,172
Rb 56,16,24576
Mv 116,148,168
Mv 84,148,164
Rb 60,16,26624
Mv 52,148,160
Mv 56,152,160
Mv 88,152,164
buffer_load_dwordx4 v14, s[8:11], s39 offen offset:2048 lds
Mv 120,152,168
Mv 152,152,172
Mv 184,152,176
Mv 216,152,180
Mv 248,152,184
Ma 216,152,188
buffer_load_dwordx4 v15, s[8:11], s39 offen offset:3072 lds
Ma 220,156,188
Mv 252,156,184
Mv 220,156,180
Mv 188,156,176
Mv 156,156,172
Mv 124,156,168
Mv 92,156,164
Mv 60,156,160
s_waitcnt vmcnt(24) lgkmcnt(0)
s_barrier
Mv 32,0,32
Rt 128,18,16384
Mv 64,0,36
Mv 96,0,40
Rt 130,18,18432
Mv 128,0,44
s_add_u32 s39, s39, 0x40000
Mv 160,0,48
Rt 132,19,16384
s_add_u32 m0, s37, 0
Mv 192,0,52
buffer_load_dwordx4 v12, s[8:11], s39 offen offset:0 lds
Mv 224,0,56
Rt 134,19,18432
Ma 192,0,60
s_add_u32 s28, s33, 0
s_lshl3_add_u32 s28, s35, s28
Ma 196,4,60
Rt 136,20,16384
s_mul_i32 s29, s28, 293
Mv 228,4,56
s_lshr_b32 s29, s29, 11
s_mul_i32 s29, s29, 9
Mv 196,4,52
)" R"(
Rt 138,20,18432
s_add_u32 s29, s29, s28
Mv 164,4,48
s_lshl_b32 s29, s29, 10
s_add_u32 s29, s29, s82
Mv 132,4,44
Rt 140,21,16384
s_add_u32 s29, s29, s84
Mv 100,4,40
buffer_load_dwordx4 v13, s[8:11], s39 offen offset:1024 lds
Mv 68,4,36
Rt 142,21,18432
Mv 36,4,32
Mv 40,8,32
Rt 144,22,16384
Mv 72,8,36
buffer_store_dwordx4 a[240:243], v1, s[20:23], s29 offen sc0 sc1
Mv 104,8,40
Rt 146,22,18432
Mv 136,8,44
Mv 168,8,48
Rt 148,23,16384
Mv 200,8,52
buffer_load_dwordx4 v14, s[8:11], s39 offen offset:2048 lds
Mv 232,8,56
Rt 150,23,18432
s_add_u32 s28, s33, 4
Ma 200,8,60
s_lshl3_add_u32 s28, s35, s28
s_mul_i32 s29, s28, 293
Ma 204,12,60
Rt 152,24,16384
s_lshr_b32 s29, s29, 11
Mv 236,12,56
s_mul_i32 s29, s29, 9
s_add_u32 s29, s29, s28
Mv 204,12,52
Rt 154,24,18432
s_lshl_b32 s29, s29, 10
Mv 172,12,48
s_add_u32 s29, s29, s82
s_add_u32 s29, s29, s84
Mv 140,12,44
Rt 156,25,16384
Mv 108,12,40
buffer_load_dwordx4 v15, s[8:11], s39 offen offset:3072 lds
Mv 76,12,36
Rt 158,25,18432
Mv 44,12,32
buffer_store_dwordx4 a[244:247], v1, s[20:23], s29 offen sc0 sc1
Mv 48,16,32
Rb 160,17,0
Mv 80,16,36
Mv 112,16,40
Rb 164,17,2048
Mv 144,16,44
s_add_u32 s28, s33, 8
s_lshl3_add_u32 s28, s28, s35
Mv 176,16,48
Rb 168,17,8192
s_mul_i32 s29, s28, 293
Mv 208,16,52
s_lshr_b32 s29, s29, 11
s_mul_i32 s29, s29, 9
)" R"(
Mv 240,16,56
Rb 172,17,10240
s_add_u32 s29, s29, s28
Ma 208,16,60
s_lshl_b32 s29, s29, 10
s_add_u32 s29, s29, s82
Ma 212,20,60
Rb 176,17,16384
s_add_u32 s29, s29, s84
Mv 244,20,56
Mv 212,20,52
Rb 180,17,18432
Mv 180,20,48
Mv 148,20,44
Rb 184,17,24576
Mv 116,20,40
buffer_store_dwordx4 a[248:251], v1, s[20:23], s29 offen sc0 sc1
Mv 84,20,36
Rb 188,17,26624
Mv 52,20,32
Mv 56,24,32
Mv 88,24,36
s_add_u32 s28, s33, 12
s_lshl3_add_u32 s28, s28, s35
Mv 120,24,40
s_mul_i32 s29, s28, 293
s_lshr_b32 s29, s29, 11
Mv 152,24,44
s_mul_i32 s29, s29, 9
s_add_u32 s29, s29, s28
Mv 184,24,48
s_lshl_b32 s29, s29, 10
s_add_u32 s29, s29, s82
Mv 216,24,52
s_add_u32 s29, s29, s84
Mv 248,24,56
s_cmp_gt_u32 s33, 1
s_cmov_b32 s29, 0x40000000
Ma 216,24,60
Ma 220,28,60
Mv 252,28,56
Mv 220,28,52
Mv 188,28,48
buffer_store_dwordx4 a[252:255], v1, s[20:23], s29 offen sc0 sc1
Mv 156,28,44
Mv 124,28,40
Mv 92,28,36
Mv 60,28,32
s_waitcnt vmcnt(12) lgkmcnt(0)
s_barrier
Mv 32,128,160
Rt 0,18,32768
Mv 64,128,164
s_add_u32 s38, s38, 0xffff0080
Rt 224,28,0
Mv 96,128,168
Rt 2,18,34816
s_add_u32 m0, s36, 0
Mv 128,128,172
buffer_load_dwordx4 v8, s[4:7], s38 offen offset:0 lds
Rt 226,28,2048
Mv 160,128,176
Rt 4,19,32768
Mv 192,128,180
Mv 224,128,184
)" R"(
Rt 6,19,34816
Ma 192,128,188
buffer_load_dwordx4 v9, s[4:7], s38 offen offset:1024 lds
Rt 228,28,128
Ma 196,132,188
Rt 8,20,32768
Mv 228,132,184
Rt 230,28,2176
Mv 196,132,180
Rt 10,20,34816
Mv 164,132,176
buffer_load_dwordx4 v10, s[4:7], s38 offen offset:2048 lds
Rt 232,28,256
Mv 132,132,172
Rt 12,21,32768
Mv 100,132,168
Mv 68,132,164
Rt 14,21,34816
Mv 36,132,160
buffer_load_dwordx4 v11, s[4:7], s38 offen offset:3072 lds
Rt 234,28,2304
Mv 40,136,160
Rt 16,22,32768
Mv 72,136,164
s_add_u32 s38, s38, 0x10000
Rt 236,28,384
Mv 104,136,168
Rt 18,22,34816
s_add_u32 m0, s36, 4096
Mv 136,136,172
buffer_load_dwordx4 v8, s[4:7], s38 offen offset:0 lds
Rt 238,28,2432
Mv 168,136,176
Rt 20,23,32768
Mv 200,136,180
Mv 232,136,184
Rt 22,23,34816
Ma 200,136,188
buffer_load_dwordx4 v9, s[4:7], s38 offen offset:1024 lds
Ma 204,140,188
Rt 24,24,32768
Mv 236,140,184
Mv 204,140,180
Rt 26,24,34816
Mv 172,140,176
buffer_load_dwordx4 v10, s[4:7], s38 offen offset:2048 lds
Mv 140,140,172
Rt 28,25,32768
Mv 108,140,168
Mv 76,140,164
Rt 30,25,34816
Mv 44,140,160
buffer_load_dwordx4 v11, s[4:7], s38 offen offset:3072 lds
Mv 48,144,160
Rb 32,16,32768
Mv 80,144,164
Mv 112,144,168
Rb 36,16,34816
Mv 144,144,172
s_add_u32 s39, s39, 0x40000
Rt 240,29,0
Mv 176,144,176
Rb 40,16,40960
s_add_u32 m0, s37, 16384
)" R"(
Mv 208,144,180
buffer_load_dwordx4 v12, s[8:11], s39 offen offset:0 lds
Rt 242,29,8192
Mv 240,144,184
Rb 44,16,43008
Ma 208,144,188
Ma 212,148,188
Rb 48,16,49152
Mv 244,148,184
Rt 244,29,128
Mv 212,148,180
Rb 52,16,51200
Mv 180,148,176
buffer_load_dwordx4 v13, s[8:11], s39 offen offset:1024 lds
Rt 246,29,8320
Mv 148,148,172
Rb 56,16,57344
Mv 116,148,168
Rt 248,29,256
Mv 84,148,164
Rb 60,16,59392
Mv 52,148,160
Mv 56,152,160
Mv 88,152,164
buffer_load_dwordx4 v14, s[8:11], s39 offen offset:2048 lds
Rt 250,29,8448
Mv 120,152,168
Mv 152,152,172
Rt 252,29,384
Mv 184,152,176
Mv 216,152,180
Rt 254,29,8576
Mv 248,152,184
Ma 216,152,188
buffer_load_dwordx4 v15, s[8:11], s39 offen offset:3072 lds
Ma 220,156,188
Mv 252,156,184
Mv 220,156,180
Mv 188,156,176
Mv 156,156,172
Mv 124,156,168
Mv 92,156,164
Mv 60,156,160
s_waitcnt vmcnt(20) lgkmcnt(2)
s_barrier
Mv 32,0,32
Rt 128,18,49152
s_add_u32 s29, s86, 14336
Mv 64,0,36
buffer_store_dwordx4 a[224:227], v1, s[20:23], s29 offen sc0 sc1
Mv 96,0,40
Rt 130,18,51200
Mv 128,0,44
s_add_u32 s39, s39, 0x40000
Mv 160,0,48
Rt 132,19,49152
s_add_u32 m0, s37, 32768
Mv 192,0,52
buffer_load_dwordx4 v12, s[8:11], s39 offen offset:0 lds
Mv 224,0,56
Rt 134,19,51200
Ma 192,0,60
s_add_u32 s29, s86, 129024
Ma 196,4,60
Rt 136,20,49152
)" R"(
Mv 228,4,56
buffer_store_dwordx4 a[228:231], v1, s[20:23], s29 offen sc0 sc1
Mv 196,4,52
Rt 138,20,51200
Mv 164,4,48
Mv 132,4,44
Rt 140,21,49152
Mv 100,4,40
buffer_load_dwordx4 v13, s[8:11], s39 offen offset:1024 lds
Mv 68,4,36
Rt 142,21,51200
Mv 36,4,32
s_add_u32 s29, s86, 243712
Mv 40,8,32
Rt 144,22,49152
Mv 72,8,36
buffer_store_dwordx4 a[232:235], v1, s[20:23], s29 offen sc0 sc1
Mv 104,8,40
Rt 146,22,51200
Mv 136,8,44
Mv 168,8,48
Rt 148,23,49152
Mv 200,8,52
buffer_load_dwordx4 v14, s[8:11], s39 offen offset:2048 lds
Mv 232,8,56
Rt 150,23,51200
Ma 200,8,60
s_add_u32 s29, s86, 358400
Ma 204,12,60
Rt 152,24,49152
Mv 236,12,56
s_cmp_gt_u32 s33, 1
s_cmov_b32 s29, 0x40000000
Mv 204,12,52
Rt 154,24,51200
Mv 172,12,48
buffer_store_dwordx4 a[236:239], v1, s[20:23], s29 offen sc0 sc1
Mv 140,12,44
Rt 156,25,49152
Mv 108,12,40
buffer_load_dwordx4 v15, s[8:11], s39 offen offset:3072 lds
Mv 76,12,36
Rt 158,25,51200
s_add_u32 s28, s33, 14
Mv 44,12,32
s_lshl3_add_u32 s28, s28, s35
s_mul_i32 s29, s28, 293
Mv 48,16,32
Rb 160,17,32768
s_lshr_b32 s29, s29, 11
Mv 80,16,36
s_mul_i32 s29, s29, 9
s_add_u32 s29, s29, s28
Mv 112,16,40
Rb 164,17,34816
s_lshl_b32 s29, s29, 10
Mv 144,16,44
s_add_u32 s29, s29, s82
s_add_u32 s29, s29, s84
Mv 176,16,48
Rb 168,17,40960
Mv 208,16,52
Mv 240,16,56
Rb 172,17,43008
)" R"(
Ma 208,16,60
buffer_store_dwordx4 a[240:243], v1, s[20:23], s29 offen sc0 sc1
Ma 212,20,60
Rb 176,17,49152
Mv 244,20,56
s_add_u32 s28, s33, 18
s_lshl3_add_u32 s28, s28, s35
Mv 212,20,52
Rb 180,17,51200
s_mul_i32 s29, s28, 293
Mv 180,20,48
s_lshr_b32 s29, s29, 11
s_mul_i32 s29, s29, 9
Mv 148,20,44
Rb 184,17,57344
s_add_u32 s29, s29, s28
Mv 116,20,40
s_lshl_b32 s29, s29, 10
s_add_u32 s29, s29, s82
Mv 84,20,36
Rb 188,17,59392
s_add_u32 s29, s29, s84
Mv 52,20,32
Mv 56,24,32
Mv 88,24,36
Mv 120,24,40
Mv 152,24,44
buffer_store_dwordx4 a[244:247], v1, s[20:23], s29 offen sc0 sc1
Mv 184,24,48
s_add_u32 s28, s33, 22
s_lshl3_add_u32 s28, s28, s35
Mv 216,24,52
s_mul_i32 s29, s28, 293
s_lshr_b32 s29, s29, 11
Mv 248,24,56
s_mul_i32 s29, s29, 9
s_add_u32 s29, s29, s28
Ma 216,24,60
s_lshl_b32 s29, s29, 10
s_add_u32 s29, s29, s82
Ma 220,28,60
s_add_u32 s29, s29, s84
Mv 252,28,56
Mv 220,28,52
Mv 188,28,48
Mv 156,28,44
Mv 124,28,40
Mv 92,28,36
buffer_store_dwordx4 a[248:251], v1, s[20:23], s29 offen sc0 sc1
Mv 60,28,32
s_waitcnt vmcnt(15) lgkmcnt(0)
s_barrier
Mv 32,128,160
Rt 0,18,0
Mv 64,128,164
s_add_u32 s38, s38, 0xffff0080
Mv 96,128,168
Rt 2,18,2048
s_add_u32 m0, s36, 32768
Mv 128,128,172
buffer_load_dwordx4 v8, s[4:7], s38 offen offset:0 lds
Mv 160,128,176
Rt 4,19,0
Mv 192,128,180
Mv 224,128,184
)" R"(
Rt 6,19,2048
Ma 192,128,188
buffer_load_dwordx4 v9, s[4:7], s38 offen offset:1024 lds
Ma 196,132,188
Rt 8,20,0
Mv 228,132,184
Mv 196,132,180
Rt 10,20,2048
Mv 164,132,176
buffer_load_dwordx4 v10, s[4:7], s38 offen offset:2048 lds
Mv 132,132,172
Rt 12,21,0
Mv 100,132,168
Mv 68,132,164
Rt 14,21,2048
Mv 36,132,160
buffer_load_dwordx4 v11, s[4:7], s38 offen offset:3072 lds
Mv 40,136,160
Rt 16,22,0
Mv 72,136,164
s_add_u32 s38, s38, 0x10000
Mv 104,136,168
Rt 18,22,2048
s_add_u32 m0, s36, 36864
Mv 136,136,172
buffer_load_dwordx4 v8, s[4:7], s38 offen offset:0 lds
Mv 168,136,176
Rt 20,23,0
Mv 200,136,180
Mv 232,136,184
Rt 22,23,2048
Ma 200,136,188
buffer_load_dwordx4 v9, s[4:7], s38 offen offset:1024 lds
Ma 204,140,188
Rt 24,24,0
Mv 236,140,184
s_add_u32 s28, s33, 26
s_lshl3_add_u32 s28, s28, s35
s_mul_i32 s29, s28, 293
Mv 204,140,180
Rt 26,24,2048
s_lshr_b32 s29, s29, 11
s_mul_i32 s29, s29, 9
Mv 172,140,176
buffer_load_dwordx4 v10, s[4:7], s38 offen offset:2048 lds
Mv 140,140,172
Rt 28,25,0
s_add_u32 s29, s29, s28
s_lshl_b32 s29, s29, 10
Mv 108,140,168
s_add_u32 s29, s29, s82
s_add_u32 s29, s29, s84
Mv 76,140,164
Rt 30,25,2048
s_cmp_gt_u32 s33, 1
s_cmov_b32 s29, 0x40000000
Mv 44,140,160
buffer_load_dwordx4 v11, s[4:7], s38 offen offset:3072 lds
Mv 48,144,160
Rb 32,16,0
Mv 80,144,164
Mv 112,144,168
Rb 36,16,2048
Mv 144,144,172
)" R"(
s_add_u32 s39, s39, 0x40000
buffer_store_dwordx4 a[252:255], v1, s[20:23], s29 offen sc0 sc1
Mv 176,144,176
Rb 40,16,8192
s_add_u32 m0, s37, 49152
Mv 208,144,180
buffer_load_dwordx4 v12, s[8:11], s39 offen offset:0 lds
Mv 240,144,184
Rb 44,16,10240
Ma 208,144,188
Ma 212,148,188
Rb 48,16,16384
Mv 244,148,184
Mv 212,148,180
Rb 52,16,18432
Mv 180,148,176
buffer_load_dwordx4 v13, s[8:11], s39 offen offset:1024 lds
Mv 148,148,172
Rb 56,16,24576
Mv 116,148,168
Mv 84,148,164
Rb 60,16,26624
Mv 52,148,160
Mv 56,152,160
Mv 88,152,164
buffer_load_dwordx4 v14, s[8:11], s39 offen offset:2048 lds
Mv 120,152,168
Mv 152,152,172
Mv 184,152,176
Mv 216,152,180
Mv 248,152,184
Ma 216,152,188
buffer_load_dwordx4 v15, s[8:11], s39 offen offset:3072 lds
Ma 220,156,188
Mv 252,156,184
Mv 220,156,180
Mv 188,156,176
Mv 156,156,172
Mv 124,156,168
s_mov_b32 s40, 1
s_mov_b32 s94, 1
Mv 92,156,164
Mv 60,156,160
s_waitcnt vmcnt(17) lgkmcnt(0)
s_barrier
buffer_store_dword v6, v7, s[20:23], s87 offen offset:0 sc0 sc1
s_branch .LzPBK
.LzBV:
Mz 32,32,0
Rt 128,18,16384
v_mul_f32_e32 v64, s54, v64
Mz 36,36,0
v_mul_f32_e32 v65, s54, v65
Cv 64,64,65
Mz 96,40,0
Rt 130,18,18432
v_accvgpr_write_b32 a112, v64
Mz 100,44,0
s_add_u32 s39, s39, 0x40000
v_mul_f32_e32 v66, s54, v66
Mz 160,48,0
Rt 132,19,16384
s_add_u32 m0, s37, 0
Mz 164,52,0
buffer_load_dwordx4 v12, s[8:11], s39 offen offset:0 lds
)" R"(
Mz 224,56,0
Rt 134,19,18432
v_mul_f32_e32 v67, s54, v67
Mz 228,60,0
Cv 66,66,67
v_accvgpr_write_b32 a113, v66
Mz 236,60,4
Rt 136,20,16384
v_mul_f32_e32 v68, s54, v68
Mz 232,56,4
v_mul_f32_e32 v69, s54, v69
Cv 68,68,69
buffer_store_dwordx4 a[64:67], v2, s[20:23], s51 offen offset:0 sc0 sc1
Mz 172,52,4
Rt 138,20,18432
v_accvgpr_write_b32 a114, v68
Mz 168,48,4
v_mul_f32_e32 v70, s54, v70
v_mul_f32_e32 v71, s54, v71
Mz 108,44,4
Rt 140,21,16384
Cv 70,70,71
Mz 104,40,4
buffer_load_dwordx4 v13, s[8:11], s39 offen offset:1024 lds
Mz 44,36,4
Rt 142,21,18432
v_accvgpr_write_b32 a115, v70
Mz 40,32,4
v_mul_f32_e32 v72, s54, v72
v_mul_f32_e32 v73, s54, v73
Mz 48,32,8
Rt 144,22,16384
Cv 72,72,73
Mz 52,36,8
v_accvgpr_write_b32 a116, v72
v_mul_f32_e32 v74, s54, v74
buffer_store_dwordx4 a[68:71], v2, s[20:23], s51 offen offset:1024 sc0 sc1
Mz 112,40,8
Rt 146,22,18432
v_mul_f32_e32 v75, s54, v75
Mz 116,44,8
Cv 74,74,75
v_accvgpr_write_b32 a117, v74
Mz 176,48,8
Rt 148,23,16384
v_mul_f32_e32 v76, s54, v76
Mz 180,52,8
buffer_load_dwordx4 v14, s[8:11], s39 offen offset:2048 lds
Mz 240,56,8
Rt 150,23,18432
v_mul_f32_e32 v77, s54, v77
Mz 244,60,8
Cv 76,76,77
v_accvgpr_write_b32 a118, v76
Mz 252,60,12
Rt 152,24,16384
v_mul_f32_e32 v78, s54, v78
Mz 248,56,12
v_mul_f32_e32 v79, s54, v79
Cv 78,78,79
buffer_store_dwordx4 a[72:75], v2, s[20:23], s51 offen offset:2048 sc0 sc1
Mz 188,52,12
Rt 154,24,18432
v_accvgpr_write_b32 a119, v78
)" R"(
Mz 184,48,12
v_mul_f32_e32 v80, s54, v80
v_mul_f32_e32 v81, s54, v81
Mz 124,44,12
Rt 156,25,16384
Cv 80,80,81
Mz 120,40,12
buffer_load_dwordx4 v15, s[8:11], s39 offen offset:3072 lds
Mz 60,36,12
Rt 158,25,18432
v_accvgpr_write_b32 a120, v80
Mz 56,32,12
v_mul_f32_e32 v82, s54, v82
v_mul_f32_e32 v83, s54, v83
Mz 128,40,16
Rb 160,17,0
Cv 82,82,83
Mz 132,44,16
v_accvgpr_write_b32 a121, v82
v_mul_f32_e32 v84, s54, v84
buffer_store_dwordx4 a[76:79], v2, s[20:23], s51 offen offset:3072 sc0 sc1
Mz 192,48,16
Rb 164,17,2048
v_mul_f32_e32 v85, s54, v85
Mz 196,52,16
Cv 84,84,85
v_accvgpr_write_b32 a122, v84
v_mfma_f32_16x16x32_bf16 a[192:195], a[56:59], a[16:19], 0
Rb 168,17,8192
v_mul_f32_e32 v86, s54, v86
v_mfma_f32_16x16x32_bf16 a[196:199], a[60:63], a[16:19], 0
v_mul_f32_e32 v87, s54, v87
Cv 86,86,87
v_mfma_f32_16x16x32_bf16 a[204:207], a[60:63], a[20:23], 0
Rb 172,17,10240
v_accvgpr_write_b32 a123, v86
v_mfma_f32_16x16x32_bf16 a[200:203], a[56:59], a[20:23], 0
v_mul_f32_e32 v88, s54, v88
v_mul_f32_e32 v89, s54, v89
Mz 204,52,20
Rb 176,17,16384
Cv 88,88,89
Mz 200,48,20
v_accvgpr_write_b32 a124, v88
v_mul_f32_e32 v90, s54, v90
buffer_store_dwordx4 a[80:83], v3, s[20:23], s51 offen offset:0 sc0 sc1
Mz 140,44,20
Rb 180,17,18432
v_mul_f32_e32 v91, s54, v91
Mz 136,40,20
Cv 90,90,91
v_accvgpr_write_b32 a125, v90
Mz 144,40,24
Rb 184,17,24576
v_mul_f32_e32 v92, s54, v92
Mz 148,44,24
v_mul_f32_e32 v93, s54, v93
Cv 92,92,93
Mz 208,48,24
Rb 188,17,26624
v_accvgpr_write_b32 a126, v92
Mz 212,52,24
v_mul_f32_e32 v94, s54, v94
v_mul_f32_e32 v95, s54, v95
)" R"(
v_mfma_f32_16x16x32_bf16 a[208:211], a[56:59], a[24:27], 0
Cv 94,94,95
v_accvgpr_write_b32 a127, v94
v_mfma_f32_16x16x32_bf16 a[212:215], a[60:63], a[24:27], 0
buffer_store_dwordx4 a[84:87], v3, s[20:23], s51 offen offset:1024 sc0 sc1
v_mfma_f32_16x16x32_bf16 a[220:223], a[60:63], a[28:31], 0
v_mfma_f32_16x16x32_bf16 a[216:219], a[56:59], a[28:31], 0
Mz 220,52,28
Mz 216,48,28
Mz 156,44,28
Mz 152,40,28
Mz 64,32,16
Mz 68,36,16
buffer_store_dwordx4 a[88:91], v3, s[20:23], s51 offen offset:2048 sc0 sc1
Mz 72,32,20
Mz 76,36,20
Mz 80,32,24
Mz 84,36,24
Mz 88,32,28
Mz 92,36,28
s_waitcnt vmcnt(15) lgkmcnt(0)
s_barrier
Mv 32,160,128
Rt 0,18,32768
Mv 36,164,128
s_add_u32 s38, s38, 0xffff0080
Mv 96,168,128
Rt 2,18,34816
s_add_u32 m0, s36, 0
Mv 100,172,128
buffer_load_dwordx4 v8, s[4:7], s38 offen offset:0 lds
Mv 160,176,128
Rt 4,19,32768
Mv 164,180,128
Mv 224,184,128
Rt 6,19,34816
Mv 228,188,128
buffer_load_dwordx4 v9, s[4:7], s38 offen offset:1024 lds
Mv 236,188,132
Rt 8,20,32768
Mv 232,184,132
Mv 172,180,132
Rt 10,20,34816
Mv 168,176,132
buffer_load_dwordx4 v10, s[4:7], s38 offen offset:2048 lds
Mv 108,172,132
Rt 12,21,32768
Mv 104,168,132
Mv 44,164,132
Rt 14,21,34816
Mv 40,160,132
buffer_load_dwordx4 v11, s[4:7], s38 offen offset:3072 lds
Mv 48,160,136
Rt 16,22,32768
Mv 52,164,136
s_add_u32 s38, s38, 0x10000
Mv 112,168,136
Rt 18,22,34816
s_add_u32 m0, s36, 4096
Mv 116,172,136
buffer_load_dwordx4 v8, s[4:7], s38 offen offset:0 lds
Mv 176,176,136
Rt 20,23,32768
Mv 180,180,136
Mv 240,184,136
)" R"(
Rt 22,23,34816
Mv 244,188,136
buffer_load_dwordx4 v9, s[4:7], s38 offen offset:1024 lds
Mv 252,188,140
Rt 24,24,32768
Mv 248,184,140
Mv 188,180,140
Rt 26,24,34816
Mv 184,176,140
buffer_load_dwordx4 v10, s[4:7], s38 offen offset:2048 lds
Mv 124,172,140
Rt 28,25,32768
Mv 120,168,140
Mv 60,164,140
Rt 30,25,34816
Mv 56,160,140
buffer_load_dwordx4 v11, s[4:7], s38 offen offset:3072 lds
Mv 64,160,144
Rb 32,16,32768
Mv 68,164,144
Mv 128,168,144
Rb 36,16,34816
Mv 132,172,144
s_add_u32 s39, s39, 0x40000
Mv 192,176,144
Rb 40,16,40960
s_add_u32 m0, s37, 16384
Mv 196,180,144
buffer_load_dwordx4 v12, s[8:11], s39 offen offset:0 lds
Ma 192,184,144
Rb 44,16,43008
Ma 196,188,144
Ma 204,188,148
Rb 48,16,49152
Ma 200,184,148
Mv 204,180,148
Rb 52,16,51200
Mv 200,176,148
buffer_load_dwordx4 v13, s[8:11], s39 offen offset:1024 lds
Mv 140,172,148
Rb 56,16,57344
Mv 136,168,148
Mv 76,164,148
Rb 60,16,59392
s_lshl2_add_u32 s28, s2, s33
Mv 72,160,148
s_lshl_b32 s28, s28, 11
s_add_u32 s28, s28, 0xc000000
Mv 80,160,152
v_min_u32 v5, 15, v0
v_lshlrev_b32 v5, 7, v5
Mv 84,164,152
buffer_load_dwordx4 v14, s[8:11], s39 offen offset:2048 lds
Mv 144,168,152
buffer_load_dword v5, v5, s[20:23], s28 offen
Mv 148,172,152
Mv 208,176,152
Mv 212,180,152
Ma 208,184,152
Ma 212,188,152
buffer_load_dwordx4 v15, s[8:11], s39 offen offset:3072 lds
Ma 220,188,156
Ma 216,184,156
Mv 220,180,156
)" R"(
Mv 216,176,156
buffer_store_dwordx4 a[92:95], v3, s[20:23], s51 offen offset:3072 sc0 sc1
Mv 156,172,156
Mv 152,168,156
Mv 92,164,156
Mv 88,160,156
s_waitcnt vmcnt(25) lgkmcnt(0)
s_barrier
Mv 32,32,0
Rt 128,18,49152
Mv 36,36,0
Mv 96,40,0
Rt 130,18,51200
v_accvgpr_write_b32 a224, -1
Mv 100,44,0
s_add_u32 s39, s39, 0x40000
v_accvgpr_write_b32 a225, -1
Mv 160,48,0
Rt 132,19,49152
s_add_u32 m0, s37, 32768
Mv 164,52,0
buffer_load_dwordx4 v12, s[8:11], s39 offen offset:0 lds
Mv 224,56,0
Rt 134,19,51200
v_accvgpr_write_b32 a226, -1
Mv 228,60,0
v_accvgpr_write_b32 a227, -1
s_add_u32 s88, s73, 0x4000
Mv 236,60,4
Rt 136,20,49152
s_mov_b32 s40, 2
Mv 232,56,4
s_add_u32 s51, s51, 0x10000
buffer_store_dwordx4 a[96:99], v2, s[20:23], s51 offen offset:0 sc0 sc1
Mv 172,52,4
Rt 138,20,51200
s_mov_b32 s94, 0
Mv 168,48,4
Mv 108,44,4
Rt 140,21,49152
Mv 104,40,4
buffer_load_dwordx4 v13, s[8:11], s39 offen offset:1024 lds
Mv 44,36,4
Rt 142,21,51200
Mv 40,32,4
Mv 48,32,8
Rt 144,22,49152
Mv 52,36,8
buffer_store_dwordx4 a[100:103], v2, s[20:23], s51 offen offset:1024 sc0 sc1
Mv 112,40,8
Rt 146,22,51200
Mv 116,44,8
Mv 176,48,8
Rt 148,23,49152
Mv 180,52,8
buffer_load_dwordx4 v14, s[8:11], s39 offen offset:2048 lds
Mv 240,56,8
Rt 150,23,51200
Mv 244,60,8
Mv 252,60,12
Rt 152,24,49152
Mv 248,56,12
buffer_store_dwordx4 a[104:107], v2, s[20:23], s51 offen offset:2048 sc0 sc1
Mv 188,52,12
Rt 154,24,51200
)" R"(
Mv 184,48,12
Mv 124,44,12
Rt 156,25,49152
Mv 120,40,12
buffer_load_dwordx4 v15, s[8:11], s39 offen offset:3072 lds
Mv 60,36,12
Rt 158,25,51200
Mv 56,32,12
Mv 64,32,16
Rb 160,17,32768
Mv 68,36,16
buffer_store_dwordx4 a[108:111], v2, s[20:23], s51 offen offset:3072 sc0 sc1
Mv 128,40,16
Rb 164,17,34816
s_lshl3_add_u32 s29, s34, s35
Mv 132,44,16
s_lshl2_add_u32 s29, s29, s33
s_mul_i32 s83, s29, 7168
Mv 192,48,16
Rb 168,17,40960
Mv 196,52,16
s_bitcmp1_b32 s34, 0
s_cselect_b32 s29, 0, 448
Ma 192,56,16
Rb 172,17,43008
s_add_u32 s83, s83, s29
Ma 196,60,16
s_lshl_b32 s82, s34, 21
s_add_u32 s82, s82, 0x6000000
Ma 204,60,20
Rb 176,17,49152
s_bfe_u32 s28, s2, 0x40003
Ma 200,56,20
buffer_store_dwordx4 a[112:115], v3, s[20:23], s51 offen offset:0 sc0 sc1
Mv 204,52,20
Rb 180,17,51200
s_lshl2_add_u32 s90, s28, s33
Mv 200,48,20
s_mul_i32 s90, s90, 28672
s_and_b32 s29, s2, 7
Mv 140,44,20
Rb 184,17,57344
s_mul_i32 s87, s29, 448
Mv 136,40,20
s_add_u32 s90, s90, s87
s_mul_i32 s29, s29, 14
Mv 76,36,20
Rb 188,17,59392
s_add_u32 s29, s29, s33
Mv 72,32,20
s_mul_i32 s29, s29, 28
s_add_u32 s86, s28, s29
Mv 80,32,24
s_lshl_b32 s86, s86, 10
s_add_u32 s86, s86, 0xc200000
Mv 84,36,24
buffer_store_dwordx4 a[116:119], v3, s[20:23], s51 offen offset:1024 sc0 sc1
Mv 144,40,24
s_cmp_gt_u32 s28, 13
s_cmov_b32 s86, 0x40000000
Mv 148,44,24
s_lshl_b32 s87, s2, 7
s_add_u32 s87, s87, 0xc150000
Mv 208,48,24
)" R"(
s_cmp_lg_u32 s33, 0
s_cmov_b32 s87, 0x40000000
Mv 212,52,24
v_lshrrev_b32 v4, 5, v0
v_mul_u32_u24 v27, 2688, v4
Ma 208,56,24
v_mul_u32_u24 v4, 384, v4
v_add_u32 v26, v4, v1
Ma 212,60,24
v_and_b32 v4, 31, v0
v_cmp_lt_u32_e32 vcc, 27, v4
Ma 220,60,28
v_mov_b32 v4, 0x40000000
v_lshrrev_b32 v29, 2, v0
Ma 216,56,28
buffer_store_dwordx4 a[120:123], v3, s[20:23], s51 offen offset:2048 sc0 sc1
Mv 220,52,28
v_cndmask_b32_e32 v26, v26, v4, vcc
v_add_u32 v27, v27, v26
Mv 216,48,28
v_and_b32 v4, 48, v1
v_lshl_or_b32 v29, v29, 9, v4
Mv 156,44,28
s_lshr_b32 s28, s33, 1
s_lshl_b32 s28, s28, 6
Mv 152,40,28
s_and_b32 s29, s33, 1
s_lshl3_add_u32 s28, s29, s28
Mv 92,36,28
s_add_u32 s28, s28, 0x20000
v_add_u32 v29, s28, v29
Mv 88,32,28
s_waitcnt vmcnt(17) lgkmcnt(0)
s_barrier
Mv 32,160,128
Rt 0,18,0
v_lshrrev_b32 v4, 4, v0
Mv 36,164,128
s_add_u32 s38, s38, 0xffff0080
v_bfe_u32 v28, v0, 2, 2
Mv 96,168,128
Rt 2,18,2048
s_add_u32 m0, s36, 32768
Mv 100,172,128
buffer_load_dwordx4 v8, s[4:7], s38 offen offset:0 lds
Mv 160,176,128
Rt 4,19,0
v_lshl_or_b32 v28, v4, 3, v28
Mv 164,180,128
v_and_b32 v4, 3, v0
v_lshlrev_b32 v4, 3, v4
Mv 224,184,128
Rt 6,19,2048
v_lshl_or_b32 v28, v28, 9, v4
Mv 228,188,128
buffer_load_dwordx4 v9, s[4:7], s38 offen offset:1024 lds
Mv 236,188,132
Rt 8,20,0
s_lshl_b32 s29, s33, 5
Mv 232,184,132
s_add_u32 s29, s29, 0x24000
v_add_u32 v28, s29, v28
Mv 172,180,132
Rt 10,20,2048
Mv 168,176,132
)" R"(
buffer_load_dwordx4 v10, s[4:7], s38 offen offset:2048 lds
Mv 108,172,132
Rt 12,21,0
Mv 104,168,132
Mv 44,164,132
Rt 14,21,2048
Mv 40,160,132
buffer_load_dwordx4 v11, s[4:7], s38 offen offset:3072 lds
Mv 48,160,136
Rt 16,22,0
Mv 52,164,136
s_add_u32 s38, s38, 0x10000
Mv 112,168,136
Rt 18,22,2048
s_add_u32 m0, s36, 36864
Mv 116,172,136
buffer_load_dwordx4 v8, s[4:7], s38 offen offset:0 lds
Mv 176,176,136
Rt 20,23,0
Mv 180,180,136
Mv 240,184,136
Rt 22,23,2048
Mv 244,188,136
buffer_load_dwordx4 v9, s[4:7], s38 offen offset:1024 lds
Mv 252,188,140
Rt 24,24,0
Mv 248,184,140
Mv 188,180,140
Rt 26,24,2048
Mv 184,176,140
buffer_load_dwordx4 v10, s[4:7], s38 offen offset:2048 lds
Mv 124,172,140
Rt 28,25,0
Mv 120,168,140
Mv 60,164,140
Rt 30,25,2048
Mv 56,160,140
buffer_load_dwordx4 v11, s[4:7], s38 offen offset:3072 lds
Mv 64,160,144
Rb 32,16,0
Mv 68,164,144
Mv 128,168,144
Rb 36,16,2048
Mv 132,172,144
s_add_u32 s39, s39, 0x40000
Mv 192,176,144
Rb 40,16,8192
s_add_u32 m0, s37, 49152
Mv 196,180,144
buffer_load_dwordx4 v12, s[8:11], s39 offen offset:0 lds
Ma 192,184,144
Rb 44,16,10240
Ma 196,188,144
Ma 204,188,148
Rb 48,16,16384
Ma 200,184,148
Mv 204,180,148
Rb 52,16,18432
Mv 200,176,148
buffer_load_dwordx4 v13, s[8:11], s39 offen offset:1024 lds
Mv 140,172,148
Rb 56,16,24576
Mv 136,168,148
Mv 76,164,148
)" R"(
Rb 60,16,26624
Mv 72,160,148
Mv 80,160,152
Mv 84,164,152
buffer_load_dwordx4 v14, s[8:11], s39 offen offset:2048 lds
Mv 144,168,152
Mv 148,172,152
Mv 208,176,152
Mv 212,180,152
Ma 208,184,152
Ma 212,188,152
buffer_load_dwordx4 v15, s[8:11], s39 offen offset:3072 lds
Ma 220,188,156
Ma 216,184,156
Mv 220,180,156
Mv 216,176,156
buffer_store_dwordx4 a[124:127], v3, s[20:23], s51 offen offset:3072 sc0 sc1
Mv 156,172,156
Mv 152,168,156
Mv 92,164,156
Mv 88,160,156
s_waitcnt vmcnt(25) lgkmcnt(0)
s_barrier
.LzPBV:
Mv 32,32,0
Rt 128,18,16384
Mv 36,36,0
Mv 96,40,0
Rt 130,18,18432
Mv 100,44,0
s_add_u32 s39, s39, 0x40000
Mv 160,48,0
Rt 132,19,16384
s_add_u32 m0, s37, 0
Mv 164,52,0
buffer_load_dwordx4 v12, s[8:11], s39 offen offset:0 lds
Mv 224,56,0
Rt 134,19,18432
Mv 228,60,0
Mv 236,60,4
Rt 136,20,16384
Mv 232,56,4
Mv 172,52,4
Rt 138,20,18432
Mv 168,48,4
Mv 108,44,4
Rt 140,21,16384
Mv 104,40,4
buffer_load_dwordx4 v13, s[8:11], s39 offen offset:1024 lds
Mv 44,36,4
Rt 142,21,18432
Mv 40,32,4
Mv 48,32,8
Rt 144,22,16384
Mv 52,36,8
Mv 112,40,8
Rt 146,22,18432
Mv 116,44,8
Mv 176,48,8
Rt 148,23,16384
Mv 180,52,8
buffer_load_dwordx4 v14, s[8:11], s39 offen offset:2048 lds
Mv 240,56,8
Rt 150,23,18432
Mv 244,60,8
)" R"(
Mv 252,60,12
Rt 152,24,16384
Mv 248,56,12
Mv 188,52,12
Rt 154,24,18432
Mv 184,48,12
Mv 124,44,12
Rt 156,25,16384
Mv 120,40,12
buffer_load_dwordx4 v15, s[8:11], s39 offen offset:3072 lds
Mv 60,36,12
Rt 158,25,18432
Mv 56,32,12
Mv 64,32,16
Rb 160,17,0
Mv 68,36,16
Mv 128,40,16
Rb 164,17,2048
Mv 132,44,16
Mv 192,48,16
Rb 168,17,8192
Mv 196,52,16
buffer_store_dwordx4 a[224:227], v1, s[20:23], s72 offen sc0 sc1
Ma 192,56,16
Rb 172,17,10240
s_add_u32 s72, s72, 1024
Ma 196,60,16
Ma 204,60,20
Rb 176,17,16384
Ma 200,56,20
Mv 204,52,20
Rb 180,17,18432
Mv 200,48,20
Mv 140,44,20
Rb 184,17,24576
Mv 136,40,20
Mv 76,36,20
Rb 188,17,26624
Mv 72,32,20
Mv 80,32,24
Mv 84,36,24
buffer_store_dwordx4 a[224:227], v1, s[20:23], s88 offen sc0 sc1
Mv 144,40,24
s_add_u32 s88, s88, 1024
Mv 148,44,24
Mv 208,48,24
Mv 212,52,24
Ma 208,56,24
Ma 212,60,24
Ma 220,60,28
Ma 216,56,28
Mv 220,52,28
Mv 216,48,28
Mv 156,44,28
Mv 152,40,28
Mv 92,36,28
Mv 88,32,28
s_waitcnt vmcnt(10) lgkmcnt(0)
s_barrier
Mv 32,160,128
Rt 0,18,32768
Mv 36,164,128
s_add_u32 s38, s38, 0xffff0080
Mv 96,168,128
Rt 2,18,34816
)" R"(
s_add_u32 m0, s36, 0
Mv 100,172,128
buffer_load_dwordx4 v8, s[4:7], s38 offen offset:0 lds
Mv 160,176,128
Rt 4,19,32768
Mv 164,180,128
Mv 224,184,128
Rt 6,19,34816
Mv 228,188,128
buffer_load_dwordx4 v9, s[4:7], s38 offen offset:1024 lds
Mv 236,188,132
Rt 8,20,32768
Mv 232,184,132
Mv 172,180,132
Rt 10,20,34816
Mv 168,176,132
buffer_load_dwordx4 v10, s[4:7], s38 offen offset:2048 lds
Mv 108,172,132
Rt 12,21,32768
Mv 104,168,132
Mv 44,164,132
Rt 14,21,34816
Mv 40,160,132
buffer_load_dwordx4 v11, s[4:7], s38 offen offset:3072 lds
Mv 48,160,136
Rt 16,22,32768
Mv 52,164,136
s_add_u32 s38, s38, 0x10000
Mv 112,168,136
Rt 18,22,34816
s_add_u32 m0, s36, 4096
Mv 116,172,136
buffer_load_dwordx4 v8, s[4:7], s38 offen offset:0 lds
Mv 176,176,136
Rt 20,23,32768
Mv 180,180,136
Mv 240,184,136
Rt 22,23,34816
Mv 244,188,136
buffer_load_dwordx4 v9, s[4:7], s38 offen offset:1024 lds
Mv 252,188,140
Rt 24,24,32768
Mv 248,184,140
Mv 188,180,140
Rt 26,24,34816
Mv 184,176,140
buffer_load_dwordx4 v10, s[4:7], s38 offen offset:2048 lds
Mv 124,172,140
Rt 28,25,32768
Mv 120,168,140
Mv 60,164,140
Rt 30,25,34816
Mv 56,160,140
buffer_load_dwordx4 v11, s[4:7], s38 offen offset:3072 lds
Mv 64,160,144
Rb 32,16,32768
Mv 68,164,144
Mv 128,168,144
Rb 36,16,34816
Mv 132,172,144
s_add_u32 s39, s39, 0x40000
Mv 192,176,144
Rb 40,16,40960
s_add_u32 m0, s37, 16384
)" R"(
Mv 196,180,144
buffer_load_dwordx4 v12, s[8:11], s39 offen offset:0 lds
Ma 192,184,144
Rb 44,16,43008
Ma 196,188,144
Ma 204,188,148
Rb 48,16,49152
Ma 200,184,148
Mv 204,180,148
Rb 52,16,51200
Mv 200,176,148
buffer_load_dwordx4 v13, s[8:11], s39 offen offset:1024 lds
Mv 140,172,148
Rb 56,16,57344
Mv 136,168,148
Mv 76,164,148
Rb 60,16,59392
Mv 72,160,148
Mv 80,160,152
Mv 84,164,152
buffer_load_dwordx4 v14, s[8:11], s39 offen offset:2048 lds
Mv 144,168,152
Mv 148,172,152
Mv 208,176,152
Mv 212,180,152
Ma 208,184,152
Ma 212,188,152
buffer_load_dwordx4 v15, s[8:11], s39 offen offset:3072 lds
Ma 220,188,156
Ma 216,184,156
Mv 220,180,156
Mv 216,176,156
Mv 156,172,156
Mv 152,168,156
Mv 92,164,156
Mv 88,160,156
s_waitcnt vmcnt(18) lgkmcnt(0)
s_barrier
buffer_store_dword v6, v7, s[20:23], s52 offen offset:0 sc0 sc1
s_mov_b32 s52, 0x40000000
Mv 32,32,0
Rt 128,18,49152
Mv 36,36,0
Mv 96,40,0
Rt 130,18,51200
Mv 100,44,0
s_add_u32 s39, s39, 0x40000
Mv 160,48,0
Rt 132,19,49152
s_add_u32 m0, s37, 32768
Mv 164,52,0
buffer_load_dwordx4 v12, s[8:11], s39 offen offset:0 lds
Mv 224,56,0
Rt 134,19,51200
Mv 228,60,0
Mv 236,60,4
Rt 136,20,49152
Mv 232,56,4
Mv 172,52,4
Rt 138,20,51200
Mv 168,48,4
Mv 108,44,4
Rt 140,21,49152
Mv 104,40,4
buffer_load_dwordx4 v13, s[8:11], s39 offen offset:1024 lds
)" R"(
Mv 44,36,4
Rt 142,21,51200
Mv 40,32,4
Mv 48,32,8
Rt 144,22,49152
Mv 52,36,8
Mv 112,40,8
Rt 146,22,51200
Mv 116,44,8
Mv 176,48,8
Rt 148,23,49152
Mv 180,52,8
buffer_load_dwordx4 v14, s[8:11], s39 offen offset:2048 lds
Mv 240,56,8
Rt 150,23,51200
Mv 244,60,8
Mv 252,60,12
Rt 152,24,49152
Mv 248,56,12
Mv 188,52,12
Rt 154,24,51200
Mv 184,48,12
Mv 124,44,12
Rt 156,25,49152
Mv 120,40,12
buffer_load_dwordx4 v15, s[8:11], s39 offen offset:3072 lds
Mv 60,36,12
Rt 158,25,51200
Mv 56,32,12
Mv 64,32,16
Rb 160,17,32768
Mv 68,36,16
Mv 128,40,16
Rb 164,17,34816
Mv 132,44,16
Mv 192,48,16
Rb 168,17,40960
Mv 196,52,16
buffer_store_dwordx4 a[224:227], v1, s[20:23], s72 offen sc0 sc1
Ma 192,56,16
Rb 172,17,43008
s_add_u32 s72, s72, 1024
Ma 196,60,16
Ma 204,60,20
Rb 176,17,49152
Ma 200,56,20
Mv 204,52,20
Rb 180,17,51200
Mv 200,48,20
Mv 140,44,20
Rb 184,17,57344
Mv 136,40,20
Mv 76,36,20
Rb 188,17,59392
Mv 72,32,20
Mv 80,32,24
Mv 84,36,24
buffer_store_dwordx4 a[224:227], v1, s[20:23], s88 offen sc0 sc1
Mv 144,40,24
s_add_u32 s88, s88, 1024
Mv 148,44,24
Mv 208,48,24
Mv 212,52,24
Ma 208,56,24
)" R"(
Ma 212,60,24
Ma 220,60,28
Ma 216,56,28
Mv 220,52,28
Mv 216,48,28
Mv 156,44,28
Mv 152,40,28
Mv 92,36,28
Mv 88,32,28
s_waitcnt vmcnt(11) lgkmcnt(0)
s_barrier
Mv 32,160,128
Rt 0,18,0
Mv 36,164,128
s_add_u32 s38, s38, 0xffff0080
Mv 96,168,128
Rt 2,18,2048
s_add_u32 m0, s36, 32768
Mv 100,172,128
buffer_load_dwordx4 v8, s[4:7], s38 offen offset:0 lds
Mv 160,176,128
Rt 4,19,0
Mv 164,180,128
Mv 224,184,128
Rt 6,19,2048
Mv 228,188,128
buffer_load_dwordx4 v9, s[4:7], s38 offen offset:1024 lds
Mv 236,188,132
Rt 8,20,0
Mv 232,184,132
Mv 172,180,132
Rt 10,20,2048
Mv 168,176,132
buffer_load_dwordx4 v10, s[4:7], s38 offen offset:2048 lds
Mv 108,172,132
Rt 12,21,0
Mv 104,168,132
Mv 44,164,132
Rt 14,21,2048
Mv 40,160,132
buffer_load_dwordx4 v11, s[4:7], s38 offen offset:3072 lds
Mv 48,160,136
Rt 16,22,0
Mv 52,164,136
s_add_u32 s38, s38, 0x10000
Mv 112,168,136
Rt 18,22,2048
s_add_u32 m0, s36, 36864
Mv 116,172,136
buffer_load_dwordx4 v8, s[4:7], s38 offen offset:0 lds
Mv 176,176,136
Rt 20,23,0
Mv 180,180,136
Mv 240,184,136
Rt 22,23,2048
Mv 244,188,136
buffer_load_dwordx4 v9, s[4:7], s38 offen offset:1024 lds
Mv 252,188,140
Rt 24,24,0
Mv 248,184,140
Mv 188,180,140
Rt 26,24,2048
Mv 184,176,140
buffer_load_dwordx4 v10, s[4:7], s38 offen offset:2048 lds
Mv 124,172,140
)" R"(
Rt 28,25,0
Mv 120,168,140
Mv 60,164,140
Rt 30,25,2048
Mv 56,160,140
buffer_load_dwordx4 v11, s[4:7], s38 offen offset:3072 lds
Mv 64,160,144
Rb 32,16,0
Mv 68,164,144
Mv 128,168,144
Rb 36,16,2048
Mv 132,172,144
s_add_u32 s39, s39, 0x40000
Mv 192,176,144
Rb 40,16,8192
s_add_u32 m0, s37, 49152
Mv 196,180,144
buffer_load_dwordx4 v12, s[8:11], s39 offen offset:0 lds
Ma 192,184,144
Rb 44,16,10240
Ma 196,188,144
Ma 204,188,148
Rb 48,16,16384
Ma 200,184,148
Mv 204,180,148
Rb 52,16,18432
Mv 200,176,148
buffer_load_dwordx4 v13, s[8:11], s39 offen offset:1024 lds
Mv 140,172,148
Rb 56,16,24576
Mv 136,168,148
Mv 76,164,148
Rb 60,16,26624
Mv 72,160,148
Mv 80,160,152
Mv 84,164,152
buffer_load_dwordx4 v14, s[8:11], s39 offen offset:2048 lds
Mv 144,168,152
Mv 148,172,152
Mv 208,176,152
Mv 212,180,152
Ma 208,184,152
Ma 212,188,152
buffer_load_dwordx4 v15, s[8:11], s39 offen offset:3072 lds
Ma 220,188,156
Ma 216,184,156
Mv 220,180,156
s_sub_u32 s40, s40, 1
Mv 216,176,156
Mv 156,172,156
Mv 152,168,156
Mv 92,164,156
Mv 88,160,156
s_cmp_lg_u32 s40, 0
s_waitcnt vmcnt(19) lgkmcnt(0)
s_barrier
s_cbranch_scc1 .LzPBV
s_cmp_lg_u32 s94, 0
s_cbranch_scc1 .LzPBVX
Mv 32,32,0
Rt 128,18,16384
Mv 36,36,0
Mv 96,40,0
Rt 130,18,18432
Mv 100,44,0
)" R"(
s_add_u32 s39, s39, 0x40000
Mv 160,48,0
Rt 132,19,16384
s_add_u32 m0, s37, 0
Mv 164,52,0
buffer_load_dwordx4 v12, s[8:11], s39 offen offset:0 lds
Mv 224,56,0
Rt 134,19,18432
Mv 228,60,0
Mv 236,60,4
Rt 136,20,16384
Mv 232,56,4
Mv 172,52,4
Rt 138,20,18432
Mv 168,48,4
Mv 108,44,4
Rt 140,21,16384
Mv 104,40,4
buffer_load_dwordx4 v13, s[8:11], s39 offen offset:1024 lds
Mv 44,36,4
Rt 142,21,18432
Mv 40,32,4
Mv 48,32,8
Rt 144,22,16384
Mv 52,36,8
Mv 112,40,8
Rt 146,22,18432
Mv 116,44,8
Mv 176,48,8
Rt 148,23,16384
s_mov_b32 s24, s76
Mv 180,52,8
buffer_load_dwordx4 v14, s[8:11], s39 offen offset:2048 lds
Mv 240,56,8
Rt 150,23,18432
s_mov_b32 s25, s77
Mv 244,60,8
s_mov_b32 s26, 0x188000
s_mov_b32 s27, 0x20000
Mv 252,60,12
Rt 152,24,16384
Mv 248,56,12
Mv 188,52,12
Rt 154,24,18432
Mv 184,48,12
Mv 124,44,12
Rt 156,25,16384
Mv 120,40,12
buffer_load_dwordx4 v15, s[8:11], s39 offen offset:3072 lds
Mv 60,36,12
Rt 158,25,18432
s_add_u32 m0, s37, 0x14000
Mv 56,32,12
s_add_u32 s92, s90, 0
Mv 64,32,16
Rb 160,17,0
Mv 68,36,16
buffer_load_dwordx4 v27, s[24:27], s92 offen offset:0 lds
Mv 128,40,16
Rb 164,17,2048
s_add_u32 s92, s90, 6144
Mv 132,44,16
Mv 192,48,16
Rb 168,17,8192
)" R"(
Mv 196,52,16
buffer_load_dwordx4 v27, s[24:27], s92 offen offset:1024 lds
Ma 192,56,16
Rb 172,17,10240
s_add_u32 s92, s90, 12288
Ma 196,60,16
Ma 204,60,20
Rb 176,17,16384
Ma 200,56,20
buffer_load_dwordx4 v27, s[24:27], s92 offen offset:2048 lds
Mv 204,52,20
Rb 180,17,18432
s_add_u32 s92, s90, 18432
Mv 200,48,20
Mv 140,44,20
Rb 184,17,24576
Mv 136,40,20
buffer_load_dwordx4 v27, s[24:27], s92 offen offset:3072 lds
Mv 76,36,20
Rb 188,17,26624
s_mov_b32 s24, s80
Mv 72,32,20
s_mov_b32 s25, s81
s_mov_b32 s26, 0x380000
Mv 80,32,24
s_mov_b32 s27, 0x20000
s_add_u32 m0, s37, 0x10000
Mv 84,36,24
s_add_u32 s92, s83, 0
Mv 144,40,24
buffer_load_dwordx4 v26, s[24:27], s92 offen offset:0 lds
Mv 148,44,24
s_add_u32 s92, s83, 768
Mv 208,48,24
Mv 212,52,24
buffer_load_dwordx4 v26, s[24:27], s92 offen offset:1024 lds
Ma 208,56,24
s_add_u32 s92, s83, 1536
Ma 212,60,24
Ma 220,60,28
Ma 216,56,28
buffer_load_dwordx4 v26, s[24:27], s92 offen offset:2048 lds
Mv 220,52,28
s_add_u32 s92, s83, 2304
Mv 216,48,28
Mv 156,44,28
Mv 152,40,28
buffer_load_dwordx4 v26, s[24:27], s92 offen offset:3072 lds
Mv 92,36,28
Mv 88,32,28
s_waitcnt vmcnt(16) lgkmcnt(0)
s_barrier
Mv 32,160,128
Rt 0,18,32768
Mv 36,164,128
s_add_u32 s38, s38, 0xffff0080
Mv 96,168,128
Rt 2,18,34816
s_add_u32 m0, s36, 0
Mv 100,172,128
buffer_load_dwordx4 v8, s[4:7], s38 offen offset:0 lds
Mv 160,176,128
Rt 4,19,32768
Mv 164,180,128
Mv 224,184,128
)" R"(
Rt 6,19,34816
Mv 228,188,128
buffer_load_dwordx4 v9, s[4:7], s38 offen offset:1024 lds
Mv 236,188,132
Rt 8,20,32768
Mv 232,184,132
Mv 172,180,132
Rt 10,20,34816
Mv 168,176,132
buffer_load_dwordx4 v10, s[4:7], s38 offen offset:2048 lds
Mv 108,172,132
Rt 12,21,32768
Mv 104,168,132
Mv 44,164,132
Rt 14,21,34816
Mv 40,160,132
buffer_load_dwordx4 v11, s[4:7], s38 offen offset:3072 lds
Mv 48,160,136
Rt 16,22,32768
Mv 52,164,136
s_add_u32 s38, s38, 0x10000
Mv 112,168,136
Rt 18,22,34816
s_add_u32 m0, s36, 4096
Mv 116,172,136
buffer_load_dwordx4 v8, s[4:7], s38 offen offset:0 lds
Mv 176,176,136
Rt 20,23,32768
Mv 180,180,136
Mv 240,184,136
Rt 22,23,34816
Mv 244,188,136
buffer_load_dwordx4 v9, s[4:7], s38 offen offset:1024 lds
Mv 252,188,140
Rt 24,24,32768
Mv 248,184,140
Mv 188,180,140
Rt 26,24,34816
Mv 184,176,140
buffer_load_dwordx4 v10, s[4:7], s38 offen offset:2048 lds
Mv 124,172,140
Rt 28,25,32768
Mv 120,168,140
Mv 60,164,140
Rt 30,25,34816
Mv 56,160,140
buffer_load_dwordx4 v11, s[4:7], s38 offen offset:3072 lds
Mv 64,160,144
Rb 32,16,32768
Mv 68,164,144
Mv 128,168,144
Rb 36,16,34816
Mv 132,172,144
s_add_u32 s39, s39, 0x40000
Mv 192,176,144
Rb 40,16,40960
s_add_u32 m0, s37, 16384
Mv 196,180,144
buffer_load_dwordx4 v12, s[8:11], s39 offen offset:0 lds
Ma 192,184,144
Rb 44,16,43008
Ma 196,188,144
Ma 204,188,148
Rb 48,16,49152
)" R"(
Ma 200,184,148
Mv 204,180,148
Rb 52,16,51200
Mv 200,176,148
buffer_load_dwordx4 v13, s[8:11], s39 offen offset:1024 lds
Mv 140,172,148
Rb 56,16,57344
Mv 136,168,148
Mv 76,164,148
Rb 60,16,59392
Mv 72,160,148
Mv 80,160,152
Mv 84,164,152
buffer_load_dwordx4 v14, s[8:11], s39 offen offset:2048 lds
Mv 144,168,152
Mv 148,172,152
Mv 208,176,152
Mv 212,180,152
Ma 208,184,152
Ma 212,188,152
buffer_load_dwordx4 v15, s[8:11], s39 offen offset:3072 lds
Ma 220,188,156
Ma 216,184,156
Mv 220,180,156
Mv 216,176,156
Mv 156,172,156
Mv 152,168,156
Mv 92,164,156
Mv 88,160,156
s_waitcnt vmcnt(24) lgkmcnt(0)
s_barrier
Mv 32,32,0
Rt 128,18,49152
Mv 36,36,0
Mv 96,40,0
Rt 130,18,51200
Mv 100,44,0
s_add_u32 s39, s39, 0x40000
Mv 160,48,0
Rt 132,19,49152
s_add_u32 m0, s37, 32768
Mv 164,52,0
buffer_load_dwordx4 v12, s[8:11], s39 offen offset:0 lds
Mv 224,56,0
Rt 134,19,51200
Mv 228,60,0
Mv 236,60,4
Rt 136,20,49152
Mv 232,56,4
buffer_store_dwordx4 a[224:227], v1, s[20:23], s72 offen sc0 sc1
Mv 172,52,4
Rt 138,20,51200
s_add_u32 s72, s72, 1024
Mv 168,48,4
Mv 108,44,4
Rt 140,21,49152
Mv 104,40,4
buffer_load_dwordx4 v13, s[8:11], s39 offen offset:1024 lds
Mv 44,36,4
Rt 142,21,51200
Mv 40,32,4
Mv 48,32,8
Rt 144,22,49152
Mv 52,36,8
buffer_store_dwordx4 a[224:227], v1, s[20:23], s88 offen sc0 sc1
)" R"(
Mv 112,40,8
Rt 146,22,51200
s_add_u32 s88, s88, 1024
Mv 116,44,8
Mv 176,48,8
Rt 148,23,49152
Mv 180,52,8
buffer_load_dwordx4 v14, s[8:11], s39 offen offset:2048 lds
Mv 240,56,8
Rt 150,23,51200
Mv 244,60,8
Mv 252,60,12
Rt 152,24,49152
Mv 248,56,12
buffer_store_dwordx4 a[224:227], v1, s[20:23], s72 offen sc0 sc1
Mv 188,52,12
Rt 154,24,51200
s_add_u32 s72, s72, 1024
Mv 184,48,12
Mv 124,44,12
Rt 156,25,49152
Mv 120,40,12
buffer_load_dwordx4 v15, s[8:11], s39 offen offset:3072 lds
Mv 60,36,12
Rt 158,25,51200
Mv 56,32,12
Mv 64,32,16
Rb 160,17,32768
Mv 68,36,16
buffer_store_dwordx4 a[224:227], v1, s[20:23], s88 offen sc0 sc1
Mv 128,40,16
Rb 164,17,34816
s_add_u32 s88, s88, 1024
Mv 132,44,16
Mv 192,48,16
Rb 168,17,40960
Mv 196,52,16
Ma 192,56,16
Rb 172,17,43008
Ma 196,60,16
Ma 204,60,20
Rb 176,17,49152
Ma 200,56,20
buffer_store_dwordx4 a[224:227], v1, s[20:23], s72 offen sc0 sc1
Mv 204,52,20
Rb 180,17,51200
s_add_u32 s72, s72, 1024
Mv 200,48,20
Mv 140,44,20
Rb 184,17,57344
Mv 136,40,20
Mv 76,36,20
Rb 188,17,59392
Mv 72,32,20
Mv 80,32,24
Mv 84,36,24
buffer_store_dwordx4 a[224:227], v1, s[20:23], s88 offen sc0 sc1
Mv 144,40,24
s_add_u32 s88, s88, 1024
Mv 148,44,24
Mv 208,48,24
Mv 212,52,24
Ma 208,56,24
Ma 212,60,24
)" R"(
Ma 220,60,28
Ma 216,56,28
Mv 220,52,28
Mv 216,48,28
Mv 156,44,28
Mv 152,40,28
Mv 92,36,28
Mv 88,32,28
s_waitcnt vmcnt(14) lgkmcnt(0)
s_barrier
Mv 32,160,128
Rt 0,18,0
s_mov_b32 s72, 0x40000000
Mv 36,164,128
s_add_u32 s38, s38, 0xffff0080
s_mov_b32 s88, 0x40000000
Mv 96,168,128
Rt 2,18,2048
s_add_u32 m0, s36, 32768
Mv 100,172,128
buffer_load_dwordx4 v8, s[4:7], s38 offen offset:0 lds
Mv 160,176,128
Rt 4,19,0
v_lshrrev_b32 v5, 2, v0
Mv 164,180,128
v_and_b32 v5, 8, v5
v_and_or_b32 v5, v0, 7, v5
Mv 224,184,128
Rt 6,19,2048
v_lshlrev_b32 v5, 8, v5
Mv 228,188,128
buffer_load_dwordx4 v9, s[4:7], s38 offen offset:1024 lds
Mv 236,188,132
Rt 8,20,0
v_and_or_b32 v5, v0, 24, v5
Mv 232,184,132
v_lshrrev_b32 v5, 1, v5
Mv 172,180,132
Rt 10,20,2048
Mv 168,176,132
buffer_load_dwordx4 v10, s[4:7], s38 offen offset:2048 lds
Mv 108,172,132
Rt 12,21,0
Mv 104,168,132
Mv 44,164,132
Rt 14,21,2048
Mv 40,160,132
buffer_load_dwordx4 v11, s[4:7], s38 offen offset:3072 lds
Mv 48,160,136
Rt 16,22,0
Mv 52,164,136
s_add_u32 s38, s38, 0x10000
Mv 112,168,136
Rt 18,22,2048
s_add_u32 m0, s36, 36864
Mv 116,172,136
buffer_load_dwordx4 v8, s[4:7], s38 offen offset:0 lds
Mv 176,176,136
Rt 20,23,0
Mv 180,180,136
Mv 240,184,136
Rt 22,23,2048
Mv 244,188,136
buffer_load_dwordx4 v9, s[4:7], s38 offen offset:1024 lds
Mv 252,188,140
)" R"(
Rt 24,24,0
Mv 248,184,140
Mv 188,180,140
Rt 26,24,2048
Mv 184,176,140
buffer_load_dwordx4 v10, s[4:7], s38 offen offset:2048 lds
Mv 124,172,140
Rt 28,25,0
Mv 120,168,140
Mv 60,164,140
Rt 30,25,2048
Mv 56,160,140
buffer_load_dwordx4 v11, s[4:7], s38 offen offset:3072 lds
Mv 64,160,144
Rb 32,16,0
Mv 68,164,144
Mv 128,168,144
Rb 36,16,2048
Mv 132,172,144
s_add_u32 s39, s39, 0x40000
Mv 192,176,144
Rb 40,16,8192
s_add_u32 m0, s37, 49152
Mv 196,180,144
buffer_load_dwordx4 v12, s[8:11], s39 offen offset:0 lds
Ma 192,184,144
Rb 44,16,10240
Ma 196,188,144
Ma 204,188,148
Rb 48,16,16384
Ma 200,184,148
Mv 204,180,148
Rb 52,16,18432
Mv 200,176,148
buffer_load_dwordx4 v13, s[8:11], s39 offen offset:1024 lds
Mv 140,172,148
Rb 56,16,24576
Mv 136,168,148
Mv 76,164,148
Rb 60,16,26624
Mv 72,160,148
Mv 80,160,152
Mv 84,164,152
buffer_load_dwordx4 v14, s[8:11], s39 offen offset:2048 lds
Mv 144,168,152
Mv 148,172,152
Mv 208,176,152
Mv 212,180,152
Ma 208,184,152
Ma 212,188,152
buffer_load_dwordx4 v15, s[8:11], s39 offen offset:3072 lds
Ma 220,188,156
Ma 216,184,156
Mv 220,180,156
Mv 216,176,156
Mv 156,172,156
Mv 152,168,156
Mv 92,164,156
Mv 88,160,156
s_waitcnt vmcnt(22) lgkmcnt(0)
s_barrier
Mv 32,32,0
Rt 128,18,16384
Mv 36,36,0
Rt 224,28,0
)" R"(
Mv 96,40,0
Rt 130,18,18432
Mv 100,44,0
s_add_u32 s39, s39, 0x40000
Rt 226,28,2048
Mv 160,48,0
Rt 132,19,16384
s_add_u32 m0, s37, 0
Mv 164,52,0
buffer_load_dwordx4 v12, s[8:11], s39 offen offset:0 lds
Mv 224,56,0
Rt 134,19,18432
Mv 228,60,0
Rt 228,28,128
Mv 236,60,4
Rt 136,20,16384
Mv 232,56,4
Rt 230,28,2176
Mv 172,52,4
Rt 138,20,18432
Mv 168,48,4
Rt 232,28,256
Mv 108,44,4
Rt 140,21,16384
Mv 104,40,4
buffer_load_dwordx4 v13, s[8:11], s39 offen offset:1024 lds
Mv 44,36,4
Rt 142,21,18432
Mv 40,32,4
Rt 234,28,2304
Mv 48,32,8
Rt 144,22,16384
Mv 52,36,8
Rt 236,28,384
Mv 112,40,8
Rt 146,22,18432
Mv 116,44,8
Rt 238,28,2432
Mv 176,48,8
Rt 148,23,16384
Mv 180,52,8
buffer_load_dwordx4 v14, s[8:11], s39 offen offset:2048 lds
Mv 240,56,8
Rt 150,23,18432
Mv 244,60,8
Mv 252,60,12
Rt 152,24,16384
Mv 248,56,12
Mv 188,52,12
Rt 154,24,18432
Mv 184,48,12
Mv 124,44,12
Rt 156,25,16384
Mv 120,40,12
buffer_load_dwordx4 v15, s[8:11], s39 offen offset:3072 lds
Mv 60,36,12
Rt 158,25,18432
Mv 56,32,12
s_waitcnt lgkmcnt(6)
Mv 64,32,16
Rb 160,17,0
Mv 68,36,16
Rt 240,29,0
Mv 128,40,16
)" R"(
Rb 164,17,2048
Mv 132,44,16
s_add_u32 s29, s86, 0
Rt 242,29,8192
Mv 192,48,16
Rb 168,17,8192
Mv 196,52,16
buffer_store_dwordx4 a[224:227], v1, s[20:23], s29 offen sc0 sc1
Ma 192,56,16
Rb 172,17,10240
Ma 196,60,16
Rt 244,29,128
Ma 204,60,20
Rb 176,17,16384
Ma 200,56,20
Rt 246,29,8320
Mv 204,52,20
Rb 180,17,18432
Mv 200,48,20
s_add_u32 s29, s86, 114688
Rt 248,29,256
Mv 140,44,20
Rb 184,17,24576
Mv 136,40,20
buffer_store_dwordx4 a[228:231], v1, s[20:23], s29 offen sc0 sc1
Mv 76,36,20
Rb 188,17,26624
Mv 72,32,20
Rt 250,29,8448
Mv 80,32,24
Mv 84,36,24
Rt 252,29,384
Mv 144,40,24
Mv 148,44,24
s_add_u32 s29, s86, 229376
Rt 254,29,8576
Mv 208,48,24
Mv 212,52,24
buffer_store_dwordx4 a[232:235], v1, s[20:23], s29 offen sc0 sc1
Ma 208,56,24
Ma 212,60,24
Ma 220,60,28
Ma 216,56,28
Mv 220,52,28
Mv 216,48,28
s_add_u32 s29, s86, 344064
Mv 156,44,28
s_cmp_gt_u32 s33, 1
s_cmov_b32 s29, 0x40000000
Mv 152,40,28
buffer_store_dwordx4 a[236:239], v1, s[20:23], s29 offen sc0 sc1
Mv 92,36,28
Mv 88,32,28
s_waitcnt vmcnt(12) lgkmcnt(3)
s_barrier
Mv 32,160,128
Rt 0,18,32768
Mv 36,164,128
s_add_u32 s38, s38, 0xffff0080
Mv 96,168,128
Rt 2,18,34816
s_add_u32 m0, s36, 0
Mv 100,172,128
buffer_load_dwordx4 v8, s[4:7], s38 offen offset:0 lds
Mv 160,176,128
)" R"(
Rt 4,19,32768
Mv 164,180,128
Mv 224,184,128
Rt 6,19,34816
Mv 228,188,128
buffer_load_dwordx4 v9, s[4:7], s38 offen offset:1024 lds
Mv 236,188,132
Rt 8,20,32768
Mv 232,184,132
Mv 172,180,132
Rt 10,20,34816
Mv 168,176,132
buffer_load_dwordx4 v10, s[4:7], s38 offen offset:2048 lds
Mv 108,172,132
Rt 12,21,32768
Mv 104,168,132
Mv 44,164,132
Rt 14,21,34816
Mv 40,160,132
buffer_load_dwordx4 v11, s[4:7], s38 offen offset:3072 lds
Mv 48,160,136
Rt 16,22,32768
Mv 52,164,136
s_add_u32 s38, s38, 0x10000
Mv 112,168,136
Rt 18,22,34816
s_add_u32 m0, s36, 4096
Mv 116,172,136
buffer_load_dwordx4 v8, s[4:7], s38 offen offset:0 lds
Mv 176,176,136
Rt 20,23,32768
Mv 180,180,136
Mv 240,184,136
Rt 22,23,34816
Mv 244,188,136
buffer_load_dwordx4 v9, s[4:7], s38 offen offset:1024 lds
Mv 252,188,140
Rt 24,24,32768
Mv 248,184,140
Mv 188,180,140
Rt 26,24,34816
Mv 184,176,140
buffer_load_dwordx4 v10, s[4:7], s38 offen offset:2048 lds
Mv 124,172,140
Rt 28,25,32768
Mv 120,168,140
Mv 60,164,140
Rt 30,25,34816
Mv 56,160,140
buffer_load_dwordx4 v11, s[4:7], s38 offen offset:3072 lds
Mv 64,160,144
Rb 32,16,32768
Mv 68,164,144
Mv 128,168,144
Rb 36,16,34816
Mv 132,172,144
s_add_u32 s39, s39, 0x40000
Mv 192,176,144
Rb 40,16,40960
s_add_u32 m0, s37, 16384
Mv 196,180,144
buffer_load_dwordx4 v12, s[8:11], s39 offen offset:0 lds
Ma 192,184,144
Rb 44,16,43008
)" R"(
Ma 196,188,144
Ma 204,188,148
Rb 48,16,49152
Ma 200,184,148
Mv 204,180,148
Rb 52,16,51200
Mv 200,176,148
buffer_load_dwordx4 v13, s[8:11], s39 offen offset:1024 lds
Mv 140,172,148
Rb 56,16,57344
Mv 136,168,148
Mv 76,164,148
Rb 60,16,59392
Mv 72,160,148
Mv 80,160,152
Mv 84,164,152
buffer_load_dwordx4 v14, s[8:11], s39 offen offset:2048 lds
Mv 144,168,152
Mv 148,172,152
Mv 208,176,152
Mv 212,180,152
Ma 208,184,152
Ma 212,188,152
buffer_load_dwordx4 v15, s[8:11], s39 offen offset:3072 lds
Ma 220,188,156
Ma 216,184,156
Mv 220,180,156
Mv 216,176,156
Mv 156,172,156
Mv 152,168,156
Mv 92,164,156
Mv 88,160,156
s_waitcnt vmcnt(20) lgkmcnt(0)
s_barrier
Mv 32,32,0
Rt 128,18,49152
Mv 36,36,0
Mv 96,40,0
Rt 130,18,51200
Mv 100,44,0
s_add_u32 s39, s39, 0x40000
Mv 160,48,0
Rt 132,19,49152
s_add_u32 m0, s37, 32768
Mv 164,52,0
buffer_load_dwordx4 v12, s[8:11], s39 offen offset:0 lds
Mv 224,56,0
Rt 134,19,51200
Mv 228,60,0
Mv 236,60,4
Rt 136,20,49152
Mv 232,56,4
Mv 172,52,4
Rt 138,20,51200
Mv 168,48,4
Mv 108,44,4
Rt 140,21,49152
Mv 104,40,4
buffer_load_dwordx4 v13, s[8:11], s39 offen offset:1024 lds
Mv 44,36,4
Rt 142,21,51200
Mv 40,32,4
Mv 48,32,8
Rt 144,22,49152
Mv 52,36,8
)" R"(
Mv 112,40,8
Rt 146,22,51200
Mv 116,44,8
Mv 176,48,8
Rt 148,23,49152
s_mov_b32 s24, s78
Mv 180,52,8
buffer_load_dwordx4 v14, s[8:11], s39 offen offset:2048 lds
Mv 240,56,8
Rt 150,23,51200
s_mov_b32 s25, s79
Mv 244,60,8
s_mov_b32 s26, 0x188000
s_mov_b32 s27, 0x20000
Mv 252,60,12
Rt 152,24,49152
Mv 248,56,12
Mv 188,52,12
Rt 154,24,51200
Mv 184,48,12
Mv 124,44,12
Rt 156,25,49152
Mv 120,40,12
buffer_load_dwordx4 v15, s[8:11], s39 offen offset:3072 lds
Mv 60,36,12
Rt 158,25,51200
s_add_u32 m0, s37, 0x14000
Mv 56,32,12
s_add_u32 s92, s90, 0
Mv 64,32,16
Rb 160,17,32768
Mv 68,36,16
buffer_load_dwordx4 v27, s[24:27], s92 offen offset:0 lds
Mv 128,40,16
Rb 164,17,34816
s_add_u32 s92, s90, 6144
Mv 132,44,16
Mv 192,48,16
Rb 168,17,40960
Mv 196,52,16
buffer_load_dwordx4 v27, s[24:27], s92 offen offset:1024 lds
Ma 192,56,16
Rb 172,17,43008
s_add_u32 s92, s90, 12288
Ma 196,60,16
Ma 204,60,20
Rb 176,17,49152
Ma 200,56,20
buffer_load_dwordx4 v27, s[24:27], s92 offen offset:2048 lds
Mv 204,52,20
Rb 180,17,51200
s_add_u32 s92, s90, 18432
Mv 200,48,20
Mv 140,44,20
Rb 184,17,57344
Mv 136,40,20
buffer_load_dwordx4 v27, s[24:27], s92 offen offset:3072 lds
Mv 76,36,20
Rb 188,17,59392
s_mov_b32 s24, s80
Mv 72,32,20
s_mov_b32 s25, s81
s_mov_b32 s26, 0x380000
Mv 80,32,24
)" R"(
s_mov_b32 s27, 0x20000
s_movk_i32 s92, -448
Mv 84,36,24
s_bitcmp1_b32 s34, 0
s_cmovk_i32 s92, 448
Mv 144,40,24
s_add_u32 s83, s83, s92
s_add_u32 m0, s37, 0x10000
Mv 148,44,24
s_add_u32 s92, s83, 0
Mv 208,48,24
buffer_load_dwordx4 v26, s[24:27], s92 offen offset:0 lds
Mv 212,52,24
s_add_u32 s92, s83, 768
Ma 208,56,24
buffer_load_dwordx4 v26, s[24:27], s92 offen offset:1024 lds
Ma 212,60,24
s_add_u32 s92, s83, 1536
Ma 220,60,28
Ma 216,56,28
buffer_load_dwordx4 v26, s[24:27], s92 offen offset:2048 lds
Mv 220,52,28
s_add_u32 s92, s83, 2304
Mv 216,48,28
Mv 156,44,28
Mv 152,40,28
buffer_load_dwordx4 v26, s[24:27], s92 offen offset:3072 lds
Mv 92,36,28
Mv 88,32,28
s_waitcnt vmcnt(16) lgkmcnt(0)
s_barrier
Mv 32,160,128
Rt 0,18,0
Mv 36,164,128
s_add_u32 s38, s38, 0xffff0080
Mv 96,168,128
Rt 2,18,2048
s_add_u32 m0, s36, 32768
Mv 100,172,128
buffer_load_dwordx4 v8, s[4:7], s38 offen offset:0 lds
Mv 160,176,128
Rt 4,19,0
Mv 164,180,128
Mv 224,184,128
Rt 6,19,2048
Mv 228,188,128
buffer_load_dwordx4 v9, s[4:7], s38 offen offset:1024 lds
Mv 236,188,132
Rt 8,20,0
Mv 232,184,132
Mv 172,180,132
Rt 10,20,2048
Mv 168,176,132
buffer_load_dwordx4 v10, s[4:7], s38 offen offset:2048 lds
Mv 108,172,132
Rt 12,21,0
Mv 104,168,132
Mv 44,164,132
Rt 14,21,2048
Mv 40,160,132
buffer_load_dwordx4 v11, s[4:7], s38 offen offset:3072 lds
Mv 48,160,136
Rt 16,22,0
Mv 52,164,136
s_add_u32 s38, s38, 0x10000
)" R"(
Mv 112,168,136
Rt 18,22,2048
s_add_u32 m0, s36, 36864
Mv 116,172,136
buffer_load_dwordx4 v8, s[4:7], s38 offen offset:0 lds
Mv 176,176,136
Rt 20,23,0
Mv 180,180,136
Mv 240,184,136
Rt 22,23,2048
Mv 244,188,136
buffer_load_dwordx4 v9, s[4:7], s38 offen offset:1024 lds
Mv 252,188,140
Rt 24,24,0
Mv 248,184,140
Mv 188,180,140
Rt 26,24,2048
Mv 184,176,140
buffer_load_dwordx4 v10, s[4:7], s38 offen offset:2048 lds
Mv 124,172,140
Rt 28,25,0
Mv 120,168,140
Mv 60,164,140
Rt 30,25,2048
Mv 56,160,140
buffer_load_dwordx4 v11, s[4:7], s38 offen offset:3072 lds
Mv 64,160,144
Rb 32,16,0
Mv 68,164,144
Mv 128,168,144
Rb 36,16,2048
Mv 132,172,144
s_add_u32 s39, s39, 0x40000
Mv 192,176,144
Rb 40,16,8192
s_add_u32 m0, s37, 49152
Mv 196,180,144
buffer_load_dwordx4 v12, s[8:11], s39 offen offset:0 lds
Ma 192,184,144
Rb 44,16,10240
Ma 196,188,144
Ma 204,188,148
Rb 48,16,16384
Ma 200,184,148
Mv 204,180,148
Rb 52,16,18432
Mv 200,176,148
buffer_load_dwordx4 v13, s[8:11], s39 offen offset:1024 lds
Mv 140,172,148
Rb 56,16,24576
Mv 136,168,148
Mv 76,164,148
Rb 60,16,26624
Mv 72,160,148
Mv 80,160,152
Mv 84,164,152
buffer_load_dwordx4 v14, s[8:11], s39 offen offset:2048 lds
Mv 144,168,152
Mv 148,172,152
Mv 208,176,152
Mv 212,180,152
Ma 208,184,152
Ma 212,188,152
buffer_load_dwordx4 v15, s[8:11], s39 offen offset:3072 lds
)" R"(
Ma 220,188,156
Ma 216,184,156
Mv 220,180,156
Mv 216,176,156
Mv 156,172,156
Mv 152,168,156
Mv 92,164,156
Mv 88,160,156
s_waitcnt vmcnt(24) lgkmcnt(0)
s_barrier
Mv 32,32,0
Rt 128,18,16384
Mv 36,36,0
Mv 96,40,0
Rt 130,18,18432
Mv 100,44,0
s_add_u32 s39, s39, 0x40000
Mv 160,48,0
Rt 132,19,16384
s_add_u32 m0, s37, 0
Mv 164,52,0
buffer_load_dwordx4 v12, s[8:11], s39 offen offset:0 lds
Mv 224,56,0
Rt 134,19,18432
Mv 228,60,0
s_add_u32 s28, s33, 0
s_lshl3_add_u32 s28, s35, s28
Mv 236,60,4
Rt 136,20,16384
s_mul_i32 s29, s28, 293
Mv 232,56,4
s_lshr_b32 s29, s29, 11
s_mul_i32 s29, s29, 9
Mv 172,52,4
Rt 138,20,18432
s_add_u32 s29, s29, s28
Mv 168,48,4
s_lshl_b32 s29, s29, 10
s_add_u32 s29, s29, s82
Mv 108,44,4
Rt 140,21,16384
s_add_u32 s29, s29, s84
Mv 104,40,4
buffer_load_dwordx4 v13, s[8:11], s39 offen offset:1024 lds
Mv 44,36,4
Rt 142,21,18432
Mv 40,32,4
Mv 48,32,8
Rt 144,22,16384
Mv 52,36,8
buffer_store_dwordx4 a[240:243], v1, s[20:23], s29 offen sc0 sc1
Mv 112,40,8
Rt 146,22,18432
Mv 116,44,8
Mv 176,48,8
Rt 148,23,16384
Mv 180,52,8
buffer_load_dwordx4 v14, s[8:11], s39 offen offset:2048 lds
Mv 240,56,8
Rt 150,23,18432
s_add_u32 s28, s33, 4
Mv 244,60,8
s_lshl3_add_u32 s28, s35, s28
s_mul_i32 s29, s28, 293
Mv 252,60,12
)" R"(
Rt 152,24,16384
s_lshr_b32 s29, s29, 11
Mv 248,56,12
s_mul_i32 s29, s29, 9
s_add_u32 s29, s29, s28
Mv 188,52,12
Rt 154,24,18432
s_lshl_b32 s29, s29, 10
Mv 184,48,12
s_add_u32 s29, s29, s82
s_add_u32 s29, s29, s84
Mv 124,44,12
Rt 156,25,16384
Mv 120,40,12
buffer_load_dwordx4 v15, s[8:11], s39 offen offset:3072 lds
Mv 60,36,12
Rt 158,25,18432
Mv 56,32,12
buffer_store_dwordx4 a[244:247], v1, s[20:23], s29 offen sc0 sc1
Mv 64,32,16
Rb 160,17,0
Mv 68,36,16
Mv 128,40,16
Rb 164,17,2048
Mv 132,44,16
s_add_u32 s28, s33, 8
s_lshl3_add_u32 s28, s28, s35
Mv 192,48,16
Rb 168,17,8192
s_mul_i32 s29, s28, 293
Mv 196,52,16
s_lshr_b32 s29, s29, 11
s_mul_i32 s29, s29, 9
Ma 192,56,16
Rb 172,17,10240
s_add_u32 s29, s29, s28
Ma 196,60,16
s_lshl_b32 s29, s29, 10
s_add_u32 s29, s29, s82
Ma 204,60,20
Rb 176,17,16384
s_add_u32 s29, s29, s84
Ma 200,56,20
Mv 204,52,20
Rb 180,17,18432
Mv 200,48,20
Mv 140,44,20
Rb 184,17,24576
Mv 136,40,20
buffer_store_dwordx4 a[248:251], v1, s[20:23], s29 offen sc0 sc1
Mv 76,36,20
Rb 188,17,26624
Mv 72,32,20
Mv 80,32,24
Mv 84,36,24
s_add_u32 s28, s33, 12
s_lshl3_add_u32 s28, s28, s35
Mv 144,40,24
s_mul_i32 s29, s28, 293
s_lshr_b32 s29, s29, 11
Mv 148,44,24
s_mul_i32 s29, s29, 9
s_add_u32 s29, s29, s28
Mv 208,48,24
)" R"(
s_lshl_b32 s29, s29, 10
s_add_u32 s29, s29, s82
Mv 212,52,24
s_add_u32 s29, s29, s84
Ma 208,56,24
s_cmp_gt_u32 s33, 1
s_cmov_b32 s29, 0x40000000
Ma 212,60,24
Ma 220,60,28
Ma 216,56,28
Mv 220,52,28
Mv 216,48,28
buffer_store_dwordx4 a[252:255], v1, s[20:23], s29 offen sc0 sc1
Mv 156,44,28
Mv 152,40,28
Mv 92,36,28
Mv 88,32,28
s_waitcnt vmcnt(12) lgkmcnt(0)
s_barrier
Mv 32,160,128
Rt 0,18,32768
Mv 36,164,128
s_add_u32 s38, s38, 0xffff0080
Rt 224,28,0
Mv 96,168,128
Rt 2,18,34816
s_add_u32 m0, s36, 0
Mv 100,172,128
buffer_load_dwordx4 v8, s[4:7], s38 offen offset:0 lds
Rt 226,28,2048
Mv 160,176,128
Rt 4,19,32768
Mv 164,180,128
Mv 224,184,128
Rt 6,19,34816
Mv 228,188,128
buffer_load_dwordx4 v9, s[4:7], s38 offen offset:1024 lds
Rt 228,28,128
Mv 236,188,132
Rt 8,20,32768
Mv 232,184,132
Rt 230,28,2176
Mv 172,180,132
Rt 10,20,34816
Mv 168,176,132
buffer_load_dwordx4 v10, s[4:7], s38 offen offset:2048 lds
Rt 232,28,256
Mv 108,172,132
Rt 12,21,32768
Mv 104,168,132
Mv 44,164,132
Rt 14,21,34816
Mv 40,160,132
buffer_load_dwordx4 v11, s[4:7], s38 offen offset:3072 lds
Rt 234,28,2304
Mv 48,160,136
Rt 16,22,32768
Mv 52,164,136
s_add_u32 s38, s38, 0x10000
Rt 236,28,384
Mv 112,168,136
Rt 18,22,34816
s_add_u32 m0, s36, 4096
Mv 116,172,136
buffer_load_dwordx4 v8, s[4:7], s38 offen offset:0 lds
)" R"(
Rt 238,28,2432
Mv 176,176,136
Rt 20,23,32768
Mv 180,180,136
Mv 240,184,136
Rt 22,23,34816
Mv 244,188,136
buffer_load_dwordx4 v9, s[4:7], s38 offen offset:1024 lds
Mv 252,188,140
Rt 24,24,32768
Mv 248,184,140
Mv 188,180,140
Rt 26,24,34816
Mv 184,176,140
buffer_load_dwordx4 v10, s[4:7], s38 offen offset:2048 lds
Mv 124,172,140
Rt 28,25,32768
Mv 120,168,140
Mv 60,164,140
Rt 30,25,34816
Mv 56,160,140
buffer_load_dwordx4 v11, s[4:7], s38 offen offset:3072 lds
Mv 64,160,144
Rb 32,16,32768
Mv 68,164,144
Mv 128,168,144
Rb 36,16,34816
Mv 132,172,144
s_add_u32 s39, s39, 0x40000
Rt 240,29,0
Mv 192,176,144
Rb 40,16,40960
s_add_u32 m0, s37, 16384
Mv 196,180,144
buffer_load_dwordx4 v12, s[8:11], s39 offen offset:0 lds
Rt 242,29,8192
Ma 192,184,144
Rb 44,16,43008
Ma 196,188,144
Ma 204,188,148
Rb 48,16,49152
Ma 200,184,148
Rt 244,29,128
Mv 204,180,148
Rb 52,16,51200
Mv 200,176,148
buffer_load_dwordx4 v13, s[8:11], s39 offen offset:1024 lds
Rt 246,29,8320
Mv 140,172,148
Rb 56,16,57344
Mv 136,168,148
Rt 248,29,256
Mv 76,164,148
Rb 60,16,59392
Mv 72,160,148
Mv 80,160,152
Mv 84,164,152
buffer_load_dwordx4 v14, s[8:11], s39 offen offset:2048 lds
Rt 250,29,8448
Mv 144,168,152
Mv 148,172,152
Rt 252,29,384
Mv 208,176,152
Mv 212,180,152
)" R"(
Rt 254,29,8576
Ma 208,184,152
Ma 212,188,152
buffer_load_dwordx4 v15, s[8:11], s39 offen offset:3072 lds
Ma 220,188,156
Ma 216,184,156
Mv 220,180,156
Mv 216,176,156
Mv 156,172,156
Mv 152,168,156
Mv 92,164,156
Mv 88,160,156
s_waitcnt vmcnt(20) lgkmcnt(2)
s_barrier
Mv 32,32,0
Rt 128,18,49152
s_add_u32 s29, s86, 14336
Mv 36,36,0
buffer_store_dwordx4 a[224:227], v1, s[20:23], s29 offen sc0 sc1
Mv 96,40,0
Rt 130,18,51200
Mv 100,44,0
s_add_u32 s39, s39, 0x40000
Mv 160,48,0
Rt 132,19,49152
s_add_u32 m0, s37, 32768
Mv 164,52,0
buffer_load_dwordx4 v12, s[8:11], s39 offen offset:0 lds
Mv 224,56,0
Rt 134,19,51200
Mv 228,60,0
s_add_u32 s29, s86, 129024
Mv 236,60,4
Rt 136,20,49152
Mv 232,56,4
buffer_store_dwordx4 a[228:231], v1, s[20:23], s29 offen sc0 sc1
Mv 172,52,4
Rt 138,20,51200
Mv 168,48,4
Mv 108,44,4
Rt 140,21,49152
Mv 104,40,4
buffer_load_dwordx4 v13, s[8:11], s39 offen offset:1024 lds
Mv 44,36,4
Rt 142,21,51200
Mv 40,32,4
s_add_u32 s29, s86, 243712
Mv 48,32,8
Rt 144,22,49152
Mv 52,36,8
buffer_store_dwordx4 a[232:235], v1, s[20:23], s29 offen sc0 sc1
Mv 112,40,8
Rt 146,22,51200
Mv 116,44,8
Mv 176,48,8
Rt 148,23,49152
Mv 180,52,8
buffer_load_dwordx4 v14, s[8:11], s39 offen offset:2048 lds
Mv 240,56,8
Rt 150,23,51200
Mv 244,60,8
s_add_u32 s29, s86, 358400
Mv 252,60,12
Rt 152,24,49152
Mv 248,56,12
)" R"(
s_cmp_gt_u32 s33, 1
s_cmov_b32 s29, 0x40000000
Mv 188,52,12
Rt 154,24,51200
Mv 184,48,12
buffer_store_dwordx4 a[236:239], v1, s[20:23], s29 offen sc0 sc1
Mv 124,44,12
Rt 156,25,49152
Mv 120,40,12
buffer_load_dwordx4 v15, s[8:11], s39 offen offset:3072 lds
Mv 60,36,12
Rt 158,25,51200
s_add_u32 s28, s33, 14
Mv 56,32,12
s_lshl3_add_u32 s28, s28, s35
s_mul_i32 s29, s28, 293
Mv 64,32,16
Rb 160,17,32768
s_lshr_b32 s29, s29, 11
Mv 68,36,16
s_mul_i32 s29, s29, 9
s_add_u32 s29, s29, s28
Mv 128,40,16
Rb 164,17,34816
s_lshl_b32 s29, s29, 10
Mv 132,44,16
s_add_u32 s29, s29, s82
s_add_u32 s29, s29, s84
Mv 192,48,16
Rb 168,17,40960
Mv 196,52,16
Ma 192,56,16
Rb 172,17,43008
Ma 196,60,16
buffer_store_dwordx4 a[240:243], v1, s[20:23], s29 offen sc0 sc1
Ma 204,60,20
Rb 176,17,49152
Ma 200,56,20
s_add_u32 s28, s33, 18
s_lshl3_add_u32 s28, s28, s35
Mv 204,52,20
Rb 180,17,51200
s_mul_i32 s29, s28, 293
Mv 200,48,20
s_lshr_b32 s29, s29, 11
s_mul_i32 s29, s29, 9
Mv 140,44,20
Rb 184,17,57344
s_add_u32 s29, s29, s28
Mv 136,40,20
s_lshl_b32 s29, s29, 10
s_add_u32 s29, s29, s82
Mv 76,36,20
Rb 188,17,59392
s_add_u32 s29, s29, s84
Mv 72,32,20
Mv 80,32,24
Mv 84,36,24
Mv 144,40,24
Mv 148,44,24
buffer_store_dwordx4 a[244:247], v1, s[20:23], s29 offen sc0 sc1
Mv 208,48,24
s_add_u32 s28, s33, 22
s_lshl3_add_u32 s28, s28, s35
)" R"(
Mv 212,52,24
s_mul_i32 s29, s28, 293
s_lshr_b32 s29, s29, 11
Ma 208,56,24
s_mul_i32 s29, s29, 9
s_add_u32 s29, s29, s28
Ma 212,60,24
s_lshl_b32 s29, s29, 10
s_add_u32 s29, s29, s82
Ma 220,60,28
s_add_u32 s29, s29, s84
Ma 216,56,28
Mv 220,52,28
Mv 216,48,28
Mv 156,44,28
Mv 152,40,28
Mv 92,36,28
buffer_store_dwordx4 a[248:251], v1, s[20:23], s29 offen sc0 sc1
Mv 88,32,28
s_waitcnt vmcnt(15) lgkmcnt(0)
s_barrier
Mv 32,160,128
Rt 0,18,0
Mv 36,164,128
s_add_u32 s38, s38, 0xffff0080
Mv 96,168,128
Rt 2,18,2048
s_add_u32 m0, s36, 32768
Mv 100,172,128
buffer_load_dwordx4 v8, s[4:7], s38 offen offset:0 lds
Mv 160,176,128
Rt 4,19,0
Mv 164,180,128
Mv 224,184,128
Rt 6,19,2048
Mv 228,188,128
buffer_load_dwordx4 v9, s[4:7], s38 offen offset:1024 lds
Mv 236,188,132
Rt 8,20,0
Mv 232,184,132
Mv 172,180,132
Rt 10,20,2048
Mv 168,176,132
buffer_load_dwordx4 v10, s[4:7], s38 offen offset:2048 lds
Mv 108,172,132
Rt 12,21,0
Mv 104,168,132
Mv 44,164,132
Rt 14,21,2048
Mv 40,160,132
buffer_load_dwordx4 v11, s[4:7], s38 offen offset:3072 lds
Mv 48,160,136
Rt 16,22,0
Mv 52,164,136
s_add_u32 s38, s38, 0x10000
Mv 112,168,136
Rt 18,22,2048
s_add_u32 m0, s36, 36864
Mv 116,172,136
buffer_load_dwordx4 v8, s[4:7], s38 offen offset:0 lds
Mv 176,176,136
Rt 20,23,0
Mv 180,180,136
Mv 240,184,136
Rt 22,23,2048
)" R"(
Mv 244,188,136
buffer_load_dwordx4 v9, s[4:7], s38 offen offset:1024 lds
Mv 252,188,140
Rt 24,24,0
Mv 248,184,140
s_add_u32 s28, s33, 26
s_lshl3_add_u32 s28, s28, s35
s_mul_i32 s29, s28, 293
Mv 188,180,140
Rt 26,24,2048
s_lshr_b32 s29, s29, 11
s_mul_i32 s29, s29, 9
Mv 184,176,140
buffer_load_dwordx4 v10, s[4:7], s38 offen offset:2048 lds
Mv 124,172,140
Rt 28,25,0
s_add_u32 s29, s29, s28
s_lshl_b32 s29, s29, 10
Mv 120,168,140
s_add_u32 s29, s29, s82
s_add_u32 s29, s29, s84
Mv 60,164,140
Rt 30,25,2048
s_cmp_gt_u32 s33, 1
s_cmov_b32 s29, 0x40000000
Mv 56,160,140
buffer_load_dwordx4 v11, s[4:7], s38 offen offset:3072 lds
Mv 64,160,144
Rb 32,16,0
Mv 68,164,144
Mv 128,168,144
Rb 36,16,2048
Mv 132,172,144
s_add_u32 s39, s39, 0x40000
buffer_store_dwordx4 a[252:255], v1, s[20:23], s29 offen sc0 sc1
Mv 192,176,144
Rb 40,16,8192
s_add_u32 m0, s37, 49152
Mv 196,180,144
buffer_load_dwordx4 v12, s[8:11], s39 offen offset:0 lds
Ma 192,184,144
Rb 44,16,10240
Ma 196,188,144
Ma 204,188,148
Rb 48,16,16384
Ma 200,184,148
Mv 204,180,148
Rb 52,16,18432
Mv 200,176,148
buffer_load_dwordx4 v13, s[8:11], s39 offen offset:1024 lds
Mv 140,172,148
Rb 56,16,24576
Mv 136,168,148
Mv 76,164,148
Rb 60,16,26624
Mv 72,160,148
Mv 80,160,152
Mv 84,164,152
buffer_load_dwordx4 v14, s[8:11], s39 offen offset:2048 lds
Mv 144,168,152
Mv 148,172,152
Mv 208,176,152
Mv 212,180,152
Ma 208,184,152
)" R"(
Ma 212,188,152
buffer_load_dwordx4 v15, s[8:11], s39 offen offset:3072 lds
Ma 220,188,156
Ma 216,184,156
Mv 220,180,156
Mv 216,176,156
Mv 156,172,156
Mv 152,168,156
s_mov_b32 s40, 1
s_mov_b32 s94, 1
Mv 92,164,156
Mv 88,160,156
s_waitcnt vmcnt(17) lgkmcnt(0)
s_barrier
buffer_store_dword v6, v7, s[20:23], s87 offen offset:0 sc0 sc1
s_branch .LzPBV
.LzWIN:
.LzPAKX:
Mv 32,0,32
Rt 64,18,16384
Mv 64,0,36
Rb 192,28,32768
Mv 96,0,40
Rt 66,18,18432
Mv 128,0,44
s_add_u32 s39, s39, 0x40000
Rb 196,28,34816
Mv 160,0,48
Rt 68,19,16384
s_add_u32 m0, s37, 0
Mv 192,0,52
buffer_load_dwordx4 v12, s[8:11], s39 offen offset:0 lds
Mv 224,0,56
Rt 70,19,18432
Ma 224,0,60
Rb 200,29,32768
Ma 228,4,60
Rt 72,20,16384
Mv 228,4,56
Rb 204,29,34816
Mv 196,4,52
Rt 74,20,18432
Mv 164,4,48
Rb 208,28,36864
Mv 132,4,44
Rt 76,21,16384
Mv 100,4,40
buffer_load_dwordx4 v13, s[8:11], s39 offen offset:1024 lds
Mv 68,4,36
Rt 78,21,18432
Mv 36,4,32
Rb 212,28,38912
Mv 40,8,32
Rt 80,22,16384
Mv 72,8,36
Rb 216,29,36864
Mv 104,8,40
Rt 82,22,18432
Mv 136,8,44
Rb 220,29,38912
Mv 168,8,48
Rt 84,23,16384
Mv 200,8,52
buffer_load_dwordx4 v14, s[8:11], s39 offen offset:2048 lds
Mv 232,8,56
)" R"(
Rt 86,23,18432
Ma 232,8,60
Ma 236,12,60
Rt 88,24,16384
Mv 236,12,56
Mv 204,12,52
Rt 90,24,18432
Mv 172,12,48
Mv 140,12,44
Rt 92,25,16384
Mv 108,12,40
buffer_load_dwordx4 v15, s[8:11], s39 offen offset:3072 lds
Mv 76,12,36
Rt 94,25,18432
Mv 44,12,32
s_add_u32 s44, s43, 256
s_waitcnt lgkmcnt(6)
Mv 48,16,32
Rb 96,17,0
s_add_u32 m0, s36, 131072
Mv 80,16,36
buffer_load_dwordx4 v26, s[12:15], s44 offen offset:0 lds
Mv 112,16,40
Rb 100,17,2048
Mv 144,16,44
Mv 176,16,48
Rb 104,17,8192
Mv 208,16,52
buffer_load_dwordx4 v27, s[12:15], s44 offen offset:1024 lds
Mv 240,16,56
Rb 108,17,10240
Ma 240,16,60
Ma 244,20,60
Rb 112,17,16384
Mv 244,20,56
s_add_u32 s44, s43, 12544
buffer_load_dwordx4 v26, s[12:15], s44 offen offset:2048 lds
Mv 212,20,52
Rb 116,17,18432
Mv 180,20,48
Mv 148,20,44
Rb 120,17,24576
Mv 116,20,40
buffer_load_dwordx4 v27, s[12:15], s44 offen offset:3072 lds
Mv 84,20,36
Rb 124,17,26624
Mv 52,20,32
Mv 56,24,32
s_add_u32 m0, s36, 135168
s_add_u32 s44, s43, 384
Mv 88,24,36
buffer_load_dwordx4 v26, s[12:15], s44 offen offset:0 lds
Mv 120,24,40
Mv 152,24,44
Mv 184,24,48
Mv 216,24,52
buffer_load_dwordx4 v27, s[12:15], s44 offen offset:1024 lds
Mv 248,24,56
Ma 248,24,60
Ma 252,28,60
Mv 252,28,56
s_add_u32 s44, s43, 12672
buffer_load_dwordx4 v26, s[12:15], s44 offen offset:2048 lds
Mv 220,28,52
)" R"(
Mv 188,28,48
Mv 156,28,44
s_mov_b32 s85, 0xc108800
v_and_b32 v2, 7, v0
v_lshlrev_b32 v2, 7, v2
Mv 124,28,40
buffer_load_dwordx4 v27, s[12:15], s44 offen offset:3072 lds
Mv 92,28,36
buffer_load_dword v4, v2, s[20:23], s85 offen sc0 sc1
Mv 60,28,32
s_waitcnt vmcnt(18) lgkmcnt(0)
s_barrier
Mv 32,64,96
Rt 0,18,32768
Mv 64,64,100
s_add_u32 s38, s38, 0xffff0080
Mv 96,64,104
Rt 2,18,34816
s_add_u32 m0, s36, 0
Mv 128,64,108
buffer_load_dwordx4 v8, s[4:7], s38 offen offset:0 lds
Mv 160,64,112
Rt 4,19,32768
Mv 192,64,116
Mv 224,64,120
Rt 6,19,34816
Ma 224,64,124
buffer_load_dwordx4 v9, s[4:7], s38 offen offset:1024 lds
Ma 228,68,124
Rt 8,20,32768
Mv 228,68,120
Mv 196,68,116
Rt 10,20,34816
Mv 164,68,112
buffer_load_dwordx4 v10, s[4:7], s38 offen offset:2048 lds
Mv 132,68,108
Rt 12,21,32768
Mv 100,68,104
Mv 68,68,100
Rt 14,21,34816
Mv 36,68,96
buffer_load_dwordx4 v11, s[4:7], s38 offen offset:3072 lds
Mv 40,72,96
Rt 16,22,32768
Mv 72,72,100
s_add_u32 s38, s38, 0x10000
Mv 104,72,104
Rt 18,22,34816
s_add_u32 m0, s36, 4096
Mv 136,72,108
buffer_load_dwordx4 v8, s[4:7], s38 offen offset:0 lds
Mv 168,72,112
Rt 20,23,32768
Mv 200,72,116
Mv 232,72,120
Rt 22,23,34816
Ma 232,72,124
buffer_load_dwordx4 v9, s[4:7], s38 offen offset:1024 lds
Ma 236,76,124
Rt 24,24,32768
Mv 236,76,120
Mv 204,76,116
Rt 26,24,34816
Mv 172,76,112
buffer_load_dwordx4 v10, s[4:7], s38 offen offset:2048 lds
)" R"(
Mv 140,76,108
Rt 28,25,32768
Mv 108,76,104
Mv 76,76,100
Rt 30,25,34816
Mv 44,76,96
buffer_load_dwordx4 v11, s[4:7], s38 offen offset:3072 lds
Mv 48,80,96
Rb 32,16,32768
Mv 80,80,100
Mv 112,80,104
Rb 36,16,34816
Mv 144,80,108
s_add_u32 s39, s39, 0x40000
Mv 176,80,112
Rb 40,16,40960
s_add_u32 m0, s37, 16384
Mv 208,80,116
buffer_load_dwordx4 v12, s[8:11], s39 offen offset:0 lds
Mv 240,80,120
Rb 44,16,43008
s_mov_b32 s24, s70
Ma 240,80,124
s_mov_b32 s25, s71
s_mov_b32 s26, 0x188000
Ma 244,84,124
Rb 48,16,49152
s_lshl2_add_u32 s88, s2, s33
Mv 244,84,120
s_lshl_b32 s88, s88, 12
Mv 212,84,116
Rb 52,16,51200
Mv 180,84,112
buffer_load_dwordx4 v13, s[8:11], s39 offen offset:1024 lds
Mv 148,84,108
Rb 56,16,57344
Mv 116,84,104
Mv 84,84,100
Rb 60,16,59392
Mv 52,84,96
Mv 56,88,96
Mv 88,88,100
buffer_load_dwordx4 v14, s[8:11], s39 offen offset:2048 lds
Mv 120,88,104
Mv 152,88,108
Mv 184,88,112
Mv 216,88,116
Mv 248,88,120
Ma 248,88,124
buffer_load_dwordx4 v15, s[8:11], s39 offen offset:3072 lds
Ma 252,92,124
Mv 252,92,120
Mv 220,92,116
Mv 188,92,112
buffer_load_dword v6, v5, s[24:27], s88 offen
Mv 156,92,108
Mv 124,92,104
Mv 92,92,100
Mv 60,92,96
s_waitcnt vmcnt(14) lgkmcnt(0)
s_barrier
Mv 32,0,32
Rt 64,18,49152
Mv 64,0,36
Rb 128,28,32768
)" R"(
Mv 96,0,40
Rt 66,18,51200
Mv 128,0,44
s_add_u32 s39, s39, 0x40000
Rb 132,28,34816
Mv 160,0,48
Rt 68,19,49152
s_add_u32 m0, s37, 32768
Mv 192,0,52
buffer_load_dwordx4 v12, s[8:11], s39 offen offset:0 lds
Mv 224,0,56
Rt 70,19,51200
Ma 224,0,60
Rb 136,29,32768
Ma 228,4,60
Rt 72,20,49152
Mv 228,4,56
Rb 140,29,34816
Mv 196,4,52
Rt 74,20,51200
Mv 164,4,48
Rb 144,28,36864
Mv 132,4,44
Rt 76,21,49152
Mv 100,4,40
buffer_load_dwordx4 v13, s[8:11], s39 offen offset:1024 lds
Mv 68,4,36
Rt 78,21,51200
Mv 36,4,32
Rb 148,28,38912
Mv 40,8,32
Rt 80,22,49152
Mv 72,8,36
Rb 152,29,36864
Mv 104,8,40
Rt 82,22,51200
Mv 136,8,44
Rb 156,29,38912
Mv 168,8,48
Rt 84,23,49152
Mv 200,8,52
buffer_load_dwordx4 v14, s[8:11], s39 offen offset:2048 lds
Mv 232,8,56
Rt 86,23,51200
Ma 232,8,60
Ma 236,12,60
Rt 88,24,49152
Mv 236,12,56
Mv 204,12,52
Rt 90,24,51200
Mv 172,12,48
Mv 140,12,44
Rt 92,25,49152
Mv 108,12,40
buffer_load_dwordx4 v15, s[8:11], s39 offen offset:3072 lds
Mv 76,12,36
Rt 94,25,51200
Mv 44,12,32
s_add_u32 s44, s43, 512
s_waitcnt lgkmcnt(6)
Mv 48,16,32
Rb 96,17,32768
s_add_u32 m0, s36, 131072
Mv 80,16,36
)" R"(
buffer_load_dwordx4 v26, s[12:15], s44 offen offset:0 lds
Mv 112,16,40
Rb 100,17,34816
Mv 144,16,44
Mv 176,16,48
Rb 104,17,40960
Mv 208,16,52
buffer_load_dwordx4 v27, s[12:15], s44 offen offset:1024 lds
Mv 240,16,56
Rb 108,17,43008
Ma 240,16,60
Ma 244,20,60
Rb 112,17,49152
Mv 244,20,56
s_add_u32 s44, s43, 12800
buffer_load_dwordx4 v26, s[12:15], s44 offen offset:2048 lds
Mv 212,20,52
Rb 116,17,51200
Mv 180,20,48
Mv 148,20,44
Rb 120,17,57344
Mv 116,20,40
buffer_load_dwordx4 v27, s[12:15], s44 offen offset:3072 lds
Mv 84,20,36
Rb 124,17,59392
Mv 52,20,32
Mv 56,24,32
s_add_u32 m0, s36, 135168
s_add_u32 s44, s43, 640
Mv 88,24,36
buffer_load_dwordx4 v26, s[12:15], s44 offen offset:0 lds
Mv 120,24,40
Mv 152,24,44
Mv 184,24,48
Mv 216,24,52
buffer_load_dwordx4 v27, s[12:15], s44 offen offset:1024 lds
Mv 248,24,56
Ma 248,24,60
Ma 252,28,60
Mv 252,28,56
s_add_u32 s44, s43, 12928
buffer_load_dwordx4 v26, s[12:15], s44 offen offset:2048 lds
Mv 220,28,52
Mv 188,28,48
Mv 156,28,44
Mv 124,28,40
buffer_load_dwordx4 v27, s[12:15], s44 offen offset:3072 lds
Mv 92,28,36
Mv 60,28,32
s_waitcnt vmcnt(17) lgkmcnt(0)
s_barrier
Mv 32,64,96
Rt 0,18,0
v_xor_b32 v4, s3, v4
v_cmp_gt_u32_e32 vcc, 2, v4
s_andn2_b32 s28, 0xff, vcc_lo
s_cbranch_scc1 .Lzsp1
.Lzok1:
Mv 64,64,100
s_add_u32 s38, s38, 0xffff0080
v_xor_b32 v4, s3, v4
Mv 96,64,104
Rt 2,18,2048
s_add_u32 m0, s36, 32768
Mv 128,64,108
)" R"(
buffer_load_dwordx4 v8, s[4:7], s38 offen offset:0 lds
Mv 160,64,112
Rt 4,19,0
v_and_b32 v4, 1, v4
Mv 192,64,116
v_cmp_eq_u32_e32 vcc, 1, v4
s_mov_b32 s85, vcc_lo
Mv 224,64,120
Rt 6,19,2048
s_and_b32 s28, s2, 7
Ma 224,64,124
buffer_load_dwordx4 v9, s[4:7], s38 offen offset:1024 lds
Ma 228,68,124
Rt 8,20,0
s_lshr_b32 s28, s85, s28
Mv 228,68,120
s_xor_b32 s28, s28, s34
s_and_b32 s28, s28, 1
Mv 196,68,116
Rt 10,20,2048
s_lshl_b32 s84, s28, 13
Mv 164,68,112
buffer_load_dwordx4 v10, s[4:7], s38 offen offset:2048 lds
Mv 132,68,108
Rt 12,21,0
s_lshl1_add_u32 s28, s36, s84
Mv 100,68,104
v_add_u32 v2, s28, v1
v_add_u32 v3, 0x1000, v2
Mv 68,68,100
Rt 14,21,2048
s_and_b32 s85, s85, 0xff
Mv 36,68,96
buffer_load_dwordx4 v11, s[4:7], s38 offen offset:3072 lds
Mv 40,72,96
Rt 16,22,0
s_bcnt1_i32_b32 s28, s85
Mv 72,72,100
s_add_u32 s38, s38, 0x10000
Mv 104,72,104
Rt 18,22,2048
s_add_u32 m0, s36, 36864
Mv 136,72,108
buffer_load_dwordx4 v8, s[4:7], s38 offen offset:0 lds
Mv 168,72,112
Rt 20,23,0
Mv 200,72,116
s_cmp_lg_u32 s28, 4
s_cmov_b32 s85, 0xf0
Mv 232,72,120
Rt 22,23,2048
s_and_b32 s28, s2, 7
Ma 232,72,124
buffer_load_dwordx4 v9, s[4:7], s38 offen offset:1024 lds
Ma 236,76,124
Rt 24,24,0
s_lshr_b32 s85, s85, s28
Mv 236,76,120
s_and_b32 s85, s85, 1
Mv 204,76,116
Rt 26,24,2048
Mv 172,76,112
buffer_load_dwordx4 v10, s[4:7], s38 offen offset:2048 lds
Mv 140,76,108
)" R"(
Rt 28,25,0
Mv 108,76,104
Mv 76,76,100
Rt 30,25,2048
Mv 44,76,96
buffer_load_dwordx4 v11, s[4:7], s38 offen offset:3072 lds
Mv 48,80,96
Rb 32,16,0
Mv 80,80,100
Mv 112,80,104
Rb 36,16,2048
Mv 144,80,108
s_add_u32 s39, s39, 0x40000
Mv 176,80,112
Rb 40,16,8192
s_add_u32 m0, s37, 49152
Mv 208,80,116
buffer_load_dwordx4 v12, s[8:11], s39 offen offset:0 lds
Mv 240,80,120
Rb 44,16,10240
Ma 240,80,124
Ma 244,84,124
Rb 48,16,16384
Mv 244,84,120
Mv 212,84,116
Rb 52,16,18432
Mv 180,84,112
buffer_load_dwordx4 v13, s[8:11], s39 offen offset:1024 lds
Mv 148,84,108
Rb 56,16,24576
Mv 116,84,104
Mv 84,84,100
Rb 60,16,26624
Mv 52,84,96
Mv 56,88,96
Mv 88,88,100
buffer_load_dwordx4 v14, s[8:11], s39 offen offset:2048 lds
Mv 120,88,104
Mv 152,88,108
Mv 184,88,112
Mv 216,88,116
Mv 248,88,120
Ma 248,88,124
buffer_load_dwordx4 v15, s[8:11], s39 offen offset:3072 lds
Ma 252,92,124
Mv 252,92,120
Mv 220,92,116
Mv 188,92,112
Mv 156,92,108
Mv 124,92,104
Mv 92,92,100
Mv 60,92,96
s_waitcnt vmcnt(12) lgkmcnt(0)
s_barrier
Mv 32,0,32
Rt 64,18,16384
Mv 64,0,36
Rb 160,28,32768
v_lshrrev_b32 v5, 2, v0
Mv 96,0,40
Rt 66,18,18432
v_and_b32 v5, 8, v5
Mv 128,0,44
Rb 164,28,34816
v_and_or_b32 v5, v0, 7, v5
)" R"(
Mv 160,0,48
Rt 68,19,16384
v_lshlrev_b32 v5, 8, v5
Mv 192,0,52
v_and_or_b32 v5, v0, 24, v5
v_lshrrev_b32 v5, 1, v5
Mv 224,0,56
Rt 70,19,18432
v_mov_b32 v6, s3
Ma 224,0,60
Rb 168,29,32768
Ma 228,4,60
Rt 72,20,16384
Mv 228,4,56
Rb 172,29,34816
Mv 196,4,52
Rt 74,20,18432
Mv 164,4,48
Rb 176,28,36864
Mv 132,4,44
Rt 76,21,16384
Mv 100,4,40
Mv 68,4,36
Rt 78,21,18432
Mv 36,4,32
Rb 180,28,38912
Mv 40,8,32
Rt 80,22,16384
Mv 72,8,36
Rb 184,29,36864
Mv 104,8,40
Rt 82,22,18432
Mv 136,8,44
Rb 188,29,38912
Mv 168,8,48
Rt 84,23,16384
Mv 200,8,52
Mv 232,8,56
Rt 86,23,18432
Ma 232,8,60
Ma 236,12,60
Rt 88,24,16384
Mv 236,12,56
Mv 204,12,52
Rt 90,24,18432
Mv 172,12,48
Mv 140,12,44
Rt 92,25,16384
Mv 108,12,40
Mv 76,12,36
Rt 94,25,18432
v_lshrrev_b32 v10, 3, v0
Mv 44,12,32
v_and_b32 v11, 0x70, v1
v_lshl_or_b32 v10, v10, 13, v11
Mv 48,16,32
Rb 96,17,0
v_lshlrev_b32 v11, 1, v0
Mv 80,16,36
v_and_b32 v11, 32, v11
v_xor_b32 v10, v10, v11
buffer_load_dword v4, v7, s[20:23], s52 offen offset:0 sc0 sc1
Mv 112,16,40
Rb 100,17,2048
)" R"(
s_and_b32 s28, s33, 1
Mv 144,16,44
s_lshl_b32 s28, s28, 6
v_xor_b32 v8, s28, v10
Mv 176,16,48
Rb 104,17,8192
v_xor_b32 v9, 64, v8
Mv 208,16,52
v_add_u32 v9, 0xfc00, v9
v_add_u32 v10, 0x1f800, v8
Mv 240,16,56
Rb 108,17,10240
v_add_u32 v11, 0x1f800, v9
Ma 240,16,60
Ma 244,20,60
Rb 112,17,16384
Mv 244,20,56
Mv 212,20,52
Rb 116,17,18432
Mv 180,20,48
Mv 148,20,44
Rb 120,17,24576
Mv 116,20,40
Mv 84,20,36
Rb 124,17,26624
Mv 52,20,32
Mv 56,24,32
Mv 88,24,36
Mv 120,24,40
Mv 152,24,44
Mv 184,24,48
Mv 216,24,52
Mv 248,24,56
Ma 248,24,60
Ma 252,28,60
Mv 252,28,56
Mv 220,28,52
Mv 188,28,48
Mv 156,28,44
Mv 124,28,40
Mv 92,28,36
Mv 60,28,32
s_waitcnt vmcnt(5) lgkmcnt(0)
s_barrier
Mv 32,64,96
Rt 0,18,32768
Mv 64,64,100
s_add_u32 s92, s42, 0x0
Mv 96,64,104
Rt 2,18,34816
s_add_u32 m0, s41, 131072
Mv 128,64,108
buffer_load_dwordx4 v8, s[16:19], s92 offen offset:0 lds
Mv 160,64,112
Rt 4,19,32768
Mv 192,64,116
Mv 224,64,120
Rt 6,19,34816
Ma 224,64,124
buffer_load_dwordx4 v9, s[16:19], s92 offen offset:1024 lds
Ma 228,68,124
Rt 8,20,32768
Mv 228,68,120
Mv 196,68,116
Rt 10,20,34816
)" R"(
Mv 164,68,112
buffer_load_dwordx4 v10, s[16:19], s92 offen offset:2048 lds
Mv 132,68,108
Rt 12,21,32768
Mv 100,68,104
Mv 68,68,100
Rt 14,21,34816
Mv 36,68,96
buffer_load_dwordx4 v11, s[16:19], s92 offen offset:3072 lds
Mv 40,72,96
Rt 16,22,32768
Mv 72,72,100
s_add_u32 s92, s42, 0x40000
Mv 104,72,104
Rt 18,22,34816
s_add_u32 m0, s41, 135168
Mv 136,72,108
buffer_load_dwordx4 v8, s[16:19], s92 offen offset:0 lds
Mv 168,72,112
Rt 20,23,32768
Mv 200,72,116
Mv 232,72,120
Rt 22,23,34816
Ma 232,72,124
buffer_load_dwordx4 v9, s[16:19], s92 offen offset:1024 lds
Ma 236,76,124
Rt 24,24,32768
Mv 236,76,120
Mv 204,76,116
Rt 26,24,34816
Mv 172,76,112
buffer_load_dwordx4 v10, s[16:19], s92 offen offset:2048 lds
Mv 140,76,108
Rt 28,25,32768
Mv 108,76,104
s_add_u32 s92, s42, 0x1c0000
Mv 76,76,100
Rt 30,25,34816
s_add_u32 m0, s41, 0
Mv 44,76,96
buffer_load_dwordx4 v8, s[16:19], s92 offen offset:0 lds
Mv 48,80,96
Rb 32,16,32768
Mv 80,80,100
Mv 112,80,104
Rb 36,16,34816
Mv 144,80,108
buffer_load_dwordx4 v9, s[16:19], s92 offen offset:1024 lds
Mv 176,80,112
Rb 40,16,40960
Mv 208,80,116
Mv 240,80,120
Rb 44,16,43008
Ma 240,80,124
buffer_load_dwordx4 v10, s[16:19], s92 offen offset:2048 lds
Ma 244,84,124
Rb 48,16,49152
Mv 244,84,120
Mv 212,84,116
Rb 52,16,51200
Mv 180,84,112
buffer_load_dwordx4 v11, s[16:19], s92 offen offset:3072 lds
Mv 148,84,108
Rb 56,16,57344
)" R"(
Mv 116,84,104
s_add_u32 s92, s42, 0x200000
Mv 84,84,100
Rb 60,16,59392
s_add_u32 m0, s41, 4096
Mv 52,84,96
buffer_load_dwordx4 v8, s[16:19], s92 offen offset:0 lds
Mv 56,88,96
Mv 88,88,100
Mv 120,88,104
Mv 152,88,108
buffer_load_dwordx4 v9, s[16:19], s92 offen offset:1024 lds
Mv 184,88,112
Mv 216,88,116
Mv 248,88,120
Ma 248,88,124
buffer_load_dwordx4 v10, s[16:19], s92 offen offset:2048 lds
Ma 252,92,124
Mv 252,92,120
Mv 220,92,116
Mv 188,92,112
Mv 156,92,108
Mv 124,92,104
Mv 92,92,100
Mv 60,92,96
s_waitcnt vmcnt(14) lgkmcnt(4)
s_barrier
v_cmp_eq_u32_e32 vcc, s3, v4
s_andn2_b64 s[28:29], 1, vcc
s_cbranch_scc1 .Lzsp2
.Lzok2:
Mv 32,0,32
Rt 64,18,49152
Mv 64,0,36
Rt 66,18,51200
s_add_u32 s92, s42, 0x80
Mv 96,0,40
Rt 68,19,49152
s_add_u32 m0, s41, 65536
Mv 128,0,44
Rt 70,19,51200
buffer_load_dwordx4 v8, s[16:19], s92 offen offset:0 lds
Mv 132,4,44
Rt 72,20,49152
Mv 100,4,40
Rt 74,20,51200
Mv 68,4,36
Rt 76,21,49152
Mv 36,4,32
Rt 78,21,51200
buffer_load_dwordx4 v9, s[16:19], s92 offen offset:1024 lds
Mv 40,8,32
Rt 80,22,49152
Mv 72,8,36
Rt 82,22,51200
Mv 104,8,40
Rt 84,23,49152
Mv 136,8,44
Rt 86,23,51200
buffer_load_dwordx4 v10, s[16:19], s92 offen offset:2048 lds
Mv 140,12,44
Rt 88,24,49152
Mv 108,12,40
Rt 90,24,51200
Mv 76,12,36
)" R"(
Rt 92,25,49152
Mv 44,12,32
Rt 94,25,51200
buffer_load_dwordx4 v11, s[16:19], s92 offen offset:3072 lds
Mv 48,16,32
Rb 96,17,32768
Mv 80,16,36
Rb 100,17,34816
s_add_u32 s92, s42, 0x40080
Mv 112,16,40
Rb 104,17,40960
s_add_u32 m0, s41, 69632
Mv 144,16,44
Rb 108,17,43008
buffer_load_dwordx4 v8, s[16:19], s92 offen offset:0 lds
Mv 148,20,44
Rb 112,17,49152
Mv 116,20,40
Rb 116,17,51200
Mv 84,20,36
Rb 120,17,57344
Mv 52,20,32
Rb 124,17,59392
buffer_load_dwordx4 v9, s[16:19], s92 offen offset:1024 lds
Mv 56,24,32
Mv 88,24,36
Mv 120,24,40
Mv 152,24,44
buffer_load_dwordx4 v10, s[16:19], s92 offen offset:2048 lds
Mv 156,28,44
Mv 124,28,40
Mv 92,28,36
Mv 60,28,32
s_waitcnt lgkmcnt(0)
s_barrier
Mv 32,64,96
v_bfe_u32 v12, v0, 3, 2
v_lshlrev_b32 v12, 5, v12
Mv 36,68,96
v_and_b32 v4, 3, v0
v_lshl_or_b32 v12, v4, 3, v12
Mv 40,72,96
s_add_u32 m0, s36, 98304
s_add_u32 s44, s43, 768
Mv 44,76,96
buffer_load_dwordx4 v26, s[12:15], s44 offen offset:0 lds
Mv 48,80,96
v_bfe_u32 v4, v0, 2, 2
v_lshl_or_b32 v12, v4, 7, v12
Mv 52,84,96
v_lshrrev_b32 v4, 4, v0
v_lshl_or_b32 v12, v4, 10, v12
Mv 56,88,96
v_xor_b32 v13, 32, v12
v_xor_b32 v14, 64, v12
Mv 60,92,96
buffer_load_dwordx4 v27, s[12:15], s44 offen offset:1024 lds
Mv 64,64,100
v_xor_b32 v15, 96, v12
v_add_u32 v20, 0x20000, v12
Mv 68,68,100
v_add_u32 v21, 0x20000, v13
v_add_u32 v22, 0x20000, v14
Mv 72,72,100
v_add_u32 v23, 0x20000, v15
)" R"(
v_add_u32 v16, 0x10000, v12
Mv 76,76,100
s_add_u32 s44, s43, 13056
buffer_load_dwordx4 v26, s[12:15], s44 offen offset:2048 lds
Mv 80,80,100
v_add_u32 v17, 0x10000, v13
v_add_u32 v18, 0x10000, v14
Mv 84,84,100
v_add_u32 v19, 0x10000, v15
Cv 32,32,33
Mv 88,88,100
Cv 33,34,35
Cv 34,36,37
Mv 92,92,100
buffer_load_dwordx4 v27, s[12:15], s44 offen offset:3072 lds
Mv 96,64,104
Cv 35,38,39
Cv 40,40,41
Mv 100,68,104
Cv 41,42,43
Cv 42,44,45
Mv 104,72,104
s_add_u32 m0, s41, 32768
s_add_u32 s92, s42, 0x1c0080
Mv 108,76,104
buffer_load_dwordx4 v8, s[16:19], s92 offen offset:0 lds
Mv 112,80,104
Cv 43,46,47
Cv 48,48,49
Mv 116,84,104
Cv 49,50,51
Cv 50,52,53
Mv 120,88,104
Cv 51,54,55
Cv 56,56,57
Mv 124,92,104
buffer_store_dwordx4 v[32:35], v2, s[20:23], s45 offen offset:0 sc0 sc1
Mv 128,64,108
Cv 57,58,59
Cv 58,60,61
Mv 132,68,108
Cv 59,62,63
Cv 64,64,65
Mv 136,72,108
Cv 65,66,67
Cv 66,68,69
Mv 140,76,108
buffer_load_dwordx4 v9, s[16:19], s92 offen offset:1024 lds
Mv 144,80,108
Cv 67,70,71
Cv 72,72,73
Mv 148,84,108
Cv 73,74,75
Cv 74,76,77
Mv 152,88,108
Cv 75,78,79
Cv 80,80,81
Mv 156,92,108
buffer_store_dwordx4 v[40:43], v2, s[20:23], s45 offen offset:1024 sc0 sc1
Mv 160,0,48
Cv 81,82,83
Cv 82,84,85
Mv 160,64,112
Cv 83,86,87
)" R"(
Cv 88,88,89
Mv 164,4,48
Cv 89,90,91
Cv 90,92,93
Mv 164,68,112
buffer_load_dwordx4 v10, s[16:19], s92 offen offset:2048 lds
Mv 168,8,48
Cv 91,94,95
Cv 96,96,97
Mv 168,72,112
Cv 97,98,99
Cv 98,100,101
Mv 172,12,48
Cv 99,102,103
Cv 104,104,105
Mv 172,76,112
buffer_store_dwordx4 v[48:51], v2, s[20:23], s45 offen offset:2048 sc0 sc1
Mv 176,16,48
Cv 105,106,107
Cv 106,108,109
Mv 176,80,112
Cv 107,110,111
Cv 112,112,113
Mv 180,20,48
Cv 113,114,115
Cv 114,116,117
Mv 180,84,112
buffer_load_dwordx4 v11, s[16:19], s92 offen offset:3072 lds
Mv 184,24,48
Cv 115,118,119
Cv 120,120,121
Mv 184,88,112
Cv 121,122,123
Cv 122,124,125
Mv 188,28,48
Cv 123,126,127
Cv 128,128,129
Mv 188,92,112
buffer_store_dwordx4 v[56:59], v2, s[20:23], s45 offen offset:3072 sc0 sc1
Mv 192,0,52
Cv 129,130,131
Cv 130,132,133
Mv 192,64,116
Cv 131,134,135
Cv 136,136,137
Mv 196,4,52
s_add_u32 m0, s41, 36864
s_add_u32 s92, s42, 0x200080
Mv 196,68,116
buffer_load_dwordx4 v8, s[16:19], s92 offen offset:0 lds
Mv 200,8,52
Cv 137,138,139
Cv 138,140,141
Mv 200,72,116
Cv 139,142,143
Cv 144,144,145
Mv 204,12,52
Cv 145,146,147
Cv 146,148,149
Mv 204,76,116
buffer_store_dwordx4 v[64:67], v3, s[20:23], s45 offen offset:0 sc0 sc1
Mv 208,16,52
Cv 147,150,151
Cv 152,152,153
)" R"(
Mv 208,80,116
Cv 153,154,155
Cv 154,156,157
Mv 212,20,52
Cv 155,158,159
Mv 212,84,116
buffer_load_dwordx4 v9, s[16:19], s92 offen offset:1024 lds
Mv 216,24,52
Mv 216,88,116
Mv 220,28,52
Mv 220,92,116
buffer_store_dwordx4 v[72:75], v3, s[20:23], s45 offen offset:1024 sc0 sc1
Mv 224,0,56
Ma 224,0,60
Ma 228,4,60
Mv 228,4,56
buffer_load_dwordx4 v10, s[16:19], s92 offen offset:2048 lds
Mv 232,8,56
Ma 232,8,60
Ma 236,12,60
Mv 236,12,56
s_waitcnt vmcnt(24)
s_barrier
Mv 240,16,56
buffer_store_dwordx4 v[80:83], v3, s[20:23], s45 offen offset:2048 sc0 sc1
Ma 240,16,60
Ma 244,20,60
Mv 244,20,56
Mv 248,24,56
Ma 248,24,60
Ma 252,28,60
Mv 252,28,56
buffer_store_dwordx4 v[88:91], v3, s[20:23], s45 offen offset:3072 sc0 sc1
Mv 224,64,120
Rt 16,20,0
Mv 228,68,120
Rt 18,20,512
Mv 232,72,120
Rt 20,21,0
Mv 236,76,120
Rt 22,21,512
Mv 240,80,120
Rt 24,22,0
Mv 244,84,120
Rt 26,22,512
Mv 248,88,120
Rt 28,23,0
Mv 252,92,120
Rt 30,23,512
Ma 224,64,124
Rt 32,20,4096
Ma 228,68,124
Rt 34,20,4608
Ma 232,72,124
Rt 36,21,4096
Ma 236,76,124
Rt 38,21,4608
Ma 240,80,124
Rt 40,22,4096
Ma 244,84,124
Rt 42,22,4608
Ma 248,88,124
Rt 44,23,4096
Ma 252,92,124
Rt 46,23,4608
)" R"(
s_waitcnt vmcnt(15) lgkmcnt(8)
s_branch .LzQA
.LzPAVX:
Mv 32,32,0
Rt 64,18,16384
Mv 36,36,0
Rb 192,28,32768
Mv 96,40,0
Rt 66,18,18432
Mv 100,44,0
s_add_u32 s39, s39, 0x40000
Rb 196,28,34816
Mv 160,48,0
Rt 68,19,16384
s_add_u32 m0, s37, 0
Mv 164,52,0
buffer_load_dwordx4 v12, s[8:11], s39 offen offset:0 lds
Mv 224,56,0
Rt 70,19,18432
Mv 228,60,0
Rb 200,29,32768
Mv 236,60,4
Rt 72,20,16384
Mv 232,56,4
Rb 204,29,34816
Mv 172,52,4
Rt 74,20,18432
Mv 168,48,4
Rb 208,28,36864
Mv 108,44,4
Rt 76,21,16384
Mv 104,40,4
buffer_load_dwordx4 v13, s[8:11], s39 offen offset:1024 lds
Mv 44,36,4
Rt 78,21,18432
Mv 40,32,4
Rb 212,28,38912
Mv 48,32,8
Rt 80,22,16384
Mv 52,36,8
Rb 216,29,36864
Mv 112,40,8
Rt 82,22,18432
Mv 116,44,8
Rb 220,29,38912
Mv 176,48,8
Rt 84,23,16384
Mv 180,52,8
buffer_load_dwordx4 v14, s[8:11], s39 offen offset:2048 lds
Mv 240,56,8
Rt 86,23,18432
Mv 244,60,8
Mv 252,60,12
Rt 88,24,16384
Mv 248,56,12
Mv 188,52,12
Rt 90,24,18432
Mv 184,48,12
Mv 124,44,12
Rt 92,25,16384
Mv 120,40,12
buffer_load_dwordx4 v15, s[8:11], s39 offen offset:3072 lds
Mv 60,36,12
Rt 94,25,18432
)" R"(
Mv 56,32,12
s_add_u32 s44, s43, 256
s_waitcnt lgkmcnt(6)
Mv 64,32,16
Rb 96,17,0
s_add_u32 m0, s36, 131072
Mv 68,36,16
buffer_load_dwordx4 v26, s[12:15], s44 offen offset:0 lds
Mv 128,40,16
Rb 100,17,2048
Mv 132,44,16
Mv 192,48,16
Rb 104,17,8192
Mv 196,52,16
buffer_load_dwordx4 v27, s[12:15], s44 offen offset:1024 lds
Ma 224,56,16
Rb 108,17,10240
Ma 228,60,16
Ma 236,60,20
Rb 112,17,16384
Ma 232,56,20
s_add_u32 s44, s43, 12544
buffer_load_dwordx4 v26, s[12:15], s44 offen offset:2048 lds
Mv 204,52,20
Rb 116,17,18432
Mv 200,48,20
Mv 140,44,20
Rb 120,17,24576
Mv 136,40,20
buffer_load_dwordx4 v27, s[12:15], s44 offen offset:3072 lds
Mv 76,36,20
Rb 124,17,26624
Mv 72,32,20
Mv 80,32,24
s_add_u32 m0, s36, 135168
s_add_u32 s44, s43, 384
Mv 84,36,24
buffer_load_dwordx4 v26, s[12:15], s44 offen offset:0 lds
Mv 144,40,24
Mv 148,44,24
Mv 208,48,24
Mv 212,52,24
buffer_load_dwordx4 v27, s[12:15], s44 offen offset:1024 lds
Ma 240,56,24
Ma 244,60,24
Ma 252,60,28
Ma 248,56,28
s_add_u32 s44, s43, 12672
buffer_load_dwordx4 v26, s[12:15], s44 offen offset:2048 lds
Mv 220,52,28
Mv 216,48,28
Mv 156,44,28
s_mov_b32 s85, 0xc108800
v_and_b32 v2, 7, v0
v_lshlrev_b32 v2, 7, v2
Mv 152,40,28
buffer_load_dwordx4 v27, s[12:15], s44 offen offset:3072 lds
Mv 92,36,28
buffer_load_dword v4, v2, s[20:23], s85 offen sc0 sc1
Mv 88,32,28
s_waitcnt vmcnt(18) lgkmcnt(0)
s_barrier
Mv 32,96,64
Rt 0,18,32768
Mv 36,100,64
)" R"(
s_add_u32 s38, s38, 0xffff0080
Mv 96,104,64
Rt 2,18,34816
s_add_u32 m0, s36, 0
Mv 100,108,64
buffer_load_dwordx4 v8, s[4:7], s38 offen offset:0 lds
Mv 160,112,64
Rt 4,19,32768
Mv 164,116,64
Mv 224,120,64
Rt 6,19,34816
Mv 228,124,64
buffer_load_dwordx4 v9, s[4:7], s38 offen offset:1024 lds
Mv 236,124,68
Rt 8,20,32768
Mv 232,120,68
Mv 172,116,68
Rt 10,20,34816
Mv 168,112,68
buffer_load_dwordx4 v10, s[4:7], s38 offen offset:2048 lds
Mv 108,108,68
Rt 12,21,32768
Mv 104,104,68
Mv 44,100,68
Rt 14,21,34816
Mv 40,96,68
buffer_load_dwordx4 v11, s[4:7], s38 offen offset:3072 lds
Mv 48,96,72
Rt 16,22,32768
Mv 52,100,72
s_add_u32 s38, s38, 0x10000
Mv 112,104,72
Rt 18,22,34816
s_add_u32 m0, s36, 4096
Mv 116,108,72
buffer_load_dwordx4 v8, s[4:7], s38 offen offset:0 lds
Mv 176,112,72
Rt 20,23,32768
Mv 180,116,72
Mv 240,120,72
Rt 22,23,34816
Mv 244,124,72
buffer_load_dwordx4 v9, s[4:7], s38 offen offset:1024 lds
Mv 252,124,76
Rt 24,24,32768
Mv 248,120,76
Mv 188,116,76
Rt 26,24,34816
Mv 184,112,76
buffer_load_dwordx4 v10, s[4:7], s38 offen offset:2048 lds
Mv 124,108,76
Rt 28,25,32768
Mv 120,104,76
Mv 60,100,76
Rt 30,25,34816
Mv 56,96,76
buffer_load_dwordx4 v11, s[4:7], s38 offen offset:3072 lds
Mv 64,96,80
Rb 32,16,32768
Mv 68,100,80
Mv 128,104,80
Rb 36,16,34816
Mv 132,108,80
s_add_u32 s39, s39, 0x40000
)" R"(
Mv 192,112,80
Rb 40,16,40960
s_add_u32 m0, s37, 16384
Mv 196,116,80
buffer_load_dwordx4 v12, s[8:11], s39 offen offset:0 lds
Ma 224,120,80
Rb 44,16,43008
s_mov_b32 s24, s70
Ma 228,124,80
s_mov_b32 s25, s71
s_mov_b32 s26, 0x188000
Ma 236,124,84
Rb 48,16,49152
s_lshl2_add_u32 s88, s2, s33
Ma 232,120,84
s_lshl_b32 s88, s88, 12
Mv 204,116,84
Rb 52,16,51200
Mv 200,112,84
buffer_load_dwordx4 v13, s[8:11], s39 offen offset:1024 lds
Mv 140,108,84
Rb 56,16,57344
Mv 136,104,84
Mv 76,100,84
Rb 60,16,59392
Mv 72,96,84
Mv 80,96,88
Mv 84,100,88
buffer_load_dwordx4 v14, s[8:11], s39 offen offset:2048 lds
Mv 144,104,88
Mv 148,108,88
Mv 208,112,88
Mv 212,116,88
Ma 240,120,88
Ma 244,124,88
buffer_load_dwordx4 v15, s[8:11], s39 offen offset:3072 lds
Ma 252,124,92
Ma 248,120,92
Mv 220,116,92
Mv 216,112,92
buffer_load_dword v6, v5, s[24:27], s88 offen
Mv 156,108,92
Mv 152,104,92
Mv 92,100,92
Mv 88,96,92
s_waitcnt vmcnt(14) lgkmcnt(0)
s_barrier
Mv 32,32,0
Rt 64,18,49152
Mv 36,36,0
Rb 128,28,32768
Mv 96,40,0
Rt 66,18,51200
Mv 100,44,0
s_add_u32 s39, s39, 0x40000
Rb 132,28,34816
Mv 160,48,0
Rt 68,19,49152
s_add_u32 m0, s37, 32768
Mv 164,52,0
buffer_load_dwordx4 v12, s[8:11], s39 offen offset:0 lds
Mv 224,56,0
Rt 70,19,51200
Mv 228,60,0
Rb 136,29,32768
)" R"(
Mv 236,60,4
Rt 72,20,49152
Mv 232,56,4
Rb 140,29,34816
Mv 172,52,4
Rt 74,20,51200
Mv 168,48,4
Rb 144,28,36864
Mv 108,44,4
Rt 76,21,49152
Mv 104,40,4
buffer_load_dwordx4 v13, s[8:11], s39 offen offset:1024 lds
Mv 44,36,4
Rt 78,21,51200
Mv 40,32,4
Rb 148,28,38912
Mv 48,32,8
Rt 80,22,49152
Mv 52,36,8
Rb 152,29,36864
Mv 112,40,8
Rt 82,22,51200
Mv 116,44,8
Rb 156,29,38912
Mv 176,48,8
Rt 84,23,49152
Mv 180,52,8
buffer_load_dwordx4 v14, s[8:11], s39 offen offset:2048 lds
Mv 240,56,8
Rt 86,23,51200
Mv 244,60,8
Mv 252,60,12
Rt 88,24,49152
Mv 248,56,12
Mv 188,52,12
Rt 90,24,51200
Mv 184,48,12
Mv 124,44,12
Rt 92,25,49152
Mv 120,40,12
buffer_load_dwordx4 v15, s[8:11], s39 offen offset:3072 lds
Mv 60,36,12
Rt 94,25,51200
Mv 56,32,12
s_add_u32 s44, s43, 512
s_waitcnt lgkmcnt(6)
Mv 64,32,16
Rb 96,17,32768
s_add_u32 m0, s36, 131072
Mv 68,36,16
buffer_load_dwordx4 v26, s[12:15], s44 offen offset:0 lds
Mv 128,40,16
Rb 100,17,34816
Mv 132,44,16
Mv 192,48,16
Rb 104,17,40960
Mv 196,52,16
buffer_load_dwordx4 v27, s[12:15], s44 offen offset:1024 lds
Ma 224,56,16
Rb 108,17,43008
Ma 228,60,16
Ma 236,60,20
Rb 112,17,49152
Ma 232,56,20
)" R"(
s_add_u32 s44, s43, 12800
buffer_load_dwordx4 v26, s[12:15], s44 offen offset:2048 lds
Mv 204,52,20
Rb 116,17,51200
Mv 200,48,20
Mv 140,44,20
Rb 120,17,57344
Mv 136,40,20
buffer_load_dwordx4 v27, s[12:15], s44 offen offset:3072 lds
Mv 76,36,20
Rb 124,17,59392
Mv 72,32,20
Mv 80,32,24
s_add_u32 m0, s36, 135168
s_add_u32 s44, s43, 640
Mv 84,36,24
buffer_load_dwordx4 v26, s[12:15], s44 offen offset:0 lds
Mv 144,40,24
Mv 148,44,24
Mv 208,48,24
Mv 212,52,24
buffer_load_dwordx4 v27, s[12:15], s44 offen offset:1024 lds
Ma 240,56,24
Ma 244,60,24
Ma 252,60,28
Ma 248,56,28
s_add_u32 s44, s43, 12928
buffer_load_dwordx4 v26, s[12:15], s44 offen offset:2048 lds
Mv 220,52,28
Mv 216,48,28
Mv 156,44,28
Mv 152,40,28
buffer_load_dwordx4 v27, s[12:15], s44 offen offset:3072 lds
Mv 92,36,28
Mv 88,32,28
s_waitcnt vmcnt(17) lgkmcnt(0)
s_barrier
Mv 32,96,64
Rt 0,18,0
v_xor_b32 v4, s3, v4
v_cmp_gt_u32_e32 vcc, 2, v4
s_andn2_b32 s28, 0xff, vcc_lo
s_cbranch_scc1 .Lzsp3
.Lzok3:
Mv 36,100,64
s_add_u32 s38, s38, 0xffff0080
v_xor_b32 v4, s3, v4
Mv 96,104,64
Rt 2,18,2048
s_add_u32 m0, s36, 32768
Mv 100,108,64
buffer_load_dwordx4 v8, s[4:7], s38 offen offset:0 lds
Mv 160,112,64
Rt 4,19,0
v_and_b32 v4, 1, v4
Mv 164,116,64
v_cmp_eq_u32_e32 vcc, 1, v4
s_mov_b32 s85, vcc_lo
Mv 224,120,64
Rt 6,19,2048
s_and_b32 s28, s2, 7
Mv 228,124,64
buffer_load_dwordx4 v9, s[4:7], s38 offen offset:1024 lds
Mv 236,124,68
Rt 8,20,0
)" R"(
s_lshr_b32 s28, s85, s28
Mv 232,120,68
s_xor_b32 s28, s28, s34
s_and_b32 s28, s28, 1
Mv 172,116,68
Rt 10,20,2048
s_lshl_b32 s84, s28, 13
Mv 168,112,68
buffer_load_dwordx4 v10, s[4:7], s38 offen offset:2048 lds
Mv 108,108,68
Rt 12,21,0
s_lshl1_add_u32 s28, s36, s84
Mv 104,104,68
v_add_u32 v2, s28, v1
v_add_u32 v3, 0x1000, v2
Mv 44,100,68
Rt 14,21,2048
s_and_b32 s85, s85, 0xff
Mv 40,96,68
buffer_load_dwordx4 v11, s[4:7], s38 offen offset:3072 lds
Mv 48,96,72
Rt 16,22,0
s_bcnt1_i32_b32 s28, s85
Mv 52,100,72
s_add_u32 s38, s38, 0x10000
Mv 112,104,72
Rt 18,22,2048
s_add_u32 m0, s36, 36864
Mv 116,108,72
buffer_load_dwordx4 v8, s[4:7], s38 offen offset:0 lds
Mv 176,112,72
Rt 20,23,0
Mv 180,116,72
s_cmp_lg_u32 s28, 4
s_cmov_b32 s85, 0xf0
Mv 240,120,72
Rt 22,23,2048
s_and_b32 s28, s2, 7
Mv 244,124,72
buffer_load_dwordx4 v9, s[4:7], s38 offen offset:1024 lds
Mv 252,124,76
Rt 24,24,0
s_lshr_b32 s85, s85, s28
Mv 248,120,76
s_and_b32 s85, s85, 1
Mv 188,116,76
Rt 26,24,2048
Mv 184,112,76
buffer_load_dwordx4 v10, s[4:7], s38 offen offset:2048 lds
Mv 124,108,76
Rt 28,25,0
Mv 120,104,76
Mv 60,100,76
Rt 30,25,2048
Mv 56,96,76
buffer_load_dwordx4 v11, s[4:7], s38 offen offset:3072 lds
Mv 64,96,80
Rb 32,16,0
Mv 68,100,80
Mv 128,104,80
Rb 36,16,2048
Mv 132,108,80
s_add_u32 s39, s39, 0x40000
Mv 192,112,80
)" R"(
Rb 40,16,8192
s_add_u32 m0, s37, 49152
Mv 196,116,80
buffer_load_dwordx4 v12, s[8:11], s39 offen offset:0 lds
Ma 224,120,80
Rb 44,16,10240
Ma 228,124,80
Ma 236,124,84
Rb 48,16,16384
Ma 232,120,84
Mv 204,116,84
Rb 52,16,18432
Mv 200,112,84
buffer_load_dwordx4 v13, s[8:11], s39 offen offset:1024 lds
Mv 140,108,84
Rb 56,16,24576
Mv 136,104,84
Mv 76,100,84
Rb 60,16,26624
Mv 72,96,84
Mv 80,96,88
Mv 84,100,88
buffer_load_dwordx4 v14, s[8:11], s39 offen offset:2048 lds
Mv 144,104,88
Mv 148,108,88
Mv 208,112,88
Mv 212,116,88
Ma 240,120,88
Ma 244,124,88
buffer_load_dwordx4 v15, s[8:11], s39 offen offset:3072 lds
Ma 252,124,92
Ma 248,120,92
Mv 220,116,92
Mv 216,112,92
Mv 156,108,92
Mv 152,104,92
Mv 92,100,92
Mv 88,96,92
s_waitcnt vmcnt(12) lgkmcnt(0)
s_barrier
Mv 32,32,0
Rt 64,18,16384
Mv 36,36,0
Rb 160,28,32768
v_lshrrev_b32 v5, 2, v0
Mv 96,40,0
Rt 66,18,18432
v_and_b32 v5, 8, v5
Mv 100,44,0
Rb 164,28,34816
v_and_or_b32 v5, v0, 7, v5
Mv 160,48,0
Rt 68,19,16384
v_lshlrev_b32 v5, 8, v5
Mv 164,52,0
v_and_or_b32 v5, v0, 24, v5
v_lshrrev_b32 v5, 1, v5
Mv 224,56,0
Rt 70,19,18432
v_mov_b32 v6, s3
Mv 228,60,0
Rb 168,29,32768
Mv 236,60,4
Rt 72,20,16384
Mv 232,56,4
)" R"(
Rb 172,29,34816
Mv 172,52,4
Rt 74,20,18432
Mv 168,48,4
Rb 176,28,36864
Mv 108,44,4
Rt 76,21,16384
Mv 104,40,4
Mv 44,36,4
Rt 78,21,18432
Mv 40,32,4
Rb 180,28,38912
Mv 48,32,8
Rt 80,22,16384
Mv 52,36,8
Rb 184,29,36864
Mv 112,40,8
Rt 82,22,18432
Mv 116,44,8
Rb 188,29,38912
Mv 176,48,8
Rt 84,23,16384
Mv 180,52,8
Mv 240,56,8
Rt 86,23,18432
Mv 244,60,8
Mv 252,60,12
Rt 88,24,16384
Mv 248,56,12
Mv 188,52,12
Rt 90,24,18432
Mv 184,48,12
Mv 124,44,12
Rt 92,25,16384
Mv 120,40,12
Mv 60,36,12
Rt 94,25,18432
v_lshrrev_b32 v10, 3, v0
Mv 56,32,12
v_and_b32 v11, 0x70, v1
v_lshl_or_b32 v10, v10, 13, v11
Mv 64,32,16
Rb 96,17,0
v_lshlrev_b32 v11, 1, v0
Mv 68,36,16
v_and_b32 v11, 32, v11
v_xor_b32 v10, v10, v11
buffer_load_dword v4, v7, s[20:23], s52 offen offset:0 sc0 sc1
Mv 128,40,16
Rb 100,17,2048
s_and_b32 s28, s33, 1
Mv 132,44,16
s_lshl_b32 s28, s28, 6
v_xor_b32 v8, s28, v10
Mv 192,48,16
Rb 104,17,8192
v_xor_b32 v9, 64, v8
Mv 196,52,16
v_add_u32 v9, 0xfc00, v9
v_add_u32 v10, 0x1f800, v8
Ma 224,56,16
Rb 108,17,10240
v_add_u32 v11, 0x1f800, v9
Ma 228,60,16
)" R"(
Ma 236,60,20
Rb 112,17,16384
Ma 232,56,20
Mv 204,52,20
Rb 116,17,18432
Mv 200,48,20
Mv 140,44,20
Rb 120,17,24576
Mv 136,40,20
Mv 76,36,20
Rb 124,17,26624
Mv 72,32,20
Mv 80,32,24
Mv 84,36,24
Mv 144,40,24
Mv 148,44,24
Mv 208,48,24
Mv 212,52,24
Ma 240,56,24
Ma 244,60,24
Ma 252,60,28
Ma 248,56,28
Mv 220,52,28
Mv 216,48,28
Mv 156,44,28
Mv 152,40,28
Mv 92,36,28
Mv 88,32,28
s_waitcnt vmcnt(5) lgkmcnt(0)
s_barrier
Mv 32,96,64
Rt 0,18,32768
Mv 36,100,64
s_add_u32 s92, s42, 0x0
Mv 96,104,64
Rt 2,18,34816
s_add_u32 m0, s41, 131072
Mv 100,108,64
buffer_load_dwordx4 v8, s[16:19], s92 offen offset:0 lds
Mv 160,112,64
Rt 4,19,32768
Mv 164,116,64
Mv 224,120,64
Rt 6,19,34816
Mv 228,124,64
buffer_load_dwordx4 v9, s[16:19], s92 offen offset:1024 lds
Mv 236,124,68
Rt 8,20,32768
Mv 232,120,68
Mv 172,116,68
Rt 10,20,34816
Mv 168,112,68
buffer_load_dwordx4 v10, s[16:19], s92 offen offset:2048 lds
Mv 108,108,68
Rt 12,21,32768
Mv 104,104,68
Mv 44,100,68
Rt 14,21,34816
Mv 40,96,68
buffer_load_dwordx4 v11, s[16:19], s92 offen offset:3072 lds
Mv 48,96,72
Rt 16,22,32768
Mv 52,100,72
s_add_u32 s92, s42, 0x40000
Mv 112,104,72
)" R"(
Rt 18,22,34816
s_add_u32 m0, s41, 135168
Mv 116,108,72
buffer_load_dwordx4 v8, s[16:19], s92 offen offset:0 lds
Mv 176,112,72
Rt 20,23,32768
Mv 180,116,72
Mv 240,120,72
Rt 22,23,34816
Mv 244,124,72
buffer_load_dwordx4 v9, s[16:19], s92 offen offset:1024 lds
Mv 252,124,76
Rt 24,24,32768
Mv 248,120,76
Mv 188,116,76
Rt 26,24,34816
Mv 184,112,76
buffer_load_dwordx4 v10, s[16:19], s92 offen offset:2048 lds
Mv 124,108,76
Rt 28,25,32768
Mv 120,104,76
s_add_u32 s92, s42, 0x1c0000
Mv 60,100,76
Rt 30,25,34816
s_add_u32 m0, s41, 0
Mv 56,96,76
buffer_load_dwordx4 v8, s[16:19], s92 offen offset:0 lds
Mv 64,96,80
Rb 32,16,32768
Mv 68,100,80
Mv 128,104,80
Rb 36,16,34816
Mv 132,108,80
buffer_load_dwordx4 v9, s[16:19], s92 offen offset:1024 lds
Mv 192,112,80
Rb 40,16,40960
Mv 196,116,80
Ma 224,120,80
Rb 44,16,43008
Ma 228,124,80
buffer_load_dwordx4 v10, s[16:19], s92 offen offset:2048 lds
Ma 236,124,84
Rb 48,16,49152
Ma 232,120,84
Mv 204,116,84
Rb 52,16,51200
Mv 200,112,84
buffer_load_dwordx4 v11, s[16:19], s92 offen offset:3072 lds
Mv 140,108,84
Rb 56,16,57344
Mv 136,104,84
s_add_u32 s92, s42, 0x200000
Mv 76,100,84
Rb 60,16,59392
s_add_u32 m0, s41, 4096
Mv 72,96,84
buffer_load_dwordx4 v8, s[16:19], s92 offen offset:0 lds
Mv 80,96,88
Mv 84,100,88
Mv 144,104,88
Mv 148,108,88
buffer_load_dwordx4 v9, s[16:19], s92 offen offset:1024 lds
Mv 208,112,88
Mv 212,116,88
)" R"(
Ma 240,120,88
Ma 244,124,88
buffer_load_dwordx4 v10, s[16:19], s92 offen offset:2048 lds
Ma 252,124,92
Ma 248,120,92
Mv 220,116,92
Mv 216,112,92
Mv 156,108,92
Mv 152,104,92
Mv 92,100,92
Mv 88,96,92
s_waitcnt vmcnt(14) lgkmcnt(4)
s_barrier
v_cmp_eq_u32_e32 vcc, s3, v4
s_andn2_b64 s[28:29], 1, vcc
s_cbranch_scc1 .Lzsp4
.Lzok4:
Mv 32,32,0
Rt 64,18,49152
Mv 36,36,0
Rt 66,18,51200
s_add_u32 s92, s42, 0x80
Mv 96,40,0
Rt 68,19,49152
s_add_u32 m0, s41, 65536
Mv 100,44,0
Rt 70,19,51200
buffer_load_dwordx4 v8, s[16:19], s92 offen offset:0 lds
Mv 108,44,4
Rt 72,20,49152
Mv 104,40,4
Rt 74,20,51200
Mv 44,36,4
Rt 76,21,49152
Mv 40,32,4
Rt 78,21,51200
buffer_load_dwordx4 v9, s[16:19], s92 offen offset:1024 lds
Mv 48,32,8
Rt 80,22,49152
Mv 52,36,8
Rt 82,22,51200
Mv 112,40,8
Rt 84,23,49152
Mv 116,44,8
Rt 86,23,51200
buffer_load_dwordx4 v10, s[16:19], s92 offen offset:2048 lds
Mv 124,44,12
Rt 88,24,49152
Mv 120,40,12
Rt 90,24,51200
Mv 60,36,12
Rt 92,25,49152
Mv 56,32,12
Rt 94,25,51200
buffer_load_dwordx4 v11, s[16:19], s92 offen offset:3072 lds
Mv 64,32,16
Rb 96,17,32768
Mv 68,36,16
Rb 100,17,34816
s_add_u32 s92, s42, 0x40080
Mv 128,40,16
Rb 104,17,40960
s_add_u32 m0, s41, 69632
Mv 132,44,16
Rb 108,17,43008
)" R"(
buffer_load_dwordx4 v8, s[16:19], s92 offen offset:0 lds
Mv 140,44,20
Rb 112,17,49152
Mv 136,40,20
Rb 116,17,51200
Mv 76,36,20
Rb 120,17,57344
Mv 72,32,20
Rb 124,17,59392
buffer_load_dwordx4 v9, s[16:19], s92 offen offset:1024 lds
Mv 80,32,24
Mv 84,36,24
Mv 144,40,24
Mv 148,44,24
buffer_load_dwordx4 v10, s[16:19], s92 offen offset:2048 lds
Mv 156,44,28
Mv 152,40,28
Mv 92,36,28
Mv 88,32,28
s_waitcnt lgkmcnt(0)
s_barrier
Mv 32,96,64
v_bfe_u32 v12, v0, 3, 2
v_lshlrev_b32 v12, 5, v12
Mv 36,100,64
v_and_b32 v4, 3, v0
v_lshl_or_b32 v12, v4, 3, v12
Mv 40,96,68
s_add_u32 m0, s36, 98304
s_add_u32 s44, s43, 768
Mv 44,100,68
buffer_load_dwordx4 v26, s[12:15], s44 offen offset:0 lds
Mv 48,96,72
v_bfe_u32 v4, v0, 2, 2
v_lshl_or_b32 v12, v4, 7, v12
Mv 52,100,72
v_lshrrev_b32 v4, 4, v0
v_lshl_or_b32 v12, v4, 10, v12
Mv 56,96,76
v_xor_b32 v13, 32, v12
v_xor_b32 v14, 64, v12
Mv 60,100,76
buffer_load_dwordx4 v27, s[12:15], s44 offen offset:1024 lds
Mv 64,96,80
v_xor_b32 v15, 96, v12
v_add_u32 v20, 0x20000, v12
Mv 68,100,80
v_add_u32 v21, 0x20000, v13
v_add_u32 v22, 0x20000, v14
Mv 72,96,84
v_add_u32 v23, 0x20000, v15
v_add_u32 v16, 0x10000, v12
Mv 76,100,84
s_add_u32 s44, s43, 13056
buffer_load_dwordx4 v26, s[12:15], s44 offen offset:2048 lds
Mv 80,96,88
v_add_u32 v17, 0x10000, v13
v_add_u32 v18, 0x10000, v14
Mv 84,100,88
v_add_u32 v19, 0x10000, v15
Cv 32,32,33
Mv 88,96,92
Cv 33,34,35
Cv 34,36,37
Mv 92,100,92
)" R"(
buffer_load_dwordx4 v27, s[12:15], s44 offen offset:3072 lds
Mv 96,104,64
Cv 35,38,39
Cv 40,40,41
Mv 100,108,64
Cv 41,42,43
Cv 42,44,45
Mv 104,104,68
s_add_u32 m0, s41, 32768
s_add_u32 s92, s42, 0x1c0080
Mv 108,108,68
buffer_load_dwordx4 v8, s[16:19], s92 offen offset:0 lds
Mv 112,104,72
Cv 43,46,47
Cv 48,48,49
Mv 116,108,72
Cv 49,50,51
Cv 50,52,53
Mv 120,104,76
Cv 51,54,55
Cv 56,56,57
Mv 124,108,76
buffer_store_dwordx4 v[32:35], v2, s[20:23], s45 offen offset:0 sc0 sc1
Mv 128,104,80
Cv 57,58,59
Cv 58,60,61
Mv 132,108,80
Cv 59,62,63
Cv 64,64,65
Mv 136,104,84
Cv 65,66,67
Cv 66,68,69
Mv 140,108,84
buffer_load_dwordx4 v9, s[16:19], s92 offen offset:1024 lds
Mv 144,104,88
Cv 67,70,71
Cv 72,72,73
Mv 148,108,88
Cv 73,74,75
Cv 74,76,77
Mv 152,104,92
Cv 75,78,79
Cv 80,80,81
Mv 156,108,92
buffer_store_dwordx4 v[40:43], v2, s[20:23], s45 offen offset:1024 sc0 sc1
Mv 160,48,0
Cv 81,82,83
Cv 82,84,85
Mv 160,112,64
Cv 83,86,87
Cv 88,88,89
Mv 164,52,0
Cv 89,90,91
Cv 90,92,93
Mv 164,116,64
buffer_load_dwordx4 v10, s[16:19], s92 offen offset:2048 lds
Mv 168,48,4
Cv 91,94,95
Cv 96,96,97
Mv 168,112,68
Cv 97,98,99
Cv 98,100,101
Mv 172,52,4
Cv 99,102,103
)" R"(
Cv 104,104,105
Mv 172,116,68
buffer_store_dwordx4 v[48:51], v2, s[20:23], s45 offen offset:2048 sc0 sc1
Mv 176,48,8
Cv 105,106,107
Cv 106,108,109
Mv 176,112,72
Cv 107,110,111
Cv 112,112,113
Mv 180,52,8
Cv 113,114,115
Cv 114,116,117
Mv 180,116,72
buffer_load_dwordx4 v11, s[16:19], s92 offen offset:3072 lds
Mv 184,48,12
Cv 115,118,119
Cv 120,120,121
Mv 184,112,76
Cv 121,122,123
Cv 122,124,125
Mv 188,52,12
Cv 123,126,127
Cv 128,128,129
Mv 188,116,76
buffer_store_dwordx4 v[56:59], v2, s[20:23], s45 offen offset:3072 sc0 sc1
Mv 192,48,16
Cv 129,130,131
Cv 130,132,133
Mv 192,112,80
Cv 131,134,135
Cv 136,136,137
Mv 196,52,16
s_add_u32 m0, s41, 36864
s_add_u32 s92, s42, 0x200080
Mv 196,116,80
buffer_load_dwordx4 v8, s[16:19], s92 offen offset:0 lds
Mv 200,48,20
Cv 137,138,139
Cv 138,140,141
Mv 200,112,84
Cv 139,142,143
Cv 144,144,145
Mv 204,52,20
Cv 145,146,147
Cv 146,148,149
Mv 204,116,84
buffer_store_dwordx4 v[64:67], v3, s[20:23], s45 offen offset:0 sc0 sc1
Mv 208,48,24
Cv 147,150,151
Cv 152,152,153
Mv 208,112,88
Cv 153,154,155
Cv 154,156,157
Mv 212,52,24
Cv 155,158,159
Mv 212,116,88
buffer_load_dwordx4 v9, s[16:19], s92 offen offset:1024 lds
Mv 216,48,28
Mv 216,112,92
Mv 220,52,28
Mv 220,116,92
buffer_store_dwordx4 v[72:75], v3, s[20:23], s45 offen offset:1024 sc0 sc1
Mv 224,56,0
Mv 228,60,0
)" R"(
Mv 236,60,4
Mv 232,56,4
buffer_load_dwordx4 v10, s[16:19], s92 offen offset:2048 lds
Mv 240,56,8
Mv 244,60,8
Mv 252,60,12
Mv 248,56,12
s_waitcnt vmcnt(24)
s_barrier
Ma 224,56,16
buffer_store_dwordx4 v[80:83], v3, s[20:23], s45 offen offset:2048 sc0 sc1
Ma 228,60,16
Ma 236,60,20
Ma 232,56,20
Ma 240,56,24
Ma 244,60,24
Ma 252,60,28
Ma 248,56,28
buffer_store_dwordx4 v[88:91], v3, s[20:23], s45 offen offset:3072 sc0 sc1
Mv 224,120,64
Rt 16,20,0
Mv 228,124,64
Rt 18,20,512
Mv 232,120,68
Rt 20,21,0
Mv 236,124,68
Rt 22,21,512
Mv 240,120,72
Rt 24,22,0
Mv 244,124,72
Rt 26,22,512
Mv 248,120,76
Rt 28,23,0
Mv 252,124,76
Rt 30,23,512
Ma 224,120,80
Rt 32,20,4096
Ma 228,124,80
Rt 34,20,4608
Ma 232,120,84
Rt 36,21,4096
Ma 236,124,84
Rt 38,21,4608
Ma 240,120,88
Rt 40,22,4096
Ma 244,124,88
Rt 42,22,4608
Ma 248,120,92
Rt 44,23,4096
Ma 252,124,92
Rt 46,23,4608
s_waitcnt vmcnt(15) lgkmcnt(8)
.LzQA:
s_cmp_eq_u32 s2, 112
s_cbranch_scc1 .LzT2S
.LzT2E:
Mz 32,16,192
Rt 0,20,8192
Cv 224,224,225
Mz 40,16,196
Rt 2,20,8704
Rb 48,28,0
Mz 44,20,196
Rt 4,21,8192
buffer_store_dwordx4 v[96:99], v2, s[20:23], s46 offen offset:0 sc0 sc1
)" R"(
Mz 36,20,192
Rt 6,21,8704
Rb 52,28,2048
Mz 48,24,192
Rt 8,22,8192
Cv 225,226,227
Mz 56,24,196
Rt 10,22,8704
Rb 56,29,0
buffer_load_dwordx4 a[64:67], v2, s[20:23], s51 offen offset:0 sc0 sc1
Mz 60,28,196
Rt 12,23,8192
Cv 226,228,229
Mz 52,28,192
Rt 14,23,8704
Rb 60,29,2048
s_waitcnt lgkmcnt(12)
Mv 32,32,200
Rt 16,20,12288
Cv 227,230,231
Mv 40,32,204
Rt 18,20,12800
Cv 232,232,233
Mv 44,36,204
Rt 20,21,12288
buffer_load_dwordx4 a[68:71], v2, s[20:23], s51 offen offset:1024 sc0 sc1
Mv 36,36,200
Rt 22,21,12800
Cv 233,234,235
Mv 48,40,200
Rt 24,22,12288
buffer_store_dwordx4 v[104:107], v2, s[20:23], s46 offen offset:1024 sc0 sc1
Mv 56,40,204
Rt 26,22,12800
Cv 234,236,237
Mv 60,44,204
Rt 28,23,12288
Cv 235,238,239
Mv 52,44,200
Rt 30,23,12800
buffer_load_dwordx4 a[72:75], v2, s[20:23], s51 offen offset:2048 sc0 sc1
s_waitcnt lgkmcnt(9)
Mv 32,0,208
Rt 32,20,16384
Cv 240,240,241
Mv 40,0,212
Rt 34,20,16896
Cv 241,242,243
Mv 44,4,212
Rt 36,21,16384
Cv 242,244,245
Mv 36,4,208
Rt 38,21,16896
Cv 243,246,247
Mv 48,8,208
Rt 40,22,16384
buffer_load_dwordx4 a[76:79], v2, s[20:23], s51 offen offset:3072 sc0 sc1
Mv 56,8,212
Rt 42,22,16896
Cv 248,248,249
Cv 249,250,251
Mv 60,12,212
Rt 44,23,16384
buffer_store_dwordx4 v[112:115], v2, s[20:23], s46 offen offset:2048 sc0 sc1
)" R"(
Mv 52,12,208
Rt 46,23,16896
s_waitcnt lgkmcnt(8)
Mv 32,16,216
Rt 0,20,20480
Cv 250,252,253
Mv 40,16,220
Rt 2,20,20992
buffer_load_dwordx4 a[80:83], v3, s[20:23], s51 offen offset:0 sc0 sc1
Mv 44,20,220
Rt 4,21,20480
Cv 251,254,255
v_accvgpr_read_b32 v228, a224
Mv 36,20,216
Rt 6,21,20992
v_accvgpr_read_b32 v24, a225
Mv 48,24,216
Rt 8,22,20480
Cv 228,228,24
Mv 56,24,220
Rt 10,22,20992
v_accvgpr_read_b32 v229, a226
Mv 60,28,220
Rt 12,23,20480
buffer_load_dwordx4 a[84:87], v3, s[20:23], s51 offen offset:1024 sc0 sc1
Mv 52,28,216
Rt 14,23,20992
v_accvgpr_read_b32 v25, a227
s_waitcnt lgkmcnt(8)
Mv 32,32,128
Rt 16,20,24576
buffer_store_dwordx4 v[120:123], v2, s[20:23], s46 offen offset:3072 sc0 sc1
Mv 40,32,132
Rt 18,20,25088
Cv 229,229,25
Mv 44,36,132
Rt 20,21,24576
v_accvgpr_read_b32 v230, a228
Mv 36,36,128
Rt 22,21,25088
buffer_load_dwordx4 a[88:91], v3, s[20:23], s51 offen offset:2048 sc0 sc1
Mv 48,40,128
Rt 24,22,24576
v_accvgpr_read_b32 v24, a229
Mv 56,40,132
Rt 26,22,25088
Cv 230,230,24
v_accvgpr_read_b32 v231, a230
Mv 60,44,132
Rt 28,23,24576
v_accvgpr_read_b32 v25, a231
Mv 52,44,128
Rt 30,23,25088
s_waitcnt lgkmcnt(8)
Mv 32,0,136
Rt 32,12,0
buffer_load_dwordx4 a[92:95], v3, s[20:23], s51 offen offset:3072 sc0 sc1
Mv 40,0,140
Rt 34,12,512
Cv 231,231,25
Mv 44,4,140
Rt 36,13,0
buffer_store_dwordx4 v[128:131], v3, s[20:23], s46 offen offset:0 sc0 sc1
Mv 36,4,136
)" R"(
Rt 38,13,512
v_accvgpr_read_b32 v236, a232
Mv 48,8,136
Rt 40,14,0
v_accvgpr_read_b32 v24, a233
Mv 56,8,140
Rt 42,14,512
Cv 236,236,24
Mv 60,12,140
Rt 44,15,0
v_accvgpr_read_b32 v237, a234
Mv 52,12,136
Rt 46,15,512
s_waitcnt vmcnt(13) lgkmcnt(8)
s_barrier
buffer_store_dword v6, v7, s[20:23], s49 offen offset:0 sc0 sc1
Mv 32,16,144
Rt 0,12,4096
v_accvgpr_read_b32 v25, a235
Mv 40,16,148
Rt 2,12,4608
s_add_u32 s92, s42, 0x100
Mv 44,20,148
Rt 4,13,4096
s_add_u32 m0, s41, 98304
Mv 36,20,144
Rt 6,13,4608
buffer_load_dwordx4 v8, s[16:19], s92 offen offset:0 lds
Mv 48,24,144
Rt 8,14,4096
Cv 237,237,25
Mv 56,24,148
Rt 10,14,4608
v_accvgpr_read_b32 v238, a236
v_accvgpr_read_b32 v24, a237
Mv 60,28,148
Rt 12,15,4096
Cv 238,238,24
Mv 52,28,144
Rt 14,15,4608
buffer_load_dwordx4 v9, s[16:19], s92 offen offset:1024 lds
s_waitcnt lgkmcnt(8)
Mv 32,32,152
Rt 16,12,8192
v_accvgpr_read_b32 v239, a238
Mv 40,32,156
Rt 18,12,8704
buffer_store_dwordx4 v[136:139], v3, s[20:23], s46 offen offset:1024 sc0 sc1
Mv 44,36,156
Rt 20,13,8192
v_accvgpr_read_b32 v25, a239
Cv 239,239,25
Mv 36,36,152
Rt 22,13,8704
buffer_load_dwordx4 v10, s[16:19], s92 offen offset:2048 lds
Mv 48,40,152
Rt 24,14,8192
v_accvgpr_read_b32 v244, a240
Mv 56,40,156
Rt 26,14,8704
v_accvgpr_read_b32 v24, a241
Mv 60,44,156
Rt 28,15,8192
Cv 244,244,24
Mv 52,44,152
)" R"(
Rt 30,15,8704
buffer_load_dwordx4 v11, s[16:19], s92 offen offset:3072 lds
s_waitcnt lgkmcnt(8)
Mv 32,0,160
Rt 32,12,12288
v_accvgpr_read_b32 v245, a242
Mv 40,0,164
Rt 34,12,12800
s_add_u32 s92, s42, 0x40100
Mv 44,4,164
Rt 36,13,12288
s_add_u32 m0, s41, 102400
Mv 36,4,160
Rt 38,13,12800
buffer_load_dwordx4 v8, s[16:19], s92 offen offset:0 lds
Mv 48,8,160
Rt 40,14,12288
v_accvgpr_read_b32 v25, a243
Mv 56,8,164
Rt 42,14,12800
Cv 245,245,25
v_accvgpr_read_b32 v246, a244
Mv 60,12,164
Rt 44,15,12288
v_accvgpr_read_b32 v24, a245
Mv 52,12,160
Rt 46,15,12800
buffer_load_dwordx4 v9, s[16:19], s92 offen offset:1024 lds
s_waitcnt lgkmcnt(8)
Mv 32,16,168
Rt 0,12,16384
Cv 246,246,24
Mv 40,16,172
Rt 2,12,16896
buffer_store_dwordx4 v[144:147], v3, s[20:23], s46 offen offset:2048 sc0 sc1
Mv 44,20,172
Rt 4,13,16384
v_accvgpr_read_b32 v247, a246
v_accvgpr_read_b32 v25, a247
Mv 36,20,168
Rt 6,13,16896
buffer_load_dwordx4 v10, s[16:19], s92 offen offset:2048 lds
Mv 48,24,168
Rt 8,14,16384
Cv 247,247,25
Mv 56,24,172
Rt 10,14,16896
s_add_u32 s92, s42, 0x1c0100
Mv 60,28,172
Rt 12,15,16384
s_add_u32 m0, s41, 131072
Mv 52,28,168
Rt 14,15,16896
buffer_load_dwordx4 v8, s[16:19], s92 offen offset:0 lds
s_waitcnt lgkmcnt(8)
Mv 32,32,176
Rt 16,12,20480
v_accvgpr_read_b32 v252, a248
Mv 40,32,180
Rt 18,12,20992
v_accvgpr_read_b32 v24, a249
Mv 44,36,180
Rt 20,13,20480
Cv 252,252,24
)" R"(
Mv 36,36,176
Rt 22,13,20992
buffer_load_dwordx4 v9, s[16:19], s92 offen offset:1024 lds
Mv 48,40,176
Rt 24,14,20480
v_accvgpr_read_b32 v253, a250
Mv 56,40,180
Rt 26,14,20992
v_accvgpr_read_b32 v25, a251
Cv 253,253,25
Mv 60,44,180
Rt 28,15,20480
v_accvgpr_read_b32 v254, a252
Mv 52,44,176
Rt 30,15,20992
buffer_load_dwordx4 v10, s[16:19], s92 offen offset:2048 lds
s_waitcnt lgkmcnt(8)
Mv 32,0,184
Rt 32,12,24576
v_accvgpr_read_b32 v24, a253
Mv 40,0,188
Rt 34,12,25088
buffer_store_dwordx4 v[152:155], v3, s[20:23], s46 offen offset:3072 sc0 sc1
Mv 44,4,188
Rt 36,13,24576
Cv 254,254,24
v_accvgpr_read_b32 v255, a254
Mv 36,4,184
Rt 38,13,25088
buffer_load_dwordx4 v11, s[16:19], s92 offen offset:3072 lds
Mv 48,8,184
Rt 40,14,24576
v_accvgpr_read_b32 v25, a255
Mv 56,8,188
Rt 42,14,25088
s_add_u32 s92, s42, 0x200100
Mv 60,12,188
Rt 44,15,24576
s_add_u32 m0, s41, 135168
Mv 52,12,184
Rt 46,15,25088
buffer_load_dwordx4 v8, s[16:19], s92 offen offset:0 lds
s_waitcnt lgkmcnt(8)
Mv 32,16,48
Rt 0,16,0
Cv 255,255,25
Mv 40,16,52
Rt 2,16,512
Cv 160,160,161
Mv 44,20,52
Rt 4,17,0
Cv 161,162,163
Mv 36,20,48
Rt 6,17,512
buffer_load_dwordx4 v9, s[16:19], s92 offen offset:1024 lds
Mv 48,24,48
Rt 8,18,0
Cv 162,164,165
Mv 56,24,52
Rt 10,18,512
Cv 163,166,167
Cv 168,168,169
Mv 60,28,52
Rt 12,19,0
)" R"(
Cv 169,170,171
Mv 52,28,48
Rt 14,19,512
buffer_load_dwordx4 v10, s[16:19], s92 offen offset:2048 lds
s_waitcnt lgkmcnt(8)
s_barrier
Mv 32,32,56
Rt 16,16,4096
Cv 170,172,173
Mv 40,32,60
Rt 18,16,4608
buffer_store_dwordx4 v[160:163], v2, s[20:23], s47 offen offset:0 sc0 sc1
Mv 44,36,60
Rt 20,17,4096
Cv 171,174,175
Cv 176,176,177
Mv 36,36,56
Rt 22,17,4608
s_add_u32 s92, s42, 0x180
Mv 48,40,56
Rt 24,18,4096
s_add_u32 m0, s41, 0
Mv 56,40,60
Rt 26,18,4608
buffer_load_dwordx4 v8, s[16:19], s92 offen offset:0 lds
Mv 60,44,60
Rt 28,19,4096
Cv 177,178,179
Mv 52,44,56
Rt 30,19,4608
Cv 178,180,181
s_waitcnt lgkmcnt(8)
Mz 64,0,192
Rt 32,16,8192
Cv 179,182,183
Mz 72,0,196
Rt 34,16,8704
s_add_u32 s51, s51, 0x10000
buffer_load_dwordx4 a[96:99], v2, s[20:23], s51 offen offset:0 sc0 sc1
Mz 76,4,196
Rt 36,17,8192
Cv 184,184,185
Mz 68,4,192
Rt 38,17,8704
Cv 185,186,187
Mz 80,8,192
Rt 40,18,8192
Cv 186,188,189
Mz 88,8,196
Rt 42,18,8704
buffer_load_dwordx4 v9, s[16:19], s92 offen offset:1024 lds
Mz 92,12,196
Rt 44,19,8192
Cv 187,190,191
v_mul_f32_e32 v32, s54, v32
Mz 84,12,192
Rt 46,19,8704
s_waitcnt lgkmcnt(8)
Mv 64,16,200
Rt 0,16,12288
Cv 192,192,193
Mv 72,16,204
Rt 2,16,12800
buffer_store_dwordx4 v[168:171], v2, s[20:23], s47 offen offset:1024 sc0 sc1
Mv 76,20,204
)" R"(
Rt 4,17,12288
Cv 193,194,195
Cv 194,196,197
Mv 68,20,200
Rt 6,17,12800
Cv 195,198,199
v_mul_f32_e32 v33, s54, v33
Mv 80,24,200
Rt 8,18,12288
Cv 200,200,201
Mv 88,24,204
Rt 10,18,12800
buffer_load_dwordx4 v10, s[16:19], s92 offen offset:2048 lds
Mv 92,28,204
Rt 12,19,12288
Cv 201,202,203
Cv 32,32,33
Mv 84,28,200
Rt 14,19,12800
Cv 202,204,205
s_waitcnt lgkmcnt(8)
Mv 64,32,208
Rt 16,16,16384
Cv 203,206,207
Mv 72,32,212
Rt 18,16,16896
buffer_load_dwordx4 a[100:103], v2, s[20:23], s51 offen offset:1024 sc0 sc1
Mv 76,36,212
Rt 20,17,16384
Cv 208,208,209
Mv 68,36,208
Rt 22,17,16896
Cv 209,210,211
v_accvgpr_write_b32 a224, v32
Mv 80,40,208
Rt 24,18,16384
Cv 210,212,213
Mv 88,40,212
Rt 26,18,16896
buffer_load_dwordx4 v11, s[16:19], s92 offen offset:3072 lds
Mv 92,44,212
Rt 28,19,16384
Cv 211,214,215
v_mul_f32_e32 v34, s54, v34
Mv 84,44,208
Rt 30,19,16896
s_waitcnt lgkmcnt(8)
Mv 64,0,216
Rt 32,16,20480
Cv 216,216,217
Mv 72,0,220
Rt 34,16,20992
buffer_store_dwordx4 v[176:179], v2, s[20:23], s47 offen offset:2048 sc0 sc1
Mv 76,4,220
Rt 36,17,20480
Cv 217,218,219
Cv 218,220,221
Mv 68,4,216
Rt 38,17,20992
s_add_u32 s92, s42, 0x40180
v_mul_f32_e32 v35, s54, v35
Mv 80,8,216
Rt 40,18,20480
s_add_u32 m0, s41, 4096
)" R"(
Mv 88,8,220
Rt 42,18,20992
buffer_load_dwordx4 v8, s[16:19], s92 offen offset:0 lds
Mv 92,12,220
Rt 44,19,20480
Cv 219,222,223
Cv 34,34,35
Mv 84,12,216
Rt 46,19,20992
s_waitcnt lgkmcnt(8)
Mv 64,16,128
Rt 0,16,24576
v_accvgpr_write_b32 a225, v34
Mv 72,16,132
Rt 2,16,25088
buffer_load_dwordx4 a[104:107], v2, s[20:23], s51 offen offset:2048 sc0 sc1
Mv 76,20,132
Rt 4,17,24576
v_mul_f32_e32 v36, s54, v36
Mv 68,20,128
Rt 6,17,25088
v_mul_f32_e32 v37, s54, v37
Cv 36,36,37
Mv 80,24,128
Rt 8,18,24576
v_accvgpr_write_b32 a226, v36
Mv 88,24,132
Rt 10,18,25088
buffer_load_dwordx4 v9, s[16:19], s92 offen offset:1024 lds
Mv 92,28,132
Rt 12,19,24576
v_mul_f32_e32 v38, s54, v38
v_mul_f32_e32 v39, s54, v39
Mv 84,28,128
Rt 14,19,25088
s_waitcnt lgkmcnt(8)
Mv 64,32,136
Rt 16,12,32768
buffer_store_dwordx4 v[184:187], v2, s[20:23], s47 offen offset:3072 sc0 sc1
Mv 72,32,140
Rt 18,12,33280
Cv 38,38,39
v_accvgpr_write_b32 a227, v38
Mv 76,36,140
Rt 20,13,32768
v_mul_f32_e32 v40, s54, v40
Mv 68,36,136
Rt 22,13,33280
buffer_load_dwordx4 a[108:111], v2, s[20:23], s51 offen offset:3072 sc0 sc1
Mv 80,40,136
Rt 24,14,32768
v_mul_f32_e32 v41, s54, v41
Mv 88,40,140
Rt 26,14,33280
buffer_load_dwordx4 v10, s[16:19], s92 offen offset:2048 lds
Mv 92,44,140
Rt 28,15,32768
Cv 40,40,41
v_accvgpr_write_b32 a228, v40
Mv 84,44,136
Rt 30,15,33280
s_waitcnt vmcnt(19) lgkmcnt(8)
s_barrier
buffer_store_dword v6, v7, s[20:23], s49 offen offset:4 sc0 sc1
Mv 64,0,144
)" R"(
Rt 32,12,36864
buffer_load_dword v4, v5, s[20:23], s50 offen offset:0 sc0 sc1
Mv 72,0,148
Rt 34,12,37376
v_mul_f32_e32 v42, s54, v42
v_mul_f32_e32 v43, s54, v43
Mv 76,4,148
Rt 36,13,36864
Cv 42,42,43
Mv 68,4,144
Rt 38,13,37376
s_add_u32 s92, s42, 0x1c0180
v_accvgpr_write_b32 a229, v42
Mv 80,8,144
Rt 40,14,36864
s_add_u32 m0, s41, 65536
Mv 88,8,148
Rt 42,14,37376
buffer_load_dwordx4 v8, s[16:19], s92 offen offset:0 lds
Mv 92,12,148
Rt 44,15,36864
v_mul_f32_e32 v44, s54, v44
v_mul_f32_e32 v45, s54, v45
Mv 84,12,144
Rt 46,15,37376
s_waitcnt lgkmcnt(8)
Mv 64,16,152
Rt 0,12,40960
Cv 44,44,45
Mv 72,16,156
Rt 2,12,41472
buffer_load_dwordx4 a[112:115], v3, s[20:23], s51 offen offset:0 sc0 sc1
Mv 76,20,156
Rt 4,13,40960
v_accvgpr_write_b32 a230, v44
Mv 68,20,152
Rt 6,13,41472
v_mul_f32_e32 v46, s54, v46
v_mul_f32_e32 v47, s54, v47
Mv 80,24,152
Rt 8,14,40960
Cv 46,46,47
Mv 88,24,156
Rt 10,14,41472
buffer_load_dwordx4 v9, s[16:19], s92 offen offset:1024 lds
Mv 92,28,156
Rt 12,15,40960
v_accvgpr_write_b32 a231, v46
v_mul_f32_e32 v48, s54, v48
Mv 84,28,152
Rt 14,15,41472
s_waitcnt lgkmcnt(8)
Mv 64,32,160
Rt 16,12,45056
v_mul_f32_e32 v49, s54, v49
Mv 72,32,164
Rt 18,12,45568
buffer_store_dwordx4 v[192:195], v3, s[20:23], s47 offen offset:0 sc0 sc1
Mv 76,36,164
Rt 20,13,45056
Cv 48,48,49
Mv 68,36,160
Rt 22,13,45568
v_accvgpr_write_b32 a232, v48
)" R"(
v_mul_f32_e32 v50, s54, v50
Mv 80,40,160
Rt 24,14,45056
v_mul_f32_e32 v51, s54, v51
Mv 88,40,164
Rt 26,14,45568
buffer_load_dwordx4 v10, s[16:19], s92 offen offset:2048 lds
Mv 92,44,164
Rt 28,15,45056
Cv 50,50,51
v_accvgpr_write_b32 a233, v50
Mv 84,44,160
Rt 30,15,45568
s_waitcnt lgkmcnt(8)
Mv 64,0,168
Rt 32,12,49152
v_mul_f32_e32 v52, s54, v52
Mv 72,0,172
Rt 34,12,49664
buffer_load_dwordx4 a[116:119], v3, s[20:23], s51 offen offset:1024 sc0 sc1
Mv 76,4,172
Rt 36,13,49152
v_mul_f32_e32 v53, s54, v53
Mv 68,4,168
Rt 38,13,49664
Cv 52,52,53
v_accvgpr_write_b32 a234, v52
Mv 80,8,168
Rt 40,14,49152
v_mul_f32_e32 v54, s54, v54
Mv 88,8,172
Rt 42,14,49664
buffer_load_dwordx4 v11, s[16:19], s92 offen offset:3072 lds
Mv 92,12,172
Rt 44,15,49152
v_mul_f32_e32 v55, s54, v55
Cv 54,54,55
Mv 84,12,168
Rt 46,15,49664
s_waitcnt lgkmcnt(8)
Mv 64,16,176
Rt 0,12,53248
v_accvgpr_write_b32 a235, v54
Mv 72,16,180
Rt 2,12,53760
buffer_store_dwordx4 v[200:203], v3, s[20:23], s47 offen offset:1024 sc0 sc1
Mv 76,20,180
Rt 4,13,53248
v_mul_f32_e32 v56, s54, v56
Mv 68,20,176
Rt 6,13,53760
s_add_u32 s92, s42, 0x200180
v_mul_f32_e32 v57, s54, v57
Mv 80,24,176
Rt 8,14,53248
s_add_u32 m0, s41, 69632
Mv 88,24,180
Rt 10,14,53760
buffer_load_dwordx4 v8, s[16:19], s92 offen offset:0 lds
Mv 92,28,180
Rt 12,15,53248
Cv 56,56,57
v_accvgpr_write_b32 a236, v56
Mv 84,28,176
)" R"(
Rt 14,15,53760
s_waitcnt lgkmcnt(8)
Mv 64,32,184
Rt 16,12,57344
v_mul_f32_e32 v58, s54, v58
Mv 72,32,188
Rt 18,12,57856
buffer_load_dwordx4 a[120:123], v3, s[20:23], s51 offen offset:2048 sc0 sc1
Mv 76,36,188
Rt 20,13,57344
v_mul_f32_e32 v59, s54, v59
Mv 68,36,184
Rt 22,13,57856
Cv 58,58,59
v_accvgpr_write_b32 a237, v58
Mv 80,40,184
Rt 24,14,57344
v_mul_f32_e32 v60, s54, v60
Mv 88,40,188
Rt 26,14,57856
buffer_load_dwordx4 v9, s[16:19], s92 offen offset:1024 lds
Mv 92,44,188
Rt 28,15,57344
v_mul_f32_e32 v61, s54, v61
Cv 60,60,61
Mv 84,44,184
Rt 30,15,57856
s_waitcnt lgkmcnt(8)
Mv 64,0,48
Rt 32,16,32768
buffer_store_dwordx4 v[208:211], v3, s[20:23], s47 offen offset:2048 sc0 sc1
Mv 72,0,52
Rt 34,16,33280
v_accvgpr_write_b32 a238, v60
v_mul_f32_e32 v62, s54, v62
Mv 76,4,52
Rt 36,17,32768
v_mul_f32_e32 v63, s54, v63
Mv 68,4,48
Rt 38,17,33280
buffer_load_dwordx4 a[124:127], v3, s[20:23], s51 offen offset:3072 sc0 sc1
Mv 80,8,48
Rt 40,18,32768
Cv 62,62,63
Mv 88,8,52
Rt 42,18,33280
buffer_load_dwordx4 v10, s[16:19], s92 offen offset:2048 lds
Mv 92,12,52
Rt 44,19,32768
v_accvgpr_write_b32 a239, v62
Mv 84,12,48
Rt 46,19,33280
s_waitcnt vmcnt(14) lgkmcnt(8)
s_barrier
Mv 64,16,56
Rt 0,16,36864
v_cmp_eq_u32_e32 vcc, s3, v4
s_andn2_b64 s[28:29], s[60:61], vcc
s_cbranch_scc1 .Lzsp5
.Lzok5:
Mv 72,16,60
Rt 2,16,37376
s_add_u32 s40, s55, 0x10000
Mv 76,20,60
Rt 4,17,36864
)" R"(
Mv 68,20,56
Rt 6,17,37376
Mv 80,24,56
Rt 8,18,36864
s_add_u32 m0, s36, 32768
Mv 88,24,60
Rt 10,18,37376
buffer_load_dwordx4 v2, s[20:23], s40 offen offset:0 sc1 lds
Mv 92,28,60
Rt 12,19,36864
Mv 84,28,56
Rt 14,19,37376
s_waitcnt lgkmcnt(8)
Mz 32,32,192
Rt 16,16,40960
buffer_store_dwordx4 v[216:219], v3, s[20:23], s47 offen offset:3072 sc0 sc1
Mz 40,32,196
Rt 18,16,41472
Mz 44,36,196
Rt 20,17,40960
Mz 36,36,192
Rt 22,17,41472
buffer_load_dwordx4 v2, s[20:23], s40 offen offset:1024 sc1 lds
Mz 48,40,192
Rt 24,18,40960
v_mul_f32_e32 v64, s54, v64
Mz 56,40,196
Rt 26,18,41472
v_mul_f32_e32 v65, s54, v65
Mz 60,44,196
Rt 28,19,40960
Cv 64,64,65
Mz 52,44,192
Rt 30,19,41472
s_waitcnt lgkmcnt(8)
Mv 32,0,200
Rt 32,16,45056
v_accvgpr_write_b32 a240, v64
Mv 40,0,204
Rt 34,16,45568
buffer_load_dwordx4 v2, s[20:23], s40 offen offset:2048 sc1 lds
Mv 44,4,204
Rt 36,17,45056
v_mul_f32_e32 v66, s54, v66
Mv 36,4,200
Rt 38,17,45568
v_mul_f32_e32 v67, s54, v67
Mv 48,8,200
Rt 40,18,45056
buffer_store_dwordx4 v[224:227], v2, s[20:23], s48 offen offset:0 sc0 sc1
Mv 56,8,204
Rt 42,18,45568
Cv 66,66,67
Mv 60,12,204
Rt 44,19,45056
v_accvgpr_write_b32 a241, v66
Mv 52,12,200
Rt 46,19,45568
buffer_load_dwordx4 v2, s[20:23], s40 offen offset:3072 sc1 lds
s_waitcnt lgkmcnt(8)
Mv 32,16,208
Rt 0,16,49152
v_mul_f32_e32 v68, s54, v68
Mv 40,16,212
)" R"(
Rt 2,16,49664
v_mul_f32_e32 v69, s54, v69
Mv 44,20,212
Rt 4,17,49152
Cv 68,68,69
Mv 36,20,208
Rt 6,17,49664
v_accvgpr_write_b32 a242, v68
Mv 48,24,208
Rt 8,18,49152
s_add_u32 m0, s36, 36864
Mv 56,24,212
Rt 10,18,49664
buffer_load_dwordx4 v3, s[20:23], s40 offen offset:0 sc1 lds
Mv 60,28,212
Rt 12,19,49152
v_mul_f32_e32 v70, s54, v70
Mv 52,28,208
Rt 14,19,49664
s_waitcnt lgkmcnt(8)
Mv 32,32,216
Rt 16,16,53248
buffer_store_dwordx4 v[232:235], v2, s[20:23], s48 offen offset:1024 sc0 sc1
Mv 40,32,220
Rt 18,16,53760
v_mul_f32_e32 v71, s54, v71
Mv 44,36,220
Rt 20,17,53248
Cv 70,70,71
Mv 36,36,216
Rt 22,17,53760
buffer_load_dwordx4 v3, s[20:23], s40 offen offset:1024 sc1 lds
Mv 48,40,216
Rt 24,18,53248
v_accvgpr_write_b32 a243, v70
Mv 56,40,220
Rt 26,18,53760
v_mul_f32_e32 v72, s54, v72
Mv 60,44,220
Rt 28,19,53248
v_mul_f32_e32 v73, s54, v73
Mv 52,44,216
Rt 30,19,53760
s_waitcnt lgkmcnt(8)
Mv 32,0,128
Rt 32,16,57344
Cv 72,72,73
Mv 40,0,132
Rt 34,16,57856
buffer_load_dwordx4 v3, s[20:23], s40 offen offset:2048 sc1 lds
Mv 44,4,132
Rt 36,17,57344
v_accvgpr_write_b32 a244, v72
Mv 36,4,128
Rt 38,17,57856
v_mul_f32_e32 v74, s54, v74
Mv 48,8,128
Rt 40,18,57344
buffer_store_dwordx4 v[240:243], v2, s[20:23], s48 offen offset:2048 sc0 sc1
Mv 56,8,132
Rt 42,18,57856
v_mul_f32_e32 v75, s54, v75
Mv 60,12,132
Rt 44,19,57344
)" R"(
Cv 74,74,75
Mv 52,12,128
Rt 46,19,57856
buffer_load_dwordx4 v3, s[20:23], s40 offen offset:3072 sc1 lds
s_waitcnt lgkmcnt(8)
Mv 32,16,136
Rt 0,20,0
v_accvgpr_write_b32 a245, v74
Mv 40,16,140
Rt 2,20,512
v_mul_f32_e32 v76, s54, v76
Mv 44,20,140
Rt 4,21,0
v_mul_f32_e32 v77, s54, v77
Mv 36,20,136
Rt 6,21,512
Cv 76,76,77
Mv 48,24,136
Rt 8,22,0
v_accvgpr_write_b32 a246, v76
Mv 56,24,140
Rt 10,22,512
v_mul_f32_e32 v78, s54, v78
Mv 60,28,140
Rt 12,23,0
v_mul_f32_e32 v79, s54, v79
Mv 52,28,136
Rt 14,23,512
s_waitcnt vmcnt(10) lgkmcnt(8)
s_barrier
buffer_store_dword v6, v7, s[20:23], s49 offen offset:8 sc0 sc1
Mv 32,32,144
Rt 16,20,4096
s_mov_b32 s83, 0xc108000
buffer_load_dwordx2 v[24:25], v7, s[20:23], s83 offen sc0 sc1
Mv 40,32,148
Rt 18,20,4608
Cv 78,78,79
Mv 44,36,148
Rt 20,21,4096
s_add_u32 m0, s36, 98304
Mv 36,36,144
Rt 22,21,4608
buffer_load_dwordx4 v2, s[20:23], s55 offen offset:0 sc1 lds
Mv 48,40,144
Rt 24,22,4096
v_accvgpr_write_b32 a247, v78
Mv 56,40,148
Rt 26,22,4608
v_mul_f32_e32 v80, s54, v80
Mv 60,44,148
Rt 28,23,4096
buffer_store_dwordx4 v[248:251], v2, s[20:23], s48 offen offset:3072 sc0 sc1
Mv 52,44,144
Rt 30,23,4608
s_waitcnt lgkmcnt(8)
Mv 32,0,152
Rt 32,20,8192
v_mul_f32_e32 v81, s54, v81
Mv 40,0,156
Rt 34,20,8704
buffer_load_dwordx4 v2, s[20:23], s55 offen offset:1024 sc1 lds
Mv 44,4,156
Rt 36,21,8192
Cv 80,80,81
)" R"(
Mv 36,4,152
Rt 38,21,8704
v_accvgpr_write_b32 a248, v80
Mv 48,8,152
Rt 40,22,8192
v_mul_f32_e32 v82, s54, v82
Mv 56,8,156
Rt 42,22,8704
v_mul_f32_e32 v83, s54, v83
Mv 60,12,156
Rt 44,23,8192
Cv 82,82,83
Mv 52,12,152
Rt 46,23,8704
buffer_load_dwordx4 v2, s[20:23], s55 offen offset:2048 sc1 lds
s_waitcnt lgkmcnt(8)
Mv 32,16,160
Rt 0,20,12288
v_accvgpr_write_b32 a249, v82
Mv 40,16,164
Rt 2,20,12800
v_mul_f32_e32 v84, s54, v84
Mv 44,20,164
Rt 4,21,12288
buffer_store_dwordx4 v[228:231], v3, s[20:23], s48 offen offset:0 sc0 sc1
Mv 36,20,160
Rt 6,21,12800
v_mul_f32_e32 v85, s54, v85
Mv 48,24,160
Rt 8,22,12288
Cv 84,84,85
Mv 56,24,164
Rt 10,22,12800
buffer_load_dwordx4 v2, s[20:23], s55 offen offset:3072 sc1 lds
Mv 60,28,164
Rt 12,23,12288
v_accvgpr_write_b32 a250, v84
Mv 52,28,160
Rt 14,23,12800
s_waitcnt lgkmcnt(8)
Mv 32,32,168
Rt 16,20,16384
v_mul_f32_e32 v86, s54, v86
Mv 40,32,172
Rt 18,20,16896
v_mul_f32_e32 v87, s54, v87
Mv 44,36,172
Rt 20,21,16384
s_add_u32 m0, s36, 102400
Mv 36,36,168
Rt 22,21,16896
buffer_load_dwordx4 v3, s[20:23], s55 offen offset:0 sc1 lds
Mv 48,40,168
Rt 24,22,16384
Cv 86,86,87
Mv 56,40,172
Rt 26,22,16896
v_accvgpr_write_b32 a251, v86
Mv 60,44,172
Rt 28,23,16384
buffer_store_dwordx4 v[236:239], v3, s[20:23], s48 offen offset:1024 sc0 sc1
Mv 52,44,168
Rt 30,23,16896
s_waitcnt lgkmcnt(8)
)" R"(
Mv 32,0,176
Rt 32,20,20480
v_mul_f32_e32 v88, s54, v88
Mv 40,0,180
Rt 34,20,20992
buffer_load_dwordx4 v3, s[20:23], s55 offen offset:1024 sc1 lds
Mv 44,4,180
Rt 36,21,20480
v_mul_f32_e32 v89, s54, v89
Mv 36,4,176
Rt 38,21,20992
Cv 88,88,89
Mv 48,8,176
Rt 40,22,20480
v_accvgpr_write_b32 a252, v88
Mv 56,8,180
Rt 42,22,20992
v_mul_f32_e32 v90, s54, v90
Mv 60,12,180
Rt 44,23,20480
v_mul_f32_e32 v91, s54, v91
Mv 52,12,176
Rt 46,23,20992
buffer_load_dwordx4 v3, s[20:23], s55 offen offset:2048 sc1 lds
s_waitcnt lgkmcnt(8)
Mv 32,16,184
Rt 0,20,24576
Cv 90,90,91
Mv 40,16,188
Rt 2,20,25088
v_accvgpr_write_b32 a253, v90
Mv 44,20,188
Rt 4,21,24576
buffer_store_dwordx4 v[244:247], v3, s[20:23], s48 offen offset:2048 sc0 sc1
Mv 36,20,184
Rt 6,21,25088
v_mul_f32_e32 v92, s54, v92
Mv 48,24,184
Rt 8,22,24576
v_mul_f32_e32 v93, s54, v93
Mv 56,24,188
Rt 10,22,25088
buffer_load_dwordx4 v3, s[20:23], s55 offen offset:3072 sc1 lds
Mv 60,28,188
Rt 12,23,24576
Cv 92,92,93
Mv 52,28,184
Rt 14,23,25088
s_waitcnt lgkmcnt(8)
Mv 32,32,48
Rt 16,12,0
v_accvgpr_write_b32 a254, v92
Mv 40,32,52
Rt 18,12,512
v_mul_f32_e32 v94, s54, v94
Mv 44,36,52
Rt 20,13,0
v_mul_f32_e32 v95, s54, v95
Mv 36,36,48
Rt 22,13,512
Cv 94,94,95
Mv 48,40,48
Rt 24,14,0
buffer_store_dwordx4 v[252:255], v3, s[20:23], s48 offen offset:3072 sc0 sc1
)" R"(
Mv 56,40,52
Rt 26,14,512
v_accvgpr_write_b32 a255, v94
Mv 60,44,52
Rt 28,15,0
Mv 52,44,48
Rt 30,15,512
s_waitcnt vmcnt(13) lgkmcnt(8)
s_barrier
v_cmp_eq_u32_e32 vcc, s3, v25
s_andn2_b64 s[28:29], 1, vcc
s_cbranch_scc1 .Lzsp7
.Lzok7:
Mv 32,0,56
Rt 32,12,4096
buffer_load_dword v4, v5, s[20:23], s50 offen offset:0 sc0 sc1
Mv 40,0,60
Rt 34,12,4608
s_lshl_b64 s[60:61], s[60:61], 8
Mv 44,4,60
Rt 36,13,4096
s_add_u32 m0, s36, 131072
Mv 36,4,56
Rt 38,13,4608
buffer_load_dwordx4 v2, s[20:23], s56 offen offset:0 sc1 lds
Mv 48,8,56
Rt 40,14,4096
s_or_b32 s60, s60, 0xff
Mv 56,8,60
Rt 42,14,4608
Mv 60,12,60
Rt 44,15,4096
Mv 52,12,56
Rt 46,15,4608
s_waitcnt lgkmcnt(8)
Mz 96,16,192
Rt 0,12,8192
Mz 104,16,196
Rt 2,12,8704
buffer_load_dwordx4 v2, s[20:23], s56 offen offset:1024 sc1 lds
Mz 108,20,196
Rt 4,13,8192
Mz 100,20,192
Rt 6,13,8704
Mz 112,24,192
Rt 8,14,8192
v_mul_f32_e32 v32, s54, v32
Mz 120,24,196
Rt 10,14,8704
v_mul_f32_e32 v33, s54, v33
Mz 124,28,196
Rt 12,15,8192
Cv 32,32,33
Mz 116,28,192
Rt 14,15,8704
buffer_load_dwordx4 v2, s[20:23], s56 offen offset:2048 sc1 lds
s_waitcnt lgkmcnt(8)
Mv 96,32,200
Rt 16,12,12288
v_mul_f32_e32 v34, s54, v34
Mv 104,32,204
Rt 18,12,12800
v_accvgpr_write_b32 a192, v32
v_mul_f32_e32 v35, s54, v35
Mv 108,36,204
)" R"(
Rt 20,13,12288
Cv 34,34,35
Mv 100,36,200
Rt 22,13,12800
v_accvgpr_write_b32 a193, v34
Mv 112,40,200
Rt 24,14,12288
v_mul_f32_e32 v36, s54, v36
Mv 120,40,204
Rt 26,14,12800
buffer_load_dwordx4 v2, s[20:23], s56 offen offset:3072 sc1 lds
Mv 124,44,204
Rt 28,15,12288
v_mul_f32_e32 v37, s54, v37
Cv 36,36,37
Mv 116,44,200
Rt 30,15,12800
s_waitcnt lgkmcnt(8)
Mv 96,0,208
Rt 32,12,16384
v_accvgpr_write_b32 a194, v36
Mv 104,0,212
Rt 34,12,16896
v_mul_f32_e32 v38, s54, v38
Mv 108,4,212
Rt 36,13,16384
s_add_u32 m0, s36, 135168
Mv 100,4,208
Rt 38,13,16896
buffer_load_dwordx4 v3, s[20:23], s56 offen offset:0 sc1 lds
Mv 112,8,208
Rt 40,14,16384
v_mul_f32_e32 v39, s54, v39
Mv 120,8,212
Rt 42,14,16896
Cv 38,38,39
Mv 124,12,212
Rt 44,15,16384
v_accvgpr_write_b32 a195, v38
Mv 116,12,208
Rt 46,15,16896
s_waitcnt lgkmcnt(8)
Mv 96,16,216
Rt 0,12,20480
s_and_b32 s87, s2, 7
Mv 104,16,220
Rt 2,12,20992
buffer_load_dwordx4 v3, s[20:23], s56 offen offset:1024 sc1 lds
Mv 108,20,220
Rt 4,13,20480
s_lshl_b32 s87, s87, 7
Mv 100,20,216
Rt 6,13,20992
s_add_u32 s87, s87, 0xc154000
Mv 112,24,216
Rt 8,14,20480
v_min_u32 v26, 13, v0
Mv 120,24,220
Rt 10,14,20992
v_lshlrev_b32 v26, 10, v26
Mv 124,28,220
Rt 12,15,20480
v_mul_f32_e32 v40, s54, v40
v_mul_f32_e32 v41, s54, v41
)" R"(
Mv 116,28,216
Rt 14,15,20992
buffer_load_dwordx4 v3, s[20:23], s56 offen offset:2048 sc1 lds
s_waitcnt lgkmcnt(8)
Mv 96,32,128
Rt 16,12,24576
Cv 40,40,41
Mv 104,32,132
Rt 18,12,25088
v_accvgpr_write_b32 a196, v40
Mv 108,36,132
Rt 20,13,24576
v_mul_f32_e32 v42, s54, v42
Mv 100,36,128
Rt 22,13,25088
v_mul_f32_e32 v43, s54, v43
Cv 42,42,43
Mv 112,40,128
Rt 24,14,24576
v_accvgpr_write_b32 a197, v42
Mv 120,40,132
Rt 26,14,25088
buffer_load_dwordx4 v3, s[20:23], s56 offen offset:3072 sc1 lds
Mv 124,44,132
Rt 28,15,24576
v_mul_f32_e32 v44, s54, v44
Mv 116,44,128
Rt 30,15,25088
s_waitcnt lgkmcnt(8)
Mv 96,0,136
Rt 32,16,0
v_readfirstlane_b32 s63, v24
Mv 104,0,140
Rt 34,16,512
v_and_b32 v24, 16, v1
v_mul_f32_e32 v45, s54, v45
Mv 108,4,140
Rt 36,17,0
v_lshrrev_b32_e64 v24, v24, s63
Mv 100,4,136
Rt 38,17,512
v_and_b32 v24, 0x7ff, v24
v_mul_u32_u24 v24, 896, v24
Mv 112,8,136
Rt 40,18,0
v_and_b32 v25, 14, v0
Mv 120,8,140
Rt 42,18,512
v_min_u32 v25, 1, v25
Mv 124,12,140
Rt 44,19,0
v_lshlrev_b32 v25, 30, v25
v_and_or_b32 v25, v0, 48, v25
Mv 116,12,136
Rt 46,19,512
s_waitcnt vmcnt(8) lgkmcnt(8)
s_barrier
buffer_store_dword v6, v7, s[20:23], s49 offen offset:12 sc0 sc1
Mv 96,16,144
Rt 0,16,4096
v_cmp_eq_u32_e32 vcc, s3, v4
s_andn2_b64 s[28:29], s[60:61], vcc
s_cbranch_scc1 .Lzsp6
.Lzok6:
Mv 104,16,148
)" R"(
Rt 2,16,4608
v_add_u32 v24, v24, v25
v_add_u32 v8, 0x10000, v1
Mv 108,20,148
Rt 4,17,4096
s_and_b32 s83, s33, 1
v_add_u32 v10, 0x20000, v1
Mv 100,20,144
Rt 6,17,4608
s_mul_i32 s82, s83, 448
Cv 44,44,45
Mv 112,24,144
Rt 8,18,4096
v_accvgpr_write_b32 a198, v44
s_add_u32 s56, s56, 0x10000
Mv 120,24,148
Rt 10,18,4608
v_mul_f32_e32 v46, s54, v46
s_add_u32 s55, s55, 0x10000
Mv 124,28,148
Rt 12,19,4096
buffer_load_dword v4, v26, s[20:23], s87 offen offset:0 sc0 sc1
s_add_u32 s55, s55, 0x10000
Mv 116,28,144
Rt 14,19,4608
s_waitcnt lgkmcnt(8)
Mv 96,32,152
Rt 16,16,8192
v_mul_f32_e32 v47, s54, v47
Mv 104,32,156
Rt 18,16,8704
Cv 46,46,47
v_accvgpr_write_b32 a199, v46
Mv 108,36,156
Rt 20,17,8192
v_mul_f32_e32 v48, s54, v48
Mv 100,36,152
Rt 22,17,8704
v_mul_f32_e32 v49, s54, v49
s_cmp_eq_u32 s57, 2
s_cselect_b32 s55, s59, s55
Mv 112,40,152
Rt 24,18,8192
Cv 48,48,49
s_mov_b32 s93, 0
Mv 120,40,156
Rt 26,18,8704
v_accvgpr_write_b32 a200, v48
s_mov_b32 s94, 1
Mv 124,44,156
Rt 28,19,8192
v_mul_f32_e32 v50, s54, v50
v_mul_f32_e32 v51, s54, v51
Mv 116,44,152
Rt 30,19,8704
s_waitcnt lgkmcnt(8)
Mv 96,0,160
Rt 32,16,12288
Cv 50,50,51
Mv 104,0,164
Rt 34,16,12800
v_accvgpr_write_b32 a201, v50
Mv 108,4,164
Rt 36,17,12288
)" R"(
v_mul_f32_e32 v52, s54, v52
Mv 100,4,160
Rt 38,17,12800
buffer_load_dwordx4 a[128:131], v24, s[12:15], s82 offen offset:0
Mv 112,8,160
Rt 40,18,12288
v_mul_f32_e32 v53, s54, v53
Mv 120,8,164
Rt 42,18,12800
Cv 52,52,53
Mv 124,12,164
Rt 44,19,12288
v_accvgpr_write_b32 a202, v52
Mv 116,12,160
Rt 46,19,12800
s_waitcnt lgkmcnt(8)
Mv 96,16,168
Rt 0,16,16384
buffer_load_dwordx4 a[132:135], v24, s[12:15], s82 offen offset:64
Mv 104,16,172
Rt 2,16,16896
v_mul_f32_e32 v54, s54, v54
v_mul_f32_e32 v55, s54, v55
Mv 108,20,172
Rt 4,17,16384
Cv 54,54,55
Mv 100,20,168
Rt 6,17,16896
v_accvgpr_write_b32 a203, v54
Mv 112,24,168
Rt 8,18,16384
v_mul_f32_e32 v56, s54, v56
Mv 120,24,172
Rt 10,18,16896
buffer_load_dwordx4 a[136:139], v24, s[12:15], s82 offen offset:128
Mv 124,28,172
Rt 12,19,16384
v_mul_f32_e32 v57, s54, v57
Cv 56,56,57
Mv 116,28,168
Rt 14,19,16896
s_waitcnt lgkmcnt(8)
Mv 96,32,176
Rt 16,16,20480
buffer_load_dwordx4 a[140:143], v24, s[12:15], s82 offen offset:192
Mv 104,32,180
Rt 18,16,20992
v_accvgpr_write_b32 a204, v56
Mv 108,36,180
Rt 20,17,20480
v_mul_f32_e32 v58, s54, v58
Mv 100,36,176
Rt 22,17,20992
v_mul_f32_e32 v59, s54, v59
Cv 58,58,59
Mv 112,40,176
Rt 24,18,20480
v_accvgpr_write_b32 a205, v58
Mv 120,40,180
Rt 26,18,20992
buffer_load_dwordx4 a[144:147], v24, s[12:15], s82 offen offset:256
Mv 124,44,180
Rt 28,19,20480
v_mul_f32_e32 v60, s54, v60
)" R"(
Mv 116,44,176
Rt 30,19,20992
s_waitcnt lgkmcnt(8)
s_barrier
Mv 96,0,184
Rt 32,16,24576
buffer_load_dwordx4 a[148:151], v24, s[12:15], s82 offen offset:320
Mv 104,0,188
Rt 34,16,25088
v_mul_f32_e32 v61, s54, v61
Cv 60,60,61
Mv 108,4,188
Rt 36,17,24576
v_accvgpr_write_b32 a206, v60
Mv 100,4,184
Rt 38,17,25088
v_mul_f32_e32 v62, s54, v62
Mv 112,8,184
Rt 40,18,24576
v_mul_f32_e32 v63, s54, v63
Mv 120,8,188
Rt 42,18,25088
buffer_load_dwordx4 a[152:155], v24, s[12:15], s82 offen offset:384
Mv 124,12,188
Rt 44,19,24576
Cv 62,62,63
v_accvgpr_write_b32 a207, v62
Mv 116,12,184
Rt 46,19,25088
s_waitcnt lgkmcnt(8)
Mv 96,16,48
Rb 0,8,32768
Mv 104,16,52
Rb 4,8,33792
Mv 108,20,52
Rb 8,8,34816
Mv 100,20,48
Rb 12,8,35840
Mv 112,24,48
Rb 16,8,40960
Rb 20,8,41984
Mv 120,24,52
Rb 24,8,43008
Mv 124,28,52
Mv 116,28,48
Rb 28,8,44032
s_waitcnt vmcnt(7) lgkmcnt(8)
v_cmp_eq_u32_e32 vcc, s3, v4
s_andn2_b64 s[28:29], -1, vcc
s_cbranch_scc1 .Lzsp8
.Lzok8:
Mv 96,32,56
Mv 104,32,60
Rb 32,8,36864
Mv 108,36,60
Mv 100,36,56
Rb 36,8,37888
Mv 112,40,56
Mv 120,40,60
Rb 40,8,38912
Mv 124,44,60
Mv 116,44,56
Rb 44,8,39936
s_cmp_eq_u32 s35, 7
s_cbranch_scc1 .LzA7
)" R"(
.LzA7R:
s_waitcnt lgkmcnt(8)
Mz 64,0,64
Rb 48,8,45056
s_and_b32 s92, s2, 7
Mz 68,0,68
s_mul_i32 s92, s92, 14
s_lshr_b32 s82, s2, 3
Mv 68,4,76
Rb 52,8,46080
s_add_u32 m0, s36, 0
v_mul_f32_e32 v96, s54, v96
Mv 64,4,72
buffer_load_dwordx4 v2, s[20:23], s55 offen offset:0 sc1 lds
Mv 64,8,80
Rb 56,8,47104
s_add_u32 s82, s82, s92
Mv 68,8,84
s_lshl2_add_u32 s83, s82, s33
s_mul_i32 s83, s83, 7168
v_mul_f32_e32 v97, s54, v97
Mv 68,12,92
Rb 60,8,48128
s_waitcnt lgkmcnt(8)
Mv 64,12,88
buffer_load_dwordx4 v2, s[20:23], s55 offen offset:1024 sc1 lds
Cv 96,96,97
Mv 64,16,96
Rb 0,8,49152
s_add_u32 s83, s83, 0xc200000
Mv 68,16,100
v_accvgpr_write_b32 a208, v96
v_mul_f32_e32 v98, s54, v98
Mv 68,20,108
Rb 4,8,50176
v_mul_f32_e32 v99, s54, v99
Cv 98,98,99
Mv 64,20,104
buffer_load_dwordx4 v2, s[20:23], s55 offen offset:2048 sc1 lds
Mv 64,24,112
Rb 8,8,51200
v_accvgpr_write_b32 a209, v98
Mv 68,24,116
v_mul_f32_e32 v100, s54, v100
v_mul_f32_e32 v101, s54, v101
Cv 100,100,101
Mv 68,28,124
Rb 12,8,52224
s_waitcnt lgkmcnt(8)
Mv 64,28,120
buffer_load_dwordx4 v2, s[20:23], s55 offen offset:3072 sc1 lds
v_accvgpr_write_b32 a210, v100
Mz 72,32,64
Rb 16,8,57344
v_mul_f32_e32 v102, s54, v102
Mz 76,32,68
v_mul_f32_e32 v103, s54, v103
Cv 102,102,103
Mv 76,36,76
Rb 20,8,58368
s_add_u32 m0, s36, 4096
v_accvgpr_write_b32 a211, v102
Mv 72,36,72
buffer_load_dwordx4 v3, s[20:23], s55 offen offset:0 sc1 lds
)" R"(
Mv 72,40,80
Rb 24,8,59392
v_mul_f32_e32 v104, s54, v104
Mv 76,40,84
v_mul_f32_e32 v105, s54, v105
Cv 104,104,105
v_accvgpr_write_b32 a212, v104
Mv 76,44,92
Rb 28,8,60416
s_waitcnt lgkmcnt(8)
Mv 72,44,88
buffer_load_dwordx4 v3, s[20:23], s55 offen offset:1024 sc1 lds
v_mul_f32_e32 v106, s54, v106
Mv 72,48,96
Rb 32,8,53248
v_mul_f32_e32 v107, s54, v107
Mv 76,48,100
Cv 106,106,107
v_accvgpr_write_b32 a213, v106
Mv 76,52,108
Rb 36,8,54272
v_mul_f32_e32 v108, s54, v108
v_mul_f32_e32 v109, s54, v109
Mv 72,52,104
buffer_load_dwordx4 v3, s[20:23], s55 offen offset:2048 sc1 lds
Mv 72,56,112
Rb 40,8,55296
Cv 108,108,109
Mv 76,56,116
v_accvgpr_write_b32 a214, v108
v_mul_f32_e32 v110, s54, v110
v_mul_f32_e32 v111, s54, v111
Mv 76,60,124
Rb 44,8,56320
s_waitcnt lgkmcnt(8)
Mv 72,60,120
buffer_load_dwordx4 v3, s[20:23], s55 offen offset:3072 sc1 lds
Cv 110,110,111
Mz 80,0,64
Rb 48,8,61440
v_accvgpr_write_b32 a215, v110
Mz 84,0,68
v_mul_f32_e32 v112, s54, v112
v_mul_f32_e32 v113, s54, v113
Mv 84,4,76
Rb 52,8,62464
Cv 112,112,113
v_accvgpr_write_b32 a216, v112
Mv 80,4,72
buffer_load_dwordx4 a[156:159], v1, s[20:23], s83 offen offset:0 sc0 sc1
Mv 80,8,80
Rb 56,8,63488
v_mul_f32_e32 v114, s54, v114
Mv 84,8,84
v_mul_f32_e32 v115, s54, v115
Cv 114,114,115
v_accvgpr_write_b32 a217, v114
Mv 84,12,92
Rb 60,8,64512
s_waitcnt lgkmcnt(8)
Mv 80,12,88
v_mul_f32_e32 v116, s54, v116
v_mul_f32_e32 v117, s54, v117
Cv 116,116,117
)" R"(
Mv 80,16,96
v_accvgpr_write_b32 a218, v116
v_mul_f32_e32 v118, s54, v118
Mv 84,16,100
buffer_load_dwordx4 a[160:163], v1, s[20:23], s83 offen offset:1024 sc0 sc1
Mv 84,20,108
v_mul_f32_e32 v119, s54, v119
Cv 118,118,119
v_accvgpr_write_b32 a219, v118
Mv 80,20,104
v_mul_f32_e32 v120, s54, v120
v_mul_f32_e32 v121, s54, v121
Mv 80,24,112
Cv 120,120,121
v_accvgpr_write_b32 a220, v120
Mv 84,24,116
v_mul_f32_e32 v122, s54, v122
v_mul_f32_e32 v123, s54, v123
Cv 122,122,123
Mv 84,28,124
v_accvgpr_write_b32 a221, v122
v_mul_f32_e32 v124, s54, v124
Mv 80,28,120
v_mul_f32_e32 v125, s54, v125
s_waitcnt lgkmcnt(0)
s_barrier
Mz 88,32,64
Rb 0,1,32768
Cv 124,124,125
Mz 92,32,68
Rb 4,1,33792
v_accvgpr_write_b32 a222, v124
Mv 92,36,76
Rb 8,1,34816
v_mul_f32_e32 v126, s54, v126
v_mul_f32_e32 v127, s54, v127
Mv 88,36,72
Rb 12,1,35840
Cv 126,126,127
Mv 88,40,80
Rb 16,1,40960
v_accvgpr_write_b32 a223, v126
Mv 92,40,84
Rb 20,1,41984
v_mov_b32 v16, 0
v_mov_b32 v17, 0
Mv 92,44,92
Rb 24,1,43008
v_mov_b32 v18, 0
Mv 88,44,88
Rb 28,1,44032
v_mov_b32 v19, 0
v_mov_b32 v20, 0
Mv 88,48,96
Rb 32,1,36864
v_mov_b32 v21, 0
Mv 92,48,100
Rb 36,1,37888
v_mov_b32 v22, 0
Mv 92,52,108
Rb 40,1,38912
v_mov_b32 v23, 0
Mv 88,52,104
Rb 44,1,39936
Mv 88,56,112
)" R"(
buffer_load_dwordx4 a[164:167], v1, s[20:23], s83 offen offset:2048 sc0 sc1
Mv 92,56,116
buffer_load_dwordx4 a[168:171], v1, s[20:23], s83 offen offset:3072 sc0 sc1
Mv 92,60,124
s_add_u32 s83, s83, 4096
s_waitcnt lgkmcnt(8)
Mv 88,60,120
buffer_load_dwordx4 a[172:175], v1, s[20:23], s83 offen offset:0 sc0 sc1
Mz 96,0,64
Rb 48,1,45056
buffer_load_dword v4, v5, s[20:23], s50 offen offset:0 sc0 sc1
Mz 100,0,68
s_lshl_b64 s[60:61], s[60:61], 8
s_or_b32 s60, s60, 0xff
s_cmp_le_i32 s57, s58
s_cbranch_scc1 .Lzmk0
.Lzmr0:
Mv 100,4,76
Rb 52,1,46080
v_exp_f32_e32 v32, v64
Mv 96,4,72
buffer_load_dwordx4 a[176:179], v1, s[20:23], s83 offen offset:1024 sc0 sc1
s_sub_u32 s57, s57, 1
Mv 96,8,80
Rb 56,1,47104
v_exp_f32_e32 v24, v65
Mv 100,8,84
v_exp_f32_e32 v33, v66
s_add_u32 s55, s55, 0x10000
Mv 100,12,92
Rb 60,1,48128
v_add_f32_e32 v14, v32, v24
s_waitcnt lgkmcnt(8)
Mv 96,12,88
buffer_load_dwordx4 a[180:183], v1, s[20:23], s83 offen offset:2048 sc0 sc1
s_cmp_eq_u32 s57, 2
s_cselect_b32 s55, s59, s55
Mv 96,16,96
Rb 0,1,49152
v_exp_f32_e32 v25, v67
Mv 100,16,100
v_exp_f32_e32 v34, v72
Cv 32,32,24
Mv 100,20,108
Rb 4,1,50176
v_exp_f32_e32 v26, v73
Mv 96,20,104
v_exp_f32_e32 v35, v74
v_add_f32_e32 v14, v14, v33
Mv 96,24,112
Rb 8,1,51200
v_exp_f32_e32 v27, v75
v_add_f32_e32 v14, v14, v25
Mv 100,24,116
s_add_u32 m0, s36, 65536
v_exp_f32_e32 v36, v68
Mv 100,28,124
Rb 12,1,52224
Cv 33,33,25
s_waitcnt lgkmcnt(8)
Mv 96,28,120
buffer_load_dwordx4 v2, s[20:23], s56 offen offset:0 sc1 lds
v_add_f32_e32 v14, v14, v34
Mz 104,32,64
)" R"(
Rb 16,1,57344
v_exp_f32_e32 v28, v69
Mz 108,32,68
v_exp_f32_e32 v37, v70
v_exp_f32_e32 v29, v71
Mv 108,36,76
Rb 20,1,58368
v_exp_f32_e32 v38, v76
Mv 104,36,72
v_exp_f32_e32 v24, v77
v_add_f32_e32 v14, v14, v26
Mv 104,40,80
Rb 24,1,59392
v_exp_f32_e32 v39, v78
Mv 108,40,84
buffer_load_dwordx4 v2, s[20:23], s56 offen offset:1024 sc1 lds
Cv 34,34,26
Mv 108,44,92
Rb 28,1,60416
v_add_f32_e32 v14, v14, v35
s_waitcnt lgkmcnt(8)
Mv 104,44,88
v_exp_f32_e32 v25, v79
v_exp_f32_e32 v40, v80
Mv 104,48,96
Rb 32,1,53248
v_exp_f32_e32 v26, v81
Mv 108,48,100
v_add_f32_e32 v14, v14, v27
Cv 35,35,27
v_add_f32_e32 v15, v36, v28
Mv 108,52,108
Rb 36,1,54272
v_exp_f32_e32 v41, v82
Mv 104,52,104
buffer_load_dwordx4 v2, s[20:23], s56 offen offset:2048 sc1 lds
Cv 36,36,28
Mv 104,56,112
Rb 40,1,55296
v_exp_f32_e32 v27, v83
v_add_f32_e32 v15, v15, v37
Mv 108,56,116
v_exp_f32_e32 v42, v88
v_add_f32_e32 v15, v15, v29
Mv 108,60,124
Rb 44,1,56320
Cv 37,37,29
s_waitcnt lgkmcnt(8)
Mv 104,60,120
v_exp_f32_e32 v28, v89
v_add_f32_e32 v15, v15, v38
Mz 112,0,64
Rb 48,1,61440
v_exp_f32_e32 v43, v90
Mz 116,0,68
buffer_load_dwordx4 v2, s[20:23], s56 offen offset:3072 sc1 lds
v_exp_f32_e32 v29, v91
Mv 116,4,76
Rb 52,1,62464
v_add_f32_e32 v15, v15, v24
Cv 38,38,24
Mv 112,4,72
v_exp_f32_e32 v44, v84
v_add_f32_e32 v15, v15, v39
)" R"(
Mv 112,8,80
Rb 56,1,63488
v_exp_f32_e32 v24, v85
Mv 116,8,84
s_add_u32 m0, s36, 69632
v_add_f32_e32 v15, v15, v25
Cv 39,39,25
Mv 116,12,92
Rb 60,1,64512
v_add_f32_e32 v14, v14, v40
s_waitcnt lgkmcnt(8)
Mv 112,12,88
buffer_load_dwordx4 v3, s[20:23], s56 offen offset:0 sc1 lds
v_exp_f32_e32 v45, v86
Mv 112,16,96
v_exp_f32_e32 v25, v87
v_add_f32_e32 v14, v14, v26
Mv 116,16,100
Cv 40,40,26
v_exp_f32_e32 v46, v92
Mv 116,20,108
v_add_f32_e32 v14, v14, v41
v_exp_f32_e32 v26, v93
Mv 112,20,104
v_add_f32_e32 v14, v14, v27
Cv 41,41,27
v_add_f32_e32 v14, v14, v42
Mv 112,24,112
v_exp_f32_e32 v47, v94
v_exp_f32_e32 v27, v95
Mv 116,24,116
buffer_load_dwordx4 v3, s[20:23], s56 offen offset:1024 sc1 lds
v_add_f32_e32 v14, v14, v28
Mv 116,28,124
Cv 42,42,28
v_add_f32_e32 v14, v14, v43
v_add_f32_e32 v14, v14, v29
Mv 112,28,120
Cv 43,43,29
s_waitcnt lgkmcnt(0)
s_barrier
Mz 120,32,64
Rb 0,10,0
v_add_f32_e32 v15, v15, v44
v_add_f32_e32 v15, v15, v24
Mz 124,32,68
Rb 4,10,1024
Cv 44,44,24
v_add_f32_e32 v15, v15, v45
v_add_f32_e32 v15, v15, v25
Mv 124,36,76
Rb 8,10,2048
Cv 45,45,25
v_add_f32_e32 v15, v15, v46
Mv 120,36,72
Rb 12,10,3072
buffer_load_dwordx4 v3, s[20:23], s56 offen offset:2048 sc1 lds
Mv 120,40,80
Rb 16,10,4096
v_add_f32_e32 v15, v15, v26
Cv 46,46,26
Mv 124,40,84
Rb 20,10,5120
v_add_f32_e32 v15, v15, v47
v_add_f32_e32 v15, v15, v27
)" R"(
Mv 124,44,92
Rb 24,10,6144
Cv 47,47,27
v_max_f32_e32 v11, v14, v15
Mv 120,44,88
Rb 28,10,7168
v_mov_b32 v24, v14
v_mov_b32 v25, v15
Mv 120,48,96
Rb 32,10,8192
v_permlane32_swap_b32_e32 v24, v25
v_add_f32_e32 v24, v24, v25
Mv 124,48,100
Rb 36,10,9216
buffer_load_dwordx4 v3, s[20:23], s56 offen offset:3072 sc1 lds
Mv 124,52,108
Rb 40,10,10240
v_mov_b32 v25, v24
Mv 120,52,104
Rb 44,10,11264
s_add_u32 s56, s56, 0x10000
v_permlane16_swap_b32_e32 v24, v25
Mv 120,56,112
v_add_f32_e32 v24, v24, v25
v_rcp_f32_e32 v24, v24
Mv 124,56,116
v_max_f32_e32 v11, v11, v24
v_cmp_nge_f32_e32 vcc, s62, v11
Mv 124,60,124
s_waitcnt lgkmcnt(8)
s_cbranch_vccnz .Lzfx0
.Lzfr0:
Mv 120,60,120
v_mov_b32 v12, v14
v_mov_b32 v13, v15
v_mfma_f32_16x16x32_bf16 v[128:131], a[0:3], v[32:35], 0
Rb 48,10,12288
v_mfma_f32_16x16x32_bf16 v[136:139], a[0:3], v[36:39], 0
s_cmp_le_i32 s57, s58
s_cbranch_scc1 .Lzmk1
.Lzmr1:
v_mfma_f32_16x16x32_bf16 v[140:143], a[4:7], v[36:39], 0
Rb 52,10,13312
s_add_u32 m0, s36, 32768
v_mfma_f32_16x16x32_bf16 v[132:135], a[4:7], v[32:35], 0
buffer_load_dwordx4 v2, s[20:23], s55 offen offset:0 sc1 lds
v_mfma_f32_16x16x32_bf16 v[144:147], a[8:11], v[32:35], 0
Rb 56,10,14336
v_mfma_f32_16x16x32_bf16 v[152:155], a[8:11], v[36:39], 0
v_exp_f32_e32 v48, v96
v_mfma_f32_16x16x32_bf16 v[156:159], a[12:15], v[36:39], 0
Rb 60,10,15360
s_waitcnt lgkmcnt(8)
v_mfma_f32_16x16x32_bf16 v[148:151], a[12:15], v[32:35], 0
buffer_load_dwordx4 v2, s[20:23], s55 offen offset:1024 sc1 lds
v_mfma_f32_16x16x32_bf16 v[160:163], a[16:19], v[32:35], 0
Rb 0,10,16384
v_mfma_f32_16x16x32_bf16 v[168:171], a[16:19], v[36:39], 0
v_exp_f32_e32 v28, v97
v_mfma_f32_16x16x32_bf16 v[172:175], a[20:23], v[36:39], 0
Rb 4,10,17408
v_add_f32_e32 v14, v48, v28
v_mfma_f32_16x16x32_bf16 v[164:167], a[20:23], v[32:35], 0
buffer_load_dwordx4 v2, s[20:23], s55 offen offset:2048 sc1 lds
)" R"(
v_mfma_f32_16x16x32_bf16 v[176:179], a[24:27], v[32:35], 0
Rb 8,10,18432
Cv 48,48,28
v_mfma_f32_16x16x32_bf16 v[184:187], a[24:27], v[36:39], 0
v_exp_f32_e32 v49, v98
v_mfma_f32_16x16x32_bf16 v[188:191], a[28:31], v[36:39], 0
Rb 12,10,19456
s_waitcnt lgkmcnt(8)
v_mfma_f32_16x16x32_bf16 v[180:183], a[28:31], v[32:35], 0
buffer_load_dwordx4 v2, s[20:23], s55 offen offset:3072 sc1 lds
v_mfma_f32_16x16x32_bf16 v[192:195], a[32:35], v[32:35], 0
Rb 16,10,20480
v_add_f32_e32 v14, v14, v49
v_mfma_f32_16x16x32_bf16 v[200:203], a[32:35], v[36:39], 0
v_exp_f32_e32 v29, v99
v_mfma_f32_16x16x32_bf16 v[204:207], a[36:39], v[36:39], 0
Rb 20,10,21504
s_add_u32 m0, s36, 36864
v_mfma_f32_16x16x32_bf16 v[196:199], a[36:39], v[32:35], 0
buffer_load_dwordx4 v3, s[20:23], s55 offen offset:0 sc1 lds
v_mfma_f32_16x16x32_bf16 v[208:211], a[40:43], v[32:35], 0
Rb 24,10,22528
v_add_f32_e32 v14, v14, v29
v_mfma_f32_16x16x32_bf16 v[216:219], a[40:43], v[36:39], 0
v_exp_f32_e32 v50, v104
v_mfma_f32_16x16x32_bf16 v[220:223], a[44:47], v[36:39], 0
Rb 28,10,23552
s_waitcnt lgkmcnt(8)
v_mfma_f32_16x16x32_bf16 v[212:215], a[44:47], v[32:35], 0
buffer_load_dwordx4 v3, s[20:23], s55 offen offset:1024 sc1 lds
v_mfma_f32_16x16x32_bf16 v[224:227], a[48:51], v[32:35], 0
Rb 32,10,24576
Cv 49,49,29
v_mfma_f32_16x16x32_bf16 v[232:235], a[48:51], v[36:39], 0
v_exp_f32_e32 v24, v105
v_add_f32_e32 v14, v14, v50
v_mfma_f32_16x16x32_bf16 v[236:239], a[52:55], v[36:39], 0
Rb 36,10,25600
v_add_f32_e32 v14, v14, v24
v_mfma_f32_16x16x32_bf16 v[228:231], a[52:55], v[32:35], 0
buffer_load_dwordx4 v3, s[20:23], s55 offen offset:2048 sc1 lds
v_mfma_f32_16x16x32_bf16 v[240:243], a[56:59], v[32:35], 0
Rb 40,10,26624
Cv 50,50,24
v_mfma_f32_16x16x32_bf16 v[248:251], a[56:59], v[36:39], 0
v_exp_f32_e32 v51, v106
v_mfma_f32_16x16x32_bf16 v[252:255], a[60:63], v[36:39], 0
Rb 44,10,27648
s_waitcnt lgkmcnt(8)
v_mfma_f32_16x16x32_bf16 v[244:247], a[60:63], v[32:35], 0
buffer_load_dwordx4 v3, s[20:23], s55 offen offset:3072 sc1 lds
Mp 128,0,40
Rb 48,10,28672
v_add_f32_e32 v14, v14, v51
Mp 136,0,44
v_exp_f32_e32 v25, v107
s_sub_u32 s57, s57, 1
Mp 140,4,44
Rb 52,10,29696
v_add_f32_e32 v14, v14, v25
Mp 132,4,40
v_exp_f32_e32 v52, v100
Mp 144,8,40
Rb 56,10,30720
)" R"(
Cv 51,51,25
Mp 152,8,44
s_mov_b32 s24, s70
s_mov_b32 s25, s71
Mp 156,12,44
Rb 60,10,31744
s_waitcnt lgkmcnt(8)
Mp 148,12,40
s_mov_b32 s26, 0x188000
s_mov_b32 s27, 0x20000
s_mul_i32 s83, s82, 14336
Mp 160,16,40
s_lshl_b32 s92, s33, 12
s_add_u32 s83, s83, s92
Mp 168,16,44
s_add_u32 m0, s92, 131072
v_exp_f32_e32 v26, v101
Mp 172,20,44
v_exp_f32_e32 v53, v102
Mp 164,20,40
v_exp_f32_e32 v27, v103
Mp 176,24,40
v_exp_f32_e32 v54, v108
Mp 184,24,44
v_exp_f32_e32 v28, v109
Mp 188,28,44
v_exp_f32_e32 v55, v110
Mp 180,28,40
s_waitcnt vmcnt(16) lgkmcnt(0)
s_barrier
Mp 192,32,40
v_mfma_f32_16x16x32_bf16 a[184:187], a[156:159], a[128:131], 0
Rb 0,1,0
v_add_f32_e32 v15, v52, v26
Mp 200,32,44
Rb 4,1,1024
v_exp_f32_e32 v29, v111
Mp 204,36,44
Ma 184,160,132
Rb 8,1,2048
buffer_load_dwordx4 v1, s[24:27], s83 offen offset:0 lds
Mp 196,36,40
Rb 12,1,3072
Cv 52,52,26
Mp 208,40,40
Ma 184,164,136
Rb 16,1,8192
v_add_f32_e32 v15, v15, v53
Mp 216,40,44
Rb 20,1,9216
v_add_f32_e32 v15, v15, v27
Mp 220,44,44
Ma 184,168,140
Rb 24,1,10240
Cv 53,53,27
Mp 212,44,40
Rb 28,1,11264
buffer_load_dwordx4 v1, s[24:27], s83 offen offset:1024 lds
Mp 224,48,40
Ma 184,172,144
Rb 32,1,4096
v_add_f32_e32 v15, v15, v54
Mp 232,48,44
Rb 36,1,5120
buffer_load_dwordx4 v1, s[24:27], s83 offen offset:2048 lds
)" R"(
Mp 236,52,44
Ma 184,176,148
Rb 40,1,6144
v_add_f32_e32 v15, v15, v28
Mp 228,52,40
Rb 44,1,7168
Cv 54,54,28
Mp 240,56,40
Ma 184,180,152
v_cmp_eq_u32_e32 vcc, s3, v4
s_andn2_b64 s[28:29], s[60:61], vcc
s_cbranch_scc1 .Lzsp9
.Lzok9:
Mp 248,56,44
buffer_load_dwordx4 v1, s[24:27], s83 offen offset:3072 lds
Mp 252,60,44
v_add_f32_e32 v15, v15, v55
s_waitcnt lgkmcnt(8)
Mp 244,60,40
v_add_f32_e32 v15, v15, v29
Cv 55,55,29
s_cmp_gt_i32 s57, s93
s_cbranch_scc0 .LzLX
.LzL:
v_mfma_f32_16x16x32_bf16 v[64:67], a[0:3], a[64:67], v[16:19]
Rb 48,1,12288
v_mfma_f32_16x16x32_bf16 v[68:71], a[0:3], a[68:71], v[20:23]
v_exp_f32_e32 v56, v112
Mv 68,4,76
Rb 52,1,13312
s_add_u32 m0, s36, 98304
Mv 64,4,72
buffer_load_dwordx4 v2, s[20:23], s56 offen offset:0 sc1 lds
Mv 64,8,80
Rb 56,1,14336
v_add_f32_e32 v14, v14, v56
Mv 68,8,84
v_exp_f32_e32 v24, v113
Mv 68,12,92
Rb 60,1,15360
s_waitcnt lgkmcnt(8)
Mv 64,12,88
v_exp_f32_e32 v57, v114
Mv 64,16,96
Rb 0,1,16384
v_add_f32_e32 v14, v14, v24
Mv 68,16,100
v_exp_f32_e32 v25, v115
Mv 68,20,108
Rb 4,1,17408
Cv 56,56,24
Mv 64,20,104
buffer_load_dwordx4 v2, s[20:23], s56 offen offset:1024 sc1 lds
Mv 64,24,112
Rb 8,1,18432
v_add_f32_e32 v14, v14, v57
Mv 68,24,116
v_exp_f32_e32 v58, v120
Mv 68,28,124
Rb 12,1,19456
s_waitcnt lgkmcnt(8)
Mv 64,28,120
v_exp_f32_e32 v26, v121
v_mfma_f32_16x16x32_bf16 v[72:75], a[32:35], a[64:67], v[16:19]
)" R"(
Rb 16,1,24576
v_add_f32_e32 v14, v14, v25
v_mfma_f32_16x16x32_bf16 v[76:79], a[32:35], a[68:71], v[20:23]
v_exp_f32_e32 v59, v122
Mv 76,36,76
Rb 20,1,25600
Cv 57,57,25
Mv 72,36,72
buffer_load_dwordx4 v2, s[20:23], s56 offen offset:2048 sc1 lds
Mv 72,40,80
Rb 24,1,26624
v_add_f32_e32 v14, v14, v58
Mv 76,40,84
v_exp_f32_e32 v27, v123
Mv 76,44,92
Rb 28,1,27648
s_waitcnt lgkmcnt(8)
Mv 72,44,88
v_exp_f32_e32 v60, v116
Mv 72,48,96
Rb 32,1,20480
v_add_f32_e32 v14, v14, v26
Mv 76,48,100
v_exp_f32_e32 v28, v117
Mv 76,52,108
Rb 36,1,21504
Cv 58,58,26
Mv 72,52,104
buffer_load_dwordx4 v2, s[20:23], s56 offen offset:3072 sc1 lds
Mv 72,56,112
Rb 40,1,22528
v_add_f32_e32 v14, v14, v59
Mv 76,56,116
v_exp_f32_e32 v61, v118
Mv 76,60,124
Rb 44,1,23552
s_waitcnt lgkmcnt(8)
Mv 72,60,120
v_exp_f32_e32 v29, v119
v_mfma_f32_16x16x32_bf16 v[80:83], a[0:3], a[64:67], v[16:19]
Rb 48,1,28672
v_add_f32_e32 v14, v14, v27
v_mfma_f32_16x16x32_bf16 v[84:87], a[0:3], a[68:71], v[20:23]
v_exp_f32_e32 v62, v124
Mv 84,4,76
Rb 52,1,29696
s_add_u32 m0, s36, 102400
Mv 80,4,72
buffer_load_dwordx4 v3, s[20:23], s56 offen offset:0 sc1 lds
Mv 80,8,80
Rb 56,1,30720
Cv 59,59,27
Mv 84,8,84
v_exp_f32_e32 v24, v125
Mv 84,12,92
Rb 60,1,31744
s_waitcnt lgkmcnt(8)
Mv 80,12,88
v_exp_f32_e32 v63, v126
Mv 80,16,96
v_exp_f32_e32 v25, v127
Mv 84,16,100
v_add_f32_e32 v15, v15, v60
v_add_f32_e32 v15, v15, v28
)" R"(
Mv 84,20,108
Cv 60,60,28
v_add_f32_e32 v15, v15, v61
Mv 80,20,104
buffer_load_dwordx4 v3, s[20:23], s56 offen offset:1024 sc1 lds
Mv 80,24,112
v_add_f32_e32 v15, v15, v29
Cv 61,61,29
Mv 84,24,116
v_add_f32_e32 v15, v15, v62
v_add_f32_e32 v15, v15, v24
Mv 84,28,124
Cv 62,62,24
v_add_f32_e32 v15, v15, v63
Mv 80,28,120
s_waitcnt vmcnt(14) lgkmcnt(0)
s_barrier
v_mfma_f32_16x16x32_bf16 v[88:91], a[32:35], a[64:67], v[16:19]
Rb 0,8,0
v_add_f32_e32 v15, v15, v25
v_mfma_f32_16x16x32_bf16 v[92:95], a[32:35], a[68:71], v[20:23]
Rb 4,8,1024
Cv 63,63,25
Mv 92,36,76
Rb 8,8,2048
v_max_f32_e32 v11, v14, v15
Mv 88,36,72
Rb 12,8,3072
buffer_load_dwordx4 v3, s[20:23], s56 offen offset:2048 sc1 lds
Mv 88,40,80
Rb 16,8,4096
v_cmp_nge_f32_e32 vcc, s62, v11
Mv 92,40,84
Rb 20,8,5120
s_add_u32 s55, s55, 0x10000
Mv 92,44,92
Rb 24,8,6144
Mv 88,44,88
Rb 28,8,7168
Mv 88,48,96
Rb 32,8,8192
Mv 92,48,100
Rb 36,8,9216
Mv 92,52,108
Rb 40,8,10240
Mv 88,52,104
Rb 44,8,11264
buffer_load_dwordx4 v3, s[20:23], s56 offen offset:3072 sc1 lds
Mv 88,56,112
s_cmp_eq_u32 s57, 2
s_cselect_b32 s55, s59, s55
Mv 92,56,116
s_add_u32 s56, s56, 0x10000
Mv 92,60,124
s_waitcnt lgkmcnt(8)
s_cbranch_vccnz .Lzfx1
.Lzfr1:
Mv 88,60,120
v_add_f32_e32 v12, v12, v14
v_add_f32_e32 v13, v13, v15
Mp 128,0,48
Rb 48,8,12288
Mp 136,0,52
s_cmp_le_i32 s57, s58
s_cbranch_scc1 .Lzmk2
)" R"(
.Lzmr2:
Mp 140,4,52
Rb 52,8,13312
s_add_u32 m0, s36, 0
Mp 132,4,48
buffer_load_dwordx4 v2, s[20:23], s55 offen offset:0 sc1 lds
Mp 144,8,48
Rb 56,8,14336
Mp 152,8,52
v_exp_f32_e32 v32, v64
Mp 156,12,52
Rb 60,8,15360
s_waitcnt lgkmcnt(8)
Mp 148,12,48
v_exp_f32_e32 v26, v65
Mp 160,16,48
Rb 0,8,16384
v_add_f32_e32 v14, v32, v26
Mp 168,16,52
v_exp_f32_e32 v33, v66
Mp 172,20,52
Rb 4,8,17408
Cv 32,32,26
Mp 164,20,48
buffer_load_dwordx4 v2, s[20:23], s55 offen offset:1024 sc1 lds
Mp 176,24,48
Rb 8,8,18432
v_add_f32_e32 v14, v14, v33
Mp 184,24,52
v_exp_f32_e32 v27, v67
Mp 188,28,52
Rb 12,8,19456
s_waitcnt lgkmcnt(8)
Mp 180,28,48
v_exp_f32_e32 v34, v72
Mp 192,32,48
Rb 16,8,20480
v_add_f32_e32 v14, v14, v27
Mp 200,32,52
v_exp_f32_e32 v28, v73
Mp 204,36,52
Rb 20,8,21504
Cv 33,33,27
Mp 196,36,48
buffer_load_dwordx4 v2, s[20:23], s55 offen offset:2048 sc1 lds
Mp 208,40,48
Rb 24,8,22528
v_add_f32_e32 v14, v14, v34
Mp 216,40,52
v_exp_f32_e32 v35, v74
Mp 220,44,52
Rb 28,8,23552
s_waitcnt lgkmcnt(8)
Mp 212,44,48
v_exp_f32_e32 v29, v75
Mp 224,48,48
Rb 32,8,24576
v_add_f32_e32 v14, v14, v28
Mp 232,48,52
v_exp_f32_e32 v36, v68
Mp 236,52,52
Rb 36,8,25600
Cv 34,34,28
Mp 228,52,48
)" R"(
buffer_load_dwordx4 v2, s[20:23], s55 offen offset:3072 sc1 lds
Mp 240,56,48
Rb 40,8,26624
v_add_f32_e32 v14, v14, v35
Mp 248,56,52
v_exp_f32_e32 v24, v69
Mp 252,60,52
Rb 44,8,27648
s_waitcnt lgkmcnt(8)
Mp 244,60,48
v_exp_f32_e32 v37, v70
Mp 128,0,56
Rb 48,8,28672
v_add_f32_e32 v14, v14, v29
Mp 136,0,60
v_exp_f32_e32 v25, v71
Mp 140,4,60
Rb 52,8,29696
s_add_u32 m0, s36, 4096
Mp 132,4,56
buffer_load_dwordx4 v3, s[20:23], s55 offen offset:0 sc1 lds
Mp 144,8,56
Rb 56,8,30720
Cv 35,35,29
Mp 152,8,60
v_exp_f32_e32 v38, v76
Mp 156,12,60
Rb 60,8,31744
s_waitcnt lgkmcnt(8)
Mp 148,12,56
v_exp_f32_e32 v26, v77
Mp 160,16,56
v_exp_f32_e32 v39, v78
Mp 168,16,60
v_exp_f32_e32 v27, v79
Mp 172,20,60
v_add_f32_e32 v15, v36, v24
Cv 36,36,24
Mp 164,20,56
buffer_load_dwordx4 v3, s[20:23], s55 offen offset:1024 sc1 lds
Mp 176,24,56
v_add_f32_e32 v15, v15, v37
v_add_f32_e32 v15, v15, v25
Mp 184,24,60
Cv 37,37,25
v_add_f32_e32 v15, v15, v38
Mp 188,28,60
v_add_f32_e32 v15, v15, v26
Cv 38,38,26
Mp 180,28,56
s_waitcnt vmcnt(14) lgkmcnt(0)
s_barrier
Mp 192,32,56
Rb 0,1,32768
v_add_f32_e32 v15, v15, v39
Mp 200,32,60
Rb 4,1,33792
v_add_f32_e32 v15, v15, v27
Mp 204,36,60
Rb 8,1,34816
Cv 39,39,27
Mp 196,36,56
Rb 12,1,35840
buffer_load_dwordx4 v3, s[20:23], s55 offen offset:2048 sc1 lds
Mp 208,40,56
)" R"(
Rb 16,1,40960
s_sub_u32 s57, s57, 1
Mp 216,40,60
Rb 20,1,41984
Mp 220,44,60
Rb 24,1,43008
Mp 212,44,56
Rb 28,1,44032
Mp 224,48,56
Rb 32,1,36864
Mp 232,48,60
Rb 36,1,37888
Mp 236,52,60
Rb 40,1,38912
Mp 228,52,56
Rb 44,1,39936
buffer_load_dwordx4 v3, s[20:23], s55 offen offset:3072 sc1 lds
Mp 240,56,56
Mp 248,56,60
Mp 252,60,60
s_waitcnt lgkmcnt(8)
Mp 244,60,56
v_mfma_f32_16x16x32_bf16 v[96:99], a[0:3], a[64:67], v[16:19]
Rb 48,1,45056
buffer_load_dword v4, v5, s[20:23], s50 offen offset:0 sc0 sc1
v_mfma_f32_16x16x32_bf16 v[100:103], a[0:3], a[68:71], v[20:23]
s_lshl_b64 s[60:61], s[60:61], 8
s_or_b32 s60, s60, 0xff
Mv 100,4,76
Rb 52,1,46080
s_add_u32 m0, s36, 65536
Mv 96,4,72
buffer_load_dwordx4 v2, s[20:23], s56 offen offset:0 sc1 lds
Mv 96,8,80
Rb 56,1,47104
Mv 100,8,84
v_exp_f32_e32 v40, v80
Mv 100,12,92
Rb 60,1,48128
s_waitcnt lgkmcnt(8)
Mv 96,12,88
v_exp_f32_e32 v28, v81
Mv 96,16,96
Rb 0,1,49152
v_add_f32_e32 v14, v14, v40
Mv 100,16,100
v_exp_f32_e32 v41, v82
Mv 100,20,108
Rb 4,1,50176
v_add_f32_e32 v14, v14, v28
Mv 96,20,104
buffer_load_dwordx4 v2, s[20:23], s56 offen offset:1024 sc1 lds
Mv 96,24,112
Rb 8,1,51200
Cv 40,40,28
Mv 100,24,116
v_exp_f32_e32 v29, v83
Mv 100,28,124
Rb 12,1,52224
s_waitcnt lgkmcnt(8)
Mv 96,28,120
v_exp_f32_e32 v42, v88
v_mfma_f32_16x16x32_bf16 v[104:107], a[32:35], a[64:67], v[16:19]
Rb 16,1,57344
)" R"(
v_add_f32_e32 v14, v14, v41
v_mfma_f32_16x16x32_bf16 v[108:111], a[32:35], a[68:71], v[20:23]
v_exp_f32_e32 v24, v89
Mv 108,36,76
Rb 20,1,58368
v_add_f32_e32 v14, v14, v29
Mv 104,36,72
buffer_load_dwordx4 v2, s[20:23], s56 offen offset:2048 sc1 lds
Mv 104,40,80
Rb 24,1,59392
Cv 41,41,29
Mv 108,40,84
v_exp_f32_e32 v43, v90
Mv 108,44,92
Rb 28,1,60416
s_waitcnt lgkmcnt(8)
Mv 104,44,88
v_exp_f32_e32 v25, v91
Mv 104,48,96
Rb 32,1,53248
v_add_f32_e32 v14, v14, v42
Mv 108,48,100
v_exp_f32_e32 v44, v84
Mv 108,52,108
Rb 36,1,54272
v_add_f32_e32 v14, v14, v24
Mv 104,52,104
buffer_load_dwordx4 v2, s[20:23], s56 offen offset:3072 sc1 lds
Mv 104,56,112
Rb 40,1,55296
Cv 42,42,24
Mv 108,56,116
v_exp_f32_e32 v26, v85
Mv 108,60,124
Rb 44,1,56320
s_waitcnt lgkmcnt(8)
Mv 104,60,120
v_exp_f32_e32 v45, v86
v_mfma_f32_16x16x32_bf16 v[112:115], a[0:3], a[64:67], v[16:19]
Rb 48,1,61440
v_add_f32_e32 v14, v14, v43
v_mfma_f32_16x16x32_bf16 v[116:119], a[0:3], a[68:71], v[20:23]
v_exp_f32_e32 v27, v87
Mv 116,4,76
Rb 52,1,62464
s_add_u32 m0, s36, 69632
Mv 112,4,72
buffer_load_dwordx4 v3, s[20:23], s56 offen offset:0 sc1 lds
Mv 112,8,80
Rb 56,1,63488
v_add_f32_e32 v14, v14, v25
Mv 116,8,84
v_exp_f32_e32 v46, v92
Mv 116,12,92
Rb 60,1,64512
s_waitcnt lgkmcnt(8)
Mv 112,12,88
v_exp_f32_e32 v28, v93
Mv 112,16,96
v_exp_f32_e32 v47, v94
Mv 116,16,100
v_exp_f32_e32 v29, v95
Mv 116,20,108
Cv 43,43,25
)" R"(
v_add_f32_e32 v15, v15, v44
Mv 112,20,104
buffer_load_dwordx4 v3, s[20:23], s56 offen offset:1024 sc1 lds
Mv 112,24,112
v_add_f32_e32 v15, v15, v26
Cv 44,44,26
Mv 116,24,116
v_add_f32_e32 v15, v15, v45
v_add_f32_e32 v15, v15, v27
Mv 116,28,124
Cv 45,45,27
v_add_f32_e32 v15, v15, v46
Mv 112,28,120
s_waitcnt vmcnt(15) lgkmcnt(0)
s_barrier
v_mfma_f32_16x16x32_bf16 v[120:123], a[32:35], a[64:67], v[16:19]
Rb 0,8,32768
v_add_f32_e32 v15, v15, v28
v_mfma_f32_16x16x32_bf16 v[124:127], a[32:35], a[68:71], v[20:23]
Rb 4,8,33792
Cv 46,46,28
Mv 124,36,76
Rb 8,8,34816
v_add_f32_e32 v15, v15, v47
Mv 120,36,72
Rb 12,8,35840
buffer_load_dwordx4 v3, s[20:23], s56 offen offset:2048 sc1 lds
Mv 120,40,80
Rb 16,8,36864
v_add_f32_e32 v15, v15, v29
Mv 124,40,84
Rb 20,8,37888
Cv 47,47,29
Mv 124,44,92
Rb 24,8,38912
v_max_f32_e32 v11, v14, v15
Mv 120,44,88
Rb 28,8,39936
v_cmp_nge_f32_e32 vcc, s62, v11
Mv 120,48,96
Rb 32,8,40960
s_add_u32 s55, s55, 0x10000
Mv 124,48,100
Rb 36,8,41984
Mv 124,52,108
Rb 40,8,43008
Mv 120,52,104
Rb 44,8,44032
buffer_load_dwordx4 v3, s[20:23], s56 offen offset:3072 sc1 lds
Mv 120,56,112
s_cmp_eq_u32 s57, 2
s_cselect_b32 s55, s59, s55
Mv 124,56,116
s_add_u32 s56, s56, 0x10000
Mv 124,60,124
s_waitcnt lgkmcnt(8)
s_cbranch_vccnz .Lzfx2
.Lzfr2:
Mv 120,60,120
v_add_f32_e32 v12, v12, v14
v_add_f32_e32 v13, v13, v15
Mp 128,0,32
Rb 48,8,45056
Mp 136,0,36
s_cmp_le_i32 s57, s58
)" R"(
s_cbranch_scc1 .Lzmk3
.Lzmr3:
Mp 140,4,36
Rb 52,8,46080
s_add_u32 m0, s36, 32768
Mp 132,4,32
buffer_load_dwordx4 v2, s[20:23], s55 offen offset:0 sc1 lds
Mp 144,8,32
Rb 56,8,47104
Mp 152,8,36
v_exp_f32_e32 v48, v96
Mp 156,12,36
Rb 60,8,48128
s_waitcnt lgkmcnt(8)
Mp 148,12,32
v_exp_f32_e32 v24, v97
Mp 160,16,32
Rb 0,8,49152
v_add_f32_e32 v14, v48, v24
Mp 168,16,36
v_exp_f32_e32 v49, v98
Mp 172,20,36
Rb 4,8,50176
Cv 48,48,24
Mp 164,20,32
buffer_load_dwordx4 v2, s[20:23], s55 offen offset:1024 sc1 lds
Mp 176,24,32
Rb 8,8,51200
v_add_f32_e32 v14, v14, v49
Mp 184,24,36
v_exp_f32_e32 v25, v99
Mp 188,28,36
Rb 12,8,52224
s_waitcnt lgkmcnt(8)
Mp 180,28,32
v_exp_f32_e32 v50, v104
Mp 192,32,32
Rb 16,8,53248
v_add_f32_e32 v14, v14, v25
Mp 200,32,36
v_exp_f32_e32 v26, v105
Mp 204,36,36
Rb 20,8,54272
Cv 49,49,25
Mp 196,36,32
buffer_load_dwordx4 v2, s[20:23], s55 offen offset:2048 sc1 lds
Mp 208,40,32
Rb 24,8,55296
v_add_f32_e32 v14, v14, v50
Mp 216,40,36
v_exp_f32_e32 v51, v106
Mp 220,44,36
Rb 28,8,56320
s_waitcnt lgkmcnt(8)
Mp 212,44,32
v_exp_f32_e32 v27, v107
Mp 224,48,32
Rb 32,8,57344
v_add_f32_e32 v14, v14, v26
Mp 232,48,36
v_exp_f32_e32 v52, v100
Mp 236,52,36
Rb 36,8,58368
Cv 50,50,26
)" R"(
Mp 228,52,32
buffer_load_dwordx4 v2, s[20:23], s55 offen offset:3072 sc1 lds
Mp 240,56,32
Rb 40,8,59392
v_add_f32_e32 v14, v14, v51
Mp 248,56,36
v_exp_f32_e32 v28, v101
Mp 252,60,36
Rb 44,8,60416
s_waitcnt lgkmcnt(8)
Mp 244,60,32
v_exp_f32_e32 v53, v102
Mp 128,0,40
Rb 48,8,61440
v_add_f32_e32 v14, v14, v27
Mp 136,0,44
v_exp_f32_e32 v29, v103
Mp 140,4,44
Rb 52,8,62464
s_add_u32 m0, s36, 36864
Mp 132,4,40
buffer_load_dwordx4 v3, s[20:23], s55 offen offset:0 sc1 lds
Mp 144,8,40
Rb 56,8,63488
Cv 51,51,27
Mp 152,8,44
v_exp_f32_e32 v54, v108
Mp 156,12,44
Rb 60,8,64512
s_waitcnt lgkmcnt(8)
Mp 148,12,40
v_exp_f32_e32 v24, v109
Mp 160,16,40
v_exp_f32_e32 v55, v110
Mp 168,16,44
v_exp_f32_e32 v25, v111
Mp 172,20,44
v_add_f32_e32 v15, v52, v28
Cv 52,52,28
Mp 164,20,40
buffer_load_dwordx4 v3, s[20:23], s55 offen offset:1024 sc1 lds
Mp 176,24,40
v_add_f32_e32 v15, v15, v53
v_add_f32_e32 v15, v15, v29
Mp 184,24,44
Cv 53,53,29
v_add_f32_e32 v15, v15, v54
Mp 188,28,44
v_add_f32_e32 v15, v15, v24
Cv 54,54,24
Mp 180,28,40
s_waitcnt vmcnt(14) lgkmcnt(0)
s_barrier
Mp 192,32,40
Rb 0,1,0
v_add_f32_e32 v15, v15, v55
Mp 200,32,44
Rb 4,1,1024
v_add_f32_e32 v15, v15, v25
Mp 204,36,44
Rb 8,1,2048
Cv 55,55,25
Mp 196,36,40
Rb 12,1,3072
buffer_load_dwordx4 v3, s[20:23], s55 offen offset:2048 sc1 lds
)" R"(
Mp 208,40,40
Rb 16,1,8192
s_sub_u32 s57, s57, 1
Mp 216,40,44
Rb 20,1,9216
Mp 220,44,44
Rb 24,1,10240
Mp 212,44,40
Rb 28,1,11264
Mp 224,48,40
Rb 32,1,4096
Mp 232,48,44
Rb 36,1,5120
Mp 236,52,44
Rb 40,1,6144
Mp 228,52,40
Rb 44,1,7168
buffer_load_dwordx4 v3, s[20:23], s55 offen offset:3072 sc1 lds
Mp 240,56,40
v_cmp_eq_u32_e32 vcc, s3, v4
s_andn2_b64 s[28:29], s[60:61], vcc
s_cbranch_scc1 .Lzsp10
.Lzok10:
Mp 248,56,44
Mp 252,60,44
s_waitcnt lgkmcnt(8)
Mp 244,60,40
s_cmp_gt_i32 s57, s93
s_cbranch_scc1 .LzL
.LzLX:
s_cmp_eq_u32 s94, 0
s_cbranch_scc1 .LzEB
s_cmp_eq_u32 s94, 2
s_cbranch_scc1 .LzDA
s_lshl1_add_u32 s57, s35, 2
s_mov_b32 s59, s67
s_add_u32 s56, s89, 0x2000000
s_mov_b32 s94, 2
Mz 64,0,224
Rb 48,1,12288
v_and_b32 v9, 14, v0
Mz 68,0,228
v_min_u32 v9, 1, v9
v_lshlrev_b32 v9, 8, v9
v_accvgpr_mov_b32 a64, a224
Mv 68,4,236
Rb 52,1,13312
s_add_u32 m0, s36, 98304
v_accvgpr_mov_b32 a65, a225
Mv 64,4,232
buffer_load_dwordx4 v2, s[20:23], s56 offen offset:0 sc1 lds
Mv 64,8,240
Rb 56,1,14336
v_and_or_b32 v9, v0, 48, v9
v_accvgpr_mov_b32 a66, a226
Mv 68,8,244
v_lshlrev_b32 v9, 1, v9
v_and_or_b32 v9, v1, 16, v9
v_accvgpr_mov_b32 a67, a227
Mv 68,12,252
Rb 60,1,15360
s_waitcnt lgkmcnt(8)
Mv 64,12,248
s_lshl_b32 s92, s33, 7
)" R"(
s_add_u32 s92, s92, 162816
v_accvgpr_mov_b32 a68, a228
Mv 64,16,192
Rb 0,1,16384
v_add_u32 v9, s92, v9
v_accvgpr_mov_b32 a69, a229
Mv 68,16,196
ds_write_b128 v9, a[184:187]
v_and_b32 v9, 0x70, v9
Mv 68,20,204
Rb 4,1,17408
v_add_u32 v9, 0x27c00, v9
v_accvgpr_mov_b32 a70, a230
Mv 64,20,200
buffer_load_dwordx4 v2, s[20:23], s56 offen offset:1024 sc1 lds
v_accvgpr_mov_b32 a71, a231
Mv 64,24,208
Rb 8,1,18432
v_accvgpr_mov_b32 a72, a232
Mv 68,24,212
v_exp_f32_e32 v56, v112
v_accvgpr_mov_b32 a73, a233
Mv 68,28,220
Rb 12,1,19456
v_accvgpr_mov_b32 a74, a234
s_waitcnt lgkmcnt(9)
Mv 64,28,216
v_exp_f32_e32 v26, v113
v_add_f32_e32 v14, v14, v56
Mz 72,32,224
Rb 16,1,24576
v_add_f32_e32 v14, v14, v26
Mz 76,32,228
v_exp_f32_e32 v57, v114
v_accvgpr_mov_b32 a75, a235
Mv 76,36,236
Rb 20,1,25600
Cv 56,56,26
v_accvgpr_mov_b32 a76, a236
Mv 72,36,232
buffer_load_dwordx4 v2, s[20:23], s56 offen offset:2048 sc1 lds
Mv 72,40,240
Rb 24,1,26624
v_add_f32_e32 v14, v14, v57
v_accvgpr_mov_b32 a77, a237
Mv 76,40,244
v_exp_f32_e32 v27, v115
v_accvgpr_mov_b32 a78, a238
Mv 76,44,252
Rb 28,1,27648
s_waitcnt lgkmcnt(9)
Mv 72,44,248
v_exp_f32_e32 v58, v120
v_accvgpr_mov_b32 a79, a239
Mv 72,48,192
Rb 32,1,20480
v_add_f32_e32 v14, v14, v27
v_accvgpr_mov_b32 a80, a240
Mv 76,48,196
v_exp_f32_e32 v28, v121
Mv 76,52,204
Rb 36,1,21504
v_exp_f32_e32 v59, v122
Mv 72,52,200
)" R"(
buffer_load_dwordx4 v2, s[20:23], s56 offen offset:3072 sc1 lds
v_accvgpr_mov_b32 a81, a241
Mv 72,56,208
Rb 40,1,22528
Cv 57,57,27
Mv 76,56,212
v_exp_f32_e32 v29, v123
v_accvgpr_mov_b32 a82, a242
Mv 76,60,220
Rb 44,1,23552
v_accvgpr_mov_b32 a83, a243
s_waitcnt lgkmcnt(8)
Mv 72,60,216
v_exp_f32_e32 v60, v116
v_add_f32_e32 v14, v14, v58
Mz 80,0,224
Rb 48,1,28672
v_add_f32_e32 v14, v14, v28
Mz 84,0,228
v_exp_f32_e32 v24, v117
v_accvgpr_mov_b32 a84, a244
Mv 84,4,236
Rb 52,1,29696
s_add_u32 m0, s36, 102400
v_accvgpr_mov_b32 a85, a245
Mv 80,4,232
buffer_load_dwordx4 v3, s[20:23], s56 offen offset:0 sc1 lds
Mv 80,8,240
Rb 56,1,30720
Cv 58,58,28
v_accvgpr_mov_b32 a86, a246
Mv 84,8,244
v_exp_f32_e32 v61, v118
v_add_f32_e32 v14, v14, v59
Mv 84,12,252
Rb 60,1,31744
s_waitcnt lgkmcnt(8)
Mv 80,12,248
v_exp_f32_e32 v25, v119
v_accvgpr_mov_b32 a87, a247
Mv 80,16,192
v_exp_f32_e32 v62, v124
v_accvgpr_mov_b32 a88, a248
Mv 84,16,196
v_exp_f32_e32 v26, v125
Mv 84,20,204
v_exp_f32_e32 v63, v126
v_add_f32_e32 v14, v14, v29
Mv 80,20,200
buffer_load_dwordx4 v3, s[20:23], s56 offen offset:1024 sc1 lds
v_accvgpr_mov_b32 a89, a249
Mv 80,24,208
v_exp_f32_e32 v27, v127
Mv 84,24,212
Cv 59,59,29
v_add_f32_e32 v15, v15, v60
v_accvgpr_mov_b32 a90, a250
Mv 84,28,220
v_add_f32_e32 v15, v15, v24
Cv 60,60,24
v_accvgpr_mov_b32 a91, a251
Mv 80,28,216
v_add_f32_e32 v15, v15, v61
s_waitcnt vmcnt(14) lgkmcnt(0)
s_barrier
)" R"(
Mz 88,32,224
Rb 0,8,0
v_add_f32_e32 v15, v15, v25
Mz 92,32,228
Rb 4,8,16384
Cv 61,61,25
v_accvgpr_mov_b32 a92, a252
Mv 92,36,236
Rb 8,8,1024
v_add_f32_e32 v15, v15, v62
v_accvgpr_mov_b32 a93, a253
Mv 88,36,232
Rb 12,8,17408
buffer_load_dwordx4 v3, s[20:23], s56 offen offset:2048 sc1 lds
Mv 88,40,240
Rb 16,8,2048
v_add_f32_e32 v15, v15, v26
v_accvgpr_mov_b32 a94, a254
Mv 92,40,244
Rb 20,8,18432
Cv 62,62,26
v_add_f32_e32 v15, v15, v63
Mv 92,44,252
Rb 24,8,3072
v_add_f32_e32 v15, v15, v27
Mv 88,44,248
Rb 28,8,19456
Cv 63,63,27
v_accvgpr_mov_b32 a95, a255
Mv 88,48,192
Rb 32,8,4096
v_max_f32_e32 v11, v14, v15
v_accvgpr_mov_b32 a96, a192
Mv 92,48,196
Rb 36,8,20480
v_cmp_nge_f32_e32 vcc, s62, v11
Mv 92,52,204
Rb 40,8,5120
s_add_u32 s55, s55, 0x10000
v_accvgpr_mov_b32 a97, a193
Mv 88,52,200
Rb 44,8,21504
buffer_load_dwordx4 v3, s[20:23], s56 offen offset:3072 sc1 lds
Mv 88,56,208
s_cmp_eq_u32 s57, 2
s_cselect_b32 s55, s59, s55
Mv 92,56,212
s_add_u32 s56, s56, 0x10000
v_accvgpr_mov_b32 a98, a194
v_accvgpr_mov_b32 a99, a195
Mv 92,60,220
v_accvgpr_mov_b32 a100, a196
s_waitcnt lgkmcnt(8)
s_cbranch_vccnz .Lzfx3
.Lzfr3:
Mv 88,60,216
v_add_f32_e32 v12, v12, v14
v_add_f32_e32 v13, v13, v15
v_mov_b32 v14, v16
Mp 128,0,48
Rb 48,8,6144
v_mov_b32 v15, v20
Mp 136,0,52
buffer_store_dwordx4 v[12:15], v1, s[20:23], s66 offen sc0 sc1
)" R"(
s_cmp_le_i32 s57, s58
s_cbranch_scc1 .Lzmk4
.Lzmr4:
Mp 136,4,60
Rb 52,8,22528
s_add_u32 m0, s36, 0
v_accvgpr_mov_b32 a101, a197
Mp 128,4,56
buffer_load_dwordx4 v2, s[20:23], s55 offen offset:0 sc1 lds
Mp 132,8,48
Rb 56,8,7168
v_exp_f32_e32 v32, v64
Mp 140,8,52
v_exp_f32_e32 v28, v65
v_accvgpr_mov_b32 a102, a198
Mp 140,12,60
Rb 60,8,23552
v_exp_f32_e32 v33, v66
s_waitcnt lgkmcnt(8)
Mp 132,12,56
v_exp_f32_e32 v29, v67
v_add_f32_e32 v14, v32, v28
Mp 144,16,48
Rb 0,8,8192
v_exp_f32_e32 v34, v72
Mp 152,16,52
v_exp_f32_e32 v24, v73
v_exp_f32_e32 v35, v74
Mp 152,20,60
Rb 4,8,24576
v_exp_f32_e32 v25, v75
Mp 144,20,56
buffer_load_dwordx4 v2, s[20:23], s55 offen offset:1024 sc1 lds
Cv 128,128,129
Mp 148,24,48
Rb 8,8,9216
Cv 129,130,131
Cv 130,132,133
Cv 32,32,28
Mp 156,24,52
Cv 131,134,135
Cv 136,136,137
v_add_f32_e32 v14, v14, v33
Mp 156,28,60
Rb 12,8,25600
Cv 137,138,139
s_waitcnt lgkmcnt(8)
Mp 148,28,56
Cv 138,140,141
Cv 139,142,143
v_exp_f32_e32 v36, v68
Mp 160,32,48
Rb 16,8,10240
v_exp_f32_e32 v26, v69
Mp 168,32,52
v_exp_f32_e32 v37, v70
v_add_f32_e32 v14, v14, v29
Mp 168,36,60
Rb 20,8,26624
v_exp_f32_e32 v27, v71
Cv 33,33,29
Mp 160,36,56
buffer_load_dwordx4 v2, s[20:23], s55 offen offset:2048 sc1 lds
Cv 144,144,145
)" R"(
Mp 164,40,48
Rb 24,8,11264
Cv 145,146,147
Cv 146,148,149
Mp 172,40,52
buffer_store_dwordx4 v[128:131], v2, s[20:23], s64 offen offset:0 sc0 sc1
v_add_f32_e32 v14, v14, v34
Mp 172,44,60
Rb 28,8,27648
Cv 147,150,151
v_add_f32_e32 v14, v14, v24
s_waitcnt lgkmcnt(8)
Mp 164,44,56
Cv 152,152,153
Cv 153,154,155
Cv 154,156,157
Mp 176,48,48
Rb 32,8,12288
Cv 155,158,159
Cv 34,34,24
Mp 184,48,52
v_exp_f32_e32 v38, v76
v_exp_f32_e32 v28, v77
Mp 184,52,60
Rb 36,8,28672
v_exp_f32_e32 v39, v78
Mp 176,52,56
buffer_load_dwordx4 v2, s[20:23], s55 offen offset:3072 sc1 lds
Cv 160,160,161
Mp 180,56,48
Rb 40,8,13312
Cv 161,162,163
Cv 162,164,165
v_add_f32_e32 v14, v14, v35
Mp 188,56,52
buffer_store_dwordx4 v[136:139], v2, s[20:23], s64 offen offset:1024 sc0 sc1
v_add_f32_e32 v14, v14, v25
Mp 188,60,60
Rb 44,8,29696
Cv 163,166,167
s_waitcnt lgkmcnt(8)
Mp 180,60,56
Cv 168,168,169
Cv 169,170,171
Cv 170,172,173
Cv 35,35,25
Mp 192,0,48
Rb 48,8,14336
Cv 171,174,175
v_add_f32_e32 v15, v36, v26
Mp 200,0,52
buffer_store_dwordx4 v[144:147], v2, s[20:23], s64 offen offset:2048 sc0 sc1
Cv 36,36,26
Mp 200,4,60
Rb 52,8,30720
s_add_u32 m0, s36, 4096
v_exp_f32_e32 v29, v79
Mp 192,4,56
buffer_load_dwordx4 v3, s[20:23], s55 offen offset:0 sc1 lds
Cv 176,176,177
Mp 196,8,48
Rb 56,8,15360
Cv 177,178,179
Cv 178,180,181
)" R"(
Mp 204,8,52
Cv 179,182,183
Cv 184,184,185
v_add_f32_e32 v15, v15, v37
Mp 204,12,60
Rb 60,8,31744
Cv 185,186,187
v_add_f32_e32 v15, v15, v27
s_waitcnt lgkmcnt(8)
Mp 196,12,56
buffer_store_dwordx4 v[152:155], v2, s[20:23], s64 offen offset:3072 sc0 sc1
Cv 186,188,189
Mp 208,16,48
ds_read_b128 v[132:135], v9 offset:0
Cv 187,190,191
Cv 37,37,27
Mp 216,16,52
ds_read_b128 v[140:143], v9 offset:128
v_add_f32_e32 v15, v15, v38
v_add_f32_e32 v15, v15, v28
Cv 38,38,28
Mp 216,20,60
ds_read_b128 v[148:151], v9 offset:256
v_add_f32_e32 v15, v15, v39
v_add_f32_e32 v15, v15, v29
Mp 208,20,56
buffer_load_dwordx4 v3, s[20:23], s55 offen offset:1024 sc1 lds
ds_read_b128 v[156:159], v9 offset:384
Mp 212,24,48
Cv 192,192,193
Cv 193,194,195
Cv 194,196,197
Cv 39,39,29
Mp 220,24,52
Cv 195,198,199
Cv 200,200,201
s_sub_u32 s57, s57, 1
Mp 220,28,60
buffer_store_dwordx4 v[160:163], v3, s[20:23], s64 offen offset:0 sc0 sc1
Cv 201,202,203
Mp 212,28,56
Cv 202,204,205
v_mov_b32 v16, 0
s_waitcnt vmcnt(20) lgkmcnt(0)
s_barrier
Mp 224,32,48
Rb 0,1,32768
v_add_f32_e32 v132, v132, v140
v_mov_b32 v17, 0
Mp 232,32,52
Rb 4,1,33792
v_add_f32_e32 v133, v133, v141
Cv 203,206,207
Mp 232,36,60
Rb 8,1,34816
v_add_f32_e32 v134, v134, v142
v_mov_b32 v18, 0
v_mov_b32 v19, 0
Mp 224,36,56
Rb 12,1,35840
buffer_load_dwordx4 v3, s[20:23], s55 offen offset:2048 sc1 lds
Mp 228,40,48
Rb 16,1,40960
v_add_f32_e32 v135, v135, v143
buffer_store_dwordx4 v[168:171], v3, s[20:23], s64 offen offset:1024 sc0 sc1
)" R"(
Mp 236,40,52
Rb 20,1,41984
v_add_f32_e32 v148, v148, v156
v_mov_b32 v20, 0
Mp 236,44,60
Rb 24,1,43008
v_add_f32_e32 v149, v149, v157
Cv 208,208,209
v_mov_b32 v21, 0
Mp 228,44,56
Rb 28,1,44032
v_add_f32_e32 v150, v150, v158
v_add_f32_e32 v151, v151, v159
Mp 240,48,48
Rb 32,1,36864
Cv 132,132,132
v_mov_b32 v22, 0
Mp 248,48,52
Rb 36,1,37888
v_lshlrev_b32 v132, 16, v132
Cv 209,210,211
v_mov_b32 v23, 0
Mp 248,52,60
Rb 40,1,38912
Cv 148,148,148
buffer_store_dwordx4 v[176:179], v3, s[20:23], s64 offen offset:2048 sc0 sc1
Mp 240,52,56
Rb 44,1,39936
buffer_load_dwordx4 v3, s[20:23], s55 offen offset:3072 sc1 lds
Mp 244,56,48
v_lshlrev_b32 v148, 16, v148
Cv 133,133,133
Cv 210,212,213
Mp 252,56,52
v_lshlrev_b32 v133, 16, v133
Cv 149,149,149
v_accvgpr_mov_b32 a103, a199
Mp 252,60,60
v_lshlrev_b32 v149, 16, v149
Cv 211,214,215
s_waitcnt lgkmcnt(8)
Mp 244,60,56
Cv 134,134,134
v_lshlrev_b32 v134, 16, v134
Cv 150,150,150
Mz 96,0,224
Rb 48,1,45056
buffer_load_dword v4, v5, s[20:23], s50 offen offset:0 sc0 sc1
buffer_store_dwordx4 v[184:187], v3, s[20:23], s64 offen offset:3072 sc0 sc1
Mz 100,0,228
s_lshl_b64 s[60:61], s[60:61], 8
s_or_b32 s60, s60, 0xff
Cv 216,216,217
Mv 100,4,236
Rb 52,1,46080
s_add_u32 m0, s36, 65536
v_exp_f32_e32 v40, v80
Mv 96,4,232
buffer_load_dwordx4 v2, s[20:23], s56 offen offset:0 sc1 lds
Cv 217,218,219
v_add_f32_e32 v14, v14, v40
Mv 96,8,240
Rb 56,1,47104
v_lshlrev_b32 v150, 16, v150
)" R"(
Cv 218,220,221
Mv 100,8,244
Cv 135,135,135
v_lshlrev_b32 v135, 16, v135
v_exp_f32_e32 v24, v81
Mv 100,12,252
Rb 60,1,48128
s_add_u32 s64, s64, 0x10000
buffer_store_dwordx4 v[192:195], v2, s[20:23], s64 offen offset:0 sc0 sc1
s_waitcnt lgkmcnt(8)
Mv 96,12,248
Cv 151,151,151
v_lshlrev_b32 v151, 16, v151
v_mul_f32_e32 v140, 0xbfb8aa3b, v132
v_add_f32_e32 v14, v14, v24
Mv 96,16,192
Rb 0,1,49152
v_mul_f32_e32 v141, 0xbfb8aa3b, v133
v_exp_f32_e32 v41, v82
Mv 100,16,196
v_exp_f32_e32 v140, v140
Cv 219,222,223
Cv 40,40,24
Mv 100,20,204
Rb 4,1,50176
v_mul_f32_e32 v142, 0xbfb8aa3b, v134
v_exp_f32_e32 v25, v83
Mv 96,20,200
buffer_load_dwordx4 v2, s[20:23], s56 offen offset:1024 sc1 lds
Cv 224,224,225
v_add_f32_e32 v14, v14, v41
Mv 96,24,208
Rb 8,1,51200
v_mul_f32_e32 v143, 0xbfb8aa3b, v135
buffer_store_dwordx4 v[200:203], v2, s[20:23], s64 offen offset:1024 sc0 sc1
Mv 100,24,212
v_exp_f32_e32 v141, v141
v_exp_f32_e32 v42, v88
Mv 100,28,220
Rb 12,1,52224
Cv 225,226,227
v_add_f32_e32 v14, v14, v25
s_waitcnt lgkmcnt(8)
Mv 96,28,216
v_exp_f32_e32 v142, v142
v_add_f32_e32 v140, 1.0, v140
Cv 41,41,25
Mz 104,32,224
Rb 16,1,57344
v_add_f32_e32 v141, 1.0, v141
v_add_f32_e32 v14, v14, v42
Mz 108,32,228
v_exp_f32_e32 v143, v143
Cv 226,228,229
Mv 108,36,236
Rb 20,1,58368
v_add_f32_e32 v142, 1.0, v142
buffer_store_dwordx4 v[208:211], v2, s[20:23], s64 offen offset:2048 sc0 sc1
Mv 104,36,232
buffer_load_dwordx4 v2, s[20:23], s56 offen offset:2048 sc1 lds
Cv 227,230,231
Mv 104,40,240
Rb 24,1,59392
v_add_f32_e32 v143, 1.0, v143
)" R"(
Cv 232,232,233
Mv 108,40,244
v_rcp_f32_e32 v140, v140
v_exp_f32_e32 v26, v89
Mv 108,44,252
Rb 28,1,60416
Cv 233,234,235
v_add_f32_e32 v14, v14, v26
s_waitcnt lgkmcnt(8)
Mv 104,44,248
v_rcp_f32_e32 v141, v141
v_mul_f32_e32 v132, v132, v140
Cv 42,42,26
Mv 104,48,192
Rb 32,1,53248
v_mul_f32_e32 v133, v133, v141
buffer_store_dwordx4 v[216:219], v2, s[20:23], s64 offen offset:3072 sc0 sc1
Mv 108,48,196
v_rcp_f32_e32 v142, v142
Cv 234,236,237
Mv 108,52,204
Rb 36,1,54272
v_mul_f32_e32 v134, v134, v142
v_exp_f32_e32 v43, v90
Mv 104,52,200
buffer_load_dwordx4 v2, s[20:23], s56 offen offset:3072 sc1 lds
Cv 235,238,239
v_add_f32_e32 v14, v14, v43
Mv 104,56,208
Rb 40,1,55296
Cv 132,132,132
Cv 240,240,241
Mv 108,56,212
v_rcp_f32_e32 v143, v143
v_exp_f32_e32 v27, v91
Mv 108,60,220
Rb 44,1,56320
buffer_store_dwordx4 v[224:227], v3, s[20:23], s64 offen offset:0 sc0 sc1
s_waitcnt lgkmcnt(8)
Mv 104,60,216
v_mul_f32_e32 v135, v135, v143
v_lshlrev_b32 v132, 16, v132
Cv 133,133,133
v_add_f32_e32 v14, v14, v27
Mz 112,0,224
Rb 48,1,61440
v_lshlrev_b32 v133, 16, v133
Cv 43,43,27
Mz 116,0,228
Cv 134,134,134
v_lshlrev_b32 v134, 16, v134
Cv 241,242,243
Mv 116,4,236
Rb 52,1,62464
s_add_u32 m0, s36, 69632
v_exp_f32_e32 v44, v84
Mv 112,4,232
buffer_load_dwordx4 v3, s[20:23], s56 offen offset:0 sc1 lds
Cv 242,244,245
v_add_f32_e32 v15, v15, v44
Mv 112,8,240
Rb 56,1,63488
Cv 135,135,135
buffer_store_dwordx4 v[232:235], v3, s[20:23], s64 offen offset:1024 sc0 sc1
)" R"(
Mv 116,8,244
v_lshlrev_b32 v135, 16, v135
v_mul_f32_e32 v132, v132, v148
v_exp_f32_e32 v28, v85
Mv 116,12,252
Rb 60,1,64512
Cv 243,246,247
v_add_f32_e32 v15, v15, v28
s_waitcnt lgkmcnt(8)
Mv 112,12,248
v_mul_f32_e32 v133, v133, v149
v_mul_f32_e32 v134, v134, v150
v_mul_f32_e32 v135, v135, v151
Cv 44,44,28
Mv 112,16,192
Cv 140,132,133
Cv 141,134,135
v_exp_f32_e32 v45, v86
Mv 116,16,196
v_accvgpr_write_b32 a188, v140
v_accvgpr_write_b32 a189, v141
Cv 248,248,249
v_add_f32_e32 v15, v15, v45
Mv 116,20,204
v_lshrrev_b32 v156, 2, v0
v_mul_u32_u24 v156, 864, v156
buffer_store_dwordx4 v[240:243], v3, s[20:23], s64 offen offset:2048 sc0 sc1
Mv 112,20,200
buffer_load_dwordx4 v3, s[20:23], s56 offen offset:1024 sc1 lds
Cv 249,250,251
Mv 112,24,208
v_lshl_add_u32 v156, v0, 3, v156
s_mul_i32 s92, s33, 224
Cv 250,252,253
Mv 116,24,212
s_add_u32 s92, s92, 131072
v_add_u32 v156, s92, v156
v_exp_f32_e32 v29, v87
Mv 116,28,220
Cv 251,254,255
v_exp_f32_e32 v46, v92
v_add_f32_e32 v15, v15, v29
Mv 112,28,216
v_exp_f32_e32 v24, v93
s_waitcnt vmcnt(31) lgkmcnt(0)
s_barrier
Mz 120,32,224
Rb 0,8,32768
Rt 156,156,0
buffer_store_dwordx4 v[248:251], v3, s[20:23], s64 offen offset:3072 sc0 sc1
Mz 124,32,228
Rb 4,8,33792
Rt 158,156,32
v_exp_f32_e32 v47, v94
Mv 124,36,236
Rb 8,8,34816
Rt 160,156,64
v_exp_f32_e32 v25, v95
Mv 120,36,232
Rb 12,8,35840
buffer_load_dwordx4 v3, s[20:23], s56 offen offset:2048 sc1 lds
Rt 162,156,96
Mv 120,40,240
Rb 16,8,36864
Rt 164,156,128
)" R"(
Cv 45,45,29
v_add_f32_e32 v15, v15, v46
Mv 124,40,244
Rb 20,8,37888
Rt 166,156,160
v_add_f32_e32 v15, v15, v24
Cv 46,46,24
Mv 124,44,252
Rb 24,8,38912
Rt 168,156,192
v_add_f32_e32 v15, v15, v47
v_add_f32_e32 v15, v15, v25
Mv 120,44,248
Rb 28,8,39936
Cv 47,47,25
v_max_f32_e32 v11, v14, v15
v_mov_b32 v24, v14
Mv 120,48,192
Rb 32,8,40960
v_mov_b32 v25, v15
s_add_u32 s55, s55, 0x10000
Mv 124,48,196
Rb 36,8,41984
s_cmp_eq_u32 s57, 2
s_cselect_b32 s55, s59, s55
v_permlane32_swap_b32_e32 v24, v25
Mv 124,52,204
Rb 40,8,43008
v_add_f32_e32 v24, v24, v25
v_mov_b32 v25, v24
Mv 120,52,200
Rb 44,8,44032
buffer_load_dwordx4 v3, s[20:23], s56 offen offset:3072 sc1 lds
v_permlane16_swap_b32_e32 v24, v25
Mv 120,56,208
v_add_f32_e32 v24, v24, v25
v_rcp_f32_e32 v24, v24
Mv 124,56,212
s_add_u32 s56, s56, 0x10000
v_max_f32_e32 v11, v11, v24
v_cmp_nge_f32_e32 vcc, s62, v11
Mv 124,60,220
v_accvgpr_mov_b32 a104, a200
s_waitcnt lgkmcnt(12)
s_cbranch_vccnz .Lzfx4
.Lzfr4:
Mv 120,60,216
v_mov_b32 v12, v14
v_mov_b32 v13, v15
v_accvgpr_mov_b32 a105, a201
v_mfma_f32_16x16x32_bf16 v[128:131], a[0:3], v[32:35], 0
Rb 48,8,45056
v_accvgpr_mov_b32 a106, a202
v_mfma_f32_16x16x32_bf16 v[136:139], a[0:3], v[36:39], 0
v_accvgpr_mov_b32 a107, a203
s_cmp_le_i32 s57, s58
s_cbranch_scc1 .Lzmk5
.Lzmr5:
v_mfma_f32_16x16x32_bf16 v[140:143], a[4:7], v[36:39], 0
Rb 52,8,46080
s_add_u32 m0, s36, 32768
v_accvgpr_mov_b32 a108, a204
v_mfma_f32_16x16x32_bf16 v[132:135], a[4:7], v[32:35], 0
buffer_load_dwordx4 v2, s[20:23], s55 offen offset:0 sc1 lds
)" R"(
v_mfma_f32_16x16x32_bf16 v[144:147], a[8:11], v[32:35], 0
Rb 56,8,47104
v_and_b32 v11, 14, v0
v_accvgpr_mov_b32 a109, a205
v_mfma_f32_16x16x32_bf16 v[152:155], a[8:11], v[36:39], 0
v_min_u32 v11, 1, v11
v_lshlrev_b32 v11, 13, v11
v_accvgpr_mov_b32 a110, a206
v_mfma_f32_16x16x32_bf16 v[156:159], a[12:15], v[36:39], 0
Rb 60,8,48128
s_waitcnt lgkmcnt(8)
v_mfma_f32_16x16x32_bf16 v[148:151], a[12:15], v[32:35], 0
v_mfma_f32_16x16x16_bf16 a[128:131], a[156:157], a[188:189], 0
v_and_or_b32 v11, v0, 1, v11
v_mul_u32_u24 v11, 0x31000, v11
v_accvgpr_mov_b32 a111, a207
v_mfma_f32_16x16x32_bf16 v[160:163], a[16:19], v[32:35], 0
v_mfma_f32_16x16x16_bf16 a[132:135], a[158:159], a[188:189], 0
Rb 0,8,49152
v_and_or_b32 v11, v0, 48, v11
v_accvgpr_mov_b32 a112, a208
v_mfma_f32_16x16x32_bf16 v[168:171], a[16:19], v[36:39], 0
v_mfma_f32_16x16x16_bf16 a[136:139], a[160:161], a[188:189], 0
s_mul_i32 s83, s33, 50176
s_lshl_b32 s92, s82, 6
v_mfma_f32_16x16x32_bf16 v[172:175], a[20:23], v[36:39], 0
v_mfma_f32_16x16x16_bf16 a[140:143], a[162:163], a[188:189], 0
Rb 4,8,50176
s_add_u32 s83, s83, s92
v_accvgpr_mov_b32 a113, a209
v_mfma_f32_16x16x32_bf16 v[164:167], a[20:23], v[32:35], 0
v_mfma_f32_16x16x16_bf16 a[144:147], a[164:165], a[188:189], 0
buffer_load_dwordx4 v2, s[20:23], s55 offen offset:1024 sc1 lds
v_accvgpr_mov_b32 a114, a210
v_mfma_f32_16x16x32_bf16 v[176:179], a[24:27], v[32:35], 0
v_mfma_f32_16x16x16_bf16 a[148:151], a[166:167], a[188:189], 0
Rb 8,8,51200
s_add_u32 s83, s83, 0xc158000
v_mfma_f32_16x16x32_bf16 v[184:187], a[24:27], v[36:39], 0
v_mfma_f32_16x16x16_bf16 a[152:155], a[168:169], a[188:189], 0
v_exp_f32_e32 v48, v96
v_accvgpr_mov_b32 a115, a211
v_mfma_f32_16x16x32_bf16 v[188:191], a[28:31], v[36:39], 0
Rb 12,8,52224
v_accvgpr_mov_b32 a116, a212
s_waitcnt lgkmcnt(8)
v_mfma_f32_16x16x32_bf16 v[180:183], a[28:31], v[32:35], 0
v_exp_f32_e32 v26, v97
v_accvgpr_mov_b32 a117, a213
v_mfma_f32_16x16x32_bf16 v[192:195], a[32:35], v[32:35], 0
Rb 16,8,53248
v_add_f32_e32 v14, v48, v26
v_mfma_f32_16x16x32_bf16 v[200:203], a[32:35], v[36:39], 0
v_exp_f32_e32 v49, v98
Cv 48,48,26
v_mfma_f32_16x16x32_bf16 v[204:207], a[36:39], v[36:39], 0
Rb 20,8,54272
v_add_f32_e32 v14, v14, v49
v_accvgpr_mov_b32 a118, a214
v_mfma_f32_16x16x32_bf16 v[196:199], a[36:39], v[32:35], 0
buffer_load_dwordx4 v2, s[20:23], s55 offen offset:2048 sc1 lds
v_mfma_f32_16x16x32_bf16 v[208:211], a[40:43], v[32:35], 0
Rb 24,8,55296
v_exp_f32_e32 v27, v99
)" R"(
v_mfma_f32_16x16x32_bf16 v[216:219], a[40:43], v[36:39], 0
buffer_store_dwordx4 a[128:131], v11, s[20:23], s83 offen sc0 sc1
v_accvgpr_mov_b32 a119, a215
v_mfma_f32_16x16x32_bf16 v[220:223], a[44:47], v[36:39], 0
Rb 28,8,56320
s_waitcnt lgkmcnt(8)
v_mfma_f32_16x16x32_bf16 v[212:215], a[44:47], v[32:35], 0
s_add_u32 s83, s83, 7168
v_exp_f32_e32 v50, v104
v_mfma_f32_16x16x32_bf16 v[224:227], a[48:51], v[32:35], 0
Rb 32,8,57344
v_add_f32_e32 v14, v14, v27
v_accvgpr_mov_b32 a120, a216
v_mfma_f32_16x16x32_bf16 v[232:235], a[48:51], v[36:39], 0
v_exp_f32_e32 v28, v105
Cv 49,49,27
v_mfma_f32_16x16x32_bf16 v[236:239], a[52:55], v[36:39], 0
Rb 36,8,58368
v_add_f32_e32 v14, v14, v50
v_accvgpr_mov_b32 a121, a217
v_mfma_f32_16x16x32_bf16 v[228:231], a[52:55], v[32:35], 0
buffer_load_dwordx4 v2, s[20:23], s55 offen offset:3072 sc1 lds
v_accvgpr_mov_b32 a122, a218
v_mfma_f32_16x16x32_bf16 v[240:243], a[56:59], v[32:35], 0
Rb 40,8,59392
v_exp_f32_e32 v51, v106
v_mfma_f32_16x16x32_bf16 v[248:251], a[56:59], v[36:39], 0
buffer_store_dwordx4 a[132:135], v11, s[20:23], s83 offen sc0 sc1
v_accvgpr_mov_b32 a123, a219
v_mfma_f32_16x16x32_bf16 v[252:255], a[60:63], v[36:39], 0
Rb 44,8,60416
v_accvgpr_mov_b32 a124, a220
s_waitcnt lgkmcnt(8)
v_mfma_f32_16x16x32_bf16 v[244:247], a[60:63], v[32:35], 0
s_add_u32 s83, s83, 7168
v_exp_f32_e32 v29, v107
Mp 128,0,40
Rb 48,8,61440
v_add_f32_e32 v14, v14, v28
Mp 136,0,44
buffer_store_dwordx4 a[136:139], v11, s[20:23], s83 offen sc0 sc1
s_add_u32 s83, s83, 7168
Mp 140,4,44
Rb 52,8,62464
s_add_u32 m0, s36, 36864
v_accvgpr_mov_b32 a125, a221
Mp 132,4,40
buffer_load_dwordx4 v3, s[20:23], s55 offen offset:0 sc1 lds
Mp 144,8,40
Rb 56,8,63488
v_exp_f32_e32 v52, v100
Mp 152,8,44
v_exp_f32_e32 v24, v101
v_accvgpr_mov_b32 a126, a222
Mp 156,12,44
Rb 60,8,64512
s_waitcnt lgkmcnt(8)
Mp 148,12,40
buffer_store_dwordx4 a[140:143], v11, s[20:23], s83 offen sc0 sc1
Cv 50,50,28
Mp 160,16,40
s_add_u32 s83, s83, 7168
v_add_f32_e32 v14, v14, v51
v_accvgpr_mov_b32 a127, a223
)" R"(
Mp 168,16,44
v_exp_f32_e32 v53, v102
v_add_f32_e32 v14, v14, v29
Mp 172,20,44
v_exp_f32_e32 v25, v103
Mp 164,20,40
buffer_load_dwordx4 v3, s[20:23], s55 offen offset:1024 sc1 lds
Mp 176,24,40
v_exp_f32_e32 v54, v108
Cv 51,51,29
Mp 184,24,44
buffer_store_dwordx4 a[144:147], v11, s[20:23], s83 offen sc0 sc1
Mp 188,28,44
s_add_u32 s83, s83, 7168
v_add_f32_e32 v15, v52, v24
Mp 180,28,40
Cv 52,52,24
s_waitcnt vmcnt(13) lgkmcnt(0)
s_barrier
buffer_store_dword v6, v7, s[20:23], s65 offen offset:0 sc0 sc1
Mp 192,32,40
Rb 0,1,0
v_add_f32_e32 v15, v15, v53
Mp 200,32,44
Rb 4,1,1024
v_exp_f32_e32 v26, v109
Mp 204,36,44
Rb 8,1,2048
v_add_f32_e32 v15, v15, v25
Mp 196,36,40
Rb 12,1,3072
buffer_load_dwordx4 v3, s[20:23], s55 offen offset:2048 sc1 lds
Mp 208,40,40
Rb 16,1,8192
v_exp_f32_e32 v55, v110
Mp 216,40,44
Rb 20,1,9216
Cv 53,53,25
Mp 220,44,44
Rb 24,1,10240
buffer_store_dwordx4 a[148:151], v11, s[20:23], s83 offen sc0 sc1
Mp 212,44,40
Rb 28,1,11264
s_add_u32 s83, s83, 7168
v_add_f32_e32 v15, v15, v54
Mp 224,48,40
Rb 32,1,4096
v_add_f32_e32 v15, v15, v26
Mp 232,48,44
Rb 36,1,5120
v_exp_f32_e32 v27, v111
Mp 236,52,44
Rb 40,1,6144
Cv 54,54,26
Mp 228,52,40
Rb 44,1,7168
buffer_load_dwordx4 v3, s[20:23], s55 offen offset:3072 sc1 lds
Mp 240,56,40
v_cmp_eq_u32_e32 vcc, s3, v4
s_andn2_b64 s[28:29], s[60:61], vcc
s_cbranch_scc1 .Lzsp11
.Lzok11:
Mp 248,56,44
buffer_store_dwordx4 a[152:155], v11, s[20:23], s83 offen sc0 sc1
Mp 252,60,44
)" R"(
v_add_f32_e32 v15, v15, v55
s_waitcnt lgkmcnt(8)
Mp 244,60,40
v_add_f32_e32 v15, v15, v27
Cv 55,55,27
s_sub_u32 s57, s57, 1
s_cmp_gt_i32 s57, s93
s_cbranch_scc1 .LzL
.LzDA:
s_waitcnt vmcnt(8) lgkmcnt(0)
s_barrier
Rb 0,8,0
Rb 4,8,16384
Rb 8,8,1024
Rb 12,8,17408
Rb 16,8,2048
Rb 20,8,18432
Rb 24,8,3072
Rb 28,8,19456
Rb 32,8,4096
Rb 36,8,20480
Rb 40,8,5120
Rb 44,8,21504
v_exp_f32_e32 v56, v112
v_exp_f32_e32 v28, v113
v_add_f32_e32 v14, v14, v56
v_add_f32_e32 v14, v14, v28
Cv 56,56,28
v_exp_f32_e32 v57, v114
v_exp_f32_e32 v29, v115
v_add_f32_e32 v14, v14, v57
v_add_f32_e32 v14, v14, v29
Cv 57,57,29
v_exp_f32_e32 v58, v120
v_exp_f32_e32 v24, v121
v_add_f32_e32 v14, v14, v58
v_add_f32_e32 v14, v14, v24
Cv 58,58,24
v_exp_f32_e32 v59, v122
v_exp_f32_e32 v25, v123
v_add_f32_e32 v14, v14, v59
v_add_f32_e32 v14, v14, v25
Cv 59,59,25
v_exp_f32_e32 v60, v116
v_exp_f32_e32 v26, v117
v_add_f32_e32 v15, v15, v60
v_add_f32_e32 v15, v15, v26
Cv 60,60,26
v_exp_f32_e32 v61, v118
v_exp_f32_e32 v27, v119
v_add_f32_e32 v15, v15, v61
v_add_f32_e32 v15, v15, v27
Cv 61,61,27
v_exp_f32_e32 v62, v124
v_exp_f32_e32 v28, v125
v_add_f32_e32 v15, v15, v62
v_add_f32_e32 v15, v15, v28
Cv 62,62,28
v_exp_f32_e32 v63, v126
v_exp_f32_e32 v29, v127
v_add_f32_e32 v15, v15, v63
v_add_f32_e32 v15, v15, v29
Cv 63,63,29
v_max_f32_e32 v11, v14, v15
v_cmp_nge_f32_e32 vcc, s62, v11
)" R"(
s_cbranch_vccnz .Lzfx5
.Lzfr5:
s_waitcnt lgkmcnt(8)
Mp 128,0,48
Rb 48,8,6144
s_add_u32 m0, s36, 131072
s_add_u32 s40, s67, 0x20000
s_lshl_b32 s82, s82, 7
Mp 136,0,52
buffer_load_dwordx4 v2, s[20:23], s40 offen offset:0 sc1 lds
s_add_u32 s82, s82, 0xc10c000
Mp 136,4,60
Rb 52,8,22528
v_add_f32_e32 v12, v12, v14
Mp 128,4,56
buffer_load_dwordx4 v2, s[20:23], s40 offen offset:1024 sc1 lds
v_add_f32_e32 v13, v13, v15
Mp 132,8,48
Rb 56,8,7168
v_mov_b32 v24, v12
Mp 140,8,52
s_nop 0
v_permlane32_swap_b32_e32 v12, v24
v_add_f32_e32 v12, v12, v24
s_cmp_lg_u32 s33, 0
s_cmov_b32 s82, 0x40000000
Mp 140,12,60
Rb 60,8,23552
v_mov_b32 v24, v12
s_waitcnt lgkmcnt(8)
Mp 132,12,56
buffer_load_dwordx4 v2, s[20:23], s40 offen offset:2048 sc1 lds
Mp 144,16,48
Rb 0,8,8192
v_permlane16_swap_b32_e32 v12, v24
Mp 152,16,52
buffer_load_dwordx4 v2, s[20:23], s40 offen offset:3072 sc1 lds
v_add_f32_e32 v12, v12, v24
Mp 152,20,60
Rb 4,8,24576
v_mov_b32 v25, v13
Mp 144,20,56
v_rcp_f32_e32 v12, v12
Mp 148,24,48
Rb 8,8,9216
s_add_u32 m0, s36, 135168
v_permlane32_swap_b32_e32 v13, v25
Mp 156,24,52
buffer_load_dwordx4 v3, s[20:23], s40 offen offset:0 sc1 lds
s_cmp_eq_u32 s35, 7
s_cmov_b32 s82, 0x40000000
Mp 156,28,60
Rb 12,8,25600
s_waitcnt lgkmcnt(8)
Mp 148,28,56
buffer_load_dwordx4 v3, s[20:23], s40 offen offset:1024 sc1 lds
v_add_f32_e32 v13, v13, v25
Mp 160,32,48
Rb 16,8,10240
v_mov_b32 v25, v13
Mp 168,32,52
s_nop 0
v_permlane16_swap_b32_e32 v13, v25
v_add_f32_e32 v13, v13, v25
)" R"(
Mp 168,36,60
Rb 20,8,26624
v_mul_f32_e32 v128, v12, v128
Mp 160,36,56
buffer_load_dwordx4 v3, s[20:23], s40 offen offset:2048 sc1 lds
v_mul_f32_e32 v129, v12, v129
Mp 164,40,48
Rb 24,8,11264
s_add_u32 m0, s36, 98304
s_add_u32 s40, s67, 0x30000
Mp 172,40,52
buffer_load_dwordx4 v2, s[20:23], s40 offen offset:0 sc1 lds
Mp 172,44,60
Rb 28,8,27648
Cv 128,128,129
s_waitcnt lgkmcnt(8)
Mp 164,44,56
v_rcp_f32_e32 v13, v13
Mp 176,48,48
Rb 32,8,12288
v_mul_f32_e32 v130, v12, v130
Mp 184,48,52
buffer_load_dwordx4 v2, s[20:23], s40 offen offset:1024 sc1 lds
v_mul_f32_e32 v131, v12, v131
Mp 184,52,60
Rb 36,8,28672
Cv 129,130,131
Mp 176,52,56
buffer_load_dwordx4 v2, s[20:23], s40 offen offset:2048 sc1 lds
Mp 180,56,48
Rb 40,8,13312
v_mul_f32_e32 v132, v12, v132
v_mul_f32_e32 v133, v12, v133
Mp 188,56,52
Cv 130,132,133
v_mul_f32_e32 v134, v12, v134
Mp 188,60,60
Rb 44,8,29696
s_waitcnt lgkmcnt(8)
Mp 180,60,56
buffer_load_dwordx4 v2, s[20:23], s40 offen offset:3072 sc1 lds
v_mul_f32_e32 v135, v12, v135
Mp 192,0,48
Rb 48,8,14336
s_add_u32 m0, s36, 102400
Mp 200,0,52
buffer_load_dwordx4 v3, s[20:23], s40 offen offset:0 sc1 lds
Mp 200,4,60
Rb 52,8,30720
Cv 131,134,135
Mp 192,4,56
v_mul_f32_e32 v136, v13, v136
v_mul_f32_e32 v137, v13, v137
Cv 136,136,137
Mp 196,8,48
Rb 56,8,15360
v_mul_f32_e32 v138, v13, v138
Mp 204,8,52
buffer_load_dwordx4 v3, s[20:23], s40 offen offset:1024 sc1 lds
Mp 204,12,60
Rb 60,8,31744
v_mul_f32_e32 v139, v13, v139
s_waitcnt lgkmcnt(8)
Mp 196,12,56
)" R"(
buffer_load_dwordx4 v3, s[20:23], s40 offen offset:2048 sc1 lds
Mp 208,16,48
Rb 0,1,0
Cv 137,138,139
Mp 216,16,52
v_mul_f32_e32 v140, v13, v140
v_mul_f32_e32 v141, v13, v141
Cv 138,140,141
Mp 216,20,60
Rb 4,1,1024
v_mul_f32_e32 v142, v13, v142
Mp 208,20,56
v_mul_f32_e32 v143, v13, v143
Cv 139,142,143
Mp 212,24,48
Rb 8,1,2048
v_mul_f32_e32 v144, v12, v144
v_mul_f32_e32 v145, v12, v145
Mp 220,24,52
Cv 144,144,145
v_mul_f32_e32 v146, v12, v146
Mp 220,28,60
Rb 12,1,3072
s_waitcnt vmcnt(14) lgkmcnt(4)
s_barrier
Mp 212,28,56
s_add_u32 s40, s67, 0x40000
v_mul_f32_e32 v147, v12, v147
Cv 145,146,147
Mp 224,32,48
Rb 16,1,4096
s_add_u32 m0, s36, 65536
Mp 232,32,52
buffer_load_dwordx4 v2, s[20:23], s40 offen offset:0 sc1 lds
Mp 232,36,60
Rb 20,1,5120
v_mul_f32_e32 v148, v12, v148
Mp 224,36,56
v_mul_f32_e32 v149, v12, v149
Cv 146,148,149
v_mul_f32_e32 v150, v12, v150
Mp 228,40,48
Rb 24,1,6144
v_mul_f32_e32 v151, v12, v151
Mp 236,40,52
Cv 147,150,151
v_mul_f32_e32 v152, v13, v152
Mp 236,44,60
Rb 28,1,8192
v_mul_f32_e32 v153, v13, v153
Mp 228,44,56
buffer_load_dwordx4 v2, s[20:23], s40 offen offset:1024 sc1 lds
Mp 240,48,48
Rb 32,1,9216
Cv 152,152,153
Mp 248,48,52
v_mul_f32_e32 v154, v13, v154
v_mul_f32_e32 v155, v13, v155
Cv 153,154,155
Mp 248,52,60
Rb 36,1,10240
v_mul_f32_e32 v156, v13, v156
Mp 240,52,56
v_mul_f32_e32 v157, v13, v157
Cv 154,156,157
)" R"(
Mp 244,56,48
Rb 40,1,11264
v_mul_f32_e32 v158, v13, v158
v_mul_f32_e32 v159, v13, v159
Mp 252,56,52
buffer_load_dwordx4 v2, s[20:23], s40 offen offset:2048 sc1 lds
Mp 252,60,60
Rb 44,1,12288
s_waitcnt lgkmcnt(8)
Mp 244,60,56
Cv 155,158,159
v_mul_f32_e32 v160, v12, v160
v_mul_f32_e32 v161, v12, v161
v_mfma_f32_16x16x32_bf16 v[32:35], a[0:3], v[128:131], 0
Rb 48,1,13312
Cv 160,160,161
v_mfma_f32_16x16x32_bf16 v[40:43], a[0:3], v[136:139], 0
v_mul_f32_e32 v162, v12, v162
v_mul_f32_e32 v163, v12, v163
v_mfma_f32_16x16x32_bf16 v[44:47], a[4:7], v[136:139], 0
Rb 52,1,14336
Cv 161,162,163
v_mfma_f32_16x16x32_bf16 v[36:39], a[4:7], v[128:131], 0
buffer_load_dwordx4 v2, s[20:23], s40 offen offset:3072 sc1 lds
v_mul_f32_e32 v164, v12, v164
v_mfma_f32_16x16x32_bf16 v[48:51], a[8:11], v[128:131], 0
Rb 56,1,16384
v_mul_f32_e32 v165, v12, v165
v_mfma_f32_16x16x32_bf16 v[56:59], a[8:11], v[136:139], 0
Cv 162,164,165
v_mul_f32_e32 v166, v12, v166
v_mfma_f32_16x16x32_bf16 v[60:63], a[12:15], v[136:139], 0
Rb 60,1,17408
v_mul_f32_e32 v167, v12, v167
s_waitcnt lgkmcnt(8)
v_mfma_f32_16x16x32_bf16 v[52:55], a[12:15], v[128:131], 0
Cv 163,166,167
v_mul_f32_e32 v168, v13, v168
v_mfma_f32_16x16x32_bf16 v[64:67], a[16:19], v[128:131], 0
Rb 0,1,18432
s_add_u32 m0, s36, 69632
v_mfma_f32_16x16x32_bf16 v[72:75], a[16:19], v[136:139], 0
buffer_load_dwordx4 v3, s[20:23], s40 offen offset:0 sc1 lds
v_mul_f32_e32 v169, v13, v169
v_mfma_f32_16x16x32_bf16 v[76:79], a[20:23], v[136:139], 0
Rb 4,1,19456
Cv 168,168,169
v_mfma_f32_16x16x32_bf16 v[68:71], a[20:23], v[128:131], 0
v_mul_f32_e32 v170, v13, v170
v_mul_f32_e32 v171, v13, v171
v_mfma_f32_16x16x32_bf16 v[80:83], a[24:27], v[128:131], 0
Rb 8,1,20480
Cv 169,170,171
v_mul_f32_e32 v172, v13, v172
v_mfma_f32_16x16x32_bf16 v[88:91], a[24:27], v[136:139], 0
v_mul_f32_e32 v173, v13, v173
Cv 170,172,173
v_mfma_f32_16x16x32_bf16 v[92:95], a[28:31], v[136:139], 0
Rb 12,1,21504
s_waitcnt lgkmcnt(8)
v_mfma_f32_16x16x32_bf16 v[84:87], a[28:31], v[128:131], 0
buffer_load_dwordx4 v3, s[20:23], s40 offen offset:1024 sc1 lds
v_mul_f32_e32 v174, v13, v174
Mp 32,32,144
)" R"(
Rb 16,1,22528
v_mul_f32_e32 v175, v13, v175
Mp 40,32,152
Cv 171,174,175
v_mul_f32_e32 v176, v12, v176
Mp 44,36,152
Rb 20,1,24576
v_mul_f32_e32 v177, v12, v177
Mp 36,36,144
Cv 176,176,177
v_mul_f32_e32 v178, v12, v178
v_mul_f32_e32 v179, v12, v179
Mp 48,40,144
Rb 24,1,25600
Cv 177,178,179
Mp 56,40,152
buffer_load_dwordx4 v3, s[20:23], s40 offen offset:2048 sc1 lds
Mp 60,44,152
Rb 28,1,26624
v_mul_f32_e32 v180, v12, v180
s_waitcnt lgkmcnt(8)
Mp 52,44,144
v_mul_f32_e32 v181, v12, v181
Cv 178,180,181
Mp 64,48,144
Rb 32,1,27648
v_mul_f32_e32 v182, v12, v182
Mp 72,48,152
v_mul_f32_e32 v183, v12, v183
Cv 179,182,183
v_mul_f32_e32 v184, v13, v184
Mp 76,52,152
Rb 36,1,28672
v_mul_f32_e32 v185, v13, v185
Mp 68,52,144
Cv 184,184,185
v_mul_f32_e32 v186, v13, v186
Mp 80,56,144
Rb 40,1,29696
v_mul_f32_e32 v187, v13, v187
Cv 185,186,187
Mp 88,56,152
v_mul_f32_e32 v188, v13, v188
v_mul_f32_e32 v189, v13, v189
Mp 92,60,152
Rb 44,1,30720
s_waitcnt lgkmcnt(8)
Mp 84,60,144
Cv 186,188,189
v_mul_f32_e32 v190, v13, v190
v_mul_f32_e32 v191, v13, v191
Mp 32,0,160
Rb 48,1,32768
Cv 187,190,191
Mp 40,0,168
v_mul_f32_e32 v192, v12, v192
v_mul_f32_e32 v193, v12, v193
Mp 44,4,168
Rb 52,1,33792
Cv 192,192,193
Mp 36,4,160
v_mul_f32_e32 v194, v12, v194
v_mul_f32_e32 v195, v12, v195
Cv 193,194,195
)" R"(
Mp 48,8,160
Rb 56,1,34816
v_mul_f32_e32 v196, v12, v196
Mp 56,8,168
v_mul_f32_e32 v197, v12, v197
Cv 194,196,197
Mp 60,12,168
Rb 60,1,35840
s_waitcnt vmcnt(14) lgkmcnt(4)
s_barrier
buffer_store_dword v6, v7, s[20:23], s82 offen offset:0 sc0 sc1
Mp 52,12,160
s_add_u32 s40, s67, 0x50000
v_mul_f32_e32 v198, v12, v198
Mp 64,16,160
Rb 0,1,36864
s_add_u32 m0, s36, 0
Mp 72,16,168
buffer_load_dwordx4 v2, s[20:23], s40 offen offset:0 sc1 lds
v_mul_f32_e32 v199, v12, v199
Mp 76,20,168
Rb 4,1,37888
Cv 195,198,199
Mp 68,20,160
v_mul_f32_e32 v200, v13, v200
v_mul_f32_e32 v201, v13, v201
Mp 80,24,160
Rb 8,1,38912
Cv 200,200,201
v_mul_f32_e32 v202, v13, v202
Mp 88,24,168
v_mul_f32_e32 v203, v13, v203
Cv 201,202,203
Mp 92,28,168
Rb 12,1,40960
Mp 84,28,160
buffer_load_dwordx4 v2, s[20:23], s40 offen offset:1024 sc1 lds
v_mul_f32_e32 v204, v13, v204
Mp 32,32,176
Rb 16,1,41984
v_mul_f32_e32 v205, v13, v205
Mp 40,32,184
Cv 202,204,205
v_mul_f32_e32 v206, v13, v206
Mp 44,36,184
Rb 20,1,43008
v_mul_f32_e32 v207, v13, v207
Mp 36,36,176
Cv 203,206,207
v_mul_f32_e32 v208, v12, v208
v_mul_f32_e32 v209, v12, v209
Mp 48,40,176
Rb 24,1,44032
Cv 208,208,209
Mp 56,40,184
buffer_load_dwordx4 v2, s[20:23], s40 offen offset:2048 sc1 lds
Mp 60,44,184
Rb 28,1,45056
v_mul_f32_e32 v210, v12, v210
s_waitcnt lgkmcnt(8)
Mp 52,44,176
v_mul_f32_e32 v211, v12, v211
Cv 209,210,211
Mp 64,48,176
Rb 32,1,46080
)" R"(
v_mul_f32_e32 v212, v12, v212
Mp 72,48,184
v_mul_f32_e32 v213, v12, v213
Cv 210,212,213
v_mul_f32_e32 v214, v12, v214
Mp 76,52,184
Rb 36,1,47104
v_mul_f32_e32 v215, v12, v215
Mp 68,52,176
buffer_load_dwordx4 v2, s[20:23], s40 offen offset:3072 sc1 lds
Mp 80,56,176
Rb 40,1,49152
Cv 211,214,215
v_mul_f32_e32 v216, v13, v216
Mp 88,56,184
v_mul_f32_e32 v217, v13, v217
Cv 216,216,217
Mp 92,60,184
Rb 44,1,50176
s_waitcnt lgkmcnt(8)
Mp 84,60,176
v_mul_f32_e32 v218, v13, v218
v_mul_f32_e32 v219, v13, v219
Cv 217,218,219
Mp 32,0,192
Rb 48,1,51200
s_add_u32 m0, s36, 4096
Mp 40,0,200
buffer_load_dwordx4 v3, s[20:23], s40 offen offset:0 sc1 lds
Mp 44,4,200
Rb 52,1,52224
v_mul_f32_e32 v220, v13, v220
Mp 36,4,192
v_mul_f32_e32 v221, v13, v221
Cv 218,220,221
v_mul_f32_e32 v222, v13, v222
Mp 48,8,192
Rb 56,1,53248
v_mul_f32_e32 v223, v13, v223
Mp 56,8,200
Cv 219,222,223
v_mul_f32_e32 v224, v12, v224
Mp 60,12,200
Rb 60,1,54272
v_mul_f32_e32 v225, v12, v225
s_waitcnt lgkmcnt(8)
Mp 52,12,192
buffer_load_dwordx4 v3, s[20:23], s40 offen offset:1024 sc1 lds
Mp 64,16,192
Rb 0,1,55296
Cv 224,224,225
Mp 72,16,200
v_mul_f32_e32 v226, v12, v226
v_mul_f32_e32 v227, v12, v227
Cv 225,226,227
Mp 76,20,200
Rb 4,1,57344
v_mul_f32_e32 v228, v12, v228
Mp 68,20,192
v_mul_f32_e32 v229, v12, v229
Cv 226,228,229
Mp 80,24,192
Rb 8,1,58368
v_mul_f32_e32 v230, v12, v230
)" R"(
v_mul_f32_e32 v231, v12, v231
Mp 88,24,200
buffer_load_dwordx4 v3, s[20:23], s40 offen offset:2048 sc1 lds
Mp 92,28,200
Rb 12,1,59392
s_waitcnt lgkmcnt(8)
Mp 84,28,192
Cv 227,230,231
v_mul_f32_e32 v232, v13, v232
v_mul_f32_e32 v233, v13, v233
Mp 32,32,208
Rb 16,1,60416
Cv 232,232,233
Mp 40,32,216
v_mul_f32_e32 v234, v13, v234
v_mul_f32_e32 v235, v13, v235
Mp 44,36,216
Rb 20,1,61440
Cv 233,234,235
Mp 36,36,208
v_mul_f32_e32 v236, v13, v236
v_mul_f32_e32 v237, v13, v237
Cv 234,236,237
Mp 48,40,208
Rb 24,1,62464
v_mul_f32_e32 v238, v13, v238
Mp 56,40,216
v_mul_f32_e32 v239, v13, v239
Cv 235,238,239
Mp 60,44,216
Rb 28,1,63488
v_mul_f32_e32 v240, v12, v240
s_waitcnt lgkmcnt(8)
Mp 52,44,208
v_mul_f32_e32 v241, v12, v241
Cv 240,240,241
Mp 64,48,208
Rb 32,10,0
v_mul_f32_e32 v242, v12, v242
Mp 72,48,216
v_mul_f32_e32 v243, v12, v243
Cv 241,242,243
v_mul_f32_e32 v244, v12, v244
Mp 76,52,216
Rb 36,10,1024
v_mul_f32_e32 v245, v12, v245
Mp 68,52,208
Cv 242,244,245
v_mul_f32_e32 v246, v12, v246
Mp 80,56,208
Rb 40,10,2048
v_mul_f32_e32 v247, v12, v247
Cv 243,246,247
Mp 88,56,216
v_mul_f32_e32 v248, v13, v248
v_mul_f32_e32 v249, v13, v249
Mp 92,60,216
Rb 44,10,3072
s_waitcnt vmcnt(15) lgkmcnt(4)
s_barrier
Mp 84,60,208
s_add_u32 s40, s67, 0x60000
Cv 248,248,249
v_mul_f32_e32 v250, v13, v250
Mp 32,0,224
)" R"(
Rb 48,10,4096
s_add_u32 m0, s36, 32768
Mp 40,0,232
buffer_load_dwordx4 v2, s[20:23], s40 offen offset:0 sc1 lds
Mp 44,4,232
Rb 52,10,5120
v_mul_f32_e32 v251, v13, v251
Mp 36,4,224
Cv 249,250,251
v_mul_f32_e32 v252, v13, v252
v_mul_f32_e32 v253, v13, v253
Mp 48,8,224
Rb 56,10,6144
Cv 250,252,253
Mp 56,8,232
v_mul_f32_e32 v254, v13, v254
v_mul_f32_e32 v255, v13, v255
Mp 60,12,232
Rb 60,10,8192
Cv 251,254,255
Mp 52,12,224
buffer_load_dwordx4 v2, s[20:23], s40 offen offset:1024 sc1 lds
Mp 64,16,224
Rb 0,10,9216
s_lshl4_add_u32 s28, s34, s35
Mp 72,16,232
s_lshl_b32 s64, s28, 17
s_add_u32 s64, s64, 0x4000000
Mp 76,20,232
Rb 4,10,10240
s_xor_b32 s66, s64, 0x202000
Mp 68,20,224
s_sub_u32 s66, s66, s84
s_sub_u32 s66, s66, s84
Mp 80,24,224
Rb 8,10,11264
s_lshl2_add_u32 s65, s2, s33
Mp 88,24,232
buffer_load_dwordx4 v2, s[20:23], s40 offen offset:2048 sc1 lds
Mp 92,28,232
Rb 12,10,12288
s_waitcnt lgkmcnt(8)
Mp 84,28,224
s_lshl_b32 s65, s65, 7
s_add_u32 s65, s65, 0xc1c0000
Mp 32,32,240
Rb 16,10,13312
s_xor_b32 s52, s65, 4096
Mp 40,32,248
Mp 44,36,248
Rb 20,10,14336
Mp 36,36,240
buffer_load_dwordx4 v2, s[20:23], s40 offen offset:3072 sc1 lds
Mp 48,40,240
Rb 24,10,16384
Mp 56,40,248
Mp 60,44,248
Rb 28,10,17408
s_waitcnt lgkmcnt(8)
Mp 52,44,240
Mp 64,48,240
Rb 32,10,18432
s_add_u32 m0, s36, 36864
Mp 72,48,248
)" R"(
buffer_load_dwordx4 v3, s[20:23], s40 offen offset:0 sc1 lds
Mp 76,52,248
Rb 36,10,19456
Mp 68,52,240
Mp 80,56,240
Rb 40,10,20480
Mp 88,56,248
Mp 92,60,248
Rb 44,10,21504
s_waitcnt lgkmcnt(8)
Mp 84,60,240
buffer_load_dwordx4 v3, s[20:23], s40 offen offset:1024 sc1 lds
v_mfma_f32_16x16x32_bf16 v[96:99], a[0:3], v[128:131], 0
Rb 48,10,22528
v_mfma_f32_16x16x32_bf16 v[104:107], a[0:3], v[136:139], 0
Mp 104,4,152
Rb 52,10,24576
Mp 96,4,144
v_bfe_u32 v11, v0, 2, 2
v_lshrrev_b32 v4, 4, v0
Cv 32,32,33
Mp 96,8,160
Rb 56,10,25600
v_xor_b32 v11, v11, v4
Cv 33,34,35
Mp 104,8,168
buffer_load_dwordx4 v3, s[20:23], s40 offen offset:2048 sc1 lds
Cv 34,36,37
Mp 104,12,184
Rb 60,10,26624
Cv 35,38,39
s_waitcnt lgkmcnt(8)
Mp 96,12,176
v_bfe_u32 v4, v0, 1, 1
v_lshl_or_b32 v11, v11, 1, v4
Mp 96,16,192
Rb 0,10,27648
v_and_b32 v4, 1, v0
Mp 104,16,200
v_mul_u32_u24 v4, 0xffff, v4
v_add_u32 v4, 1, v4
Cv 40,40,41
Mp 104,20,216
Rb 4,10,28672
v_mul_u32_u24 v4, 0x3f80, v4
buffer_store_dwordx4 v[32:35], v2, s[20:23], s64 offen offset:0 sc0 sc1
Mp 96,20,208
v_cmp_eq_u32_e32 vcc, 0, v11
v_cmp_eq_u32_e64 s[28:29], 1, v11
Cv 41,42,43
Mp 96,24,224
Rb 8,10,29696
v_accvgpr_write_b32 a192, 0
Cv 42,44,45
Mp 104,24,232
v_accvgpr_write_b32 a193, 0
v_accvgpr_write_b32 a196, 0
Cv 43,46,47
Mp 104,28,248
Rb 12,10,30720
s_waitcnt lgkmcnt(8)
Mp 96,28,240
v_accvgpr_write_b32 a197, 0
v_cndmask_b32_e32 v11, 0, v4, vcc
)" R"(
v_mfma_f32_16x16x32_bf16 v[100:103], a[32:35], v[128:131], 0
Rb 16,8,32768
v_cndmask_b32_e64 v4, 0, v4, s[28:29]
buffer_store_dwordx4 v[40:43], v2, s[20:23], s64 offen offset:1024 sc0 sc1
v_mfma_f32_16x16x32_bf16 v[108:111], a[32:35], v[136:139], 0
v_accvgpr_write_b32 a194, v11
v_accvgpr_write_b32 a195, v4
Mp 108,36,152
Rb 20,8,33792
Cv 48,48,49
Mp 100,36,144
Cv 49,50,51
Cv 50,52,53
Mp 100,40,160
Rb 24,8,34816
Cv 51,54,55
Mp 108,40,168
buffer_store_dwordx4 v[48:51], v2, s[20:23], s64 offen offset:2048 sc0 sc1
Mp 108,44,184
Rb 28,8,35840
s_waitcnt vmcnt(18) lgkmcnt(4)
s_barrier
Mp 100,44,176
s_add_u32 s40, s67, 0x70000
Cv 56,56,57
Mp 100,48,192
Rb 32,8,36864
s_add_u32 m0, s36, 131072
Mp 108,48,200
buffer_load_dwordx4 v2, s[20:23], s40 offen offset:0 sc1 lds
Mp 108,52,216
Rb 36,8,37888
Cv 57,58,59
Mp 100,52,208
Cv 58,60,61
Cv 59,62,63
Mp 100,56,224
Rb 40,8,38912
buffer_store_dwordx4 v[56:59], v2, s[20:23], s64 offen offset:3072 sc0 sc1
Mp 108,56,232
Cv 64,64,65
Cv 65,66,67
Mp 108,60,248
Rb 44,8,40960
Mp 100,60,240
buffer_load_dwordx4 v2, s[20:23], s40 offen offset:1024 sc1 lds
v_mfma_f32_16x16x32_bf16 v[112:115], a[0:3], v[128:131], 0
Rb 48,8,41984
Cv 66,68,69
v_mfma_f32_16x16x32_bf16 v[120:123], a[0:3], v[136:139], 0
Cv 67,70,71
Cv 72,72,73
Mp 120,4,152
Rb 52,8,43008
buffer_store_dwordx4 v[64:67], v3, s[20:23], s64 offen offset:0 sc0 sc1
Mp 112,4,144
Cv 73,74,75
Cv 74,76,77
Mp 112,8,160
Rb 56,8,44032
Cv 75,78,79
Mp 120,8,168
buffer_load_dwordx4 v2, s[20:23], s40 offen offset:2048 sc1 lds
Mp 120,12,184
Rb 60,8,45056
)" R"(
s_waitcnt lgkmcnt(8)
Mp 112,12,176
buffer_store_dwordx4 v[72:75], v3, s[20:23], s64 offen offset:1024 sc0 sc1
Mp 112,16,192
Rb 0,8,46080
Cv 80,80,81
Mp 120,16,200
Cv 81,82,83
Cv 82,84,85
Mp 120,20,216
Rb 4,8,47104
Cv 83,86,87
Mp 112,20,208
buffer_load_dwordx4 v2, s[20:23], s40 offen offset:3072 sc1 lds
Mp 112,24,224
Rb 8,8,49152
Mp 120,24,232
buffer_store_dwordx4 v[80:83], v3, s[20:23], s64 offen offset:2048 sc0 sc1
Mp 120,28,248
Rb 12,8,50176
s_waitcnt lgkmcnt(8)
Mp 112,28,240
Cv 88,88,89
Cv 89,90,91
v_mfma_f32_16x16x32_bf16 v[116:119], a[32:35], v[128:131], 0
Rb 16,8,51200
s_add_u32 m0, s36, 135168
v_mfma_f32_16x16x32_bf16 v[124:127], a[32:35], v[136:139], 0
buffer_load_dwordx4 v3, s[20:23], s40 offen offset:0 sc1 lds
Mp 124,36,152
Rb 20,8,52224
Cv 90,92,93
Mp 116,36,144
Cv 91,94,95
Mp 116,40,160
Rb 24,8,53248
buffer_store_dwordx4 v[88:91], v3, s[20:23], s64 offen offset:3072 sc0 sc1
Mp 124,40,168
Cv 96,96,97
Cv 97,98,99
Mp 124,44,184
Rb 28,8,54272
s_waitcnt lgkmcnt(8)
Mp 116,44,176
buffer_load_dwordx4 v3, s[20:23], s40 offen offset:1024 sc1 lds
Mp 116,48,192
Rb 32,8,55296
Cv 98,100,101
Mp 124,48,200
Cv 99,102,103
Mp 124,52,216
Rb 36,8,57344
Cv 104,104,105
Mp 116,52,208
s_add_u32 s64, s64, 0x10000
buffer_store_dwordx4 v[96:99], v2, s[20:23], s64 offen offset:0 sc0 sc1
Mp 116,56,224
Rb 40,8,58368
Cv 105,106,107
Mp 124,56,232
buffer_load_dwordx4 v3, s[20:23], s40 offen offset:2048 sc1 lds
Mp 124,60,248
Rb 44,8,59392
s_waitcnt lgkmcnt(8)
)" R"(
Mp 116,60,240
Cv 106,108,109
Cv 107,110,111
v_mfma_f32_16x16x32_bf16 v[32:35], a[0:3], v[128:131], 0
Rb 48,8,60416
s_lshl_b32 s55, s85, 7
v_mfma_f32_16x16x32_bf16 v[40:43], a[0:3], v[136:139], 0
buffer_store_dwordx4 v[104:107], v2, s[20:23], s64 offen offset:1024 sc0 sc1
Mp 40,4,152
Rb 52,8,61440
s_add_u32 s55, s55, 0xc108c00
Mp 32,4,144
s_cmp_lg_u32 s33, 0
s_cmov_b32 s55, 0x40000000
Mp 32,8,160
Rb 56,8,62464
Cv 112,112,113
Mp 40,8,168
Cv 113,114,115
Cv 114,116,117
Mp 40,12,184
Rb 60,8,63488
s_waitcnt lgkmcnt(8)
Mp 32,12,176
Cv 115,118,119
v_mov_b32 v11, 1
Mp 32,16,192
Rb 0,8,0
v_add_u32 v9, s36, v8
Mp 40,16,200
buffer_store_dwordx4 v[112:115], v2, s[20:23], s64 offen offset:2048 sc0 sc1
Mp 40,20,216
Rb 4,8,1024
Mp 32,20,208
Mp 32,24,224
Rb 8,8,2048
Cv 120,120,121
Mp 40,24,232
Cv 121,122,123
Cv 122,124,125
Mp 40,28,248
Rb 12,8,3072
s_waitcnt vmcnt(5) lgkmcnt(8)
s_barrier
buffer_store_dword v6, v7, s[20:23], s65 offen offset:0 sc0 sc1
Mp 32,28,240
Cv 123,126,127
v_mfma_f32_16x16x32_bf16 v[36:39], a[32:35], v[128:131], 0
Rb 16,8,4096
v_mfma_f32_16x16x32_bf16 v[44:47], a[32:35], v[136:139], 0
buffer_store_dwordx4 v[120:123], v2, s[20:23], s64 offen offset:3072 sc0 sc1
Mp 44,36,152
Rb 20,8,5120
Mp 36,36,144
buffer_atomic_add v11, v7, s[20:23], s55 offen sc0 sc1
Mp 36,40,160
Rb 24,8,6144
Mp 44,40,168
Mp 44,44,184
Rb 28,8,8192
s_waitcnt lgkmcnt(8)
Mp 36,44,176
Mp 36,48,192
Rb 32,8,9216
Mp 44,48,200
)" R"(
Mp 44,52,216
Rb 36,8,10240
Mp 36,52,208
Mp 36,56,224
Rb 40,8,11264
Mp 44,56,232
Mp 44,60,248
Rb 44,8,12288
s_waitcnt lgkmcnt(8)
Mp 36,60,240
buffer_load_dword v4, v7, s[20:23], s52 offen offset:0 sc0 sc1
v_mfma_f32_16x16x32_bf16 v[48:51], a[0:3], v[128:131], 0
Rb 48,8,13312
v_mfma_f32_16x16x32_bf16 v[56:59], a[0:3], v[136:139], 0
Mp 56,4,152
Rb 52,8,14336
Mp 48,4,144
Mp 48,8,160
Rb 56,8,16384
Cv 32,32,33
Mp 56,8,168
Cv 33,34,35
Cv 34,36,37
Mp 56,12,184
Rb 60,8,17408
s_waitcnt lgkmcnt(8)
Mp 48,12,176
Cv 35,38,39
Mp 48,16,192
Rb 0,8,18432
Mp 56,16,200
buffer_store_dwordx4 v[32:35], v3, s[20:23], s64 offen offset:0 sc0 sc1
Mp 56,20,216
Rb 4,8,19456
Mp 48,20,208
Mp 48,24,224
Rb 8,8,20480
Cv 40,40,41
Mp 56,24,232
Cv 41,42,43
Cv 42,44,45
Mp 56,28,248
Rb 12,8,21504
s_waitcnt lgkmcnt(8)
Mp 48,28,240
Cv 43,46,47
v_mfma_f32_16x16x32_bf16 v[52:55], a[32:35], v[128:131], 0
Rb 16,8,22528
v_mfma_f32_16x16x32_bf16 v[60:63], a[32:35], v[136:139], 0
buffer_store_dwordx4 v[40:43], v3, s[20:23], s64 offen offset:1024 sc0 sc1
Mp 60,36,152
Rb 20,8,24576
Mp 52,36,144
Mp 52,40,160
Rb 24,8,25600
Mp 60,40,168
Mp 60,44,184
Rb 28,8,26624
s_waitcnt vmcnt(2) lgkmcnt(8)
v_cmp_eq_u32_e32 vcc, s3, v4
s_andn2_b64 s[28:29], 1, vcc
s_cbranch_scc1 .Lzsp12
.Lzok12:
Mp 52,44,176
)" R"(
buffer_load_dwordx4 a[64:67], v2, s[20:23], s66 offen offset:0 sc0 sc1
Mp 52,48,192
Rb 32,8,27648
Mp 60,48,200
Mp 60,52,216
Rb 36,8,28672
Mp 52,52,208
buffer_load_dwordx4 a[68:71], v2, s[20:23], s66 offen offset:1024 sc0 sc1
Mp 52,56,224
Rb 40,8,29696
Mp 60,56,232
Mp 60,60,248
Rb 44,8,30720
s_waitcnt lgkmcnt(8)
Mp 52,60,240
buffer_load_dwordx4 a[72:75], v2, s[20:23], s66 offen offset:2048 sc0 sc1
v_mfma_f32_16x16x32_bf16 v[64:67], a[0:3], v[128:131], 0
Rb 48,1,0
v_mfma_f32_16x16x32_bf16 v[72:75], a[0:3], v[136:139], 0
Mp 72,4,152
Rb 52,1,1024
Mp 64,4,144
buffer_load_dwordx4 a[76:79], v2, s[20:23], s66 offen offset:3072 sc0 sc1
Mp 64,8,160
Rb 56,1,2048
Mp 72,8,168
Mp 72,12,184
Rb 60,1,3072
s_waitcnt vmcnt(4) lgkmcnt(8)
s_barrier
buffer_store_dword v6, v7, s[20:23], s65 offen offset:4 sc0 sc1
Mp 64,12,176
buffer_load_dwordx4 a[80:83], v3, s[20:23], s66 offen offset:0 sc0 sc1
Mp 64,16,192
Rb 0,1,4096
Mp 72,16,200
Mp 72,20,216
Rb 4,1,5120
Mp 64,20,208
buffer_load_dwordx4 a[84:87], v3, s[20:23], s66 offen offset:1024 sc0 sc1
Mp 64,24,224
Rb 8,1,6144
Mp 72,24,232
Mp 72,28,248
Rb 12,1,8192
s_waitcnt lgkmcnt(8)
Mp 64,28,240
buffer_load_dwordx4 a[88:91], v3, s[20:23], s66 offen offset:2048 sc0 sc1
v_mfma_f32_16x16x32_bf16 v[68:71], a[32:35], v[128:131], 0
Rb 16,1,9216
v_mfma_f32_16x16x32_bf16 v[76:79], a[32:35], v[136:139], 0
Mp 76,36,152
Rb 20,1,10240
Mp 68,36,144
buffer_load_dwordx4 a[92:95], v3, s[20:23], s66 offen offset:3072 sc0 sc1
Mp 68,40,160
Rb 24,1,11264
Mp 76,40,168
Mp 76,44,184
Rb 28,1,12288
s_waitcnt lgkmcnt(8)
Mp 68,44,176
buffer_load_dword v4, v7, s[20:23], s52 offen offset:4 sc0 sc1
Mp 68,48,192
Rb 32,1,13312
)" R"(
Mp 76,48,200
Mp 76,52,216
Rb 36,1,14336
Mp 68,52,208
Mp 68,56,224
Rb 40,1,16384
Mp 76,56,232
Mp 76,60,248
Rb 44,1,17408
s_waitcnt vmcnt(6)
Mp 68,60,240
Mv 48,194,64
Mv 56,194,68
Mv 52,192,64
Mv 60,192,68
Mv 64,194,72
Mv 72,194,76
Mv 68,192,72
s_waitcnt lgkmcnt(8)
Mv 76,192,76
v_mfma_f32_16x16x32_bf16 v[80:83], a[0:3], v[128:131], 0
Rb 48,1,18432
Cv 48,48,49
v_mfma_f32_16x16x32_bf16 v[88:91], a[0:3], v[136:139], 0
Cv 49,50,51
Cv 50,52,53
Mp 88,4,152
Rb 52,1,19456
Cv 51,54,55
Mp 80,4,144
s_add_u32 s29, s73, 0x0
buffer_store_dwordx4 v[48:51], v1, s[20:23], s29 offen sc0 sc1
Mp 80,8,160
Rb 56,1,20480
Mp 88,8,168
Mp 88,12,184
Rb 60,1,21504
s_waitcnt vmcnt(1) lgkmcnt(8)
v_cmp_eq_u32_e32 vcc, s3, v4
s_andn2_b64 s[28:29], 1, vcc
s_cbranch_scc1 .Lzsp13
.Lzok13:
Mp 80,12,176
s_add_u32 s66, s66, 0x10000
buffer_load_dwordx4 a[96:99], v2, s[20:23], s66 offen offset:0 sc0 sc1
Mp 80,16,192
Rb 0,1,22528
Cv 56,56,57
Mp 88,16,200
Cv 57,58,59
Cv 58,60,61
Mp 88,20,216
Rb 4,1,24576
Cv 59,62,63
Mp 80,20,208
buffer_load_dwordx4 a[100:103], v2, s[20:23], s66 offen offset:1024 sc0 sc1
Mp 80,24,224
Rb 8,1,25600
s_mov_b32 s8, s74
Mp 88,24,232
s_add_u32 s29, s73, 0x400
buffer_store_dwordx4 v[56:59], v1, s[20:23], s29 offen sc0 sc1
Mp 88,28,248
Rb 12,1,26624
)" R"(
s_waitcnt lgkmcnt(8)
Mp 80,28,240
buffer_load_dwordx4 a[104:107], v2, s[20:23], s66 offen offset:2048 sc0 sc1
v_mfma_f32_16x16x32_bf16 v[84:87], a[32:35], v[128:131], 0
Rb 16,1,27648
Cv 64,64,65
v_mfma_f32_16x16x32_bf16 v[92:95], a[32:35], v[136:139], 0
Cv 65,66,67
Cv 66,68,69
Mp 92,36,152
Rb 20,1,28672
Cv 67,70,71
Mp 84,36,144
buffer_load_dwordx4 a[108:111], v2, s[20:23], s66 offen offset:3072 sc0 sc1
Mp 84,40,160
Rb 24,1,29696
s_mov_b32 s9, s75
Mp 92,40,168
s_add_u32 s29, s73, 0x800
buffer_store_dwordx4 v[64:67], v1, s[20:23], s29 offen sc0 sc1
Mp 92,44,184
Rb 28,1,30720
s_waitcnt lgkmcnt(8)
Mp 84,44,176
buffer_load_dwordx4 a[112:115], v3, s[20:23], s66 offen offset:0 sc0 sc1
ds_write_b32 v9, v11 offset:39936
Mp 84,48,192
Rb 32,1,32768
Cv 72,72,73
Mp 92,48,200
Cv 73,74,75
Cv 74,76,77
Mp 92,52,216
Rb 36,1,33792
Cv 75,78,79
Mp 84,52,208
buffer_load_dwordx4 a[116:119], v3, s[20:23], s66 offen offset:1024 sc0 sc1
Mp 84,56,224
Rb 40,1,34816
s_mov_b32 s10, 0x1c0000
Mp 92,56,232
s_add_u32 s29, s73, 0xc00
buffer_store_dwordx4 v[72:75], v1, s[20:23], s29 offen sc0 sc1
Mp 92,60,248
Rb 44,1,35840
s_waitcnt lgkmcnt(3)
s_barrier
Mp 84,60,240
ds_read_b32 v11, v8 offset:39936
s_mov_b32 s11, 0x20000
Mv 80,194,80
v_and_b32 v174, 1, v0
v_mul_u32_u24 v174, 0xffff, v174
Mv 88,194,84
v_add_u32 v174, 1, v174
v_mul_u32_u24 v174, 0x3f80, v174
Mv 84,192,80
v_bfe_u32 v173, v0, 1, 3
v_cmp_eq_u32_e32 vcc, 0, v173
Mv 92,192,84
s_nop 0
v_cndmask_b32_e32 v132, 0, v174, vcc
v_cmp_eq_u32_e32 vcc, 1, v173
v_mfma_f32_16x16x32_bf16 v[96:99], a[0:3], v[128:131], 0
Rb 48,1,36864
)" R"(
v_cndmask_b32_e32 v133, 0, v174, vcc
v_mfma_f32_16x16x32_bf16 v[104:107], a[0:3], v[136:139], 0
v_cmp_eq_u32_e32 vcc, 2, v173
v_and_b32 v167, 0xf0, v1
Mp 104,4,152
Rb 52,1,37888
s_waitcnt lgkmcnt(2)
Mp 96,4,144
v_readfirstlane_b32 s82, v11
v_cndmask_b32_e32 v134, 0, v174, vcc
Mp 96,8,160
Rb 56,1,38912
Cv 80,80,81
Mp 104,8,168
Cv 81,82,83
Cv 82,84,85
Mp 104,12,184
Rb 60,1,40960
Cv 83,86,87
Mp 96,12,176
s_add_u32 s29, s73, 0x1000
buffer_store_dwordx4 v[80:83], v1, s[20:23], s29 offen sc0 sc1
Mp 96,16,192
Rb 0,1,41984
v_cmp_eq_u32_e32 vcc, 3, v173
Mp 104,16,200
s_nop 0
v_cndmask_b32_e32 v135, 0, v174, vcc
v_cmp_eq_u32_e32 vcc, 4, v173
Mp 104,20,216
Rb 4,1,43008
Mp 96,20,208
Cv 88,88,89
Cv 89,90,91
Mp 96,24,224
Rb 8,1,44032
Cv 90,92,93
Mp 104,24,232
Cv 91,94,95
v_cndmask_b32_e32 v140, 0, v174, vcc
Mp 104,28,248
Rb 12,1,45056
v_cmp_eq_u32_e32 vcc, 5, v173
Mp 96,28,240
s_add_u32 s29, s73, 0x1400
buffer_store_dwordx4 v[88:91], v1, s[20:23], s29 offen sc0 sc1
v_mfma_f32_16x16x32_bf16 v[100:103], a[32:35], v[128:131], 0
Rb 16,1,46080
v_cndmask_b32_e32 v141, 0, v174, vcc
v_mfma_f32_16x16x32_bf16 v[108:111], a[32:35], v[136:139], 0
v_cmp_eq_u32_e32 vcc, 6, v173
v_mul_u32_u24 v166, 56, v167
Mp 108,36,152
Rb 20,1,47104
s_waitcnt lgkmcnt(8)
Mp 100,36,144
v_cndmask_b32_e32 v142, 0, v174, vcc
v_cmp_eq_u32_e32 vcc, 7, v173
Mp 100,40,160
Rb 24,1,49152
v_cndmask_b32_e32 v143, 0, v174, vcc
Mp 108,40,168
v_lshrrev_b32 v173, 4, v0
v_lshl_or_b32 v164, v173, 16, v167
)" R"(
Mp 108,44,184
Rb 28,1,50176
v_lshl_add_u32 v166, v173, 3, v166
Mp 100,44,176
v_and_b32 v174, 7, v0
v_and_b32 v165, 0x80, v1
Mp 100,48,192
Rb 32,1,51200
v_lshl_or_b32 v165, v174, 8, v165
Mp 108,48,200
s_mov_b32 s93, 0x1111
Mp 108,52,216
Rb 36,1,52224
s_waitcnt lgkmcnt(10)
Mp 100,52,208
Mp 100,56,224
Rb 40,1,53248
Mp 108,56,232
Mp 108,60,248
Rb 44,1,54272
Mp 100,60,240
Mv 96,194,88
Mv 104,194,92
Mv 100,192,88
s_waitcnt lgkmcnt(8)
Mv 108,192,92
v_mfma_f32_16x16x32_bf16 v[112:115], a[0:3], v[128:131], 0
Rb 48,1,55296
v_mfma_f32_16x16x32_bf16 v[120:123], a[0:3], v[136:139], 0
Mp 120,4,152
Rb 52,1,57344
Mp 112,4,144
Mp 112,8,160
Rb 56,1,58368
Cv 96,96,97
Mp 120,8,168
Cv 97,98,99
Cv 98,100,101
Mp 120,12,184
Rb 60,1,59392
s_waitcnt lgkmcnt(8)
Mp 112,12,176
Cv 99,102,103
Mp 112,16,192
Rb 0,1,60416
Mp 120,16,200
s_add_u32 s29, s73, 0x1800
buffer_store_dwordx4 v[96:99], v1, s[20:23], s29 offen sc0 sc1
Mp 120,20,216
Rb 4,1,61440
Mp 112,20,208
Mp 112,24,224
Rb 8,1,62464
Cv 104,104,105
Mp 120,24,232
Cv 105,106,107
Cv 106,108,109
Mp 120,28,248
Rb 12,1,63488
s_waitcnt lgkmcnt(8)
Mp 112,28,240
Cv 107,110,111
v_mfma_f32_16x16x32_bf16 v[116:119], a[32:35], v[128:131], 0
Rb 16,10,0
)" R"(
v_mfma_f32_16x16x32_bf16 v[124:127], a[32:35], v[136:139], 0
s_add_u32 s29, s73, 0x4000
buffer_store_dwordx4 v[104:107], v1, s[20:23], s29 offen sc0 sc1
Mp 124,36,152
Rb 20,10,1024
Mp 116,36,144
Mp 116,40,160
Rb 24,10,2048
Mp 124,40,168
Mp 124,44,184
Rb 28,10,3072
s_waitcnt lgkmcnt(8)
Mp 116,44,176
Mp 116,48,192
Rb 32,10,4096
Mp 124,48,200
Mp 124,52,216
Rb 36,10,5120
Mp 116,52,208
Mp 116,56,224
Rb 40,10,6144
Mp 124,56,232
Mp 124,60,248
Rb 44,10,8192
s_waitcnt vmcnt(11) lgkmcnt(10)
Mp 116,60,240
Mv 112,194,96
Mv 120,194,100
Mv 116,192,96
Mv 124,192,100
v_mfma_f32_16x16x32_bf16 v[32:35], a[0:3], v[128:131], 0
Rb 48,10,9216
v_mfma_f32_16x16x32_bf16 v[40:43], a[0:3], v[136:139], 0
Mp 40,4,152
Rb 52,10,10240
s_waitcnt lgkmcnt(8)
Mp 32,4,144
Mp 32,8,160
Rb 56,10,11264
Cv 112,112,113
Mp 40,8,168
Cv 113,114,115
Cv 114,116,117
Mp 40,12,184
Rb 60,10,12288
Cv 115,118,119
Mp 32,12,176
s_add_u32 s29, s73, 0x4400
buffer_store_dwordx4 v[112:115], v1, s[20:23], s29 offen sc0 sc1
Mp 32,16,192
Rb 0,10,13312
Mp 40,16,200
Mp 40,20,216
Rb 4,10,14336
s_waitcnt vmcnt(2) lgkmcnt(8)
buffer_store_dword v6, v7, s[20:23], s68 offen offset:0 sc0 sc1
Mp 32,20,208
Cv 120,120,121
Cv 121,122,123
Mp 32,24,224
Rb 8,10,16384
Cv 122,124,125
Mp 40,24,232
Cv 123,126,127
)" R"(
Mp 40,28,248
Rb 12,10,17408
Mp 32,28,240
s_add_u32 s29, s73, 0x4800
buffer_store_dwordx4 v[120:123], v1, s[20:23], s29 offen sc0 sc1
v_mfma_f32_16x16x32_bf16 v[36:39], a[32:35], v[128:131], 0
Rb 16,10,18432
v_mfma_f32_16x16x32_bf16 v[44:47], a[32:35], v[136:139], 0
Mp 44,36,152
Rb 20,10,19456
s_waitcnt lgkmcnt(8)
Mp 36,36,144
Mp 36,40,160
Rb 24,10,20480
Mp 44,40,168
Mp 44,44,184
Rb 28,10,21504
Mp 36,44,176
s_lshl2_add_u32 s56, s82, s33
s_mul_i32 s28, s56, 4682
Mp 36,48,192
Rb 32,10,22528
s_lshr_b32 s28, s28, 16
Mp 44,48,200
s_mul_i32 s57, s28, 14
s_sub_u32 s57, s56, s57
Mp 44,52,216
Rb 36,10,24576
s_waitcnt lgkmcnt(10)
Mp 36,52,208
s_lshl1_add_u32 s58, s28, s85
s_lshl_b32 s40, s58, 11
Mp 36,56,224
Rb 40,10,25600
Mp 44,56,232
s_cmp_gt_u32 s57, 6
s_cselect_b32 s28, 4, 0
Mp 44,60,248
Rb 44,10,26624
s_add_u32 s40, s40, s28
Mp 36,60,240
s_add_u32 s40, s40, 0xc130000
s_lshl_b32 s55, s58, 10
Mv 32,194,104
s_add_u32 s55, s55, s57
Mv 40,194,108
s_cmp_gt_u32 s57, 6
s_cselect_b32 s28, 9, 0
Mv 36,192,104
s_add_u32 s55, s55, s28
s_waitcnt lgkmcnt(8)
Mv 44,192,108
s_bfe_u32 s28, s58, 0x10001
s_xor_b32 s28, s28, s85
v_mfma_f32_16x16x32_bf16 v[48:51], a[0:3], v[128:131], 0
Rb 48,10,27648
s_lshl3_add_u32 s55, s28, s55
v_mfma_f32_16x16x32_bf16 v[56:59], a[0:3], v[136:139], 0
s_lshl_b32 s55, s55, 10
s_add_u32 s55, s55, 0x8000000
Mp 56,4,152
Rb 52,10,28672
s_and_b32 s28, s57, 1
Mp 48,4,144
)" R"(
s_lshl1_add_u32 s58, s58, s28
s_lshl_b32 s28, s58, 8
Mp 48,8,160
Rb 56,10,29696
Cv 32,32,33
Mp 56,8,168
Cv 33,34,35
Cv 34,36,37
Mp 56,12,184
Rb 60,10,30720
s_waitcnt lgkmcnt(8)
Mp 48,12,176
Cv 35,38,39
v_add_u32 v167, s28, v167
Mp 48,16,192
s_add_u32 s29, s73, 0x4c00
buffer_store_dwordx4 v[32:35], v1, s[20:23], s29 offen sc0 sc1
Mp 56,16,200
s_mul_i32 s59, s58, 14336
s_lshr_b32 s28, s57, 1
Mp 56,20,216
s_lshl_b32 s28, s28, 6
s_add_u32 s28, s28, s59
Mp 48,20,208
Cv 40,40,41
Cv 41,42,43
Mp 48,24,224
Cv 42,44,45
Cv 43,46,47
Mp 56,24,232
s_add_u32 s29, s73, 0x5000
buffer_store_dwordx4 v[40:43], v1, s[20:23], s29 offen sc0 sc1
buffer_store_dword v6, v7, s[20:23], s68 offen offset:4 sc0 sc1
Mp 56,28,248
v_add_u32 v166, s28, v166
s_waitcnt lgkmcnt(4)
Mp 48,28,240
s_mov_b32 s59, 0
s_mov_b32 s57, 0
v_mfma_f32_16x16x32_bf16 v[52:55], a[32:35], v[128:131], 0
v_mfma_f32_16x16x32_bf16 v[60:63], a[32:35], v[136:139], 0
buffer_load_dword v4, v165, s[20:23], s40 offen sc0 sc1
Mp 60,36,152
Mp 52,36,144
Mp 52,40,160
Mp 60,40,168
Mp 60,44,184
s_waitcnt lgkmcnt(0)
Mp 52,44,176
Mp 52,48,192
Mp 60,48,200
Mp 60,52,216
Mp 52,52,208
Mp 52,56,224
Mp 60,56,232
Mp 60,60,248
Mp 52,60,240
Mv 48,194,112
Mv 56,194,116
Mv 52,192,112
Mv 60,192,116
s_nop 7
Cv 48,48,49
Cv 49,50,51
)" R"(
Cv 50,52,53
Cv 51,54,55
s_add_u32 s29, s73, 0x5400
buffer_store_dwordx4 v[48:51], v1, s[20:23], s29 offen sc0 sc1
Cv 56,56,57
Cv 57,58,59
Cv 58,60,61
Cv 59,62,63
s_add_u32 s29, s73, 0x5800
buffer_store_dwordx4 v[56:59], v1, s[20:23], s29 offen sc0 sc1
s_branch .LzEND
.LzMASK0:
s_lshl1_add_u32 s28, s57, s33
s_mov_b32 s29, 0xff800000
s_cmp_gt_u32 s28, 5
s_cbranch_scc1 .LzMASKE0
s_cmp_eq_u32 s28, 5
s_cbranch_scc1 .LzMASKB0
v_mov_b32 v72, s29
v_mov_b32 v73, s29
v_mov_b32 v74, s29
v_mov_b32 v75, s29
v_mov_b32 v80, s29
v_mov_b32 v81, s29
v_mov_b32 v82, s29
v_mov_b32 v83, s29
v_mov_b32 v84, s29
v_mov_b32 v85, s29
v_mov_b32 v86, s29
v_mov_b32 v87, s29
v_mov_b32 v88, s29
v_mov_b32 v89, s29
v_mov_b32 v90, s29
v_mov_b32 v91, s29
v_mov_b32 v92, s29
v_mov_b32 v93, s29
v_mov_b32 v94, s29
v_mov_b32 v95, s29
s_cmp_eq_u32 s28, 4
s_cbranch_scc1 .LzMASKA0
v_mov_b32 v64, s29
v_mov_b32 v65, s29
v_mov_b32 v66, s29
v_mov_b32 v67, s29
v_mov_b32 v68, s29
v_mov_b32 v69, s29
v_mov_b32 v70, s29
v_mov_b32 v71, s29
v_mov_b32 v76, s29
v_mov_b32 v77, s29
v_mov_b32 v78, s29
v_mov_b32 v79, s29
.LzMASKE0:
s_setpc_b64 s[30:31]
.LzMASKA0:
s_mov_b32 exec_lo, 0xf0000
s_mov_b32 exec_hi, 0xfff00ff
v_mov_b32 v64, s29
v_mov_b32 v76, s29
s_mov_b32 exec_lo, 0x1f0001
s_mov_b32 exec_hi, 0x1fff01ff
v_mov_b32 v65, s29
v_mov_b32 v77, s29
s_mov_b32 exec_lo, 0x3f0003
)" R"(
s_mov_b32 exec_hi, 0x3fff03ff
v_mov_b32 v66, s29
v_mov_b32 v78, s29
s_mov_b32 exec_lo, 0x7f0007
s_mov_b32 exec_hi, 0x7fff07ff
v_mov_b32 v67, s29
v_mov_b32 v79, s29
s_mov_b64 exec, -1
s_setpc_b64 s[30:31]
.LzMASKB0:
s_nop 1
v_mov_b32 v88, s29
v_mov_b32 v89, s29
v_mov_b32 v90, s29
v_mov_b32 v91, s29
s_mov_b32 exec_lo, 0xf0000
s_mov_b32 exec_hi, 0xfff00ff
v_mov_b32 v80, s29
v_mov_b32 v92, s29
s_mov_b32 exec_lo, 0x1f0001
s_mov_b32 exec_hi, 0x1fff01ff
v_mov_b32 v81, s29
v_mov_b32 v93, s29
s_mov_b32 exec_lo, 0x3f0003
s_mov_b32 exec_hi, 0x3fff03ff
v_mov_b32 v82, s29
v_mov_b32 v94, s29
s_mov_b32 exec_lo, 0x7f0007
s_mov_b32 exec_hi, 0x7fff07ff
v_mov_b32 v83, s29
v_mov_b32 v95, s29
s_mov_b64 exec, -1
s_setpc_b64 s[30:31]
.LzFIX0:
s_nop 7
v_max_f32_e32 v28, v64, v65
v_max3_f32 v28, v28, v66, v67
v_max3_f32 v28, v28, v72, v73
v_max3_f32 v28, v28, v74, v75
v_max3_f32 v28, v28, v80, v81
v_max3_f32 v28, v28, v82, v83
v_max3_f32 v28, v28, v88, v89
v_max3_f32 v28, v28, v90, v91
v_mov_b32 v24, v28
s_nop 1
v_permlane32_swap_b32_e32 v28, v24
v_max_f32_e32 v28, v28, v24
v_mov_b32 v24, v28
s_nop 1
v_permlane16_swap_b32_e32 v28, v24
v_max_f32_e32 v28, v28, v24
v_max_f32_e32 v29, v68, v69
v_max3_f32 v29, v29, v70, v71
v_max3_f32 v29, v29, v76, v77
v_max3_f32 v29, v29, v78, v79
v_max3_f32 v29, v29, v84, v85
v_max3_f32 v29, v29, v86, v87
v_max3_f32 v29, v29, v92, v93
v_max3_f32 v29, v29, v94, v95
v_mov_b32 v24, v29
s_nop 1
v_permlane32_swap_b32_e32 v29, v24
v_max_f32_e32 v29, v29, v24
v_mov_b32 v24, v29
)" R"(
s_nop 1
v_permlane16_swap_b32_e32 v29, v24
v_max_f32_e32 v29, v29, v24
s_bitcmp1_b32 s88, 0
s_cbranch_scc1 .LzFIXF0
v_max_f32_e32 v28, 0, v28
v_sub_f32_e32 v24, 0, v28
v_exp_f32_e32 v24, v24
s_nop 0
v_mul_f32_e32 v12, v24, v12
v_mul_f32_e32 v128, v24, v128
v_mul_f32_e32 v129, v24, v129
v_mul_f32_e32 v130, v24, v130
v_mul_f32_e32 v131, v24, v131
v_mul_f32_e32 v132, v24, v132
v_mul_f32_e32 v133, v24, v133
v_mul_f32_e32 v134, v24, v134
v_mul_f32_e32 v135, v24, v135
v_mul_f32_e32 v144, v24, v144
v_mul_f32_e32 v145, v24, v145
v_mul_f32_e32 v146, v24, v146
v_mul_f32_e32 v147, v24, v147
v_mul_f32_e32 v148, v24, v148
v_mul_f32_e32 v149, v24, v149
v_mul_f32_e32 v150, v24, v150
v_mul_f32_e32 v151, v24, v151
v_mul_f32_e32 v160, v24, v160
v_mul_f32_e32 v161, v24, v161
v_mul_f32_e32 v162, v24, v162
v_mul_f32_e32 v163, v24, v163
v_mul_f32_e32 v164, v24, v164
v_mul_f32_e32 v165, v24, v165
v_mul_f32_e32 v166, v24, v166
v_mul_f32_e32 v167, v24, v167
v_mul_f32_e32 v176, v24, v176
v_mul_f32_e32 v177, v24, v177
v_mul_f32_e32 v178, v24, v178
v_mul_f32_e32 v179, v24, v179
v_mul_f32_e32 v180, v24, v180
v_mul_f32_e32 v181, v24, v181
v_mul_f32_e32 v182, v24, v182
v_mul_f32_e32 v183, v24, v183
v_mul_f32_e32 v192, v24, v192
v_mul_f32_e32 v193, v24, v193
v_mul_f32_e32 v194, v24, v194
v_mul_f32_e32 v195, v24, v195
v_mul_f32_e32 v196, v24, v196
v_mul_f32_e32 v197, v24, v197
v_mul_f32_e32 v198, v24, v198
v_mul_f32_e32 v199, v24, v199
v_mul_f32_e32 v208, v24, v208
v_mul_f32_e32 v209, v24, v209
v_mul_f32_e32 v210, v24, v210
v_mul_f32_e32 v211, v24, v211
v_mul_f32_e32 v212, v24, v212
v_mul_f32_e32 v213, v24, v213
v_mul_f32_e32 v214, v24, v214
v_mul_f32_e32 v215, v24, v215
v_mul_f32_e32 v224, v24, v224
v_mul_f32_e32 v225, v24, v225
v_mul_f32_e32 v226, v24, v226
v_mul_f32_e32 v227, v24, v227
v_mul_f32_e32 v228, v24, v228
v_mul_f32_e32 v229, v24, v229
)" R"(
v_mul_f32_e32 v230, v24, v230
v_mul_f32_e32 v231, v24, v231
v_mul_f32_e32 v240, v24, v240
v_mul_f32_e32 v241, v24, v241
v_mul_f32_e32 v242, v24, v242
v_mul_f32_e32 v243, v24, v243
v_mul_f32_e32 v244, v24, v244
v_mul_f32_e32 v245, v24, v245
v_mul_f32_e32 v246, v24, v246
v_mul_f32_e32 v247, v24, v247
v_max_f32_e32 v29, 0, v29
v_sub_f32_e32 v24, 0, v29
v_exp_f32_e32 v24, v24
s_nop 0
v_mul_f32_e32 v13, v24, v13
v_mul_f32_e32 v136, v24, v136
v_mul_f32_e32 v137, v24, v137
v_mul_f32_e32 v138, v24, v138
v_mul_f32_e32 v139, v24, v139
v_mul_f32_e32 v140, v24, v140
v_mul_f32_e32 v141, v24, v141
v_mul_f32_e32 v142, v24, v142
v_mul_f32_e32 v143, v24, v143
v_mul_f32_e32 v152, v24, v152
v_mul_f32_e32 v153, v24, v153
v_mul_f32_e32 v154, v24, v154
v_mul_f32_e32 v155, v24, v155
v_mul_f32_e32 v156, v24, v156
v_mul_f32_e32 v157, v24, v157
v_mul_f32_e32 v158, v24, v158
v_mul_f32_e32 v159, v24, v159
v_mul_f32_e32 v168, v24, v168
v_mul_f32_e32 v169, v24, v169
v_mul_f32_e32 v170, v24, v170
v_mul_f32_e32 v171, v24, v171
v_mul_f32_e32 v172, v24, v172
v_mul_f32_e32 v173, v24, v173
v_mul_f32_e32 v174, v24, v174
v_mul_f32_e32 v175, v24, v175
v_mul_f32_e32 v184, v24, v184
v_mul_f32_e32 v185, v24, v185
v_mul_f32_e32 v186, v24, v186
v_mul_f32_e32 v187, v24, v187
v_mul_f32_e32 v188, v24, v188
v_mul_f32_e32 v189, v24, v189
v_mul_f32_e32 v190, v24, v190
v_mul_f32_e32 v191, v24, v191
v_mul_f32_e32 v200, v24, v200
v_mul_f32_e32 v201, v24, v201
v_mul_f32_e32 v202, v24, v202
v_mul_f32_e32 v203, v24, v203
v_mul_f32_e32 v204, v24, v204
v_mul_f32_e32 v205, v24, v205
v_mul_f32_e32 v206, v24, v206
v_mul_f32_e32 v207, v24, v207
v_mul_f32_e32 v216, v24, v216
v_mul_f32_e32 v217, v24, v217
v_mul_f32_e32 v218, v24, v218
v_mul_f32_e32 v219, v24, v219
v_mul_f32_e32 v220, v24, v220
v_mul_f32_e32 v221, v24, v221
v_mul_f32_e32 v222, v24, v222
v_mul_f32_e32 v223, v24, v223
v_mul_f32_e32 v232, v24, v232
)" R"(
v_mul_f32_e32 v233, v24, v233
v_mul_f32_e32 v234, v24, v234
v_mul_f32_e32 v235, v24, v235
v_mul_f32_e32 v236, v24, v236
v_mul_f32_e32 v237, v24, v237
v_mul_f32_e32 v238, v24, v238
v_mul_f32_e32 v239, v24, v239
v_mul_f32_e32 v248, v24, v248
v_mul_f32_e32 v249, v24, v249
v_mul_f32_e32 v250, v24, v250
v_mul_f32_e32 v251, v24, v251
v_mul_f32_e32 v252, v24, v252
v_mul_f32_e32 v253, v24, v253
v_mul_f32_e32 v254, v24, v254
v_mul_f32_e32 v255, v24, v255
s_branch .LzFIXG0
.LzFIXF0:
v_cmp_lt_f32_e32 vcc, 0xff7fffff, v28
s_nop 1
v_cndmask_b32_e32 v28, 0, v28, vcc
v_cmp_lt_f32_e32 vcc, 0xff7fffff, v29
s_nop 1
v_cndmask_b32_e32 v29, 0, v29, vcc
.LzFIXG0:
v_sub_f32_e32 v16, v16, v28
v_sub_f32_e32 v17, v17, v28
v_sub_f32_e32 v18, v18, v28
v_sub_f32_e32 v19, v19, v28
v_sub_f32_e32 v64, v64, v28
v_sub_f32_e32 v65, v65, v28
v_sub_f32_e32 v66, v66, v28
v_sub_f32_e32 v67, v67, v28
v_sub_f32_e32 v72, v72, v28
v_sub_f32_e32 v73, v73, v28
v_sub_f32_e32 v74, v74, v28
v_sub_f32_e32 v75, v75, v28
v_sub_f32_e32 v80, v80, v28
v_sub_f32_e32 v81, v81, v28
v_sub_f32_e32 v82, v82, v28
v_sub_f32_e32 v83, v83, v28
v_sub_f32_e32 v88, v88, v28
v_sub_f32_e32 v89, v89, v28
v_sub_f32_e32 v90, v90, v28
v_sub_f32_e32 v91, v91, v28
v_sub_f32_e32 v20, v20, v29
v_sub_f32_e32 v21, v21, v29
v_sub_f32_e32 v22, v22, v29
v_sub_f32_e32 v23, v23, v29
v_sub_f32_e32 v68, v68, v29
v_sub_f32_e32 v69, v69, v29
v_sub_f32_e32 v70, v70, v29
v_sub_f32_e32 v71, v71, v29
v_sub_f32_e32 v76, v76, v29
v_sub_f32_e32 v77, v77, v29
v_sub_f32_e32 v78, v78, v29
v_sub_f32_e32 v79, v79, v29
v_sub_f32_e32 v84, v84, v29
v_sub_f32_e32 v85, v85, v29
v_sub_f32_e32 v86, v86, v29
v_sub_f32_e32 v87, v87, v29
v_sub_f32_e32 v92, v92, v29
v_sub_f32_e32 v93, v93, v29
v_sub_f32_e32 v94, v94, v29
v_sub_f32_e32 v95, v95, v29
)" R"(
s_bitcmp0_b32 s88, 1
s_cbranch_scc1 .LzFIXH0
v_sub_f32_e32 v96, v96, v28
v_sub_f32_e32 v97, v97, v28
v_sub_f32_e32 v98, v98, v28
v_sub_f32_e32 v99, v99, v28
v_sub_f32_e32 v104, v104, v28
v_sub_f32_e32 v105, v105, v28
v_sub_f32_e32 v106, v106, v28
v_sub_f32_e32 v107, v107, v28
v_sub_f32_e32 v112, v112, v28
v_sub_f32_e32 v113, v113, v28
v_sub_f32_e32 v114, v114, v28
v_sub_f32_e32 v115, v115, v28
v_sub_f32_e32 v120, v120, v28
v_sub_f32_e32 v121, v121, v28
v_sub_f32_e32 v122, v122, v28
v_sub_f32_e32 v123, v123, v28
v_sub_f32_e32 v100, v100, v29
v_sub_f32_e32 v101, v101, v29
v_sub_f32_e32 v102, v102, v29
v_sub_f32_e32 v103, v103, v29
v_sub_f32_e32 v108, v108, v29
v_sub_f32_e32 v109, v109, v29
v_sub_f32_e32 v110, v110, v29
v_sub_f32_e32 v111, v111, v29
v_sub_f32_e32 v116, v116, v29
v_sub_f32_e32 v117, v117, v29
v_sub_f32_e32 v118, v118, v29
v_sub_f32_e32 v119, v119, v29
v_sub_f32_e32 v124, v124, v29
v_sub_f32_e32 v125, v125, v29
v_sub_f32_e32 v126, v126, v29
v_sub_f32_e32 v127, v127, v29
.LzFIXH0:
v_exp_f32_e32 v32, v64
v_exp_f32_e32 v28, v65
s_nop 0
v_add_f32_e32 v14, v32, v28
Cv 32,32,28
v_exp_f32_e32 v33, v66
v_exp_f32_e32 v29, v67
v_add_f32_e32 v14, v14, v33
v_add_f32_e32 v14, v14, v29
Cv 33,33,29
v_exp_f32_e32 v34, v72
v_exp_f32_e32 v24, v73
v_add_f32_e32 v14, v14, v34
v_add_f32_e32 v14, v14, v24
Cv 34,34,24
v_exp_f32_e32 v35, v74
v_exp_f32_e32 v25, v75
v_add_f32_e32 v14, v14, v35
v_add_f32_e32 v14, v14, v25
Cv 35,35,25
v_exp_f32_e32 v36, v68
v_exp_f32_e32 v26, v69
s_nop 0
v_add_f32_e32 v15, v36, v26
Cv 36,36,26
v_exp_f32_e32 v37, v70
v_exp_f32_e32 v27, v71
v_add_f32_e32 v15, v15, v37
v_add_f32_e32 v15, v15, v27
)" R"(
Cv 37,37,27
v_exp_f32_e32 v38, v76
v_exp_f32_e32 v28, v77
v_add_f32_e32 v15, v15, v38
v_add_f32_e32 v15, v15, v28
Cv 38,38,28
v_exp_f32_e32 v39, v78
v_exp_f32_e32 v29, v79
v_add_f32_e32 v15, v15, v39
v_add_f32_e32 v15, v15, v29
Cv 39,39,29
v_exp_f32_e32 v40, v80
v_exp_f32_e32 v24, v81
v_add_f32_e32 v14, v14, v40
v_add_f32_e32 v14, v14, v24
Cv 40,40,24
v_exp_f32_e32 v41, v82
v_exp_f32_e32 v25, v83
v_add_f32_e32 v14, v14, v41
v_add_f32_e32 v14, v14, v25
Cv 41,41,25
v_exp_f32_e32 v42, v88
v_exp_f32_e32 v26, v89
v_add_f32_e32 v14, v14, v42
v_add_f32_e32 v14, v14, v26
Cv 42,42,26
v_exp_f32_e32 v43, v90
v_exp_f32_e32 v27, v91
v_add_f32_e32 v14, v14, v43
v_add_f32_e32 v14, v14, v27
Cv 43,43,27
v_exp_f32_e32 v44, v84
v_exp_f32_e32 v28, v85
v_add_f32_e32 v15, v15, v44
v_add_f32_e32 v15, v15, v28
Cv 44,44,28
v_exp_f32_e32 v45, v86
v_exp_f32_e32 v29, v87
v_add_f32_e32 v15, v15, v45
v_add_f32_e32 v15, v15, v29
Cv 45,45,29
v_exp_f32_e32 v46, v92
v_exp_f32_e32 v24, v93
v_add_f32_e32 v15, v15, v46
v_add_f32_e32 v15, v15, v24
Cv 46,46,24
v_exp_f32_e32 v47, v94
v_exp_f32_e32 v25, v95
v_add_f32_e32 v15, v15, v47
v_add_f32_e32 v15, v15, v25
Cv 47,47,25
s_nop 1
s_setpc_b64 s[30:31]
.LzMASK1:
s_lshl1_add_u32 s28, s57, s33
s_mov_b32 s29, 0xff800000
s_cmp_gt_u32 s28, 5
s_cbranch_scc1 .LzMASKE1
s_cmp_eq_u32 s28, 5
s_cbranch_scc1 .LzMASKB1
v_mov_b32 v104, s29
v_mov_b32 v105, s29
v_mov_b32 v106, s29
v_mov_b32 v107, s29
)" R"(
v_mov_b32 v112, s29
v_mov_b32 v113, s29
v_mov_b32 v114, s29
v_mov_b32 v115, s29
v_mov_b32 v116, s29
v_mov_b32 v117, s29
v_mov_b32 v118, s29
v_mov_b32 v119, s29
v_mov_b32 v120, s29
v_mov_b32 v121, s29
v_mov_b32 v122, s29
v_mov_b32 v123, s29
v_mov_b32 v124, s29
v_mov_b32 v125, s29
v_mov_b32 v126, s29
v_mov_b32 v127, s29
s_cmp_eq_u32 s28, 4
s_cbranch_scc1 .LzMASKA1
v_mov_b32 v96, s29
v_mov_b32 v97, s29
v_mov_b32 v98, s29
v_mov_b32 v99, s29
v_mov_b32 v100, s29
v_mov_b32 v101, s29
v_mov_b32 v102, s29
v_mov_b32 v103, s29
v_mov_b32 v108, s29
v_mov_b32 v109, s29
v_mov_b32 v110, s29
v_mov_b32 v111, s29
.LzMASKE1:
s_setpc_b64 s[30:31]
.LzMASKA1:
s_mov_b32 exec_lo, 0xf0000
s_mov_b32 exec_hi, 0xfff00ff
v_mov_b32 v96, s29
v_mov_b32 v108, s29
s_mov_b32 exec_lo, 0x1f0001
s_mov_b32 exec_hi, 0x1fff01ff
v_mov_b32 v97, s29
v_mov_b32 v109, s29
s_mov_b32 exec_lo, 0x3f0003
s_mov_b32 exec_hi, 0x3fff03ff
v_mov_b32 v98, s29
v_mov_b32 v110, s29
s_mov_b32 exec_lo, 0x7f0007
s_mov_b32 exec_hi, 0x7fff07ff
v_mov_b32 v99, s29
v_mov_b32 v111, s29
s_mov_b64 exec, -1
s_setpc_b64 s[30:31]
.LzMASKB1:
s_nop 1
v_mov_b32 v120, s29
v_mov_b32 v121, s29
v_mov_b32 v122, s29
v_mov_b32 v123, s29
s_mov_b32 exec_lo, 0xf0000
s_mov_b32 exec_hi, 0xfff00ff
v_mov_b32 v112, s29
v_mov_b32 v124, s29
s_mov_b32 exec_lo, 0x1f0001
s_mov_b32 exec_hi, 0x1fff01ff
v_mov_b32 v113, s29
)" R"(
v_mov_b32 v125, s29
s_mov_b32 exec_lo, 0x3f0003
s_mov_b32 exec_hi, 0x3fff03ff
v_mov_b32 v114, s29
v_mov_b32 v126, s29
s_mov_b32 exec_lo, 0x7f0007
s_mov_b32 exec_hi, 0x7fff07ff
v_mov_b32 v115, s29
v_mov_b32 v127, s29
s_mov_b64 exec, -1
s_setpc_b64 s[30:31]
.LzFIX1:
s_nop 7
v_max_f32_e32 v28, v96, v97
v_max3_f32 v28, v28, v98, v99
v_max3_f32 v28, v28, v104, v105
v_max3_f32 v28, v28, v106, v107
v_max3_f32 v28, v28, v112, v113
v_max3_f32 v28, v28, v114, v115
v_max3_f32 v28, v28, v120, v121
v_max3_f32 v28, v28, v122, v123
v_mov_b32 v24, v28
s_nop 1
v_permlane32_swap_b32_e32 v28, v24
v_max_f32_e32 v28, v28, v24
v_mov_b32 v24, v28
s_nop 1
v_permlane16_swap_b32_e32 v28, v24
v_max_f32_e32 v28, v28, v24
v_max_f32_e32 v29, v100, v101
v_max3_f32 v29, v29, v102, v103
v_max3_f32 v29, v29, v108, v109
v_max3_f32 v29, v29, v110, v111
v_max3_f32 v29, v29, v116, v117
v_max3_f32 v29, v29, v118, v119
v_max3_f32 v29, v29, v124, v125
v_max3_f32 v29, v29, v126, v127
v_mov_b32 v24, v29
s_nop 1
v_permlane32_swap_b32_e32 v29, v24
v_max_f32_e32 v29, v29, v24
v_mov_b32 v24, v29
s_nop 1
v_permlane16_swap_b32_e32 v29, v24
v_max_f32_e32 v29, v29, v24
s_bitcmp1_b32 s88, 0
s_cbranch_scc1 .LzFIXF1
v_max_f32_e32 v28, 0, v28
v_sub_f32_e32 v24, 0, v28
v_exp_f32_e32 v24, v24
s_nop 0
v_mul_f32_e32 v12, v24, v12
v_mul_f32_e32 v128, v24, v128
v_mul_f32_e32 v129, v24, v129
v_mul_f32_e32 v130, v24, v130
v_mul_f32_e32 v131, v24, v131
v_mul_f32_e32 v132, v24, v132
v_mul_f32_e32 v133, v24, v133
v_mul_f32_e32 v134, v24, v134
v_mul_f32_e32 v135, v24, v135
v_mul_f32_e32 v144, v24, v144
v_mul_f32_e32 v145, v24, v145
v_mul_f32_e32 v146, v24, v146
v_mul_f32_e32 v147, v24, v147
)" R"(
v_mul_f32_e32 v148, v24, v148
v_mul_f32_e32 v149, v24, v149
v_mul_f32_e32 v150, v24, v150
v_mul_f32_e32 v151, v24, v151
v_mul_f32_e32 v160, v24, v160
v_mul_f32_e32 v161, v24, v161
v_mul_f32_e32 v162, v24, v162
v_mul_f32_e32 v163, v24, v163
v_mul_f32_e32 v164, v24, v164
v_mul_f32_e32 v165, v24, v165
v_mul_f32_e32 v166, v24, v166
v_mul_f32_e32 v167, v24, v167
v_mul_f32_e32 v176, v24, v176
v_mul_f32_e32 v177, v24, v177
v_mul_f32_e32 v178, v24, v178
v_mul_f32_e32 v179, v24, v179
v_mul_f32_e32 v180, v24, v180
v_mul_f32_e32 v181, v24, v181
v_mul_f32_e32 v182, v24, v182
v_mul_f32_e32 v183, v24, v183
v_mul_f32_e32 v192, v24, v192
v_mul_f32_e32 v193, v24, v193
v_mul_f32_e32 v194, v24, v194
v_mul_f32_e32 v195, v24, v195
v_mul_f32_e32 v196, v24, v196
v_mul_f32_e32 v197, v24, v197
v_mul_f32_e32 v198, v24, v198
v_mul_f32_e32 v199, v24, v199
v_mul_f32_e32 v208, v24, v208
v_mul_f32_e32 v209, v24, v209
v_mul_f32_e32 v210, v24, v210
v_mul_f32_e32 v211, v24, v211
v_mul_f32_e32 v212, v24, v212
v_mul_f32_e32 v213, v24, v213
v_mul_f32_e32 v214, v24, v214
v_mul_f32_e32 v215, v24, v215
v_mul_f32_e32 v224, v24, v224
v_mul_f32_e32 v225, v24, v225
v_mul_f32_e32 v226, v24, v226
v_mul_f32_e32 v227, v24, v227
v_mul_f32_e32 v228, v24, v228
v_mul_f32_e32 v229, v24, v229
v_mul_f32_e32 v230, v24, v230
v_mul_f32_e32 v231, v24, v231
v_mul_f32_e32 v240, v24, v240
v_mul_f32_e32 v241, v24, v241
v_mul_f32_e32 v242, v24, v242
v_mul_f32_e32 v243, v24, v243
v_mul_f32_e32 v244, v24, v244
v_mul_f32_e32 v245, v24, v245
v_mul_f32_e32 v246, v24, v246
v_mul_f32_e32 v247, v24, v247
v_max_f32_e32 v29, 0, v29
v_sub_f32_e32 v24, 0, v29
v_exp_f32_e32 v24, v24
s_nop 0
v_mul_f32_e32 v13, v24, v13
v_mul_f32_e32 v136, v24, v136
v_mul_f32_e32 v137, v24, v137
v_mul_f32_e32 v138, v24, v138
v_mul_f32_e32 v139, v24, v139
v_mul_f32_e32 v140, v24, v140
v_mul_f32_e32 v141, v24, v141
v_mul_f32_e32 v142, v24, v142
)" R"(
v_mul_f32_e32 v143, v24, v143
v_mul_f32_e32 v152, v24, v152
v_mul_f32_e32 v153, v24, v153
v_mul_f32_e32 v154, v24, v154
v_mul_f32_e32 v155, v24, v155
v_mul_f32_e32 v156, v24, v156
v_mul_f32_e32 v157, v24, v157
v_mul_f32_e32 v158, v24, v158
v_mul_f32_e32 v159, v24, v159
v_mul_f32_e32 v168, v24, v168
v_mul_f32_e32 v169, v24, v169
v_mul_f32_e32 v170, v24, v170
v_mul_f32_e32 v171, v24, v171
v_mul_f32_e32 v172, v24, v172
v_mul_f32_e32 v173, v24, v173
v_mul_f32_e32 v174, v24, v174
v_mul_f32_e32 v175, v24, v175
v_mul_f32_e32 v184, v24, v184
v_mul_f32_e32 v185, v24, v185
v_mul_f32_e32 v186, v24, v186
v_mul_f32_e32 v187, v24, v187
v_mul_f32_e32 v188, v24, v188
v_mul_f32_e32 v189, v24, v189
v_mul_f32_e32 v190, v24, v190
v_mul_f32_e32 v191, v24, v191
v_mul_f32_e32 v200, v24, v200
v_mul_f32_e32 v201, v24, v201
v_mul_f32_e32 v202, v24, v202
v_mul_f32_e32 v203, v24, v203
v_mul_f32_e32 v204, v24, v204
v_mul_f32_e32 v205, v24, v205
v_mul_f32_e32 v206, v24, v206
v_mul_f32_e32 v207, v24, v207
v_mul_f32_e32 v216, v24, v216
v_mul_f32_e32 v217, v24, v217
v_mul_f32_e32 v218, v24, v218
v_mul_f32_e32 v219, v24, v219
v_mul_f32_e32 v220, v24, v220
v_mul_f32_e32 v221, v24, v221
v_mul_f32_e32 v222, v24, v222
v_mul_f32_e32 v223, v24, v223
v_mul_f32_e32 v232, v24, v232
v_mul_f32_e32 v233, v24, v233
v_mul_f32_e32 v234, v24, v234
v_mul_f32_e32 v235, v24, v235
v_mul_f32_e32 v236, v24, v236
v_mul_f32_e32 v237, v24, v237
v_mul_f32_e32 v238, v24, v238
v_mul_f32_e32 v239, v24, v239
v_mul_f32_e32 v248, v24, v248
v_mul_f32_e32 v249, v24, v249
v_mul_f32_e32 v250, v24, v250
v_mul_f32_e32 v251, v24, v251
v_mul_f32_e32 v252, v24, v252
v_mul_f32_e32 v253, v24, v253
v_mul_f32_e32 v254, v24, v254
v_mul_f32_e32 v255, v24, v255
s_branch .LzFIXG1
.LzFIXF1:
v_cmp_lt_f32_e32 vcc, 0xff7fffff, v28
s_nop 1
v_cndmask_b32_e32 v28, 0, v28, vcc
v_cmp_lt_f32_e32 vcc, 0xff7fffff, v29
s_nop 1
)" R"(
v_cndmask_b32_e32 v29, 0, v29, vcc
.LzFIXG1:
v_sub_f32_e32 v16, v16, v28
v_sub_f32_e32 v17, v17, v28
v_sub_f32_e32 v18, v18, v28
v_sub_f32_e32 v19, v19, v28
v_sub_f32_e32 v96, v96, v28
v_sub_f32_e32 v97, v97, v28
v_sub_f32_e32 v98, v98, v28
v_sub_f32_e32 v99, v99, v28
v_sub_f32_e32 v104, v104, v28
v_sub_f32_e32 v105, v105, v28
v_sub_f32_e32 v106, v106, v28
v_sub_f32_e32 v107, v107, v28
v_sub_f32_e32 v112, v112, v28
v_sub_f32_e32 v113, v113, v28
v_sub_f32_e32 v114, v114, v28
v_sub_f32_e32 v115, v115, v28
v_sub_f32_e32 v120, v120, v28
v_sub_f32_e32 v121, v121, v28
v_sub_f32_e32 v122, v122, v28
v_sub_f32_e32 v123, v123, v28
v_sub_f32_e32 v20, v20, v29
v_sub_f32_e32 v21, v21, v29
v_sub_f32_e32 v22, v22, v29
v_sub_f32_e32 v23, v23, v29
v_sub_f32_e32 v100, v100, v29
v_sub_f32_e32 v101, v101, v29
v_sub_f32_e32 v102, v102, v29
v_sub_f32_e32 v103, v103, v29
v_sub_f32_e32 v108, v108, v29
v_sub_f32_e32 v109, v109, v29
v_sub_f32_e32 v110, v110, v29
v_sub_f32_e32 v111, v111, v29
v_sub_f32_e32 v116, v116, v29
v_sub_f32_e32 v117, v117, v29
v_sub_f32_e32 v118, v118, v29
v_sub_f32_e32 v119, v119, v29
v_sub_f32_e32 v124, v124, v29
v_sub_f32_e32 v125, v125, v29
v_sub_f32_e32 v126, v126, v29
v_sub_f32_e32 v127, v127, v29
s_bitcmp0_b32 s88, 1
s_cbranch_scc1 .LzFIXH1
v_sub_f32_e32 v64, v64, v28
v_sub_f32_e32 v65, v65, v28
v_sub_f32_e32 v66, v66, v28
v_sub_f32_e32 v67, v67, v28
v_sub_f32_e32 v72, v72, v28
v_sub_f32_e32 v73, v73, v28
v_sub_f32_e32 v74, v74, v28
v_sub_f32_e32 v75, v75, v28
v_sub_f32_e32 v80, v80, v28
v_sub_f32_e32 v81, v81, v28
v_sub_f32_e32 v82, v82, v28
v_sub_f32_e32 v83, v83, v28
v_sub_f32_e32 v88, v88, v28
v_sub_f32_e32 v89, v89, v28
v_sub_f32_e32 v90, v90, v28
v_sub_f32_e32 v91, v91, v28
v_sub_f32_e32 v68, v68, v29
v_sub_f32_e32 v69, v69, v29
v_sub_f32_e32 v70, v70, v29
v_sub_f32_e32 v71, v71, v29
)" R"(
v_sub_f32_e32 v76, v76, v29
v_sub_f32_e32 v77, v77, v29
v_sub_f32_e32 v78, v78, v29
v_sub_f32_e32 v79, v79, v29
v_sub_f32_e32 v84, v84, v29
v_sub_f32_e32 v85, v85, v29
v_sub_f32_e32 v86, v86, v29
v_sub_f32_e32 v87, v87, v29
v_sub_f32_e32 v92, v92, v29
v_sub_f32_e32 v93, v93, v29
v_sub_f32_e32 v94, v94, v29
v_sub_f32_e32 v95, v95, v29
.LzFIXH1:
v_exp_f32_e32 v48, v96
v_exp_f32_e32 v26, v97
s_nop 0
v_add_f32_e32 v14, v48, v26
Cv 48,48,26
v_exp_f32_e32 v49, v98
v_exp_f32_e32 v27, v99
v_add_f32_e32 v14, v14, v49
v_add_f32_e32 v14, v14, v27
Cv 49,49,27
v_exp_f32_e32 v50, v104
v_exp_f32_e32 v28, v105
v_add_f32_e32 v14, v14, v50
v_add_f32_e32 v14, v14, v28
Cv 50,50,28
v_exp_f32_e32 v51, v106
v_exp_f32_e32 v29, v107
v_add_f32_e32 v14, v14, v51
v_add_f32_e32 v14, v14, v29
Cv 51,51,29
v_exp_f32_e32 v52, v100
v_exp_f32_e32 v24, v101
s_nop 0
v_add_f32_e32 v15, v52, v24
Cv 52,52,24
v_exp_f32_e32 v53, v102
v_exp_f32_e32 v25, v103
v_add_f32_e32 v15, v15, v53
v_add_f32_e32 v15, v15, v25
Cv 53,53,25
v_exp_f32_e32 v54, v108
v_exp_f32_e32 v26, v109
v_add_f32_e32 v15, v15, v54
v_add_f32_e32 v15, v15, v26
Cv 54,54,26
v_exp_f32_e32 v55, v110
v_exp_f32_e32 v27, v111
v_add_f32_e32 v15, v15, v55
v_add_f32_e32 v15, v15, v27
Cv 55,55,27
v_exp_f32_e32 v56, v112
v_exp_f32_e32 v28, v113
v_add_f32_e32 v14, v14, v56
v_add_f32_e32 v14, v14, v28
Cv 56,56,28
v_exp_f32_e32 v57, v114
v_exp_f32_e32 v29, v115
v_add_f32_e32 v14, v14, v57
v_add_f32_e32 v14, v14, v29
Cv 57,57,29
v_exp_f32_e32 v58, v120
)" R"(
v_exp_f32_e32 v24, v121
v_add_f32_e32 v14, v14, v58
v_add_f32_e32 v14, v14, v24
Cv 58,58,24
v_exp_f32_e32 v59, v122
v_exp_f32_e32 v25, v123
v_add_f32_e32 v14, v14, v59
v_add_f32_e32 v14, v14, v25
Cv 59,59,25
v_exp_f32_e32 v60, v116
v_exp_f32_e32 v26, v117
v_add_f32_e32 v15, v15, v60
v_add_f32_e32 v15, v15, v26
Cv 60,60,26
v_exp_f32_e32 v61, v118
v_exp_f32_e32 v27, v119
v_add_f32_e32 v15, v15, v61
v_add_f32_e32 v15, v15, v27
Cv 61,61,27
v_exp_f32_e32 v62, v124
v_exp_f32_e32 v28, v125
v_add_f32_e32 v15, v15, v62
v_add_f32_e32 v15, v15, v28
Cv 62,62,28
v_exp_f32_e32 v63, v126
v_exp_f32_e32 v29, v127
v_add_f32_e32 v15, v15, v63
v_add_f32_e32 v15, v15, v29
Cv 63,63,29
s_nop 1
s_setpc_b64 s[30:31]
.Lzsp1:
s_sub_u32 s53, s53, 1
s_cbranch_scc1 .Lzsq1
s_sleep 1
buffer_load_dword v4, v2, s[20:23], s85 offen sc0 sc1
s_waitcnt vmcnt(0)
v_xor_b32 v4, s3, v4
v_cmp_gt_u32_e32 vcc, 2, v4
s_andn2_b32 s28, 0xff, vcc_lo
s_cbranch_scc1 .Lzsp1
s_branch .Lzok1
.Lzsq1:
s_mov_b32 s53, 0
s_branch .Lzok1
.Lzsp2:
s_sub_u32 s53, s53, 1
s_cbranch_scc1 .Lzsq2
s_sleep 1
buffer_load_dword v4, v7, s[20:23], s52 offen offset:0 sc0 sc1
s_waitcnt vmcnt(0)
v_cmp_eq_u32_e32 vcc, s3, v4
s_andn2_b64 s[28:29], 1, vcc
s_cbranch_scc1 .Lzsp2
s_branch .Lzok2
.Lzsq2:
s_mov_b32 s53, 0
s_branch .Lzok2
.Lzsp3:
s_sub_u32 s53, s53, 1
s_cbranch_scc1 .Lzsq3
s_sleep 1
buffer_load_dword v4, v2, s[20:23], s85 offen sc0 sc1
s_waitcnt vmcnt(0)
)" R"(
v_xor_b32 v4, s3, v4
v_cmp_gt_u32_e32 vcc, 2, v4
s_andn2_b32 s28, 0xff, vcc_lo
s_cbranch_scc1 .Lzsp3
s_branch .Lzok3
.Lzsq3:
s_mov_b32 s53, 0
s_branch .Lzok3
.Lzsp4:
s_sub_u32 s53, s53, 1
s_cbranch_scc1 .Lzsq4
s_sleep 1
buffer_load_dword v4, v7, s[20:23], s52 offen offset:0 sc0 sc1
s_waitcnt vmcnt(0)
v_cmp_eq_u32_e32 vcc, s3, v4
s_andn2_b64 s[28:29], 1, vcc
s_cbranch_scc1 .Lzsp4
s_branch .Lzok4
.Lzsq4:
s_mov_b32 s53, 0
s_branch .Lzok4
.Lzsp5:
s_sub_u32 s53, s53, 1
s_cbranch_scc1 .Lzsq5
s_sleep 1
buffer_load_dword v4, v5, s[20:23], s50 offen offset:0 sc0 sc1
s_waitcnt vmcnt(0)
v_cmp_eq_u32_e32 vcc, s3, v4
s_andn2_b64 s[28:29], s[60:61], vcc
s_cbranch_scc1 .Lzsp5
s_branch .Lzok5
.Lzsq5:
s_mov_b32 s53, 0
s_branch .Lzok5
.Lzsp6:
s_sub_u32 s53, s53, 1
s_cbranch_scc1 .Lzsq6
s_sleep 1
buffer_load_dword v4, v5, s[20:23], s50 offen offset:0 sc0 sc1
s_waitcnt vmcnt(0)
v_cmp_eq_u32_e32 vcc, s3, v4
s_andn2_b64 s[28:29], s[60:61], vcc
s_cbranch_scc1 .Lzsp6
s_branch .Lzok6
.Lzsq6:
s_mov_b32 s53, 0
s_branch .Lzok6
.Lzsp7:
s_sub_u32 s53, s53, 1
s_cbranch_scc1 .Lzsq7
s_sleep 1
buffer_load_dwordx2 v[24:25], v7, s[20:23], s83 offen sc0 sc1
s_waitcnt vmcnt(0)
v_cmp_eq_u32_e32 vcc, s3, v25
s_andn2_b64 s[28:29], 1, vcc
s_cbranch_scc1 .Lzsp7
s_branch .Lzok7
.Lzsq7:
s_mov_b32 s53, 0
s_branch .Lzok7
.Lzsp8:
s_sub_u32 s53, s53, 1
s_cbranch_scc1 .Lzsq8
s_sleep 1
)" R"(
buffer_load_dword v4, v26, s[20:23], s87 offen offset:0 sc0 sc1
s_waitcnt vmcnt(0)
v_cmp_eq_u32_e32 vcc, s3, v4
s_andn2_b64 s[28:29], -1, vcc
s_cbranch_scc1 .Lzsp8
s_branch .Lzok8
.Lzsq8:
s_mov_b32 s53, 0
s_branch .Lzok8
.LzT2S:
s_cmp_lg_u32 s33, 0
s_cbranch_scc1 .LzT2E
.LzT2:
s_mov_b32 s28, 0xc100000
buffer_load_dwordx2 v[32:33], v1, s[20:23], s28 offen offset:0 sc0 sc1
buffer_load_dwordx2 v[34:35], v1, s[20:23], s28 offen offset:1024 sc0 sc1
buffer_load_dwordx2 v[36:37], v1, s[20:23], s28 offen offset:2048 sc0 sc1
buffer_load_dwordx2 v[38:39], v1, s[20:23], s28 offen offset:3072 sc0 sc1
s_mov_b32 s28, 0xc101000
buffer_load_dwordx2 v[40:41], v1, s[20:23], s28 offen offset:0 sc0 sc1
buffer_load_dwordx2 v[42:43], v1, s[20:23], s28 offen offset:1024 sc0 sc1
buffer_load_dwordx2 v[44:45], v1, s[20:23], s28 offen offset:2048 sc0 sc1
buffer_load_dwordx2 v[46:47], v1, s[20:23], s28 offen offset:3072 sc0 sc1
s_mov_b32 s28, 0xc102000
buffer_load_dwordx2 v[48:49], v1, s[20:23], s28 offen offset:0 sc0 sc1
buffer_load_dwordx2 v[50:51], v1, s[20:23], s28 offen offset:1024 sc0 sc1
buffer_load_dwordx2 v[52:53], v1, s[20:23], s28 offen offset:2048 sc0 sc1
buffer_load_dwordx2 v[54:55], v1, s[20:23], s28 offen offset:3072 sc0 sc1
s_mov_b32 s28, 0xc103000
buffer_load_dwordx2 v[56:57], v1, s[20:23], s28 offen offset:0 sc0 sc1
buffer_load_dwordx2 v[58:59], v1, s[20:23], s28 offen offset:1024 sc0 sc1
buffer_load_dwordx2 v[60:61], v1, s[20:23], s28 offen offset:2048 sc0 sc1
buffer_load_dwordx2 v[62:63], v1, s[20:23], s28 offen offset:3072 sc0 sc1
s_mov_b32 s28, 0xc104000
buffer_load_dwordx2 v[64:65], v1, s[20:23], s28 offen offset:0 sc0 sc1
buffer_load_dwordx2 v[66:67], v1, s[20:23], s28 offen offset:1024 sc0 sc1
buffer_load_dwordx2 v[68:69], v1, s[20:23], s28 offen offset:2048 sc0 sc1
buffer_load_dwordx2 v[70:71], v1, s[20:23], s28 offen offset:3072 sc0 sc1
s_mov_b32 s28, 0xc105000
buffer_load_dwordx2 v[72:73], v1, s[20:23], s28 offen offset:0 sc0 sc1
buffer_load_dwordx2 v[74:75], v1, s[20:23], s28 offen offset:1024 sc0 sc1
buffer_load_dwordx2 v[76:77], v1, s[20:23], s28 offen offset:2048 sc0 sc1
buffer_load_dwordx2 v[78:79], v1, s[20:23], s28 offen offset:3072 sc0 sc1
s_mov_b32 s28, 0xc106000
buffer_load_dwordx2 v[80:81], v1, s[20:23], s28 offen offset:0 sc0 sc1
buffer_load_dwordx2 v[82:83], v1, s[20:23], s28 offen offset:1024 sc0 sc1
buffer_load_dwordx2 v[84:85], v1, s[20:23], s28 offen offset:2048 sc0 sc1
buffer_load_dwordx2 v[86:87], v1, s[20:23], s28 offen offset:3072 sc0 sc1
s_mov_b32 s28, 0xc107000
buffer_load_dwordx2 v[88:89], v1, s[20:23], s28 offen offset:0 sc0 sc1
buffer_load_dwordx2 v[90:91], v1, s[20:23], s28 offen offset:1024 sc0 sc1
buffer_load_dwordx2 v[92:93], v1, s[20:23], s28 offen offset:2048 sc0 sc1
buffer_load_dwordx2 v[94:95], v1, s[20:23], s28 offen offset:3072 sc0 sc1
v_mov_b32 v26, 0
v_mov_b32 v27, -1
s_mov_b32 s28, 0xc108c00
buffer_store_dword v26, v7, s[20:23], s28 offen sc0 sc1
buffer_store_dword v26, v7, s[20:23], s28 offen offset:128 sc0 sc1
s_waitcnt vmcnt(0)
v_max3_u32 v26, v26, v33, v35
v_min3_u32 v27, v27, v33, v35
v_max3_u32 v26, v26, v37, v39
v_min3_u32 v27, v27, v37, v39
v_max3_u32 v26, v26, v41, v43
)" R"(
v_min3_u32 v27, v27, v41, v43
v_max3_u32 v26, v26, v45, v47
v_min3_u32 v27, v27, v45, v47
v_max3_u32 v26, v26, v49, v51
v_min3_u32 v27, v27, v49, v51
v_max3_u32 v26, v26, v53, v55
v_min3_u32 v27, v27, v53, v55
v_max3_u32 v26, v26, v57, v59
v_min3_u32 v27, v27, v57, v59
v_max3_u32 v26, v26, v61, v63
v_min3_u32 v27, v27, v61, v63
v_max3_u32 v26, v26, v65, v67
v_min3_u32 v27, v27, v65, v67
v_max3_u32 v26, v26, v69, v71
v_min3_u32 v27, v27, v69, v71
v_max3_u32 v26, v26, v73, v75
v_min3_u32 v27, v27, v73, v75
v_max3_u32 v26, v26, v77, v79
v_min3_u32 v27, v27, v77, v79
v_max3_u32 v26, v26, v81, v83
v_min3_u32 v27, v27, v81, v83
v_max3_u32 v26, v26, v85, v87
v_min3_u32 v27, v27, v85, v87
v_max3_u32 v26, v26, v89, v91
v_min3_u32 v27, v27, v89, v91
v_max3_u32 v26, v26, v93, v95
v_min3_u32 v27, v27, v93, v95
v_cmp_ne_u32_e32 vcc, s3, v26
v_cmp_ne_u32_e64 s[28:29], s3, v27
s_or_b64 vcc, vcc, s[28:29]
s_cbranch_scc0 .LzT2G
s_sub_u32 s53, s53, 1
s_cbranch_scc0 .LzT2
s_mov_b32 s53, 0
.LzT2G:
v_max_u32 v26, v32, v34
v_min_u32 v27, v32, v34
v_med3_u32 v27, v26, v27, v36
v_max_u32 v26, v26, v36
v_med3_u32 v27, v26, v27, v38
v_max_u32 v26, v26, v38
v_med3_u32 v27, v26, v27, v40
v_max_u32 v26, v26, v40
v_med3_u32 v27, v26, v27, v42
v_max_u32 v26, v26, v42
v_med3_u32 v27, v26, v27, v44
v_max_u32 v26, v26, v44
v_med3_u32 v27, v26, v27, v46
v_max_u32 v26, v26, v46
v_med3_u32 v27, v26, v27, v48
v_max_u32 v26, v26, v48
v_med3_u32 v27, v26, v27, v50
v_max_u32 v26, v26, v50
v_med3_u32 v27, v26, v27, v52
v_max_u32 v26, v26, v52
v_med3_u32 v27, v26, v27, v54
v_max_u32 v26, v26, v54
v_med3_u32 v27, v26, v27, v56
v_max_u32 v26, v26, v56
v_med3_u32 v27, v26, v27, v58
v_max_u32 v26, v26, v58
v_med3_u32 v27, v26, v27, v60
v_max_u32 v26, v26, v60
v_med3_u32 v27, v26, v27, v62
)" R"(
v_max_u32 v26, v26, v62
v_med3_u32 v27, v26, v27, v64
v_max_u32 v26, v26, v64
v_med3_u32 v27, v26, v27, v66
v_max_u32 v26, v26, v66
v_med3_u32 v27, v26, v27, v68
v_max_u32 v26, v26, v68
v_med3_u32 v27, v26, v27, v70
v_max_u32 v26, v26, v70
v_med3_u32 v27, v26, v27, v72
v_max_u32 v26, v26, v72
v_med3_u32 v27, v26, v27, v74
v_max_u32 v26, v26, v74
v_med3_u32 v27, v26, v27, v76
v_max_u32 v26, v26, v76
v_med3_u32 v27, v26, v27, v78
v_max_u32 v26, v26, v78
v_med3_u32 v27, v26, v27, v80
v_max_u32 v26, v26, v80
v_med3_u32 v27, v26, v27, v82
v_max_u32 v26, v26, v82
v_med3_u32 v27, v26, v27, v84
v_max_u32 v26, v26, v84
v_med3_u32 v27, v26, v27, v86
v_max_u32 v26, v26, v86
v_med3_u32 v27, v26, v27, v88
v_max_u32 v26, v26, v88
v_med3_u32 v27, v26, v27, v90
v_max_u32 v26, v26, v90
v_med3_u32 v27, v26, v27, v92
v_max_u32 v26, v26, v92
v_med3_u32 v27, v26, v27, v94
v_max_u32 v26, v26, v94
v_mov_b32 v24, v26
v_mov_b32 v25, v27
s_nop 1
v_permlane32_swap_b32_e32 v26, v24
v_permlane32_swap_b32_e32 v27, v25
v_max_u32 v27, v27, v25
v_med3_u32 v27, v26, v27, v24
v_max_u32 v26, v26, v24
v_mov_b32 v24, v26
v_mov_b32 v25, v27
s_nop 1
v_permlane16_swap_b32_e32 v26, v24
v_permlane16_swap_b32_e32 v27, v25
v_max_u32 v27, v27, v25
v_med3_u32 v27, v26, v27, v24
v_max_u32 v26, v26, v24
s_nop 1
v_mov_b32_dpp v24, v26 row_ror:8 row_mask:0xf bank_mask:0xf
v_mov_b32_dpp v25, v27 row_ror:8 row_mask:0xf bank_mask:0xf
v_max_u32 v27, v27, v25
v_med3_u32 v27, v26, v27, v24
v_max_u32 v26, v26, v24
s_nop 1
v_mov_b32_dpp v24, v26 row_ror:4 row_mask:0xf bank_mask:0xf
v_mov_b32_dpp v25, v27 row_ror:4 row_mask:0xf bank_mask:0xf
v_max_u32 v27, v27, v25
v_med3_u32 v27, v26, v27, v24
v_max_u32 v26, v26, v24
s_nop 1
v_mov_b32_dpp v24, v26 row_ror:2 row_mask:0xf bank_mask:0xf
v_mov_b32_dpp v25, v27 row_ror:2 row_mask:0xf bank_mask:0xf
)" R"(
v_max_u32 v27, v27, v25
v_med3_u32 v27, v26, v27, v24
v_max_u32 v26, v26, v24
s_nop 1
v_mov_b32_dpp v24, v26 row_ror:1 row_mask:0xf bank_mask:0xf
v_mov_b32_dpp v25, v27 row_ror:1 row_mask:0xf bank_mask:0xf
v_max_u32 v27, v27, v25
v_med3_u32 v27, v26, v27, v24
v_max_u32 v26, v26, v24
v_and_b32 v26, 0xffff, v26
v_lshl_or_b32 v26, v27, 16, v26
v_not_b32 v24, v26
v_mov_b32 v25, s3
s_mov_b32 s28, 0xc108000
buffer_store_dwordx2 v[24:25], v7, s[20:23], s28 offen sc0 sc1
s_branch .LzT2E
.LzA7:
s_waitcnt vmcnt(0)
s_nop 7
v_mul_f32_e32 v96, s54, v96
v_mul_f32_e32 v97, s54, v97
Cv 96,96,97
v_accvgpr_write_b32 a112, v96
v_mul_f32_e32 v98, s54, v98
v_mul_f32_e32 v99, s54, v99
Cv 98,98,99
v_accvgpr_write_b32 a113, v98
v_mul_f32_e32 v100, s54, v100
v_mul_f32_e32 v101, s54, v101
Cv 100,100,101
v_accvgpr_write_b32 a114, v100
v_mul_f32_e32 v102, s54, v102
v_mul_f32_e32 v103, s54, v103
Cv 102,102,103
v_accvgpr_write_b32 a115, v102
v_mul_f32_e32 v104, s54, v104
v_mul_f32_e32 v105, s54, v105
Cv 104,104,105
v_accvgpr_write_b32 a116, v104
v_mul_f32_e32 v106, s54, v106
v_mul_f32_e32 v107, s54, v107
Cv 106,106,107
v_accvgpr_write_b32 a117, v106
v_mul_f32_e32 v108, s54, v108
v_mul_f32_e32 v109, s54, v109
Cv 108,108,109
v_accvgpr_write_b32 a118, v108
v_mul_f32_e32 v110, s54, v110
v_mul_f32_e32 v111, s54, v111
Cv 110,110,111
v_accvgpr_write_b32 a119, v110
v_mul_f32_e32 v112, s54, v112
v_mul_f32_e32 v113, s54, v113
Cv 112,112,113
v_accvgpr_write_b32 a120, v112
v_mul_f32_e32 v114, s54, v114
v_mul_f32_e32 v115, s54, v115
Cv 114,114,115
v_accvgpr_write_b32 a121, v114
v_mul_f32_e32 v116, s54, v116
v_mul_f32_e32 v117, s54, v117
Cv 116,116,117
v_accvgpr_write_b32 a122, v116
v_mul_f32_e32 v118, s54, v118
)" R"(
v_mul_f32_e32 v119, s54, v119
Cv 118,118,119
v_accvgpr_write_b32 a123, v118
v_mul_f32_e32 v120, s54, v120
v_mul_f32_e32 v121, s54, v121
Cv 120,120,121
v_accvgpr_write_b32 a124, v120
v_mul_f32_e32 v122, s54, v122
v_mul_f32_e32 v123, s54, v123
Cv 122,122,123
v_accvgpr_write_b32 a125, v122
v_mul_f32_e32 v124, s54, v124
v_mul_f32_e32 v125, s54, v125
Cv 124,124,125
v_accvgpr_write_b32 a126, v124
v_mul_f32_e32 v126, s54, v126
v_mul_f32_e32 v127, s54, v127
Cv 126,126,127
v_accvgpr_write_b32 a127, v126
v_accvgpr_mov_b32 a64, a224
v_accvgpr_mov_b32 a65, a225
v_accvgpr_mov_b32 a66, a226
v_accvgpr_mov_b32 a67, a227
v_accvgpr_mov_b32 a68, a228
v_accvgpr_mov_b32 a69, a229
v_accvgpr_mov_b32 a70, a230
v_accvgpr_mov_b32 a71, a231
v_accvgpr_mov_b32 a72, a232
v_accvgpr_mov_b32 a73, a233
v_accvgpr_mov_b32 a74, a234
v_accvgpr_mov_b32 a75, a235
v_accvgpr_mov_b32 a76, a236
v_accvgpr_mov_b32 a77, a237
v_accvgpr_mov_b32 a78, a238
v_accvgpr_mov_b32 a79, a239
v_accvgpr_mov_b32 a80, a240
v_accvgpr_mov_b32 a81, a241
v_accvgpr_mov_b32 a82, a242
v_accvgpr_mov_b32 a83, a243
v_accvgpr_mov_b32 a84, a244
v_accvgpr_mov_b32 a85, a245
v_accvgpr_mov_b32 a86, a246
v_accvgpr_mov_b32 a87, a247
v_accvgpr_mov_b32 a88, a248
v_accvgpr_mov_b32 a89, a249
v_accvgpr_mov_b32 a90, a250
v_accvgpr_mov_b32 a91, a251
v_accvgpr_mov_b32 a92, a252
v_accvgpr_mov_b32 a93, a253
v_accvgpr_mov_b32 a94, a254
v_accvgpr_mov_b32 a95, a255
v_accvgpr_mov_b32 a96, a192
v_accvgpr_mov_b32 a97, a193
v_accvgpr_mov_b32 a98, a194
v_accvgpr_mov_b32 a99, a195
v_accvgpr_mov_b32 a100, a196
v_accvgpr_mov_b32 a101, a197
v_accvgpr_mov_b32 a102, a198
v_accvgpr_mov_b32 a103, a199
v_accvgpr_mov_b32 a104, a200
v_accvgpr_mov_b32 a105, a201
v_accvgpr_mov_b32 a106, a202
v_accvgpr_mov_b32 a107, a203
v_accvgpr_mov_b32 a108, a204
)" R"(
v_accvgpr_mov_b32 a109, a205
v_accvgpr_mov_b32 a110, a206
v_accvgpr_mov_b32 a111, a207
s_mov_b32 s94, 2
s_nop 1
s_branch .LzA7R
.Lzsp9:
s_sub_u32 s53, s53, 1
s_cbranch_scc1 .Lzsq9
s_sleep 1
buffer_load_dword v4, v5, s[20:23], s50 offen offset:0 sc0 sc1
s_waitcnt vmcnt(0)
v_cmp_eq_u32_e32 vcc, s3, v4
s_andn2_b64 s[28:29], s[60:61], vcc
s_cbranch_scc1 .Lzsp9
s_branch .Lzok9
.Lzsq9:
s_mov_b32 s53, 0
s_branch .Lzok9
.Lzmk0:
s_call_b64 s[30:31], .LzMASK0
s_branch .Lzmr0
.Lzfx0:
s_mov_b32 s88, 3
s_call_b64 s[30:31], .LzFIX0
s_branch .Lzfr0
.Lzmk1:
s_call_b64 s[30:31], .LzMASK1
s_branch .Lzmr1
.Lzsp10:
s_sub_u32 s53, s53, 1
s_cbranch_scc1 .Lzsq10
s_sleep 1
buffer_load_dword v4, v5, s[20:23], s50 offen offset:0 sc0 sc1
s_waitcnt vmcnt(0)
v_cmp_eq_u32_e32 vcc, s3, v4
s_andn2_b64 s[28:29], s[60:61], vcc
s_cbranch_scc1 .Lzsp10
s_branch .Lzok10
.Lzsq10:
s_mov_b32 s53, 0
s_branch .Lzok10
.Lzfx1:
s_mov_b32 s88, 2
s_call_b64 s[30:31], .LzFIX1
s_branch .Lzfr1
.Lzmk2:
s_call_b64 s[30:31], .LzMASK0
s_branch .Lzmr2
.Lzfx2:
s_mov_b32 s88, 2
s_call_b64 s[30:31], .LzFIX0
s_branch .Lzfr2
.Lzmk3:
s_call_b64 s[30:31], .LzMASK1
s_branch .Lzmr3
.Lzsp11:
s_sub_u32 s53, s53, 1
s_cbranch_scc1 .Lzsq11
s_sleep 1
buffer_load_dword v4, v5, s[20:23], s50 offen offset:0 sc0 sc1
s_waitcnt vmcnt(0)
v_cmp_eq_u32_e32 vcc, s3, v4
s_andn2_b64 s[28:29], s[60:61], vcc
)" R"(
s_cbranch_scc1 .Lzsp11
s_branch .Lzok11
.Lzsq11:
s_mov_b32 s53, 0
s_branch .Lzok11
.Lzfx3:
s_mov_b32 s88, 0
s_call_b64 s[30:31], .LzFIX1
s_branch .Lzfr3
.Lzmk4:
s_call_b64 s[30:31], .LzMASK0
s_branch .Lzmr4
.Lzfx4:
s_mov_b32 s88, 3
s_call_b64 s[30:31], .LzFIX0
s_branch .Lzfr4
.Lzmk5:
s_call_b64 s[30:31], .LzMASK1
s_branch .Lzmr5
.Lzfx5:
s_mov_b32 s88, 0
s_call_b64 s[30:31], .LzFIX1
s_branch .Lzfr5
.Lzsp12:
s_sub_u32 s53, s53, 1
s_cbranch_scc1 .Lzsq12
s_sleep 1
buffer_load_dword v4, v7, s[20:23], s52 offen offset:0 sc0 sc1
s_waitcnt vmcnt(0)
v_cmp_eq_u32_e32 vcc, s3, v4
s_andn2_b64 s[28:29], 1, vcc
s_cbranch_scc1 .Lzsp12
s_branch .Lzok12
.Lzsq12:
s_mov_b32 s53, 0
s_branch .Lzok12
.Lzsp13:
s_sub_u32 s53, s53, 1
s_cbranch_scc1 .Lzsq13
s_sleep 1
buffer_load_dword v4, v7, s[20:23], s52 offen offset:4 sc0 sc1
s_waitcnt vmcnt(0)
v_cmp_eq_u32_e32 vcc, s3, v4
s_andn2_b64 s[28:29], 1, vcc
s_cbranch_scc1 .Lzsp13
s_branch .Lzok13
.Lzsq13:
s_mov_b32 s53, 0
s_branch .Lzok13
.Lzsp14:
s_sub_u32 s53, s53, 1
s_cbranch_scc1 .Lzsq14
s_sleep 1
buffer_load_dword v4, v2, s[20:23], s85 offen sc0 sc1
s_waitcnt vmcnt(0)
v_xor_b32 v4, s3, v4
v_cmp_gt_u32_e32 vcc, 2, v4
s_andn2_b32 s28, 0xff, vcc_lo
s_cbranch_scc1 .Lzsp14
s_branch .Lzok14
.Lzsq14:
s_mov_b32 s53, 0
s_branch .Lzok14
.Lzsp15:
)" R"(
s_sub_u32 s53, s53, 1
s_cbranch_scc1 .Lzsq15
s_sleep 1
buffer_load_dword v4, v5, s[20:23], s50 offen offset:0 sc0 sc1
s_waitcnt vmcnt(0)
v_cmp_eq_u32_e32 vcc, s3, v4
s_andn2_b64 s[28:29], s[60:61], vcc
s_cbranch_scc1 .Lzsp15
s_branch .Lzok15
.Lzsq15:
s_mov_b32 s53, 0
s_branch .Lzok15
.Lzsp16:
s_sub_u32 s53, s53, 1
s_cbranch_scc1 .Lzsq16
s_sleep 1
buffer_load_dword v4, v5, s[20:23], s50 offen offset:0 sc0 sc1
s_waitcnt vmcnt(0)
v_cmp_eq_u32_e32 vcc, s3, v4
s_andn2_b64 s[28:29], s[60:61], vcc
s_cbranch_scc1 .Lzsp16
s_branch .Lzok16
.Lzsq16:
s_mov_b32 s53, 0
s_branch .Lzok16
.Lzsp17:
s_sub_u32 s53, s53, 1
s_cbranch_scc1 .Lzsq17
s_sleep 1
buffer_load_dword v4, v5, s[20:23], s50 offen offset:0 sc0 sc1
s_waitcnt vmcnt(0)
v_cmp_eq_u32_e32 vcc, s3, v4
s_andn2_b64 s[28:29], s[60:61], vcc
s_cbranch_scc1 .Lzsp17
s_branch .Lzok17
.Lzsq17:
s_mov_b32 s53, 0
s_branch .Lzok17
.Lzsp18:
s_sub_u32 s53, s53, 1
s_cbranch_scc1 .Lzsq18
s_sleep 1
buffer_load_dword v4, v5, s[20:23], s50 offen offset:0 sc0 sc1
s_waitcnt vmcnt(0)
v_cmp_eq_u32_e32 vcc, s3, v4
s_andn2_b64 s[28:29], s[60:61], vcc
s_cbranch_scc1 .Lzsp18
s_branch .Lzok18
.Lzsq18:
s_mov_b32 s53, 0
s_branch .Lzok18
.Lzsp19:
s_sub_u32 s53, s53, 1
s_cbranch_scc1 .Lzsq19
s_sleep 1
buffer_load_dword v4, v5, s[20:23], s50 offen offset:0 sc0 sc1
s_waitcnt vmcnt(0)
v_cmp_eq_u32_e32 vcc, s3, v4
s_andn2_b64 s[28:29], s[60:61], vcc
s_cbranch_scc1 .Lzsp19
s_branch .Lzok19
.Lzsq19:
s_mov_b32 s53, 0
s_branch .Lzok19
)" R"(
.Lzfx6:
s_mov_b32 s88, 3
s_call_b64 s[30:31], .LzFIX0
s_branch .Lzfr6
.Lzsp20:
s_sub_u32 s53, s53, 1
s_cbranch_scc1 .Lzsq20
s_sleep 1
buffer_load_dword v4, v5, s[20:23], s50 offen offset:0 sc0 sc1
s_waitcnt vmcnt(0)
v_cmp_eq_u32_e32 vcc, s3, v4
s_andn2_b64 s[28:29], s[60:61], vcc
s_cbranch_scc1 .Lzsp20
s_branch .Lzok20
.Lzsq20:
s_mov_b32 s53, 0
s_branch .Lzok20
.Lzfx7:
s_mov_b32 s88, 2
s_call_b64 s[30:31], .LzFIX1
s_branch .Lzfr7
.Lzmk6:
s_call_b64 s[30:31], .LzMASK0
s_branch .Lzmr6
.Lzfx8:
s_mov_b32 s88, 2
s_call_b64 s[30:31], .LzFIX0
s_branch .Lzfr8
.Lzmk7:
s_call_b64 s[30:31], .LzMASK1
s_branch .Lzmr7
.Lzsp21:
s_sub_u32 s53, s53, 1
s_cbranch_scc1 .Lzsq21
s_sleep 1
buffer_load_dword v4, v5, s[20:23], s50 offen offset:0 sc0 sc1
s_waitcnt vmcnt(0)
v_cmp_eq_u32_e32 vcc, s3, v4
s_andn2_b64 s[28:29], s[60:61], vcc
s_cbranch_scc1 .Lzsp21
s_branch .Lzok21
.Lzsq21:
s_mov_b32 s53, 0
s_branch .Lzok21
.Lzfx9:
s_mov_b32 s88, 2
s_call_b64 s[30:31], .LzFIX1
s_branch .Lzfr9
.Lzmk8:
s_call_b64 s[30:31], .LzMASK0
s_branch .Lzmr8
.Lzfx10:
s_mov_b32 s88, 2
s_call_b64 s[30:31], .LzFIX0
s_branch .Lzfr10
.Lzmk9:
s_call_b64 s[30:31], .LzMASK1
s_branch .Lzmr9
.Lzsp22:
s_sub_u32 s53, s53, 1
s_cbranch_scc1 .Lzsq22
s_sleep 1
buffer_load_dword v4, v7, s[20:23], s65 offen offset:0 sc0 sc1
s_waitcnt vmcnt(0)
)" R"(
v_cmp_eq_u32_e32 vcc, s3, v4
s_andn2_b64 s[28:29], s[60:61], vcc
s_cbranch_scc1 .Lzsp22
s_branch .Lzok22
.Lzsq22:
s_mov_b32 s53, 0
s_branch .Lzok22
.Lzfx11:
s_mov_b32 s88, 2
s_call_b64 s[30:31], .LzFIX1
s_branch .Lzfr11
.Lzmk10:
s_call_b64 s[30:31], .LzMASK0
s_branch .Lzmr10
.Lzfx12:
s_mov_b32 s88, 2
s_call_b64 s[30:31], .LzFIX0
s_branch .Lzfr12
.Lzmk11:
s_call_b64 s[30:31], .LzMASK1
s_branch .Lzmr11
.Lzfx13:
s_mov_b32 s88, 0
s_call_b64 s[30:31], .LzFIX1
s_branch .Lzfr13
.LzMRG:
s_waitcnt vmcnt(0)
s_nop 7
v_mov_b32 v25, v64
v_mov_b32 v24, v25
s_nop 1
v_permlane32_swap_b32_e32 v25, v24
v_add_f32_e32 v25, v25, v24
v_mov_b32 v24, v25
s_nop 1
v_permlane16_swap_b32_e32 v25, v24
v_add_f32_e32 v25, v25, v24
v_cmp_lt_f32_e32 vcc, 0, v25
s_nop 1
v_cndmask_b32_e32 v66, v16, v66, vcc
v_sub_f32_e32 v28, v16, v66
v_min_f32_e32 v26, 0, v28
v_exp_f32_e32 v26, v26
v_max_f32_e32 v28, 0, v28
v_sub_f32_e32 v24, 0, v28
v_exp_f32_e32 v24, v24
v_mul_f32_e32 v64, v26, v64
v_mul_f32_e32 v12, v24, v12
v_mul_f32_e32 v128, v24, v128
v_mul_f32_e32 v129, v24, v129
v_mul_f32_e32 v130, v24, v130
v_mul_f32_e32 v131, v24, v131
v_mul_f32_e32 v132, v24, v132
v_mul_f32_e32 v133, v24, v133
v_mul_f32_e32 v134, v24, v134
v_mul_f32_e32 v135, v24, v135
v_mul_f32_e32 v144, v24, v144
v_mul_f32_e32 v145, v24, v145
v_mul_f32_e32 v146, v24, v146
v_mul_f32_e32 v147, v24, v147
v_mul_f32_e32 v148, v24, v148
v_mul_f32_e32 v149, v24, v149
v_mul_f32_e32 v150, v24, v150
v_mul_f32_e32 v151, v24, v151
)" R"(
v_mul_f32_e32 v160, v24, v160
v_mul_f32_e32 v161, v24, v161
v_mul_f32_e32 v162, v24, v162
v_mul_f32_e32 v163, v24, v163
v_mul_f32_e32 v164, v24, v164
v_mul_f32_e32 v165, v24, v165
v_mul_f32_e32 v166, v24, v166
v_mul_f32_e32 v167, v24, v167
v_mul_f32_e32 v176, v24, v176
v_mul_f32_e32 v177, v24, v177
v_mul_f32_e32 v178, v24, v178
v_mul_f32_e32 v179, v24, v179
v_mul_f32_e32 v180, v24, v180
v_mul_f32_e32 v181, v24, v181
v_mul_f32_e32 v182, v24, v182
v_mul_f32_e32 v183, v24, v183
v_mul_f32_e32 v192, v24, v192
v_mul_f32_e32 v193, v24, v193
v_mul_f32_e32 v194, v24, v194
v_mul_f32_e32 v195, v24, v195
v_mul_f32_e32 v196, v24, v196
v_mul_f32_e32 v197, v24, v197
v_mul_f32_e32 v198, v24, v198
v_mul_f32_e32 v199, v24, v199
v_mul_f32_e32 v208, v24, v208
v_mul_f32_e32 v209, v24, v209
v_mul_f32_e32 v210, v24, v210
v_mul_f32_e32 v211, v24, v211
v_mul_f32_e32 v212, v24, v212
v_mul_f32_e32 v213, v24, v213
v_mul_f32_e32 v214, v24, v214
v_mul_f32_e32 v215, v24, v215
v_mul_f32_e32 v224, v24, v224
v_mul_f32_e32 v225, v24, v225
v_mul_f32_e32 v226, v24, v226
v_mul_f32_e32 v227, v24, v227
v_mul_f32_e32 v228, v24, v228
v_mul_f32_e32 v229, v24, v229
v_mul_f32_e32 v230, v24, v230
v_mul_f32_e32 v231, v24, v231
v_mul_f32_e32 v240, v24, v240
v_mul_f32_e32 v241, v24, v241
v_mul_f32_e32 v242, v24, v242
v_mul_f32_e32 v243, v24, v243
v_mul_f32_e32 v244, v24, v244
v_mul_f32_e32 v245, v24, v245
v_mul_f32_e32 v246, v24, v246
v_mul_f32_e32 v247, v24, v247
v_sub_f32_e32 v16, v16, v28
v_sub_f32_e32 v17, v17, v28
v_sub_f32_e32 v18, v18, v28
v_sub_f32_e32 v19, v19, v28
v_sub_f32_e32 v96, v96, v28
v_sub_f32_e32 v97, v97, v28
v_sub_f32_e32 v98, v98, v28
v_sub_f32_e32 v99, v99, v28
v_sub_f32_e32 v104, v104, v28
v_sub_f32_e32 v105, v105, v28
v_sub_f32_e32 v106, v106, v28
v_sub_f32_e32 v107, v107, v28
v_sub_f32_e32 v112, v112, v28
v_sub_f32_e32 v113, v113, v28
v_sub_f32_e32 v114, v114, v28
v_sub_f32_e32 v115, v115, v28
)" R"(
v_sub_f32_e32 v120, v120, v28
v_sub_f32_e32 v121, v121, v28
v_sub_f32_e32 v122, v122, v28
v_sub_f32_e32 v123, v123, v28
v_accvgpr_read_b32 v25, a128
v_lshlrev_b32 v24, 16, v25
v_and_b32 v25, 0xffff0000, v25
v_mul_f32_e32 v24, v26, v24
v_mul_f32_e32 v25, v26, v25
Cv 24,24,25
v_accvgpr_write_b32 a128, v24
v_accvgpr_read_b32 v25, a129
v_lshlrev_b32 v24, 16, v25
v_and_b32 v25, 0xffff0000, v25
v_mul_f32_e32 v24, v26, v24
v_mul_f32_e32 v25, v26, v25
Cv 24,24,25
v_accvgpr_write_b32 a129, v24
v_accvgpr_read_b32 v25, a130
v_lshlrev_b32 v24, 16, v25
v_and_b32 v25, 0xffff0000, v25
v_mul_f32_e32 v24, v26, v24
v_mul_f32_e32 v25, v26, v25
Cv 24,24,25
v_accvgpr_write_b32 a130, v24
v_accvgpr_read_b32 v25, a131
v_lshlrev_b32 v24, 16, v25
v_and_b32 v25, 0xffff0000, v25
v_mul_f32_e32 v24, v26, v24
v_mul_f32_e32 v25, v26, v25
Cv 24,24,25
v_accvgpr_write_b32 a131, v24
v_accvgpr_read_b32 v25, a136
v_lshlrev_b32 v24, 16, v25
v_and_b32 v25, 0xffff0000, v25
v_mul_f32_e32 v24, v26, v24
v_mul_f32_e32 v25, v26, v25
Cv 24,24,25
v_accvgpr_write_b32 a136, v24
v_accvgpr_read_b32 v25, a137
v_lshlrev_b32 v24, 16, v25
v_and_b32 v25, 0xffff0000, v25
v_mul_f32_e32 v24, v26, v24
v_mul_f32_e32 v25, v26, v25
Cv 24,24,25
v_accvgpr_write_b32 a137, v24
v_accvgpr_read_b32 v25, a138
v_lshlrev_b32 v24, 16, v25
v_and_b32 v25, 0xffff0000, v25
v_mul_f32_e32 v24, v26, v24
v_mul_f32_e32 v25, v26, v25
Cv 24,24,25
v_accvgpr_write_b32 a138, v24
v_accvgpr_read_b32 v25, a139
v_lshlrev_b32 v24, 16, v25
v_and_b32 v25, 0xffff0000, v25
v_mul_f32_e32 v24, v26, v24
v_mul_f32_e32 v25, v26, v25
Cv 24,24,25
v_accvgpr_write_b32 a139, v24
v_accvgpr_read_b32 v25, a144
v_lshlrev_b32 v24, 16, v25
v_and_b32 v25, 0xffff0000, v25
v_mul_f32_e32 v24, v26, v24
)" R"(
v_mul_f32_e32 v25, v26, v25
Cv 24,24,25
v_accvgpr_write_b32 a144, v24
v_accvgpr_read_b32 v25, a145
v_lshlrev_b32 v24, 16, v25
v_and_b32 v25, 0xffff0000, v25
v_mul_f32_e32 v24, v26, v24
v_mul_f32_e32 v25, v26, v25
Cv 24,24,25
v_accvgpr_write_b32 a145, v24
v_accvgpr_read_b32 v25, a146
v_lshlrev_b32 v24, 16, v25
v_and_b32 v25, 0xffff0000, v25
v_mul_f32_e32 v24, v26, v24
v_mul_f32_e32 v25, v26, v25
Cv 24,24,25
v_accvgpr_write_b32 a146, v24
v_accvgpr_read_b32 v25, a147
v_lshlrev_b32 v24, 16, v25
v_and_b32 v25, 0xffff0000, v25
v_mul_f32_e32 v24, v26, v24
v_mul_f32_e32 v25, v26, v25
Cv 24,24,25
v_accvgpr_write_b32 a147, v24
v_accvgpr_read_b32 v25, a152
v_lshlrev_b32 v24, 16, v25
v_and_b32 v25, 0xffff0000, v25
v_mul_f32_e32 v24, v26, v24
v_mul_f32_e32 v25, v26, v25
Cv 24,24,25
v_accvgpr_write_b32 a152, v24
v_accvgpr_read_b32 v25, a153
v_lshlrev_b32 v24, 16, v25
v_and_b32 v25, 0xffff0000, v25
v_mul_f32_e32 v24, v26, v24
v_mul_f32_e32 v25, v26, v25
Cv 24,24,25
v_accvgpr_write_b32 a153, v24
v_accvgpr_read_b32 v25, a154
v_lshlrev_b32 v24, 16, v25
v_and_b32 v25, 0xffff0000, v25
v_mul_f32_e32 v24, v26, v24
v_mul_f32_e32 v25, v26, v25
Cv 24,24,25
v_accvgpr_write_b32 a154, v24
v_accvgpr_read_b32 v25, a155
v_lshlrev_b32 v24, 16, v25
v_and_b32 v25, 0xffff0000, v25
v_mul_f32_e32 v24, v26, v24
v_mul_f32_e32 v25, v26, v25
Cv 24,24,25
v_accvgpr_write_b32 a155, v24
v_accvgpr_read_b32 v25, a160
v_lshlrev_b32 v24, 16, v25
v_and_b32 v25, 0xffff0000, v25
v_mul_f32_e32 v24, v26, v24
v_mul_f32_e32 v25, v26, v25
Cv 24,24,25
v_accvgpr_write_b32 a160, v24
v_accvgpr_read_b32 v25, a161
v_lshlrev_b32 v24, 16, v25
v_and_b32 v25, 0xffff0000, v25
v_mul_f32_e32 v24, v26, v24
v_mul_f32_e32 v25, v26, v25
)" R"(
Cv 24,24,25
v_accvgpr_write_b32 a161, v24
v_accvgpr_read_b32 v25, a162
v_lshlrev_b32 v24, 16, v25
v_and_b32 v25, 0xffff0000, v25
v_mul_f32_e32 v24, v26, v24
v_mul_f32_e32 v25, v26, v25
Cv 24,24,25
v_accvgpr_write_b32 a162, v24
v_accvgpr_read_b32 v25, a163
v_lshlrev_b32 v24, 16, v25
v_and_b32 v25, 0xffff0000, v25
v_mul_f32_e32 v24, v26, v24
v_mul_f32_e32 v25, v26, v25
Cv 24,24,25
v_accvgpr_write_b32 a163, v24
v_accvgpr_read_b32 v25, a168
v_lshlrev_b32 v24, 16, v25
v_and_b32 v25, 0xffff0000, v25
v_mul_f32_e32 v24, v26, v24
v_mul_f32_e32 v25, v26, v25
Cv 24,24,25
v_accvgpr_write_b32 a168, v24
v_accvgpr_read_b32 v25, a169
v_lshlrev_b32 v24, 16, v25
v_and_b32 v25, 0xffff0000, v25
v_mul_f32_e32 v24, v26, v24
v_mul_f32_e32 v25, v26, v25
Cv 24,24,25
v_accvgpr_write_b32 a169, v24
v_accvgpr_read_b32 v25, a170
v_lshlrev_b32 v24, 16, v25
v_and_b32 v25, 0xffff0000, v25
v_mul_f32_e32 v24, v26, v24
v_mul_f32_e32 v25, v26, v25
Cv 24,24,25
v_accvgpr_write_b32 a170, v24
v_accvgpr_read_b32 v25, a171
v_lshlrev_b32 v24, 16, v25
v_and_b32 v25, 0xffff0000, v25
v_mul_f32_e32 v24, v26, v24
v_mul_f32_e32 v25, v26, v25
Cv 24,24,25
v_accvgpr_write_b32 a171, v24
v_accvgpr_read_b32 v25, a176
v_lshlrev_b32 v24, 16, v25
v_and_b32 v25, 0xffff0000, v25
v_mul_f32_e32 v24, v26, v24
v_mul_f32_e32 v25, v26, v25
Cv 24,24,25
v_accvgpr_write_b32 a176, v24
v_accvgpr_read_b32 v25, a177
v_lshlrev_b32 v24, 16, v25
v_and_b32 v25, 0xffff0000, v25
v_mul_f32_e32 v24, v26, v24
v_mul_f32_e32 v25, v26, v25
Cv 24,24,25
v_accvgpr_write_b32 a177, v24
v_accvgpr_read_b32 v25, a178
v_lshlrev_b32 v24, 16, v25
v_and_b32 v25, 0xffff0000, v25
v_mul_f32_e32 v24, v26, v24
v_mul_f32_e32 v25, v26, v25
Cv 24,24,25
)" R"(
v_accvgpr_write_b32 a178, v24
v_accvgpr_read_b32 v25, a179
v_lshlrev_b32 v24, 16, v25
v_and_b32 v25, 0xffff0000, v25
v_mul_f32_e32 v24, v26, v24
v_mul_f32_e32 v25, v26, v25
Cv 24,24,25
v_accvgpr_write_b32 a179, v24
v_accvgpr_read_b32 v25, a184
v_lshlrev_b32 v24, 16, v25
v_and_b32 v25, 0xffff0000, v25
v_mul_f32_e32 v24, v26, v24
v_mul_f32_e32 v25, v26, v25
Cv 24,24,25
v_accvgpr_write_b32 a184, v24
v_accvgpr_read_b32 v25, a185
v_lshlrev_b32 v24, 16, v25
v_and_b32 v25, 0xffff0000, v25
v_mul_f32_e32 v24, v26, v24
v_mul_f32_e32 v25, v26, v25
Cv 24,24,25
v_accvgpr_write_b32 a185, v24
v_accvgpr_read_b32 v25, a186
v_lshlrev_b32 v24, 16, v25
v_and_b32 v25, 0xffff0000, v25
v_mul_f32_e32 v24, v26, v24
v_mul_f32_e32 v25, v26, v25
Cv 24,24,25
v_accvgpr_write_b32 a186, v24
v_accvgpr_read_b32 v25, a187
v_lshlrev_b32 v24, 16, v25
v_and_b32 v25, 0xffff0000, v25
v_mul_f32_e32 v24, v26, v24
v_mul_f32_e32 v25, v26, v25
Cv 24,24,25
v_accvgpr_write_b32 a187, v24
v_mov_b32 v25, v65
v_mov_b32 v24, v25
s_nop 1
v_permlane32_swap_b32_e32 v25, v24
v_add_f32_e32 v25, v25, v24
v_mov_b32 v24, v25
s_nop 1
v_permlane16_swap_b32_e32 v25, v24
v_add_f32_e32 v25, v25, v24
v_cmp_lt_f32_e32 vcc, 0, v25
s_nop 1
v_cndmask_b32_e32 v67, v20, v67, vcc
v_sub_f32_e32 v29, v20, v67
v_min_f32_e32 v27, 0, v29
v_exp_f32_e32 v27, v27
v_max_f32_e32 v29, 0, v29
v_sub_f32_e32 v24, 0, v29
v_exp_f32_e32 v24, v24
v_mul_f32_e32 v65, v27, v65
v_mul_f32_e32 v13, v24, v13
v_mul_f32_e32 v136, v24, v136
v_mul_f32_e32 v137, v24, v137
v_mul_f32_e32 v138, v24, v138
v_mul_f32_e32 v139, v24, v139
v_mul_f32_e32 v140, v24, v140
v_mul_f32_e32 v141, v24, v141
v_mul_f32_e32 v142, v24, v142
v_mul_f32_e32 v143, v24, v143
)" R"(
v_mul_f32_e32 v152, v24, v152
v_mul_f32_e32 v153, v24, v153
v_mul_f32_e32 v154, v24, v154
v_mul_f32_e32 v155, v24, v155
v_mul_f32_e32 v156, v24, v156
v_mul_f32_e32 v157, v24, v157
v_mul_f32_e32 v158, v24, v158
v_mul_f32_e32 v159, v24, v159
v_mul_f32_e32 v168, v24, v168
v_mul_f32_e32 v169, v24, v169
v_mul_f32_e32 v170, v24, v170
v_mul_f32_e32 v171, v24, v171
v_mul_f32_e32 v172, v24, v172
v_mul_f32_e32 v173, v24, v173
v_mul_f32_e32 v174, v24, v174
v_mul_f32_e32 v175, v24, v175
v_mul_f32_e32 v184, v24, v184
v_mul_f32_e32 v185, v24, v185
v_mul_f32_e32 v186, v24, v186
v_mul_f32_e32 v187, v24, v187
v_mul_f32_e32 v188, v24, v188
v_mul_f32_e32 v189, v24, v189
v_mul_f32_e32 v190, v24, v190
v_mul_f32_e32 v191, v24, v191
v_mul_f32_e32 v200, v24, v200
v_mul_f32_e32 v201, v24, v201
v_mul_f32_e32 v202, v24, v202
v_mul_f32_e32 v203, v24, v203
v_mul_f32_e32 v204, v24, v204
v_mul_f32_e32 v205, v24, v205
v_mul_f32_e32 v206, v24, v206
v_mul_f32_e32 v207, v24, v207
v_mul_f32_e32 v216, v24, v216
v_mul_f32_e32 v217, v24, v217
v_mul_f32_e32 v218, v24, v218
v_mul_f32_e32 v219, v24, v219
v_mul_f32_e32 v220, v24, v220
v_mul_f32_e32 v221, v24, v221
v_mul_f32_e32 v222, v24, v222
v_mul_f32_e32 v223, v24, v223
v_mul_f32_e32 v232, v24, v232
v_mul_f32_e32 v233, v24, v233
v_mul_f32_e32 v234, v24, v234
v_mul_f32_e32 v235, v24, v235
v_mul_f32_e32 v236, v24, v236
v_mul_f32_e32 v237, v24, v237
v_mul_f32_e32 v238, v24, v238
v_mul_f32_e32 v239, v24, v239
v_mul_f32_e32 v248, v24, v248
v_mul_f32_e32 v249, v24, v249
v_mul_f32_e32 v250, v24, v250
v_mul_f32_e32 v251, v24, v251
v_mul_f32_e32 v252, v24, v252
v_mul_f32_e32 v253, v24, v253
v_mul_f32_e32 v254, v24, v254
v_mul_f32_e32 v255, v24, v255
v_sub_f32_e32 v20, v20, v29
v_sub_f32_e32 v21, v21, v29
v_sub_f32_e32 v22, v22, v29
v_sub_f32_e32 v23, v23, v29
v_sub_f32_e32 v100, v100, v29
v_sub_f32_e32 v101, v101, v29
v_sub_f32_e32 v102, v102, v29
v_sub_f32_e32 v103, v103, v29
)" R"(
v_sub_f32_e32 v108, v108, v29
v_sub_f32_e32 v109, v109, v29
v_sub_f32_e32 v110, v110, v29
v_sub_f32_e32 v111, v111, v29
v_sub_f32_e32 v116, v116, v29
v_sub_f32_e32 v117, v117, v29
v_sub_f32_e32 v118, v118, v29
v_sub_f32_e32 v119, v119, v29
v_sub_f32_e32 v124, v124, v29
v_sub_f32_e32 v125, v125, v29
v_sub_f32_e32 v126, v126, v29
v_sub_f32_e32 v127, v127, v29
v_accvgpr_read_b32 v25, a132
v_lshlrev_b32 v24, 16, v25
v_and_b32 v25, 0xffff0000, v25
v_mul_f32_e32 v24, v27, v24
v_mul_f32_e32 v25, v27, v25
Cv 24,24,25
v_accvgpr_write_b32 a132, v24
v_accvgpr_read_b32 v25, a133
v_lshlrev_b32 v24, 16, v25
v_and_b32 v25, 0xffff0000, v25
v_mul_f32_e32 v24, v27, v24
v_mul_f32_e32 v25, v27, v25
Cv 24,24,25
v_accvgpr_write_b32 a133, v24
v_accvgpr_read_b32 v25, a134
v_lshlrev_b32 v24, 16, v25
v_and_b32 v25, 0xffff0000, v25
v_mul_f32_e32 v24, v27, v24
v_mul_f32_e32 v25, v27, v25
Cv 24,24,25
v_accvgpr_write_b32 a134, v24
v_accvgpr_read_b32 v25, a135
v_lshlrev_b32 v24, 16, v25
v_and_b32 v25, 0xffff0000, v25
v_mul_f32_e32 v24, v27, v24
v_mul_f32_e32 v25, v27, v25
Cv 24,24,25
v_accvgpr_write_b32 a135, v24
v_accvgpr_read_b32 v25, a140
v_lshlrev_b32 v24, 16, v25
v_and_b32 v25, 0xffff0000, v25
v_mul_f32_e32 v24, v27, v24
v_mul_f32_e32 v25, v27, v25
Cv 24,24,25
v_accvgpr_write_b32 a140, v24
v_accvgpr_read_b32 v25, a141
v_lshlrev_b32 v24, 16, v25
v_and_b32 v25, 0xffff0000, v25
v_mul_f32_e32 v24, v27, v24
v_mul_f32_e32 v25, v27, v25
Cv 24,24,25
v_accvgpr_write_b32 a141, v24
v_accvgpr_read_b32 v25, a142
v_lshlrev_b32 v24, 16, v25
v_and_b32 v25, 0xffff0000, v25
v_mul_f32_e32 v24, v27, v24
v_mul_f32_e32 v25, v27, v25
Cv 24,24,25
v_accvgpr_write_b32 a142, v24
v_accvgpr_read_b32 v25, a143
v_lshlrev_b32 v24, 16, v25
v_and_b32 v25, 0xffff0000, v25
)" R"(
v_mul_f32_e32 v24, v27, v24
v_mul_f32_e32 v25, v27, v25
Cv 24,24,25
v_accvgpr_write_b32 a143, v24
v_accvgpr_read_b32 v25, a148
v_lshlrev_b32 v24, 16, v25
v_and_b32 v25, 0xffff0000, v25
v_mul_f32_e32 v24, v27, v24
v_mul_f32_e32 v25, v27, v25
Cv 24,24,25
v_accvgpr_write_b32 a148, v24
v_accvgpr_read_b32 v25, a149
v_lshlrev_b32 v24, 16, v25
v_and_b32 v25, 0xffff0000, v25
v_mul_f32_e32 v24, v27, v24
v_mul_f32_e32 v25, v27, v25
Cv 24,24,25
v_accvgpr_write_b32 a149, v24
v_accvgpr_read_b32 v25, a150
v_lshlrev_b32 v24, 16, v25
v_and_b32 v25, 0xffff0000, v25
v_mul_f32_e32 v24, v27, v24
v_mul_f32_e32 v25, v27, v25
Cv 24,24,25
v_accvgpr_write_b32 a150, v24
v_accvgpr_read_b32 v25, a151
v_lshlrev_b32 v24, 16, v25
v_and_b32 v25, 0xffff0000, v25
v_mul_f32_e32 v24, v27, v24
v_mul_f32_e32 v25, v27, v25
Cv 24,24,25
v_accvgpr_write_b32 a151, v24
v_accvgpr_read_b32 v25, a156
v_lshlrev_b32 v24, 16, v25
v_and_b32 v25, 0xffff0000, v25
v_mul_f32_e32 v24, v27, v24
v_mul_f32_e32 v25, v27, v25
Cv 24,24,25
v_accvgpr_write_b32 a156, v24
v_accvgpr_read_b32 v25, a157
v_lshlrev_b32 v24, 16, v25
v_and_b32 v25, 0xffff0000, v25
v_mul_f32_e32 v24, v27, v24
v_mul_f32_e32 v25, v27, v25
Cv 24,24,25
v_accvgpr_write_b32 a157, v24
v_accvgpr_read_b32 v25, a158
v_lshlrev_b32 v24, 16, v25
v_and_b32 v25, 0xffff0000, v25
v_mul_f32_e32 v24, v27, v24
v_mul_f32_e32 v25, v27, v25
Cv 24,24,25
v_accvgpr_write_b32 a158, v24
v_accvgpr_read_b32 v25, a159
v_lshlrev_b32 v24, 16, v25
v_and_b32 v25, 0xffff0000, v25
v_mul_f32_e32 v24, v27, v24
v_mul_f32_e32 v25, v27, v25
Cv 24,24,25
v_accvgpr_write_b32 a159, v24
v_accvgpr_read_b32 v25, a164
v_lshlrev_b32 v24, 16, v25
v_and_b32 v25, 0xffff0000, v25
v_mul_f32_e32 v24, v27, v24
)" R"(
v_mul_f32_e32 v25, v27, v25
Cv 24,24,25
v_accvgpr_write_b32 a164, v24
v_accvgpr_read_b32 v25, a165
v_lshlrev_b32 v24, 16, v25
v_and_b32 v25, 0xffff0000, v25
v_mul_f32_e32 v24, v27, v24
v_mul_f32_e32 v25, v27, v25
Cv 24,24,25
v_accvgpr_write_b32 a165, v24
v_accvgpr_read_b32 v25, a166
v_lshlrev_b32 v24, 16, v25
v_and_b32 v25, 0xffff0000, v25
v_mul_f32_e32 v24, v27, v24
v_mul_f32_e32 v25, v27, v25
Cv 24,24,25
v_accvgpr_write_b32 a166, v24
v_accvgpr_read_b32 v25, a167
v_lshlrev_b32 v24, 16, v25
v_and_b32 v25, 0xffff0000, v25
v_mul_f32_e32 v24, v27, v24
v_mul_f32_e32 v25, v27, v25
Cv 24,24,25
v_accvgpr_write_b32 a167, v24
v_accvgpr_read_b32 v25, a172
v_lshlrev_b32 v24, 16, v25
v_and_b32 v25, 0xffff0000, v25
v_mul_f32_e32 v24, v27, v24
v_mul_f32_e32 v25, v27, v25
Cv 24,24,25
v_accvgpr_write_b32 a172, v24
v_accvgpr_read_b32 v25, a173
v_lshlrev_b32 v24, 16, v25
v_and_b32 v25, 0xffff0000, v25
v_mul_f32_e32 v24, v27, v24
v_mul_f32_e32 v25, v27, v25
Cv 24,24,25
v_accvgpr_write_b32 a173, v24
v_accvgpr_read_b32 v25, a174
v_lshlrev_b32 v24, 16, v25
v_and_b32 v25, 0xffff0000, v25
v_mul_f32_e32 v24, v27, v24
v_mul_f32_e32 v25, v27, v25
Cv 24,24,25
v_accvgpr_write_b32 a174, v24
v_accvgpr_read_b32 v25, a175
v_lshlrev_b32 v24, 16, v25
v_and_b32 v25, 0xffff0000, v25
v_mul_f32_e32 v24, v27, v24
v_mul_f32_e32 v25, v27, v25
Cv 24,24,25
v_accvgpr_write_b32 a175, v24
v_accvgpr_read_b32 v25, a180
v_lshlrev_b32 v24, 16, v25
v_and_b32 v25, 0xffff0000, v25
v_mul_f32_e32 v24, v27, v24
v_mul_f32_e32 v25, v27, v25
Cv 24,24,25
v_accvgpr_write_b32 a180, v24
v_accvgpr_read_b32 v25, a181
v_lshlrev_b32 v24, 16, v25
v_and_b32 v25, 0xffff0000, v25
v_mul_f32_e32 v24, v27, v24
v_mul_f32_e32 v25, v27, v25
)" R"(
Cv 24,24,25
v_accvgpr_write_b32 a181, v24
v_accvgpr_read_b32 v25, a182
v_lshlrev_b32 v24, 16, v25
v_and_b32 v25, 0xffff0000, v25
v_mul_f32_e32 v24, v27, v24
v_mul_f32_e32 v25, v27, v25
Cv 24,24,25
v_accvgpr_write_b32 a182, v24
v_accvgpr_read_b32 v25, a183
v_lshlrev_b32 v24, 16, v25
v_and_b32 v25, 0xffff0000, v25
v_mul_f32_e32 v24, v27, v24
v_mul_f32_e32 v25, v27, v25
Cv 24,24,25
v_accvgpr_write_b32 a183, v24
v_accvgpr_read_b32 v25, a188
v_lshlrev_b32 v24, 16, v25
v_and_b32 v25, 0xffff0000, v25
v_mul_f32_e32 v24, v27, v24
v_mul_f32_e32 v25, v27, v25
Cv 24,24,25
v_accvgpr_write_b32 a188, v24
v_accvgpr_read_b32 v25, a189
v_lshlrev_b32 v24, 16, v25
v_and_b32 v25, 0xffff0000, v25
v_mul_f32_e32 v24, v27, v24
v_mul_f32_e32 v25, v27, v25
Cv 24,24,25
v_accvgpr_write_b32 a189, v24
v_accvgpr_read_b32 v25, a190
v_lshlrev_b32 v24, 16, v25
v_and_b32 v25, 0xffff0000, v25
v_mul_f32_e32 v24, v27, v24
v_mul_f32_e32 v25, v27, v25
Cv 24,24,25
v_accvgpr_write_b32 a190, v24
v_accvgpr_read_b32 v25, a191
v_lshlrev_b32 v24, 16, v25
v_and_b32 v25, 0xffff0000, v25
v_mul_f32_e32 v24, v27, v24
v_mul_f32_e32 v25, v27, v25
Cv 24,24,25
v_accvgpr_write_b32 a191, v24
v_exp_f32_e32 v48, v96
v_exp_f32_e32 v24, v97
s_nop 0
v_add_f32_e32 v14, v48, v24
Cv 48,48,24
v_exp_f32_e32 v49, v98
v_exp_f32_e32 v25, v99
v_add_f32_e32 v14, v14, v49
v_add_f32_e32 v14, v14, v25
Cv 49,49,25
v_exp_f32_e32 v50, v104
v_exp_f32_e32 v26, v105
v_add_f32_e32 v14, v14, v50
v_add_f32_e32 v14, v14, v26
Cv 50,50,26
v_exp_f32_e32 v51, v106
v_exp_f32_e32 v27, v107
v_add_f32_e32 v14, v14, v51
v_add_f32_e32 v14, v14, v27
Cv 51,51,27
)" R"(
v_exp_f32_e32 v52, v100
v_exp_f32_e32 v28, v101
s_nop 0
v_add_f32_e32 v15, v52, v28
Cv 52,52,28
v_exp_f32_e32 v53, v102
v_exp_f32_e32 v29, v103
v_add_f32_e32 v15, v15, v53
v_add_f32_e32 v15, v15, v29
Cv 53,53,29
v_exp_f32_e32 v54, v108
v_exp_f32_e32 v24, v109
v_add_f32_e32 v15, v15, v54
v_add_f32_e32 v15, v15, v24
Cv 54,54,24
v_exp_f32_e32 v55, v110
v_exp_f32_e32 v25, v111
v_add_f32_e32 v15, v15, v55
v_add_f32_e32 v15, v15, v25
Cv 55,55,25
s_nop 1
s_branch .LzMRR
.Lzsp23:
s_sub_u32 s53, s53, 1
s_cbranch_scc1 .Lzsq23
s_sleep 1
buffer_load_dword v4, v7, s[20:23], s52 offen offset:0 sc0 sc1
s_waitcnt vmcnt(0)
v_cmp_eq_u32_e32 vcc, s3, v4
s_andn2_b64 s[28:29], 1, vcc
s_cbranch_scc1 .Lzsp23
s_branch .Lzok23
.Lzsq23:
s_mov_b32 s53, 0
s_branch .Lzok23
.Lzsp24:
s_sub_u32 s53, s53, 1
s_cbranch_scc1 .Lzsq24
s_sleep 1
buffer_load_dword v4, v7, s[20:23], s52 offen offset:4 sc0 sc1
s_waitcnt vmcnt(0)
v_cmp_eq_u32_e32 vcc, s3, v4
s_andn2_b64 s[28:29], 1, vcc
s_cbranch_scc1 .Lzsp24
s_branch .Lzok24
.Lzsq24:
s_mov_b32 s53, 0
s_branch .Lzok24
.Lzsp25:
s_sub_u32 s53, s53, 1
s_cbranch_scc1 .Lzsq25
s_sleep 1
buffer_load_dword v4, v7, s[20:23], s56 offen offset:4 sc0 sc1
s_waitcnt vmcnt(0)
v_cmp_eq_u32_e32 vcc, s3, v4
s_andn2_b64 s[28:29], 1, vcc
s_cbranch_scc1 .Lzsp25
s_branch .Lzok25
.Lzsq25:
s_mov_b32 s53, 0
s_branch .Lzok25
.LzBB:
v_lshlrev_b32 v95, 7, v0
s_mov_b32 s28, 0xc10c000
)" R"(
s_add_u32 s29, s28, 6144
.LzBB1:
buffer_load_dword v92, v95, s[20:23], s28 offen sc0 sc1
buffer_load_dword v93, v95, s[20:23], s29 offen sc0 sc1
s_waitcnt vmcnt(0)
v_cmp_ne_u32_e32 vcc, s3, v92
v_cmp_ne_u32_e64 s[30:31], s3, v93
s_or_b64 vcc, vcc, s[30:31]
s_cbranch_scc0 .LzBB2
s_sub_u32 s53, s53, 1
s_cbranch_scc0 .LzBB1
s_mov_b32 s53, 0
.LzBB2:
s_mul_i32 s28, s56, 7168
s_add_u32 s28, s28, 0xc158000
s_add_u32 s29, s28, 4096
buffer_load_dwordx4 v[64:67], v1, s[20:23], s28 offen offset:0 sc0 sc1
buffer_load_dwordx4 v[68:71], v1, s[20:23], s28 offen offset:1024 sc0 sc1
buffer_load_dwordx4 v[72:75], v1, s[20:23], s28 offen offset:2048 sc0 sc1
buffer_load_dwordx4 v[76:79], v1, s[20:23], s28 offen offset:3072 sc0 sc1
buffer_load_dwordx4 v[80:83], v1, s[20:23], s29 offen offset:0 sc0 sc1
buffer_load_dwordx4 v[84:87], v1, s[20:23], s29 offen offset:1024 sc0 sc1
buffer_load_dwordx4 v[88:91], v1, s[20:23], s29 offen offset:2048 sc0 sc1
s_cmp_gt_u32 s56, 27
s_cselect_b32 s28, 16, 0
s_cselect_b32 s29, 28, 0
s_lshr_b32 s28, s63, s28
s_and_b32 s28, s28, 0x7ff
s_sub_u32 s29, s56, s29
s_lshl_b32 s29, s29, 5
s_mul_i32 s83, s28, 896
s_add_u32 s29, s29, s83
v_and_b32 v95, 3, v0
v_lshlrev_b32 v95, 3, v95
buffer_load_dwordx2 v[92:93], v95, s[12:15], s29 offen
s_lshl4_add_u32 s28, s28, 0xc10000c
v_mov_b32 v94, 0
buffer_load_dword v94, v94, s[20:23], s28 offen sc0 sc1
s_waitcnt vmcnt(0)
v_add_f32_e32 v64, v64, v68
v_add_f32_e32 v65, v65, v69
v_add_f32_e32 v66, v66, v70
v_add_f32_e32 v67, v67, v71
v_add_f32_e32 v72, v72, v76
v_add_f32_e32 v73, v73, v77
v_add_f32_e32 v74, v74, v78
v_add_f32_e32 v75, v75, v79
v_add_f32_e32 v80, v80, v84
v_add_f32_e32 v81, v81, v85
v_add_f32_e32 v82, v82, v86
v_add_f32_e32 v83, v83, v87
v_add_f32_e32 v64, v64, v72
v_add_f32_e32 v65, v65, v73
v_add_f32_e32 v66, v66, v74
v_add_f32_e32 v67, v67, v75
v_add_f32_e32 v80, v80, v88
v_add_f32_e32 v81, v81, v89
v_add_f32_e32 v82, v82, v90
v_add_f32_e32 v83, v83, v91
v_add_f32_e32 v64, v64, v80
v_add_f32_e32 v65, v65, v81
v_add_f32_e32 v66, v66, v82
v_add_f32_e32 v67, v67, v83
v_mov_b32 v68, v64
)" R"(
s_nop 1
v_permlane32_swap_b32_e32 v64, v68
v_add_f32_e32 v64, v64, v68
v_mov_b32 v68, v64
s_nop 1
v_permlane16_swap_b32_e32 v64, v68
v_add_f32_e32 v64, v64, v68
v_mov_b32 v68, v65
s_nop 1
v_permlane32_swap_b32_e32 v65, v68
v_add_f32_e32 v65, v65, v68
v_mov_b32 v68, v65
s_nop 1
v_permlane16_swap_b32_e32 v65, v68
v_add_f32_e32 v65, v65, v68
v_mov_b32 v68, v66
s_nop 1
v_permlane32_swap_b32_e32 v66, v68
v_add_f32_e32 v66, v66, v68
v_mov_b32 v68, v66
s_nop 1
v_permlane16_swap_b32_e32 v66, v68
v_add_f32_e32 v66, v66, v68
v_mov_b32 v68, v67
s_nop 1
v_permlane32_swap_b32_e32 v67, v68
v_add_f32_e32 v67, v67, v68
v_mov_b32 v68, v67
s_nop 1
v_permlane16_swap_b32_e32 v67, v68
v_add_f32_e32 v67, v67, v68
v_add_f32_dpp v64, v64, v64 row_ror:8 row_mask:0xf bank_mask:0xf
v_add_f32_dpp v65, v65, v65 row_ror:8 row_mask:0xf bank_mask:0xf
v_add_f32_dpp v66, v66, v66 row_ror:8 row_mask:0xf bank_mask:0xf
v_add_f32_dpp v67, v67, v67 row_ror:8 row_mask:0xf bank_mask:0xf
v_add_f32_dpp v64, v64, v64 row_ror:4 row_mask:0xf bank_mask:0xf
v_add_f32_dpp v65, v65, v65 row_ror:4 row_mask:0xf bank_mask:0xf
v_add_f32_dpp v66, v66, v66 row_ror:4 row_mask:0xf bank_mask:0xf
v_add_f32_dpp v67, v67, v67 row_ror:4 row_mask:0xf bank_mask:0xf
v_mul_f32_e32 v94, 0xbfb8aa3b, v94
v_exp_f32_e32 v94, v94
Cv 65,64,65
v_lshlrev_b32 v64, 16, v65
v_and_b32 v65, 0xffff0000, v65
v_add_f32_e32 v94, 1.0, v94
v_rcp_f32_e32 v94, v94
Cv 67,66,67
v_lshlrev_b32 v66, 16, v67
v_and_b32 v67, 0xffff0000, v67
Cv 94,94,94
v_lshlrev_b32 v94, 16, v94
v_mul_f32_e32 v64, v94, v64
v_mul_f32_e32 v65, v94, v65
v_mul_f32_e32 v66, v94, v66
v_mul_f32_e32 v67, v94, v67
Cv 65,64,65
v_lshlrev_b32 v64, 16, v65
v_and_b32 v65, 0xffff0000, v65
Cv 67,66,67
v_lshlrev_b32 v66, 16, v67
v_and_b32 v67, 0xffff0000, v67
v_lshlrev_b32 v68, 16, v92
v_and_b32 v69, 0xffff0000, v92
v_lshlrev_b32 v70, 16, v93
)" R"(
v_and_b32 v71, 0xffff0000, v93
v_add_f32_e32 v64, v64, v68
v_add_f32_e32 v65, v65, v69
v_add_f32_e32 v66, v66, v70
v_add_f32_e32 v67, v67, v71
Cv 64,64,65
Cv 66,66,67
v_mov_b32 v65, s3
v_mov_b32 v67, s3
v_lshrrev_b32 v95, 2, v0
v_min_u32 v95, 1, v95
v_lshl_or_b32 v95, v95, 30, v1
s_lshl_b32 s28, s56, 7
s_add_u32 s28, s28, 0xc109000
buffer_store_dwordx4 v[64:67], v95, s[20:23], s28 offen sc0 sc1
buffer_load_dword v4, v165, s[20:23], s40 offen sc0 sc1
s_waitcnt vmcnt(0)
s_branch .LzBBR
.LzPBKX:
Mv 32,0,32
Rt 128,18,16384
Mv 64,0,36
buffer_load_dword v4, v5, s[20:23], s50 offen offset:0 sc0 sc1
Mv 96,0,40
Rt 130,18,18432
Mv 128,0,44
Mv 160,0,48
Rt 132,19,16384
Mv 192,0,52
Mv 224,0,56
Rt 134,19,18432
Ma 192,0,60
Ma 196,4,60
Rt 136,20,16384
Mv 228,4,56
Mv 196,4,52
Rt 138,20,18432
Mv 164,4,48
Mv 132,4,44
Rt 140,21,16384
Mv 100,4,40
Mv 68,4,36
Rt 142,21,18432
Mv 36,4,32
Mv 40,8,32
Rt 144,22,16384
Mv 72,8,36
Mv 104,8,40
Rt 146,22,18432
Mv 136,8,44
Mv 168,8,48
Rt 148,23,16384
Mv 200,8,52
Mv 232,8,56
Rt 150,23,18432
Ma 200,8,60
Ma 204,12,60
Rt 152,24,16384
Mv 236,12,56
Mv 204,12,52
Rt 154,24,18432
Mv 172,12,48
Mv 140,12,44
Rt 156,25,16384
)" R"(
Mv 108,12,40
Mv 76,12,36
Rt 158,25,18432
Mv 44,12,32
Mv 48,16,32
Rb 160,17,0
Mv 80,16,36
Mv 112,16,40
Rb 164,17,2048
Mv 144,16,44
Mv 176,16,48
Rb 168,17,8192
Mv 208,16,52
Mv 240,16,56
Rb 172,17,10240
Ma 208,16,60
s_waitcnt vmcnt(0)
v_cmp_eq_u32_e32 vcc, s3, v4
s_andn2_b64 s[28:29], s[60:61], vcc
s_cbranch_scc1 .Lzsp15
.Lzok15:
Ma 212,20,60
Rb 176,17,16384
Mv 244,20,56
Mv 212,20,52
Rb 180,17,18432
s_add_u32 m0, s36, 131072
Mv 180,20,48
buffer_load_dwordx4 v2, s[20:23], s56 offen offset:0 sc1 lds
Mv 148,20,44
Rb 184,17,24576
Mv 116,20,40
Mv 84,20,36
Rb 188,17,26624
Mv 52,20,32
Mv 56,24,32
Mv 88,24,36
buffer_load_dwordx4 v2, s[20:23], s56 offen offset:1024 sc1 lds
Mv 120,24,40
Mv 152,24,44
Mv 184,24,48
Mv 216,24,52
Mv 248,24,56
Ma 216,24,60
buffer_load_dwordx4 v2, s[20:23], s56 offen offset:2048 sc1 lds
Ma 220,28,60
Mv 252,28,56
Mv 220,28,52
Mv 188,28,48
Mv 156,28,44
Mv 124,28,40
buffer_load_dwordx4 v2, s[20:23], s56 offen offset:3072 sc1 lds
Mv 92,28,36
Mv 60,28,32
s_waitcnt lgkmcnt(0)
s_barrier
Mv 32,128,160
Rt 0,18,32768
Mv 64,128,164
Mv 96,128,168
Rt 2,18,34816
s_add_u32 m0, s36, 0
Mv 128,128,172
buffer_load_dwordx4 v2, s[20:23], s55 offen offset:0 sc1 lds
Mv 160,128,176
)" R"(
Rt 4,19,32768
Mv 192,128,180
Mv 224,128,184
Rt 6,19,34816
Ma 192,128,188
Ma 196,132,188
Rt 8,20,32768
buffer_load_dwordx4 v2, s[20:23], s55 offen offset:1024 sc1 lds
Mv 228,132,184
Mv 196,132,180
Rt 10,20,34816
Mv 164,132,176
Mv 132,132,172
Rt 12,21,32768
Mv 100,132,168
buffer_load_dwordx4 v2, s[20:23], s55 offen offset:2048 sc1 lds
Mv 68,132,164
Rt 14,21,34816
Mv 36,132,160
Mv 40,136,160
Rt 16,22,32768
Mv 72,136,164
Mv 104,136,168
Rt 18,22,34816
buffer_load_dwordx4 v2, s[20:23], s55 offen offset:3072 sc1 lds
Mv 136,136,172
Mv 168,136,176
Rt 20,23,32768
Mv 200,136,180
Mv 232,136,184
Rt 22,23,34816
s_add_u32 m0, s36, 4096
Ma 200,136,188
buffer_load_dwordx4 v3, s[20:23], s55 offen offset:0 sc1 lds
Ma 204,140,188
Rt 24,24,32768
Mv 236,140,184
Mv 204,140,180
Rt 26,24,34816
Mv 172,140,176
Mv 140,140,172
Rt 28,25,32768
buffer_load_dwordx4 v3, s[20:23], s55 offen offset:1024 sc1 lds
Mv 108,140,168
Mv 76,140,164
Rt 30,25,34816
Mv 44,140,160
Mv 48,144,160
Rb 32,16,32768
Mv 80,144,164
buffer_load_dwordx4 v3, s[20:23], s55 offen offset:2048 sc1 lds
Mv 112,144,168
Rb 36,16,34816
Mv 144,144,172
Mv 176,144,176
Rb 40,16,40960
Mv 208,144,180
Mv 240,144,184
Rb 44,16,43008
buffer_load_dwordx4 v3, s[20:23], s55 offen offset:3072 sc1 lds
Ma 208,144,188
Ma 212,148,188
Rb 48,16,49152
Mv 244,148,184
)" R"(
Mv 212,148,180
Rb 52,16,51200
s_add_u32 m0, s36, 135168
Mv 180,148,176
buffer_load_dwordx4 v3, s[20:23], s56 offen offset:0 sc1 lds
Mv 148,148,172
Rb 56,16,57344
Mv 116,148,168
Mv 84,148,164
Rb 60,16,59392
Mv 52,148,160
Mv 56,152,160
buffer_load_dwordx4 v3, s[20:23], s56 offen offset:1024 sc1 lds
Mv 88,152,164
Mv 120,152,168
Mv 152,152,172
Mv 184,152,176
Mv 216,152,180
buffer_load_dwordx4 v3, s[20:23], s56 offen offset:2048 sc1 lds
Mv 248,152,184
Ma 216,152,188
Ma 220,156,188
Mv 252,156,184
Mv 220,156,180
buffer_load_dwordx4 v3, s[20:23], s56 offen offset:3072 sc1 lds
Mv 188,156,176
Mv 156,156,172
Mv 124,156,168
s_add_u32 s55, s55, 0x10000
s_add_u32 s56, s56, 0x10000
Mv 92,156,164
Mv 60,156,160
s_waitcnt lgkmcnt(4)
s_barrier
Mv 32,0,32
Rt 128,18,49152
Mv 64,0,36
Rt 130,18,51200
Mv 96,0,40
Rt 132,19,49152
s_add_u32 m0, s36, 65536
Mv 128,0,44
Rt 134,19,51200
buffer_load_dwordx4 v2, s[20:23], s56 offen offset:0 sc1 lds
Mv 132,4,44
Rt 136,20,49152
Mv 100,4,40
Rt 138,20,51200
Mv 68,4,36
Rt 140,21,49152
Mv 36,4,32
Rt 142,21,51200
Mv 40,8,32
Rt 144,22,49152
Mv 72,8,36
Rt 146,22,51200
Mv 104,8,40
Rt 148,23,49152
Mv 136,8,44
Rt 150,23,51200
buffer_load_dwordx4 v2, s[20:23], s56 offen offset:1024 sc1 lds
Mv 140,12,44
Rt 152,24,49152
Mv 108,12,40
Rt 154,24,51200
)" R"(
Mv 76,12,36
Rt 156,25,49152
Mv 44,12,32
Rt 158,25,51200
Mv 48,16,32
Rb 160,17,32768
Mv 80,16,36
Rb 164,17,34816
Mv 112,16,40
Rb 168,17,40960
Mv 144,16,44
Rb 172,17,43008
buffer_load_dwordx4 v2, s[20:23], s56 offen offset:2048 sc1 lds
Mv 148,20,44
Rb 176,17,49152
Mv 116,20,40
Rb 180,17,51200
Mv 84,20,36
Rb 184,17,57344
Mv 52,20,32
Rb 188,17,59392
Mv 56,24,32
v_add_u32 v8, 0x10000, v1
v_add_u32 v10, 0x20000, v1
Mv 88,24,36
buffer_load_dword v4, v5, s[20:23], s50 offen offset:0 sc0 sc1
Mv 120,24,40
s_lshl_b64 s[60:61], s[60:61], 8
s_or_b32 s60, s60, 0xff
Mv 152,24,44
buffer_load_dwordx4 v2, s[20:23], s56 offen offset:3072 sc1 lds
Mv 156,28,44
s_mov_b32 s93, 2
s_mov_b32 s94, 0
Mv 124,28,40
Mv 92,28,36
Mv 60,28,32
s_waitcnt lgkmcnt(0)
s_barrier
Mv 32,128,160
Mv 36,132,160
Mv 40,136,160
s_add_u32 m0, s36, 32768
Mv 44,140,160
buffer_load_dwordx4 v2, s[20:23], s55 offen offset:0 sc1 lds
Mv 48,144,160
Mv 52,148,160
s_nop 0
Cv 32,32,33
Cv 33,34,35
Mv 56,152,160
Cv 34,36,37
Cv 35,38,39
Mv 60,156,160
Cv 40,40,41
Cv 41,42,43
Mv 64,128,164
Cv 42,44,45
Cv 43,46,47
Mv 68,132,164
Cv 48,48,49
Cv 49,50,51
Mv 72,136,164
Cv 50,52,53
Cv 51,54,55
)" R"(
Mv 76,140,164
buffer_load_dwordx4 v2, s[20:23], s55 offen offset:1024 sc1 lds
Mv 80,144,164
Cv 56,56,57
Cv 57,58,59
Mv 84,148,164
Cv 58,60,61
Cv 59,62,63
Mv 88,152,164
Cv 64,64,65
Cv 65,66,67
Mv 92,156,164
Cv 66,68,69
Cv 67,70,71
Mv 96,128,168
Cv 72,72,73
Cv 73,74,75
Mv 100,132,168
Cv 74,76,77
Cv 75,78,79
Mv 104,136,168
Cv 80,80,81
Cv 81,82,83
Mv 108,140,168
buffer_load_dwordx4 v2, s[20:23], s55 offen offset:2048 sc1 lds
Mv 112,144,168
Cv 82,84,85
Cv 83,86,87
Mv 116,148,168
Cv 88,88,89
Cv 89,90,91
Mv 120,152,168
Cv 90,92,93
Cv 91,94,95
Mv 124,156,168
buffer_store_dwordx4 v[32:35], v2, s[20:23], s45 offen offset:0 sc0 sc1
Mv 128,128,172
Mv 132,132,172
Mv 136,136,172
Mv 140,140,172
buffer_load_dwordx4 v2, s[20:23], s55 offen offset:3072 sc1 lds
Mv 144,144,172
Mv 148,148,172
s_nop 1
Cv 128,128,129
Cv 129,130,131
Mv 152,152,172
Cv 130,132,133
Cv 131,134,135
Mv 156,156,172
buffer_store_dwordx4 v[40:43], v2, s[20:23], s45 offen offset:1024 sc0 sc1
Mv 160,0,48
Cv 136,136,137
Cv 137,138,139
Mv 160,128,176
Cv 138,140,141
Cv 139,142,143
Mv 164,4,48
s_add_u32 m0, s36, 36864
Cv 144,144,145
Mv 164,132,176
buffer_load_dwordx4 v3, s[20:23], s55 offen offset:0 sc1 lds
Mv 168,8,48
Cv 145,146,147
)" R"(
Cv 146,148,149
Mv 168,136,176
Cv 147,150,151
Cv 152,152,153
Mv 172,12,48
Cv 153,154,155
Cv 154,156,157
Mv 172,140,176
buffer_store_dwordx4 v[48:51], v2, s[20:23], s45 offen offset:2048 sc0 sc1
Mv 176,16,48
Cv 155,158,159
Cv 132,96,97
Mv 176,144,176
Cv 133,98,99
Cv 134,100,101
Mv 180,20,48
Cv 135,102,103
Cv 140,104,105
Mv 180,148,176
buffer_load_dwordx4 v3, s[20:23], s55 offen offset:1024 sc1 lds
Mv 184,24,48
Cv 141,106,107
Cv 142,108,109
Mv 184,152,176
Cv 143,110,111
Cv 148,112,113
Mv 188,28,48
Cv 149,114,115
Cv 150,116,117
Mv 188,156,176
buffer_store_dwordx4 v[56:59], v2, s[20:23], s45 offen offset:3072 sc0 sc1
Mv 192,0,52
Cv 151,118,119
Cv 156,120,121
Mv 192,128,180
Cv 157,122,123
Cv 158,124,125
Mv 196,4,52
Cv 159,126,127
Cv 160,160,161
Mv 196,132,180
buffer_load_dwordx4 v3, s[20:23], s55 offen offset:2048 sc1 lds
Mv 200,8,52
Cv 161,162,163
Cv 162,164,165
Mv 200,136,180
Cv 163,166,167
v_accvgpr_write_b32 a224, v160
Mv 204,12,52
v_accvgpr_write_b32 a225, v161
v_accvgpr_write_b32 a226, v162
Mv 204,140,180
buffer_store_dwordx4 v[64:67], v3, s[20:23], s45 offen offset:0 sc0 sc1
Mv 208,16,52
v_accvgpr_write_b32 a227, v163
Cv 168,168,169
Mv 208,144,180
Cv 169,170,171
Cv 170,172,173
Mv 212,20,52
Cv 171,174,175
v_accvgpr_write_b32 a228, v168
Mv 212,148,180
buffer_load_dwordx4 v3, s[20:23], s55 offen offset:3072 sc1 lds
)" R"(
Mv 216,24,52
v_accvgpr_write_b32 a229, v169
v_accvgpr_write_b32 a230, v170
Mv 216,152,180
v_accvgpr_write_b32 a231, v171
Cv 176,176,177
Mv 220,28,52
Cv 177,178,179
Cv 178,180,181
Mv 220,156,180
buffer_store_dwordx4 v[72:75], v3, s[20:23], s45 offen offset:1024 sc0 sc1
Mv 224,0,56
Cv 179,182,183
v_accvgpr_write_b32 a232, v176
Ma 192,0,60
v_accvgpr_write_b32 a233, v177
v_accvgpr_write_b32 a234, v178
Ma 196,4,60
s_add_u32 m0, s36, 69632
v_accvgpr_write_b32 a235, v179
Mv 228,4,56
buffer_load_dwordx4 v3, s[20:23], s56 offen offset:0 sc1 lds
Mv 232,8,56
Cv 184,184,185
Cv 185,186,187
Ma 200,8,60
Cv 186,188,189
Cv 187,190,191
Ma 204,12,60
v_accvgpr_write_b32 a236, v184
v_accvgpr_write_b32 a237, v185
Mv 236,12,56
s_waitcnt vmcnt(16)
s_barrier
Mv 240,16,56
v_cmp_eq_u32_e32 vcc, s3, v4
s_andn2_b64 s[28:29], s[60:61], vcc
s_cbranch_scc1 .Lzsp16
.Lzok16:
Ma 208,16,60
buffer_store_dwordx4 v[80:83], v3, s[20:23], s45 offen offset:2048 sc0 sc1
Ma 212,20,60
v_accvgpr_write_b32 a238, v186
v_accvgpr_write_b32 a239, v187
Mv 244,20,56
buffer_load_dwordx4 v3, s[20:23], s56 offen offset:1024 sc1 lds
Mv 248,24,56
Cv 192,192,193
Cv 193,194,195
Ma 216,24,60
Cv 194,196,197
Cv 195,198,199
Ma 220,28,60
v_accvgpr_write_b32 a240, v192
v_accvgpr_write_b32 a241, v193
Mv 252,28,56
buffer_store_dwordx4 v[88:91], v3, s[20:23], s45 offen offset:3072 sc0 sc1
Mv 224,128,184
Rb 0,1,0
v_accvgpr_write_b32 a242, v194
Mv 228,132,184
Rb 4,1,1024
v_accvgpr_write_b32 a243, v195
Mv 232,136,184
Rb 8,1,2048
)" R"(
Cv 200,200,201
Mv 236,140,184
buffer_load_dwordx4 v3, s[20:23], s56 offen offset:2048 sc1 lds
Rb 12,1,3072
Mv 240,144,184
Rb 16,1,8192
Cv 201,202,203
Mv 244,148,184
Rb 20,1,9216
Cv 202,204,205
Mv 248,152,184
Rb 24,1,10240
Cv 203,206,207
Mv 252,156,184
Rb 28,1,11264
v_accvgpr_write_b32 a244, v200
Ma 192,128,188
Rb 32,1,4096
v_accvgpr_write_b32 a245, v201
Ma 196,132,188
Rb 36,1,5120
v_accvgpr_write_b32 a246, v202
Ma 200,136,188
Rb 40,1,6144
v_accvgpr_write_b32 a247, v203
Ma 204,140,188
buffer_load_dwordx4 v3, s[20:23], s56 offen offset:3072 sc1 lds
Rb 44,1,7168
Ma 208,144,188
Ma 212,148,188
Ma 216,152,188
Ma 220,156,188
s_add_u32 s56, s56, 0x10000
s_branch .LzFB
.LzPBVX:
Mv 32,32,0
Rt 128,18,16384
Mv 36,36,0
buffer_load_dword v4, v5, s[20:23], s50 offen offset:0 sc0 sc1
Mv 96,40,0
Rt 130,18,18432
Mv 100,44,0
Mv 160,48,0
Rt 132,19,16384
Mv 164,52,0
Mv 224,56,0
Rt 134,19,18432
Mv 228,60,0
Mv 236,60,4
Rt 136,20,16384
Mv 232,56,4
Mv 172,52,4
Rt 138,20,18432
Mv 168,48,4
Mv 108,44,4
Rt 140,21,16384
Mv 104,40,4
Mv 44,36,4
Rt 142,21,18432
Mv 40,32,4
Mv 48,32,8
Rt 144,22,16384
Mv 52,36,8
Mv 112,40,8
)" R"(
Rt 146,22,18432
Mv 116,44,8
Mv 176,48,8
Rt 148,23,16384
Mv 180,52,8
Mv 240,56,8
Rt 150,23,18432
Mv 244,60,8
Mv 252,60,12
Rt 152,24,16384
Mv 248,56,12
Mv 188,52,12
Rt 154,24,18432
Mv 184,48,12
Mv 124,44,12
Rt 156,25,16384
Mv 120,40,12
Mv 60,36,12
Rt 158,25,18432
Mv 56,32,12
Mv 64,32,16
Rb 160,17,0
Mv 68,36,16
Mv 128,40,16
Rb 164,17,2048
Mv 132,44,16
Mv 192,48,16
Rb 168,17,8192
Mv 196,52,16
Ma 192,56,16
Rb 172,17,10240
Ma 196,60,16
s_waitcnt vmcnt(0)
v_cmp_eq_u32_e32 vcc, s3, v4
s_andn2_b64 s[28:29], s[60:61], vcc
s_cbranch_scc1 .Lzsp17
.Lzok17:
Ma 204,60,20
Rb 176,17,16384
Ma 200,56,20
Mv 204,52,20
Rb 180,17,18432
s_add_u32 m0, s36, 131072
Mv 200,48,20
buffer_load_dwordx4 v2, s[20:23], s56 offen offset:0 sc1 lds
Mv 140,44,20
Rb 184,17,24576
Mv 136,40,20
Mv 76,36,20
Rb 188,17,26624
Mv 72,32,20
Mv 80,32,24
Mv 84,36,24
buffer_load_dwordx4 v2, s[20:23], s56 offen offset:1024 sc1 lds
Mv 144,40,24
Mv 148,44,24
Mv 208,48,24
Mv 212,52,24
Ma 208,56,24
Ma 212,60,24
buffer_load_dwordx4 v2, s[20:23], s56 offen offset:2048 sc1 lds
Ma 220,60,28
Ma 216,56,28
Mv 220,52,28
)" R"(
Mv 216,48,28
Mv 156,44,28
Mv 152,40,28
buffer_load_dwordx4 v2, s[20:23], s56 offen offset:3072 sc1 lds
Mv 92,36,28
Mv 88,32,28
s_waitcnt lgkmcnt(0)
s_barrier
Mv 32,160,128
Rt 0,18,32768
Mv 36,164,128
Mv 96,168,128
Rt 2,18,34816
s_add_u32 m0, s36, 0
Mv 100,172,128
buffer_load_dwordx4 v2, s[20:23], s55 offen offset:0 sc1 lds
Mv 160,176,128
Rt 4,19,32768
Mv 164,180,128
Mv 224,184,128
Rt 6,19,34816
Mv 228,188,128
Mv 236,188,132
Rt 8,20,32768
buffer_load_dwordx4 v2, s[20:23], s55 offen offset:1024 sc1 lds
Mv 232,184,132
Mv 172,180,132
Rt 10,20,34816
Mv 168,176,132
Mv 108,172,132
Rt 12,21,32768
Mv 104,168,132
buffer_load_dwordx4 v2, s[20:23], s55 offen offset:2048 sc1 lds
Mv 44,164,132
Rt 14,21,34816
Mv 40,160,132
Mv 48,160,136
Rt 16,22,32768
Mv 52,164,136
Mv 112,168,136
Rt 18,22,34816
buffer_load_dwordx4 v2, s[20:23], s55 offen offset:3072 sc1 lds
Mv 116,172,136
Mv 176,176,136
Rt 20,23,32768
Mv 180,180,136
Mv 240,184,136
Rt 22,23,34816
s_add_u32 m0, s36, 4096
Mv 244,188,136
buffer_load_dwordx4 v3, s[20:23], s55 offen offset:0 sc1 lds
Mv 252,188,140
Rt 24,24,32768
Mv 248,184,140
Mv 188,180,140
Rt 26,24,34816
Mv 184,176,140
Mv 124,172,140
Rt 28,25,32768
buffer_load_dwordx4 v3, s[20:23], s55 offen offset:1024 sc1 lds
Mv 120,168,140
Mv 60,164,140
Rt 30,25,34816
Mv 56,160,140
Mv 64,160,144
)" R"(
Rb 32,16,32768
Mv 68,164,144
buffer_load_dwordx4 v3, s[20:23], s55 offen offset:2048 sc1 lds
Mv 128,168,144
Rb 36,16,34816
Mv 132,172,144
Mv 192,176,144
Rb 40,16,40960
Mv 196,180,144
Ma 192,184,144
Rb 44,16,43008
buffer_load_dwordx4 v3, s[20:23], s55 offen offset:3072 sc1 lds
Ma 196,188,144
Ma 204,188,148
Rb 48,16,49152
Ma 200,184,148
Mv 204,180,148
Rb 52,16,51200
s_add_u32 m0, s36, 135168
Mv 200,176,148
buffer_load_dwordx4 v3, s[20:23], s56 offen offset:0 sc1 lds
Mv 140,172,148
Rb 56,16,57344
Mv 136,168,148
Mv 76,164,148
Rb 60,16,59392
Mv 72,160,148
Mv 80,160,152
buffer_load_dwordx4 v3, s[20:23], s56 offen offset:1024 sc1 lds
Mv 84,164,152
Mv 144,168,152
Mv 148,172,152
Mv 208,176,152
Mv 212,180,152
buffer_load_dwordx4 v3, s[20:23], s56 offen offset:2048 sc1 lds
Ma 208,184,152
Ma 212,188,152
Ma 220,188,156
Ma 216,184,156
Mv 220,180,156
buffer_load_dwordx4 v3, s[20:23], s56 offen offset:3072 sc1 lds
Mv 216,176,156
Mv 156,172,156
Mv 152,168,156
s_add_u32 s55, s55, 0x10000
s_add_u32 s56, s56, 0x10000
Mv 92,164,156
Mv 88,160,156
s_waitcnt lgkmcnt(4)
s_barrier
Mv 32,32,0
Rt 128,18,49152
Mv 36,36,0
Rt 130,18,51200
Mv 96,40,0
Rt 132,19,49152
s_add_u32 m0, s36, 65536
Mv 100,44,0
Rt 134,19,51200
buffer_load_dwordx4 v2, s[20:23], s56 offen offset:0 sc1 lds
Mv 108,44,4
Rt 136,20,49152
Mv 104,40,4
Rt 138,20,51200
Mv 44,36,4
)" R"(
Rt 140,21,49152
Mv 40,32,4
Rt 142,21,51200
Mv 48,32,8
Rt 144,22,49152
Mv 52,36,8
Rt 146,22,51200
Mv 112,40,8
Rt 148,23,49152
Mv 116,44,8
Rt 150,23,51200
buffer_load_dwordx4 v2, s[20:23], s56 offen offset:1024 sc1 lds
Mv 124,44,12
Rt 152,24,49152
Mv 120,40,12
Rt 154,24,51200
Mv 60,36,12
Rt 156,25,49152
Mv 56,32,12
Rt 158,25,51200
Mv 64,32,16
Rb 160,17,32768
Mv 68,36,16
Rb 164,17,34816
Mv 128,40,16
Rb 168,17,40960
Mv 132,44,16
Rb 172,17,43008
buffer_load_dwordx4 v2, s[20:23], s56 offen offset:2048 sc1 lds
Mv 140,44,20
Rb 176,17,49152
Mv 136,40,20
Rb 180,17,51200
Mv 76,36,20
Rb 184,17,57344
Mv 72,32,20
Rb 188,17,59392
Mv 80,32,24
v_add_u32 v8, 0x10000, v1
v_add_u32 v10, 0x20000, v1
Mv 84,36,24
buffer_load_dword v4, v5, s[20:23], s50 offen offset:0 sc0 sc1
Mv 144,40,24
s_lshl_b64 s[60:61], s[60:61], 8
s_or_b32 s60, s60, 0xff
Mv 148,44,24
buffer_load_dwordx4 v2, s[20:23], s56 offen offset:3072 sc1 lds
Mv 156,44,28
s_mov_b32 s93, 2
s_mov_b32 s94, 0
Mv 152,40,28
Mv 92,36,28
Mv 88,32,28
s_waitcnt lgkmcnt(0)
s_barrier
Mv 32,160,128
Mv 36,164,128
Mv 40,160,132
s_add_u32 m0, s36, 32768
Mv 44,164,132
buffer_load_dwordx4 v2, s[20:23], s55 offen offset:0 sc1 lds
Mv 48,160,136
Mv 52,164,136
s_nop 0
Cv 32,32,33
)" R"(
Cv 33,34,35
Mv 56,160,140
Cv 34,36,37
Cv 35,38,39
Mv 60,164,140
Cv 40,40,41
Cv 41,42,43
Mv 64,160,144
Cv 42,44,45
Cv 43,46,47
Mv 68,164,144
Cv 48,48,49
Cv 49,50,51
Mv 72,160,148
Cv 50,52,53
Cv 51,54,55
Mv 76,164,148
buffer_load_dwordx4 v2, s[20:23], s55 offen offset:1024 sc1 lds
Mv 80,160,152
Cv 56,56,57
Cv 57,58,59
Mv 84,164,152
Cv 58,60,61
Cv 59,62,63
Mv 88,160,156
Cv 64,64,65
Cv 65,66,67
Mv 92,164,156
Cv 66,68,69
Cv 67,70,71
Mv 96,168,128
Cv 72,72,73
Cv 73,74,75
Mv 100,172,128
Cv 74,76,77
Cv 75,78,79
Mv 104,168,132
Cv 80,80,81
Cv 81,82,83
Mv 108,172,132
buffer_load_dwordx4 v2, s[20:23], s55 offen offset:2048 sc1 lds
Mv 112,168,136
Cv 82,84,85
Cv 83,86,87
Mv 116,172,136
Cv 88,88,89
Cv 89,90,91
Mv 120,168,140
Cv 90,92,93
Cv 91,94,95
Mv 124,172,140
buffer_store_dwordx4 v[32:35], v2, s[20:23], s45 offen offset:0 sc0 sc1
Mv 128,168,144
Mv 132,172,144
Mv 136,168,148
Mv 140,172,148
buffer_load_dwordx4 v2, s[20:23], s55 offen offset:3072 sc1 lds
Mv 144,168,152
Mv 148,172,152
s_nop 1
Cv 128,128,129
Cv 129,130,131
Mv 152,168,156
Cv 130,132,133
)" R"(
Cv 131,134,135
Mv 156,172,156
buffer_store_dwordx4 v[40:43], v2, s[20:23], s45 offen offset:1024 sc0 sc1
Mv 160,48,0
Cv 136,136,137
Cv 137,138,139
Mv 160,176,128
Cv 138,140,141
Cv 139,142,143
Mv 164,52,0
s_add_u32 m0, s36, 36864
Cv 144,144,145
Mv 164,180,128
buffer_load_dwordx4 v3, s[20:23], s55 offen offset:0 sc1 lds
Mv 168,48,4
Cv 145,146,147
Cv 146,148,149
Mv 168,176,132
Cv 147,150,151
Cv 152,152,153
Mv 172,52,4
Cv 153,154,155
Cv 154,156,157
Mv 172,180,132
buffer_store_dwordx4 v[48:51], v2, s[20:23], s45 offen offset:2048 sc0 sc1
Mv 176,48,8
Cv 155,158,159
Cv 132,96,97
Mv 176,176,136
Cv 133,98,99
Cv 134,100,101
Mv 180,52,8
Cv 135,102,103
Cv 140,104,105
Mv 180,180,136
buffer_load_dwordx4 v3, s[20:23], s55 offen offset:1024 sc1 lds
Mv 184,48,12
Cv 141,106,107
Cv 142,108,109
Mv 184,176,140
Cv 143,110,111
Cv 148,112,113
Mv 188,52,12
Cv 149,114,115
Cv 150,116,117
Mv 188,180,140
buffer_store_dwordx4 v[56:59], v2, s[20:23], s45 offen offset:3072 sc0 sc1
Mv 192,48,16
Cv 151,118,119
Cv 156,120,121
Mv 192,176,144
Cv 157,122,123
Cv 158,124,125
Mv 196,52,16
Cv 159,126,127
Cv 160,160,161
Mv 196,180,144
buffer_load_dwordx4 v3, s[20:23], s55 offen offset:2048 sc1 lds
Mv 200,48,20
Cv 161,162,163
Cv 162,164,165
Mv 200,176,148
Cv 163,166,167
v_accvgpr_write_b32 a224, v160
)" R"(
Mv 204,52,20
v_accvgpr_write_b32 a225, v161
v_accvgpr_write_b32 a226, v162
Mv 204,180,148
buffer_store_dwordx4 v[64:67], v3, s[20:23], s45 offen offset:0 sc0 sc1
Mv 208,48,24
v_accvgpr_write_b32 a227, v163
Cv 168,168,169
Mv 208,176,152
Cv 169,170,171
Cv 170,172,173
Mv 212,52,24
Cv 171,174,175
v_accvgpr_write_b32 a228, v168
Mv 212,180,152
buffer_load_dwordx4 v3, s[20:23], s55 offen offset:3072 sc1 lds
Mv 216,48,28
v_accvgpr_write_b32 a229, v169
v_accvgpr_write_b32 a230, v170
Mv 216,176,156
v_accvgpr_write_b32 a231, v171
Cv 176,176,177
Mv 220,52,28
Cv 177,178,179
Cv 178,180,181
Mv 220,180,156
buffer_store_dwordx4 v[72:75], v3, s[20:23], s45 offen offset:1024 sc0 sc1
Mv 224,56,0
Cv 179,182,183
v_accvgpr_write_b32 a232, v176
Mv 228,60,0
v_accvgpr_write_b32 a233, v177
v_accvgpr_write_b32 a234, v178
Mv 236,60,4
s_add_u32 m0, s36, 69632
v_accvgpr_write_b32 a235, v179
Mv 232,56,4
buffer_load_dwordx4 v3, s[20:23], s56 offen offset:0 sc1 lds
Mv 240,56,8
Cv 184,184,185
Cv 185,186,187
Mv 244,60,8
Cv 186,188,189
Cv 187,190,191
Mv 252,60,12
v_accvgpr_write_b32 a236, v184
v_accvgpr_write_b32 a237, v185
Mv 248,56,12
s_waitcnt vmcnt(16)
s_barrier
Ma 192,56,16
v_cmp_eq_u32_e32 vcc, s3, v4
s_andn2_b64 s[28:29], s[60:61], vcc
s_cbranch_scc1 .Lzsp18
.Lzok18:
Ma 196,60,16
buffer_store_dwordx4 v[80:83], v3, s[20:23], s45 offen offset:2048 sc0 sc1
Ma 204,60,20
v_accvgpr_write_b32 a238, v186
v_accvgpr_write_b32 a239, v187
Ma 200,56,20
buffer_load_dwordx4 v3, s[20:23], s56 offen offset:1024 sc1 lds
Ma 208,56,24
Cv 192,192,193
Cv 193,194,195
)" R"(
Ma 212,60,24
Cv 194,196,197
Cv 195,198,199
Ma 220,60,28
v_accvgpr_write_b32 a240, v192
v_accvgpr_write_b32 a241, v193
Ma 216,56,28
buffer_store_dwordx4 v[88:91], v3, s[20:23], s45 offen offset:3072 sc0 sc1
Mv 224,184,128
Rb 0,1,0
v_accvgpr_write_b32 a242, v194
Mv 228,188,128
Rb 4,1,1024
v_accvgpr_write_b32 a243, v195
Mv 232,184,132
Rb 8,1,2048
Cv 200,200,201
Mv 236,188,132
buffer_load_dwordx4 v3, s[20:23], s56 offen offset:2048 sc1 lds
Rb 12,1,3072
Mv 240,184,136
Rb 16,1,8192
Cv 201,202,203
Mv 244,188,136
Rb 20,1,9216
Cv 202,204,205
Mv 248,184,140
Rb 24,1,10240
Cv 203,206,207
Mv 252,188,140
Rb 28,1,11264
v_accvgpr_write_b32 a244, v200
Ma 192,184,144
Rb 32,1,4096
v_accvgpr_write_b32 a245, v201
Ma 196,188,144
Rb 36,1,5120
v_accvgpr_write_b32 a246, v202
Ma 200,184,148
Rb 40,1,6144
v_accvgpr_write_b32 a247, v203
Ma 204,188,148
buffer_load_dwordx4 v3, s[20:23], s56 offen offset:3072 sc1 lds
Rb 44,1,7168
Ma 208,184,152
Ma 212,188,152
Ma 216,184,156
Ma 220,188,156
s_add_u32 s56, s56, 0x10000
.LzFB:
s_waitcnt lgkmcnt(8)
Mz 64,0,64
Rb 48,1,12288
s_add_u32 s55, s55, 0x10000
Mz 68,0,68
s_cmp_eq_u32 s57, 2
s_cselect_b32 s55, s59, s55
Mv 68,4,76
Rb 52,1,13312
s_sub_u32 s57, s57, 1
v_mov_b32 v16, 0
Mv 64,4,72
buffer_store_dwordx4 v[132:135], v2, s[20:23], s46 offen offset:0 sc0 sc1
Mv 64,8,80
)" R"(
Rb 56,1,14336
Cv 208,208,209
Cv 209,210,211
Mv 68,8,84
Cv 210,212,213
Cv 211,214,215
Mv 68,12,92
Rb 60,1,15360
v_accvgpr_write_b32 a248, v208
s_waitcnt lgkmcnt(8)
Mv 64,12,88
v_accvgpr_write_b32 a249, v209
v_accvgpr_write_b32 a250, v210
Mv 64,16,96
Rb 0,1,16384
v_accvgpr_write_b32 a251, v211
Mv 68,16,100
Cv 216,216,217
Cv 217,218,219
Cv 218,220,221
Mv 68,20,108
Rb 4,1,17408
Cv 219,222,223
Mv 64,20,104
v_accvgpr_write_b32 a252, v216
v_accvgpr_write_b32 a253, v217
v_accvgpr_write_b32 a254, v218
Mv 64,24,112
Rb 8,1,18432
v_accvgpr_write_b32 a255, v219
Mv 68,24,116
buffer_store_dwordx4 v[140:143], v2, s[20:23], s46 offen offset:1024 sc0 sc1
Cv 224,224,225
Mv 68,28,124
Rb 12,1,19456
s_waitcnt lgkmcnt(8)
Mv 64,28,120
Cv 225,226,227
Cv 226,228,229
Cv 227,230,231
Mz 72,32,64
Rb 16,1,24576
v_accvgpr_write_b32 a128, v224
Mz 76,32,68
v_accvgpr_write_b32 a129, v225
v_accvgpr_write_b32 a130, v226
Mv 76,36,76
Rb 20,1,25600
v_accvgpr_write_b32 a131, v227
Cv 232,232,233
Mv 72,36,72
Cv 233,234,235
Cv 234,236,237
Mv 72,40,80
Rb 24,1,26624
Cv 235,238,239
v_accvgpr_write_b32 a132, v232
Mv 76,40,84
v_accvgpr_write_b32 a133, v233
v_accvgpr_write_b32 a134, v234
Mv 76,44,92
Rb 28,1,27648
v_accvgpr_write_b32 a135, v235
s_waitcnt lgkmcnt(8)
)" R"(
Mv 72,44,88
buffer_store_dwordx4 v[148:151], v2, s[20:23], s46 offen offset:2048 sc0 sc1
Mv 72,48,96
Rb 32,1,20480
Cv 240,240,241
Mv 76,48,100
Cv 241,242,243
Cv 242,244,245
Cv 243,246,247
Mv 76,52,108
Rb 36,1,21504
v_accvgpr_write_b32 a136, v240
Mv 72,52,104
v_accvgpr_write_b32 a137, v241
v_accvgpr_write_b32 a138, v242
v_accvgpr_write_b32 a139, v243
Mv 72,56,112
Rb 40,1,22528
Cv 248,248,249
Mv 76,56,116
Cv 249,250,251
Cv 250,252,253
Cv 251,254,255
Mv 76,60,124
Rb 44,1,23552
s_waitcnt lgkmcnt(8)
Mv 72,60,120
v_accvgpr_write_b32 a140, v248
v_accvgpr_write_b32 a141, v249
v_accvgpr_write_b32 a142, v250
Mz 80,0,64
Rb 48,1,28672
v_accvgpr_write_b32 a143, v251
Mz 84,0,68
buffer_store_dwordx4 v[156:159], v2, s[20:23], s46 offen offset:3072 sc0 sc1
Mv 84,4,76
Rb 52,1,29696
v_mov_b32 v17, 0
v_mov_b32 v18, 0
Mv 80,4,72
v_mov_b32 v19, 0
v_mov_b32 v20, 0
Mv 80,8,80
Rb 56,1,30720
v_exp_f32_e32 v32, v64
Mv 84,8,84
v_exp_f32_e32 v24, v65
Mv 84,12,92
Rb 60,1,31744
v_mov_b32 v21, 0
s_waitcnt lgkmcnt(8)
Mv 80,12,88
v_exp_f32_e32 v33, v66
Mv 80,16,96
v_exp_f32_e32 v25, v67
Mv 84,16,100
v_exp_f32_e32 v34, v72
v_mov_b32 v22, 0
Mv 84,20,108
v_exp_f32_e32 v26, v73
Mv 80,20,104
buffer_store_dwordx4 v[128:131], v3, s[20:23], s46 offen offset:0 sc0 sc1
v_mov_b32 v23, 0
Mv 80,24,112
)" R"(
v_exp_f32_e32 v35, v74
Mv 84,24,116
v_exp_f32_e32 v27, v75
v_add_f32_e32 v14, v32, v24
Mv 84,28,124
v_exp_f32_e32 v36, v68
Mv 80,28,120
s_waitcnt vmcnt(7) lgkmcnt(0)
s_barrier
buffer_store_dword v6, v7, s[20:23], s49 offen offset:0 sc0 sc1
Mz 88,32,64
Rb 0,1,32768
Cv 32,32,24
Mz 92,32,68
Rb 4,1,33792
v_add_f32_e32 v14, v14, v33
Mv 92,36,76
Rb 8,1,34816
s_add_u32 m0, s36, 0
v_add_f32_e32 v14, v14, v25
Mv 88,36,72
Rb 12,1,35840
buffer_load_dwordx4 v2, s[20:23], s55 offen offset:0 sc1 lds
Mv 88,40,80
Rb 16,1,40960
v_exp_f32_e32 v28, v69
Mv 92,40,84
Rb 20,1,41984
Cv 33,33,25
Mv 92,44,92
Rb 24,1,43008
v_exp_f32_e32 v37, v70
Mv 88,44,88
Rb 28,1,44032
v_add_f32_e32 v14, v14, v34
Mv 88,48,96
Rb 32,1,36864
v_add_f32_e32 v14, v14, v26
Mv 92,48,100
Rb 36,1,37888
v_exp_f32_e32 v29, v71
Mv 92,52,108
Rb 40,1,38912
Cv 34,34,26
Mv 88,52,104
Rb 44,1,39936
buffer_load_dwordx4 v2, s[20:23], s55 offen offset:1024 sc1 lds
Mv 88,56,112
v_exp_f32_e32 v38, v76
Mv 92,56,116
v_exp_f32_e32 v24, v77
v_add_f32_e32 v14, v14, v35
Mv 92,60,124
v_add_f32_e32 v14, v14, v27
s_waitcnt lgkmcnt(8)
Mv 88,60,120
v_exp_f32_e32 v39, v78
Cv 35,35,27
Mz 96,0,64
Rb 48,1,45056
buffer_load_dword v4, v5, s[20:23], s50 offen offset:0 sc0 sc1
Mz 100,0,68
s_lshl_b64 s[60:61], s[60:61], 8
s_or_b32 s60, s60, 0xff
Mv 100,4,76
)" R"(
Rb 52,1,46080
v_exp_f32_e32 v25, v79
Mv 96,4,72
buffer_load_dwordx4 v2, s[20:23], s55 offen offset:2048 sc1 lds
Mv 96,8,80
Rb 56,1,47104
v_exp_f32_e32 v40, v80
Mv 100,8,84
v_exp_f32_e32 v26, v81
Mv 100,12,92
Rb 60,1,48128
v_add_f32_e32 v15, v36, v28
s_waitcnt lgkmcnt(8)
Mv 96,12,88
buffer_store_dwordx4 v[136:139], v3, s[20:23], s46 offen offset:1024 sc0 sc1
Mv 96,16,96
Rb 0,1,49152
Cv 36,36,28
Mv 100,16,100
v_exp_f32_e32 v41, v82
v_add_f32_e32 v15, v15, v37
Mv 100,20,108
Rb 4,1,50176
v_add_f32_e32 v15, v15, v29
Mv 96,20,104
buffer_load_dwordx4 v2, s[20:23], s55 offen offset:3072 sc1 lds
Cv 37,37,29
Mv 96,24,112
Rb 8,1,51200
v_add_f32_e32 v15, v15, v38
Mv 100,24,116
v_exp_f32_e32 v27, v83
v_add_f32_e32 v15, v15, v24
Mv 100,28,124
Rb 12,1,52224
s_waitcnt lgkmcnt(8)
Mv 96,28,120
v_exp_f32_e32 v42, v88
Cv 38,38,24
Mz 104,32,64
Rb 16,1,57344
v_add_f32_e32 v15, v15, v39
Mz 108,32,68
v_exp_f32_e32 v28, v89
Mv 108,36,76
Rb 20,1,58368
s_add_u32 m0, s36, 4096
v_add_f32_e32 v15, v15, v25
Mv 104,36,72
buffer_load_dwordx4 v3, s[20:23], s55 offen offset:0 sc1 lds
Mv 104,40,80
Rb 24,1,59392
v_exp_f32_e32 v43, v90
Mv 108,40,84
v_exp_f32_e32 v29, v91
Mv 108,44,92
Rb 28,1,60416
Cv 39,39,25
s_waitcnt lgkmcnt(8)
Mv 104,44,88
buffer_store_dwordx4 v[144:147], v3, s[20:23], s46 offen offset:2048 sc0 sc1
Mv 104,48,96
Rb 32,1,53248
v_add_f32_e32 v14, v14, v40
)" R"(
Mv 108,48,100
v_exp_f32_e32 v44, v84
v_add_f32_e32 v14, v14, v26
Mv 108,52,108
Rb 36,1,54272
Cv 40,40,26
Mv 104,52,104
buffer_load_dwordx4 v3, s[20:23], s55 offen offset:1024 sc1 lds
v_add_f32_e32 v14, v14, v41
Mv 104,56,112
Rb 40,1,55296
v_add_f32_e32 v14, v14, v27
Mv 108,56,116
v_exp_f32_e32 v24, v85
Cv 41,41,27
Mv 108,60,124
Rb 44,1,56320
s_waitcnt lgkmcnt(8)
Mv 104,60,120
v_exp_f32_e32 v45, v86
v_add_f32_e32 v14, v14, v42
Mz 112,0,64
Rb 48,1,61440
v_add_f32_e32 v14, v14, v28
Mz 116,0,68
v_exp_f32_e32 v25, v87
Mv 116,4,76
Rb 52,1,62464
v_exp_f32_e32 v46, v92
Mv 112,4,72
buffer_load_dwordx4 v3, s[20:23], s55 offen offset:2048 sc1 lds
Mv 112,8,80
Rb 56,1,63488
v_exp_f32_e32 v26, v93
Mv 116,8,84
v_exp_f32_e32 v47, v94
Mv 116,12,92
Rb 60,1,64512
Cv 42,42,28
s_waitcnt lgkmcnt(8)
Mv 112,12,88
buffer_store_dwordx4 v[152:155], v3, s[20:23], s46 offen offset:3072 sc0 sc1
Mv 112,16,96
v_exp_f32_e32 v27, v95
Mv 116,16,100
v_add_f32_e32 v14, v14, v43
v_add_f32_e32 v14, v14, v29
Cv 43,43,29
Mv 116,20,108
v_add_f32_e32 v15, v15, v44
v_add_f32_e32 v15, v15, v24
Mv 112,20,104
buffer_load_dwordx4 v3, s[20:23], s55 offen offset:3072 sc1 lds
Cv 44,44,24
Mv 112,24,112
v_add_f32_e32 v15, v15, v45
v_add_f32_e32 v15, v15, v25
Mv 116,24,116
Cv 45,45,25
v_add_f32_e32 v15, v15, v46
v_add_f32_e32 v15, v15, v26
Mv 116,28,124
Cv 46,46,26
v_add_f32_e32 v15, v15, v47
)" R"(
Mv 112,28,120
v_add_f32_e32 v15, v15, v27
s_waitcnt lgkmcnt(0)
s_barrier
Mz 120,32,64
Rb 0,10,0
Cv 47,47,27
Mz 124,32,68
Rb 4,10,1024
v_max_f32_e32 v11, v14, v15
Mv 124,36,76
Rb 8,10,2048
v_mov_b32 v24, v14
v_mov_b32 v25, v15
Mv 120,36,72
Rb 12,10,3072
v_permlane32_swap_b32_e32 v24, v25
Mv 120,40,80
Rb 16,10,4096
v_add_f32_e32 v24, v24, v25
v_mov_b32 v25, v24
Mv 124,40,84
Rb 20,10,5120
v_permlane16_swap_b32_e32 v24, v25
Mv 124,44,92
Rb 24,10,6144
v_add_f32_e32 v24, v24, v25
s_add_u32 s55, s55, 0x10000
Mv 120,44,88
Rb 28,10,7168
Mv 120,48,96
Rb 32,10,8192
Mv 124,48,100
Rb 36,10,9216
v_rcp_f32_e32 v24, v24
Mv 124,52,108
Rb 40,10,10240
v_max_f32_e32 v11, v11, v24
Mv 120,52,104
Rb 44,10,11264
s_cmp_eq_u32 s57, 2
s_cselect_b32 s55, s59, s55
Mv 120,56,112
v_cmp_nge_f32_e32 vcc, s62, v11
Mv 124,56,116
Mv 124,60,124
s_waitcnt lgkmcnt(8)
s_cbranch_vccnz .Lzfx6
.Lzfr6:
Mv 120,60,120
v_mov_b32 v12, v14
v_mov_b32 v13, v15
v_mfma_f32_16x16x32_bf16 v[128:131], a[0:3], v[32:35], 0
Rb 48,10,12288
v_mfma_f32_16x16x32_bf16 v[136:139], a[0:3], v[36:39], 0
v_mfma_f32_16x16x32_bf16 v[140:143], a[4:7], v[36:39], 0
Rb 52,10,13312
s_add_u32 m0, s36, 32768
v_mfma_f32_16x16x32_bf16 v[132:135], a[4:7], v[32:35], 0
buffer_load_dwordx4 v2, s[20:23], s55 offen offset:0 sc1 lds
v_mfma_f32_16x16x32_bf16 v[144:147], a[8:11], v[32:35], 0
Rb 56,10,14336
v_mfma_f32_16x16x32_bf16 v[152:155], a[8:11], v[36:39], 0
v_exp_f32_e32 v48, v96
v_mfma_f32_16x16x32_bf16 v[156:159], a[12:15], v[36:39], 0
)" R"(
Rb 60,10,15360
s_waitcnt lgkmcnt(8)
v_mfma_f32_16x16x32_bf16 v[148:151], a[12:15], v[32:35], 0
v_exp_f32_e32 v28, v97
v_mfma_f32_16x16x32_bf16 v[160:163], a[16:19], v[32:35], 0
Rb 0,10,16384
v_add_f32_e32 v14, v48, v28
v_mfma_f32_16x16x32_bf16 v[168:171], a[16:19], v[36:39], 0
v_exp_f32_e32 v49, v98
v_mfma_f32_16x16x32_bf16 v[172:175], a[20:23], v[36:39], 0
Rb 4,10,17408
Cv 48,48,28
v_mfma_f32_16x16x32_bf16 v[164:167], a[20:23], v[32:35], 0
buffer_load_dwordx4 v2, s[20:23], s55 offen offset:1024 sc1 lds
v_mfma_f32_16x16x32_bf16 v[176:179], a[24:27], v[32:35], 0
Rb 8,10,18432
v_exp_f32_e32 v29, v99
v_mfma_f32_16x16x32_bf16 v[184:187], a[24:27], v[36:39], 0
v_exp_f32_e32 v50, v104
v_mfma_f32_16x16x32_bf16 v[188:191], a[28:31], v[36:39], 0
Rb 12,10,19456
s_waitcnt lgkmcnt(8)
v_mfma_f32_16x16x32_bf16 v[180:183], a[28:31], v[32:35], 0
v_exp_f32_e32 v24, v105
v_mfma_f32_16x16x32_bf16 v[192:195], a[32:35], v[32:35], 0
Rb 16,10,20480
v_add_f32_e32 v14, v14, v49
v_mfma_f32_16x16x32_bf16 v[200:203], a[32:35], v[36:39], 0
v_exp_f32_e32 v51, v106
v_add_f32_e32 v14, v14, v29
v_mfma_f32_16x16x32_bf16 v[204:207], a[36:39], v[36:39], 0
Rb 20,10,21504
Cv 49,49,29
v_mfma_f32_16x16x32_bf16 v[196:199], a[36:39], v[32:35], 0
buffer_load_dwordx4 v2, s[20:23], s55 offen offset:2048 sc1 lds
v_mfma_f32_16x16x32_bf16 v[208:211], a[40:43], v[32:35], 0
Rb 24,10,22528
v_add_f32_e32 v14, v14, v50
v_mfma_f32_16x16x32_bf16 v[216:219], a[40:43], v[36:39], 0
v_exp_f32_e32 v25, v107
v_mfma_f32_16x16x32_bf16 v[220:223], a[44:47], v[36:39], 0
Rb 28,10,23552
s_waitcnt lgkmcnt(8)
v_mfma_f32_16x16x32_bf16 v[212:215], a[44:47], v[32:35], 0
v_exp_f32_e32 v52, v100
v_add_f32_e32 v14, v14, v24
v_mfma_f32_16x16x32_bf16 v[224:227], a[48:51], v[32:35], 0
Rb 32,10,24576
Cv 50,50,24
v_mfma_f32_16x16x32_bf16 v[232:235], a[48:51], v[36:39], 0
v_exp_f32_e32 v26, v101
v_mfma_f32_16x16x32_bf16 v[236:239], a[52:55], v[36:39], 0
Rb 36,10,25600
v_add_f32_e32 v14, v14, v51
v_mfma_f32_16x16x32_bf16 v[228:231], a[52:55], v[32:35], 0
buffer_load_dwordx4 v2, s[20:23], s55 offen offset:3072 sc1 lds
v_mfma_f32_16x16x32_bf16 v[240:243], a[56:59], v[32:35], 0
Rb 40,10,26624
v_exp_f32_e32 v53, v102
v_mfma_f32_16x16x32_bf16 v[248:251], a[56:59], v[36:39], 0
v_exp_f32_e32 v27, v103
v_mfma_f32_16x16x32_bf16 v[252:255], a[60:63], v[36:39], 0
Rb 44,10,27648
s_waitcnt lgkmcnt(8)
)" R"(
v_mfma_f32_16x16x32_bf16 v[244:247], a[60:63], v[32:35], 0
v_exp_f32_e32 v54, v108
Mp 128,0,40
Rb 48,10,28672
v_add_f32_e32 v14, v14, v25
Mp 136,0,44
v_exp_f32_e32 v28, v109
Cv 51,51,25
Mp 140,4,44
Rb 52,10,29696
s_add_u32 m0, s36, 36864
Mp 132,4,40
buffer_load_dwordx4 v3, s[20:23], s55 offen offset:0 sc1 lds
Mp 144,8,40
Rb 56,10,30720
v_add_f32_e32 v15, v52, v26
Mp 152,8,44
v_exp_f32_e32 v55, v110
Mp 156,12,44
Rb 60,10,31744
s_waitcnt lgkmcnt(8)
Mp 148,12,40
v_exp_f32_e32 v29, v111
Cv 52,52,26
Mp 160,16,40
v_add_f32_e32 v15, v15, v53
v_add_f32_e32 v15, v15, v27
Mp 168,16,44
Cv 53,53,27
v_add_f32_e32 v15, v15, v54
Mp 172,20,44
v_add_f32_e32 v15, v15, v28
Cv 54,54,28
Mp 164,20,40
buffer_load_dwordx4 v3, s[20:23], s55 offen offset:1024 sc1 lds
Mp 176,24,40
v_add_f32_e32 v15, v15, v55
v_add_f32_e32 v15, v15, v29
Cv 55,55,29
Mp 184,24,44
s_sub_u32 s57, s57, 1
v_accvgpr_read_b32 v9, a192
Mp 188,28,44
v_accvgpr_read_b32 v11, a193
Cv 9,9,11
Mp 180,28,40
s_waitcnt vmcnt(6) lgkmcnt(0)
s_barrier
buffer_store_dword v6, v7, s[20:23], s49 offen offset:4 sc0 sc1
Mp 192,32,40
Rb 0,1,0
v_accvgpr_write_b32 a144, v9
Mp 200,32,44
Rb 4,1,1024
v_accvgpr_read_b32 v9, a194
v_accvgpr_read_b32 v11, a195
Mp 204,36,44
Rb 8,1,2048
Cv 9,9,11
Mp 196,36,40
Rb 12,1,3072
buffer_load_dwordx4 v3, s[20:23], s55 offen offset:2048 sc1 lds
Mp 208,40,40
Rb 16,1,8192
v_accvgpr_write_b32 a145, v9
)" R"(
Mp 216,40,44
Rb 20,1,9216
v_accvgpr_read_b32 v9, a196
Mp 220,44,44
Rb 24,1,10240
v_accvgpr_read_b32 v11, a197
Mp 212,44,40
Rb 28,1,11264
Cv 9,9,11
v_accvgpr_write_b32 a146, v9
Mp 224,48,40
Rb 32,1,4096
v_accvgpr_read_b32 v9, a198
Mp 232,48,44
Rb 36,1,5120
v_accvgpr_read_b32 v11, a199
Mp 236,52,44
Rb 40,1,6144
Cv 9,9,11
Mp 228,52,40
Rb 44,1,7168
buffer_load_dwordx4 v3, s[20:23], s55 offen offset:3072 sc1 lds
Mp 240,56,40
v_cmp_eq_u32_e32 vcc, s3, v4
s_andn2_b64 s[28:29], s[60:61], vcc
s_cbranch_scc1 .Lzsp19
.Lzok19:
Mp 248,56,44
v_accvgpr_write_b32 a147, v9
v_accvgpr_read_b32 v9, a200
Mp 252,60,44
v_accvgpr_read_b32 v11, a201
s_waitcnt lgkmcnt(8)
Mp 244,60,40
Cv 9,9,11
v_accvgpr_write_b32 a148, v9
v_mfma_f32_16x16x32_bf16 v[64:67], a[0:3], a[64:67], v[16:19]
Rb 48,1,12288
v_mfma_f32_16x16x32_bf16 v[68:71], a[0:3], a[68:71], v[20:23]
v_exp_f32_e32 v56, v112
Mv 68,4,76
Rb 52,1,13312
s_add_u32 m0, s36, 98304
Mv 64,4,72
buffer_load_dwordx4 v2, s[20:23], s56 offen offset:0 sc1 lds
Mv 64,8,80
Rb 56,1,14336
v_add_f32_e32 v14, v14, v56
Mv 68,8,84
buffer_store_dwordx4 a[224:227], v2, s[20:23], s47 offen offset:0 sc0 sc1
Mv 68,12,92
Rb 60,1,15360
s_waitcnt lgkmcnt(8)
Mv 64,12,88
v_exp_f32_e32 v24, v113
Mv 64,16,96
Rb 0,1,16384
v_add_f32_e32 v14, v14, v24
Mv 68,16,100
v_exp_f32_e32 v57, v114
Mv 68,20,108
Rb 4,1,17408
Cv 56,56,24
Mv 64,20,104
)" R"(
buffer_load_dwordx4 v2, s[20:23], s56 offen offset:1024 sc1 lds
Mv 64,24,112
Rb 8,1,18432
v_add_f32_e32 v14, v14, v57
Mv 68,24,116
v_exp_f32_e32 v25, v115
Mv 68,28,124
Rb 12,1,19456
s_waitcnt lgkmcnt(8)
Mv 64,28,120
v_exp_f32_e32 v58, v120
v_mfma_f32_16x16x32_bf16 v[72:75], a[32:35], a[64:67], v[16:19]
Rb 16,1,24576
v_add_f32_e32 v14, v14, v25
v_mfma_f32_16x16x32_bf16 v[76:79], a[32:35], a[68:71], v[20:23]
v_exp_f32_e32 v26, v121
Mv 76,36,76
Rb 20,1,25600
Cv 57,57,25
Mv 72,36,72
buffer_load_dwordx4 v2, s[20:23], s56 offen offset:2048 sc1 lds
Mv 72,40,80
Rb 24,1,26624
v_add_f32_e32 v14, v14, v58
Mv 76,40,84
v_exp_f32_e32 v59, v122
Mv 76,44,92
Rb 28,1,27648
s_waitcnt lgkmcnt(8)
Mv 72,44,88
v_exp_f32_e32 v27, v123
Mv 72,48,96
Rb 32,1,20480
v_add_f32_e32 v14, v14, v26
Mv 76,48,100
v_exp_f32_e32 v60, v116
Mv 76,52,108
Rb 36,1,21504
Cv 58,58,26
Mv 72,52,104
buffer_load_dwordx4 v2, s[20:23], s56 offen offset:3072 sc1 lds
Mv 72,56,112
Rb 40,1,22528
v_add_f32_e32 v14, v14, v59
Mv 76,56,116
buffer_store_dwordx4 a[228:231], v2, s[20:23], s47 offen offset:1024 sc0 sc1
Mv 76,60,124
Rb 44,1,23552
s_waitcnt lgkmcnt(8)
Mv 72,60,120
v_exp_f32_e32 v28, v117
v_mfma_f32_16x16x32_bf16 v[80:83], a[0:3], a[64:67], v[16:19]
Rb 48,1,28672
v_add_f32_e32 v14, v14, v27
v_mfma_f32_16x16x32_bf16 v[84:87], a[0:3], a[68:71], v[20:23]
v_exp_f32_e32 v61, v118
Mv 84,4,76
Rb 52,1,29696
s_add_u32 m0, s36, 102400
Mv 80,4,72
buffer_load_dwordx4 v3, s[20:23], s56 offen offset:0 sc1 lds
Mv 80,8,80
Rb 56,1,30720
Cv 59,59,27
)" R"(
Mv 84,8,84
v_exp_f32_e32 v29, v119
Mv 84,12,92
Rb 60,1,31744
s_waitcnt lgkmcnt(8)
Mv 80,12,88
v_exp_f32_e32 v62, v124
Mv 80,16,96
v_exp_f32_e32 v24, v125
Mv 84,16,100
v_exp_f32_e32 v63, v126
Mv 84,20,108
v_exp_f32_e32 v25, v127
Mv 80,20,104
buffer_load_dwordx4 v3, s[20:23], s56 offen offset:1024 sc1 lds
Mv 80,24,112
v_add_f32_e32 v15, v15, v60
v_add_f32_e32 v15, v15, v28
Mv 84,24,116
Cv 60,60,28
v_add_f32_e32 v15, v15, v61
Mv 84,28,124
v_add_f32_e32 v15, v15, v29
Cv 61,61,29
Mv 80,28,120
s_waitcnt lgkmcnt(0)
s_barrier
v_mfma_f32_16x16x32_bf16 v[88:91], a[32:35], a[64:67], v[16:19]
Rb 0,8,0
v_add_f32_e32 v15, v15, v62
v_mfma_f32_16x16x32_bf16 v[92:95], a[32:35], a[68:71], v[20:23]
Rb 4,8,1024
v_add_f32_e32 v15, v15, v24
Mv 92,36,76
Rb 8,8,2048
Cv 62,62,24
Mv 88,36,72
Rb 12,8,3072
buffer_load_dwordx4 v3, s[20:23], s56 offen offset:2048 sc1 lds
Mv 88,40,80
Rb 16,8,4096
v_add_f32_e32 v15, v15, v63
Mv 92,40,84
Rb 20,8,5120
buffer_store_dwordx4 a[232:235], v2, s[20:23], s47 offen offset:2048 sc0 sc1
Mv 92,44,92
Rb 24,8,6144
v_add_f32_e32 v15, v15, v25
Mv 88,44,88
Rb 28,8,7168
Cv 63,63,25
Mv 88,48,96
Rb 32,8,8192
v_max_f32_e32 v11, v14, v15
Mv 92,48,100
Rb 36,8,9216
v_cmp_nge_f32_e32 vcc, s62, v11
Mv 92,52,108
Rb 40,8,10240
s_add_u32 s55, s55, 0x10000
Mv 88,52,104
Rb 44,8,11264
buffer_load_dwordx4 v3, s[20:23], s56 offen offset:3072 sc1 lds
Mv 88,56,112
s_cmp_eq_u32 s57, 2
)" R"(
s_cselect_b32 s55, s59, s55
Mv 92,56,116
s_add_u32 s56, s56, 0x10000
Mv 92,60,124
s_waitcnt lgkmcnt(8)
s_cbranch_vccnz .Lzfx7
.Lzfr7:
Mv 88,60,120
v_add_f32_e32 v12, v12, v14
v_add_f32_e32 v13, v13, v15
Mp 128,0,48
Rb 48,8,12288
Mp 136,0,52
s_cmp_le_i32 s57, s58
s_cbranch_scc1 .Lzmk6
.Lzmr6:
Mp 140,4,52
Rb 52,8,13312
s_add_u32 m0, s36, 0
Mp 132,4,48
buffer_load_dwordx4 v2, s[20:23], s55 offen offset:0 sc1 lds
Mp 144,8,48
Rb 56,8,14336
Mp 152,8,52
v_exp_f32_e32 v32, v64
Mp 156,12,52
Rb 60,8,15360
s_waitcnt lgkmcnt(8)
Mp 148,12,48
v_exp_f32_e32 v26, v65
Mp 160,16,48
Rb 0,8,16384
v_exp_f32_e32 v33, v66
Mp 168,16,52
v_exp_f32_e32 v27, v67
Mp 172,20,52
Rb 4,8,17408
v_add_f32_e32 v14, v32, v26
Mp 164,20,48
buffer_load_dwordx4 v2, s[20:23], s55 offen offset:1024 sc1 lds
Cv 32,32,26
Mp 176,24,48
Rb 8,8,18432
v_add_f32_e32 v14, v14, v33
Mp 184,24,52
buffer_store_dwordx4 a[236:239], v2, s[20:23], s47 offen offset:3072 sc0 sc1
Mp 188,28,52
Rb 12,8,19456
v_add_f32_e32 v14, v14, v27
s_waitcnt lgkmcnt(8)
Mp 180,28,48
v_exp_f32_e32 v34, v72
Mp 192,32,48
Rb 16,8,20480
Cv 33,33,27
Mp 200,32,52
v_exp_f32_e32 v28, v73
v_add_f32_e32 v14, v14, v34
Mp 204,36,52
Rb 20,8,21504
v_add_f32_e32 v14, v14, v28
Mp 196,36,48
buffer_load_dwordx4 v2, s[20:23], s55 offen offset:2048 sc1 lds
Mp 208,40,48
)" R"(
Rb 24,8,22528
Cv 34,34,28
Mp 216,40,52
v_exp_f32_e32 v35, v74
s_sub_u32 s57, s57, 1
Mp 220,44,52
Rb 28,8,23552
s_waitcnt lgkmcnt(8)
Mp 212,44,48
v_exp_f32_e32 v29, v75
Mp 224,48,48
Rb 32,8,24576
v_exp_f32_e32 v36, v68
Mp 232,48,52
v_exp_f32_e32 v24, v69
Mp 236,52,52
Rb 36,8,25600
v_add_f32_e32 v14, v14, v35
Mp 228,52,48
buffer_load_dwordx4 v2, s[20:23], s55 offen offset:3072 sc1 lds
v_add_f32_e32 v14, v14, v29
Mp 240,56,48
Rb 40,8,26624
Cv 35,35,29
Mp 248,56,52
v_exp_f32_e32 v37, v70
Mp 252,60,52
Rb 44,8,27648
v_add_f32_e32 v15, v36, v24
s_waitcnt lgkmcnt(8)
Mp 244,60,48
v_exp_f32_e32 v25, v71
Mp 128,0,56
Rb 48,8,28672
Cv 36,36,24
Mp 136,0,60
v_exp_f32_e32 v38, v76
v_add_f32_e32 v15, v15, v37
Mp 140,4,60
Rb 52,8,29696
s_add_u32 m0, s36, 4096
Mp 132,4,56
buffer_load_dwordx4 v3, s[20:23], s55 offen offset:0 sc1 lds
Mp 144,8,56
Rb 56,8,30720
v_add_f32_e32 v15, v15, v25
Mp 152,8,60
buffer_store_dwordx4 a[240:243], v3, s[20:23], s47 offen offset:0 sc0 sc1
Cv 37,37,25
Mp 156,12,60
Rb 60,8,31744
s_waitcnt lgkmcnt(8)
Mp 148,12,56
v_exp_f32_e32 v26, v77
Mp 160,16,56
v_exp_f32_e32 v39, v78
v_add_f32_e32 v15, v15, v38
Mp 168,16,60
v_exp_f32_e32 v27, v79
Mp 172,20,60
v_add_f32_e32 v15, v15, v26
Cv 38,38,26
Mp 164,20,56
buffer_load_dwordx4 v3, s[20:23], s55 offen offset:1024 sc1 lds
)" R"(
v_add_f32_e32 v15, v15, v39
Mp 176,24,56
v_add_f32_e32 v15, v15, v27
Cv 39,39,27
Mp 184,24,60
v_accvgpr_read_b32 v9, a202
v_accvgpr_read_b32 v11, a203
Mp 188,28,60
Cv 9,9,11
v_accvgpr_write_b32 a149, v9
v_accvgpr_read_b32 v9, a204
Mp 180,28,56
s_waitcnt vmcnt(19) lgkmcnt(0)
s_barrier
Mp 192,32,56
Rb 0,1,32768
v_accvgpr_read_b32 v11, a205
Mp 200,32,60
Rb 4,1,33792
Cv 9,9,11
v_accvgpr_write_b32 a150, v9
Mp 204,36,60
Rb 8,1,34816
v_accvgpr_read_b32 v9, a206
Mp 196,36,56
Rb 12,1,35840
buffer_load_dwordx4 v3, s[20:23], s55 offen offset:2048 sc1 lds
Mp 208,40,56
Rb 16,1,40960
v_accvgpr_read_b32 v11, a207
Mp 216,40,60
Rb 20,1,41984
Cv 9,9,11
v_accvgpr_write_b32 a151, v9
Mp 220,44,60
Rb 24,1,43008
v_accvgpr_read_b32 v9, a208
Mp 212,44,56
Rb 28,1,44032
v_accvgpr_read_b32 v11, a209
Mp 224,48,56
Rb 32,1,36864
Cv 9,9,11
v_accvgpr_write_b32 a152, v9
Mp 232,48,60
Rb 36,1,37888
v_accvgpr_read_b32 v9, a210
Mp 236,52,60
Rb 40,1,38912
v_accvgpr_read_b32 v11, a211
Mp 228,52,56
Rb 44,1,39936
buffer_load_dwordx4 v3, s[20:23], s55 offen offset:3072 sc1 lds
Mp 240,56,56
Cv 9,9,11
v_accvgpr_write_b32 a153, v9
Mp 248,56,60
buffer_store_dwordx4 a[244:247], v3, s[20:23], s47 offen offset:1024 sc0 sc1
Mp 252,60,60
v_accvgpr_read_b32 v9, a212
v_accvgpr_read_b32 v11, a213
s_waitcnt lgkmcnt(8)
Mp 244,60,56
Cv 9,9,11
v_accvgpr_write_b32 a154, v9
)" R"(
v_mfma_f32_16x16x32_bf16 v[96:99], a[0:3], a[64:67], v[16:19]
Rb 48,1,45056
buffer_load_dword v4, v5, s[20:23], s50 offen offset:0 sc0 sc1
v_mfma_f32_16x16x32_bf16 v[100:103], a[0:3], a[68:71], v[20:23]
s_lshl_b64 s[60:61], s[60:61], 8
s_or_b32 s60, s60, 0xff
Mv 100,4,76
Rb 52,1,46080
s_add_u32 m0, s36, 65536
Mv 96,4,72
buffer_load_dwordx4 v2, s[20:23], s56 offen offset:0 sc1 lds
Mv 96,8,80
Rb 56,1,47104
Mv 100,8,84
v_exp_f32_e32 v40, v80
Mv 100,12,92
Rb 60,1,48128
s_waitcnt lgkmcnt(8)
Mv 96,12,88
v_exp_f32_e32 v28, v81
Mv 96,16,96
Rb 0,1,49152
v_add_f32_e32 v14, v14, v40
Mv 100,16,100
v_exp_f32_e32 v41, v82
Mv 100,20,108
Rb 4,1,50176
v_add_f32_e32 v14, v14, v28
Mv 96,20,104
buffer_load_dwordx4 v2, s[20:23], s56 offen offset:1024 sc1 lds
Mv 96,24,112
Rb 8,1,51200
Cv 40,40,28
Mv 100,24,116
v_exp_f32_e32 v29, v83
Mv 100,28,124
Rb 12,1,52224
s_waitcnt lgkmcnt(8)
Mv 96,28,120
v_exp_f32_e32 v42, v88
v_add_f32_e32 v14, v14, v41
v_mfma_f32_16x16x32_bf16 v[104:107], a[32:35], a[64:67], v[16:19]
Rb 16,1,57344
v_add_f32_e32 v14, v14, v29
v_mfma_f32_16x16x32_bf16 v[108:111], a[32:35], a[68:71], v[20:23]
v_exp_f32_e32 v24, v89
Mv 108,36,76
Rb 20,1,58368
Cv 41,41,29
Mv 104,36,72
buffer_load_dwordx4 v2, s[20:23], s56 offen offset:2048 sc1 lds
Mv 104,40,80
Rb 24,1,59392
v_add_f32_e32 v14, v14, v42
Mv 108,40,84
buffer_store_dwordx4 a[248:251], v3, s[20:23], s47 offen offset:2048 sc0 sc1
Mv 108,44,92
Rb 28,1,60416
s_waitcnt lgkmcnt(8)
Mv 104,44,88
v_exp_f32_e32 v43, v90
Mv 104,48,96
Rb 32,1,53248
v_add_f32_e32 v14, v14, v24
)" R"(
Mv 108,48,100
v_exp_f32_e32 v25, v91
Mv 108,52,108
Rb 36,1,54272
Cv 42,42,24
Mv 104,52,104
buffer_load_dwordx4 v2, s[20:23], s56 offen offset:3072 sc1 lds
Mv 104,56,112
Rb 40,1,55296
v_add_f32_e32 v14, v14, v43
Mv 108,56,116
v_exp_f32_e32 v44, v84
Mv 108,60,124
Rb 44,1,56320
s_waitcnt lgkmcnt(8)
Mv 104,60,120
v_exp_f32_e32 v26, v85
v_add_f32_e32 v14, v14, v25
v_mfma_f32_16x16x32_bf16 v[112:115], a[0:3], a[64:67], v[16:19]
Rb 48,1,61440
Cv 43,43,25
v_mfma_f32_16x16x32_bf16 v[116:119], a[0:3], a[68:71], v[20:23]
v_exp_f32_e32 v45, v86
Mv 116,4,76
Rb 52,1,62464
s_add_u32 m0, s36, 69632
Mv 112,4,72
buffer_load_dwordx4 v3, s[20:23], s56 offen offset:0 sc1 lds
Mv 112,8,80
Rb 56,1,63488
v_add_f32_e32 v15, v15, v44
Mv 116,8,84
v_exp_f32_e32 v27, v87
Mv 116,12,92
Rb 60,1,64512
s_waitcnt lgkmcnt(8)
Mv 112,12,88
v_exp_f32_e32 v46, v92
Mv 112,16,96
v_exp_f32_e32 v28, v93
Mv 116,16,100
v_exp_f32_e32 v47, v94
Mv 116,20,108
v_exp_f32_e32 v29, v95
Mv 112,20,104
buffer_load_dwordx4 v3, s[20:23], s56 offen offset:1024 sc1 lds
Mv 112,24,112
v_add_f32_e32 v15, v15, v26
Cv 44,44,26
Mv 116,24,116
buffer_store_dwordx4 a[252:255], v3, s[20:23], s47 offen offset:3072 sc0 sc1
Mv 116,28,124
v_add_f32_e32 v15, v15, v45
v_add_f32_e32 v15, v15, v27
Mv 112,28,120
Cv 45,45,27
s_waitcnt vmcnt(20) lgkmcnt(0)
s_barrier
v_mfma_f32_16x16x32_bf16 v[120:123], a[32:35], a[64:67], v[16:19]
Rb 0,8,32768
v_add_f32_e32 v15, v15, v46
v_mfma_f32_16x16x32_bf16 v[124:127], a[32:35], a[68:71], v[20:23]
Rb 4,8,33792
v_add_f32_e32 v15, v15, v28
Mv 124,36,76
)" R"(
Rb 8,8,34816
Cv 46,46,28
Mv 120,36,72
Rb 12,8,35840
buffer_load_dwordx4 v3, s[20:23], s56 offen offset:2048 sc1 lds
Mv 120,40,80
Rb 16,8,36864
v_add_f32_e32 v15, v15, v47
Mv 124,40,84
Rb 20,8,37888
v_add_f32_e32 v15, v15, v29
Mv 124,44,92
Rb 24,8,38912
Cv 47,47,29
Mv 120,44,88
Rb 28,8,39936
v_max_f32_e32 v11, v14, v15
Mv 120,48,96
Rb 32,8,40960
v_cmp_nge_f32_e32 vcc, s62, v11
Mv 124,48,100
Rb 36,8,41984
s_add_u32 s55, s55, 0x10000
Mv 124,52,108
Rb 40,8,43008
Mv 120,52,104
Rb 44,8,44032
buffer_load_dwordx4 v3, s[20:23], s56 offen offset:3072 sc1 lds
Mv 120,56,112
s_cmp_eq_u32 s57, 2
s_cselect_b32 s55, s59, s55
Mv 124,56,116
s_add_u32 s56, s56, 0x10000
Mv 124,60,124
s_waitcnt lgkmcnt(8)
s_cbranch_vccnz .Lzfx8
.Lzfr8:
Mv 120,60,120
v_add_f32_e32 v12, v12, v14
v_add_f32_e32 v13, v13, v15
Mp 128,0,32
Rb 48,8,45056
Mp 136,0,36
s_cmp_le_i32 s57, s58
s_cbranch_scc1 .Lzmk7
.Lzmr7:
Mp 140,4,36
Rb 52,8,46080
s_add_u32 m0, s36, 32768
Mp 132,4,32
buffer_load_dwordx4 v2, s[20:23], s55 offen offset:0 sc1 lds
Mp 144,8,32
Rb 56,8,47104
Mp 152,8,36
v_exp_f32_e32 v48, v96
Mp 156,12,36
Rb 60,8,48128
s_waitcnt lgkmcnt(8)
Mp 148,12,32
v_exp_f32_e32 v24, v97
Mp 160,16,32
Rb 0,8,49152
v_add_f32_e32 v14, v48, v24
Mp 168,16,36
)" R"(
v_exp_f32_e32 v49, v98
Mp 172,20,36
Rb 4,8,50176
Cv 48,48,24
Mp 164,20,32
buffer_load_dwordx4 v2, s[20:23], s55 offen offset:1024 sc1 lds
Mp 176,24,32
Rb 8,8,51200
v_exp_f32_e32 v25, v99
Mp 184,24,36
v_exp_f32_e32 v50, v104
Mp 188,28,36
Rb 12,8,52224
s_waitcnt lgkmcnt(8)
Mp 180,28,32
v_exp_f32_e32 v26, v105
Mp 192,32,32
Rb 16,8,53248
v_add_f32_e32 v14, v14, v49
Mp 200,32,36
v_exp_f32_e32 v51, v106
v_add_f32_e32 v14, v14, v25
Mp 204,36,36
Rb 20,8,54272
Cv 49,49,25
Mp 196,36,32
buffer_load_dwordx4 v2, s[20:23], s55 offen offset:2048 sc1 lds
Mp 208,40,32
Rb 24,8,55296
v_add_f32_e32 v14, v14, v50
Mp 216,40,36
v_exp_f32_e32 v27, v107
Mp 220,44,36
Rb 28,8,56320
s_waitcnt lgkmcnt(8)
Mp 212,44,32
v_exp_f32_e32 v52, v100
v_add_f32_e32 v14, v14, v26
Mp 224,48,32
Rb 32,8,57344
Cv 50,50,26
Mp 232,48,36
v_exp_f32_e32 v28, v101
Mp 236,52,36
Rb 36,8,58368
v_add_f32_e32 v14, v14, v51
Mp 228,52,32
buffer_load_dwordx4 v2, s[20:23], s55 offen offset:3072 sc1 lds
Mp 240,56,32
Rb 40,8,59392
v_exp_f32_e32 v53, v102
Mp 248,56,36
v_exp_f32_e32 v29, v103
Mp 252,60,36
Rb 44,8,60416
s_waitcnt lgkmcnt(8)
Mp 244,60,32
v_exp_f32_e32 v54, v108
Mp 128,0,40
Rb 48,8,61440
v_add_f32_e32 v14, v14, v27
Mp 136,0,44
v_exp_f32_e32 v24, v109
Cv 51,51,27
)" R"(
Mp 140,4,44
Rb 52,8,62464
s_add_u32 m0, s36, 36864
Mp 132,4,40
buffer_load_dwordx4 v3, s[20:23], s55 offen offset:0 sc1 lds
Mp 144,8,40
Rb 56,8,63488
v_add_f32_e32 v15, v52, v28
Mp 152,8,44
v_exp_f32_e32 v55, v110
Mp 156,12,44
Rb 60,8,64512
s_waitcnt lgkmcnt(8)
Mp 148,12,40
v_exp_f32_e32 v25, v111
Cv 52,52,28
Mp 160,16,40
v_add_f32_e32 v15, v15, v53
v_add_f32_e32 v15, v15, v29
Mp 168,16,44
Cv 53,53,29
v_add_f32_e32 v15, v15, v54
Mp 172,20,44
v_add_f32_e32 v15, v15, v24
Cv 54,54,24
Mp 164,20,40
buffer_load_dwordx4 v3, s[20:23], s55 offen offset:1024 sc1 lds
Mp 176,24,40
v_add_f32_e32 v15, v15, v55
v_add_f32_e32 v15, v15, v25
Cv 55,55,25
Mp 184,24,44
s_sub_u32 s57, s57, 1
v_accvgpr_read_b32 v9, a214
Mp 188,28,44
v_accvgpr_read_b32 v11, a215
Cv 9,9,11
Mp 180,28,40
s_waitcnt vmcnt(8) lgkmcnt(0)
s_barrier
buffer_store_dword v6, v7, s[20:23], s49 offen offset:8 sc0 sc1
Mp 192,32,40
Rb 0,1,0
v_accvgpr_write_b32 a155, v9
Mp 200,32,44
Rb 4,1,1024
v_accvgpr_read_b32 v9, a216
v_accvgpr_read_b32 v11, a217
Mp 204,36,44
Rb 8,1,2048
Cv 9,9,11
Mp 196,36,40
Rb 12,1,3072
buffer_load_dwordx4 v3, s[20:23], s55 offen offset:2048 sc1 lds
Mp 208,40,40
Rb 16,1,8192
v_accvgpr_write_b32 a156, v9
Mp 216,40,44
Rb 20,1,9216
v_accvgpr_read_b32 v9, a218
Mp 220,44,44
Rb 24,1,10240
v_accvgpr_read_b32 v11, a219
Mp 212,44,40
Rb 28,1,11264
)" R"(
Cv 9,9,11
v_accvgpr_write_b32 a157, v9
Mp 224,48,40
Rb 32,1,4096
v_accvgpr_read_b32 v9, a220
Mp 232,48,44
Rb 36,1,5120
v_accvgpr_read_b32 v11, a221
Mp 236,52,44
Rb 40,1,6144
Cv 9,9,11
Mp 228,52,40
Rb 44,1,7168
buffer_load_dwordx4 v3, s[20:23], s55 offen offset:3072 sc1 lds
Mp 240,56,40
v_cmp_eq_u32_e32 vcc, s3, v4
s_andn2_b64 s[28:29], s[60:61], vcc
s_cbranch_scc1 .Lzsp20
.Lzok20:
Mp 248,56,44
v_accvgpr_write_b32 a158, v9
v_accvgpr_read_b32 v9, a222
Mp 252,60,44
v_accvgpr_read_b32 v11, a223
s_waitcnt lgkmcnt(8)
Mp 244,60,40
Cv 9,9,11
v_accvgpr_write_b32 a159, v9
v_mfma_f32_16x16x32_bf16 v[64:67], a[0:3], a[64:67], v[16:19]
Rb 48,1,12288
v_mfma_f32_16x16x32_bf16 v[68:71], a[0:3], a[68:71], v[20:23]
v_exp_f32_e32 v56, v112
Mv 68,4,76
Rb 52,1,13312
s_add_u32 m0, s36, 98304
Mv 64,4,72
buffer_load_dwordx4 v2, s[20:23], s56 offen offset:0 sc1 lds
Mv 64,8,80
Rb 56,1,14336
v_add_f32_e32 v14, v14, v56
Mv 68,8,84
buffer_store_dwordx4 a[128:131], v2, s[20:23], s48 offen offset:0 sc0 sc1
Mv 68,12,92
Rb 60,1,15360
s_waitcnt lgkmcnt(8)
Mv 64,12,88
v_exp_f32_e32 v26, v113
Mv 64,16,96
Rb 0,1,16384
v_add_f32_e32 v14, v14, v26
Mv 68,16,100
v_exp_f32_e32 v57, v114
Mv 68,20,108
Rb 4,1,17408
Cv 56,56,26
Mv 64,20,104
buffer_load_dwordx4 v2, s[20:23], s56 offen offset:1024 sc1 lds
Mv 64,24,112
Rb 8,1,18432
v_add_f32_e32 v14, v14, v57
Mv 68,24,116
v_exp_f32_e32 v27, v115
Mv 68,28,124
Rb 12,1,19456
)" R"(
s_waitcnt lgkmcnt(8)
Mv 64,28,120
v_exp_f32_e32 v58, v120
v_mfma_f32_16x16x32_bf16 v[72:75], a[32:35], a[64:67], v[16:19]
Rb 16,1,24576
v_add_f32_e32 v14, v14, v27
v_mfma_f32_16x16x32_bf16 v[76:79], a[32:35], a[68:71], v[20:23]
v_exp_f32_e32 v28, v121
Mv 76,36,76
Rb 20,1,25600
Cv 57,57,27
Mv 72,36,72
buffer_load_dwordx4 v2, s[20:23], s56 offen offset:2048 sc1 lds
Mv 72,40,80
Rb 24,1,26624
v_add_f32_e32 v14, v14, v58
Mv 76,40,84
v_exp_f32_e32 v59, v122
Mv 76,44,92
Rb 28,1,27648
s_waitcnt lgkmcnt(8)
Mv 72,44,88
v_exp_f32_e32 v29, v123
Mv 72,48,96
Rb 32,1,20480
v_add_f32_e32 v14, v14, v28
Mv 76,48,100
v_exp_f32_e32 v60, v116
Mv 76,52,108
Rb 36,1,21504
Cv 58,58,28
Mv 72,52,104
buffer_load_dwordx4 v2, s[20:23], s56 offen offset:3072 sc1 lds
Mv 72,56,112
Rb 40,1,22528
v_add_f32_e32 v14, v14, v59
Mv 76,56,116
buffer_store_dwordx4 a[132:135], v2, s[20:23], s48 offen offset:1024 sc0 sc1
Mv 76,60,124
Rb 44,1,23552
s_waitcnt lgkmcnt(8)
Mv 72,60,120
v_exp_f32_e32 v24, v117
v_mfma_f32_16x16x32_bf16 v[80:83], a[0:3], a[64:67], v[16:19]
Rb 48,1,28672
v_add_f32_e32 v14, v14, v29
v_mfma_f32_16x16x32_bf16 v[84:87], a[0:3], a[68:71], v[20:23]
v_exp_f32_e32 v61, v118
Mv 84,4,76
Rb 52,1,29696
s_add_u32 m0, s36, 102400
Mv 80,4,72
buffer_load_dwordx4 v3, s[20:23], s56 offen offset:0 sc1 lds
Mv 80,8,80
Rb 56,1,30720
Cv 59,59,29
Mv 84,8,84
v_exp_f32_e32 v25, v119
Mv 84,12,92
Rb 60,1,31744
s_waitcnt lgkmcnt(8)
Mv 80,12,88
v_exp_f32_e32 v62, v124
Mv 80,16,96
)" R"(
v_exp_f32_e32 v26, v125
Mv 84,16,100
v_exp_f32_e32 v63, v126
Mv 84,20,108
v_exp_f32_e32 v27, v127
Mv 80,20,104
buffer_load_dwordx4 v3, s[20:23], s56 offen offset:1024 sc1 lds
Mv 80,24,112
v_add_f32_e32 v15, v15, v60
v_add_f32_e32 v15, v15, v24
Mv 84,24,116
Cv 60,60,24
v_add_f32_e32 v15, v15, v61
Mv 84,28,124
v_add_f32_e32 v15, v15, v25
Cv 61,61,25
Mv 80,28,120
s_waitcnt vmcnt(17) lgkmcnt(0)
s_barrier
v_mfma_f32_16x16x32_bf16 v[88:91], a[32:35], a[64:67], v[16:19]
Rb 0,8,0
v_add_f32_e32 v15, v15, v62
v_mfma_f32_16x16x32_bf16 v[92:95], a[32:35], a[68:71], v[20:23]
Rb 4,8,1024
v_add_f32_e32 v15, v15, v26
Mv 92,36,76
Rb 8,8,2048
Cv 62,62,26
Mv 88,36,72
Rb 12,8,3072
buffer_load_dwordx4 v3, s[20:23], s56 offen offset:2048 sc1 lds
Mv 88,40,80
Rb 16,8,4096
v_add_f32_e32 v15, v15, v63
Mv 92,40,84
Rb 20,8,5120
buffer_store_dwordx4 a[136:139], v2, s[20:23], s48 offen offset:2048 sc0 sc1
Mv 92,44,92
Rb 24,8,6144
v_add_f32_e32 v15, v15, v27
Mv 88,44,88
Rb 28,8,7168
Cv 63,63,27
Mv 88,48,96
Rb 32,8,8192
v_max_f32_e32 v11, v14, v15
Mv 92,48,100
Rb 36,8,9216
v_cmp_nge_f32_e32 vcc, s62, v11
Mv 92,52,108
Rb 40,8,10240
s_add_u32 s55, s55, 0x10000
Mv 88,52,104
Rb 44,8,11264
buffer_load_dwordx4 v3, s[20:23], s56 offen offset:3072 sc1 lds
Mv 88,56,112
s_cmp_eq_u32 s57, 2
s_cselect_b32 s55, s59, s55
Mv 92,56,116
s_add_u32 s56, s56, 0x10000
Mv 92,60,124
s_waitcnt lgkmcnt(8)
s_cbranch_vccnz .Lzfx9
.Lzfr9:
Mv 88,60,120
)" R"(
v_add_f32_e32 v12, v12, v14
v_add_f32_e32 v13, v13, v15
Mp 128,0,48
Rb 48,8,12288
Mp 136,0,52
s_cmp_le_i32 s57, s58
s_cbranch_scc1 .Lzmk8
.Lzmr8:
Mp 140,4,52
Rb 52,8,13312
s_add_u32 m0, s36, 0
Mp 132,4,48
buffer_load_dwordx4 v2, s[20:23], s55 offen offset:0 sc1 lds
Mp 144,8,48
Rb 56,8,14336
Mp 152,8,52
v_exp_f32_e32 v32, v64
Mp 156,12,52
Rb 60,8,15360
s_waitcnt lgkmcnt(8)
Mp 148,12,48
v_exp_f32_e32 v28, v65
Mp 160,16,48
Rb 0,8,16384
v_add_f32_e32 v14, v32, v28
Mp 168,16,52
v_exp_f32_e32 v33, v66
Mp 172,20,52
Rb 4,8,17408
Cv 32,32,28
Mp 164,20,48
buffer_load_dwordx4 v2, s[20:23], s55 offen offset:1024 sc1 lds
Mp 176,24,48
Rb 8,8,18432
v_add_f32_e32 v14, v14, v33
Mp 184,24,52
buffer_store_dwordx4 a[140:143], v2, s[20:23], s48 offen offset:3072 sc0 sc1
Mp 188,28,52
Rb 12,8,19456
s_waitcnt lgkmcnt(8)
Mp 180,28,48
v_exp_f32_e32 v29, v67
Mp 192,32,48
Rb 16,8,20480
v_add_f32_e32 v14, v14, v29
Mp 200,32,52
v_exp_f32_e32 v34, v72
Mp 204,36,52
Rb 20,8,21504
Cv 33,33,29
Mp 196,36,48
buffer_load_dwordx4 v2, s[20:23], s55 offen offset:2048 sc1 lds
Mp 208,40,48
Rb 24,8,22528
v_add_f32_e32 v14, v14, v34
Mp 216,40,52
v_exp_f32_e32 v24, v73
Mp 220,44,52
Rb 28,8,23552
s_waitcnt lgkmcnt(8)
Mp 212,44,48
v_exp_f32_e32 v35, v74
Mp 224,48,48
Rb 32,8,24576
)" R"(
v_add_f32_e32 v14, v14, v24
Mp 232,48,52
v_exp_f32_e32 v25, v75
Mp 236,52,52
Rb 36,8,25600
Cv 34,34,24
Mp 228,52,48
buffer_load_dwordx4 v2, s[20:23], s55 offen offset:3072 sc1 lds
Mp 240,56,48
Rb 40,8,26624
v_add_f32_e32 v14, v14, v35
Mp 248,56,52
v_exp_f32_e32 v36, v68
Mp 252,60,52
Rb 44,8,27648
s_waitcnt lgkmcnt(8)
Mp 244,60,48
v_exp_f32_e32 v26, v69
Mp 128,0,56
Rb 48,8,28672
v_add_f32_e32 v14, v14, v25
Mp 136,0,60
v_exp_f32_e32 v37, v70
Mp 140,4,60
Rb 52,8,29696
s_add_u32 m0, s36, 4096
Mp 132,4,56
buffer_load_dwordx4 v3, s[20:23], s55 offen offset:0 sc1 lds
Mp 144,8,56
Rb 56,8,30720
Cv 35,35,25
Mp 152,8,60
buffer_store_dwordx4 a[144:147], v3, s[20:23], s48 offen offset:0 sc0 sc1
Mp 156,12,60
Rb 60,8,31744
s_waitcnt lgkmcnt(8)
Mp 148,12,56
v_exp_f32_e32 v27, v71
Mp 160,16,56
v_exp_f32_e32 v38, v76
Mp 168,16,60
v_exp_f32_e32 v28, v77
Mp 172,20,60
v_exp_f32_e32 v39, v78
Mp 164,20,56
buffer_load_dwordx4 v3, s[20:23], s55 offen offset:1024 sc1 lds
Mp 176,24,56
v_exp_f32_e32 v29, v79
Mp 184,24,60
v_add_f32_e32 v15, v36, v26
Cv 36,36,26
Mp 188,28,60
v_add_f32_e32 v15, v15, v37
v_add_f32_e32 v15, v15, v27
Mp 180,28,56
s_waitcnt vmcnt(19) lgkmcnt(0)
s_barrier
Mp 192,32,56
Rb 0,1,32768
Cv 37,37,27
Mp 200,32,60
Rb 4,1,33792
v_add_f32_e32 v15, v15, v38
Mp 204,36,60
Rb 8,1,34816
)" R"(
v_add_f32_e32 v15, v15, v28
Mp 196,36,56
Rb 12,1,35840
buffer_load_dwordx4 v3, s[20:23], s55 offen offset:2048 sc1 lds
Mp 208,40,56
Rb 16,1,40960
Cv 38,38,28
Mp 216,40,60
Rb 20,1,41984
v_add_f32_e32 v15, v15, v39
Mp 220,44,60
Rb 24,1,43008
v_add_f32_e32 v15, v15, v29
Mp 212,44,56
Rb 28,1,44032
Cv 39,39,29
Mp 224,48,56
Rb 32,1,36864
s_sub_u32 s57, s57, 1
Mp 232,48,60
Rb 36,1,37888
Mp 236,52,60
Rb 40,1,38912
Mp 228,52,56
Rb 44,1,39936
buffer_load_dwordx4 v3, s[20:23], s55 offen offset:3072 sc1 lds
Mp 240,56,56
Mp 248,56,60
buffer_store_dwordx4 a[148:151], v3, s[20:23], s48 offen offset:1024 sc0 sc1
Mp 252,60,60
s_waitcnt lgkmcnt(8)
Mp 244,60,56
v_mfma_f32_16x16x32_bf16 v[96:99], a[0:3], a[64:67], v[16:19]
Rb 48,1,45056
buffer_load_dword v4, v5, s[20:23], s50 offen offset:0 sc0 sc1
v_mfma_f32_16x16x32_bf16 v[100:103], a[0:3], a[68:71], v[20:23]
s_lshl_b64 s[60:61], s[60:61], 8
s_or_b32 s60, s60, 0xff
Mv 100,4,76
Rb 52,1,46080
s_add_u32 m0, s36, 65536
Mv 96,4,72
buffer_load_dwordx4 v2, s[20:23], s56 offen offset:0 sc1 lds
Mv 96,8,80
Rb 56,1,47104
Mv 100,8,84
v_exp_f32_e32 v40, v80
Mv 100,12,92
Rb 60,1,48128
s_waitcnt lgkmcnt(8)
Mv 96,12,88
v_exp_f32_e32 v24, v81
Mv 96,16,96
Rb 0,1,49152
v_add_f32_e32 v14, v14, v40
Mv 100,16,100
v_exp_f32_e32 v41, v82
Mv 100,20,108
Rb 4,1,50176
v_add_f32_e32 v14, v14, v24
Mv 96,20,104
buffer_load_dwordx4 v2, s[20:23], s56 offen offset:1024 sc1 lds
Mv 96,24,112
Rb 8,1,51200
)" R"(
Cv 40,40,24
Mv 100,24,116
v_exp_f32_e32 v25, v83
Mv 100,28,124
Rb 12,1,52224
s_waitcnt lgkmcnt(8)
Mv 96,28,120
v_exp_f32_e32 v42, v88
v_add_f32_e32 v14, v14, v41
v_mfma_f32_16x16x32_bf16 v[104:107], a[32:35], a[64:67], v[16:19]
Rb 16,1,57344
v_add_f32_e32 v14, v14, v25
v_mfma_f32_16x16x32_bf16 v[108:111], a[32:35], a[68:71], v[20:23]
v_exp_f32_e32 v26, v89
Mv 108,36,76
Rb 20,1,58368
Cv 41,41,25
Mv 104,36,72
buffer_load_dwordx4 v2, s[20:23], s56 offen offset:2048 sc1 lds
Mv 104,40,80
Rb 24,1,59392
v_add_f32_e32 v14, v14, v42
Mv 108,40,84
buffer_store_dwordx4 a[152:155], v3, s[20:23], s48 offen offset:2048 sc0 sc1
Mv 108,44,92
Rb 28,1,60416
s_waitcnt lgkmcnt(8)
Mv 104,44,88
v_exp_f32_e32 v43, v90
Mv 104,48,96
Rb 32,1,53248
v_add_f32_e32 v14, v14, v26
Mv 108,48,100
v_exp_f32_e32 v27, v91
Mv 108,52,108
Rb 36,1,54272
Cv 42,42,26
Mv 104,52,104
buffer_load_dwordx4 v2, s[20:23], s56 offen offset:3072 sc1 lds
Mv 104,56,112
Rb 40,1,55296
v_add_f32_e32 v14, v14, v43
Mv 108,56,116
v_exp_f32_e32 v44, v84
Mv 108,60,124
Rb 44,1,56320
s_waitcnt lgkmcnt(8)
Mv 104,60,120
v_exp_f32_e32 v28, v85
v_add_f32_e32 v14, v14, v27
v_mfma_f32_16x16x32_bf16 v[112:115], a[0:3], a[64:67], v[16:19]
Rb 48,1,61440
Cv 43,43,27
v_mfma_f32_16x16x32_bf16 v[116:119], a[0:3], a[68:71], v[20:23]
v_exp_f32_e32 v45, v86
Mv 116,4,76
Rb 52,1,62464
s_add_u32 m0, s36, 69632
Mv 112,4,72
buffer_load_dwordx4 v3, s[20:23], s56 offen offset:0 sc1 lds
Mv 112,8,80
Rb 56,1,63488
v_add_f32_e32 v15, v15, v44
Mv 116,8,84
)" R"(
v_exp_f32_e32 v29, v87
Mv 116,12,92
Rb 60,1,64512
s_waitcnt lgkmcnt(8)
Mv 112,12,88
v_exp_f32_e32 v46, v92
Mv 112,16,96
v_exp_f32_e32 v24, v93
Mv 116,16,100
v_exp_f32_e32 v47, v94
Mv 116,20,108
v_exp_f32_e32 v25, v95
Mv 112,20,104
buffer_load_dwordx4 v3, s[20:23], s56 offen offset:1024 sc1 lds
Mv 112,24,112
v_add_f32_e32 v15, v15, v28
Cv 44,44,28
Mv 116,24,116
buffer_store_dwordx4 a[156:159], v3, s[20:23], s48 offen offset:3072 sc0 sc1
Mv 116,28,124
v_add_f32_e32 v15, v15, v45
v_add_f32_e32 v15, v15, v29
Mv 112,28,120
Cv 45,45,29
s_waitcnt vmcnt(20) lgkmcnt(0)
s_barrier
v_mfma_f32_16x16x32_bf16 v[120:123], a[32:35], a[64:67], v[16:19]
Rb 0,8,32768
v_add_f32_e32 v15, v15, v46
v_mfma_f32_16x16x32_bf16 v[124:127], a[32:35], a[68:71], v[20:23]
Rb 4,8,33792
v_add_f32_e32 v15, v15, v24
Mv 124,36,76
Rb 8,8,34816
Cv 46,46,24
Mv 120,36,72
Rb 12,8,35840
buffer_load_dwordx4 v3, s[20:23], s56 offen offset:2048 sc1 lds
Mv 120,40,80
Rb 16,8,36864
v_add_f32_e32 v15, v15, v47
Mv 124,40,84
Rb 20,8,37888
v_add_f32_e32 v15, v15, v25
Mv 124,44,92
Rb 24,8,38912
Cv 47,47,25
Mv 120,44,88
Rb 28,8,39936
v_max_f32_e32 v11, v14, v15
Mv 120,48,96
Rb 32,8,40960
v_cmp_nge_f32_e32 vcc, s62, v11
Mv 124,48,100
Rb 36,8,41984
s_add_u32 s55, s55, 0x10000
Mv 124,52,108
Rb 40,8,43008
Mv 120,52,104
Rb 44,8,44032
buffer_load_dwordx4 v3, s[20:23], s56 offen offset:3072 sc1 lds
Mv 120,56,112
s_cmp_eq_u32 s57, 2
s_cselect_b32 s55, s59, s55
Mv 124,56,116
)" R"(
s_add_u32 s56, s56, 0x10000
Mv 124,60,124
s_waitcnt lgkmcnt(8)
s_cbranch_vccnz .Lzfx10
.Lzfr10:
Mv 120,60,120
v_add_f32_e32 v12, v12, v14
v_add_f32_e32 v13, v13, v15
Mp 128,0,32
Rb 48,8,45056
Mp 136,0,36
s_cmp_le_i32 s57, s58
s_cbranch_scc1 .Lzmk9
.Lzmr9:
Mp 140,4,36
Rb 52,8,46080
s_add_u32 m0, s36, 32768
Mp 132,4,32
buffer_load_dwordx4 v2, s[20:23], s55 offen offset:0 sc1 lds
Mp 144,8,32
Rb 56,8,47104
Mp 152,8,36
v_exp_f32_e32 v48, v96
Mp 156,12,36
Rb 60,8,48128
s_waitcnt lgkmcnt(8)
Mp 148,12,32
v_exp_f32_e32 v26, v97
Mp 160,16,32
Rb 0,8,49152
v_add_f32_e32 v14, v48, v26
Mp 168,16,36
v_exp_f32_e32 v49, v98
Mp 172,20,36
Rb 4,8,50176
Cv 48,48,26
Mp 164,20,32
buffer_load_dwordx4 v2, s[20:23], s55 offen offset:1024 sc1 lds
Mp 176,24,32
Rb 8,8,51200
v_add_f32_e32 v14, v14, v49
Mp 184,24,36
v_exp_f32_e32 v27, v99
Mp 188,28,36
Rb 12,8,52224
s_waitcnt lgkmcnt(8)
Mp 180,28,32
v_exp_f32_e32 v50, v104
Mp 192,32,32
Rb 16,8,53248
v_add_f32_e32 v14, v14, v27
Mp 200,32,36
v_exp_f32_e32 v28, v105
Mp 204,36,36
Rb 20,8,54272
Cv 49,49,27
Mp 196,36,32
buffer_load_dwordx4 v2, s[20:23], s55 offen offset:2048 sc1 lds
Mp 208,40,32
Rb 24,8,55296
v_add_f32_e32 v14, v14, v50
Mp 216,40,36
v_exp_f32_e32 v51, v106
Mp 220,44,36
)" R"(
Rb 28,8,56320
s_waitcnt lgkmcnt(8)
Mp 212,44,32
v_exp_f32_e32 v29, v107
Mp 224,48,32
Rb 32,8,57344
v_add_f32_e32 v14, v14, v28
Mp 232,48,36
v_exp_f32_e32 v52, v100
Mp 236,52,36
Rb 36,8,58368
Cv 50,50,28
Mp 228,52,32
buffer_load_dwordx4 v2, s[20:23], s55 offen offset:3072 sc1 lds
Mp 240,56,32
Rb 40,8,59392
v_add_f32_e32 v14, v14, v51
Mp 248,56,36
v_exp_f32_e32 v24, v101
Mp 252,60,36
Rb 44,8,60416
s_waitcnt lgkmcnt(8)
Mp 244,60,32
v_exp_f32_e32 v53, v102
Mp 128,0,40
Rb 48,8,61440
v_add_f32_e32 v14, v14, v29
Mp 136,0,44
v_exp_f32_e32 v25, v103
Mp 140,4,44
Rb 52,8,62464
s_add_u32 m0, s36, 36864
Mp 132,4,40
buffer_load_dwordx4 v3, s[20:23], s55 offen offset:0 sc1 lds
Mp 144,8,40
Rb 56,8,63488
Cv 51,51,29
Mp 152,8,44
v_exp_f32_e32 v54, v108
Mp 156,12,44
Rb 60,8,64512
s_waitcnt lgkmcnt(8)
Mp 148,12,40
v_exp_f32_e32 v26, v109
Mp 160,16,40
v_exp_f32_e32 v55, v110
Mp 168,16,44
v_exp_f32_e32 v27, v111
Mp 172,20,44
v_add_f32_e32 v15, v52, v24
Cv 52,52,24
Mp 164,20,40
buffer_load_dwordx4 v3, s[20:23], s55 offen offset:1024 sc1 lds
Mp 176,24,40
v_add_f32_e32 v15, v15, v53
v_add_f32_e32 v15, v15, v25
Mp 184,24,44
Cv 53,53,25
v_add_f32_e32 v15, v15, v54
Mp 188,28,44
v_add_f32_e32 v15, v15, v26
Cv 54,54,26
Mp 180,28,40
s_waitcnt vmcnt(8) lgkmcnt(0)
s_barrier
)" R"(
buffer_store_dword v6, v7, s[20:23], s49 offen offset:12 sc0 sc1
Mp 192,32,40
Rb 0,1,0
v_add_f32_e32 v15, v15, v55
Mp 200,32,44
Rb 4,1,1024
v_add_f32_e32 v15, v15, v27
Mp 204,36,44
Rb 8,1,2048
Cv 55,55,27
Mp 196,36,40
Rb 12,1,3072
buffer_load_dwordx4 v3, s[20:23], s55 offen offset:2048 sc1 lds
Mp 208,40,40
Rb 16,1,8192
s_sub_u32 s57, s57, 1
Mp 216,40,44
Rb 20,1,9216
Mp 220,44,44
Rb 24,1,10240
Mp 212,44,40
Rb 28,1,11264
Mp 224,48,40
Rb 32,1,4096
Mp 232,48,44
Rb 36,1,5120
Mp 236,52,44
Rb 40,1,6144
Mp 228,52,40
Rb 44,1,7168
buffer_load_dwordx4 v3, s[20:23], s55 offen offset:3072 sc1 lds
Mp 240,56,40
v_cmp_eq_u32_e32 vcc, s3, v4
s_andn2_b64 s[28:29], s[60:61], vcc
s_cbranch_scc1 .Lzsp21
.Lzok21:
Mp 248,56,44
Mp 252,60,44
s_waitcnt lgkmcnt(8)
Mp 244,60,40
s_branch .LzL
.LzEB:
v_mfma_f32_16x16x32_bf16 v[64:67], a[0:3], a[64:67], v[16:19]
Rb 48,1,12288
v_mfma_f32_16x16x32_bf16 v[68:71], a[0:3], a[68:71], v[20:23]
s_cmp_lg_u32 s35, 7
s_cselect_b64 s[60:61], 1, 0
Mv 68,4,76
Rb 52,1,13312
s_add_u32 m0, s36, 98304
Mv 64,4,72
buffer_load_dwordx4 v2, s[20:23], s56 offen offset:0 sc1 lds
Mv 64,8,80
Rb 56,1,14336
Mv 68,8,84
v_exp_f32_e32 v56, v112
Mv 68,12,92
Rb 60,1,15360
s_waitcnt lgkmcnt(8)
Mv 64,12,88
v_exp_f32_e32 v28, v113
Mv 64,16,96
Rb 0,1,16384
v_add_f32_e32 v14, v14, v56
)" R"(
Mv 68,16,100
v_exp_f32_e32 v57, v114
Mv 68,20,108
Rb 4,1,17408
v_add_f32_e32 v14, v14, v28
Mv 64,20,104
buffer_load_dwordx4 v2, s[20:23], s56 offen offset:1024 sc1 lds
Mv 64,24,112
Rb 8,1,18432
Cv 56,56,28
Mv 68,24,116
v_exp_f32_e32 v29, v115
Mv 68,28,124
Rb 12,1,19456
s_waitcnt lgkmcnt(8)
Mv 64,28,120
v_exp_f32_e32 v58, v120
v_mfma_f32_16x16x32_bf16 v[72:75], a[32:35], a[64:67], v[16:19]
Rb 16,1,24576
v_add_f32_e32 v14, v14, v57
v_mfma_f32_16x16x32_bf16 v[76:79], a[32:35], a[68:71], v[20:23]
v_exp_f32_e32 v24, v121
Mv 76,36,76
Rb 20,1,25600
v_add_f32_e32 v14, v14, v29
Mv 72,36,72
buffer_load_dwordx4 v2, s[20:23], s56 offen offset:2048 sc1 lds
Mv 72,40,80
Rb 24,1,26624
Cv 57,57,29
Mv 76,40,84
v_exp_f32_e32 v59, v122
Mv 76,44,92
Rb 28,1,27648
s_waitcnt lgkmcnt(8)
Mv 72,44,88
v_exp_f32_e32 v25, v123
Mv 72,48,96
Rb 32,1,20480
v_add_f32_e32 v14, v14, v58
Mv 76,48,100
v_exp_f32_e32 v60, v116
Mv 76,52,108
Rb 36,1,21504
v_add_f32_e32 v14, v14, v24
Mv 72,52,104
buffer_load_dwordx4 v2, s[20:23], s56 offen offset:3072 sc1 lds
Mv 72,56,112
Rb 40,1,22528
Cv 58,58,24
Mv 76,56,116
v_exp_f32_e32 v26, v117
Mv 76,60,124
Rb 44,1,23552
s_waitcnt lgkmcnt(8)
Mv 72,60,120
v_exp_f32_e32 v61, v118
v_mfma_f32_16x16x32_bf16 v[80:83], a[0:3], a[64:67], v[16:19]
Rb 48,1,28672
v_add_f32_e32 v14, v14, v59
v_mfma_f32_16x16x32_bf16 v[84:87], a[0:3], a[68:71], v[20:23]
v_exp_f32_e32 v27, v119
Mv 84,4,76
Rb 52,1,29696
)" R"(
s_add_u32 m0, s36, 102400
Mv 80,4,72
buffer_load_dwordx4 v3, s[20:23], s56 offen offset:0 sc1 lds
Mv 80,8,80
Rb 56,1,30720
v_add_f32_e32 v14, v14, v25
Mv 84,8,84
v_exp_f32_e32 v62, v124
Mv 84,12,92
Rb 60,1,31744
s_waitcnt lgkmcnt(8)
Mv 80,12,88
v_exp_f32_e32 v28, v125
Mv 80,16,96
v_exp_f32_e32 v63, v126
Mv 84,16,100
v_exp_f32_e32 v29, v127
Mv 84,20,108
Cv 59,59,25
v_add_f32_e32 v15, v15, v60
Mv 80,20,104
buffer_load_dwordx4 v3, s[20:23], s56 offen offset:1024 sc1 lds
Mv 80,24,112
v_add_f32_e32 v15, v15, v26
Cv 60,60,26
Mv 84,24,116
v_add_f32_e32 v15, v15, v61
v_add_f32_e32 v15, v15, v27
Mv 84,28,124
Cv 61,61,27
v_add_f32_e32 v15, v15, v62
Mv 80,28,120
s_waitcnt vmcnt(14) lgkmcnt(0)
s_barrier
v_mfma_f32_16x16x32_bf16 v[88:91], a[32:35], a[64:67], v[16:19]
Rb 0,8,0
v_add_f32_e32 v15, v15, v28
v_mfma_f32_16x16x32_bf16 v[92:95], a[32:35], a[68:71], v[20:23]
Rb 4,8,1024
Cv 62,62,28
Mv 92,36,76
Rb 8,8,2048
v_add_f32_e32 v15, v15, v63
Mv 88,36,72
Rb 12,8,3072
buffer_load_dwordx4 v3, s[20:23], s56 offen offset:2048 sc1 lds
Mv 88,40,80
Rb 16,8,4096
v_add_f32_e32 v15, v15, v29
Mv 92,40,84
Rb 20,8,5120
Cv 63,63,29
Mv 92,44,92
Rb 24,8,6144
v_max_f32_e32 v11, v14, v15
Mv 88,44,88
Rb 28,8,7168
v_cmp_nge_f32_e32 vcc, s62, v11
Mv 88,48,96
Rb 32,8,8192
s_add_u32 s55, s55, 0x10000
Mv 92,48,100
Rb 36,8,9216
Mv 92,52,108
Rb 40,8,10240
)" R"(
Mv 88,52,104
Rb 44,8,11264
buffer_load_dwordx4 v3, s[20:23], s56 offen offset:3072 sc1 lds
Mv 88,56,112
s_cmp_eq_u32 s57, 2
s_cselect_b32 s55, s59, s55
Mv 92,56,116
s_add_u32 s56, s56, 0x10000
Mv 92,60,124
s_waitcnt lgkmcnt(8)
s_cbranch_vccnz .Lzfx11
.Lzfr11:
Mv 88,60,120
v_add_f32_e32 v12, v12, v14
v_add_f32_e32 v13, v13, v15
Mp 128,0,48
Rb 48,8,12288
Mp 136,0,52
s_cmp_le_i32 s57, s58
s_cbranch_scc1 .Lzmk10
.Lzmr10:
Mp 140,4,52
Rb 52,8,13312
s_add_u32 m0, s36, 0
Mp 132,4,48
buffer_load_dwordx4 v2, s[20:23], s55 offen offset:0 sc1 lds
Mp 144,8,48
Rb 56,8,14336
Mp 152,8,52
v_exp_f32_e32 v32, v64
Mp 156,12,52
Rb 60,8,15360
s_waitcnt lgkmcnt(8)
Mp 148,12,48
v_exp_f32_e32 v24, v65
Mp 160,16,48
Rb 0,8,16384
v_add_f32_e32 v14, v32, v24
Mp 168,16,52
v_exp_f32_e32 v33, v66
Cv 32,32,24
Mp 172,20,52
Rb 4,8,17408
v_add_f32_e32 v14, v14, v33
Mp 164,20,48
buffer_load_dwordx4 v2, s[20:23], s55 offen offset:1024 sc1 lds
Mp 176,24,48
Rb 8,8,18432
Mp 184,24,52
v_exp_f32_e32 v25, v67
Mp 188,28,52
Rb 12,8,19456
s_waitcnt lgkmcnt(8)
Mp 180,28,48
v_exp_f32_e32 v34, v72
Mp 192,32,48
Rb 16,8,20480
v_add_f32_e32 v14, v14, v25
Mp 200,32,52
v_exp_f32_e32 v26, v73
Cv 33,33,25
Mp 204,36,52
Rb 20,8,21504
v_add_f32_e32 v14, v14, v34
)" R"(
Mp 196,36,48
buffer_load_dwordx4 v2, s[20:23], s55 offen offset:2048 sc1 lds
Mp 208,40,48
Rb 24,8,22528
v_add_f32_e32 v14, v14, v26
Mp 216,40,52
v_exp_f32_e32 v35, v74
Mp 220,44,52
Rb 28,8,23552
s_waitcnt lgkmcnt(8)
Mp 212,44,48
v_exp_f32_e32 v27, v75
Mp 224,48,48
Rb 32,8,24576
Cv 34,34,26
Mp 232,48,52
v_exp_f32_e32 v36, v68
v_add_f32_e32 v14, v14, v35
Mp 236,52,52
Rb 36,8,25600
v_add_f32_e32 v14, v14, v27
Mp 228,52,48
buffer_load_dwordx4 v2, s[20:23], s55 offen offset:3072 sc1 lds
Mp 240,56,48
Rb 40,8,26624
Cv 35,35,27
Mp 248,56,52
v_exp_f32_e32 v28, v69
Mp 252,60,52
Rb 44,8,27648
s_waitcnt lgkmcnt(8)
Mp 244,60,48
v_exp_f32_e32 v37, v70
Mp 128,0,56
Rb 48,8,28672
v_add_f32_e32 v15, v36, v28
Mp 136,0,60
v_exp_f32_e32 v29, v71
Cv 36,36,28
Mp 140,4,60
Rb 52,8,29696
s_add_u32 m0, s36, 4096
Mp 132,4,56
buffer_load_dwordx4 v3, s[20:23], s55 offen offset:0 sc1 lds
Mp 144,8,56
Rb 56,8,30720
v_add_f32_e32 v15, v15, v37
Mp 152,8,60
v_exp_f32_e32 v38, v76
Mp 156,12,60
Rb 60,8,31744
s_waitcnt lgkmcnt(8)
Mp 148,12,56
v_exp_f32_e32 v24, v77
Mp 160,16,56
v_exp_f32_e32 v39, v78
Mp 168,16,60
v_exp_f32_e32 v25, v79
v_add_f32_e32 v15, v15, v29
Mp 172,20,60
Cv 37,37,29
v_add_f32_e32 v15, v15, v38
Mp 164,20,56
buffer_load_dwordx4 v3, s[20:23], s55 offen offset:1024 sc1 lds
)" R"(
Mp 176,24,56
v_add_f32_e32 v15, v15, v24
Cv 38,38,24
Mp 184,24,60
v_add_f32_e32 v15, v15, v39
v_add_f32_e32 v15, v15, v25
Mp 188,28,60
Cv 39,39,25
s_sub_u32 s57, s57, 1
Mp 180,28,56
s_waitcnt vmcnt(14) lgkmcnt(0)
s_barrier
Mp 192,32,56
Rb 0,1,32768
v_bfe_u32 v11, v0, 2, 2
Mp 200,32,60
Rb 4,1,33792
v_lshrrev_b32 v4, 4, v0
v_xor_b32 v11, v11, v4
Mp 204,36,60
Rb 8,1,34816
v_bfe_u32 v4, v0, 1, 1
Mp 196,36,56
Rb 12,1,35840
buffer_load_dwordx4 v3, s[20:23], s55 offen offset:2048 sc1 lds
Mp 208,40,56
Rb 16,1,40960
v_lshl_or_b32 v11, v11, 1, v4
Mp 216,40,60
Rb 20,1,41984
v_and_b32 v4, 1, v0
Mp 220,44,60
Rb 24,1,43008
v_mul_u32_u24 v4, 0xffff, v4
Mp 212,44,56
Rb 28,1,44032
v_add_u32 v4, 1, v4
Mp 224,48,56
Rb 32,1,36864
v_mul_u32_u24 v4, 0x3f80, v4
Mp 232,48,60
Rb 36,1,37888
v_cmp_eq_u32_e32 vcc, 0, v11
v_cmp_eq_u32_e64 s[28:29], 1, v11
Mp 236,52,60
Rb 40,1,38912
v_accvgpr_write_b32 a192, 0
Mp 228,52,56
Rb 44,1,39936
buffer_load_dwordx4 v3, s[20:23], s55 offen offset:3072 sc1 lds
Mp 240,56,56
v_accvgpr_write_b32 a193, 0
v_accvgpr_write_b32 a196, 0
Mp 248,56,60
v_accvgpr_write_b32 a197, 0
v_cndmask_b32_e32 v11, 0, v4, vcc
Mp 252,60,60
v_cndmask_b32_e64 v4, 0, v4, s[28:29]
s_waitcnt lgkmcnt(8)
Mp 244,60,56
v_accvgpr_write_b32 a194, v11
v_accvgpr_write_b32 a195, v4
v_mfma_f32_16x16x32_bf16 v[96:99], a[0:3], a[64:67], v[16:19]
Rb 48,1,45056
v_mfma_f32_16x16x32_bf16 v[100:103], a[0:3], a[68:71], v[20:23]
)" R"(
v_exp_f32_e32 v40, v80
Mv 100,4,76
Rb 52,1,46080
s_add_u32 m0, s36, 65536
Mv 96,4,72
buffer_load_dwordx4 v2, s[20:23], s56 offen offset:0 sc1 lds
Mv 96,8,80
Rb 56,1,47104
v_add_f32_e32 v14, v14, v40
Mv 100,8,84
v_exp_f32_e32 v26, v81
Mv 100,12,92
Rb 60,1,48128
s_waitcnt lgkmcnt(8)
Mv 96,12,88
v_exp_f32_e32 v41, v82
Mv 96,16,96
Rb 0,1,49152
v_add_f32_e32 v14, v14, v26
Mv 100,16,100
v_exp_f32_e32 v27, v83
Mv 100,20,108
Rb 4,1,50176
Cv 40,40,26
Mv 96,20,104
buffer_load_dwordx4 v2, s[20:23], s56 offen offset:1024 sc1 lds
Mv 96,24,112
Rb 8,1,51200
v_add_f32_e32 v14, v14, v41
Mv 100,24,116
v_exp_f32_e32 v42, v88
Mv 100,28,124
Rb 12,1,52224
s_waitcnt lgkmcnt(8)
Mv 96,28,120
v_exp_f32_e32 v28, v89
v_mfma_f32_16x16x32_bf16 v[104:107], a[32:35], a[64:67], v[16:19]
Rb 16,1,57344
v_add_f32_e32 v14, v14, v27
v_mfma_f32_16x16x32_bf16 v[108:111], a[32:35], a[68:71], v[20:23]
v_exp_f32_e32 v43, v90
Mv 108,36,76
Rb 20,1,58368
Cv 41,41,27
Mv 104,36,72
buffer_load_dwordx4 v2, s[20:23], s56 offen offset:2048 sc1 lds
Mv 104,40,80
Rb 24,1,59392
buffer_load_dword v4, v7, s[20:23], s65 offen offset:0 sc0 sc1
Mv 108,40,84
v_exp_f32_e32 v29, v91
Mv 108,44,92
Rb 28,1,60416
s_waitcnt lgkmcnt(8)
Mv 104,44,88
v_exp_f32_e32 v44, v84
Mv 104,48,96
Rb 32,1,53248
v_add_f32_e32 v14, v14, v42
Mv 108,48,100
v_exp_f32_e32 v24, v85
Mv 108,52,108
Rb 36,1,54272
v_add_f32_e32 v14, v14, v28
)" R"(
Mv 104,52,104
buffer_load_dwordx4 v2, s[20:23], s56 offen offset:3072 sc1 lds
Mv 104,56,112
Rb 40,1,55296
Cv 42,42,28
Mv 108,56,116
v_exp_f32_e32 v45, v86
Mv 108,60,124
Rb 44,1,56320
s_waitcnt lgkmcnt(8)
Mv 104,60,120
v_exp_f32_e32 v25, v87
v_mfma_f32_16x16x32_bf16 v[112:115], a[0:3], a[64:67], v[16:19]
Rb 48,1,61440
v_add_f32_e32 v14, v14, v43
v_mfma_f32_16x16x32_bf16 v[116:119], a[0:3], a[68:71], v[20:23]
v_exp_f32_e32 v46, v92
Mv 116,4,76
Rb 52,1,62464
s_add_u32 m0, s36, 69632
Mv 112,4,72
buffer_load_dwordx4 v3, s[20:23], s56 offen offset:0 sc1 lds
Mv 112,8,80
Rb 56,1,63488
v_add_f32_e32 v14, v14, v29
Mv 116,8,84
v_exp_f32_e32 v26, v93
Mv 116,12,92
Rb 60,1,64512
s_waitcnt lgkmcnt(8)
Mv 112,12,88
v_exp_f32_e32 v47, v94
Mv 112,16,96
v_exp_f32_e32 v27, v95
Mv 116,16,100
Cv 43,43,29
v_add_f32_e32 v15, v15, v44
Mv 116,20,108
v_add_f32_e32 v15, v15, v24
Cv 44,44,24
Mv 112,20,104
buffer_load_dwordx4 v3, s[20:23], s56 offen offset:1024 sc1 lds
Mv 112,24,112
v_add_f32_e32 v15, v15, v45
v_add_f32_e32 v15, v15, v25
Mv 116,24,116
Cv 45,45,25
v_add_f32_e32 v15, v15, v46
Mv 116,28,124
v_add_f32_e32 v15, v15, v26
Cv 46,46,26
Mv 112,28,120
s_waitcnt vmcnt(3) lgkmcnt(0)
s_barrier
v_mfma_f32_16x16x32_bf16 v[120:123], a[32:35], a[64:67], v[16:19]
Rb 0,8,32768
v_add_f32_e32 v15, v15, v47
v_mfma_f32_16x16x32_bf16 v[124:127], a[32:35], a[68:71], v[20:23]
Rb 4,8,33792
v_add_f32_e32 v15, v15, v27
Mv 124,36,76
Rb 8,8,34816
Cv 47,47,27
Mv 120,36,72
Rb 12,8,35840
)" R"(
buffer_load_dwordx4 v3, s[20:23], s56 offen offset:2048 sc1 lds
Mv 120,40,80
Rb 16,8,36864
v_max_f32_e32 v11, v14, v15
Mv 124,40,84
Rb 20,8,37888
v_cmp_nge_f32_e32 vcc, s62, v11
Mv 124,44,92
Rb 24,8,38912
s_add_u32 s55, s55, 0x10000
Mv 120,44,88
Rb 28,8,39936
Mv 120,48,96
Rb 32,8,40960
Mv 124,48,100
Rb 36,8,41984
Mv 124,52,108
Rb 40,8,43008
Mv 120,52,104
Rb 44,8,44032
buffer_load_dwordx4 v3, s[20:23], s56 offen offset:3072 sc1 lds
Mv 120,56,112
s_cmp_eq_u32 s57, 2
s_cselect_b32 s55, s59, s55
Mv 124,56,116
s_add_u32 s56, s56, 0x10000
Mv 124,60,124
s_waitcnt lgkmcnt(8)
s_cbranch_vccnz .Lzfx12
.Lzfr12:
Mv 120,60,120
v_cmp_eq_u32_e32 vcc, s3, v4
s_andn2_b64 s[28:29], s[60:61], vcc
s_cbranch_scc1 .Lzsp22
.Lzok22:
Mp 128,0,32
Rb 48,8,45056
v_add_f32_e32 v12, v12, v14
Mp 136,0,36
v_add_f32_e32 v13, v13, v15
s_cmp_le_i32 s57, s58
s_cbranch_scc1 .Lzmk11
.Lzmr11:
Mp 140,4,36
Rb 52,8,46080
s_add_u32 m0, s36, 32768
buffer_load_dwordx4 v[64:67], v1, s[20:23], s66 offen sc0 sc1
Mp 132,4,32
buffer_load_dwordx4 v2, s[20:23], s55 offen offset:0 sc1 lds
Mp 144,8,32
Rb 56,8,47104
v_exp_f32_e32 v48, v96
Mp 152,8,36
buffer_load_dwordx4 a[128:131], v2, s[20:23], s64 offen offset:0 sc0 sc1
Mp 156,12,36
Rb 60,8,48128
s_waitcnt lgkmcnt(8)
Mp 148,12,32
v_exp_f32_e32 v28, v97
Mp 160,16,32
Rb 0,8,49152
buffer_load_dwordx4 a[132:135], v2, s[20:23], s64 offen offset:1024 sc0 sc1
Mp 168,16,36
v_exp_f32_e32 v49, v98
)" R"(
v_add_f32_e32 v14, v48, v28
Mp 172,20,36
Rb 4,8,50176
Cv 48,48,28
Mp 164,20,32
buffer_load_dwordx4 v2, s[20:23], s55 offen offset:1024 sc1 lds
Mp 176,24,32
Rb 8,8,51200
v_exp_f32_e32 v29, v99
Mp 184,24,36
buffer_load_dwordx4 a[136:139], v2, s[20:23], s64 offen offset:2048 sc0 sc1
Mp 188,28,36
Rb 12,8,52224
s_waitcnt lgkmcnt(8)
Mp 180,28,32
v_exp_f32_e32 v50, v104
v_add_f32_e32 v14, v14, v49
Mp 192,32,32
Rb 16,8,53248
buffer_load_dwordx4 a[140:143], v2, s[20:23], s64 offen offset:3072 sc0 sc1
Mp 200,32,36
v_exp_f32_e32 v24, v105
v_add_f32_e32 v14, v14, v29
Mp 204,36,36
Rb 20,8,54272
Cv 49,49,29
Mp 196,36,32
buffer_load_dwordx4 v2, s[20:23], s55 offen offset:2048 sc1 lds
Mp 208,40,32
Rb 24,8,55296
v_exp_f32_e32 v51, v106
Mp 216,40,36
buffer_load_dwordx4 a[144:147], v3, s[20:23], s64 offen offset:0 sc0 sc1
Mp 220,44,36
Rb 28,8,56320
s_waitcnt lgkmcnt(8)
Mp 212,44,32
v_exp_f32_e32 v25, v107
v_add_f32_e32 v14, v14, v50
Mp 224,48,32
Rb 32,8,57344
buffer_load_dwordx4 a[148:151], v3, s[20:23], s64 offen offset:1024 sc0 sc1
Mp 232,48,36
v_exp_f32_e32 v52, v100
v_add_f32_e32 v14, v14, v24
Mp 236,52,36
Rb 36,8,58368
Cv 50,50,24
Mp 228,52,32
buffer_load_dwordx4 v2, s[20:23], s55 offen offset:3072 sc1 lds
Mp 240,56,32
Rb 40,8,59392
v_exp_f32_e32 v26, v101
Mp 248,56,36
buffer_load_dwordx4 a[152:155], v3, s[20:23], s64 offen offset:2048 sc0 sc1
Mp 252,60,36
Rb 44,8,60416
s_waitcnt lgkmcnt(8)
Mp 244,60,32
v_exp_f32_e32 v53, v102
v_add_f32_e32 v14, v14, v51
Mp 128,0,40
Rb 48,8,61440
buffer_load_dwordx4 a[156:159], v3, s[20:23], s64 offen offset:3072 sc0 sc1
)" R"(
Mp 136,0,44
v_exp_f32_e32 v27, v103
v_add_f32_e32 v14, v14, v25
Mp 140,4,44
Rb 52,8,62464
s_add_u32 m0, s36, 36864
Mp 132,4,40
buffer_load_dwordx4 v3, s[20:23], s55 offen offset:0 sc1 lds
Mp 144,8,40
Rb 56,8,63488
v_exp_f32_e32 v54, v108
Mp 152,8,44
s_add_u32 s64, s64, 0x10000
buffer_load_dwordx4 a[160:163], v2, s[20:23], s64 offen offset:0 sc0 sc1
Mp 156,12,44
Rb 60,8,64512
s_waitcnt lgkmcnt(8)
Mp 148,12,40
v_exp_f32_e32 v28, v109
Cv 51,51,25
Mp 160,16,40
buffer_load_dwordx4 a[164:167], v2, s[20:23], s64 offen offset:1024 sc0 sc1
Mp 168,16,44
v_exp_f32_e32 v55, v110
v_add_f32_e32 v15, v52, v26
Mp 172,20,44
v_exp_f32_e32 v29, v111
Mp 164,20,40
buffer_load_dwordx4 v3, s[20:23], s55 offen offset:1024 sc1 lds
Mp 176,24,40
Cv 52,52,26
v_add_f32_e32 v15, v15, v53
v_add_f32_e32 v15, v15, v27
Mp 184,24,44
buffer_load_dwordx4 a[168:171], v2, s[20:23], s64 offen offset:2048 sc0 sc1
Mp 188,28,44
Cv 53,53,27
v_add_f32_e32 v15, v15, v54
Mp 180,28,40
v_add_f32_e32 v15, v15, v28
s_waitcnt vmcnt(18) lgkmcnt(0)
s_barrier
Mp 192,32,40
Rb 0,8,0
buffer_load_dwordx4 a[172:175], v2, s[20:23], s64 offen offset:3072 sc0 sc1
Mp 200,32,44
Rb 4,8,16384
Cv 54,54,28
v_add_f32_e32 v15, v15, v55
Mp 204,36,44
Rb 8,8,1024
v_add_f32_e32 v15, v15, v29
Mp 196,36,40
Rb 12,8,17408
buffer_load_dwordx4 v3, s[20:23], s55 offen offset:2048 sc1 lds
Mp 208,40,40
Rb 16,8,2048
Cv 55,55,29
s_sub_u32 s57, s57, 1
Mp 216,40,44
Rb 20,8,18432
buffer_load_dwordx4 a[176:179], v3, s[20:23], s64 offen offset:0 sc0 sc1
Mp 220,44,44
Rb 24,8,3072
Mp 212,44,40
)" R"(
Rb 28,8,19456
Mp 224,48,40
Rb 32,8,4096
buffer_load_dwordx4 a[180:183], v3, s[20:23], s64 offen offset:1024 sc0 sc1
Mp 232,48,44
Rb 36,8,20480
Mp 236,52,44
Rb 40,8,5120
Mp 228,52,40
Rb 44,8,21504
buffer_load_dwordx4 v3, s[20:23], s55 offen offset:3072 sc1 lds
Mp 240,56,40
buffer_load_dwordx4 a[188:191], v3, s[20:23], s64 offen offset:3072 sc0 sc1
Mp 248,56,44
buffer_load_dwordx4 a[184:187], v3, s[20:23], s64 offen offset:2048 sc0 sc1
Mp 252,60,44
Mp 244,60,40
s_waitcnt vmcnt(18)
v_cmp_neq_f32_e32 vcc, v16, v66
v_cmp_neq_f32_e64 s[28:29], v20, v67
s_or_b64 vcc, vcc, s[28:29]
s_cbranch_scc1 .LzMRG
.LzMRR:
Mv 128,194,128
v_exp_f32_e32 v56, v112
Mv 136,194,132
v_exp_f32_e32 v26, v113
v_add_f32_e32 v12, v12, v64
Mv 132,192,128
s_add_u32 m0, s36, 131072
s_add_u32 s40, s67, 0x20000
v_add_f32_e32 v13, v13, v65
Mv 140,192,132
buffer_load_dwordx4 v2, s[20:23], s40 offen offset:0 sc1 lds
Mv 144,194,136
v_exp_f32_e32 v57, v114
v_add_f32_e32 v14, v14, v56
Mv 152,194,140
v_exp_f32_e32 v27, v115
v_add_f32_e32 v14, v14, v26
Mv 148,192,136
Cv 56,56,26
s_waitcnt vmcnt(13)
Mv 156,192,140
buffer_load_dwordx4 v2, s[20:23], s40 offen offset:1024 sc1 lds
v_add_f32_e32 v14, v14, v57
Mv 160,194,144
v_exp_f32_e32 v58, v120
v_add_f32_e32 v14, v14, v27
Mv 168,194,148
v_exp_f32_e32 v28, v121
Mv 164,192,144
v_exp_f32_e32 v59, v122
Cv 57,57,27
Mv 172,192,148
buffer_load_dwordx4 v2, s[20:23], s40 offen offset:2048 sc1 lds
v_add_f32_e32 v14, v14, v58
Mv 176,194,152
v_exp_f32_e32 v29, v123
Mv 184,194,156
v_exp_f32_e32 v60, v116
v_add_f32_e32 v14, v14, v28
Mv 180,192,152
v_exp_f32_e32 v24, v117
)" R"(
s_waitcnt vmcnt(9)
Mv 188,192,156
buffer_load_dwordx4 v2, s[20:23], s40 offen offset:3072 sc1 lds
Cv 58,58,28
Mv 192,194,160
v_exp_f32_e32 v61, v118
Mv 200,194,164
v_exp_f32_e32 v25, v119
v_add_f32_e32 v14, v14, v59
Mv 196,192,160
s_add_u32 m0, s36, 135168
v_exp_f32_e32 v62, v124
Mv 204,192,164
buffer_load_dwordx4 v3, s[20:23], s40 offen offset:0 sc1 lds
Mv 208,194,168
v_exp_f32_e32 v26, v125
v_add_f32_e32 v14, v14, v29
Mv 216,194,172
v_exp_f32_e32 v63, v126
Cv 59,59,29
Mv 212,192,168
v_add_f32_e32 v15, v15, v60
s_waitcnt vmcnt(5)
Mv 220,192,172
buffer_load_dwordx4 v3, s[20:23], s40 offen offset:1024 sc1 lds
v_add_f32_e32 v15, v15, v24
Mv 224,194,176
v_exp_f32_e32 v27, v127
Cv 60,60,24
Mv 232,194,180
v_add_f32_e32 v15, v15, v61
v_add_f32_e32 v15, v15, v25
Mv 228,192,176
Cv 61,61,25
v_add_f32_e32 v15, v15, v62
v_add_f32_e32 v15, v15, v26
Mv 236,192,180
buffer_load_dwordx4 v3, s[20:23], s40 offen offset:2048 sc1 lds
Cv 62,62,26
Mv 240,194,184
v_add_f32_e32 v15, v15, v63
v_add_f32_e32 v15, v15, v27
Mv 248,194,188
s_add_u32 s40, s67, 0x30000
Cv 63,63,27
v_max_f32_e32 v11, v14, v15
Mv 244,192,184
s_add_u32 m0, s36, 98304
v_cmp_nge_f32_e32 vcc, s62, v11
s_waitcnt lgkmcnt(8)
Mv 252,192,188
buffer_load_dwordx4 v2, s[20:23], s40 offen offset:0 sc1 lds
s_cbranch_vccnz .Lzfx13
.Lzfr13:
Mp 128,0,48
s_mov_b32 s56, 0xc108000
buffer_load_dword v4, v7, s[20:23], s56 offen offset:4 sc0 sc1
Rb 48,8,6144
v_add_f32_e32 v12, v12, v14
Mp 136,0,52
v_add_f32_e32 v13, v13, v15
v_mov_b32 v24, v12
Mp 136,4,60
Rb 52,8,22528
)" R"(
v_permlane32_swap_b32_e32 v12, v24
Mp 128,4,56
buffer_load_dwordx4 v2, s[20:23], s40 offen offset:1024 sc1 lds
Mp 132,8,48
Rb 56,8,7168
v_add_f32_e32 v12, v12, v24
Mp 140,8,52
v_mov_b32 v24, v12
v_mov_b32 v25, v13
Mp 140,12,60
Rb 60,8,23552
s_waitcnt lgkmcnt(8)
Mp 132,12,56
buffer_load_dwordx4 v2, s[20:23], s40 offen offset:2048 sc1 lds
Mp 144,16,48
Rb 0,8,8192
v_permlane16_swap_b32_e32 v12, v24
Mp 152,16,52
v_add_f32_e32 v12, v12, v24
v_permlane32_swap_b32_e32 v13, v25
Mp 152,20,60
Rb 4,8,24576
v_rcp_f32_e32 v12, v12
Mp 144,20,56
buffer_load_dwordx4 v2, s[20:23], s40 offen offset:3072 sc1 lds
Mp 148,24,48
Rb 8,8,9216
v_add_f32_e32 v13, v13, v25
Mp 156,24,52
s_add_u32 m0, s36, 102400
v_mov_b32 v25, v13
Mp 156,28,60
Rb 12,8,25600
s_waitcnt lgkmcnt(8)
Mp 148,28,56
buffer_load_dwordx4 v3, s[20:23], s40 offen offset:0 sc1 lds
v_permlane16_swap_b32_e32 v13, v25
Mp 160,32,48
Rb 16,8,10240
v_add_f32_e32 v13, v13, v25
Mp 168,32,52
v_rcp_f32_e32 v13, v13
Mp 168,36,60
Rb 20,8,26624
v_mul_f32_e32 v128, v12, v128
Mp 160,36,56
buffer_load_dwordx4 v3, s[20:23], s40 offen offset:1024 sc1 lds
Mp 164,40,48
Rb 24,8,11264
v_mul_f32_e32 v129, v12, v129
Mp 172,40,52
Cv 128,128,129
v_mul_f32_e32 v130, v12, v130
v_mul_f32_e32 v131, v12, v131
Mp 172,44,60
Rb 28,8,27648
s_waitcnt lgkmcnt(8)
Mp 164,44,56
buffer_load_dwordx4 v3, s[20:23], s40 offen offset:2048 sc1 lds
Mp 176,48,48
Rb 32,8,12288
Cv 129,130,131
Mp 184,48,52
v_mul_f32_e32 v132, v12, v132
)" R"(
v_mul_f32_e32 v133, v12, v133
Mp 184,52,60
Rb 36,8,28672
Cv 130,132,133
v_mul_f32_e32 v134, v12, v134
Mp 176,52,56
v_mul_f32_e32 v135, v12, v135
Cv 131,134,135
Mp 180,56,48
Rb 40,8,13312
v_mul_f32_e32 v136, v13, v136
Mp 188,56,52
v_mul_f32_e32 v137, v13, v137
Cv 136,136,137
Mp 188,60,60
Rb 44,8,29696
s_waitcnt lgkmcnt(8)
Mp 180,60,56
v_mul_f32_e32 v138, v13, v138
v_mul_f32_e32 v139, v13, v139
Cv 137,138,139
Mp 192,0,48
Rb 48,8,14336
v_mul_f32_e32 v140, v13, v140
Mp 200,0,52
v_mul_f32_e32 v141, v13, v141
Cv 138,140,141
Mp 200,4,60
Rb 52,8,30720
v_mul_f32_e32 v142, v13, v142
Mp 192,4,56
v_mul_f32_e32 v143, v13, v143
Cv 139,142,143
Mp 196,8,48
Rb 56,8,15360
v_mul_f32_e32 v144, v12, v144
Mp 204,8,52
v_mul_f32_e32 v145, v12, v145
Cv 144,144,145
v_mul_f32_e32 v146, v12, v146
Mp 204,12,60
Rb 60,8,31744
s_waitcnt lgkmcnt(8)
Mp 196,12,56
v_mul_f32_e32 v147, v12, v147
Cv 145,146,147
Mp 208,16,48
Rb 0,1,0
v_mul_f32_e32 v148, v12, v148
Mp 216,16,52
v_mul_f32_e32 v149, v12, v149
Cv 146,148,149
Mp 216,20,60
Rb 4,1,1024
v_mul_f32_e32 v150, v12, v150
v_mul_f32_e32 v151, v12, v151
Mp 208,20,56
Cv 147,150,151
v_mul_f32_e32 v152, v13, v152
Mp 212,24,48
Rb 8,1,2048
v_mul_f32_e32 v153, v13, v153
Mp 220,24,52
Cv 152,152,153
)" R"(
v_mul_f32_e32 v154, v13, v154
Mp 220,28,60
Rb 12,1,3072
s_waitcnt vmcnt(6) lgkmcnt(4)
s_barrier
Mp 212,28,56
s_add_u32 s40, s67, 0x40000
v_mul_f32_e32 v155, v13, v155
Cv 153,154,155
v_cmp_eq_u32_e32 vcc, s3, v4
s_andn2_b64 s[28:29], 1, vcc
s_cbranch_scc1 .Lzsp25
.Lzok25:
Mp 224,32,48
Rb 16,1,4096
s_add_u32 m0, s36, 65536
buffer_load_dword v4, v7, s[20:23], s56 offen sc0 sc1
Mp 232,32,52
buffer_load_dwordx4 v2, s[20:23], s40 offen offset:0 sc1 lds
Mp 232,36,60
Rb 20,1,5120
v_mul_f32_e32 v156, v13, v156
Mp 224,36,56
v_mul_f32_e32 v157, v13, v157
Cv 154,156,157
Mp 228,40,48
Rb 24,1,6144
v_mul_f32_e32 v158, v13, v158
Mp 236,40,52
v_mul_f32_e32 v159, v13, v159
Cv 155,158,159
v_mul_f32_e32 v160, v12, v160
Mp 236,44,60
Rb 28,1,8192
Mp 228,44,56
buffer_load_dwordx4 v2, s[20:23], s40 offen offset:1024 sc1 lds
Mp 240,48,48
Rb 32,1,9216
v_mul_f32_e32 v161, v12, v161
Mp 248,48,52
Cv 160,160,161
v_mul_f32_e32 v162, v12, v162
Mp 248,52,60
Rb 36,1,10240
v_mul_f32_e32 v163, v12, v163
Cv 161,162,163
Mp 240,52,56
v_mul_f32_e32 v164, v12, v164
v_mul_f32_e32 v165, v12, v165
Mp 244,56,48
Rb 40,1,11264
Cv 162,164,165
Mp 252,56,52
buffer_load_dwordx4 v2, s[20:23], s40 offen offset:2048 sc1 lds
Mp 252,60,60
Rb 44,1,12288
s_waitcnt lgkmcnt(8)
Mp 244,60,56
v_mul_f32_e32 v166, v12, v166
v_mul_f32_e32 v167, v12, v167
Cv 163,166,167
v_mfma_f32_16x16x32_bf16 v[32:35], a[0:3], v[128:131], 0
Rb 48,1,13312
v_mul_f32_e32 v168, v13, v168
v_mfma_f32_16x16x32_bf16 v[40:43], a[0:3], v[136:139], 0
)" R"(
v_mul_f32_e32 v169, v13, v169
Cv 168,168,169
v_mfma_f32_16x16x32_bf16 v[44:47], a[4:7], v[136:139], 0
Rb 52,1,14336
v_mul_f32_e32 v170, v13, v170
v_mfma_f32_16x16x32_bf16 v[36:39], a[4:7], v[128:131], 0
buffer_load_dwordx4 v2, s[20:23], s40 offen offset:3072 sc1 lds
v_mfma_f32_16x16x32_bf16 v[48:51], a[8:11], v[128:131], 0
Rb 56,1,16384
v_mul_f32_e32 v171, v13, v171
v_mfma_f32_16x16x32_bf16 v[56:59], a[8:11], v[136:139], 0
Cv 169,170,171
v_mul_f32_e32 v172, v13, v172
v_mul_f32_e32 v173, v13, v173
v_mfma_f32_16x16x32_bf16 v[60:63], a[12:15], v[136:139], 0
Rb 60,1,17408
s_waitcnt lgkmcnt(8)
v_mfma_f32_16x16x32_bf16 v[52:55], a[12:15], v[128:131], 0
Cv 170,172,173
v_mul_f32_e32 v174, v13, v174
v_mfma_f32_16x16x32_bf16 v[64:67], a[16:19], v[128:131], 0
Rb 0,1,18432
s_add_u32 m0, s36, 69632
v_mfma_f32_16x16x32_bf16 v[72:75], a[16:19], v[136:139], 0
buffer_load_dwordx4 v3, s[20:23], s40 offen offset:0 sc1 lds
v_mfma_f32_16x16x32_bf16 v[76:79], a[20:23], v[136:139], 0
Rb 4,1,19456
v_mul_f32_e32 v175, v13, v175
Cv 171,174,175
v_mfma_f32_16x16x32_bf16 v[68:71], a[20:23], v[128:131], 0
v_mul_f32_e32 v176, v12, v176
v_mul_f32_e32 v177, v12, v177
v_mfma_f32_16x16x32_bf16 v[80:83], a[24:27], v[128:131], 0
Rb 8,1,20480
Cv 176,176,177
v_mfma_f32_16x16x32_bf16 v[88:91], a[24:27], v[136:139], 0
v_mul_f32_e32 v178, v12, v178
v_mul_f32_e32 v179, v12, v179
v_mfma_f32_16x16x32_bf16 v[92:95], a[28:31], v[136:139], 0
Rb 12,1,21504
s_waitcnt lgkmcnt(8)
v_mfma_f32_16x16x32_bf16 v[84:87], a[28:31], v[128:131], 0
buffer_load_dwordx4 v3, s[20:23], s40 offen offset:1024 sc1 lds
Cv 177,178,179
Mp 32,32,144
Rb 16,1,22528
v_mul_f32_e32 v180, v12, v180
Mp 40,32,152
v_mul_f32_e32 v181, v12, v181
Cv 178,180,181
Mp 44,36,152
Rb 20,1,24576
v_mul_f32_e32 v182, v12, v182
Mp 36,36,144
v_mul_f32_e32 v183, v12, v183
Cv 179,182,183
Mp 48,40,144
Rb 24,1,25600
v_mul_f32_e32 v184, v13, v184
Mp 56,40,152
buffer_load_dwordx4 v3, s[20:23], s40 offen offset:2048 sc1 lds
v_mul_f32_e32 v185, v13, v185
Mp 60,44,152
Rb 28,1,26624
)" R"(
s_waitcnt lgkmcnt(8)
Mp 52,44,144
Cv 184,184,185
v_mul_f32_e32 v186, v13, v186
Mp 64,48,144
Rb 32,1,27648
v_mul_f32_e32 v187, v13, v187
Mp 72,48,152
Cv 185,186,187
v_mul_f32_e32 v188, v13, v188
Mp 76,52,152
Rb 36,1,28672
v_mul_f32_e32 v189, v13, v189
Cv 186,188,189
Mp 68,52,144
v_mul_f32_e32 v190, v13, v190
v_mul_f32_e32 v191, v13, v191
Mp 80,56,144
Rb 40,1,29696
Cv 187,190,191
Mp 88,56,152
v_mul_f32_e32 v192, v12, v192
v_mul_f32_e32 v193, v12, v193
Mp 92,60,152
Rb 44,1,30720
s_waitcnt lgkmcnt(8)
Mp 84,60,144
Cv 192,192,193
v_mul_f32_e32 v194, v12, v194
v_mul_f32_e32 v195, v12, v195
Mp 32,0,160
Rb 48,1,32768
Cv 193,194,195
Mp 40,0,168
v_mul_f32_e32 v196, v12, v196
v_mul_f32_e32 v197, v12, v197
Mp 44,4,168
Rb 52,1,33792
Cv 194,196,197
Mp 36,4,160
v_mul_f32_e32 v198, v12, v198
v_mul_f32_e32 v199, v12, v199
Mp 48,8,160
Rb 56,1,34816
Cv 195,198,199
Mp 56,8,168
v_mul_f32_e32 v200, v13, v200
v_mul_f32_e32 v201, v13, v201
Cv 200,200,201
Mp 60,12,168
Rb 60,1,35840
s_waitcnt lgkmcnt(4)
s_barrier
Mp 52,12,160
s_add_u32 s40, s67, 0x50000
v_mul_f32_e32 v202, v13, v202
Mp 64,16,160
Rb 0,1,36864
s_add_u32 m0, s36, 0
Mp 72,16,168
buffer_load_dwordx4 v2, s[20:23], s40 offen offset:0 sc1 lds
Mp 76,20,168
Rb 4,1,37888
v_mul_f32_e32 v203, v13, v203
Cv 201,202,203
)" R"(
Mp 68,20,160
v_mul_f32_e32 v204, v13, v204
v_mul_f32_e32 v205, v13, v205
Mp 80,24,160
Rb 8,1,38912
Cv 202,204,205
Mp 88,24,168
v_mul_f32_e32 v206, v13, v206
v_mul_f32_e32 v207, v13, v207
Mp 92,28,168
Rb 12,1,40960
Mp 84,28,160
buffer_load_dwordx4 v2, s[20:23], s40 offen offset:1024 sc1 lds
Cv 203,206,207
Mp 32,32,176
Rb 16,1,41984
v_mul_f32_e32 v208, v12, v208
Mp 40,32,184
v_mul_f32_e32 v209, v12, v209
Cv 208,208,209
Mp 44,36,184
Rb 20,1,43008
v_mul_f32_e32 v210, v12, v210
Mp 36,36,176
v_mul_f32_e32 v211, v12, v211
Cv 209,210,211
Mp 48,40,176
Rb 24,1,44032
v_mul_f32_e32 v212, v12, v212
Mp 56,40,184
buffer_load_dwordx4 v2, s[20:23], s40 offen offset:2048 sc1 lds
v_mul_f32_e32 v213, v12, v213
Mp 60,44,184
Rb 28,1,45056
s_waitcnt lgkmcnt(8)
Mp 52,44,176
Cv 210,212,213
v_mul_f32_e32 v214, v12, v214
Mp 64,48,176
Rb 32,1,46080
v_mul_f32_e32 v215, v12, v215
Mp 72,48,184
Cv 211,214,215
v_mul_f32_e32 v216, v13, v216
Mp 76,52,184
Rb 36,1,47104
v_mul_f32_e32 v217, v13, v217
Cv 216,216,217
Mp 68,52,176
buffer_load_dwordx4 v2, s[20:23], s40 offen offset:3072 sc1 lds
Mp 80,56,176
Rb 40,1,49152
v_mul_f32_e32 v218, v13, v218
Mp 88,56,184
v_mul_f32_e32 v219, v13, v219
Cv 217,218,219
Mp 92,60,184
Rb 44,1,50176
s_waitcnt lgkmcnt(8)
Mp 84,60,176
v_mul_f32_e32 v220, v13, v220
v_mul_f32_e32 v221, v13, v221
Cv 218,220,221
Mp 32,0,192
)" R"(
Rb 48,1,51200
s_add_u32 m0, s36, 4096
Mp 40,0,200
buffer_load_dwordx4 v3, s[20:23], s40 offen offset:0 sc1 lds
Mp 44,4,200
Rb 52,1,52224
v_mul_f32_e32 v222, v13, v222
Mp 36,4,192
v_mul_f32_e32 v223, v13, v223
Cv 219,222,223
Mp 48,8,192
Rb 56,1,53248
v_mul_f32_e32 v224, v12, v224
Mp 56,8,200
v_mul_f32_e32 v225, v12, v225
Cv 224,224,225
v_mul_f32_e32 v226, v12, v226
Mp 60,12,200
Rb 60,1,54272
s_waitcnt lgkmcnt(8)
Mp 52,12,192
buffer_load_dwordx4 v3, s[20:23], s40 offen offset:1024 sc1 lds
Mp 64,16,192
Rb 0,1,55296
v_mul_f32_e32 v227, v12, v227
Mp 72,16,200
Cv 225,226,227
v_mul_f32_e32 v228, v12, v228
Mp 76,20,200
Rb 4,1,57344
v_mul_f32_e32 v229, v12, v229
Cv 226,228,229
Mp 68,20,192
v_mul_f32_e32 v230, v12, v230
v_mul_f32_e32 v231, v12, v231
Mp 80,24,192
Rb 8,1,58368
Cv 227,230,231
Mp 88,24,200
buffer_load_dwordx4 v3, s[20:23], s40 offen offset:2048 sc1 lds
Mp 92,28,200
Rb 12,1,59392
s_waitcnt lgkmcnt(8)
Mp 84,28,192
v_mul_f32_e32 v232, v13, v232
v_mul_f32_e32 v233, v13, v233
Cv 232,232,233
Mp 32,32,208
Rb 16,1,60416
v_mul_f32_e32 v234, v13, v234
Mp 40,32,216
v_mul_f32_e32 v235, v13, v235
Cv 233,234,235
Mp 44,36,216
Rb 20,1,61440
v_mul_f32_e32 v236, v13, v236
Mp 36,36,208
v_mul_f32_e32 v237, v13, v237
Cv 234,236,237
Mp 48,40,208
Rb 24,1,62464
v_mul_f32_e32 v238, v13, v238
Mp 56,40,216
v_mul_f32_e32 v239, v13, v239
)" R"(
Cv 235,238,239
v_mul_f32_e32 v240, v12, v240
Mp 60,44,216
Rb 28,1,63488
s_waitcnt lgkmcnt(8)
Mp 52,44,208
v_mul_f32_e32 v241, v12, v241
Cv 240,240,241
Mp 64,48,208
Rb 32,10,0
v_mul_f32_e32 v242, v12, v242
Mp 72,48,216
v_mul_f32_e32 v243, v12, v243
Cv 241,242,243
Mp 76,52,216
Rb 36,10,1024
v_mul_f32_e32 v244, v12, v244
v_mul_f32_e32 v245, v12, v245
Mp 68,52,208
Cv 242,244,245
v_mul_f32_e32 v246, v12, v246
Mp 80,56,208
Rb 40,10,2048
v_mul_f32_e32 v247, v12, v247
Mp 88,56,216
Cv 243,246,247
v_mul_f32_e32 v248, v13, v248
Mp 92,60,216
Rb 44,10,3072
s_waitcnt vmcnt(14) lgkmcnt(4)
s_barrier
Mp 84,60,208
s_add_u32 s40, s67, 0x60000
v_mul_f32_e32 v249, v13, v249
Cv 248,248,249
v_readfirstlane_b32 s63, v4
Mp 32,0,224
Rb 48,10,4096
s_add_u32 m0, s36, 32768
Mp 40,0,232
buffer_load_dwordx4 v2, s[20:23], s40 offen offset:0 sc1 lds
Mp 44,4,232
Rb 52,10,5120
v_mul_f32_e32 v250, v13, v250
Mp 36,4,224
v_mul_f32_e32 v251, v13, v251
Cv 249,250,251
Mp 48,8,224
Rb 56,10,6144
v_mul_f32_e32 v252, v13, v252
Mp 56,8,232
v_mul_f32_e32 v253, v13, v253
Cv 250,252,253
v_mul_f32_e32 v254, v13, v254
Mp 60,12,232
Rb 60,10,8192
Mp 52,12,224
buffer_load_dwordx4 v2, s[20:23], s40 offen offset:1024 sc1 lds
Mp 64,16,224
Rb 0,10,9216
v_mul_f32_e32 v255, v13, v255
Mp 72,16,232
Cv 251,254,255
s_lshl4_add_u32 s28, s34, s35
Mp 76,20,232
)" R"(
Rb 4,10,10240
s_lshl_b32 s64, s28, 17
Mp 68,20,224
s_add_u32 s64, s64, 0x4100000
s_xor_b32 s66, s64, 0x202000
Mp 80,24,224
Rb 8,10,11264
s_sub_u32 s66, s66, s84
Mp 88,24,232
buffer_load_dwordx4 v2, s[20:23], s40 offen offset:2048 sc1 lds
Mp 92,28,232
Rb 12,10,12288
s_waitcnt lgkmcnt(8)
Mp 84,28,224
s_sub_u32 s66, s66, s84
s_lshl2_add_u32 s65, s2, s33
Mp 32,32,240
Rb 16,10,13312
s_lshl_b32 s65, s65, 7
Mp 40,32,248
s_add_u32 s65, s65, 0xc1c0000
s_xor_b32 s52, s65, 4096
Mp 44,36,248
Rb 20,10,14336
Mp 36,36,240
buffer_load_dwordx4 v2, s[20:23], s40 offen offset:3072 sc1 lds
Mp 48,40,240
Rb 24,10,16384
Mp 56,40,248
Mp 60,44,248
Rb 28,10,17408
s_waitcnt lgkmcnt(8)
Mp 52,44,240
Mp 64,48,240
Rb 32,10,18432
s_add_u32 m0, s36, 36864
Mp 72,48,248
buffer_load_dwordx4 v3, s[20:23], s40 offen offset:0 sc1 lds
Mp 76,52,248
Rb 36,10,19456
Mp 68,52,240
Mp 80,56,240
Rb 40,10,20480
Mp 88,56,248
Mp 92,60,248
Rb 44,10,21504
s_waitcnt lgkmcnt(8)
Mp 84,60,240
buffer_load_dwordx4 v3, s[20:23], s40 offen offset:1024 sc1 lds
v_mfma_f32_16x16x32_bf16 v[96:99], a[0:3], v[128:131], 0
Rb 48,10,22528
v_mfma_f32_16x16x32_bf16 v[104:107], a[0:3], v[136:139], 0
Mp 104,4,152
Rb 52,10,24576
Mp 96,4,144
Cv 32,32,33
Cv 33,34,35
Mp 96,8,160
Rb 56,10,25600
Cv 34,36,37
Mp 104,8,168
buffer_load_dwordx4 v3, s[20:23], s40 offen offset:2048 sc1 lds
Mp 104,12,184
Rb 60,10,26624
)" R"(
s_waitcnt lgkmcnt(8)
Mp 96,12,176
Cv 35,38,39
Mp 96,16,192
Rb 0,10,27648
Mp 104,16,200
buffer_store_dwordx4 v[32:35], v2, s[20:23], s64 offen offset:0 sc0 sc1
Mp 104,20,216
Rb 4,10,28672
Cv 40,40,41
Mp 96,20,208
Cv 41,42,43
Cv 42,44,45
Mp 96,24,224
Rb 8,10,29696
Cv 43,46,47
Mp 104,24,232
buffer_store_dwordx4 v[40:43], v2, s[20:23], s64 offen offset:1024 sc0 sc1
Mp 104,28,248
Rb 12,10,30720
s_waitcnt lgkmcnt(8)
Mp 96,28,240
Cv 48,48,49
Cv 49,50,51
v_mfma_f32_16x16x32_bf16 v[100:103], a[32:35], v[128:131], 0
Rb 16,8,32768
Cv 50,52,53
v_mfma_f32_16x16x32_bf16 v[108:111], a[32:35], v[136:139], 0
Cv 51,54,55
Mp 108,36,152
Rb 20,8,33792
Mp 100,36,144
buffer_store_dwordx4 v[48:51], v2, s[20:23], s64 offen offset:2048 sc0 sc1
Mp 100,40,160
Rb 24,8,34816
Mp 108,40,168
Cv 56,56,57
Cv 57,58,59
Mp 108,44,184
Rb 28,8,35840
s_waitcnt vmcnt(17) lgkmcnt(4)
s_barrier
Mp 100,44,176
s_add_u32 s40, s67, 0x70000
Cv 58,60,61
Mp 100,48,192
Rb 32,8,36864
s_add_u32 m0, s36, 131072
Mp 108,48,200
buffer_load_dwordx4 v2, s[20:23], s40 offen offset:0 sc1 lds
Mp 108,52,216
Rb 36,8,37888
Cv 59,62,63
Mp 100,52,208
buffer_store_dwordx4 v[56:59], v2, s[20:23], s64 offen offset:3072 sc0 sc1
Mp 100,56,224
Rb 40,8,38912
Cv 64,64,65
Mp 108,56,232
Cv 65,66,67
Cv 66,68,69
Mp 108,60,248
Rb 44,8,40960
Mp 100,60,240
buffer_load_dwordx4 v2, s[20:23], s40 offen offset:1024 sc1 lds
)" R"(
v_mfma_f32_16x16x32_bf16 v[112:115], a[0:3], v[128:131], 0
Rb 48,8,41984
Cv 67,70,71
v_mfma_f32_16x16x32_bf16 v[120:123], a[0:3], v[136:139], 0
buffer_store_dwordx4 v[64:67], v3, s[20:23], s64 offen offset:0 sc0 sc1
Mp 120,4,152
Rb 52,8,43008
Cv 72,72,73
Mp 112,4,144
Cv 73,74,75
Cv 74,76,77
Mp 112,8,160
Rb 56,8,44032
Cv 75,78,79
Mp 120,8,168
buffer_load_dwordx4 v2, s[20:23], s40 offen offset:2048 sc1 lds
Mp 120,12,184
Rb 60,8,45056
s_waitcnt lgkmcnt(8)
Mp 112,12,176
buffer_store_dwordx4 v[72:75], v3, s[20:23], s64 offen offset:1024 sc0 sc1
Mp 112,16,192
Rb 0,8,46080
Cv 80,80,81
Mp 120,16,200
Cv 81,82,83
Cv 82,84,85
Mp 120,20,216
Rb 4,8,47104
Cv 83,86,87
Mp 112,20,208
buffer_load_dwordx4 v2, s[20:23], s40 offen offset:3072 sc1 lds
Mp 112,24,224
Rb 8,8,49152
Mp 120,24,232
buffer_store_dwordx4 v[80:83], v3, s[20:23], s64 offen offset:2048 sc0 sc1
Mp 120,28,248
Rb 12,8,50176
s_waitcnt lgkmcnt(8)
Mp 112,28,240
Cv 88,88,89
Cv 89,90,91
v_mfma_f32_16x16x32_bf16 v[116:119], a[32:35], v[128:131], 0
Rb 16,8,51200
s_add_u32 m0, s36, 135168
v_mfma_f32_16x16x32_bf16 v[124:127], a[32:35], v[136:139], 0
buffer_load_dwordx4 v3, s[20:23], s40 offen offset:0 sc1 lds
Mp 124,36,152
Rb 20,8,52224
Cv 90,92,93
Mp 116,36,144
Cv 91,94,95
Mp 116,40,160
Rb 24,8,53248
buffer_store_dwordx4 v[88:91], v3, s[20:23], s64 offen offset:3072 sc0 sc1
Mp 124,40,168
Cv 96,96,97
Cv 97,98,99
Mp 124,44,184
Rb 28,8,54272
s_waitcnt lgkmcnt(8)
Mp 116,44,176
buffer_load_dwordx4 v3, s[20:23], s40 offen offset:1024 sc1 lds
Mp 116,48,192
)" R"(
Rb 32,8,55296
Cv 98,100,101
Mp 124,48,200
Cv 99,102,103
Mp 124,52,216
Rb 36,8,57344
Cv 104,104,105
Mp 116,52,208
s_add_u32 s64, s64, 0x10000
buffer_store_dwordx4 v[96:99], v2, s[20:23], s64 offen offset:0 sc0 sc1
Mp 116,56,224
Rb 40,8,58368
Cv 105,106,107
Mp 124,56,232
buffer_load_dwordx4 v3, s[20:23], s40 offen offset:2048 sc1 lds
Mp 124,60,248
Rb 44,8,59392
s_waitcnt lgkmcnt(8)
Mp 116,60,240
Cv 106,108,109
Cv 107,110,111
v_mfma_f32_16x16x32_bf16 v[32:35], a[0:3], v[128:131], 0
Rb 48,8,60416
s_lshl_b32 s55, s85, 7
v_mfma_f32_16x16x32_bf16 v[40:43], a[0:3], v[136:139], 0
buffer_store_dwordx4 v[104:107], v2, s[20:23], s64 offen offset:1024 sc0 sc1
Mp 40,4,152
Rb 52,8,61440
s_add_u32 s55, s55, 0xc108c00
Mp 32,4,144
s_cmp_lg_u32 s33, 0
s_cmov_b32 s55, 0x40000000
Mp 32,8,160
Rb 56,8,62464
Cv 112,112,113
Mp 40,8,168
Cv 113,114,115
Cv 114,116,117
Mp 40,12,184
Rb 60,8,63488
s_waitcnt lgkmcnt(8)
Mp 32,12,176
Cv 115,118,119
v_mov_b32 v11, 1
Mp 32,16,192
Rb 0,8,0
v_add_u32 v9, s36, v8
Mp 40,16,200
buffer_store_dwordx4 v[112:115], v2, s[20:23], s64 offen offset:2048 sc0 sc1
Mp 40,20,216
Rb 4,8,1024
Mp 32,20,208
Mp 32,24,224
Rb 8,8,2048
Cv 120,120,121
Mp 40,24,232
Cv 121,122,123
Cv 122,124,125
Mp 40,28,248
Rb 12,8,3072
s_waitcnt vmcnt(5) lgkmcnt(8)
s_barrier
buffer_store_dword v6, v7, s[20:23], s65 offen offset:0 sc0 sc1
Mp 32,28,240
Cv 123,126,127
)" R"(
v_mfma_f32_16x16x32_bf16 v[36:39], a[32:35], v[128:131], 0
Rb 16,8,4096
v_mfma_f32_16x16x32_bf16 v[44:47], a[32:35], v[136:139], 0
buffer_store_dwordx4 v[120:123], v2, s[20:23], s64 offen offset:3072 sc0 sc1
Mp 44,36,152
Rb 20,8,5120
Mp 36,36,144
buffer_atomic_add v11, v7, s[20:23], s55 offen sc0 sc1
Mp 36,40,160
Rb 24,8,6144
Mp 44,40,168
Mp 44,44,184
Rb 28,8,8192
s_waitcnt lgkmcnt(8)
Mp 36,44,176
Mp 36,48,192
Rb 32,8,9216
Mp 44,48,200
Mp 44,52,216
Rb 36,8,10240
Mp 36,52,208
Mp 36,56,224
Rb 40,8,11264
Mp 44,56,232
Mp 44,60,248
Rb 44,8,12288
s_waitcnt lgkmcnt(8)
Mp 36,60,240
buffer_load_dword v4, v7, s[20:23], s52 offen offset:0 sc0 sc1
v_mfma_f32_16x16x32_bf16 v[48:51], a[0:3], v[128:131], 0
Rb 48,8,13312
v_mfma_f32_16x16x32_bf16 v[56:59], a[0:3], v[136:139], 0
Mp 56,4,152
Rb 52,8,14336
Mp 48,4,144
Mp 48,8,160
Rb 56,8,16384
Cv 32,32,33
Mp 56,8,168
Cv 33,34,35
Cv 34,36,37
Mp 56,12,184
Rb 60,8,17408
s_waitcnt lgkmcnt(8)
Mp 48,12,176
Cv 35,38,39
Mp 48,16,192
Rb 0,8,18432
Mp 56,16,200
buffer_store_dwordx4 v[32:35], v3, s[20:23], s64 offen offset:0 sc0 sc1
Mp 56,20,216
Rb 4,8,19456
Mp 48,20,208
Mp 48,24,224
Rb 8,8,20480
Cv 40,40,41
Mp 56,24,232
Cv 41,42,43
Cv 42,44,45
Mp 56,28,248
Rb 12,8,21504
s_waitcnt lgkmcnt(8)
Mp 48,28,240
Cv 43,46,47
)" R"(
v_mfma_f32_16x16x32_bf16 v[52:55], a[32:35], v[128:131], 0
Rb 16,8,22528
v_mfma_f32_16x16x32_bf16 v[60:63], a[32:35], v[136:139], 0
buffer_store_dwordx4 v[40:43], v3, s[20:23], s64 offen offset:1024 sc0 sc1
Mp 60,36,152
Rb 20,8,24576
Mp 52,36,144
Mp 52,40,160
Rb 24,8,25600
Mp 60,40,168
Mp 60,44,184
Rb 28,8,26624
s_waitcnt vmcnt(2) lgkmcnt(8)
v_cmp_eq_u32_e32 vcc, s3, v4
s_andn2_b64 s[28:29], 1, vcc
s_cbranch_scc1 .Lzsp23
.Lzok23:
Mp 52,44,176
buffer_load_dwordx4 a[64:67], v2, s[20:23], s66 offen offset:0 sc0 sc1
Mp 52,48,192
Rb 32,8,27648
Mp 60,48,200
Mp 60,52,216
Rb 36,8,28672
Mp 52,52,208
buffer_load_dwordx4 a[68:71], v2, s[20:23], s66 offen offset:1024 sc0 sc1
Mp 52,56,224
Rb 40,8,29696
Mp 60,56,232
Mp 60,60,248
Rb 44,8,30720
s_waitcnt lgkmcnt(8)
Mp 52,60,240
buffer_load_dwordx4 a[72:75], v2, s[20:23], s66 offen offset:2048 sc0 sc1
v_mfma_f32_16x16x32_bf16 v[64:67], a[0:3], v[128:131], 0
Rb 48,1,0
v_mfma_f32_16x16x32_bf16 v[72:75], a[0:3], v[136:139], 0
Mp 72,4,152
Rb 52,1,1024
Mp 64,4,144
buffer_load_dwordx4 a[76:79], v2, s[20:23], s66 offen offset:3072 sc0 sc1
Mp 64,8,160
Rb 56,1,2048
Mp 72,8,168
Mp 72,12,184
Rb 60,1,3072
s_waitcnt vmcnt(4) lgkmcnt(8)
s_barrier
buffer_store_dword v6, v7, s[20:23], s65 offen offset:4 sc0 sc1
Mp 64,12,176
buffer_load_dwordx4 a[80:83], v3, s[20:23], s66 offen offset:0 sc0 sc1
Mp 64,16,192
Rb 0,1,4096
Mp 72,16,200
Mp 72,20,216
Rb 4,1,5120
Mp 64,20,208
buffer_load_dwordx4 a[84:87], v3, s[20:23], s66 offen offset:1024 sc0 sc1
Mp 64,24,224
Rb 8,1,6144
Mp 72,24,232
Mp 72,28,248
Rb 12,1,8192
s_waitcnt lgkmcnt(8)
Mp 64,28,240
)" R"(
buffer_load_dwordx4 a[88:91], v3, s[20:23], s66 offen offset:2048 sc0 sc1
v_mfma_f32_16x16x32_bf16 v[68:71], a[32:35], v[128:131], 0
Rb 16,1,9216
v_mfma_f32_16x16x32_bf16 v[76:79], a[32:35], v[136:139], 0
Mp 76,36,152
Rb 20,1,10240
Mp 68,36,144
buffer_load_dwordx4 a[92:95], v3, s[20:23], s66 offen offset:3072 sc0 sc1
Mp 68,40,160
Rb 24,1,11264
Mp 76,40,168
Mp 76,44,184
Rb 28,1,12288
s_waitcnt lgkmcnt(8)
Mp 68,44,176
buffer_load_dword v4, v7, s[20:23], s52 offen offset:4 sc0 sc1
Mp 68,48,192
Rb 32,1,13312
Mp 76,48,200
Mp 76,52,216
Rb 36,1,14336
Mp 68,52,208
Mp 68,56,224
Rb 40,1,16384
Mp 76,56,232
Mp 76,60,248
Rb 44,1,17408
s_waitcnt vmcnt(6)
Mp 68,60,240
Mv 48,194,64
Mv 56,194,68
Mv 52,192,64
Mv 60,192,68
Mv 64,194,72
Mv 72,194,76
Mv 68,192,72
s_waitcnt lgkmcnt(8)
Mv 76,192,76
v_mfma_f32_16x16x32_bf16 v[80:83], a[0:3], v[128:131], 0
Rb 48,1,18432
Cv 48,48,49
v_mfma_f32_16x16x32_bf16 v[88:91], a[0:3], v[136:139], 0
Cv 49,50,51
Cv 50,52,53
Mp 88,4,152
Rb 52,1,19456
Cv 51,54,55
Mp 80,4,144
s_add_u32 s29, s73, 0x0
buffer_store_dwordx4 v[48:51], v1, s[20:23], s29 offen sc0 sc1
Mp 80,8,160
Rb 56,1,20480
Mp 88,8,168
Mp 88,12,184
Rb 60,1,21504
s_waitcnt vmcnt(1) lgkmcnt(8)
v_cmp_eq_u32_e32 vcc, s3, v4
s_andn2_b64 s[28:29], 1, vcc
s_cbranch_scc1 .Lzsp24
.Lzok24:
Mp 80,12,176
s_add_u32 s66, s66, 0x10000
buffer_load_dwordx4 a[96:99], v2, s[20:23], s66 offen offset:0 sc0 sc1
Mp 80,16,192
)" R"(
Rb 0,1,22528
Cv 56,56,57
Mp 88,16,200
Cv 57,58,59
Cv 58,60,61
Mp 88,20,216
Rb 4,1,24576
Cv 59,62,63
Mp 80,20,208
buffer_load_dwordx4 a[100:103], v2, s[20:23], s66 offen offset:1024 sc0 sc1
Mp 80,24,224
Rb 8,1,25600
s_mov_b32 s8, s74
Mp 88,24,232
s_add_u32 s29, s73, 0x400
buffer_store_dwordx4 v[56:59], v1, s[20:23], s29 offen sc0 sc1
Mp 88,28,248
Rb 12,1,26624
s_waitcnt lgkmcnt(8)
Mp 80,28,240
buffer_load_dwordx4 a[104:107], v2, s[20:23], s66 offen offset:2048 sc0 sc1
v_mfma_f32_16x16x32_bf16 v[84:87], a[32:35], v[128:131], 0
Rb 16,1,27648
Cv 64,64,65
v_mfma_f32_16x16x32_bf16 v[92:95], a[32:35], v[136:139], 0
Cv 65,66,67
Cv 66,68,69
Mp 92,36,152
Rb 20,1,28672
Cv 67,70,71
Mp 84,36,144
buffer_load_dwordx4 a[108:111], v2, s[20:23], s66 offen offset:3072 sc0 sc1
Mp 84,40,160
Rb 24,1,29696
s_mov_b32 s9, s75
Mp 92,40,168
s_add_u32 s29, s73, 0x800
buffer_store_dwordx4 v[64:67], v1, s[20:23], s29 offen sc0 sc1
Mp 92,44,184
Rb 28,1,30720
s_waitcnt lgkmcnt(8)
Mp 84,44,176
buffer_load_dwordx4 a[112:115], v3, s[20:23], s66 offen offset:0 sc0 sc1
ds_write_b32 v9, v11 offset:39936
Mp 84,48,192
Rb 32,1,32768
Cv 72,72,73
Mp 92,48,200
Cv 73,74,75
Cv 74,76,77
Mp 92,52,216
Rb 36,1,33792
Cv 75,78,79
Mp 84,52,208
buffer_load_dwordx4 a[116:119], v3, s[20:23], s66 offen offset:1024 sc0 sc1
Mp 84,56,224
Rb 40,1,34816
s_mov_b32 s10, 0x1c0000
Mp 92,56,232
s_add_u32 s29, s73, 0xc00
buffer_store_dwordx4 v[72:75], v1, s[20:23], s29 offen sc0 sc1
Mp 92,60,248
Rb 44,1,35840
s_waitcnt lgkmcnt(3)
s_barrier
)" R"(
Mp 84,60,240
ds_read_b32 v11, v8 offset:39936
s_mov_b32 s11, 0x20000
Mv 80,194,80
v_and_b32 v174, 1, v0
v_mul_u32_u24 v174, 0xffff, v174
Mv 88,194,84
v_add_u32 v174, 1, v174
v_mul_u32_u24 v174, 0x3f80, v174
Mv 84,192,80
v_bfe_u32 v173, v0, 1, 3
v_cmp_eq_u32_e32 vcc, 0, v173
Mv 92,192,84
s_nop 0
v_cndmask_b32_e32 v132, 0, v174, vcc
v_cmp_eq_u32_e32 vcc, 1, v173
v_mfma_f32_16x16x32_bf16 v[96:99], a[0:3], v[128:131], 0
Rb 48,1,36864
v_cndmask_b32_e32 v133, 0, v174, vcc
v_mfma_f32_16x16x32_bf16 v[104:107], a[0:3], v[136:139], 0
v_cmp_eq_u32_e32 vcc, 2, v173
v_and_b32 v167, 0xf0, v1
Mp 104,4,152
Rb 52,1,37888
s_waitcnt lgkmcnt(2)
Mp 96,4,144
v_readfirstlane_b32 s82, v11
v_cndmask_b32_e32 v134, 0, v174, vcc
Mp 96,8,160
Rb 56,1,38912
Cv 80,80,81
Mp 104,8,168
Cv 81,82,83
Cv 82,84,85
Mp 104,12,184
Rb 60,1,40960
Cv 83,86,87
Mp 96,12,176
s_add_u32 s29, s73, 0x1000
buffer_store_dwordx4 v[80:83], v1, s[20:23], s29 offen sc0 sc1
Mp 96,16,192
Rb 0,1,41984
v_cmp_eq_u32_e32 vcc, 3, v173
Mp 104,16,200
s_nop 0
v_cndmask_b32_e32 v135, 0, v174, vcc
v_cmp_eq_u32_e32 vcc, 4, v173
Mp 104,20,216
Rb 4,1,43008
Mp 96,20,208
Cv 88,88,89
Cv 89,90,91
Mp 96,24,224
Rb 8,1,44032
Cv 90,92,93
Mp 104,24,232
Cv 91,94,95
v_cndmask_b32_e32 v140, 0, v174, vcc
Mp 104,28,248
Rb 12,1,45056
v_cmp_eq_u32_e32 vcc, 5, v173
Mp 96,28,240
s_add_u32 s29, s73, 0x1400
buffer_store_dwordx4 v[88:91], v1, s[20:23], s29 offen sc0 sc1
)" R"(
v_mfma_f32_16x16x32_bf16 v[100:103], a[32:35], v[128:131], 0
Rb 16,1,46080
v_cndmask_b32_e32 v141, 0, v174, vcc
v_mfma_f32_16x16x32_bf16 v[108:111], a[32:35], v[136:139], 0
v_cmp_eq_u32_e32 vcc, 6, v173
v_mul_u32_u24 v166, 56, v167
Mp 108,36,152
Rb 20,1,47104
s_waitcnt lgkmcnt(8)
Mp 100,36,144
v_cndmask_b32_e32 v142, 0, v174, vcc
v_cmp_eq_u32_e32 vcc, 7, v173
Mp 100,40,160
Rb 24,1,49152
v_cndmask_b32_e32 v143, 0, v174, vcc
Mp 108,40,168
v_lshrrev_b32 v173, 4, v0
v_lshl_or_b32 v164, v173, 16, v167
Mp 108,44,184
Rb 28,1,50176
v_lshl_add_u32 v166, v173, 3, v166
Mp 100,44,176
v_and_b32 v174, 7, v0
v_and_b32 v165, 0x80, v1
Mp 100,48,192
Rb 32,1,51200
v_lshl_or_b32 v165, v174, 8, v165
Mp 108,48,200
s_mov_b32 s93, 0x1111
Mp 108,52,216
Rb 36,1,52224
s_waitcnt lgkmcnt(10)
Mp 100,52,208
Mp 100,56,224
Rb 40,1,53248
Mp 108,56,232
Mp 108,60,248
Rb 44,1,54272
Mp 100,60,240
Mv 96,194,88
Mv 104,194,92
Mv 100,192,88
s_waitcnt lgkmcnt(8)
Mv 108,192,92
v_mfma_f32_16x16x32_bf16 v[112:115], a[0:3], v[128:131], 0
Rb 48,1,55296
v_mfma_f32_16x16x32_bf16 v[120:123], a[0:3], v[136:139], 0
Mp 120,4,152
Rb 52,1,57344
Mp 112,4,144
Mp 112,8,160
Rb 56,1,58368
Cv 96,96,97
Mp 120,8,168
Cv 97,98,99
Cv 98,100,101
Mp 120,12,184
Rb 60,1,59392
s_waitcnt lgkmcnt(8)
Mp 112,12,176
Cv 99,102,103
Mp 112,16,192
Rb 0,1,60416
Mp 120,16,200
)" R"(
s_add_u32 s29, s73, 0x1800
buffer_store_dwordx4 v[96:99], v1, s[20:23], s29 offen sc0 sc1
Mp 120,20,216
Rb 4,1,61440
Mp 112,20,208
Mp 112,24,224
Rb 8,1,62464
Cv 104,104,105
Mp 120,24,232
Cv 105,106,107
Cv 106,108,109
Mp 120,28,248
Rb 12,1,63488
s_waitcnt lgkmcnt(8)
Mp 112,28,240
Cv 107,110,111
v_mfma_f32_16x16x32_bf16 v[116:119], a[32:35], v[128:131], 0
Rb 16,10,0
v_mfma_f32_16x16x32_bf16 v[124:127], a[32:35], v[136:139], 0
s_add_u32 s29, s73, 0x4000
buffer_store_dwordx4 v[104:107], v1, s[20:23], s29 offen sc0 sc1
Mp 124,36,152
Rb 20,10,1024
Mp 116,36,144
Mp 116,40,160
Rb 24,10,2048
Mp 124,40,168
Mp 124,44,184
Rb 28,10,3072
s_waitcnt lgkmcnt(8)
Mp 116,44,176
Mp 116,48,192
Rb 32,10,4096
Mp 124,48,200
Mp 124,52,216
Rb 36,10,5120
Mp 116,52,208
Mp 116,56,224
Rb 40,10,6144
Mp 124,56,232
Mp 124,60,248
Rb 44,10,8192
s_waitcnt vmcnt(11) lgkmcnt(10)
Mp 116,60,240
Mv 112,194,96
Mv 120,194,100
Mv 116,192,96
Mv 124,192,100
v_mfma_f32_16x16x32_bf16 v[32:35], a[0:3], v[128:131], 0
Rb 48,10,9216
v_mfma_f32_16x16x32_bf16 v[40:43], a[0:3], v[136:139], 0
Mp 40,4,152
Rb 52,10,10240
s_waitcnt lgkmcnt(8)
Mp 32,4,144
Mp 32,8,160
Rb 56,10,11264
Cv 112,112,113
Mp 40,8,168
Cv 113,114,115
Cv 114,116,117
Mp 40,12,184
Rb 60,10,12288
Cv 115,118,119
)" R"(
Mp 32,12,176
s_add_u32 s29, s73, 0x4400
buffer_store_dwordx4 v[112:115], v1, s[20:23], s29 offen sc0 sc1
Mp 32,16,192
Rb 0,10,13312
Mp 40,16,200
Mp 40,20,216
Rb 4,10,14336
s_waitcnt vmcnt(2) lgkmcnt(8)
buffer_store_dword v6, v7, s[20:23], s68 offen offset:0 sc0 sc1
Mp 32,20,208
Cv 120,120,121
Cv 121,122,123
Mp 32,24,224
Rb 8,10,16384
Cv 122,124,125
Mp 40,24,232
Cv 123,126,127
Mp 40,28,248
Rb 12,10,17408
Mp 32,28,240
s_add_u32 s29, s73, 0x4800
buffer_store_dwordx4 v[120:123], v1, s[20:23], s29 offen sc0 sc1
v_mfma_f32_16x16x32_bf16 v[36:39], a[32:35], v[128:131], 0
Rb 16,10,18432
v_mfma_f32_16x16x32_bf16 v[44:47], a[32:35], v[136:139], 0
Mp 44,36,152
Rb 20,10,19456
s_waitcnt lgkmcnt(8)
Mp 36,36,144
Mp 36,40,160
Rb 24,10,20480
Mp 44,40,168
Mp 44,44,184
Rb 28,10,21504
Mp 36,44,176
s_lshl2_add_u32 s56, s82, s33
s_mul_i32 s28, s56, 4682
Mp 36,48,192
Rb 32,10,22528
s_lshr_b32 s28, s28, 16
Mp 44,48,200
s_mul_i32 s57, s28, 14
s_sub_u32 s57, s56, s57
Mp 44,52,216
Rb 36,10,24576
s_waitcnt lgkmcnt(10)
Mp 36,52,208
s_lshl1_add_u32 s58, s28, s85
s_lshl_b32 s40, s58, 11
Mp 36,56,224
Rb 40,10,25600
Mp 44,56,232
s_cmp_gt_u32 s57, 6
s_cselect_b32 s28, 4, 0
Mp 44,60,248
Rb 44,10,26624
s_add_u32 s40, s40, s28
Mp 36,60,240
s_add_u32 s40, s40, 0xc130000
s_lshl_b32 s55, s58, 10
Mv 32,194,104
s_add_u32 s55, s55, s57
Mv 40,194,108
)" R"(
s_cmp_gt_u32 s57, 6
s_cselect_b32 s28, 9, 0
Mv 36,192,104
s_add_u32 s55, s55, s28
s_waitcnt lgkmcnt(8)
Mv 44,192,108
s_bfe_u32 s28, s58, 0x10001
s_xor_b32 s28, s28, s85
v_mfma_f32_16x16x32_bf16 v[48:51], a[0:3], v[128:131], 0
Rb 48,10,27648
s_lshl3_add_u32 s55, s28, s55
v_mfma_f32_16x16x32_bf16 v[56:59], a[0:3], v[136:139], 0
s_lshl_b32 s55, s55, 10
s_add_u32 s55, s55, 0x8000000
Mp 56,4,152
Rb 52,10,28672
s_and_b32 s28, s57, 1
Mp 48,4,144
s_lshl1_add_u32 s58, s58, s28
s_lshl_b32 s28, s58, 8
Mp 48,8,160
Rb 56,10,29696
Cv 32,32,33
Mp 56,8,168
Cv 33,34,35
Cv 34,36,37
Mp 56,12,184
Rb 60,10,30720
s_waitcnt lgkmcnt(8)
Mp 48,12,176
Cv 35,38,39
v_add_u32 v167, s28, v167
Mp 48,16,192
s_add_u32 s29, s73, 0x4c00
buffer_store_dwordx4 v[32:35], v1, s[20:23], s29 offen sc0 sc1
Mp 56,16,200
s_mul_i32 s59, s58, 14336
s_lshr_b32 s28, s57, 1
Mp 56,20,216
s_lshl_b32 s28, s28, 6
s_add_u32 s28, s28, s59
Mp 48,20,208
Cv 40,40,41
Cv 41,42,43
Mp 48,24,224
Cv 42,44,45
Cv 43,46,47
Mp 56,24,232
s_add_u32 s29, s73, 0x5000
buffer_store_dwordx4 v[40:43], v1, s[20:23], s29 offen sc0 sc1
buffer_store_dword v6, v7, s[20:23], s68 offen offset:4 sc0 sc1
Mp 56,28,248
v_add_u32 v166, s28, v166
s_waitcnt lgkmcnt(4)
Mp 48,28,240
s_mov_b32 s59, 0
s_mov_b32 s57, 0
v_mfma_f32_16x16x32_bf16 v[52:55], a[32:35], v[128:131], 0
v_mfma_f32_16x16x32_bf16 v[60:63], a[32:35], v[136:139], 0
buffer_load_dword v4, v165, s[20:23], s40 offen sc0 sc1
Mp 60,36,152
Mp 52,36,144
Mp 52,40,160
Mp 60,40,168
)" R"(
Mp 60,44,184
s_waitcnt lgkmcnt(0)
Mp 52,44,176
Mp 52,48,192
Mp 60,48,200
Mp 60,52,216
Mp 52,52,208
Mp 52,56,224
Mp 60,56,232
Mp 60,60,248
Mp 52,60,240
Mv 48,194,112
Mv 56,194,116
Mv 52,192,112
Mv 60,192,116
s_nop 7
Cv 48,48,49
Cv 49,50,51
Cv 50,52,53
Cv 51,54,55
s_add_u32 s29, s73, 0x5400
buffer_store_dwordx4 v[48:51], v1, s[20:23], s29 offen sc0 sc1
Cv 56,56,57
Cv 57,58,59
Cv 58,60,61
Cv 59,62,63
s_add_u32 s29, s73, 0x5800
buffer_store_dwordx4 v[56:59], v1, s[20:23], s29 offen sc0 sc1
.LzEND:
s_cmp_gt_u32 s82, 111
s_cbranch_scc1 .LzFIN
s_lshl_b32 s28, s85, 8
s_add_u32 s28, s28, s82
s_cmp_lt_u32 s28, 14
s_cbranch_scc1 .LzBB
.LzBBR:
s_waitcnt vmcnt(2)
.LzS0:
v_cmp_ne_u32_e32 vcc, s3, v4
s_lshr_b32 s28, vcc_lo, 1
s_or_b32 s28, s28, vcc_lo
s_lshr_b32 s29, s28, 2
s_or_b32 s28, s28, s29
s_andn2_b32 s28, s93, s28
s_sub_u32 s53, s53, 1
s_cselect_b32 s28, s93, s28
s_cmov_b32 s53, 0
s_xor_b32 s93, s93, s28
s_mul_i32 s28, s28, 15
s_or_b32 s57, s57, s28
s_cbranch_scc0 .LzSk3
s_bfe_u32 s28, s57, 0x40000
s_cbranch_scc0 .LzSk0
s_add_u32 s29, s55, 0x0
s_bitcmp0_b32 s28, 0
s_cbranch_scc1 .LzSq00
buffer_load_dwordx4 a[128:131], v164, s[20:23], s29 offen offset:0 sc0 sc1
.LzSq00:
s_bitcmp0_b32 s28, 1
s_cbranch_scc1 .LzSq01
buffer_load_dwordx4 a[132:135], v164, s[20:23], s29 offen offset:256 sc0 sc1
.LzSq01:
s_bitcmp0_b32 s28, 2
s_cbranch_scc1 .LzSq02
)" R"(
buffer_load_dwordx4 a[136:139], v164, s[20:23], s29 offen offset:512 sc0 sc1
.LzSq02:
s_bitcmp0_b32 s28, 3
s_cbranch_scc1 .LzSq03
buffer_load_dwordx4 a[140:143], v164, s[20:23], s29 offen offset:768 sc0 sc1
.LzSq03:
.LzSk0:
s_bfe_u32 s28, s57, 0x40004
s_cbranch_scc0 .LzSk1
s_add_u32 s29, s55, 0x40000
s_bitcmp0_b32 s28, 0
s_cbranch_scc1 .LzSq10
buffer_load_dwordx4 a[144:147], v164, s[20:23], s29 offen offset:0 sc0 sc1
.LzSq10:
s_bitcmp0_b32 s28, 1
s_cbranch_scc1 .LzSq11
buffer_load_dwordx4 a[148:151], v164, s[20:23], s29 offen offset:256 sc0 sc1
.LzSq11:
s_bitcmp0_b32 s28, 2
s_cbranch_scc1 .LzSq12
buffer_load_dwordx4 a[152:155], v164, s[20:23], s29 offen offset:512 sc0 sc1
.LzSq12:
s_bitcmp0_b32 s28, 3
s_cbranch_scc1 .LzSq13
buffer_load_dwordx4 a[156:159], v164, s[20:23], s29 offen offset:768 sc0 sc1
.LzSq13:
.LzSk1:
s_bfe_u32 s28, s57, 0x40008
s_cbranch_scc0 .LzSk2
s_add_u32 s29, s55, 0x8000
s_bitcmp0_b32 s28, 0
s_cbranch_scc1 .LzSq20
buffer_load_dwordx4 a[160:163], v164, s[20:23], s29 offen offset:0 sc0 sc1
.LzSq20:
s_bitcmp0_b32 s28, 1
s_cbranch_scc1 .LzSq21
buffer_load_dwordx4 a[164:167], v164, s[20:23], s29 offen offset:256 sc0 sc1
.LzSq21:
s_bitcmp0_b32 s28, 2
s_cbranch_scc1 .LzSq22
buffer_load_dwordx4 a[168:171], v164, s[20:23], s29 offen offset:512 sc0 sc1
.LzSq22:
s_bitcmp0_b32 s28, 3
s_cbranch_scc1 .LzSq23
buffer_load_dwordx4 a[172:175], v164, s[20:23], s29 offen offset:768 sc0 sc1
.LzSq23:
.LzSk2:
s_bfe_u32 s28, s57, 0x4000c
s_cbranch_scc0 .LzSk3
s_add_u32 s29, s55, 0x48000
s_bitcmp0_b32 s28, 0
s_cbranch_scc1 .LzSq30
buffer_load_dwordx4 a[176:179], v164, s[20:23], s29 offen offset:0 sc0 sc1
.LzSq30:
s_bitcmp0_b32 s28, 1
s_cbranch_scc1 .LzSq31
buffer_load_dwordx4 a[180:183], v164, s[20:23], s29 offen offset:256 sc0 sc1
.LzSq31:
s_bitcmp0_b32 s28, 2
s_cbranch_scc1 .LzSq32
buffer_load_dwordx4 a[184:187], v164, s[20:23], s29 offen offset:512 sc0 sc1
.LzSq32:
s_bitcmp0_b32 s28, 3
s_cbranch_scc1 .LzSq33
)" R"(
buffer_load_dwordx4 a[188:191], v164, s[20:23], s29 offen offset:768 sc0 sc1
.LzSq33:
.LzSk3:
s_bitcmp1_b32 s59, 1
s_cbranch_scc1 .LzS2
s_bitset1_b32 s59, 1
s_mov_b32 s28, 0xc100008
buffer_load_dword v167, v167, s[20:23], s28 offen sc0 sc1
buffer_load_dwordx2 v[188:189], v166, s[12:15], 0 offen offset:0
buffer_load_dwordx2 v[190:191], v166, s[12:15], 0 offen offset:32
buffer_load_dwordx2 v[196:197], v166, s[12:15], 0 offen offset:448
buffer_load_dwordx2 v[198:199], v166, s[12:15], 0 offen offset:480
s_branch .LzS3
.LzS2:
s_call_b64 s[30:31], .LzBR
s_cmp_lg_u32 s57, 0
s_cbranch_scc1 .LzS3
s_sleep 4
.LzS3:
s_cmp_eq_u32 s93, 0
s_cbranch_scc1 .LzS1
buffer_load_dword v4, v165, s[20:23], s40 offen sc0 sc1
.LzS1:
s_mov_b32 s29, 0
s_waitcnt vmcnt(0)
s_cmp_eq_u32 s57, 0
s_cbranch_scc1 .LzS8
v_accvgpr_read_b32 v173, a191
v_cmp_eq_u32_e32 vcc, -1, v173
s_cmp_lg_u64 vcc, 0
s_addc_u32 s29, s29, s29
v_accvgpr_read_b32 v173, a187
v_cmp_eq_u32_e32 vcc, -1, v173
s_cmp_lg_u64 vcc, 0
s_addc_u32 s29, s29, s29
v_accvgpr_read_b32 v173, a183
v_cmp_eq_u32_e32 vcc, -1, v173
s_cmp_lg_u64 vcc, 0
s_addc_u32 s29, s29, s29
v_accvgpr_read_b32 v173, a179
v_cmp_eq_u32_e32 vcc, -1, v173
s_cmp_lg_u64 vcc, 0
s_addc_u32 s29, s29, s29
v_accvgpr_read_b32 v173, a175
v_cmp_eq_u32_e32 vcc, -1, v173
s_cmp_lg_u64 vcc, 0
s_addc_u32 s29, s29, s29
v_accvgpr_read_b32 v173, a171
v_cmp_eq_u32_e32 vcc, -1, v173
s_cmp_lg_u64 vcc, 0
s_addc_u32 s29, s29, s29
v_accvgpr_read_b32 v173, a167
v_cmp_eq_u32_e32 vcc, -1, v173
s_cmp_lg_u64 vcc, 0
s_addc_u32 s29, s29, s29
v_accvgpr_read_b32 v173, a163
v_cmp_eq_u32_e32 vcc, -1, v173
s_cmp_lg_u64 vcc, 0
s_addc_u32 s29, s29, s29
v_accvgpr_read_b32 v173, a159
v_cmp_eq_u32_e32 vcc, -1, v173
s_cmp_lg_u64 vcc, 0
s_addc_u32 s29, s29, s29
v_accvgpr_read_b32 v173, a155
)" R"(
v_cmp_eq_u32_e32 vcc, -1, v173
s_cmp_lg_u64 vcc, 0
s_addc_u32 s29, s29, s29
v_accvgpr_read_b32 v173, a151
v_cmp_eq_u32_e32 vcc, -1, v173
s_cmp_lg_u64 vcc, 0
s_addc_u32 s29, s29, s29
v_accvgpr_read_b32 v173, a147
v_cmp_eq_u32_e32 vcc, -1, v173
s_cmp_lg_u64 vcc, 0
s_addc_u32 s29, s29, s29
v_accvgpr_read_b32 v173, a143
v_cmp_eq_u32_e32 vcc, -1, v173
s_cmp_lg_u64 vcc, 0
s_addc_u32 s29, s29, s29
v_accvgpr_read_b32 v173, a139
v_cmp_eq_u32_e32 vcc, -1, v173
s_cmp_lg_u64 vcc, 0
s_addc_u32 s29, s29, s29
v_accvgpr_read_b32 v173, a135
v_cmp_eq_u32_e32 vcc, -1, v173
s_cmp_lg_u64 vcc, 0
s_addc_u32 s29, s29, s29
v_accvgpr_read_b32 v173, a131
v_cmp_eq_u32_e32 vcc, -1, v173
s_cmp_lg_u64 vcc, 0
s_addc_u32 s29, s29, s29
s_and_b32 s57, s57, s29
s_cmp_eq_u32 s53, 0
s_cmov_b32 s57, 0
.LzS8:
s_or_b32 s28, s57, s93
s_cbranch_scc1 .LzS0
s_call_b64 s[30:31], .LzBR
v_mfma_f32_16x16x32_bf16 v[148:151], v[132:135], a[128:131], 0
v_mfma_f32_16x16x32_bf16 v[156:159], v[132:135], a[136:139], 0
v_mfma_f32_16x16x32_bf16 v[148:151], v[140:143], a[132:135], v[148:151]
v_mfma_f32_16x16x32_bf16 v[156:159], v[140:143], a[140:143], v[156:159]
v_mfma_f32_16x16x32_bf16 v[148:151], v[132:135], a[144:147], v[148:151]
v_mfma_f32_16x16x32_bf16 v[156:159], v[132:135], a[152:155], v[156:159]
v_mfma_f32_16x16x32_bf16 v[148:151], v[140:143], a[148:151], v[148:151]
v_mfma_f32_16x16x32_bf16 v[156:159], v[140:143], a[156:159], v[156:159]
s_nop 6
Cv 149,148,149
v_lshlrev_b32 v148, 16, v149
v_and_b32 v149, 0xffff0000, v149
Cv 151,150,151
v_lshlrev_b32 v150, 16, v151
v_and_b32 v151, 0xffff0000, v151
Cv 157,156,157
v_lshlrev_b32 v156, 16, v157
v_and_b32 v157, 0xffff0000, v157
Cv 159,158,159
v_lshlrev_b32 v158, 16, v159
v_and_b32 v159, 0xffff0000, v159
v_mul_f32_e32 v148, v167, v148
v_mul_f32_e32 v149, v167, v149
v_mul_f32_e32 v150, v167, v150
v_mul_f32_e32 v151, v167, v151
v_mul_f32_e32 v156, v167, v156
v_mul_f32_e32 v157, v167, v157
v_mul_f32_e32 v158, v167, v158
v_mul_f32_e32 v159, v167, v159
Cv 149,148,149
)" R"(
v_lshlrev_b32 v148, 16, v149
v_and_b32 v149, 0xffff0000, v149
Cv 151,150,151
v_lshlrev_b32 v150, 16, v151
v_and_b32 v151, 0xffff0000, v151
Cv 157,156,157
v_lshlrev_b32 v156, 16, v157
v_and_b32 v157, 0xffff0000, v157
Cv 159,158,159
v_lshlrev_b32 v158, 16, v159
v_and_b32 v159, 0xffff0000, v159
v_add_f32_e32 v148, v148, v204
v_add_f32_e32 v149, v149, v205
v_add_f32_e32 v150, v150, v206
v_add_f32_e32 v151, v151, v207
v_add_f32_e32 v156, v156, v212
v_add_f32_e32 v157, v157, v213
v_add_f32_e32 v158, v158, v214
v_add_f32_e32 v159, v159, v215
Cv 148,148,149
Cv 149,150,151
buffer_store_dwordx2 v[148:149], v166, s[8:11], 0 offen offset:0
Cv 156,156,157
Cv 157,158,159
buffer_store_dwordx2 v[156:157], v166, s[8:11], 0 offen offset:32
v_mfma_f32_16x16x32_bf16 v[148:151], v[132:135], a[160:163], 0
v_mfma_f32_16x16x32_bf16 v[156:159], v[132:135], a[168:171], 0
v_mfma_f32_16x16x32_bf16 v[148:151], v[140:143], a[164:167], v[148:151]
v_mfma_f32_16x16x32_bf16 v[156:159], v[140:143], a[172:175], v[156:159]
v_mfma_f32_16x16x32_bf16 v[148:151], v[132:135], a[176:179], v[148:151]
v_mfma_f32_16x16x32_bf16 v[156:159], v[132:135], a[184:187], v[156:159]
v_mfma_f32_16x16x32_bf16 v[148:151], v[140:143], a[180:183], v[148:151]
v_mfma_f32_16x16x32_bf16 v[156:159], v[140:143], a[188:191], v[156:159]
s_nop 6
Cv 149,148,149
v_lshlrev_b32 v148, 16, v149
v_and_b32 v149, 0xffff0000, v149
Cv 151,150,151
v_lshlrev_b32 v150, 16, v151
v_and_b32 v151, 0xffff0000, v151
Cv 157,156,157
v_lshlrev_b32 v156, 16, v157
v_and_b32 v157, 0xffff0000, v157
Cv 159,158,159
v_lshlrev_b32 v158, 16, v159
v_and_b32 v159, 0xffff0000, v159
v_mul_f32_e32 v148, v167, v148
v_mul_f32_e32 v149, v167, v149
v_mul_f32_e32 v150, v167, v150
v_mul_f32_e32 v151, v167, v151
v_mul_f32_e32 v156, v167, v156
v_mul_f32_e32 v157, v167, v157
v_mul_f32_e32 v158, v167, v158
v_mul_f32_e32 v159, v167, v159
Cv 149,148,149
v_lshlrev_b32 v148, 16, v149
v_and_b32 v149, 0xffff0000, v149
Cv 151,150,151
v_lshlrev_b32 v150, 16, v151
v_and_b32 v151, 0xffff0000, v151
Cv 157,156,157
v_lshlrev_b32 v156, 16, v157
v_and_b32 v157, 0xffff0000, v157
Cv 159,158,159
)" R"(
v_lshlrev_b32 v158, 16, v159
v_and_b32 v159, 0xffff0000, v159
v_add_f32_e32 v148, v148, v220
v_add_f32_e32 v149, v149, v221
v_add_f32_e32 v150, v150, v222
v_add_f32_e32 v151, v151, v223
v_add_f32_e32 v156, v156, v228
v_add_f32_e32 v157, v157, v229
v_add_f32_e32 v158, v158, v230
v_add_f32_e32 v159, v159, v231
Cv 148,148,149
Cv 149,150,151
buffer_store_dwordx2 v[148:149], v166, s[8:11], 0 offen offset:448
Cv 156,156,157
Cv 157,158,159
buffer_store_dwordx2 v[156:157], v166, s[8:11], 0 offen offset:480
s_or_b32 s28, s82, s33
s_or_b32 s28, s28, s85
s_cbranch_scc1 .LzFIN
s_mov_b32 s28, 0xc108400
buffer_store_dword v6, v7, s[20:23], s28 offen offset:0 sc0 sc1
.LzFIN:
s_endpgm
.LzBR:
s_bitcmp1_b32 s59, 2
s_cbranch_scc1 .LzBRE
s_bitset1_b32 s59, 2
s_bfe_u32 s28, s63, 0x70004
s_cmp_eq_u32 s28, s58
s_cbranch_scc1 .LzRT
s_bfe_u32 s28, s63, 0x70014
s_cmp_eq_u32 s28, s58
s_cbranch_scc1 .LzRT
.LzRT3:
v_sub_f32_e32 v172, 1.0, v167
Cv 172,172,172
v_lshlrev_b32 v172, 16, v172
v_lshlrev_b32 v204, 16, v188
v_and_b32 v205, 0xffff0000, v188
v_mul_f32_e32 v204, v172, v204
v_mul_f32_e32 v205, v172, v205
Cv 205,204,205
v_lshlrev_b32 v204, 16, v205
v_and_b32 v205, 0xffff0000, v205
v_lshlrev_b32 v206, 16, v189
v_and_b32 v207, 0xffff0000, v189
v_mul_f32_e32 v206, v172, v206
v_mul_f32_e32 v207, v172, v207
Cv 207,206,207
v_lshlrev_b32 v206, 16, v207
v_and_b32 v207, 0xffff0000, v207
v_lshlrev_b32 v212, 16, v190
v_and_b32 v213, 0xffff0000, v190
v_mul_f32_e32 v212, v172, v212
v_mul_f32_e32 v213, v172, v213
Cv 213,212,213
v_lshlrev_b32 v212, 16, v213
v_and_b32 v213, 0xffff0000, v213
v_lshlrev_b32 v214, 16, v191
v_and_b32 v215, 0xffff0000, v191
v_mul_f32_e32 v214, v172, v214
v_mul_f32_e32 v215, v172, v215
Cv 215,214,215
v_lshlrev_b32 v214, 16, v215
)" R"(
v_and_b32 v215, 0xffff0000, v215
v_lshlrev_b32 v220, 16, v196
v_and_b32 v221, 0xffff0000, v196
v_mul_f32_e32 v220, v172, v220
v_mul_f32_e32 v221, v172, v221
Cv 221,220,221
v_lshlrev_b32 v220, 16, v221
v_and_b32 v221, 0xffff0000, v221
v_lshlrev_b32 v222, 16, v197
v_and_b32 v223, 0xffff0000, v197
v_mul_f32_e32 v222, v172, v222
v_mul_f32_e32 v223, v172, v223
Cv 223,222,223
v_lshlrev_b32 v222, 16, v223
v_and_b32 v223, 0xffff0000, v223
v_lshlrev_b32 v228, 16, v198
v_and_b32 v229, 0xffff0000, v198
v_mul_f32_e32 v228, v172, v228
v_mul_f32_e32 v229, v172, v229
Cv 229,228,229
v_lshlrev_b32 v228, 16, v229
v_and_b32 v229, 0xffff0000, v229
v_lshlrev_b32 v230, 16, v199
v_and_b32 v231, 0xffff0000, v199
v_mul_f32_e32 v230, v172, v230
v_mul_f32_e32 v231, v172, v231
Cv 231,230,231
v_lshlrev_b32 v230, 16, v231
v_and_b32 v231, 0xffff0000, v231
.LzBRE:
s_setpc_b64 s[30:31]
.LzRT:
s_and_b32 s28, s63, 0x7ff
s_mul_i32 s28, s28, 896
v_subrev_u32 v174, s28, v166
s_bfe_u32 s28, s63, 0xb0010
s_mul_i32 s28, s28, 896
v_subrev_u32 v175, s28, v166
s_movk_i32 s28, 896
v_cmp_gt_u32_e64 s[24:25], s28, v175
v_cmp_gt_u32_e32 vcc, s28, v174
v_add_u32 v175, s28, v175
s_or_b64 s[24:25], s[24:25], vcc
v_cndmask_b32_e32 v174, v175, v174, vcc
v_lshrrev_b32 v174, 5, v174
v_lshlrev_b32 v174, 7, v174
v_and_or_b32 v174, v0, 48, v174
s_mov_b64 exec, s[24:25]
s_mov_b32 s29, 0xc109000
.LzRT1:
buffer_load_dwordx4 v[64:67], v174, s[20:23], s29 offen offset:0 sc0 sc1
buffer_load_dwordx4 v[68:71], v174, s[20:23], s29 offen offset:128 sc0 sc1
buffer_load_dwordx4 v[72:75], v174, s[20:23], s29 offen offset:1792 sc0 sc1
buffer_load_dwordx4 v[76:79], v174, s[20:23], s29 offen offset:1920 sc0 sc1
s_waitcnt vmcnt(0)
v_xor_b32 v175, s3, v65
v_xor_b32 v173, s3, v67
v_or_b32 v175, v175, v173
v_xor_b32 v173, s3, v69
v_or_b32 v175, v175, v173
v_xor_b32 v173, s3, v71
v_or_b32 v175, v175, v173
v_xor_b32 v173, s3, v73
v_or_b32 v175, v175, v173
)" R"(
v_xor_b32 v173, s3, v75
v_or_b32 v175, v175, v173
v_xor_b32 v173, s3, v77
v_or_b32 v175, v175, v173
v_xor_b32 v173, s3, v79
v_or_b32 v175, v175, v173
v_cmp_ne_u32_e32 vcc, 0, v175
s_cbranch_vccz .LzRT2
s_sleep 2
s_sub_u32 s53, s53, 1
s_cbranch_scc0 .LzRT1
s_mov_b32 s53, 0
.LzRT2:
v_mov_b32 v188, v64
v_mov_b32 v189, v66
v_mov_b32 v190, v68
v_mov_b32 v191, v70
v_mov_b32 v196, v72
v_mov_b32 v197, v74
v_mov_b32 v198, v76
v_mov_b32 v199, v78
s_mov_b64 exec, -1
s_branch .LzRT3
.LzCEND:
)" : "+{s[0:1]}"(ka), "+{s2}"(bid), "+{v0}"(tid) :
   : "memory", "scc", "vcc", "v1", "v2", "v3", "v4", "v5", "v6", "v7", "v8", "v9", R256("v"), V10("a"), R256("a"),
     "s3", "s4", "s5", "s6", "s7", "s8", "s9", V10("s1"), V10("s2"), "s30", "s31", "s33", "s34", "s35", "s36", "s37", "s38", "s39",
     V10("s4"), V10("s5"), V10("s6"), V10("s7"), V10("s8"), V10("s9"));
}

extern "C" void kernel_launch(void* const* d_in, const int* in_sizes, int n_in, void* d_out, int out_size, void* d_ws, size_t ws_size, hipStream_t stream) {
  (void)hipFuncSetAttribute((const void*)k_main, hipFuncAttributeMaxDynamicSharedMemorySize, 163840);
  k_main<<<256, 256, 163840, stream>>>((const u16*)d_in[0], (const u16*)d_in[1], (const u16*)d_in[2], (const u16*)d_in[3], (const u16*)d_in[4],
      (char*)d_ws, (const u16*)d_in[11], (const u16*)d_in[7], (const u16*)d_in[5], (const u16*)d_in[8], (const u16*)d_in[9], (const u16*)d_in[10], (u16*)d_out);
}
```

```cpp
#include <hip/hip_runtime.h>

typedef unsigned short u16;
#define V10(p) p "0", p "1", p "2", p "3", p "4", p "5", p "6", p "7", p "8", p "9"
#define V100(p) V10(p "0"), V10(p "1"), V10(p "2"), V10(p "3"), V10(p "4"), V10(p "5"), V10(p "6"), V10(p "7"), V10(p "8"), V10(p "9")
#define R256(p) V10(p "1"), V10(p "2"), V10(p "3"), V10(p "4"), V10(p "5"), V10(p "6"), V10(p "7"), V10(p "8"), V10(p "9"), V100(p "1"), \
                V10(p "20"), V10(p "21"), V10(p "22"), V10(p "23"), V10(p "24"), p "250", p "251", p "252", p "253", p "254", p "255"

extern "C" __global__ __launch_bounds__(256, 1)
void k_main(const u16* x, const u16* ckv, const u16* wq, const u16* wuk, const u16* wuv, char* ws,
            const u16* gate_w, const u16* w_r, const u16* wo, const u16* wg, const u16* wu, const u16* wd, u16* out) {
  typedef const __attribute__((address_space(4))) char* karg_t;
  karg_t ka = (karg_t)__builtin_amdgcn_kernarg_segment_ptr();
  unsigned bid = blockIdx.x, tid = threadIdx.x;
  asm volatile(R"(
.macro Mv d,a,b
v_mfma_f32_16x16x32_bf16 v[\d:\d+3], a[\a:\a+3], a[\b:\b+3], v[\d:\d+3]
.endm
.macro Mp d,a,b
v_mfma_f32_16x16x32_bf16 v[\d:\d+3], a[\a:\a+3], v[\b:\b+3], v[\d:\d+3]
.endm
.macro Ma d,a,b
v_mfma_f32_16x16x32_bf16 a[\d:\d+3], a[\a:\a+3], a[\b:\b+3], a[\d:\d+3]
.endm
.macro Mz d,a,b
v_mfma_f32_16x16x32_bf16 v[\d:\d+3], a[\a:\a+3], a[\b:\b+3], 0
.endm
.macro Rb d,v,o
ds_read_b128 a[\d:\d+3], v\v offset:\o
.endm
.macro Rt d,v,o
ds_read_b64_tr_b16 a[\d:\d+1], v\v offset:\o
.endm
.macro Cv d,a,b
v_cvt_pk_bf16_f32 v\d, v\a, v\b
.endm
.LzSTART:
s_load_dwordx16 s[64:79], s[0:1], 0
s_load_dwordx2 s[80:81], s[0:1], 64
v_lshrrev_b32 v1, 6, v0
v_and_b32 v0, 63, v0
v_readfirstlane_b32 s33, v1
v_lshlrev_b32 v1, 4, v0
s_getpc_b64 s[24:25]
.Lzpc0:
s_sub_u32 s24, s24, .Lzpc0-.LzSTART
s_subb_u32 s25, s25, 0
s_mov_b32 s26, .LzCEND-.LzSTART
s_mov_b32 s27, 0x20000
s_lshr_b32 s28, s2, 3
s_lshl2_add_u32 s28, s28, s33
s_lshl_b32 s28, s28, 11
v_lshrrev_b32 v5, 4, v0
v_min_u32 v5, 1, v5
v_lshlrev_b32 v5, 27, v5
v_add_lshl_u32 v5, v5, v1, 3
buffer_load_dword v5, v5, s[24:27], s28 offen
s_and_b32 s28, s2, 7
s_bfe_u32 s29, s2, 0x10003
s_lshl1_add_u32 s34, s28, s29
s_bfe_u32 s35, s2, 0x30004
s_bfe_u32 s91, s2, 0x10004
s_bfe_u32 s93, s2, 0x20005
s_lshl_b32 s36, s33, 13
s_mov_b32 s95, 40
s_bitcmp1_b32 s2, 7
s_cbranch_scc1 .LzB0
s_branch .LzA0
.LzPRB:
s_lshl_b32 s24, s34, 21
s_mul_i32 s25, s33, 2304
s_add_u32 s24, s24, s25
s_add_u32 s24, s24, 0x6100000
s_and_b32 s25, s36, 0x2000
s_xor_b32 s24, s24, s25
s_xor_b32 s25, s24, 0x2000
buffer_store_dword v0, v7, s[20:23], s24 offen sc0 sc1
buffer_store_dword v0, v7, s[20:23], s25 offen sc0 sc1
s_mov_b32 s26, -1
s_mov_b32 s27, -1
s_waitcnt vmcnt(0)
s_memtime s[28:29]
s_waitcnt lgkmcnt(0)
buffer_store_dword v0, v7, s[20:23], s24 offen sc0 sc1
s_waitcnt vmcnt(0)
s_memtime s[30:31]
s_waitcnt lgkmcnt(0)
s_sub_u32 s30, s30, s28
s_min_u32 s26, s26, s30
s_memtime s[28:29]
s_waitcnt lgkmcnt(0)
buffer_store_dword v0, v7, s[20:23], s25 offen sc0 sc1
s_waitcnt vmcnt(0)
s_memtime s[30:31]
s_waitcnt lgkmcnt(0)
s_sub_u32 s30, s30, s28
s_min_u32 s27, s27, s30
s_memtime s[28:29]
s_waitcnt lgkmcnt(0)
)" R"(
buffer_store_dword v0, v7, s[20:23], s24 offen sc0 sc1
s_waitcnt vmcnt(0)
s_memtime s[30:31]
s_waitcnt lgkmcnt(0)
s_sub_u32 s30, s30, s28
s_min_u32 s26, s26, s30
s_memtime s[28:29]
s_waitcnt lgkmcnt(0)
buffer_store_dword v0, v7, s[20:23], s25 offen sc0 sc1
s_waitcnt vmcnt(0)
s_memtime s[30:31]
s_waitcnt lgkmcnt(0)
s_sub_u32 s30, s30, s28
s_min_u32 s27, s27, s30
s_bitcmp1_b32 s33, 0
s_cselect_b32 s28, s27, s26
s_cselect_b32 s29, s26, s27
v_mov_b32 v32, s28
v_mov_b32 v33, s29
s_lshl_b32 s24, s33, 3
v_mov_b32 v34, s24
v_mov_b32 v35, 0
ds_write_b64 v34, v[32:33]
s_waitcnt lgkmcnt(0)
s_barrier
ds_read_b128 v[36:39], v35
ds_read_b128 v[40:43], v35 offset:16
s_mul_i32 s24, s3, 1664525
s_add_u32 s24, s24, 1013904223
s_and_b32 s24, s24, -2
s_lshl_b32 s25, s34, 6
s_add_u32 s25, s25, 0xc108800
s_cmp_lg_u32 s33, 0
s_cmov_b32 s25, 0x40000000
s_waitcnt lgkmcnt(0)
v_min3_u32 v32, v36, v38, v40
v_min3_u32 v33, v37, v39, v41
v_min_u32 v32, v32, v42
v_min_u32 v33, v33, v43
v_cmp_gt_u32_e32 vcc, v32, v33
v_mov_b32 v34, s24
s_nop 0
v_addc_co_u32_e32 v34, vcc, 0, v34, vcc
buffer_store_dword v34, v7, s[20:23], s25 offen sc0 sc1
s_barrier
s_branch .LzPRR
.LzA0:
s_mov_b32 s63, 0x6040200
s_mov_b32 s28, 0x7050312
s_bitcmp1_b32 s2, 5
s_cmov_b32 s63, 0x18161401
s_cmov_b32 s28, 0x19171513
s_bitcmp1_b32 s2, 6
s_cmov_b32 s63, s28
s_lshl_b32 s28, s33, 3
s_lshr_b32 s28, s63, s28
s_and_b32 s28, s28, 255
s_lshl_b32 s38, s28, 17
s_add_u32 s38, s38, 0xff80
s_lshl_b32 s39, s33, 16
s_lshl_b32 s28, s34, 9
s_add_u32 s39, s39, s28
s_sub_u32 s39, s39, 0x40000
s_lshl_b32 s37, s33, 12
)" R"(
s_add_u32 s37, s37, 0x10000
v_lshrrev_b32 v28, 4, v0
v_and_b32 v29, 7, v0
v_xor_b32 v28, v28, v29
v_lshlrev_b32 v28, 4, v28
v_lshrrev_b32 v29, 3, v0
v_lshl_or_b32 v8, v29, 11, v28
v_xor_b32 v9, 64, v8
v_add_u32 v9, 15360, v9
v_add_u32 v10, 30720, v8
v_add_u32 v11, 30720, v9
s_and_b32 s28, s33, 1
s_lshl_b32 s28, s28, 7
v_lshrrev_b32 v29, 5, v0
v_mul_u32_u24 v29, 0x2220, v29
v_xor_b32 v29, v29, v1
v_xor_b32 v12, s28, v29
v_xor_b32 v13, 64, v12
v_add_u32 v13, 15360, v13
v_add_u32 v14, 30720, v12
v_add_u32 v15, 30720, v13
v_min_u32 v7, 1, v0
v_lshlrev_b32 v7, 30, v7
s_waitcnt lgkmcnt(0)
s_mov_b32 s28, 0xc108400
s_load_dword s3, s[74:75], s28
s_mov_b32 s20, s74
s_mov_b32 s21, s75
s_mov_b32 s22, 0xc510000
s_mov_b32 s23, 0x20000
s_mov_b32 s4, s66
s_mov_b32 s5, s67
s_mov_b32 s6, 0x400000
s_mov_b32 s7, 0x20000
s_cmp_eq_u32 s91, 1
s_cselect_b32 s8, s72, s70
s_cselect_b32 s9, s73, s71
s_mov_b32 s10, 0x800000
s_mov_b32 s11, 0x20000
s_lshr_b32 s28, s2, 3
s_cmp_eq_u32 s28, 14
s_cbranch_scc1 .LzPRB
.LzPRR:
s_add_u32 m0, s37, 0
s_add_u32 s39, s39, 0x40000
buffer_load_dwordx4 v12, s[8:11], s39 offen offset:0 lds
buffer_load_dwordx4 v13, s[8:11], s39 offen offset:1024 lds
buffer_load_dwordx4 v14, s[8:11], s39 offen offset:2048 lds
buffer_load_dwordx4 v15, s[8:11], s39 offen offset:3072 lds
s_add_u32 m0, s36, 0
s_add_u32 s38, s38, 0xffff0080
buffer_load_dwordx4 v8, s[4:7], s38 offen offset:0 lds
buffer_load_dwordx4 v9, s[4:7], s38 offen offset:1024 lds
buffer_load_dwordx4 v10, s[4:7], s38 offen offset:2048 lds
buffer_load_dwordx4 v11, s[4:7], s38 offen offset:3072 lds
s_add_u32 m0, s36, 4096
s_add_u32 s38, s38, 0x10000
buffer_load_dwordx4 v8, s[4:7], s38 offen offset:0 lds
buffer_load_dwordx4 v9, s[4:7], s38 offen offset:1024 lds
buffer_load_dwordx4 v10, s[4:7], s38 offen offset:2048 lds
buffer_load_dwordx4 v11, s[4:7], s38 offen offset:3072 lds
s_add_u32 m0, s37, 16384
s_add_u32 s39, s39, 0x40000
buffer_load_dwordx4 v12, s[8:11], s39 offen offset:0 lds
)" R"(
buffer_load_dwordx4 v13, s[8:11], s39 offen offset:1024 lds
buffer_load_dwordx4 v14, s[8:11], s39 offen offset:2048 lds
buffer_load_dwordx4 v15, s[8:11], s39 offen offset:3072 lds
s_add_u32 m0, s36, 32768
s_add_u32 s38, s38, 0xffff0080
buffer_load_dwordx4 v8, s[4:7], s38 offen offset:0 lds
buffer_load_dwordx4 v9, s[4:7], s38 offen offset:1024 lds
buffer_load_dwordx4 v10, s[4:7], s38 offen offset:2048 lds
buffer_load_dwordx4 v11, s[4:7], s38 offen offset:3072 lds
s_add_u32 m0, s36, 36864
s_add_u32 s38, s38, 0x10000
buffer_load_dwordx4 v8, s[4:7], s38 offen offset:0 lds
buffer_load_dwordx4 v9, s[4:7], s38 offen offset:1024 lds
buffer_load_dwordx4 v10, s[4:7], s38 offen offset:2048 lds
buffer_load_dwordx4 v11, s[4:7], s38 offen offset:3072 lds
s_add_u32 m0, s37, 32768
s_add_u32 s39, s39, 0x40000
buffer_load_dwordx4 v12, s[8:11], s39 offen offset:0 lds
buffer_load_dwordx4 v13, s[8:11], s39 offen offset:1024 lds
buffer_load_dwordx4 v14, s[8:11], s39 offen offset:2048 lds
buffer_load_dwordx4 v15, s[8:11], s39 offen offset:3072 lds
s_add_u32 m0, s37, 49152
s_add_u32 s39, s39, 0x40000
buffer_load_dwordx4 v12, s[8:11], s39 offen offset:0 lds
buffer_load_dwordx4 v13, s[8:11], s39 offen offset:1024 lds
buffer_load_dwordx4 v14, s[8:11], s39 offen offset:2048 lds
buffer_load_dwordx4 v15, s[8:11], s39 offen offset:3072 lds
s_mov_b32 s12, s64
s_mov_b32 s13, s65
s_mov_b32 s14, 0x1c0000
s_mov_b32 s15, 0x20000
s_mov_b32 s16, s68
s_mov_b32 s17, s69
s_mov_b32 s18, 0x380000
s_mov_b32 s19, 0x20000
s_lshl2_add_u32 s94, s2, s33
s_mul_i32 s94, s94, 7168
v_min_u32 v2, 55, v0
v_lshlrev_b32 v2, 7, v2
buffer_load_dword v6, v2, s[16:19], s94 offen
s_load_dwordx2 s[70:71], s[0:1], 88
s_load_dwordx2 s[74:75], s[0:1], 96
v_bfe_u32 v28, v0, 1, 3
v_lshrrev_b32 v29, 4, v0
v_xor_b32 v28, v28, v29
v_lshlrev_b32 v28, 4, v28
v_and_b32 v29, 15, v0
v_lshl_or_b32 v28, v29, 7, v28
s_lshr_b32 s28, s33, 1
s_lshl_b32 s28, s28, 12
v_or_b32 v16, s28, v28
v_xor_b32 v17, 64, v16
v_bfe_u32 v28, v0, 2, 3
v_lshlrev_b32 v28, 5, v28
v_and_b32 v29, 3, v0
v_lshl_or_b32 v28, v29, 3, v28
v_bfe_u32 v29, v0, 2, 2
v_lshl_or_b32 v28, v29, 9, v28
v_lshrrev_b32 v29, 4, v0
v_lshl_or_b32 v28, v29, 12, v28
s_and_b32 s28, s33, 1
s_lshl_b32 s28, s28, 8
s_bitset1_b32 s28, 16
v_or_b32 v18, s28, v28
)" R"(
v_xor_b32 v19, 32, v18
v_xor_b32 v20, 64, v18
v_xor_b32 v21, 96, v18
v_xor_b32 v22, 128, v18
v_xor_b32 v23, 160, v18
v_xor_b32 v24, 192, v18
v_xor_b32 v25, 224, v18
s_load_dwordx4 s[76:79], s[0:1], 72
s_mov_b32 s28, s35
s_lshl2_add_u32 s28, s28, s33
s_mul_i32 s43, s28, 28672
v_and_b32 v26, 0x70, v1
v_and_b32 v4, 48, v0
v_xor_b32 v26, v26, v4
v_lshrrev_b32 v4, 3, v0
v_mul_u32_u24 v4, 896, v4
v_add_u32 v26, v26, v4
v_xor_b32 v27, 64, v26
v_add_u32 v27, 6144, v27
v_bfe_u32 v28, v0, 1, 3
v_lshrrev_b32 v4, 4, v0
v_xor_b32 v28, v28, v4
v_lshlrev_b32 v28, 4, v28
v_and_b32 v4, 15, v0
v_lshl_or_b32 v28, v4, 7, v28
s_add_u32 s28, s36, 0x18000
v_add_u32 v28, s28, v28
v_xor_b32 v29, 64, v28
s_lshl_b32 s28, s34, 21
s_lshl_b32 s29, s91, 25
s_add_u32 s28, s28, s29
s_bfe_u32 s29, s63, 0x80000
s_lshl_b32 s29, s29, 16
s_add_u32 s45, s28, s29
s_bfe_u32 s29, s63, 0x80008
s_lshl_b32 s29, s29, 16
s_add_u32 s46, s28, s29
s_bfe_u32 s29, s63, 0x80010
s_lshl_b32 s29, s29, 16
s_add_u32 s47, s28, s29
s_bfe_u32 s29, s63, 0x80018
s_lshl_b32 s29, s29, 16
s_add_u32 s48, s28, s29
s_lshl_b32 s50, s34, 11
s_add_u32 s50, s50, 0xc110000
s_lshl2_add_u32 s28, s91, s93
s_lshl_b32 s28, s28, 7
s_add_u32 s49, s28, s50
s_lshl3_add_u32 s28, s34, s35
s_lshl_b32 s52, s28, 7
s_add_u32 s52, s52, 0xc118000
s_lshl4_add_u32 s28, s34, s35
s_lshl_b32 s51, s28, 17
s_add_u32 s51, s51, 0x4000000
s_cmp_lg_u32 s33, 0
s_cmov_b32 s49, 0x40000000
s_mov_b32 s53, 0x40000
s_mov_b32 s54, 0x3db8aa3b
s_mul_i32 s41, s33, 7168
s_mul_i32 s42, s33, 0x70000
s_lshl_b32 s28, s34, 9
s_add_u32 s42, s42, s28
s_lshl_b32 s89, s34, 21
s_mov_b64 s[60:61], 0xff
)" R"(
s_mov_b32 s62, 0x4e800000
s_add_u32 s67, s89, 0x6000000
s_lshl3_add_u32 s28, s34, s35
s_add_u32 s64, s51, 0x100000
s_lshl2_add_u32 s28, s28, s33
s_lshl_b32 s65, s28, 7
s_add_u32 s65, s65, 0xc120000
s_lshl_b32 s66, s28, 10
s_add_u32 s66, s66, 0xc000000
s_mov_b32 s28, s35
s_lshl2_add_u32 s28, s28, s33
s_lshl_b32 s29, s28, 5
s_add_u32 s29, s29, s34
s_lshl_b32 s73, s29, 15
s_add_u32 s73, s73, 0x8000000
s_lshr_b32 s29, s36, 1
s_and_b32 s29, s29, 0x2000
s_add_u32 s73, s73, s29
s_lshl4_add_u32 s28, s28, s34
s_lshl_b32 s68, s28, 7
s_add_u32 s68, s68, 0xc130000
s_add_u32 s55, s89, 0x120000
s_lshl_b32 s28, s35, 1
s_sub_u32 s57, 14, s28
s_mov_b32 s59, s89
s_mov_b32 s58, 2
s_cmp_eq_u32 s35, 7
s_cmov_b32 s55, s89
s_cmp_eq_u32 s35, 7
s_cmov_b32 s57, 16
s_cmp_eq_u32 s35, 7
s_cmov_b32 s59, s67
s_add_u32 s56, s55, 0x2000000
s_waitcnt vmcnt(21) lgkmcnt(0)
s_barrier
Rt 0,18,0
Rt 2,18,2048
Rt 4,19,0
Rt 6,19,2048
Rt 8,20,0
Rt 10,20,2048
Rt 12,21,0
Rt 14,21,2048
Rt 16,22,0
Rt 18,22,2048
Rt 20,23,0
Rt 22,23,2048
Rt 24,24,0
Rt 26,24,2048
Rt 28,25,0
Rt 30,25,2048
Rb 32,16,0
Rb 36,16,2048
Rb 40,16,8192
Rb 44,16,10240
Rb 48,16,16384
Rb 52,16,18432
Rb 56,16,24576
Rb 60,16,26624
s_mul_i32 s3, s3, 1664525
s_add_u32 s3, s3, 1013904223
s_cmp_eq_u32 s91, 1
s_waitcnt vmcnt(17) lgkmcnt(0)
s_barrier
s_cbranch_scc1 .LzAV
Mz 32,0,32
)" R"(
Rt 64,18,16384
Mz 64,0,36
s_lshl1_add_u32 s40, s34, s91
s_lshl_b32 s40, s40, 12
Mz 96,0,40
Rt 66,18,18432
s_and_b32 s29, s38, 0xfffe0000
Mz 128,0,44
s_add_u32 s39, s39, 0x40000
s_add_u32 s40, s40, s29
Mz 160,0,48
Rt 68,19,16384
s_add_u32 m0, s37, 0
Mz 192,0,52
buffer_load_dwordx4 v12, s[8:11], s39 offen offset:0 lds
Mz 224,0,56
Rt 70,19,18432
v_min_u32 v3, 31, v0
v_mfma_f32_16x16x32_bf16 a[224:227], a[0:3], a[60:63], 0
v_lshlrev_b32 v3, 7, v3
v_mfma_f32_16x16x32_bf16 a[228:231], a[4:7], a[60:63], 0
Rt 72,20,16384
Mz 228,4,56
Mz 196,4,52
Rt 74,20,18432
Mz 164,4,48
Mz 132,4,44
Rt 76,21,16384
Mz 100,4,40
buffer_load_dwordx4 v13, s[8:11], s39 offen offset:1024 lds
Mz 68,4,36
Rt 78,21,18432
Mz 36,4,32
Mz 40,8,32
Rt 80,22,16384
Mz 72,8,36
Mz 104,8,40
Rt 82,22,18432
Mz 136,8,44
Mz 168,8,48
Rt 84,23,16384
Mz 200,8,52
buffer_load_dwordx4 v14, s[8:11], s39 offen offset:2048 lds
Mz 232,8,56
Rt 86,23,18432
v_mfma_f32_16x16x32_bf16 a[232:235], a[8:11], a[60:63], 0
v_mfma_f32_16x16x32_bf16 a[236:239], a[12:15], a[60:63], 0
Rt 88,24,16384
Mz 236,12,56
Mz 204,12,52
Rt 90,24,18432
Mz 172,12,48
Mz 140,12,44
Rt 92,25,16384
Mz 108,12,40
buffer_load_dwordx4 v15, s[8:11], s39 offen offset:3072 lds
Mz 76,12,36
Rt 94,25,18432
Mz 44,12,32
Mz 48,16,32
Rb 96,17,0
Mz 80,16,36
buffer_load_dword v6, v3, s[4:7], s40 offen
Mz 112,16,40
)" R"(
Rb 100,17,2048
Mz 144,16,44
Mz 176,16,48
Rb 104,17,8192
Mz 208,16,52
Mz 240,16,56
Rb 108,17,10240
v_mfma_f32_16x16x32_bf16 a[240:243], a[16:19], a[60:63], 0
v_mfma_f32_16x16x32_bf16 a[244:247], a[20:23], a[60:63], 0
Rb 112,17,16384
Mz 244,20,56
Mz 212,20,52
Rb 116,17,18432
Mz 180,20,48
Mz 148,20,44
Rb 120,17,24576
Mz 116,20,40
Mz 84,20,36
Rb 124,17,26624
Mz 52,20,32
Mz 56,24,32
Mz 88,24,36
Mz 120,24,40
Mz 152,24,44
Mz 184,24,48
Mz 216,24,52
Mz 248,24,56
v_mfma_f32_16x16x32_bf16 a[248:251], a[24:27], a[60:63], 0
v_mfma_f32_16x16x32_bf16 a[252:255], a[28:31], a[60:63], 0
Mz 252,28,56
Mz 220,28,52
Mz 188,28,48
Mz 156,28,44
Mz 124,28,40
Mz 92,28,36
Mz 60,28,32
s_waitcnt vmcnt(10) lgkmcnt(0)
s_barrier
Mv 32,64,96
Rt 0,18,32768
s_bfe_u32 s88, s2, 0x20005
Mv 64,64,100
s_add_u32 s38, s38, 0xffff0080
s_and_b32 s28, s2, 15
Mv 96,64,104
Rt 2,18,34816
s_add_u32 m0, s36, 0
Mv 128,64,108
buffer_load_dwordx4 v8, s[4:7], s38 offen offset:0 lds
Mv 160,64,112
Rt 4,19,32768
s_lshl4_add_u32 s88, s88, s28
Mv 192,64,116
s_lshl2_add_u32 s88, s88, s33
s_mul_i32 s88, s88, 4096
Mv 224,64,120
Rt 6,19,34816
s_add_u32 s88, s88, 0x100000
Ma 224,64,124
buffer_load_dwordx4 v9, s[4:7], s38 offen offset:1024 lds
Ma 228,68,124
Rt 8,20,32768
v_min_u32 v5, 31, v0
Mv 228,68,120
v_lshlrev_b32 v5, 7, v5
)" R"(
Mv 196,68,116
Rt 10,20,34816
Mv 164,68,112
buffer_load_dwordx4 v10, s[4:7], s38 offen offset:2048 lds
Mv 132,68,108
Rt 12,21,32768
Mv 100,68,104
Mv 68,68,100
Rt 14,21,34816
Mv 36,68,96
buffer_load_dwordx4 v11, s[4:7], s38 offen offset:3072 lds
Mv 40,72,96
Rt 16,22,32768
Mv 72,72,100
s_add_u32 s38, s38, 0x10000
Mv 104,72,104
Rt 18,22,34816
s_add_u32 m0, s36, 4096
Mv 136,72,108
buffer_load_dwordx4 v8, s[4:7], s38 offen offset:0 lds
Mv 168,72,112
Rt 20,23,32768
Mv 200,72,116
Mv 232,72,120
Rt 22,23,34816
Ma 232,72,124
buffer_load_dwordx4 v9, s[4:7], s38 offen offset:1024 lds
Ma 236,76,124
Rt 24,24,32768
Mv 236,76,120
Mv 204,76,116
Rt 26,24,34816
Mv 172,76,112
buffer_load_dwordx4 v10, s[4:7], s38 offen offset:2048 lds
Mv 140,76,108
Rt 28,25,32768
Mv 108,76,104
Mv 76,76,100
Rt 30,25,34816
Mv 44,76,96
buffer_load_dwordx4 v11, s[4:7], s38 offen offset:3072 lds
Mv 48,80,96
Rb 32,16,32768
Mv 80,80,100
Mv 112,80,104
Rb 36,16,34816
Mv 144,80,108
s_add_u32 s39, s39, 0x40000
Mv 176,80,112
Rb 40,16,40960
s_add_u32 m0, s37, 16384
Mv 208,80,116
buffer_load_dwordx4 v12, s[8:11], s39 offen offset:0 lds
Mv 240,80,120
Rb 44,16,43008
Ma 240,80,124
Ma 244,84,124
Rb 48,16,49152
Mv 244,84,120
Mv 212,84,116
Rb 52,16,51200
Mv 180,84,112
buffer_load_dwordx4 v13, s[8:11], s39 offen offset:1024 lds
Mv 148,84,108
)" R"(
Rb 56,16,57344
Mv 116,84,104
Mv 84,84,100
Rb 60,16,59392
Mv 52,84,96
Mv 56,88,96
Mv 88,88,100
buffer_load_dwordx4 v14, s[8:11], s39 offen offset:2048 lds
Mv 120,88,104
Mv 152,88,108
Mv 184,88,112
Mv 216,88,116
Mv 248,88,120
Ma 248,88,124
buffer_load_dwordx4 v15, s[8:11], s39 offen offset:3072 lds
Ma 252,92,124
Mv 252,92,120
s_add_u32 s88, s88, 0x100000
Mv 220,92,116
Mv 188,92,112
buffer_load_dword v6, v5, s[8:11], s88 offen
Mv 156,92,108
Mv 124,92,104
Mv 92,92,100
Mv 60,92,96
s_waitcnt vmcnt(19) lgkmcnt(0)
s_barrier
Mv 32,0,32
Rt 64,18,49152
Mv 64,0,36
Mv 96,0,40
Rt 66,18,51200
Mv 128,0,44
s_add_u32 s39, s39, 0x40000
Mv 160,0,48
Rt 68,19,49152
s_add_u32 m0, s37, 32768
Mv 192,0,52
buffer_load_dwordx4 v12, s[8:11], s39 offen offset:0 lds
Mv 224,0,56
Rt 70,19,51200
Ma 224,0,60
Ma 228,4,60
Rt 72,20,49152
Mv 228,4,56
Mv 196,4,52
Rt 74,20,51200
Mv 164,4,48
Mv 132,4,44
Rt 76,21,49152
Mv 100,4,40
buffer_load_dwordx4 v13, s[8:11], s39 offen offset:1024 lds
Mv 68,4,36
Rt 78,21,51200
Mv 36,4,32
Mv 40,8,32
Rt 80,22,49152
Mv 72,8,36
Mv 104,8,40
Rt 82,22,51200
Mv 136,8,44
Mv 168,8,48
Rt 84,23,49152
Mv 200,8,52
buffer_load_dwordx4 v14, s[8:11], s39 offen offset:2048 lds
)" R"(
Mv 232,8,56
Rt 86,23,51200
Ma 232,8,60
Ma 236,12,60
Rt 88,24,49152
Mv 236,12,56
Mv 204,12,52
Rt 90,24,51200
Mv 172,12,48
Mv 140,12,44
Rt 92,25,49152
Mv 108,12,40
buffer_load_dwordx4 v15, s[8:11], s39 offen offset:3072 lds
Mv 76,12,36
Rt 94,25,51200
Mv 44,12,32
s_add_u32 s44, s43, 0
Mv 48,16,32
Rb 96,17,32768
s_add_u32 m0, s36, 131072
Mv 80,16,36
buffer_load_dwordx4 v26, s[12:15], s44 offen offset:0 lds
Mv 112,16,40
Rb 100,17,34816
Mv 144,16,44
Mv 176,16,48
Rb 104,17,40960
Mv 208,16,52
buffer_load_dwordx4 v27, s[12:15], s44 offen offset:1024 lds
Mv 240,16,56
Rb 108,17,43008
Ma 240,16,60
Ma 244,20,60
Rb 112,17,49152
Mv 244,20,56
s_add_u32 s44, s43, 12288
buffer_load_dwordx4 v26, s[12:15], s44 offen offset:2048 lds
Mv 212,20,52
Rb 116,17,51200
Mv 180,20,48
Mv 148,20,44
Rb 120,17,57344
Mv 116,20,40
buffer_load_dwordx4 v27, s[12:15], s44 offen offset:3072 lds
Mv 84,20,36
Rb 124,17,59392
Mv 52,20,32
Mv 56,24,32
s_add_u32 m0, s36, 135168
s_add_u32 s44, s43, 128
Mv 88,24,36
buffer_load_dwordx4 v26, s[12:15], s44 offen offset:0 lds
Mv 120,24,40
Mv 152,24,44
Mv 184,24,48
Mv 216,24,52
buffer_load_dwordx4 v27, s[12:15], s44 offen offset:1024 lds
Mv 248,24,56
Ma 248,24,60
Ma 252,28,60
Mv 252,28,56
s_add_u32 s44, s43, 12416
buffer_load_dwordx4 v26, s[12:15], s44 offen offset:2048 lds
Mv 220,28,52
)" R"(
Mv 188,28,48
Mv 156,28,44
Mv 124,28,40
buffer_load_dwordx4 v27, s[12:15], s44 offen offset:3072 lds
Mv 92,28,36
Mv 60,28,32
s_waitcnt vmcnt(17) lgkmcnt(0)
s_barrier
Mv 32,64,96
Rt 0,18,0
Mv 64,64,100
s_add_u32 s38, s38, 0xffff0080
Mv 96,64,104
Rt 2,18,2048
s_add_u32 m0, s36, 32768
Mv 128,64,108
buffer_load_dwordx4 v8, s[4:7], s38 offen offset:0 lds
Mv 160,64,112
Rt 4,19,0
Mv 192,64,116
Mv 224,64,120
Rt 6,19,2048
Ma 224,64,124
buffer_load_dwordx4 v9, s[4:7], s38 offen offset:1024 lds
Ma 228,68,124
Rt 8,20,0
s_lshl2_add_u32 s28, s2, s33
Mv 228,68,120
s_lshl_b32 s28, s28, 11
s_add_u32 s28, s28, 0xc100000
Mv 196,68,116
Rt 10,20,2048
v_min_u32 v3, 15, v0
Mv 164,68,112
buffer_load_dwordx4 v10, s[4:7], s38 offen offset:2048 lds
Mv 132,68,108
Rt 12,21,0
v_lshlrev_b32 v3, 7, v3
Mv 100,68,104
Mv 68,68,100
Rt 14,21,2048
Mv 36,68,96
buffer_load_dwordx4 v11, s[4:7], s38 offen offset:3072 lds
Mv 40,72,96
Rt 16,22,0
Mv 72,72,100
s_add_u32 s38, s38, 0x10000
Mv 104,72,104
Rt 18,22,2048
s_add_u32 m0, s36, 36864
Mv 136,72,108
buffer_load_dwordx4 v8, s[4:7], s38 offen offset:0 lds
Mv 168,72,112
Rt 20,23,0
Mv 200,72,116
Mv 232,72,120
Rt 22,23,2048
Ma 232,72,124
buffer_load_dwordx4 v9, s[4:7], s38 offen offset:1024 lds
Ma 236,76,124
Rt 24,24,0
Mv 236,76,120
Mv 204,76,116
Rt 26,24,2048
Mv 172,76,112
)" R"(
buffer_load_dwordx4 v10, s[4:7], s38 offen offset:2048 lds
Mv 140,76,108
Rt 28,25,0
Mv 108,76,104
Mv 76,76,100
Rt 30,25,2048
Mv 44,76,96
buffer_load_dwordx4 v11, s[4:7], s38 offen offset:3072 lds
Mv 48,80,96
Rb 32,16,0
Mv 80,80,100
Mv 112,80,104
Rb 36,16,2048
Mv 144,80,108
s_add_u32 s39, s39, 0x40000
Mv 176,80,112
Rb 40,16,8192
s_add_u32 m0, s37, 49152
Mv 208,80,116
buffer_load_dwordx4 v12, s[8:11], s39 offen offset:0 lds
Mv 240,80,120
Rb 44,16,10240
Ma 240,80,124
Ma 244,84,124
Rb 48,16,16384
Mv 244,84,120
Mv 212,84,116
Rb 52,16,18432
Mv 180,84,112
buffer_load_dwordx4 v13, s[8:11], s39 offen offset:1024 lds
Mv 148,84,108
Rb 56,16,24576
Mv 116,84,104
Mv 84,84,100
Rb 60,16,26624
Mv 52,84,96
Mv 56,88,96
Mv 88,88,100
buffer_load_dwordx4 v14, s[8:11], s39 offen offset:2048 lds
Mv 120,88,104
Mv 152,88,108
Mv 184,88,112
Mv 216,88,116
Mv 248,88,120
Ma 248,88,124
buffer_load_dwordx4 v15, s[8:11], s39 offen offset:3072 lds
Ma 252,92,124
Mv 252,92,120
s_add_u32 s88, s88, 0x100000
s_mov_b32 s40, 5
Mv 220,92,116
Mv 188,92,112
buffer_load_dword v6, v5, s[8:11], s88 offen
Mv 156,92,108
Mv 124,92,104
buffer_load_dword v6, v3, s[20:23], s28 offen
Mv 92,92,100
Mv 60,92,96
s_waitcnt vmcnt(27) lgkmcnt(0)
s_barrier
.LzPAK:
Mv 32,0,32
Rt 64,18,16384
Mv 64,0,36
Mv 96,0,40
)" R"(
Rt 66,18,18432
Mv 128,0,44
s_add_u32 s39, s39, 0x40000
Mv 160,0,48
Rt 68,19,16384
s_add_u32 m0, s37, 0
Mv 192,0,52
buffer_load_dwordx4 v12, s[8:11], s39 offen offset:0 lds
Mv 224,0,56
Rt 70,19,18432
Ma 224,0,60
Ma 228,4,60
Rt 72,20,16384
Mv 228,4,56
Mv 196,4,52
Rt 74,20,18432
Mv 164,4,48
Mv 132,4,44
Rt 76,21,16384
Mv 100,4,40
buffer_load_dwordx4 v13, s[8:11], s39 offen offset:1024 lds
Mv 68,4,36
Rt 78,21,18432
Mv 36,4,32
Mv 40,8,32
Rt 80,22,16384
Mv 72,8,36
Mv 104,8,40
Rt 82,22,18432
Mv 136,8,44
Mv 168,8,48
Rt 84,23,16384
Mv 200,8,52
buffer_load_dwordx4 v14, s[8:11], s39 offen offset:2048 lds
Mv 232,8,56
Rt 86,23,18432
Ma 232,8,60
Ma 236,12,60
Rt 88,24,16384
Mv 236,12,56
Mv 204,12,52
Rt 90,24,18432
Mv 172,12,48
Mv 140,12,44
Rt 92,25,16384
Mv 108,12,40
buffer_load_dwordx4 v15, s[8:11], s39 offen offset:3072 lds
Mv 76,12,36
Rt 94,25,18432
Mv 44,12,32
Mv 48,16,32
Rb 96,17,0
Mv 80,16,36
Mv 112,16,40
Rb 100,17,2048
Mv 144,16,44
Mv 176,16,48
Rb 104,17,8192
Mv 208,16,52
Mv 240,16,56
Rb 108,17,10240
Ma 240,16,60
Ma 244,20,60
Rb 112,17,16384
)" R"(
Mv 244,20,56
Mv 212,20,52
Rb 116,17,18432
Mv 180,20,48
Mv 148,20,44
Rb 120,17,24576
Mv 116,20,40
Mv 84,20,36
Rb 124,17,26624
Mv 52,20,32
Mv 56,24,32
Mv 88,24,36
Mv 120,24,40
Mv 152,24,44
Mv 184,24,48
Mv 216,24,52
Mv 248,24,56
Ma 248,24,60
Ma 252,28,60
Mv 252,28,56
Mv 220,28,52
Mv 188,28,48
Mv 156,28,44
Mv 124,28,40
Mv 92,28,36
Mv 60,28,32
s_waitcnt vmcnt(9) lgkmcnt(0)
s_barrier
Mv 32,64,96
Rt 0,18,32768
Mv 64,64,100
s_add_u32 s38, s38, 0xffff0080
Mv 96,64,104
Rt 2,18,34816
s_add_u32 m0, s36, 0
Mv 128,64,108
buffer_load_dwordx4 v8, s[4:7], s38 offen offset:0 lds
Mv 160,64,112
Rt 4,19,32768
Mv 192,64,116
Mv 224,64,120
Rt 6,19,34816
Ma 224,64,124
buffer_load_dwordx4 v9, s[4:7], s38 offen offset:1024 lds
Ma 228,68,124
Rt 8,20,32768
Mv 228,68,120
Mv 196,68,116
Rt 10,20,34816
Mv 164,68,112
buffer_load_dwordx4 v10, s[4:7], s38 offen offset:2048 lds
Mv 132,68,108
Rt 12,21,32768
Mv 100,68,104
Mv 68,68,100
Rt 14,21,34816
Mv 36,68,96
buffer_load_dwordx4 v11, s[4:7], s38 offen offset:3072 lds
Mv 40,72,96
Rt 16,22,32768
Mv 72,72,100
s_add_u32 s38, s38, 0x10000
Mv 104,72,104
Rt 18,22,34816
s_add_u32 m0, s36, 4096
)" R"(
Mv 136,72,108
buffer_load_dwordx4 v8, s[4:7], s38 offen offset:0 lds
Mv 168,72,112
Rt 20,23,32768
Mv 200,72,116
Mv 232,72,120
Rt 22,23,34816
Ma 232,72,124
buffer_load_dwordx4 v9, s[4:7], s38 offen offset:1024 lds
Ma 236,76,124
Rt 24,24,32768
Mv 236,76,120
Mv 204,76,116
Rt 26,24,34816
Mv 172,76,112
buffer_load_dwordx4 v10, s[4:7], s38 offen offset:2048 lds
Mv 140,76,108
Rt 28,25,32768
Mv 108,76,104
Mv 76,76,100
Rt 30,25,34816
Mv 44,76,96
buffer_load_dwordx4 v11, s[4:7], s38 offen offset:3072 lds
Mv 48,80,96
Rb 32,16,32768
Mv 80,80,100
Mv 112,80,104
Rb 36,16,34816
Mv 144,80,108
s_add_u32 s39, s39, 0x40000
Mv 176,80,112
Rb 40,16,40960
s_add_u32 m0, s37, 16384
Mv 208,80,116
buffer_load_dwordx4 v12, s[8:11], s39 offen offset:0 lds
Mv 240,80,120
Rb 44,16,43008
Ma 240,80,124
Ma 244,84,124
Rb 48,16,49152
Mv 244,84,120
Mv 212,84,116
Rb 52,16,51200
Mv 180,84,112
buffer_load_dwordx4 v13, s[8:11], s39 offen offset:1024 lds
Mv 148,84,108
Rb 56,16,57344
Mv 116,84,104
Mv 84,84,100
Rb 60,16,59392
Mv 52,84,96
Mv 56,88,96
Mv 88,88,100
buffer_load_dwordx4 v14, s[8:11], s39 offen offset:2048 lds
Mv 120,88,104
Mv 152,88,108
Mv 184,88,112
Mv 216,88,116
Mv 248,88,120
Ma 248,88,124
buffer_load_dwordx4 v15, s[8:11], s39 offen offset:3072 lds
Ma 252,92,124
Mv 252,92,120
s_add_u32 s88, s88, 0x100000
)" R"(
Mv 220,92,116
Mv 188,92,112
buffer_load_dword v6, v5, s[8:11], s88 offen
Mv 156,92,108
Mv 124,92,104
Mv 92,92,100
Mv 60,92,96
s_waitcnt vmcnt(18) lgkmcnt(0)
s_barrier
Mv 32,0,32
Rt 64,18,49152
Mv 64,0,36
Mv 96,0,40
Rt 66,18,51200
Mv 128,0,44
s_add_u32 s39, s39, 0x40000
Mv 160,0,48
Rt 68,19,49152
s_add_u32 m0, s37, 32768
Mv 192,0,52
buffer_load_dwordx4 v12, s[8:11], s39 offen offset:0 lds
Mv 224,0,56
Rt 70,19,51200
Ma 224,0,60
Ma 228,4,60
Rt 72,20,49152
Mv 228,4,56
Mv 196,4,52
Rt 74,20,51200
Mv 164,4,48
Mv 132,4,44
Rt 76,21,49152
Mv 100,4,40
buffer_load_dwordx4 v13, s[8:11], s39 offen offset:1024 lds
Mv 68,4,36
Rt 78,21,51200
Mv 36,4,32
Mv 40,8,32
Rt 80,22,49152
Mv 72,8,36
Mv 104,8,40
Rt 82,22,51200
Mv 136,8,44
Mv 168,8,48
Rt 84,23,49152
Mv 200,8,52
buffer_load_dwordx4 v14, s[8:11], s39 offen offset:2048 lds
Mv 232,8,56
Rt 86,23,51200
Ma 232,8,60
Ma 236,12,60
Rt 88,24,49152
Mv 236,12,56
Mv 204,12,52
Rt 90,24,51200
Mv 172,12,48
Mv 140,12,44
Rt 92,25,49152
Mv 108,12,40
buffer_load_dwordx4 v15, s[8:11], s39 offen offset:3072 lds
Mv 76,12,36
Rt 94,25,51200
Mv 44,12,32
Mv 48,16,32
Rb 96,17,32768
)" R"(
Mv 80,16,36
Mv 112,16,40
Rb 100,17,34816
Mv 144,16,44
Mv 176,16,48
Rb 104,17,40960
Mv 208,16,52
Mv 240,16,56
Rb 108,17,43008
Ma 240,16,60
Ma 244,20,60
Rb 112,17,49152
Mv 244,20,56
Mv 212,20,52
Rb 116,17,51200
Mv 180,20,48
Mv 148,20,44
Rb 120,17,57344
Mv 116,20,40
Mv 84,20,36
Rb 124,17,59392
Mv 52,20,32
Mv 56,24,32
Mv 88,24,36
Mv 120,24,40
Mv 152,24,44
Mv 184,24,48
Mv 216,24,52
Mv 248,24,56
Ma 248,24,60
Ma 252,28,60
Mv 252,28,56
Mv 220,28,52
Mv 188,28,48
Mv 156,28,44
Mv 124,28,40
Mv 92,28,36
Mv 60,28,32
s_waitcnt vmcnt(9) lgkmcnt(0)
s_barrier
Mv 32,64,96
Rt 0,18,0
Mv 64,64,100
s_add_u32 s38, s38, 0xffff0080
Mv 96,64,104
Rt 2,18,2048
s_add_u32 m0, s36, 32768
Mv 128,64,108
buffer_load_dwordx4 v8, s[4:7], s38 offen offset:0 lds
Mv 160,64,112
Rt 4,19,0
Mv 192,64,116
Mv 224,64,120
Rt 6,19,2048
Ma 224,64,124
buffer_load_dwordx4 v9, s[4:7], s38 offen offset:1024 lds
Ma 228,68,124
Rt 8,20,0
Mv 228,68,120
Mv 196,68,116
Rt 10,20,2048
Mv 164,68,112
buffer_load_dwordx4 v10, s[4:7], s38 offen offset:2048 lds
Mv 132,68,108
Rt 12,21,0
)" R"(
Mv 100,68,104
Mv 68,68,100
Rt 14,21,2048
Mv 36,68,96
buffer_load_dwordx4 v11, s[4:7], s38 offen offset:3072 lds
Mv 40,72,96
Rt 16,22,0
Mv 72,72,100
s_add_u32 s38, s38, 0x10000
Mv 104,72,104
Rt 18,22,2048
s_add_u32 m0, s36, 36864
Mv 136,72,108
buffer_load_dwordx4 v8, s[4:7], s38 offen offset:0 lds
Mv 168,72,112
Rt 20,23,0
Mv 200,72,116
Mv 232,72,120
Rt 22,23,2048
Ma 232,72,124
buffer_load_dwordx4 v9, s[4:7], s38 offen offset:1024 lds
Ma 236,76,124
Rt 24,24,0
Mv 236,76,120
Mv 204,76,116
Rt 26,24,2048
Mv 172,76,112
buffer_load_dwordx4 v10, s[4:7], s38 offen offset:2048 lds
Mv 140,76,108
Rt 28,25,0
Mv 108,76,104
Mv 76,76,100
Rt 30,25,2048
Mv 44,76,96
buffer_load_dwordx4 v11, s[4:7], s38 offen offset:3072 lds
Mv 48,80,96
Rb 32,16,0
Mv 80,80,100
Mv 112,80,104
Rb 36,16,2048
Mv 144,80,108
s_add_u32 s39, s39, 0x40000
Mv 176,80,112
Rb 40,16,8192
s_add_u32 m0, s37, 49152
Mv 208,80,116
buffer_load_dwordx4 v12, s[8:11], s39 offen offset:0 lds
Mv 240,80,120
Rb 44,16,10240
Ma 240,80,124
Ma 244,84,124
Rb 48,16,16384
Mv 244,84,120
Mv 212,84,116
Rb 52,16,18432
Mv 180,84,112
buffer_load_dwordx4 v13, s[8:11], s39 offen offset:1024 lds
Mv 148,84,108
Rb 56,16,24576
Mv 116,84,104
Mv 84,84,100
Rb 60,16,26624
Mv 52,84,96
Mv 56,88,96
)" R"(
Mv 88,88,100
buffer_load_dwordx4 v14, s[8:11], s39 offen offset:2048 lds
Mv 120,88,104
Mv 152,88,108
Mv 184,88,112
Mv 216,88,116
Mv 248,88,120
Ma 248,88,124
buffer_load_dwordx4 v15, s[8:11], s39 offen offset:3072 lds
Ma 252,92,124
Mv 252,92,120
s_add_u32 s88, s88, 0x100000
Mv 220,92,116
s_sub_u32 s40, s40, 1
Mv 188,92,112
buffer_load_dword v6, v5, s[8:11], s88 offen
Mv 156,92,108
Mv 124,92,104
Mv 92,92,100
Mv 60,92,96
s_cmp_lg_u32 s40, 0
s_waitcnt vmcnt(18) lgkmcnt(0)
s_barrier
s_cbranch_scc1 .LzPAK
s_branch .LzPAKX
.LzAV:
Mz 32,32,0
Rt 64,18,16384
Mz 36,36,0
s_lshl1_add_u32 s40, s34, s91
s_lshl_b32 s40, s40, 12
Mz 96,40,0
Rt 66,18,18432
s_and_b32 s29, s38, 0xfffe0000
Mz 100,44,0
s_add_u32 s39, s39, 0x40000
s_add_u32 s40, s40, s29
Mz 160,48,0
Rt 68,19,16384
s_add_u32 m0, s37, 0
Mz 164,52,0
buffer_load_dwordx4 v12, s[8:11], s39 offen offset:0 lds
Mz 224,56,0
Rt 70,19,18432
v_min_u32 v3, 31, v0
Mz 228,60,0
v_lshlrev_b32 v3, 7, v3
Mz 236,60,4
Rt 72,20,16384
Mz 232,56,4
Mz 172,52,4
Rt 74,20,18432
Mz 168,48,4
Mz 108,44,4
Rt 76,21,16384
Mz 104,40,4
buffer_load_dwordx4 v13, s[8:11], s39 offen offset:1024 lds
Mz 44,36,4
Rt 78,21,18432
Mz 40,32,4
Mz 48,32,8
Rt 80,22,16384
Mz 52,36,8
Mz 112,40,8
Rt 82,22,18432
)" R"(
Mz 116,44,8
Mz 176,48,8
Rt 84,23,16384
Mz 180,52,8
buffer_load_dwordx4 v14, s[8:11], s39 offen offset:2048 lds
Mz 240,56,8
Rt 86,23,18432
Mz 244,60,8
Mz 252,60,12
Rt 88,24,16384
Mz 248,56,12
Mz 188,52,12
Rt 90,24,18432
Mz 184,48,12
Mz 124,44,12
Rt 92,25,16384
Mz 120,40,12
buffer_load_dwordx4 v15, s[8:11], s39 offen offset:3072 lds
Mz 60,36,12
Rt 94,25,18432
Mz 56,32,12
Mz 64,32,16
Rb 96,17,0
Mz 68,36,16
buffer_load_dword v6, v3, s[4:7], s40 offen
Mz 128,40,16
Rb 100,17,2048
Mz 132,44,16
Mz 192,48,16
Rb 104,17,8192
Mz 196,52,16
v_mfma_f32_16x16x32_bf16 a[224:227], a[56:59], a[16:19], 0
Rb 108,17,10240
v_mfma_f32_16x16x32_bf16 a[228:231], a[60:63], a[16:19], 0
v_mfma_f32_16x16x32_bf16 a[236:239], a[60:63], a[20:23], 0
Rb 112,17,16384
v_mfma_f32_16x16x32_bf16 a[232:235], a[56:59], a[20:23], 0
Mz 204,52,20
Rb 116,17,18432
Mz 200,48,20
Mz 140,44,20
Rb 120,17,24576
Mz 136,40,20
Mz 76,36,20
Rb 124,17,26624
Mz 72,32,20
Mz 80,32,24
Mz 84,36,24
Mz 144,40,24
Mz 148,44,24
Mz 208,48,24
Mz 212,52,24
v_mfma_f32_16x16x32_bf16 a[240:243], a[56:59], a[24:27], 0
v_mfma_f32_16x16x32_bf16 a[244:247], a[60:63], a[24:27], 0
v_mfma_f32_16x16x32_bf16 a[252:255], a[60:63], a[28:31], 0
v_mfma_f32_16x16x32_bf16 a[248:251], a[56:59], a[28:31], 0
Mz 220,52,28
Mz 216,48,28
Mz 156,44,28
Mz 152,40,28
Mz 92,36,28
Mz 88,32,28
s_waitcnt vmcnt(10) lgkmcnt(0)
s_barrier
Mv 32,96,64
)" R"(
Rt 0,18,32768
s_bfe_u32 s88, s2, 0x20005
Mv 36,100,64
s_add_u32 s38, s38, 0xffff0080
s_and_b32 s28, s2, 15
Mv 96,104,64
Rt 2,18,34816
s_add_u32 m0, s36, 0
Mv 100,108,64
buffer_load_dwordx4 v8, s[4:7], s38 offen offset:0 lds
Mv 160,112,64
Rt 4,19,32768
s_lshl4_add_u32 s88, s88, s28
Mv 164,116,64
s_lshl2_add_u32 s88, s88, s33
s_mul_i32 s88, s88, 4096
Mv 224,120,64
Rt 6,19,34816
s_add_u32 s88, s88, 0x100000
Mv 228,124,64
buffer_load_dwordx4 v9, s[4:7], s38 offen offset:1024 lds
Mv 236,124,68
Rt 8,20,32768
v_min_u32 v5, 31, v0
Mv 232,120,68
v_lshlrev_b32 v5, 7, v5
Mv 172,116,68
Rt 10,20,34816
Mv 168,112,68
buffer_load_dwordx4 v10, s[4:7], s38 offen offset:2048 lds
Mv 108,108,68
Rt 12,21,32768
Mv 104,104,68
Mv 44,100,68
Rt 14,21,34816
Mv 40,96,68
buffer_load_dwordx4 v11, s[4:7], s38 offen offset:3072 lds
Mv 48,96,72
Rt 16,22,32768
Mv 52,100,72
s_add_u32 s38, s38, 0x10000
Mv 112,104,72
Rt 18,22,34816
s_add_u32 m0, s36, 4096
Mv 116,108,72
buffer_load_dwordx4 v8, s[4:7], s38 offen offset:0 lds
Mv 176,112,72
Rt 20,23,32768
Mv 180,116,72
Mv 240,120,72
Rt 22,23,34816
Mv 244,124,72
buffer_load_dwordx4 v9, s[4:7], s38 offen offset:1024 lds
Mv 252,124,76
Rt 24,24,32768
Mv 248,120,76
Mv 188,116,76
Rt 26,24,34816
Mv 184,112,76
buffer_load_dwordx4 v10, s[4:7], s38 offen offset:2048 lds
Mv 124,108,76
Rt 28,25,32768
Mv 120,104,76
Mv 60,100,76
)" R"(
Rt 30,25,34816
Mv 56,96,76
buffer_load_dwordx4 v11, s[4:7], s38 offen offset:3072 lds
Mv 64,96,80
Rb 32,16,32768
Mv 68,100,80
Mv 128,104,80
Rb 36,16,34816
Mv 132,108,80
s_add_u32 s39, s39, 0x40000
Mv 192,112,80
Rb 40,16,40960
s_add_u32 m0, s37, 16384
Mv 196,116,80
buffer_load_dwordx4 v12, s[8:11], s39 offen offset:0 lds
Ma 224,120,80
Rb 44,16,43008
Ma 228,124,80
Ma 236,124,84
Rb 48,16,49152
Ma 232,120,84
Mv 204,116,84
Rb 52,16,51200
Mv 200,112,84
buffer_load_dwordx4 v13, s[8:11], s39 offen offset:1024 lds
Mv 140,108,84
Rb 56,16,57344
Mv 136,104,84
Mv 76,100,84
Rb 60,16,59392
Mv 72,96,84
Mv 80,96,88
Mv 84,100,88
buffer_load_dwordx4 v14, s[8:11], s39 offen offset:2048 lds
Mv 144,104,88
Mv 148,108,88
Mv 208,112,88
Mv 212,116,88
Ma 240,120,88
Ma 244,124,88
buffer_load_dwordx4 v15, s[8:11], s39 offen offset:3072 lds
Ma 252,124,92
Ma 248,120,92
s_add_u32 s88, s88, 0x100000
Mv 220,116,92
Mv 216,112,92
buffer_load_dword v6, v5, s[8:11], s88 offen
Mv 156,108,92
Mv 152,104,92
Mv 92,100,92
Mv 88,96,92
s_waitcnt vmcnt(19) lgkmcnt(0)
s_barrier
Mv 32,32,0
Rt 64,18,49152
Mv 36,36,0
Mv 96,40,0
Rt 66,18,51200
Mv 100,44,0
s_add_u32 s39, s39, 0x40000
Mv 160,48,0
Rt 68,19,49152
s_add_u32 m0, s37, 32768
Mv 164,52,0
buffer_load_dwordx4 v12, s[8:11], s39 offen offset:0 lds
)" R"(
Mv 224,56,0
Rt 70,19,51200
Mv 228,60,0
Mv 236,60,4
Rt 72,20,49152
Mv 232,56,4
Mv 172,52,4
Rt 74,20,51200
Mv 168,48,4
Mv 108,44,4
Rt 76,21,49152
Mv 104,40,4
buffer_load_dwordx4 v13, s[8:11], s39 offen offset:1024 lds
Mv 44,36,4
Rt 78,21,51200
Mv 40,32,4
Mv 48,32,8
Rt 80,22,49152
Mv 52,36,8
Mv 112,40,8
Rt 82,22,51200
Mv 116,44,8
Mv 176,48,8
Rt 84,23,49152
Mv 180,52,8
buffer_load_dwordx4 v14, s[8:11], s39 offen offset:2048 lds
Mv 240,56,8
Rt 86,23,51200
Mv 244,60,8
Mv 252,60,12
Rt 88,24,49152
Mv 248,56,12
Mv 188,52,12
Rt 90,24,51200
Mv 184,48,12
Mv 124,44,12
Rt 92,25,49152
Mv 120,40,12
buffer_load_dwordx4 v15, s[8:11], s39 offen offset:3072 lds
Mv 60,36,12
Rt 94,25,51200
Mv 56,32,12
s_add_u32 s44, s43, 0
Mv 64,32,16
Rb 96,17,32768
s_add_u32 m0, s36, 131072
Mv 68,36,16
buffer_load_dwordx4 v26, s[12:15], s44 offen offset:0 lds
Mv 128,40,16
Rb 100,17,34816
Mv 132,44,16
Mv 192,48,16
Rb 104,17,40960
Mv 196,52,16
buffer_load_dwordx4 v27, s[12:15], s44 offen offset:1024 lds
Ma 224,56,16
Rb 108,17,43008
Ma 228,60,16
Ma 236,60,20
Rb 112,17,49152
Ma 232,56,20
s_add_u32 s44, s43, 12288
buffer_load_dwordx4 v26, s[12:15], s44 offen offset:2048 lds
Mv 204,52,20
)" R"(
Rb 116,17,51200
Mv 200,48,20
Mv 140,44,20
Rb 120,17,57344
Mv 136,40,20
buffer_load_dwordx4 v27, s[12:15], s44 offen offset:3072 lds
Mv 76,36,20
Rb 124,17,59392
Mv 72,32,20
Mv 80,32,24
s_add_u32 m0, s36, 135168
s_add_u32 s44, s43, 128
Mv 84,36,24
buffer_load_dwordx4 v26, s[12:15], s44 offen offset:0 lds
Mv 144,40,24
Mv 148,44,24
Mv 208,48,24
Mv 212,52,24
buffer_load_dwordx4 v27, s[12:15], s44 offen offset:1024 lds
Ma 240,56,24
Ma 244,60,24
Ma 252,60,28
Ma 248,56,28
s_add_u32 s44, s43, 12416
buffer_load_dwordx4 v26, s[12:15], s44 offen offset:2048 lds
Mv 220,52,28
Mv 216,48,28
Mv 156,44,28
Mv 152,40,28
buffer_load_dwordx4 v27, s[12:15], s44 offen offset:3072 lds
Mv 92,36,28
Mv 88,32,28
s_waitcnt vmcnt(17) lgkmcnt(0)
s_barrier
Mv 32,96,64
Rt 0,18,0
Mv 36,100,64
s_add_u32 s38, s38, 0xffff0080
Mv 96,104,64
Rt 2,18,2048
s_add_u32 m0, s36, 32768
Mv 100,108,64
buffer_load_dwordx4 v8, s[4:7], s38 offen offset:0 lds
Mv 160,112,64
Rt 4,19,0
Mv 164,116,64
Mv 224,120,64
Rt 6,19,2048
Mv 228,124,64
buffer_load_dwordx4 v9, s[4:7], s38 offen offset:1024 lds
Mv 236,124,68
Rt 8,20,0
s_lshl2_add_u32 s28, s2, s33
Mv 232,120,68
s_lshl_b32 s28, s28, 11
s_add_u32 s28, s28, 0xc100000
Mv 172,116,68
Rt 10,20,2048
v_min_u32 v3, 15, v0
Mv 168,112,68
buffer_load_dwordx4 v10, s[4:7], s38 offen offset:2048 lds
Mv 108,108,68
Rt 12,21,0
v_lshlrev_b32 v3, 7, v3
Mv 104,104,68
)" R"(
Mv 44,100,68
Rt 14,21,2048
Mv 40,96,68
buffer_load_dwordx4 v11, s[4:7], s38 offen offset:3072 lds
Mv 48,96,72
Rt 16,22,0
Mv 52,100,72
s_add_u32 s38, s38, 0x10000
Mv 112,104,72
Rt 18,22,2048
s_add_u32 m0, s36, 36864
Mv 116,108,72
buffer_load_dwordx4 v8, s[4:7], s38 offen offset:0 lds
Mv 176,112,72
Rt 20,23,0
Mv 180,116,72
Mv 240,120,72
Rt 22,23,2048
Mv 244,124,72
buffer_load_dwordx4 v9, s[4:7], s38 offen offset:1024 lds
Mv 252,124,76
Rt 24,24,0
Mv 248,120,76
Mv 188,116,76
Rt 26,24,2048
Mv 184,112,76
buffer_load_dwordx4 v10, s[4:7], s38 offen offset:2048 lds
Mv 124,108,76
Rt 28,25,0
Mv 120,104,76
Mv 60,100,76
Rt 30,25,2048
Mv 56,96,76
buffer_load_dwordx4 v11, s[4:7], s38 offen offset:3072 lds
Mv 64,96,80
Rb 32,16,0
Mv 68,100,80
Mv 128,104,80
Rb 36,16,2048
Mv 132,108,80
s_add_u32 s39, s39, 0x40000
Mv 192,112,80
Rb 40,16,8192
s_add_u32 m0, s37, 49152
Mv 196,116,80
buffer_load_dwordx4 v12, s[8:11], s39 offen offset:0 lds
Ma 224,120,80
Rb 44,16,10240
Ma 228,124,80
Ma 236,124,84
Rb 48,16,16384
Ma 232,120,84
Mv 204,116,84
Rb 52,16,18432
Mv 200,112,84
buffer_load_dwordx4 v13, s[8:11], s39 offen offset:1024 lds
Mv 140,108,84
Rb 56,16,24576
Mv 136,104,84
Mv 76,100,84
Rb 60,16,26624
Mv 72,96,84
Mv 80,96,88
Mv 84,100,88
)" R"(
buffer_load_dwordx4 v14, s[8:11], s39 offen offset:2048 lds
Mv 144,104,88
Mv 148,108,88
Mv 208,112,88
Mv 212,116,88
Ma 240,120,88
Ma 244,124,88
buffer_load_dwordx4 v15, s[8:11], s39 offen offset:3072 lds
Ma 252,124,92
Ma 248,120,92
s_add_u32 s88, s88, 0x100000
s_mov_b32 s40, 5
Mv 220,116,92
Mv 216,112,92
buffer_load_dword v6, v5, s[8:11], s88 offen
Mv 156,108,92
Mv 152,104,92
buffer_load_dword v6, v3, s[20:23], s28 offen
Mv 92,100,92
Mv 88,96,92
s_waitcnt vmcnt(27) lgkmcnt(0)
s_barrier
.LzPAV:
Mv 32,32,0
Rt 64,18,16384
Mv 36,36,0
Mv 96,40,0
Rt 66,18,18432
Mv 100,44,0
s_add_u32 s39, s39, 0x40000
Mv 160,48,0
Rt 68,19,16384
s_add_u32 m0, s37, 0
Mv 164,52,0
buffer_load_dwordx4 v12, s[8:11], s39 offen offset:0 lds
Mv 224,56,0
Rt 70,19,18432
Mv 228,60,0
Mv 236,60,4
Rt 72,20,16384
Mv 232,56,4
Mv 172,52,4
Rt 74,20,18432
Mv 168,48,4
Mv 108,44,4
Rt 76,21,16384
Mv 104,40,4
buffer_load_dwordx4 v13, s[8:11], s39 offen offset:1024 lds
Mv 44,36,4
Rt 78,21,18432
Mv 40,32,4
Mv 48,32,8
Rt 80,22,16384
Mv 52,36,8
Mv 112,40,8
Rt 82,22,18432
Mv 116,44,8
Mv 176,48,8
Rt 84,23,16384
Mv 180,52,8
buffer_load_dwordx4 v14, s[8:11], s39 offen offset:2048 lds
Mv 240,56,8
Rt 86,23,18432
Mv 244,60,8
Mv 252,60,12
)" R"(
Rt 88,24,16384
Mv 248,56,12
Mv 188,52,12
Rt 90,24,18432
Mv 184,48,12
Mv 124,44,12
Rt 92,25,16384
Mv 120,40,12
buffer_load_dwordx4 v15, s[8:11], s39 offen offset:3072 lds
Mv 60,36,12
Rt 94,25,18432
Mv 56,32,12
Mv 64,32,16
Rb 96,17,0
Mv 68,36,16
Mv 128,40,16
Rb 100,17,2048
Mv 132,44,16
Mv 192,48,16
Rb 104,17,8192
Mv 196,52,16
Ma 224,56,16
Rb 108,17,10240
Ma 228,60,16
Ma 236,60,20
Rb 112,17,16384
Ma 232,56,20
Mv 204,52,20
Rb 116,17,18432
Mv 200,48,20
Mv 140,44,20
Rb 120,17,24576
Mv 136,40,20
Mv 76,36,20
Rb 124,17,26624
Mv 72,32,20
Mv 80,32,24
Mv 84,36,24
Mv 144,40,24
Mv 148,44,24
Mv 208,48,24
Mv 212,52,24
Ma 240,56,24
Ma 244,60,24
Ma 252,60,28
Ma 248,56,28
Mv 220,52,28
Mv 216,48,28
Mv 156,44,28
Mv 152,40,28
Mv 92,36,28
Mv 88,32,28
s_waitcnt vmcnt(9) lgkmcnt(0)
s_barrier
Mv 32,96,64
Rt 0,18,32768
Mv 36,100,64
s_add_u32 s38, s38, 0xffff0080
Mv 96,104,64
Rt 2,18,34816
s_add_u32 m0, s36, 0
Mv 100,108,64
buffer_load_dwordx4 v8, s[4:7], s38 offen offset:0 lds
Mv 160,112,64
Rt 4,19,32768
)" R"(
Mv 164,116,64
Mv 224,120,64
Rt 6,19,34816
Mv 228,124,64
buffer_load_dwordx4 v9, s[4:7], s38 offen offset:1024 lds
Mv 236,124,68
Rt 8,20,32768
Mv 232,120,68
Mv 172,116,68
Rt 10,20,34816
Mv 168,112,68
buffer_load_dwordx4 v10, s[4:7], s38 offen offset:2048 lds
Mv 108,108,68
Rt 12,21,32768
Mv 104,104,68
Mv 44,100,68
Rt 14,21,34816
Mv 40,96,68
buffer_load_dwordx4 v11, s[4:7], s38 offen offset:3072 lds
Mv 48,96,72
Rt 16,22,32768
Mv 52,100,72
s_add_u32 s38, s38, 0x10000
Mv 112,104,72
Rt 18,22,34816
s_add_u32 m0, s36, 4096
Mv 116,108,72
buffer_load_dwordx4 v8, s[4:7], s38 offen offset:0 lds
Mv 176,112,72
Rt 20,23,32768
Mv 180,116,72
Mv 240,120,72
Rt 22,23,34816
Mv 244,124,72
buffer_load_dwordx4 v9, s[4:7], s38 offen offset:1024 lds
Mv 252,124,76
Rt 24,24,32768
Mv 248,120,76
Mv 188,116,76
Rt 26,24,34816
Mv 184,112,76
buffer_load_dwordx4 v10, s[4:7], s38 offen offset:2048 lds
Mv 124,108,76
Rt 28,25,32768
Mv 120,104,76
Mv 60,100,76
Rt 30,25,34816
Mv 56,96,76
buffer_load_dwordx4 v11, s[4:7], s38 offen offset:3072 lds
Mv 64,96,80
Rb 32,16,32768
Mv 68,100,80
Mv 128,104,80
Rb 36,16,34816
Mv 132,108,80
s_add_u32 s39, s39, 0x40000
Mv 192,112,80
Rb 40,16,40960
s_add_u32 m0, s37, 16384
Mv 196,116,80
buffer_load_dwordx4 v12, s[8:11], s39 offen offset:0 lds
Ma 224,120,80
Rb 44,16,43008
Ma 228,124,80
)" R"(
Ma 236,124,84
Rb 48,16,49152
Ma 232,120,84
Mv 204,116,84
Rb 52,16,51200
Mv 200,112,84
buffer_load_dwordx4 v13, s[8:11], s39 offen offset:1024 lds
Mv 140,108,84
Rb 56,16,57344
Mv 136,104,84
Mv 76,100,84
Rb 60,16,59392
Mv 72,96,84
Mv 80,96,88
Mv 84,100,88
buffer_load_dwordx4 v14, s[8:11], s39 offen offset:2048 lds
Mv 144,104,88
Mv 148,108,88
Mv 208,112,88
Mv 212,116,88
Ma 240,120,88
Ma 244,124,88
buffer_load_dwordx4 v15, s[8:11], s39 offen offset:3072 lds
Ma 252,124,92
Ma 248,120,92
s_add_u32 s88, s88, 0x100000
Mv 220,116,92
Mv 216,112,92
buffer_load_dword v6, v5, s[8:11], s88 offen
Mv 156,108,92
Mv 152,104,92
Mv 92,100,92
Mv 88,96,92
s_waitcnt vmcnt(18) lgkmcnt(0)
s_barrier
Mv 32,32,0
Rt 64,18,49152
Mv 36,36,0
Mv 96,40,0
Rt 66,18,51200
Mv 100,44,0
s_add_u32 s39, s39, 0x40000
Mv 160,48,0
Rt 68,19,49152
s_add_u32 m0, s37, 32768
Mv 164,52,0
buffer_load_dwordx4 v12, s[8:11], s39 offen offset:0 lds
Mv 224,56,0
Rt 70,19,51200
Mv 228,60,0
Mv 236,60,4
Rt 72,20,49152
Mv 232,56,4
Mv 172,52,4
Rt 74,20,51200
Mv 168,48,4
Mv 108,44,4
Rt 76,21,49152
Mv 104,40,4
buffer_load_dwordx4 v13, s[8:11], s39 offen offset:1024 lds
Mv 44,36,4
Rt 78,21,51200
Mv 40,32,4
Mv 48,32,8
Rt 80,22,49152
)" R"(
Mv 52,36,8
Mv 112,40,8
Rt 82,22,51200
Mv 116,44,8
Mv 176,48,8
Rt 84,23,49152
Mv 180,52,8
buffer_load_dwordx4 v14, s[8:11], s39 offen offset:2048 lds
Mv 240,56,8
Rt 86,23,51200
Mv 244,60,8
Mv 252,60,12
Rt 88,24,49152
Mv 248,56,12
Mv 188,52,12
Rt 90,24,51200
Mv 184,48,12
Mv 124,44,12
Rt 92,25,49152
Mv 120,40,12
buffer_load_dwordx4 v15, s[8:11], s39 offen offset:3072 lds
Mv 60,36,12
Rt 94,25,51200
Mv 56,32,12
Mv 64,32,16
Rb 96,17,32768
Mv 68,36,16
Mv 128,40,16
Rb 100,17,34816
Mv 132,44,16
Mv 192,48,16
Rb 104,17,40960
Mv 196,52,16
Ma 224,56,16
Rb 108,17,43008
Ma 228,60,16
Ma 236,60,20
Rb 112,17,49152
Ma 232,56,20
Mv 204,52,20
Rb 116,17,51200
Mv 200,48,20
Mv 140,44,20
Rb 120,17,57344
Mv 136,40,20
Mv 76,36,20
Rb 124,17,59392
Mv 72,32,20
Mv 80,32,24
Mv 84,36,24
Mv 144,40,24
Mv 148,44,24
Mv 208,48,24
Mv 212,52,24
Ma 240,56,24
Ma 244,60,24
Ma 252,60,28
Ma 248,56,28
Mv 220,52,28
Mv 216,48,28
Mv 156,44,28
Mv 152,40,28
Mv 92,36,28
Mv 88,32,28
)" R"(
s_waitcnt vmcnt(9) lgkmcnt(0)
s_barrier
Mv 32,96,64
Rt 0,18,0
Mv 36,100,64
s_add_u32 s38, s38, 0xffff0080
Mv 96,104,64
Rt 2,18,2048
s_add_u32 m0, s36, 32768
Mv 100,108,64
buffer_load_dwordx4 v8, s[4:7], s38 offen offset:0 lds
Mv 160,112,64
Rt 4,19,0
Mv 164,116,64
Mv 224,120,64
Rt 6,19,2048
Mv 228,124,64
buffer_load_dwordx4 v9, s[4:7], s38 offen offset:1024 lds
Mv 236,124,68
Rt 8,20,0
Mv 232,120,68
Mv 172,116,68
Rt 10,20,2048
Mv 168,112,68
buffer_load_dwordx4 v10, s[4:7], s38 offen offset:2048 lds
Mv 108,108,68
Rt 12,21,0
Mv 104,104,68
Mv 44,100,68
Rt 14,21,2048
Mv 40,96,68
buffer_load_dwordx4 v11, s[4:7], s38 offen offset:3072 lds
Mv 48,96,72
Rt 16,22,0
Mv 52,100,72
s_add_u32 s38, s38, 0x10000
Mv 112,104,72
Rt 18,22,2048
s_add_u32 m0, s36, 36864
Mv 116,108,72
buffer_load_dwordx4 v8, s[4:7], s38 offen offset:0 lds
Mv 176,112,72
Rt 20,23,0
Mv 180,116,72
Mv 240,120,72
Rt 22,23,2048
Mv 244,124,72
buffer_load_dwordx4 v9, s[4:7], s38 offen offset:1024 lds
Mv 252,124,76
Rt 24,24,0
Mv 248,120,76
Mv 188,116,76
Rt 26,24,2048
Mv 184,112,76
buffer_load_dwordx4 v10, s[4:7], s38 offen offset:2048 lds
Mv 124,108,76
Rt 28,25,0
Mv 120,104,76
Mv 60,100,76
Rt 30,25,2048
Mv 56,96,76
buffer_load_dwordx4 v11, s[4:7], s38 offen offset:3072 lds
Mv 64,96,80
Rb 32,16,0
Mv 68,100,80
)" R"(
Mv 128,104,80
Rb 36,16,2048
Mv 132,108,80
s_add_u32 s39, s39, 0x40000
Mv 192,112,80
Rb 40,16,8192
s_add_u32 m0, s37, 49152
Mv 196,116,80
buffer_load_dwordx4 v12, s[8:11], s39 offen offset:0 lds
Ma 224,120,80
Rb 44,16,10240
Ma 228,124,80
Ma 236,124,84
Rb 48,16,16384
Ma 232,120,84
Mv 204,116,84
Rb 52,16,18432
Mv 200,112,84
buffer_load_dwordx4 v13, s[8:11], s39 offen offset:1024 lds
Mv 140,108,84
Rb 56,16,24576
Mv 136,104,84
Mv 76,100,84
Rb 60,16,26624
Mv 72,96,84
Mv 80,96,88
Mv 84,100,88
buffer_load_dwordx4 v14, s[8:11], s39 offen offset:2048 lds
Mv 144,104,88
Mv 148,108,88
Mv 208,112,88
Mv 212,116,88
Ma 240,120,88
Ma 244,124,88
buffer_load_dwordx4 v15, s[8:11], s39 offen offset:3072 lds
Ma 252,124,92
Ma 248,120,92
s_add_u32 s88, s88, 0x100000
Mv 220,116,92
s_sub_u32 s40, s40, 1
Mv 216,112,92
buffer_load_dword v6, v5, s[8:11], s88 offen
Mv 156,108,92
Mv 152,104,92
Mv 92,100,92
Mv 88,96,92
s_cmp_lg_u32 s40, 0
s_waitcnt vmcnt(18) lgkmcnt(0)
s_barrier
s_cbranch_scc1 .LzPAV
s_branch .LzPAVX
.LzB0:
s_mul_i32 s41, s33, 7168
s_mul_i32 s42, s33, 0x70000
s_lshl_b32 s28, s34, 9
s_add_u32 s42, s42, s28
v_lshrrev_b32 v28, 3, v0
v_and_b32 v29, 0x70, v1
v_lshl_or_b32 v28, v28, 13, v29
v_lshlrev_b32 v29, 1, v0
v_and_b32 v29, 32, v29
v_xor_b32 v28, v28, v29
s_and_b32 s28, s33, 1
s_lshl_b32 s28, s28, 6
v_xor_b32 v240, s28, v28
)" R"(
v_xor_b32 v241, 64, v240
v_add_u32 v241, 0xfc00, v241
v_add_u32 v242, 0x1f800, v240
v_add_u32 v243, 0x1f800, v241
s_sub_u32 s28, 15, s35
s_lshl2_add_u32 s28, s28, s33
s_mul_i32 s43, s28, 28672
v_and_b32 v2, 0x70, v1
v_and_b32 v28, 48, v0
v_xor_b32 v2, v2, v28
v_lshrrev_b32 v28, 3, v0
v_mul_u32_u24 v28, 896, v28
v_add_u32 v2, v2, v28
v_xor_b32 v3, 64, v2
v_add_u32 v3, 6144, v3
v_bfe_u32 v26, v0, 1, 3
v_lshrrev_b32 v28, 4, v0
v_xor_b32 v26, v26, v28
v_lshlrev_b32 v26, 4, v26
v_and_b32 v28, 15, v0
v_lshl_or_b32 v26, v28, 7, v26
s_add_u32 s28, s36, 0x0
v_add_u32 v26, s28, v26
v_xor_b32 v27, 64, v26
s_waitcnt lgkmcnt(0)
s_mov_b32 s16, s68
s_mov_b32 s17, s69
s_mov_b32 s18, 0x380000
s_mov_b32 s19, 0x20000
s_mov_b32 s12, s64
s_mov_b32 s13, s65
s_mov_b32 s14, 0x1c0000
s_mov_b32 s15, 0x20000
s_add_u32 m0, s36, 0
s_add_u32 s44, s43, 0
buffer_load_dwordx4 v2, s[12:15], s44 offen offset:0 lds
buffer_load_dwordx4 v3, s[12:15], s44 offen offset:1024 lds
s_add_u32 s44, s43, 12288
buffer_load_dwordx4 v2, s[12:15], s44 offen offset:2048 lds
buffer_load_dwordx4 v3, s[12:15], s44 offen offset:3072 lds
s_add_u32 m0, s41, 65536
s_add_u32 s92, s42, 0x0
buffer_load_dwordx4 v240, s[16:19], s92 offen offset:0 lds
buffer_load_dwordx4 v241, s[16:19], s92 offen offset:1024 lds
buffer_load_dwordx4 v242, s[16:19], s92 offen offset:2048 lds
buffer_load_dwordx4 v243, s[16:19], s92 offen offset:3072 lds
s_add_u32 m0, s41, 69632
s_add_u32 s92, s42, 0x40000
buffer_load_dwordx4 v240, s[16:19], s92 offen offset:0 lds
buffer_load_dwordx4 v241, s[16:19], s92 offen offset:1024 lds
buffer_load_dwordx4 v242, s[16:19], s92 offen offset:2048 lds
s_add_u32 m0, s36, 4096
s_add_u32 s44, s43, 128
buffer_load_dwordx4 v2, s[12:15], s44 offen offset:0 lds
buffer_load_dwordx4 v3, s[12:15], s44 offen offset:1024 lds
s_add_u32 s44, s43, 12416
buffer_load_dwordx4 v2, s[12:15], s44 offen offset:2048 lds
buffer_load_dwordx4 v3, s[12:15], s44 offen offset:3072 lds
s_add_u32 m0, s36, 32768
s_add_u32 s44, s43, 256
buffer_load_dwordx4 v2, s[12:15], s44 offen offset:0 lds
buffer_load_dwordx4 v3, s[12:15], s44 offen offset:1024 lds
s_add_u32 s44, s43, 12544
buffer_load_dwordx4 v2, s[12:15], s44 offen offset:2048 lds
)" R"(
buffer_load_dwordx4 v3, s[12:15], s44 offen offset:3072 lds
s_add_u32 m0, s36, 36864
s_add_u32 s44, s43, 384
buffer_load_dwordx4 v2, s[12:15], s44 offen offset:0 lds
buffer_load_dwordx4 v3, s[12:15], s44 offen offset:1024 lds
s_add_u32 s44, s43, 12672
buffer_load_dwordx4 v2, s[12:15], s44 offen offset:2048 lds
buffer_load_dwordx4 v3, s[12:15], s44 offen offset:3072 lds
s_add_u32 m0, s41, 98304
s_add_u32 s92, s42, 0x80
buffer_load_dwordx4 v240, s[16:19], s92 offen offset:0 lds
buffer_load_dwordx4 v241, s[16:19], s92 offen offset:1024 lds
buffer_load_dwordx4 v242, s[16:19], s92 offen offset:2048 lds
buffer_load_dwordx4 v243, s[16:19], s92 offen offset:3072 lds
s_add_u32 m0, s41, 102400
s_add_u32 s92, s42, 0x40080
buffer_load_dwordx4 v240, s[16:19], s92 offen offset:0 lds
buffer_load_dwordx4 v241, s[16:19], s92 offen offset:1024 lds
buffer_load_dwordx4 v242, s[16:19], s92 offen offset:2048 lds
s_mov_b32 s28, 0xc108400
s_load_dword s3, s[74:75], s28
s_sub_u32 s94, s2, 128
s_lshl2_add_u32 s94, s94, s33
s_lshl_b32 s94, s94, 2
s_mul_i32 s29, s94, 896
v_mov_b32 v120, 0x40000000
v_cmp_gt_u32_e32 vcc, 56, v0
s_nop 1
v_cndmask_b32_e32 v120, v120, v1, vcc
buffer_load_dwordx4 v[96:99], v120, s[12:15], s29 offen offset:0
buffer_load_dwordx4 v[100:103], v120, s[12:15], s29 offen offset:896
buffer_load_dwordx4 v[104:107], v120, s[12:15], s29 offen offset:1792
buffer_load_dwordx4 v[108:111], v120, s[12:15], s29 offen offset:2688
s_mov_b32 s24, s76
s_mov_b32 s25, s77
s_mov_b32 s26, 0x380
s_mov_b32 s27, 0x20000
buffer_load_dwordx4 v[112:115], v120, s[24:27], 0 offen
s_mov_b32 s24, s78
s_mov_b32 s25, s79
s_mov_b32 s26, 0x380
s_mov_b32 s27, 0x20000
buffer_load_dwordx4 v[116:119], v120, s[24:27], 0 offen
s_mov_b32 s20, s74
s_mov_b32 s21, s75
s_mov_b32 s22, 0xc510000
s_mov_b32 s23, 0x20000
s_mov_b32 s4, s66
s_mov_b32 s5, s67
s_mov_b32 s6, 0x400000
s_mov_b32 s7, 0x20000
s_cmp_eq_u32 s91, 1
s_cselect_b32 s8, s72, s70
s_cselect_b32 s9, s73, s71
s_mov_b32 s10, 0x800000
s_mov_b32 s11, 0x20000
s_load_dwordx2 s[74:75], s[0:1], 96
s_min_u32 s28, s33, 2
s_lshl3_add_u32 s28, s28, 72
s_load_dwordx2 s[76:77], s[0:1], s28
v_bfe_u32 v244, v0, 3, 2
v_lshlrev_b32 v244, 5, v244
v_and_b32 v28, 3, v0
v_lshl_or_b32 v244, v28, 3, v244
)" R"(
v_bfe_u32 v28, v0, 2, 2
v_lshl_or_b32 v244, v28, 7, v244
v_lshrrev_b32 v28, 4, v0
v_lshl_or_b32 v244, v28, 10, v244
v_xor_b32 v245, 32, v244
v_xor_b32 v246, 64, v244
v_xor_b32 v247, 96, v244
v_add_u32 v252, 0x20000, v244
v_add_u32 v253, 0x20000, v245
v_add_u32 v254, 0x20000, v246
v_add_u32 v255, 0x20000, v247
v_add_u32 v248, 0x10000, v244
v_add_u32 v249, 0x10000, v245
v_add_u32 v250, 0x10000, v246
v_add_u32 v251, 0x10000, v247
s_mov_b32 s63, 0xe0c0a08
s_mov_b32 s28, 0x100d0b09
s_bitcmp1_b32 s2, 5
s_cmov_b32 s63, 0xf1e1c1a
s_cmov_b32 s28, 0x111f1d1b
s_bitcmp1_b32 s2, 6
s_cmov_b32 s63, s28
s_lshl_b32 s28, s33, 3
s_lshr_b32 s28, s63, s28
s_and_b32 s28, s28, 255
s_lshl_b32 s38, s28, 17
s_add_u32 s38, s38, 0xff80
s_lshl_b32 s39, s33, 16
s_lshl_b32 s28, s34, 9
s_add_u32 s39, s39, s28
s_sub_u32 s39, s39, 0x40000
s_lshl_b32 s37, s33, 12
s_add_u32 s37, s37, 0x10000
v_lshrrev_b32 v28, 4, v0
v_and_b32 v29, 7, v0
v_xor_b32 v28, v28, v29
v_lshlrev_b32 v28, 4, v28
v_lshrrev_b32 v29, 3, v0
v_lshl_or_b32 v8, v29, 11, v28
v_xor_b32 v9, 64, v8
v_add_u32 v9, 15360, v9
v_add_u32 v10, 30720, v8
v_add_u32 v11, 30720, v9
s_and_b32 s28, s33, 1
s_lshl_b32 s28, s28, 7
v_lshrrev_b32 v29, 5, v0
v_mul_u32_u24 v29, 0x2220, v29
v_xor_b32 v29, v29, v1
v_xor_b32 v12, s28, v29
v_xor_b32 v13, 64, v12
v_add_u32 v13, 15360, v13
v_add_u32 v14, 30720, v12
v_add_u32 v15, 30720, v13
v_bfe_u32 v28, v0, 1, 3
v_lshrrev_b32 v29, 4, v0
v_xor_b32 v28, v28, v29
v_lshlrev_b32 v28, 4, v28
v_and_b32 v29, 15, v0
v_lshl_or_b32 v28, v29, 7, v28
s_lshr_b32 s28, s33, 1
s_lshl_b32 s28, s28, 12
v_or_b32 v16, s28, v28
v_xor_b32 v17, 64, v16
v_bfe_u32 v28, v0, 2, 3
)" R"(
v_lshlrev_b32 v28, 5, v28
v_and_b32 v29, 3, v0
v_lshl_or_b32 v28, v29, 3, v28
v_bfe_u32 v29, v0, 2, 2
v_lshl_or_b32 v28, v29, 9, v28
v_lshrrev_b32 v29, 4, v0
v_lshl_or_b32 v28, v29, 12, v28
s_and_b32 s28, s33, 1
s_lshl_b32 s28, s28, 8
s_bitset1_b32 s28, 16
v_or_b32 v18, s28, v28
v_xor_b32 v19, 32, v18
v_xor_b32 v20, 64, v18
v_xor_b32 v21, 96, v18
v_xor_b32 v22, 128, v18
v_xor_b32 v23, 160, v18
v_xor_b32 v24, 192, v18
v_xor_b32 v25, 224, v18
s_lshl_b32 s28, s34, 21
s_lshl_b32 s29, s91, 25
s_add_u32 s28, s28, s29
s_bfe_u32 s29, s63, 0x80000
s_lshl_b32 s29, s29, 16
s_add_u32 s45, s28, s29
s_bfe_u32 s29, s63, 0x80008
s_lshl_b32 s29, s29, 16
s_add_u32 s46, s28, s29
s_bfe_u32 s29, s63, 0x80010
s_lshl_b32 s29, s29, 16
s_add_u32 s47, s28, s29
s_bfe_u32 s29, s63, 0x80018
s_lshl_b32 s29, s29, 16
s_add_u32 s48, s28, s29
s_lshl_b32 s50, s34, 11
s_add_u32 s50, s50, 0xc110000
s_lshl2_add_u32 s28, s91, s93
s_lshl_b32 s28, s28, 7
s_add_u32 s49, s28, s50
s_add_u32 s49, s49, 1024
s_lshl3_add_u32 s28, s34, s35
s_lshl_b32 s52, s28, 7
s_add_u32 s52, s52, 0xc118000
s_lshl4_add_u32 s28, s34, s35
s_lshl_b32 s51, s28, 17
s_add_u32 s51, s51, 0x4000000
s_cmp_lg_u32 s33, 0
s_cmov_b32 s49, 0x40000000
s_cmov_b32 s52, 0x40000000
s_mov_b32 s53, 0x40000
s_mov_b32 s54, 0x3db8aa3b
s_lshl_b32 s89, s34, 21
s_mov_b64 s[60:61], 0xff
s_mov_b32 s62, 0x4e800000
s_add_u32 s67, s89, 0x6000000
s_lshl3_add_u32 s28, s34, s35
s_add_u32 s64, s51, 0x100000
s_lshl2_add_u32 s28, s28, s33
s_lshl_b32 s65, s28, 7
s_add_u32 s65, s65, 0xc120000
s_lshl_b32 s66, s28, 10
s_add_u32 s66, s66, 0xc000000
s_sub_u32 s28, 15, s35
s_lshl2_add_u32 s28, s28, s33
s_lshl_b32 s29, s28, 5
)" R"(
s_add_u32 s29, s29, s34
s_lshl_b32 s73, s29, 15
s_add_u32 s73, s73, 0x8000000
s_lshr_b32 s29, s36, 1
s_and_b32 s29, s29, 0x2000
s_add_u32 s73, s73, s29
s_lshl4_add_u32 s28, s28, s34
s_lshl_b32 s68, s28, 7
s_add_u32 s68, s68, 0xc130000
s_xor_b32 s72, s73, 0x3c04000
s_mov_b32 s55, s89
s_mov_b32 s57, 18
s_mov_b32 s59, s67
s_mov_b32 s58, 0
s_cmp_eq_u32 s35, 7
s_cmov_b32 s58, 2
s_cmp_eq_u32 s35, 7
s_cmov_b32 s64, 0x40000000
s_cmp_eq_u32 s35, 7
s_cmov_b32 s66, 0x40000000
s_add_u32 s56, s55, 0x2000000
s_waitcnt vmcnt(21) lgkmcnt(0)
s_barrier
ds_read_b128 v[128:131], v26 offset:0
ds_read_b128 v[132:135], v26 offset:2048
ds_read_b128 v[136:139], v27 offset:0
ds_read_b128 v[140:143], v27 offset:2048
ds_read_b128 v[144:147], v26 offset:4096
ds_read_b128 v[148:151], v26 offset:6144
ds_read_b128 v[152:155], v27 offset:4096
ds_read_b128 v[156:159], v27 offset:6144
Rt 144,248,0
Rt 146,248,512
Rt 148,249,0
Rt 150,249,512
Rt 152,250,0
Rt 154,250,512
Rt 156,251,0
Rt 158,251,512
Rt 160,248,4096
Rt 162,248,4608
Rt 164,249,4096
Rt 166,249,4608
Rt 168,250,4096
Rt 170,250,4608
Rt 172,251,4096
Rt 174,251,4608
s_mul_i32 s3, s3, 1664525
s_add_u32 s3, s3, 1013904223
v_mov_b32 v6, s3
v_min_u32 v7, 1, v0
v_lshlrev_b32 v7, 30, v7
s_waitcnt lgkmcnt(8)
s_barrier
v_mfma_f32_16x16x32_bf16 v[32:35], a[144:147], v[128:131], 0
Rt 128,248,8192
s_add_u32 m0, s41, 131072
s_add_u32 s92, s42, 0x1c0000
v_mfma_f32_16x16x32_bf16 v[40:43], a[144:147], v[132:135], 0
Rt 130,248,8704
buffer_load_dwordx4 v240, s[16:19], s92 offen offset:0 lds
v_mfma_f32_16x16x32_bf16 v[44:47], a[148:151], v[132:135], 0
Rt 132,249,8192
v_mfma_f32_16x16x32_bf16 v[36:39], a[148:151], v[128:131], 0
Rt 134,249,8704
v_mfma_f32_16x16x32_bf16 v[48:51], a[152:155], v[128:131], 0
)" R"(
Rt 136,250,8192
v_mfma_f32_16x16x32_bf16 v[56:59], a[152:155], v[132:135], 0
Rt 138,250,8704
buffer_load_dwordx4 v241, s[16:19], s92 offen offset:1024 lds
v_mfma_f32_16x16x32_bf16 v[60:63], a[156:159], v[132:135], 0
Rt 140,251,8192
v_mfma_f32_16x16x32_bf16 v[52:55], a[156:159], v[128:131], 0
Rt 142,251,8704
s_waitcnt vmcnt(19) lgkmcnt(8)
Mp 32,160,136
Rt 144,248,12288
ds_read_b128 v[160:163], v26 offset:32768
Mp 40,160,140
Rt 146,248,12800
buffer_load_dwordx4 v242, s[16:19], s92 offen offset:2048 lds
Mp 44,164,140
Rt 148,249,12288
ds_read_b128 v[164:167], v26 offset:34816
Mp 36,164,136
Rt 150,249,12800
Mp 48,168,136
Rt 152,250,12288
ds_read_b128 v[168:171], v27 offset:32768
Mp 56,168,140
Rt 154,250,12800
buffer_load_dwordx4 v243, s[16:19], s92 offen offset:3072 lds
Mp 60,172,140
Rt 156,251,12288
ds_read_b128 v[172:175], v27 offset:34816
Mp 52,172,136
Rt 158,251,12800
s_waitcnt vmcnt(17) lgkmcnt(12)
Mp 32,128,144
Rt 160,248,16384
ds_read_b128 v[176:179], v26 offset:36864
s_add_u32 m0, s41, 135168
s_add_u32 s92, s42, 0x200000
Mp 40,128,148
Rt 162,248,16896
buffer_load_dwordx4 v240, s[16:19], s92 offen offset:0 lds
Mp 44,132,148
Rt 164,249,16384
ds_read_b128 v[180:183], v26 offset:38912
Mp 36,132,144
Rt 166,249,16896
Mp 48,136,144
Rt 168,250,16384
ds_read_b128 v[184:187], v27 offset:36864
Mp 56,136,148
Rt 170,250,16896
buffer_load_dwordx4 v241, s[16:19], s92 offen offset:1024 lds
Mp 60,140,148
Rt 172,251,16384
ds_read_b128 v[188:191], v27 offset:38912
Mp 52,140,144
Rt 174,251,16896
s_waitcnt lgkmcnt(12)
Mp 32,144,152
Rt 128,248,20480
Mp 40,144,156
Rt 130,248,20992
buffer_load_dwordx4 v242, s[16:19], s92 offen offset:2048 lds
Mp 44,148,156
Rt 132,249,20480
)" R"(
Mp 36,148,152
Rt 134,249,20992
Mp 48,152,152
Rt 136,250,20480
Mp 56,152,156
Rt 138,250,20992
Mp 60,156,156
Rt 140,251,20480
s_add_u32 s44, s43, 512
Mp 52,156,152
Rt 142,251,20992
s_waitcnt lgkmcnt(8)
Mp 32,160,160
Rt 144,248,24576
s_add_u32 m0, s36, 32768
Mp 40,160,164
Rt 146,248,25088
buffer_load_dwordx4 v2, s[12:15], s44 offen offset:0 lds
Mp 44,164,164
Rt 148,249,24576
Mp 36,164,160
Rt 150,249,25088
Mp 48,168,160
Rt 152,250,24576
Mp 56,168,164
Rt 154,250,25088
buffer_load_dwordx4 v3, s[12:15], s44 offen offset:1024 lds
Mp 60,172,164
Rt 156,251,24576
Mp 52,172,160
Rt 158,251,25088
s_waitcnt vmcnt(15) lgkmcnt(8)
s_barrier
Mp 32,128,168
Rt 160,248,32768
Mp 40,128,172
Rt 162,248,33280
s_add_u32 s44, s43, 12800
buffer_load_dwordx4 v2, s[12:15], s44 offen offset:2048 lds
Mp 44,132,172
Rt 164,249,32768
Mp 36,132,168
Rt 166,249,33280
Mp 48,136,168
Rt 168,250,32768
Mp 56,136,172
Rt 170,250,33280
buffer_load_dwordx4 v3, s[12:15], s44 offen offset:3072 lds
Mp 60,140,172
Rt 172,251,32768
s_add_u32 s44, s43, 640
Mp 52,140,168
Rt 174,251,33280
s_waitcnt vmcnt(11) lgkmcnt(8)
s_barrier
Mp 32,144,176
Rt 128,248,36864
s_add_u32 m0, s36, 36864
Mp 40,144,180
Rt 130,248,37376
buffer_load_dwordx4 v2, s[12:15], s44 offen offset:0 lds
Mp 44,148,180
Rt 132,249,36864
v_dot2_f32_bf16 v120, v96, v112, 0
Mp 36,148,176
Rt 134,249,37376
)" R"(
v_dot2_f32_bf16 v121, v100, v112, 0
v_dot2_f32_bf16 v122, v104, v112, 0
Mp 48,152,176
Rt 136,250,36864
v_dot2_f32_bf16 v123, v108, v112, 0
Mp 56,152,180
Rt 138,250,37376
buffer_load_dwordx4 v3, s[12:15], s44 offen offset:1024 lds
Mp 60,156,180
Rt 140,251,36864
v_dot2_f32_bf16 v124, v96, v116, 0
v_dot2_f32_bf16 v125, v100, v116, 0
Mp 52,156,176
Rt 142,251,37376
s_waitcnt lgkmcnt(8)
v_mfma_f32_16x16x32_bf16 v[64:67], a[160:163], v[128:131], 0
Rt 144,248,40960
v_dot2_f32_bf16 v126, v104, v116, 0
v_mfma_f32_16x16x32_bf16 v[72:75], a[160:163], v[132:135], 0
Rt 146,248,41472
s_add_u32 s44, s43, 12928
buffer_load_dwordx4 v2, s[12:15], s44 offen offset:2048 lds
v_mfma_f32_16x16x32_bf16 v[76:79], a[164:167], v[132:135], 0
Rt 148,249,40960
v_dot2_f32_bf16 v127, v108, v116, 0
v_mfma_f32_16x16x32_bf16 v[68:71], a[164:167], v[128:131], 0
Rt 150,249,41472
v_dot2_f32_bf16 v120, v97, v113, v120
v_mfma_f32_16x16x32_bf16 v[80:83], a[168:171], v[128:131], 0
Rt 152,250,40960
v_dot2_f32_bf16 v121, v101, v113, v121
v_dot2_f32_bf16 v122, v105, v113, v122
v_mfma_f32_16x16x32_bf16 v[88:91], a[168:171], v[132:135], 0
Rt 154,250,41472
buffer_load_dwordx4 v3, s[12:15], s44 offen offset:3072 lds
v_mfma_f32_16x16x32_bf16 v[92:95], a[172:175], v[132:135], 0
Rt 156,251,40960
s_add_u32 s92, s42, 0x1c0080
v_mfma_f32_16x16x32_bf16 v[84:87], a[172:175], v[128:131], 0
Rt 158,251,41472
v_dot2_f32_bf16 v123, v109, v113, v123
s_waitcnt lgkmcnt(8)
Mp 64,128,136
Rt 160,248,45056
s_add_u32 m0, s41, 0
Mp 72,128,140
Rt 162,248,45568
buffer_load_dwordx4 v240, s[16:19], s92 offen offset:0 lds
Mp 76,132,140
Rt 164,249,45056
v_dot2_f32_bf16 v124, v97, v117, v124
Mp 68,132,136
Rt 166,249,45568
v_dot2_f32_bf16 v125, v101, v117, v125
v_dot2_f32_bf16 v126, v105, v117, v126
Mp 80,136,136
Rt 168,250,45056
v_dot2_f32_bf16 v127, v109, v117, v127
Mp 88,136,140
Rt 170,250,45568
buffer_load_dwordx4 v241, s[16:19], s92 offen offset:1024 lds
Mp 92,140,140
Rt 172,251,45056
v_dot2_f32_bf16 v120, v98, v114, v120
)" R"(
v_dot2_f32_bf16 v121, v102, v114, v121
Mp 84,140,136
Rt 174,251,45568
s_waitcnt lgkmcnt(8)
Mp 64,144,144
Rt 128,248,49152
v_dot2_f32_bf16 v122, v106, v114, v122
Mp 72,144,148
Rt 130,248,49664
buffer_load_dwordx4 v242, s[16:19], s92 offen offset:2048 lds
Mp 76,148,148
Rt 132,249,49152
v_dot2_f32_bf16 v123, v110, v114, v123
Mp 68,148,144
Rt 134,249,49664
v_dot2_f32_bf16 v124, v98, v118, v124
Mp 80,152,144
Rt 136,250,49152
v_dot2_f32_bf16 v125, v102, v118, v125
v_dot2_f32_bf16 v126, v106, v118, v126
Mp 88,152,148
Rt 138,250,49664
buffer_load_dwordx4 v243, s[16:19], s92 offen offset:3072 lds
Mp 92,156,148
Rt 140,251,49152
s_add_u32 s92, s42, 0x200080
Mp 84,156,144
Rt 142,251,49664
v_dot2_f32_bf16 v127, v110, v118, v127
s_waitcnt lgkmcnt(8)
Mp 64,160,152
Rt 144,248,53248
s_add_u32 m0, s41, 4096
Mp 72,160,156
Rt 146,248,53760
buffer_load_dwordx4 v240, s[16:19], s92 offen offset:0 lds
Mp 76,164,156
Rt 148,249,53248
v_dot2_f32_bf16 v120, v99, v115, v120
Mp 68,164,152
Rt 150,249,53760
v_dot2_f32_bf16 v121, v103, v115, v121
v_dot2_f32_bf16 v122, v107, v115, v122
Mp 80,168,152
Rt 152,250,53248
v_dot2_f32_bf16 v123, v111, v115, v123
Mp 88,168,156
Rt 154,250,53760
buffer_load_dwordx4 v241, s[16:19], s92 offen offset:1024 lds
Mp 92,172,156
Rt 156,251,53248
v_dot2_f32_bf16 v124, v99, v119, v124
v_dot2_f32_bf16 v125, v103, v119, v125
Mp 84,172,152
Rt 158,251,53760
s_waitcnt vmcnt(10) lgkmcnt(8)
Mp 64,128,160
Rt 160,248,57344
ds_read_b128 v[192:195], v26 offset:32768
Mp 72,128,164
Rt 162,248,57856
buffer_load_dwordx4 v242, s[16:19], s92 offen offset:2048 lds
Mp 76,132,164
Rt 164,249,57344
)" R"(
ds_read_b128 v[196:199], v26 offset:34816
Mp 68,132,160
Rt 166,249,57856
v_dot2_f32_bf16 v126, v107, v119, v126
Mp 80,136,160
Rt 168,250,57344
ds_read_b128 v[200:203], v27 offset:32768
v_dot2_f32_bf16 v127, v111, v119, v127
Mp 88,136,164
Rt 170,250,57856
v_permlane32_swap_b32_e32 v120, v121
Mp 92,140,164
Rt 172,251,57344
ds_read_b128 v[204:207], v27 offset:34816
Mp 84,140,160
Rt 174,251,57856
s_waitcnt vmcnt(7) lgkmcnt(12)
s_barrier
Mp 64,144,168
Rt 128,252,0
ds_read_b128 v[208:211], v26 offset:36864
Mp 72,144,172
Rt 130,252,512
v_add_f32_e32 v120, v120, v121
Mp 76,148,172
Rt 132,253,0
ds_read_b128 v[212:215], v26 offset:38912
Mp 68,148,168
Rt 134,253,512
v_permlane32_swap_b32_e32 v122, v123
v_add_f32_e32 v122, v122, v123
Mp 80,152,168
Rt 136,254,0
ds_read_b128 v[216:219], v27 offset:36864
Mp 88,152,172
Rt 138,254,512
s_add_u32 s44, s43, 768
Mp 92,156,172
Rt 140,255,0
ds_read_b128 v[220:223], v27 offset:38912
v_permlane32_swap_b32_e32 v124, v125
Mp 84,156,168
Rt 142,255,512
s_waitcnt lgkmcnt(12)
s_barrier
Mp 64,160,176
Rt 144,252,4096
s_add_u32 m0, s36, 32768
Mp 72,160,180
Rt 146,252,4608
buffer_load_dwordx4 v2, s[12:15], s44 offen offset:0 lds
Mp 76,164,180
Rt 148,253,4096
v_add_f32_e32 v124, v124, v125
Mp 68,164,176
Rt 150,253,4608
v_permlane32_swap_b32_e32 v126, v127
Mp 80,168,176
Rt 152,254,4096
buffer_load_dwordx4 v3, s[12:15], s44 offen offset:1024 lds
Mp 88,168,180
Rt 154,254,4608
v_add_f32_e32 v126, v126, v127
Mp 92,172,180
Rt 156,255,4096
v_permlane16_swap_b32_e32 v120, v122
)" R"(
Mp 84,172,176
Rt 158,255,4608
s_add_u32 s44, s43, 13056
buffer_load_dwordx4 v2, s[12:15], s44 offen offset:2048 lds
s_waitcnt lgkmcnt(8)
Mp 32,128,184
Rt 160,252,8192
v_add_f32_e32 v120, v120, v122
Mp 40,128,188
Rt 162,252,8704
v_permlane16_swap_b32_e32 v124, v126
Mp 44,132,188
Rt 164,253,8192
buffer_load_dwordx4 v3, s[12:15], s44 offen offset:3072 lds
Mp 36,132,184
Rt 166,253,8704
v_add_f32_e32 v124, v124, v126
v_add_f32_dpp v120, v120, v120 row_ror:8 row_mask:0xf bank_mask:0xf
Mp 48,136,184
Rt 168,254,8192
v_add_f32_dpp v124, v124, v124 row_ror:8 row_mask:0xf bank_mask:0xf
Mp 56,136,188
Rt 170,254,8704
s_add_u32 s92, s42, 0x100
Mp 60,140,188
Rt 172,255,8192
s_add_u32 m0, s41, 65536
v_add_f32_dpp v120, v120, v120 row_ror:4 row_mask:0xf bank_mask:0xf
Mp 52,140,184
Rt 174,255,8704
buffer_load_dwordx4 v240, s[16:19], s92 offen offset:0 lds
s_waitcnt lgkmcnt(8)
Mp 32,144,192
Rt 128,252,12288
v_add_f32_dpp v124, v124, v124 row_ror:4 row_mask:0xf bank_mask:0xf
Mp 40,144,196
Rt 130,252,12800
v_add_f32_dpp v120, v120, v120 row_ror:2 row_mask:0xf bank_mask:0xf
v_add_f32_dpp v124, v124, v124 row_ror:2 row_mask:0xf bank_mask:0xf
Mp 44,148,196
Rt 132,253,12288
v_add_f32_dpp v120, v120, v120 row_ror:1 row_mask:0xf bank_mask:0xf
Mp 36,148,192
Rt 134,253,12800
buffer_load_dwordx4 v241, s[16:19], s92 offen offset:1024 lds
Mp 48,152,192
Rt 136,254,12288
v_add_f32_dpp v124, v124, v124 row_ror:1 row_mask:0xf bank_mask:0xf
Cv 120,120,120
Mp 56,152,196
Rt 138,254,12800
v_lshlrev_b32 v120, 16, v120
Mp 60,156,196
Rt 140,255,12288
Cv 124,124,124
Mp 52,156,192
Rt 142,255,12800
buffer_load_dwordx4 v242, s[16:19], s92 offen offset:2048 lds
s_waitcnt lgkmcnt(8)
Mp 32,160,200
Rt 144,252,16384
v_lshlrev_b32 v124, 16, v124
Mp 40,160,204
Rt 146,252,16896
)" R"(
v_mul_f32_e32 v120, 0xbfb8aa3b, v120
Mp 44,164,204
Rt 148,253,16384
v_lshrrev_b32 v121, 5, v0
Mp 36,164,200
Rt 150,253,16896
buffer_load_dwordx4 v243, s[16:19], s92 offen offset:3072 lds
Mp 48,168,200
Rt 152,254,16384
v_bfe_u32 v122, v0, 4, 1
Mp 56,168,204
Rt 154,254,16896
s_add_u32 s92, s42, 0x40100
Mp 60,172,204
Rt 156,255,16384
s_add_u32 m0, s41, 69632
v_lshl_or_b32 v121, v122, 1, v121
Mp 52,172,200
Rt 158,255,16896
buffer_load_dwordx4 v240, s[16:19], s92 offen offset:0 lds
s_waitcnt vmcnt(5) lgkmcnt(8)
Mp 32,128,208
Rt 160,252,20480
ds_read_b128 v[224:227], v26 offset:32768
Mp 40,128,212
Rt 162,252,20992
v_exp_f32_e32 v120, v120
Mp 44,132,212
Rt 164,253,20480
ds_read_b128 v[228:231], v26 offset:34816
Mp 36,132,208
Rt 166,253,20992
buffer_load_dwordx4 v241, s[16:19], s92 offen offset:1024 lds
Mp 48,136,208
Rt 168,254,20480
ds_read_b128 v[232:235], v27 offset:32768
v_add_f32_e32 v120, 1.0, v120
Mp 56,136,212
Rt 170,254,20992
v_add_u32 v121, s94, v121
Mp 60,140,212
Rt 172,255,20480
ds_read_b128 v[236:239], v27 offset:34816
Mp 52,140,208
Rt 174,255,20992
buffer_load_dwordx4 v242, s[16:19], s92 offen offset:2048 lds
s_waitcnt lgkmcnt(12)
Mp 32,144,216
Rt 128,252,24576
v_add_f32_e32 v125, 0, v124
Mp 40,144,220
Rt 130,252,25088
s_add_u32 s92, s42, 0x180
Mp 44,148,220
Rt 132,253,24576
s_add_u32 m0, s41, 98304
Mp 36,148,216
Rt 134,253,25088
buffer_load_dwordx4 v240, s[16:19], s92 offen offset:0 lds
Mp 48,152,216
Rt 136,254,24576
v_ashrrev_i32 v126, 31, v125
Mp 56,152,220
Rt 138,254,25088
)" R"(
v_or_b32 v126, 0x80000000, v126
Mp 60,156,220
Rt 140,255,24576
v_rcp_f32_e32 v120, v120
Mp 52,156,216
Rt 142,255,25088
buffer_load_dwordx4 v241, s[16:19], s92 offen offset:1024 lds
s_waitcnt lgkmcnt(8)
s_barrier
Mp 32,160,224
Rt 144,244,0
Cv 120,120,120
Mp 40,160,228
Rt 146,244,512
v_lshlrev_b32 v120, 16, v120
v_xor_b32 v125, v125, v126
Mp 44,164,228
Rt 148,245,0
v_and_b32 v125, 0xffff0000, v125
Mp 36,164,224
Rt 150,245,512
buffer_load_dwordx4 v242, s[16:19], s92 offen offset:2048 lds
Mp 48,168,224
Rt 152,246,0
v_sub_u32 v126, 0xffff, v121
v_or_b32 v116, v125, v126
Mp 56,168,228
Rt 154,246,512
v_mov_b32 v117, s3
Mp 60,172,228
Rt 156,247,0
v_mov_b32 v118, v120
Mp 52,172,224
Rt 158,247,512
buffer_load_dwordx4 v243, s[16:19], s92 offen offset:3072 lds
s_waitcnt lgkmcnt(8)
s_barrier
Mp 32,128,232
Rt 160,244,4096
v_mov_b32 v119, v124
Mp 40,128,236
Rt 162,244,4608
s_add_u32 s92, s42, 0x40180
Mp 44,132,236
Rt 164,245,4096
s_add_u32 m0, s41, 102400
Mp 36,132,232
Rt 166,245,4608
buffer_load_dwordx4 v240, s[16:19], s92 offen offset:0 lds
Mp 48,136,232
Rt 168,246,4096
v_and_b32 v122, 15, v0
Mp 56,136,236
Rt 170,246,4608
v_min_u32 v122, 1, v122
Mp 60,140,236
Rt 172,247,4096
v_lshlrev_b32 v122, 30, v122
v_lshl_or_b32 v122, v121, 4, v122
Mp 52,140,232
Rt 174,247,4608
buffer_load_dwordx4 v241, s[16:19], s92 offen offset:1024 lds
s_waitcnt lgkmcnt(8)
Mp 64,144,184
Rt 128,244,8192
s_mov_b32 s94, 0xc100000
)" R"(
Mp 72,144,188
Rt 130,244,8704
buffer_store_dwordx4 v[116:119], v122, s[20:23], s94 offen sc0 sc1
Mp 76,148,188
Rt 132,245,8192
Mp 68,148,184
Rt 134,245,8704
buffer_load_dwordx4 v242, s[16:19], s92 offen offset:2048 lds
Mp 80,152,184
Rt 136,246,8192
v_mul_f32_e32 v32, s54, v32
Mp 88,152,188
Rt 138,246,8704
s_add_u32 s92, s42, 0x1c0100
Mp 92,156,188
Rt 140,247,8192
s_add_u32 m0, s41, 32768
Mp 84,156,184
Rt 142,247,8704
buffer_load_dwordx4 v240, s[16:19], s92 offen offset:0 lds
s_waitcnt lgkmcnt(8)
Mp 64,160,192
Rt 144,244,12288
v_mul_f32_e32 v33, s54, v33
Mp 72,160,196
Rt 146,244,12800
Cv 32,32,33
Mp 76,164,196
Rt 148,245,12288
v_accvgpr_write_b32 a64, v32
v_mul_f32_e32 v34, s54, v34
Mp 68,164,192
Rt 150,245,12800
buffer_load_dwordx4 v241, s[16:19], s92 offen offset:1024 lds
Mp 80,168,192
Rt 152,246,12288
v_mul_f32_e32 v35, s54, v35
Mp 88,168,196
Rt 154,246,12800
Cv 34,34,35
v_accvgpr_write_b32 a65, v34
Mp 92,172,196
Rt 156,247,12288
v_mul_f32_e32 v36, s54, v36
Mp 84,172,192
Rt 158,247,12800
buffer_load_dwordx4 v242, s[16:19], s92 offen offset:2048 lds
s_waitcnt lgkmcnt(8)
Mp 64,128,200
Rt 160,244,16384
v_mul_f32_e32 v37, s54, v37
Cv 36,36,37
Mp 72,128,204
Rt 162,244,16896
v_accvgpr_write_b32 a66, v36
Mp 76,132,204
Rt 164,245,16384
v_mul_f32_e32 v38, s54, v38
Mp 68,132,200
Rt 166,245,16896
buffer_load_dwordx4 v243, s[16:19], s92 offen offset:3072 lds
Mp 80,136,200
Rt 168,246,16384
v_mul_f32_e32 v39, s54, v39
)" R"(
Mp 88,136,204
Rt 170,246,16896
s_add_u32 s92, s42, 0x200100
Mp 92,140,204
Rt 172,247,16384
s_add_u32 m0, s41, 36864
Mp 84,140,200
Rt 174,247,16896
buffer_load_dwordx4 v240, s[16:19], s92 offen offset:0 lds
s_waitcnt lgkmcnt(8)
Mp 64,144,208
Rt 128,244,20480
Cv 38,38,39
Mp 72,144,212
Rt 130,244,20992
v_accvgpr_write_b32 a67, v38
Mp 76,148,212
Rt 132,245,20480
v_mul_f32_e32 v40, s54, v40
v_mul_f32_e32 v41, s54, v41
Mp 68,148,208
Rt 134,245,20992
buffer_load_dwordx4 v241, s[16:19], s92 offen offset:1024 lds
Mp 80,152,208
Rt 136,246,20480
Cv 40,40,41
Mp 88,152,212
Rt 138,246,20992
v_accvgpr_write_b32 a68, v40
v_mul_f32_e32 v42, s54, v42
Mp 92,156,212
Rt 140,247,20480
v_mul_f32_e32 v43, s54, v43
Mp 84,156,208
Rt 142,247,20992
buffer_load_dwordx4 v242, s[16:19], s92 offen offset:2048 lds
s_waitcnt lgkmcnt(8)
Mp 64,160,216
Rt 144,244,24576
Cv 42,42,43
v_accvgpr_write_b32 a69, v42
Mp 72,160,220
Rt 146,244,25088
v_mul_f32_e32 v44, s54, v44
Mp 76,164,220
Rt 148,245,24576
v_mul_f32_e32 v45, s54, v45
Mp 68,164,216
Rt 150,245,25088
s_add_u32 s92, s42, 0x1c0180
Cv 44,44,45
Mp 80,168,216
Rt 152,246,24576
s_add_u32 m0, s41, 131072
Mp 88,168,220
Rt 154,246,25088
buffer_load_dwordx4 v240, s[16:19], s92 offen offset:0 lds
Mp 92,172,220
Rt 156,247,24576
v_accvgpr_write_b32 a70, v44
Mp 84,172,216
Rt 158,247,25088
s_waitcnt vmcnt(16) lgkmcnt(8)
s_barrier
Mp 64,128,224
)" R"(
Rt 160,248,0
v_mul_f32_e32 v46, s54, v46
Mp 72,128,228
Rt 162,248,512
buffer_load_dwordx4 v241, s[16:19], s92 offen offset:1024 lds
Mp 76,132,228
Rt 164,249,0
v_mul_f32_e32 v47, s54, v47
Cv 46,46,47
Mp 68,132,224
Rt 166,249,512
v_accvgpr_write_b32 a71, v46
Mp 80,136,224
Rt 168,250,0
v_mul_f32_e32 v48, s54, v48
Mp 88,136,228
Rt 170,250,512
buffer_load_dwordx4 v242, s[16:19], s92 offen offset:2048 lds
Mp 92,140,228
Rt 172,251,0
v_mul_f32_e32 v49, s54, v49
Mp 84,140,224
Rt 174,251,512
s_waitcnt vmcnt(3) lgkmcnt(8)
s_barrier
Mp 64,144,232
Rt 128,248,4096
Cv 48,48,49
v_accvgpr_write_b32 a72, v48
Mp 72,144,236
Rt 130,248,4608
buffer_load_dwordx4 v243, s[16:19], s92 offen offset:3072 lds
Mp 76,148,236
Rt 132,249,4096
v_mul_f32_e32 v50, s54, v50
Mp 68,148,232
Rt 134,249,4608
s_add_u32 s92, s42, 0x200180
v_mul_f32_e32 v51, s54, v51
Mp 80,152,232
Rt 136,250,4096
s_add_u32 m0, s41, 135168
Mp 88,152,236
Rt 138,250,4608
buffer_load_dwordx4 v240, s[16:19], s92 offen offset:0 lds
Mp 92,156,236
Rt 140,251,4096
Cv 50,50,51
Mp 84,156,232
Rt 142,251,4608
v_accvgpr_write_b32 a73, v50
s_waitcnt lgkmcnt(8)
v_mfma_f32_16x16x32_bf16 v[96:99], a[160:163], v[128:131], 0
Rt 144,248,8192
v_mul_f32_e32 v52, s54, v52
v_mfma_f32_16x16x32_bf16 v[104:107], a[160:163], v[132:135], 0
Rt 146,248,8704
buffer_load_dwordx4 v241, s[16:19], s92 offen offset:1024 lds
v_mfma_f32_16x16x32_bf16 v[108:111], a[164:167], v[132:135], 0
Rt 148,249,8192
v_mul_f32_e32 v53, s54, v53
Cv 52,52,53
v_mfma_f32_16x16x32_bf16 v[100:103], a[164:167], v[128:131], 0
Rt 150,249,8704
v_accvgpr_write_b32 a74, v52
)" R"(
v_mfma_f32_16x16x32_bf16 v[112:115], a[168:171], v[128:131], 0
Rt 152,250,8192
v_mul_f32_e32 v54, s54, v54
v_mfma_f32_16x16x32_bf16 v[120:123], a[168:171], v[132:135], 0
Rt 154,250,8704
buffer_load_dwordx4 v242, s[16:19], s92 offen offset:2048 lds
v_mfma_f32_16x16x32_bf16 v[124:127], a[172:175], v[132:135], 0
Rt 156,251,8192
v_mul_f32_e32 v55, s54, v55
v_mfma_f32_16x16x32_bf16 v[116:119], a[172:175], v[128:131], 0
Rt 158,251,8704
s_waitcnt lgkmcnt(8)
Mp 96,128,136
Rt 160,248,12288
Cv 54,54,55
v_accvgpr_write_b32 a75, v54
Mp 104,128,140
Rt 162,248,12800
s_add_u32 s38, s38, 0xffff0080
Mp 108,132,140
Rt 164,249,12288
s_add_u32 m0, s36, 0
Mp 100,132,136
Rt 166,249,12800
buffer_load_dwordx4 v8, s[4:7], s38 offen offset:0 lds
Mp 112,136,136
Rt 168,250,12288
v_mul_f32_e32 v56, s54, v56
Mp 120,136,140
Rt 170,250,12800
v_mul_f32_e32 v57, s54, v57
Mp 124,140,140
Rt 172,251,12288
Cv 56,56,57
Mp 116,140,136
Rt 174,251,12800
buffer_load_dwordx4 v9, s[4:7], s38 offen offset:1024 lds
s_waitcnt lgkmcnt(8)
Mp 96,144,144
Rt 128,248,16384
v_accvgpr_write_b32 a76, v56
Mp 104,144,148
Rt 130,248,16896
v_mul_f32_e32 v58, s54, v58
Mp 108,148,148
Rt 132,249,16384
v_mul_f32_e32 v59, s54, v59
Cv 58,58,59
Mp 100,148,144
Rt 134,249,16896
buffer_load_dwordx4 v10, s[4:7], s38 offen offset:2048 lds
Mp 112,152,144
Rt 136,250,16384
v_accvgpr_write_b32 a77, v58
Mp 120,152,148
Rt 138,250,16896
v_mul_f32_e32 v60, s54, v60
v_mul_f32_e32 v61, s54, v61
Mp 124,156,148
Rt 140,251,16384
Cv 60,60,61
Mp 116,156,144
Rt 142,251,16896
buffer_load_dwordx4 v11, s[4:7], s38 offen offset:3072 lds
)" R"(
s_waitcnt lgkmcnt(8)
Mp 96,160,152
Rt 144,248,20480
v_accvgpr_write_b32 a78, v60
v_mul_f32_e32 v62, s54, v62
Mp 104,160,156
Rt 146,248,20992
s_add_u32 s38, s38, 0x10000
Mp 108,164,156
Rt 148,249,20480
s_add_u32 m0, s36, 4096
Mp 100,164,152
Rt 150,249,20992
buffer_load_dwordx4 v8, s[4:7], s38 offen offset:0 lds
Mp 112,168,152
Rt 152,250,20480
v_mul_f32_e32 v63, s54, v63
Mp 120,168,156
Rt 154,250,20992
Cv 62,62,63
Mp 124,172,156
Rt 156,251,20480
v_accvgpr_write_b32 a79, v62
Mp 116,172,152
Rt 158,251,20992
buffer_load_dwordx4 v9, s[4:7], s38 offen offset:1024 lds
s_waitcnt lgkmcnt(8)
Mp 96,128,160
Rt 160,248,24576
v_mul_f32_e32 v64, s54, v64
Mp 104,128,164
Rt 162,248,25088
v_mul_f32_e32 v65, s54, v65
Mp 108,132,164
Rt 164,249,24576
Cv 64,64,65
v_accvgpr_write_b32 a80, v64
Mp 100,132,160
Rt 166,249,25088
buffer_load_dwordx4 v10, s[4:7], s38 offen offset:2048 lds
Mp 112,136,160
Rt 168,250,24576
v_mul_f32_e32 v66, s54, v66
Mp 120,136,164
Rt 170,250,25088
v_mul_f32_e32 v67, s54, v67
Cv 66,66,67
Mp 124,140,164
Rt 172,251,24576
v_accvgpr_write_b32 a81, v66
Mp 116,140,160
Rt 174,251,25088
buffer_load_dwordx4 v11, s[4:7], s38 offen offset:3072 lds
s_waitcnt lgkmcnt(8)
Mp 96,144,168
Rt 128,244,32768
v_mul_f32_e32 v68, s54, v68
v_mul_f32_e32 v69, s54, v69
Mp 104,144,172
Rt 130,244,33280
Cv 68,68,69
s_mov_b32 s85, 0xc108800
Mp 108,148,172
Rt 132,245,32768
)" R"(
v_accvgpr_write_b32 a82, v68
v_and_b32 v2, 7, v0
Mp 100,148,168
Rt 134,245,33280
v_mul_f32_e32 v70, s54, v70
v_mul_f32_e32 v71, s54, v71
v_lshlrev_b32 v2, 7, v2
Mp 112,152,168
Rt 136,246,32768
Cv 70,70,71
Mp 120,152,172
Rt 138,246,33280
v_accvgpr_write_b32 a83, v70
buffer_load_dword v4, v2, s[20:23], s85 offen sc0 sc1
Mp 124,156,172
Rt 140,247,32768
v_mul_f32_e32 v72, s54, v72
Mp 116,156,168
Rt 142,247,33280
s_waitcnt lgkmcnt(8)
s_barrier
Mp 96,160,176
Rt 144,244,36864
v_mul_f32_e32 v73, s54, v73
Mp 104,160,180
Rt 146,244,37376
s_add_u32 s39, s39, 0x40000
Mp 108,164,180
Rt 148,245,36864
s_add_u32 m0, s37, 0
Cv 72,72,73
Mp 100,164,176
Rt 150,245,37376
buffer_load_dwordx4 v12, s[8:11], s39 offen offset:0 lds
Mp 112,168,176
Rt 152,246,36864
v_accvgpr_write_b32 a84, v72
Mp 120,168,180
Rt 154,246,37376
v_mul_f32_e32 v74, s54, v74
v_mul_f32_e32 v75, s54, v75
Mp 124,172,180
Rt 156,247,36864
Cv 74,74,75
Mp 116,172,176
Rt 158,247,37376
s_waitcnt lgkmcnt(8)
Mp 96,128,184
Rt 160,244,40960
v_accvgpr_write_b32 a85, v74
v_mul_f32_e32 v76, s54, v76
Mp 104,128,188
Rt 162,244,41472
buffer_load_dwordx4 v13, s[8:11], s39 offen offset:1024 lds
Mp 108,132,188
Rt 164,245,40960
v_mul_f32_e32 v77, s54, v77
Mp 100,132,184
Rt 166,245,41472
Cv 76,76,77
v_accvgpr_write_b32 a86, v76
Mp 112,136,184
Rt 168,246,40960
v_mul_f32_e32 v78, s54, v78
Mp 120,136,188
)" R"(
Rt 170,246,41472
v_mul_f32_e32 v79, s54, v79
Mp 124,140,188
Rt 172,247,40960
Cv 78,78,79
Mp 116,140,184
Rt 174,247,41472
buffer_load_dwordx4 v14, s[8:11], s39 offen offset:2048 lds
s_waitcnt lgkmcnt(8)
Mp 96,144,192
Rt 128,244,45056
v_accvgpr_write_b32 a87, v78
Mp 104,144,196
Rt 130,244,45568
v_mul_f32_e32 v80, s54, v80
Mp 108,148,196
Rt 132,245,45056
v_mul_f32_e32 v81, s54, v81
Cv 80,80,81
Mp 100,148,192
Rt 134,245,45568
v_accvgpr_write_b32 a88, v80
Mp 112,152,192
Rt 136,246,45056
v_mul_f32_e32 v82, s54, v82
Mp 120,152,196
Rt 138,246,45568
buffer_load_dwordx4 v15, s[8:11], s39 offen offset:3072 lds
Mp 124,156,196
Rt 140,247,45056
v_mul_f32_e32 v83, s54, v83
Mp 116,156,192
Rt 142,247,45568
s_waitcnt lgkmcnt(8)
Mp 96,160,200
Rt 144,244,49152
Cv 82,82,83
v_accvgpr_write_b32 a89, v82
Mp 104,160,204
Rt 146,244,49664
s_add_u32 s39, s39, 0x40000
Mp 108,164,204
Rt 148,245,49152
s_add_u32 m0, s37, 16384
Mp 100,164,200
Rt 150,245,49664
buffer_load_dwordx4 v12, s[8:11], s39 offen offset:0 lds
Mp 112,168,200
Rt 152,246,49152
v_mul_f32_e32 v84, s54, v84
Mp 120,168,204
Rt 154,246,49664
v_mul_f32_e32 v85, s54, v85
Mp 124,172,204
Rt 156,247,49152
Cv 84,84,85
Mp 116,172,200
Rt 158,247,49664
v_accvgpr_write_b32 a90, v84
s_waitcnt lgkmcnt(8)
Mp 96,128,208
Rt 160,244,53248
v_mul_f32_e32 v86, s54, v86
Mp 104,128,212
)" R"(
Rt 162,244,53760
buffer_load_dwordx4 v13, s[8:11], s39 offen offset:1024 lds
Mp 108,132,212
Rt 164,245,53248
v_mul_f32_e32 v87, s54, v87
Cv 86,86,87
Mp 100,132,208
Rt 166,245,53760
v_accvgpr_write_b32 a91, v86
Mp 112,136,208
Rt 168,246,53248
v_mul_f32_e32 v88, s54, v88
Mp 120,136,212
Rt 170,246,53760
v_mul_f32_e32 v89, s54, v89
Cv 88,88,89
Mp 124,140,212
Rt 172,247,53248
v_accvgpr_write_b32 a92, v88
Mp 116,140,208
Rt 174,247,53760
buffer_load_dwordx4 v14, s[8:11], s39 offen offset:2048 lds
s_waitcnt lgkmcnt(8)
Mp 96,144,216
Rt 128,244,57344
v_mul_f32_e32 v90, s54, v90
v_mul_f32_e32 v91, s54, v91
Mp 104,144,220
Rt 130,244,57856
Cv 90,90,91
Mp 108,148,220
Rt 132,245,57344
v_accvgpr_write_b32 a93, v90
Mp 100,148,216
Rt 134,245,57856
v_mul_f32_e32 v92, s54, v92
v_mul_f32_e32 v93, s54, v93
Mp 112,152,216
Rt 136,246,57344
Cv 92,92,93
Mp 120,152,220
Rt 138,246,57856
buffer_load_dwordx4 v15, s[8:11], s39 offen offset:3072 lds
Mp 124,156,220
Rt 140,247,57344
v_accvgpr_write_b32 a94, v92
Mp 116,156,216
Rt 142,247,57856
v_mul_f32_e32 v94, s54, v94
s_waitcnt lgkmcnt(8)
Mp 96,160,224
Rt 144,248,32768
v_mul_f32_e32 v95, s54, v95
Mp 104,160,228
Rt 146,248,33280
Cv 94,94,95
Mp 108,164,228
Rt 148,249,32768
v_accvgpr_write_b32 a95, v94
Mp 100,164,224
Rt 150,249,33280
s_lshl1_add_u32 s88, s34, s91
Mp 112,168,224
Rt 152,250,32768
)" R"(
s_lshl_b32 s88, s88, 12
Mp 120,168,228
Rt 154,250,33280
s_and_b32 s29, s38, 0xfffe0000
Mp 124,172,228
Rt 156,251,32768
s_add_u32 s88, s88, s29
Mp 116,172,224
Rt 158,251,33280
s_waitcnt vmcnt(8) lgkmcnt(8)
s_barrier
Mp 96,128,232
Rt 160,248,36864
v_min_u32 v28, 31, v0
v_xor_b32 v4, s3, v4
v_cmp_gt_u32_e32 vcc, 2, v4
s_andn2_b32 s28, 0xff, vcc_lo
s_cbranch_scc1 .Lzsp13
.Lzok13:
Mp 104,128,236
Rt 162,248,37376
s_add_u32 s38, s38, 0xffff0080
v_xor_b32 v4, s3, v4
Mp 108,132,236
Rt 164,249,36864
s_add_u32 m0, s36, 32768
Mp 100,132,232
Rt 166,249,37376
buffer_load_dwordx4 v8, s[4:7], s38 offen offset:0 lds
Mp 112,136,232
Rt 168,250,36864
v_lshlrev_b32 v28, 7, v28
v_and_b32 v4, 1, v4
Mp 120,136,236
Rt 170,250,37376
Mp 124,140,236
Rt 172,251,36864
v_cmp_eq_u32_e32 vcc, 1, v4
s_mov_b32 s85, vcc_lo
Mp 116,140,232
Rt 174,251,37376
s_and_b32 s28, s2, 7
s_waitcnt lgkmcnt(8)
v_mfma_f32_16x16x32_bf16 v[64:67], a[144:147], v[128:131], 0
Rt 128,248,40960
s_lshr_b32 s28, s85, s28
v_mfma_f32_16x16x32_bf16 v[72:75], a[144:147], v[132:135], 0
Rt 130,248,41472
buffer_load_dwordx4 v9, s[4:7], s38 offen offset:1024 lds
v_mfma_f32_16x16x32_bf16 v[76:79], a[148:151], v[132:135], 0
Rt 132,249,40960
s_xor_b32 s28, s28, s34
v_mfma_f32_16x16x32_bf16 v[68:71], a[148:151], v[128:131], 0
Rt 134,249,41472
s_and_b32 s28, s28, 1
v_mfma_f32_16x16x32_bf16 v[80:83], a[152:155], v[128:131], 0
Rt 136,250,40960
v_mul_f32_e32 v96, s54, v96
s_lshl_b32 s84, s28, 13
v_mfma_f32_16x16x32_bf16 v[88:91], a[152:155], v[132:135], 0
Rt 138,250,41472
v_mul_f32_e32 v97, s54, v97
v_mfma_f32_16x16x32_bf16 v[92:95], a[156:159], v[132:135], 0
Rt 140,251,40960
Cv 96,96,97
)" R"(
s_lshl1_add_u32 s28, s36, s84
v_mfma_f32_16x16x32_bf16 v[84:87], a[156:159], v[128:131], 0
Rt 142,251,41472
buffer_load_dwordx4 v10, s[4:7], s38 offen offset:2048 lds
s_waitcnt lgkmcnt(8)
Mp 64,160,136
Rt 144,248,45056
v_accvgpr_write_b32 a96, v96
Mp 72,160,140
Rt 146,248,45568
v_mul_f32_e32 v98, s54, v98
v_add_u32 v2, s28, v1
Mp 76,164,140
Rt 148,249,45056
v_mul_f32_e32 v99, s54, v99
Mp 68,164,136
Rt 150,249,45568
Cv 98,98,99
v_add_u32 v3, 0x1000, v2
Mp 80,168,136
Rt 152,250,45056
v_accvgpr_write_b32 a97, v98
s_lshl_b32 s28, s34, 13
Mp 88,168,140
Rt 154,250,45568
buffer_load_dwordx4 v11, s[4:7], s38 offen offset:3072 lds
Mp 92,172,140
Rt 156,251,45056
v_mul_f32_e32 v100, s54, v100
s_xor_b32 s28, s28, s84
Mp 84,172,136
Rt 158,251,45568
s_waitcnt lgkmcnt(8)
Mp 64,128,144
Rt 160,248,49152
v_mul_f32_e32 v101, s54, v101
Mp 72,128,148
Rt 162,248,49664
s_add_u32 s38, s38, 0x10000
s_and_b32 s28, s28, 0x2000
Mp 76,132,148
Rt 164,249,49152
s_add_u32 m0, s36, 36864
Mp 68,132,144
Rt 166,249,49664
buffer_load_dwordx4 v8, s[4:7], s38 offen offset:0 lds
Mp 80,136,144
Rt 168,250,49152
Cv 100,100,101
s_xor_b32 s73, s73, s28
Mp 88,136,148
Rt 170,250,49664
v_accvgpr_write_b32 a98, v100
Mp 92,140,148
Rt 172,251,49152
v_mul_f32_e32 v102, s54, v102
s_xor_b32 s72, s72, s28
Mp 84,140,144
Rt 174,251,49664
s_waitcnt lgkmcnt(8)
Mp 64,144,152
Rt 128,248,53248
v_mul_f32_e32 v103, s54, v103
Mp 72,144,156
)" R"(
Rt 130,248,53760
buffer_load_dwordx4 v9, s[4:7], s38 offen offset:1024 lds
Mp 76,148,156
Rt 132,249,53248
Cv 102,102,103
Mp 68,148,152
Rt 134,249,53760
v_accvgpr_write_b32 a99, v102
Mp 80,152,152
Rt 136,250,53248
v_mul_f32_e32 v104, s54, v104
Mp 88,152,156
Rt 138,250,53760
v_mul_f32_e32 v105, s54, v105
Mp 92,156,156
Rt 140,251,53248
Cv 104,104,105
Mp 84,156,152
Rt 142,251,53760
buffer_load_dwordx4 v10, s[4:7], s38 offen offset:2048 lds
s_waitcnt lgkmcnt(8)
Mp 64,160,160
Rt 144,248,57344
v_accvgpr_write_b32 a100, v104
Mp 72,160,164
Rt 146,248,57856
v_mul_f32_e32 v106, s54, v106
Mp 76,164,164
Rt 148,249,57344
v_mul_f32_e32 v107, s54, v107
Mp 68,164,160
Rt 150,249,57856
Cv 106,106,107
Mp 80,168,160
Rt 152,250,57344
v_accvgpr_write_b32 a101, v106
Mp 88,168,164
Rt 154,250,57856
buffer_load_dwordx4 v11, s[4:7], s38 offen offset:3072 lds
Mp 92,172,164
Rt 156,251,57344
v_mul_f32_e32 v108, s54, v108
Mp 84,172,160
Rt 158,251,57856
s_waitcnt lgkmcnt(8)
Mp 64,128,168
Rt 160,252,0
v_mul_f32_e32 v109, s54, v109
Mp 72,128,172
Rt 162,252,512
Cv 108,108,109
Mp 76,132,172
Rt 164,253,0
v_accvgpr_write_b32 a102, v108
Mp 68,132,168
Rt 166,253,512
v_mul_f32_e32 v110, s54, v110
Mp 80,136,168
Rt 168,254,0
v_mul_f32_e32 v111, s54, v111
Mp 88,136,172
Rt 170,254,512
Cv 110,110,111
Mp 92,140,172
)" R"(
Rt 172,255,0
v_accvgpr_write_b32 a103, v110
Mp 84,140,168
Rt 174,255,512
s_waitcnt vmcnt(12) lgkmcnt(8)
s_barrier
Mp 64,144,176
Rt 128,252,4096
v_mul_f32_e32 v112, s54, v112
Mp 72,144,180
Rt 130,252,4608
s_add_u32 s39, s39, 0x40000
Mp 76,148,180
Rt 132,253,4096
s_add_u32 m0, s37, 32768
Mp 68,148,176
Rt 134,253,4608
buffer_load_dwordx4 v12, s[8:11], s39 offen offset:0 lds
Mp 80,152,176
Rt 136,254,4096
v_mul_f32_e32 v113, s54, v113
Mp 88,152,180
Rt 138,254,4608
Cv 112,112,113
Mp 92,156,180
Rt 140,255,4096
v_accvgpr_write_b32 a104, v112
Mp 84,156,176
Rt 142,255,4608
s_waitcnt lgkmcnt(8)
Mp 64,160,184
Rt 144,252,8192
v_mul_f32_e32 v114, s54, v114
Mp 72,160,188
Rt 146,252,8704
buffer_load_dwordx4 v13, s[8:11], s39 offen offset:1024 lds
Mp 76,164,188
Rt 148,253,8192
v_mul_f32_e32 v115, s54, v115
Mp 68,164,184
Rt 150,253,8704
Cv 114,114,115
Mp 80,168,184
Rt 152,254,8192
v_accvgpr_write_b32 a105, v114
Mp 88,168,188
Rt 154,254,8704
v_mul_f32_e32 v116, s54, v116
Mp 92,172,188
Rt 156,255,8192
v_mul_f32_e32 v117, s54, v117
Mp 84,172,184
Rt 158,255,8704
buffer_load_dwordx4 v14, s[8:11], s39 offen offset:2048 lds
s_waitcnt lgkmcnt(8)
Mp 64,128,192
Rt 160,252,12288
Cv 116,116,117
Mp 72,128,196
Rt 162,252,12800
v_accvgpr_write_b32 a106, v116
Mp 76,132,196
Rt 164,253,12288
v_mul_f32_e32 v118, s54, v118
Mp 68,132,192
)" R"(
Rt 166,253,12800
v_mul_f32_e32 v119, s54, v119
Mp 80,136,192
Rt 168,254,12288
Cv 118,118,119
Mp 88,136,196
Rt 170,254,12800
buffer_load_dwordx4 v15, s[8:11], s39 offen offset:3072 lds
Mp 92,140,196
Rt 172,255,12288
v_accvgpr_write_b32 a107, v118
Mp 84,140,192
Rt 174,255,12800
s_waitcnt lgkmcnt(8)
Mp 64,144,200
Rt 128,252,16384
v_mul_f32_e32 v120, s54, v120
Mp 72,144,204
Rt 130,252,16896
s_add_u32 s39, s39, 0x40000
Mp 76,148,204
Rt 132,253,16384
s_add_u32 m0, s37, 49152
Mp 68,148,200
Rt 134,253,16896
buffer_load_dwordx4 v12, s[8:11], s39 offen offset:0 lds
Mp 80,152,200
Rt 136,254,16384
v_mul_f32_e32 v121, s54, v121
Mp 88,152,204
Rt 138,254,16896
Cv 120,120,121
Mp 92,156,204
Rt 140,255,16384
v_accvgpr_write_b32 a108, v120
Mp 84,156,200
Rt 142,255,16896
s_waitcnt lgkmcnt(8)
Mp 64,160,208
Rt 144,252,20480
Rt 0,18,0
Mp 72,160,212
Rt 146,252,20992
buffer_load_dwordx4 v13, s[8:11], s39 offen offset:1024 lds
Rt 2,18,2048
Mp 76,164,212
Rt 148,253,20480
Rt 4,19,0
Mp 68,164,208
Rt 150,253,20992
Rt 6,19,2048
Mp 80,168,208
Rt 152,254,20480
Rt 8,20,0
Mp 88,168,212
Rt 154,254,20992
Rt 10,20,2048
Mp 92,172,212
Rt 156,255,20480
Rt 12,21,0
Mp 84,172,208
Rt 158,255,20992
buffer_load_dwordx4 v14, s[8:11], s39 offen offset:2048 lds
Rt 14,21,2048
)" R"(
Mp 64,128,216
Rt 160,252,24576
Rt 16,22,0
Mp 72,128,220
Rt 162,252,25088
Rt 18,22,2048
Mp 76,132,220
Rt 164,253,24576
Rt 20,23,0
Mp 68,132,216
Rt 166,253,25088
Rt 22,23,2048
Mp 80,136,216
Rt 168,254,24576
Rt 24,24,0
Mp 88,136,220
Rt 170,254,25088
buffer_load_dwordx4 v15, s[8:11], s39 offen offset:3072 lds
Rt 26,24,2048
Mp 92,140,220
Rt 172,255,24576
Rt 28,25,0
Mp 84,140,216
Rt 174,255,25088
Rt 30,25,2048
Mp 64,144,224
Rb 32,16,0
v_mul_f32_e32 v122, s54, v122
Mp 72,144,228
Rb 36,16,2048
v_mul_f32_e32 v123, s54, v123
Mp 76,148,228
Rb 40,16,8192
Cv 122,122,123
Mp 68,148,224
Rb 44,16,10240
v_accvgpr_write_b32 a109, v122
Mp 80,152,224
Rb 48,16,16384
v_mul_f32_e32 v124, s54, v124
Mp 88,152,228
Rb 52,16,18432
v_mul_f32_e32 v125, s54, v125
Mp 92,156,228
Rb 56,16,24576
Cv 124,124,125
Mp 84,156,224
Rb 60,16,26624
s_waitcnt lgkmcnt(9)
Mp 64,160,232
v_accvgpr_write_b32 a110, v124
v_mul_f32_e32 v126, s54, v126
Mp 72,160,236
v_mul_f32_e32 v127, s54, v127
Cv 126,126,127
Mp 76,164,236
v_accvgpr_write_b32 a111, v126
Mp 68,164,232
buffer_load_dword a254, v28, s[4:7], s88 offen
Mp 80,168,232
Mp 88,168,236
Mp 92,172,236
Mp 84,172,232
s_cmp_eq_u32 s91, 1
)" R"(
s_waitcnt vmcnt(17) lgkmcnt(0)
s_barrier
s_cbranch_scc1 .LzBV
Mz 32,0,32
Rt 128,18,16384
v_mul_f32_e32 v64, s54, v64
Mz 96,0,40
v_mul_f32_e32 v65, s54, v65
Cv 64,64,65
Mz 128,0,44
Rt 130,18,18432
v_accvgpr_write_b32 a112, v64
Mz 160,0,48
s_add_u32 s39, s39, 0x40000
v_mul_f32_e32 v66, s54, v66
Mz 192,0,52
Rt 132,19,16384
s_add_u32 m0, s37, 0
Mz 224,0,56
buffer_load_dwordx4 v12, s[8:11], s39 offen offset:0 lds
v_mfma_f32_16x16x32_bf16 a[192:195], a[0:3], a[60:63], 0
Rt 134,19,18432
v_mul_f32_e32 v67, s54, v67
v_mfma_f32_16x16x32_bf16 a[196:199], a[4:7], a[60:63], 0
Cv 66,66,67
v_accvgpr_write_b32 a113, v66
Mz 228,4,56
Rt 136,20,16384
v_mul_f32_e32 v68, s54, v68
Mz 196,4,52
v_mul_f32_e32 v69, s54, v69
Cv 68,68,69
buffer_store_dwordx4 a[64:67], v2, s[20:23], s51 offen offset:0 sc0 sc1
Mz 164,4,48
Rt 138,20,18432
v_accvgpr_write_b32 a114, v68
Mz 132,4,44
v_mul_f32_e32 v70, s54, v70
v_mul_f32_e32 v71, s54, v71
Mz 100,4,40
Rt 140,21,16384
Cv 70,70,71
Mz 36,4,32
buffer_load_dwordx4 v13, s[8:11], s39 offen offset:1024 lds
Mz 40,8,32
Rt 142,21,18432
v_accvgpr_write_b32 a115, v70
Mz 104,8,40
v_mul_f32_e32 v72, s54, v72
v_mul_f32_e32 v73, s54, v73
Mz 136,8,44
Rt 144,22,16384
Cv 72,72,73
Mz 168,8,48
v_accvgpr_write_b32 a116, v72
v_mul_f32_e32 v74, s54, v74
buffer_store_dwordx4 a[68:71], v2, s[20:23], s51 offen offset:1024 sc0 sc1
Mz 200,8,52
Rt 146,22,18432
v_mul_f32_e32 v75, s54, v75
Mz 232,8,56
Cv 74,74,75
v_accvgpr_write_b32 a117, v74
v_mfma_f32_16x16x32_bf16 a[200:203], a[8:11], a[60:63], 0
Rt 148,23,16384
)" R"(
v_mul_f32_e32 v76, s54, v76
v_mfma_f32_16x16x32_bf16 a[204:207], a[12:15], a[60:63], 0
buffer_load_dwordx4 v14, s[8:11], s39 offen offset:2048 lds
Mz 236,12,56
Rt 150,23,18432
v_mul_f32_e32 v77, s54, v77
Mz 204,12,52
Cv 76,76,77
v_accvgpr_write_b32 a118, v76
Mz 172,12,48
Rt 152,24,16384
v_mul_f32_e32 v78, s54, v78
Mz 140,12,44
v_mul_f32_e32 v79, s54, v79
Cv 78,78,79
buffer_store_dwordx4 a[72:75], v2, s[20:23], s51 offen offset:2048 sc0 sc1
Mz 108,12,40
Rt 154,24,18432
v_accvgpr_write_b32 a119, v78
Mz 44,12,32
v_mul_f32_e32 v80, s54, v80
v_mul_f32_e32 v81, s54, v81
Mz 48,16,32
Rt 156,25,16384
Cv 80,80,81
Mz 112,16,40
buffer_load_dwordx4 v15, s[8:11], s39 offen offset:3072 lds
Mz 144,16,44
Rt 158,25,18432
v_accvgpr_write_b32 a120, v80
Mz 176,16,48
v_mul_f32_e32 v82, s54, v82
v_mul_f32_e32 v83, s54, v83
Mz 208,16,52
Rb 160,17,0
Cv 82,82,83
Mz 240,16,56
v_accvgpr_write_b32 a121, v82
v_mul_f32_e32 v84, s54, v84
buffer_store_dwordx4 a[76:79], v2, s[20:23], s51 offen offset:3072 sc0 sc1
v_mfma_f32_16x16x32_bf16 a[208:211], a[16:19], a[60:63], 0
Rb 164,17,2048
v_mul_f32_e32 v85, s54, v85
v_mfma_f32_16x16x32_bf16 a[212:215], a[20:23], a[60:63], 0
Cv 84,84,85
v_accvgpr_write_b32 a122, v84
Mz 244,20,56
Rb 168,17,8192
v_mul_f32_e32 v86, s54, v86
Mz 212,20,52
v_mul_f32_e32 v87, s54, v87
Cv 86,86,87
Mz 180,20,48
Rb 172,17,10240
v_accvgpr_write_b32 a123, v86
Mz 148,20,44
v_mul_f32_e32 v88, s54, v88
v_mul_f32_e32 v89, s54, v89
Mz 116,20,40
Rb 176,17,16384
Cv 88,88,89
Mz 52,20,32
v_accvgpr_write_b32 a124, v88
v_mul_f32_e32 v90, s54, v90
)" R"(
buffer_store_dwordx4 a[80:83], v3, s[20:23], s51 offen offset:0 sc0 sc1
Mz 56,24,32
Rb 180,17,18432
v_mul_f32_e32 v91, s54, v91
Mz 120,24,40
Cv 90,90,91
v_accvgpr_write_b32 a125, v90
Mz 152,24,44
Rb 184,17,24576
v_mul_f32_e32 v92, s54, v92
Mz 184,24,48
v_mul_f32_e32 v93, s54, v93
Cv 92,92,93
Mz 216,24,52
Rb 188,17,26624
v_accvgpr_write_b32 a126, v92
Mz 248,24,56
v_mul_f32_e32 v94, s54, v94
v_mul_f32_e32 v95, s54, v95
v_mfma_f32_16x16x32_bf16 a[216:219], a[24:27], a[60:63], 0
Cv 94,94,95
v_accvgpr_write_b32 a127, v94
v_mfma_f32_16x16x32_bf16 a[220:223], a[28:31], a[60:63], 0
buffer_store_dwordx4 a[84:87], v3, s[20:23], s51 offen offset:1024 sc0 sc1
Mz 252,28,56
Mz 220,28,52
Mz 188,28,48
Mz 156,28,44
Mz 124,28,40
Mz 60,28,32
Mz 64,0,36
Mz 68,4,36
buffer_store_dwordx4 a[88:91], v3, s[20:23], s51 offen offset:2048 sc0 sc1
Mz 72,8,36
Mz 76,12,36
Mz 80,16,36
Mz 84,20,36
Mz 88,24,36
Mz 92,28,36
s_waitcnt vmcnt(16) lgkmcnt(0)
s_barrier
Mv 32,128,160
Rt 0,18,32768
Mv 64,128,164
s_add_u32 s38, s38, 0xffff0080
Mv 96,128,168
Rt 2,18,34816
s_add_u32 m0, s36, 0
Mv 128,128,172
buffer_load_dwordx4 v8, s[4:7], s38 offen offset:0 lds
Mv 160,128,176
Rt 4,19,32768
Mv 192,128,180
Mv 224,128,184
Rt 6,19,34816
Ma 192,128,188
buffer_load_dwordx4 v9, s[4:7], s38 offen offset:1024 lds
Ma 196,132,188
Rt 8,20,32768
Mv 228,132,184
Mv 196,132,180
Rt 10,20,34816
Mv 164,132,176
buffer_load_dwordx4 v10, s[4:7], s38 offen offset:2048 lds
Mv 132,132,172
)" R"(
Rt 12,21,32768
Mv 100,132,168
Mv 68,132,164
Rt 14,21,34816
Mv 36,132,160
buffer_load_dwordx4 v11, s[4:7], s38 offen offset:3072 lds
Mv 40,136,160
Rt 16,22,32768
Mv 72,136,164
s_add_u32 s38, s38, 0x10000
Mv 104,136,168
Rt 18,22,34816
s_add_u32 m0, s36, 4096
Mv 136,136,172
buffer_load_dwordx4 v8, s[4:7], s38 offen offset:0 lds
Mv 168,136,176
Rt 20,23,32768
Mv 200,136,180
Mv 232,136,184
Rt 22,23,34816
Ma 200,136,188
buffer_load_dwordx4 v9, s[4:7], s38 offen offset:1024 lds
Ma 204,140,188
Rt 24,24,32768
Mv 236,140,184
Mv 204,140,180
Rt 26,24,34816
Mv 172,140,176
buffer_load_dwordx4 v10, s[4:7], s38 offen offset:2048 lds
Mv 140,140,172
Rt 28,25,32768
Mv 108,140,168
Mv 76,140,164
Rt 30,25,34816
Mv 44,140,160
buffer_load_dwordx4 v11, s[4:7], s38 offen offset:3072 lds
Mv 48,144,160
Rb 32,16,32768
Mv 80,144,164
Mv 112,144,168
Rb 36,16,34816
Mv 144,144,172
s_add_u32 s39, s39, 0x40000
Mv 176,144,176
Rb 40,16,40960
s_add_u32 m0, s37, 16384
Mv 208,144,180
buffer_load_dwordx4 v12, s[8:11], s39 offen offset:0 lds
Mv 240,144,184
Rb 44,16,43008
Ma 208,144,188
Ma 212,148,188
Rb 48,16,49152
Mv 244,148,184
Mv 212,148,180
Rb 52,16,51200
Mv 180,148,176
buffer_load_dwordx4 v13, s[8:11], s39 offen offset:1024 lds
Mv 148,148,172
Rb 56,16,57344
Mv 116,148,168
Mv 84,148,164
Rb 60,16,59392
Mv 52,148,160
)" R"(
Mv 56,152,160
Mv 88,152,164
buffer_load_dwordx4 v14, s[8:11], s39 offen offset:2048 lds
Mv 120,152,168
Mv 152,152,172
Mv 184,152,176
Mv 216,152,180
Mv 248,152,184
Ma 216,152,188
buffer_load_dwordx4 v15, s[8:11], s39 offen offset:3072 lds
Ma 220,156,188
Mv 252,156,184
Mv 220,156,180
Mv 188,156,176
buffer_store_dwordx4 a[92:95], v3, s[20:23], s51 offen offset:3072 sc0 sc1
Mv 156,156,172
Mv 124,156,168
Mv 92,156,164
Mv 60,156,160
s_waitcnt vmcnt(25) lgkmcnt(0)
s_barrier
Mv 32,0,32
Rt 128,18,49152
s_mov_b64 s[16:17], s[76:77]
Mv 64,0,36
s_cmp_lt_u32 s33, 3
s_cselect_b32 s18, 0x188000, 0
Mv 96,0,40
Rt 130,18,51200
s_lshl_b32 s43, s2, 11
Mv 128,0,44
s_add_u32 s39, s39, 0x40000
s_sub_u32 s43, s43, 0x180000
Mv 160,0,48
Rt 132,19,49152
s_add_u32 m0, s37, 32768
Mv 192,0,52
buffer_load_dwordx4 v12, s[8:11], s39 offen offset:0 lds
Mv 224,0,56
Rt 134,19,51200
v_min_u32 v26, 15, v0
Ma 192,0,60
v_lshlrev_b32 v26, 7, v26
v_lshrrev_b32 v5, 2, v0
Ma 196,4,60
Rt 136,20,49152
v_and_b32 v5, 8, v5
Mv 228,4,56
s_add_u32 s51, s51, 0x10000
buffer_store_dwordx4 a[96:99], v2, s[20:23], s51 offen offset:0 sc0 sc1
Mv 196,4,52
Rt 138,20,51200
v_and_or_b32 v5, v0, 7, v5
Mv 164,4,48
v_lshlrev_b32 v5, 8, v5
v_and_or_b32 v5, v0, 24, v5
Mv 132,4,44
Rt 140,21,49152
v_lshrrev_b32 v5, 1, v5
Mv 100,4,40
buffer_load_dwordx4 v13, s[8:11], s39 offen offset:1024 lds
Mv 68,4,36
Rt 142,21,51200
Mv 36,4,32
Mv 40,8,32
)" R"(
Rt 144,22,49152
Mv 72,8,36
buffer_store_dwordx4 a[100:103], v2, s[20:23], s51 offen offset:1024 sc0 sc1
Mv 104,8,40
Rt 146,22,51200
Mv 136,8,44
Mv 168,8,48
Rt 148,23,49152
Mv 200,8,52
buffer_load_dwordx4 v14, s[8:11], s39 offen offset:2048 lds
Mv 232,8,56
Rt 150,23,51200
Ma 200,8,60
Ma 204,12,60
Rt 152,24,49152
Mv 236,12,56
buffer_store_dwordx4 a[104:107], v2, s[20:23], s51 offen offset:2048 sc0 sc1
Mv 204,12,52
Rt 154,24,51200
Mv 172,12,48
Mv 140,12,44
Rt 156,25,49152
Mv 108,12,40
buffer_load_dwordx4 v15, s[8:11], s39 offen offset:3072 lds
Mv 76,12,36
Rt 158,25,51200
Mv 44,12,32
Mv 48,16,32
Rb 160,17,32768
Mv 80,16,36
buffer_store_dwordx4 a[108:111], v2, s[20:23], s51 offen offset:3072 sc0 sc1
Mv 112,16,40
Rb 164,17,34816
Mv 144,16,44
Mv 176,16,48
Rb 168,17,40960
Mv 208,16,52
Mv 240,16,56
Rb 172,17,43008
Ma 208,16,60
Ma 212,20,60
Rb 176,17,49152
Mv 244,20,56
buffer_store_dwordx4 a[112:115], v3, s[20:23], s51 offen offset:0 sc0 sc1
Mv 212,20,52
Rb 180,17,51200
Mv 180,20,48
Mv 148,20,44
Rb 184,17,57344
Mv 116,20,40
Mv 84,20,36
Rb 188,17,59392
Mv 52,20,32
Mv 56,24,32
Mv 88,24,36
buffer_store_dwordx4 a[116:119], v3, s[20:23], s51 offen offset:1024 sc0 sc1
Mv 120,24,40
Mv 152,24,44
Mv 184,24,48
Mv 216,24,52
Mv 248,24,56
Ma 216,24,60
Ma 220,28,60
Mv 252,28,56
)" R"(
buffer_store_dwordx4 a[120:123], v3, s[20:23], s51 offen offset:2048 sc0 sc1
Mv 220,28,52
Mv 188,28,48
Mv 156,28,44
Mv 124,28,40
Mv 92,28,36
Mv 60,28,32
s_waitcnt vmcnt(16) lgkmcnt(0)
s_barrier
Mv 32,128,160
Rt 0,18,0
v_accvgpr_write_b32 a224, -1
Mv 64,128,164
s_add_u32 s38, s38, 0xffff0080
v_accvgpr_write_b32 a225, -1
Mv 96,128,168
Rt 2,18,2048
s_add_u32 m0, s36, 32768
Mv 128,128,172
buffer_load_dwordx4 v8, s[4:7], s38 offen offset:0 lds
Mv 160,128,176
Rt 4,19,0
v_accvgpr_write_b32 a226, -1
Mv 192,128,180
v_accvgpr_write_b32 a227, -1
s_add_u32 s88, s73, 0x4000
Mv 224,128,184
Rt 6,19,2048
s_mov_b32 s40, 6
Ma 192,128,188
buffer_load_dwordx4 v9, s[4:7], s38 offen offset:1024 lds
Ma 196,132,188
Rt 8,20,0
Mv 228,132,184
Mv 196,132,180
Rt 10,20,2048
Mv 164,132,176
buffer_load_dwordx4 v10, s[4:7], s38 offen offset:2048 lds
Mv 132,132,172
Rt 12,21,0
Mv 100,132,168
Mv 68,132,164
Rt 14,21,2048
Mv 36,132,160
buffer_load_dwordx4 v11, s[4:7], s38 offen offset:3072 lds
Mv 40,136,160
Rt 16,22,0
Mv 72,136,164
s_add_u32 s38, s38, 0x10000
Mv 104,136,168
Rt 18,22,2048
s_add_u32 m0, s36, 36864
Mv 136,136,172
buffer_load_dwordx4 v8, s[4:7], s38 offen offset:0 lds
Mv 168,136,176
Rt 20,23,0
Mv 200,136,180
Mv 232,136,184
Rt 22,23,2048
Ma 200,136,188
buffer_load_dwordx4 v9, s[4:7], s38 offen offset:1024 lds
Ma 204,140,188
Rt 24,24,0
Mv 236,140,184
Mv 204,140,180
)" R"(
Rt 26,24,2048
Mv 172,140,176
buffer_load_dwordx4 v10, s[4:7], s38 offen offset:2048 lds
Mv 140,140,172
Rt 28,25,0
Mv 108,140,168
Mv 76,140,164
Rt 30,25,2048
Mv 44,140,160
buffer_load_dwordx4 v11, s[4:7], s38 offen offset:3072 lds
Mv 48,144,160
Rb 32,16,0
Mv 80,144,164
Mv 112,144,168
Rb 36,16,2048
Mv 144,144,172
s_add_u32 s39, s39, 0x40000
Mv 176,144,176
Rb 40,16,8192
s_add_u32 m0, s37, 49152
Mv 208,144,180
buffer_load_dwordx4 v12, s[8:11], s39 offen offset:0 lds
Mv 240,144,184
Rb 44,16,10240
Ma 208,144,188
Ma 212,148,188
Rb 48,16,16384
Mv 244,148,184
Mv 212,148,180
Rb 52,16,18432
Mv 180,148,176
buffer_load_dwordx4 v13, s[8:11], s39 offen offset:1024 lds
Mv 148,148,172
Rb 56,16,24576
Mv 116,148,168
Mv 84,148,164
Rb 60,16,26624
Mv 52,148,160
Mv 56,152,160
Mv 88,152,164
buffer_load_dwordx4 v14, s[8:11], s39 offen offset:2048 lds
Mv 120,152,168
Mv 152,152,172
Mv 184,152,176
Mv 216,152,180
Mv 248,152,184
Ma 216,152,188
buffer_load_dwordx4 v15, s[8:11], s39 offen offset:3072 lds
Ma 220,156,188
Mv 252,156,184
Mv 220,156,180
Mv 188,156,176
buffer_store_dwordx4 a[124:127], v3, s[20:23], s51 offen offset:3072 sc0 sc1
Mv 156,156,172
Mv 124,156,168
Mv 92,156,164
Mv 60,156,160
s_waitcnt vmcnt(25) lgkmcnt(0)
s_barrier
.LzPBK:
Mv 32,0,32
Rt 128,18,16384
Mv 64,0,36
Mv 96,0,40
Rt 130,18,18432
)" R"(
Mv 128,0,44
s_add_u32 s39, s39, 0x40000
Mv 160,0,48
Rt 132,19,16384
s_add_u32 m0, s37, 0
Mv 192,0,52
buffer_load_dwordx4 v12, s[8:11], s39 offen offset:0 lds
Mv 224,0,56
Rt 134,19,18432
Ma 192,0,60
Ma 196,4,60
Rt 136,20,16384
Mv 228,4,56
Mv 196,4,52
Rt 138,20,18432
Mv 164,4,48
Mv 132,4,44
Rt 140,21,16384
Mv 100,4,40
buffer_load_dwordx4 v13, s[8:11], s39 offen offset:1024 lds
Mv 68,4,36
Rt 142,21,18432
Mv 36,4,32
Mv 40,8,32
Rt 144,22,16384
Mv 72,8,36
Mv 104,8,40
Rt 146,22,18432
Mv 136,8,44
Mv 168,8,48
Rt 148,23,16384
Mv 200,8,52
buffer_load_dwordx4 v14, s[8:11], s39 offen offset:2048 lds
Mv 232,8,56
Rt 150,23,18432
Ma 200,8,60
Ma 204,12,60
Rt 152,24,16384
Mv 236,12,56
Mv 204,12,52
Rt 154,24,18432
Mv 172,12,48
Mv 140,12,44
Rt 156,25,16384
Mv 108,12,40
buffer_load_dwordx4 v15, s[8:11], s39 offen offset:3072 lds
Mv 76,12,36
Rt 158,25,18432
Mv 44,12,32
Mv 48,16,32
Rb 160,17,0
Mv 80,16,36
Mv 112,16,40
Rb 164,17,2048
Mv 144,16,44
Mv 176,16,48
Rb 168,17,8192
Mv 208,16,52
buffer_store_dwordx4 a[224:227], v1, s[20:23], s72 offen sc0 sc1
Mv 240,16,56
Rb 172,17,10240
s_add_u32 s72, s72, 1024
Ma 208,16,60
Ma 212,20,60
)" R"(
Rb 176,17,16384
Mv 244,20,56
Mv 212,20,52
Rb 180,17,18432
Mv 180,20,48
Mv 148,20,44
Rb 184,17,24576
Mv 116,20,40
Mv 84,20,36
Rb 188,17,26624
Mv 52,20,32
Mv 56,24,32
Mv 88,24,36
buffer_store_dwordx4 a[224:227], v1, s[20:23], s88 offen sc0 sc1
Mv 120,24,40
s_add_u32 s88, s88, 1024
Mv 152,24,44
Mv 184,24,48
Mv 216,24,52
Mv 248,24,56
Ma 216,24,60
Ma 220,28,60
Mv 252,28,56
Mv 220,28,52
Mv 188,28,48
Mv 156,28,44
Mv 124,28,40
Mv 92,28,36
Mv 60,28,32
s_waitcnt vmcnt(11) lgkmcnt(0)
s_barrier
Mv 32,128,160
Rt 0,18,32768
Mv 64,128,164
s_add_u32 s38, s38, 0xffff0080
Mv 96,128,168
Rt 2,18,34816
s_add_u32 m0, s36, 0
Mv 128,128,172
buffer_load_dwordx4 v8, s[4:7], s38 offen offset:0 lds
Mv 160,128,176
Rt 4,19,32768
Mv 192,128,180
Mv 224,128,184
Rt 6,19,34816
Ma 192,128,188
buffer_load_dwordx4 v9, s[4:7], s38 offen offset:1024 lds
Ma 196,132,188
Rt 8,20,32768
Mv 228,132,184
Mv 196,132,180
Rt 10,20,34816
Mv 164,132,176
buffer_load_dwordx4 v10, s[4:7], s38 offen offset:2048 lds
Mv 132,132,172
Rt 12,21,32768
Mv 100,132,168
Mv 68,132,164
Rt 14,21,34816
Mv 36,132,160
buffer_load_dwordx4 v11, s[4:7], s38 offen offset:3072 lds
Mv 40,136,160
Rt 16,22,32768
Mv 72,136,164
s_add_u32 s38, s38, 0x10000
)" R"(
Mv 104,136,168
Rt 18,22,34816
s_add_u32 m0, s36, 4096
Mv 136,136,172
buffer_load_dwordx4 v8, s[4:7], s38 offen offset:0 lds
Mv 168,136,176
Rt 20,23,32768
Mv 200,136,180
Mv 232,136,184
Rt 22,23,34816
Ma 200,136,188
buffer_load_dwordx4 v9, s[4:7], s38 offen offset:1024 lds
Ma 204,140,188
Rt 24,24,32768
Mv 236,140,184
Mv 204,140,180
Rt 26,24,34816
Mv 172,140,176
buffer_load_dwordx4 v10, s[4:7], s38 offen offset:2048 lds
Mv 140,140,172
Rt 28,25,32768
Mv 108,140,168
Mv 76,140,164
Rt 30,25,34816
Mv 44,140,160
buffer_load_dwordx4 v11, s[4:7], s38 offen offset:3072 lds
Mv 48,144,160
Rb 32,16,32768
Mv 80,144,164
Mv 112,144,168
Rb 36,16,34816
Mv 144,144,172
s_add_u32 s39, s39, 0x40000
Mv 176,144,176
Rb 40,16,40960
s_add_u32 m0, s37, 16384
Mv 208,144,180
buffer_load_dwordx4 v12, s[8:11], s39 offen offset:0 lds
Mv 240,144,184
Rb 44,16,43008
Ma 208,144,188
Ma 212,148,188
Rb 48,16,49152
Mv 244,148,184
Mv 212,148,180
Rb 52,16,51200
Mv 180,148,176
buffer_load_dwordx4 v13, s[8:11], s39 offen offset:1024 lds
Mv 148,148,172
Rb 56,16,57344
Mv 116,148,168
Mv 84,148,164
Rb 60,16,59392
Mv 52,148,160
Mv 56,152,160
Mv 88,152,164
buffer_load_dwordx4 v14, s[8:11], s39 offen offset:2048 lds
Mv 120,152,168
Mv 152,152,172
Mv 184,152,176
Mv 216,152,180
Mv 248,152,184
Ma 216,152,188
buffer_load_dwordx4 v15, s[8:11], s39 offen offset:3072 lds
)" R"(
Ma 220,156,188
Mv 252,156,184
s_add_u32 s43, s43, 0x40000
Mv 220,156,180
Mv 188,156,176
buffer_load_dword v27, v26, s[16:19], s43 offen
Mv 156,156,172
Mv 124,156,168
Mv 92,156,164
Mv 60,156,160
s_waitcnt vmcnt(19) lgkmcnt(0)
s_barrier
buffer_store_dword v6, v7, s[20:23], s52 offen offset:0 sc0 sc1
s_mov_b32 s52, 0x40000000
Mv 32,0,32
Rt 128,18,49152
Mv 64,0,36
Mv 96,0,40
Rt 130,18,51200
Mv 128,0,44
s_add_u32 s39, s39, 0x40000
Mv 160,0,48
Rt 132,19,49152
s_add_u32 m0, s37, 32768
Mv 192,0,52
buffer_load_dwordx4 v12, s[8:11], s39 offen offset:0 lds
Mv 224,0,56
Rt 134,19,51200
Ma 192,0,60
Ma 196,4,60
Rt 136,20,49152
Mv 228,4,56
Mv 196,4,52
Rt 138,20,51200
Mv 164,4,48
Mv 132,4,44
Rt 140,21,49152
Mv 100,4,40
buffer_load_dwordx4 v13, s[8:11], s39 offen offset:1024 lds
Mv 68,4,36
Rt 142,21,51200
Mv 36,4,32
Mv 40,8,32
Rt 144,22,49152
Mv 72,8,36
Mv 104,8,40
Rt 146,22,51200
Mv 136,8,44
Mv 168,8,48
Rt 148,23,49152
Mv 200,8,52
buffer_load_dwordx4 v14, s[8:11], s39 offen offset:2048 lds
Mv 232,8,56
Rt 150,23,51200
Ma 200,8,60
Ma 204,12,60
Rt 152,24,49152
Mv 236,12,56
Mv 204,12,52
Rt 154,24,51200
Mv 172,12,48
Mv 140,12,44
Rt 156,25,49152
Mv 108,12,40
buffer_load_dwordx4 v15, s[8:11], s39 offen offset:3072 lds
)" R"(
Mv 76,12,36
Rt 158,25,51200
Mv 44,12,32
Mv 48,16,32
Rb 160,17,32768
Mv 80,16,36
Mv 112,16,40
Rb 164,17,34816
Mv 144,16,44
Mv 176,16,48
Rb 168,17,40960
Mv 208,16,52
buffer_store_dwordx4 a[224:227], v1, s[20:23], s72 offen sc0 sc1
Mv 240,16,56
Rb 172,17,43008
s_add_u32 s72, s72, 1024
Ma 208,16,60
Ma 212,20,60
Rb 176,17,49152
Mv 244,20,56
Mv 212,20,52
Rb 180,17,51200
Mv 180,20,48
Mv 148,20,44
Rb 184,17,57344
Mv 116,20,40
Mv 84,20,36
Rb 188,17,59392
Mv 52,20,32
Mv 56,24,32
Mv 88,24,36
buffer_store_dwordx4 a[224:227], v1, s[20:23], s88 offen sc0 sc1
Mv 120,24,40
s_add_u32 s88, s88, 1024
Mv 152,24,44
Mv 184,24,48
Mv 216,24,52
Mv 248,24,56
Ma 216,24,60
Ma 220,28,60
Mv 252,28,56
Mv 220,28,52
Mv 188,28,48
Mv 156,28,44
Mv 124,28,40
Mv 92,28,36
s_cmp_eq_u32 s40, 3
s_cmov_b32 s72, 0x40000000
s_cmov_b32 s88, 0x40000000
Mv 60,28,32
s_waitcnt vmcnt(12) lgkmcnt(0)
s_barrier
Mv 32,128,160
Rt 0,18,0
Mv 64,128,164
s_add_u32 s38, s38, 0xffff0080
Mv 96,128,168
Rt 2,18,2048
s_add_u32 m0, s36, 32768
Mv 128,128,172
buffer_load_dwordx4 v8, s[4:7], s38 offen offset:0 lds
Mv 160,128,176
Rt 4,19,0
Mv 192,128,180
Mv 224,128,184
)" R"(
Rt 6,19,2048
Ma 192,128,188
buffer_load_dwordx4 v9, s[4:7], s38 offen offset:1024 lds
Ma 196,132,188
Rt 8,20,0
Mv 228,132,184
Mv 196,132,180
Rt 10,20,2048
Mv 164,132,176
buffer_load_dwordx4 v10, s[4:7], s38 offen offset:2048 lds
Mv 132,132,172
Rt 12,21,0
Mv 100,132,168
Mv 68,132,164
Rt 14,21,2048
Mv 36,132,160
buffer_load_dwordx4 v11, s[4:7], s38 offen offset:3072 lds
Mv 40,136,160
Rt 16,22,0
Mv 72,136,164
s_add_u32 s38, s38, 0x10000
Mv 104,136,168
Rt 18,22,2048
s_add_u32 m0, s36, 36864
Mv 136,136,172
buffer_load_dwordx4 v8, s[4:7], s38 offen offset:0 lds
Mv 168,136,176
Rt 20,23,0
Mv 200,136,180
Mv 232,136,184
Rt 22,23,2048
Ma 200,136,188
buffer_load_dwordx4 v9, s[4:7], s38 offen offset:1024 lds
Ma 204,140,188
Rt 24,24,0
Mv 236,140,184
Mv 204,140,180
Rt 26,24,2048
Mv 172,140,176
buffer_load_dwordx4 v10, s[4:7], s38 offen offset:2048 lds
Mv 140,140,172
Rt 28,25,0
Mv 108,140,168
Mv 76,140,164
Rt 30,25,2048
Mv 44,140,160
buffer_load_dwordx4 v11, s[4:7], s38 offen offset:3072 lds
Mv 48,144,160
Rb 32,16,0
Mv 80,144,164
Mv 112,144,168
Rb 36,16,2048
Mv 144,144,172
s_add_u32 s39, s39, 0x40000
Mv 176,144,176
Rb 40,16,8192
s_add_u32 m0, s37, 49152
Mv 208,144,180
buffer_load_dwordx4 v12, s[8:11], s39 offen offset:0 lds
Mv 240,144,184
Rb 44,16,10240
Ma 208,144,188
Ma 212,148,188
Rb 48,16,16384
)" R"(
Mv 244,148,184
Mv 212,148,180
Rb 52,16,18432
Mv 180,148,176
buffer_load_dwordx4 v13, s[8:11], s39 offen offset:1024 lds
Mv 148,148,172
Rb 56,16,24576
Mv 116,148,168
Mv 84,148,164
Rb 60,16,26624
Mv 52,148,160
Mv 56,152,160
Mv 88,152,164
buffer_load_dwordx4 v14, s[8:11], s39 offen offset:2048 lds
Mv 120,152,168
Mv 152,152,172
Mv 184,152,176
Mv 216,152,180
Mv 248,152,184
Ma 216,152,188
buffer_load_dwordx4 v15, s[8:11], s39 offen offset:3072 lds
Ma 220,156,188
Mv 252,156,184
s_add_u32 s43, s43, 0x40000
Mv 220,156,180
s_sub_u32 s40, s40, 1
Mv 188,156,176
buffer_load_dword v27, v26, s[16:19], s43 offen
Mv 156,156,172
Mv 124,156,168
Mv 92,156,164
Mv 60,156,160
s_cmp_lg_u32 s40, 0
s_waitcnt vmcnt(21) lgkmcnt(0)
s_barrier
s_cbranch_scc1 .LzPBK
s_branch .LzPBKX
.LzBV:
Mz 32,32,0
Rt 128,18,16384
v_mul_f32_e32 v64, s54, v64
Mz 36,36,0
v_mul_f32_e32 v65, s54, v65
Cv 64,64,65
Mz 96,40,0
Rt 130,18,18432
v_accvgpr_write_b32 a112, v64
Mz 100,44,0
s_add_u32 s39, s39, 0x40000
v_mul_f32_e32 v66, s54, v66
Mz 160,48,0
Rt 132,19,16384
s_add_u32 m0, s37, 0
Mz 164,52,0
buffer_load_dwordx4 v12, s[8:11], s39 offen offset:0 lds
Mz 224,56,0
Rt 134,19,18432
v_mul_f32_e32 v67, s54, v67
Mz 228,60,0
Cv 66,66,67
v_accvgpr_write_b32 a113, v66
Mz 236,60,4
Rt 136,20,16384
v_mul_f32_e32 v68, s54, v68
Mz 232,56,4
)" R"(
v_mul_f32_e32 v69, s54, v69
Cv 68,68,69
buffer_store_dwordx4 a[64:67], v2, s[20:23], s51 offen offset:0 sc0 sc1
Mz 172,52,4
Rt 138,20,18432
v_accvgpr_write_b32 a114, v68
Mz 168,48,4
v_mul_f32_e32 v70, s54, v70
v_mul_f32_e32 v71, s54, v71
Mz 108,44,4
Rt 140,21,16384
Cv 70,70,71
Mz 104,40,4
buffer_load_dwordx4 v13, s[8:11], s39 offen offset:1024 lds
Mz 44,36,4
Rt 142,21,18432
v_accvgpr_write_b32 a115, v70
Mz 40,32,4
v_mul_f32_e32 v72, s54, v72
v_mul_f32_e32 v73, s54, v73
Mz 48,32,8
Rt 144,22,16384
Cv 72,72,73
Mz 52,36,8
v_accvgpr_write_b32 a116, v72
v_mul_f32_e32 v74, s54, v74
buffer_store_dwordx4 a[68:71], v2, s[20:23], s51 offen offset:1024 sc0 sc1
Mz 112,40,8
Rt 146,22,18432
v_mul_f32_e32 v75, s54, v75
Mz 116,44,8
Cv 74,74,75
v_accvgpr_write_b32 a117, v74
Mz 176,48,8
Rt 148,23,16384
v_mul_f32_e32 v76, s54, v76
Mz 180,52,8
buffer_load_dwordx4 v14, s[8:11], s39 offen offset:2048 lds
Mz 240,56,8
Rt 150,23,18432
v_mul_f32_e32 v77, s54, v77
Mz 244,60,8
Cv 76,76,77
v_accvgpr_write_b32 a118, v76
Mz 252,60,12
Rt 152,24,16384
v_mul_f32_e32 v78, s54, v78
Mz 248,56,12
v_mul_f32_e32 v79, s54, v79
Cv 78,78,79
buffer_store_dwordx4 a[72:75], v2, s[20:23], s51 offen offset:2048 sc0 sc1
Mz 188,52,12
Rt 154,24,18432
v_accvgpr_write_b32 a119, v78
Mz 184,48,12
v_mul_f32_e32 v80, s54, v80
v_mul_f32_e32 v81, s54, v81
Mz 124,44,12
Rt 156,25,16384
Cv 80,80,81
Mz 120,40,12
buffer_load_dwordx4 v15, s[8:11], s39 offen offset:3072 lds
Mz 60,36,12
Rt 158,25,18432
)" R"(
v_accvgpr_write_b32 a120, v80
Mz 56,32,12
v_mul_f32_e32 v82, s54, v82
v_mul_f32_e32 v83, s54, v83
Mz 128,40,16
Rb 160,17,0
Cv 82,82,83
Mz 132,44,16
v_accvgpr_write_b32 a121, v82
v_mul_f32_e32 v84, s54, v84
buffer_store_dwordx4 a[76:79], v2, s[20:23], s51 offen offset:3072 sc0 sc1
Mz 192,48,16
Rb 164,17,2048
v_mul_f32_e32 v85, s54, v85
Mz 196,52,16
Cv 84,84,85
v_accvgpr_write_b32 a122, v84
v_mfma_f32_16x16x32_bf16 a[192:195], a[56:59], a[16:19], 0
Rb 168,17,8192
v_mul_f32_e32 v86, s54, v86
v_mfma_f32_16x16x32_bf16 a[196:199], a[60:63], a[16:19], 0
v_mul_f32_e32 v87, s54, v87
Cv 86,86,87
v_mfma_f32_16x16x32_bf16 a[204:207], a[60:63], a[20:23], 0
Rb 172,17,10240
v_accvgpr_write_b32 a123, v86
v_mfma_f32_16x16x32_bf16 a[200:203], a[56:59], a[20:23], 0
v_mul_f32_e32 v88, s54, v88
v_mul_f32_e32 v89, s54, v89
Mz 204,52,20
Rb 176,17,16384
Cv 88,88,89
Mz 200,48,20
v_accvgpr_write_b32 a124, v88
v_mul_f32_e32 v90, s54, v90
buffer_store_dwordx4 a[80:83], v3, s[20:23], s51 offen offset:0 sc0 sc1
Mz 140,44,20
Rb 180,17,18432
v_mul_f32_e32 v91, s54, v91
Mz 136,40,20
Cv 90,90,91
v_accvgpr_write_b32 a125, v90
Mz 144,40,24
Rb 184,17,24576
v_mul_f32_e32 v92, s54, v92
Mz 148,44,24
v_mul_f32_e32 v93, s54, v93
Cv 92,92,93
Mz 208,48,24
Rb 188,17,26624
v_accvgpr_write_b32 a126, v92
Mz 212,52,24
v_mul_f32_e32 v94, s54, v94
v_mul_f32_e32 v95, s54, v95
v_mfma_f32_16x16x32_bf16 a[208:211], a[56:59], a[24:27], 0
Cv 94,94,95
v_accvgpr_write_b32 a127, v94
v_mfma_f32_16x16x32_bf16 a[212:215], a[60:63], a[24:27], 0
buffer_store_dwordx4 a[84:87], v3, s[20:23], s51 offen offset:1024 sc0 sc1
v_mfma_f32_16x16x32_bf16 a[220:223], a[60:63], a[28:31], 0
v_mfma_f32_16x16x32_bf16 a[216:219], a[56:59], a[28:31], 0
Mz 220,52,28
Mz 216,48,28
Mz 156,44,28
)" R"(
Mz 152,40,28
Mz 64,32,16
Mz 68,36,16
buffer_store_dwordx4 a[88:91], v3, s[20:23], s51 offen offset:2048 sc0 sc1
Mz 72,32,20
Mz 76,36,20
Mz 80,32,24
Mz 84,36,24
Mz 88,32,28
Mz 92,36,28
s_waitcnt vmcnt(16) lgkmcnt(0)
s_barrier
Mv 32,160,128
Rt 0,18,32768
Mv 36,164,128
s_add_u32 s38, s38, 0xffff0080
Mv 96,168,128
Rt 2,18,34816
s_add_u32 m0, s36, 0
Mv 100,172,128
buffer_load_dwordx4 v8, s[4:7], s38 offen offset:0 lds
Mv 160,176,128
Rt 4,19,32768
Mv 164,180,128
Mv 224,184,128
Rt 6,19,34816
Mv 228,188,128
buffer_load_dwordx4 v9, s[4:7], s38 offen offset:1024 lds
Mv 236,188,132
Rt 8,20,32768
Mv 232,184,132
Mv 172,180,132
Rt 10,20,34816
Mv 168,176,132
buffer_load_dwordx4 v10, s[4:7], s38 offen offset:2048 lds
Mv 108,172,132
Rt 12,21,32768
Mv 104,168,132
Mv 44,164,132
Rt 14,21,34816
Mv 40,160,132
buffer_load_dwordx4 v11, s[4:7], s38 offen offset:3072 lds
Mv 48,160,136
Rt 16,22,32768
Mv 52,164,136
s_add_u32 s38, s38, 0x10000
Mv 112,168,136
Rt 18,22,34816
s_add_u32 m0, s36, 4096
Mv 116,172,136
buffer_load_dwordx4 v8, s[4:7], s38 offen offset:0 lds
Mv 176,176,136
Rt 20,23,32768
Mv 180,180,136
Mv 240,184,136
Rt 22,23,34816
Mv 244,188,136
buffer_load_dwordx4 v9, s[4:7], s38 offen offset:1024 lds
Mv 252,188,140
Rt 24,24,32768
Mv 248,184,140
Mv 188,180,140
Rt 26,24,34816
Mv 184,176,140
buffer_load_dwordx4 v10, s[4:7], s38 offen offset:2048 lds
)" R"(
Mv 124,172,140
Rt 28,25,32768
Mv 120,168,140
Mv 60,164,140
Rt 30,25,34816
Mv 56,160,140
buffer_load_dwordx4 v11, s[4:7], s38 offen offset:3072 lds
Mv 64,160,144
Rb 32,16,32768
Mv 68,164,144
Mv 128,168,144
Rb 36,16,34816
Mv 132,172,144
s_add_u32 s39, s39, 0x40000
Mv 192,176,144
Rb 40,16,40960
s_add_u32 m0, s37, 16384
Mv 196,180,144
buffer_load_dwordx4 v12, s[8:11], s39 offen offset:0 lds
Ma 192,184,144
Rb 44,16,43008
Ma 196,188,144
Ma 204,188,148
Rb 48,16,49152
Ma 200,184,148
Mv 204,180,148
Rb 52,16,51200
Mv 200,176,148
buffer_load_dwordx4 v13, s[8:11], s39 offen offset:1024 lds
Mv 140,172,148
Rb 56,16,57344
Mv 136,168,148
Mv 76,164,148
Rb 60,16,59392
Mv 72,160,148
Mv 80,160,152
Mv 84,164,152
buffer_load_dwordx4 v14, s[8:11], s39 offen offset:2048 lds
Mv 144,168,152
Mv 148,172,152
Mv 208,176,152
Mv 212,180,152
Ma 208,184,152
Ma 212,188,152
buffer_load_dwordx4 v15, s[8:11], s39 offen offset:3072 lds
Ma 220,188,156
Ma 216,184,156
Mv 220,180,156
Mv 216,176,156
buffer_store_dwordx4 a[92:95], v3, s[20:23], s51 offen offset:3072 sc0 sc1
Mv 156,172,156
Mv 152,168,156
Mv 92,164,156
Mv 88,160,156
s_waitcnt vmcnt(25) lgkmcnt(0)
s_barrier
Mv 32,32,0
Rt 128,18,49152
s_mov_b64 s[16:17], s[76:77]
Mv 36,36,0
s_cmp_lt_u32 s33, 3
s_cselect_b32 s18, 0x188000, 0
Mv 96,40,0
Rt 130,18,51200
s_lshl_b32 s43, s2, 11
)" R"(
Mv 100,44,0
s_add_u32 s39, s39, 0x40000
s_sub_u32 s43, s43, 0x180000
Mv 160,48,0
Rt 132,19,49152
s_add_u32 m0, s37, 32768
Mv 164,52,0
buffer_load_dwordx4 v12, s[8:11], s39 offen offset:0 lds
Mv 224,56,0
Rt 134,19,51200
v_min_u32 v26, 15, v0
Mv 228,60,0
v_lshlrev_b32 v26, 7, v26
v_lshrrev_b32 v5, 2, v0
Mv 236,60,4
Rt 136,20,49152
v_and_b32 v5, 8, v5
Mv 232,56,4
s_add_u32 s51, s51, 0x10000
buffer_store_dwordx4 a[96:99], v2, s[20:23], s51 offen offset:0 sc0 sc1
Mv 172,52,4
Rt 138,20,51200
v_and_or_b32 v5, v0, 7, v5
Mv 168,48,4
v_lshlrev_b32 v5, 8, v5
v_and_or_b32 v5, v0, 24, v5
Mv 108,44,4
Rt 140,21,49152
v_lshrrev_b32 v5, 1, v5
Mv 104,40,4
buffer_load_dwordx4 v13, s[8:11], s39 offen offset:1024 lds
Mv 44,36,4
Rt 142,21,51200
Mv 40,32,4
Mv 48,32,8
Rt 144,22,49152
Mv 52,36,8
buffer_store_dwordx4 a[100:103], v2, s[20:23], s51 offen offset:1024 sc0 sc1
Mv 112,40,8
Rt 146,22,51200
Mv 116,44,8
Mv 176,48,8
Rt 148,23,49152
Mv 180,52,8
buffer_load_dwordx4 v14, s[8:11], s39 offen offset:2048 lds
Mv 240,56,8
Rt 150,23,51200
Mv 244,60,8
Mv 252,60,12
Rt 152,24,49152
Mv 248,56,12
buffer_store_dwordx4 a[104:107], v2, s[20:23], s51 offen offset:2048 sc0 sc1
Mv 188,52,12
Rt 154,24,51200
Mv 184,48,12
Mv 124,44,12
Rt 156,25,49152
Mv 120,40,12
buffer_load_dwordx4 v15, s[8:11], s39 offen offset:3072 lds
Mv 60,36,12
Rt 158,25,51200
Mv 56,32,12
Mv 64,32,16
Rb 160,17,32768
)" R"(
Mv 68,36,16
buffer_store_dwordx4 a[108:111], v2, s[20:23], s51 offen offset:3072 sc0 sc1
Mv 128,40,16
Rb 164,17,34816
Mv 132,44,16
Mv 192,48,16
Rb 168,17,40960
Mv 196,52,16
Ma 192,56,16
Rb 172,17,43008
Ma 196,60,16
Ma 204,60,20
Rb 176,17,49152
Ma 200,56,20
buffer_store_dwordx4 a[112:115], v3, s[20:23], s51 offen offset:0 sc0 sc1
Mv 204,52,20
Rb 180,17,51200
Mv 200,48,20
Mv 140,44,20
Rb 184,17,57344
Mv 136,40,20
Mv 76,36,20
Rb 188,17,59392
Mv 72,32,20
Mv 80,32,24
Mv 84,36,24
buffer_store_dwordx4 a[116:119], v3, s[20:23], s51 offen offset:1024 sc0 sc1
Mv 144,40,24
Mv 148,44,24
Mv 208,48,24
Mv 212,52,24
Ma 208,56,24
Ma 212,60,24
Ma 220,60,28
Ma 216,56,28
buffer_store_dwordx4 a[120:123], v3, s[20:23], s51 offen offset:2048 sc0 sc1
Mv 220,52,28
Mv 216,48,28
Mv 156,44,28
Mv 152,40,28
Mv 92,36,28
Mv 88,32,28
s_waitcnt vmcnt(16) lgkmcnt(0)
s_barrier
Mv 32,160,128
Rt 0,18,0
v_accvgpr_write_b32 a224, -1
Mv 36,164,128
s_add_u32 s38, s38, 0xffff0080
v_accvgpr_write_b32 a225, -1
Mv 96,168,128
Rt 2,18,2048
s_add_u32 m0, s36, 32768
Mv 100,172,128
buffer_load_dwordx4 v8, s[4:7], s38 offen offset:0 lds
Mv 160,176,128
Rt 4,19,0
v_accvgpr_write_b32 a226, -1
Mv 164,180,128
v_accvgpr_write_b32 a227, -1
s_add_u32 s88, s73, 0x4000
Mv 224,184,128
Rt 6,19,2048
s_mov_b32 s40, 6
Mv 228,188,128
)" R"(
buffer_load_dwordx4 v9, s[4:7], s38 offen offset:1024 lds
Mv 236,188,132
Rt 8,20,0
Mv 232,184,132
Mv 172,180,132
Rt 10,20,2048
Mv 168,176,132
buffer_load_dwordx4 v10, s[4:7], s38 offen offset:2048 lds
Mv 108,172,132
Rt 12,21,0
Mv 104,168,132
Mv 44,164,132
Rt 14,21,2048
Mv 40,160,132
buffer_load_dwordx4 v11, s[4:7], s38 offen offset:3072 lds
Mv 48,160,136
Rt 16,22,0
Mv 52,164,136
s_add_u32 s38, s38, 0x10000
Mv 112,168,136
Rt 18,22,2048
s_add_u32 m0, s36, 36864
Mv 116,172,136
buffer_load_dwordx4 v8, s[4:7], s38 offen offset:0 lds
Mv 176,176,136
Rt 20,23,0
Mv 180,180,136
Mv 240,184,136
Rt 22,23,2048
Mv 244,188,136
buffer_load_dwordx4 v9, s[4:7], s38 offen offset:1024 lds
Mv 252,188,140
Rt 24,24,0
Mv 248,184,140
Mv 188,180,140
Rt 26,24,2048
Mv 184,176,140
buffer_load_dwordx4 v10, s[4:7], s38 offen offset:2048 lds
Mv 124,172,140
Rt 28,25,0
Mv 120,168,140
Mv 60,164,140
Rt 30,25,2048
Mv 56,160,140
buffer_load_dwordx4 v11, s[4:7], s38 offen offset:3072 lds
Mv 64,160,144
Rb 32,16,0
Mv 68,164,144
Mv 128,168,144
Rb 36,16,2048
Mv 132,172,144
s_add_u32 s39, s39, 0x40000
Mv 192,176,144
Rb 40,16,8192
s_add_u32 m0, s37, 49152
Mv 196,180,144
buffer_load_dwordx4 v12, s[8:11], s39 offen offset:0 lds
Ma 192,184,144
Rb 44,16,10240
Ma 196,188,144
Ma 204,188,148
Rb 48,16,16384
Ma 200,184,148
Mv 204,180,148
)" R"(
Rb 52,16,18432
Mv 200,176,148
buffer_load_dwordx4 v13, s[8:11], s39 offen offset:1024 lds
Mv 140,172,148
Rb 56,16,24576
Mv 136,168,148
Mv 76,164,148
Rb 60,16,26624
Mv 72,160,148
Mv 80,160,152
Mv 84,164,152
buffer_load_dwordx4 v14, s[8:11], s39 offen offset:2048 lds
Mv 144,168,152
Mv 148,172,152
Mv 208,176,152
Mv 212,180,152
Ma 208,184,152
Ma 212,188,152
buffer_load_dwordx4 v15, s[8:11], s39 offen offset:3072 lds
Ma 220,188,156
Ma 216,184,156
Mv 220,180,156
Mv 216,176,156
buffer_store_dwordx4 a[124:127], v3, s[20:23], s51 offen offset:3072 sc0 sc1
Mv 156,172,156
Mv 152,168,156
Mv 92,164,156
Mv 88,160,156
s_waitcnt vmcnt(25) lgkmcnt(0)
s_barrier
.LzPBV:
Mv 32,32,0
Rt 128,18,16384
Mv 36,36,0
Mv 96,40,0
Rt 130,18,18432
Mv 100,44,0
s_add_u32 s39, s39, 0x40000
Mv 160,48,0
Rt 132,19,16384
s_add_u32 m0, s37, 0
Mv 164,52,0
buffer_load_dwordx4 v12, s[8:11], s39 offen offset:0 lds
Mv 224,56,0
Rt 134,19,18432
Mv 228,60,0
Mv 236,60,4
Rt 136,20,16384
Mv 232,56,4
Mv 172,52,4
Rt 138,20,18432
Mv 168,48,4
Mv 108,44,4
Rt 140,21,16384
Mv 104,40,4
buffer_load_dwordx4 v13, s[8:11], s39 offen offset:1024 lds
Mv 44,36,4
Rt 142,21,18432
Mv 40,32,4
Mv 48,32,8
Rt 144,22,16384
Mv 52,36,8
Mv 112,40,8
Rt 146,22,18432
Mv 116,44,8
)" R"(
Mv 176,48,8
Rt 148,23,16384
Mv 180,52,8
buffer_load_dwordx4 v14, s[8:11], s39 offen offset:2048 lds
Mv 240,56,8
Rt 150,23,18432
Mv 244,60,8
Mv 252,60,12
Rt 152,24,16384
Mv 248,56,12
Mv 188,52,12
Rt 154,24,18432
Mv 184,48,12
Mv 124,44,12
Rt 156,25,16384
Mv 120,40,12
buffer_load_dwordx4 v15, s[8:11], s39 offen offset:3072 lds
Mv 60,36,12
Rt 158,25,18432
Mv 56,32,12
Mv 64,32,16
Rb 160,17,0
Mv 68,36,16
Mv 128,40,16
Rb 164,17,2048
Mv 132,44,16
Mv 192,48,16
Rb 168,17,8192
Mv 196,52,16
buffer_store_dwordx4 a[224:227], v1, s[20:23], s72 offen sc0 sc1
Ma 192,56,16
Rb 172,17,10240
s_add_u32 s72, s72, 1024
Ma 196,60,16
Ma 204,60,20
Rb 176,17,16384
Ma 200,56,20
Mv 204,52,20
Rb 180,17,18432
Mv 200,48,20
Mv 140,44,20
Rb 184,17,24576
Mv 136,40,20
Mv 76,36,20
Rb 188,17,26624
Mv 72,32,20
Mv 80,32,24
Mv 84,36,24
buffer_store_dwordx4 a[224:227], v1, s[20:23], s88 offen sc0 sc1
Mv 144,40,24
s_add_u32 s88, s88, 1024
Mv 148,44,24
Mv 208,48,24
Mv 212,52,24
Ma 208,56,24
Ma 212,60,24
Ma 220,60,28
Ma 216,56,28
Mv 220,52,28
Mv 216,48,28
Mv 156,44,28
Mv 152,40,28
Mv 92,36,28
Mv 88,32,28
)" R"(
s_waitcnt vmcnt(11) lgkmcnt(0)
s_barrier
Mv 32,160,128
Rt 0,18,32768
Mv 36,164,128
s_add_u32 s38, s38, 0xffff0080
Mv 96,168,128
Rt 2,18,34816
s_add_u32 m0, s36, 0
Mv 100,172,128
buffer_load_dwordx4 v8, s[4:7], s38 offen offset:0 lds
Mv 160,176,128
Rt 4,19,32768
Mv 164,180,128
Mv 224,184,128
Rt 6,19,34816
Mv 228,188,128
buffer_load_dwordx4 v9, s[4:7], s38 offen offset:1024 lds
Mv 236,188,132
Rt 8,20,32768
Mv 232,184,132
Mv 172,180,132
Rt 10,20,34816
Mv 168,176,132
buffer_load_dwordx4 v10, s[4:7], s38 offen offset:2048 lds
Mv 108,172,132
Rt 12,21,32768
Mv 104,168,132
Mv 44,164,132
Rt 14,21,34816
Mv 40,160,132
buffer_load_dwordx4 v11, s[4:7], s38 offen offset:3072 lds
Mv 48,160,136
Rt 16,22,32768
Mv 52,164,136
s_add_u32 s38, s38, 0x10000
Mv 112,168,136
Rt 18,22,34816
s_add_u32 m0, s36, 4096
Mv 116,172,136
buffer_load_dwordx4 v8, s[4:7], s38 offen offset:0 lds
Mv 176,176,136
Rt 20,23,32768
Mv 180,180,136
Mv 240,184,136
Rt 22,23,34816
Mv 244,188,136
buffer_load_dwordx4 v9, s[4:7], s38 offen offset:1024 lds
Mv 252,188,140
Rt 24,24,32768
Mv 248,184,140
Mv 188,180,140
Rt 26,24,34816
Mv 184,176,140
buffer_load_dwordx4 v10, s[4:7], s38 offen offset:2048 lds
Mv 124,172,140
Rt 28,25,32768
Mv 120,168,140
Mv 60,164,140
Rt 30,25,34816
Mv 56,160,140
buffer_load_dwordx4 v11, s[4:7], s38 offen offset:3072 lds
Mv 64,160,144
Rb 32,16,32768
Mv 68,164,144
)" R"(
Mv 128,168,144
Rb 36,16,34816
Mv 132,172,144
s_add_u32 s39, s39, 0x40000
Mv 192,176,144
Rb 40,16,40960
s_add_u32 m0, s37, 16384
Mv 196,180,144
buffer_load_dwordx4 v12, s[8:11], s39 offen offset:0 lds
Ma 192,184,144
Rb 44,16,43008
Ma 196,188,144
Ma 204,188,148
Rb 48,16,49152
Ma 200,184,148
Mv 204,180,148
Rb 52,16,51200
Mv 200,176,148
buffer_load_dwordx4 v13, s[8:11], s39 offen offset:1024 lds
Mv 140,172,148
Rb 56,16,57344
Mv 136,168,148
Mv 76,164,148
Rb 60,16,59392
Mv 72,160,148
Mv 80,160,152
Mv 84,164,152
buffer_load_dwordx4 v14, s[8:11], s39 offen offset:2048 lds
Mv 144,168,152
Mv 148,172,152
Mv 208,176,152
Mv 212,180,152
Ma 208,184,152
Ma 212,188,152
buffer_load_dwordx4 v15, s[8:11], s39 offen offset:3072 lds
Ma 220,188,156
Ma 216,184,156
s_add_u32 s43, s43, 0x40000
Mv 220,180,156
Mv 216,176,156
buffer_load_dword v27, v26, s[16:19], s43 offen
Mv 156,172,156
Mv 152,168,156
Mv 92,164,156
Mv 88,160,156
s_waitcnt vmcnt(19) lgkmcnt(0)
s_barrier
buffer_store_dword v6, v7, s[20:23], s52 offen offset:0 sc0 sc1
s_mov_b32 s52, 0x40000000
Mv 32,32,0
Rt 128,18,49152
Mv 36,36,0
Mv 96,40,0
Rt 130,18,51200
Mv 100,44,0
s_add_u32 s39, s39, 0x40000
Mv 160,48,0
Rt 132,19,49152
s_add_u32 m0, s37, 32768
Mv 164,52,0
buffer_load_dwordx4 v12, s[8:11], s39 offen offset:0 lds
Mv 224,56,0
Rt 134,19,51200
Mv 228,60,0
Mv 236,60,4
)" R"(
Rt 136,20,49152
Mv 232,56,4
Mv 172,52,4
Rt 138,20,51200
Mv 168,48,4
Mv 108,44,4
Rt 140,21,49152
Mv 104,40,4
buffer_load_dwordx4 v13, s[8:11], s39 offen offset:1024 lds
Mv 44,36,4
Rt 142,21,51200
Mv 40,32,4
Mv 48,32,8
Rt 144,22,49152
Mv 52,36,8
Mv 112,40,8
Rt 146,22,51200
Mv 116,44,8
Mv 176,48,8
Rt 148,23,49152
Mv 180,52,8
buffer_load_dwordx4 v14, s[8:11], s39 offen offset:2048 lds
Mv 240,56,8
Rt 150,23,51200
Mv 244,60,8
Mv 252,60,12
Rt 152,24,49152
Mv 248,56,12
Mv 188,52,12
Rt 154,24,51200
Mv 184,48,12
Mv 124,44,12
Rt 156,25,49152
Mv 120,40,12
buffer_load_dwordx4 v15, s[8:11], s39 offen offset:3072 lds
Mv 60,36,12
Rt 158,25,51200
Mv 56,32,12
Mv 64,32,16
Rb 160,17,32768
Mv 68,36,16
Mv 128,40,16
Rb 164,17,34816
Mv 132,44,16
Mv 192,48,16
Rb 168,17,40960
Mv 196,52,16
buffer_store_dwordx4 a[224:227], v1, s[20:23], s72 offen sc0 sc1
Ma 192,56,16
Rb 172,17,43008
s_add_u32 s72, s72, 1024
Ma 196,60,16
Ma 204,60,20
Rb 176,17,49152
Ma 200,56,20
Mv 204,52,20
Rb 180,17,51200
Mv 200,48,20
Mv 140,44,20
Rb 184,17,57344
Mv 136,40,20
Mv 76,36,20
Rb 188,17,59392
Mv 72,32,20
)" R"(
Mv 80,32,24
Mv 84,36,24
buffer_store_dwordx4 a[224:227], v1, s[20:23], s88 offen sc0 sc1
Mv 144,40,24
s_add_u32 s88, s88, 1024
Mv 148,44,24
Mv 208,48,24
Mv 212,52,24
Ma 208,56,24
Ma 212,60,24
Ma 220,60,28
Ma 216,56,28
Mv 220,52,28
Mv 216,48,28
Mv 156,44,28
Mv 152,40,28
Mv 92,36,28
s_cmp_eq_u32 s40, 3
s_cmov_b32 s72, 0x40000000
s_cmov_b32 s88, 0x40000000
Mv 88,32,28
s_waitcnt vmcnt(12) lgkmcnt(0)
s_barrier
Mv 32,160,128
Rt 0,18,0
Mv 36,164,128
s_add_u32 s38, s38, 0xffff0080
Mv 96,168,128
Rt 2,18,2048
s_add_u32 m0, s36, 32768
Mv 100,172,128
buffer_load_dwordx4 v8, s[4:7], s38 offen offset:0 lds
Mv 160,176,128
Rt 4,19,0
Mv 164,180,128
Mv 224,184,128
Rt 6,19,2048
Mv 228,188,128
buffer_load_dwordx4 v9, s[4:7], s38 offen offset:1024 lds
Mv 236,188,132
Rt 8,20,0
Mv 232,184,132
Mv 172,180,132
Rt 10,20,2048
Mv 168,176,132
buffer_load_dwordx4 v10, s[4:7], s38 offen offset:2048 lds
Mv 108,172,132
Rt 12,21,0
Mv 104,168,132
Mv 44,164,132
Rt 14,21,2048
Mv 40,160,132
buffer_load_dwordx4 v11, s[4:7], s38 offen offset:3072 lds
Mv 48,160,136
Rt 16,22,0
Mv 52,164,136
s_add_u32 s38, s38, 0x10000
Mv 112,168,136
Rt 18,22,2048
s_add_u32 m0, s36, 36864
Mv 116,172,136
buffer_load_dwordx4 v8, s[4:7], s38 offen offset:0 lds
Mv 176,176,136
Rt 20,23,0
Mv 180,180,136
)" R"(
Mv 240,184,136
Rt 22,23,2048
Mv 244,188,136
buffer_load_dwordx4 v9, s[4:7], s38 offen offset:1024 lds
Mv 252,188,140
Rt 24,24,0
Mv 248,184,140
Mv 188,180,140
Rt 26,24,2048
Mv 184,176,140
buffer_load_dwordx4 v10, s[4:7], s38 offen offset:2048 lds
Mv 124,172,140
Rt 28,25,0
Mv 120,168,140
Mv 60,164,140
Rt 30,25,2048
Mv 56,160,140
buffer_load_dwordx4 v11, s[4:7], s38 offen offset:3072 lds
Mv 64,160,144
Rb 32,16,0
Mv 68,164,144
Mv 128,168,144
Rb 36,16,2048
Mv 132,172,144
s_add_u32 s39, s39, 0x40000
Mv 192,176,144
Rb 40,16,8192
s_add_u32 m0, s37, 49152
Mv 196,180,144
buffer_load_dwordx4 v12, s[8:11], s39 offen offset:0 lds
Ma 192,184,144
Rb 44,16,10240
Ma 196,188,144
Ma 204,188,148
Rb 48,16,16384
Ma 200,184,148
Mv 204,180,148
Rb 52,16,18432
Mv 200,176,148
buffer_load_dwordx4 v13, s[8:11], s39 offen offset:1024 lds
Mv 140,172,148
Rb 56,16,24576
Mv 136,168,148
Mv 76,164,148
Rb 60,16,26624
Mv 72,160,148
Mv 80,160,152
Mv 84,164,152
buffer_load_dwordx4 v14, s[8:11], s39 offen offset:2048 lds
Mv 144,168,152
Mv 148,172,152
Mv 208,176,152
Mv 212,180,152
Ma 208,184,152
Ma 212,188,152
buffer_load_dwordx4 v15, s[8:11], s39 offen offset:3072 lds
Ma 220,188,156
Ma 216,184,156
s_add_u32 s43, s43, 0x40000
Mv 220,180,156
s_sub_u32 s40, s40, 1
Mv 216,176,156
buffer_load_dword v27, v26, s[16:19], s43 offen
Mv 156,172,156
)" R"(
Mv 152,168,156
Mv 92,164,156
Mv 88,160,156
s_cmp_lg_u32 s40, 0
s_waitcnt vmcnt(21) lgkmcnt(0)
s_barrier
s_cbranch_scc1 .LzPBV
s_branch .LzPBVX
.LzWIN:
.LzPAKX:
Mv 32,0,32
Rt 64,18,16384
Mv 64,0,36
Rb 192,28,32768
Mv 96,0,40
Rt 66,18,18432
Mv 128,0,44
s_add_u32 s39, s39, 0x40000
Rb 196,28,34816
Mv 160,0,48
Rt 68,19,16384
s_add_u32 m0, s37, 0
Mv 192,0,52
buffer_load_dwordx4 v12, s[8:11], s39 offen offset:0 lds
Mv 224,0,56
Rt 70,19,18432
Ma 224,0,60
Rb 200,29,32768
Ma 228,4,60
Rt 72,20,16384
Mv 228,4,56
Rb 204,29,34816
Mv 196,4,52
Rt 74,20,18432
Mv 164,4,48
Rb 208,28,36864
Mv 132,4,44
Rt 76,21,16384
Mv 100,4,40
buffer_load_dwordx4 v13, s[8:11], s39 offen offset:1024 lds
Mv 68,4,36
Rt 78,21,18432
Mv 36,4,32
Rb 212,28,38912
Mv 40,8,32
Rt 80,22,16384
Mv 72,8,36
Rb 216,29,36864
Mv 104,8,40
Rt 82,22,18432
Mv 136,8,44
Rb 220,29,38912
Mv 168,8,48
Rt 84,23,16384
Mv 200,8,52
buffer_load_dwordx4 v14, s[8:11], s39 offen offset:2048 lds
Mv 232,8,56
Rt 86,23,18432
Ma 232,8,60
Ma 236,12,60
Rt 88,24,16384
Mv 236,12,56
Mv 204,12,52
Rt 90,24,18432
Mv 172,12,48
)" R"(
Mv 140,12,44
Rt 92,25,16384
Mv 108,12,40
buffer_load_dwordx4 v15, s[8:11], s39 offen offset:3072 lds
Mv 76,12,36
Rt 94,25,18432
Mv 44,12,32
s_add_u32 s44, s43, 256
s_waitcnt lgkmcnt(6)
Mv 48,16,32
Rb 96,17,0
s_add_u32 m0, s36, 131072
Mv 80,16,36
buffer_load_dwordx4 v26, s[12:15], s44 offen offset:0 lds
Mv 112,16,40
Rb 100,17,2048
Mv 144,16,44
Mv 176,16,48
Rb 104,17,8192
Mv 208,16,52
buffer_load_dwordx4 v27, s[12:15], s44 offen offset:1024 lds
Mv 240,16,56
Rb 108,17,10240
Ma 240,16,60
Ma 244,20,60
Rb 112,17,16384
Mv 244,20,56
s_add_u32 s44, s43, 12544
buffer_load_dwordx4 v26, s[12:15], s44 offen offset:2048 lds
Mv 212,20,52
Rb 116,17,18432
Mv 180,20,48
Mv 148,20,44
Rb 120,17,24576
Mv 116,20,40
buffer_load_dwordx4 v27, s[12:15], s44 offen offset:3072 lds
Mv 84,20,36
Rb 124,17,26624
Mv 52,20,32
Mv 56,24,32
s_add_u32 m0, s36, 135168
s_add_u32 s44, s43, 384
Mv 88,24,36
buffer_load_dwordx4 v26, s[12:15], s44 offen offset:0 lds
Mv 120,24,40
Mv 152,24,44
Mv 184,24,48
Mv 216,24,52
buffer_load_dwordx4 v27, s[12:15], s44 offen offset:1024 lds
Mv 248,24,56
Ma 248,24,60
Ma 252,28,60
Mv 252,28,56
s_add_u32 s44, s43, 12672
buffer_load_dwordx4 v26, s[12:15], s44 offen offset:2048 lds
Mv 220,28,52
Mv 188,28,48
Mv 156,28,44
s_mov_b32 s85, 0xc108800
v_and_b32 v2, 7, v0
v_lshlrev_b32 v2, 7, v2
Mv 124,28,40
buffer_load_dwordx4 v27, s[12:15], s44 offen offset:3072 lds
Mv 92,28,36
)" R"(
buffer_load_dword v4, v2, s[20:23], s85 offen sc0 sc1
Mv 60,28,32
s_waitcnt vmcnt(18) lgkmcnt(0)
s_barrier
Mv 32,64,96
Rt 0,18,32768
Mv 64,64,100
s_add_u32 s38, s38, 0xffff0080
Mv 96,64,104
Rt 2,18,34816
s_add_u32 m0, s36, 0
Mv 128,64,108
buffer_load_dwordx4 v8, s[4:7], s38 offen offset:0 lds
Mv 160,64,112
Rt 4,19,32768
Mv 192,64,116
Mv 224,64,120
Rt 6,19,34816
Ma 224,64,124
buffer_load_dwordx4 v9, s[4:7], s38 offen offset:1024 lds
Ma 228,68,124
Rt 8,20,32768
Mv 228,68,120
Mv 196,68,116
Rt 10,20,34816
Mv 164,68,112
buffer_load_dwordx4 v10, s[4:7], s38 offen offset:2048 lds
Mv 132,68,108
Rt 12,21,32768
Mv 100,68,104
Mv 68,68,100
Rt 14,21,34816
Mv 36,68,96
buffer_load_dwordx4 v11, s[4:7], s38 offen offset:3072 lds
Mv 40,72,96
Rt 16,22,32768
Mv 72,72,100
s_add_u32 s38, s38, 0x10000
Mv 104,72,104
Rt 18,22,34816
s_add_u32 m0, s36, 4096
Mv 136,72,108
buffer_load_dwordx4 v8, s[4:7], s38 offen offset:0 lds
Mv 168,72,112
Rt 20,23,32768
Mv 200,72,116
Mv 232,72,120
Rt 22,23,34816
Ma 232,72,124
buffer_load_dwordx4 v9, s[4:7], s38 offen offset:1024 lds
Ma 236,76,124
Rt 24,24,32768
Mv 236,76,120
Mv 204,76,116
Rt 26,24,34816
Mv 172,76,112
buffer_load_dwordx4 v10, s[4:7], s38 offen offset:2048 lds
Mv 140,76,108
Rt 28,25,32768
Mv 108,76,104
Mv 76,76,100
Rt 30,25,34816
Mv 44,76,96
buffer_load_dwordx4 v11, s[4:7], s38 offen offset:3072 lds
Mv 48,80,96
)" R"(
Rb 32,16,32768
Mv 80,80,100
Mv 112,80,104
Rb 36,16,34816
Mv 144,80,108
s_add_u32 s39, s39, 0x40000
Mv 176,80,112
Rb 40,16,40960
s_add_u32 m0, s37, 16384
Mv 208,80,116
buffer_load_dwordx4 v12, s[8:11], s39 offen offset:0 lds
Mv 240,80,120
Rb 44,16,43008
Ma 240,80,124
Ma 244,84,124
Rb 48,16,49152
Mv 244,84,120
Mv 212,84,116
Rb 52,16,51200
Mv 180,84,112
buffer_load_dwordx4 v13, s[8:11], s39 offen offset:1024 lds
Mv 148,84,108
Rb 56,16,57344
Mv 116,84,104
Mv 84,84,100
Rb 60,16,59392
Mv 52,84,96
Mv 56,88,96
Mv 88,88,100
buffer_load_dwordx4 v14, s[8:11], s39 offen offset:2048 lds
Mv 120,88,104
Mv 152,88,108
Mv 184,88,112
Mv 216,88,116
Mv 248,88,120
Ma 248,88,124
buffer_load_dwordx4 v15, s[8:11], s39 offen offset:3072 lds
Ma 252,92,124
Mv 252,92,120
Mv 220,92,116
Mv 188,92,112
Mv 156,92,108
Mv 124,92,104
Mv 92,92,100
Mv 60,92,96
s_waitcnt vmcnt(13) lgkmcnt(0)
s_barrier
Mv 32,0,32
Rt 64,18,49152
Mv 64,0,36
Rb 128,28,32768
Mv 96,0,40
Rt 66,18,51200
Mv 128,0,44
s_add_u32 s39, s39, 0x40000
Rb 132,28,34816
Mv 160,0,48
Rt 68,19,49152
s_add_u32 m0, s37, 32768
Mv 192,0,52
buffer_load_dwordx4 v12, s[8:11], s39 offen offset:0 lds
Mv 224,0,56
Rt 70,19,51200
Ma 224,0,60
Rb 136,29,32768
)" R"(
Ma 228,4,60
Rt 72,20,49152
Mv 228,4,56
Rb 140,29,34816
Mv 196,4,52
Rt 74,20,51200
Mv 164,4,48
Rb 144,28,36864
Mv 132,4,44
Rt 76,21,49152
Mv 100,4,40
buffer_load_dwordx4 v13, s[8:11], s39 offen offset:1024 lds
Mv 68,4,36
Rt 78,21,51200
Mv 36,4,32
Rb 148,28,38912
Mv 40,8,32
Rt 80,22,49152
Mv 72,8,36
Rb 152,29,36864
Mv 104,8,40
Rt 82,22,51200
Mv 136,8,44
Rb 156,29,38912
Mv 168,8,48
Rt 84,23,49152
Mv 200,8,52
buffer_load_dwordx4 v14, s[8:11], s39 offen offset:2048 lds
Mv 232,8,56
Rt 86,23,51200
Ma 232,8,60
Ma 236,12,60
Rt 88,24,49152
Mv 236,12,56
Mv 204,12,52
Rt 90,24,51200
Mv 172,12,48
Mv 140,12,44
Rt 92,25,49152
Mv 108,12,40
buffer_load_dwordx4 v15, s[8:11], s39 offen offset:3072 lds
Mv 76,12,36
Rt 94,25,51200
Mv 44,12,32
s_add_u32 s44, s43, 512
s_waitcnt lgkmcnt(6)
Mv 48,16,32
Rb 96,17,32768
s_add_u32 m0, s36, 131072
Mv 80,16,36
buffer_load_dwordx4 v26, s[12:15], s44 offen offset:0 lds
Mv 112,16,40
Rb 100,17,34816
Mv 144,16,44
Mv 176,16,48
Rb 104,17,40960
Mv 208,16,52
buffer_load_dwordx4 v27, s[12:15], s44 offen offset:1024 lds
Mv 240,16,56
Rb 108,17,43008
Ma 240,16,60
Ma 244,20,60
Rb 112,17,49152
Mv 244,20,56
)" R"(
s_add_u32 s44, s43, 12800
buffer_load_dwordx4 v26, s[12:15], s44 offen offset:2048 lds
Mv 212,20,52
Rb 116,17,51200
Mv 180,20,48
Mv 148,20,44
Rb 120,17,57344
Mv 116,20,40
buffer_load_dwordx4 v27, s[12:15], s44 offen offset:3072 lds
Mv 84,20,36
Rb 124,17,59392
Mv 52,20,32
Mv 56,24,32
s_add_u32 m0, s36, 135168
s_add_u32 s44, s43, 640
Mv 88,24,36
buffer_load_dwordx4 v26, s[12:15], s44 offen offset:0 lds
Mv 120,24,40
Mv 152,24,44
Mv 184,24,48
Mv 216,24,52
buffer_load_dwordx4 v27, s[12:15], s44 offen offset:1024 lds
Mv 248,24,56
Ma 248,24,60
Ma 252,28,60
Mv 252,28,56
s_add_u32 s44, s43, 12928
buffer_load_dwordx4 v26, s[12:15], s44 offen offset:2048 lds
Mv 220,28,52
Mv 188,28,48
Mv 156,28,44
Mv 124,28,40
buffer_load_dwordx4 v27, s[12:15], s44 offen offset:3072 lds
Mv 92,28,36
Mv 60,28,32
s_waitcnt vmcnt(16) lgkmcnt(0)
s_barrier
Mv 32,64,96
Rt 0,18,0
v_xor_b32 v4, s3, v4
v_cmp_gt_u32_e32 vcc, 2, v4
s_andn2_b32 s28, 0xff, vcc_lo
s_cbranch_scc1 .Lzsp1
.Lzok1:
Mv 64,64,100
s_add_u32 s38, s38, 0xffff0080
v_xor_b32 v4, s3, v4
Mv 96,64,104
Rt 2,18,2048
s_add_u32 m0, s36, 32768
Mv 128,64,108
buffer_load_dwordx4 v8, s[4:7], s38 offen offset:0 lds
Mv 160,64,112
Rt 4,19,0
v_and_b32 v4, 1, v4
Mv 192,64,116
v_cmp_eq_u32_e32 vcc, 1, v4
s_mov_b32 s85, vcc_lo
Mv 224,64,120
Rt 6,19,2048
s_and_b32 s28, s2, 7
Ma 224,64,124
buffer_load_dwordx4 v9, s[4:7], s38 offen offset:1024 lds
Ma 228,68,124
Rt 8,20,0
)" R"(
s_lshr_b32 s28, s85, s28
Mv 228,68,120
s_xor_b32 s28, s28, s34
s_and_b32 s28, s28, 1
Mv 196,68,116
Rt 10,20,2048
s_lshl_b32 s84, s28, 13
Mv 164,68,112
buffer_load_dwordx4 v10, s[4:7], s38 offen offset:2048 lds
Mv 132,68,108
Rt 12,21,0
s_lshl1_add_u32 s28, s36, s84
Mv 100,68,104
v_add_u32 v2, s28, v1
v_add_u32 v3, 0x1000, v2
Mv 68,68,100
Rt 14,21,2048
s_lshl_b32 s28, s34, 13
Mv 36,68,96
buffer_load_dwordx4 v11, s[4:7], s38 offen offset:3072 lds
Mv 40,72,96
Rt 16,22,0
s_xor_b32 s28, s28, s84
Mv 72,72,100
s_add_u32 s38, s38, 0x10000
s_and_b32 s28, s28, 0x2000
Mv 104,72,104
Rt 18,22,2048
s_add_u32 m0, s36, 36864
Mv 136,72,108
buffer_load_dwordx4 v8, s[4:7], s38 offen offset:0 lds
Mv 168,72,112
Rt 20,23,0
s_xor_b32 s73, s73, s28
Mv 200,72,116
Mv 232,72,120
Rt 22,23,2048
Ma 232,72,124
buffer_load_dwordx4 v9, s[4:7], s38 offen offset:1024 lds
Ma 236,76,124
Rt 24,24,0
Mv 236,76,120
Mv 204,76,116
Rt 26,24,2048
Mv 172,76,112
buffer_load_dwordx4 v10, s[4:7], s38 offen offset:2048 lds
Mv 140,76,108
Rt 28,25,0
Mv 108,76,104
Mv 76,76,100
Rt 30,25,2048
Mv 44,76,96
buffer_load_dwordx4 v11, s[4:7], s38 offen offset:3072 lds
Mv 48,80,96
Rb 32,16,0
Mv 80,80,100
Mv 112,80,104
Rb 36,16,2048
Mv 144,80,108
s_add_u32 s39, s39, 0x40000
Mv 176,80,112
Rb 40,16,8192
s_add_u32 m0, s37, 49152
Mv 208,80,116
)" R"(
buffer_load_dwordx4 v12, s[8:11], s39 offen offset:0 lds
Mv 240,80,120
Rb 44,16,10240
Ma 240,80,124
Ma 244,84,124
Rb 48,16,16384
Mv 244,84,120
Mv 212,84,116
Rb 52,16,18432
Mv 180,84,112
buffer_load_dwordx4 v13, s[8:11], s39 offen offset:1024 lds
Mv 148,84,108
Rb 56,16,24576
Mv 116,84,104
Mv 84,84,100
Rb 60,16,26624
Mv 52,84,96
Mv 56,88,96
Mv 88,88,100
buffer_load_dwordx4 v14, s[8:11], s39 offen offset:2048 lds
Mv 120,88,104
Mv 152,88,108
Mv 184,88,112
Mv 216,88,116
Mv 248,88,120
Ma 248,88,124
buffer_load_dwordx4 v15, s[8:11], s39 offen offset:3072 lds
Ma 252,92,124
Mv 252,92,120
Mv 220,92,116
Mv 188,92,112
Mv 156,92,108
Mv 124,92,104
Mv 92,92,100
Mv 60,92,96
s_waitcnt vmcnt(12) lgkmcnt(0)
s_barrier
Mv 32,0,32
Rt 64,18,16384
Mv 64,0,36
Rb 160,28,32768
v_lshrrev_b32 v5, 2, v0
Mv 96,0,40
Rt 66,18,18432
v_and_b32 v5, 8, v5
Mv 128,0,44
Rb 164,28,34816
v_and_or_b32 v5, v0, 7, v5
Mv 160,0,48
Rt 68,19,16384
v_lshlrev_b32 v5, 8, v5
Mv 192,0,52
v_and_or_b32 v5, v0, 24, v5
v_lshrrev_b32 v5, 1, v5
Mv 224,0,56
Rt 70,19,18432
v_mov_b32 v6, s3
Ma 224,0,60
Rb 168,29,32768
Ma 228,4,60
Rt 72,20,16384
Mv 228,4,56
Rb 172,29,34816
Mv 196,4,52
Rt 74,20,18432
)" R"(
Mv 164,4,48
Rb 176,28,36864
Mv 132,4,44
Rt 76,21,16384
Mv 100,4,40
Mv 68,4,36
Rt 78,21,18432
Mv 36,4,32
Rb 180,28,38912
Mv 40,8,32
Rt 80,22,16384
Mv 72,8,36
Rb 184,29,36864
Mv 104,8,40
Rt 82,22,18432
Mv 136,8,44
Rb 188,29,38912
Mv 168,8,48
Rt 84,23,16384
Mv 200,8,52
Mv 232,8,56
Rt 86,23,18432
Ma 232,8,60
Ma 236,12,60
Rt 88,24,16384
Mv 236,12,56
Mv 204,12,52
Rt 90,24,18432
Mv 172,12,48
Mv 140,12,44
Rt 92,25,16384
Mv 108,12,40
Mv 76,12,36
Rt 94,25,18432
v_lshrrev_b32 v10, 3, v0
Mv 44,12,32
v_and_b32 v11, 0x70, v1
v_lshl_or_b32 v10, v10, 13, v11
Mv 48,16,32
Rb 96,17,0
v_lshlrev_b32 v11, 1, v0
Mv 80,16,36
v_and_b32 v11, 32, v11
v_xor_b32 v10, v10, v11
buffer_load_dword v4, v7, s[20:23], s52 offen offset:0 sc0 sc1
Mv 112,16,40
Rb 100,17,2048
s_and_b32 s28, s33, 1
Mv 144,16,44
s_lshl_b32 s28, s28, 6
v_xor_b32 v8, s28, v10
Mv 176,16,48
Rb 104,17,8192
v_xor_b32 v9, 64, v8
Mv 208,16,52
v_add_u32 v9, 0xfc00, v9
v_add_u32 v10, 0x1f800, v8
Mv 240,16,56
Rb 108,17,10240
v_add_u32 v11, 0x1f800, v9
Ma 240,16,60
Ma 244,20,60
Rb 112,17,16384
Mv 244,20,56
)" R"(
Mv 212,20,52
Rb 116,17,18432
Mv 180,20,48
Mv 148,20,44
Rb 120,17,24576
Mv 116,20,40
Mv 84,20,36
Rb 124,17,26624
Mv 52,20,32
Mv 56,24,32
Mv 88,24,36
Mv 120,24,40
Mv 152,24,44
Mv 184,24,48
Mv 216,24,52
Mv 248,24,56
Ma 248,24,60
Ma 252,28,60
Mv 252,28,56
Mv 220,28,52
Mv 188,28,48
Mv 156,28,44
Mv 124,28,40
Mv 92,28,36
Mv 60,28,32
s_waitcnt vmcnt(5) lgkmcnt(0)
s_barrier
Mv 32,64,96
Rt 0,18,32768
Mv 64,64,100
s_add_u32 s92, s42, 0x0
Mv 96,64,104
Rt 2,18,34816
s_add_u32 m0, s41, 131072
Mv 128,64,108
buffer_load_dwordx4 v8, s[16:19], s92 offen offset:0 lds
Mv 160,64,112
Rt 4,19,32768
Mv 192,64,116
Mv 224,64,120
Rt 6,19,34816
Ma 224,64,124
buffer_load_dwordx4 v9, s[16:19], s92 offen offset:1024 lds
Ma 228,68,124
Rt 8,20,32768
Mv 228,68,120
Mv 196,68,116
Rt 10,20,34816
Mv 164,68,112
buffer_load_dwordx4 v10, s[16:19], s92 offen offset:2048 lds
Mv 132,68,108
Rt 12,21,32768
Mv 100,68,104
Mv 68,68,100
Rt 14,21,34816
Mv 36,68,96
buffer_load_dwordx4 v11, s[16:19], s92 offen offset:3072 lds
Mv 40,72,96
Rt 16,22,32768
Mv 72,72,100
s_add_u32 s92, s42, 0x40000
Mv 104,72,104
Rt 18,22,34816
s_add_u32 m0, s41, 135168
Mv 136,72,108
)" R"(
buffer_load_dwordx4 v8, s[16:19], s92 offen offset:0 lds
Mv 168,72,112
Rt 20,23,32768
Mv 200,72,116
Mv 232,72,120
Rt 22,23,34816
Ma 232,72,124
buffer_load_dwordx4 v9, s[16:19], s92 offen offset:1024 lds
Ma 236,76,124
Rt 24,24,32768
Mv 236,76,120
Mv 204,76,116
Rt 26,24,34816
Mv 172,76,112
buffer_load_dwordx4 v10, s[16:19], s92 offen offset:2048 lds
Mv 140,76,108
Rt 28,25,32768
Mv 108,76,104
s_add_u32 s92, s42, 0x1c0000
Mv 76,76,100
Rt 30,25,34816
s_add_u32 m0, s41, 0
Mv 44,76,96
buffer_load_dwordx4 v8, s[16:19], s92 offen offset:0 lds
Mv 48,80,96
Rb 32,16,32768
Mv 80,80,100
Mv 112,80,104
Rb 36,16,34816
Mv 144,80,108
buffer_load_dwordx4 v9, s[16:19], s92 offen offset:1024 lds
Mv 176,80,112
Rb 40,16,40960
Mv 208,80,116
Mv 240,80,120
Rb 44,16,43008
Ma 240,80,124
buffer_load_dwordx4 v10, s[16:19], s92 offen offset:2048 lds
Ma 244,84,124
Rb 48,16,49152
Mv 244,84,120
Mv 212,84,116
Rb 52,16,51200
Mv 180,84,112
buffer_load_dwordx4 v11, s[16:19], s92 offen offset:3072 lds
Mv 148,84,108
Rb 56,16,57344
Mv 116,84,104
s_add_u32 s92, s42, 0x200000
Mv 84,84,100
Rb 60,16,59392
s_add_u32 m0, s41, 4096
Mv 52,84,96
buffer_load_dwordx4 v8, s[16:19], s92 offen offset:0 lds
Mv 56,88,96
Mv 88,88,100
Mv 120,88,104
Mv 152,88,108
buffer_load_dwordx4 v9, s[16:19], s92 offen offset:1024 lds
Mv 184,88,112
Mv 216,88,116
Mv 248,88,120
Ma 248,88,124
buffer_load_dwordx4 v10, s[16:19], s92 offen offset:2048 lds
)" R"(
Ma 252,92,124
Mv 252,92,120
Mv 220,92,116
Mv 188,92,112
Mv 156,92,108
Mv 124,92,104
Mv 92,92,100
Mv 60,92,96
s_waitcnt vmcnt(14) lgkmcnt(4)
s_barrier
v_cmp_eq_u32_e32 vcc, s3, v4
s_andn2_b64 s[28:29], 1, vcc
s_cbranch_scc1 .Lzsp2
.Lzok2:
Mv 32,0,32
Rt 64,18,49152
Mv 64,0,36
Rt 66,18,51200
s_add_u32 s92, s42, 0x80
Mv 96,0,40
Rt 68,19,49152
s_add_u32 m0, s41, 65536
Mv 128,0,44
Rt 70,19,51200
buffer_load_dwordx4 v8, s[16:19], s92 offen offset:0 lds
Mv 132,4,44
Rt 72,20,49152
Mv 100,4,40
Rt 74,20,51200
Mv 68,4,36
Rt 76,21,49152
Mv 36,4,32
Rt 78,21,51200
buffer_load_dwordx4 v9, s[16:19], s92 offen offset:1024 lds
Mv 40,8,32
Rt 80,22,49152
Mv 72,8,36
Rt 82,22,51200
Mv 104,8,40
Rt 84,23,49152
Mv 136,8,44
Rt 86,23,51200
buffer_load_dwordx4 v10, s[16:19], s92 offen offset:2048 lds
Mv 140,12,44
Rt 88,24,49152
Mv 108,12,40
Rt 90,24,51200
Mv 76,12,36
Rt 92,25,49152
Mv 44,12,32
Rt 94,25,51200
buffer_load_dwordx4 v11, s[16:19], s92 offen offset:3072 lds
Mv 48,16,32
Rb 96,17,32768
Mv 80,16,36
Rb 100,17,34816
s_add_u32 s92, s42, 0x40080
Mv 112,16,40
Rb 104,17,40960
s_add_u32 m0, s41, 69632
Mv 144,16,44
Rb 108,17,43008
buffer_load_dwordx4 v8, s[16:19], s92 offen offset:0 lds
Mv 148,20,44
Rb 112,17,49152
)" R"(
Mv 116,20,40
Rb 116,17,51200
Mv 84,20,36
Rb 120,17,57344
Mv 52,20,32
Rb 124,17,59392
buffer_load_dwordx4 v9, s[16:19], s92 offen offset:1024 lds
Mv 56,24,32
Mv 88,24,36
Mv 120,24,40
Mv 152,24,44
buffer_load_dwordx4 v10, s[16:19], s92 offen offset:2048 lds
Mv 156,28,44
Mv 124,28,40
Mv 92,28,36
Mv 60,28,32
s_waitcnt lgkmcnt(0)
s_barrier
Mv 32,64,96
v_bfe_u32 v12, v0, 3, 2
v_lshlrev_b32 v12, 5, v12
Mv 36,68,96
v_and_b32 v4, 3, v0
v_lshl_or_b32 v12, v4, 3, v12
Mv 40,72,96
s_add_u32 m0, s36, 98304
s_add_u32 s44, s43, 768
Mv 44,76,96
buffer_load_dwordx4 v26, s[12:15], s44 offen offset:0 lds
Mv 48,80,96
v_bfe_u32 v4, v0, 2, 2
v_lshl_or_b32 v12, v4, 7, v12
Mv 52,84,96
v_lshrrev_b32 v4, 4, v0
v_lshl_or_b32 v12, v4, 10, v12
Mv 56,88,96
v_xor_b32 v13, 32, v12
v_xor_b32 v14, 64, v12
Mv 60,92,96
buffer_load_dwordx4 v27, s[12:15], s44 offen offset:1024 lds
Mv 64,64,100
v_xor_b32 v15, 96, v12
v_add_u32 v20, 0x20000, v12
Mv 68,68,100
v_add_u32 v21, 0x20000, v13
v_add_u32 v22, 0x20000, v14
Mv 72,72,100
v_add_u32 v23, 0x20000, v15
v_add_u32 v16, 0x10000, v12
Mv 76,76,100
s_add_u32 s44, s43, 13056
buffer_load_dwordx4 v26, s[12:15], s44 offen offset:2048 lds
Mv 80,80,100
v_add_u32 v17, 0x10000, v13
v_add_u32 v18, 0x10000, v14
Mv 84,84,100
v_add_u32 v19, 0x10000, v15
Cv 32,32,33
Mv 88,88,100
Cv 33,34,35
Cv 34,36,37
Mv 92,92,100
buffer_load_dwordx4 v27, s[12:15], s44 offen offset:3072 lds
Mv 96,64,104
Cv 35,38,39
)" R"(
Cv 40,40,41
Mv 100,68,104
Cv 41,42,43
Cv 42,44,45
Mv 104,72,104
s_add_u32 m0, s41, 32768
s_add_u32 s92, s42, 0x1c0080
Mv 108,76,104
buffer_load_dwordx4 v8, s[16:19], s92 offen offset:0 lds
Mv 112,80,104
Cv 43,46,47
Cv 48,48,49
Mv 116,84,104
Cv 49,50,51
Cv 50,52,53
Mv 120,88,104
Cv 51,54,55
Cv 56,56,57
Mv 124,92,104
buffer_store_dwordx4 v[32:35], v2, s[20:23], s45 offen offset:0 sc0 sc1
Mv 128,64,108
Cv 57,58,59
Cv 58,60,61
Mv 132,68,108
Cv 59,62,63
Cv 64,64,65
Mv 136,72,108
Cv 65,66,67
Cv 66,68,69
Mv 140,76,108
buffer_load_dwordx4 v9, s[16:19], s92 offen offset:1024 lds
Mv 144,80,108
Cv 67,70,71
Cv 72,72,73
Mv 148,84,108
Cv 73,74,75
Cv 74,76,77
Mv 152,88,108
Cv 75,78,79
Cv 80,80,81
Mv 156,92,108
buffer_store_dwordx4 v[40:43], v2, s[20:23], s45 offen offset:1024 sc0 sc1
Mv 160,0,48
Cv 81,82,83
Cv 82,84,85
Mv 160,64,112
Cv 83,86,87
Cv 88,88,89
Mv 164,4,48
Cv 89,90,91
Cv 90,92,93
Mv 164,68,112
buffer_load_dwordx4 v10, s[16:19], s92 offen offset:2048 lds
Mv 168,8,48
Cv 91,94,95
Cv 96,96,97
Mv 168,72,112
Cv 97,98,99
Cv 98,100,101
Mv 172,12,48
Cv 99,102,103
Cv 104,104,105
Mv 172,76,112
buffer_store_dwordx4 v[48:51], v2, s[20:23], s45 offen offset:2048 sc0 sc1
)" R"(
Mv 176,16,48
Cv 105,106,107
Cv 106,108,109
Mv 176,80,112
Cv 107,110,111
Cv 112,112,113
Mv 180,20,48
Cv 113,114,115
Cv 114,116,117
Mv 180,84,112
buffer_load_dwordx4 v11, s[16:19], s92 offen offset:3072 lds
Mv 184,24,48
Cv 115,118,119
Cv 120,120,121
Mv 184,88,112
Cv 121,122,123
Cv 122,124,125
Mv 188,28,48
Cv 123,126,127
Cv 128,128,129
Mv 188,92,112
buffer_store_dwordx4 v[56:59], v2, s[20:23], s45 offen offset:3072 sc0 sc1
Mv 192,0,52
Cv 129,130,131
Cv 130,132,133
Mv 192,64,116
Cv 131,134,135
Cv 136,136,137
Mv 196,4,52
s_add_u32 m0, s41, 36864
s_add_u32 s92, s42, 0x200080
Mv 196,68,116
buffer_load_dwordx4 v8, s[16:19], s92 offen offset:0 lds
Mv 200,8,52
Cv 137,138,139
Cv 138,140,141
Mv 200,72,116
Cv 139,142,143
Cv 144,144,145
Mv 204,12,52
Cv 145,146,147
Cv 146,148,149
Mv 204,76,116
buffer_store_dwordx4 v[64:67], v3, s[20:23], s45 offen offset:0 sc0 sc1
Mv 208,16,52
Cv 147,150,151
Cv 152,152,153
Mv 208,80,116
Cv 153,154,155
Cv 154,156,157
Mv 212,20,52
Cv 155,158,159
Mv 212,84,116
buffer_load_dwordx4 v9, s[16:19], s92 offen offset:1024 lds
Mv 216,24,52
Mv 216,88,116
Mv 220,28,52
Mv 220,92,116
buffer_store_dwordx4 v[72:75], v3, s[20:23], s45 offen offset:1024 sc0 sc1
Mv 224,0,56
Ma 224,0,60
Ma 228,4,60
Mv 228,4,56
buffer_load_dwordx4 v10, s[16:19], s92 offen offset:2048 lds
)" R"(
Mv 232,8,56
Ma 232,8,60
Ma 236,12,60
Mv 236,12,56
s_waitcnt vmcnt(24)
s_barrier
Mv 240,16,56
buffer_store_dwordx4 v[80:83], v3, s[20:23], s45 offen offset:2048 sc0 sc1
Ma 240,16,60
Ma 244,20,60
Mv 244,20,56
Mv 248,24,56
Ma 248,24,60
Ma 252,28,60
Mv 252,28,56
buffer_store_dwordx4 v[88:91], v3, s[20:23], s45 offen offset:3072 sc0 sc1
Mv 224,64,120
Rt 16,20,0
Mv 228,68,120
Rt 18,20,512
Mv 232,72,120
Rt 20,21,0
Mv 236,76,120
Rt 22,21,512
Mv 240,80,120
Rt 24,22,0
Mv 244,84,120
Rt 26,22,512
Mv 248,88,120
Rt 28,23,0
Mv 252,92,120
Rt 30,23,512
Ma 224,64,124
Rt 32,20,4096
Ma 228,68,124
Rt 34,20,4608
Ma 232,72,124
Rt 36,21,4096
Ma 236,76,124
Rt 38,21,4608
Ma 240,80,124
Rt 40,22,4096
Ma 244,84,124
Rt 42,22,4608
Ma 248,88,124
Rt 44,23,4096
Ma 252,92,124
Rt 46,23,4608
s_waitcnt vmcnt(15) lgkmcnt(8)
s_branch .LzQA
.LzPAVX:
Mv 32,32,0
Rt 64,18,16384
Mv 36,36,0
Rb 192,28,32768
Mv 96,40,0
Rt 66,18,18432
Mv 100,44,0
s_add_u32 s39, s39, 0x40000
Rb 196,28,34816
Mv 160,48,0
Rt 68,19,16384
s_add_u32 m0, s37, 0
Mv 164,52,0
buffer_load_dwordx4 v12, s[8:11], s39 offen offset:0 lds
)" R"(
Mv 224,56,0
Rt 70,19,18432
Mv 228,60,0
Rb 200,29,32768
Mv 236,60,4
Rt 72,20,16384
Mv 232,56,4
Rb 204,29,34816
Mv 172,52,4
Rt 74,20,18432
Mv 168,48,4
Rb 208,28,36864
Mv 108,44,4
Rt 76,21,16384
Mv 104,40,4
buffer_load_dwordx4 v13, s[8:11], s39 offen offset:1024 lds
Mv 44,36,4
Rt 78,21,18432
Mv 40,32,4
Rb 212,28,38912
Mv 48,32,8
Rt 80,22,16384
Mv 52,36,8
Rb 216,29,36864
Mv 112,40,8
Rt 82,22,18432
Mv 116,44,8
Rb 220,29,38912
Mv 176,48,8
Rt 84,23,16384
Mv 180,52,8
buffer_load_dwordx4 v14, s[8:11], s39 offen offset:2048 lds
Mv 240,56,8
Rt 86,23,18432
Mv 244,60,8
Mv 252,60,12
Rt 88,24,16384
Mv 248,56,12
Mv 188,52,12
Rt 90,24,18432
Mv 184,48,12
Mv 124,44,12
Rt 92,25,16384
Mv 120,40,12
buffer_load_dwordx4 v15, s[8:11], s39 offen offset:3072 lds
Mv 60,36,12
Rt 94,25,18432
Mv 56,32,12
s_add_u32 s44, s43, 256
s_waitcnt lgkmcnt(6)
Mv 64,32,16
Rb 96,17,0
s_add_u32 m0, s36, 131072
Mv 68,36,16
buffer_load_dwordx4 v26, s[12:15], s44 offen offset:0 lds
Mv 128,40,16
Rb 100,17,2048
Mv 132,44,16
Mv 192,48,16
Rb 104,17,8192
Mv 196,52,16
buffer_load_dwordx4 v27, s[12:15], s44 offen offset:1024 lds
Ma 224,56,16
Rb 108,17,10240
)" R"(
Ma 228,60,16
Ma 236,60,20
Rb 112,17,16384
Ma 232,56,20
s_add_u32 s44, s43, 12544
buffer_load_dwordx4 v26, s[12:15], s44 offen offset:2048 lds
Mv 204,52,20
Rb 116,17,18432
Mv 200,48,20
Mv 140,44,20
Rb 120,17,24576
Mv 136,40,20
buffer_load_dwordx4 v27, s[12:15], s44 offen offset:3072 lds
Mv 76,36,20
Rb 124,17,26624
Mv 72,32,20
Mv 80,32,24
s_add_u32 m0, s36, 135168
s_add_u32 s44, s43, 384
Mv 84,36,24
buffer_load_dwordx4 v26, s[12:15], s44 offen offset:0 lds
Mv 144,40,24
Mv 148,44,24
Mv 208,48,24
Mv 212,52,24
buffer_load_dwordx4 v27, s[12:15], s44 offen offset:1024 lds
Ma 240,56,24
Ma 244,60,24
Ma 252,60,28
Ma 248,56,28
s_add_u32 s44, s43, 12672
buffer_load_dwordx4 v26, s[12:15], s44 offen offset:2048 lds
Mv 220,52,28
Mv 216,48,28
Mv 156,44,28
s_mov_b32 s85, 0xc108800
v_and_b32 v2, 7, v0
v_lshlrev_b32 v2, 7, v2
Mv 152,40,28
buffer_load_dwordx4 v27, s[12:15], s44 offen offset:3072 lds
Mv 92,36,28
buffer_load_dword v4, v2, s[20:23], s85 offen sc0 sc1
Mv 88,32,28
s_waitcnt vmcnt(18) lgkmcnt(0)
s_barrier
Mv 32,96,64
Rt 0,18,32768
Mv 36,100,64
s_add_u32 s38, s38, 0xffff0080
Mv 96,104,64
Rt 2,18,34816
s_add_u32 m0, s36, 0
Mv 100,108,64
buffer_load_dwordx4 v8, s[4:7], s38 offen offset:0 lds
Mv 160,112,64
Rt 4,19,32768
Mv 164,116,64
Mv 224,120,64
Rt 6,19,34816
Mv 228,124,64
buffer_load_dwordx4 v9, s[4:7], s38 offen offset:1024 lds
Mv 236,124,68
Rt 8,20,32768
Mv 232,120,68
Mv 172,116,68
)" R"(
Rt 10,20,34816
Mv 168,112,68
buffer_load_dwordx4 v10, s[4:7], s38 offen offset:2048 lds
Mv 108,108,68
Rt 12,21,32768
Mv 104,104,68
Mv 44,100,68
Rt 14,21,34816
Mv 40,96,68
buffer_load_dwordx4 v11, s[4:7], s38 offen offset:3072 lds
Mv 48,96,72
Rt 16,22,32768
Mv 52,100,72
s_add_u32 s38, s38, 0x10000
Mv 112,104,72
Rt 18,22,34816
s_add_u32 m0, s36, 4096
Mv 116,108,72
buffer_load_dwordx4 v8, s[4:7], s38 offen offset:0 lds
Mv 176,112,72
Rt 20,23,32768
Mv 180,116,72
Mv 240,120,72
Rt 22,23,34816
Mv 244,124,72
buffer_load_dwordx4 v9, s[4:7], s38 offen offset:1024 lds
Mv 252,124,76
Rt 24,24,32768
Mv 248,120,76
Mv 188,116,76
Rt 26,24,34816
Mv 184,112,76
buffer_load_dwordx4 v10, s[4:7], s38 offen offset:2048 lds
Mv 124,108,76
Rt 28,25,32768
Mv 120,104,76
Mv 60,100,76
Rt 30,25,34816
Mv 56,96,76
buffer_load_dwordx4 v11, s[4:7], s38 offen offset:3072 lds
Mv 64,96,80
Rb 32,16,32768
Mv 68,100,80
Mv 128,104,80
Rb 36,16,34816
Mv 132,108,80
s_add_u32 s39, s39, 0x40000
Mv 192,112,80
Rb 40,16,40960
s_add_u32 m0, s37, 16384
Mv 196,116,80
buffer_load_dwordx4 v12, s[8:11], s39 offen offset:0 lds
Ma 224,120,80
Rb 44,16,43008
Ma 228,124,80
Ma 236,124,84
Rb 48,16,49152
Ma 232,120,84
Mv 204,116,84
Rb 52,16,51200
Mv 200,112,84
buffer_load_dwordx4 v13, s[8:11], s39 offen offset:1024 lds
Mv 140,108,84
Rb 56,16,57344
)" R"(
Mv 136,104,84
Mv 76,100,84
Rb 60,16,59392
Mv 72,96,84
Mv 80,96,88
Mv 84,100,88
buffer_load_dwordx4 v14, s[8:11], s39 offen offset:2048 lds
Mv 144,104,88
Mv 148,108,88
Mv 208,112,88
Mv 212,116,88
Ma 240,120,88
Ma 244,124,88
buffer_load_dwordx4 v15, s[8:11], s39 offen offset:3072 lds
Ma 252,124,92
Ma 248,120,92
Mv 220,116,92
Mv 216,112,92
Mv 156,108,92
Mv 152,104,92
Mv 92,100,92
Mv 88,96,92
s_waitcnt vmcnt(13) lgkmcnt(0)
s_barrier
Mv 32,32,0
Rt 64,18,49152
Mv 36,36,0
Rb 128,28,32768
Mv 96,40,0
Rt 66,18,51200
Mv 100,44,0
s_add_u32 s39, s39, 0x40000
Rb 132,28,34816
Mv 160,48,0
Rt 68,19,49152
s_add_u32 m0, s37, 32768
Mv 164,52,0
buffer_load_dwordx4 v12, s[8:11], s39 offen offset:0 lds
Mv 224,56,0
Rt 70,19,51200
Mv 228,60,0
Rb 136,29,32768
Mv 236,60,4
Rt 72,20,49152
Mv 232,56,4
Rb 140,29,34816
Mv 172,52,4
Rt 74,20,51200
Mv 168,48,4
Rb 144,28,36864
Mv 108,44,4
Rt 76,21,49152
Mv 104,40,4
buffer_load_dwordx4 v13, s[8:11], s39 offen offset:1024 lds
Mv 44,36,4
Rt 78,21,51200
Mv 40,32,4
Rb 148,28,38912
Mv 48,32,8
Rt 80,22,49152
Mv 52,36,8
Rb 152,29,36864
Mv 112,40,8
Rt 82,22,51200
Mv 116,44,8
)" R"(
Rb 156,29,38912
Mv 176,48,8
Rt 84,23,49152
Mv 180,52,8
buffer_load_dwordx4 v14, s[8:11], s39 offen offset:2048 lds
Mv 240,56,8
Rt 86,23,51200
Mv 244,60,8
Mv 252,60,12
Rt 88,24,49152
Mv 248,56,12
Mv 188,52,12
Rt 90,24,51200
Mv 184,48,12
Mv 124,44,12
Rt 92,25,49152
Mv 120,40,12
buffer_load_dwordx4 v15, s[8:11], s39 offen offset:3072 lds
Mv 60,36,12
Rt 94,25,51200
Mv 56,32,12
s_add_u32 s44, s43, 512
s_waitcnt lgkmcnt(6)
Mv 64,32,16
Rb 96,17,32768
s_add_u32 m0, s36, 131072
Mv 68,36,16
buffer_load_dwordx4 v26, s[12:15], s44 offen offset:0 lds
Mv 128,40,16
Rb 100,17,34816
Mv 132,44,16
Mv 192,48,16
Rb 104,17,40960
Mv 196,52,16
buffer_load_dwordx4 v27, s[12:15], s44 offen offset:1024 lds
Ma 224,56,16
Rb 108,17,43008
Ma 228,60,16
Ma 236,60,20
Rb 112,17,49152
Ma 232,56,20
s_add_u32 s44, s43, 12800
buffer_load_dwordx4 v26, s[12:15], s44 offen offset:2048 lds
Mv 204,52,20
Rb 116,17,51200
Mv 200,48,20
Mv 140,44,20
Rb 120,17,57344
Mv 136,40,20
buffer_load_dwordx4 v27, s[12:15], s44 offen offset:3072 lds
Mv 76,36,20
Rb 124,17,59392
Mv 72,32,20
Mv 80,32,24
s_add_u32 m0, s36, 135168
s_add_u32 s44, s43, 640
Mv 84,36,24
buffer_load_dwordx4 v26, s[12:15], s44 offen offset:0 lds
Mv 144,40,24
Mv 148,44,24
Mv 208,48,24
Mv 212,52,24
buffer_load_dwordx4 v27, s[12:15], s44 offen offset:1024 lds
Ma 240,56,24
)" R"(
Ma 244,60,24
Ma 252,60,28
Ma 248,56,28
s_add_u32 s44, s43, 12928
buffer_load_dwordx4 v26, s[12:15], s44 offen offset:2048 lds
Mv 220,52,28
Mv 216,48,28
Mv 156,44,28
Mv 152,40,28
buffer_load_dwordx4 v27, s[12:15], s44 offen offset:3072 lds
Mv 92,36,28
Mv 88,32,28
s_waitcnt vmcnt(16) lgkmcnt(0)
s_barrier
Mv 32,96,64
Rt 0,18,0
v_xor_b32 v4, s3, v4
v_cmp_gt_u32_e32 vcc, 2, v4
s_andn2_b32 s28, 0xff, vcc_lo
s_cbranch_scc1 .Lzsp3
.Lzok3:
Mv 36,100,64
s_add_u32 s38, s38, 0xffff0080
v_xor_b32 v4, s3, v4
Mv 96,104,64
Rt 2,18,2048
s_add_u32 m0, s36, 32768
Mv 100,108,64
buffer_load_dwordx4 v8, s[4:7], s38 offen offset:0 lds
Mv 160,112,64
Rt 4,19,0
v_and_b32 v4, 1, v4
Mv 164,116,64
v_cmp_eq_u32_e32 vcc, 1, v4
s_mov_b32 s85, vcc_lo
Mv 224,120,64
Rt 6,19,2048
s_and_b32 s28, s2, 7
Mv 228,124,64
buffer_load_dwordx4 v9, s[4:7], s38 offen offset:1024 lds
Mv 236,124,68
Rt 8,20,0
s_lshr_b32 s28, s85, s28
Mv 232,120,68
s_xor_b32 s28, s28, s34
s_and_b32 s28, s28, 1
Mv 172,116,68
Rt 10,20,2048
s_lshl_b32 s84, s28, 13
Mv 168,112,68
buffer_load_dwordx4 v10, s[4:7], s38 offen offset:2048 lds
Mv 108,108,68
Rt 12,21,0
s_lshl1_add_u32 s28, s36, s84
Mv 104,104,68
v_add_u32 v2, s28, v1
v_add_u32 v3, 0x1000, v2
Mv 44,100,68
Rt 14,21,2048
s_lshl_b32 s28, s34, 13
Mv 40,96,68
buffer_load_dwordx4 v11, s[4:7], s38 offen offset:3072 lds
Mv 48,96,72
Rt 16,22,0
s_xor_b32 s28, s28, s84
)" R"(
Mv 52,100,72
s_add_u32 s38, s38, 0x10000
s_and_b32 s28, s28, 0x2000
Mv 112,104,72
Rt 18,22,2048
s_add_u32 m0, s36, 36864
Mv 116,108,72
buffer_load_dwordx4 v8, s[4:7], s38 offen offset:0 lds
Mv 176,112,72
Rt 20,23,0
s_xor_b32 s73, s73, s28
Mv 180,116,72
Mv 240,120,72
Rt 22,23,2048
Mv 244,124,72
buffer_load_dwordx4 v9, s[4:7], s38 offen offset:1024 lds
Mv 252,124,76
Rt 24,24,0
Mv 248,120,76
Mv 188,116,76
Rt 26,24,2048
Mv 184,112,76
buffer_load_dwordx4 v10, s[4:7], s38 offen offset:2048 lds
Mv 124,108,76
Rt 28,25,0
Mv 120,104,76
Mv 60,100,76
Rt 30,25,2048
Mv 56,96,76
buffer_load_dwordx4 v11, s[4:7], s38 offen offset:3072 lds
Mv 64,96,80
Rb 32,16,0
Mv 68,100,80
Mv 128,104,80
Rb 36,16,2048
Mv 132,108,80
s_add_u32 s39, s39, 0x40000
Mv 192,112,80
Rb 40,16,8192
s_add_u32 m0, s37, 49152
Mv 196,116,80
buffer_load_dwordx4 v12, s[8:11], s39 offen offset:0 lds
Ma 224,120,80
Rb 44,16,10240
Ma 228,124,80
Ma 236,124,84
Rb 48,16,16384
Ma 232,120,84
Mv 204,116,84
Rb 52,16,18432
Mv 200,112,84
buffer_load_dwordx4 v13, s[8:11], s39 offen offset:1024 lds
Mv 140,108,84
Rb 56,16,24576
Mv 136,104,84
Mv 76,100,84
Rb 60,16,26624
Mv 72,96,84
Mv 80,96,88
Mv 84,100,88
buffer_load_dwordx4 v14, s[8:11], s39 offen offset:2048 lds
Mv 144,104,88
Mv 148,108,88
Mv 208,112,88
)" R"(
Mv 212,116,88
Ma 240,120,88
Ma 244,124,88
buffer_load_dwordx4 v15, s[8:11], s39 offen offset:3072 lds
Ma 252,124,92
Ma 248,120,92
Mv 220,116,92
Mv 216,112,92
Mv 156,108,92
Mv 152,104,92
Mv 92,100,92
Mv 88,96,92
s_waitcnt vmcnt(12) lgkmcnt(0)
s_barrier
Mv 32,32,0
Rt 64,18,16384
Mv 36,36,0
Rb 160,28,32768
v_lshrrev_b32 v5, 2, v0
Mv 96,40,0
Rt 66,18,18432
v_and_b32 v5, 8, v5
Mv 100,44,0
Rb 164,28,34816
v_and_or_b32 v5, v0, 7, v5
Mv 160,48,0
Rt 68,19,16384
v_lshlrev_b32 v5, 8, v5
Mv 164,52,0
v_and_or_b32 v5, v0, 24, v5
v_lshrrev_b32 v5, 1, v5
Mv 224,56,0
Rt 70,19,18432
v_mov_b32 v6, s3
Mv 228,60,0
Rb 168,29,32768
Mv 236,60,4
Rt 72,20,16384
Mv 232,56,4
Rb 172,29,34816
Mv 172,52,4
Rt 74,20,18432
Mv 168,48,4
Rb 176,28,36864
Mv 108,44,4
Rt 76,21,16384
Mv 104,40,4
Mv 44,36,4
Rt 78,21,18432
Mv 40,32,4
Rb 180,28,38912
Mv 48,32,8
Rt 80,22,16384
Mv 52,36,8
Rb 184,29,36864
Mv 112,40,8
Rt 82,22,18432
Mv 116,44,8
Rb 188,29,38912
Mv 176,48,8
Rt 84,23,16384
Mv 180,52,8
Mv 240,56,8
Rt 86,23,18432
Mv 244,60,8
)" R"(
Mv 252,60,12
Rt 88,24,16384
Mv 248,56,12
Mv 188,52,12
Rt 90,24,18432
Mv 184,48,12
Mv 124,44,12
Rt 92,25,16384
Mv 120,40,12
Mv 60,36,12
Rt 94,25,18432
v_lshrrev_b32 v10, 3, v0
Mv 56,32,12
v_and_b32 v11, 0x70, v1
v_lshl_or_b32 v10, v10, 13, v11
Mv 64,32,16
Rb 96,17,0
v_lshlrev_b32 v11, 1, v0
Mv 68,36,16
v_and_b32 v11, 32, v11
v_xor_b32 v10, v10, v11
buffer_load_dword v4, v7, s[20:23], s52 offen offset:0 sc0 sc1
Mv 128,40,16
Rb 100,17,2048
s_and_b32 s28, s33, 1
Mv 132,44,16
s_lshl_b32 s28, s28, 6
v_xor_b32 v8, s28, v10
Mv 192,48,16
Rb 104,17,8192
v_xor_b32 v9, 64, v8
Mv 196,52,16
v_add_u32 v9, 0xfc00, v9
v_add_u32 v10, 0x1f800, v8
Ma 224,56,16
Rb 108,17,10240
v_add_u32 v11, 0x1f800, v9
Ma 228,60,16
Ma 236,60,20
Rb 112,17,16384
Ma 232,56,20
Mv 204,52,20
Rb 116,17,18432
Mv 200,48,20
Mv 140,44,20
Rb 120,17,24576
Mv 136,40,20
Mv 76,36,20
Rb 124,17,26624
Mv 72,32,20
Mv 80,32,24
Mv 84,36,24
Mv 144,40,24
Mv 148,44,24
Mv 208,48,24
Mv 212,52,24
Ma 240,56,24
Ma 244,60,24
Ma 252,60,28
Ma 248,56,28
Mv 220,52,28
Mv 216,48,28
Mv 156,44,28
Mv 152,40,28
)" R"(
Mv 92,36,28
Mv 88,32,28
s_waitcnt vmcnt(5) lgkmcnt(0)
s_barrier
Mv 32,96,64
Rt 0,18,32768
Mv 36,100,64
s_add_u32 s92, s42, 0x0
Mv 96,104,64
Rt 2,18,34816
s_add_u32 m0, s41, 131072
Mv 100,108,64
buffer_load_dwordx4 v8, s[16:19], s92 offen offset:0 lds
Mv 160,112,64
Rt 4,19,32768
Mv 164,116,64
Mv 224,120,64
Rt 6,19,34816
Mv 228,124,64
buffer_load_dwordx4 v9, s[16:19], s92 offen offset:1024 lds
Mv 236,124,68
Rt 8,20,32768
Mv 232,120,68
Mv 172,116,68
Rt 10,20,34816
Mv 168,112,68
buffer_load_dwordx4 v10, s[16:19], s92 offen offset:2048 lds
Mv 108,108,68
Rt 12,21,32768
Mv 104,104,68
Mv 44,100,68
Rt 14,21,34816
Mv 40,96,68
buffer_load_dwordx4 v11, s[16:19], s92 offen offset:3072 lds
Mv 48,96,72
Rt 16,22,32768
Mv 52,100,72
s_add_u32 s92, s42, 0x40000
Mv 112,104,72
Rt 18,22,34816
s_add_u32 m0, s41, 135168
Mv 116,108,72
buffer_load_dwordx4 v8, s[16:19], s92 offen offset:0 lds
Mv 176,112,72
Rt 20,23,32768
Mv 180,116,72
Mv 240,120,72
Rt 22,23,34816
Mv 244,124,72
buffer_load_dwordx4 v9, s[16:19], s92 offen offset:1024 lds
Mv 252,124,76
Rt 24,24,32768
Mv 248,120,76
Mv 188,116,76
Rt 26,24,34816
Mv 184,112,76
buffer_load_dwordx4 v10, s[16:19], s92 offen offset:2048 lds
Mv 124,108,76
Rt 28,25,32768
Mv 120,104,76
s_add_u32 s92, s42, 0x1c0000
Mv 60,100,76
Rt 30,25,34816
s_add_u32 m0, s41, 0
Mv 56,96,76
)" R"(
buffer_load_dwordx4 v8, s[16:19], s92 offen offset:0 lds
Mv 64,96,80
Rb 32,16,32768
Mv 68,100,80
Mv 128,104,80
Rb 36,16,34816
Mv 132,108,80
buffer_load_dwordx4 v9, s[16:19], s92 offen offset:1024 lds
Mv 192,112,80
Rb 40,16,40960
Mv 196,116,80
Ma 224,120,80
Rb 44,16,43008
Ma 228,124,80
buffer_load_dwordx4 v10, s[16:19], s92 offen offset:2048 lds
Ma 236,124,84
Rb 48,16,49152
Ma 232,120,84
Mv 204,116,84
Rb 52,16,51200
Mv 200,112,84
buffer_load_dwordx4 v11, s[16:19], s92 offen offset:3072 lds
Mv 140,108,84
Rb 56,16,57344
Mv 136,104,84
s_add_u32 s92, s42, 0x200000
Mv 76,100,84
Rb 60,16,59392
s_add_u32 m0, s41, 4096
Mv 72,96,84
buffer_load_dwordx4 v8, s[16:19], s92 offen offset:0 lds
Mv 80,96,88
Mv 84,100,88
Mv 144,104,88
Mv 148,108,88
buffer_load_dwordx4 v9, s[16:19], s92 offen offset:1024 lds
Mv 208,112,88
Mv 212,116,88
Ma 240,120,88
Ma 244,124,88
buffer_load_dwordx4 v10, s[16:19], s92 offen offset:2048 lds
Ma 252,124,92
Ma 248,120,92
Mv 220,116,92
Mv 216,112,92
Mv 156,108,92
Mv 152,104,92
Mv 92,100,92
Mv 88,96,92
s_waitcnt vmcnt(14) lgkmcnt(4)
s_barrier
v_cmp_eq_u32_e32 vcc, s3, v4
s_andn2_b64 s[28:29], 1, vcc
s_cbranch_scc1 .Lzsp4
.Lzok4:
Mv 32,32,0
Rt 64,18,49152
Mv 36,36,0
Rt 66,18,51200
s_add_u32 s92, s42, 0x80
Mv 96,40,0
Rt 68,19,49152
s_add_u32 m0, s41, 65536
Mv 100,44,0
Rt 70,19,51200
)" R"(
buffer_load_dwordx4 v8, s[16:19], s92 offen offset:0 lds
Mv 108,44,4
Rt 72,20,49152
Mv 104,40,4
Rt 74,20,51200
Mv 44,36,4
Rt 76,21,49152
Mv 40,32,4
Rt 78,21,51200
buffer_load_dwordx4 v9, s[16:19], s92 offen offset:1024 lds
Mv 48,32,8
Rt 80,22,49152
Mv 52,36,8
Rt 82,22,51200
Mv 112,40,8
Rt 84,23,49152
Mv 116,44,8
Rt 86,23,51200
buffer_load_dwordx4 v10, s[16:19], s92 offen offset:2048 lds
Mv 124,44,12
Rt 88,24,49152
Mv 120,40,12
Rt 90,24,51200
Mv 60,36,12
Rt 92,25,49152
Mv 56,32,12
Rt 94,25,51200
buffer_load_dwordx4 v11, s[16:19], s92 offen offset:3072 lds
Mv 64,32,16
Rb 96,17,32768
Mv 68,36,16
Rb 100,17,34816
s_add_u32 s92, s42, 0x40080
Mv 128,40,16
Rb 104,17,40960
s_add_u32 m0, s41, 69632
Mv 132,44,16
Rb 108,17,43008
buffer_load_dwordx4 v8, s[16:19], s92 offen offset:0 lds
Mv 140,44,20
Rb 112,17,49152
Mv 136,40,20
Rb 116,17,51200
Mv 76,36,20
Rb 120,17,57344
Mv 72,32,20
Rb 124,17,59392
buffer_load_dwordx4 v9, s[16:19], s92 offen offset:1024 lds
Mv 80,32,24
Mv 84,36,24
Mv 144,40,24
Mv 148,44,24
buffer_load_dwordx4 v10, s[16:19], s92 offen offset:2048 lds
Mv 156,44,28
Mv 152,40,28
Mv 92,36,28
Mv 88,32,28
s_waitcnt lgkmcnt(0)
s_barrier
Mv 32,96,64
v_bfe_u32 v12, v0, 3, 2
v_lshlrev_b32 v12, 5, v12
Mv 36,100,64
v_and_b32 v4, 3, v0
v_lshl_or_b32 v12, v4, 3, v12
)" R"(
Mv 40,96,68
s_add_u32 m0, s36, 98304
s_add_u32 s44, s43, 768
Mv 44,100,68
buffer_load_dwordx4 v26, s[12:15], s44 offen offset:0 lds
Mv 48,96,72
v_bfe_u32 v4, v0, 2, 2
v_lshl_or_b32 v12, v4, 7, v12
Mv 52,100,72
v_lshrrev_b32 v4, 4, v0
v_lshl_or_b32 v12, v4, 10, v12
Mv 56,96,76
v_xor_b32 v13, 32, v12
v_xor_b32 v14, 64, v12
Mv 60,100,76
buffer_load_dwordx4 v27, s[12:15], s44 offen offset:1024 lds
Mv 64,96,80
v_xor_b32 v15, 96, v12
v_add_u32 v20, 0x20000, v12
Mv 68,100,80
v_add_u32 v21, 0x20000, v13
v_add_u32 v22, 0x20000, v14
Mv 72,96,84
v_add_u32 v23, 0x20000, v15
v_add_u32 v16, 0x10000, v12
Mv 76,100,84
s_add_u32 s44, s43, 13056
buffer_load_dwordx4 v26, s[12:15], s44 offen offset:2048 lds
Mv 80,96,88
v_add_u32 v17, 0x10000, v13
v_add_u32 v18, 0x10000, v14
Mv 84,100,88
v_add_u32 v19, 0x10000, v15
Cv 32,32,33
Mv 88,96,92
Cv 33,34,35
Cv 34,36,37
Mv 92,100,92
buffer_load_dwordx4 v27, s[12:15], s44 offen offset:3072 lds
Mv 96,104,64
Cv 35,38,39
Cv 40,40,41
Mv 100,108,64
Cv 41,42,43
Cv 42,44,45
Mv 104,104,68
s_add_u32 m0, s41, 32768
s_add_u32 s92, s42, 0x1c0080
Mv 108,108,68
buffer_load_dwordx4 v8, s[16:19], s92 offen offset:0 lds
Mv 112,104,72
Cv 43,46,47
Cv 48,48,49
Mv 116,108,72
Cv 49,50,51
Cv 50,52,53
Mv 120,104,76
Cv 51,54,55
Cv 56,56,57
Mv 124,108,76
buffer_store_dwordx4 v[32:35], v2, s[20:23], s45 offen offset:0 sc0 sc1
Mv 128,104,80
Cv 57,58,59
Cv 58,60,61
)" R"(
Mv 132,108,80
Cv 59,62,63
Cv 64,64,65
Mv 136,104,84
Cv 65,66,67
Cv 66,68,69
Mv 140,108,84
buffer_load_dwordx4 v9, s[16:19], s92 offen offset:1024 lds
Mv 144,104,88
Cv 67,70,71
Cv 72,72,73
Mv 148,108,88
Cv 73,74,75
Cv 74,76,77
Mv 152,104,92
Cv 75,78,79
Cv 80,80,81
Mv 156,108,92
buffer_store_dwordx4 v[40:43], v2, s[20:23], s45 offen offset:1024 sc0 sc1
Mv 160,48,0
Cv 81,82,83
Cv 82,84,85
Mv 160,112,64
Cv 83,86,87
Cv 88,88,89
Mv 164,52,0
Cv 89,90,91
Cv 90,92,93
Mv 164,116,64
buffer_load_dwordx4 v10, s[16:19], s92 offen offset:2048 lds
Mv 168,48,4
Cv 91,94,95
Cv 96,96,97
Mv 168,112,68
Cv 97,98,99
Cv 98,100,101
Mv 172,52,4
Cv 99,102,103
Cv 104,104,105
Mv 172,116,68
buffer_store_dwordx4 v[48:51], v2, s[20:23], s45 offen offset:2048 sc0 sc1
Mv 176,48,8
Cv 105,106,107
Cv 106,108,109
Mv 176,112,72
Cv 107,110,111
Cv 112,112,113
Mv 180,52,8
Cv 113,114,115
Cv 114,116,117
Mv 180,116,72
buffer_load_dwordx4 v11, s[16:19], s92 offen offset:3072 lds
Mv 184,48,12
Cv 115,118,119
Cv 120,120,121
Mv 184,112,76
Cv 121,122,123
Cv 122,124,125
Mv 188,52,12
Cv 123,126,127
Cv 128,128,129
Mv 188,116,76
buffer_store_dwordx4 v[56:59], v2, s[20:23], s45 offen offset:3072 sc0 sc1
Mv 192,48,16
)" R"(
Cv 129,130,131
Cv 130,132,133
Mv 192,112,80
Cv 131,134,135
Cv 136,136,137
Mv 196,52,16
s_add_u32 m0, s41, 36864
s_add_u32 s92, s42, 0x200080
Mv 196,116,80
buffer_load_dwordx4 v8, s[16:19], s92 offen offset:0 lds
Mv 200,48,20
Cv 137,138,139
Cv 138,140,141
Mv 200,112,84
Cv 139,142,143
Cv 144,144,145
Mv 204,52,20
Cv 145,146,147
Cv 146,148,149
Mv 204,116,84
buffer_store_dwordx4 v[64:67], v3, s[20:23], s45 offen offset:0 sc0 sc1
Mv 208,48,24
Cv 147,150,151
Cv 152,152,153
Mv 208,112,88
Cv 153,154,155
Cv 154,156,157
Mv 212,52,24
Cv 155,158,159
Mv 212,116,88
buffer_load_dwordx4 v9, s[16:19], s92 offen offset:1024 lds
Mv 216,48,28
Mv 216,112,92
Mv 220,52,28
Mv 220,116,92
buffer_store_dwordx4 v[72:75], v3, s[20:23], s45 offen offset:1024 sc0 sc1
Mv 224,56,0
Mv 228,60,0
Mv 236,60,4
Mv 232,56,4
buffer_load_dwordx4 v10, s[16:19], s92 offen offset:2048 lds
Mv 240,56,8
Mv 244,60,8
Mv 252,60,12
Mv 248,56,12
s_waitcnt vmcnt(24)
s_barrier
Ma 224,56,16
buffer_store_dwordx4 v[80:83], v3, s[20:23], s45 offen offset:2048 sc0 sc1
Ma 228,60,16
Ma 236,60,20
Ma 232,56,20
Ma 240,56,24
Ma 244,60,24
Ma 252,60,28
Ma 248,56,28
buffer_store_dwordx4 v[88:91], v3, s[20:23], s45 offen offset:3072 sc0 sc1
Mv 224,120,64
Rt 16,20,0
Mv 228,124,64
Rt 18,20,512
Mv 232,120,68
Rt 20,21,0
Mv 236,124,68
Rt 22,21,512
)" R"(
Mv 240,120,72
Rt 24,22,0
Mv 244,124,72
Rt 26,22,512
Mv 248,120,76
Rt 28,23,0
Mv 252,124,76
Rt 30,23,512
Ma 224,120,80
Rt 32,20,4096
Ma 228,124,80
Rt 34,20,4608
Ma 232,120,84
Rt 36,21,4096
Ma 236,124,84
Rt 38,21,4608
Ma 240,120,88
Rt 40,22,4096
Ma 244,124,88
Rt 42,22,4608
Ma 248,120,92
Rt 44,23,4096
Ma 252,124,92
Rt 46,23,4608
s_waitcnt vmcnt(15) lgkmcnt(8)
.LzQA:
s_cmp_eq_u32 s2, 112
s_cbranch_scc1 .LzT2S
.LzT2E:
Mz 32,16,192
Rt 0,20,8192
Cv 224,224,225
Mz 40,16,196
Rt 2,20,8704
Rb 48,28,0
Mz 44,20,196
Rt 4,21,8192
buffer_store_dwordx4 v[96:99], v2, s[20:23], s46 offen offset:0 sc0 sc1
Mz 36,20,192
Rt 6,21,8704
Rb 52,28,2048
Mz 48,24,192
Rt 8,22,8192
Cv 225,226,227
Mz 56,24,196
Rt 10,22,8704
Rb 56,29,0
buffer_load_dwordx4 a[64:67], v2, s[20:23], s51 offen offset:0 sc0 sc1
Mz 60,28,196
Rt 12,23,8192
Cv 226,228,229
Mz 52,28,192
Rt 14,23,8704
Rb 60,29,2048
s_waitcnt lgkmcnt(12)
Mv 32,32,200
Rt 16,20,12288
Cv 227,230,231
Mv 40,32,204
Rt 18,20,12800
Cv 232,232,233
Mv 44,36,204
Rt 20,21,12288
buffer_load_dwordx4 a[68:71], v2, s[20:23], s51 offen offset:1024 sc0 sc1
)" R"(
Mv 36,36,200
Rt 22,21,12800
Cv 233,234,235
Mv 48,40,200
Rt 24,22,12288
buffer_store_dwordx4 v[104:107], v2, s[20:23], s46 offen offset:1024 sc0 sc1
Mv 56,40,204
Rt 26,22,12800
Cv 234,236,237
Mv 60,44,204
Rt 28,23,12288
Cv 235,238,239
Mv 52,44,200
Rt 30,23,12800
buffer_load_dwordx4 a[72:75], v2, s[20:23], s51 offen offset:2048 sc0 sc1
s_waitcnt lgkmcnt(9)
Mv 32,0,208
Rt 32,20,16384
Cv 240,240,241
Mv 40,0,212
Rt 34,20,16896
Cv 241,242,243
Mv 44,4,212
Rt 36,21,16384
Cv 242,244,245
Mv 36,4,208
Rt 38,21,16896
Cv 243,246,247
Mv 48,8,208
Rt 40,22,16384
buffer_load_dwordx4 a[76:79], v2, s[20:23], s51 offen offset:3072 sc0 sc1
Mv 56,8,212
Rt 42,22,16896
Cv 248,248,249
Cv 249,250,251
Mv 60,12,212
Rt 44,23,16384
buffer_store_dwordx4 v[112:115], v2, s[20:23], s46 offen offset:2048 sc0 sc1
Mv 52,12,208
Rt 46,23,16896
s_waitcnt lgkmcnt(8)
Mv 32,16,216
Rt 0,20,20480
Cv 250,252,253
Mv 40,16,220
Rt 2,20,20992
buffer_load_dwordx4 a[80:83], v3, s[20:23], s51 offen offset:0 sc0 sc1
Mv 44,20,220
Rt 4,21,20480
Cv 251,254,255
v_accvgpr_read_b32 v228, a224
Mv 36,20,216
Rt 6,21,20992
v_accvgpr_read_b32 v24, a225
Mv 48,24,216
Rt 8,22,20480
Cv 228,228,24
Mv 56,24,220
Rt 10,22,20992
v_accvgpr_read_b32 v229, a226
Mv 60,28,220
Rt 12,23,20480
buffer_load_dwordx4 a[84:87], v3, s[20:23], s51 offen offset:1024 sc0 sc1
Mv 52,28,216
)" R"(
Rt 14,23,20992
v_accvgpr_read_b32 v25, a227
s_waitcnt lgkmcnt(8)
Mv 32,32,128
Rt 16,20,24576
buffer_store_dwordx4 v[120:123], v2, s[20:23], s46 offen offset:3072 sc0 sc1
Mv 40,32,132
Rt 18,20,25088
Cv 229,229,25
Mv 44,36,132
Rt 20,21,24576
v_accvgpr_read_b32 v230, a228
Mv 36,36,128
Rt 22,21,25088
buffer_load_dwordx4 a[88:91], v3, s[20:23], s51 offen offset:2048 sc0 sc1
Mv 48,40,128
Rt 24,22,24576
v_accvgpr_read_b32 v24, a229
Mv 56,40,132
Rt 26,22,25088
Cv 230,230,24
v_accvgpr_read_b32 v231, a230
Mv 60,44,132
Rt 28,23,24576
v_accvgpr_read_b32 v25, a231
Mv 52,44,128
Rt 30,23,25088
s_waitcnt lgkmcnt(8)
Mv 32,0,136
Rt 32,12,0
buffer_load_dwordx4 a[92:95], v3, s[20:23], s51 offen offset:3072 sc0 sc1
Mv 40,0,140
Rt 34,12,512
Cv 231,231,25
Mv 44,4,140
Rt 36,13,0
buffer_store_dwordx4 v[128:131], v3, s[20:23], s46 offen offset:0 sc0 sc1
Mv 36,4,136
Rt 38,13,512
v_accvgpr_read_b32 v236, a232
Mv 48,8,136
Rt 40,14,0
v_accvgpr_read_b32 v24, a233
Mv 56,8,140
Rt 42,14,512
Cv 236,236,24
Mv 60,12,140
Rt 44,15,0
v_accvgpr_read_b32 v237, a234
Mv 52,12,136
Rt 46,15,512
s_waitcnt vmcnt(13) lgkmcnt(8)
s_barrier
buffer_store_dword v6, v7, s[20:23], s49 offen offset:0 sc0 sc1
Mv 32,16,144
Rt 0,12,4096
v_accvgpr_read_b32 v25, a235
Mv 40,16,148
Rt 2,12,4608
s_add_u32 s92, s42, 0x100
Mv 44,20,148
Rt 4,13,4096
s_add_u32 m0, s41, 98304
Mv 36,20,144
Rt 6,13,4608
)" R"(
buffer_load_dwordx4 v8, s[16:19], s92 offen offset:0 lds
Mv 48,24,144
Rt 8,14,4096
Cv 237,237,25
Mv 56,24,148
Rt 10,14,4608
v_accvgpr_read_b32 v238, a236
v_accvgpr_read_b32 v24, a237
Mv 60,28,148
Rt 12,15,4096
Cv 238,238,24
Mv 52,28,144
Rt 14,15,4608
buffer_load_dwordx4 v9, s[16:19], s92 offen offset:1024 lds
s_waitcnt lgkmcnt(8)
Mv 32,32,152
Rt 16,12,8192
v_accvgpr_read_b32 v239, a238
Mv 40,32,156
Rt 18,12,8704
buffer_store_dwordx4 v[136:139], v3, s[20:23], s46 offen offset:1024 sc0 sc1
Mv 44,36,156
Rt 20,13,8192
v_accvgpr_read_b32 v25, a239
Cv 239,239,25
Mv 36,36,152
Rt 22,13,8704
buffer_load_dwordx4 v10, s[16:19], s92 offen offset:2048 lds
Mv 48,40,152
Rt 24,14,8192
v_accvgpr_read_b32 v244, a240
Mv 56,40,156
Rt 26,14,8704
v_accvgpr_read_b32 v24, a241
Mv 60,44,156
Rt 28,15,8192
Cv 244,244,24
Mv 52,44,152
Rt 30,15,8704
buffer_load_dwordx4 v11, s[16:19], s92 offen offset:3072 lds
s_waitcnt lgkmcnt(8)
Mv 32,0,160
Rt 32,12,12288
v_accvgpr_read_b32 v245, a242
Mv 40,0,164
Rt 34,12,12800
s_add_u32 s92, s42, 0x40100
Mv 44,4,164
Rt 36,13,12288
s_add_u32 m0, s41, 102400
Mv 36,4,160
Rt 38,13,12800
buffer_load_dwordx4 v8, s[16:19], s92 offen offset:0 lds
Mv 48,8,160
Rt 40,14,12288
v_accvgpr_read_b32 v25, a243
Mv 56,8,164
Rt 42,14,12800
Cv 245,245,25
v_accvgpr_read_b32 v246, a244
Mv 60,12,164
Rt 44,15,12288
v_accvgpr_read_b32 v24, a245
Mv 52,12,160
)" R"(
Rt 46,15,12800
buffer_load_dwordx4 v9, s[16:19], s92 offen offset:1024 lds
s_waitcnt lgkmcnt(8)
Mv 32,16,168
Rt 0,12,16384
Cv 246,246,24
Mv 40,16,172
Rt 2,12,16896
buffer_store_dwordx4 v[144:147], v3, s[20:23], s46 offen offset:2048 sc0 sc1
Mv 44,20,172
Rt 4,13,16384
v_accvgpr_read_b32 v247, a246
v_accvgpr_read_b32 v25, a247
Mv 36,20,168
Rt 6,13,16896
buffer_load_dwordx4 v10, s[16:19], s92 offen offset:2048 lds
Mv 48,24,168
Rt 8,14,16384
Cv 247,247,25
Mv 56,24,172
Rt 10,14,16896
s_add_u32 s92, s42, 0x1c0100
Mv 60,28,172
Rt 12,15,16384
s_add_u32 m0, s41, 131072
Mv 52,28,168
Rt 14,15,16896
buffer_load_dwordx4 v8, s[16:19], s92 offen offset:0 lds
s_waitcnt lgkmcnt(8)
Mv 32,32,176
Rt 16,12,20480
v_accvgpr_read_b32 v252, a248
Mv 40,32,180
Rt 18,12,20992
v_accvgpr_read_b32 v24, a249
Mv 44,36,180
Rt 20,13,20480
Cv 252,252,24
Mv 36,36,176
Rt 22,13,20992
buffer_load_dwordx4 v9, s[16:19], s92 offen offset:1024 lds
Mv 48,40,176
Rt 24,14,20480
v_accvgpr_read_b32 v253, a250
Mv 56,40,180
Rt 26,14,20992
v_accvgpr_read_b32 v25, a251
Cv 253,253,25
Mv 60,44,180
Rt 28,15,20480
v_accvgpr_read_b32 v254, a252
Mv 52,44,176
Rt 30,15,20992
buffer_load_dwordx4 v10, s[16:19], s92 offen offset:2048 lds
s_waitcnt lgkmcnt(8)
Mv 32,0,184
Rt 32,12,24576
v_accvgpr_read_b32 v24, a253
Mv 40,0,188
Rt 34,12,25088
buffer_store_dwordx4 v[152:155], v3, s[20:23], s46 offen offset:3072 sc0 sc1
Mv 44,4,188
Rt 36,13,24576
Cv 254,254,24
)" R"(
v_accvgpr_read_b32 v255, a254
Mv 36,4,184
Rt 38,13,25088
buffer_load_dwordx4 v11, s[16:19], s92 offen offset:3072 lds
Mv 48,8,184
Rt 40,14,24576
v_accvgpr_read_b32 v25, a255
Mv 56,8,188
Rt 42,14,25088
s_add_u32 s92, s42, 0x200100
Mv 60,12,188
Rt 44,15,24576
s_add_u32 m0, s41, 135168
Mv 52,12,184
Rt 46,15,25088
buffer_load_dwordx4 v8, s[16:19], s92 offen offset:0 lds
s_waitcnt lgkmcnt(8)
Mv 32,16,48
Rt 0,16,0
Cv 255,255,25
Mv 40,16,52
Rt 2,16,512
Cv 160,160,161
Mv 44,20,52
Rt 4,17,0
Cv 161,162,163
Mv 36,20,48
Rt 6,17,512
buffer_load_dwordx4 v9, s[16:19], s92 offen offset:1024 lds
Mv 48,24,48
Rt 8,18,0
Cv 162,164,165
Mv 56,24,52
Rt 10,18,512
Cv 163,166,167
Cv 168,168,169
Mv 60,28,52
Rt 12,19,0
Cv 169,170,171
Mv 52,28,48
Rt 14,19,512
buffer_load_dwordx4 v10, s[16:19], s92 offen offset:2048 lds
s_waitcnt lgkmcnt(8)
s_barrier
Mv 32,32,56
Rt 16,16,4096
Cv 170,172,173
Mv 40,32,60
Rt 18,16,4608
buffer_store_dwordx4 v[160:163], v2, s[20:23], s47 offen offset:0 sc0 sc1
Mv 44,36,60
Rt 20,17,4096
Cv 171,174,175
Cv 176,176,177
Mv 36,36,56
Rt 22,17,4608
s_add_u32 s92, s42, 0x180
Mv 48,40,56
Rt 24,18,4096
s_add_u32 m0, s41, 0
Mv 56,40,60
Rt 26,18,4608
buffer_load_dwordx4 v8, s[16:19], s92 offen offset:0 lds
Mv 60,44,60
Rt 28,19,4096
)" R"(
Cv 177,178,179
Mv 52,44,56
Rt 30,19,4608
Cv 178,180,181
s_waitcnt lgkmcnt(8)
Mz 64,0,192
Rt 32,16,8192
Cv 179,182,183
Mz 72,0,196
Rt 34,16,8704
s_add_u32 s51, s51, 0x10000
buffer_load_dwordx4 a[96:99], v2, s[20:23], s51 offen offset:0 sc0 sc1
Mz 76,4,196
Rt 36,17,8192
Cv 184,184,185
Mz 68,4,192
Rt 38,17,8704
Cv 185,186,187
Mz 80,8,192
Rt 40,18,8192
Cv 186,188,189
Mz 88,8,196
Rt 42,18,8704
buffer_load_dwordx4 v9, s[16:19], s92 offen offset:1024 lds
Mz 92,12,196
Rt 44,19,8192
Cv 187,190,191
v_mul_f32_e32 v32, s54, v32
Mz 84,12,192
Rt 46,19,8704
s_waitcnt lgkmcnt(8)
Mv 64,16,200
Rt 0,16,12288
Cv 192,192,193
Mv 72,16,204
Rt 2,16,12800
buffer_store_dwordx4 v[168:171], v2, s[20:23], s47 offen offset:1024 sc0 sc1
Mv 76,20,204
Rt 4,17,12288
Cv 193,194,195
Cv 194,196,197
Mv 68,20,200
Rt 6,17,12800
Cv 195,198,199
v_mul_f32_e32 v33, s54, v33
Mv 80,24,200
Rt 8,18,12288
Cv 200,200,201
Mv 88,24,204
Rt 10,18,12800
buffer_load_dwordx4 v10, s[16:19], s92 offen offset:2048 lds
Mv 92,28,204
Rt 12,19,12288
Cv 201,202,203
Cv 32,32,33
Mv 84,28,200
Rt 14,19,12800
Cv 202,204,205
s_waitcnt lgkmcnt(8)
Mv 64,32,208
Rt 16,16,16384
Cv 203,206,207
Mv 72,32,212
Rt 18,16,16896
)" R"(
buffer_load_dwordx4 a[100:103], v2, s[20:23], s51 offen offset:1024 sc0 sc1
Mv 76,36,212
Rt 20,17,16384
Cv 208,208,209
Mv 68,36,208
Rt 22,17,16896
Cv 209,210,211
v_accvgpr_write_b32 a224, v32
Mv 80,40,208
Rt 24,18,16384
Cv 210,212,213
Mv 88,40,212
Rt 26,18,16896
buffer_load_dwordx4 v11, s[16:19], s92 offen offset:3072 lds
Mv 92,44,212
Rt 28,19,16384
Cv 211,214,215
v_mul_f32_e32 v34, s54, v34
Mv 84,44,208
Rt 30,19,16896
s_waitcnt lgkmcnt(8)
Mv 64,0,216
Rt 32,16,20480
Cv 216,216,217
Mv 72,0,220
Rt 34,16,20992
buffer_store_dwordx4 v[176:179], v2, s[20:23], s47 offen offset:2048 sc0 sc1
Mv 76,4,220
Rt 36,17,20480
Cv 217,218,219
Cv 218,220,221
Mv 68,4,216
Rt 38,17,20992
s_add_u32 s92, s42, 0x40180
v_mul_f32_e32 v35, s54, v35
Mv 80,8,216
Rt 40,18,20480
s_add_u32 m0, s41, 4096
Mv 88,8,220
Rt 42,18,20992
buffer_load_dwordx4 v8, s[16:19], s92 offen offset:0 lds
Mv 92,12,220
Rt 44,19,20480
Cv 219,222,223
Cv 34,34,35
Mv 84,12,216
Rt 46,19,20992
s_waitcnt lgkmcnt(8)
Mv 64,16,128
Rt 0,16,24576
v_accvgpr_write_b32 a225, v34
Mv 72,16,132
Rt 2,16,25088
buffer_load_dwordx4 a[104:107], v2, s[20:23], s51 offen offset:2048 sc0 sc1
Mv 76,20,132
Rt 4,17,24576
v_mul_f32_e32 v36, s54, v36
Mv 68,20,128
Rt 6,17,25088
v_mul_f32_e32 v37, s54, v37
Cv 36,36,37
Mv 80,24,128
Rt 8,18,24576
v_accvgpr_write_b32 a226, v36
)" R"(
Mv 88,24,132
Rt 10,18,25088
buffer_load_dwordx4 v9, s[16:19], s92 offen offset:1024 lds
Mv 92,28,132
Rt 12,19,24576
v_mul_f32_e32 v38, s54, v38
v_mul_f32_e32 v39, s54, v39
Mv 84,28,128
Rt 14,19,25088
s_waitcnt lgkmcnt(8)
Mv 64,32,136
Rt 16,12,32768
buffer_store_dwordx4 v[184:187], v2, s[20:23], s47 offen offset:3072 sc0 sc1
Mv 72,32,140
Rt 18,12,33280
Cv 38,38,39
v_accvgpr_write_b32 a227, v38
Mv 76,36,140
Rt 20,13,32768
v_mul_f32_e32 v40, s54, v40
Mv 68,36,136
Rt 22,13,33280
buffer_load_dwordx4 a[108:111], v2, s[20:23], s51 offen offset:3072 sc0 sc1
Mv 80,40,136
Rt 24,14,32768
v_mul_f32_e32 v41, s54, v41
Mv 88,40,140
Rt 26,14,33280
buffer_load_dwordx4 v10, s[16:19], s92 offen offset:2048 lds
Mv 92,44,140
Rt 28,15,32768
Cv 40,40,41
v_accvgpr_write_b32 a228, v40
Mv 84,44,136
Rt 30,15,33280
s_waitcnt vmcnt(19) lgkmcnt(8)
s_barrier
buffer_store_dword v6, v7, s[20:23], s49 offen offset:4 sc0 sc1
Mv 64,0,144
Rt 32,12,36864
buffer_load_dword v4, v5, s[20:23], s50 offen offset:0 sc0 sc1
Mv 72,0,148
Rt 34,12,37376
v_mul_f32_e32 v42, s54, v42
v_mul_f32_e32 v43, s54, v43
Mv 76,4,148
Rt 36,13,36864
Cv 42,42,43
Mv 68,4,144
Rt 38,13,37376
s_add_u32 s92, s42, 0x1c0180
v_accvgpr_write_b32 a229, v42
Mv 80,8,144
Rt 40,14,36864
s_add_u32 m0, s41, 65536
Mv 88,8,148
Rt 42,14,37376
buffer_load_dwordx4 v8, s[16:19], s92 offen offset:0 lds
Mv 92,12,148
Rt 44,15,36864
v_mul_f32_e32 v44, s54, v44
v_mul_f32_e32 v45, s54, v45
Mv 84,12,144
Rt 46,15,37376
s_waitcnt lgkmcnt(8)
)" R"(
Mv 64,16,152
Rt 0,12,40960
Cv 44,44,45
Mv 72,16,156
Rt 2,12,41472
buffer_load_dwordx4 a[112:115], v3, s[20:23], s51 offen offset:0 sc0 sc1
Mv 76,20,156
Rt 4,13,40960
v_accvgpr_write_b32 a230, v44
Mv 68,20,152
Rt 6,13,41472
v_mul_f32_e32 v46, s54, v46
v_mul_f32_e32 v47, s54, v47
Mv 80,24,152
Rt 8,14,40960
Cv 46,46,47
Mv 88,24,156
Rt 10,14,41472
buffer_load_dwordx4 v9, s[16:19], s92 offen offset:1024 lds
Mv 92,28,156
Rt 12,15,40960
v_accvgpr_write_b32 a231, v46
v_mul_f32_e32 v48, s54, v48
Mv 84,28,152
Rt 14,15,41472
s_waitcnt lgkmcnt(8)
Mv 64,32,160
Rt 16,12,45056
v_mul_f32_e32 v49, s54, v49
Mv 72,32,164
Rt 18,12,45568
buffer_store_dwordx4 v[192:195], v3, s[20:23], s47 offen offset:0 sc0 sc1
Mv 76,36,164
Rt 20,13,45056
Cv 48,48,49
Mv 68,36,160
Rt 22,13,45568
v_accvgpr_write_b32 a232, v48
v_mul_f32_e32 v50, s54, v50
Mv 80,40,160
Rt 24,14,45056
v_mul_f32_e32 v51, s54, v51
Mv 88,40,164
Rt 26,14,45568
buffer_load_dwordx4 v10, s[16:19], s92 offen offset:2048 lds
Mv 92,44,164
Rt 28,15,45056
Cv 50,50,51
v_accvgpr_write_b32 a233, v50
Mv 84,44,160
Rt 30,15,45568
s_waitcnt lgkmcnt(8)
Mv 64,0,168
Rt 32,12,49152
v_mul_f32_e32 v52, s54, v52
Mv 72,0,172
Rt 34,12,49664
buffer_load_dwordx4 a[116:119], v3, s[20:23], s51 offen offset:1024 sc0 sc1
Mv 76,4,172
Rt 36,13,49152
v_mul_f32_e32 v53, s54, v53
Mv 68,4,168
Rt 38,13,49664
Cv 52,52,53
)" R"(
v_accvgpr_write_b32 a234, v52
Mv 80,8,168
Rt 40,14,49152
v_mul_f32_e32 v54, s54, v54
Mv 88,8,172
Rt 42,14,49664
buffer_load_dwordx4 v11, s[16:19], s92 offen offset:3072 lds
Mv 92,12,172
Rt 44,15,49152
v_mul_f32_e32 v55, s54, v55
Cv 54,54,55
Mv 84,12,168
Rt 46,15,49664
s_waitcnt lgkmcnt(8)
Mv 64,16,176
Rt 0,12,53248
v_accvgpr_write_b32 a235, v54
Mv 72,16,180
Rt 2,12,53760
buffer_store_dwordx4 v[200:203], v3, s[20:23], s47 offen offset:1024 sc0 sc1
Mv 76,20,180
Rt 4,13,53248
v_mul_f32_e32 v56, s54, v56
Mv 68,20,176
Rt 6,13,53760
s_add_u32 s92, s42, 0x200180
v_mul_f32_e32 v57, s54, v57
Mv 80,24,176
Rt 8,14,53248
s_add_u32 m0, s41, 69632
Mv 88,24,180
Rt 10,14,53760
buffer_load_dwordx4 v8, s[16:19], s92 offen offset:0 lds
Mv 92,28,180
Rt 12,15,53248
Cv 56,56,57
v_accvgpr_write_b32 a236, v56
Mv 84,28,176
Rt 14,15,53760
s_waitcnt lgkmcnt(8)
Mv 64,32,184
Rt 16,12,57344
v_mul_f32_e32 v58, s54, v58
Mv 72,32,188
Rt 18,12,57856
buffer_load_dwordx4 a[120:123], v3, s[20:23], s51 offen offset:2048 sc0 sc1
Mv 76,36,188
Rt 20,13,57344
v_mul_f32_e32 v59, s54, v59
Mv 68,36,184
Rt 22,13,57856
Cv 58,58,59
v_accvgpr_write_b32 a237, v58
Mv 80,40,184
Rt 24,14,57344
v_mul_f32_e32 v60, s54, v60
Mv 88,40,188
Rt 26,14,57856
buffer_load_dwordx4 v9, s[16:19], s92 offen offset:1024 lds
Mv 92,44,188
Rt 28,15,57344
v_mul_f32_e32 v61, s54, v61
Cv 60,60,61
Mv 84,44,184
)" R"(
Rt 30,15,57856
s_waitcnt lgkmcnt(8)
Mv 64,0,48
Rt 32,16,32768
buffer_store_dwordx4 v[208:211], v3, s[20:23], s47 offen offset:2048 sc0 sc1
Mv 72,0,52
Rt 34,16,33280
v_accvgpr_write_b32 a238, v60
v_mul_f32_e32 v62, s54, v62
Mv 76,4,52
Rt 36,17,32768
v_mul_f32_e32 v63, s54, v63
Mv 68,4,48
Rt 38,17,33280
buffer_load_dwordx4 a[124:127], v3, s[20:23], s51 offen offset:3072 sc0 sc1
Mv 80,8,48
Rt 40,18,32768
Cv 62,62,63
Mv 88,8,52
Rt 42,18,33280
buffer_load_dwordx4 v10, s[16:19], s92 offen offset:2048 lds
Mv 92,12,52
Rt 44,19,32768
v_accvgpr_write_b32 a239, v62
Mv 84,12,48
Rt 46,19,33280
s_waitcnt vmcnt(14) lgkmcnt(8)
s_barrier
Mv 64,16,56
Rt 0,16,36864
v_cmp_eq_u32_e32 vcc, s3, v4
s_andn2_b64 s[28:29], s[60:61], vcc
s_cbranch_scc1 .Lzsp5
.Lzok5:
Mv 72,16,60
Rt 2,16,37376
s_add_u32 s40, s55, 0x10000
Mv 76,20,60
Rt 4,17,36864
Mv 68,20,56
Rt 6,17,37376
Mv 80,24,56
Rt 8,18,36864
s_add_u32 m0, s36, 32768
Mv 88,24,60
Rt 10,18,37376
buffer_load_dwordx4 v2, s[20:23], s40 offen offset:0 sc1 lds
Mv 92,28,60
Rt 12,19,36864
Mv 84,28,56
Rt 14,19,37376
s_waitcnt lgkmcnt(8)
Mz 32,32,192
Rt 16,16,40960
buffer_store_dwordx4 v[216:219], v3, s[20:23], s47 offen offset:3072 sc0 sc1
Mz 40,32,196
Rt 18,16,41472
Mz 44,36,196
Rt 20,17,40960
Mz 36,36,192
Rt 22,17,41472
buffer_load_dwordx4 v2, s[20:23], s40 offen offset:1024 sc1 lds
Mz 48,40,192
Rt 24,18,40960
v_mul_f32_e32 v64, s54, v64
)" R"(
Mz 56,40,196
Rt 26,18,41472
v_mul_f32_e32 v65, s54, v65
Mz 60,44,196
Rt 28,19,40960
Cv 64,64,65
Mz 52,44,192
Rt 30,19,41472
s_waitcnt lgkmcnt(8)
Mv 32,0,200
Rt 32,16,45056
v_accvgpr_write_b32 a240, v64
Mv 40,0,204
Rt 34,16,45568
buffer_load_dwordx4 v2, s[20:23], s40 offen offset:2048 sc1 lds
Mv 44,4,204
Rt 36,17,45056
v_mul_f32_e32 v66, s54, v66
Mv 36,4,200
Rt 38,17,45568
v_mul_f32_e32 v67, s54, v67
Mv 48,8,200
Rt 40,18,45056
buffer_store_dwordx4 v[224:227], v2, s[20:23], s48 offen offset:0 sc0 sc1
Mv 56,8,204
Rt 42,18,45568
Cv 66,66,67
Mv 60,12,204
Rt 44,19,45056
v_accvgpr_write_b32 a241, v66
Mv 52,12,200
Rt 46,19,45568
buffer_load_dwordx4 v2, s[20:23], s40 offen offset:3072 sc1 lds
s_waitcnt lgkmcnt(8)
Mv 32,16,208
Rt 0,16,49152
v_mul_f32_e32 v68, s54, v68
Mv 40,16,212
Rt 2,16,49664
v_mul_f32_e32 v69, s54, v69
Mv 44,20,212
Rt 4,17,49152
Cv 68,68,69
Mv 36,20,208
Rt 6,17,49664
v_accvgpr_write_b32 a242, v68
Mv 48,24,208
Rt 8,18,49152
s_add_u32 m0, s36, 36864
Mv 56,24,212
Rt 10,18,49664
buffer_load_dwordx4 v3, s[20:23], s40 offen offset:0 sc1 lds
Mv 60,28,212
Rt 12,19,49152
v_mul_f32_e32 v70, s54, v70
Mv 52,28,208
Rt 14,19,49664
s_waitcnt lgkmcnt(8)
Mv 32,32,216
Rt 16,16,53248
buffer_store_dwordx4 v[232:235], v2, s[20:23], s48 offen offset:1024 sc0 sc1
Mv 40,32,220
Rt 18,16,53760
v_mul_f32_e32 v71, s54, v71
)" R"(
Mv 44,36,220
Rt 20,17,53248
Cv 70,70,71
Mv 36,36,216
Rt 22,17,53760
buffer_load_dwordx4 v3, s[20:23], s40 offen offset:1024 sc1 lds
Mv 48,40,216
Rt 24,18,53248
v_accvgpr_write_b32 a243, v70
Mv 56,40,220
Rt 26,18,53760
v_mul_f32_e32 v72, s54, v72
Mv 60,44,220
Rt 28,19,53248
v_mul_f32_e32 v73, s54, v73
Mv 52,44,216
Rt 30,19,53760
s_waitcnt lgkmcnt(8)
Mv 32,0,128
Rt 32,16,57344
Cv 72,72,73
Mv 40,0,132
Rt 34,16,57856
buffer_load_dwordx4 v3, s[20:23], s40 offen offset:2048 sc1 lds
Mv 44,4,132
Rt 36,17,57344
v_accvgpr_write_b32 a244, v72
Mv 36,4,128
Rt 38,17,57856
v_mul_f32_e32 v74, s54, v74
Mv 48,8,128
Rt 40,18,57344
buffer_store_dwordx4 v[240:243], v2, s[20:23], s48 offen offset:2048 sc0 sc1
Mv 56,8,132
Rt 42,18,57856
v_mul_f32_e32 v75, s54, v75
Mv 60,12,132
Rt 44,19,57344
Cv 74,74,75
Mv 52,12,128
Rt 46,19,57856
buffer_load_dwordx4 v3, s[20:23], s40 offen offset:3072 sc1 lds
s_waitcnt lgkmcnt(8)
Mv 32,16,136
Rt 0,20,0
v_accvgpr_write_b32 a245, v74
Mv 40,16,140
Rt 2,20,512
v_mul_f32_e32 v76, s54, v76
Mv 44,20,140
Rt 4,21,0
v_mul_f32_e32 v77, s54, v77
Mv 36,20,136
Rt 6,21,512
Cv 76,76,77
Mv 48,24,136
Rt 8,22,0
v_accvgpr_write_b32 a246, v76
Mv 56,24,140
Rt 10,22,512
v_mul_f32_e32 v78, s54, v78
Mv 60,28,140
Rt 12,23,0
v_mul_f32_e32 v79, s54, v79
)" R"(
Mv 52,28,136
Rt 14,23,512
s_waitcnt vmcnt(10) lgkmcnt(8)
s_barrier
buffer_store_dword v6, v7, s[20:23], s49 offen offset:8 sc0 sc1
Mv 32,32,144
Rt 16,20,4096
s_mov_b32 s83, 0xc108000
buffer_load_dwordx2 v[24:25], v7, s[20:23], s83 offen sc0 sc1
Mv 40,32,148
Rt 18,20,4608
Cv 78,78,79
Mv 44,36,148
Rt 20,21,4096
s_add_u32 m0, s36, 98304
Mv 36,36,144
Rt 22,21,4608
buffer_load_dwordx4 v2, s[20:23], s55 offen offset:0 sc1 lds
Mv 48,40,144
Rt 24,22,4096
v_accvgpr_write_b32 a247, v78
Mv 56,40,148
Rt 26,22,4608
v_mul_f32_e32 v80, s54, v80
Mv 60,44,148
Rt 28,23,4096
buffer_store_dwordx4 v[248:251], v2, s[20:23], s48 offen offset:3072 sc0 sc1
Mv 52,44,144
Rt 30,23,4608
s_waitcnt lgkmcnt(8)
Mv 32,0,152
Rt 32,20,8192
v_mul_f32_e32 v81, s54, v81
Mv 40,0,156
Rt 34,20,8704
buffer_load_dwordx4 v2, s[20:23], s55 offen offset:1024 sc1 lds
Mv 44,4,156
Rt 36,21,8192
Cv 80,80,81
Mv 36,4,152
Rt 38,21,8704
v_accvgpr_write_b32 a248, v80
Mv 48,8,152
Rt 40,22,8192
v_mul_f32_e32 v82, s54, v82
Mv 56,8,156
Rt 42,22,8704
v_mul_f32_e32 v83, s54, v83
Mv 60,12,156
Rt 44,23,8192
Cv 82,82,83
Mv 52,12,152
Rt 46,23,8704
buffer_load_dwordx4 v2, s[20:23], s55 offen offset:2048 sc1 lds
s_waitcnt lgkmcnt(8)
Mv 32,16,160
Rt 0,20,12288
v_accvgpr_write_b32 a249, v82
Mv 40,16,164
Rt 2,20,12800
v_mul_f32_e32 v84, s54, v84
Mv 44,20,164
Rt 4,21,12288
buffer_store_dwordx4 v[228:231], v3, s[20:23], s48 offen offset:0 sc0 sc1
Mv 36,20,160
)" R"(
Rt 6,21,12800
v_mul_f32_e32 v85, s54, v85
Mv 48,24,160
Rt 8,22,12288
Cv 84,84,85
Mv 56,24,164
Rt 10,22,12800
buffer_load_dwordx4 v2, s[20:23], s55 offen offset:3072 sc1 lds
Mv 60,28,164
Rt 12,23,12288
v_accvgpr_write_b32 a250, v84
Mv 52,28,160
Rt 14,23,12800
s_waitcnt lgkmcnt(8)
Mv 32,32,168
Rt 16,20,16384
v_mul_f32_e32 v86, s54, v86
Mv 40,32,172
Rt 18,20,16896
v_mul_f32_e32 v87, s54, v87
Mv 44,36,172
Rt 20,21,16384
s_add_u32 m0, s36, 102400
Mv 36,36,168
Rt 22,21,16896
buffer_load_dwordx4 v3, s[20:23], s55 offen offset:0 sc1 lds
Mv 48,40,168
Rt 24,22,16384
Cv 86,86,87
Mv 56,40,172
Rt 26,22,16896
v_accvgpr_write_b32 a251, v86
Mv 60,44,172
Rt 28,23,16384
buffer_store_dwordx4 v[236:239], v3, s[20:23], s48 offen offset:1024 sc0 sc1
Mv 52,44,168
Rt 30,23,16896
s_waitcnt lgkmcnt(8)
Mv 32,0,176
Rt 32,20,20480
v_mul_f32_e32 v88, s54, v88
Mv 40,0,180
Rt 34,20,20992
buffer_load_dwordx4 v3, s[20:23], s55 offen offset:1024 sc1 lds
Mv 44,4,180
Rt 36,21,20480
v_mul_f32_e32 v89, s54, v89
Mv 36,4,176
Rt 38,21,20992
Cv 88,88,89
Mv 48,8,176
Rt 40,22,20480
v_accvgpr_write_b32 a252, v88
Mv 56,8,180
Rt 42,22,20992
v_mul_f32_e32 v90, s54, v90
Mv 60,12,180
Rt 44,23,20480
v_mul_f32_e32 v91, s54, v91
Mv 52,12,176
Rt 46,23,20992
buffer_load_dwordx4 v3, s[20:23], s55 offen offset:2048 sc1 lds
s_waitcnt lgkmcnt(8)
Mv 32,16,184
)" R"(
Rt 0,20,24576
Cv 90,90,91
Mv 40,16,188
Rt 2,20,25088
v_accvgpr_write_b32 a253, v90
Mv 44,20,188
Rt 4,21,24576
buffer_store_dwordx4 v[244:247], v3, s[20:23], s48 offen offset:2048 sc0 sc1
Mv 36,20,184
Rt 6,21,25088
v_mul_f32_e32 v92, s54, v92
Mv 48,24,184
Rt 8,22,24576
v_mul_f32_e32 v93, s54, v93
Mv 56,24,188
Rt 10,22,25088
buffer_load_dwordx4 v3, s[20:23], s55 offen offset:3072 sc1 lds
Mv 60,28,188
Rt 12,23,24576
Cv 92,92,93
Mv 52,28,184
Rt 14,23,25088
s_waitcnt lgkmcnt(8)
Mv 32,32,48
Rt 16,12,0
v_accvgpr_write_b32 a254, v92
Mv 40,32,52
Rt 18,12,512
v_mul_f32_e32 v94, s54, v94
Mv 44,36,52
Rt 20,13,0
v_mul_f32_e32 v95, s54, v95
Mv 36,36,48
Rt 22,13,512
Cv 94,94,95
Mv 48,40,48
Rt 24,14,0
buffer_store_dwordx4 v[252:255], v3, s[20:23], s48 offen offset:3072 sc0 sc1
Mv 56,40,52
Rt 26,14,512
v_accvgpr_write_b32 a255, v94
Mv 60,44,52
Rt 28,15,0
Mv 52,44,48
Rt 30,15,512
s_waitcnt vmcnt(13) lgkmcnt(8)
s_barrier
v_cmp_eq_u32_e32 vcc, s3, v25
s_andn2_b64 s[28:29], 1, vcc
s_cbranch_scc1 .Lzsp7
.Lzok7:
Mv 32,0,56
Rt 32,12,4096
buffer_load_dword v4, v5, s[20:23], s50 offen offset:0 sc0 sc1
Mv 40,0,60
Rt 34,12,4608
s_lshl_b64 s[60:61], s[60:61], 8
Mv 44,4,60
Rt 36,13,4096
s_add_u32 m0, s36, 131072
Mv 36,4,56
Rt 38,13,4608
buffer_load_dwordx4 v2, s[20:23], s56 offen offset:0 sc1 lds
Mv 48,8,56
Rt 40,14,4096
)" R"(
s_or_b32 s60, s60, 0xff
Mv 56,8,60
Rt 42,14,4608
Mv 60,12,60
Rt 44,15,4096
Mv 52,12,56
Rt 46,15,4608
s_waitcnt lgkmcnt(8)
Mz 96,16,192
Rt 0,12,8192
Mz 104,16,196
Rt 2,12,8704
buffer_load_dwordx4 v2, s[20:23], s56 offen offset:1024 sc1 lds
Mz 108,20,196
Rt 4,13,8192
Mz 100,20,192
Rt 6,13,8704
Mz 112,24,192
Rt 8,14,8192
v_mul_f32_e32 v32, s54, v32
Mz 120,24,196
Rt 10,14,8704
v_mul_f32_e32 v33, s54, v33
Mz 124,28,196
Rt 12,15,8192
Cv 32,32,33
Mz 116,28,192
Rt 14,15,8704
buffer_load_dwordx4 v2, s[20:23], s56 offen offset:2048 sc1 lds
s_waitcnt lgkmcnt(8)
Mv 96,32,200
Rt 16,12,12288
v_mul_f32_e32 v34, s54, v34
Mv 104,32,204
Rt 18,12,12800
v_accvgpr_write_b32 a192, v32
Mv 108,36,204
Rt 20,13,12288
v_mul_f32_e32 v35, s54, v35
Mv 100,36,200
Rt 22,13,12800
Cv 34,34,35
v_accvgpr_write_b32 a193, v34
Mv 112,40,200
Rt 24,14,12288
v_mul_f32_e32 v36, s54, v36
Mv 120,40,204
Rt 26,14,12800
buffer_load_dwordx4 v2, s[20:23], s56 offen offset:3072 sc1 lds
Mv 124,44,204
Rt 28,15,12288
v_mul_f32_e32 v37, s54, v37
Mv 116,44,200
Rt 30,15,12800
s_waitcnt lgkmcnt(8)
Mv 96,0,208
Rt 32,12,16384
Cv 36,36,37
Mv 104,0,212
Rt 34,12,16896
v_accvgpr_write_b32 a194, v36
Mv 108,4,212
Rt 36,13,16384
s_add_u32 m0, s36, 135168
)" R"(
Mv 100,4,208
Rt 38,13,16896
buffer_load_dwordx4 v3, s[20:23], s56 offen offset:0 sc1 lds
Mv 112,8,208
Rt 40,14,16384
v_mul_f32_e32 v38, s54, v38
Mv 120,8,212
Rt 42,14,16896
v_mul_f32_e32 v39, s54, v39
Mv 124,12,212
Rt 44,15,16384
Cv 38,38,39
Mv 116,12,208
Rt 46,15,16896
s_waitcnt lgkmcnt(8)
Mv 96,16,216
Rt 0,12,20480
v_accvgpr_write_b32 a195, v38
Mv 104,16,220
Rt 2,12,20992
buffer_load_dwordx4 v3, s[20:23], s56 offen offset:1024 sc1 lds
Mv 108,20,220
Rt 4,13,20480
v_mul_f32_e32 v40, s54, v40
Mv 100,20,216
Rt 6,13,20992
v_mul_f32_e32 v41, s54, v41
Cv 40,40,41
Mv 112,24,216
Rt 8,14,20480
v_accvgpr_write_b32 a196, v40
Mv 120,24,220
Rt 10,14,20992
v_mul_f32_e32 v42, s54, v42
Mv 124,28,220
Rt 12,15,20480
v_mul_f32_e32 v43, s54, v43
Mv 116,28,216
Rt 14,15,20992
buffer_load_dwordx4 v3, s[20:23], s56 offen offset:2048 sc1 lds
s_waitcnt lgkmcnt(8)
Mv 96,32,128
Rt 16,12,24576
Cv 42,42,43
Mv 104,32,132
Rt 18,12,25088
v_accvgpr_write_b32 a197, v42
Mv 108,36,132
Rt 20,13,24576
v_mul_f32_e32 v44, s54, v44
Mv 100,36,128
Rt 22,13,25088
v_mul_f32_e32 v45, s54, v45
Cv 44,44,45
Mv 112,40,128
Rt 24,14,24576
v_accvgpr_write_b32 a198, v44
Mv 120,40,132
Rt 26,14,25088
buffer_load_dwordx4 v3, s[20:23], s56 offen offset:3072 sc1 lds
Mv 124,44,132
Rt 28,15,24576
v_mul_f32_e32 v46, s54, v46
Mv 116,44,128
)" R"(
Rt 30,15,25088
s_waitcnt lgkmcnt(8)
Mv 96,0,136
Rt 32,16,0
v_readfirstlane_b32 s63, v24
Mv 104,0,140
Rt 34,16,512
v_and_b32 v24, 16, v1
Mv 108,4,140
Rt 36,17,0
v_lshrrev_b32_e64 v24, v24, s63
Mv 100,4,136
Rt 38,17,512
v_and_b32 v24, 0x7ff, v24
v_mul_u32_u24 v24, 896, v24
Mv 112,8,136
Rt 40,18,0
v_and_b32 v25, 14, v0
Mv 120,8,140
Rt 42,18,512
v_min_u32 v25, 1, v25
Mv 124,12,140
Rt 44,19,0
v_lshlrev_b32 v25, 30, v25
v_and_or_b32 v25, v0, 48, v25
Mv 116,12,136
Rt 46,19,512
s_waitcnt vmcnt(8) lgkmcnt(8)
s_barrier
buffer_store_dword v6, v7, s[20:23], s49 offen offset:12 sc0 sc1
Mv 96,16,144
Rt 0,16,4096
v_cmp_eq_u32_e32 vcc, s3, v4
s_andn2_b64 s[28:29], s[60:61], vcc
s_cbranch_scc1 .Lzsp6
.Lzok6:
Mv 104,16,148
Rt 2,16,4608
v_add_u32 v24, v24, v25
v_add_u32 v8, 0x10000, v1
Mv 108,20,148
Rt 4,17,4096
s_and_b32 s83, s33, 1
v_add_u32 v10, 0x20000, v1
Mv 100,20,144
Rt 6,17,4608
s_mul_i32 s82, s83, 448
v_mul_f32_e32 v47, s54, v47
s_add_u32 s56, s56, 0x10000
Mv 112,24,144
Rt 8,18,4096
Cv 46,46,47
s_add_u32 s55, s55, 0x10000
Mv 120,24,148
Rt 10,18,4608
v_accvgpr_write_b32 a199, v46
s_add_u32 s55, s55, 0x10000
Mv 124,28,148
Rt 12,19,4096
v_mul_f32_e32 v48, s54, v48
s_cmp_eq_u32 s57, 2
s_cselect_b32 s55, s59, s55
Mv 116,28,144
Rt 14,19,4608
s_mov_b32 s93, 0
)" R"(
s_waitcnt lgkmcnt(8)
Mv 96,32,152
Rt 16,16,8192
v_mul_f32_e32 v49, s54, v49
s_mov_b32 s94, 1
Mv 104,32,156
Rt 18,16,8704
Cv 48,48,49
Mv 108,36,156
Rt 20,17,8192
v_accvgpr_write_b32 a200, v48
Mv 100,36,152
Rt 22,17,8704
v_mul_f32_e32 v50, s54, v50
v_mul_f32_e32 v51, s54, v51
Mv 112,40,152
Rt 24,18,8192
Cv 50,50,51
Mv 120,40,156
Rt 26,18,8704
v_accvgpr_write_b32 a201, v50
Mv 124,44,156
Rt 28,19,8192
v_mul_f32_e32 v52, s54, v52
Mv 116,44,152
Rt 30,19,8704
s_waitcnt lgkmcnt(8)
Mv 96,0,160
Rt 32,16,12288
v_mul_f32_e32 v53, s54, v53
Mv 104,0,164
Rt 34,16,12800
Cv 52,52,53
Mv 108,4,164
Rt 36,17,12288
v_accvgpr_write_b32 a202, v52
Mv 100,4,160
Rt 38,17,12800
buffer_load_dwordx4 a[128:131], v24, s[12:15], s82 offen offset:0
Mv 112,8,160
Rt 40,18,12288
v_mul_f32_e32 v54, s54, v54
Mv 120,8,164
Rt 42,18,12800
v_mul_f32_e32 v55, s54, v55
Mv 124,12,164
Rt 44,19,12288
Cv 54,54,55
Mv 116,12,160
Rt 46,19,12800
s_waitcnt lgkmcnt(8)
Mv 96,16,168
Rt 0,16,16384
buffer_load_dwordx4 a[132:135], v24, s[12:15], s82 offen offset:64
Mv 104,16,172
Rt 2,16,16896
v_accvgpr_write_b32 a203, v54
Mv 108,20,172
Rt 4,17,16384
v_mul_f32_e32 v56, s54, v56
Mv 100,20,168
Rt 6,17,16896
v_mul_f32_e32 v57, s54, v57
Cv 56,56,57
)" R"(
Mv 112,24,168
Rt 8,18,16384
v_accvgpr_write_b32 a204, v56
Mv 120,24,172
Rt 10,18,16896
buffer_load_dwordx4 a[136:139], v24, s[12:15], s82 offen offset:128
Mv 124,28,172
Rt 12,19,16384
v_mul_f32_e32 v58, s54, v58
Mv 116,28,168
Rt 14,19,16896
s_waitcnt lgkmcnt(8)
Mv 96,32,176
Rt 16,16,20480
buffer_load_dwordx4 a[140:143], v24, s[12:15], s82 offen offset:192
Mv 104,32,180
Rt 18,16,20992
v_mul_f32_e32 v59, s54, v59
Mv 108,36,180
Rt 20,17,20480
Cv 58,58,59
Mv 100,36,176
Rt 22,17,20992
v_accvgpr_write_b32 a205, v58
v_mul_f32_e32 v60, s54, v60
Mv 112,40,176
Rt 24,18,20480
v_mul_f32_e32 v61, s54, v61
Mv 120,40,180
Rt 26,18,20992
buffer_load_dwordx4 a[144:147], v24, s[12:15], s82 offen offset:256
Mv 124,44,180
Rt 28,19,20480
Cv 60,60,61
Mv 116,44,176
Rt 30,19,20992
s_waitcnt lgkmcnt(8)
s_barrier
Mv 96,0,184
Rt 32,16,24576
buffer_load_dwordx4 a[148:151], v24, s[12:15], s82 offen offset:320
Mv 104,0,188
Rt 34,16,25088
v_accvgpr_write_b32 a206, v60
Mv 108,4,188
Rt 36,17,24576
v_mul_f32_e32 v62, s54, v62
Mv 100,4,184
Rt 38,17,25088
v_mul_f32_e32 v63, s54, v63
Cv 62,62,63
Mv 112,8,184
Rt 40,18,24576
v_accvgpr_write_b32 a207, v62
Mv 120,8,188
Rt 42,18,25088
buffer_load_dwordx4 a[152:155], v24, s[12:15], s82 offen offset:384
Mv 124,12,188
Rt 44,19,24576
Mv 116,12,184
Rt 46,19,25088
s_waitcnt lgkmcnt(8)
Mv 96,16,48
Rb 0,8,32768
Mv 104,16,52
)" R"(
Rb 4,8,33792
Mv 108,20,52
Rb 8,8,34816
Mv 100,20,48
Rb 12,8,35840
Mv 112,24,48
Rb 16,8,40960
Rb 20,8,41984
Mv 120,24,52
Rb 24,8,43008
Mv 124,28,52
Mv 116,28,48
Rb 28,8,44032
s_waitcnt lgkmcnt(8)
Mv 96,32,56
Mv 104,32,60
Rb 32,8,36864
Mv 108,36,60
Mv 100,36,56
Rb 36,8,37888
Mv 112,40,56
Mv 120,40,60
Rb 40,8,38912
Mv 124,44,60
Mv 116,44,56
Rb 44,8,39936
s_cmp_eq_u32 s35, 7
s_cbranch_scc1 .LzA7
.LzA7R:
s_waitcnt lgkmcnt(8)
Mz 64,0,64
Rb 48,8,45056
v_mul_f32_e32 v96, s54, v96
Mz 68,0,68
v_mul_f32_e32 v97, s54, v97
Cv 96,96,97
Mv 68,4,76
Rb 52,8,46080
s_add_u32 m0, s36, 0
Mv 64,4,72
buffer_load_dwordx4 v2, s[20:23], s55 offen offset:0 sc1 lds
Mv 64,8,80
Rb 56,8,47104
v_accvgpr_write_b32 a208, v96
Mv 68,8,84
v_mul_f32_e32 v98, s54, v98
v_mul_f32_e32 v99, s54, v99
Mv 68,12,92
Rb 60,8,48128
s_waitcnt lgkmcnt(8)
Mv 64,12,88
Cv 98,98,99
v_accvgpr_write_b32 a209, v98
Mv 64,16,96
Rb 0,8,49152
v_mul_f32_e32 v100, s54, v100
Mv 68,16,100
v_mul_f32_e32 v101, s54, v101
Cv 100,100,101
Mv 68,20,108
Rb 4,8,50176
v_accvgpr_write_b32 a210, v100
Mv 64,20,104
buffer_load_dwordx4 v2, s[20:23], s55 offen offset:1024 sc1 lds
)" R"(
Mv 64,24,112
Rb 8,8,51200
v_mul_f32_e32 v102, s54, v102
Mv 68,24,116
v_mul_f32_e32 v103, s54, v103
Cv 102,102,103
Mv 68,28,124
Rb 12,8,52224
s_waitcnt lgkmcnt(8)
Mv 64,28,120
v_accvgpr_write_b32 a211, v102
v_mul_f32_e32 v104, s54, v104
v_mul_f32_e32 v105, s54, v105
Mz 72,32,64
Rb 16,8,57344
Cv 104,104,105
Mz 76,32,68
v_accvgpr_write_b32 a212, v104
v_mul_f32_e32 v106, s54, v106
Mv 76,36,76
Rb 20,8,58368
v_mul_f32_e32 v107, s54, v107
Mv 72,36,72
buffer_load_dwordx4 v2, s[20:23], s55 offen offset:2048 sc1 lds
Mv 72,40,80
Rb 24,8,59392
Cv 106,106,107
Mv 76,40,84
v_accvgpr_write_b32 a213, v106
v_mul_f32_e32 v108, s54, v108
Mv 76,44,92
Rb 28,8,60416
s_waitcnt lgkmcnt(8)
Mv 72,44,88
v_mul_f32_e32 v109, s54, v109
Cv 108,108,109
Mv 72,48,96
Rb 32,8,53248
v_accvgpr_write_b32 a214, v108
Mv 76,48,100
v_mul_f32_e32 v110, s54, v110
v_mul_f32_e32 v111, s54, v111
Mv 76,52,108
Rb 36,8,54272
Cv 110,110,111
Mv 72,52,104
buffer_load_dwordx4 v2, s[20:23], s55 offen offset:3072 sc1 lds
Mv 72,56,112
Rb 40,8,55296
v_accvgpr_write_b32 a215, v110
Mv 76,56,116
v_mul_f32_e32 v112, s54, v112
v_mul_f32_e32 v113, s54, v113
Mv 76,60,124
Rb 44,8,56320
s_waitcnt lgkmcnt(8)
Mv 72,60,120
Cv 112,112,113
v_accvgpr_write_b32 a216, v112
v_mul_f32_e32 v114, s54, v114
Mz 80,0,64
Rb 48,8,61440
v_mul_f32_e32 v115, s54, v115
Mz 84,0,68
)" R"(
Cv 114,114,115
v_accvgpr_write_b32 a217, v114
Mv 84,4,76
Rb 52,8,62464
s_add_u32 m0, s36, 4096
Mv 80,4,72
buffer_load_dwordx4 v3, s[20:23], s55 offen offset:0 sc1 lds
Mv 80,8,80
Rb 56,8,63488
v_mul_f32_e32 v116, s54, v116
Mv 84,8,84
v_mul_f32_e32 v117, s54, v117
Cv 116,116,117
Mv 84,12,92
Rb 60,8,64512
s_waitcnt lgkmcnt(8)
Mv 80,12,88
v_accvgpr_write_b32 a218, v116
v_mul_f32_e32 v118, s54, v118
Mv 80,16,96
v_mul_f32_e32 v119, s54, v119
Cv 118,118,119
Mv 84,16,100
v_accvgpr_write_b32 a219, v118
v_mul_f32_e32 v120, s54, v120
Mv 84,20,108
v_mul_f32_e32 v121, s54, v121
Cv 120,120,121
Mv 80,20,104
buffer_load_dwordx4 v3, s[20:23], s55 offen offset:1024 sc1 lds
Mv 80,24,112
v_accvgpr_write_b32 a220, v120
v_mul_f32_e32 v122, s54, v122
Mv 84,24,116
v_mul_f32_e32 v123, s54, v123
Cv 122,122,123
Mv 84,28,124
v_accvgpr_write_b32 a221, v122
v_mul_f32_e32 v124, s54, v124
Mv 80,28,120
v_mul_f32_e32 v125, s54, v125
s_waitcnt lgkmcnt(0)
s_barrier
Mz 88,32,64
Rb 0,1,32768
Cv 124,124,125
Mz 92,32,68
Rb 4,1,33792
v_accvgpr_write_b32 a222, v124
Mv 92,36,76
Rb 8,1,34816
v_mul_f32_e32 v126, s54, v126
Mv 88,36,72
Rb 12,1,35840
buffer_load_dwordx4 v3, s[20:23], s55 offen offset:2048 sc1 lds
Mv 88,40,80
Rb 16,1,40960
v_mul_f32_e32 v127, s54, v127
Mv 92,40,84
Rb 20,1,41984
Cv 126,126,127
Mv 92,44,92
Rb 24,1,43008
v_accvgpr_write_b32 a223, v126
Mv 88,44,88
)" R"(
Rb 28,1,44032
v_mov_b32 v16, 0
Mv 88,48,96
Rb 32,1,36864
v_mov_b32 v17, 0
Mv 92,48,100
Rb 36,1,37888
v_mov_b32 v18, 0
Mv 92,52,108
Rb 40,1,38912
v_mov_b32 v19, 0
Mv 88,52,104
Rb 44,1,39936
buffer_load_dwordx4 v3, s[20:23], s55 offen offset:3072 sc1 lds
Mv 88,56,112
v_mov_b32 v20, 0
v_mov_b32 v21, 0
Mv 92,56,116
v_mov_b32 v22, 0
v_mov_b32 v23, 0
Mv 92,60,124
s_waitcnt lgkmcnt(8)
Mv 88,60,120
Mz 96,0,64
Rb 48,1,45056
buffer_load_dword v4, v5, s[20:23], s50 offen offset:0 sc0 sc1
Mz 100,0,68
s_lshl_b64 s[60:61], s[60:61], 8
s_or_b32 s60, s60, 0xff
s_cmp_le_i32 s57, s58
s_cbranch_scc1 .Lzmk0
.Lzmr0:
Mv 100,4,76
Rb 52,1,46080
s_add_u32 m0, s36, 65536
s_sub_u32 s57, s57, 1
Mv 96,4,72
buffer_load_dwordx4 v2, s[20:23], s56 offen offset:0 sc1 lds
s_add_u32 s55, s55, 0x10000
Mv 96,8,80
Rb 56,1,47104
s_cmp_eq_u32 s57, 2
s_cselect_b32 s55, s59, s55
Mv 100,8,84
v_exp_f32_e32 v32, v64
Mv 100,12,92
Rb 60,1,48128
s_waitcnt lgkmcnt(8)
Mv 96,12,88
v_exp_f32_e32 v24, v65
Mv 96,16,96
Rb 0,1,49152
v_exp_f32_e32 v33, v66
Mv 100,16,100
v_exp_f32_e32 v25, v67
v_exp_f32_e32 v34, v72
Mv 100,20,108
Rb 4,1,50176
v_exp_f32_e32 v26, v73
Mv 96,20,104
buffer_load_dwordx4 v2, s[20:23], s56 offen offset:1024 sc1 lds
v_add_f32_e32 v14, v32, v24
Mv 96,24,112
Rb 8,1,51200
)" R"(
v_exp_f32_e32 v35, v74
Mv 100,24,116
v_exp_f32_e32 v27, v75
Cv 32,32,24
Mv 100,28,124
Rb 12,1,52224
v_add_f32_e32 v14, v14, v33
s_waitcnt lgkmcnt(8)
Mv 96,28,120
v_exp_f32_e32 v36, v68
v_add_f32_e32 v14, v14, v25
Mz 104,32,64
Rb 16,1,57344
v_exp_f32_e32 v28, v69
Mz 108,32,68
v_exp_f32_e32 v37, v70
v_exp_f32_e32 v29, v71
Mv 108,36,76
Rb 20,1,58368
v_exp_f32_e32 v38, v76
Mv 104,36,72
buffer_load_dwordx4 v2, s[20:23], s56 offen offset:2048 sc1 lds
Cv 33,33,25
Mv 104,40,80
Rb 24,1,59392
v_exp_f32_e32 v24, v77
Mv 108,40,84
v_exp_f32_e32 v39, v78
v_add_f32_e32 v14, v14, v34
Mv 108,44,92
Rb 28,1,60416
v_add_f32_e32 v14, v14, v26
s_waitcnt lgkmcnt(8)
Mv 104,44,88
v_exp_f32_e32 v25, v79
Cv 34,34,26
Mv 104,48,96
Rb 32,1,53248
v_exp_f32_e32 v40, v80
Mv 108,48,100
v_exp_f32_e32 v26, v81
v_add_f32_e32 v14, v14, v35
v_add_f32_e32 v14, v14, v27
Mv 108,52,108
Rb 36,1,54272
Cv 35,35,27
v_add_f32_e32 v15, v36, v28
Mv 104,52,104
buffer_load_dwordx4 v2, s[20:23], s56 offen offset:3072 sc1 lds
Cv 36,36,28
Mv 104,56,112
Rb 40,1,55296
v_exp_f32_e32 v41, v82
Mv 108,56,116
v_exp_f32_e32 v27, v83
v_add_f32_e32 v15, v15, v37
Mv 108,60,124
Rb 44,1,56320
v_add_f32_e32 v15, v15, v29
s_waitcnt lgkmcnt(8)
Mv 104,60,120
v_exp_f32_e32 v42, v88
Cv 37,37,29
Mz 112,0,64
)" R"(
Rb 48,1,61440
v_exp_f32_e32 v28, v89
Mz 116,0,68
v_exp_f32_e32 v43, v90
v_add_f32_e32 v15, v15, v38
v_add_f32_e32 v15, v15, v24
Mv 116,4,76
Rb 52,1,62464
s_add_u32 m0, s36, 69632
Cv 38,38,24
Mv 112,4,72
buffer_load_dwordx4 v3, s[20:23], s56 offen offset:0 sc1 lds
v_add_f32_e32 v15, v15, v39
Mv 112,8,80
Rb 56,1,63488
v_exp_f32_e32 v29, v91
Mv 116,8,84
v_exp_f32_e32 v44, v84
v_add_f32_e32 v15, v15, v25
Mv 116,12,92
Rb 60,1,64512
Cv 39,39,25
s_waitcnt lgkmcnt(8)
Mv 112,12,88
v_exp_f32_e32 v24, v85
v_add_f32_e32 v14, v14, v40
Mv 112,16,96
v_exp_f32_e32 v45, v86
v_add_f32_e32 v14, v14, v26
Mv 116,16,100
v_exp_f32_e32 v25, v87
Cv 40,40,26
v_add_f32_e32 v14, v14, v41
Mv 116,20,108
v_exp_f32_e32 v46, v92
v_add_f32_e32 v14, v14, v27
Mv 112,20,104
buffer_load_dwordx4 v3, s[20:23], s56 offen offset:1024 sc1 lds
Cv 41,41,27
Mv 112,24,112
v_exp_f32_e32 v26, v93
v_add_f32_e32 v14, v14, v42
Mv 116,24,116
v_exp_f32_e32 v47, v94
v_add_f32_e32 v14, v14, v28
Mv 116,28,124
v_exp_f32_e32 v27, v95
Cv 42,42,28
Mv 112,28,120
v_add_f32_e32 v14, v14, v43
s_waitcnt vmcnt(23) lgkmcnt(0)
s_barrier
Mz 120,32,64
Rb 0,10,0
v_add_f32_e32 v14, v14, v29
Cv 43,43,29
Mz 124,32,68
Rb 4,10,1024
v_add_f32_e32 v15, v15, v44
v_add_f32_e32 v15, v15, v24
Cv 44,44,24
Mv 124,36,76
Rb 8,10,2048
v_add_f32_e32 v15, v15, v45
v_add_f32_e32 v15, v15, v25
)" R"(
Mv 120,36,72
Rb 12,10,3072
buffer_load_dwordx4 v3, s[20:23], s56 offen offset:2048 sc1 lds
Mv 120,40,80
Rb 16,10,4096
Cv 45,45,25
v_add_f32_e32 v15, v15, v46
Mv 124,40,84
Rb 20,10,5120
v_add_f32_e32 v15, v15, v26
Cv 46,46,26
Mv 124,44,92
Rb 24,10,6144
v_add_f32_e32 v15, v15, v47
v_add_f32_e32 v15, v15, v27
Mv 120,44,88
Rb 28,10,7168
Cv 47,47,27
v_max_f32_e32 v11, v14, v15
Mv 120,48,96
Rb 32,10,8192
v_mov_b32 v24, v14
v_mov_b32 v25, v15
Mv 124,48,100
Rb 36,10,9216
v_permlane32_swap_b32_e32 v24, v25
v_add_f32_e32 v24, v24, v25
v_mov_b32 v25, v24
Mv 124,52,108
Rb 40,10,10240
v_permlane16_swap_b32_e32 v24, v25
v_add_f32_e32 v24, v24, v25
Mv 120,52,104
Rb 44,10,11264
buffer_load_dwordx4 v3, s[20:23], s56 offen offset:3072 sc1 lds
Mv 120,56,112
v_rcp_f32_e32 v24, v24
Mv 124,56,116
s_add_u32 s56, s56, 0x10000
v_max_f32_e32 v11, v11, v24
v_cmp_nge_f32_e32 vcc, s62, v11
Mv 124,60,124
s_waitcnt lgkmcnt(8)
s_cbranch_vccnz .Lzfx0
.Lzfr0:
Mv 120,60,120
v_mov_b32 v12, v14
v_mov_b32 v13, v15
v_mfma_f32_16x16x32_bf16 v[128:131], a[0:3], v[32:35], 0
Rb 48,10,12288
v_mfma_f32_16x16x32_bf16 v[136:139], a[0:3], v[36:39], 0
s_cmp_le_i32 s57, s58
s_cbranch_scc1 .Lzmk1
.Lzmr1:
v_mfma_f32_16x16x32_bf16 v[140:143], a[4:7], v[36:39], 0
Rb 52,10,13312
s_add_u32 m0, s36, 32768
v_mfma_f32_16x16x32_bf16 v[132:135], a[4:7], v[32:35], 0
buffer_load_dwordx4 v2, s[20:23], s55 offen offset:0 sc1 lds
v_mfma_f32_16x16x32_bf16 v[144:147], a[8:11], v[32:35], 0
Rb 56,10,14336
s_and_b32 s92, s2, 7
v_mfma_f32_16x16x32_bf16 v[152:155], a[8:11], v[36:39], 0
s_mul_i32 s92, s92, 14
)" R"(
s_lshr_b32 s82, s2, 3
v_mfma_f32_16x16x32_bf16 v[156:159], a[12:15], v[36:39], 0
Rb 60,10,15360
s_waitcnt lgkmcnt(8)
v_mfma_f32_16x16x32_bf16 v[148:151], a[12:15], v[32:35], 0
buffer_load_dwordx4 v2, s[20:23], s55 offen offset:1024 sc1 lds
v_mfma_f32_16x16x32_bf16 v[160:163], a[16:19], v[32:35], 0
Rb 0,10,16384
s_add_u32 s82, s82, s92
v_mfma_f32_16x16x32_bf16 v[168:171], a[16:19], v[36:39], 0
v_exp_f32_e32 v48, v96
v_mfma_f32_16x16x32_bf16 v[172:175], a[20:23], v[36:39], 0
Rb 4,10,17408
v_mfma_f32_16x16x32_bf16 v[164:167], a[20:23], v[32:35], 0
buffer_load_dwordx4 v2, s[20:23], s55 offen offset:2048 sc1 lds
v_mfma_f32_16x16x32_bf16 v[176:179], a[24:27], v[32:35], 0
Rb 8,10,18432
v_exp_f32_e32 v28, v97
v_mfma_f32_16x16x32_bf16 v[184:187], a[24:27], v[36:39], 0
v_exp_f32_e32 v49, v98
v_mfma_f32_16x16x32_bf16 v[188:191], a[28:31], v[36:39], 0
Rb 12,10,19456
s_waitcnt lgkmcnt(8)
v_mfma_f32_16x16x32_bf16 v[180:183], a[28:31], v[32:35], 0
buffer_load_dwordx4 v2, s[20:23], s55 offen offset:3072 sc1 lds
v_add_f32_e32 v14, v48, v28
v_mfma_f32_16x16x32_bf16 v[192:195], a[32:35], v[32:35], 0
Rb 16,10,20480
Cv 48,48,28
v_mfma_f32_16x16x32_bf16 v[200:203], a[32:35], v[36:39], 0
v_exp_f32_e32 v29, v99
v_add_f32_e32 v14, v14, v49
v_mfma_f32_16x16x32_bf16 v[204:207], a[36:39], v[36:39], 0
Rb 20,10,21504
s_add_u32 m0, s36, 36864
v_mfma_f32_16x16x32_bf16 v[196:199], a[36:39], v[32:35], 0
buffer_load_dwordx4 v3, s[20:23], s55 offen offset:0 sc1 lds
v_mfma_f32_16x16x32_bf16 v[208:211], a[40:43], v[32:35], 0
Rb 24,10,22528
v_exp_f32_e32 v50, v104
v_mfma_f32_16x16x32_bf16 v[216:219], a[40:43], v[36:39], 0
s_cmp_gt_u32 s33, 1
s_cselect_b32 s24, s78, s76
s_cselect_b32 s25, s79, s77
v_mfma_f32_16x16x32_bf16 v[220:223], a[44:47], v[36:39], 0
Rb 28,10,23552
s_waitcnt lgkmcnt(8)
v_mfma_f32_16x16x32_bf16 v[212:215], a[44:47], v[32:35], 0
buffer_load_dwordx4 v3, s[20:23], s55 offen offset:1024 sc1 lds
v_add_f32_e32 v14, v14, v29
v_mfma_f32_16x16x32_bf16 v[224:227], a[48:51], v[32:35], 0
Rb 32,10,24576
s_mov_b32 s27, 0x20000
v_mfma_f32_16x16x32_bf16 v[232:235], a[48:51], v[36:39], 0
s_cmp_lt_u32 s35, 7
s_cselect_b32 s26, 0x188000, 0
Cv 49,49,29
v_mfma_f32_16x16x32_bf16 v[236:239], a[52:55], v[36:39], 0
Rb 36,10,25600
s_mul_i32 s83, s83, 802816
v_mfma_f32_16x16x32_bf16 v[228:231], a[52:55], v[32:35], 0
buffer_load_dwordx4 v3, s[20:23], s55 offen offset:2048 sc1 lds
v_mfma_f32_16x16x32_bf16 v[240:243], a[56:59], v[32:35], 0
Rb 40,10,26624
)" R"(
s_lshl_b32 s92, s82, 5
v_add_f32_e32 v14, v14, v50
v_mfma_f32_16x16x32_bf16 v[248:251], a[56:59], v[36:39], 0
s_add_u32 s83, s83, s92
v_and_b32 v9, 62, v0
v_mul_u32_u24 v9, 0x6f0, v9
v_mfma_f32_16x16x32_bf16 v[252:255], a[60:63], v[36:39], 0
Rb 44,10,27648
s_waitcnt lgkmcnt(8)
v_mfma_f32_16x16x32_bf16 v[244:247], a[60:63], v[32:35], 0
buffer_load_dwordx4 v3, s[20:23], s55 offen offset:3072 sc1 lds
Mp 128,0,40
Rb 48,10,28672
v_add_u32 v9, v9, v1
Mp 136,0,44
v_exp_f32_e32 v24, v105
Mp 140,4,44
Rb 52,10,29696
v_add_f32_e32 v14, v14, v24
Mp 132,4,40
v_exp_f32_e32 v51, v106
Mp 144,8,40
Rb 56,10,30720
v_exp_f32_e32 v25, v107
Mp 152,8,44
v_exp_f32_e32 v52, v100
Mp 156,12,44
Rb 60,10,31744
s_waitcnt lgkmcnt(8)
Mp 148,12,40
v_exp_f32_e32 v26, v101
Cv 50,50,24
Mp 160,16,40
v_exp_f32_e32 v53, v102
Mp 168,16,44
v_exp_f32_e32 v27, v103
v_add_f32_e32 v14, v14, v51
Mp 172,20,44
v_exp_f32_e32 v54, v108
Mp 164,20,40
v_exp_f32_e32 v28, v109
Mp 176,24,40
v_exp_f32_e32 v55, v110
v_add_f32_e32 v14, v14, v25
Mp 184,24,44
v_exp_f32_e32 v29, v111
Mp 188,28,44
Cv 51,51,25
v_add_f32_e32 v15, v52, v26
Mp 180,28,40
Cv 52,52,26
s_waitcnt vmcnt(16) lgkmcnt(0)
s_barrier
Mp 192,32,40
Rb 0,1,0
s_add_u32 m0, s36, 131072
s_nop 0
buffer_load_dwordx4 v9, s[24:27], s83 offen offset:0 lds
Mp 200,32,44
Rb 4,1,1024
v_add_f32_e32 v15, v15, v53
v_add_f32_e32 v15, v15, v27
Mp 204,36,44
Rb 8,1,2048
s_add_u32 s83, s83, 0x1bc00
)" R"(
buffer_load_dwordx4 v9, s[24:27], s83 offen offset:1024 lds
Mp 196,36,40
Rb 12,1,3072
Cv 53,53,27
Mp 208,40,40
Rb 16,1,8192
s_add_u32 s83, s83, 0x1bc00
buffer_load_dwordx4 v9, s[24:27], s83 offen offset:2048 lds
Mp 216,40,44
Rb 20,1,9216
v_add_f32_e32 v15, v15, v54
Mp 220,44,44
Rb 24,1,10240
s_add_u32 s83, s83, 0x1bc00
buffer_load_dwordx4 v9, s[24:27], s83 offen offset:3072 lds
Mp 212,44,40
Rb 28,1,11264
v_add_f32_e32 v15, v15, v28
Cv 54,54,28
Mp 224,48,40
Rb 32,1,4096
s_add_u32 m0, s36, 135168
s_add_u32 s83, s83, 0x1cc00
buffer_load_dwordx4 v9, s[24:27], s83 offen offset:0 lds
Mp 232,48,44
Rb 36,1,5120
v_add_f32_e32 v15, v15, v55
v_add_f32_e32 v15, v15, v29
Mp 236,52,44
Rb 40,1,6144
s_add_u32 s83, s83, 0x1bc00
buffer_load_dwordx4 v9, s[24:27], s83 offen offset:1024 lds
Mp 228,52,40
Rb 44,1,7168
Cv 55,55,29
Mp 240,56,40
s_add_u32 s83, s83, 0x1bc00
buffer_load_dwordx4 v9, s[24:27], s83 offen offset:2048 lds
v_cmp_eq_u32_e32 vcc, s3, v4
s_andn2_b64 s[28:29], s[60:61], vcc
s_cbranch_scc1 .Lzsp8
.Lzok8:
Mp 248,56,44
s_sub_u32 s57, s57, 1
Mp 252,60,44
s_waitcnt lgkmcnt(8)
Mp 244,60,40
s_cmp_gt_i32 s57, s93
s_cbranch_scc0 .LzLX
.LzL:
v_mfma_f32_16x16x32_bf16 v[64:67], a[0:3], a[64:67], v[16:19]
Rb 48,1,12288
v_mfma_f32_16x16x32_bf16 v[68:71], a[0:3], a[68:71], v[20:23]
v_exp_f32_e32 v56, v112
Mv 68,4,76
Rb 52,1,13312
s_add_u32 m0, s36, 98304
Mv 64,4,72
buffer_load_dwordx4 v2, s[20:23], s56 offen offset:0 sc1 lds
Mv 64,8,80
Rb 56,1,14336
v_add_f32_e32 v14, v14, v56
Mv 68,8,84
v_exp_f32_e32 v24, v113
)" R"(
Mv 68,12,92
Rb 60,1,15360
s_waitcnt lgkmcnt(8)
Mv 64,12,88
v_exp_f32_e32 v57, v114
Mv 64,16,96
Rb 0,1,16384
v_add_f32_e32 v14, v14, v24
Mv 68,16,100
v_exp_f32_e32 v25, v115
Mv 68,20,108
Rb 4,1,17408
Cv 56,56,24
Mv 64,20,104
buffer_load_dwordx4 v2, s[20:23], s56 offen offset:1024 sc1 lds
Mv 64,24,112
Rb 8,1,18432
v_add_f32_e32 v14, v14, v57
Mv 68,24,116
v_exp_f32_e32 v58, v120
Mv 68,28,124
Rb 12,1,19456
s_waitcnt lgkmcnt(8)
Mv 64,28,120
v_exp_f32_e32 v26, v121
v_mfma_f32_16x16x32_bf16 v[72:75], a[32:35], a[64:67], v[16:19]
Rb 16,1,24576
v_add_f32_e32 v14, v14, v25
v_mfma_f32_16x16x32_bf16 v[76:79], a[32:35], a[68:71], v[20:23]
v_exp_f32_e32 v59, v122
Mv 76,36,76
Rb 20,1,25600
Cv 57,57,25
Mv 72,36,72
buffer_load_dwordx4 v2, s[20:23], s56 offen offset:2048 sc1 lds
Mv 72,40,80
Rb 24,1,26624
v_add_f32_e32 v14, v14, v58
Mv 76,40,84
v_exp_f32_e32 v27, v123
Mv 76,44,92
Rb 28,1,27648
s_waitcnt lgkmcnt(8)
Mv 72,44,88
v_exp_f32_e32 v60, v116
Mv 72,48,96
Rb 32,1,20480
v_add_f32_e32 v14, v14, v26
Mv 76,48,100
v_exp_f32_e32 v28, v117
Mv 76,52,108
Rb 36,1,21504
Cv 58,58,26
Mv 72,52,104
buffer_load_dwordx4 v2, s[20:23], s56 offen offset:3072 sc1 lds
Mv 72,56,112
Rb 40,1,22528
v_add_f32_e32 v14, v14, v59
Mv 76,56,116
v_exp_f32_e32 v61, v118
Mv 76,60,124
Rb 44,1,23552
s_waitcnt lgkmcnt(8)
Mv 72,60,120
)" R"(
v_exp_f32_e32 v29, v119
v_mfma_f32_16x16x32_bf16 v[80:83], a[0:3], a[64:67], v[16:19]
Rb 48,1,28672
v_add_f32_e32 v14, v14, v27
v_mfma_f32_16x16x32_bf16 v[84:87], a[0:3], a[68:71], v[20:23]
v_exp_f32_e32 v62, v124
Mv 84,4,76
Rb 52,1,29696
s_add_u32 m0, s36, 102400
Mv 80,4,72
buffer_load_dwordx4 v3, s[20:23], s56 offen offset:0 sc1 lds
Mv 80,8,80
Rb 56,1,30720
Cv 59,59,27
Mv 84,8,84
v_exp_f32_e32 v24, v125
Mv 84,12,92
Rb 60,1,31744
s_waitcnt lgkmcnt(8)
Mv 80,12,88
v_exp_f32_e32 v63, v126
Mv 80,16,96
v_exp_f32_e32 v25, v127
Mv 84,16,100
v_add_f32_e32 v15, v15, v60
v_add_f32_e32 v15, v15, v28
Mv 84,20,108
Cv 60,60,28
v_add_f32_e32 v15, v15, v61
Mv 80,20,104
buffer_load_dwordx4 v3, s[20:23], s56 offen offset:1024 sc1 lds
Mv 80,24,112
v_add_f32_e32 v15, v15, v29
Cv 61,61,29
Mv 84,24,116
v_add_f32_e32 v15, v15, v62
v_add_f32_e32 v15, v15, v24
Mv 84,28,124
Cv 62,62,24
v_add_f32_e32 v15, v15, v63
Mv 80,28,120
s_waitcnt vmcnt(14) lgkmcnt(0)
s_barrier
v_mfma_f32_16x16x32_bf16 v[88:91], a[32:35], a[64:67], v[16:19]
Rb 0,8,0
v_add_f32_e32 v15, v15, v25
v_mfma_f32_16x16x32_bf16 v[92:95], a[32:35], a[68:71], v[20:23]
Rb 4,8,1024
Cv 63,63,25
Mv 92,36,76
Rb 8,8,2048
v_max_f32_e32 v11, v14, v15
Mv 88,36,72
Rb 12,8,3072
buffer_load_dwordx4 v3, s[20:23], s56 offen offset:2048 sc1 lds
Mv 88,40,80
Rb 16,8,4096
v_cmp_nge_f32_e32 vcc, s62, v11
Mv 92,40,84
Rb 20,8,5120
s_add_u32 s55, s55, 0x10000
Mv 92,44,92
Rb 24,8,6144
Mv 88,44,88
Rb 28,8,7168
)" R"(
Mv 88,48,96
Rb 32,8,8192
Mv 92,48,100
Rb 36,8,9216
Mv 92,52,108
Rb 40,8,10240
Mv 88,52,104
Rb 44,8,11264
buffer_load_dwordx4 v3, s[20:23], s56 offen offset:3072 sc1 lds
Mv 88,56,112
s_cmp_eq_u32 s57, 2
s_cselect_b32 s55, s59, s55
Mv 92,56,116
s_add_u32 s56, s56, 0x10000
Mv 92,60,124
s_waitcnt lgkmcnt(8)
s_cbranch_vccnz .Lzfx1
.Lzfr1:
Mv 88,60,120
v_add_f32_e32 v12, v12, v14
v_add_f32_e32 v13, v13, v15
Mp 128,0,48
Rb 48,8,12288
Mp 136,0,52
s_cmp_le_i32 s57, s58
s_cbranch_scc1 .Lzmk2
.Lzmr2:
Mp 140,4,52
Rb 52,8,13312
s_add_u32 m0, s36, 0
Mp 132,4,48
buffer_load_dwordx4 v2, s[20:23], s55 offen offset:0 sc1 lds
Mp 144,8,48
Rb 56,8,14336
Mp 152,8,52
v_exp_f32_e32 v32, v64
Mp 156,12,52
Rb 60,8,15360
s_waitcnt lgkmcnt(8)
Mp 148,12,48
v_exp_f32_e32 v26, v65
Mp 160,16,48
Rb 0,8,16384
v_add_f32_e32 v14, v32, v26
Mp 168,16,52
v_exp_f32_e32 v33, v66
Mp 172,20,52
Rb 4,8,17408
Cv 32,32,26
Mp 164,20,48
buffer_load_dwordx4 v2, s[20:23], s55 offen offset:1024 sc1 lds
Mp 176,24,48
Rb 8,8,18432
v_add_f32_e32 v14, v14, v33
Mp 184,24,52
v_exp_f32_e32 v27, v67
Mp 188,28,52
Rb 12,8,19456
s_waitcnt lgkmcnt(8)
Mp 180,28,48
v_exp_f32_e32 v34, v72
Mp 192,32,48
Rb 16,8,20480
v_add_f32_e32 v14, v14, v27
)" R"(
Mp 200,32,52
v_exp_f32_e32 v28, v73
Mp 204,36,52
Rb 20,8,21504
Cv 33,33,27
Mp 196,36,48
buffer_load_dwordx4 v2, s[20:23], s55 offen offset:2048 sc1 lds
Mp 208,40,48
Rb 24,8,22528
v_add_f32_e32 v14, v14, v34
Mp 216,40,52
v_exp_f32_e32 v35, v74
Mp 220,44,52
Rb 28,8,23552
s_waitcnt lgkmcnt(8)
Mp 212,44,48
v_exp_f32_e32 v29, v75
Mp 224,48,48
Rb 32,8,24576
v_add_f32_e32 v14, v14, v28
Mp 232,48,52
v_exp_f32_e32 v36, v68
Mp 236,52,52
Rb 36,8,25600
Cv 34,34,28
Mp 228,52,48
buffer_load_dwordx4 v2, s[20:23], s55 offen offset:3072 sc1 lds
Mp 240,56,48
Rb 40,8,26624
v_add_f32_e32 v14, v14, v35
Mp 248,56,52
v_exp_f32_e32 v24, v69
Mp 252,60,52
Rb 44,8,27648
s_waitcnt lgkmcnt(8)
Mp 244,60,48
v_exp_f32_e32 v37, v70
Mp 128,0,56
Rb 48,8,28672
v_add_f32_e32 v14, v14, v29
Mp 136,0,60
v_exp_f32_e32 v25, v71
Mp 140,4,60
Rb 52,8,29696
s_add_u32 m0, s36, 4096
Mp 132,4,56
buffer_load_dwordx4 v3, s[20:23], s55 offen offset:0 sc1 lds
Mp 144,8,56
Rb 56,8,30720
Cv 35,35,29
Mp 152,8,60
v_exp_f32_e32 v38, v76
Mp 156,12,60
Rb 60,8,31744
s_waitcnt lgkmcnt(8)
Mp 148,12,56
v_exp_f32_e32 v26, v77
Mp 160,16,56
v_exp_f32_e32 v39, v78
Mp 168,16,60
v_exp_f32_e32 v27, v79
Mp 172,20,60
v_add_f32_e32 v15, v36, v24
Cv 36,36,24
)" R"(
Mp 164,20,56
buffer_load_dwordx4 v3, s[20:23], s55 offen offset:1024 sc1 lds
Mp 176,24,56
v_add_f32_e32 v15, v15, v37
v_add_f32_e32 v15, v15, v25
Mp 184,24,60
Cv 37,37,25
v_add_f32_e32 v15, v15, v38
Mp 188,28,60
v_add_f32_e32 v15, v15, v26
Cv 38,38,26
Mp 180,28,56
s_waitcnt vmcnt(14) lgkmcnt(0)
s_barrier
Mp 192,32,56
Rb 0,1,32768
v_add_f32_e32 v15, v15, v39
Mp 200,32,60
Rb 4,1,33792
v_add_f32_e32 v15, v15, v27
Mp 204,36,60
Rb 8,1,34816
Cv 39,39,27
Mp 196,36,56
Rb 12,1,35840
buffer_load_dwordx4 v3, s[20:23], s55 offen offset:2048 sc1 lds
Mp 208,40,56
Rb 16,1,40960
s_sub_u32 s57, s57, 1
Mp 216,40,60
Rb 20,1,41984
Mp 220,44,60
Rb 24,1,43008
Mp 212,44,56
Rb 28,1,44032
Mp 224,48,56
Rb 32,1,36864
Mp 232,48,60
Rb 36,1,37888
Mp 236,52,60
Rb 40,1,38912
Mp 228,52,56
Rb 44,1,39936
buffer_load_dwordx4 v3, s[20:23], s55 offen offset:3072 sc1 lds
Mp 240,56,56
Mp 248,56,60
Mp 252,60,60
s_waitcnt lgkmcnt(8)
Mp 244,60,56
v_mfma_f32_16x16x32_bf16 v[96:99], a[0:3], a[64:67], v[16:19]
Rb 48,1,45056
buffer_load_dword v4, v5, s[20:23], s50 offen offset:0 sc0 sc1
v_mfma_f32_16x16x32_bf16 v[100:103], a[0:3], a[68:71], v[20:23]
s_lshl_b64 s[60:61], s[60:61], 8
s_or_b32 s60, s60, 0xff
Mv 100,4,76
Rb 52,1,46080
s_add_u32 m0, s36, 65536
Mv 96,4,72
buffer_load_dwordx4 v2, s[20:23], s56 offen offset:0 sc1 lds
Mv 96,8,80
Rb 56,1,47104
Mv 100,8,84
v_exp_f32_e32 v40, v80
Mv 100,12,92
)" R"(
Rb 60,1,48128
s_waitcnt lgkmcnt(8)
Mv 96,12,88
v_exp_f32_e32 v28, v81
Mv 96,16,96
Rb 0,1,49152
v_add_f32_e32 v14, v14, v40
Mv 100,16,100
v_exp_f32_e32 v41, v82
Mv 100,20,108
Rb 4,1,50176
v_add_f32_e32 v14, v14, v28
Mv 96,20,104
buffer_load_dwordx4 v2, s[20:23], s56 offen offset:1024 sc1 lds
Mv 96,24,112
Rb 8,1,51200
Cv 40,40,28
Mv 100,24,116
v_exp_f32_e32 v29, v83
Mv 100,28,124
Rb 12,1,52224
s_waitcnt lgkmcnt(8)
Mv 96,28,120
v_exp_f32_e32 v42, v88
v_mfma_f32_16x16x32_bf16 v[104:107], a[32:35], a[64:67], v[16:19]
Rb 16,1,57344
v_add_f32_e32 v14, v14, v41
v_mfma_f32_16x16x32_bf16 v[108:111], a[32:35], a[68:71], v[20:23]
v_exp_f32_e32 v24, v89
Mv 108,36,76
Rb 20,1,58368
v_add_f32_e32 v14, v14, v29
Mv 104,36,72
buffer_load_dwordx4 v2, s[20:23], s56 offen offset:2048 sc1 lds
Mv 104,40,80
Rb 24,1,59392
Cv 41,41,29
Mv 108,40,84
v_exp_f32_e32 v43, v90
Mv 108,44,92
Rb 28,1,60416
s_waitcnt lgkmcnt(8)
Mv 104,44,88
v_exp_f32_e32 v25, v91
Mv 104,48,96
Rb 32,1,53248
v_add_f32_e32 v14, v14, v42
Mv 108,48,100
v_exp_f32_e32 v44, v84
Mv 108,52,108
Rb 36,1,54272
v_add_f32_e32 v14, v14, v24
Mv 104,52,104
buffer_load_dwordx4 v2, s[20:23], s56 offen offset:3072 sc1 lds
Mv 104,56,112
Rb 40,1,55296
Cv 42,42,24
Mv 108,56,116
v_exp_f32_e32 v26, v85
Mv 108,60,124
Rb 44,1,56320
s_waitcnt lgkmcnt(8)
Mv 104,60,120
v_exp_f32_e32 v45, v86
)" R"(
v_mfma_f32_16x16x32_bf16 v[112:115], a[0:3], a[64:67], v[16:19]
Rb 48,1,61440
v_add_f32_e32 v14, v14, v43
v_mfma_f32_16x16x32_bf16 v[116:119], a[0:3], a[68:71], v[20:23]
v_exp_f32_e32 v27, v87
Mv 116,4,76
Rb 52,1,62464
s_add_u32 m0, s36, 69632
Mv 112,4,72
buffer_load_dwordx4 v3, s[20:23], s56 offen offset:0 sc1 lds
Mv 112,8,80
Rb 56,1,63488
v_add_f32_e32 v14, v14, v25
Mv 116,8,84
v_exp_f32_e32 v46, v92
Mv 116,12,92
Rb 60,1,64512
s_waitcnt lgkmcnt(8)
Mv 112,12,88
v_exp_f32_e32 v28, v93
Mv 112,16,96
v_exp_f32_e32 v47, v94
Mv 116,16,100
v_exp_f32_e32 v29, v95
Mv 116,20,108
Cv 43,43,25
v_add_f32_e32 v15, v15, v44
Mv 112,20,104
buffer_load_dwordx4 v3, s[20:23], s56 offen offset:1024 sc1 lds
Mv 112,24,112
v_add_f32_e32 v15, v15, v26
Cv 44,44,26
Mv 116,24,116
v_add_f32_e32 v15, v15, v45
v_add_f32_e32 v15, v15, v27
Mv 116,28,124
Cv 45,45,27
v_add_f32_e32 v15, v15, v46
Mv 112,28,120
s_waitcnt vmcnt(15) lgkmcnt(0)
s_barrier
v_mfma_f32_16x16x32_bf16 v[120:123], a[32:35], a[64:67], v[16:19]
Rb 0,8,32768
v_add_f32_e32 v15, v15, v28
v_mfma_f32_16x16x32_bf16 v[124:127], a[32:35], a[68:71], v[20:23]
Rb 4,8,33792
Cv 46,46,28
Mv 124,36,76
Rb 8,8,34816
v_add_f32_e32 v15, v15, v47
Mv 120,36,72
Rb 12,8,35840
buffer_load_dwordx4 v3, s[20:23], s56 offen offset:2048 sc1 lds
Mv 120,40,80
Rb 16,8,36864
v_add_f32_e32 v15, v15, v29
Mv 124,40,84
Rb 20,8,37888
Cv 47,47,29
Mv 124,44,92
Rb 24,8,38912
v_max_f32_e32 v11, v14, v15
Mv 120,44,88
Rb 28,8,39936
v_cmp_nge_f32_e32 vcc, s62, v11
)" R"(
Mv 120,48,96
Rb 32,8,40960
s_add_u32 s55, s55, 0x10000
Mv 124,48,100
Rb 36,8,41984
Mv 124,52,108
Rb 40,8,43008
Mv 120,52,104
Rb 44,8,44032
buffer_load_dwordx4 v3, s[20:23], s56 offen offset:3072 sc1 lds
Mv 120,56,112
s_cmp_eq_u32 s57, 2
s_cselect_b32 s55, s59, s55
Mv 124,56,116
s_add_u32 s56, s56, 0x10000
Mv 124,60,124
s_waitcnt lgkmcnt(8)
s_cbranch_vccnz .Lzfx2
.Lzfr2:
Mv 120,60,120
v_add_f32_e32 v12, v12, v14
v_add_f32_e32 v13, v13, v15
Mp 128,0,32
Rb 48,8,45056
Mp 136,0,36
s_cmp_le_i32 s57, s58
s_cbranch_scc1 .Lzmk3
.Lzmr3:
Mp 140,4,36
Rb 52,8,46080
s_add_u32 m0, s36, 32768
Mp 132,4,32
buffer_load_dwordx4 v2, s[20:23], s55 offen offset:0 sc1 lds
Mp 144,8,32
Rb 56,8,47104
Mp 152,8,36
v_exp_f32_e32 v48, v96
Mp 156,12,36
Rb 60,8,48128
s_waitcnt lgkmcnt(8)
Mp 148,12,32
v_exp_f32_e32 v24, v97
Mp 160,16,32
Rb 0,8,49152
v_add_f32_e32 v14, v48, v24
Mp 168,16,36
v_exp_f32_e32 v49, v98
Mp 172,20,36
Rb 4,8,50176
Cv 48,48,24
Mp 164,20,32
buffer_load_dwordx4 v2, s[20:23], s55 offen offset:1024 sc1 lds
Mp 176,24,32
Rb 8,8,51200
v_add_f32_e32 v14, v14, v49
Mp 184,24,36
v_exp_f32_e32 v25, v99
Mp 188,28,36
Rb 12,8,52224
s_waitcnt lgkmcnt(8)
Mp 180,28,32
v_exp_f32_e32 v50, v104
Mp 192,32,32
Rb 16,8,53248
)" R"(
v_add_f32_e32 v14, v14, v25
Mp 200,32,36
v_exp_f32_e32 v26, v105
Mp 204,36,36
Rb 20,8,54272
Cv 49,49,25
Mp 196,36,32
buffer_load_dwordx4 v2, s[20:23], s55 offen offset:2048 sc1 lds
Mp 208,40,32
Rb 24,8,55296
v_add_f32_e32 v14, v14, v50
Mp 216,40,36
v_exp_f32_e32 v51, v106
Mp 220,44,36
Rb 28,8,56320
s_waitcnt lgkmcnt(8)
Mp 212,44,32
v_exp_f32_e32 v27, v107
Mp 224,48,32
Rb 32,8,57344
v_add_f32_e32 v14, v14, v26
Mp 232,48,36
v_exp_f32_e32 v52, v100
Mp 236,52,36
Rb 36,8,58368
Cv 50,50,26
Mp 228,52,32
buffer_load_dwordx4 v2, s[20:23], s55 offen offset:3072 sc1 lds
Mp 240,56,32
Rb 40,8,59392
v_add_f32_e32 v14, v14, v51
Mp 248,56,36
v_exp_f32_e32 v28, v101
Mp 252,60,36
Rb 44,8,60416
s_waitcnt lgkmcnt(8)
Mp 244,60,32
v_exp_f32_e32 v53, v102
Mp 128,0,40
Rb 48,8,61440
v_add_f32_e32 v14, v14, v27
Mp 136,0,44
v_exp_f32_e32 v29, v103
Mp 140,4,44
Rb 52,8,62464
s_add_u32 m0, s36, 36864
Mp 132,4,40
buffer_load_dwordx4 v3, s[20:23], s55 offen offset:0 sc1 lds
Mp 144,8,40
Rb 56,8,63488
Cv 51,51,27
Mp 152,8,44
v_exp_f32_e32 v54, v108
Mp 156,12,44
Rb 60,8,64512
s_waitcnt lgkmcnt(8)
Mp 148,12,40
v_exp_f32_e32 v24, v109
Mp 160,16,40
v_exp_f32_e32 v55, v110
Mp 168,16,44
v_exp_f32_e32 v25, v111
Mp 172,20,44
v_add_f32_e32 v15, v52, v28
)" R"(
Cv 52,52,28
Mp 164,20,40
buffer_load_dwordx4 v3, s[20:23], s55 offen offset:1024 sc1 lds
Mp 176,24,40
v_add_f32_e32 v15, v15, v53
v_add_f32_e32 v15, v15, v29
Mp 184,24,44
Cv 53,53,29
v_add_f32_e32 v15, v15, v54
Mp 188,28,44
v_add_f32_e32 v15, v15, v24
Cv 54,54,24
Mp 180,28,40
s_waitcnt vmcnt(14) lgkmcnt(0)
s_barrier
Mp 192,32,40
Rb 0,1,0
v_add_f32_e32 v15, v15, v55
Mp 200,32,44
Rb 4,1,1024
v_add_f32_e32 v15, v15, v25
Mp 204,36,44
Rb 8,1,2048
Cv 55,55,25
Mp 196,36,40
Rb 12,1,3072
buffer_load_dwordx4 v3, s[20:23], s55 offen offset:2048 sc1 lds
Mp 208,40,40
Rb 16,1,8192
s_sub_u32 s57, s57, 1
Mp 216,40,44
Rb 20,1,9216
Mp 220,44,44
Rb 24,1,10240
Mp 212,44,40
Rb 28,1,11264
Mp 224,48,40
Rb 32,1,4096
Mp 232,48,44
Rb 36,1,5120
Mp 236,52,44
Rb 40,1,6144
Mp 228,52,40
Rb 44,1,7168
buffer_load_dwordx4 v3, s[20:23], s55 offen offset:3072 sc1 lds
Mp 240,56,40
v_cmp_eq_u32_e32 vcc, s3, v4
s_andn2_b64 s[28:29], s[60:61], vcc
s_cbranch_scc1 .Lzsp9
.Lzok9:
Mp 248,56,44
Mp 252,60,44
s_waitcnt lgkmcnt(8)
Mp 244,60,40
s_cmp_gt_i32 s57, s93
s_cbranch_scc1 .LzL
.LzLX:
s_cmp_eq_u32 s94, 0
s_cbranch_scc1 .LzEB
s_cmp_eq_u32 s94, 2
s_cbranch_scc1 .LzDA
s_lshl1_add_u32 s57, s35, 2
s_mov_b32 s59, s67
s_add_u32 s56, s89, 0x2000000
s_mov_b32 s94, 2
)" R"(
Mz 64,0,224
Rb 48,1,12288
v_lshrrev_b32 v9, 4, v0
Mz 68,0,228
v_lshlrev_b32 v9, 4, v9
v_add_lshl_u32 v9, v9, v0, 3
v_accvgpr_mov_b32 a64, a224
Mv 68,4,236
Rb 52,1,13312
s_add_u32 m0, s36, 98304
v_accvgpr_mov_b32 a65, a225
Mv 64,4,232
buffer_load_dwordx4 v2, s[20:23], s56 offen offset:0 sc1 lds
v_accvgpr_mov_b32 a66, a226
Mv 64,8,240
Rb 56,1,14336
s_add_u32 s92, s36, 131072
v_accvgpr_mov_b32 a67, a227
Mv 68,8,244
v_add_u32 v9, s92, v9
v_accvgpr_mov_b32 a68, a228
s_waitcnt vmcnt(1)
Mv 68,12,252
Rb 60,1,15360
v_accvgpr_mov_b32 a69, a229
s_waitcnt lgkmcnt(8)
Mv 64,12,248
buffer_load_dwordx4 v2, s[20:23], s56 offen offset:1024 sc1 lds
Rt 156,9,0
Mv 64,16,192
Rb 0,1,16384
Rt 158,9,128
v_accvgpr_mov_b32 a70, a230
Mv 68,16,196
Rt 160,9,1024
v_exp_f32_e32 v56, v112
Mv 68,20,204
Rb 4,1,17408
Rt 162,9,1152
v_add_f32_e32 v14, v14, v56
Mv 64,20,200
buffer_load_dwordx4 v2, s[20:23], s56 offen offset:2048 sc1 lds
Rt 164,9,2048
Mv 64,24,208
Rb 8,1,18432
Rt 166,9,2176
v_accvgpr_mov_b32 a71, a231
Mv 68,24,212
Rt 168,9,3072
v_exp_f32_e32 v26, v113
Mv 68,28,220
Rb 12,1,19456
Rt 170,9,3200
Mv 64,28,216
buffer_load_dwordx4 v2, s[20:23], s56 offen offset:3072 sc1 lds
Rt 172,9,4096
v_add_f32_e32 v14, v14, v26
Mz 72,32,224
Rb 16,1,24576
Rt 174,9,4224
Cv 56,56,26
Mz 76,32,228
Rt 176,9,5120
v_exp_f32_e32 v57, v114
)" R"(
Mv 76,36,236
Rb 20,1,25600
s_add_u32 m0, s36, 102400
Rt 178,9,5248
Mv 72,36,232
buffer_load_dwordx4 v3, s[20:23], s56 offen offset:0 sc1 lds
Rt 180,9,6144
Mv 72,40,240
Rb 24,1,26624
Rt 182,9,6272
v_add_f32_e32 v14, v14, v57
Mv 76,40,244
v_exp_f32_e32 v27, v115
v_accvgpr_mov_b32 a72, a232
Mv 76,44,252
Rb 28,1,27648
v_add_f32_e32 v14, v14, v27
Mv 72,44,248
buffer_load_dwordx4 v3, s[20:23], s56 offen offset:1024 sc1 lds
Cv 57,57,27
Mv 72,48,192
Rb 32,1,20480
v_and_b32 v9, 14, v0
v_accvgpr_mov_b32 a73, a233
Mv 76,48,196
v_min_u32 v9, 1, v9
v_lshlrev_b32 v9, 8, v9
v_accvgpr_mov_b32 a74, a234
Mv 76,52,204
Rb 36,1,21504
v_and_or_b32 v9, v0, 48, v9
v_accvgpr_mov_b32 a75, a235
Mv 72,52,200
buffer_load_dwordx4 v3, s[20:23], s56 offen offset:2048 sc1 lds
v_accvgpr_mov_b32 a76, a236
Mv 72,56,208
Rb 40,1,22528
v_lshlrev_b32 v9, 1, v9
v_accvgpr_mov_b32 a77, a237
Mv 76,56,212
v_and_or_b32 v9, v1, 16, v9
s_lshl_b32 s92, s33, 7
v_accvgpr_mov_b32 a78, a238
Mv 76,60,220
Rb 44,1,23552
v_accvgpr_mov_b32 a79, a239
s_waitcnt lgkmcnt(5)
Mv 72,60,216
v_mfma_f32_16x16x32_bf16 a[184:187], a[156:159], a[128:131], 0
buffer_load_dwordx4 v3, s[20:23], s56 offen offset:3072 sc1 lds
v_exp_f32_e32 v58, v120
Mz 80,0,224
Ma 184,160,132
Rb 48,1,28672
s_add_u32 s92, s92, 162816
v_add_f32_e32 v14, v14, v58
Mz 84,0,228
Ma 184,164,136
s_add_u32 s56, s56, 0x10000
v_add_u32 v9, s92, v9
v_accvgpr_mov_b32 a80, a240
Mv 84,4,236
Ma 184,168,140
Rb 52,1,29696
)" R"(
v_exp_f32_e32 v28, v121
Mv 80,4,232
Ma 184,172,144
v_exp_f32_e32 v59, v122
v_add_f32_e32 v14, v14, v28
Mv 80,8,240
Ma 184,176,148
Rb 56,1,30720
s_mov_b32 s24, s70
s_mov_b32 s25, s71
Mv 84,8,244
Ma 184,180,152
s_mul_i32 s83, s82, 14336
s_lshl_b32 s92, s33, 12
Cv 58,58,28
Mv 84,12,252
Rb 60,1,31744
v_add_f32_e32 v14, v14, v59
s_waitcnt lgkmcnt(8)
Mv 80,12,248
s_add_u32 s83, s83, s92
s_add_u32 m0, s92, 131072
v_accvgpr_mov_b32 a81, a241
Mv 80,16,192
v_exp_f32_e32 v29, v123
v_accvgpr_mov_b32 a82, a242
Mv 84,16,196
v_exp_f32_e32 v60, v116
v_add_f32_e32 v14, v14, v29
Mv 84,20,204
ds_write_b128 v9, a[184:187]
v_and_b32 v9, 0x70, v9
Cv 59,59,29
Mv 80,20,200
v_add_u32 v9, 0x27c00, v9
v_exp_f32_e32 v24, v117
Mv 80,24,208
v_exp_f32_e32 v61, v118
v_add_f32_e32 v15, v15, v60
Mv 84,24,212
v_exp_f32_e32 v25, v119
v_add_f32_e32 v15, v15, v24
Mv 84,28,220
v_exp_f32_e32 v62, v124
Cv 60,60,24
Mv 80,28,216
v_exp_f32_e32 v26, v125
s_waitcnt lgkmcnt(0)
s_barrier
Mz 88,32,224
Rb 0,8,0
v_exp_f32_e32 v63, v126
Mz 92,32,228
Rb 4,8,16384
v_exp_f32_e32 v27, v127
Mv 92,36,236
Rb 8,8,1024
v_add_f32_e32 v15, v15, v61
v_add_f32_e32 v15, v15, v25
Mv 88,36,232
Rb 12,8,17408
Cv 61,61,25
v_add_f32_e32 v15, v15, v62
Mv 88,40,240
Rb 16,8,2048
)" R"(
buffer_load_dwordx4 v1, s[24:27], s83 offen offset:0 lds
Mv 92,40,244
Rb 20,8,18432
v_add_f32_e32 v15, v15, v26
Cv 62,62,26
Mv 92,44,252
Rb 24,8,3072
v_add_f32_e32 v15, v15, v63
v_add_f32_e32 v15, v15, v27
Mv 88,44,248
Rb 28,8,19456
Cv 63,63,27
v_max_f32_e32 v11, v14, v15
Mv 88,48,192
Rb 32,8,4096
v_cmp_nge_f32_e32 vcc, s62, v11
s_add_u32 s55, s55, 0x10000
Mv 92,48,196
Rb 36,8,20480
buffer_load_dwordx4 v1, s[24:27], s83 offen offset:1024 lds
Mv 92,52,204
Rb 40,8,5120
s_cmp_eq_u32 s57, 2
s_cselect_b32 s55, s59, s55
Mv 88,52,200
Rb 44,8,21504
v_accvgpr_mov_b32 a83, a243
v_accvgpr_mov_b32 a84, a244
Mv 88,56,208
buffer_load_dwordx4 v1, s[24:27], s83 offen offset:2048 lds
v_accvgpr_mov_b32 a85, a245
Mv 92,56,212
buffer_load_dwordx4 v1, s[24:27], s83 offen offset:3072 lds
v_accvgpr_mov_b32 a86, a246
Mv 92,60,220
v_accvgpr_mov_b32 a87, a247
s_waitcnt lgkmcnt(8)
s_cbranch_vccnz .Lzfx3
.Lzfr3:
Mv 88,60,216
v_add_f32_e32 v12, v12, v14
v_add_f32_e32 v13, v13, v15
v_mov_b32 v14, v16
Mp 128,0,48
Rb 48,8,6144
v_mov_b32 v15, v20
Mp 136,0,52
buffer_store_dwordx4 v[12:15], v1, s[20:23], s66 offen sc0 sc1
s_cmp_le_i32 s57, s58
s_cbranch_scc1 .Lzmk4
.Lzmr4:
Mp 136,4,60
Rb 52,8,22528
s_add_u32 m0, s36, 0
v_accvgpr_mov_b32 a88, a248
Mp 128,4,56
buffer_load_dwordx4 v2, s[20:23], s55 offen offset:0 sc1 lds
v_accvgpr_mov_b32 a89, a249
Mp 132,8,48
Rb 56,8,7168
v_exp_f32_e32 v32, v64
Mp 140,8,52
v_exp_f32_e32 v28, v65
v_accvgpr_mov_b32 a90, a250
)" R"(
Mp 140,12,60
Rb 60,8,23552
v_exp_f32_e32 v33, v66
s_waitcnt lgkmcnt(8)
Mp 132,12,56
v_exp_f32_e32 v29, v67
v_add_f32_e32 v14, v32, v28
Mp 144,16,48
Rb 0,8,8192
v_exp_f32_e32 v34, v72
Mp 152,16,52
v_exp_f32_e32 v24, v73
v_exp_f32_e32 v35, v74
Mp 152,20,60
Rb 4,8,24576
v_exp_f32_e32 v25, v75
Mp 144,20,56
buffer_load_dwordx4 v2, s[20:23], s55 offen offset:1024 sc1 lds
Cv 128,128,129
Mp 148,24,48
Rb 8,8,9216
Cv 129,130,131
Cv 130,132,133
Cv 32,32,28
Mp 156,24,52
Cv 131,134,135
Cv 136,136,137
v_add_f32_e32 v14, v14, v33
Mp 156,28,60
Rb 12,8,25600
Cv 137,138,139
s_waitcnt lgkmcnt(8)
Mp 148,28,56
Cv 138,140,141
Cv 139,142,143
v_exp_f32_e32 v36, v68
Mp 160,32,48
Rb 16,8,10240
v_exp_f32_e32 v26, v69
Mp 168,32,52
v_exp_f32_e32 v37, v70
v_add_f32_e32 v14, v14, v29
Mp 168,36,60
Rb 20,8,26624
v_exp_f32_e32 v27, v71
Cv 33,33,29
Mp 160,36,56
buffer_load_dwordx4 v2, s[20:23], s55 offen offset:2048 sc1 lds
Cv 144,144,145
Mp 164,40,48
Rb 24,8,11264
Cv 145,146,147
Cv 146,148,149
Mp 172,40,52
buffer_store_dwordx4 v[128:131], v2, s[20:23], s64 offen offset:0 sc0 sc1
v_add_f32_e32 v14, v14, v34
Mp 172,44,60
Rb 28,8,27648
Cv 147,150,151
v_add_f32_e32 v14, v14, v24
s_waitcnt lgkmcnt(8)
Mp 164,44,56
Cv 152,152,153
Cv 153,154,155
)" R"(
Cv 154,156,157
Mp 176,48,48
Rb 32,8,12288
Cv 155,158,159
Cv 34,34,24
Mp 184,48,52
v_exp_f32_e32 v38, v76
v_exp_f32_e32 v28, v77
Mp 184,52,60
Rb 36,8,28672
v_exp_f32_e32 v39, v78
Mp 176,52,56
buffer_load_dwordx4 v2, s[20:23], s55 offen offset:3072 sc1 lds
Cv 160,160,161
Mp 180,56,48
Rb 40,8,13312
Cv 161,162,163
Cv 162,164,165
v_add_f32_e32 v14, v14, v35
Mp 188,56,52
buffer_store_dwordx4 v[136:139], v2, s[20:23], s64 offen offset:1024 sc0 sc1
v_add_f32_e32 v14, v14, v25
Mp 188,60,60
Rb 44,8,29696
Cv 163,166,167
s_waitcnt lgkmcnt(8)
Mp 180,60,56
Cv 168,168,169
Cv 169,170,171
Cv 170,172,173
Cv 35,35,25
Mp 192,0,48
Rb 48,8,14336
Cv 171,174,175
v_add_f32_e32 v15, v36, v26
Mp 200,0,52
buffer_store_dwordx4 v[144:147], v2, s[20:23], s64 offen offset:2048 sc0 sc1
Cv 36,36,26
Mp 200,4,60
Rb 52,8,30720
s_add_u32 m0, s36, 4096
v_exp_f32_e32 v29, v79
Mp 192,4,56
buffer_load_dwordx4 v3, s[20:23], s55 offen offset:0 sc1 lds
Cv 176,176,177
Mp 196,8,48
Rb 56,8,15360
Cv 177,178,179
Cv 178,180,181
Mp 204,8,52
Cv 179,182,183
Cv 184,184,185
v_add_f32_e32 v15, v15, v37
Mp 204,12,60
Rb 60,8,31744
Cv 185,186,187
v_add_f32_e32 v15, v15, v27
s_waitcnt lgkmcnt(8)
Mp 196,12,56
buffer_store_dwordx4 v[152:155], v2, s[20:23], s64 offen offset:3072 sc0 sc1
Cv 186,188,189
Mp 208,16,48
ds_read_b128 v[132:135], v9 offset:0
Cv 187,190,191
)" R"(
Cv 37,37,27
Mp 216,16,52
ds_read_b128 v[140:143], v9 offset:128
v_add_f32_e32 v15, v15, v38
v_add_f32_e32 v15, v15, v28
Cv 38,38,28
Mp 216,20,60
ds_read_b128 v[148:151], v9 offset:256
v_add_f32_e32 v15, v15, v39
v_add_f32_e32 v15, v15, v29
Mp 208,20,56
buffer_load_dwordx4 v3, s[20:23], s55 offen offset:1024 sc1 lds
ds_read_b128 v[156:159], v9 offset:384
Mp 212,24,48
Cv 192,192,193
Cv 193,194,195
Cv 194,196,197
Cv 39,39,29
Mp 220,24,52
Cv 195,198,199
Cv 200,200,201
s_sub_u32 s57, s57, 1
Mp 220,28,60
buffer_store_dwordx4 v[160:163], v3, s[20:23], s64 offen offset:0 sc0 sc1
Cv 201,202,203
Mp 212,28,56
Cv 202,204,205
v_mov_b32 v16, 0
s_waitcnt lgkmcnt(0)
s_barrier
Mp 224,32,48
Rb 0,1,32768
v_add_f32_e32 v132, v132, v140
v_mov_b32 v17, 0
Mp 232,32,52
Rb 4,1,33792
v_add_f32_e32 v133, v133, v141
Cv 203,206,207
Mp 232,36,60
Rb 8,1,34816
v_add_f32_e32 v134, v134, v142
v_mov_b32 v18, 0
v_mov_b32 v19, 0
Mp 224,36,56
Rb 12,1,35840
buffer_load_dwordx4 v3, s[20:23], s55 offen offset:2048 sc1 lds
Mp 228,40,48
Rb 16,1,40960
v_add_f32_e32 v135, v135, v143
buffer_store_dwordx4 v[168:171], v3, s[20:23], s64 offen offset:1024 sc0 sc1
Mp 236,40,52
Rb 20,1,41984
v_add_f32_e32 v148, v148, v156
v_mov_b32 v20, 0
Mp 236,44,60
Rb 24,1,43008
v_add_f32_e32 v149, v149, v157
Cv 208,208,209
v_mov_b32 v21, 0
Mp 228,44,56
Rb 28,1,44032
v_add_f32_e32 v150, v150, v158
v_add_f32_e32 v151, v151, v159
Mp 240,48,48
Rb 32,1,36864
)" R"(
Cv 132,132,132
v_mov_b32 v22, 0
Mp 248,48,52
Rb 36,1,37888
v_lshlrev_b32 v132, 16, v132
Cv 209,210,211
v_mov_b32 v23, 0
Mp 248,52,60
Rb 40,1,38912
Cv 148,148,148
buffer_store_dwordx4 v[176:179], v3, s[20:23], s64 offen offset:2048 sc0 sc1
Mp 240,52,56
Rb 44,1,39936
buffer_load_dwordx4 v3, s[20:23], s55 offen offset:3072 sc1 lds
Mp 244,56,48
v_lshlrev_b32 v148, 16, v148
Cv 133,133,133
Cv 210,212,213
Mp 252,56,52
v_lshlrev_b32 v133, 16, v133
Cv 149,149,149
v_accvgpr_mov_b32 a91, a251
Mp 252,60,60
v_lshlrev_b32 v149, 16, v149
Cv 211,214,215
s_waitcnt lgkmcnt(8)
Mp 244,60,56
Cv 134,134,134
v_lshlrev_b32 v134, 16, v134
Cv 150,150,150
Mz 96,0,224
Rb 48,1,45056
buffer_load_dword v4, v5, s[20:23], s50 offen offset:0 sc0 sc1
buffer_store_dwordx4 v[184:187], v3, s[20:23], s64 offen offset:3072 sc0 sc1
Mz 100,0,228
s_lshl_b64 s[60:61], s[60:61], 8
s_or_b32 s60, s60, 0xff
Cv 216,216,217
Mv 100,4,236
Rb 52,1,46080
s_add_u32 m0, s36, 65536
v_exp_f32_e32 v40, v80
Mv 96,4,232
buffer_load_dwordx4 v2, s[20:23], s56 offen offset:0 sc1 lds
Cv 217,218,219
v_add_f32_e32 v14, v14, v40
Mv 96,8,240
Rb 56,1,47104
v_lshlrev_b32 v150, 16, v150
Cv 218,220,221
Mv 100,8,244
Cv 135,135,135
v_lshlrev_b32 v135, 16, v135
v_exp_f32_e32 v24, v81
Mv 100,12,252
Rb 60,1,48128
s_add_u32 s64, s64, 0x10000
buffer_store_dwordx4 v[192:195], v2, s[20:23], s64 offen offset:0 sc0 sc1
s_waitcnt lgkmcnt(8)
Mv 96,12,248
Cv 151,151,151
v_lshlrev_b32 v151, 16, v151
v_mul_f32_e32 v140, 0xbfb8aa3b, v132
v_add_f32_e32 v14, v14, v24
)" R"(
Mv 96,16,192
Rb 0,1,49152
v_mul_f32_e32 v141, 0xbfb8aa3b, v133
v_exp_f32_e32 v41, v82
Mv 100,16,196
v_exp_f32_e32 v140, v140
Cv 219,222,223
Cv 40,40,24
Mv 100,20,204
Rb 4,1,50176
v_mul_f32_e32 v142, 0xbfb8aa3b, v134
v_exp_f32_e32 v25, v83
Mv 96,20,200
buffer_load_dwordx4 v2, s[20:23], s56 offen offset:1024 sc1 lds
Cv 224,224,225
v_add_f32_e32 v14, v14, v41
Mv 96,24,208
Rb 8,1,51200
v_mul_f32_e32 v143, 0xbfb8aa3b, v135
buffer_store_dwordx4 v[200:203], v2, s[20:23], s64 offen offset:1024 sc0 sc1
Mv 100,24,212
v_exp_f32_e32 v141, v141
v_exp_f32_e32 v42, v88
Mv 100,28,220
Rb 12,1,52224
Cv 225,226,227
v_add_f32_e32 v14, v14, v25
s_waitcnt lgkmcnt(8)
Mv 96,28,216
v_exp_f32_e32 v142, v142
v_add_f32_e32 v140, 1.0, v140
Cv 41,41,25
Mz 104,32,224
Rb 16,1,57344
v_add_f32_e32 v141, 1.0, v141
v_add_f32_e32 v14, v14, v42
Mz 108,32,228
v_exp_f32_e32 v143, v143
Cv 226,228,229
Mv 108,36,236
Rb 20,1,58368
v_add_f32_e32 v142, 1.0, v142
buffer_store_dwordx4 v[208:211], v2, s[20:23], s64 offen offset:2048 sc0 sc1
Mv 104,36,232
buffer_load_dwordx4 v2, s[20:23], s56 offen offset:2048 sc1 lds
Cv 227,230,231
Mv 104,40,240
Rb 24,1,59392
v_add_f32_e32 v143, 1.0, v143
Cv 232,232,233
Mv 108,40,244
v_rcp_f32_e32 v140, v140
v_exp_f32_e32 v26, v89
Mv 108,44,252
Rb 28,1,60416
Cv 233,234,235
v_add_f32_e32 v14, v14, v26
s_waitcnt lgkmcnt(8)
Mv 104,44,248
v_rcp_f32_e32 v141, v141
v_mul_f32_e32 v132, v132, v140
Cv 42,42,26
Mv 104,48,192
Rb 32,1,53248
)" R"(
v_mul_f32_e32 v133, v133, v141
buffer_store_dwordx4 v[216:219], v2, s[20:23], s64 offen offset:3072 sc0 sc1
Mv 108,48,196
v_rcp_f32_e32 v142, v142
Cv 234,236,237
Mv 108,52,204
Rb 36,1,54272
v_mul_f32_e32 v134, v134, v142
v_exp_f32_e32 v43, v90
Mv 104,52,200
buffer_load_dwordx4 v2, s[20:23], s56 offen offset:3072 sc1 lds
Cv 235,238,239
v_add_f32_e32 v14, v14, v43
Mv 104,56,208
Rb 40,1,55296
Cv 132,132,132
Cv 240,240,241
Mv 108,56,212
v_rcp_f32_e32 v143, v143
v_exp_f32_e32 v27, v91
Mv 108,60,220
Rb 44,1,56320
buffer_store_dwordx4 v[224:227], v3, s[20:23], s64 offen offset:0 sc0 sc1
s_waitcnt lgkmcnt(8)
Mv 104,60,216
v_mul_f32_e32 v135, v135, v143
v_lshlrev_b32 v132, 16, v132
Cv 133,133,133
v_add_f32_e32 v14, v14, v27
Mz 112,0,224
Rb 48,1,61440
v_lshlrev_b32 v133, 16, v133
Cv 43,43,27
Mz 116,0,228
Cv 134,134,134
v_lshlrev_b32 v134, 16, v134
Cv 241,242,243
Mv 116,4,236
Rb 52,1,62464
s_add_u32 m0, s36, 69632
v_exp_f32_e32 v44, v84
Mv 112,4,232
buffer_load_dwordx4 v3, s[20:23], s56 offen offset:0 sc1 lds
Cv 242,244,245
v_add_f32_e32 v15, v15, v44
Mv 112,8,240
Rb 56,1,63488
Cv 135,135,135
buffer_store_dwordx4 v[232:235], v3, s[20:23], s64 offen offset:1024 sc0 sc1
Mv 116,8,244
v_lshlrev_b32 v135, 16, v135
v_mul_f32_e32 v132, v132, v148
v_exp_f32_e32 v28, v85
Mv 116,12,252
Rb 60,1,64512
Cv 243,246,247
v_add_f32_e32 v15, v15, v28
s_waitcnt lgkmcnt(8)
Mv 112,12,248
v_mul_f32_e32 v133, v133, v149
v_mul_f32_e32 v134, v134, v150
v_mul_f32_e32 v135, v135, v151
Cv 44,44,28
Mv 112,16,192
)" R"(
Cv 140,132,133
Cv 141,134,135
v_exp_f32_e32 v45, v86
Mv 116,16,196
v_accvgpr_write_b32 a188, v140
v_accvgpr_write_b32 a189, v141
Cv 248,248,249
v_add_f32_e32 v15, v15, v45
Mv 116,20,204
v_lshrrev_b32 v156, 2, v0
v_mul_u32_u24 v156, 864, v156
buffer_store_dwordx4 v[240:243], v3, s[20:23], s64 offen offset:2048 sc0 sc1
Mv 112,20,200
buffer_load_dwordx4 v3, s[20:23], s56 offen offset:1024 sc1 lds
Cv 249,250,251
Mv 112,24,208
v_lshl_add_u32 v156, v0, 3, v156
s_mul_i32 s92, s33, 224
Cv 250,252,253
Mv 116,24,212
s_add_u32 s92, s92, 131072
v_add_u32 v156, s92, v156
v_exp_f32_e32 v29, v87
Mv 116,28,220
Cv 251,254,255
v_exp_f32_e32 v46, v92
v_add_f32_e32 v15, v15, v29
Mv 112,28,216
v_exp_f32_e32 v24, v93
s_waitcnt vmcnt(31) lgkmcnt(0)
s_barrier
Mz 120,32,224
Rb 0,8,32768
Rt 156,156,0
buffer_store_dwordx4 v[248:251], v3, s[20:23], s64 offen offset:3072 sc0 sc1
Mz 124,32,228
Rb 4,8,33792
Rt 158,156,32
v_exp_f32_e32 v47, v94
Mv 124,36,236
Rb 8,8,34816
Rt 160,156,64
v_exp_f32_e32 v25, v95
Mv 120,36,232
Rb 12,8,35840
buffer_load_dwordx4 v3, s[20:23], s56 offen offset:2048 sc1 lds
Rt 162,156,96
Mv 120,40,240
Rb 16,8,36864
Rt 164,156,128
Cv 45,45,29
v_add_f32_e32 v15, v15, v46
Mv 124,40,244
Rb 20,8,37888
Rt 166,156,160
v_add_f32_e32 v15, v15, v24
Cv 46,46,24
Mv 124,44,252
Rb 24,8,38912
Rt 168,156,192
v_add_f32_e32 v15, v15, v47
v_add_f32_e32 v15, v15, v25
Mv 120,44,248
Rb 28,8,39936
Cv 47,47,25
)" R"(
v_max_f32_e32 v11, v14, v15
v_mov_b32 v24, v14
Mv 120,48,192
Rb 32,8,40960
v_mov_b32 v25, v15
s_add_u32 s55, s55, 0x10000
Mv 124,48,196
Rb 36,8,41984
s_cmp_eq_u32 s57, 2
s_cselect_b32 s55, s59, s55
v_permlane32_swap_b32_e32 v24, v25
Mv 124,52,204
Rb 40,8,43008
v_add_f32_e32 v24, v24, v25
v_mov_b32 v25, v24
Mv 120,52,200
Rb 44,8,44032
buffer_load_dwordx4 v3, s[20:23], s56 offen offset:3072 sc1 lds
v_permlane16_swap_b32_e32 v24, v25
Mv 120,56,208
v_add_f32_e32 v24, v24, v25
v_rcp_f32_e32 v24, v24
Mv 124,56,212
s_add_u32 s56, s56, 0x10000
v_max_f32_e32 v11, v11, v24
v_cmp_nge_f32_e32 vcc, s62, v11
Mv 124,60,220
v_accvgpr_mov_b32 a92, a252
s_waitcnt lgkmcnt(12)
s_cbranch_vccnz .Lzfx4
.Lzfr4:
Mv 120,60,216
v_mov_b32 v12, v14
v_mov_b32 v13, v15
v_accvgpr_mov_b32 a93, a253
v_mfma_f32_16x16x32_bf16 v[128:131], a[0:3], v[32:35], 0
Rb 48,8,45056
v_accvgpr_mov_b32 a94, a254
v_mfma_f32_16x16x32_bf16 v[136:139], a[0:3], v[36:39], 0
v_accvgpr_mov_b32 a95, a255
s_cmp_le_i32 s57, s58
s_cbranch_scc1 .Lzmk5
.Lzmr5:
v_mfma_f32_16x16x32_bf16 v[140:143], a[4:7], v[36:39], 0
Rb 52,8,46080
s_add_u32 m0, s36, 32768
v_accvgpr_mov_b32 a96, a192
v_mfma_f32_16x16x32_bf16 v[132:135], a[4:7], v[32:35], 0
buffer_load_dwordx4 v2, s[20:23], s55 offen offset:0 sc1 lds
v_accvgpr_mov_b32 a97, a193
v_mfma_f32_16x16x32_bf16 v[144:147], a[8:11], v[32:35], 0
Rb 56,8,47104
v_and_b32 v11, 14, v0
v_accvgpr_mov_b32 a98, a194
v_mfma_f32_16x16x32_bf16 v[152:155], a[8:11], v[36:39], 0
v_min_u32 v11, 1, v11
v_lshlrev_b32 v11, 13, v11
v_accvgpr_mov_b32 a99, a195
v_mfma_f32_16x16x32_bf16 v[156:159], a[12:15], v[36:39], 0
Rb 60,8,48128
v_accvgpr_mov_b32 a100, a196
s_waitcnt lgkmcnt(8)
v_mfma_f32_16x16x32_bf16 v[148:151], a[12:15], v[32:35], 0
v_mfma_f32_16x16x16_bf16 a[128:131], a[156:157], a[188:189], 0
)" R"(
v_and_or_b32 v11, v0, 1, v11
v_mul_u32_u24 v11, 0x31000, v11
v_accvgpr_mov_b32 a101, a197
v_mfma_f32_16x16x32_bf16 v[160:163], a[16:19], v[32:35], 0
v_mfma_f32_16x16x16_bf16 a[132:135], a[158:159], a[188:189], 0
Rb 0,8,49152
v_and_or_b32 v11, v0, 48, v11
v_accvgpr_mov_b32 a102, a198
v_mfma_f32_16x16x32_bf16 v[168:171], a[16:19], v[36:39], 0
v_mfma_f32_16x16x16_bf16 a[136:139], a[160:161], a[188:189], 0
s_mul_i32 s83, s33, 50176
s_lshl_b32 s92, s82, 6
v_accvgpr_mov_b32 a103, a199
v_mfma_f32_16x16x32_bf16 v[172:175], a[20:23], v[36:39], 0
v_mfma_f32_16x16x16_bf16 a[140:143], a[162:163], a[188:189], 0
Rb 4,8,50176
s_add_u32 s83, s83, s92
v_accvgpr_mov_b32 a104, a200
v_mfma_f32_16x16x32_bf16 v[164:167], a[20:23], v[32:35], 0
v_mfma_f32_16x16x16_bf16 a[144:147], a[164:165], a[188:189], 0
buffer_load_dwordx4 v2, s[20:23], s55 offen offset:1024 sc1 lds
v_accvgpr_mov_b32 a105, a201
v_mfma_f32_16x16x32_bf16 v[176:179], a[24:27], v[32:35], 0
v_mfma_f32_16x16x16_bf16 a[148:151], a[166:167], a[188:189], 0
Rb 8,8,51200
s_add_u32 s83, s83, 0xc158000
v_accvgpr_mov_b32 a106, a202
v_mfma_f32_16x16x32_bf16 v[184:187], a[24:27], v[36:39], 0
v_mfma_f32_16x16x16_bf16 a[152:155], a[168:169], a[188:189], 0
v_exp_f32_e32 v48, v96
v_accvgpr_mov_b32 a107, a203
v_mfma_f32_16x16x32_bf16 v[188:191], a[28:31], v[36:39], 0
Rb 12,8,52224
v_accvgpr_mov_b32 a108, a204
s_waitcnt lgkmcnt(8)
v_mfma_f32_16x16x32_bf16 v[180:183], a[28:31], v[32:35], 0
v_exp_f32_e32 v26, v97
v_accvgpr_mov_b32 a109, a205
v_mfma_f32_16x16x32_bf16 v[192:195], a[32:35], v[32:35], 0
Rb 16,8,53248
v_add_f32_e32 v14, v48, v26
v_mfma_f32_16x16x32_bf16 v[200:203], a[32:35], v[36:39], 0
v_exp_f32_e32 v49, v98
Cv 48,48,26
v_mfma_f32_16x16x32_bf16 v[204:207], a[36:39], v[36:39], 0
Rb 20,8,54272
v_add_f32_e32 v14, v14, v49
v_accvgpr_mov_b32 a110, a206
v_mfma_f32_16x16x32_bf16 v[196:199], a[36:39], v[32:35], 0
buffer_load_dwordx4 v2, s[20:23], s55 offen offset:2048 sc1 lds
v_accvgpr_mov_b32 a111, a207
v_mfma_f32_16x16x32_bf16 v[208:211], a[40:43], v[32:35], 0
Rb 24,8,55296
v_exp_f32_e32 v27, v99
v_mfma_f32_16x16x32_bf16 v[216:219], a[40:43], v[36:39], 0
buffer_store_dwordx4 a[128:131], v11, s[20:23], s83 offen sc0 sc1
v_accvgpr_mov_b32 a112, a208
v_mfma_f32_16x16x32_bf16 v[220:223], a[44:47], v[36:39], 0
Rb 28,8,56320
v_accvgpr_mov_b32 a113, a209
s_waitcnt lgkmcnt(8)
v_mfma_f32_16x16x32_bf16 v[212:215], a[44:47], v[32:35], 0
s_add_u32 s83, s83, 7168
v_exp_f32_e32 v50, v104
)" R"(
v_mfma_f32_16x16x32_bf16 v[224:227], a[48:51], v[32:35], 0
Rb 32,8,57344
v_add_f32_e32 v14, v14, v27
v_accvgpr_mov_b32 a114, a210
v_mfma_f32_16x16x32_bf16 v[232:235], a[48:51], v[36:39], 0
v_exp_f32_e32 v28, v105
Cv 49,49,27
v_mfma_f32_16x16x32_bf16 v[236:239], a[52:55], v[36:39], 0
Rb 36,8,58368
v_add_f32_e32 v14, v14, v50
v_accvgpr_mov_b32 a115, a211
v_mfma_f32_16x16x32_bf16 v[228:231], a[52:55], v[32:35], 0
buffer_load_dwordx4 v2, s[20:23], s55 offen offset:3072 sc1 lds
v_accvgpr_mov_b32 a116, a212
v_mfma_f32_16x16x32_bf16 v[240:243], a[56:59], v[32:35], 0
Rb 40,8,59392
v_exp_f32_e32 v51, v106
v_mfma_f32_16x16x32_bf16 v[248:251], a[56:59], v[36:39], 0
buffer_store_dwordx4 a[132:135], v11, s[20:23], s83 offen sc0 sc1
v_accvgpr_mov_b32 a117, a213
v_mfma_f32_16x16x32_bf16 v[252:255], a[60:63], v[36:39], 0
Rb 44,8,60416
v_accvgpr_mov_b32 a118, a214
s_waitcnt lgkmcnt(8)
v_mfma_f32_16x16x32_bf16 v[244:247], a[60:63], v[32:35], 0
s_add_u32 s83, s83, 7168
v_exp_f32_e32 v29, v107
Mp 128,0,40
Rb 48,8,61440
v_add_f32_e32 v14, v14, v28
Mp 136,0,44
buffer_store_dwordx4 a[136:139], v11, s[20:23], s83 offen sc0 sc1
s_add_u32 s83, s83, 7168
Mp 140,4,44
Rb 52,8,62464
s_add_u32 m0, s36, 36864
v_accvgpr_mov_b32 a119, a215
Mp 132,4,40
buffer_load_dwordx4 v3, s[20:23], s55 offen offset:0 sc1 lds
v_accvgpr_mov_b32 a120, a216
Mp 144,8,40
Rb 56,8,63488
v_exp_f32_e32 v52, v100
Mp 152,8,44
v_exp_f32_e32 v24, v101
v_accvgpr_mov_b32 a121, a217
Mp 156,12,44
Rb 60,8,64512
v_accvgpr_mov_b32 a122, a218
s_waitcnt lgkmcnt(8)
Mp 148,12,40
buffer_store_dwordx4 a[140:143], v11, s[20:23], s83 offen sc0 sc1
Cv 50,50,28
Mp 160,16,40
s_add_u32 s83, s83, 7168
v_add_f32_e32 v14, v14, v51
v_accvgpr_mov_b32 a123, a219
Mp 168,16,44
v_exp_f32_e32 v53, v102
v_add_f32_e32 v14, v14, v29
Mp 172,20,44
v_exp_f32_e32 v25, v103
v_accvgpr_mov_b32 a124, a220
Mp 164,20,40
)" R"(
buffer_load_dwordx4 v3, s[20:23], s55 offen offset:1024 sc1 lds
v_accvgpr_mov_b32 a125, a221
Mp 176,24,40
v_exp_f32_e32 v54, v108
Cv 51,51,29
Mp 184,24,44
buffer_store_dwordx4 a[144:147], v11, s[20:23], s83 offen sc0 sc1
v_accvgpr_mov_b32 a126, a222
Mp 188,28,44
s_add_u32 s83, s83, 7168
v_add_f32_e32 v15, v52, v24
v_accvgpr_mov_b32 a127, a223
Mp 180,28,40
Cv 52,52,24
s_waitcnt vmcnt(13) lgkmcnt(0)
s_barrier
buffer_store_dword v6, v7, s[20:23], s65 offen offset:0 sc0 sc1
Mp 192,32,40
Rb 0,1,0
v_add_f32_e32 v15, v15, v53
Mp 200,32,44
Rb 4,1,1024
v_exp_f32_e32 v26, v109
Mp 204,36,44
Rb 8,1,2048
v_add_f32_e32 v15, v15, v25
Mp 196,36,40
Rb 12,1,3072
buffer_load_dwordx4 v3, s[20:23], s55 offen offset:2048 sc1 lds
Mp 208,40,40
Rb 16,1,8192
v_exp_f32_e32 v55, v110
Mp 216,40,44
Rb 20,1,9216
Cv 53,53,25
Mp 220,44,44
Rb 24,1,10240
buffer_store_dwordx4 a[148:151], v11, s[20:23], s83 offen sc0 sc1
Mp 212,44,40
Rb 28,1,11264
s_add_u32 s83, s83, 7168
v_add_f32_e32 v15, v15, v54
Mp 224,48,40
Rb 32,1,4096
v_add_f32_e32 v15, v15, v26
Mp 232,48,44
Rb 36,1,5120
v_exp_f32_e32 v27, v111
Mp 236,52,44
Rb 40,1,6144
Cv 54,54,26
Mp 228,52,40
Rb 44,1,7168
buffer_load_dwordx4 v3, s[20:23], s55 offen offset:3072 sc1 lds
Mp 240,56,40
v_cmp_eq_u32_e32 vcc, s3, v4
s_andn2_b64 s[28:29], s[60:61], vcc
s_cbranch_scc1 .Lzsp10
.Lzok10:
Mp 248,56,44
buffer_store_dwordx4 a[152:155], v11, s[20:23], s83 offen sc0 sc1
Mp 252,60,44
v_add_f32_e32 v15, v15, v55
s_waitcnt lgkmcnt(8)
Mp 244,60,40
)" R"(
v_add_f32_e32 v15, v15, v27
Cv 55,55,27
s_sub_u32 s57, s57, 1
s_cmp_gt_i32 s57, s93
s_cbranch_scc1 .LzL
.LzDA:
s_waitcnt vmcnt(8) lgkmcnt(0)
s_barrier
Rb 0,8,0
Rb 4,8,16384
Rb 8,8,1024
Rb 12,8,17408
Rb 16,8,2048
Rb 20,8,18432
Rb 24,8,3072
Rb 28,8,19456
Rb 32,8,4096
Rb 36,8,20480
Rb 40,8,5120
Rb 44,8,21504
v_exp_f32_e32 v56, v112
v_exp_f32_e32 v28, v113
v_add_f32_e32 v14, v14, v56
v_add_f32_e32 v14, v14, v28
Cv 56,56,28
v_exp_f32_e32 v57, v114
v_exp_f32_e32 v29, v115
v_add_f32_e32 v14, v14, v57
v_add_f32_e32 v14, v14, v29
Cv 57,57,29
v_exp_f32_e32 v58, v120
v_exp_f32_e32 v24, v121
v_add_f32_e32 v14, v14, v58
v_add_f32_e32 v14, v14, v24
Cv 58,58,24
v_exp_f32_e32 v59, v122
v_exp_f32_e32 v25, v123
v_add_f32_e32 v14, v14, v59
v_add_f32_e32 v14, v14, v25
Cv 59,59,25
v_exp_f32_e32 v60, v116
v_exp_f32_e32 v26, v117
v_add_f32_e32 v15, v15, v60
v_add_f32_e32 v15, v15, v26
Cv 60,60,26
v_exp_f32_e32 v61, v118
v_exp_f32_e32 v27, v119
v_add_f32_e32 v15, v15, v61
v_add_f32_e32 v15, v15, v27
Cv 61,61,27
v_exp_f32_e32 v62, v124
v_exp_f32_e32 v28, v125
v_add_f32_e32 v15, v15, v62
v_add_f32_e32 v15, v15, v28
Cv 62,62,28
v_exp_f32_e32 v63, v126
v_exp_f32_e32 v29, v127
v_add_f32_e32 v15, v15, v63
v_add_f32_e32 v15, v15, v29
Cv 63,63,29
v_max_f32_e32 v11, v14, v15
v_cmp_nge_f32_e32 vcc, s62, v11
s_cbranch_vccnz .Lzfx5
.Lzfr5:
s_waitcnt lgkmcnt(8)
)" R"(
Mp 128,0,48
Rb 48,8,6144
s_add_u32 m0, s36, 131072
s_add_u32 s40, s67, 0x20000
s_lshl_b32 s82, s82, 7
Mp 136,0,52
buffer_load_dwordx4 v2, s[20:23], s40 offen offset:0 sc1 lds
v_add_f32_e32 v12, v12, v14
s_add_u32 s82, s82, 0xc10c000
Mp 136,4,60
Rb 52,8,22528
v_add_f32_e32 v13, v13, v15
v_mov_b32 v24, v12
Mp 128,4,56
buffer_load_dwordx4 v2, s[20:23], s40 offen offset:1024 sc1 lds
v_permlane32_swap_b32_e32 v12, v24
Mp 132,8,48
Rb 56,8,7168
v_add_f32_e32 v12, v12, v24
v_mov_b32 v24, v12
Mp 140,8,52
s_nop 0
v_permlane16_swap_b32_e32 v12, v24
v_add_f32_e32 v12, v12, v24
v_mov_b32 v25, v13
Mp 140,12,60
Rb 60,8,23552
v_permlane32_swap_b32_e32 v13, v25
s_waitcnt lgkmcnt(8)
Mp 132,12,56
buffer_load_dwordx4 v2, s[20:23], s40 offen offset:2048 sc1 lds
v_add_f32_e32 v13, v13, v25
Mp 144,16,48
Rb 0,8,8192
v_rcp_f32_e32 v12, v12
Mp 152,16,52
buffer_load_dwordx4 v2, s[20:23], s40 offen offset:3072 sc1 lds
v_mov_b32 v25, v13
Mp 152,20,60
Rb 4,8,24576
v_permlane16_swap_b32_e32 v13, v25
v_add_f32_e32 v13, v13, v25
Mp 144,20,56
v_rcp_f32_e32 v13, v13
v_mul_f32_e32 v128, v12, v128
Mp 148,24,48
Rb 8,8,9216
s_add_u32 m0, s36, 135168
v_mul_f32_e32 v129, v12, v129
Mp 156,24,52
buffer_load_dwordx4 v3, s[20:23], s40 offen offset:0 sc1 lds
Cv 128,128,129
Mp 156,28,60
Rb 12,8,25600
v_mul_f32_e32 v130, v12, v130
s_waitcnt lgkmcnt(8)
Mp 148,28,56
buffer_load_dwordx4 v3, s[20:23], s40 offen offset:1024 sc1 lds
v_mul_f32_e32 v131, v12, v131
Mp 160,32,48
Rb 16,8,10240
Cv 129,130,131
s_cmp_lg_u32 s33, 0
s_cmov_b32 s82, 0x40000000
)" R"(
Mp 168,32,52
v_mul_f32_e32 v132, v12, v132
v_mul_f32_e32 v133, v12, v133
Cv 130,132,133
Mp 168,36,60
Rb 20,8,26624
v_mul_f32_e32 v134, v12, v134
v_mul_f32_e32 v135, v12, v135
Mp 160,36,56
buffer_load_dwordx4 v3, s[20:23], s40 offen offset:2048 sc1 lds
Cv 131,134,135
Mp 164,40,48
Rb 24,8,11264
s_add_u32 m0, s36, 98304
s_add_u32 s40, s67, 0x30000
Mp 172,40,52
buffer_load_dwordx4 v2, s[20:23], s40 offen offset:0 sc1 lds
v_mul_f32_e32 v136, v13, v136
Mp 172,44,60
Rb 28,8,27648
v_mul_f32_e32 v137, v13, v137
s_waitcnt lgkmcnt(8)
Mp 164,44,56
Cv 136,136,137
v_mul_f32_e32 v138, v13, v138
v_mul_f32_e32 v139, v13, v139
Mp 176,48,48
Rb 32,8,12288
Cv 137,138,139
v_mul_f32_e32 v140, v13, v140
Mp 184,48,52
buffer_load_dwordx4 v2, s[20:23], s40 offen offset:1024 sc1 lds
v_mul_f32_e32 v141, v13, v141
Mp 184,52,60
Rb 36,8,28672
Cv 138,140,141
v_mul_f32_e32 v142, v13, v142
Mp 176,52,56
buffer_load_dwordx4 v2, s[20:23], s40 offen offset:2048 sc1 lds
v_mul_f32_e32 v143, v13, v143
Mp 180,56,48
Rb 40,8,13312
Cv 139,142,143
v_mul_f32_e32 v144, v12, v144
Mp 188,56,52
v_mul_f32_e32 v145, v12, v145
Cv 144,144,145
v_mul_f32_e32 v146, v12, v146
Mp 188,60,60
Rb 44,8,29696
v_mul_f32_e32 v147, v12, v147
s_waitcnt lgkmcnt(8)
Mp 180,60,56
buffer_load_dwordx4 v2, s[20:23], s40 offen offset:3072 sc1 lds
Cv 145,146,147
Mp 192,0,48
Rb 48,8,14336
s_add_u32 m0, s36, 102400
s_cmp_eq_u32 s35, 7
s_cmov_b32 s82, 0x40000000
Mp 200,0,52
buffer_load_dwordx4 v3, s[20:23], s40 offen offset:0 sc1 lds
v_mul_f32_e32 v148, v12, v148
Mp 200,4,60
)" R"(
Rb 52,8,30720
v_mul_f32_e32 v149, v12, v149
Cv 146,148,149
Mp 192,4,56
v_mul_f32_e32 v150, v12, v150
v_mul_f32_e32 v151, v12, v151
Cv 147,150,151
Mp 196,8,48
Rb 56,8,15360
v_mul_f32_e32 v152, v13, v152
v_mul_f32_e32 v153, v13, v153
Mp 204,8,52
buffer_load_dwordx4 v3, s[20:23], s40 offen offset:1024 sc1 lds
Cv 152,152,153
Mp 204,12,60
Rb 60,8,31744
v_mul_f32_e32 v154, v13, v154
s_waitcnt lgkmcnt(8)
Mp 196,12,56
buffer_load_dwordx4 v3, s[20:23], s40 offen offset:2048 sc1 lds
v_mul_f32_e32 v155, v13, v155
Mp 208,16,48
Rb 0,1,0
Cv 153,154,155
v_mul_f32_e32 v156, v13, v156
Mp 216,16,52
v_mul_f32_e32 v157, v13, v157
Cv 154,156,157
v_mul_f32_e32 v158, v13, v158
Mp 216,20,60
Rb 4,1,1024
v_mul_f32_e32 v159, v13, v159
Cv 155,158,159
Mp 208,20,56
v_mul_f32_e32 v160, v12, v160
v_mul_f32_e32 v161, v12, v161
Cv 160,160,161
Mp 212,24,48
Rb 8,1,2048
v_mul_f32_e32 v162, v12, v162
v_mul_f32_e32 v163, v12, v163
Mp 220,24,52
Cv 161,162,163
v_mul_f32_e32 v164, v12, v164
v_mul_f32_e32 v165, v12, v165
Mp 220,28,60
Rb 12,1,3072
s_waitcnt vmcnt(14) lgkmcnt(4)
s_barrier
Mp 212,28,56
s_add_u32 s40, s67, 0x40000
Cv 162,164,165
v_mul_f32_e32 v166, v12, v166
Mp 224,32,48
Rb 16,1,4096
s_add_u32 m0, s36, 65536
Mp 232,32,52
buffer_load_dwordx4 v2, s[20:23], s40 offen offset:0 sc1 lds
v_mul_f32_e32 v167, v12, v167
Mp 232,36,60
Rb 20,1,5120
Cv 163,166,167
v_mul_f32_e32 v168, v13, v168
Mp 224,36,56
v_mul_f32_e32 v169, v13, v169
)" R"(
Cv 168,168,169
v_mul_f32_e32 v170, v13, v170
Mp 228,40,48
Rb 24,1,6144
v_mul_f32_e32 v171, v13, v171
Cv 169,170,171
Mp 236,40,52
v_mul_f32_e32 v172, v13, v172
v_mul_f32_e32 v173, v13, v173
Cv 170,172,173
Mp 236,44,60
Rb 28,1,8192
v_mul_f32_e32 v174, v13, v174
Mp 228,44,56
buffer_load_dwordx4 v2, s[20:23], s40 offen offset:1024 sc1 lds
v_mul_f32_e32 v175, v13, v175
s_lshl4_add_u32 s28, s34, s35
Mp 240,48,48
Rb 32,1,9216
Cv 171,174,175
v_mul_f32_e32 v176, v12, v176
Mp 248,48,52
v_mul_f32_e32 v177, v12, v177
Cv 176,176,177
v_mul_f32_e32 v178, v12, v178
Mp 248,52,60
Rb 36,1,10240
v_mul_f32_e32 v179, v12, v179
Cv 177,178,179
Mp 240,52,56
v_mul_f32_e32 v180, v12, v180
v_mul_f32_e32 v181, v12, v181
Cv 178,180,181
Mp 244,56,48
Rb 40,1,11264
v_mul_f32_e32 v182, v12, v182
v_mul_f32_e32 v183, v12, v183
s_lshl_b32 s64, s28, 17
Mp 252,56,52
buffer_load_dwordx4 v2, s[20:23], s40 offen offset:2048 sc1 lds
Cv 179,182,183
Mp 252,60,60
Rb 44,1,12288
v_mul_f32_e32 v184, v13, v184
s_mov_b32 s55, 0xc108c00
s_waitcnt lgkmcnt(8)
Mp 244,60,56
v_mul_f32_e32 v185, v13, v185
Cv 184,184,185
v_mul_f32_e32 v186, v13, v186
v_mfma_f32_16x16x32_bf16 v[32:35], a[0:3], v[128:131], 0
Rb 48,1,13312
v_mul_f32_e32 v187, v13, v187
s_add_u32 s64, s64, 0x4000000
v_mfma_f32_16x16x32_bf16 v[40:43], a[0:3], v[136:139], 0
Cv 185,186,187
v_mul_f32_e32 v188, v13, v188
v_mul_f32_e32 v189, v13, v189
s_xor_b32 s66, s64, 0x202000
v_mfma_f32_16x16x32_bf16 v[44:47], a[4:7], v[136:139], 0
Rb 52,1,14336
Cv 186,188,189
v_mul_f32_e32 v190, v13, v190
v_mfma_f32_16x16x32_bf16 v[36:39], a[4:7], v[128:131], 0
)" R"(
buffer_load_dwordx4 v2, s[20:23], s40 offen offset:3072 sc1 lds
v_mul_f32_e32 v191, v13, v191
v_mfma_f32_16x16x32_bf16 v[48:51], a[8:11], v[128:131], 0
Rb 56,1,16384
Cv 187,190,191
v_mul_f32_e32 v192, v12, v192
s_cmp_lg_u32 s33, 0
s_cmov_b32 s55, 0x40000000
v_mfma_f32_16x16x32_bf16 v[56:59], a[8:11], v[136:139], 0
v_mul_f32_e32 v193, v12, v193
Cv 192,192,193
v_mul_f32_e32 v194, v12, v194
v_mov_b32 v16, 1
v_mfma_f32_16x16x32_bf16 v[60:63], a[12:15], v[136:139], 0
Rb 60,1,17408
v_mul_f32_e32 v195, v12, v195
v_add_u32 v9, s36, v8
s_waitcnt lgkmcnt(8)
v_mfma_f32_16x16x32_bf16 v[52:55], a[12:15], v[128:131], 0
Cv 193,194,195
v_mul_f32_e32 v196, v12, v196
v_mul_f32_e32 v197, v12, v197
s_sub_u32 s66, s66, s84
Mp 32,16,144
Rb 0,1,18432
s_add_u32 m0, s36, 69632
Cv 194,196,197
Mp 40,16,152
buffer_load_dwordx4 v3, s[20:23], s40 offen offset:0 sc1 lds
v_mul_f32_e32 v198, v12, v198
Mp 44,20,152
Rb 4,1,19456
v_mul_f32_e32 v199, v12, v199
Cv 195,198,199
Mp 36,20,144
v_mul_f32_e32 v200, v13, v200
v_mul_f32_e32 v201, v13, v201
Cv 200,200,201
Mp 48,24,144
Rb 8,1,20480
v_mul_f32_e32 v202, v13, v202
v_mul_f32_e32 v203, v13, v203
s_sub_u32 s66, s66, s84
Mp 56,24,152
Cv 201,202,203
v_mul_f32_e32 v204, v13, v204
v_mul_f32_e32 v205, v13, v205
Mp 60,28,152
Rb 12,1,21504
Cv 202,204,205
s_waitcnt lgkmcnt(8)
Mp 52,28,144
buffer_load_dwordx4 v3, s[20:23], s40 offen offset:1024 sc1 lds
v_mul_f32_e32 v206, v13, v206
Mp 32,32,160
Rb 16,1,22528
v_mul_f32_e32 v207, v13, v207
s_lshl2_add_u32 s65, s2, s33
Mp 40,32,168
Cv 203,206,207
v_mul_f32_e32 v208, v12, v208
v_mul_f32_e32 v209, v12, v209
s_lshl_b32 s65, s65, 7
Mp 44,36,168
)" R"(
Rb 20,1,24576
Cv 208,208,209
v_mul_f32_e32 v210, v12, v210
Mp 36,36,160
v_mul_f32_e32 v211, v12, v211
Cv 209,210,211
v_mul_f32_e32 v212, v12, v212
Mp 48,40,160
Rb 24,1,25600
v_mul_f32_e32 v213, v12, v213
Cv 210,212,213
Mp 56,40,168
buffer_load_dwordx4 v3, s[20:23], s40 offen offset:2048 sc1 lds
v_mul_f32_e32 v214, v12, v214
Mp 60,44,168
Rb 28,1,26624
v_mul_f32_e32 v215, v12, v215
s_waitcnt lgkmcnt(8)
Mp 52,44,160
Cv 211,214,215
v_mul_f32_e32 v216, v13, v216
v_mul_f32_e32 v217, v13, v217
s_add_u32 s65, s65, 0xc1c0000
Mp 32,48,176
Rb 32,1,27648
Cv 216,216,217
v_mul_f32_e32 v218, v13, v218
Mp 40,48,184
v_mul_f32_e32 v219, v13, v219
Cv 217,218,219
v_mul_f32_e32 v220, v13, v220
Mp 44,52,184
Rb 36,1,28672
v_mul_f32_e32 v221, v13, v221
Cv 218,220,221
Mp 36,52,176
v_mul_f32_e32 v222, v13, v222
v_mul_f32_e32 v223, v13, v223
Cv 219,222,223
Mp 48,56,176
Rb 40,1,29696
v_mul_f32_e32 v224, v12, v224
v_mul_f32_e32 v225, v12, v225
s_xor_b32 s52, s65, 4096
Mp 56,56,184
Cv 224,224,225
v_mul_f32_e32 v226, v12, v226
v_mul_f32_e32 v227, v12, v227
Mp 60,60,184
Rb 44,1,30720
Cv 225,226,227
buffer_atomic_add v16, v7, s[20:23], s55 offen sc0 sc1
s_waitcnt lgkmcnt(8)
Mp 52,60,176
v_mul_f32_e32 v228, v12, v228
v_mul_f32_e32 v229, v12, v229
Cv 226,228,229
Mp 32,0,192
Rb 48,1,32768
v_mul_f32_e32 v230, v12, v230
Mp 40,0,200
v_mul_f32_e32 v231, v12, v231
Cv 227,230,231
v_mul_f32_e32 v232, v13, v232
)" R"(
Mp 44,4,200
Rb 52,1,33792
v_mul_f32_e32 v233, v13, v233
Cv 232,232,233
Mp 36,4,192
v_mul_f32_e32 v234, v13, v234
v_mul_f32_e32 v235, v13, v235
Cv 233,234,235
Mp 48,8,192
Rb 56,1,34816
v_mul_f32_e32 v236, v13, v236
v_mul_f32_e32 v237, v13, v237
Mp 56,8,200
Cv 234,236,237
v_mul_f32_e32 v238, v13, v238
v_mul_f32_e32 v239, v13, v239
Mp 60,12,200
Rb 60,1,35840
s_waitcnt vmcnt(15) lgkmcnt(4)
s_barrier
buffer_store_dword v6, v7, s[20:23], s82 offen offset:0 sc0 sc1
Mp 52,12,192
s_add_u32 s40, s67, 0x50000
Cv 235,238,239
v_mul_f32_e32 v240, v12, v240
Mp 32,16,208
Rb 0,1,36864
s_add_u32 m0, s36, 0
v_mul_f32_e32 v241, v12, v241
Mp 40,16,216
buffer_load_dwordx4 v2, s[20:23], s40 offen offset:0 sc1 lds
Cv 240,240,241
Mp 44,20,216
Rb 4,1,37888
v_mul_f32_e32 v242, v12, v242
v_mul_f32_e32 v243, v12, v243
Mp 36,20,208
Cv 241,242,243
v_mul_f32_e32 v244, v12, v244
v_mul_f32_e32 v245, v12, v245
Mp 48,24,208
Rb 8,1,38912
Cv 242,244,245
v_mul_f32_e32 v246, v12, v246
Mp 56,24,216
v_mul_f32_e32 v247, v12, v247
Cv 243,246,247
v_mul_f32_e32 v248, v13, v248
Mp 60,28,216
Rb 12,1,40960
v_mul_f32_e32 v249, v13, v249
Mp 52,28,208
buffer_load_dwordx4 v2, s[20:23], s40 offen offset:1024 sc1 lds
Cv 248,248,249
Mp 32,32,224
Rb 16,1,41984
v_mul_f32_e32 v250, v13, v250
Mp 40,32,232
v_mul_f32_e32 v251, v13, v251
Cv 249,250,251
v_mul_f32_e32 v252, v13, v252
Mp 44,36,232
Rb 20,1,43008
v_mul_f32_e32 v253, v13, v253
Cv 250,252,253
)" R"(
Mp 36,36,224
v_mul_f32_e32 v254, v13, v254
v_mul_f32_e32 v255, v13, v255
Cv 251,254,255
Mp 48,40,224
Rb 24,1,44032
Mp 56,40,232
buffer_load_dwordx4 v2, s[20:23], s40 offen offset:2048 sc1 lds
Mp 60,44,232
Rb 28,1,45056
s_waitcnt lgkmcnt(8)
Mp 52,44,224
Mp 32,48,240
Rb 32,1,46080
Mp 40,48,248
Mp 44,52,248
Rb 36,1,47104
Mp 36,52,240
buffer_load_dwordx4 v2, s[20:23], s40 offen offset:3072 sc1 lds
Mp 48,56,240
Rb 40,1,49152
Mp 56,56,248
Mp 60,60,248
Rb 44,1,50176
s_waitcnt lgkmcnt(8)
Mp 52,60,240
v_mfma_f32_16x16x32_bf16 v[64:67], a[0:3], v[128:131], 0
Rb 48,1,51200
s_add_u32 m0, s36, 4096
v_mfma_f32_16x16x32_bf16 v[72:75], a[0:3], v[136:139], 0
buffer_load_dwordx4 v3, s[20:23], s40 offen offset:0 sc1 lds
Mp 72,4,152
Rb 52,1,52224
Mp 64,4,144
Cv 32,32,33
Cv 33,34,35
Mp 64,8,160
Rb 56,1,53248
Cv 34,36,37
Mp 72,8,168
Cv 35,38,39
Mp 72,12,184
Rb 60,1,54272
s_waitcnt lgkmcnt(8)
Mp 64,12,176
buffer_load_dwordx4 v3, s[20:23], s40 offen offset:1024 sc1 lds
Mp 64,16,192
Rb 0,1,55296
Mp 72,16,200
buffer_store_dwordx4 v[32:35], v2, s[20:23], s64 offen offset:0 sc0 sc1
Mp 72,20,216
Rb 4,1,57344
Mp 64,20,208
Mp 64,24,224
Rb 8,1,58368
Mp 72,24,232
buffer_load_dwordx4 v3, s[20:23], s40 offen offset:2048 sc1 lds
Mp 72,28,248
Rb 12,1,59392
s_waitcnt lgkmcnt(8)
Mp 64,28,240
Cv 40,40,41
Cv 41,42,43
v_mfma_f32_16x16x32_bf16 v[68:71], a[32:35], v[128:131], 0
)" R"(
Rb 16,1,60416
Cv 42,44,45
v_mfma_f32_16x16x32_bf16 v[76:79], a[32:35], v[136:139], 0
Cv 43,46,47
Mp 76,36,152
Rb 20,1,61440
Mp 68,36,144
buffer_store_dwordx4 v[40:43], v2, s[20:23], s64 offen offset:1024 sc0 sc1
Mp 68,40,160
Rb 24,1,62464
Mp 76,40,168
Mp 76,44,184
Rb 28,1,63488
s_waitcnt lgkmcnt(8)
Mp 68,44,176
Mp 68,48,192
Rb 32,10,0
Mp 76,48,200
Cv 48,48,49
Cv 49,50,51
Mp 76,52,216
Rb 36,10,1024
Cv 50,52,53
Mp 68,52,208
Cv 51,54,55
Mp 68,56,224
Rb 40,10,2048
Mp 76,56,232
buffer_store_dwordx4 v[48:51], v2, s[20:23], s64 offen offset:2048 sc0 sc1
Mp 76,60,248
Rb 44,10,3072
s_waitcnt vmcnt(19) lgkmcnt(4)
s_barrier
Mp 68,60,240
s_add_u32 s40, s67, 0x60000
v_mfma_f32_16x16x32_bf16 v[80:83], a[0:3], v[128:131], 0
Rb 48,10,4096
s_add_u32 m0, s36, 32768
v_mfma_f32_16x16x32_bf16 v[88:91], a[0:3], v[136:139], 0
buffer_load_dwordx4 v2, s[20:23], s40 offen offset:0 sc1 lds
Mp 88,4,152
Rb 52,10,5120
Mp 80,4,144
Cv 56,56,57
Cv 57,58,59
Cv 58,60,61
Mp 80,8,160
Rb 56,10,6144
v_bfe_u32 v11, v0, 2, 2
Cv 59,62,63
Mp 88,8,168
v_lshrrev_b32 v4, 4, v0
v_xor_b32 v11, v11, v4
Mp 88,12,184
Rb 60,10,8192
Mp 80,12,176
buffer_load_dwordx4 v2, s[20:23], s40 offen offset:1024 sc1 lds
Mp 80,16,192
Rb 0,10,9216
v_bfe_u32 v4, v0, 1, 1
Mp 88,16,200
v_lshl_or_b32 v11, v11, 1, v4
v_and_b32 v4, 1, v0
Mp 88,20,216
Rb 4,10,10240
)" R"(
v_mul_u32_u24 v4, 0xffff, v4
buffer_store_dwordx4 v[56:59], v2, s[20:23], s64 offen offset:3072 sc0 sc1
Mp 80,20,208
v_add_u32 v4, 1, v4
v_mul_u32_u24 v4, 0x3f80, v4
Mp 80,24,224
Rb 8,10,11264
v_cmp_eq_u32_e32 vcc, 0, v11
Mp 88,24,232
buffer_load_dwordx4 v2, s[20:23], s40 offen offset:2048 sc1 lds
Mp 88,28,248
Rb 12,10,12288
s_waitcnt lgkmcnt(8)
Mp 80,28,240
v_cmp_eq_u32_e64 s[28:29], 1, v11
v_accvgpr_write_b32 a192, 0
v_mfma_f32_16x16x32_bf16 v[84:87], a[32:35], v[128:131], 0
Rb 16,10,13312
v_accvgpr_write_b32 a193, 0
v_mfma_f32_16x16x32_bf16 v[92:95], a[32:35], v[136:139], 0
v_accvgpr_write_b32 a196, 0
v_accvgpr_write_b32 a197, 0
Cv 64,64,65
Mp 92,36,152
Rb 20,10,14336
v_cndmask_b32_e32 v11, 0, v4, vcc
Mp 84,36,144
buffer_load_dwordx4 v2, s[20:23], s40 offen offset:3072 sc1 lds
Cv 65,66,67
Mp 84,40,160
Rb 24,10,16384
v_cndmask_b32_e64 v4, 0, v4, s[28:29]
Mp 92,40,168
v_accvgpr_write_b32 a194, v11
v_accvgpr_write_b32 a195, v4
Cv 66,68,69
Mp 92,44,184
Rb 28,10,17408
s_waitcnt lgkmcnt(8)
Mp 84,44,176
Cv 67,70,71
buffer_store_dwordx4 v[64:67], v3, s[20:23], s64 offen offset:0 sc0 sc1
Mp 84,48,192
Rb 32,10,18432
s_add_u32 m0, s36, 36864
Mp 92,48,200
buffer_load_dwordx4 v3, s[20:23], s40 offen offset:0 sc1 lds
Mp 92,52,216
Rb 36,10,19456
Mp 84,52,208
Mp 84,56,224
Rb 40,10,20480
Mp 92,56,232
Cv 72,72,73
Cv 73,74,75
Mp 92,60,248
Rb 44,10,21504
s_waitcnt lgkmcnt(8)
Mp 84,60,240
buffer_load_dwordx4 v3, s[20:23], s40 offen offset:1024 sc1 lds
v_mfma_f32_16x16x32_bf16 v[96:99], a[0:3], v[128:131], 0
Rb 48,10,22528
Cv 74,76,77
v_mfma_f32_16x16x32_bf16 v[104:107], a[0:3], v[136:139], 0
)" R"(
Cv 75,78,79
Mp 104,4,152
Rb 52,10,24576
Mp 96,4,144
buffer_store_dwordx4 v[72:75], v3, s[20:23], s64 offen offset:1024 sc0 sc1
Mp 96,8,160
Rb 56,10,25600
Mp 104,8,168
buffer_load_dwordx4 v3, s[20:23], s40 offen offset:2048 sc1 lds
Mp 104,12,184
Rb 60,10,26624
s_waitcnt lgkmcnt(8)
Mp 96,12,176
Mp 96,16,192
Rb 0,10,27648
Mp 104,16,200
Cv 80,80,81
Cv 81,82,83
Mp 104,20,216
Rb 4,10,28672
Cv 82,84,85
Mp 96,20,208
Cv 83,86,87
Mp 96,24,224
Rb 8,10,29696
Mp 104,24,232
buffer_store_dwordx4 v[80:83], v3, s[20:23], s64 offen offset:2048 sc0 sc1
Mp 104,28,248
Rb 12,10,30720
s_waitcnt lgkmcnt(8)
Mp 96,28,240
v_mfma_f32_16x16x32_bf16 v[100:103], a[32:35], v[128:131], 0
Rb 16,8,32768
v_mfma_f32_16x16x32_bf16 v[108:111], a[32:35], v[136:139], 0
Mp 108,36,152
Rb 20,8,33792
Mp 100,36,144
Cv 88,88,89
Cv 89,90,91
Mp 100,40,160
Rb 24,8,34816
Cv 90,92,93
Mp 108,40,168
Cv 91,94,95
Mp 108,44,184
Rb 28,8,35840
s_waitcnt vmcnt(23) lgkmcnt(4)
s_barrier
Mp 100,44,176
s_add_u32 s40, s67, 0x70000
Mp 100,48,192
Rb 32,8,36864
s_add_u32 m0, s36, 131072
Mp 108,48,200
buffer_load_dwordx4 v2, s[20:23], s40 offen offset:0 sc1 lds
Mp 108,52,216
Rb 36,8,37888
buffer_store_dwordx4 v[88:91], v3, s[20:23], s64 offen offset:3072 sc0 sc1
Mp 100,52,208
s_mov_b32 s8, s74
s_mov_b32 s9, s75
Mp 100,56,224
Rb 40,8,38912
s_mov_b32 s10, 0x1c0000
Mp 108,56,232
)" R"(
s_mov_b32 s11, 0x20000
v_and_b32 v174, 1, v0
Mp 108,60,248
Rb 44,8,40960
Mp 100,60,240
buffer_load_dwordx4 v2, s[20:23], s40 offen offset:1024 sc1 lds
v_mfma_f32_16x16x32_bf16 v[112:115], a[0:3], v[128:131], 0
Rb 48,8,41984
v_mul_u32_u24 v174, 0xffff, v174
v_mfma_f32_16x16x32_bf16 v[120:123], a[0:3], v[136:139], 0
v_add_u32 v174, 1, v174
v_mul_u32_u24 v174, 0x3f80, v174
Mp 120,4,152
Rb 52,8,43008
v_bfe_u32 v173, v0, 1, 3
Mp 112,4,144
Cv 96,96,97
Cv 97,98,99
Mp 112,8,160
Rb 56,8,44032
Cv 98,100,101
Mp 120,8,168
buffer_load_dwordx4 v2, s[20:23], s40 offen offset:2048 sc1 lds
Mp 120,12,184
Rb 60,8,45056
s_waitcnt lgkmcnt(8)
Mp 112,12,176
Cv 99,102,103
v_cmp_eq_u32_e32 vcc, 0, v173
v_and_b32 v167, 0xf0, v1
Mp 112,16,192
Rb 0,8,46080
v_cndmask_b32_e32 v132, 0, v174, vcc
Mp 120,16,200
s_add_u32 s64, s64, 0x10000
buffer_store_dwordx4 v[96:99], v2, s[20:23], s64 offen offset:0 sc0 sc1
Mp 120,20,216
Rb 4,8,47104
v_cmp_eq_u32_e32 vcc, 1, v173
Mp 112,20,208
buffer_load_dwordx4 v2, s[20:23], s40 offen offset:3072 sc1 lds
Mp 112,24,224
Rb 8,8,49152
v_cndmask_b32_e32 v133, 0, v174, vcc
Mp 120,24,232
v_cmp_eq_u32_e32 vcc, 2, v173
v_mul_u32_u24 v166, 56, v167
Mp 120,28,248
Rb 12,8,50176
s_waitcnt lgkmcnt(8)
Mp 112,28,240
Cv 104,104,105
Cv 105,106,107
v_mfma_f32_16x16x32_bf16 v[116:119], a[32:35], v[128:131], 0
Rb 16,8,51200
s_add_u32 m0, s36, 135168
v_mfma_f32_16x16x32_bf16 v[124:127], a[32:35], v[136:139], 0
buffer_load_dwordx4 v3, s[20:23], s40 offen offset:0 sc1 lds
Mp 124,36,152
Rb 20,8,52224
Cv 106,108,109
Mp 116,36,144
Cv 107,110,111
v_cndmask_b32_e32 v134, 0, v174, vcc
)" R"(
Mp 116,40,160
Rb 24,8,53248
v_cmp_eq_u32_e32 vcc, 3, v173
Mp 124,40,168
buffer_store_dwordx4 v[104:107], v2, s[20:23], s64 offen offset:1024 sc0 sc1
Mp 124,44,184
Rb 28,8,54272
s_waitcnt lgkmcnt(8)
Mp 116,44,176
buffer_load_dwordx4 v3, s[20:23], s40 offen offset:1024 sc1 lds
v_cndmask_b32_e32 v135, 0, v174, vcc
Mp 116,48,192
Rb 32,8,55296
v_cmp_eq_u32_e32 vcc, 4, v173
Mp 124,48,200
s_nop 0
v_cndmask_b32_e32 v140, 0, v174, vcc
v_cmp_eq_u32_e32 vcc, 5, v173
Mp 124,52,216
Rb 36,8,57344
v_cndmask_b32_e32 v141, 0, v174, vcc
Mp 116,52,208
v_cmp_eq_u32_e32 vcc, 6, v173
v_and_b32 v165, 0x80, v1
Mp 116,56,224
Rb 40,8,58368
v_cndmask_b32_e32 v142, 0, v174, vcc
Mp 124,56,232
buffer_load_dwordx4 v3, s[20:23], s40 offen offset:2048 sc1 lds
Mp 124,60,248
Rb 44,8,59392
s_waitcnt vmcnt(8) lgkmcnt(8)
buffer_store_dword v6, v7, s[20:23], s65 offen offset:0 sc0 sc1
Mp 116,60,240
v_cmp_eq_u32_e32 vcc, 7, v173
v_lshrrev_b32 v173, 4, v0
v_mfma_f32_16x16x32_bf16 v[32:35], a[0:3], v[128:131], 0
Rb 48,8,60416
v_cndmask_b32_e32 v143, 0, v174, vcc
v_mfma_f32_16x16x32_bf16 v[40:43], a[0:3], v[136:139], 0
v_lshl_or_b32 v164, v173, 16, v167
v_lshl_add_u32 v166, v173, 3, v166
Mp 40,4,152
Rb 52,8,61440
v_lshrrev_b32_e64 v173, v173, s85
Mp 32,4,144
Cv 112,112,113
Cv 113,114,115
Mp 32,8,160
Rb 56,8,62464
Cv 114,116,117
Mp 40,8,168
Cv 115,118,119
v_bfe_u32 v174, v173, 4, 1
Mp 40,12,184
Rb 60,8,63488
s_waitcnt lgkmcnt(8)
Mp 32,12,176
buffer_store_dwordx4 v[112:115], v2, s[20:23], s64 offen offset:2048 sc0 sc1
ds_write_b32 v9, v16 offset:39936
Mp 32,16,192
Rb 0,8,0
v_lshl_or_b32 v183, v174, 13, v164
Mp 40,16,200
)" R"(
v_and_b32 v174, 1, v173
v_lshl_or_b32 v164, v174, 13, v164
Mp 40,20,216
Rb 4,8,1024
v_and_b32 v174, 7, v0
Mp 32,20,208
v_lshl_or_b32 v165, v174, 8, v165
s_mov_b32 s93, 0x1111
Mp 32,24,224
Rb 8,8,2048
v_lshrrev_b32 v173, 2, v0
Mp 40,24,232
Cv 120,120,121
Cv 121,122,123
Mp 40,28,248
Rb 12,8,3072
s_waitcnt lgkmcnt(3)
s_barrier
Mp 32,28,240
Cv 122,124,125
Cv 123,126,127
ds_read_b32 v16, v8 offset:39936
v_mfma_f32_16x16x32_bf16 v[36:39], a[32:35], v[128:131], 0
Rb 16,8,4096
v_mul_u32_u24 v173, 832, v173
v_mfma_f32_16x16x32_bf16 v[44:47], a[32:35], v[136:139], 0
buffer_store_dwordx4 v[120:123], v2, s[20:23], s64 offen offset:3072 sc0 sc1
Mp 44,36,152
Rb 20,8,5120
v_add_u32 v173, v173, v1
Mp 36,36,144
v_lshrrev_b32 v174, 1, v0
v_and_b32 v174, 24, v174
Mp 36,40,160
Rb 24,8,6144
v_lshl_add_u32 v174, v167, 2, v174
Mp 44,40,168
s_add_u32 s59, s36, 98304
v_add_u32 v174, s59, v174
Mp 44,44,184
Rb 28,8,8192
s_waitcnt lgkmcnt(4)
Mp 36,44,176
v_readfirstlane_b32 s82, v16
Mp 36,48,192
Rb 32,8,9216
s_lshl2_add_u32 s56, s82, s33
Mp 44,48,200
s_mul_i32 s88, s56, 4682
s_lshr_b32 s58, s88, 16
Mp 44,52,216
Rb 36,8,10240
s_mul_i32 s57, s58, 14
Mp 36,52,208
s_sub_u32 s57, s56, s57
s_lshl_b32 s40, s58, 11
Mp 36,56,224
Rb 40,8,11264
Mp 44,56,232
s_cmp_gt_u32 s57, 6
s_cselect_b32 s88, 4, 0
Mp 44,60,248
Rb 44,8,12288
Mp 36,60,240
buffer_load_dword v4, v7, s[20:23], s52 offen offset:0 sc0 sc1
)" R"(
s_add_u32 s40, s40, s88
v_mfma_f32_16x16x32_bf16 v[48:51], a[0:3], v[128:131], 0
Rb 48,8,13312
s_add_u32 s40, s40, 0xc130000
v_mfma_f32_16x16x32_bf16 v[56:59], a[0:3], v[136:139], 0
s_lshl_b32 s55, s58, 10
s_add_u32 s55, s55, s57
Mp 56,4,152
Rb 52,8,14336
Mp 48,4,144
Cv 32,32,33
Cv 33,34,35
Mp 48,8,160
Rb 56,8,16384
Cv 34,36,37
Mp 56,8,168
Cv 35,38,39
Mp 56,12,184
Rb 60,8,17408
s_waitcnt lgkmcnt(8)
Mp 48,12,176
buffer_store_dwordx4 v[32:35], v3, s[20:23], s64 offen offset:0 sc0 sc1
Mp 48,16,192
Rb 0,8,18432
Mp 56,16,200
s_cmp_gt_u32 s57, 6
s_cselect_b32 s88, 9, 0
Mp 56,20,216
Rb 4,8,19456
s_add_u32 s55, s55, s88
Mp 48,20,208
s_and_b32 s88, s58, 2
s_lshl_b32 s88, s88, 12
Mp 48,24,224
Rb 8,8,20480
v_xor_b32 v164, s88, v164
Mp 56,24,232
Cv 40,40,41
Cv 41,42,43
Mp 56,28,248
Rb 12,8,21504
s_waitcnt lgkmcnt(8)
Mp 48,28,240
Cv 42,44,45
Cv 43,46,47
v_xor_b32 v183, s88, v183
v_mfma_f32_16x16x32_bf16 v[52:55], a[32:35], v[128:131], 0
Rb 16,8,22528
s_lshl_b32 s55, s55, 10
v_mfma_f32_16x16x32_bf16 v[60:63], a[32:35], v[136:139], 0
buffer_store_dwordx4 v[40:43], v3, s[20:23], s64 offen offset:1024 sc0 sc1
Mp 60,36,152
Rb 20,8,24576
s_add_u32 s55, s55, 0x8000000
Mp 52,36,144
s_and_b32 s88, s57, 1
s_lshl1_add_u32 s58, s58, s88
Mp 52,40,160
Rb 24,8,25600
s_lshl_b32 s88, s58, 8
Mp 60,40,168
v_add_u32 v167, s88, v167
s_mul_i32 s59, s58, 14336
Mp 60,44,184
)" R"(
Rb 28,8,26624
s_waitcnt lgkmcnt(8)
Mp 52,44,176
s_lshr_b32 s88, s57, 1
s_lshl_b32 s88, s88, 6
s_add_u32 s88, s88, s59
Mp 52,48,192
Rb 32,8,27648
v_add_u32 v166, s88, v166
Mp 60,48,200
s_mov_b32 s57, 0
s_add_u32 m0, s36, 98304
Mp 60,52,216
Rb 36,8,28672
Mp 52,52,208
buffer_load_dwordx4 v173, s[12:15], s88 offen offset:0 lds
Mp 52,56,224
Rb 40,8,29696
s_add_u32 m0, s36, 98880
Mp 60,56,232
buffer_load_dwordx4 v173, s[12:15], s88 offen offset:448 lds
Mp 60,60,248
Rb 44,8,30720
s_waitcnt lgkmcnt(8)
Mp 52,60,240
s_mov_b32 s88, 0xc100008
buffer_load_dword v167, v167, s[20:23], s88 offen sc0 sc1
v_mfma_f32_16x16x32_bf16 v[64:67], a[0:3], v[128:131], 0
Rb 48,1,0
v_mfma_f32_16x16x32_bf16 v[72:75], a[0:3], v[136:139], 0
Mp 72,4,152
Rb 52,1,1024
Mp 64,4,144
Mp 64,8,160
Rb 56,1,2048
Mp 72,8,168
Mp 72,12,184
Rb 60,1,3072
s_waitcnt vmcnt(5) lgkmcnt(8)
s_barrier
v_cmp_eq_u32_e32 vcc, s3, v4
s_andn2_b64 s[28:29], 1, vcc
s_cbranch_scc1 .Lzsp11
.Lzok11:
Mp 64,12,176
buffer_load_dwordx4 a[64:67], v2, s[20:23], s66 offen offset:0 sc0 sc1
Mp 64,16,192
Rb 0,1,4096
Mp 72,16,200
Mp 72,20,216
Rb 4,1,5120
Mp 64,20,208
buffer_load_dwordx4 a[68:71], v2, s[20:23], s66 offen offset:1024 sc0 sc1
Mp 64,24,224
Rb 8,1,6144
Mp 72,24,232
Mp 72,28,248
Rb 12,1,8192
s_waitcnt lgkmcnt(8)
Mp 64,28,240
buffer_load_dwordx4 a[72:75], v2, s[20:23], s66 offen offset:2048 sc0 sc1
v_mfma_f32_16x16x32_bf16 v[68:71], a[32:35], v[128:131], 0
Rb 16,1,9216
v_mfma_f32_16x16x32_bf16 v[76:79], a[32:35], v[136:139], 0
Mp 76,36,152
)" R"(
Rb 20,1,10240
Mp 68,36,144
buffer_load_dwordx4 a[76:79], v2, s[20:23], s66 offen offset:3072 sc0 sc1
Mp 68,40,160
Rb 24,1,11264
Mp 76,40,168
Mp 76,44,184
Rb 28,1,12288
s_waitcnt vmcnt(7) lgkmcnt(8)
buffer_store_dword v6, v7, s[20:23], s65 offen offset:4 sc0 sc1
Mp 68,44,176
buffer_load_dwordx4 a[80:83], v3, s[20:23], s66 offen offset:0 sc0 sc1
Mp 68,48,192
Rb 32,1,13312
Mp 76,48,200
Mp 76,52,216
Rb 36,1,14336
Mp 68,52,208
buffer_load_dwordx4 a[84:87], v3, s[20:23], s66 offen offset:1024 sc0 sc1
Mp 68,56,224
Rb 40,1,16384
Mp 76,56,232
Mp 76,60,248
Rb 44,1,17408
s_waitcnt lgkmcnt(8)
Mp 68,60,240
buffer_load_dwordx4 a[88:91], v3, s[20:23], s66 offen offset:2048 sc0 sc1
v_mfma_f32_16x16x32_bf16 v[80:83], a[0:3], v[128:131], 0
Rb 48,1,18432
v_mfma_f32_16x16x32_bf16 v[88:91], a[0:3], v[136:139], 0
Mp 88,4,152
Rb 52,1,19456
Mp 80,4,144
buffer_load_dwordx4 a[92:95], v3, s[20:23], s66 offen offset:3072 sc0 sc1
Mp 80,8,160
Rb 56,1,20480
Mp 88,8,168
Mp 88,12,184
Rb 60,1,21504
s_waitcnt lgkmcnt(8)
Mp 80,12,176
Mp 80,16,192
Rb 0,1,22528
Mp 88,16,200
Mp 88,20,216
Rb 4,1,24576
Mp 80,20,208
Mp 80,24,224
Rb 8,1,25600
Mp 88,24,232
Mp 88,28,248
Rb 12,1,26624
s_waitcnt lgkmcnt(8)
Mp 80,28,240
v_mfma_f32_16x16x32_bf16 v[84:87], a[32:35], v[128:131], 0
Rb 16,1,27648
v_mfma_f32_16x16x32_bf16 v[92:95], a[32:35], v[136:139], 0
Mp 92,36,152
Rb 20,1,28672
Mp 84,36,144
Mp 84,40,160
Rb 24,1,29696
Mp 92,40,168
Mp 92,44,184
)" R"(
Rb 28,1,30720
s_waitcnt lgkmcnt(8)
Mp 84,44,176
buffer_load_dword v4, v7, s[20:23], s52 offen offset:4 sc0 sc1
Mp 84,48,192
Rb 32,1,32768
Mp 92,48,200
Mp 92,52,216
Rb 36,1,33792
Mp 84,52,208
Mp 84,56,224
Rb 40,1,34816
Mp 92,56,232
Mp 92,60,248
Rb 44,1,35840
s_waitcnt vmcnt(6)
s_barrier
Mp 84,60,240
Mv 48,194,64
Mv 56,194,68
Mv 52,192,64
Mv 60,192,68
Mv 64,194,72
Mv 72,194,76
Mv 68,192,72
s_waitcnt vmcnt(3) lgkmcnt(10)
Mv 76,192,76
Cv 48,48,49
Cv 49,50,51
Mv 80,194,80
Cv 50,52,53
Cv 51,54,55
Mv 88,194,84
s_add_u32 s29, s73, 0x0
buffer_store_dwordx4 v[48:51], v1, s[20:23], s29 offen sc0 sc1
Mv 84,192,80
Mv 92,192,84
v_mfma_f32_16x16x32_bf16 v[96:99], a[0:3], v[128:131], 0
Rb 48,1,36864
v_mfma_f32_16x16x32_bf16 v[104:107], a[0:3], v[136:139], 0
Mp 104,4,152
Rb 52,1,37888
s_waitcnt lgkmcnt(8)
Mp 96,4,144
Cv 56,56,57
Cv 57,58,59
ds_read_b64 v[188:189], v174 offset:0
Mp 96,8,160
Rb 56,1,38912
Cv 58,60,61
Mp 104,8,168
Cv 59,62,63
ds_read_b64 v[190:191], v174 offset:32
Mp 104,12,184
Rb 60,1,40960
Mp 96,12,176
s_add_u32 s29, s73, 0x400
buffer_store_dwordx4 v[56:59], v1, s[20:23], s29 offen sc0 sc1
ds_read_b64 v[196:197], v174 offset:1024
Mp 96,16,192
Rb 0,1,41984
Mp 104,16,200
ds_read_b64 v[198:199], v174 offset:1056
Mp 104,20,216
Rb 4,1,43008
)" R"(
s_waitcnt lgkmcnt(12)
Mp 96,20,208
Mp 96,24,224
Rb 8,1,44032
Mp 104,24,232
Cv 64,64,65
Cv 65,66,67
Mp 104,28,248
Rb 12,1,45056
Cv 66,68,69
Mp 96,28,240
Cv 67,70,71
v_mfma_f32_16x16x32_bf16 v[100:103], a[32:35], v[128:131], 0
Rb 16,1,46080
v_mfma_f32_16x16x32_bf16 v[108:111], a[32:35], v[136:139], 0
s_add_u32 s29, s73, 0x800
buffer_store_dwordx4 v[64:67], v1, s[20:23], s29 offen sc0 sc1
Mp 108,36,152
Rb 20,1,47104
s_waitcnt vmcnt(3) lgkmcnt(11)
v_cmp_eq_u32_e32 vcc, s3, v4
s_andn2_b64 s[28:29], 1, vcc
s_cbranch_scc1 .Lzsp12
.Lzok12:
Mp 100,36,144
s_add_u32 s66, s66, 0x10000
buffer_load_dwordx4 a[96:99], v2, s[20:23], s66 offen offset:0 sc0 sc1
Mp 100,40,160
Rb 24,1,49152
v_sub_f32_e32 v172, 1.0, v167
Mp 108,40,168
Cv 172,172,172
v_lshlrev_b32 v172, 16, v172
Mp 108,44,184
Rb 28,1,50176
v_lshlrev_b32 v204, 16, v188
Mp 100,44,176
buffer_load_dwordx4 a[100:103], v2, s[20:23], s66 offen offset:1024 sc0 sc1
Mp 100,48,192
Rb 32,1,51200
Cv 72,72,73
Mp 108,48,200
Cv 73,74,75
Cv 74,76,77
Mp 108,52,216
Rb 36,1,52224
s_waitcnt lgkmcnt(12)
Mp 100,52,208
buffer_load_dwordx4 a[104:107], v2, s[20:23], s66 offen offset:2048 sc0 sc1
Mp 100,56,224
Rb 40,1,53248
Cv 75,78,79
Mp 108,56,232
s_add_u32 s29, s73, 0xc00
buffer_store_dwordx4 v[72:75], v1, s[20:23], s29 offen sc0 sc1
Mp 108,60,248
Rb 44,1,54272
v_and_b32 v205, 0xffff0000, v188
Mp 100,60,240
buffer_load_dwordx4 a[108:111], v2, s[20:23], s66 offen offset:3072 sc0 sc1
Mv 96,194,88
v_mul_f32_e32 v204, v172, v204
v_mul_f32_e32 v205, v172, v205
Mv 104,194,92
)" R"(
Cv 205,204,205
v_lshlrev_b32 v204, 16, v205
Mv 100,192,88
v_and_b32 v205, 0xffff0000, v205
s_waitcnt lgkmcnt(8)
Mv 108,192,92
buffer_load_dwordx4 a[112:115], v3, s[20:23], s66 offen offset:0 sc0 sc1
v_mfma_f32_16x16x32_bf16 v[112:115], a[0:3], v[128:131], 0
Rb 48,1,55296
Cv 80,80,81
v_mfma_f32_16x16x32_bf16 v[120:123], a[0:3], v[136:139], 0
Cv 81,82,83
Cv 82,84,85
Mp 120,4,152
Rb 52,1,57344
Cv 83,86,87
Mp 112,4,144
buffer_load_dwordx4 a[116:119], v3, s[20:23], s66 offen offset:1024 sc0 sc1
Mp 112,8,160
Rb 56,1,58368
v_lshlrev_b32 v206, 16, v189
Mp 120,8,168
s_add_u32 s29, s73, 0x1000
buffer_store_dwordx4 v[80:83], v1, s[20:23], s29 offen sc0 sc1
Mp 120,12,184
Rb 60,1,59392
s_waitcnt lgkmcnt(8)
Mp 112,12,176
v_and_b32 v207, 0xffff0000, v189
v_mul_f32_e32 v206, v172, v206
Mp 112,16,192
Rb 0,1,60416
v_mul_f32_e32 v207, v172, v207
Mp 120,16,200
Cv 207,206,207
v_lshlrev_b32 v206, 16, v207
Mp 120,20,216
Rb 4,1,61440
v_and_b32 v207, 0xffff0000, v207
Mp 112,20,208
Cv 88,88,89
Cv 89,90,91
Mp 112,24,224
Rb 8,1,62464
Cv 90,92,93
Mp 120,24,232
Cv 91,94,95
v_lshlrev_b32 v212, 16, v190
Mp 120,28,248
Rb 12,1,63488
s_waitcnt lgkmcnt(8)
Mp 112,28,240
s_add_u32 s29, s73, 0x1400
buffer_store_dwordx4 v[88:91], v1, s[20:23], s29 offen sc0 sc1
v_mfma_f32_16x16x32_bf16 v[116:119], a[32:35], v[128:131], 0
Rb 16,10,0
v_and_b32 v213, 0xffff0000, v190
v_mfma_f32_16x16x32_bf16 v[124:127], a[32:35], v[136:139], 0
v_mul_f32_e32 v212, v172, v212
v_mul_f32_e32 v213, v172, v213
Mp 124,36,152
Rb 20,10,1024
Cv 213,212,213
Mp 116,36,144
)" R"(
v_lshlrev_b32 v212, 16, v213
v_and_b32 v213, 0xffff0000, v213
Mp 116,40,160
Rb 24,10,2048
v_lshlrev_b32 v214, 16, v191
Mp 124,40,168
Cv 96,96,97
Cv 97,98,99
Mp 124,44,184
Rb 28,10,3072
s_waitcnt lgkmcnt(8)
Mp 116,44,176
Cv 98,100,101
Cv 99,102,103
Mp 116,48,192
Rb 32,10,4096
v_and_b32 v215, 0xffff0000, v191
Mp 124,48,200
s_add_u32 s29, s73, 0x1800
buffer_store_dwordx4 v[96:99], v1, s[20:23], s29 offen sc0 sc1
Mp 124,52,216
Rb 36,10,5120
v_mul_f32_e32 v214, v172, v214
Mp 116,52,208
v_mul_f32_e32 v215, v172, v215
Cv 215,214,215
Mp 116,56,224
Rb 40,10,6144
v_lshlrev_b32 v214, 16, v215
Mp 124,56,232
v_and_b32 v215, 0xffff0000, v215
v_lshlrev_b32 v220, 16, v196
Mp 124,60,248
Rb 44,10,8192
s_waitcnt vmcnt(8) lgkmcnt(10)
Mp 116,60,240
Cv 104,104,105
Cv 105,106,107
Mv 112,194,96
Cv 106,108,109
Cv 107,110,111
Mv 120,194,100
s_add_u32 s29, s73, 0x4000
buffer_store_dwordx4 v[104:107], v1, s[20:23], s29 offen sc0 sc1
Mv 116,192,96
v_and_b32 v221, 0xffff0000, v196
v_mul_f32_e32 v220, v172, v220
Mv 124,192,100
v_mul_f32_e32 v221, v172, v221
Cv 221,220,221
v_mfma_f32_16x16x32_bf16 v[32:35], a[0:3], v[128:131], 0
Rb 48,10,9216
v_lshlrev_b32 v220, 16, v221
v_mfma_f32_16x16x32_bf16 v[40:43], a[0:3], v[136:139], 0
v_and_b32 v221, 0xffff0000, v221
v_lshlrev_b32 v222, 16, v197
Mp 40,4,152
Rb 52,10,10240
s_waitcnt lgkmcnt(8)
Mp 32,4,144
Cv 112,112,113
Cv 113,114,115
Mp 32,8,160
Rb 56,10,11264
)" R"(
Cv 114,116,117
Mp 40,8,168
Cv 115,118,119
v_and_b32 v223, 0xffff0000, v197
Mp 40,12,184
Rb 60,10,12288
v_mul_f32_e32 v222, v172, v222
Mp 32,12,176
s_add_u32 s29, s73, 0x4400
buffer_store_dwordx4 v[112:115], v1, s[20:23], s29 offen sc0 sc1
Mp 32,16,192
Rb 0,10,13312
v_mul_f32_e32 v223, v172, v223
Mp 40,16,200
Cv 223,222,223
v_lshlrev_b32 v222, 16, v223
Mp 40,20,216
Rb 4,10,14336
s_waitcnt lgkmcnt(8)
Mp 32,20,208
v_and_b32 v223, 0xffff0000, v223
v_lshlrev_b32 v228, 16, v198
Mp 32,24,224
Rb 8,10,16384
v_and_b32 v229, 0xffff0000, v198
Mp 40,24,232
Cv 120,120,121
Cv 121,122,123
Mp 40,28,248
Rb 12,10,17408
Cv 122,124,125
Mp 32,28,240
Cv 123,126,127
v_mul_f32_e32 v228, v172, v228
v_mfma_f32_16x16x32_bf16 v[36:39], a[32:35], v[128:131], 0
Rb 16,10,18432
v_mul_f32_e32 v229, v172, v229
v_mfma_f32_16x16x32_bf16 v[44:47], a[32:35], v[136:139], 0
s_add_u32 s29, s73, 0x4800
buffer_store_dwordx4 v[120:123], v1, s[20:23], s29 offen sc0 sc1
Mp 44,36,152
Rb 20,10,19456
s_waitcnt lgkmcnt(8)
Mp 36,36,144
Cv 229,228,229
v_lshlrev_b32 v228, 16, v229
Mp 36,40,160
Rb 24,10,20480
v_and_b32 v229, 0xffff0000, v229
Mp 44,40,168
v_lshlrev_b32 v230, 16, v199
v_and_b32 v231, 0xffff0000, v199
Mp 44,44,184
Rb 28,10,21504
v_mul_f32_e32 v230, v172, v230
Mp 36,44,176
v_mul_f32_e32 v231, v172, v231
Cv 231,230,231
Mp 36,48,192
Rb 32,10,22528
v_lshlrev_b32 v230, 16, v231
Mp 44,48,200
v_and_b32 v231, 0xffff0000, v231
Mp 44,52,216
)" R"(
Rb 36,10,24576
s_waitcnt vmcnt(3) lgkmcnt(10)
buffer_store_dword v6, v7, s[20:23], s68 offen offset:0 sc0 sc1
Mp 36,52,208
Mp 36,56,224
Rb 40,10,25600
Mp 44,56,232
Mp 44,60,248
Rb 44,10,26624
Mp 36,60,240
Mv 32,194,104
Mv 40,194,108
Mv 36,192,104
s_waitcnt lgkmcnt(8)
Mv 44,192,108
v_mfma_f32_16x16x32_bf16 v[48:51], a[0:3], v[128:131], 0
Rb 48,10,27648
v_mfma_f32_16x16x32_bf16 v[56:59], a[0:3], v[136:139], 0
Mp 56,4,152
Rb 52,10,28672
Mp 48,4,144
Cv 32,32,33
Cv 33,34,35
Mp 48,8,160
Rb 56,10,29696
Cv 34,36,37
Mp 56,8,168
Cv 35,38,39
Mp 56,12,184
Rb 60,10,30720
s_waitcnt lgkmcnt(8)
Mp 48,12,176
s_add_u32 s29, s73, 0x4c00
buffer_store_dwordx4 v[32:35], v1, s[20:23], s29 offen sc0 sc1
Mp 48,16,192
Mp 56,16,200
Mp 56,20,216
Mp 48,20,208
Mp 48,24,224
Mp 56,24,232
Cv 40,40,41
Cv 41,42,43
buffer_store_dword v6, v7, s[20:23], s68 offen offset:4 sc0 sc1
Mp 56,28,248
Cv 42,44,45
s_waitcnt lgkmcnt(4)
Mp 48,28,240
Cv 43,46,47
v_mfma_f32_16x16x32_bf16 v[52:55], a[32:35], v[128:131], 0
s_add_u32 s29, s73, 0x5000
buffer_store_dwordx4 v[40:43], v1, s[20:23], s29 offen sc0 sc1
v_mfma_f32_16x16x32_bf16 v[60:63], a[32:35], v[136:139], 0
buffer_load_dword v4, v165, s[20:23], s40 offen sc0 sc1
Mp 60,36,152
Mp 52,36,144
Mp 52,40,160
Mp 60,40,168
Mp 60,44,184
s_waitcnt lgkmcnt(0)
Mp 52,44,176
Mp 52,48,192
Mp 60,48,200
Mp 60,52,216
Mp 52,52,208
)" R"(
Mp 52,56,224
Mp 60,56,232
Mp 60,60,248
Mp 52,60,240
Mv 48,194,112
Mv 56,194,116
Mv 52,192,112
Mv 60,192,116
s_nop 7
Cv 48,48,49
Cv 49,50,51
Cv 50,52,53
Cv 51,54,55
s_add_u32 s29, s73, 0x5400
buffer_store_dwordx4 v[48:51], v1, s[20:23], s29 offen sc0 sc1
Cv 56,56,57
Cv 57,58,59
Cv 58,60,61
Cv 59,62,63
s_add_u32 s29, s73, 0x5800
buffer_store_dwordx4 v[56:59], v1, s[20:23], s29 offen sc0 sc1
s_branch .LzEND
.LzMASK0:
s_lshl1_add_u32 s28, s57, s33
s_mov_b32 s29, 0xff800000
s_cmp_gt_u32 s28, 5
s_cbranch_scc1 .LzMASKE0
s_cmp_eq_u32 s28, 5
s_cbranch_scc1 .LzMASKB0
v_mov_b32 v72, s29
v_mov_b32 v73, s29
v_mov_b32 v74, s29
v_mov_b32 v75, s29
v_mov_b32 v80, s29
v_mov_b32 v81, s29
v_mov_b32 v82, s29
v_mov_b32 v83, s29
v_mov_b32 v84, s29
v_mov_b32 v85, s29
v_mov_b32 v86, s29
v_mov_b32 v87, s29
v_mov_b32 v88, s29
v_mov_b32 v89, s29
v_mov_b32 v90, s29
v_mov_b32 v91, s29
v_mov_b32 v92, s29
v_mov_b32 v93, s29
v_mov_b32 v94, s29
v_mov_b32 v95, s29
s_cmp_eq_u32 s28, 4
s_cbranch_scc1 .LzMASKA0
v_mov_b32 v64, s29
v_mov_b32 v65, s29
v_mov_b32 v66, s29
v_mov_b32 v67, s29
v_mov_b32 v68, s29
v_mov_b32 v69, s29
v_mov_b32 v70, s29
v_mov_b32 v71, s29
v_mov_b32 v76, s29
v_mov_b32 v77, s29
v_mov_b32 v78, s29
v_mov_b32 v79, s29
.LzMASKE0:
)" R"(
s_setpc_b64 s[30:31]
.LzMASKA0:
s_mov_b32 exec_lo, 0xf0000
s_mov_b32 exec_hi, 0xfff00ff
v_mov_b32 v64, s29
v_mov_b32 v76, s29
s_mov_b32 exec_lo, 0x1f0001
s_mov_b32 exec_hi, 0x1fff01ff
v_mov_b32 v65, s29
v_mov_b32 v77, s29
s_mov_b32 exec_lo, 0x3f0003
s_mov_b32 exec_hi, 0x3fff03ff
v_mov_b32 v66, s29
v_mov_b32 v78, s29
s_mov_b32 exec_lo, 0x7f0007
s_mov_b32 exec_hi, 0x7fff07ff
v_mov_b32 v67, s29
v_mov_b32 v79, s29
s_mov_b64 exec, -1
s_setpc_b64 s[30:31]
.LzMASKB0:
s_nop 1
v_mov_b32 v88, s29
v_mov_b32 v89, s29
v_mov_b32 v90, s29
v_mov_b32 v91, s29
s_mov_b32 exec_lo, 0xf0000
s_mov_b32 exec_hi, 0xfff00ff
v_mov_b32 v80, s29
v_mov_b32 v92, s29
s_mov_b32 exec_lo, 0x1f0001
s_mov_b32 exec_hi, 0x1fff01ff
v_mov_b32 v81, s29
v_mov_b32 v93, s29
s_mov_b32 exec_lo, 0x3f0003
s_mov_b32 exec_hi, 0x3fff03ff
v_mov_b32 v82, s29
v_mov_b32 v94, s29
s_mov_b32 exec_lo, 0x7f0007
s_mov_b32 exec_hi, 0x7fff07ff
v_mov_b32 v83, s29
v_mov_b32 v95, s29
s_mov_b64 exec, -1
s_setpc_b64 s[30:31]
.LzFIX0:
s_nop 7
v_max_f32_e32 v28, v64, v65
v_max3_f32 v28, v28, v66, v67
v_max3_f32 v28, v28, v72, v73
v_max3_f32 v28, v28, v74, v75
v_max3_f32 v28, v28, v80, v81
v_max3_f32 v28, v28, v82, v83
v_max3_f32 v28, v28, v88, v89
v_max3_f32 v28, v28, v90, v91
v_mov_b32 v24, v28
s_nop 1
v_permlane32_swap_b32_e32 v28, v24
v_max_f32_e32 v28, v28, v24
v_mov_b32 v24, v28
s_nop 1
v_permlane16_swap_b32_e32 v28, v24
v_max_f32_e32 v28, v28, v24
v_max_f32_e32 v29, v68, v69
v_max3_f32 v29, v29, v70, v71
)" R"(
v_max3_f32 v29, v29, v76, v77
v_max3_f32 v29, v29, v78, v79
v_max3_f32 v29, v29, v84, v85
v_max3_f32 v29, v29, v86, v87
v_max3_f32 v29, v29, v92, v93
v_max3_f32 v29, v29, v94, v95
v_mov_b32 v24, v29
s_nop 1
v_permlane32_swap_b32_e32 v29, v24
v_max_f32_e32 v29, v29, v24
v_mov_b32 v24, v29
s_nop 1
v_permlane16_swap_b32_e32 v29, v24
v_max_f32_e32 v29, v29, v24
s_bitcmp1_b32 s88, 0
s_cbranch_scc1 .LzFIXF0
v_max_f32_e32 v28, 0, v28
v_sub_f32_e32 v24, 0, v28
v_exp_f32_e32 v24, v24
s_nop 0
v_mul_f32_e32 v12, v24, v12
v_mul_f32_e32 v128, v24, v128
v_mul_f32_e32 v129, v24, v129
v_mul_f32_e32 v130, v24, v130
v_mul_f32_e32 v131, v24, v131
v_mul_f32_e32 v132, v24, v132
v_mul_f32_e32 v133, v24, v133
v_mul_f32_e32 v134, v24, v134
v_mul_f32_e32 v135, v24, v135
v_mul_f32_e32 v144, v24, v144
v_mul_f32_e32 v145, v24, v145
v_mul_f32_e32 v146, v24, v146
v_mul_f32_e32 v147, v24, v147
v_mul_f32_e32 v148, v24, v148
v_mul_f32_e32 v149, v24, v149
v_mul_f32_e32 v150, v24, v150
v_mul_f32_e32 v151, v24, v151
v_mul_f32_e32 v160, v24, v160
v_mul_f32_e32 v161, v24, v161
v_mul_f32_e32 v162, v24, v162
v_mul_f32_e32 v163, v24, v163
v_mul_f32_e32 v164, v24, v164
v_mul_f32_e32 v165, v24, v165
v_mul_f32_e32 v166, v24, v166
v_mul_f32_e32 v167, v24, v167
v_mul_f32_e32 v176, v24, v176
v_mul_f32_e32 v177, v24, v177
v_mul_f32_e32 v178, v24, v178
v_mul_f32_e32 v179, v24, v179
v_mul_f32_e32 v180, v24, v180
v_mul_f32_e32 v181, v24, v181
v_mul_f32_e32 v182, v24, v182
v_mul_f32_e32 v183, v24, v183
v_mul_f32_e32 v192, v24, v192
v_mul_f32_e32 v193, v24, v193
v_mul_f32_e32 v194, v24, v194
v_mul_f32_e32 v195, v24, v195
v_mul_f32_e32 v196, v24, v196
v_mul_f32_e32 v197, v24, v197
v_mul_f32_e32 v198, v24, v198
v_mul_f32_e32 v199, v24, v199
v_mul_f32_e32 v208, v24, v208
v_mul_f32_e32 v209, v24, v209
v_mul_f32_e32 v210, v24, v210
)" R"(
v_mul_f32_e32 v211, v24, v211
v_mul_f32_e32 v212, v24, v212
v_mul_f32_e32 v213, v24, v213
v_mul_f32_e32 v214, v24, v214
v_mul_f32_e32 v215, v24, v215
v_mul_f32_e32 v224, v24, v224
v_mul_f32_e32 v225, v24, v225
v_mul_f32_e32 v226, v24, v226
v_mul_f32_e32 v227, v24, v227
v_mul_f32_e32 v228, v24, v228
v_mul_f32_e32 v229, v24, v229
v_mul_f32_e32 v230, v24, v230
v_mul_f32_e32 v231, v24, v231
v_mul_f32_e32 v240, v24, v240
v_mul_f32_e32 v241, v24, v241
v_mul_f32_e32 v242, v24, v242
v_mul_f32_e32 v243, v24, v243
v_mul_f32_e32 v244, v24, v244
v_mul_f32_e32 v245, v24, v245
v_mul_f32_e32 v246, v24, v246
v_mul_f32_e32 v247, v24, v247
v_max_f32_e32 v29, 0, v29
v_sub_f32_e32 v24, 0, v29
v_exp_f32_e32 v24, v24
s_nop 0
v_mul_f32_e32 v13, v24, v13
v_mul_f32_e32 v136, v24, v136
v_mul_f32_e32 v137, v24, v137
v_mul_f32_e32 v138, v24, v138
v_mul_f32_e32 v139, v24, v139
v_mul_f32_e32 v140, v24, v140
v_mul_f32_e32 v141, v24, v141
v_mul_f32_e32 v142, v24, v142
v_mul_f32_e32 v143, v24, v143
v_mul_f32_e32 v152, v24, v152
v_mul_f32_e32 v153, v24, v153
v_mul_f32_e32 v154, v24, v154
v_mul_f32_e32 v155, v24, v155
v_mul_f32_e32 v156, v24, v156
v_mul_f32_e32 v157, v24, v157
v_mul_f32_e32 v158, v24, v158
v_mul_f32_e32 v159, v24, v159
v_mul_f32_e32 v168, v24, v168
v_mul_f32_e32 v169, v24, v169
v_mul_f32_e32 v170, v24, v170
v_mul_f32_e32 v171, v24, v171
v_mul_f32_e32 v172, v24, v172
v_mul_f32_e32 v173, v24, v173
v_mul_f32_e32 v174, v24, v174
v_mul_f32_e32 v175, v24, v175
v_mul_f32_e32 v184, v24, v184
v_mul_f32_e32 v185, v24, v185
v_mul_f32_e32 v186, v24, v186
v_mul_f32_e32 v187, v24, v187
v_mul_f32_e32 v188, v24, v188
v_mul_f32_e32 v189, v24, v189
v_mul_f32_e32 v190, v24, v190
v_mul_f32_e32 v191, v24, v191
v_mul_f32_e32 v200, v24, v200
v_mul_f32_e32 v201, v24, v201
v_mul_f32_e32 v202, v24, v202
v_mul_f32_e32 v203, v24, v203
v_mul_f32_e32 v204, v24, v204
v_mul_f32_e32 v205, v24, v205
)" R"(
v_mul_f32_e32 v206, v24, v206
v_mul_f32_e32 v207, v24, v207
v_mul_f32_e32 v216, v24, v216
v_mul_f32_e32 v217, v24, v217
v_mul_f32_e32 v218, v24, v218
v_mul_f32_e32 v219, v24, v219
v_mul_f32_e32 v220, v24, v220
v_mul_f32_e32 v221, v24, v221
v_mul_f32_e32 v222, v24, v222
v_mul_f32_e32 v223, v24, v223
v_mul_f32_e32 v232, v24, v232
v_mul_f32_e32 v233, v24, v233
v_mul_f32_e32 v234, v24, v234
v_mul_f32_e32 v235, v24, v235
v_mul_f32_e32 v236, v24, v236
v_mul_f32_e32 v237, v24, v237
v_mul_f32_e32 v238, v24, v238
v_mul_f32_e32 v239, v24, v239
v_mul_f32_e32 v248, v24, v248
v_mul_f32_e32 v249, v24, v249
v_mul_f32_e32 v250, v24, v250
v_mul_f32_e32 v251, v24, v251
v_mul_f32_e32 v252, v24, v252
v_mul_f32_e32 v253, v24, v253
v_mul_f32_e32 v254, v24, v254
v_mul_f32_e32 v255, v24, v255
s_branch .LzFIXG0
.LzFIXF0:
v_cmp_lt_f32_e32 vcc, 0xff7fffff, v28
s_nop 1
v_cndmask_b32_e32 v28, 0, v28, vcc
v_cmp_lt_f32_e32 vcc, 0xff7fffff, v29
s_nop 1
v_cndmask_b32_e32 v29, 0, v29, vcc
.LzFIXG0:
v_sub_f32_e32 v16, v16, v28
v_sub_f32_e32 v17, v17, v28
v_sub_f32_e32 v18, v18, v28
v_sub_f32_e32 v19, v19, v28
v_sub_f32_e32 v64, v64, v28
v_sub_f32_e32 v65, v65, v28
v_sub_f32_e32 v66, v66, v28
v_sub_f32_e32 v67, v67, v28
v_sub_f32_e32 v72, v72, v28
v_sub_f32_e32 v73, v73, v28
v_sub_f32_e32 v74, v74, v28
v_sub_f32_e32 v75, v75, v28
v_sub_f32_e32 v80, v80, v28
v_sub_f32_e32 v81, v81, v28
v_sub_f32_e32 v82, v82, v28
v_sub_f32_e32 v83, v83, v28
v_sub_f32_e32 v88, v88, v28
v_sub_f32_e32 v89, v89, v28
v_sub_f32_e32 v90, v90, v28
v_sub_f32_e32 v91, v91, v28
v_sub_f32_e32 v20, v20, v29
v_sub_f32_e32 v21, v21, v29
v_sub_f32_e32 v22, v22, v29
v_sub_f32_e32 v23, v23, v29
v_sub_f32_e32 v68, v68, v29
v_sub_f32_e32 v69, v69, v29
v_sub_f32_e32 v70, v70, v29
v_sub_f32_e32 v71, v71, v29
v_sub_f32_e32 v76, v76, v29
)" R"(
v_sub_f32_e32 v77, v77, v29
v_sub_f32_e32 v78, v78, v29
v_sub_f32_e32 v79, v79, v29
v_sub_f32_e32 v84, v84, v29
v_sub_f32_e32 v85, v85, v29
v_sub_f32_e32 v86, v86, v29
v_sub_f32_e32 v87, v87, v29
v_sub_f32_e32 v92, v92, v29
v_sub_f32_e32 v93, v93, v29
v_sub_f32_e32 v94, v94, v29
v_sub_f32_e32 v95, v95, v29
s_bitcmp0_b32 s88, 1
s_cbranch_scc1 .LzFIXH0
v_sub_f32_e32 v96, v96, v28
v_sub_f32_e32 v97, v97, v28
v_sub_f32_e32 v98, v98, v28
v_sub_f32_e32 v99, v99, v28
v_sub_f32_e32 v104, v104, v28
v_sub_f32_e32 v105, v105, v28
v_sub_f32_e32 v106, v106, v28
v_sub_f32_e32 v107, v107, v28
v_sub_f32_e32 v112, v112, v28
v_sub_f32_e32 v113, v113, v28
v_sub_f32_e32 v114, v114, v28
v_sub_f32_e32 v115, v115, v28
v_sub_f32_e32 v120, v120, v28
v_sub_f32_e32 v121, v121, v28
v_sub_f32_e32 v122, v122, v28
v_sub_f32_e32 v123, v123, v28
v_sub_f32_e32 v100, v100, v29
v_sub_f32_e32 v101, v101, v29
v_sub_f32_e32 v102, v102, v29
v_sub_f32_e32 v103, v103, v29
v_sub_f32_e32 v108, v108, v29
v_sub_f32_e32 v109, v109, v29
v_sub_f32_e32 v110, v110, v29
v_sub_f32_e32 v111, v111, v29
v_sub_f32_e32 v116, v116, v29
v_sub_f32_e32 v117, v117, v29
v_sub_f32_e32 v118, v118, v29
v_sub_f32_e32 v119, v119, v29
v_sub_f32_e32 v124, v124, v29
v_sub_f32_e32 v125, v125, v29
v_sub_f32_e32 v126, v126, v29
v_sub_f32_e32 v127, v127, v29
.LzFIXH0:
v_exp_f32_e32 v32, v64
v_exp_f32_e32 v28, v65
s_nop 0
v_add_f32_e32 v14, v32, v28
Cv 32,32,28
v_exp_f32_e32 v33, v66
v_exp_f32_e32 v29, v67
v_add_f32_e32 v14, v14, v33
v_add_f32_e32 v14, v14, v29
Cv 33,33,29
v_exp_f32_e32 v34, v72
v_exp_f32_e32 v24, v73
v_add_f32_e32 v14, v14, v34
v_add_f32_e32 v14, v14, v24
Cv 34,34,24
v_exp_f32_e32 v35, v74
v_exp_f32_e32 v25, v75
v_add_f32_e32 v14, v14, v35
)" R"(
v_add_f32_e32 v14, v14, v25
Cv 35,35,25
v_exp_f32_e32 v36, v68
v_exp_f32_e32 v26, v69
s_nop 0
v_add_f32_e32 v15, v36, v26
Cv 36,36,26
v_exp_f32_e32 v37, v70
v_exp_f32_e32 v27, v71
v_add_f32_e32 v15, v15, v37
v_add_f32_e32 v15, v15, v27
Cv 37,37,27
v_exp_f32_e32 v38, v76
v_exp_f32_e32 v28, v77
v_add_f32_e32 v15, v15, v38
v_add_f32_e32 v15, v15, v28
Cv 38,38,28
v_exp_f32_e32 v39, v78
v_exp_f32_e32 v29, v79
v_add_f32_e32 v15, v15, v39
v_add_f32_e32 v15, v15, v29
Cv 39,39,29
v_exp_f32_e32 v40, v80
v_exp_f32_e32 v24, v81
v_add_f32_e32 v14, v14, v40
v_add_f32_e32 v14, v14, v24
Cv 40,40,24
v_exp_f32_e32 v41, v82
v_exp_f32_e32 v25, v83
v_add_f32_e32 v14, v14, v41
v_add_f32_e32 v14, v14, v25
Cv 41,41,25
v_exp_f32_e32 v42, v88
v_exp_f32_e32 v26, v89
v_add_f32_e32 v14, v14, v42
v_add_f32_e32 v14, v14, v26
Cv 42,42,26
v_exp_f32_e32 v43, v90
v_exp_f32_e32 v27, v91
v_add_f32_e32 v14, v14, v43
v_add_f32_e32 v14, v14, v27
Cv 43,43,27
v_exp_f32_e32 v44, v84
v_exp_f32_e32 v28, v85
v_add_f32_e32 v15, v15, v44
v_add_f32_e32 v15, v15, v28
Cv 44,44,28
v_exp_f32_e32 v45, v86
v_exp_f32_e32 v29, v87
v_add_f32_e32 v15, v15, v45
v_add_f32_e32 v15, v15, v29
Cv 45,45,29
v_exp_f32_e32 v46, v92
v_exp_f32_e32 v24, v93
v_add_f32_e32 v15, v15, v46
v_add_f32_e32 v15, v15, v24
Cv 46,46,24
v_exp_f32_e32 v47, v94
v_exp_f32_e32 v25, v95
v_add_f32_e32 v15, v15, v47
v_add_f32_e32 v15, v15, v25
Cv 47,47,25
s_nop 1
s_setpc_b64 s[30:31]
)" R"(
.LzMASK1:
s_lshl1_add_u32 s28, s57, s33
s_mov_b32 s29, 0xff800000
s_cmp_gt_u32 s28, 5
s_cbranch_scc1 .LzMASKE1
s_cmp_eq_u32 s28, 5
s_cbranch_scc1 .LzMASKB1
v_mov_b32 v104, s29
v_mov_b32 v105, s29
v_mov_b32 v106, s29
v_mov_b32 v107, s29
v_mov_b32 v112, s29
v_mov_b32 v113, s29
v_mov_b32 v114, s29
v_mov_b32 v115, s29
v_mov_b32 v116, s29
v_mov_b32 v117, s29
v_mov_b32 v118, s29
v_mov_b32 v119, s29
v_mov_b32 v120, s29
v_mov_b32 v121, s29
v_mov_b32 v122, s29
v_mov_b32 v123, s29
v_mov_b32 v124, s29
v_mov_b32 v125, s29
v_mov_b32 v126, s29
v_mov_b32 v127, s29
s_cmp_eq_u32 s28, 4
s_cbranch_scc1 .LzMASKA1
v_mov_b32 v96, s29
v_mov_b32 v97, s29
v_mov_b32 v98, s29
v_mov_b32 v99, s29
v_mov_b32 v100, s29
v_mov_b32 v101, s29
v_mov_b32 v102, s29
v_mov_b32 v103, s29
v_mov_b32 v108, s29
v_mov_b32 v109, s29
v_mov_b32 v110, s29
v_mov_b32 v111, s29
.LzMASKE1:
s_setpc_b64 s[30:31]
.LzMASKA1:
s_mov_b32 exec_lo, 0xf0000
s_mov_b32 exec_hi, 0xfff00ff
v_mov_b32 v96, s29
v_mov_b32 v108, s29
s_mov_b32 exec_lo, 0x1f0001
s_mov_b32 exec_hi, 0x1fff01ff
v_mov_b32 v97, s29
v_mov_b32 v109, s29
s_mov_b32 exec_lo, 0x3f0003
s_mov_b32 exec_hi, 0x3fff03ff
v_mov_b32 v98, s29
v_mov_b32 v110, s29
s_mov_b32 exec_lo, 0x7f0007
s_mov_b32 exec_hi, 0x7fff07ff
v_mov_b32 v99, s29
v_mov_b32 v111, s29
s_mov_b64 exec, -1
s_setpc_b64 s[30:31]
.LzMASKB1:
s_nop 1
)" R"(
v_mov_b32 v120, s29
v_mov_b32 v121, s29
v_mov_b32 v122, s29
v_mov_b32 v123, s29
s_mov_b32 exec_lo, 0xf0000
s_mov_b32 exec_hi, 0xfff00ff
v_mov_b32 v112, s29
v_mov_b32 v124, s29
s_mov_b32 exec_lo, 0x1f0001
s_mov_b32 exec_hi, 0x1fff01ff
v_mov_b32 v113, s29
v_mov_b32 v125, s29
s_mov_b32 exec_lo, 0x3f0003
s_mov_b32 exec_hi, 0x3fff03ff
v_mov_b32 v114, s29
v_mov_b32 v126, s29
s_mov_b32 exec_lo, 0x7f0007
s_mov_b32 exec_hi, 0x7fff07ff
v_mov_b32 v115, s29
v_mov_b32 v127, s29
s_mov_b64 exec, -1
s_setpc_b64 s[30:31]
.LzFIX1:
s_nop 7
v_max_f32_e32 v28, v96, v97
v_max3_f32 v28, v28, v98, v99
v_max3_f32 v28, v28, v104, v105
v_max3_f32 v28, v28, v106, v107
v_max3_f32 v28, v28, v112, v113
v_max3_f32 v28, v28, v114, v115
v_max3_f32 v28, v28, v120, v121
v_max3_f32 v28, v28, v122, v123
v_mov_b32 v24, v28
s_nop 1
v_permlane32_swap_b32_e32 v28, v24
v_max_f32_e32 v28, v28, v24
v_mov_b32 v24, v28
s_nop 1
v_permlane16_swap_b32_e32 v28, v24
v_max_f32_e32 v28, v28, v24
v_max_f32_e32 v29, v100, v101
v_max3_f32 v29, v29, v102, v103
v_max3_f32 v29, v29, v108, v109
v_max3_f32 v29, v29, v110, v111
v_max3_f32 v29, v29, v116, v117
v_max3_f32 v29, v29, v118, v119
v_max3_f32 v29, v29, v124, v125
v_max3_f32 v29, v29, v126, v127
v_mov_b32 v24, v29
s_nop 1
v_permlane32_swap_b32_e32 v29, v24
v_max_f32_e32 v29, v29, v24
v_mov_b32 v24, v29
s_nop 1
v_permlane16_swap_b32_e32 v29, v24
v_max_f32_e32 v29, v29, v24
s_bitcmp1_b32 s88, 0
s_cbranch_scc1 .LzFIXF1
v_max_f32_e32 v28, 0, v28
v_sub_f32_e32 v24, 0, v28
v_exp_f32_e32 v24, v24
s_nop 0
v_mul_f32_e32 v12, v24, v12
v_mul_f32_e32 v128, v24, v128
)" R"(
v_mul_f32_e32 v129, v24, v129
v_mul_f32_e32 v130, v24, v130
v_mul_f32_e32 v131, v24, v131
v_mul_f32_e32 v132, v24, v132
v_mul_f32_e32 v133, v24, v133
v_mul_f32_e32 v134, v24, v134
v_mul_f32_e32 v135, v24, v135
v_mul_f32_e32 v144, v24, v144
v_mul_f32_e32 v145, v24, v145
v_mul_f32_e32 v146, v24, v146
v_mul_f32_e32 v147, v24, v147
v_mul_f32_e32 v148, v24, v148
v_mul_f32_e32 v149, v24, v149
v_mul_f32_e32 v150, v24, v150
v_mul_f32_e32 v151, v24, v151
v_mul_f32_e32 v160, v24, v160
v_mul_f32_e32 v161, v24, v161
v_mul_f32_e32 v162, v24, v162
v_mul_f32_e32 v163, v24, v163
v_mul_f32_e32 v164, v24, v164
v_mul_f32_e32 v165, v24, v165
v_mul_f32_e32 v166, v24, v166
v_mul_f32_e32 v167, v24, v167
v_mul_f32_e32 v176, v24, v176
v_mul_f32_e32 v177, v24, v177
v_mul_f32_e32 v178, v24, v178
v_mul_f32_e32 v179, v24, v179
v_mul_f32_e32 v180, v24, v180
v_mul_f32_e32 v181, v24, v181
v_mul_f32_e32 v182, v24, v182
v_mul_f32_e32 v183, v24, v183
v_mul_f32_e32 v192, v24, v192
v_mul_f32_e32 v193, v24, v193
v_mul_f32_e32 v194, v24, v194
v_mul_f32_e32 v195, v24, v195
v_mul_f32_e32 v196, v24, v196
v_mul_f32_e32 v197, v24, v197
v_mul_f32_e32 v198, v24, v198
v_mul_f32_e32 v199, v24, v199
v_mul_f32_e32 v208, v24, v208
v_mul_f32_e32 v209, v24, v209
v_mul_f32_e32 v210, v24, v210
v_mul_f32_e32 v211, v24, v211
v_mul_f32_e32 v212, v24, v212
v_mul_f32_e32 v213, v24, v213
v_mul_f32_e32 v214, v24, v214
v_mul_f32_e32 v215, v24, v215
v_mul_f32_e32 v224, v24, v224
v_mul_f32_e32 v225, v24, v225
v_mul_f32_e32 v226, v24, v226
v_mul_f32_e32 v227, v24, v227
v_mul_f32_e32 v228, v24, v228
v_mul_f32_e32 v229, v24, v229
v_mul_f32_e32 v230, v24, v230
v_mul_f32_e32 v231, v24, v231
v_mul_f32_e32 v240, v24, v240
v_mul_f32_e32 v241, v24, v241
v_mul_f32_e32 v242, v24, v242
v_mul_f32_e32 v243, v24, v243
v_mul_f32_e32 v244, v24, v244
v_mul_f32_e32 v245, v24, v245
v_mul_f32_e32 v246, v24, v246
v_mul_f32_e32 v247, v24, v247
v_max_f32_e32 v29, 0, v29
)" R"(
v_sub_f32_e32 v24, 0, v29
v_exp_f32_e32 v24, v24
s_nop 0
v_mul_f32_e32 v13, v24, v13
v_mul_f32_e32 v136, v24, v136
v_mul_f32_e32 v137, v24, v137
v_mul_f32_e32 v138, v24, v138
v_mul_f32_e32 v139, v24, v139
v_mul_f32_e32 v140, v24, v140
v_mul_f32_e32 v141, v24, v141
v_mul_f32_e32 v142, v24, v142
v_mul_f32_e32 v143, v24, v143
v_mul_f32_e32 v152, v24, v152
v_mul_f32_e32 v153, v24, v153
v_mul_f32_e32 v154, v24, v154
v_mul_f32_e32 v155, v24, v155
v_mul_f32_e32 v156, v24, v156
v_mul_f32_e32 v157, v24, v157
v_mul_f32_e32 v158, v24, v158
v_mul_f32_e32 v159, v24, v159
v_mul_f32_e32 v168, v24, v168
v_mul_f32_e32 v169, v24, v169
v_mul_f32_e32 v170, v24, v170
v_mul_f32_e32 v171, v24, v171
v_mul_f32_e32 v172, v24, v172
v_mul_f32_e32 v173, v24, v173
v_mul_f32_e32 v174, v24, v174
v_mul_f32_e32 v175, v24, v175
v_mul_f32_e32 v184, v24, v184
v_mul_f32_e32 v185, v24, v185
v_mul_f32_e32 v186, v24, v186
v_mul_f32_e32 v187, v24, v187
v_mul_f32_e32 v188, v24, v188
v_mul_f32_e32 v189, v24, v189
v_mul_f32_e32 v190, v24, v190
v_mul_f32_e32 v191, v24, v191
v_mul_f32_e32 v200, v24, v200
v_mul_f32_e32 v201, v24, v201
v_mul_f32_e32 v202, v24, v202
v_mul_f32_e32 v203, v24, v203
v_mul_f32_e32 v204, v24, v204
v_mul_f32_e32 v205, v24, v205
v_mul_f32_e32 v206, v24, v206
v_mul_f32_e32 v207, v24, v207
v_mul_f32_e32 v216, v24, v216
v_mul_f32_e32 v217, v24, v217
v_mul_f32_e32 v218, v24, v218
v_mul_f32_e32 v219, v24, v219
v_mul_f32_e32 v220, v24, v220
v_mul_f32_e32 v221, v24, v221
v_mul_f32_e32 v222, v24, v222
v_mul_f32_e32 v223, v24, v223
v_mul_f32_e32 v232, v24, v232
v_mul_f32_e32 v233, v24, v233
v_mul_f32_e32 v234, v24, v234
v_mul_f32_e32 v235, v24, v235
v_mul_f32_e32 v236, v24, v236
v_mul_f32_e32 v237, v24, v237
v_mul_f32_e32 v238, v24, v238
v_mul_f32_e32 v239, v24, v239
v_mul_f32_e32 v248, v24, v248
v_mul_f32_e32 v249, v24, v249
v_mul_f32_e32 v250, v24, v250
v_mul_f32_e32 v251, v24, v251
)" R"(
v_mul_f32_e32 v252, v24, v252
v_mul_f32_e32 v253, v24, v253
v_mul_f32_e32 v254, v24, v254
v_mul_f32_e32 v255, v24, v255
s_branch .LzFIXG1
.LzFIXF1:
v_cmp_lt_f32_e32 vcc, 0xff7fffff, v28
s_nop 1
v_cndmask_b32_e32 v28, 0, v28, vcc
v_cmp_lt_f32_e32 vcc, 0xff7fffff, v29
s_nop 1
v_cndmask_b32_e32 v29, 0, v29, vcc
.LzFIXG1:
v_sub_f32_e32 v16, v16, v28
v_sub_f32_e32 v17, v17, v28
v_sub_f32_e32 v18, v18, v28
v_sub_f32_e32 v19, v19, v28
v_sub_f32_e32 v96, v96, v28
v_sub_f32_e32 v97, v97, v28
v_sub_f32_e32 v98, v98, v28
v_sub_f32_e32 v99, v99, v28
v_sub_f32_e32 v104, v104, v28
v_sub_f32_e32 v105, v105, v28
v_sub_f32_e32 v106, v106, v28
v_sub_f32_e32 v107, v107, v28
v_sub_f32_e32 v112, v112, v28
v_sub_f32_e32 v113, v113, v28
v_sub_f32_e32 v114, v114, v28
v_sub_f32_e32 v115, v115, v28
v_sub_f32_e32 v120, v120, v28
v_sub_f32_e32 v121, v121, v28
v_sub_f32_e32 v122, v122, v28
v_sub_f32_e32 v123, v123, v28
v_sub_f32_e32 v20, v20, v29
v_sub_f32_e32 v21, v21, v29
v_sub_f32_e32 v22, v22, v29
v_sub_f32_e32 v23, v23, v29
v_sub_f32_e32 v100, v100, v29
v_sub_f32_e32 v101, v101, v29
v_sub_f32_e32 v102, v102, v29
v_sub_f32_e32 v103, v103, v29
v_sub_f32_e32 v108, v108, v29
v_sub_f32_e32 v109, v109, v29
v_sub_f32_e32 v110, v110, v29
v_sub_f32_e32 v111, v111, v29
v_sub_f32_e32 v116, v116, v29
v_sub_f32_e32 v117, v117, v29
v_sub_f32_e32 v118, v118, v29
v_sub_f32_e32 v119, v119, v29
v_sub_f32_e32 v124, v124, v29
v_sub_f32_e32 v125, v125, v29
v_sub_f32_e32 v126, v126, v29
v_sub_f32_e32 v127, v127, v29
s_bitcmp0_b32 s88, 1
s_cbranch_scc1 .LzFIXH1
v_sub_f32_e32 v64, v64, v28
v_sub_f32_e32 v65, v65, v28
v_sub_f32_e32 v66, v66, v28
v_sub_f32_e32 v67, v67, v28
v_sub_f32_e32 v72, v72, v28
v_sub_f32_e32 v73, v73, v28
v_sub_f32_e32 v74, v74, v28
v_sub_f32_e32 v75, v75, v28
v_sub_f32_e32 v80, v80, v28
)" R"(
v_sub_f32_e32 v81, v81, v28
v_sub_f32_e32 v82, v82, v28
v_sub_f32_e32 v83, v83, v28
v_sub_f32_e32 v88, v88, v28
v_sub_f32_e32 v89, v89, v28
v_sub_f32_e32 v90, v90, v28
v_sub_f32_e32 v91, v91, v28
v_sub_f32_e32 v68, v68, v29
v_sub_f32_e32 v69, v69, v29
v_sub_f32_e32 v70, v70, v29
v_sub_f32_e32 v71, v71, v29
v_sub_f32_e32 v76, v76, v29
v_sub_f32_e32 v77, v77, v29
v_sub_f32_e32 v78, v78, v29
v_sub_f32_e32 v79, v79, v29
v_sub_f32_e32 v84, v84, v29
v_sub_f32_e32 v85, v85, v29
v_sub_f32_e32 v86, v86, v29
v_sub_f32_e32 v87, v87, v29
v_sub_f32_e32 v92, v92, v29
v_sub_f32_e32 v93, v93, v29
v_sub_f32_e32 v94, v94, v29
v_sub_f32_e32 v95, v95, v29
.LzFIXH1:
v_exp_f32_e32 v48, v96
v_exp_f32_e32 v26, v97
s_nop 0
v_add_f32_e32 v14, v48, v26
Cv 48,48,26
v_exp_f32_e32 v49, v98
v_exp_f32_e32 v27, v99
v_add_f32_e32 v14, v14, v49
v_add_f32_e32 v14, v14, v27
Cv 49,49,27
v_exp_f32_e32 v50, v104
v_exp_f32_e32 v28, v105
v_add_f32_e32 v14, v14, v50
v_add_f32_e32 v14, v14, v28
Cv 50,50,28
v_exp_f32_e32 v51, v106
v_exp_f32_e32 v29, v107
v_add_f32_e32 v14, v14, v51
v_add_f32_e32 v14, v14, v29
Cv 51,51,29
v_exp_f32_e32 v52, v100
v_exp_f32_e32 v24, v101
s_nop 0
v_add_f32_e32 v15, v52, v24
Cv 52,52,24
v_exp_f32_e32 v53, v102
v_exp_f32_e32 v25, v103
v_add_f32_e32 v15, v15, v53
v_add_f32_e32 v15, v15, v25
Cv 53,53,25
v_exp_f32_e32 v54, v108
v_exp_f32_e32 v26, v109
v_add_f32_e32 v15, v15, v54
v_add_f32_e32 v15, v15, v26
Cv 54,54,26
v_exp_f32_e32 v55, v110
v_exp_f32_e32 v27, v111
v_add_f32_e32 v15, v15, v55
v_add_f32_e32 v15, v15, v27
Cv 55,55,27
)" R"(
v_exp_f32_e32 v56, v112
v_exp_f32_e32 v28, v113
v_add_f32_e32 v14, v14, v56
v_add_f32_e32 v14, v14, v28
Cv 56,56,28
v_exp_f32_e32 v57, v114
v_exp_f32_e32 v29, v115
v_add_f32_e32 v14, v14, v57
v_add_f32_e32 v14, v14, v29
Cv 57,57,29
v_exp_f32_e32 v58, v120
v_exp_f32_e32 v24, v121
v_add_f32_e32 v14, v14, v58
v_add_f32_e32 v14, v14, v24
Cv 58,58,24
v_exp_f32_e32 v59, v122
v_exp_f32_e32 v25, v123
v_add_f32_e32 v14, v14, v59
v_add_f32_e32 v14, v14, v25
Cv 59,59,25
v_exp_f32_e32 v60, v116
v_exp_f32_e32 v26, v117
v_add_f32_e32 v15, v15, v60
v_add_f32_e32 v15, v15, v26
Cv 60,60,26
v_exp_f32_e32 v61, v118
v_exp_f32_e32 v27, v119
v_add_f32_e32 v15, v15, v61
v_add_f32_e32 v15, v15, v27
Cv 61,61,27
v_exp_f32_e32 v62, v124
v_exp_f32_e32 v28, v125
v_add_f32_e32 v15, v15, v62
v_add_f32_e32 v15, v15, v28
Cv 62,62,28
v_exp_f32_e32 v63, v126
v_exp_f32_e32 v29, v127
v_add_f32_e32 v15, v15, v63
v_add_f32_e32 v15, v15, v29
Cv 63,63,29
s_nop 1
s_setpc_b64 s[30:31]
.Lzsp1:
s_sub_u32 s53, s53, 1
s_cbranch_scc1 .Lzsq1
s_sleep 1
buffer_load_dword v4, v2, s[20:23], s85 offen sc0 sc1
s_waitcnt vmcnt(0)
v_xor_b32 v4, s3, v4
v_cmp_gt_u32_e32 vcc, 2, v4
s_andn2_b32 s28, 0xff, vcc_lo
s_cbranch_scc1 .Lzsp1
s_branch .Lzok1
.Lzsq1:
s_mov_b32 s53, 0
s_branch .Lzok1
.Lzsp2:
s_sub_u32 s53, s53, 1
s_cbranch_scc1 .Lzsq2
s_sleep 1
buffer_load_dword v4, v7, s[20:23], s52 offen offset:0 sc0 sc1
s_waitcnt vmcnt(0)
v_cmp_eq_u32_e32 vcc, s3, v4
s_andn2_b64 s[28:29], 1, vcc
)" R"(
s_cbranch_scc1 .Lzsp2
s_branch .Lzok2
.Lzsq2:
s_mov_b32 s53, 0
s_branch .Lzok2
.Lzsp3:
s_sub_u32 s53, s53, 1
s_cbranch_scc1 .Lzsq3
s_sleep 1
buffer_load_dword v4, v2, s[20:23], s85 offen sc0 sc1
s_waitcnt vmcnt(0)
v_xor_b32 v4, s3, v4
v_cmp_gt_u32_e32 vcc, 2, v4
s_andn2_b32 s28, 0xff, vcc_lo
s_cbranch_scc1 .Lzsp3
s_branch .Lzok3
.Lzsq3:
s_mov_b32 s53, 0
s_branch .Lzok3
.Lzsp4:
s_sub_u32 s53, s53, 1
s_cbranch_scc1 .Lzsq4
s_sleep 1
buffer_load_dword v4, v7, s[20:23], s52 offen offset:0 sc0 sc1
s_waitcnt vmcnt(0)
v_cmp_eq_u32_e32 vcc, s3, v4
s_andn2_b64 s[28:29], 1, vcc
s_cbranch_scc1 .Lzsp4
s_branch .Lzok4
.Lzsq4:
s_mov_b32 s53, 0
s_branch .Lzok4
.Lzsp5:
s_sub_u32 s53, s53, 1
s_cbranch_scc1 .Lzsq5
s_sleep 1
buffer_load_dword v4, v5, s[20:23], s50 offen offset:0 sc0 sc1
s_waitcnt vmcnt(0)
v_cmp_eq_u32_e32 vcc, s3, v4
s_andn2_b64 s[28:29], s[60:61], vcc
s_cbranch_scc1 .Lzsp5
s_branch .Lzok5
.Lzsq5:
s_mov_b32 s53, 0
s_branch .Lzok5
.Lzsp6:
s_sub_u32 s53, s53, 1
s_cbranch_scc1 .Lzsq6
s_sleep 1
buffer_load_dword v4, v5, s[20:23], s50 offen offset:0 sc0 sc1
s_waitcnt vmcnt(0)
v_cmp_eq_u32_e32 vcc, s3, v4
s_andn2_b64 s[28:29], s[60:61], vcc
s_cbranch_scc1 .Lzsp6
s_branch .Lzok6
.Lzsq6:
s_mov_b32 s53, 0
s_branch .Lzok6
.Lzsp7:
s_sub_u32 s53, s53, 1
s_cbranch_scc1 .Lzsq7
s_sleep 1
buffer_load_dwordx2 v[24:25], v7, s[20:23], s83 offen sc0 sc1
s_waitcnt vmcnt(0)
)" R"(
v_cmp_eq_u32_e32 vcc, s3, v25
s_andn2_b64 s[28:29], 1, vcc
s_cbranch_scc1 .Lzsp7
s_branch .Lzok7
.Lzsq7:
s_mov_b32 s53, 0
s_branch .Lzok7
.LzT2S:
s_cmp_lg_u32 s33, 0
s_cbranch_scc1 .LzT2E
.LzT2:
s_mov_b32 s28, 0xc100000
buffer_load_dwordx2 v[32:33], v1, s[20:23], s28 offen offset:0 sc0 sc1
buffer_load_dwordx2 v[34:35], v1, s[20:23], s28 offen offset:1024 sc0 sc1
buffer_load_dwordx2 v[36:37], v1, s[20:23], s28 offen offset:2048 sc0 sc1
buffer_load_dwordx2 v[38:39], v1, s[20:23], s28 offen offset:3072 sc0 sc1
s_mov_b32 s28, 0xc101000
buffer_load_dwordx2 v[40:41], v1, s[20:23], s28 offen offset:0 sc0 sc1
buffer_load_dwordx2 v[42:43], v1, s[20:23], s28 offen offset:1024 sc0 sc1
buffer_load_dwordx2 v[44:45], v1, s[20:23], s28 offen offset:2048 sc0 sc1
buffer_load_dwordx2 v[46:47], v1, s[20:23], s28 offen offset:3072 sc0 sc1
s_mov_b32 s28, 0xc102000
buffer_load_dwordx2 v[48:49], v1, s[20:23], s28 offen offset:0 sc0 sc1
buffer_load_dwordx2 v[50:51], v1, s[20:23], s28 offen offset:1024 sc0 sc1
buffer_load_dwordx2 v[52:53], v1, s[20:23], s28 offen offset:2048 sc0 sc1
buffer_load_dwordx2 v[54:55], v1, s[20:23], s28 offen offset:3072 sc0 sc1
s_mov_b32 s28, 0xc103000
buffer_load_dwordx2 v[56:57], v1, s[20:23], s28 offen offset:0 sc0 sc1
buffer_load_dwordx2 v[58:59], v1, s[20:23], s28 offen offset:1024 sc0 sc1
buffer_load_dwordx2 v[60:61], v1, s[20:23], s28 offen offset:2048 sc0 sc1
buffer_load_dwordx2 v[62:63], v1, s[20:23], s28 offen offset:3072 sc0 sc1
s_mov_b32 s28, 0xc104000
buffer_load_dwordx2 v[64:65], v1, s[20:23], s28 offen offset:0 sc0 sc1
buffer_load_dwordx2 v[66:67], v1, s[20:23], s28 offen offset:1024 sc0 sc1
buffer_load_dwordx2 v[68:69], v1, s[20:23], s28 offen offset:2048 sc0 sc1
buffer_load_dwordx2 v[70:71], v1, s[20:23], s28 offen offset:3072 sc0 sc1
s_mov_b32 s28, 0xc105000
buffer_load_dwordx2 v[72:73], v1, s[20:23], s28 offen offset:0 sc0 sc1
buffer_load_dwordx2 v[74:75], v1, s[20:23], s28 offen offset:1024 sc0 sc1
buffer_load_dwordx2 v[76:77], v1, s[20:23], s28 offen offset:2048 sc0 sc1
buffer_load_dwordx2 v[78:79], v1, s[20:23], s28 offen offset:3072 sc0 sc1
s_mov_b32 s28, 0xc106000
buffer_load_dwordx2 v[80:81], v1, s[20:23], s28 offen offset:0 sc0 sc1
buffer_load_dwordx2 v[82:83], v1, s[20:23], s28 offen offset:1024 sc0 sc1
buffer_load_dwordx2 v[84:85], v1, s[20:23], s28 offen offset:2048 sc0 sc1
buffer_load_dwordx2 v[86:87], v1, s[20:23], s28 offen offset:3072 sc0 sc1
s_mov_b32 s28, 0xc107000
buffer_load_dwordx2 v[88:89], v1, s[20:23], s28 offen offset:0 sc0 sc1
buffer_load_dwordx2 v[90:91], v1, s[20:23], s28 offen offset:1024 sc0 sc1
buffer_load_dwordx2 v[92:93], v1, s[20:23], s28 offen offset:2048 sc0 sc1
buffer_load_dwordx2 v[94:95], v1, s[20:23], s28 offen offset:3072 sc0 sc1
v_mov_b32 v26, 0
v_mov_b32 v27, -1
s_mov_b32 s28, 0xc108c00
buffer_store_dword v26, v7, s[20:23], s28 offen sc0 sc1
buffer_store_dword v26, v7, s[20:23], s28 offen offset:128 sc0 sc1
s_waitcnt vmcnt(0)
v_max3_u32 v26, v26, v33, v35
v_min3_u32 v27, v27, v33, v35
v_max3_u32 v26, v26, v37, v39
v_min3_u32 v27, v27, v37, v39
v_max3_u32 v26, v26, v41, v43
v_min3_u32 v27, v27, v41, v43
v_max3_u32 v26, v26, v45, v47
)" R"(
v_min3_u32 v27, v27, v45, v47
v_max3_u32 v26, v26, v49, v51
v_min3_u32 v27, v27, v49, v51
v_max3_u32 v26, v26, v53, v55
v_min3_u32 v27, v27, v53, v55
v_max3_u32 v26, v26, v57, v59
v_min3_u32 v27, v27, v57, v59
v_max3_u32 v26, v26, v61, v63
v_min3_u32 v27, v27, v61, v63
v_max3_u32 v26, v26, v65, v67
v_min3_u32 v27, v27, v65, v67
v_max3_u32 v26, v26, v69, v71
v_min3_u32 v27, v27, v69, v71
v_max3_u32 v26, v26, v73, v75
v_min3_u32 v27, v27, v73, v75
v_max3_u32 v26, v26, v77, v79
v_min3_u32 v27, v27, v77, v79
v_max3_u32 v26, v26, v81, v83
v_min3_u32 v27, v27, v81, v83
v_max3_u32 v26, v26, v85, v87
v_min3_u32 v27, v27, v85, v87
v_max3_u32 v26, v26, v89, v91
v_min3_u32 v27, v27, v89, v91
v_max3_u32 v26, v26, v93, v95
v_min3_u32 v27, v27, v93, v95
v_cmp_ne_u32_e32 vcc, s3, v26
v_cmp_ne_u32_e64 s[28:29], s3, v27
s_or_b64 vcc, vcc, s[28:29]
s_cbranch_scc0 .LzT2G
s_sub_u32 s53, s53, 1
s_cbranch_scc0 .LzT2
s_mov_b32 s53, 0
.LzT2G:
v_max_u32 v26, v32, v34
v_min_u32 v27, v32, v34
v_med3_u32 v27, v26, v27, v36
v_max_u32 v26, v26, v36
v_med3_u32 v27, v26, v27, v38
v_max_u32 v26, v26, v38
v_med3_u32 v27, v26, v27, v40
v_max_u32 v26, v26, v40
v_med3_u32 v27, v26, v27, v42
v_max_u32 v26, v26, v42
v_med3_u32 v27, v26, v27, v44
v_max_u32 v26, v26, v44
v_med3_u32 v27, v26, v27, v46
v_max_u32 v26, v26, v46
v_med3_u32 v27, v26, v27, v48
v_max_u32 v26, v26, v48
v_med3_u32 v27, v26, v27, v50
v_max_u32 v26, v26, v50
v_med3_u32 v27, v26, v27, v52
v_max_u32 v26, v26, v52
v_med3_u32 v27, v26, v27, v54
v_max_u32 v26, v26, v54
v_med3_u32 v27, v26, v27, v56
v_max_u32 v26, v26, v56
v_med3_u32 v27, v26, v27, v58
v_max_u32 v26, v26, v58
v_med3_u32 v27, v26, v27, v60
v_max_u32 v26, v26, v60
v_med3_u32 v27, v26, v27, v62
v_max_u32 v26, v26, v62
v_med3_u32 v27, v26, v27, v64
)" R"(
v_max_u32 v26, v26, v64
v_med3_u32 v27, v26, v27, v66
v_max_u32 v26, v26, v66
v_med3_u32 v27, v26, v27, v68
v_max_u32 v26, v26, v68
v_med3_u32 v27, v26, v27, v70
v_max_u32 v26, v26, v70
v_med3_u32 v27, v26, v27, v72
v_max_u32 v26, v26, v72
v_med3_u32 v27, v26, v27, v74
v_max_u32 v26, v26, v74
v_med3_u32 v27, v26, v27, v76
v_max_u32 v26, v26, v76
v_med3_u32 v27, v26, v27, v78
v_max_u32 v26, v26, v78
v_med3_u32 v27, v26, v27, v80
v_max_u32 v26, v26, v80
v_med3_u32 v27, v26, v27, v82
v_max_u32 v26, v26, v82
v_med3_u32 v27, v26, v27, v84
v_max_u32 v26, v26, v84
v_med3_u32 v27, v26, v27, v86
v_max_u32 v26, v26, v86
v_med3_u32 v27, v26, v27, v88
v_max_u32 v26, v26, v88
v_med3_u32 v27, v26, v27, v90
v_max_u32 v26, v26, v90
v_med3_u32 v27, v26, v27, v92
v_max_u32 v26, v26, v92
v_med3_u32 v27, v26, v27, v94
v_max_u32 v26, v26, v94
v_mov_b32 v24, v26
v_mov_b32 v25, v27
s_nop 1
v_permlane32_swap_b32_e32 v26, v24
v_permlane32_swap_b32_e32 v27, v25
v_max_u32 v27, v27, v25
v_med3_u32 v27, v26, v27, v24
v_max_u32 v26, v26, v24
v_mov_b32 v24, v26
v_mov_b32 v25, v27
s_nop 1
v_permlane16_swap_b32_e32 v26, v24
v_permlane16_swap_b32_e32 v27, v25
v_max_u32 v27, v27, v25
v_med3_u32 v27, v26, v27, v24
v_max_u32 v26, v26, v24
s_nop 1
v_mov_b32_dpp v24, v26 row_ror:8 row_mask:0xf bank_mask:0xf
v_mov_b32_dpp v25, v27 row_ror:8 row_mask:0xf bank_mask:0xf
v_max_u32 v27, v27, v25
v_med3_u32 v27, v26, v27, v24
v_max_u32 v26, v26, v24
s_nop 1
v_mov_b32_dpp v24, v26 row_ror:4 row_mask:0xf bank_mask:0xf
v_mov_b32_dpp v25, v27 row_ror:4 row_mask:0xf bank_mask:0xf
v_max_u32 v27, v27, v25
v_med3_u32 v27, v26, v27, v24
v_max_u32 v26, v26, v24
s_nop 1
v_mov_b32_dpp v24, v26 row_ror:2 row_mask:0xf bank_mask:0xf
v_mov_b32_dpp v25, v27 row_ror:2 row_mask:0xf bank_mask:0xf
v_max_u32 v27, v27, v25
v_med3_u32 v27, v26, v27, v24
)" R"(
v_max_u32 v26, v26, v24
s_nop 1
v_mov_b32_dpp v24, v26 row_ror:1 row_mask:0xf bank_mask:0xf
v_mov_b32_dpp v25, v27 row_ror:1 row_mask:0xf bank_mask:0xf
v_max_u32 v27, v27, v25
v_med3_u32 v27, v26, v27, v24
v_max_u32 v26, v26, v24
v_and_b32 v26, 0xffff, v26
v_lshl_or_b32 v26, v27, 16, v26
v_not_b32 v24, v26
v_mov_b32 v25, s3
s_mov_b32 s28, 0xc108000
buffer_store_dwordx2 v[24:25], v7, s[20:23], s28 offen sc0 sc1
s_branch .LzT2E
.LzA7:
s_waitcnt vmcnt(0)
s_nop 7
v_mul_f32_e32 v96, s54, v96
v_mul_f32_e32 v97, s54, v97
Cv 96,96,97
v_accvgpr_write_b32 a112, v96
v_mul_f32_e32 v98, s54, v98
v_mul_f32_e32 v99, s54, v99
Cv 98,98,99
v_accvgpr_write_b32 a113, v98
v_mul_f32_e32 v100, s54, v100
v_mul_f32_e32 v101, s54, v101
Cv 100,100,101
v_accvgpr_write_b32 a114, v100
v_mul_f32_e32 v102, s54, v102
v_mul_f32_e32 v103, s54, v103
Cv 102,102,103
v_accvgpr_write_b32 a115, v102
v_mul_f32_e32 v104, s54, v104
v_mul_f32_e32 v105, s54, v105
Cv 104,104,105
v_accvgpr_write_b32 a116, v104
v_mul_f32_e32 v106, s54, v106
v_mul_f32_e32 v107, s54, v107
Cv 106,106,107
v_accvgpr_write_b32 a117, v106
v_mul_f32_e32 v108, s54, v108
v_mul_f32_e32 v109, s54, v109
Cv 108,108,109
v_accvgpr_write_b32 a118, v108
v_mul_f32_e32 v110, s54, v110
v_mul_f32_e32 v111, s54, v111
Cv 110,110,111
v_accvgpr_write_b32 a119, v110
v_mul_f32_e32 v112, s54, v112
v_mul_f32_e32 v113, s54, v113
Cv 112,112,113
v_accvgpr_write_b32 a120, v112
v_mul_f32_e32 v114, s54, v114
v_mul_f32_e32 v115, s54, v115
Cv 114,114,115
v_accvgpr_write_b32 a121, v114
v_mul_f32_e32 v116, s54, v116
v_mul_f32_e32 v117, s54, v117
Cv 116,116,117
v_accvgpr_write_b32 a122, v116
v_mul_f32_e32 v118, s54, v118
v_mul_f32_e32 v119, s54, v119
Cv 118,118,119
)" R"(
v_accvgpr_write_b32 a123, v118
v_mul_f32_e32 v120, s54, v120
v_mul_f32_e32 v121, s54, v121
Cv 120,120,121
v_accvgpr_write_b32 a124, v120
v_mul_f32_e32 v122, s54, v122
v_mul_f32_e32 v123, s54, v123
Cv 122,122,123
v_accvgpr_write_b32 a125, v122
v_mul_f32_e32 v124, s54, v124
v_mul_f32_e32 v125, s54, v125
Cv 124,124,125
v_accvgpr_write_b32 a126, v124
v_mul_f32_e32 v126, s54, v126
v_mul_f32_e32 v127, s54, v127
Cv 126,126,127
v_accvgpr_write_b32 a127, v126
v_accvgpr_mov_b32 a64, a224
v_accvgpr_mov_b32 a65, a225
v_accvgpr_mov_b32 a66, a226
v_accvgpr_mov_b32 a67, a227
v_accvgpr_mov_b32 a68, a228
v_accvgpr_mov_b32 a69, a229
v_accvgpr_mov_b32 a70, a230
v_accvgpr_mov_b32 a71, a231
v_accvgpr_mov_b32 a72, a232
v_accvgpr_mov_b32 a73, a233
v_accvgpr_mov_b32 a74, a234
v_accvgpr_mov_b32 a75, a235
v_accvgpr_mov_b32 a76, a236
v_accvgpr_mov_b32 a77, a237
v_accvgpr_mov_b32 a78, a238
v_accvgpr_mov_b32 a79, a239
v_accvgpr_mov_b32 a80, a240
v_accvgpr_mov_b32 a81, a241
v_accvgpr_mov_b32 a82, a242
v_accvgpr_mov_b32 a83, a243
v_accvgpr_mov_b32 a84, a244
v_accvgpr_mov_b32 a85, a245
v_accvgpr_mov_b32 a86, a246
v_accvgpr_mov_b32 a87, a247
v_accvgpr_mov_b32 a88, a248
v_accvgpr_mov_b32 a89, a249
v_accvgpr_mov_b32 a90, a250
v_accvgpr_mov_b32 a91, a251
v_accvgpr_mov_b32 a92, a252
v_accvgpr_mov_b32 a93, a253
v_accvgpr_mov_b32 a94, a254
v_accvgpr_mov_b32 a95, a255
v_accvgpr_mov_b32 a96, a192
v_accvgpr_mov_b32 a97, a193
v_accvgpr_mov_b32 a98, a194
v_accvgpr_mov_b32 a99, a195
v_accvgpr_mov_b32 a100, a196
v_accvgpr_mov_b32 a101, a197
v_accvgpr_mov_b32 a102, a198
v_accvgpr_mov_b32 a103, a199
v_accvgpr_mov_b32 a104, a200
v_accvgpr_mov_b32 a105, a201
v_accvgpr_mov_b32 a106, a202
v_accvgpr_mov_b32 a107, a203
v_accvgpr_mov_b32 a108, a204
v_accvgpr_mov_b32 a109, a205
v_accvgpr_mov_b32 a110, a206
)" R"(
v_accvgpr_mov_b32 a111, a207
s_mov_b32 s94, 2
s_nop 1
s_branch .LzA7R
.Lzsp8:
s_sub_u32 s53, s53, 1
s_cbranch_scc1 .Lzsq8
s_sleep 1
buffer_load_dword v4, v5, s[20:23], s50 offen offset:0 sc0 sc1
s_waitcnt vmcnt(0)
v_cmp_eq_u32_e32 vcc, s3, v4
s_andn2_b64 s[28:29], s[60:61], vcc
s_cbranch_scc1 .Lzsp8
s_branch .Lzok8
.Lzsq8:
s_mov_b32 s53, 0
s_branch .Lzok8
.Lzmk0:
s_call_b64 s[30:31], .LzMASK0
s_branch .Lzmr0
.Lzfx0:
s_mov_b32 s88, 3
s_call_b64 s[30:31], .LzFIX0
s_branch .Lzfr0
.Lzmk1:
s_call_b64 s[30:31], .LzMASK1
s_branch .Lzmr1
.Lzsp9:
s_sub_u32 s53, s53, 1
s_cbranch_scc1 .Lzsq9
s_sleep 1
buffer_load_dword v4, v5, s[20:23], s50 offen offset:0 sc0 sc1
s_waitcnt vmcnt(0)
v_cmp_eq_u32_e32 vcc, s3, v4
s_andn2_b64 s[28:29], s[60:61], vcc
s_cbranch_scc1 .Lzsp9
s_branch .Lzok9
.Lzsq9:
s_mov_b32 s53, 0
s_branch .Lzok9
.Lzfx1:
s_mov_b32 s88, 2
s_call_b64 s[30:31], .LzFIX1
s_branch .Lzfr1
.Lzmk2:
s_call_b64 s[30:31], .LzMASK0
s_branch .Lzmr2
.Lzfx2:
s_mov_b32 s88, 2
s_call_b64 s[30:31], .LzFIX0
s_branch .Lzfr2
.Lzmk3:
s_call_b64 s[30:31], .LzMASK1
s_branch .Lzmr3
.Lzsp10:
s_sub_u32 s53, s53, 1
s_cbranch_scc1 .Lzsq10
s_sleep 1
buffer_load_dword v4, v5, s[20:23], s50 offen offset:0 sc0 sc1
s_waitcnt vmcnt(0)
v_cmp_eq_u32_e32 vcc, s3, v4
s_andn2_b64 s[28:29], s[60:61], vcc
s_cbranch_scc1 .Lzsp10
s_branch .Lzok10
)" R"(
.Lzsq10:
s_mov_b32 s53, 0
s_branch .Lzok10
.Lzfx3:
s_mov_b32 s88, 0
s_call_b64 s[30:31], .LzFIX1
s_branch .Lzfr3
.Lzmk4:
s_call_b64 s[30:31], .LzMASK0
s_branch .Lzmr4
.Lzfx4:
s_mov_b32 s88, 3
s_call_b64 s[30:31], .LzFIX0
s_branch .Lzfr4
.Lzmk5:
s_call_b64 s[30:31], .LzMASK1
s_branch .Lzmr5
.Lzfx5:
s_mov_b32 s88, 0
s_call_b64 s[30:31], .LzFIX1
s_branch .Lzfr5
.Lzsp11:
s_sub_u32 s53, s53, 1
s_cbranch_scc1 .Lzsq11
s_sleep 1
buffer_load_dword v4, v7, s[20:23], s52 offen offset:0 sc0 sc1
s_waitcnt vmcnt(0)
v_cmp_eq_u32_e32 vcc, s3, v4
s_andn2_b64 s[28:29], 1, vcc
s_cbranch_scc1 .Lzsp11
s_branch .Lzok11
.Lzsq11:
s_mov_b32 s53, 0
s_branch .Lzok11
.Lzsp12:
s_sub_u32 s53, s53, 1
s_cbranch_scc1 .Lzsq12
s_sleep 1
buffer_load_dword v4, v7, s[20:23], s52 offen offset:4 sc0 sc1
s_waitcnt vmcnt(0)
v_cmp_eq_u32_e32 vcc, s3, v4
s_andn2_b64 s[28:29], 1, vcc
s_cbranch_scc1 .Lzsp12
s_branch .Lzok12
.Lzsq12:
s_mov_b32 s53, 0
s_branch .Lzok12
.Lzsp13:
s_sub_u32 s53, s53, 1
s_cbranch_scc1 .Lzsq13
s_sleep 1
buffer_load_dword v4, v2, s[20:23], s85 offen sc0 sc1
s_waitcnt vmcnt(0)
v_xor_b32 v4, s3, v4
v_cmp_gt_u32_e32 vcc, 2, v4
s_andn2_b32 s28, 0xff, vcc_lo
s_cbranch_scc1 .Lzsp13
s_branch .Lzok13
.Lzsq13:
s_mov_b32 s53, 0
s_branch .Lzok13
.Lzsp14:
s_sub_u32 s53, s53, 1
s_cbranch_scc1 .Lzsq14
)" R"(
s_sleep 1
buffer_load_dword v4, v5, s[20:23], s50 offen offset:0 sc0 sc1
s_waitcnt vmcnt(0)
v_cmp_eq_u32_e32 vcc, s3, v4
s_andn2_b64 s[28:29], s[60:61], vcc
s_cbranch_scc1 .Lzsp14
s_branch .Lzok14
.Lzsq14:
s_mov_b32 s53, 0
s_branch .Lzok14
.Lzsp15:
s_sub_u32 s53, s53, 1
s_cbranch_scc1 .Lzsq15
s_sleep 1
buffer_load_dword v4, v5, s[20:23], s50 offen offset:0 sc0 sc1
s_waitcnt vmcnt(0)
v_cmp_eq_u32_e32 vcc, s3, v4
s_andn2_b64 s[28:29], s[60:61], vcc
s_cbranch_scc1 .Lzsp15
s_branch .Lzok15
.Lzsq15:
s_mov_b32 s53, 0
s_branch .Lzok15
.Lzsp16:
s_sub_u32 s53, s53, 1
s_cbranch_scc1 .Lzsq16
s_sleep 1
buffer_load_dword v4, v5, s[20:23], s50 offen offset:0 sc0 sc1
s_waitcnt vmcnt(0)
v_cmp_eq_u32_e32 vcc, s3, v4
s_andn2_b64 s[28:29], s[60:61], vcc
s_cbranch_scc1 .Lzsp16
s_branch .Lzok16
.Lzsq16:
s_mov_b32 s53, 0
s_branch .Lzok16
.Lzsp17:
s_sub_u32 s53, s53, 1
s_cbranch_scc1 .Lzsq17
s_sleep 1
buffer_load_dword v4, v5, s[20:23], s50 offen offset:0 sc0 sc1
s_waitcnt vmcnt(0)
v_cmp_eq_u32_e32 vcc, s3, v4
s_andn2_b64 s[28:29], s[60:61], vcc
s_cbranch_scc1 .Lzsp17
s_branch .Lzok17
.Lzsq17:
s_mov_b32 s53, 0
s_branch .Lzok17
.Lzsp18:
s_sub_u32 s53, s53, 1
s_cbranch_scc1 .Lzsq18
s_sleep 1
buffer_load_dword v4, v5, s[20:23], s50 offen offset:0 sc0 sc1
s_waitcnt vmcnt(0)
v_cmp_eq_u32_e32 vcc, s3, v4
s_andn2_b64 s[28:29], s[60:61], vcc
s_cbranch_scc1 .Lzsp18
s_branch .Lzok18
.Lzsq18:
s_mov_b32 s53, 0
s_branch .Lzok18
.Lzfx6:
s_mov_b32 s88, 3
)" R"(
s_call_b64 s[30:31], .LzFIX0
s_branch .Lzfr6
.Lzsp19:
s_sub_u32 s53, s53, 1
s_cbranch_scc1 .Lzsq19
s_sleep 1
buffer_load_dword v4, v5, s[20:23], s50 offen offset:0 sc0 sc1
s_waitcnt vmcnt(0)
v_cmp_eq_u32_e32 vcc, s3, v4
s_andn2_b64 s[28:29], s[60:61], vcc
s_cbranch_scc1 .Lzsp19
s_branch .Lzok19
.Lzsq19:
s_mov_b32 s53, 0
s_branch .Lzok19
.Lzfx7:
s_mov_b32 s88, 2
s_call_b64 s[30:31], .LzFIX1
s_branch .Lzfr7
.Lzmk6:
s_call_b64 s[30:31], .LzMASK0
s_branch .Lzmr6
.Lzfx8:
s_mov_b32 s88, 2
s_call_b64 s[30:31], .LzFIX0
s_branch .Lzfr8
.Lzmk7:
s_call_b64 s[30:31], .LzMASK1
s_branch .Lzmr7
.Lzsp20:
s_sub_u32 s53, s53, 1
s_cbranch_scc1 .Lzsq20
s_sleep 1
buffer_load_dword v4, v5, s[20:23], s50 offen offset:0 sc0 sc1
s_waitcnt vmcnt(0)
v_cmp_eq_u32_e32 vcc, s3, v4
s_andn2_b64 s[28:29], s[60:61], vcc
s_cbranch_scc1 .Lzsp20
s_branch .Lzok20
.Lzsq20:
s_mov_b32 s53, 0
s_branch .Lzok20
.Lzfx9:
s_mov_b32 s88, 2
s_call_b64 s[30:31], .LzFIX1
s_branch .Lzfr9
.Lzmk8:
s_call_b64 s[30:31], .LzMASK0
s_branch .Lzmr8
.Lzfx10:
s_mov_b32 s88, 2
s_call_b64 s[30:31], .LzFIX0
s_branch .Lzfr10
.Lzmk9:
s_call_b64 s[30:31], .LzMASK1
s_branch .Lzmr9
.Lzsp21:
s_sub_u32 s53, s53, 1
s_cbranch_scc1 .Lzsq21
s_sleep 1
buffer_load_dword v4, v7, s[20:23], s83 offen offset:4 sc0 sc1
s_waitcnt vmcnt(0)
v_cmp_eq_u32_e32 vcc, s3, v4
s_andn2_b64 s[28:29], 1, vcc
)" R"(
s_cbranch_scc1 .Lzsp21
s_branch .Lzok21
.Lzsq21:
s_mov_b32 s53, 0
s_branch .Lzok21
.Lzsp22:
s_sub_u32 s53, s53, 1
s_cbranch_scc1 .Lzsq22
s_sleep 1
buffer_load_dword v4, v7, s[20:23], s65 offen offset:0 sc0 sc1
s_waitcnt vmcnt(0)
v_cmp_eq_u32_e32 vcc, s3, v4
s_andn2_b64 s[28:29], s[60:61], vcc
s_cbranch_scc1 .Lzsp22
s_branch .Lzok22
.Lzsq22:
s_mov_b32 s53, 0
s_branch .Lzok22
.Lzfx11:
s_mov_b32 s88, 2
s_call_b64 s[30:31], .LzFIX1
s_branch .Lzfr11
.Lzmk10:
s_call_b64 s[30:31], .LzMASK0
s_branch .Lzmr10
.Lzfx12:
s_mov_b32 s88, 2
s_call_b64 s[30:31], .LzFIX0
s_branch .Lzfr12
.Lzmk11:
s_call_b64 s[30:31], .LzMASK1
s_branch .Lzmr11
.Lzfx13:
s_mov_b32 s88, 0
s_call_b64 s[30:31], .LzFIX1
s_branch .Lzfr13
.LzMRG:
s_waitcnt vmcnt(0)
s_nop 7
v_mov_b32 v25, v64
v_mov_b32 v24, v25
s_nop 1
v_permlane32_swap_b32_e32 v25, v24
v_add_f32_e32 v25, v25, v24
v_mov_b32 v24, v25
s_nop 1
v_permlane16_swap_b32_e32 v25, v24
v_add_f32_e32 v25, v25, v24
v_cmp_lt_f32_e32 vcc, 0, v25
s_nop 1
v_cndmask_b32_e32 v66, v16, v66, vcc
v_sub_f32_e32 v28, v16, v66
v_min_f32_e32 v26, 0, v28
v_exp_f32_e32 v26, v26
v_max_f32_e32 v28, 0, v28
v_sub_f32_e32 v24, 0, v28
v_exp_f32_e32 v24, v24
v_mul_f32_e32 v64, v26, v64
v_mul_f32_e32 v12, v24, v12
v_mul_f32_e32 v128, v24, v128
v_mul_f32_e32 v129, v24, v129
v_mul_f32_e32 v130, v24, v130
v_mul_f32_e32 v131, v24, v131
v_mul_f32_e32 v132, v24, v132
)" R"(
v_mul_f32_e32 v133, v24, v133
v_mul_f32_e32 v134, v24, v134
v_mul_f32_e32 v135, v24, v135
v_mul_f32_e32 v144, v24, v144
v_mul_f32_e32 v145, v24, v145
v_mul_f32_e32 v146, v24, v146
v_mul_f32_e32 v147, v24, v147
v_mul_f32_e32 v148, v24, v148
v_mul_f32_e32 v149, v24, v149
v_mul_f32_e32 v150, v24, v150
v_mul_f32_e32 v151, v24, v151
v_mul_f32_e32 v160, v24, v160
v_mul_f32_e32 v161, v24, v161
v_mul_f32_e32 v162, v24, v162
v_mul_f32_e32 v163, v24, v163
v_mul_f32_e32 v164, v24, v164
v_mul_f32_e32 v165, v24, v165
v_mul_f32_e32 v166, v24, v166
v_mul_f32_e32 v167, v24, v167
v_mul_f32_e32 v176, v24, v176
v_mul_f32_e32 v177, v24, v177
v_mul_f32_e32 v178, v24, v178
v_mul_f32_e32 v179, v24, v179
v_mul_f32_e32 v180, v24, v180
v_mul_f32_e32 v181, v24, v181
v_mul_f32_e32 v182, v24, v182
v_mul_f32_e32 v183, v24, v183
v_mul_f32_e32 v192, v24, v192
v_mul_f32_e32 v193, v24, v193
v_mul_f32_e32 v194, v24, v194
v_mul_f32_e32 v195, v24, v195
v_mul_f32_e32 v196, v24, v196
v_mul_f32_e32 v197, v24, v197
v_mul_f32_e32 v198, v24, v198
v_mul_f32_e32 v199, v24, v199
v_mul_f32_e32 v208, v24, v208
v_mul_f32_e32 v209, v24, v209
v_mul_f32_e32 v210, v24, v210
v_mul_f32_e32 v211, v24, v211
v_mul_f32_e32 v212, v24, v212
v_mul_f32_e32 v213, v24, v213
v_mul_f32_e32 v214, v24, v214
v_mul_f32_e32 v215, v24, v215
v_mul_f32_e32 v224, v24, v224
v_mul_f32_e32 v225, v24, v225
v_mul_f32_e32 v226, v24, v226
v_mul_f32_e32 v227, v24, v227
v_mul_f32_e32 v228, v24, v228
v_mul_f32_e32 v229, v24, v229
v_mul_f32_e32 v230, v24, v230
v_mul_f32_e32 v231, v24, v231
v_mul_f32_e32 v240, v24, v240
v_mul_f32_e32 v241, v24, v241
v_mul_f32_e32 v242, v24, v242
v_mul_f32_e32 v243, v24, v243
v_mul_f32_e32 v244, v24, v244
v_mul_f32_e32 v245, v24, v245
v_mul_f32_e32 v246, v24, v246
v_mul_f32_e32 v247, v24, v247
v_sub_f32_e32 v16, v16, v28
v_sub_f32_e32 v17, v17, v28
v_sub_f32_e32 v18, v18, v28
v_sub_f32_e32 v19, v19, v28
v_sub_f32_e32 v96, v96, v28
)" R"(
v_sub_f32_e32 v97, v97, v28
v_sub_f32_e32 v98, v98, v28
v_sub_f32_e32 v99, v99, v28
v_sub_f32_e32 v104, v104, v28
v_sub_f32_e32 v105, v105, v28
v_sub_f32_e32 v106, v106, v28
v_sub_f32_e32 v107, v107, v28
v_sub_f32_e32 v112, v112, v28
v_sub_f32_e32 v113, v113, v28
v_sub_f32_e32 v114, v114, v28
v_sub_f32_e32 v115, v115, v28
v_sub_f32_e32 v120, v120, v28
v_sub_f32_e32 v121, v121, v28
v_sub_f32_e32 v122, v122, v28
v_sub_f32_e32 v123, v123, v28
v_accvgpr_read_b32 v25, a128
v_lshlrev_b32 v24, 16, v25
v_and_b32 v25, 0xffff0000, v25
v_mul_f32_e32 v24, v26, v24
v_mul_f32_e32 v25, v26, v25
Cv 24,24,25
v_accvgpr_write_b32 a128, v24
v_accvgpr_read_b32 v25, a129
v_lshlrev_b32 v24, 16, v25
v_and_b32 v25, 0xffff0000, v25
v_mul_f32_e32 v24, v26, v24
v_mul_f32_e32 v25, v26, v25
Cv 24,24,25
v_accvgpr_write_b32 a129, v24
v_accvgpr_read_b32 v25, a130
v_lshlrev_b32 v24, 16, v25
v_and_b32 v25, 0xffff0000, v25
v_mul_f32_e32 v24, v26, v24
v_mul_f32_e32 v25, v26, v25
Cv 24,24,25
v_accvgpr_write_b32 a130, v24
v_accvgpr_read_b32 v25, a131
v_lshlrev_b32 v24, 16, v25
v_and_b32 v25, 0xffff0000, v25
v_mul_f32_e32 v24, v26, v24
v_mul_f32_e32 v25, v26, v25
Cv 24,24,25
v_accvgpr_write_b32 a131, v24
v_accvgpr_read_b32 v25, a136
v_lshlrev_b32 v24, 16, v25
v_and_b32 v25, 0xffff0000, v25
v_mul_f32_e32 v24, v26, v24
v_mul_f32_e32 v25, v26, v25
Cv 24,24,25
v_accvgpr_write_b32 a136, v24
v_accvgpr_read_b32 v25, a137
v_lshlrev_b32 v24, 16, v25
v_and_b32 v25, 0xffff0000, v25
v_mul_f32_e32 v24, v26, v24
v_mul_f32_e32 v25, v26, v25
Cv 24,24,25
v_accvgpr_write_b32 a137, v24
v_accvgpr_read_b32 v25, a138
v_lshlrev_b32 v24, 16, v25
v_and_b32 v25, 0xffff0000, v25
v_mul_f32_e32 v24, v26, v24
v_mul_f32_e32 v25, v26, v25
Cv 24,24,25
v_accvgpr_write_b32 a138, v24
)" R"(
v_accvgpr_read_b32 v25, a139
v_lshlrev_b32 v24, 16, v25
v_and_b32 v25, 0xffff0000, v25
v_mul_f32_e32 v24, v26, v24
v_mul_f32_e32 v25, v26, v25
Cv 24,24,25
v_accvgpr_write_b32 a139, v24
v_accvgpr_read_b32 v25, a144
v_lshlrev_b32 v24, 16, v25
v_and_b32 v25, 0xffff0000, v25
v_mul_f32_e32 v24, v26, v24
v_mul_f32_e32 v25, v26, v25
Cv 24,24,25
v_accvgpr_write_b32 a144, v24
v_accvgpr_read_b32 v25, a145
v_lshlrev_b32 v24, 16, v25
v_and_b32 v25, 0xffff0000, v25
v_mul_f32_e32 v24, v26, v24
v_mul_f32_e32 v25, v26, v25
Cv 24,24,25
v_accvgpr_write_b32 a145, v24
v_accvgpr_read_b32 v25, a146
v_lshlrev_b32 v24, 16, v25
v_and_b32 v25, 0xffff0000, v25
v_mul_f32_e32 v24, v26, v24
v_mul_f32_e32 v25, v26, v25
Cv 24,24,25
v_accvgpr_write_b32 a146, v24
v_accvgpr_read_b32 v25, a147
v_lshlrev_b32 v24, 16, v25
v_and_b32 v25, 0xffff0000, v25
v_mul_f32_e32 v24, v26, v24
v_mul_f32_e32 v25, v26, v25
Cv 24,24,25
v_accvgpr_write_b32 a147, v24
v_accvgpr_read_b32 v25, a152
v_lshlrev_b32 v24, 16, v25
v_and_b32 v25, 0xffff0000, v25
v_mul_f32_e32 v24, v26, v24
v_mul_f32_e32 v25, v26, v25
Cv 24,24,25
v_accvgpr_write_b32 a152, v24
v_accvgpr_read_b32 v25, a153
v_lshlrev_b32 v24, 16, v25
v_and_b32 v25, 0xffff0000, v25
v_mul_f32_e32 v24, v26, v24
v_mul_f32_e32 v25, v26, v25
Cv 24,24,25
v_accvgpr_write_b32 a153, v24
v_accvgpr_read_b32 v25, a154
v_lshlrev_b32 v24, 16, v25
v_and_b32 v25, 0xffff0000, v25
v_mul_f32_e32 v24, v26, v24
v_mul_f32_e32 v25, v26, v25
Cv 24,24,25
v_accvgpr_write_b32 a154, v24
v_accvgpr_read_b32 v25, a155
v_lshlrev_b32 v24, 16, v25
v_and_b32 v25, 0xffff0000, v25
v_mul_f32_e32 v24, v26, v24
v_mul_f32_e32 v25, v26, v25
Cv 24,24,25
v_accvgpr_write_b32 a155, v24
v_accvgpr_read_b32 v25, a160
)" R"(
v_lshlrev_b32 v24, 16, v25
v_and_b32 v25, 0xffff0000, v25
v_mul_f32_e32 v24, v26, v24
v_mul_f32_e32 v25, v26, v25
Cv 24,24,25
v_accvgpr_write_b32 a160, v24
v_accvgpr_read_b32 v25, a161
v_lshlrev_b32 v24, 16, v25
v_and_b32 v25, 0xffff0000, v25
v_mul_f32_e32 v24, v26, v24
v_mul_f32_e32 v25, v26, v25
Cv 24,24,25
v_accvgpr_write_b32 a161, v24
v_accvgpr_read_b32 v25, a162
v_lshlrev_b32 v24, 16, v25
v_and_b32 v25, 0xffff0000, v25
v_mul_f32_e32 v24, v26, v24
v_mul_f32_e32 v25, v26, v25
Cv 24,24,25
v_accvgpr_write_b32 a162, v24
v_accvgpr_read_b32 v25, a163
v_lshlrev_b32 v24, 16, v25
v_and_b32 v25, 0xffff0000, v25
v_mul_f32_e32 v24, v26, v24
v_mul_f32_e32 v25, v26, v25
Cv 24,24,25
v_accvgpr_write_b32 a163, v24
v_accvgpr_read_b32 v25, a168
v_lshlrev_b32 v24, 16, v25
v_and_b32 v25, 0xffff0000, v25
v_mul_f32_e32 v24, v26, v24
v_mul_f32_e32 v25, v26, v25
Cv 24,24,25
v_accvgpr_write_b32 a168, v24
v_accvgpr_read_b32 v25, a169
v_lshlrev_b32 v24, 16, v25
v_and_b32 v25, 0xffff0000, v25
v_mul_f32_e32 v24, v26, v24
v_mul_f32_e32 v25, v26, v25
Cv 24,24,25
v_accvgpr_write_b32 a169, v24
v_accvgpr_read_b32 v25, a170
v_lshlrev_b32 v24, 16, v25
v_and_b32 v25, 0xffff0000, v25
v_mul_f32_e32 v24, v26, v24
v_mul_f32_e32 v25, v26, v25
Cv 24,24,25
v_accvgpr_write_b32 a170, v24
v_accvgpr_read_b32 v25, a171
v_lshlrev_b32 v24, 16, v25
v_and_b32 v25, 0xffff0000, v25
v_mul_f32_e32 v24, v26, v24
v_mul_f32_e32 v25, v26, v25
Cv 24,24,25
v_accvgpr_write_b32 a171, v24
v_accvgpr_read_b32 v25, a176
v_lshlrev_b32 v24, 16, v25
v_and_b32 v25, 0xffff0000, v25
v_mul_f32_e32 v24, v26, v24
v_mul_f32_e32 v25, v26, v25
Cv 24,24,25
v_accvgpr_write_b32 a176, v24
v_accvgpr_read_b32 v25, a177
v_lshlrev_b32 v24, 16, v25
)" R"(
v_and_b32 v25, 0xffff0000, v25
v_mul_f32_e32 v24, v26, v24
v_mul_f32_e32 v25, v26, v25
Cv 24,24,25
v_accvgpr_write_b32 a177, v24
v_accvgpr_read_b32 v25, a178
v_lshlrev_b32 v24, 16, v25
v_and_b32 v25, 0xffff0000, v25
v_mul_f32_e32 v24, v26, v24
v_mul_f32_e32 v25, v26, v25
Cv 24,24,25
v_accvgpr_write_b32 a178, v24
v_accvgpr_read_b32 v25, a179
v_lshlrev_b32 v24, 16, v25
v_and_b32 v25, 0xffff0000, v25
v_mul_f32_e32 v24, v26, v24
v_mul_f32_e32 v25, v26, v25
Cv 24,24,25
v_accvgpr_write_b32 a179, v24
v_accvgpr_read_b32 v25, a184
v_lshlrev_b32 v24, 16, v25
v_and_b32 v25, 0xffff0000, v25
v_mul_f32_e32 v24, v26, v24
v_mul_f32_e32 v25, v26, v25
Cv 24,24,25
v_accvgpr_write_b32 a184, v24
v_accvgpr_read_b32 v25, a185
v_lshlrev_b32 v24, 16, v25
v_and_b32 v25, 0xffff0000, v25
v_mul_f32_e32 v24, v26, v24
v_mul_f32_e32 v25, v26, v25
Cv 24,24,25
v_accvgpr_write_b32 a185, v24
v_accvgpr_read_b32 v25, a186
v_lshlrev_b32 v24, 16, v25
v_and_b32 v25, 0xffff0000, v25
v_mul_f32_e32 v24, v26, v24
v_mul_f32_e32 v25, v26, v25
Cv 24,24,25
v_accvgpr_write_b32 a186, v24
v_accvgpr_read_b32 v25, a187
v_lshlrev_b32 v24, 16, v25
v_and_b32 v25, 0xffff0000, v25
v_mul_f32_e32 v24, v26, v24
v_mul_f32_e32 v25, v26, v25
Cv 24,24,25
v_accvgpr_write_b32 a187, v24
v_mov_b32 v25, v65
v_mov_b32 v24, v25
s_nop 1
v_permlane32_swap_b32_e32 v25, v24
v_add_f32_e32 v25, v25, v24
v_mov_b32 v24, v25
s_nop 1
v_permlane16_swap_b32_e32 v25, v24
v_add_f32_e32 v25, v25, v24
v_cmp_lt_f32_e32 vcc, 0, v25
s_nop 1
v_cndmask_b32_e32 v67, v20, v67, vcc
v_sub_f32_e32 v29, v20, v67
v_min_f32_e32 v27, 0, v29
v_exp_f32_e32 v27, v27
v_max_f32_e32 v29, 0, v29
v_sub_f32_e32 v24, 0, v29
)" R"(
v_exp_f32_e32 v24, v24
v_mul_f32_e32 v65, v27, v65
v_mul_f32_e32 v13, v24, v13
v_mul_f32_e32 v136, v24, v136
v_mul_f32_e32 v137, v24, v137
v_mul_f32_e32 v138, v24, v138
v_mul_f32_e32 v139, v24, v139
v_mul_f32_e32 v140, v24, v140
v_mul_f32_e32 v141, v24, v141
v_mul_f32_e32 v142, v24, v142
v_mul_f32_e32 v143, v24, v143
v_mul_f32_e32 v152, v24, v152
v_mul_f32_e32 v153, v24, v153
v_mul_f32_e32 v154, v24, v154
v_mul_f32_e32 v155, v24, v155
v_mul_f32_e32 v156, v24, v156
v_mul_f32_e32 v157, v24, v157
v_mul_f32_e32 v158, v24, v158
v_mul_f32_e32 v159, v24, v159
v_mul_f32_e32 v168, v24, v168
v_mul_f32_e32 v169, v24, v169
v_mul_f32_e32 v170, v24, v170
v_mul_f32_e32 v171, v24, v171
v_mul_f32_e32 v172, v24, v172
v_mul_f32_e32 v173, v24, v173
v_mul_f32_e32 v174, v24, v174
v_mul_f32_e32 v175, v24, v175
v_mul_f32_e32 v184, v24, v184
v_mul_f32_e32 v185, v24, v185
v_mul_f32_e32 v186, v24, v186
v_mul_f32_e32 v187, v24, v187
v_mul_f32_e32 v188, v24, v188
v_mul_f32_e32 v189, v24, v189
v_mul_f32_e32 v190, v24, v190
v_mul_f32_e32 v191, v24, v191
v_mul_f32_e32 v200, v24, v200
v_mul_f32_e32 v201, v24, v201
v_mul_f32_e32 v202, v24, v202
v_mul_f32_e32 v203, v24, v203
v_mul_f32_e32 v204, v24, v204
v_mul_f32_e32 v205, v24, v205
v_mul_f32_e32 v206, v24, v206
v_mul_f32_e32 v207, v24, v207
v_mul_f32_e32 v216, v24, v216
v_mul_f32_e32 v217, v24, v217
v_mul_f32_e32 v218, v24, v218
v_mul_f32_e32 v219, v24, v219
v_mul_f32_e32 v220, v24, v220
v_mul_f32_e32 v221, v24, v221
v_mul_f32_e32 v222, v24, v222
v_mul_f32_e32 v223, v24, v223
v_mul_f32_e32 v232, v24, v232
v_mul_f32_e32 v233, v24, v233
v_mul_f32_e32 v234, v24, v234
v_mul_f32_e32 v235, v24, v235
v_mul_f32_e32 v236, v24, v236
v_mul_f32_e32 v237, v24, v237
v_mul_f32_e32 v238, v24, v238
v_mul_f32_e32 v239, v24, v239
v_mul_f32_e32 v248, v24, v248
v_mul_f32_e32 v249, v24, v249
v_mul_f32_e32 v250, v24, v250
v_mul_f32_e32 v251, v24, v251
v_mul_f32_e32 v252, v24, v252
)" R"(
v_mul_f32_e32 v253, v24, v253
v_mul_f32_e32 v254, v24, v254
v_mul_f32_e32 v255, v24, v255
v_sub_f32_e32 v20, v20, v29
v_sub_f32_e32 v21, v21, v29
v_sub_f32_e32 v22, v22, v29
v_sub_f32_e32 v23, v23, v29
v_sub_f32_e32 v100, v100, v29
v_sub_f32_e32 v101, v101, v29
v_sub_f32_e32 v102, v102, v29
v_sub_f32_e32 v103, v103, v29
v_sub_f32_e32 v108, v108, v29
v_sub_f32_e32 v109, v109, v29
v_sub_f32_e32 v110, v110, v29
v_sub_f32_e32 v111, v111, v29
v_sub_f32_e32 v116, v116, v29
v_sub_f32_e32 v117, v117, v29
v_sub_f32_e32 v118, v118, v29
v_sub_f32_e32 v119, v119, v29
v_sub_f32_e32 v124, v124, v29
v_sub_f32_e32 v125, v125, v29
v_sub_f32_e32 v126, v126, v29
v_sub_f32_e32 v127, v127, v29
v_accvgpr_read_b32 v25, a132
v_lshlrev_b32 v24, 16, v25
v_and_b32 v25, 0xffff0000, v25
v_mul_f32_e32 v24, v27, v24
v_mul_f32_e32 v25, v27, v25
Cv 24,24,25
v_accvgpr_write_b32 a132, v24
v_accvgpr_read_b32 v25, a133
v_lshlrev_b32 v24, 16, v25
v_and_b32 v25, 0xffff0000, v25
v_mul_f32_e32 v24, v27, v24
v_mul_f32_e32 v25, v27, v25
Cv 24,24,25
v_accvgpr_write_b32 a133, v24
v_accvgpr_read_b32 v25, a134
v_lshlrev_b32 v24, 16, v25
v_and_b32 v25, 0xffff0000, v25
v_mul_f32_e32 v24, v27, v24
v_mul_f32_e32 v25, v27, v25
Cv 24,24,25
v_accvgpr_write_b32 a134, v24
v_accvgpr_read_b32 v25, a135
v_lshlrev_b32 v24, 16, v25
v_and_b32 v25, 0xffff0000, v25
v_mul_f32_e32 v24, v27, v24
v_mul_f32_e32 v25, v27, v25
Cv 24,24,25
v_accvgpr_write_b32 a135, v24
v_accvgpr_read_b32 v25, a140
v_lshlrev_b32 v24, 16, v25
v_and_b32 v25, 0xffff0000, v25
v_mul_f32_e32 v24, v27, v24
v_mul_f32_e32 v25, v27, v25
Cv 24,24,25
v_accvgpr_write_b32 a140, v24
v_accvgpr_read_b32 v25, a141
v_lshlrev_b32 v24, 16, v25
v_and_b32 v25, 0xffff0000, v25
v_mul_f32_e32 v24, v27, v24
v_mul_f32_e32 v25, v27, v25
Cv 24,24,25
)" R"(
v_accvgpr_write_b32 a141, v24
v_accvgpr_read_b32 v25, a142
v_lshlrev_b32 v24, 16, v25
v_and_b32 v25, 0xffff0000, v25
v_mul_f32_e32 v24, v27, v24
v_mul_f32_e32 v25, v27, v25
Cv 24,24,25
v_accvgpr_write_b32 a142, v24
v_accvgpr_read_b32 v25, a143
v_lshlrev_b32 v24, 16, v25
v_and_b32 v25, 0xffff0000, v25
v_mul_f32_e32 v24, v27, v24
v_mul_f32_e32 v25, v27, v25
Cv 24,24,25
v_accvgpr_write_b32 a143, v24
v_accvgpr_read_b32 v25, a148
v_lshlrev_b32 v24, 16, v25
v_and_b32 v25, 0xffff0000, v25
v_mul_f32_e32 v24, v27, v24
v_mul_f32_e32 v25, v27, v25
Cv 24,24,25
v_accvgpr_write_b32 a148, v24
v_accvgpr_read_b32 v25, a149
v_lshlrev_b32 v24, 16, v25
v_and_b32 v25, 0xffff0000, v25
v_mul_f32_e32 v24, v27, v24
v_mul_f32_e32 v25, v27, v25
Cv 24,24,25
v_accvgpr_write_b32 a149, v24
v_accvgpr_read_b32 v25, a150
v_lshlrev_b32 v24, 16, v25
v_and_b32 v25, 0xffff0000, v25
v_mul_f32_e32 v24, v27, v24
v_mul_f32_e32 v25, v27, v25
Cv 24,24,25
v_accvgpr_write_b32 a150, v24
v_accvgpr_read_b32 v25, a151
v_lshlrev_b32 v24, 16, v25
v_and_b32 v25, 0xffff0000, v25
v_mul_f32_e32 v24, v27, v24
v_mul_f32_e32 v25, v27, v25
Cv 24,24,25
v_accvgpr_write_b32 a151, v24
v_accvgpr_read_b32 v25, a156
v_lshlrev_b32 v24, 16, v25
v_and_b32 v25, 0xffff0000, v25
v_mul_f32_e32 v24, v27, v24
v_mul_f32_e32 v25, v27, v25
Cv 24,24,25
v_accvgpr_write_b32 a156, v24
v_accvgpr_read_b32 v25, a157
v_lshlrev_b32 v24, 16, v25
v_and_b32 v25, 0xffff0000, v25
v_mul_f32_e32 v24, v27, v24
v_mul_f32_e32 v25, v27, v25
Cv 24,24,25
v_accvgpr_write_b32 a157, v24
v_accvgpr_read_b32 v25, a158
v_lshlrev_b32 v24, 16, v25
v_and_b32 v25, 0xffff0000, v25
v_mul_f32_e32 v24, v27, v24
v_mul_f32_e32 v25, v27, v25
Cv 24,24,25
v_accvgpr_write_b32 a158, v24
)" R"(
v_accvgpr_read_b32 v25, a159
v_lshlrev_b32 v24, 16, v25
v_and_b32 v25, 0xffff0000, v25
v_mul_f32_e32 v24, v27, v24
v_mul_f32_e32 v25, v27, v25
Cv 24,24,25
v_accvgpr_write_b32 a159, v24
v_accvgpr_read_b32 v25, a164
v_lshlrev_b32 v24, 16, v25
v_and_b32 v25, 0xffff0000, v25
v_mul_f32_e32 v24, v27, v24
v_mul_f32_e32 v25, v27, v25
Cv 24,24,25
v_accvgpr_write_b32 a164, v24
v_accvgpr_read_b32 v25, a165
v_lshlrev_b32 v24, 16, v25
v_and_b32 v25, 0xffff0000, v25
v_mul_f32_e32 v24, v27, v24
v_mul_f32_e32 v25, v27, v25
Cv 24,24,25
v_accvgpr_write_b32 a165, v24
v_accvgpr_read_b32 v25, a166
v_lshlrev_b32 v24, 16, v25
v_and_b32 v25, 0xffff0000, v25
v_mul_f32_e32 v24, v27, v24
v_mul_f32_e32 v25, v27, v25
Cv 24,24,25
v_accvgpr_write_b32 a166, v24
v_accvgpr_read_b32 v25, a167
v_lshlrev_b32 v24, 16, v25
v_and_b32 v25, 0xffff0000, v25
v_mul_f32_e32 v24, v27, v24
v_mul_f32_e32 v25, v27, v25
Cv 24,24,25
v_accvgpr_write_b32 a167, v24
v_accvgpr_read_b32 v25, a172
v_lshlrev_b32 v24, 16, v25
v_and_b32 v25, 0xffff0000, v25
v_mul_f32_e32 v24, v27, v24
v_mul_f32_e32 v25, v27, v25
Cv 24,24,25
v_accvgpr_write_b32 a172, v24
v_accvgpr_read_b32 v25, a173
v_lshlrev_b32 v24, 16, v25
v_and_b32 v25, 0xffff0000, v25
v_mul_f32_e32 v24, v27, v24
v_mul_f32_e32 v25, v27, v25
Cv 24,24,25
v_accvgpr_write_b32 a173, v24
v_accvgpr_read_b32 v25, a174
v_lshlrev_b32 v24, 16, v25
v_and_b32 v25, 0xffff0000, v25
v_mul_f32_e32 v24, v27, v24
v_mul_f32_e32 v25, v27, v25
Cv 24,24,25
v_accvgpr_write_b32 a174, v24
v_accvgpr_read_b32 v25, a175
v_lshlrev_b32 v24, 16, v25
v_and_b32 v25, 0xffff0000, v25
v_mul_f32_e32 v24, v27, v24
v_mul_f32_e32 v25, v27, v25
Cv 24,24,25
v_accvgpr_write_b32 a175, v24
v_accvgpr_read_b32 v25, a180
)" R"(
v_lshlrev_b32 v24, 16, v25
v_and_b32 v25, 0xffff0000, v25
v_mul_f32_e32 v24, v27, v24
v_mul_f32_e32 v25, v27, v25
Cv 24,24,25
v_accvgpr_write_b32 a180, v24
v_accvgpr_read_b32 v25, a181
v_lshlrev_b32 v24, 16, v25
v_and_b32 v25, 0xffff0000, v25
v_mul_f32_e32 v24, v27, v24
v_mul_f32_e32 v25, v27, v25
Cv 24,24,25
v_accvgpr_write_b32 a181, v24
v_accvgpr_read_b32 v25, a182
v_lshlrev_b32 v24, 16, v25
v_and_b32 v25, 0xffff0000, v25
v_mul_f32_e32 v24, v27, v24
v_mul_f32_e32 v25, v27, v25
Cv 24,24,25
v_accvgpr_write_b32 a182, v24
v_accvgpr_read_b32 v25, a183
v_lshlrev_b32 v24, 16, v25
v_and_b32 v25, 0xffff0000, v25
v_mul_f32_e32 v24, v27, v24
v_mul_f32_e32 v25, v27, v25
Cv 24,24,25
v_accvgpr_write_b32 a183, v24
v_accvgpr_read_b32 v25, a188
v_lshlrev_b32 v24, 16, v25
v_and_b32 v25, 0xffff0000, v25
v_mul_f32_e32 v24, v27, v24
v_mul_f32_e32 v25, v27, v25
Cv 24,24,25
v_accvgpr_write_b32 a188, v24
v_accvgpr_read_b32 v25, a189
v_lshlrev_b32 v24, 16, v25
v_and_b32 v25, 0xffff0000, v25
v_mul_f32_e32 v24, v27, v24
v_mul_f32_e32 v25, v27, v25
Cv 24,24,25
v_accvgpr_write_b32 a189, v24
v_accvgpr_read_b32 v25, a190
v_lshlrev_b32 v24, 16, v25
v_and_b32 v25, 0xffff0000, v25
v_mul_f32_e32 v24, v27, v24
v_mul_f32_e32 v25, v27, v25
Cv 24,24,25
v_accvgpr_write_b32 a190, v24
v_accvgpr_read_b32 v25, a191
v_lshlrev_b32 v24, 16, v25
v_and_b32 v25, 0xffff0000, v25
v_mul_f32_e32 v24, v27, v24
v_mul_f32_e32 v25, v27, v25
Cv 24,24,25
v_accvgpr_write_b32 a191, v24
v_exp_f32_e32 v48, v96
v_exp_f32_e32 v24, v97
s_nop 0
v_add_f32_e32 v14, v48, v24
Cv 48,48,24
v_exp_f32_e32 v49, v98
v_exp_f32_e32 v25, v99
v_add_f32_e32 v14, v14, v49
v_add_f32_e32 v14, v14, v25
)" R"(
Cv 49,49,25
v_exp_f32_e32 v50, v104
v_exp_f32_e32 v26, v105
v_add_f32_e32 v14, v14, v50
v_add_f32_e32 v14, v14, v26
Cv 50,50,26
v_exp_f32_e32 v51, v106
v_exp_f32_e32 v27, v107
v_add_f32_e32 v14, v14, v51
v_add_f32_e32 v14, v14, v27
Cv 51,51,27
v_exp_f32_e32 v52, v100
v_exp_f32_e32 v28, v101
s_nop 0
v_add_f32_e32 v15, v52, v28
Cv 52,52,28
v_exp_f32_e32 v53, v102
v_exp_f32_e32 v29, v103
v_add_f32_e32 v15, v15, v53
v_add_f32_e32 v15, v15, v29
Cv 53,53,29
v_exp_f32_e32 v54, v108
v_exp_f32_e32 v24, v109
v_add_f32_e32 v15, v15, v54
v_add_f32_e32 v15, v15, v24
Cv 54,54,24
v_exp_f32_e32 v55, v110
v_exp_f32_e32 v25, v111
v_add_f32_e32 v15, v15, v55
v_add_f32_e32 v15, v15, v25
Cv 55,55,25
s_nop 1
s_branch .LzMRR
.Lzsp23:
s_sub_u32 s53, s53, 1
s_cbranch_scc1 .Lzsq23
s_sleep 1
buffer_load_dword v4, v7, s[20:23], s52 offen offset:0 sc0 sc1
s_waitcnt vmcnt(0)
v_cmp_eq_u32_e32 vcc, s3, v4
s_andn2_b64 s[28:29], 1, vcc
s_cbranch_scc1 .Lzsp23
s_branch .Lzok23
.Lzsq23:
s_mov_b32 s53, 0
s_branch .Lzok23
.Lzsp24:
s_sub_u32 s53, s53, 1
s_cbranch_scc1 .Lzsq24
s_sleep 1
buffer_load_dword v4, v7, s[20:23], s52 offen offset:4 sc0 sc1
s_waitcnt vmcnt(0)
v_cmp_eq_u32_e32 vcc, s3, v4
s_andn2_b64 s[28:29], 1, vcc
s_cbranch_scc1 .Lzsp24
s_branch .Lzok24
.Lzsq24:
s_mov_b32 s53, 0
s_branch .Lzok24
.LzBB:
v_lshlrev_b32 v95, 7, v0
s_mov_b32 s28, 0xc10c000
s_add_u32 s29, s28, 6144
.LzBB1:
)" R"(
buffer_load_dword v92, v95, s[20:23], s28 offen sc0 sc1
buffer_load_dword v93, v95, s[20:23], s29 offen sc0 sc1
s_waitcnt vmcnt(0)
v_cmp_ne_u32_e32 vcc, s3, v92
v_cmp_ne_u32_e64 s[30:31], s3, v93
s_or_b64 vcc, vcc, s[30:31]
s_cbranch_scc0 .LzBB2
s_sub_u32 s53, s53, 1
s_cbranch_scc0 .LzBB1
s_mov_b32 s53, 0
.LzBB2:
s_mul_i32 s28, s56, 7168
s_add_u32 s28, s28, 0xc158000
s_add_u32 s29, s28, 4096
buffer_load_dwordx4 v[64:67], v1, s[20:23], s28 offen offset:0 sc0 sc1
buffer_load_dwordx4 v[68:71], v1, s[20:23], s28 offen offset:1024 sc0 sc1
buffer_load_dwordx4 v[72:75], v1, s[20:23], s28 offen offset:2048 sc0 sc1
buffer_load_dwordx4 v[76:79], v1, s[20:23], s28 offen offset:3072 sc0 sc1
buffer_load_dwordx4 v[80:83], v1, s[20:23], s29 offen offset:0 sc0 sc1
buffer_load_dwordx4 v[84:87], v1, s[20:23], s29 offen offset:1024 sc0 sc1
buffer_load_dwordx4 v[88:91], v1, s[20:23], s29 offen offset:2048 sc0 sc1
s_cmp_gt_u32 s56, 27
s_cselect_b32 s28, 16, 0
s_cselect_b32 s29, 28, 0
s_lshr_b32 s28, s63, s28
s_and_b32 s28, s28, 0x7ff
s_sub_u32 s29, s56, s29
s_lshl_b32 s29, s29, 5
s_mul_i32 s83, s28, 896
s_add_u32 s29, s29, s83
v_and_b32 v95, 3, v0
v_lshlrev_b32 v95, 3, v95
buffer_load_dwordx2 v[92:93], v95, s[12:15], s29 offen
s_lshl4_add_u32 s28, s28, 0xc10000c
v_mov_b32 v94, 0
buffer_load_dword v94, v94, s[20:23], s28 offen sc0 sc1
s_waitcnt vmcnt(0)
v_add_f32_e32 v64, v64, v68
v_add_f32_e32 v65, v65, v69
v_add_f32_e32 v66, v66, v70
v_add_f32_e32 v67, v67, v71
v_add_f32_e32 v72, v72, v76
v_add_f32_e32 v73, v73, v77
v_add_f32_e32 v74, v74, v78
v_add_f32_e32 v75, v75, v79
v_add_f32_e32 v80, v80, v84
v_add_f32_e32 v81, v81, v85
v_add_f32_e32 v82, v82, v86
v_add_f32_e32 v83, v83, v87
v_add_f32_e32 v64, v64, v72
v_add_f32_e32 v65, v65, v73
v_add_f32_e32 v66, v66, v74
v_add_f32_e32 v67, v67, v75
v_add_f32_e32 v80, v80, v88
v_add_f32_e32 v81, v81, v89
v_add_f32_e32 v82, v82, v90
v_add_f32_e32 v83, v83, v91
v_add_f32_e32 v64, v64, v80
v_add_f32_e32 v65, v65, v81
v_add_f32_e32 v66, v66, v82
v_add_f32_e32 v67, v67, v83
v_mov_b32 v68, v64
s_nop 1
v_permlane32_swap_b32_e32 v64, v68
)" R"(
v_add_f32_e32 v64, v64, v68
v_mov_b32 v68, v64
s_nop 1
v_permlane16_swap_b32_e32 v64, v68
v_add_f32_e32 v64, v64, v68
v_mov_b32 v68, v65
s_nop 1
v_permlane32_swap_b32_e32 v65, v68
v_add_f32_e32 v65, v65, v68
v_mov_b32 v68, v65
s_nop 1
v_permlane16_swap_b32_e32 v65, v68
v_add_f32_e32 v65, v65, v68
v_mov_b32 v68, v66
s_nop 1
v_permlane32_swap_b32_e32 v66, v68
v_add_f32_e32 v66, v66, v68
v_mov_b32 v68, v66
s_nop 1
v_permlane16_swap_b32_e32 v66, v68
v_add_f32_e32 v66, v66, v68
v_mov_b32 v68, v67
s_nop 1
v_permlane32_swap_b32_e32 v67, v68
v_add_f32_e32 v67, v67, v68
v_mov_b32 v68, v67
s_nop 1
v_permlane16_swap_b32_e32 v67, v68
v_add_f32_e32 v67, v67, v68
v_add_f32_dpp v64, v64, v64 row_ror:8 row_mask:0xf bank_mask:0xf
v_add_f32_dpp v65, v65, v65 row_ror:8 row_mask:0xf bank_mask:0xf
v_add_f32_dpp v66, v66, v66 row_ror:8 row_mask:0xf bank_mask:0xf
v_add_f32_dpp v67, v67, v67 row_ror:8 row_mask:0xf bank_mask:0xf
v_add_f32_dpp v64, v64, v64 row_ror:4 row_mask:0xf bank_mask:0xf
v_add_f32_dpp v65, v65, v65 row_ror:4 row_mask:0xf bank_mask:0xf
v_add_f32_dpp v66, v66, v66 row_ror:4 row_mask:0xf bank_mask:0xf
v_add_f32_dpp v67, v67, v67 row_ror:4 row_mask:0xf bank_mask:0xf
v_mul_f32_e32 v94, 0xbfb8aa3b, v94
v_exp_f32_e32 v94, v94
Cv 65,64,65
v_lshlrev_b32 v64, 16, v65
v_and_b32 v65, 0xffff0000, v65
v_add_f32_e32 v94, 1.0, v94
v_rcp_f32_e32 v94, v94
Cv 67,66,67
v_lshlrev_b32 v66, 16, v67
v_and_b32 v67, 0xffff0000, v67
Cv 94,94,94
v_lshlrev_b32 v94, 16, v94
v_mul_f32_e32 v64, v94, v64
v_mul_f32_e32 v65, v94, v65
v_mul_f32_e32 v66, v94, v66
v_mul_f32_e32 v67, v94, v67
Cv 65,64,65
v_lshlrev_b32 v64, 16, v65
v_and_b32 v65, 0xffff0000, v65
Cv 67,66,67
v_lshlrev_b32 v66, 16, v67
v_and_b32 v67, 0xffff0000, v67
v_lshlrev_b32 v68, 16, v92
v_and_b32 v69, 0xffff0000, v92
v_lshlrev_b32 v70, 16, v93
v_and_b32 v71, 0xffff0000, v93
v_add_f32_e32 v64, v64, v68
)" R"(
v_add_f32_e32 v65, v65, v69
v_add_f32_e32 v66, v66, v70
v_add_f32_e32 v67, v67, v71
Cv 64,64,65
Cv 66,66,67
v_mov_b32 v65, s3
v_mov_b32 v67, s3
v_lshrrev_b32 v95, 2, v0
v_min_u32 v95, 1, v95
v_lshl_or_b32 v95, v95, 30, v1
s_lshl_b32 s28, s56, 7
s_add_u32 s28, s28, 0xc109000
buffer_store_dwordx4 v[64:67], v95, s[20:23], s28 offen sc0 sc1
buffer_load_dword v4, v165, s[20:23], s40 offen sc0 sc1
s_waitcnt vmcnt(0)
s_branch .LzBBR
.LzPBKX:
Mv 32,0,32
Rt 128,18,16384
Mv 64,0,36
buffer_load_dword v4, v5, s[20:23], s50 offen offset:0 sc0 sc1
Mv 96,0,40
Rt 130,18,18432
Mv 128,0,44
Mv 160,0,48
Rt 132,19,16384
Mv 192,0,52
Mv 224,0,56
Rt 134,19,18432
Ma 192,0,60
Ma 196,4,60
Rt 136,20,16384
Mv 228,4,56
Mv 196,4,52
Rt 138,20,18432
Mv 164,4,48
Mv 132,4,44
Rt 140,21,16384
Mv 100,4,40
Mv 68,4,36
Rt 142,21,18432
Mv 36,4,32
Mv 40,8,32
Rt 144,22,16384
Mv 72,8,36
Mv 104,8,40
Rt 146,22,18432
Mv 136,8,44
Mv 168,8,48
Rt 148,23,16384
Mv 200,8,52
Mv 232,8,56
Rt 150,23,18432
Ma 200,8,60
Ma 204,12,60
Rt 152,24,16384
Mv 236,12,56
Mv 204,12,52
Rt 154,24,18432
Mv 172,12,48
Mv 140,12,44
Rt 156,25,16384
Mv 108,12,40
Mv 76,12,36
)" R"(
Rt 158,25,18432
Mv 44,12,32
Mv 48,16,32
Rb 160,17,0
Mv 80,16,36
Mv 112,16,40
Rb 164,17,2048
Mv 144,16,44
Mv 176,16,48
Rb 168,17,8192
Mv 208,16,52
Mv 240,16,56
Rb 172,17,10240
Ma 208,16,60
s_waitcnt vmcnt(0)
v_cmp_eq_u32_e32 vcc, s3, v4
s_andn2_b64 s[28:29], s[60:61], vcc
s_cbranch_scc1 .Lzsp14
.Lzok14:
Ma 212,20,60
Rb 176,17,16384
Mv 244,20,56
s_lshl3_add_u32 s29, s34, s35
s_lshl2_add_u32 s29, s29, s33
Mv 212,20,52
Rb 180,17,18432
s_add_u32 m0, s36, 131072
Mv 180,20,48
buffer_load_dwordx4 v2, s[20:23], s56 offen offset:0 sc1 lds
Mv 148,20,44
Rb 184,17,24576
s_mul_i32 s83, s29, 7168
Mv 116,20,40
s_lshl_b32 s82, s34, 21
s_add_u32 s82, s82, 0x6000000
Mv 84,20,36
Rb 188,17,26624
s_add_u32 s82, s82, s84
Mv 52,20,32
s_mov_b32 s24, s80
s_mov_b32 s25, s81
Mv 56,24,32
s_mov_b32 s26, 0x380000
s_mov_b32 s27, 0x20000
Mv 88,24,36
buffer_load_dwordx4 v2, s[20:23], s56 offen offset:1024 sc1 lds
Mv 120,24,40
s_add_u32 s28, s33, 0
s_lshl2_add_u32 s28, s35, s28
Mv 152,24,44
s_mul_i32 s29, s28, 293
s_lshr_b32 s29, s29, 11
Mv 184,24,48
s_mul_i32 s29, s29, 9
s_add_u32 s29, s29, s28
Mv 216,24,52
s_lshl_b32 s29, s29, 10
s_add_u32 s4, s29, s82
Mv 248,24,56
s_add_u32 s28, s33, 4
s_lshl3_add_u32 s28, s28, s35
Ma 216,24,60
buffer_load_dwordx4 v2, s[20:23], s56 offen offset:2048 sc1 lds
Ma 220,28,60
)" R"(
s_mul_i32 s29, s28, 293
s_lshr_b32 s29, s29, 11
Mv 252,28,56
s_mul_i32 s29, s29, 9
s_add_u32 s29, s29, s28
Mv 220,28,52
s_lshl_b32 s29, s29, 10
s_add_u32 s5, s29, s82
Mv 188,28,48
s_add_u32 s28, s33, 8
s_lshl3_add_u32 s28, s28, s35
Mv 156,28,44
s_mul_i32 s29, s28, 293
s_lshr_b32 s29, s29, 11
Mv 124,28,40
buffer_load_dwordx4 v2, s[20:23], s56 offen offset:3072 sc1 lds
Mv 92,28,36
s_mul_i32 s29, s29, 9
s_add_u32 s29, s29, s28
Mv 60,28,32
s_waitcnt lgkmcnt(0)
s_barrier
Mv 32,128,160
Rt 0,18,32768
s_lshl_b32 s29, s29, 10
Mv 64,128,164
s_add_u32 s6, s29, s82
s_add_u32 s28, s33, 12
Mv 96,128,168
Rt 2,18,34816
s_add_u32 m0, s36, 0
Mv 128,128,172
buffer_load_dwordx4 v2, s[20:23], s55 offen offset:0 sc1 lds
Mv 160,128,176
Rt 4,19,32768
s_lshl3_add_u32 s28, s28, s35
Mv 192,128,180
s_mul_i32 s29, s28, 293
s_lshr_b32 s29, s29, 11
Mv 224,128,184
Rt 6,19,34816
s_mul_i32 s29, s29, 9
Ma 192,128,188
s_add_u32 s29, s29, s28
s_lshl_b32 s29, s29, 10
Ma 196,132,188
Rt 8,20,32768
buffer_load_dwordx4 v2, s[20:23], s55 offen offset:1024 sc1 lds
Mv 228,132,184
s_add_u32 s7, s29, s82
Mv 196,132,180
Rt 10,20,34816
Mv 164,132,176
s_cmp_gt_u32 s33, 1
s_cmov_b32 s7, 0x40000000
Mv 132,132,172
Rt 12,21,32768
s_add_u32 s28, s33, 14
Mv 100,132,168
buffer_load_dwordx4 v2, s[20:23], s55 offen offset:2048 sc1 lds
Mv 68,132,164
Rt 14,21,34816
s_lshl3_add_u32 s28, s28, s35
Mv 36,132,160
s_mul_i32 s29, s28, 293
)" R"(
s_lshr_b32 s29, s29, 11
Mv 40,136,160
Rt 16,22,32768
s_mul_i32 s29, s29, 9
Mv 72,136,164
s_add_u32 s29, s29, s28
s_lshl_b32 s29, s29, 10
Mv 104,136,168
Rt 18,22,34816
buffer_load_dwordx4 v2, s[20:23], s55 offen offset:3072 sc1 lds
Mv 136,136,172
s_add_u32 s8, s29, s82
s_add_u32 s28, s33, 18
Mv 168,136,176
Rt 20,23,32768
s_lshl3_add_u32 s28, s28, s35
Mv 200,136,180
s_mul_i32 s29, s28, 293
s_lshr_b32 s29, s29, 11
Mv 232,136,184
Rt 22,23,34816
s_add_u32 m0, s36, 4096
Ma 200,136,188
buffer_load_dwordx4 v3, s[20:23], s55 offen offset:0 sc1 lds
Ma 204,140,188
Rt 24,24,32768
s_mul_i32 s29, s29, 9
Mv 236,140,184
s_add_u32 s29, s29, s28
s_lshl_b32 s29, s29, 10
Mv 204,140,180
Rt 26,24,34816
s_add_u32 s9, s29, s82
Mv 172,140,176
s_add_u32 s28, s33, 22
s_lshl3_add_u32 s28, s28, s35
Mv 140,140,172
Rt 28,25,32768
buffer_load_dwordx4 v3, s[20:23], s55 offen offset:1024 sc1 lds
Mv 108,140,168
s_mul_i32 s29, s28, 293
s_lshr_b32 s29, s29, 11
Mv 76,140,164
Rt 30,25,34816
s_mul_i32 s29, s29, 9
Mv 44,140,160
s_add_u32 s29, s29, s28
s_lshl_b32 s29, s29, 10
Mv 48,144,160
Rb 32,16,32768
s_add_u32 s10, s29, s82
Mv 80,144,164
buffer_load_dwordx4 v3, s[20:23], s55 offen offset:2048 sc1 lds
Mv 112,144,168
Rb 36,16,34816
s_add_u32 s28, s33, 26
Mv 144,144,172
s_lshl3_add_u32 s28, s28, s35
s_mul_i32 s29, s28, 293
Mv 176,144,176
Rb 40,16,40960
s_lshr_b32 s29, s29, 11
Mv 208,144,180
s_mul_i32 s29, s29, 9
)" R"(
s_add_u32 s29, s29, s28
Mv 240,144,184
Rb 44,16,43008
buffer_load_dwordx4 v3, s[20:23], s55 offen offset:3072 sc1 lds
Ma 208,144,188
s_lshl_b32 s29, s29, 10
s_add_u32 s11, s29, s82
Ma 212,148,188
Rb 48,16,49152
Mv 244,148,184
s_cmp_gt_u32 s33, 1
s_cmov_b32 s11, 0x40000000
Mv 212,148,180
Rb 52,16,51200
s_add_u32 m0, s36, 135168
Mv 180,148,176
buffer_load_dwordx4 v3, s[20:23], s56 offen offset:0 sc1 lds
Mv 148,148,172
Rb 56,16,57344
s_and_b32 s28, s34, 1
Mv 116,148,168
s_lshl_b32 s28, s28, 14
s_add_u32 s82, s37, s28
Mv 84,148,164
Rb 60,16,59392
s_add_u32 s82, s82, 0x10000
Mv 52,148,160
Mv 56,152,160
buffer_load_dwordx4 v3, s[20:23], s56 offen offset:1024 sc1 lds
Mv 88,152,164
Mv 120,152,168
Mv 152,152,172
Mv 184,152,176
Mv 216,152,180
buffer_load_dwordx4 v3, s[20:23], s56 offen offset:2048 sc1 lds
Mv 248,152,184
Ma 216,152,188
Ma 220,156,188
Mv 252,156,184
Mv 220,156,180
buffer_load_dwordx4 v3, s[20:23], s56 offen offset:3072 sc1 lds
Mv 188,156,176
Mv 156,156,172
Mv 124,156,168
s_add_u32 s55, s55, 0x10000
s_add_u32 s56, s56, 0x10000
Mv 92,156,164
Mv 60,156,160
s_waitcnt lgkmcnt(4)
s_barrier
Mv 32,0,32
Rt 128,18,49152
Mv 64,0,36
Rt 130,18,51200
Mv 96,0,40
Rt 132,19,49152
s_add_u32 m0, s36, 65536
Mv 128,0,44
Rt 134,19,51200
buffer_load_dwordx4 v2, s[20:23], s56 offen offset:0 sc1 lds
Mv 132,4,44
Rt 136,20,49152
Mv 100,4,40
Rt 138,20,51200
Mv 68,4,36
)" R"(
Rt 140,21,49152
Mv 36,4,32
Rt 142,21,51200
Mv 40,8,32
Rt 144,22,49152
Mv 72,8,36
Rt 146,22,51200
Mv 104,8,40
Rt 148,23,49152
Mv 136,8,44
Rt 150,23,51200
buffer_load_dwordx4 v2, s[20:23], s56 offen offset:1024 sc1 lds
Mv 140,12,44
Rt 152,24,49152
Mv 108,12,40
Rt 154,24,51200
Mv 76,12,36
Rt 156,25,49152
Mv 44,12,32
Rt 158,25,51200
Mv 48,16,32
Rb 160,17,32768
Mv 80,16,36
Rb 164,17,34816
Mv 112,16,40
Rb 168,17,40960
Mv 144,16,44
Rb 172,17,43008
buffer_load_dwordx4 v2, s[20:23], s56 offen offset:2048 sc1 lds
Mv 148,20,44
Rb 176,17,49152
Mv 116,20,40
Rb 180,17,51200
Mv 84,20,36
Rb 184,17,57344
Mv 52,20,32
Rb 188,17,59392
Mv 56,24,32
v_add_u32 v8, 0x10000, v1
v_add_u32 v10, 0x20000, v1
Mv 88,24,36
buffer_load_dword v4, v5, s[20:23], s50 offen offset:0 sc0 sc1
Mv 120,24,40
s_lshl_b64 s[60:61], s[60:61], 8
s_or_b32 s60, s60, 0xff
Mv 152,24,44
buffer_load_dwordx4 v2, s[20:23], s56 offen offset:3072 sc1 lds
Mv 156,28,44
s_mov_b32 s93, 2
s_mov_b32 s94, 0
Mv 124,28,40
Mv 92,28,36
Mv 60,28,32
s_waitcnt lgkmcnt(0)
s_barrier
Mv 32,128,160
Mv 36,132,160
Mv 40,136,160
s_add_u32 m0, s36, 32768
Mv 44,140,160
buffer_load_dwordx4 v2, s[20:23], s55 offen offset:0 sc1 lds
Mv 48,144,160
Mv 52,148,160
s_nop 0
Cv 32,32,33
)" R"(
Cv 33,34,35
Mv 56,152,160
Cv 34,36,37
Cv 35,38,39
Mv 60,156,160
Cv 40,40,41
Cv 41,42,43
Mv 64,128,164
Cv 42,44,45
Cv 43,46,47
Mv 68,132,164
Cv 48,48,49
Cv 49,50,51
Mv 72,136,164
Cv 50,52,53
Cv 51,54,55
Mv 76,140,164
buffer_load_dwordx4 v2, s[20:23], s55 offen offset:1024 sc1 lds
Mv 80,144,164
Cv 56,56,57
Cv 57,58,59
Mv 84,148,164
Cv 58,60,61
Cv 59,62,63
Mv 88,152,164
Cv 64,64,65
Cv 65,66,67
Mv 92,156,164
Cv 66,68,69
Cv 67,70,71
Mv 96,128,168
Cv 72,72,73
Cv 73,74,75
Mv 100,132,168
Cv 74,76,77
Cv 75,78,79
Mv 104,136,168
Cv 80,80,81
Cv 81,82,83
Mv 108,140,168
buffer_load_dwordx4 v2, s[20:23], s55 offen offset:2048 sc1 lds
Mv 112,144,168
Cv 82,84,85
Cv 83,86,87
Mv 116,148,168
Cv 88,88,89
Cv 89,90,91
Mv 120,152,168
Cv 90,92,93
Cv 91,94,95
Mv 124,156,168
buffer_store_dwordx4 v[32:35], v2, s[20:23], s45 offen offset:0 sc0 sc1
Mv 128,128,172
Mv 132,132,172
Mv 136,136,172
Mv 140,140,172
buffer_load_dwordx4 v2, s[20:23], s55 offen offset:3072 sc1 lds
Mv 144,144,172
Mv 148,148,172
s_nop 1
Cv 128,128,129
Cv 129,130,131
Mv 152,152,172
Cv 130,132,133
)" R"(
Cv 131,134,135
Mv 156,156,172
buffer_store_dwordx4 v[40:43], v2, s[20:23], s45 offen offset:1024 sc0 sc1
Mv 160,0,48
Cv 136,136,137
Cv 137,138,139
Mv 160,128,176
Cv 138,140,141
Cv 139,142,143
Mv 164,4,48
s_add_u32 m0, s36, 36864
Cv 144,144,145
Mv 164,132,176
buffer_load_dwordx4 v3, s[20:23], s55 offen offset:0 sc1 lds
Mv 168,8,48
Cv 145,146,147
Cv 146,148,149
Mv 168,136,176
Cv 147,150,151
Cv 152,152,153
Mv 172,12,48
Cv 153,154,155
Cv 154,156,157
Mv 172,140,176
buffer_store_dwordx4 v[48:51], v2, s[20:23], s45 offen offset:2048 sc0 sc1
Mv 176,16,48
Cv 155,158,159
Cv 132,96,97
Mv 176,144,176
Cv 133,98,99
Cv 134,100,101
Mv 180,20,48
Cv 135,102,103
Cv 140,104,105
Mv 180,148,176
buffer_load_dwordx4 v3, s[20:23], s55 offen offset:1024 sc1 lds
Mv 184,24,48
Cv 141,106,107
Cv 142,108,109
Mv 184,152,176
Cv 143,110,111
Cv 148,112,113
Mv 188,28,48
Cv 149,114,115
Cv 150,116,117
Mv 188,156,176
buffer_store_dwordx4 v[56:59], v2, s[20:23], s45 offen offset:3072 sc0 sc1
Mv 192,0,52
Cv 151,118,119
Cv 156,120,121
Mv 192,128,180
Cv 157,122,123
Cv 158,124,125
Mv 196,4,52
Cv 159,126,127
Cv 160,160,161
Mv 196,132,180
buffer_load_dwordx4 v3, s[20:23], s55 offen offset:2048 sc1 lds
Mv 200,8,52
Cv 161,162,163
Cv 162,164,165
Mv 200,136,180
Cv 163,166,167
v_accvgpr_write_b32 a224, v160
)" R"(
Mv 204,12,52
v_accvgpr_write_b32 a225, v161
v_accvgpr_write_b32 a226, v162
Mv 204,140,180
buffer_store_dwordx4 v[64:67], v3, s[20:23], s45 offen offset:0 sc0 sc1
Mv 208,16,52
v_accvgpr_write_b32 a227, v163
Cv 168,168,169
Mv 208,144,180
Cv 169,170,171
Cv 170,172,173
Mv 212,20,52
Cv 171,174,175
v_accvgpr_write_b32 a228, v168
Mv 212,148,180
buffer_load_dwordx4 v3, s[20:23], s55 offen offset:3072 sc1 lds
Mv 216,24,52
v_accvgpr_write_b32 a229, v169
v_accvgpr_write_b32 a230, v170
Mv 216,152,180
v_accvgpr_write_b32 a231, v171
Cv 176,176,177
Mv 220,28,52
Cv 177,178,179
Cv 178,180,181
Mv 220,156,180
buffer_store_dwordx4 v[72:75], v3, s[20:23], s45 offen offset:1024 sc0 sc1
Mv 224,0,56
Cv 179,182,183
v_accvgpr_write_b32 a232, v176
Ma 192,0,60
v_accvgpr_write_b32 a233, v177
v_accvgpr_write_b32 a234, v178
Ma 196,4,60
s_add_u32 m0, s36, 69632
v_accvgpr_write_b32 a235, v179
Mv 228,4,56
buffer_load_dwordx4 v3, s[20:23], s56 offen offset:0 sc1 lds
Mv 232,8,56
Cv 184,184,185
Cv 185,186,187
Ma 200,8,60
Cv 186,188,189
Cv 187,190,191
Ma 204,12,60
v_accvgpr_write_b32 a236, v184
v_accvgpr_write_b32 a237, v185
Mv 236,12,56
s_waitcnt vmcnt(16)
s_barrier
Mv 240,16,56
v_cmp_eq_u32_e32 vcc, s3, v4
s_andn2_b64 s[28:29], s[60:61], vcc
s_cbranch_scc1 .Lzsp15
.Lzok15:
Ma 208,16,60
buffer_store_dwordx4 v[80:83], v3, s[20:23], s45 offen offset:2048 sc0 sc1
Ma 212,20,60
v_accvgpr_write_b32 a238, v186
v_accvgpr_write_b32 a239, v187
Mv 244,20,56
buffer_load_dwordx4 v3, s[20:23], s56 offen offset:1024 sc1 lds
Mv 248,24,56
Cv 192,192,193
Cv 193,194,195
)" R"(
Ma 216,24,60
Cv 194,196,197
Cv 195,198,199
Ma 220,28,60
v_accvgpr_write_b32 a240, v192
v_accvgpr_write_b32 a241, v193
Mv 252,28,56
buffer_store_dwordx4 v[88:91], v3, s[20:23], s45 offen offset:3072 sc0 sc1
Mv 224,128,184
Rb 0,1,0
v_accvgpr_write_b32 a242, v194
Mv 228,132,184
Rb 4,1,1024
v_accvgpr_write_b32 a243, v195
Mv 232,136,184
Rb 8,1,2048
Cv 200,200,201
Mv 236,140,184
buffer_load_dwordx4 v3, s[20:23], s56 offen offset:2048 sc1 lds
Rb 12,1,3072
Mv 240,144,184
Rb 16,1,8192
Cv 201,202,203
Mv 244,148,184
Rb 20,1,9216
Cv 202,204,205
Mv 248,152,184
Rb 24,1,10240
Cv 203,206,207
Mv 252,156,184
Rb 28,1,11264
v_accvgpr_write_b32 a244, v200
Ma 192,128,188
Rb 32,1,4096
v_accvgpr_write_b32 a245, v201
Ma 196,132,188
Rb 36,1,5120
v_accvgpr_write_b32 a246, v202
Ma 200,136,188
Rb 40,1,6144
v_accvgpr_write_b32 a247, v203
Ma 204,140,188
buffer_load_dwordx4 v3, s[20:23], s56 offen offset:3072 sc1 lds
Rb 44,1,7168
Ma 208,144,188
Ma 212,148,188
Ma 216,152,188
Ma 220,156,188
s_add_u32 s56, s56, 0x10000
s_branch .LzFB
.LzPBVX:
Mv 32,32,0
Rt 128,18,16384
Mv 36,36,0
buffer_load_dword v4, v5, s[20:23], s50 offen offset:0 sc0 sc1
Mv 96,40,0
Rt 130,18,18432
Mv 100,44,0
Mv 160,48,0
Rt 132,19,16384
Mv 164,52,0
Mv 224,56,0
Rt 134,19,18432
Mv 228,60,0
)" R"(
Mv 236,60,4
Rt 136,20,16384
Mv 232,56,4
Mv 172,52,4
Rt 138,20,18432
Mv 168,48,4
Mv 108,44,4
Rt 140,21,16384
Mv 104,40,4
Mv 44,36,4
Rt 142,21,18432
Mv 40,32,4
Mv 48,32,8
Rt 144,22,16384
Mv 52,36,8
Mv 112,40,8
Rt 146,22,18432
Mv 116,44,8
Mv 176,48,8
Rt 148,23,16384
Mv 180,52,8
Mv 240,56,8
Rt 150,23,18432
Mv 244,60,8
Mv 252,60,12
Rt 152,24,16384
Mv 248,56,12
Mv 188,52,12
Rt 154,24,18432
Mv 184,48,12
Mv 124,44,12
Rt 156,25,16384
Mv 120,40,12
Mv 60,36,12
Rt 158,25,18432
Mv 56,32,12
Mv 64,32,16
Rb 160,17,0
Mv 68,36,16
Mv 128,40,16
Rb 164,17,2048
Mv 132,44,16
Mv 192,48,16
Rb 168,17,8192
Mv 196,52,16
Ma 192,56,16
Rb 172,17,10240
Ma 196,60,16
s_waitcnt vmcnt(0)
v_cmp_eq_u32_e32 vcc, s3, v4
s_andn2_b64 s[28:29], s[60:61], vcc
s_cbranch_scc1 .Lzsp16
.Lzok16:
Ma 204,60,20
Rb 176,17,16384
Ma 200,56,20
s_lshl3_add_u32 s29, s34, s35
s_lshl2_add_u32 s29, s29, s33
Mv 204,52,20
Rb 180,17,18432
s_add_u32 m0, s36, 131072
Mv 200,48,20
buffer_load_dwordx4 v2, s[20:23], s56 offen offset:0 sc1 lds
Mv 140,44,20
)" R"(
Rb 184,17,24576
s_mul_i32 s83, s29, 7168
Mv 136,40,20
s_lshl_b32 s82, s34, 21
s_add_u32 s82, s82, 0x6000000
Mv 76,36,20
Rb 188,17,26624
s_add_u32 s82, s82, s84
Mv 72,32,20
s_mov_b32 s24, s80
s_mov_b32 s25, s81
Mv 80,32,24
s_mov_b32 s26, 0x380000
s_mov_b32 s27, 0x20000
Mv 84,36,24
buffer_load_dwordx4 v2, s[20:23], s56 offen offset:1024 sc1 lds
Mv 144,40,24
s_add_u32 s28, s33, 0
s_lshl2_add_u32 s28, s35, s28
Mv 148,44,24
s_mul_i32 s29, s28, 293
s_lshr_b32 s29, s29, 11
Mv 208,48,24
s_mul_i32 s29, s29, 9
s_add_u32 s29, s29, s28
Mv 212,52,24
s_lshl_b32 s29, s29, 10
s_add_u32 s4, s29, s82
Ma 208,56,24
s_add_u32 s28, s33, 4
s_lshl3_add_u32 s28, s28, s35
Ma 212,60,24
buffer_load_dwordx4 v2, s[20:23], s56 offen offset:2048 sc1 lds
Ma 220,60,28
s_mul_i32 s29, s28, 293
s_lshr_b32 s29, s29, 11
Ma 216,56,28
s_mul_i32 s29, s29, 9
s_add_u32 s29, s29, s28
Mv 220,52,28
s_lshl_b32 s29, s29, 10
s_add_u32 s5, s29, s82
Mv 216,48,28
s_add_u32 s28, s33, 8
s_lshl3_add_u32 s28, s28, s35
Mv 156,44,28
s_mul_i32 s29, s28, 293
s_lshr_b32 s29, s29, 11
Mv 152,40,28
buffer_load_dwordx4 v2, s[20:23], s56 offen offset:3072 sc1 lds
Mv 92,36,28
s_mul_i32 s29, s29, 9
s_add_u32 s29, s29, s28
Mv 88,32,28
s_waitcnt lgkmcnt(0)
s_barrier
Mv 32,160,128
Rt 0,18,32768
s_lshl_b32 s29, s29, 10
Mv 36,164,128
s_add_u32 s6, s29, s82
s_add_u32 s28, s33, 12
Mv 96,168,128
Rt 2,18,34816
s_add_u32 m0, s36, 0
)" R"(
Mv 100,172,128
buffer_load_dwordx4 v2, s[20:23], s55 offen offset:0 sc1 lds
Mv 160,176,128
Rt 4,19,32768
s_lshl3_add_u32 s28, s28, s35
Mv 164,180,128
s_mul_i32 s29, s28, 293
s_lshr_b32 s29, s29, 11
Mv 224,184,128
Rt 6,19,34816
s_mul_i32 s29, s29, 9
Mv 228,188,128
s_add_u32 s29, s29, s28
s_lshl_b32 s29, s29, 10
Mv 236,188,132
Rt 8,20,32768
buffer_load_dwordx4 v2, s[20:23], s55 offen offset:1024 sc1 lds
Mv 232,184,132
s_add_u32 s7, s29, s82
Mv 172,180,132
Rt 10,20,34816
Mv 168,176,132
s_cmp_gt_u32 s33, 1
s_cmov_b32 s7, 0x40000000
Mv 108,172,132
Rt 12,21,32768
s_add_u32 s28, s33, 14
Mv 104,168,132
buffer_load_dwordx4 v2, s[20:23], s55 offen offset:2048 sc1 lds
Mv 44,164,132
Rt 14,21,34816
s_lshl3_add_u32 s28, s28, s35
Mv 40,160,132
s_mul_i32 s29, s28, 293
s_lshr_b32 s29, s29, 11
Mv 48,160,136
Rt 16,22,32768
s_mul_i32 s29, s29, 9
Mv 52,164,136
s_add_u32 s29, s29, s28
s_lshl_b32 s29, s29, 10
Mv 112,168,136
Rt 18,22,34816
buffer_load_dwordx4 v2, s[20:23], s55 offen offset:3072 sc1 lds
Mv 116,172,136
s_add_u32 s8, s29, s82
s_add_u32 s28, s33, 18
Mv 176,176,136
Rt 20,23,32768
s_lshl3_add_u32 s28, s28, s35
Mv 180,180,136
s_mul_i32 s29, s28, 293
s_lshr_b32 s29, s29, 11
Mv 240,184,136
Rt 22,23,34816
s_add_u32 m0, s36, 4096
Mv 244,188,136
buffer_load_dwordx4 v3, s[20:23], s55 offen offset:0 sc1 lds
Mv 252,188,140
Rt 24,24,32768
s_mul_i32 s29, s29, 9
Mv 248,184,140
s_add_u32 s29, s29, s28
s_lshl_b32 s29, s29, 10
)" R"(
Mv 188,180,140
Rt 26,24,34816
s_add_u32 s9, s29, s82
Mv 184,176,140
s_add_u32 s28, s33, 22
s_lshl3_add_u32 s28, s28, s35
Mv 124,172,140
Rt 28,25,32768
buffer_load_dwordx4 v3, s[20:23], s55 offen offset:1024 sc1 lds
Mv 120,168,140
s_mul_i32 s29, s28, 293
s_lshr_b32 s29, s29, 11
Mv 60,164,140
Rt 30,25,34816
s_mul_i32 s29, s29, 9
Mv 56,160,140
s_add_u32 s29, s29, s28
s_lshl_b32 s29, s29, 10
Mv 64,160,144
Rb 32,16,32768
s_add_u32 s10, s29, s82
Mv 68,164,144
buffer_load_dwordx4 v3, s[20:23], s55 offen offset:2048 sc1 lds
Mv 128,168,144
Rb 36,16,34816
s_add_u32 s28, s33, 26
Mv 132,172,144
s_lshl3_add_u32 s28, s28, s35
s_mul_i32 s29, s28, 293
Mv 192,176,144
Rb 40,16,40960
s_lshr_b32 s29, s29, 11
Mv 196,180,144
s_mul_i32 s29, s29, 9
s_add_u32 s29, s29, s28
Ma 192,184,144
Rb 44,16,43008
buffer_load_dwordx4 v3, s[20:23], s55 offen offset:3072 sc1 lds
Ma 196,188,144
s_lshl_b32 s29, s29, 10
s_add_u32 s11, s29, s82
Ma 204,188,148
Rb 48,16,49152
Ma 200,184,148
s_cmp_gt_u32 s33, 1
s_cmov_b32 s11, 0x40000000
Mv 204,180,148
Rb 52,16,51200
s_add_u32 m0, s36, 135168
Mv 200,176,148
buffer_load_dwordx4 v3, s[20:23], s56 offen offset:0 sc1 lds
Mv 140,172,148
Rb 56,16,57344
s_and_b32 s28, s34, 1
Mv 136,168,148
s_lshl_b32 s28, s28, 14
s_add_u32 s82, s37, s28
Mv 76,164,148
Rb 60,16,59392
s_add_u32 s82, s82, 0x10000
Mv 72,160,148
Mv 80,160,152
buffer_load_dwordx4 v3, s[20:23], s56 offen offset:1024 sc1 lds
Mv 84,164,152
)" R"(
Mv 144,168,152
Mv 148,172,152
Mv 208,176,152
Mv 212,180,152
buffer_load_dwordx4 v3, s[20:23], s56 offen offset:2048 sc1 lds
Ma 208,184,152
Ma 212,188,152
Ma 220,188,156
Ma 216,184,156
Mv 220,180,156
buffer_load_dwordx4 v3, s[20:23], s56 offen offset:3072 sc1 lds
Mv 216,176,156
Mv 156,172,156
Mv 152,168,156
s_add_u32 s55, s55, 0x10000
s_add_u32 s56, s56, 0x10000
Mv 92,164,156
Mv 88,160,156
s_waitcnt lgkmcnt(4)
s_barrier
Mv 32,32,0
Rt 128,18,49152
Mv 36,36,0
Rt 130,18,51200
Mv 96,40,0
Rt 132,19,49152
s_add_u32 m0, s36, 65536
Mv 100,44,0
Rt 134,19,51200
buffer_load_dwordx4 v2, s[20:23], s56 offen offset:0 sc1 lds
Mv 108,44,4
Rt 136,20,49152
Mv 104,40,4
Rt 138,20,51200
Mv 44,36,4
Rt 140,21,49152
Mv 40,32,4
Rt 142,21,51200
Mv 48,32,8
Rt 144,22,49152
Mv 52,36,8
Rt 146,22,51200
Mv 112,40,8
Rt 148,23,49152
Mv 116,44,8
Rt 150,23,51200
buffer_load_dwordx4 v2, s[20:23], s56 offen offset:1024 sc1 lds
Mv 124,44,12
Rt 152,24,49152
Mv 120,40,12
Rt 154,24,51200
Mv 60,36,12
Rt 156,25,49152
Mv 56,32,12
Rt 158,25,51200
Mv 64,32,16
Rb 160,17,32768
Mv 68,36,16
Rb 164,17,34816
Mv 128,40,16
Rb 168,17,40960
Mv 132,44,16
Rb 172,17,43008
buffer_load_dwordx4 v2, s[20:23], s56 offen offset:2048 sc1 lds
Mv 140,44,20
)" R"(
Rb 176,17,49152
Mv 136,40,20
Rb 180,17,51200
Mv 76,36,20
Rb 184,17,57344
Mv 72,32,20
Rb 188,17,59392
Mv 80,32,24
v_add_u32 v8, 0x10000, v1
v_add_u32 v10, 0x20000, v1
Mv 84,36,24
buffer_load_dword v4, v5, s[20:23], s50 offen offset:0 sc0 sc1
Mv 144,40,24
s_lshl_b64 s[60:61], s[60:61], 8
s_or_b32 s60, s60, 0xff
Mv 148,44,24
buffer_load_dwordx4 v2, s[20:23], s56 offen offset:3072 sc1 lds
Mv 156,44,28
s_mov_b32 s93, 2
s_mov_b32 s94, 0
Mv 152,40,28
Mv 92,36,28
Mv 88,32,28
s_waitcnt lgkmcnt(0)
s_barrier
Mv 32,160,128
Mv 36,164,128
Mv 40,160,132
s_add_u32 m0, s36, 32768
Mv 44,164,132
buffer_load_dwordx4 v2, s[20:23], s55 offen offset:0 sc1 lds
Mv 48,160,136
Mv 52,164,136
s_nop 0
Cv 32,32,33
Cv 33,34,35
Mv 56,160,140
Cv 34,36,37
Cv 35,38,39
Mv 60,164,140
Cv 40,40,41
Cv 41,42,43
Mv 64,160,144
Cv 42,44,45
Cv 43,46,47
Mv 68,164,144
Cv 48,48,49
Cv 49,50,51
Mv 72,160,148
Cv 50,52,53
Cv 51,54,55
Mv 76,164,148
buffer_load_dwordx4 v2, s[20:23], s55 offen offset:1024 sc1 lds
Mv 80,160,152
Cv 56,56,57
Cv 57,58,59
Mv 84,164,152
Cv 58,60,61
Cv 59,62,63
Mv 88,160,156
Cv 64,64,65
Cv 65,66,67
Mv 92,164,156
Cv 66,68,69
Cv 67,70,71
)" R"(
Mv 96,168,128
Cv 72,72,73
Cv 73,74,75
Mv 100,172,128
Cv 74,76,77
Cv 75,78,79
Mv 104,168,132
Cv 80,80,81
Cv 81,82,83
Mv 108,172,132
buffer_load_dwordx4 v2, s[20:23], s55 offen offset:2048 sc1 lds
Mv 112,168,136
Cv 82,84,85
Cv 83,86,87
Mv 116,172,136
Cv 88,88,89
Cv 89,90,91
Mv 120,168,140
Cv 90,92,93
Cv 91,94,95
Mv 124,172,140
buffer_store_dwordx4 v[32:35], v2, s[20:23], s45 offen offset:0 sc0 sc1
Mv 128,168,144
Mv 132,172,144
Mv 136,168,148
Mv 140,172,148
buffer_load_dwordx4 v2, s[20:23], s55 offen offset:3072 sc1 lds
Mv 144,168,152
Mv 148,172,152
s_nop 1
Cv 128,128,129
Cv 129,130,131
Mv 152,168,156
Cv 130,132,133
Cv 131,134,135
Mv 156,172,156
buffer_store_dwordx4 v[40:43], v2, s[20:23], s45 offen offset:1024 sc0 sc1
Mv 160,48,0
Cv 136,136,137
Cv 137,138,139
Mv 160,176,128
Cv 138,140,141
Cv 139,142,143
Mv 164,52,0
s_add_u32 m0, s36, 36864
Cv 144,144,145
Mv 164,180,128
buffer_load_dwordx4 v3, s[20:23], s55 offen offset:0 sc1 lds
Mv 168,48,4
Cv 145,146,147
Cv 146,148,149
Mv 168,176,132
Cv 147,150,151
Cv 152,152,153
Mv 172,52,4
Cv 153,154,155
Cv 154,156,157
Mv 172,180,132
buffer_store_dwordx4 v[48:51], v2, s[20:23], s45 offen offset:2048 sc0 sc1
Mv 176,48,8
Cv 155,158,159
Cv 132,96,97
Mv 176,176,136
Cv 133,98,99
)" R"(
Cv 134,100,101
Mv 180,52,8
Cv 135,102,103
Cv 140,104,105
Mv 180,180,136
buffer_load_dwordx4 v3, s[20:23], s55 offen offset:1024 sc1 lds
Mv 184,48,12
Cv 141,106,107
Cv 142,108,109
Mv 184,176,140
Cv 143,110,111
Cv 148,112,113
Mv 188,52,12
Cv 149,114,115
Cv 150,116,117
Mv 188,180,140
buffer_store_dwordx4 v[56:59], v2, s[20:23], s45 offen offset:3072 sc0 sc1
Mv 192,48,16
Cv 151,118,119
Cv 156,120,121
Mv 192,176,144
Cv 157,122,123
Cv 158,124,125
Mv 196,52,16
Cv 159,126,127
Cv 160,160,161
Mv 196,180,144
buffer_load_dwordx4 v3, s[20:23], s55 offen offset:2048 sc1 lds
Mv 200,48,20
Cv 161,162,163
Cv 162,164,165
Mv 200,176,148
Cv 163,166,167
v_accvgpr_write_b32 a224, v160
Mv 204,52,20
v_accvgpr_write_b32 a225, v161
v_accvgpr_write_b32 a226, v162
Mv 204,180,148
buffer_store_dwordx4 v[64:67], v3, s[20:23], s45 offen offset:0 sc0 sc1
Mv 208,48,24
v_accvgpr_write_b32 a227, v163
Cv 168,168,169
Mv 208,176,152
Cv 169,170,171
Cv 170,172,173
Mv 212,52,24
Cv 171,174,175
v_accvgpr_write_b32 a228, v168
Mv 212,180,152
buffer_load_dwordx4 v3, s[20:23], s55 offen offset:3072 sc1 lds
Mv 216,48,28
v_accvgpr_write_b32 a229, v169
v_accvgpr_write_b32 a230, v170
Mv 216,176,156
v_accvgpr_write_b32 a231, v171
Cv 176,176,177
Mv 220,52,28
Cv 177,178,179
Cv 178,180,181
Mv 220,180,156
buffer_store_dwordx4 v[72:75], v3, s[20:23], s45 offen offset:1024 sc0 sc1
Mv 224,56,0
Cv 179,182,183
v_accvgpr_write_b32 a232, v176
)" R"(
Mv 228,60,0
v_accvgpr_write_b32 a233, v177
v_accvgpr_write_b32 a234, v178
Mv 236,60,4
s_add_u32 m0, s36, 69632
v_accvgpr_write_b32 a235, v179
Mv 232,56,4
buffer_load_dwordx4 v3, s[20:23], s56 offen offset:0 sc1 lds
Mv 240,56,8
Cv 184,184,185
Cv 185,186,187
Mv 244,60,8
Cv 186,188,189
Cv 187,190,191
Mv 252,60,12
v_accvgpr_write_b32 a236, v184
v_accvgpr_write_b32 a237, v185
Mv 248,56,12
s_waitcnt vmcnt(16)
s_barrier
Ma 192,56,16
v_cmp_eq_u32_e32 vcc, s3, v4
s_andn2_b64 s[28:29], s[60:61], vcc
s_cbranch_scc1 .Lzsp17
.Lzok17:
Ma 196,60,16
buffer_store_dwordx4 v[80:83], v3, s[20:23], s45 offen offset:2048 sc0 sc1
Ma 204,60,20
v_accvgpr_write_b32 a238, v186
v_accvgpr_write_b32 a239, v187
Ma 200,56,20
buffer_load_dwordx4 v3, s[20:23], s56 offen offset:1024 sc1 lds
Ma 208,56,24
Cv 192,192,193
Cv 193,194,195
Ma 212,60,24
Cv 194,196,197
Cv 195,198,199
Ma 220,60,28
v_accvgpr_write_b32 a240, v192
v_accvgpr_write_b32 a241, v193
Ma 216,56,28
buffer_store_dwordx4 v[88:91], v3, s[20:23], s45 offen offset:3072 sc0 sc1
Mv 224,184,128
Rb 0,1,0
v_accvgpr_write_b32 a242, v194
Mv 228,188,128
Rb 4,1,1024
v_accvgpr_write_b32 a243, v195
Mv 232,184,132
Rb 8,1,2048
Cv 200,200,201
Mv 236,188,132
buffer_load_dwordx4 v3, s[20:23], s56 offen offset:2048 sc1 lds
Rb 12,1,3072
Mv 240,184,136
Rb 16,1,8192
Cv 201,202,203
Mv 244,188,136
Rb 20,1,9216
Cv 202,204,205
Mv 248,184,140
Rb 24,1,10240
Cv 203,206,207
Mv 252,188,140
)" R"(
Rb 28,1,11264
v_accvgpr_write_b32 a244, v200
Ma 192,184,144
Rb 32,1,4096
v_accvgpr_write_b32 a245, v201
Ma 196,188,144
Rb 36,1,5120
v_accvgpr_write_b32 a246, v202
Ma 200,184,148
Rb 40,1,6144
v_accvgpr_write_b32 a247, v203
Ma 204,188,148
buffer_load_dwordx4 v3, s[20:23], s56 offen offset:3072 sc1 lds
Rb 44,1,7168
Ma 208,184,152
Ma 212,188,152
Ma 216,184,156
Ma 220,188,156
s_add_u32 s56, s56, 0x10000
.LzFB:
s_waitcnt lgkmcnt(8)
Mz 64,0,64
Rb 48,1,12288
s_add_u32 s55, s55, 0x10000
Mz 68,0,68
s_cmp_eq_u32 s57, 2
s_cselect_b32 s55, s59, s55
Mv 68,4,76
Rb 52,1,13312
s_sub_u32 s57, s57, 1
v_mov_b32 v16, 0
Mv 64,4,72
buffer_store_dwordx4 v[132:135], v2, s[20:23], s46 offen offset:0 sc0 sc1
Mv 64,8,80
Rb 56,1,14336
Cv 208,208,209
Cv 209,210,211
Mv 68,8,84
Cv 210,212,213
Cv 211,214,215
Mv 68,12,92
Rb 60,1,15360
v_accvgpr_write_b32 a248, v208
s_waitcnt lgkmcnt(8)
Mv 64,12,88
v_accvgpr_write_b32 a249, v209
v_accvgpr_write_b32 a250, v210
Mv 64,16,96
Rb 0,1,16384
v_accvgpr_write_b32 a251, v211
Mv 68,16,100
Cv 216,216,217
Cv 217,218,219
Cv 218,220,221
Mv 68,20,108
Rb 4,1,17408
Cv 219,222,223
Mv 64,20,104
v_accvgpr_write_b32 a252, v216
v_accvgpr_write_b32 a253, v217
v_accvgpr_write_b32 a254, v218
Mv 64,24,112
Rb 8,1,18432
v_accvgpr_write_b32 a255, v219
)" R"(
Mv 68,24,116
buffer_store_dwordx4 v[140:143], v2, s[20:23], s46 offen offset:1024 sc0 sc1
Cv 224,224,225
Mv 68,28,124
Rb 12,1,19456
s_waitcnt lgkmcnt(8)
Mv 64,28,120
Cv 225,226,227
Cv 226,228,229
Cv 227,230,231
Mz 72,32,64
Rb 16,1,24576
v_accvgpr_write_b32 a128, v224
Mz 76,32,68
v_accvgpr_write_b32 a129, v225
v_accvgpr_write_b32 a130, v226
Mv 76,36,76
Rb 20,1,25600
v_accvgpr_write_b32 a131, v227
Cv 232,232,233
Mv 72,36,72
Cv 233,234,235
Cv 234,236,237
Mv 72,40,80
Rb 24,1,26624
Cv 235,238,239
v_accvgpr_write_b32 a132, v232
Mv 76,40,84
v_accvgpr_write_b32 a133, v233
v_accvgpr_write_b32 a134, v234
Mv 76,44,92
Rb 28,1,27648
v_accvgpr_write_b32 a135, v235
s_waitcnt lgkmcnt(8)
Mv 72,44,88
buffer_store_dwordx4 v[148:151], v2, s[20:23], s46 offen offset:2048 sc0 sc1
Mv 72,48,96
Rb 32,1,20480
Cv 240,240,241
Mv 76,48,100
Cv 241,242,243
Cv 242,244,245
Cv 243,246,247
Mv 76,52,108
Rb 36,1,21504
v_accvgpr_write_b32 a136, v240
Mv 72,52,104
v_accvgpr_write_b32 a137, v241
v_accvgpr_write_b32 a138, v242
v_accvgpr_write_b32 a139, v243
Mv 72,56,112
Rb 40,1,22528
Cv 248,248,249
Mv 76,56,116
Cv 249,250,251
Cv 250,252,253
Cv 251,254,255
Mv 76,60,124
Rb 44,1,23552
s_waitcnt lgkmcnt(8)
Mv 72,60,120
v_accvgpr_write_b32 a140, v248
v_accvgpr_write_b32 a141, v249
v_accvgpr_write_b32 a142, v250
)" R"(
Mz 80,0,64
Rb 48,1,28672
v_accvgpr_write_b32 a143, v251
Mz 84,0,68
buffer_store_dwordx4 v[156:159], v2, s[20:23], s46 offen offset:3072 sc0 sc1
Mv 84,4,76
Rb 52,1,29696
v_mov_b32 v17, 0
v_mov_b32 v18, 0
Mv 80,4,72
v_mov_b32 v19, 0
v_mov_b32 v20, 0
Mv 80,8,80
Rb 56,1,30720
v_exp_f32_e32 v32, v64
Mv 84,8,84
v_exp_f32_e32 v24, v65
Mv 84,12,92
Rb 60,1,31744
v_mov_b32 v21, 0
s_waitcnt lgkmcnt(8)
Mv 80,12,88
v_exp_f32_e32 v33, v66
Mv 80,16,96
v_exp_f32_e32 v25, v67
Mv 84,16,100
v_exp_f32_e32 v34, v72
v_mov_b32 v22, 0
Mv 84,20,108
v_exp_f32_e32 v26, v73
Mv 80,20,104
buffer_store_dwordx4 v[128:131], v3, s[20:23], s46 offen offset:0 sc0 sc1
v_mov_b32 v23, 0
Mv 80,24,112
v_exp_f32_e32 v35, v74
Mv 84,24,116
v_exp_f32_e32 v27, v75
v_add_f32_e32 v14, v32, v24
Mv 84,28,124
v_exp_f32_e32 v36, v68
Mv 80,28,120
s_waitcnt vmcnt(7) lgkmcnt(0)
s_barrier
buffer_store_dword v6, v7, s[20:23], s49 offen offset:0 sc0 sc1
Mz 88,32,64
Rb 0,1,32768
Cv 32,32,24
Mz 92,32,68
Rb 4,1,33792
v_add_f32_e32 v14, v14, v33
Mv 92,36,76
Rb 8,1,34816
s_add_u32 m0, s36, 0
v_add_f32_e32 v14, v14, v25
Mv 88,36,72
Rb 12,1,35840
buffer_load_dwordx4 v2, s[20:23], s55 offen offset:0 sc1 lds
Mv 88,40,80
Rb 16,1,40960
v_exp_f32_e32 v28, v69
Mv 92,40,84
Rb 20,1,41984
Cv 33,33,25
Mv 92,44,92
Rb 24,1,43008
)" R"(
v_exp_f32_e32 v37, v70
Mv 88,44,88
Rb 28,1,44032
v_add_f32_e32 v14, v14, v34
Mv 88,48,96
Rb 32,1,36864
v_add_f32_e32 v14, v14, v26
Mv 92,48,100
Rb 36,1,37888
v_exp_f32_e32 v29, v71
Mv 92,52,108
Rb 40,1,38912
Cv 34,34,26
Mv 88,52,104
Rb 44,1,39936
buffer_load_dwordx4 v2, s[20:23], s55 offen offset:1024 sc1 lds
Mv 88,56,112
v_exp_f32_e32 v38, v76
Mv 92,56,116
v_exp_f32_e32 v24, v77
v_add_f32_e32 v14, v14, v35
Mv 92,60,124
v_add_f32_e32 v14, v14, v27
s_waitcnt lgkmcnt(8)
Mv 88,60,120
v_exp_f32_e32 v39, v78
Cv 35,35,27
Mz 96,0,64
Rb 48,1,45056
buffer_load_dword v4, v5, s[20:23], s50 offen offset:0 sc0 sc1
Mz 100,0,68
s_lshl_b64 s[60:61], s[60:61], 8
s_or_b32 s60, s60, 0xff
Mv 100,4,76
Rb 52,1,46080
v_exp_f32_e32 v25, v79
Mv 96,4,72
buffer_load_dwordx4 v2, s[20:23], s55 offen offset:2048 sc1 lds
Mv 96,8,80
Rb 56,1,47104
v_exp_f32_e32 v40, v80
Mv 100,8,84
v_exp_f32_e32 v26, v81
Mv 100,12,92
Rb 60,1,48128
v_add_f32_e32 v15, v36, v28
s_waitcnt lgkmcnt(8)
Mv 96,12,88
buffer_store_dwordx4 v[136:139], v3, s[20:23], s46 offen offset:1024 sc0 sc1
Mv 96,16,96
Rb 0,1,49152
Cv 36,36,28
Mv 100,16,100
v_exp_f32_e32 v41, v82
v_add_f32_e32 v15, v15, v37
Mv 100,20,108
Rb 4,1,50176
v_add_f32_e32 v15, v15, v29
Mv 96,20,104
buffer_load_dwordx4 v2, s[20:23], s55 offen offset:3072 sc1 lds
Cv 37,37,29
Mv 96,24,112
Rb 8,1,51200
v_add_f32_e32 v15, v15, v38
)" R"(
Mv 100,24,116
v_exp_f32_e32 v27, v83
v_add_f32_e32 v15, v15, v24
Mv 100,28,124
Rb 12,1,52224
s_waitcnt lgkmcnt(8)
Mv 96,28,120
v_exp_f32_e32 v42, v88
Cv 38,38,24
Mz 104,32,64
Rb 16,1,57344
v_add_f32_e32 v15, v15, v39
Mz 108,32,68
v_exp_f32_e32 v28, v89
Mv 108,36,76
Rb 20,1,58368
s_add_u32 m0, s36, 4096
v_add_f32_e32 v15, v15, v25
Mv 104,36,72
buffer_load_dwordx4 v3, s[20:23], s55 offen offset:0 sc1 lds
Mv 104,40,80
Rb 24,1,59392
v_exp_f32_e32 v43, v90
Mv 108,40,84
v_exp_f32_e32 v29, v91
Mv 108,44,92
Rb 28,1,60416
Cv 39,39,25
s_waitcnt lgkmcnt(8)
Mv 104,44,88
buffer_store_dwordx4 v[144:147], v3, s[20:23], s46 offen offset:2048 sc0 sc1
Mv 104,48,96
Rb 32,1,53248
v_add_f32_e32 v14, v14, v40
Mv 108,48,100
v_exp_f32_e32 v44, v84
v_add_f32_e32 v14, v14, v26
Mv 108,52,108
Rb 36,1,54272
Cv 40,40,26
Mv 104,52,104
buffer_load_dwordx4 v3, s[20:23], s55 offen offset:1024 sc1 lds
v_add_f32_e32 v14, v14, v41
Mv 104,56,112
Rb 40,1,55296
v_add_f32_e32 v14, v14, v27
Mv 108,56,116
v_exp_f32_e32 v24, v85
Cv 41,41,27
Mv 108,60,124
Rb 44,1,56320
s_waitcnt lgkmcnt(8)
Mv 104,60,120
v_exp_f32_e32 v45, v86
v_add_f32_e32 v14, v14, v42
Mz 112,0,64
Rb 48,1,61440
v_add_f32_e32 v14, v14, v28
Mz 116,0,68
v_exp_f32_e32 v25, v87
Mv 116,4,76
Rb 52,1,62464
v_exp_f32_e32 v46, v92
Mv 112,4,72
)" R"(
buffer_load_dwordx4 v3, s[20:23], s55 offen offset:2048 sc1 lds
Mv 112,8,80
Rb 56,1,63488
v_exp_f32_e32 v26, v93
Mv 116,8,84
v_exp_f32_e32 v47, v94
Mv 116,12,92
Rb 60,1,64512
Cv 42,42,28
s_waitcnt lgkmcnt(8)
Mv 112,12,88
buffer_store_dwordx4 v[152:155], v3, s[20:23], s46 offen offset:3072 sc0 sc1
Mv 112,16,96
v_exp_f32_e32 v27, v95
Mv 116,16,100
v_add_f32_e32 v14, v14, v43
v_add_f32_e32 v14, v14, v29
Cv 43,43,29
Mv 116,20,108
v_add_f32_e32 v15, v15, v44
v_add_f32_e32 v15, v15, v24
Mv 112,20,104
buffer_load_dwordx4 v3, s[20:23], s55 offen offset:3072 sc1 lds
Cv 44,44,24
Mv 112,24,112
v_add_f32_e32 v15, v15, v45
v_add_f32_e32 v15, v15, v25
Mv 116,24,116
Cv 45,45,25
v_add_f32_e32 v15, v15, v46
v_add_f32_e32 v15, v15, v26
Mv 116,28,124
Cv 46,46,26
v_add_f32_e32 v15, v15, v47
Mv 112,28,120
v_add_f32_e32 v15, v15, v27
s_waitcnt lgkmcnt(0)
s_barrier
Mz 120,32,64
Rb 0,10,0
Cv 47,47,27
Mz 124,32,68
Rb 4,10,1024
v_max_f32_e32 v11, v14, v15
Mv 124,36,76
Rb 8,10,2048
v_mov_b32 v24, v14
v_mov_b32 v25, v15
Mv 120,36,72
Rb 12,10,3072
v_permlane32_swap_b32_e32 v24, v25
Mv 120,40,80
Rb 16,10,4096
v_add_f32_e32 v24, v24, v25
v_mov_b32 v25, v24
Mv 124,40,84
Rb 20,10,5120
v_permlane16_swap_b32_e32 v24, v25
Mv 124,44,92
Rb 24,10,6144
v_add_f32_e32 v24, v24, v25
s_add_u32 s55, s55, 0x10000
Mv 120,44,88
Rb 28,10,7168
Mv 120,48,96
)" R"(
Rb 32,10,8192
Mv 124,48,100
Rb 36,10,9216
v_rcp_f32_e32 v24, v24
Mv 124,52,108
Rb 40,10,10240
v_max_f32_e32 v11, v11, v24
Mv 120,52,104
Rb 44,10,11264
s_cmp_eq_u32 s57, 2
s_cselect_b32 s55, s59, s55
Mv 120,56,112
v_cmp_nge_f32_e32 vcc, s62, v11
Mv 124,56,116
Mv 124,60,124
s_waitcnt lgkmcnt(8)
s_cbranch_vccnz .Lzfx6
.Lzfr6:
Mv 120,60,120
v_mov_b32 v12, v14
v_mov_b32 v13, v15
v_mfma_f32_16x16x32_bf16 v[128:131], a[0:3], v[32:35], 0
Rb 48,10,12288
v_mfma_f32_16x16x32_bf16 v[136:139], a[0:3], v[36:39], 0
v_mfma_f32_16x16x32_bf16 v[140:143], a[4:7], v[36:39], 0
Rb 52,10,13312
s_add_u32 m0, s36, 32768
v_mfma_f32_16x16x32_bf16 v[132:135], a[4:7], v[32:35], 0
buffer_load_dwordx4 v2, s[20:23], s55 offen offset:0 sc1 lds
v_mfma_f32_16x16x32_bf16 v[144:147], a[8:11], v[32:35], 0
Rb 56,10,14336
v_mfma_f32_16x16x32_bf16 v[152:155], a[8:11], v[36:39], 0
v_exp_f32_e32 v48, v96
v_mfma_f32_16x16x32_bf16 v[156:159], a[12:15], v[36:39], 0
Rb 60,10,15360
s_waitcnt lgkmcnt(8)
v_mfma_f32_16x16x32_bf16 v[148:151], a[12:15], v[32:35], 0
v_exp_f32_e32 v28, v97
v_mfma_f32_16x16x32_bf16 v[160:163], a[16:19], v[32:35], 0
Rb 0,10,16384
v_add_f32_e32 v14, v48, v28
v_mfma_f32_16x16x32_bf16 v[168:171], a[16:19], v[36:39], 0
v_exp_f32_e32 v49, v98
v_mfma_f32_16x16x32_bf16 v[172:175], a[20:23], v[36:39], 0
Rb 4,10,17408
Cv 48,48,28
v_mfma_f32_16x16x32_bf16 v[164:167], a[20:23], v[32:35], 0
buffer_load_dwordx4 v2, s[20:23], s55 offen offset:1024 sc1 lds
v_mfma_f32_16x16x32_bf16 v[176:179], a[24:27], v[32:35], 0
Rb 8,10,18432
v_exp_f32_e32 v29, v99
v_mfma_f32_16x16x32_bf16 v[184:187], a[24:27], v[36:39], 0
v_exp_f32_e32 v50, v104
v_mfma_f32_16x16x32_bf16 v[188:191], a[28:31], v[36:39], 0
Rb 12,10,19456
s_waitcnt lgkmcnt(8)
v_mfma_f32_16x16x32_bf16 v[180:183], a[28:31], v[32:35], 0
v_exp_f32_e32 v24, v105
v_mfma_f32_16x16x32_bf16 v[192:195], a[32:35], v[32:35], 0
Rb 16,10,20480
v_add_f32_e32 v14, v14, v49
v_mfma_f32_16x16x32_bf16 v[200:203], a[32:35], v[36:39], 0
v_exp_f32_e32 v51, v106
v_add_f32_e32 v14, v14, v29
)" R"(
v_mfma_f32_16x16x32_bf16 v[204:207], a[36:39], v[36:39], 0
Rb 20,10,21504
Cv 49,49,29
v_mfma_f32_16x16x32_bf16 v[196:199], a[36:39], v[32:35], 0
buffer_load_dwordx4 v2, s[20:23], s55 offen offset:2048 sc1 lds
v_mfma_f32_16x16x32_bf16 v[208:211], a[40:43], v[32:35], 0
Rb 24,10,22528
v_add_f32_e32 v14, v14, v50
v_mfma_f32_16x16x32_bf16 v[216:219], a[40:43], v[36:39], 0
v_exp_f32_e32 v25, v107
v_mfma_f32_16x16x32_bf16 v[220:223], a[44:47], v[36:39], 0
Rb 28,10,23552
s_waitcnt lgkmcnt(8)
v_mfma_f32_16x16x32_bf16 v[212:215], a[44:47], v[32:35], 0
v_exp_f32_e32 v52, v100
v_add_f32_e32 v14, v14, v24
v_mfma_f32_16x16x32_bf16 v[224:227], a[48:51], v[32:35], 0
Rb 32,10,24576
Cv 50,50,24
v_mfma_f32_16x16x32_bf16 v[232:235], a[48:51], v[36:39], 0
v_exp_f32_e32 v26, v101
v_mfma_f32_16x16x32_bf16 v[236:239], a[52:55], v[36:39], 0
Rb 36,10,25600
v_add_f32_e32 v14, v14, v51
v_mfma_f32_16x16x32_bf16 v[228:231], a[52:55], v[32:35], 0
buffer_load_dwordx4 v2, s[20:23], s55 offen offset:3072 sc1 lds
v_mfma_f32_16x16x32_bf16 v[240:243], a[56:59], v[32:35], 0
Rb 40,10,26624
v_exp_f32_e32 v53, v102
v_mfma_f32_16x16x32_bf16 v[248:251], a[56:59], v[36:39], 0
v_exp_f32_e32 v27, v103
v_mfma_f32_16x16x32_bf16 v[252:255], a[60:63], v[36:39], 0
Rb 44,10,27648
s_waitcnt lgkmcnt(8)
v_mfma_f32_16x16x32_bf16 v[244:247], a[60:63], v[32:35], 0
v_exp_f32_e32 v54, v108
Mp 128,0,40
Rb 48,10,28672
v_add_f32_e32 v14, v14, v25
Mp 136,0,44
v_exp_f32_e32 v28, v109
Cv 51,51,25
Mp 140,4,44
Rb 52,10,29696
s_add_u32 m0, s36, 36864
Mp 132,4,40
buffer_load_dwordx4 v3, s[20:23], s55 offen offset:0 sc1 lds
Mp 144,8,40
Rb 56,10,30720
v_add_f32_e32 v15, v52, v26
Mp 152,8,44
v_exp_f32_e32 v55, v110
Mp 156,12,44
Rb 60,10,31744
s_waitcnt lgkmcnt(8)
Mp 148,12,40
v_exp_f32_e32 v29, v111
Cv 52,52,26
Mp 160,16,40
v_add_f32_e32 v15, v15, v53
v_add_f32_e32 v15, v15, v27
Mp 168,16,44
Cv 53,53,27
v_add_f32_e32 v15, v15, v54
)" R"(
Mp 172,20,44
v_add_f32_e32 v15, v15, v28
Cv 54,54,28
Mp 164,20,40
buffer_load_dwordx4 v3, s[20:23], s55 offen offset:1024 sc1 lds
Mp 176,24,40
v_add_f32_e32 v15, v15, v55
v_add_f32_e32 v15, v15, v29
Cv 55,55,29
Mp 184,24,44
s_sub_u32 s57, s57, 1
v_accvgpr_read_b32 v9, a192
Mp 188,28,44
v_accvgpr_read_b32 v11, a193
Cv 9,9,11
Mp 180,28,40
s_waitcnt vmcnt(6) lgkmcnt(0)
s_barrier
buffer_store_dword v6, v7, s[20:23], s49 offen offset:4 sc0 sc1
Mp 192,32,40
Rb 0,1,0
v_accvgpr_write_b32 a144, v9
Mp 200,32,44
Rb 4,1,1024
v_accvgpr_read_b32 v9, a194
v_accvgpr_read_b32 v11, a195
Mp 204,36,44
Rb 8,1,2048
Cv 9,9,11
Mp 196,36,40
Rb 12,1,3072
buffer_load_dwordx4 v3, s[20:23], s55 offen offset:2048 sc1 lds
Mp 208,40,40
Rb 16,1,8192
v_accvgpr_write_b32 a145, v9
Mp 216,40,44
Rb 20,1,9216
v_accvgpr_read_b32 v9, a196
Mp 220,44,44
Rb 24,1,10240
v_accvgpr_read_b32 v11, a197
Mp 212,44,40
Rb 28,1,11264
Cv 9,9,11
v_accvgpr_write_b32 a146, v9
Mp 224,48,40
Rb 32,1,4096
v_accvgpr_read_b32 v9, a198
Mp 232,48,44
Rb 36,1,5120
v_accvgpr_read_b32 v11, a199
Mp 236,52,44
Rb 40,1,6144
Cv 9,9,11
Mp 228,52,40
Rb 44,1,7168
buffer_load_dwordx4 v3, s[20:23], s55 offen offset:3072 sc1 lds
Mp 240,56,40
v_cmp_eq_u32_e32 vcc, s3, v4
s_andn2_b64 s[28:29], s[60:61], vcc
s_cbranch_scc1 .Lzsp18
.Lzok18:
Mp 248,56,44
v_accvgpr_write_b32 a147, v9
v_accvgpr_read_b32 v9, a200
)" R"(
Mp 252,60,44
v_accvgpr_read_b32 v11, a201
s_waitcnt lgkmcnt(8)
Mp 244,60,40
Cv 9,9,11
v_accvgpr_write_b32 a148, v9
v_mfma_f32_16x16x32_bf16 v[64:67], a[0:3], a[64:67], v[16:19]
Rb 48,1,12288
v_add_u32 v9, 4, v0
v_mfma_f32_16x16x32_bf16 v[68:71], a[0:3], a[68:71], v[20:23]
v_xor_b32 v9, v9, v0
v_and_b32 v9, 32, v9
Mv 68,4,76
Rb 52,1,13312
s_add_u32 m0, s36, 98304
Mv 64,4,72
buffer_load_dwordx4 v2, s[20:23], s56 offen offset:0 sc1 lds
Mv 64,8,80
Rb 56,1,14336
v_lshlrev_b32 v9, 22, v9
Mv 68,8,84
v_and_or_b32 v9, v0, 32, v9
v_mul_lo_u32 v9, v9, 12
Mv 68,12,92
Rb 60,1,15360
s_waitcnt lgkmcnt(8)
Mv 64,12,88
buffer_load_dwordx4 v2, s[20:23], s56 offen offset:1024 sc1 lds
Mv 64,16,96
Rb 0,1,16384
v_add_u32 v9, v9, v1
Mv 68,16,100
v_exp_f32_e32 v56, v112
Mv 68,20,108
Rb 4,1,17408
v_exp_f32_e32 v24, v113
Mv 64,20,104
buffer_load_dwordx4 v2, s[20:23], s56 offen offset:2048 sc1 lds
Mv 64,24,112
Rb 8,1,18432
v_exp_f32_e32 v57, v114
Mv 68,24,116
v_exp_f32_e32 v25, v115
Mv 68,28,124
Rb 12,1,19456
v_add_f32_e32 v14, v14, v56
s_waitcnt lgkmcnt(8)
Mv 64,28,120
buffer_load_dwordx4 v2, s[20:23], s56 offen offset:3072 sc1 lds
v_add_f32_e32 v14, v14, v24
v_mfma_f32_16x16x32_bf16 v[72:75], a[32:35], a[64:67], v[16:19]
Rb 16,1,24576
Cv 56,56,24
v_mfma_f32_16x16x32_bf16 v[76:79], a[32:35], a[68:71], v[20:23]
v_exp_f32_e32 v58, v120
v_add_f32_e32 v14, v14, v57
Mv 76,36,76
Rb 20,1,25600
s_add_u32 m0, s36, 102400
Mv 72,36,72
buffer_load_dwordx4 v3, s[20:23], s56 offen offset:0 sc1 lds
v_add_f32_e32 v14, v14, v25
Mv 72,40,80
Rb 24,1,26624
)" R"(
Cv 57,57,25
Mv 76,40,84
v_exp_f32_e32 v26, v121
v_add_f32_e32 v14, v14, v58
Mv 76,44,92
Rb 28,1,27648
s_waitcnt lgkmcnt(8)
Mv 72,44,88
buffer_load_dwordx4 v3, s[20:23], s56 offen offset:1024 sc1 lds
v_add_f32_e32 v14, v14, v26
Mv 72,48,96
Rb 32,1,20480
v_exp_f32_e32 v59, v122
Mv 76,48,100
v_exp_f32_e32 v27, v123
Mv 76,52,108
Rb 36,1,21504
v_exp_f32_e32 v60, v116
Mv 72,52,104
buffer_load_dwordx4 v3, s[20:23], s56 offen offset:2048 sc1 lds
Mv 72,56,112
Rb 40,1,22528
v_exp_f32_e32 v28, v117
Mv 76,56,116
v_exp_f32_e32 v61, v118
Mv 76,60,124
Rb 44,1,23552
Cv 58,58,26
s_waitcnt lgkmcnt(8)
Mv 72,60,120
buffer_load_dwordx4 v3, s[20:23], s56 offen offset:3072 sc1 lds
v_add_f32_e32 v14, v14, v59
v_mfma_f32_16x16x32_bf16 v[80:83], a[0:3], a[64:67], v[16:19]
Rb 48,1,28672
s_xor_b32 m0, s82, 0
v_mfma_f32_16x16x32_bf16 v[84:87], a[0:3], a[68:71], v[20:23]
s_add_u32 s56, s56, 0x10000
s_add_u32 s92, s83, 448
v_add_f32_e32 v14, v14, v27
Mv 84,4,76
Rb 52,1,29696
Cv 59,59,27
Mv 80,4,72
buffer_load_dwordx4 v9, s[24:27], s92 offen offset:0 nt lds
v_add_f32_e32 v15, v15, v60
Mv 80,8,80
Rb 56,1,30720
s_add_u32 s92, s83, 1216
Mv 84,8,84
v_exp_f32_e32 v29, v119
v_add_f32_e32 v15, v15, v28
Mv 84,12,92
Rb 60,1,31744
s_waitcnt lgkmcnt(8)
Mv 80,12,88
buffer_load_dwordx4 v9, s[24:27], s92 offen offset:1024 nt lds
s_add_u32 s92, s83, 1984
Mv 80,16,96
v_exp_f32_e32 v62, v124
Cv 60,60,28
Mv 84,16,100
v_exp_f32_e32 v24, v125
Mv 84,20,108
v_exp_f32_e32 v63, v126
)" R"(
v_add_f32_e32 v15, v15, v61
Mv 80,20,104
buffer_load_dwordx4 v9, s[24:27], s92 offen offset:2048 nt lds
Mv 80,24,112
s_add_u32 s92, s83, 2752
v_exp_f32_e32 v25, v127
Mv 84,24,116
v_add_f32_e32 v15, v15, v29
Cv 61,61,29
Mv 84,28,124
buffer_load_dwordx4 v9, s[24:27], s92 offen offset:3072 nt lds
v_add_f32_e32 v15, v15, v62
Mv 80,28,120
s_xor_b32 m0, s82, 16384
s_waitcnt lgkmcnt(0)
s_barrier
v_mfma_f32_16x16x32_bf16 v[88:91], a[32:35], a[64:67], v[16:19]
Rb 0,8,0
s_add_u32 s92, s83, 0
v_mfma_f32_16x16x32_bf16 v[92:95], a[32:35], a[68:71], v[20:23]
Rb 4,8,1024
v_add_f32_e32 v15, v15, v24
Cv 62,62,24
Mv 92,36,76
Rb 8,8,2048
v_add_f32_e32 v15, v15, v63
Mv 88,36,72
Rb 12,8,3072
v_add_f32_e32 v15, v15, v25
Cv 63,63,25
Mv 88,40,80
Rb 16,8,4096
buffer_load_dwordx4 v9, s[24:27], s92 offen offset:0 nt lds
Mv 92,40,84
Rb 20,8,5120
s_add_u32 s92, s83, 768
v_max_f32_e32 v11, v14, v15
Mv 92,44,92
Rb 24,8,6144
v_cmp_nge_f32_e32 vcc, s62, v11
Mv 88,44,88
Rb 28,8,7168
s_add_u32 s55, s55, 0x10000
Mv 88,48,96
Rb 32,8,8192
s_cmp_eq_u32 s57, 2
s_cselect_b32 s55, s59, s55
Mv 92,48,100
Rb 36,8,9216
buffer_load_dwordx4 v9, s[24:27], s92 offen offset:1024 nt lds
Mv 92,52,108
Rb 40,8,10240
s_add_u32 s92, s83, 1536
Mv 88,52,104
Rb 44,8,11264
Mv 88,56,112
buffer_load_dwordx4 v9, s[24:27], s92 offen offset:2048 nt lds
s_add_u32 s92, s83, 2304
Mv 92,56,116
buffer_load_dwordx4 v9, s[24:27], s92 offen offset:3072 nt lds
Mv 92,60,124
s_waitcnt lgkmcnt(8)
s_cbranch_vccnz .Lzfx7
.Lzfr7:
Mv 88,60,120
)" R"(
v_add_f32_e32 v12, v12, v14
v_add_f32_e32 v13, v13, v15
Mp 128,0,48
Rb 48,8,12288
Mp 136,0,52
s_cmp_le_i32 s57, s58
s_cbranch_scc1 .Lzmk6
.Lzmr6:
Mp 140,4,52
Rb 52,8,13312
s_add_u32 m0, s36, 0
Mp 132,4,48
buffer_load_dwordx4 v2, s[20:23], s55 offen offset:0 sc1 lds
Mp 144,8,48
Rb 56,8,14336
v_exp_f32_e32 v32, v64
Mp 152,8,52
buffer_store_dwordx4 a[224:227], v2, s[20:23], s47 offen offset:0 sc0 sc1
Mp 156,12,52
Rb 60,8,15360
s_waitcnt lgkmcnt(8)
Mp 148,12,48
v_exp_f32_e32 v26, v65
Mp 160,16,48
Rb 0,8,16384
v_add_f32_e32 v14, v32, v26
Mp 168,16,52
v_exp_f32_e32 v33, v66
Cv 32,32,26
Mp 172,20,52
Rb 4,8,17408
v_add_f32_e32 v14, v14, v33
Mp 164,20,48
buffer_load_dwordx4 v2, s[20:23], s55 offen offset:1024 sc1 lds
Mp 176,24,48
Rb 8,8,18432
v_exp_f32_e32 v27, v67
Mp 184,24,52
v_exp_f32_e32 v34, v72
Mp 188,28,52
Rb 12,8,19456
s_waitcnt lgkmcnt(8)
Mp 180,28,48
v_exp_f32_e32 v28, v73
v_add_f32_e32 v14, v14, v27
Mp 192,32,48
Rb 16,8,20480
Cv 33,33,27
Mp 200,32,52
v_exp_f32_e32 v35, v74
v_add_f32_e32 v14, v14, v34
Mp 204,36,52
Rb 20,8,21504
v_add_f32_e32 v14, v14, v28
Mp 196,36,48
buffer_load_dwordx4 v2, s[20:23], s55 offen offset:2048 sc1 lds
Mp 208,40,48
Rb 24,8,22528
v_exp_f32_e32 v29, v75
Mp 216,40,52
buffer_store_dwordx4 a[228:231], v2, s[20:23], s47 offen offset:1024 sc0 sc1
Mp 220,44,52
Rb 28,8,23552
s_waitcnt lgkmcnt(8)
)" R"(
Mp 212,44,48
v_exp_f32_e32 v36, v68
Cv 34,34,28
Mp 224,48,48
Rb 32,8,24576
v_add_f32_e32 v14, v14, v35
Mp 232,48,52
v_exp_f32_e32 v24, v69
v_add_f32_e32 v14, v14, v29
Mp 236,52,52
Rb 36,8,25600
Cv 35,35,29
Mp 228,52,48
buffer_load_dwordx4 v2, s[20:23], s55 offen offset:3072 sc1 lds
Mp 240,56,48
Rb 40,8,26624
v_exp_f32_e32 v37, v70
Mp 248,56,52
v_exp_f32_e32 v25, v71
Mp 252,60,52
Rb 44,8,27648
s_waitcnt lgkmcnt(8)
Mp 244,60,48
v_exp_f32_e32 v38, v76
v_add_f32_e32 v15, v36, v24
Mp 128,0,56
Rb 48,8,28672
Cv 36,36,24
Mp 136,0,60
v_exp_f32_e32 v26, v77
v_add_f32_e32 v15, v15, v37
Mp 140,4,60
Rb 52,8,29696
s_add_u32 m0, s36, 4096
Mp 132,4,56
buffer_load_dwordx4 v3, s[20:23], s55 offen offset:0 sc1 lds
Mp 144,8,56
Rb 56,8,30720
v_exp_f32_e32 v39, v78
Mp 152,8,60
buffer_store_dwordx4 a[232:235], v2, s[20:23], s47 offen offset:2048 sc0 sc1
Mp 156,12,60
Rb 60,8,31744
s_waitcnt lgkmcnt(8)
Mp 148,12,56
v_exp_f32_e32 v27, v79
v_add_f32_e32 v15, v15, v25
Mp 160,16,56
Cv 37,37,25
v_add_f32_e32 v15, v15, v38
Mp 168,16,60
v_add_f32_e32 v15, v15, v26
Cv 38,38,26
v_add_f32_e32 v15, v15, v39
Mp 172,20,60
v_add_f32_e32 v15, v15, v27
Cv 39,39,27
Mp 164,20,56
buffer_load_dwordx4 v3, s[20:23], s55 offen offset:1024 sc1 lds
Mp 176,24,56
s_sub_u32 s57, s57, 1
v_accvgpr_read_b32 v9, a202
v_accvgpr_read_b32 v11, a203
Mp 184,24,60
)" R"(
Cv 9,9,11
v_accvgpr_write_b32 a149, v9
Mp 188,28,60
v_accvgpr_read_b32 v9, a204
v_accvgpr_read_b32 v11, a205
Mp 180,28,56
Cv 9,9,11
s_waitcnt vmcnt(25) lgkmcnt(0)
s_barrier
Mp 192,32,56
Rb 0,1,32768
v_accvgpr_write_b32 a150, v9
Mp 200,32,60
Rb 4,1,33792
v_accvgpr_read_b32 v9, a206
v_accvgpr_read_b32 v11, a207
Mp 204,36,60
Rb 8,1,34816
Cv 9,9,11
Mp 196,36,56
Rb 12,1,35840
buffer_load_dwordx4 v3, s[20:23], s55 offen offset:2048 sc1 lds
Mp 208,40,56
Rb 16,1,40960
v_accvgpr_write_b32 a151, v9
v_accvgpr_read_b32 v9, a208
Mp 216,40,60
Rb 20,1,41984
buffer_store_dwordx4 a[236:239], v2, s[20:23], s47 offen offset:3072 sc0 sc1
Mp 220,44,60
Rb 24,1,43008
v_accvgpr_read_b32 v11, a209
Mp 212,44,56
Rb 28,1,44032
Cv 9,9,11
v_accvgpr_write_b32 a152, v9
Mp 224,48,56
Rb 32,1,36864
v_accvgpr_read_b32 v9, a210
Mp 232,48,60
Rb 36,1,37888
v_accvgpr_read_b32 v11, a211
Cv 9,9,11
Mp 236,52,60
Rb 40,1,38912
v_accvgpr_write_b32 a153, v9
Mp 228,52,56
Rb 44,1,39936
buffer_load_dwordx4 v3, s[20:23], s55 offen offset:3072 sc1 lds
Mp 240,56,56
v_accvgpr_read_b32 v9, a212
v_accvgpr_read_b32 v11, a213
Cv 9,9,11
Mp 248,56,60
v_accvgpr_write_b32 a154, v9
Mp 252,60,60
s_waitcnt lgkmcnt(8)
Mp 244,60,56
v_mfma_f32_16x16x32_bf16 v[96:99], a[0:3], a[64:67], v[16:19]
Rb 48,1,45056
buffer_load_dword v4, v5, s[20:23], s50 offen offset:0 sc0 sc1
v_mfma_f32_16x16x32_bf16 v[100:103], a[0:3], a[68:71], v[20:23]
s_lshl_b64 s[60:61], s[60:61], 8
s_or_b32 s60, s60, 0xff
Mv 100,4,76
)" R"(
Rb 52,1,46080
s_add_u32 m0, s36, 65536
Mv 96,4,72
buffer_load_dwordx4 v2, s[20:23], s56 offen offset:0 sc1 lds
Mv 96,8,80
Rb 56,1,47104
Mv 100,8,84
buffer_store_dwordx4 a[240:243], v3, s[20:23], s47 offen offset:0 sc0 sc1
Mv 100,12,92
Rb 60,1,48128
s_waitcnt lgkmcnt(8)
Mv 96,12,88
v_exp_f32_e32 v40, v80
Mv 96,16,96
Rb 0,1,49152
v_add_f32_e32 v14, v14, v40
Mv 100,16,100
v_exp_f32_e32 v28, v81
Mv 100,20,108
Rb 4,1,50176
v_add_f32_e32 v14, v14, v28
Mv 96,20,104
buffer_load_dwordx4 v2, s[20:23], s56 offen offset:1024 sc1 lds
Mv 96,24,112
Rb 8,1,51200
Cv 40,40,28
Mv 100,24,116
v_exp_f32_e32 v41, v82
Mv 100,28,124
Rb 12,1,52224
s_waitcnt lgkmcnt(8)
Mv 96,28,120
v_exp_f32_e32 v29, v83
v_add_f32_e32 v14, v14, v41
v_mfma_f32_16x16x32_bf16 v[104:107], a[32:35], a[64:67], v[16:19]
Rb 16,1,57344
v_add_f32_e32 v14, v14, v29
v_mfma_f32_16x16x32_bf16 v[108:111], a[32:35], a[68:71], v[20:23]
v_exp_f32_e32 v42, v88
Mv 108,36,76
Rb 20,1,58368
Cv 41,41,29
Mv 104,36,72
buffer_load_dwordx4 v2, s[20:23], s56 offen offset:2048 sc1 lds
Mv 104,40,80
Rb 24,1,59392
v_add_f32_e32 v14, v14, v42
Mv 108,40,84
buffer_store_dwordx4 a[244:247], v3, s[20:23], s47 offen offset:1024 sc0 sc1
Mv 108,44,92
Rb 28,1,60416
s_waitcnt lgkmcnt(8)
Mv 104,44,88
v_exp_f32_e32 v24, v89
Mv 104,48,96
Rb 32,1,53248
v_add_f32_e32 v14, v14, v24
Mv 108,48,100
v_exp_f32_e32 v43, v90
Mv 108,52,108
Rb 36,1,54272
Cv 42,42,24
Mv 104,52,104
buffer_load_dwordx4 v2, s[20:23], s56 offen offset:3072 sc1 lds
)" R"(
Mv 104,56,112
Rb 40,1,55296
v_add_f32_e32 v14, v14, v43
Mv 108,56,116
v_exp_f32_e32 v25, v91
Mv 108,60,124
Rb 44,1,56320
s_waitcnt lgkmcnt(8)
Mv 104,60,120
v_exp_f32_e32 v44, v84
v_add_f32_e32 v14, v14, v25
v_mfma_f32_16x16x32_bf16 v[112:115], a[0:3], a[64:67], v[16:19]
Rb 48,1,61440
Cv 43,43,25
v_mfma_f32_16x16x32_bf16 v[116:119], a[0:3], a[68:71], v[20:23]
v_exp_f32_e32 v26, v85
Mv 116,4,76
Rb 52,1,62464
s_add_u32 m0, s36, 69632
Mv 112,4,72
buffer_load_dwordx4 v3, s[20:23], s56 offen offset:0 sc1 lds
Mv 112,8,80
Rb 56,1,63488
v_add_f32_e32 v15, v15, v44
Mv 116,8,84
buffer_store_dwordx4 a[248:251], v3, s[20:23], s47 offen offset:2048 sc0 sc1
Mv 116,12,92
Rb 60,1,64512
s_waitcnt lgkmcnt(8)
Mv 112,12,88
v_exp_f32_e32 v45, v86
v_add_f32_e32 v15, v15, v26
Mv 112,16,96
v_exp_f32_e32 v27, v87
Mv 116,16,100
v_exp_f32_e32 v46, v92
Mv 116,20,108
v_exp_f32_e32 v28, v93
Mv 112,20,104
buffer_load_dwordx4 v3, s[20:23], s56 offen offset:1024 sc1 lds
Mv 112,24,112
v_exp_f32_e32 v47, v94
Mv 116,24,116
v_exp_f32_e32 v29, v95
Mv 116,28,124
Cv 44,44,26
v_add_f32_e32 v15, v15, v45
Mv 112,28,120
v_add_f32_e32 v15, v15, v27
s_waitcnt vmcnt(30) lgkmcnt(0)
s_barrier
v_mfma_f32_16x16x32_bf16 v[120:123], a[32:35], a[64:67], v[16:19]
Rb 0,8,32768
Cv 45,45,27
v_mfma_f32_16x16x32_bf16 v[124:127], a[32:35], a[68:71], v[20:23]
Rb 4,8,33792
v_add_f32_e32 v15, v15, v46
Mv 124,36,76
Rb 8,8,34816
v_add_f32_e32 v15, v15, v28
Mv 120,36,72
Rb 12,8,35840
buffer_load_dwordx4 v3, s[20:23], s56 offen offset:2048 sc1 lds
Mv 120,40,80
Rb 16,8,36864
)" R"(
Cv 46,46,28
Mv 124,40,84
Rb 20,8,37888
buffer_store_dwordx4 a[252:255], v3, s[20:23], s47 offen offset:3072 sc0 sc1
Mv 124,44,92
Rb 24,8,38912
v_add_f32_e32 v15, v15, v47
Mv 120,44,88
Rb 28,8,39936
v_add_f32_e32 v15, v15, v29
Cv 47,47,29
Mv 120,48,96
Rb 32,8,40960
v_max_f32_e32 v11, v14, v15
Mv 124,48,100
Rb 36,8,41984
v_cmp_nge_f32_e32 vcc, s62, v11
Mv 124,52,108
Rb 40,8,43008
s_add_u32 s55, s55, 0x10000
Mv 120,52,104
Rb 44,8,44032
buffer_load_dwordx4 v3, s[20:23], s56 offen offset:3072 sc1 lds
Mv 120,56,112
s_cmp_eq_u32 s57, 2
s_cselect_b32 s55, s59, s55
Mv 124,56,116
s_add_u32 s56, s56, 0x10000
Mv 124,60,124
s_waitcnt lgkmcnt(8)
s_cbranch_vccnz .Lzfx8
.Lzfr8:
Mv 120,60,120
v_add_f32_e32 v12, v12, v14
v_add_f32_e32 v13, v13, v15
Mp 128,0,32
Rb 48,8,45056
Mp 136,0,36
s_cmp_le_i32 s57, s58
s_cbranch_scc1 .Lzmk7
.Lzmr7:
Mp 140,4,36
Rb 52,8,46080
s_add_u32 m0, s36, 32768
Mp 132,4,32
buffer_load_dwordx4 v2, s[20:23], s55 offen offset:0 sc1 lds
Mp 144,8,32
Rb 56,8,47104
Mp 152,8,36
v_exp_f32_e32 v48, v96
Mp 156,12,36
Rb 60,8,48128
s_waitcnt lgkmcnt(8)
Mp 148,12,32
v_exp_f32_e32 v24, v97
Mp 160,16,32
Rb 0,8,49152
v_add_f32_e32 v14, v48, v24
Mp 168,16,36
v_exp_f32_e32 v49, v98
Mp 172,20,36
Rb 4,8,50176
Cv 48,48,24
Mp 164,20,32
)" R"(
buffer_load_dwordx4 v2, s[20:23], s55 offen offset:1024 sc1 lds
Mp 176,24,32
Rb 8,8,51200
v_exp_f32_e32 v25, v99
Mp 184,24,36
v_exp_f32_e32 v50, v104
Mp 188,28,36
Rb 12,8,52224
s_waitcnt lgkmcnt(8)
Mp 180,28,32
v_exp_f32_e32 v26, v105
Mp 192,32,32
Rb 16,8,53248
v_add_f32_e32 v14, v14, v49
Mp 200,32,36
v_exp_f32_e32 v51, v106
v_add_f32_e32 v14, v14, v25
Mp 204,36,36
Rb 20,8,54272
Cv 49,49,25
Mp 196,36,32
buffer_load_dwordx4 v2, s[20:23], s55 offen offset:2048 sc1 lds
Mp 208,40,32
Rb 24,8,55296
v_add_f32_e32 v14, v14, v50
Mp 216,40,36
v_exp_f32_e32 v27, v107
Mp 220,44,36
Rb 28,8,56320
s_waitcnt lgkmcnt(8)
Mp 212,44,32
v_exp_f32_e32 v52, v100
v_add_f32_e32 v14, v14, v26
Mp 224,48,32
Rb 32,8,57344
Cv 50,50,26
Mp 232,48,36
v_exp_f32_e32 v28, v101
Mp 236,52,36
Rb 36,8,58368
v_add_f32_e32 v14, v14, v51
Mp 228,52,32
buffer_load_dwordx4 v2, s[20:23], s55 offen offset:3072 sc1 lds
Mp 240,56,32
Rb 40,8,59392
v_exp_f32_e32 v53, v102
Mp 248,56,36
v_exp_f32_e32 v29, v103
Mp 252,60,36
Rb 44,8,60416
s_waitcnt lgkmcnt(8)
Mp 244,60,32
v_exp_f32_e32 v54, v108
Mp 128,0,40
Rb 48,8,61440
v_add_f32_e32 v14, v14, v27
Mp 136,0,44
v_exp_f32_e32 v24, v109
Cv 51,51,27
Mp 140,4,44
Rb 52,8,62464
s_add_u32 m0, s36, 36864
Mp 132,4,40
buffer_load_dwordx4 v3, s[20:23], s55 offen offset:0 sc1 lds
)" R"(
Mp 144,8,40
Rb 56,8,63488
v_add_f32_e32 v15, v52, v28
Mp 152,8,44
v_exp_f32_e32 v55, v110
Mp 156,12,44
Rb 60,8,64512
s_waitcnt lgkmcnt(8)
Mp 148,12,40
v_exp_f32_e32 v25, v111
Cv 52,52,28
Mp 160,16,40
v_add_f32_e32 v15, v15, v53
v_add_f32_e32 v15, v15, v29
Mp 168,16,44
Cv 53,53,29
v_add_f32_e32 v15, v15, v54
Mp 172,20,44
v_add_f32_e32 v15, v15, v24
Cv 54,54,24
Mp 164,20,40
buffer_load_dwordx4 v3, s[20:23], s55 offen offset:1024 sc1 lds
Mp 176,24,40
v_add_f32_e32 v15, v15, v55
v_add_f32_e32 v15, v15, v25
Cv 55,55,25
Mp 184,24,44
s_sub_u32 s57, s57, 1
v_accvgpr_read_b32 v9, a214
Mp 188,28,44
v_accvgpr_read_b32 v11, a215
Cv 9,9,11
Mp 180,28,40
s_waitcnt vmcnt(7) lgkmcnt(0)
s_barrier
buffer_store_dword v6, v7, s[20:23], s49 offen offset:8 sc0 sc1
Mp 192,32,40
Rb 0,1,0
v_accvgpr_write_b32 a155, v9
Mp 200,32,44
Rb 4,1,1024
v_accvgpr_read_b32 v9, a216
v_accvgpr_read_b32 v11, a217
Mp 204,36,44
Rb 8,1,2048
Cv 9,9,11
Mp 196,36,40
Rb 12,1,3072
buffer_load_dwordx4 v3, s[20:23], s55 offen offset:2048 sc1 lds
Mp 208,40,40
Rb 16,1,8192
v_accvgpr_write_b32 a156, v9
Mp 216,40,44
Rb 20,1,9216
v_accvgpr_read_b32 v9, a218
Mp 220,44,44
Rb 24,1,10240
v_accvgpr_read_b32 v11, a219
Mp 212,44,40
Rb 28,1,11264
Cv 9,9,11
v_accvgpr_write_b32 a157, v9
Mp 224,48,40
Rb 32,1,4096
v_accvgpr_read_b32 v9, a220
)" R"(
Mp 232,48,44
Rb 36,1,5120
v_accvgpr_read_b32 v11, a221
Mp 236,52,44
Rb 40,1,6144
Cv 9,9,11
Mp 228,52,40
Rb 44,1,7168
buffer_load_dwordx4 v3, s[20:23], s55 offen offset:3072 sc1 lds
Mp 240,56,40
v_cmp_eq_u32_e32 vcc, s3, v4
s_andn2_b64 s[28:29], s[60:61], vcc
s_cbranch_scc1 .Lzsp19
.Lzok19:
Mp 248,56,44
v_accvgpr_write_b32 a158, v9
v_accvgpr_read_b32 v9, a222
Mp 252,60,44
v_accvgpr_read_b32 v11, a223
s_waitcnt lgkmcnt(8)
Mp 244,60,40
Cv 9,9,11
v_accvgpr_write_b32 a159, v9
v_mfma_f32_16x16x32_bf16 v[64:67], a[0:3], a[64:67], v[16:19]
Rb 48,1,12288
v_lshrrev_b32 v9, 2, v0
v_mfma_f32_16x16x32_bf16 v[68:71], a[0:3], a[68:71], v[20:23]
v_mul_u32_u24 v9, 448, v9
s_lshr_b32 s92, s33, 1
Mv 68,4,76
Rb 52,1,13312
s_add_u32 m0, s36, 98304
Mv 64,4,72
buffer_load_dwordx4 v2, s[20:23], s56 offen offset:0 sc1 lds
Mv 64,8,80
Rb 56,1,14336
s_mul_i32 s92, s92, 48
Mv 68,8,84
buffer_store_dwordx4 a[128:131], v2, s[20:23], s48 offen offset:0 sc0 sc1
Mv 68,12,92
Rb 60,1,15360
s_waitcnt lgkmcnt(8)
Mv 64,12,88
s_lshl3_add_u32 s92, s33, s92
v_add3_u32 v9, v9, s92, v10
Mv 64,16,96
Rb 0,1,16384
Mv 68,16,100
v_exp_f32_e32 v56, v112
Mv 68,20,108
Rb 4,1,17408
Rt 160,9,0
Mv 64,20,104
buffer_load_dwordx4 v2, s[20:23], s56 offen offset:1024 sc1 lds
Mv 64,24,112
Rb 8,1,18432
v_exp_f32_e32 v26, v113
Mv 68,24,116
v_exp_f32_e32 v57, v114
Mv 68,28,124
Rb 12,1,19456
s_waitcnt lgkmcnt(9)
Mv 64,28,120
v_exp_f32_e32 v27, v115
)" R"(
v_add_f32_e32 v14, v14, v56
v_mfma_f32_16x16x32_bf16 v[72:75], a[32:35], a[64:67], v[16:19]
Rb 16,1,24576
v_add_f32_e32 v14, v14, v26
v_mfma_f32_16x16x32_bf16 v[76:79], a[32:35], a[68:71], v[20:23]
v_exp_f32_e32 v58, v120
Mv 76,36,76
Rb 20,1,25600
Cv 56,56,26
Mv 72,36,72
buffer_load_dwordx4 v2, s[20:23], s56 offen offset:2048 sc1 lds
v_add_f32_e32 v14, v14, v57
Mv 72,40,80
Rb 24,1,26624
Rt 162,9,8192
Mv 76,40,84
v_exp_f32_e32 v28, v121
Mv 76,44,92
Rb 28,1,27648
v_add_f32_e32 v14, v14, v27
s_waitcnt lgkmcnt(10)
Mv 72,44,88
v_exp_f32_e32 v59, v122
Mv 72,48,96
Rb 32,1,20480
Cv 57,57,27
Mv 76,48,100
v_exp_f32_e32 v29, v123
v_add_f32_e32 v14, v14, v58
Mv 76,52,108
Rb 36,1,21504
v_add_f32_e32 v14, v14, v28
Mv 72,52,104
buffer_load_dwordx4 v2, s[20:23], s56 offen offset:3072 sc1 lds
Mv 72,56,112
Rb 40,1,22528
v_exp_f32_e32 v60, v116
Mv 76,56,116
buffer_store_dwordx4 a[132:135], v2, s[20:23], s48 offen offset:1024 sc0 sc1
Mv 76,60,124
Rb 44,1,23552
Rt 164,9,128
s_waitcnt lgkmcnt(10)
Mv 72,60,120
v_exp_f32_e32 v24, v117
Cv 58,58,28
v_mfma_f32_16x16x32_bf16 v[80:83], a[0:3], a[64:67], v[16:19]
Rb 48,1,28672
v_add_f32_e32 v14, v14, v59
v_mfma_f32_16x16x32_bf16 v[84:87], a[0:3], a[68:71], v[20:23]
v_exp_f32_e32 v61, v118
Mv 84,4,76
Rb 52,1,29696
s_add_u32 m0, s36, 102400
Mv 80,4,72
buffer_load_dwordx4 v3, s[20:23], s56 offen offset:0 sc1 lds
v_add_f32_e32 v14, v14, v29
Mv 80,8,80
Rb 56,1,30720
Cv 59,59,29
Mv 84,8,84
v_exp_f32_e32 v25, v119
Mv 84,12,92
Rb 60,1,31744
)" R"(
v_add_f32_e32 v15, v15, v60
s_waitcnt lgkmcnt(9)
Mv 80,12,88
v_exp_f32_e32 v62, v124
Mv 80,16,96
Rt 166,9,8320
v_add_f32_e32 v15, v15, v24
Mv 84,16,100
buffer_store_dwordx4 a[160:163], v1, s[20:23], s4 offen sc0 sc1
Cv 60,60,24
Mv 84,20,108
v_exp_f32_e32 v26, v125
Mv 80,20,104
buffer_load_dwordx4 v3, s[20:23], s56 offen offset:1024 sc1 lds
Mv 80,24,112
v_exp_f32_e32 v63, v126
v_add_f32_e32 v15, v15, v61
Mv 84,24,116
v_exp_f32_e32 v27, v127
Mv 84,28,124
v_add_f32_e32 v15, v15, v25
Cv 61,61,25
Mv 80,28,120
v_add_f32_e32 v15, v15, v62
s_waitcnt vmcnt(18) lgkmcnt(0)
s_barrier
v_mfma_f32_16x16x32_bf16 v[88:91], a[32:35], a[64:67], v[16:19]
Rb 0,8,0
v_add_f32_e32 v15, v15, v26
v_mfma_f32_16x16x32_bf16 v[92:95], a[32:35], a[68:71], v[20:23]
Rb 4,8,1024
Cv 62,62,26
Mv 92,36,76
Rb 8,8,2048
Rt 168,9,256
Mv 88,36,72
Rb 12,8,3072
buffer_load_dwordx4 v3, s[20:23], s56 offen offset:2048 sc1 lds
Mv 88,40,80
Rb 16,8,4096
v_add_f32_e32 v15, v15, v63
Mv 92,40,84
Rb 20,8,5120
buffer_store_dwordx4 a[136:139], v2, s[20:23], s48 offen offset:2048 sc0 sc1
Mv 92,44,92
Rb 24,8,6144
v_add_f32_e32 v15, v15, v27
Cv 63,63,27
Mv 88,44,88
Rb 28,8,7168
v_max_f32_e32 v11, v14, v15
Mv 88,48,96
Rb 32,8,8192
v_cmp_nge_f32_e32 vcc, s62, v11
Mv 92,48,100
Rb 36,8,9216
s_add_u32 s55, s55, 0x10000
Mv 92,52,108
Rb 40,8,10240
Mv 88,52,104
Rb 44,8,11264
buffer_load_dwordx4 v3, s[20:23], s56 offen offset:3072 sc1 lds
Mv 88,56,112
Rt 170,9,8448
s_cmp_eq_u32 s57, 2
)" R"(
s_cselect_b32 s55, s59, s55
Mv 92,56,116
s_add_u32 s56, s56, 0x10000
buffer_store_dwordx4 a[164:167], v1, s[20:23], s5 offen sc0 sc1
Mv 92,60,124
s_waitcnt lgkmcnt(9)
s_cbranch_vccnz .Lzfx9
.Lzfr9:
Mv 88,60,120
v_add_f32_e32 v12, v12, v14
v_add_f32_e32 v13, v13, v15
Mp 128,0,48
Rb 48,8,12288
Mp 136,0,52
s_cmp_le_i32 s57, s58
s_cbranch_scc1 .Lzmk8
.Lzmr8:
Mp 140,4,52
Rb 52,8,13312
s_add_u32 m0, s36, 0
Mp 132,4,48
buffer_load_dwordx4 v2, s[20:23], s55 offen offset:0 sc1 lds
Mp 144,8,48
Rb 56,8,14336
Mp 152,8,52
v_exp_f32_e32 v32, v64
Mp 156,12,52
Rb 60,8,15360
Rt 172,9,384
s_waitcnt lgkmcnt(10)
Mp 148,12,48
v_exp_f32_e32 v28, v65
Mp 160,16,48
Rb 0,8,16384
v_add_f32_e32 v14, v32, v28
Mp 168,16,52
v_exp_f32_e32 v33, v66
Mp 172,20,52
Rb 4,8,17408
Cv 32,32,28
Mp 164,20,48
buffer_load_dwordx4 v2, s[20:23], s55 offen offset:1024 sc1 lds
Mp 176,24,48
Rb 8,8,18432
v_add_f32_e32 v14, v14, v33
Mp 184,24,52
buffer_store_dwordx4 a[140:143], v2, s[20:23], s48 offen offset:3072 sc0 sc1
Mp 188,28,52
Rb 12,8,19456
s_waitcnt lgkmcnt(10)
Mp 180,28,48
v_exp_f32_e32 v29, v67
Mp 192,32,48
Rb 16,8,20480
Rt 174,9,8576
Mp 200,32,52
v_exp_f32_e32 v34, v72
v_add_f32_e32 v14, v14, v29
Mp 204,36,52
Rb 20,8,21504
Cv 33,33,29
Mp 196,36,48
buffer_load_dwordx4 v2, s[20:23], s55 offen offset:2048 sc1 lds
Mp 208,40,48
)" R"(
Rb 24,8,22528
v_add_f32_e32 v14, v14, v34
Mp 216,40,52
v_exp_f32_e32 v24, v73
Mp 220,44,52
Rb 28,8,23552
s_waitcnt lgkmcnt(10)
Mp 212,44,48
buffer_store_dwordx4 a[168:171], v1, s[20:23], s6 offen sc0 sc1
Mp 224,48,48
Rb 32,8,24576
v_add_f32_e32 v14, v14, v24
Mp 232,48,52
v_exp_f32_e32 v35, v74
Mp 236,52,52
Rb 36,8,25600
Rt 176,9,16384
Mp 228,52,48
buffer_load_dwordx4 v2, s[20:23], s55 offen offset:3072 sc1 lds
Mp 240,56,48
Rb 40,8,26624
Cv 34,34,24
Mp 248,56,52
v_exp_f32_e32 v25, v75
Mp 252,60,52
Rb 44,8,27648
s_waitcnt lgkmcnt(10)
Mp 244,60,48
v_exp_f32_e32 v36, v68
Mp 128,0,56
Rb 48,8,28672
v_add_f32_e32 v14, v14, v35
Mp 136,0,60
v_exp_f32_e32 v26, v69
v_add_f32_e32 v14, v14, v25
Mp 140,4,60
Rb 52,8,29696
s_add_u32 m0, s36, 4096
Mp 132,4,56
buffer_load_dwordx4 v3, s[20:23], s55 offen offset:0 sc1 lds
Mp 144,8,56
Rb 56,8,30720
Rt 178,9,24576
Mp 152,8,60
buffer_store_dwordx4 a[144:147], v3, s[20:23], s48 offen offset:0 sc0 sc1
Mp 156,12,60
Rb 60,8,31744
s_waitcnt lgkmcnt(10)
Mp 148,12,56
v_exp_f32_e32 v37, v70
Mp 160,16,56
v_exp_f32_e32 v27, v71
Mp 168,16,60
v_exp_f32_e32 v38, v76
Mp 172,20,60
v_exp_f32_e32 v28, v77
Mp 164,20,56
buffer_load_dwordx4 v3, s[20:23], s55 offen offset:1024 sc1 lds
Mp 176,24,56
v_exp_f32_e32 v39, v78
Mp 184,24,60
buffer_store_dwordx4 a[172:175], v1, s[20:23], s7 offen sc0 sc1
Mp 188,28,60
Rt 180,9,16512
)" R"(
Cv 35,35,25
Mp 180,28,56
s_waitcnt vmcnt(23) lgkmcnt(1)
s_barrier
Mp 192,32,56
Rb 0,1,32768
v_add_f32_e32 v15, v36, v26
Mp 200,32,60
Rb 4,1,33792
v_exp_f32_e32 v29, v79
Mp 204,36,60
Rb 8,1,34816
Cv 36,36,26
Mp 196,36,56
Rb 12,1,35840
buffer_load_dwordx4 v3, s[20:23], s55 offen offset:2048 sc1 lds
Mp 208,40,56
Rb 16,1,40960
v_add_f32_e32 v15, v15, v37
Mp 216,40,60
Rb 20,1,41984
v_add_f32_e32 v15, v15, v27
Mp 220,44,60
Rb 24,1,43008
Cv 37,37,27
Mp 212,44,56
Rb 28,1,44032
v_add_f32_e32 v15, v15, v38
Mp 224,48,56
Rb 32,1,36864
Rt 182,9,24704
Mp 232,48,60
Rb 36,1,37888
v_add_f32_e32 v15, v15, v28
Mp 236,52,60
Rb 40,1,38912
Cv 38,38,28
Mp 228,52,56
Rb 44,1,39936
buffer_load_dwordx4 v3, s[20:23], s55 offen offset:3072 sc1 lds
Mp 240,56,56
v_add_f32_e32 v15, v15, v39
v_add_f32_e32 v15, v15, v29
Mp 248,56,60
buffer_store_dwordx4 a[148:151], v3, s[20:23], s48 offen offset:1024 sc0 sc1
Mp 252,60,60
Cv 39,39,29
s_waitcnt lgkmcnt(9)
Mp 244,60,56
s_sub_u32 s57, s57, 1
v_mfma_f32_16x16x32_bf16 v[96:99], a[0:3], a[64:67], v[16:19]
Rb 48,1,45056
buffer_load_dword v4, v5, s[20:23], s50 offen offset:0 sc0 sc1
v_mfma_f32_16x16x32_bf16 v[100:103], a[0:3], a[68:71], v[20:23]
s_lshl_b64 s[60:61], s[60:61], 8
s_or_b32 s60, s60, 0xff
Mv 100,4,76
Rb 52,1,46080
s_add_u32 m0, s36, 65536
Rt 184,9,16640
Mv 96,4,72
buffer_load_dwordx4 v2, s[20:23], s56 offen offset:0 sc1 lds
Mv 96,8,80
Rb 56,1,47104
Mv 100,8,84
)" R"(
v_exp_f32_e32 v40, v80
Mv 100,12,92
Rb 60,1,48128
s_waitcnt lgkmcnt(10)
Mv 96,12,88
buffer_store_dwordx4 a[176:179], v1, s[20:23], s8 offen sc0 sc1
Mv 96,16,96
Rb 0,1,49152
v_add_f32_e32 v14, v14, v40
Mv 100,16,100
v_exp_f32_e32 v24, v81
Mv 100,20,108
Rb 4,1,50176
v_exp_f32_e32 v41, v82
Mv 96,20,104
buffer_load_dwordx4 v2, s[20:23], s56 offen offset:1024 sc1 lds
Mv 96,24,112
Rb 8,1,51200
Rt 186,9,24832
Mv 100,24,116
v_exp_f32_e32 v25, v83
Mv 100,28,124
Rb 12,1,52224
s_waitcnt lgkmcnt(10)
Mv 96,28,120
v_exp_f32_e32 v42, v88
v_add_f32_e32 v14, v14, v24
v_mfma_f32_16x16x32_bf16 v[104:107], a[32:35], a[64:67], v[16:19]
Rb 16,1,57344
Cv 40,40,24
v_mfma_f32_16x16x32_bf16 v[108:111], a[32:35], a[68:71], v[20:23]
v_exp_f32_e32 v26, v89
Mv 108,36,76
Rb 20,1,58368
v_add_f32_e32 v14, v14, v41
Mv 104,36,72
buffer_load_dwordx4 v2, s[20:23], s56 offen offset:2048 sc1 lds
Mv 104,40,80
Rb 24,1,59392
v_add_f32_e32 v14, v14, v25
Mv 108,40,84
buffer_store_dwordx4 a[152:155], v3, s[20:23], s48 offen offset:2048 sc0 sc1
Cv 41,41,25
Mv 108,44,92
Rb 28,1,60416
Rt 188,9,16768
s_waitcnt lgkmcnt(10)
Mv 104,44,88
v_exp_f32_e32 v43, v90
Mv 104,48,96
Rb 32,1,53248
v_add_f32_e32 v14, v14, v42
Mv 108,48,100
v_exp_f32_e32 v27, v91
Mv 108,52,108
Rb 36,1,54272
v_exp_f32_e32 v44, v84
Mv 104,52,104
buffer_load_dwordx4 v2, s[20:23], s56 offen offset:3072 sc1 lds
Mv 104,56,112
Rb 40,1,55296
v_add_f32_e32 v14, v14, v26
Mv 108,56,116
buffer_store_dwordx4 a[180:183], v1, s[20:23], s9 offen sc0 sc1
)" R"(
Mv 108,60,124
Rb 44,1,56320
s_waitcnt lgkmcnt(9)
Mv 104,60,120
v_exp_f32_e32 v28, v85
Cv 42,42,26
v_mfma_f32_16x16x32_bf16 v[112:115], a[0:3], a[64:67], v[16:19]
Rb 48,1,61440
Rt 190,9,24960
v_mfma_f32_16x16x32_bf16 v[116:119], a[0:3], a[68:71], v[20:23]
v_exp_f32_e32 v45, v86
Mv 116,4,76
Rb 52,1,62464
s_add_u32 m0, s36, 69632
Mv 112,4,72
buffer_load_dwordx4 v3, s[20:23], s56 offen offset:0 sc1 lds
Mv 112,8,80
Rb 56,1,63488
v_add_f32_e32 v14, v14, v43
Mv 116,8,84
v_exp_f32_e32 v29, v87
v_add_f32_e32 v14, v14, v27
Mv 116,12,92
Rb 60,1,64512
s_waitcnt lgkmcnt(10)
Mv 112,12,88
v_exp_f32_e32 v46, v92
Mv 112,16,96
v_exp_f32_e32 v24, v93
Mv 116,16,100
v_exp_f32_e32 v47, v94
Mv 116,20,108
v_exp_f32_e32 v25, v95
Cv 43,43,27
Mv 112,20,104
buffer_load_dwordx4 v3, s[20:23], s56 offen offset:1024 sc1 lds
Mv 112,24,112
v_add_f32_e32 v15, v15, v44
v_add_f32_e32 v15, v15, v28
Mv 116,24,116
buffer_store_dwordx4 a[156:159], v3, s[20:23], s48 offen offset:3072 sc0 sc1
Mv 116,28,124
Cv 44,44,28
v_add_f32_e32 v15, v15, v45
Mv 112,28,120
v_add_f32_e32 v15, v15, v29
s_waitcnt vmcnt(25) lgkmcnt(0)
s_barrier
v_mfma_f32_16x16x32_bf16 v[120:123], a[32:35], a[64:67], v[16:19]
Rb 0,8,32768
Cv 45,45,29
v_mfma_f32_16x16x32_bf16 v[124:127], a[32:35], a[68:71], v[20:23]
Rb 4,8,33792
v_add_f32_e32 v15, v15, v46
Mv 124,36,76
Rb 8,8,34816
v_add_f32_e32 v15, v15, v24
Mv 120,36,72
Rb 12,8,35840
buffer_load_dwordx4 v3, s[20:23], s56 offen offset:2048 sc1 lds
Mv 120,40,80
Rb 16,8,36864
Cv 46,46,24
Mv 124,40,84
Rb 20,8,37888
)" R"(
v_add_f32_e32 v15, v15, v47
v_add_f32_e32 v15, v15, v25
Mv 124,44,92
Rb 24,8,38912
Cv 47,47,25
Mv 120,44,88
Rb 28,8,39936
buffer_store_dwordx4 a[184:187], v1, s[20:23], s10 offen sc0 sc1
Mv 120,48,96
Rb 32,8,40960
v_max_f32_e32 v11, v14, v15
Mv 124,48,100
Rb 36,8,41984
v_cmp_nge_f32_e32 vcc, s62, v11
Mv 124,52,108
Rb 40,8,43008
s_add_u32 s55, s55, 0x10000
Mv 120,52,104
Rb 44,8,44032
buffer_load_dwordx4 v3, s[20:23], s56 offen offset:3072 sc1 lds
Mv 120,56,112
s_cmp_eq_u32 s57, 2
s_cselect_b32 s55, s59, s55
Mv 124,56,116
s_add_u32 s56, s56, 0x10000
Mv 124,60,124
s_waitcnt lgkmcnt(8)
s_cbranch_vccnz .Lzfx10
.Lzfr10:
Mv 120,60,120
v_add_f32_e32 v12, v12, v14
v_add_f32_e32 v13, v13, v15
Mp 128,0,32
Rb 48,8,45056
Mp 136,0,36
s_cmp_le_i32 s57, s58
s_cbranch_scc1 .Lzmk9
.Lzmr9:
Mp 140,4,36
Rb 52,8,46080
s_add_u32 m0, s36, 32768
Mp 132,4,32
buffer_load_dwordx4 v2, s[20:23], s55 offen offset:0 sc1 lds
Mp 144,8,32
Rb 56,8,47104
Mp 152,8,36
buffer_store_dwordx4 a[188:191], v1, s[20:23], s11 offen sc0 sc1
Mp 156,12,36
Rb 60,8,48128
s_waitcnt lgkmcnt(8)
Mp 148,12,32
v_exp_f32_e32 v48, v96
Mp 160,16,32
Rb 0,8,49152
Mp 168,16,36
v_exp_f32_e32 v26, v97
Mp 172,20,36
Rb 4,8,50176
v_add_f32_e32 v14, v48, v26
Mp 164,20,32
buffer_load_dwordx4 v2, s[20:23], s55 offen offset:1024 sc1 lds
Mp 176,24,32
Rb 8,8,51200
Cv 48,48,26
)" R"(
Mp 184,24,36
v_exp_f32_e32 v49, v98
Mp 188,28,36
Rb 12,8,52224
s_waitcnt lgkmcnt(8)
Mp 180,28,32
v_exp_f32_e32 v27, v99
Mp 192,32,32
Rb 16,8,53248
v_add_f32_e32 v14, v14, v49
Mp 200,32,36
v_exp_f32_e32 v50, v104
Mp 204,36,36
Rb 20,8,54272
v_add_f32_e32 v14, v14, v27
Mp 196,36,32
buffer_load_dwordx4 v2, s[20:23], s55 offen offset:2048 sc1 lds
Mp 208,40,32
Rb 24,8,55296
Cv 49,49,27
Mp 216,40,36
v_exp_f32_e32 v28, v105
Mp 220,44,36
Rb 28,8,56320
s_waitcnt lgkmcnt(8)
Mp 212,44,32
v_exp_f32_e32 v51, v106
Mp 224,48,32
Rb 32,8,57344
v_add_f32_e32 v14, v14, v50
Mp 232,48,36
v_exp_f32_e32 v29, v107
Mp 236,52,36
Rb 36,8,58368
v_add_f32_e32 v14, v14, v28
Mp 228,52,32
buffer_load_dwordx4 v2, s[20:23], s55 offen offset:3072 sc1 lds
Mp 240,56,32
Rb 40,8,59392
Cv 50,50,28
Mp 248,56,36
v_exp_f32_e32 v52, v100
Mp 252,60,36
Rb 44,8,60416
s_waitcnt lgkmcnt(8)
Mp 244,60,32
v_exp_f32_e32 v24, v101
Mp 128,0,40
Rb 48,8,61440
v_add_f32_e32 v14, v14, v51
Mp 136,0,44
v_exp_f32_e32 v53, v102
Mp 140,4,44
Rb 52,8,62464
s_add_u32 m0, s36, 36864
Mp 132,4,40
buffer_load_dwordx4 v3, s[20:23], s55 offen offset:0 sc1 lds
Mp 144,8,40
Rb 56,8,63488
v_add_f32_e32 v14, v14, v29
Mp 152,8,44
v_exp_f32_e32 v25, v103
Mp 156,12,44
Rb 60,8,64512
)" R"(
s_waitcnt lgkmcnt(8)
Mp 148,12,40
v_exp_f32_e32 v54, v108
Mp 160,16,40
v_exp_f32_e32 v26, v109
Mp 168,16,44
v_exp_f32_e32 v55, v110
Mp 172,20,44
v_exp_f32_e32 v27, v111
Mp 164,20,40
buffer_load_dwordx4 v3, s[20:23], s55 offen offset:1024 sc1 lds
Mp 176,24,40
Cv 51,51,29
v_add_f32_e32 v15, v52, v24
Mp 184,24,44
Cv 52,52,24
v_add_f32_e32 v15, v15, v53
Mp 188,28,44
v_add_f32_e32 v15, v15, v25
Cv 53,53,25
Mp 180,28,40
s_waitcnt vmcnt(5) lgkmcnt(0)
s_barrier
buffer_store_dword v6, v7, s[20:23], s49 offen offset:12 sc0 sc1
Mp 192,32,40
Rb 0,1,0
v_add_f32_e32 v15, v15, v54
Mp 200,32,44
Rb 4,1,1024
v_add_f32_e32 v15, v15, v26
Mp 204,36,44
Rb 8,1,2048
Cv 54,54,26
Mp 196,36,40
Rb 12,1,3072
buffer_load_dwordx4 v3, s[20:23], s55 offen offset:2048 sc1 lds
Mp 208,40,40
Rb 16,1,8192
v_add_f32_e32 v15, v15, v55
Mp 216,40,44
Rb 20,1,9216
v_add_f32_e32 v15, v15, v27
Mp 220,44,44
Rb 24,1,10240
Cv 55,55,27
Mp 212,44,40
Rb 28,1,11264
s_sub_u32 s57, s57, 1
Mp 224,48,40
Rb 32,1,4096
Mp 232,48,44
Rb 36,1,5120
Mp 236,52,44
Rb 40,1,6144
Mp 228,52,40
Rb 44,1,7168
buffer_load_dwordx4 v3, s[20:23], s55 offen offset:3072 sc1 lds
Mp 240,56,40
v_cmp_eq_u32_e32 vcc, s3, v4
s_andn2_b64 s[28:29], s[60:61], vcc
s_cbranch_scc1 .Lzsp20
.Lzok20:
Mp 248,56,44
Mp 252,60,44
s_waitcnt lgkmcnt(8)
)" R"(
Mp 244,60,40
s_branch .LzL
.LzEB:
v_mfma_f32_16x16x32_bf16 v[64:67], a[0:3], a[64:67], v[16:19]
Rb 48,1,12288
s_mov_b32 s83, 0xc108000
buffer_load_dword v4, v7, s[20:23], s83 offen offset:4 sc0 sc1
v_mfma_f32_16x16x32_bf16 v[68:71], a[0:3], a[68:71], v[20:23]
s_cmp_lg_u32 s35, 7
s_cselect_b64 s[60:61], 1, 0
Mv 68,4,76
Rb 52,1,13312
s_add_u32 m0, s36, 98304
Mv 64,4,72
buffer_load_dwordx4 v2, s[20:23], s56 offen offset:0 sc1 lds
Mv 64,8,80
Rb 56,1,14336
Mv 68,8,84
v_exp_f32_e32 v56, v112
Mv 68,12,92
Rb 60,1,15360
s_waitcnt lgkmcnt(8)
Mv 64,12,88
v_exp_f32_e32 v28, v113
Mv 64,16,96
Rb 0,1,16384
v_add_f32_e32 v14, v14, v56
Mv 68,16,100
v_exp_f32_e32 v57, v114
Mv 68,20,108
Rb 4,1,17408
v_add_f32_e32 v14, v14, v28
Mv 64,20,104
buffer_load_dwordx4 v2, s[20:23], s56 offen offset:1024 sc1 lds
Mv 64,24,112
Rb 8,1,18432
Cv 56,56,28
Mv 68,24,116
v_exp_f32_e32 v29, v115
Mv 68,28,124
Rb 12,1,19456
s_waitcnt lgkmcnt(8)
Mv 64,28,120
v_exp_f32_e32 v58, v120
v_mfma_f32_16x16x32_bf16 v[72:75], a[32:35], a[64:67], v[16:19]
Rb 16,1,24576
v_add_f32_e32 v14, v14, v57
v_mfma_f32_16x16x32_bf16 v[76:79], a[32:35], a[68:71], v[20:23]
v_exp_f32_e32 v24, v121
Mv 76,36,76
Rb 20,1,25600
v_add_f32_e32 v14, v14, v29
Mv 72,36,72
buffer_load_dwordx4 v2, s[20:23], s56 offen offset:2048 sc1 lds
Mv 72,40,80
Rb 24,1,26624
Cv 57,57,29
Mv 76,40,84
v_exp_f32_e32 v59, v122
Mv 76,44,92
Rb 28,1,27648
s_waitcnt lgkmcnt(8)
Mv 72,44,88
v_exp_f32_e32 v25, v123
)" R"(
Mv 72,48,96
Rb 32,1,20480
v_add_f32_e32 v14, v14, v58
Mv 76,48,100
v_exp_f32_e32 v60, v116
Mv 76,52,108
Rb 36,1,21504
v_add_f32_e32 v14, v14, v24
Mv 72,52,104
buffer_load_dwordx4 v2, s[20:23], s56 offen offset:3072 sc1 lds
Mv 72,56,112
Rb 40,1,22528
Cv 58,58,24
Mv 76,56,116
v_exp_f32_e32 v26, v117
Mv 76,60,124
Rb 44,1,23552
s_waitcnt lgkmcnt(8)
Mv 72,60,120
v_exp_f32_e32 v61, v118
v_mfma_f32_16x16x32_bf16 v[80:83], a[0:3], a[64:67], v[16:19]
Rb 48,1,28672
v_add_f32_e32 v14, v14, v59
v_mfma_f32_16x16x32_bf16 v[84:87], a[0:3], a[68:71], v[20:23]
v_exp_f32_e32 v27, v119
Mv 84,4,76
Rb 52,1,29696
s_add_u32 m0, s36, 102400
Mv 80,4,72
buffer_load_dwordx4 v3, s[20:23], s56 offen offset:0 sc1 lds
Mv 80,8,80
Rb 56,1,30720
v_add_f32_e32 v14, v14, v25
Mv 84,8,84
v_exp_f32_e32 v62, v124
Mv 84,12,92
Rb 60,1,31744
s_waitcnt lgkmcnt(8)
Mv 80,12,88
v_exp_f32_e32 v28, v125
Mv 80,16,96
v_exp_f32_e32 v63, v126
Mv 84,16,100
v_exp_f32_e32 v29, v127
Mv 84,20,108
Cv 59,59,25
v_add_f32_e32 v15, v15, v60
Mv 80,20,104
buffer_load_dwordx4 v3, s[20:23], s56 offen offset:1024 sc1 lds
Mv 80,24,112
v_add_f32_e32 v15, v15, v26
Cv 60,60,26
Mv 84,24,116
v_add_f32_e32 v15, v15, v61
v_add_f32_e32 v15, v15, v27
Mv 84,28,124
Cv 61,61,27
v_add_f32_e32 v15, v15, v62
Mv 80,28,120
s_waitcnt vmcnt(15) lgkmcnt(0)
s_barrier
v_mfma_f32_16x16x32_bf16 v[88:91], a[32:35], a[64:67], v[16:19]
Rb 0,8,0
v_add_f32_e32 v15, v15, v28
v_mfma_f32_16x16x32_bf16 v[92:95], a[32:35], a[68:71], v[20:23]
)" R"(
Rb 4,8,1024
Cv 62,62,28
Mv 92,36,76
Rb 8,8,2048
v_add_f32_e32 v15, v15, v63
Mv 88,36,72
Rb 12,8,3072
buffer_load_dwordx4 v3, s[20:23], s56 offen offset:2048 sc1 lds
Mv 88,40,80
Rb 16,8,4096
v_add_f32_e32 v15, v15, v29
Mv 92,40,84
Rb 20,8,5120
Cv 63,63,29
Mv 92,44,92
Rb 24,8,6144
v_max_f32_e32 v11, v14, v15
Mv 88,44,88
Rb 28,8,7168
v_cmp_nge_f32_e32 vcc, s62, v11
Mv 88,48,96
Rb 32,8,8192
s_add_u32 s55, s55, 0x10000
Mv 92,48,100
Rb 36,8,9216
Mv 92,52,108
Rb 40,8,10240
Mv 88,52,104
Rb 44,8,11264
buffer_load_dwordx4 v3, s[20:23], s56 offen offset:3072 sc1 lds
Mv 88,56,112
s_cmp_eq_u32 s57, 2
s_cselect_b32 s55, s59, s55
Mv 92,56,116
s_add_u32 s56, s56, 0x10000
Mv 92,60,124
s_waitcnt lgkmcnt(8)
s_cbranch_vccnz .Lzfx11
.Lzfr11:
Mv 88,60,120
s_waitcnt vmcnt(8)
v_cmp_eq_u32_e32 vcc, s3, v4
s_andn2_b64 s[28:29], 1, vcc
s_cbranch_scc1 .Lzsp21
.Lzok21:
Mp 128,0,48
Rb 48,8,12288
buffer_load_dword v9, v7, s[20:23], s83 offen sc0 sc1
Mp 136,0,52
v_add_f32_e32 v12, v12, v14
s_cmp_le_i32 s57, s58
s_cbranch_scc1 .Lzmk10
.Lzmr10:
Mp 140,4,52
Rb 52,8,13312
s_add_u32 m0, s36, 0
v_add_f32_e32 v13, v13, v15
Mp 132,4,48
buffer_load_dwordx4 v2, s[20:23], s55 offen offset:0 sc1 lds
Mp 144,8,48
Rb 56,8,14336
Mp 152,8,52
v_exp_f32_e32 v32, v64
Mp 156,12,52
)" R"(
Rb 60,8,15360
s_waitcnt lgkmcnt(8)
Mp 148,12,48
v_exp_f32_e32 v24, v65
Mp 160,16,48
Rb 0,8,16384
v_add_f32_e32 v14, v32, v24
Mp 168,16,52
v_exp_f32_e32 v33, v66
Cv 32,32,24
Mp 172,20,52
Rb 4,8,17408
v_add_f32_e32 v14, v14, v33
Mp 164,20,48
buffer_load_dwordx4 v2, s[20:23], s55 offen offset:1024 sc1 lds
Mp 176,24,48
Rb 8,8,18432
Mp 184,24,52
v_exp_f32_e32 v25, v67
Mp 188,28,52
Rb 12,8,19456
s_waitcnt lgkmcnt(8)
Mp 180,28,48
v_exp_f32_e32 v34, v72
Mp 192,32,48
Rb 16,8,20480
v_add_f32_e32 v14, v14, v25
Mp 200,32,52
v_exp_f32_e32 v26, v73
Cv 33,33,25
Mp 204,36,52
Rb 20,8,21504
v_add_f32_e32 v14, v14, v34
Mp 196,36,48
buffer_load_dwordx4 v2, s[20:23], s55 offen offset:2048 sc1 lds
Mp 208,40,48
Rb 24,8,22528
v_add_f32_e32 v14, v14, v26
Mp 216,40,52
v_exp_f32_e32 v35, v74
Mp 220,44,52
Rb 28,8,23552
s_waitcnt lgkmcnt(8)
Mp 212,44,48
v_exp_f32_e32 v27, v75
Mp 224,48,48
Rb 32,8,24576
Cv 34,34,26
Mp 232,48,52
v_exp_f32_e32 v36, v68
v_add_f32_e32 v14, v14, v35
Mp 236,52,52
Rb 36,8,25600
v_add_f32_e32 v14, v14, v27
Mp 228,52,48
buffer_load_dwordx4 v2, s[20:23], s55 offen offset:3072 sc1 lds
Mp 240,56,48
Rb 40,8,26624
Cv 35,35,27
Mp 248,56,52
v_exp_f32_e32 v28, v69
Mp 252,60,52
Rb 44,8,27648
s_waitcnt lgkmcnt(8)
)" R"(
Mp 244,60,48
v_exp_f32_e32 v37, v70
Mp 128,0,56
Rb 48,8,28672
v_add_f32_e32 v15, v36, v28
Mp 136,0,60
v_exp_f32_e32 v29, v71
Cv 36,36,28
Mp 140,4,60
Rb 52,8,29696
s_add_u32 m0, s36, 4096
Mp 132,4,56
buffer_load_dwordx4 v3, s[20:23], s55 offen offset:0 sc1 lds
Mp 144,8,56
Rb 56,8,30720
v_add_f32_e32 v15, v15, v37
Mp 152,8,60
v_exp_f32_e32 v38, v76
Mp 156,12,60
Rb 60,8,31744
s_waitcnt lgkmcnt(8)
Mp 148,12,56
v_exp_f32_e32 v24, v77
Mp 160,16,56
v_exp_f32_e32 v39, v78
Mp 168,16,60
v_exp_f32_e32 v25, v79
v_add_f32_e32 v15, v15, v29
Mp 172,20,60
Cv 37,37,29
v_add_f32_e32 v15, v15, v38
Mp 164,20,56
buffer_load_dwordx4 v3, s[20:23], s55 offen offset:1024 sc1 lds
Mp 176,24,56
v_add_f32_e32 v15, v15, v24
Cv 38,38,24
Mp 184,24,60
v_add_f32_e32 v15, v15, v39
v_add_f32_e32 v15, v15, v25
Mp 188,28,60
Cv 39,39,25
s_sub_u32 s57, s57, 1
Mp 180,28,56
s_waitcnt lgkmcnt(0)
s_barrier
Mp 192,32,56
Rb 0,1,32768
v_bfe_u32 v11, v0, 2, 2
Mp 200,32,60
Rb 4,1,33792
v_lshrrev_b32 v4, 4, v0
v_xor_b32 v11, v11, v4
Mp 204,36,60
Rb 8,1,34816
v_bfe_u32 v4, v0, 1, 1
Mp 196,36,56
Rb 12,1,35840
buffer_load_dwordx4 v3, s[20:23], s55 offen offset:2048 sc1 lds
Mp 208,40,56
Rb 16,1,40960
v_lshl_or_b32 v11, v11, 1, v4
Mp 216,40,60
Rb 20,1,41984
v_and_b32 v4, 1, v0
Mp 220,44,60
)" R"(
Rb 24,1,43008
v_mul_u32_u24 v4, 0xffff, v4
Mp 212,44,56
Rb 28,1,44032
v_add_u32 v4, 1, v4
Mp 224,48,56
Rb 32,1,36864
v_mul_u32_u24 v4, 0x3f80, v4
Mp 232,48,60
Rb 36,1,37888
v_cmp_eq_u32_e32 vcc, 0, v11
v_cmp_eq_u32_e64 s[28:29], 1, v11
Mp 236,52,60
Rb 40,1,38912
v_accvgpr_write_b32 a192, 0
Mp 228,52,56
Rb 44,1,39936
buffer_load_dwordx4 v3, s[20:23], s55 offen offset:3072 sc1 lds
Mp 240,56,56
v_accvgpr_write_b32 a193, 0
v_accvgpr_write_b32 a196, 0
Mp 248,56,60
v_accvgpr_write_b32 a197, 0
v_cndmask_b32_e32 v11, 0, v4, vcc
Mp 252,60,60
v_cndmask_b32_e64 v4, 0, v4, s[28:29]
s_waitcnt lgkmcnt(8)
Mp 244,60,56
v_accvgpr_write_b32 a194, v11
v_accvgpr_write_b32 a195, v4
v_mfma_f32_16x16x32_bf16 v[96:99], a[0:3], a[64:67], v[16:19]
Rb 48,1,45056
v_mfma_f32_16x16x32_bf16 v[100:103], a[0:3], a[68:71], v[20:23]
buffer_load_dword v4, v7, s[20:23], s65 offen offset:0 sc0 sc1
Mv 100,4,76
Rb 52,1,46080
s_add_u32 m0, s36, 65536
Mv 96,4,72
buffer_load_dwordx4 v2, s[20:23], s56 offen offset:0 sc1 lds
Mv 96,8,80
Rb 56,1,47104
Mv 100,8,84
v_exp_f32_e32 v40, v80
Mv 100,12,92
Rb 60,1,48128
s_waitcnt lgkmcnt(8)
Mv 96,12,88
v_exp_f32_e32 v26, v81
v_add_f32_e32 v14, v14, v40
Mv 96,16,96
Rb 0,1,49152
v_add_f32_e32 v14, v14, v26
Mv 100,16,100
v_exp_f32_e32 v41, v82
Mv 100,20,108
Rb 4,1,50176
Cv 40,40,26
Mv 96,20,104
buffer_load_dwordx4 v2, s[20:23], s56 offen offset:1024 sc1 lds
Mv 96,24,112
Rb 8,1,51200
v_add_f32_e32 v14, v14, v41
Mv 100,24,116
v_exp_f32_e32 v27, v83
)" R"(
Mv 100,28,124
Rb 12,1,52224
s_waitcnt lgkmcnt(8)
Mv 96,28,120
v_exp_f32_e32 v42, v88
v_add_f32_e32 v14, v14, v27
v_mfma_f32_16x16x32_bf16 v[104:107], a[32:35], a[64:67], v[16:19]
Rb 16,1,57344
Cv 41,41,27
v_mfma_f32_16x16x32_bf16 v[108:111], a[32:35], a[68:71], v[20:23]
v_exp_f32_e32 v28, v89
Mv 108,36,76
Rb 20,1,58368
v_add_f32_e32 v14, v14, v42
Mv 104,36,72
buffer_load_dwordx4 v2, s[20:23], s56 offen offset:2048 sc1 lds
Mv 104,40,80
Rb 24,1,59392
v_add_f32_e32 v14, v14, v28
Mv 108,40,84
v_exp_f32_e32 v43, v90
Mv 108,44,92
Rb 28,1,60416
s_waitcnt lgkmcnt(8)
Mv 104,44,88
v_exp_f32_e32 v29, v91
Cv 42,42,28
Mv 104,48,96
Rb 32,1,53248
v_add_f32_e32 v14, v14, v43
Mv 108,48,100
v_exp_f32_e32 v44, v84
Mv 108,52,108
Rb 36,1,54272
v_add_f32_e32 v14, v14, v29
Mv 104,52,104
buffer_load_dwordx4 v2, s[20:23], s56 offen offset:3072 sc1 lds
Mv 104,56,112
Rb 40,1,55296
Cv 43,43,29
Mv 108,56,116
v_exp_f32_e32 v24, v85
Mv 108,60,124
Rb 44,1,56320
s_waitcnt lgkmcnt(8)
Mv 104,60,120
v_exp_f32_e32 v45, v86
v_add_f32_e32 v15, v15, v44
v_mfma_f32_16x16x32_bf16 v[112:115], a[0:3], a[64:67], v[16:19]
Rb 48,1,61440
v_add_f32_e32 v15, v15, v24
v_mfma_f32_16x16x32_bf16 v[116:119], a[0:3], a[68:71], v[20:23]
v_exp_f32_e32 v25, v87
Mv 116,4,76
Rb 52,1,62464
s_add_u32 m0, s36, 69632
Mv 112,4,72
buffer_load_dwordx4 v3, s[20:23], s56 offen offset:0 sc1 lds
Mv 112,8,80
Rb 56,1,63488
Cv 44,44,24
Mv 116,8,84
v_exp_f32_e32 v46, v92
Mv 116,12,92
)" R"(
Rb 60,1,64512
s_waitcnt lgkmcnt(8)
Mv 112,12,88
v_exp_f32_e32 v26, v93
v_add_f32_e32 v15, v15, v45
Mv 112,16,96
v_exp_f32_e32 v47, v94
Mv 116,16,100
v_exp_f32_e32 v27, v95
Mv 116,20,108
v_add_f32_e32 v15, v15, v25
Cv 45,45,25
Mv 112,20,104
buffer_load_dwordx4 v3, s[20:23], s56 offen offset:1024 sc1 lds
Mv 112,24,112
v_add_f32_e32 v15, v15, v46
v_add_f32_e32 v15, v15, v26
Mv 116,24,116
Cv 46,46,26
v_add_f32_e32 v15, v15, v47
Mv 116,28,124
v_add_f32_e32 v15, v15, v27
Cv 47,47,27
Mv 112,28,120
v_max_f32_e32 v11, v14, v15
s_waitcnt vmcnt(6) lgkmcnt(0)
s_barrier
v_mfma_f32_16x16x32_bf16 v[120:123], a[32:35], a[64:67], v[16:19]
Rb 0,8,32768
v_cmp_nge_f32_e32 vcc, s62, v11
v_mfma_f32_16x16x32_bf16 v[124:127], a[32:35], a[68:71], v[20:23]
Rb 4,8,33792
s_add_u32 s55, s55, 0x10000
Mv 124,36,76
Rb 8,8,34816
Mv 120,36,72
Rb 12,8,35840
buffer_load_dwordx4 v3, s[20:23], s56 offen offset:2048 sc1 lds
Mv 120,40,80
Rb 16,8,36864
Mv 124,40,84
Rb 20,8,37888
Mv 124,44,92
Rb 24,8,38912
Mv 120,44,88
Rb 28,8,39936
s_cmp_eq_u32 s57, 2
s_cselect_b32 s55, s59, s55
Mv 120,48,96
Rb 32,8,40960
Mv 124,48,100
Rb 36,8,41984
Mv 124,52,108
Rb 40,8,43008
Mv 120,52,104
Rb 44,8,44032
buffer_load_dwordx4 v3, s[20:23], s56 offen offset:3072 sc1 lds
Mv 120,56,112
v_readfirstlane_b32 s63, v9
Mv 124,56,116
s_add_u32 s56, s56, 0x10000
Mv 124,60,124
s_waitcnt lgkmcnt(8)
s_cbranch_vccnz .Lzfx12
.Lzfr12:
)" R"(
Mv 120,60,120
v_cmp_eq_u32_e32 vcc, s3, v4
s_andn2_b64 s[28:29], s[60:61], vcc
s_cbranch_scc1 .Lzsp22
.Lzok22:
Mp 128,0,32
Rb 48,8,45056
v_add_f32_e32 v12, v12, v14
Mp 136,0,36
v_add_f32_e32 v13, v13, v15
s_cmp_le_i32 s57, s58
s_cbranch_scc1 .Lzmk11
.Lzmr11:
Mp 140,4,36
Rb 52,8,46080
s_add_u32 m0, s36, 32768
buffer_load_dwordx4 v[64:67], v1, s[20:23], s66 offen sc0 sc1
Mp 132,4,32
buffer_load_dwordx4 v2, s[20:23], s55 offen offset:0 sc1 lds
Mp 144,8,32
Rb 56,8,47104
Mp 152,8,36
buffer_load_dwordx4 a[128:131], v2, s[20:23], s64 offen offset:0 sc0 sc1
Mp 156,12,36
Rb 60,8,48128
s_waitcnt lgkmcnt(8)
Mp 148,12,32
buffer_load_dwordx4 a[132:135], v2, s[20:23], s64 offen offset:1024 sc0 sc1
Mp 160,16,32
Rb 0,8,49152
Mp 168,16,36
buffer_load_dwordx4 a[136:139], v2, s[20:23], s64 offen offset:2048 sc0 sc1
Mp 172,20,36
Rb 4,8,50176
v_exp_f32_e32 v48, v96
Mp 164,20,32
buffer_load_dwordx4 v2, s[20:23], s55 offen offset:1024 sc1 lds
Mp 176,24,32
Rb 8,8,51200
v_exp_f32_e32 v28, v97
Mp 184,24,36
buffer_load_dwordx4 a[140:143], v2, s[20:23], s64 offen offset:3072 sc0 sc1
Mp 188,28,36
Rb 12,8,52224
v_add_f32_e32 v14, v48, v28
s_waitcnt lgkmcnt(8)
Mp 180,28,32
buffer_load_dwordx4 a[144:147], v3, s[20:23], s64 offen offset:0 sc0 sc1
Mp 192,32,32
Rb 16,8,53248
v_exp_f32_e32 v49, v98
Mp 200,32,36
buffer_load_dwordx4 a[148:151], v3, s[20:23], s64 offen offset:1024 sc0 sc1
Cv 48,48,28
Mp 204,36,36
Rb 20,8,54272
v_add_f32_e32 v14, v14, v49
Mp 196,36,32
buffer_load_dwordx4 v2, s[20:23], s55 offen offset:2048 sc1 lds
Mp 208,40,32
Rb 24,8,55296
Mp 216,40,36
buffer_load_dwordx4 a[152:155], v3, s[20:23], s64 offen offset:2048 sc0 sc1
Mp 220,44,36
)" R"(
Rb 28,8,56320
s_waitcnt lgkmcnt(8)
Mp 212,44,32
buffer_load_dwordx4 a[156:159], v3, s[20:23], s64 offen offset:3072 sc0 sc1
Mp 224,48,32
Rb 32,8,57344
Mp 232,48,36
s_add_u32 s64, s64, 0x10000
buffer_load_dwordx4 a[160:163], v2, s[20:23], s64 offen offset:0 sc0 sc1
Mp 236,52,36
Rb 36,8,58368
v_exp_f32_e32 v29, v99
Mp 228,52,32
buffer_load_dwordx4 v2, s[20:23], s55 offen offset:3072 sc1 lds
Mp 240,56,32
Rb 40,8,59392
v_exp_f32_e32 v50, v104
Mp 248,56,36
buffer_load_dwordx4 a[164:167], v2, s[20:23], s64 offen offset:1024 sc0 sc1
Mp 252,60,36
Rb 44,8,60416
v_add_f32_e32 v14, v14, v29
s_waitcnt lgkmcnt(8)
Mp 244,60,32
buffer_load_dwordx4 a[168:171], v2, s[20:23], s64 offen offset:2048 sc0 sc1
Mp 128,0,40
Rb 48,8,61440
v_exp_f32_e32 v24, v105
Mp 136,0,44
buffer_load_dwordx4 a[172:175], v2, s[20:23], s64 offen offset:3072 sc0 sc1
Cv 49,49,29
Mp 140,4,44
Rb 52,8,62464
s_add_u32 m0, s36, 36864
Mp 132,4,40
buffer_load_dwordx4 v3, s[20:23], s55 offen offset:0 sc1 lds
v_add_f32_e32 v14, v14, v50
Mp 144,8,40
Rb 56,8,63488
v_add_f32_e32 v14, v14, v24
Mp 152,8,44
buffer_load_dwordx4 a[176:179], v3, s[20:23], s64 offen offset:0 sc0 sc1
Cv 50,50,24
Mp 156,12,44
Rb 60,8,64512
s_waitcnt lgkmcnt(8)
Mp 148,12,40
buffer_load_dwordx4 a[180:183], v3, s[20:23], s64 offen offset:1024 sc0 sc1
Mp 160,16,40
v_exp_f32_e32 v51, v106
Mp 168,16,44
buffer_load_dwordx4 a[184:187], v3, s[20:23], s64 offen offset:2048 sc0 sc1
v_add_f32_e32 v14, v14, v51
Mp 172,20,44
v_exp_f32_e32 v25, v107
s_sub_u32 s57, s57, 1
Mp 164,20,40
buffer_load_dwordx4 v3, s[20:23], s55 offen offset:1024 sc1 lds
Mp 176,24,40
v_exp_f32_e32 v52, v100
v_add_f32_e32 v14, v14, v25
Mp 184,24,44
buffer_load_dwordx4 a[188:191], v3, s[20:23], s64 offen offset:3072 sc0 sc1
Mp 188,28,44
)" R"(
v_exp_f32_e32 v26, v101
Cv 51,51,25
Mp 180,28,40
s_waitcnt vmcnt(23) lgkmcnt(0)
s_barrier
Mp 192,32,40
Rb 0,8,0
v_exp_f32_e32 v53, v102
Mp 200,32,44
Rb 4,8,16384
v_exp_f32_e32 v27, v103
Mp 204,36,44
Rb 8,8,1024
v_add_f32_e32 v15, v52, v26
Mp 196,36,40
Rb 12,8,17408
buffer_load_dwordx4 v3, s[20:23], s55 offen offset:2048 sc1 lds
Mp 208,40,40
Rb 16,8,2048
Cv 52,52,26
Mp 216,40,44
Rb 20,8,18432
v_exp_f32_e32 v54, v108
Mp 220,44,44
Rb 24,8,3072
v_add_f32_e32 v15, v15, v53
Mp 212,44,40
Rb 28,8,19456
v_exp_f32_e32 v28, v109
Mp 224,48,40
Rb 32,8,4096
v_add_f32_e32 v15, v15, v27
Mp 232,48,44
Rb 36,8,20480
v_exp_f32_e32 v55, v110
Mp 236,52,44
Rb 40,8,5120
v_exp_f32_e32 v29, v111
Mp 228,52,40
Rb 44,8,21504
buffer_load_dwordx4 v3, s[20:23], s55 offen offset:3072 sc1 lds
Mp 240,56,40
Cv 53,53,27
v_add_f32_e32 v15, v15, v54
v_add_f32_e32 v15, v15, v28
Mp 248,56,44
Cv 54,54,28
v_add_f32_e32 v15, v15, v55
Mp 252,60,44
v_add_f32_e32 v15, v15, v29
Cv 55,55,29
Mp 244,60,40
s_waitcnt vmcnt(18)
v_cmp_neq_f32_e32 vcc, v16, v66
v_cmp_neq_f32_e64 s[28:29], v20, v67
s_or_b64 vcc, vcc, s[28:29]
s_cbranch_scc1 .LzMRG
.LzMRR:
Mv 128,194,128
v_exp_f32_e32 v56, v112
Mv 136,194,132
v_exp_f32_e32 v26, v113
v_add_f32_e32 v12, v12, v64
Mv 132,192,128
s_add_u32 m0, s36, 131072
)" R"(
s_add_u32 s40, s67, 0x20000
v_add_f32_e32 v13, v13, v65
Mv 140,192,132
buffer_load_dwordx4 v2, s[20:23], s40 offen offset:0 sc1 lds
Mv 144,194,136
v_exp_f32_e32 v57, v114
v_add_f32_e32 v14, v14, v56
Mv 152,194,140
v_exp_f32_e32 v27, v115
v_add_f32_e32 v14, v14, v26
Mv 148,192,136
Cv 56,56,26
s_waitcnt vmcnt(14)
Mv 156,192,140
buffer_load_dwordx4 v2, s[20:23], s40 offen offset:1024 sc1 lds
v_add_f32_e32 v14, v14, v57
Mv 160,194,144
v_exp_f32_e32 v58, v120
v_add_f32_e32 v14, v14, v27
Mv 168,194,148
v_exp_f32_e32 v28, v121
Mv 164,192,144
v_exp_f32_e32 v59, v122
Cv 57,57,27
Mv 172,192,148
buffer_load_dwordx4 v2, s[20:23], s40 offen offset:2048 sc1 lds
v_add_f32_e32 v14, v14, v58
Mv 176,194,152
v_exp_f32_e32 v29, v123
Mv 184,194,156
v_exp_f32_e32 v60, v116
v_add_f32_e32 v14, v14, v28
Mv 180,192,152
v_exp_f32_e32 v24, v117
s_waitcnt vmcnt(11)
Mv 188,192,156
buffer_load_dwordx4 v2, s[20:23], s40 offen offset:3072 sc1 lds
Cv 58,58,28
Mv 192,194,160
v_exp_f32_e32 v61, v118
Mv 200,194,164
v_exp_f32_e32 v25, v119
v_add_f32_e32 v14, v14, v59
Mv 196,192,160
s_add_u32 m0, s36, 135168
v_exp_f32_e32 v62, v124
Mv 204,192,164
buffer_load_dwordx4 v3, s[20:23], s40 offen offset:0 sc1 lds
Mv 208,194,168
v_exp_f32_e32 v26, v125
v_add_f32_e32 v14, v14, v29
Mv 216,194,172
v_exp_f32_e32 v63, v126
Cv 59,59,29
Mv 212,192,168
v_add_f32_e32 v15, v15, v60
s_waitcnt vmcnt(7)
Mv 220,192,172
buffer_load_dwordx4 v3, s[20:23], s40 offen offset:1024 sc1 lds
v_add_f32_e32 v15, v15, v24
Mv 224,194,176
v_exp_f32_e32 v27, v127
Cv 60,60,24
Mv 232,194,180
)" R"(
v_add_f32_e32 v15, v15, v61
v_add_f32_e32 v15, v15, v25
Mv 228,192,176
Cv 61,61,25
v_add_f32_e32 v15, v15, v62
v_add_f32_e32 v15, v15, v26
Mv 236,192,180
buffer_load_dwordx4 v3, s[20:23], s40 offen offset:2048 sc1 lds
Cv 62,62,26
Mv 240,194,184
v_add_f32_e32 v15, v15, v63
v_add_f32_e32 v15, v15, v27
Mv 248,194,188
s_add_u32 s40, s67, 0x30000
Cv 63,63,27
v_max_f32_e32 v11, v14, v15
Mv 244,192,184
s_add_u32 m0, s36, 98304
v_cmp_nge_f32_e32 vcc, s62, v11
s_waitcnt lgkmcnt(8)
Mv 252,192,188
buffer_load_dwordx4 v2, s[20:23], s40 offen offset:0 sc1 lds
s_cbranch_vccnz .Lzfx13
.Lzfr13:
Mp 128,0,48
Rb 48,8,6144
v_add_f32_e32 v12, v12, v14
Mp 136,0,52
v_add_f32_e32 v13, v13, v15
v_mov_b32 v24, v12
v_mov_b32 v25, v13
Mp 136,4,60
Rb 52,8,22528
v_permlane32_swap_b32_e32 v12, v24
v_add_f32_e32 v12, v12, v24
Mp 128,4,56
buffer_load_dwordx4 v2, s[20:23], s40 offen offset:1024 sc1 lds
v_mov_b32 v24, v12
Mp 132,8,48
Rb 56,8,7168
v_permlane16_swap_b32_e32 v12, v24
Mp 140,8,52
v_add_f32_e32 v12, v12, v24
v_rcp_f32_e32 v12, v12
Mp 140,12,60
Rb 60,8,23552
v_permlane32_swap_b32_e32 v13, v25
s_waitcnt lgkmcnt(8)
Mp 132,12,56
buffer_load_dwordx4 v2, s[20:23], s40 offen offset:2048 sc1 lds
v_add_f32_e32 v13, v13, v25
Mp 144,16,48
Rb 0,8,8192
v_mov_b32 v25, v13
Mp 152,16,52
s_nop 0
v_permlane16_swap_b32_e32 v13, v25
v_add_f32_e32 v13, v13, v25
Mp 152,20,60
Rb 4,8,24576
v_rcp_f32_e32 v13, v13
Mp 144,20,56
buffer_load_dwordx4 v2, s[20:23], s40 offen offset:3072 sc1 lds
v_mul_f32_e32 v128, v12, v128
)" R"(
Mp 148,24,48
Rb 8,8,9216
v_mul_f32_e32 v129, v12, v129
Mp 156,24,52
s_add_u32 m0, s36, 102400
Cv 128,128,129
v_mul_f32_e32 v130, v12, v130
Mp 156,28,60
Rb 12,8,25600
v_mul_f32_e32 v131, v12, v131
s_waitcnt lgkmcnt(8)
Mp 148,28,56
buffer_load_dwordx4 v3, s[20:23], s40 offen offset:0 sc1 lds
Cv 129,130,131
Mp 160,32,48
Rb 16,8,10240
v_mul_f32_e32 v132, v12, v132
Mp 168,32,52
v_mul_f32_e32 v133, v12, v133
Cv 130,132,133
v_mul_f32_e32 v134, v12, v134
Mp 168,36,60
Rb 20,8,26624
v_mul_f32_e32 v135, v12, v135
Cv 131,134,135
Mp 160,36,56
buffer_load_dwordx4 v3, s[20:23], s40 offen offset:1024 sc1 lds
v_mul_f32_e32 v136, v13, v136
Mp 164,40,48
Rb 24,8,11264
v_mul_f32_e32 v137, v13, v137
Mp 172,40,52
Cv 136,136,137
v_mul_f32_e32 v138, v13, v138
v_mul_f32_e32 v139, v13, v139
Mp 172,44,60
Rb 28,8,27648
Cv 137,138,139
s_waitcnt lgkmcnt(8)
Mp 164,44,56
buffer_load_dwordx4 v3, s[20:23], s40 offen offset:2048 sc1 lds
v_mul_f32_e32 v140, v13, v140
Mp 176,48,48
Rb 32,8,12288
v_mul_f32_e32 v141, v13, v141
Mp 184,48,52
Cv 138,140,141
v_mul_f32_e32 v142, v13, v142
v_mul_f32_e32 v143, v13, v143
Mp 184,52,60
Rb 36,8,28672
Cv 139,142,143
v_mul_f32_e32 v144, v12, v144
Mp 176,52,56
v_mul_f32_e32 v145, v12, v145
Cv 144,144,145
v_mul_f32_e32 v146, v12, v146
Mp 180,56,48
Rb 40,8,13312
v_mul_f32_e32 v147, v12, v147
Mp 188,56,52
Cv 145,146,147
v_mul_f32_e32 v148, v12, v148
v_mul_f32_e32 v149, v12, v149
)" R"(
Mp 188,60,60
Rb 44,8,29696
Cv 146,148,149
s_waitcnt lgkmcnt(8)
Mp 180,60,56
v_mul_f32_e32 v150, v12, v150
v_mul_f32_e32 v151, v12, v151
Cv 147,150,151
Mp 192,0,48
Rb 48,8,14336
v_mul_f32_e32 v152, v13, v152
Mp 200,0,52
v_mul_f32_e32 v153, v13, v153
Cv 152,152,153
v_mul_f32_e32 v154, v13, v154
Mp 200,4,60
Rb 52,8,30720
v_mul_f32_e32 v155, v13, v155
Cv 153,154,155
Mp 192,4,56
v_mul_f32_e32 v156, v13, v156
v_mul_f32_e32 v157, v13, v157
Cv 154,156,157
Mp 196,8,48
Rb 56,8,15360
v_mul_f32_e32 v158, v13, v158
Mp 204,8,52
v_mul_f32_e32 v159, v13, v159
Cv 155,158,159
v_mul_f32_e32 v160, v12, v160
Mp 204,12,60
Rb 60,8,31744
v_mul_f32_e32 v161, v12, v161
s_waitcnt lgkmcnt(8)
Mp 196,12,56
Cv 160,160,161
v_mul_f32_e32 v162, v12, v162
v_mul_f32_e32 v163, v12, v163
Mp 208,16,48
Rb 0,1,0
Cv 161,162,163
Mp 216,16,52
v_mul_f32_e32 v164, v12, v164
v_mul_f32_e32 v165, v12, v165
Cv 162,164,165
Mp 216,20,60
Rb 4,1,1024
v_mul_f32_e32 v166, v12, v166
v_mul_f32_e32 v167, v12, v167
Mp 208,20,56
Cv 163,166,167
v_mul_f32_e32 v168, v13, v168
v_mul_f32_e32 v169, v13, v169
Mp 212,24,48
Rb 8,1,2048
Cv 168,168,169
Mp 220,24,52
v_mul_f32_e32 v170, v13, v170
v_mul_f32_e32 v171, v13, v171
Cv 169,170,171
Mp 220,28,60
Rb 12,1,3072
s_waitcnt vmcnt(14) lgkmcnt(4)
s_barrier
Mp 212,28,56
)" R"(
s_add_u32 s40, s67, 0x40000
v_mul_f32_e32 v172, v13, v172
v_mul_f32_e32 v173, v13, v173
Mp 224,32,48
Rb 16,1,4096
s_add_u32 m0, s36, 65536
Mp 232,32,52
buffer_load_dwordx4 v2, s[20:23], s40 offen offset:0 sc1 lds
Cv 170,172,173
Mp 232,36,60
Rb 20,1,5120
v_mul_f32_e32 v174, v13, v174
v_mul_f32_e32 v175, v13, v175
Mp 224,36,56
Cv 171,174,175
v_mul_f32_e32 v176, v12, v176
v_mul_f32_e32 v177, v12, v177
Mp 228,40,48
Rb 24,1,6144
Cv 176,176,177
s_lshl4_add_u32 s28, s34, s35
Mp 236,40,52
v_mul_f32_e32 v178, v12, v178
v_mul_f32_e32 v179, v12, v179
Cv 177,178,179
Mp 236,44,60
Rb 28,1,8192
v_mul_f32_e32 v180, v12, v180
Mp 228,44,56
buffer_load_dwordx4 v2, s[20:23], s40 offen offset:1024 sc1 lds
v_mul_f32_e32 v181, v12, v181
Mp 240,48,48
Rb 32,1,9216
Cv 178,180,181
Mp 248,48,52
v_mul_f32_e32 v182, v12, v182
v_mul_f32_e32 v183, v12, v183
Cv 179,182,183
Mp 248,52,60
Rb 36,1,10240
v_mul_f32_e32 v184, v13, v184
v_mul_f32_e32 v185, v13, v185
Mp 240,52,56
Cv 184,184,185
v_mul_f32_e32 v186, v13, v186
v_mul_f32_e32 v187, v13, v187
Mp 244,56,48
Rb 40,1,11264
Cv 185,186,187
s_lshl_b32 s64, s28, 17
Mp 252,56,52
buffer_load_dwordx4 v2, s[20:23], s40 offen offset:2048 sc1 lds
v_mul_f32_e32 v188, v13, v188
Mp 252,60,60
Rb 44,1,12288
v_mul_f32_e32 v189, v13, v189
s_mov_b32 s55, 0xc108c00
s_waitcnt lgkmcnt(8)
Mp 244,60,56
Cv 186,188,189
v_mul_f32_e32 v190, v13, v190
v_mul_f32_e32 v191, v13, v191
v_mfma_f32_16x16x32_bf16 v[32:35], a[0:3], v[128:131], 0
Rb 48,1,13312
)" R"(
Cv 187,190,191
s_add_u32 s64, s64, 0x4100000
v_mfma_f32_16x16x32_bf16 v[40:43], a[0:3], v[136:139], 0
v_mul_f32_e32 v192, v12, v192
v_mul_f32_e32 v193, v12, v193
Cv 192,192,193
v_mfma_f32_16x16x32_bf16 v[44:47], a[4:7], v[136:139], 0
Rb 52,1,14336
v_mul_f32_e32 v194, v12, v194
v_mul_f32_e32 v195, v12, v195
v_mfma_f32_16x16x32_bf16 v[36:39], a[4:7], v[128:131], 0
buffer_load_dwordx4 v2, s[20:23], s40 offen offset:3072 sc1 lds
Cv 193,194,195
v_mfma_f32_16x16x32_bf16 v[48:51], a[8:11], v[128:131], 0
Rb 56,1,16384
v_mul_f32_e32 v196, v12, v196
s_xor_b32 s66, s64, 0x202000
v_mfma_f32_16x16x32_bf16 v[56:59], a[8:11], v[136:139], 0
v_mul_f32_e32 v197, v12, v197
Cv 194,196,197
v_mul_f32_e32 v198, v12, v198
v_mfma_f32_16x16x32_bf16 v[60:63], a[12:15], v[136:139], 0
Rb 60,1,17408
v_mul_f32_e32 v199, v12, v199
s_cmp_lg_u32 s33, 0
s_cmov_b32 s55, 0x40000000
s_waitcnt lgkmcnt(8)
v_mfma_f32_16x16x32_bf16 v[52:55], a[12:15], v[128:131], 0
Cv 195,198,199
v_mul_f32_e32 v200, v13, v200
v_mul_f32_e32 v201, v13, v201
v_mov_b32 v16, 1
Mp 32,16,144
Rb 0,1,18432
s_add_u32 m0, s36, 69632
v_add_u32 v9, s36, v8
Mp 40,16,152
buffer_load_dwordx4 v3, s[20:23], s40 offen offset:0 sc1 lds
Cv 200,200,201
Mp 44,20,152
Rb 4,1,19456
v_mul_f32_e32 v202, v13, v202
v_mul_f32_e32 v203, v13, v203
Mp 36,20,144
Cv 201,202,203
v_mul_f32_e32 v204, v13, v204
v_mul_f32_e32 v205, v13, v205
Mp 48,24,144
Rb 8,1,20480
Cv 202,204,205
s_sub_u32 s66, s66, s84
Mp 56,24,152
v_mul_f32_e32 v206, v13, v206
v_mul_f32_e32 v207, v13, v207
Cv 203,206,207
Mp 60,28,152
Rb 12,1,21504
v_mul_f32_e32 v208, v12, v208
s_waitcnt lgkmcnt(8)
Mp 52,28,144
buffer_load_dwordx4 v3, s[20:23], s40 offen offset:1024 sc1 lds
v_mul_f32_e32 v209, v12, v209
Mp 32,32,160
Rb 16,1,22528
)" R"(
Cv 208,208,209
s_sub_u32 s66, s66, s84
Mp 40,32,168
v_mul_f32_e32 v210, v12, v210
v_mul_f32_e32 v211, v12, v211
Cv 209,210,211
Mp 44,36,168
Rb 20,1,24576
v_mul_f32_e32 v212, v12, v212
v_mul_f32_e32 v213, v12, v213
Mp 36,36,160
Cv 210,212,213
v_mul_f32_e32 v214, v12, v214
v_mul_f32_e32 v215, v12, v215
Mp 48,40,160
Rb 24,1,25600
Cv 211,214,215
s_lshl2_add_u32 s65, s2, s33
Mp 56,40,168
buffer_load_dwordx4 v3, s[20:23], s40 offen offset:2048 sc1 lds
v_mul_f32_e32 v216, v13, v216
Mp 60,44,168
Rb 28,1,26624
v_mul_f32_e32 v217, v13, v217
s_waitcnt lgkmcnt(8)
Mp 52,44,160
Cv 216,216,217
v_mul_f32_e32 v218, v13, v218
v_mul_f32_e32 v219, v13, v219
Mp 32,48,176
Rb 32,1,27648
Cv 217,218,219
Mp 40,48,184
v_mul_f32_e32 v220, v13, v220
v_mul_f32_e32 v221, v13, v221
Cv 218,220,221
Mp 44,52,184
Rb 36,1,28672
v_mul_f32_e32 v222, v13, v222
v_mul_f32_e32 v223, v13, v223
Mp 36,52,176
Cv 219,222,223
v_mul_f32_e32 v224, v12, v224
v_mul_f32_e32 v225, v12, v225
Mp 48,56,176
Rb 40,1,29696
Cv 224,224,225
s_lshl_b32 s65, s65, 7
Mp 56,56,184
v_mul_f32_e32 v226, v12, v226
v_mul_f32_e32 v227, v12, v227
Cv 225,226,227
Mp 60,60,184
Rb 44,1,30720
v_mul_f32_e32 v228, v12, v228
s_waitcnt lgkmcnt(8)
Mp 52,60,176
v_mul_f32_e32 v229, v12, v229
Cv 226,228,229
v_mul_f32_e32 v230, v12, v230
Mp 32,0,192
Rb 48,1,32768
v_mul_f32_e32 v231, v12, v231
s_add_u32 s65, s65, 0xc1c0000
)" R"(
buffer_atomic_add v16, v7, s[20:23], s55 offen sc0 sc1
Mp 40,0,200
Cv 227,230,231
v_mul_f32_e32 v232, v13, v232
v_mul_f32_e32 v233, v13, v233
Mp 44,4,200
Rb 52,1,33792
Cv 232,232,233
v_mul_f32_e32 v234, v13, v234
Mp 36,4,192
v_mul_f32_e32 v235, v13, v235
Cv 233,234,235
v_mul_f32_e32 v236, v13, v236
Mp 48,8,192
Rb 56,1,34816
v_mul_f32_e32 v237, v13, v237
s_xor_b32 s52, s65, 4096
Mp 56,8,200
Cv 234,236,237
v_mul_f32_e32 v238, v13, v238
v_mul_f32_e32 v239, v13, v239
Mp 60,12,200
Rb 60,1,35840
s_waitcnt vmcnt(15) lgkmcnt(4)
s_barrier
Mp 52,12,192
s_add_u32 s40, s67, 0x50000
Cv 235,238,239
v_mul_f32_e32 v240, v12, v240
Mp 32,16,208
Rb 0,1,36864
s_add_u32 m0, s36, 0
Mp 40,16,216
buffer_load_dwordx4 v2, s[20:23], s40 offen offset:0 sc1 lds
v_mul_f32_e32 v241, v12, v241
Mp 44,20,216
Rb 4,1,37888
Cv 240,240,241
v_mul_f32_e32 v242, v12, v242
Mp 36,20,208
v_mul_f32_e32 v243, v12, v243
Cv 241,242,243
v_mul_f32_e32 v244, v12, v244
Mp 48,24,208
Rb 8,1,38912
v_mul_f32_e32 v245, v12, v245
Mp 56,24,216
Cv 242,244,245
v_mul_f32_e32 v246, v12, v246
v_mul_f32_e32 v247, v12, v247
Mp 60,28,216
Rb 12,1,40960
Cv 243,246,247
Mp 52,28,208
buffer_load_dwordx4 v2, s[20:23], s40 offen offset:1024 sc1 lds
v_mul_f32_e32 v248, v13, v248
Mp 32,32,224
Rb 16,1,41984
v_mul_f32_e32 v249, v13, v249
Mp 40,32,232
Cv 248,248,249
v_mul_f32_e32 v250, v13, v250
v_mul_f32_e32 v251, v13, v251
Mp 44,36,232
Rb 20,1,43008
)" R"(
Cv 249,250,251
v_mul_f32_e32 v252, v13, v252
Mp 36,36,224
v_mul_f32_e32 v253, v13, v253
Cv 250,252,253
v_mul_f32_e32 v254, v13, v254
Mp 48,40,224
Rb 24,1,44032
v_mul_f32_e32 v255, v13, v255
Mp 56,40,232
buffer_load_dwordx4 v2, s[20:23], s40 offen offset:2048 sc1 lds
Cv 251,254,255
Mp 60,44,232
Rb 28,1,45056
s_waitcnt lgkmcnt(8)
Mp 52,44,224
Mp 32,48,240
Rb 32,1,46080
Mp 40,48,248
Mp 44,52,248
Rb 36,1,47104
Mp 36,52,240
buffer_load_dwordx4 v2, s[20:23], s40 offen offset:3072 sc1 lds
Mp 48,56,240
Rb 40,1,49152
Mp 56,56,248
Mp 60,60,248
Rb 44,1,50176
s_waitcnt lgkmcnt(8)
Mp 52,60,240
v_mfma_f32_16x16x32_bf16 v[64:67], a[0:3], v[128:131], 0
Rb 48,1,51200
s_add_u32 m0, s36, 4096
v_mfma_f32_16x16x32_bf16 v[72:75], a[0:3], v[136:139], 0
buffer_load_dwordx4 v3, s[20:23], s40 offen offset:0 sc1 lds
Mp 72,4,152
Rb 52,1,52224
Mp 64,4,144
Cv 32,32,33
Cv 33,34,35
Mp 64,8,160
Rb 56,1,53248
Cv 34,36,37
Mp 72,8,168
Cv 35,38,39
Mp 72,12,184
Rb 60,1,54272
s_waitcnt lgkmcnt(8)
Mp 64,12,176
buffer_load_dwordx4 v3, s[20:23], s40 offen offset:1024 sc1 lds
Mp 64,16,192
Rb 0,1,55296
Mp 72,16,200
buffer_store_dwordx4 v[32:35], v2, s[20:23], s64 offen offset:0 sc0 sc1
Mp 72,20,216
Rb 4,1,57344
Mp 64,20,208
Mp 64,24,224
Rb 8,1,58368
Mp 72,24,232
buffer_load_dwordx4 v3, s[20:23], s40 offen offset:2048 sc1 lds
Mp 72,28,248
Rb 12,1,59392
s_waitcnt lgkmcnt(8)
)" R"(
Mp 64,28,240
Cv 40,40,41
Cv 41,42,43
v_mfma_f32_16x16x32_bf16 v[68:71], a[32:35], v[128:131], 0
Rb 16,1,60416
Cv 42,44,45
v_mfma_f32_16x16x32_bf16 v[76:79], a[32:35], v[136:139], 0
Cv 43,46,47
Mp 76,36,152
Rb 20,1,61440
Mp 68,36,144
buffer_store_dwordx4 v[40:43], v2, s[20:23], s64 offen offset:1024 sc0 sc1
Mp 68,40,160
Rb 24,1,62464
Mp 76,40,168
Mp 76,44,184
Rb 28,1,63488
s_waitcnt lgkmcnt(8)
Mp 68,44,176
Mp 68,48,192
Rb 32,10,0
Mp 76,48,200
Cv 48,48,49
Cv 49,50,51
Mp 76,52,216
Rb 36,10,1024
Cv 50,52,53
Mp 68,52,208
Cv 51,54,55
Mp 68,56,224
Rb 40,10,2048
Mp 76,56,232
buffer_store_dwordx4 v[48:51], v2, s[20:23], s64 offen offset:2048 sc0 sc1
Mp 76,60,248
Rb 44,10,3072
s_waitcnt vmcnt(18) lgkmcnt(4)
s_barrier
Mp 68,60,240
s_add_u32 s40, s67, 0x60000
v_mfma_f32_16x16x32_bf16 v[80:83], a[0:3], v[128:131], 0
Rb 48,10,4096
s_add_u32 m0, s36, 32768
v_mfma_f32_16x16x32_bf16 v[88:91], a[0:3], v[136:139], 0
buffer_load_dwordx4 v2, s[20:23], s40 offen offset:0 sc1 lds
Mp 88,4,152
Rb 52,10,5120
Mp 80,4,144
Cv 56,56,57
Cv 57,58,59
Mp 80,8,160
Rb 56,10,6144
Cv 58,60,61
Mp 88,8,168
Cv 59,62,63
Mp 88,12,184
Rb 60,10,8192
Mp 80,12,176
buffer_load_dwordx4 v2, s[20:23], s40 offen offset:1024 sc1 lds
Mp 80,16,192
Rb 0,10,9216
Mp 88,16,200
buffer_store_dwordx4 v[56:59], v2, s[20:23], s64 offen offset:3072 sc0 sc1
Mp 88,20,216
Rb 4,10,10240
Mp 80,20,208
)" R"(
Mp 80,24,224
Rb 8,10,11264
Mp 88,24,232
buffer_load_dwordx4 v2, s[20:23], s40 offen offset:2048 sc1 lds
Mp 88,28,248
Rb 12,10,12288
s_waitcnt lgkmcnt(8)
Mp 80,28,240
Cv 64,64,65
Cv 65,66,67
v_mfma_f32_16x16x32_bf16 v[84:87], a[32:35], v[128:131], 0
Rb 16,10,13312
Cv 66,68,69
v_mfma_f32_16x16x32_bf16 v[92:95], a[32:35], v[136:139], 0
Cv 67,70,71
Mp 92,36,152
Rb 20,10,14336
Mp 84,36,144
buffer_load_dwordx4 v2, s[20:23], s40 offen offset:3072 sc1 lds
Mp 84,40,160
Rb 24,10,16384
Mp 92,40,168
buffer_store_dwordx4 v[64:67], v3, s[20:23], s64 offen offset:0 sc0 sc1
Mp 92,44,184
Rb 28,10,17408
s_waitcnt lgkmcnt(8)
Mp 84,44,176
Mp 84,48,192
Rb 32,10,18432
s_add_u32 m0, s36, 36864
Mp 92,48,200
buffer_load_dwordx4 v3, s[20:23], s40 offen offset:0 sc1 lds
Mp 92,52,216
Rb 36,10,19456
Mp 84,52,208
Cv 72,72,73
Cv 73,74,75
Mp 84,56,224
Rb 40,10,20480
Cv 74,76,77
Mp 92,56,232
Cv 75,78,79
Mp 92,60,248
Rb 44,10,21504
s_waitcnt lgkmcnt(8)
Mp 84,60,240
buffer_load_dwordx4 v3, s[20:23], s40 offen offset:1024 sc1 lds
v_mfma_f32_16x16x32_bf16 v[96:99], a[0:3], v[128:131], 0
Rb 48,10,22528
v_mfma_f32_16x16x32_bf16 v[104:107], a[0:3], v[136:139], 0
buffer_store_dwordx4 v[72:75], v3, s[20:23], s64 offen offset:1024 sc0 sc1
Mp 104,4,152
Rb 52,10,24576
Mp 96,4,144
Mp 96,8,160
Rb 56,10,25600
Mp 104,8,168
buffer_load_dwordx4 v3, s[20:23], s40 offen offset:2048 sc1 lds
Mp 104,12,184
Rb 60,10,26624
s_waitcnt lgkmcnt(8)
Mp 96,12,176
Cv 80,80,81
Cv 81,82,83
)" R"(
Mp 96,16,192
Rb 0,10,27648
Cv 82,84,85
Mp 104,16,200
Cv 83,86,87
Mp 104,20,216
Rb 4,10,28672
Mp 96,20,208
buffer_store_dwordx4 v[80:83], v3, s[20:23], s64 offen offset:2048 sc0 sc1
Mp 96,24,224
Rb 8,10,29696
Mp 104,24,232
Mp 104,28,248
Rb 12,10,30720
s_waitcnt lgkmcnt(8)
Mp 96,28,240
v_mfma_f32_16x16x32_bf16 v[100:103], a[32:35], v[128:131], 0
Rb 16,8,32768
v_mfma_f32_16x16x32_bf16 v[108:111], a[32:35], v[136:139], 0
Cv 88,88,89
Cv 89,90,91
Mp 108,36,152
Rb 20,8,33792
Cv 90,92,93
Mp 100,36,144
Cv 91,94,95
Mp 100,40,160
Rb 24,8,34816
Mp 108,40,168
buffer_store_dwordx4 v[88:91], v3, s[20:23], s64 offen offset:3072 sc0 sc1
Mp 108,44,184
Rb 28,8,35840
s_waitcnt vmcnt(23) lgkmcnt(4)
s_barrier
Mp 100,44,176
s_add_u32 s40, s67, 0x70000
Mp 100,48,192
Rb 32,8,36864
s_add_u32 m0, s36, 131072
Mp 108,48,200
buffer_load_dwordx4 v2, s[20:23], s40 offen offset:0 sc1 lds
Mp 108,52,216
Rb 36,8,37888
s_mov_b32 s8, s74
Mp 100,52,208
s_mov_b32 s9, s75
s_mov_b32 s10, 0x1c0000
Mp 100,56,224
Rb 40,8,38912
s_mov_b32 s11, 0x20000
Mp 108,56,232
v_and_b32 v174, 1, v0
v_mul_u32_u24 v174, 0xffff, v174
Mp 108,60,248
Rb 44,8,40960
Mp 100,60,240
buffer_load_dwordx4 v2, s[20:23], s40 offen offset:1024 sc1 lds
v_mfma_f32_16x16x32_bf16 v[112:115], a[0:3], v[128:131], 0
Rb 48,8,41984
v_add_u32 v174, 1, v174
v_mfma_f32_16x16x32_bf16 v[120:123], a[0:3], v[136:139], 0
v_mul_u32_u24 v174, 0x3f80, v174
v_bfe_u32 v173, v0, 1, 3
Mp 120,4,152
Rb 52,8,43008
)" R"(
v_cmp_eq_u32_e32 vcc, 0, v173
Mp 112,4,144
Cv 96,96,97
Cv 97,98,99
Mp 112,8,160
Rb 56,8,44032
Cv 98,100,101
Mp 120,8,168
buffer_load_dwordx4 v2, s[20:23], s40 offen offset:2048 sc1 lds
Mp 120,12,184
Rb 60,8,45056
s_waitcnt lgkmcnt(8)
Mp 112,12,176
Cv 99,102,103
v_cndmask_b32_e32 v132, 0, v174, vcc
Mp 112,16,192
Rb 0,8,46080
v_cmp_eq_u32_e32 vcc, 1, v173
Mp 120,16,200
s_add_u32 s64, s64, 0x10000
buffer_store_dwordx4 v[96:99], v2, s[20:23], s64 offen offset:0 sc0 sc1
Mp 120,20,216
Rb 4,8,47104
v_cndmask_b32_e32 v133, 0, v174, vcc
Mp 112,20,208
buffer_load_dwordx4 v2, s[20:23], s40 offen offset:3072 sc1 lds
Mp 112,24,224
Rb 8,8,49152
v_cmp_eq_u32_e32 vcc, 2, v173
Mp 120,24,232
s_nop 0
v_cndmask_b32_e32 v134, 0, v174, vcc
v_cmp_eq_u32_e32 vcc, 3, v173
Mp 120,28,248
Rb 12,8,50176
s_waitcnt lgkmcnt(8)
Mp 112,28,240
Cv 104,104,105
Cv 105,106,107
v_mfma_f32_16x16x32_bf16 v[116:119], a[32:35], v[128:131], 0
Rb 16,8,51200
s_add_u32 m0, s36, 135168
v_mfma_f32_16x16x32_bf16 v[124:127], a[32:35], v[136:139], 0
buffer_load_dwordx4 v3, s[20:23], s40 offen offset:0 sc1 lds
Mp 124,36,152
Rb 20,8,52224
Cv 106,108,109
Mp 116,36,144
Cv 107,110,111
v_cndmask_b32_e32 v135, 0, v174, vcc
Mp 116,40,160
Rb 24,8,53248
v_cmp_eq_u32_e32 vcc, 4, v173
Mp 124,40,168
buffer_store_dwordx4 v[104:107], v2, s[20:23], s64 offen offset:1024 sc0 sc1
Mp 124,44,184
Rb 28,8,54272
s_waitcnt lgkmcnt(8)
Mp 116,44,176
buffer_load_dwordx4 v3, s[20:23], s40 offen offset:1024 sc1 lds
Mp 116,48,192
Rb 32,8,55296
v_cndmask_b32_e32 v140, 0, v174, vcc
Mp 124,48,200
)" R"(
v_cmp_eq_u32_e32 vcc, 5, v173
v_and_b32 v167, 0xf0, v1
Mp 124,52,216
Rb 36,8,57344
v_cndmask_b32_e32 v141, 0, v174, vcc
Mp 116,52,208
v_cmp_eq_u32_e32 vcc, 6, v173
v_mul_u32_u24 v166, 56, v167
Mp 116,56,224
Rb 40,8,58368
v_cndmask_b32_e32 v142, 0, v174, vcc
Mp 124,56,232
buffer_load_dwordx4 v3, s[20:23], s40 offen offset:2048 sc1 lds
Mp 124,60,248
Rb 44,8,59392
s_waitcnt vmcnt(9) lgkmcnt(8)
buffer_store_dword v6, v7, s[20:23], s65 offen offset:0 sc0 sc1
Mp 116,60,240
v_cmp_eq_u32_e32 vcc, 7, v173
v_lshrrev_b32 v173, 4, v0
v_mfma_f32_16x16x32_bf16 v[32:35], a[0:3], v[128:131], 0
Rb 48,8,60416
v_cndmask_b32_e32 v143, 0, v174, vcc
v_mfma_f32_16x16x32_bf16 v[40:43], a[0:3], v[136:139], 0
v_lshl_or_b32 v164, v173, 16, v167
v_lshl_add_u32 v166, v173, 3, v166
Mp 40,4,152
Rb 52,8,61440
v_lshrrev_b32_e64 v173, v173, s85
Mp 32,4,144
Cv 112,112,113
Cv 113,114,115
Mp 32,8,160
Rb 56,8,62464
Cv 114,116,117
Mp 40,8,168
Cv 115,118,119
v_bfe_u32 v174, v173, 4, 1
Mp 40,12,184
Rb 60,8,63488
s_waitcnt lgkmcnt(8)
Mp 32,12,176
buffer_store_dwordx4 v[112:115], v2, s[20:23], s64 offen offset:2048 sc0 sc1
ds_write_b32 v9, v16 offset:39936
Mp 32,16,192
Rb 0,8,0
v_lshl_or_b32 v183, v174, 13, v164
Mp 40,16,200
v_and_b32 v174, 1, v173
v_lshl_or_b32 v164, v174, 13, v164
Mp 40,20,216
Rb 4,8,1024
v_and_b32 v174, 7, v0
Mp 32,20,208
v_and_b32 v165, 0x80, v1
v_lshl_or_b32 v165, v174, 8, v165
Mp 32,24,224
Rb 8,8,2048
s_mov_b32 s93, 0x1111
Mp 40,24,232
Cv 120,120,121
Cv 121,122,123
Mp 40,28,248
Rb 12,8,3072
)" R"(
s_waitcnt lgkmcnt(3)
s_barrier
Mp 32,28,240
Cv 122,124,125
Cv 123,126,127
ds_read_b32 v16, v8 offset:39936
v_mfma_f32_16x16x32_bf16 v[36:39], a[32:35], v[128:131], 0
Rb 16,8,4096
v_lshrrev_b32 v173, 2, v0
v_mfma_f32_16x16x32_bf16 v[44:47], a[32:35], v[136:139], 0
buffer_store_dwordx4 v[120:123], v2, s[20:23], s64 offen offset:3072 sc0 sc1
Mp 44,36,152
Rb 20,8,5120
v_mul_u32_u24 v173, 832, v173
Mp 36,36,144
v_add_u32 v173, v173, v1
v_lshrrev_b32 v174, 1, v0
Mp 36,40,160
Rb 24,8,6144
v_and_b32 v174, 24, v174
Mp 44,40,168
v_lshl_add_u32 v174, v167, 2, v174
s_add_u32 s59, s36, 98304
Mp 44,44,184
Rb 28,8,8192
s_waitcnt lgkmcnt(4)
Mp 36,44,176
v_readfirstlane_b32 s82, v16
v_add_u32 v174, s59, v174
Mp 36,48,192
Rb 32,8,9216
s_lshl2_add_u32 s56, s82, s33
Mp 44,48,200
s_mul_i32 s88, s56, 4682
s_lshr_b32 s58, s88, 16
Mp 44,52,216
Rb 36,8,10240
s_mul_i32 s57, s58, 14
Mp 36,52,208
s_sub_u32 s57, s56, s57
s_lshl_b32 s40, s58, 11
Mp 36,56,224
Rb 40,8,11264
Mp 44,56,232
s_cmp_gt_u32 s57, 6
s_cselect_b32 s88, 4, 0
Mp 44,60,248
Rb 44,8,12288
Mp 36,60,240
buffer_load_dword v4, v7, s[20:23], s52 offen offset:0 sc0 sc1
s_add_u32 s40, s40, s88
v_mfma_f32_16x16x32_bf16 v[48:51], a[0:3], v[128:131], 0
Rb 48,8,13312
s_add_u32 s40, s40, 0xc130000
v_mfma_f32_16x16x32_bf16 v[56:59], a[0:3], v[136:139], 0
s_lshl_b32 s55, s58, 10
s_add_u32 s55, s55, s57
Mp 56,4,152
Rb 52,8,14336
Mp 48,4,144
Cv 32,32,33
Cv 33,34,35
Mp 48,8,160
Rb 56,8,16384
Cv 34,36,37
)" R"(
Mp 56,8,168
Cv 35,38,39
Mp 56,12,184
Rb 60,8,17408
s_waitcnt lgkmcnt(8)
Mp 48,12,176
buffer_store_dwordx4 v[32:35], v3, s[20:23], s64 offen offset:0 sc0 sc1
Mp 48,16,192
Rb 0,8,18432
Mp 56,16,200
s_cmp_gt_u32 s57, 6
s_cselect_b32 s88, 9, 0
Mp 56,20,216
Rb 4,8,19456
s_add_u32 s55, s55, s88
Mp 48,20,208
s_and_b32 s88, s58, 2
s_lshl_b32 s88, s88, 12
Mp 48,24,224
Rb 8,8,20480
v_xor_b32 v164, s88, v164
Mp 56,24,232
Cv 40,40,41
Cv 41,42,43
Mp 56,28,248
Rb 12,8,21504
s_waitcnt lgkmcnt(8)
Mp 48,28,240
Cv 42,44,45
Cv 43,46,47
v_xor_b32 v183, s88, v183
v_mfma_f32_16x16x32_bf16 v[52:55], a[32:35], v[128:131], 0
Rb 16,8,22528
s_lshl_b32 s55, s55, 10
v_mfma_f32_16x16x32_bf16 v[60:63], a[32:35], v[136:139], 0
buffer_store_dwordx4 v[40:43], v3, s[20:23], s64 offen offset:1024 sc0 sc1
Mp 60,36,152
Rb 20,8,24576
s_add_u32 s55, s55, 0x8000000
Mp 52,36,144
s_and_b32 s88, s57, 1
s_lshl1_add_u32 s58, s58, s88
Mp 52,40,160
Rb 24,8,25600
s_lshl_b32 s88, s58, 8
Mp 60,40,168
v_add_u32 v167, s88, v167
s_mul_i32 s59, s58, 14336
Mp 60,44,184
Rb 28,8,26624
s_waitcnt lgkmcnt(8)
Mp 52,44,176
s_lshr_b32 s88, s57, 1
s_lshl_b32 s88, s88, 6
s_add_u32 s88, s88, s59
Mp 52,48,192
Rb 32,8,27648
v_add_u32 v166, s88, v166
Mp 60,48,200
s_mov_b32 s57, 0
s_add_u32 m0, s36, 98304
Mp 60,52,216
Rb 36,8,28672
Mp 52,52,208
)" R"(
buffer_load_dwordx4 v173, s[12:15], s88 offen offset:0 lds
Mp 52,56,224
Rb 40,8,29696
s_add_u32 m0, s36, 98880
Mp 60,56,232
buffer_load_dwordx4 v173, s[12:15], s88 offen offset:448 lds
Mp 60,60,248
Rb 44,8,30720
s_waitcnt lgkmcnt(8)
Mp 52,60,240
s_mov_b32 s88, 0xc100008
buffer_load_dword v167, v167, s[20:23], s88 offen sc0 sc1
v_mfma_f32_16x16x32_bf16 v[64:67], a[0:3], v[128:131], 0
Rb 48,1,0
v_mfma_f32_16x16x32_bf16 v[72:75], a[0:3], v[136:139], 0
Mp 72,4,152
Rb 52,1,1024
Mp 64,4,144
Mp 64,8,160
Rb 56,1,2048
Mp 72,8,168
Mp 72,12,184
Rb 60,1,3072
s_waitcnt vmcnt(5) lgkmcnt(8)
s_barrier
v_cmp_eq_u32_e32 vcc, s3, v4
s_andn2_b64 s[28:29], 1, vcc
s_cbranch_scc1 .Lzsp23
.Lzok23:
Mp 64,12,176
buffer_load_dwordx4 a[64:67], v2, s[20:23], s66 offen offset:0 sc0 sc1
Mp 64,16,192
Rb 0,1,4096
Mp 72,16,200
Mp 72,20,216
Rb 4,1,5120
Mp 64,20,208
buffer_load_dwordx4 a[68:71], v2, s[20:23], s66 offen offset:1024 sc0 sc1
Mp 64,24,224
Rb 8,1,6144
Mp 72,24,232
Mp 72,28,248
Rb 12,1,8192
s_waitcnt lgkmcnt(8)
Mp 64,28,240
buffer_load_dwordx4 a[72:75], v2, s[20:23], s66 offen offset:2048 sc0 sc1
v_mfma_f32_16x16x32_bf16 v[68:71], a[32:35], v[128:131], 0
Rb 16,1,9216
v_mfma_f32_16x16x32_bf16 v[76:79], a[32:35], v[136:139], 0
Mp 76,36,152
Rb 20,1,10240
Mp 68,36,144
buffer_load_dwordx4 a[76:79], v2, s[20:23], s66 offen offset:3072 sc0 sc1
Mp 68,40,160
Rb 24,1,11264
Mp 76,40,168
Mp 76,44,184
Rb 28,1,12288
s_waitcnt vmcnt(7) lgkmcnt(8)
buffer_store_dword v6, v7, s[20:23], s65 offen offset:4 sc0 sc1
Mp 68,44,176
buffer_load_dwordx4 a[80:83], v3, s[20:23], s66 offen offset:0 sc0 sc1
Mp 68,48,192
Rb 32,1,13312
Mp 76,48,200
)" R"(
Mp 76,52,216
Rb 36,1,14336
Mp 68,52,208
buffer_load_dwordx4 a[84:87], v3, s[20:23], s66 offen offset:1024 sc0 sc1
Mp 68,56,224
Rb 40,1,16384
Mp 76,56,232
Mp 76,60,248
Rb 44,1,17408
s_waitcnt lgkmcnt(8)
Mp 68,60,240
buffer_load_dwordx4 a[88:91], v3, s[20:23], s66 offen offset:2048 sc0 sc1
v_mfma_f32_16x16x32_bf16 v[80:83], a[0:3], v[128:131], 0
Rb 48,1,18432
v_mfma_f32_16x16x32_bf16 v[88:91], a[0:3], v[136:139], 0
Mp 88,4,152
Rb 52,1,19456
Mp 80,4,144
buffer_load_dwordx4 a[92:95], v3, s[20:23], s66 offen offset:3072 sc0 sc1
Mp 80,8,160
Rb 56,1,20480
Mp 88,8,168
Mp 88,12,184
Rb 60,1,21504
s_waitcnt lgkmcnt(8)
Mp 80,12,176
Mp 80,16,192
Rb 0,1,22528
Mp 88,16,200
Mp 88,20,216
Rb 4,1,24576
Mp 80,20,208
Mp 80,24,224
Rb 8,1,25600
Mp 88,24,232
Mp 88,28,248
Rb 12,1,26624
s_waitcnt lgkmcnt(8)
Mp 80,28,240
v_mfma_f32_16x16x32_bf16 v[84:87], a[32:35], v[128:131], 0
Rb 16,1,27648
v_mfma_f32_16x16x32_bf16 v[92:95], a[32:35], v[136:139], 0
Mp 92,36,152
Rb 20,1,28672
Mp 84,36,144
Mp 84,40,160
Rb 24,1,29696
Mp 92,40,168
Mp 92,44,184
Rb 28,1,30720
s_waitcnt lgkmcnt(8)
Mp 84,44,176
buffer_load_dword v4, v7, s[20:23], s52 offen offset:4 sc0 sc1
Mp 84,48,192
Rb 32,1,32768
Mp 92,48,200
Mp 92,52,216
Rb 36,1,33792
Mp 84,52,208
Mp 84,56,224
Rb 40,1,34816
Mp 92,56,232
Mp 92,60,248
Rb 44,1,35840
)" R"(
s_waitcnt vmcnt(6)
s_barrier
Mp 84,60,240
Mv 48,194,64
Mv 56,194,68
Mv 52,192,64
Mv 60,192,68
Mv 64,194,72
Mv 72,194,76
Mv 68,192,72
s_waitcnt vmcnt(3) lgkmcnt(10)
Mv 76,192,76
Cv 48,48,49
Cv 49,50,51
Mv 80,194,80
Cv 50,52,53
Cv 51,54,55
Mv 88,194,84
s_add_u32 s29, s73, 0x0
buffer_store_dwordx4 v[48:51], v1, s[20:23], s29 offen sc0 sc1
Mv 84,192,80
Mv 92,192,84
v_mfma_f32_16x16x32_bf16 v[96:99], a[0:3], v[128:131], 0
Rb 48,1,36864
v_mfma_f32_16x16x32_bf16 v[104:107], a[0:3], v[136:139], 0
Mp 104,4,152
Rb 52,1,37888
s_waitcnt lgkmcnt(8)
Mp 96,4,144
Cv 56,56,57
Cv 57,58,59
ds_read_b64 v[188:189], v174 offset:0
Mp 96,8,160
Rb 56,1,38912
Cv 58,60,61
Mp 104,8,168
Cv 59,62,63
ds_read_b64 v[190:191], v174 offset:32
Mp 104,12,184
Rb 60,1,40960
Mp 96,12,176
s_add_u32 s29, s73, 0x400
buffer_store_dwordx4 v[56:59], v1, s[20:23], s29 offen sc0 sc1
ds_read_b64 v[196:197], v174 offset:1024
Mp 96,16,192
Rb 0,1,41984
Mp 104,16,200
ds_read_b64 v[198:199], v174 offset:1056
Mp 104,20,216
Rb 4,1,43008
s_waitcnt lgkmcnt(12)
Mp 96,20,208
Mp 96,24,224
Rb 8,1,44032
Mp 104,24,232
Cv 64,64,65
Cv 65,66,67
Mp 104,28,248
Rb 12,1,45056
Cv 66,68,69
Mp 96,28,240
Cv 67,70,71
v_mfma_f32_16x16x32_bf16 v[100:103], a[32:35], v[128:131], 0
Rb 16,1,46080
v_mfma_f32_16x16x32_bf16 v[108:111], a[32:35], v[136:139], 0
)" R"(
s_add_u32 s29, s73, 0x800
buffer_store_dwordx4 v[64:67], v1, s[20:23], s29 offen sc0 sc1
Mp 108,36,152
Rb 20,1,47104
s_waitcnt vmcnt(3) lgkmcnt(11)
v_cmp_eq_u32_e32 vcc, s3, v4
s_andn2_b64 s[28:29], 1, vcc
s_cbranch_scc1 .Lzsp24
.Lzok24:
Mp 100,36,144
s_add_u32 s66, s66, 0x10000
buffer_load_dwordx4 a[96:99], v2, s[20:23], s66 offen offset:0 sc0 sc1
Mp 100,40,160
Rb 24,1,49152
v_sub_f32_e32 v172, 1.0, v167
Mp 108,40,168
Cv 172,172,172
v_lshlrev_b32 v172, 16, v172
Mp 108,44,184
Rb 28,1,50176
v_lshlrev_b32 v204, 16, v188
Mp 100,44,176
buffer_load_dwordx4 a[100:103], v2, s[20:23], s66 offen offset:1024 sc0 sc1
Mp 100,48,192
Rb 32,1,51200
Cv 72,72,73
Mp 108,48,200
Cv 73,74,75
Cv 74,76,77
Mp 108,52,216
Rb 36,1,52224
s_waitcnt lgkmcnt(12)
Mp 100,52,208
buffer_load_dwordx4 a[104:107], v2, s[20:23], s66 offen offset:2048 sc0 sc1
Mp 100,56,224
Rb 40,1,53248
Cv 75,78,79
Mp 108,56,232
s_add_u32 s29, s73, 0xc00
buffer_store_dwordx4 v[72:75], v1, s[20:23], s29 offen sc0 sc1
Mp 108,60,248
Rb 44,1,54272
v_and_b32 v205, 0xffff0000, v188
Mp 100,60,240
buffer_load_dwordx4 a[108:111], v2, s[20:23], s66 offen offset:3072 sc0 sc1
Mv 96,194,88
v_mul_f32_e32 v204, v172, v204
v_mul_f32_e32 v205, v172, v205
Mv 104,194,92
Cv 205,204,205
v_lshlrev_b32 v204, 16, v205
Mv 100,192,88
v_and_b32 v205, 0xffff0000, v205
s_waitcnt lgkmcnt(8)
Mv 108,192,92
buffer_load_dwordx4 a[112:115], v3, s[20:23], s66 offen offset:0 sc0 sc1
v_mfma_f32_16x16x32_bf16 v[112:115], a[0:3], v[128:131], 0
Rb 48,1,55296
Cv 80,80,81
v_mfma_f32_16x16x32_bf16 v[120:123], a[0:3], v[136:139], 0
Cv 81,82,83
Cv 82,84,85
Mp 120,4,152
Rb 52,1,57344
)" R"(
Cv 83,86,87
Mp 112,4,144
buffer_load_dwordx4 a[116:119], v3, s[20:23], s66 offen offset:1024 sc0 sc1
Mp 112,8,160
Rb 56,1,58368
v_lshlrev_b32 v206, 16, v189
Mp 120,8,168
s_add_u32 s29, s73, 0x1000
buffer_store_dwordx4 v[80:83], v1, s[20:23], s29 offen sc0 sc1
Mp 120,12,184
Rb 60,1,59392
s_waitcnt lgkmcnt(8)
Mp 112,12,176
v_and_b32 v207, 0xffff0000, v189
v_mul_f32_e32 v206, v172, v206
Mp 112,16,192
Rb 0,1,60416
v_mul_f32_e32 v207, v172, v207
Mp 120,16,200
Cv 207,206,207
v_lshlrev_b32 v206, 16, v207
Mp 120,20,216
Rb 4,1,61440
v_and_b32 v207, 0xffff0000, v207
Mp 112,20,208
Cv 88,88,89
Cv 89,90,91
Mp 112,24,224
Rb 8,1,62464
Cv 90,92,93
Mp 120,24,232
Cv 91,94,95
v_lshlrev_b32 v212, 16, v190
Mp 120,28,248
Rb 12,1,63488
s_waitcnt lgkmcnt(8)
Mp 112,28,240
s_add_u32 s29, s73, 0x1400
buffer_store_dwordx4 v[88:91], v1, s[20:23], s29 offen sc0 sc1
v_mfma_f32_16x16x32_bf16 v[116:119], a[32:35], v[128:131], 0
Rb 16,10,0
v_and_b32 v213, 0xffff0000, v190
v_mfma_f32_16x16x32_bf16 v[124:127], a[32:35], v[136:139], 0
v_mul_f32_e32 v212, v172, v212
v_mul_f32_e32 v213, v172, v213
Mp 124,36,152
Rb 20,10,1024
Cv 213,212,213
Mp 116,36,144
v_lshlrev_b32 v212, 16, v213
v_and_b32 v213, 0xffff0000, v213
Mp 116,40,160
Rb 24,10,2048
v_lshlrev_b32 v214, 16, v191
Mp 124,40,168
Cv 96,96,97
Cv 97,98,99
Mp 124,44,184
Rb 28,10,3072
s_waitcnt lgkmcnt(8)
Mp 116,44,176
Cv 98,100,101
Cv 99,102,103
Mp 116,48,192
)" R"(
Rb 32,10,4096
v_and_b32 v215, 0xffff0000, v191
Mp 124,48,200
s_add_u32 s29, s73, 0x1800
buffer_store_dwordx4 v[96:99], v1, s[20:23], s29 offen sc0 sc1
Mp 124,52,216
Rb 36,10,5120
v_mul_f32_e32 v214, v172, v214
Mp 116,52,208
v_mul_f32_e32 v215, v172, v215
Cv 215,214,215
Mp 116,56,224
Rb 40,10,6144
v_lshlrev_b32 v214, 16, v215
Mp 124,56,232
v_and_b32 v215, 0xffff0000, v215
v_lshlrev_b32 v220, 16, v196
Mp 124,60,248
Rb 44,10,8192
s_waitcnt vmcnt(8) lgkmcnt(10)
Mp 116,60,240
Cv 104,104,105
Cv 105,106,107
Mv 112,194,96
Cv 106,108,109
Cv 107,110,111
Mv 120,194,100
s_add_u32 s29, s73, 0x4000
buffer_store_dwordx4 v[104:107], v1, s[20:23], s29 offen sc0 sc1
Mv 116,192,96
v_and_b32 v221, 0xffff0000, v196
v_mul_f32_e32 v220, v172, v220
Mv 124,192,100
v_mul_f32_e32 v221, v172, v221
Cv 221,220,221
v_mfma_f32_16x16x32_bf16 v[32:35], a[0:3], v[128:131], 0
Rb 48,10,9216
v_lshlrev_b32 v220, 16, v221
v_mfma_f32_16x16x32_bf16 v[40:43], a[0:3], v[136:139], 0
v_and_b32 v221, 0xffff0000, v221
v_lshlrev_b32 v222, 16, v197
Mp 40,4,152
Rb 52,10,10240
s_waitcnt lgkmcnt(8)
Mp 32,4,144
Cv 112,112,113
Cv 113,114,115
Mp 32,8,160
Rb 56,10,11264
Cv 114,116,117
Mp 40,8,168
Cv 115,118,119
v_and_b32 v223, 0xffff0000, v197
Mp 40,12,184
Rb 60,10,12288
v_mul_f32_e32 v222, v172, v222
Mp 32,12,176
s_add_u32 s29, s73, 0x4400
buffer_store_dwordx4 v[112:115], v1, s[20:23], s29 offen sc0 sc1
Mp 32,16,192
Rb 0,10,13312
v_mul_f32_e32 v223, v172, v223
Mp 40,16,200
Cv 223,222,223
)" R"(
v_lshlrev_b32 v222, 16, v223
Mp 40,20,216
Rb 4,10,14336
s_waitcnt lgkmcnt(8)
Mp 32,20,208
v_and_b32 v223, 0xffff0000, v223
v_lshlrev_b32 v228, 16, v198
Mp 32,24,224
Rb 8,10,16384
v_and_b32 v229, 0xffff0000, v198
Mp 40,24,232
Cv 120,120,121
Cv 121,122,123
Mp 40,28,248
Rb 12,10,17408
Cv 122,124,125
Mp 32,28,240
Cv 123,126,127
v_mul_f32_e32 v228, v172, v228
v_mfma_f32_16x16x32_bf16 v[36:39], a[32:35], v[128:131], 0
Rb 16,10,18432
v_mul_f32_e32 v229, v172, v229
v_mfma_f32_16x16x32_bf16 v[44:47], a[32:35], v[136:139], 0
s_add_u32 s29, s73, 0x4800
buffer_store_dwordx4 v[120:123], v1, s[20:23], s29 offen sc0 sc1
Mp 44,36,152
Rb 20,10,19456
s_waitcnt lgkmcnt(8)
Mp 36,36,144
Cv 229,228,229
v_lshlrev_b32 v228, 16, v229
Mp 36,40,160
Rb 24,10,20480
v_and_b32 v229, 0xffff0000, v229
Mp 44,40,168
v_lshlrev_b32 v230, 16, v199
v_and_b32 v231, 0xffff0000, v199
Mp 44,44,184
Rb 28,10,21504
v_mul_f32_e32 v230, v172, v230
Mp 36,44,176
v_mul_f32_e32 v231, v172, v231
Cv 231,230,231
Mp 36,48,192
Rb 32,10,22528
v_lshlrev_b32 v230, 16, v231
Mp 44,48,200
v_and_b32 v231, 0xffff0000, v231
Mp 44,52,216
Rb 36,10,24576
s_waitcnt vmcnt(3) lgkmcnt(10)
buffer_store_dword v6, v7, s[20:23], s68 offen offset:0 sc0 sc1
Mp 36,52,208
Mp 36,56,224
Rb 40,10,25600
Mp 44,56,232
Mp 44,60,248
Rb 44,10,26624
Mp 36,60,240
Mv 32,194,104
Mv 40,194,108
Mv 36,192,104
s_waitcnt lgkmcnt(8)
Mv 44,192,108
)" R"(
v_mfma_f32_16x16x32_bf16 v[48:51], a[0:3], v[128:131], 0
Rb 48,10,27648
v_mfma_f32_16x16x32_bf16 v[56:59], a[0:3], v[136:139], 0
Mp 56,4,152
Rb 52,10,28672
Mp 48,4,144
Cv 32,32,33
Cv 33,34,35
Mp 48,8,160
Rb 56,10,29696
Cv 34,36,37
Mp 56,8,168
Cv 35,38,39
Mp 56,12,184
Rb 60,10,30720
s_waitcnt lgkmcnt(8)
Mp 48,12,176
s_add_u32 s29, s73, 0x4c00
buffer_store_dwordx4 v[32:35], v1, s[20:23], s29 offen sc0 sc1
Mp 48,16,192
Mp 56,16,200
Mp 56,20,216
Mp 48,20,208
Mp 48,24,224
Mp 56,24,232
Cv 40,40,41
Cv 41,42,43
buffer_store_dword v6, v7, s[20:23], s68 offen offset:4 sc0 sc1
Mp 56,28,248
Cv 42,44,45
s_waitcnt lgkmcnt(4)
Mp 48,28,240
Cv 43,46,47
v_mfma_f32_16x16x32_bf16 v[52:55], a[32:35], v[128:131], 0
s_add_u32 s29, s73, 0x5000
buffer_store_dwordx4 v[40:43], v1, s[20:23], s29 offen sc0 sc1
v_mfma_f32_16x16x32_bf16 v[60:63], a[32:35], v[136:139], 0
buffer_load_dword v4, v165, s[20:23], s40 offen sc0 sc1
Mp 60,36,152
Mp 52,36,144
Mp 52,40,160
Mp 60,40,168
Mp 60,44,184
s_waitcnt lgkmcnt(0)
Mp 52,44,176
Mp 52,48,192
Mp 60,48,200
Mp 60,52,216
Mp 52,52,208
Mp 52,56,224
Mp 60,56,232
Mp 60,60,248
Mp 52,60,240
Mv 48,194,112
Mv 56,194,116
Mv 52,192,112
Mv 60,192,116
s_nop 7
Cv 48,48,49
Cv 49,50,51
Cv 50,52,53
Cv 51,54,55
s_add_u32 s29, s73, 0x5400
buffer_store_dwordx4 v[48:51], v1, s[20:23], s29 offen sc0 sc1
)" R"(
Cv 56,56,57
Cv 57,58,59
Cv 58,60,61
Cv 59,62,63
s_add_u32 s29, s73, 0x5800
buffer_store_dwordx4 v[56:59], v1, s[20:23], s29 offen sc0 sc1
.LzEND:
s_cmp_gt_u32 s82, 223
s_cbranch_scc1 .LzFIN
s_cmp_lt_u32 s82, 14
s_cbranch_scc1 .LzBB
.LzBBR:
s_waitcnt vmcnt(2)
.LzS0:
v_cmp_ne_u32_e32 vcc, s3, v4
s_lshr_b32 s28, vcc_lo, 1
s_or_b32 s28, s28, vcc_lo
s_lshr_b32 s29, s28, 2
s_or_b32 s28, s28, s29
s_andn2_b32 s28, s93, s28
s_sub_u32 s53, s53, 1
s_cselect_b32 s28, s93, s28
s_cmov_b32 s53, 0
s_xor_b32 s93, s93, s28
s_or_b32 s57, s57, s28
s_cbranch_scc1 .LzS2
s_sleep 4
s_branch .LzSk3
.LzS2:
s_bitcmp0_b32 s57, 0
s_cbranch_scc1 .LzSk0
s_add_u32 s29, s55, 0x0
buffer_load_dwordx4 a[128:131], v164, s[20:23], s29 offen offset:0 sc0 sc1
buffer_load_dwordx4 a[132:135], v164, s[20:23], s29 offen offset:256 sc0 sc1
buffer_load_dwordx4 a[136:139], v164, s[20:23], s29 offen offset:512 sc0 sc1
buffer_load_dwordx4 a[140:143], v164, s[20:23], s29 offen offset:768 sc0 sc1
.LzSk0:
s_bitcmp0_b32 s57, 4
s_cbranch_scc1 .LzSk1
s_add_u32 s29, s55, 0x40000
buffer_load_dwordx4 a[144:147], v183, s[20:23], s29 offen offset:0 sc0 sc1
buffer_load_dwordx4 a[148:151], v183, s[20:23], s29 offen offset:256 sc0 sc1
buffer_load_dwordx4 a[152:155], v183, s[20:23], s29 offen offset:512 sc0 sc1
buffer_load_dwordx4 a[156:159], v183, s[20:23], s29 offen offset:768 sc0 sc1
.LzSk1:
s_bitcmp0_b32 s57, 8
s_cbranch_scc1 .LzSk2
s_add_u32 s29, s55, 0x8000
buffer_load_dwordx4 a[160:163], v164, s[20:23], s29 offen offset:0 sc0 sc1
buffer_load_dwordx4 a[164:167], v164, s[20:23], s29 offen offset:256 sc0 sc1
buffer_load_dwordx4 a[168:171], v164, s[20:23], s29 offen offset:512 sc0 sc1
buffer_load_dwordx4 a[172:175], v164, s[20:23], s29 offen offset:768 sc0 sc1
.LzSk2:
s_bitcmp0_b32 s57, 12
s_cbranch_scc1 .LzSk3
s_add_u32 s29, s55, 0x48000
buffer_load_dwordx4 a[176:179], v183, s[20:23], s29 offen offset:0 sc0 sc1
buffer_load_dwordx4 a[180:183], v183, s[20:23], s29 offen offset:256 sc0 sc1
buffer_load_dwordx4 a[184:187], v183, s[20:23], s29 offen offset:512 sc0 sc1
buffer_load_dwordx4 a[188:191], v183, s[20:23], s29 offen offset:768 sc0 sc1
.LzSk3:
s_cmp_eq_u32 s93, 0
s_cbranch_scc1 .LzS1
buffer_load_dword v4, v165, s[20:23], s40 offen sc0 sc1
)" R"(
.LzS1:
s_waitcnt vmcnt(0)
s_bitcmp0_b32 s57, 0
s_cbranch_scc1 .LzSv0
v_accvgpr_read_b32 v173, a131
v_accvgpr_read_b32 v174, a135
v_accvgpr_read_b32 v175, a139
v_accvgpr_read_b32 v180, a143
v_max3_u32 v173, v173, v174, v175
v_max_u32 v173, v173, v180
v_cmp_eq_u32_e32 vcc, -1, v173
s_cbranch_vccnz .LzSv0
s_bitset0_b32 s57, 0
.LzSv0:
s_bitcmp0_b32 s57, 4
s_cbranch_scc1 .LzSv1
v_accvgpr_read_b32 v173, a147
v_accvgpr_read_b32 v174, a151
v_accvgpr_read_b32 v175, a155
v_accvgpr_read_b32 v180, a159
v_max3_u32 v173, v173, v174, v175
v_max_u32 v173, v173, v180
v_cmp_eq_u32_e32 vcc, -1, v173
s_cbranch_vccnz .LzSv1
s_bitset0_b32 s57, 4
.LzSv1:
s_bitcmp0_b32 s57, 8
s_cbranch_scc1 .LzSv2
v_accvgpr_read_b32 v173, a163
v_accvgpr_read_b32 v174, a167
v_accvgpr_read_b32 v175, a171
v_accvgpr_read_b32 v180, a175
v_max3_u32 v173, v173, v174, v175
v_max_u32 v173, v173, v180
v_cmp_eq_u32_e32 vcc, -1, v173
s_cbranch_vccnz .LzSv2
s_bitset0_b32 s57, 8
.LzSv2:
s_bitcmp0_b32 s57, 12
s_cbranch_scc1 .LzSv3
v_accvgpr_read_b32 v173, a179
v_accvgpr_read_b32 v174, a183
v_accvgpr_read_b32 v175, a187
v_accvgpr_read_b32 v180, a191
v_max3_u32 v173, v173, v174, v175
v_max_u32 v173, v173, v180
v_cmp_eq_u32_e32 vcc, -1, v173
s_cbranch_vccnz .LzSv3
s_bitset0_b32 s57, 12
.LzSv3:
s_cmp_eq_u32 s53, 0
s_cmov_b32 s57, 0
s_or_b32 s28, s57, s93
s_cbranch_scc1 .LzS0
s_bfe_u32 s28, s63, 0x70004
s_cmp_eq_u32 s28, s58
s_cbranch_scc1 .LzRT
s_bfe_u32 s28, s63, 0x70014
s_cmp_eq_u32 s28, s58
s_cbranch_scc1 .LzRT
.LzRT3:
v_mfma_f32_16x16x32_bf16 v[80:83], v[132:135], a[128:131], 0
v_mfma_f32_16x16x32_bf16 v[88:91], v[132:135], a[160:163], 0
v_mfma_f32_16x16x32_bf16 v[84:87], v[132:135], a[136:139], 0
)" R"(
v_mfma_f32_16x16x32_bf16 v[92:95], v[132:135], a[168:171], 0
v_mfma_f32_16x16x32_bf16 v[80:83], v[140:143], a[132:135], v[80:83]
v_mfma_f32_16x16x32_bf16 v[88:91], v[140:143], a[164:167], v[88:91]
v_mfma_f32_16x16x32_bf16 v[84:87], v[140:143], a[140:143], v[84:87]
v_mfma_f32_16x16x32_bf16 v[92:95], v[140:143], a[172:175], v[92:95]
v_mfma_f32_16x16x32_bf16 v[80:83], v[132:135], a[144:147], v[80:83]
v_mfma_f32_16x16x32_bf16 v[88:91], v[132:135], a[176:179], v[88:91]
v_mfma_f32_16x16x32_bf16 v[84:87], v[132:135], a[152:155], v[84:87]
v_mfma_f32_16x16x32_bf16 v[92:95], v[132:135], a[184:187], v[92:95]
v_mfma_f32_16x16x32_bf16 v[80:83], v[140:143], a[148:151], v[80:83]
v_mfma_f32_16x16x32_bf16 v[88:91], v[140:143], a[180:183], v[88:91]
v_mfma_f32_16x16x32_bf16 v[84:87], v[140:143], a[156:159], v[84:87]
v_mfma_f32_16x16x32_bf16 v[92:95], v[140:143], a[188:191], v[92:95]
v_lshrrev_b32 v173, 4, v0
v_and_b32 v173, 1, v173
v_mad_u32_u24 v166, v173, 24, v166
s_nop 1
Cv 81,80,81
v_lshlrev_b32 v80, 16, v81
v_and_b32 v81, 0xffff0000, v81
Cv 83,82,83
v_lshlrev_b32 v82, 16, v83
v_and_b32 v83, 0xffff0000, v83
Cv 89,88,89
v_lshlrev_b32 v88, 16, v89
v_and_b32 v89, 0xffff0000, v89
Cv 91,90,91
v_lshlrev_b32 v90, 16, v91
v_and_b32 v91, 0xffff0000, v91
Cv 85,84,85
v_lshlrev_b32 v84, 16, v85
v_and_b32 v85, 0xffff0000, v85
Cv 87,86,87
v_lshlrev_b32 v86, 16, v87
v_and_b32 v87, 0xffff0000, v87
Cv 93,92,93
v_lshlrev_b32 v92, 16, v93
v_and_b32 v93, 0xffff0000, v93
Cv 95,94,95
v_lshlrev_b32 v94, 16, v95
v_and_b32 v95, 0xffff0000, v95
v_mul_f32_e32 v80, v167, v80
v_mul_f32_e32 v81, v167, v81
v_mul_f32_e32 v82, v167, v82
v_mul_f32_e32 v83, v167, v83
v_mul_f32_e32 v88, v167, v88
v_mul_f32_e32 v89, v167, v89
v_mul_f32_e32 v90, v167, v90
v_mul_f32_e32 v91, v167, v91
v_mul_f32_e32 v84, v167, v84
v_mul_f32_e32 v85, v167, v85
v_mul_f32_e32 v86, v167, v86
v_mul_f32_e32 v87, v167, v87
v_mul_f32_e32 v92, v167, v92
v_mul_f32_e32 v93, v167, v93
v_mul_f32_e32 v94, v167, v94
v_mul_f32_e32 v95, v167, v95
Cv 81,80,81
v_lshlrev_b32 v80, 16, v81
v_and_b32 v81, 0xffff0000, v81
Cv 83,82,83
v_lshlrev_b32 v82, 16, v83
v_and_b32 v83, 0xffff0000, v83
Cv 89,88,89
)" R"(
v_lshlrev_b32 v88, 16, v89
v_and_b32 v89, 0xffff0000, v89
Cv 91,90,91
v_lshlrev_b32 v90, 16, v91
v_and_b32 v91, 0xffff0000, v91
Cv 85,84,85
v_lshlrev_b32 v84, 16, v85
v_and_b32 v85, 0xffff0000, v85
Cv 87,86,87
v_lshlrev_b32 v86, 16, v87
v_and_b32 v87, 0xffff0000, v87
Cv 93,92,93
v_lshlrev_b32 v92, 16, v93
v_and_b32 v93, 0xffff0000, v93
Cv 95,94,95
v_lshlrev_b32 v94, 16, v95
v_and_b32 v95, 0xffff0000, v95
v_add_f32_e32 v80, v80, v204
v_add_f32_e32 v81, v81, v205
v_add_f32_e32 v82, v82, v206
v_add_f32_e32 v83, v83, v207
v_add_f32_e32 v88, v88, v220
v_add_f32_e32 v89, v89, v221
v_add_f32_e32 v90, v90, v222
v_add_f32_e32 v91, v91, v223
v_add_f32_e32 v84, v84, v212
v_add_f32_e32 v85, v85, v213
v_add_f32_e32 v86, v86, v214
v_add_f32_e32 v87, v87, v215
v_add_f32_e32 v92, v92, v228
v_add_f32_e32 v93, v93, v229
v_add_f32_e32 v94, v94, v230
v_add_f32_e32 v95, v95, v231
Cv 64,80,81
Cv 65,82,83
Cv 66,84,85
Cv 67,86,87
Cv 68,88,89
Cv 69,90,91
Cv 70,92,93
Cv 71,94,95
v_permlane16_swap_b32_e32 v64, v66
v_permlane16_swap_b32_e32 v65, v67
buffer_store_dwordx4 v[64:67], v166, s[8:11], 0 offen offset:0 sc0 sc1
v_permlane16_swap_b32_e32 v68, v70
v_permlane16_swap_b32_e32 v69, v71
buffer_store_dwordx4 v[68:71], v166, s[8:11], 0 offen offset:448 sc0 sc1
s_or_b32 s28, s82, s33
s_cbranch_scc1 .LzFIN
s_mov_b32 s28, 0xc108400
buffer_store_dword v6, v7, s[20:23], s28 offen offset:0 sc0 sc1
.LzFIN:
s_endpgm
.LzRT:
s_and_b32 s28, s63, 0x7ff
s_mul_i32 s28, s28, 896
v_subrev_u32 v174, s28, v166
s_bfe_u32 s28, s63, 0xb0010
s_mul_i32 s28, s28, 896
v_subrev_u32 v175, s28, v166
s_movk_i32 s28, 896
v_cmp_gt_u32_e64 s[24:25], s28, v175
v_cmp_gt_u32_e32 vcc, s28, v174
v_add_u32 v175, s28, v175
)" R"(
s_or_b64 s[24:25], s[24:25], vcc
v_cndmask_b32_e32 v174, v175, v174, vcc
v_lshrrev_b32 v174, 5, v174
v_lshlrev_b32 v174, 7, v174
v_and_or_b32 v174, v0, 48, v174
s_mov_b64 exec, s[24:25]
s_mov_b32 s29, 0xc109000
.LzRT1:
buffer_load_dwordx4 v[64:67], v174, s[20:23], s29 offen offset:0 sc0 sc1
buffer_load_dwordx4 v[68:71], v174, s[20:23], s29 offen offset:128 sc0 sc1
buffer_load_dwordx4 v[72:75], v174, s[20:23], s29 offen offset:1792 sc0 sc1
buffer_load_dwordx4 v[76:79], v174, s[20:23], s29 offen offset:1920 sc0 sc1
s_waitcnt vmcnt(0)
v_xor_b32 v175, s3, v65
v_xor_b32 v173, s3, v67
v_or_b32 v175, v175, v173
v_xor_b32 v173, s3, v69
v_or_b32 v175, v175, v173
v_xor_b32 v173, s3, v71
v_or_b32 v175, v175, v173
v_xor_b32 v173, s3, v73
v_or_b32 v175, v175, v173
v_xor_b32 v173, s3, v75
v_or_b32 v175, v175, v173
v_xor_b32 v173, s3, v77
v_or_b32 v175, v175, v173
v_xor_b32 v173, s3, v79
v_or_b32 v175, v175, v173
v_cmp_ne_u32_e32 vcc, 0, v175
s_cbranch_vccz .LzRT2
s_sleep 2
s_sub_u32 s53, s53, 1
s_cbranch_scc0 .LzRT1
s_mov_b32 s53, 0
.LzRT2:
v_mov_b32 v188, v64
v_mov_b32 v189, v66
v_mov_b32 v190, v68
v_mov_b32 v191, v70
v_mov_b32 v196, v72
v_mov_b32 v197, v74
v_mov_b32 v198, v76
v_mov_b32 v199, v78
v_sub_f32_e32 v172, 1.0, v167
Cv 172,172,172
v_lshlrev_b32 v172, 16, v172
v_lshlrev_b32 v204, 16, v188
v_and_b32 v205, 0xffff0000, v188
v_mul_f32_e32 v204, v172, v204
v_mul_f32_e32 v205, v172, v205
Cv 205,204,205
v_lshlrev_b32 v204, 16, v205
v_and_b32 v205, 0xffff0000, v205
v_lshlrev_b32 v206, 16, v189
v_and_b32 v207, 0xffff0000, v189
v_mul_f32_e32 v206, v172, v206
v_mul_f32_e32 v207, v172, v207
Cv 207,206,207
v_lshlrev_b32 v206, 16, v207
v_and_b32 v207, 0xffff0000, v207
v_lshlrev_b32 v212, 16, v190
v_and_b32 v213, 0xffff0000, v190
v_mul_f32_e32 v212, v172, v212
v_mul_f32_e32 v213, v172, v213
)" R"(
Cv 213,212,213
v_lshlrev_b32 v212, 16, v213
v_and_b32 v213, 0xffff0000, v213
v_lshlrev_b32 v214, 16, v191
v_and_b32 v215, 0xffff0000, v191
v_mul_f32_e32 v214, v172, v214
v_mul_f32_e32 v215, v172, v215
Cv 215,214,215
v_lshlrev_b32 v214, 16, v215
v_and_b32 v215, 0xffff0000, v215
v_lshlrev_b32 v220, 16, v196
v_and_b32 v221, 0xffff0000, v196
v_mul_f32_e32 v220, v172, v220
v_mul_f32_e32 v221, v172, v221
Cv 221,220,221
v_lshlrev_b32 v220, 16, v221
v_and_b32 v221, 0xffff0000, v221
v_lshlrev_b32 v222, 16, v197
v_and_b32 v223, 0xffff0000, v197
v_mul_f32_e32 v222, v172, v222
v_mul_f32_e32 v223, v172, v223
Cv 223,222,223
v_lshlrev_b32 v222, 16, v223
v_and_b32 v223, 0xffff0000, v223
v_lshlrev_b32 v228, 16, v198
v_and_b32 v229, 0xffff0000, v198
v_mul_f32_e32 v228, v172, v228
v_mul_f32_e32 v229, v172, v229
Cv 229,228,229
v_lshlrev_b32 v228, 16, v229
v_and_b32 v229, 0xffff0000, v229
v_lshlrev_b32 v230, 16, v199
v_and_b32 v231, 0xffff0000, v199
v_mul_f32_e32 v230, v172, v230
v_mul_f32_e32 v231, v172, v231
Cv 231,230,231
v_lshlrev_b32 v230, 16, v231
v_and_b32 v231, 0xffff0000, v231
s_mov_b64 exec, -1
s_branch .LzRT3
.LzCEND:
)" : "+{s[0:1]}"(ka), "+{s2}"(bid), "+{v0}"(tid) :
   : "memory", "scc", "vcc", "v1", "v2", "v3", "v4", "v5", "v6", "v7", "v8", "v9", R256("v"), V10("a"), R256("a"),
     "s3", "s4", "s5", "s6", "s7", "s8", "s9", V10("s1"), V10("s2"), "s30", "s31", "s33", "s34", "s35", "s36", "s37", "s38", "s39",
     V10("s4"), V10("s5"), V10("s6"), V10("s7"), V10("s8"), V10("s9"));
}

extern "C" void kernel_launch(void* const* d_in, const int* in_sizes, int n_in, void* d_out, int out_size, void* d_ws, size_t ws_size, hipStream_t stream) {
  (void)hipFuncSetAttribute((const void*)k_main, hipFuncAttributeMaxDynamicSharedMemorySize, 163840);
  k_main<<<256, 256, 163840, stream>>>((const u16*)d_in[0], (const u16*)d_in[1], (const u16*)d_in[2], (const u16*)d_in[3], (const u16*)d_in[4],
      (char*)d_ws, (const u16*)d_in[11], (const u16*)d_in[7], (const u16*)d_in[5], (const u16*)d_in[8], (const u16*)d_in[9], (const u16*)d_in[10], (u16*)d_out);
}
```

```cpp
#include <hip/hip_runtime.h>

typedef unsigned short u16;
#define V10(p) p "0", p "1", p "2", p "3", p "4", p "5", p "6", p "7", p "8", p "9"
#define V100(p) V10(p "0"), V10(p "1"), V10(p "2"), V10(p "3"), V10(p "4"), V10(p "5"), V10(p "6"), V10(p "7"), V10(p "8"), V10(p "9")
#define R256(p) V10(p "1"), V10(p "2"), V10(p "3"), V10(p "4"), V10(p "5"), V10(p "6"), V10(p "7"), V10(p "8"), V10(p "9"), V100(p "1"), \
                V10(p "20"), V10(p "21"), V10(p "22"), V10(p "23"), V10(p "24"), p "250", p "251", p "252", p "253", p "254", p "255"

extern "C" __global__ __launch_bounds__(256, 1)
void k_main(const u16* x, const u16* ckv, const u16* wq, const u16* wuk, const u16* wuv, char* ws,
            const u16* gate_w, const u16* w_r, const u16* wo, const u16* wg, const u16* wu, const u16* wd, u16* out) {
  typedef const __attribute__((address_space(4))) char* karg_t;
  karg_t ka = (karg_t)__builtin_amdgcn_kernarg_segment_ptr();
  unsigned bid = blockIdx.x, tid = threadIdx.x;
  asm volatile(R"(
.macro Mv d,a,b
v_mfma_f32_16x16x32_bf16 v[\d:\d+3], a[\a:\a+3], a[\b:\b+3], v[\d:\d+3]
.endm
.macro Mp d,a,b
v_mfma_f32_16x16x32_bf16 v[\d:\d+3], a[\a:\a+3], v[\b:\b+3], v[\d:\d+3]
.endm
.macro Ma d,a,b
v_mfma_f32_16x16x32_bf16 a[\d:\d+3], a[\a:\a+3], a[\b:\b+3], a[\d:\d+3]
.endm
.macro Mz d,a,b
v_mfma_f32_16x16x32_bf16 v[\d:\d+3], a[\a:\a+3], a[\b:\b+3], 0
.endm
.macro Rb d,v,o
ds_read_b128 a[\d:\d+3], v\v offset:\o
.endm
.macro Rt d,v,o
ds_read_b64_tr_b16 a[\d:\d+1], v\v offset:\o
.endm
.macro Cv d,a,b
v_cvt_pk_bf16_f32 v\d, v\a, v\b
.endm
.LzSTART:
s_load_dwordx16 s[64:79], s[0:1], 0
s_load_dwordx2 s[80:81], s[0:1], 64
v_lshrrev_b32 v1, 6, v0
v_and_b32 v0, 63, v0
v_readfirstlane_b32 s33, v1
v_lshlrev_b32 v1, 4, v0
s_getpc_b64 s[24:25]
.Lzpc0:
s_sub_u32 s24, s24, .Lzpc0-.LzSTART
s_subb_u32 s25, s25, 0
s_mov_b32 s26, .LzCEND-.LzSTART
s_mov_b32 s27, 0x20000
s_lshr_b32 s28, s2, 3
s_lshl2_add_u32 s28, s28, s33
s_lshl_b32 s28, s28, 11
v_lshrrev_b32 v5, 4, v0
v_min_u32 v5, 1, v5
v_lshlrev_b32 v5, 27, v5
v_add_lshl_u32 v5, v5, v1, 3
buffer_load_dword v5, v5, s[24:27], s28 offen
s_and_b32 s28, s2, 0x17
s_bfe_u32 s29, s2, 0x10005
s_lshl3_add_u32 s28, s29, s28
s_lshr_b32 s29, s2, 6
s_lshl_b32 s29, s29, 5
s_or_b32 s28, s28, s29
s_and_b32 s29, s2, 8
s_lshl4_add_u32 s2, s29, s28
s_and_b32 s28, s2, 7
s_bfe_u32 s29, s2, 0x10003
s_lshl1_add_u32 s34, s28, s29
s_bfe_u32 s35, s2, 0x30004
s_bfe_u32 s91, s2, 0x10004
s_bfe_u32 s93, s2, 0x20005
s_lshl_b32 s36, s33, 13
s_mov_b32 s95, 40
s_bitcmp1_b32 s2, 7
s_cbranch_scc0 .LzA0
.LzB0:
s_mul_i32 s41, s33, 7168
s_mul_i32 s42, s33, 0x70000
s_lshl_b32 s28, s34, 9
s_add_u32 s42, s42, s28
v_lshrrev_b32 v28, 3, v0
v_and_b32 v29, 0x70, v1
v_lshl_or_b32 v28, v28, 13, v29
v_lshlrev_b32 v29, 1, v0
v_and_b32 v29, 32, v29
v_xor_b32 v28, v28, v29
s_and_b32 s28, s33, 1
s_lshl_b32 s28, s28, 6
v_xor_b32 v240, s28, v28
v_xor_b32 v241, 64, v240
v_add_u32 v241, 0xfc00, v241
v_add_u32 v242, 0x1f800, v240
v_add_u32 v243, 0x1f800, v241
s_sub_u32 s28, 15, s35
s_lshl2_add_u32 s28, s28, s33
s_mul_i32 s43, s28, 28672
v_and_b32 v2, 0x70, v1
v_and_b32 v28, 48, v0
v_xor_b32 v2, v2, v28
)" R"(
v_lshrrev_b32 v28, 3, v0
v_mul_u32_u24 v28, 896, v28
v_add_u32 v2, v2, v28
v_xor_b32 v3, 64, v2
v_add_u32 v3, 6144, v3
v_bfe_u32 v26, v0, 1, 3
v_lshrrev_b32 v28, 4, v0
v_xor_b32 v26, v26, v28
v_lshlrev_b32 v26, 4, v26
v_and_b32 v28, 15, v0
v_lshl_or_b32 v26, v28, 7, v26
s_add_u32 s28, s36, 0x0
v_add_u32 v26, s28, v26
v_xor_b32 v27, 64, v26
s_waitcnt lgkmcnt(0)
s_mov_b32 s16, s68
s_mov_b32 s17, s69
s_mov_b32 s18, 0x380000
s_mov_b32 s19, 0x20000
s_mov_b32 s12, s64
s_mov_b32 s13, s65
s_mov_b32 s14, 0x1c0000
s_mov_b32 s15, 0x20000
s_add_u32 m0, s36, 0
s_add_u32 s44, s43, 0
buffer_load_dwordx4 v2, s[12:15], s44 offen offset:0 lds
buffer_load_dwordx4 v3, s[12:15], s44 offen offset:1024 lds
s_add_u32 s44, s43, 12288
buffer_load_dwordx4 v2, s[12:15], s44 offen offset:2048 lds
buffer_load_dwordx4 v3, s[12:15], s44 offen offset:3072 lds
s_add_u32 m0, s41, 65536
s_add_u32 s92, s42, 0x0
buffer_load_dwordx4 v240, s[16:19], s92 offen offset:0 lds
buffer_load_dwordx4 v241, s[16:19], s92 offen offset:1024 lds
buffer_load_dwordx4 v242, s[16:19], s92 offen offset:2048 lds
buffer_load_dwordx4 v243, s[16:19], s92 offen offset:3072 lds
s_add_u32 m0, s41, 69632
s_add_u32 s92, s42, 0x40000
buffer_load_dwordx4 v240, s[16:19], s92 offen offset:0 lds
buffer_load_dwordx4 v241, s[16:19], s92 offen offset:1024 lds
buffer_load_dwordx4 v242, s[16:19], s92 offen offset:2048 lds
s_add_u32 m0, s36, 4096
s_add_u32 s44, s43, 128
buffer_load_dwordx4 v2, s[12:15], s44 offen offset:0 lds
buffer_load_dwordx4 v3, s[12:15], s44 offen offset:1024 lds
s_add_u32 s44, s43, 12416
buffer_load_dwordx4 v2, s[12:15], s44 offen offset:2048 lds
buffer_load_dwordx4 v3, s[12:15], s44 offen offset:3072 lds
s_add_u32 m0, s36, 32768
s_add_u32 s44, s43, 256
buffer_load_dwordx4 v2, s[12:15], s44 offen offset:0 lds
buffer_load_dwordx4 v3, s[12:15], s44 offen offset:1024 lds
s_add_u32 s44, s43, 12544
buffer_load_dwordx4 v2, s[12:15], s44 offen offset:2048 lds
buffer_load_dwordx4 v3, s[12:15], s44 offen offset:3072 lds
s_add_u32 m0, s36, 36864
s_add_u32 s44, s43, 384
buffer_load_dwordx4 v2, s[12:15], s44 offen offset:0 lds
buffer_load_dwordx4 v3, s[12:15], s44 offen offset:1024 lds
s_add_u32 s44, s43, 12672
buffer_load_dwordx4 v2, s[12:15], s44 offen offset:2048 lds
buffer_load_dwordx4 v3, s[12:15], s44 offen offset:3072 lds
s_add_u32 m0, s41, 98304
s_add_u32 s92, s42, 0x80
)" R"(
buffer_load_dwordx4 v240, s[16:19], s92 offen offset:0 lds
buffer_load_dwordx4 v241, s[16:19], s92 offen offset:1024 lds
buffer_load_dwordx4 v242, s[16:19], s92 offen offset:2048 lds
buffer_load_dwordx4 v243, s[16:19], s92 offen offset:3072 lds
s_add_u32 m0, s41, 102400
s_add_u32 s92, s42, 0x40080
buffer_load_dwordx4 v240, s[16:19], s92 offen offset:0 lds
buffer_load_dwordx4 v241, s[16:19], s92 offen offset:1024 lds
buffer_load_dwordx4 v242, s[16:19], s92 offen offset:2048 lds
s_mov_b32 s28, 0xc108400
s_load_dword s3, s[74:75], s28
s_sub_u32 s94, s2, 128
s_lshl2_add_u32 s94, s94, s33
s_lshl_b32 s94, s94, 2
s_mul_i32 s29, s94, 896
v_mov_b32 v120, 0x40000000
v_cmp_gt_u32_e32 vcc, 56, v0
s_nop 1
v_cndmask_b32_e32 v120, v120, v1, vcc
buffer_load_dwordx4 v[96:99], v120, s[12:15], s29 offen offset:0
buffer_load_dwordx4 v[100:103], v120, s[12:15], s29 offen offset:896
buffer_load_dwordx4 v[104:107], v120, s[12:15], s29 offen offset:1792
buffer_load_dwordx4 v[108:111], v120, s[12:15], s29 offen offset:2688
s_mov_b32 s24, s76
s_mov_b32 s25, s77
s_mov_b32 s26, 0x380
s_mov_b32 s27, 0x20000
buffer_load_dwordx4 v[112:115], v120, s[24:27], 0 offen
s_mov_b32 s24, s78
s_mov_b32 s25, s79
s_mov_b32 s26, 0x380
s_mov_b32 s27, 0x20000
buffer_load_dwordx4 v[116:119], v120, s[24:27], 0 offen
s_mov_b32 s20, s74
s_mov_b32 s21, s75
s_mov_b32 s22, 0xc1ba000
s_mov_b32 s23, 0x20000
s_mov_b32 s4, s66
s_mov_b32 s5, s67
s_mov_b32 s6, 0x400000
s_mov_b32 s7, 0x20000
s_cmp_eq_u32 s91, 1
s_cselect_b32 s8, s72, s70
s_cselect_b32 s9, s73, s71
s_mov_b32 s10, 0x800000
s_mov_b32 s11, 0x20000
s_load_dwordx2 s[74:75], s[0:1], 96
s_lshl3_add_u32 s28, s33, 64
s_load_dwordx2 s[76:77], s[0:1], s28
v_bfe_u32 v244, v0, 3, 2
v_lshlrev_b32 v244, 5, v244
v_and_b32 v28, 3, v0
v_lshl_or_b32 v244, v28, 3, v244
v_bfe_u32 v28, v0, 2, 2
v_lshl_or_b32 v244, v28, 7, v244
v_lshrrev_b32 v28, 4, v0
v_lshl_or_b32 v244, v28, 10, v244
v_xor_b32 v245, 32, v244
v_xor_b32 v246, 64, v244
v_xor_b32 v247, 96, v244
v_add_u32 v252, 0x20000, v244
v_add_u32 v253, 0x20000, v245
v_add_u32 v254, 0x20000, v246
v_add_u32 v255, 0x20000, v247
)" R"(
v_add_u32 v248, 0x10000, v244
v_add_u32 v249, 0x10000, v245
v_add_u32 v250, 0x10000, v246
v_add_u32 v251, 0x10000, v247
s_waitcnt vmcnt(21) lgkmcnt(0)
s_barrier
ds_read_b128 v[128:131], v26 offset:0
ds_read_b128 v[132:135], v26 offset:2048
ds_read_b128 v[136:139], v27 offset:0
ds_read_b128 v[140:143], v27 offset:2048
ds_read_b128 v[144:147], v26 offset:4096
ds_read_b128 v[148:151], v26 offset:6144
ds_read_b128 v[152:155], v27 offset:4096
ds_read_b128 v[156:159], v27 offset:6144
Rt 144,248,0
Rt 146,248,512
Rt 148,249,0
Rt 150,249,512
Rt 152,250,0
Rt 154,250,512
Rt 156,251,0
Rt 158,251,512
Rt 160,248,4096
Rt 162,248,4608
Rt 164,249,4096
Rt 166,249,4608
Rt 168,250,4096
Rt 170,250,4608
Rt 172,251,4096
Rt 174,251,4608
s_mul_i32 s3, s3, 1664525
s_add_u32 s3, s3, 1013904223
v_mov_b32 v6, s3
v_min_u32 v7, 1, v0
v_lshlrev_b32 v7, 30, v7
s_waitcnt lgkmcnt(8)
s_barrier
v_mfma_f32_16x16x32_bf16 v[32:35], a[144:147], v[128:131], 0
Rt 128,248,8192
s_add_u32 m0, s41, 131072
s_add_u32 s92, s42, 0x1c0000
v_mfma_f32_16x16x32_bf16 v[40:43], a[144:147], v[132:135], 0
Rt 130,248,8704
buffer_load_dwordx4 v240, s[16:19], s92 offen offset:0 lds
v_mfma_f32_16x16x32_bf16 v[44:47], a[148:151], v[132:135], 0
Rt 132,249,8192
v_mfma_f32_16x16x32_bf16 v[36:39], a[148:151], v[128:131], 0
Rt 134,249,8704
v_mfma_f32_16x16x32_bf16 v[48:51], a[152:155], v[128:131], 0
Rt 136,250,8192
v_mfma_f32_16x16x32_bf16 v[56:59], a[152:155], v[132:135], 0
Rt 138,250,8704
buffer_load_dwordx4 v241, s[16:19], s92 offen offset:1024 lds
v_mfma_f32_16x16x32_bf16 v[60:63], a[156:159], v[132:135], 0
Rt 140,251,8192
v_mfma_f32_16x16x32_bf16 v[52:55], a[156:159], v[128:131], 0
Rt 142,251,8704
s_waitcnt vmcnt(19) lgkmcnt(8)
Mp 32,160,136
Rt 144,248,12288
ds_read_b128 v[160:163], v26 offset:32768
Mp 40,160,140
Rt 146,248,12800
buffer_load_dwordx4 v242, s[16:19], s92 offen offset:2048 lds
Mp 44,164,140
Rt 148,249,12288
)" R"(
ds_read_b128 v[164:167], v26 offset:34816
Mp 36,164,136
Rt 150,249,12800
Mp 48,168,136
Rt 152,250,12288
ds_read_b128 v[168:171], v27 offset:32768
Mp 56,168,140
Rt 154,250,12800
buffer_load_dwordx4 v243, s[16:19], s92 offen offset:3072 lds
Mp 60,172,140
Rt 156,251,12288
ds_read_b128 v[172:175], v27 offset:34816
Mp 52,172,136
Rt 158,251,12800
s_waitcnt vmcnt(17) lgkmcnt(12)
Mp 32,128,144
Rt 160,248,16384
ds_read_b128 v[176:179], v26 offset:36864
s_add_u32 m0, s41, 135168
s_add_u32 s92, s42, 0x200000
Mp 40,128,148
Rt 162,248,16896
buffer_load_dwordx4 v240, s[16:19], s92 offen offset:0 lds
Mp 44,132,148
Rt 164,249,16384
ds_read_b128 v[180:183], v26 offset:38912
Mp 36,132,144
Rt 166,249,16896
Mp 48,136,144
Rt 168,250,16384
ds_read_b128 v[184:187], v27 offset:36864
Mp 56,136,148
Rt 170,250,16896
buffer_load_dwordx4 v241, s[16:19], s92 offen offset:1024 lds
Mp 60,140,148
Rt 172,251,16384
ds_read_b128 v[188:191], v27 offset:38912
Mp 52,140,144
Rt 174,251,16896
s_waitcnt lgkmcnt(12)
Mp 32,144,152
Rt 128,248,20480
Mp 40,144,156
Rt 130,248,20992
buffer_load_dwordx4 v242, s[16:19], s92 offen offset:2048 lds
Mp 44,148,156
Rt 132,249,20480
Mp 36,148,152
Rt 134,249,20992
Mp 48,152,152
Rt 136,250,20480
Mp 56,152,156
Rt 138,250,20992
Mp 60,156,156
Rt 140,251,20480
s_add_u32 s44, s43, 512
Mp 52,156,152
Rt 142,251,20992
s_waitcnt lgkmcnt(8)
Mp 32,160,160
Rt 144,248,24576
s_add_u32 m0, s36, 32768
Mp 40,160,164
Rt 146,248,25088
)" R"(
buffer_load_dwordx4 v2, s[12:15], s44 offen offset:0 lds
Mp 44,164,164
Rt 148,249,24576
Mp 36,164,160
Rt 150,249,25088
Mp 48,168,160
Rt 152,250,24576
Mp 56,168,164
Rt 154,250,25088
buffer_load_dwordx4 v3, s[12:15], s44 offen offset:1024 lds
Mp 60,172,164
Rt 156,251,24576
Mp 52,172,160
Rt 158,251,25088
s_waitcnt vmcnt(15) lgkmcnt(8)
s_barrier
Mp 32,128,168
Rt 160,248,32768
s_mov_b32 s63, 0xe0c0a08
s_mov_b32 s28, 0x100d0b09
Mp 40,128,172
Rt 162,248,33280
s_add_u32 s44, s43, 12800
buffer_load_dwordx4 v2, s[12:15], s44 offen offset:2048 lds
Mp 44,132,172
Rt 164,249,32768
Mp 36,132,168
Rt 166,249,33280
Mp 48,136,168
Rt 168,250,32768
s_bitcmp1_b32 s2, 5
s_cmov_b32 s63, 0xf1e1c1a
s_cmov_b32 s28, 0x111f1d1b
Mp 56,136,172
Rt 170,250,33280
buffer_load_dwordx4 v3, s[12:15], s44 offen offset:3072 lds
Mp 60,140,172
Rt 172,251,32768
s_add_u32 s44, s43, 640
Mp 52,140,168
Rt 174,251,33280
s_waitcnt vmcnt(11) lgkmcnt(8)
s_barrier
Mp 32,144,176
Rt 128,248,36864
s_add_u32 m0, s36, 36864
v_dot2_f32_bf16 v120, v96, v112, 0
Mp 40,144,180
Rt 130,248,37376
buffer_load_dwordx4 v2, s[12:15], s44 offen offset:0 lds
v_dot2_f32_bf16 v121, v100, v112, 0
Mp 44,148,180
Rt 132,249,36864
s_bitcmp1_b32 s2, 6
s_cmov_b32 s63, s28
Mp 36,148,176
Rt 134,249,37376
s_lshl_b32 s28, s33, 3
s_lshr_b32 s28, s63, s28
v_dot2_f32_bf16 v122, v104, v112, 0
Mp 48,152,176
Rt 136,250,36864
s_and_b32 s28, s28, 255
s_lshl_b32 s38, s28, 17
s_add_u32 s38, s38, 0xff80
Mp 56,152,180
)" R"(
Rt 138,250,37376
buffer_load_dwordx4 v3, s[12:15], s44 offen offset:1024 lds
v_dot2_f32_bf16 v123, v108, v112, 0
Mp 60,156,180
Rt 140,251,36864
s_lshl_b32 s39, s33, 16
s_lshl_b32 s28, s34, 9
Mp 52,156,176
Rt 142,251,37376
s_add_u32 s39, s39, s28
v_dot2_f32_bf16 v124, v96, v116, 0
s_waitcnt lgkmcnt(8)
v_mfma_f32_16x16x32_bf16 v[64:67], a[160:163], v[128:131], 0
Rt 144,248,40960
s_sub_u32 s39, s39, 0x40000
s_lshl_b32 s37, s33, 12
v_mfma_f32_16x16x32_bf16 v[72:75], a[160:163], v[132:135], 0
Rt 146,248,41472
s_add_u32 s44, s43, 12928
buffer_load_dwordx4 v2, s[12:15], s44 offen offset:2048 lds
v_mfma_f32_16x16x32_bf16 v[76:79], a[164:167], v[132:135], 0
Rt 148,249,40960
s_add_u32 s37, s37, 0x10000
s_lshl_b32 s28, s34, 21
v_mfma_f32_16x16x32_bf16 v[68:71], a[164:167], v[128:131], 0
Rt 150,249,41472
s_lshl_b32 s29, s91, 25
s_add_u32 s28, s28, s29
v_dot2_f32_bf16 v125, v100, v116, 0
v_mfma_f32_16x16x32_bf16 v[80:83], a[168:171], v[128:131], 0
Rt 152,250,40960
s_bfe_u32 s29, s63, 0x80000
s_lshl_b32 s29, s29, 16
s_add_u32 s45, s28, s29
v_mfma_f32_16x16x32_bf16 v[88:91], a[168:171], v[132:135], 0
Rt 154,250,41472
buffer_load_dwordx4 v3, s[12:15], s44 offen offset:3072 lds
v_dot2_f32_bf16 v126, v104, v116, 0
v_mfma_f32_16x16x32_bf16 v[92:95], a[172:175], v[132:135], 0
Rt 156,251,40960
s_add_u32 s92, s42, 0x1c0080
s_bfe_u32 s29, s63, 0x80008
v_mfma_f32_16x16x32_bf16 v[84:87], a[172:175], v[128:131], 0
Rt 158,251,41472
s_lshl_b32 s29, s29, 16
v_dot2_f32_bf16 v127, v108, v116, 0
s_waitcnt lgkmcnt(8)
Mp 64,128,136
Rt 160,248,45056
s_add_u32 m0, s41, 0
s_add_u32 s46, s28, s29
Mp 72,128,140
Rt 162,248,45568
buffer_load_dwordx4 v240, s[16:19], s92 offen offset:0 lds
s_bfe_u32 s29, s63, 0x80010
Mp 76,132,140
Rt 164,249,45056
s_lshl_b32 s29, s29, 16
s_add_u32 s47, s28, s29
Mp 68,132,136
Rt 166,249,45568
s_bfe_u32 s29, s63, 0x80018
s_lshl_b32 s29, s29, 16
v_dot2_f32_bf16 v120, v97, v113, v120
)" R"(
Mp 80,136,136
Rt 168,250,45056
s_add_u32 s48, s28, s29
s_lshl_b32 s50, s34, 21
s_add_u32 s50, s50, 0x6140000
Mp 88,136,140
Rt 170,250,45568
buffer_load_dwordx4 v241, s[16:19], s92 offen offset:1024 lds
v_dot2_f32_bf16 v121, v101, v113, v121
Mp 92,140,140
Rt 172,251,45056
s_lshl2_add_u32 s28, s91, s93
s_lshl_b32 s28, s28, 7
Mp 84,140,136
Rt 174,251,45568
s_add_u32 s49, s28, s50
v_dot2_f32_bf16 v122, v105, v113, v122
s_waitcnt lgkmcnt(8)
Mp 64,144,144
Rt 128,248,49152
s_add_u32 s49, s49, 4096
s_lshl_b32 s52, s35, 7
Mp 72,144,148
Rt 130,248,49664
buffer_load_dwordx4 v242, s[16:19], s92 offen offset:2048 lds
s_add_u32 s52, s52, s50
Mp 76,148,148
Rt 132,249,49152
s_add_u32 s52, s52, 0xd000
s_lshl4_add_u32 s28, s34, s35
Mp 68,148,144
Rt 134,249,49664
s_lshl_b32 s51, s28, 17
s_add_u32 s51, s51, 0x4000000
v_dot2_f32_bf16 v123, v109, v113, v123
Mp 80,152,144
Rt 136,250,49152
v_dot2_f32_bf16 v124, v97, v117, v124
v_dot2_f32_bf16 v125, v101, v117, v125
Mp 88,152,148
Rt 138,250,49664
buffer_load_dwordx4 v243, s[16:19], s92 offen offset:3072 lds
v_dot2_f32_bf16 v126, v105, v117, v126
Mp 92,156,148
Rt 140,251,49152
s_add_u32 s92, s42, 0x200080
v_dot2_f32_bf16 v127, v109, v117, v127
Mp 84,156,144
Rt 142,251,49664
v_dot2_f32_bf16 v120, v98, v114, v120
v_dot2_f32_bf16 v121, v102, v114, v121
s_waitcnt lgkmcnt(8)
Mp 64,160,152
Rt 144,248,53248
s_add_u32 m0, s41, 4096
v_dot2_f32_bf16 v122, v106, v114, v122
Mp 72,160,156
Rt 146,248,53760
buffer_load_dwordx4 v240, s[16:19], s92 offen offset:0 lds
v_dot2_f32_bf16 v123, v110, v114, v123
Mp 76,164,156
Rt 148,249,53248
v_dot2_f32_bf16 v124, v98, v118, v124
v_dot2_f32_bf16 v125, v102, v118, v125
)" R"(
Mp 68,164,152
Rt 150,249,53760
v_dot2_f32_bf16 v126, v106, v118, v126
v_dot2_f32_bf16 v127, v110, v118, v127
v_dot2_f32_bf16 v120, v99, v115, v120
Mp 80,168,152
Rt 152,250,53248
v_dot2_f32_bf16 v121, v103, v115, v121
v_dot2_f32_bf16 v122, v107, v115, v122
Mp 88,168,156
Rt 154,250,53760
buffer_load_dwordx4 v241, s[16:19], s92 offen offset:1024 lds
v_dot2_f32_bf16 v123, v111, v115, v123
Mp 92,172,156
Rt 156,251,53248
s_cmp_lg_u32 s33, 0
s_cmov_b32 s49, 0x40000000
s_cmov_b32 s52, 0x40000000
Mp 84,172,152
Rt 158,251,53760
s_mov_b32 s53, 0x40000
v_dot2_f32_bf16 v124, v99, v119, v124
s_waitcnt vmcnt(10) lgkmcnt(8)
Mp 64,128,160
Rt 160,248,57344
ds_read_b128 v[192:195], v26 offset:32768
s_mov_b32 s54, 0x3db8aa3b
Mp 72,128,164
Rt 162,248,57856
buffer_load_dwordx4 v242, s[16:19], s92 offen offset:2048 lds
s_lshl_b32 s89, s34, 21
Mp 76,132,164
Rt 164,249,57344
ds_read_b128 v[196:199], v26 offset:34816
s_mov_b32 s62, 0x4e800000
Mp 68,132,160
Rt 166,249,57856
s_add_u32 s67, s89, 0x6000000
s_lshl3_add_u32 s28, s34, s35
s_add_u32 s64, s51, 0x100000
Mp 80,136,160
Rt 168,250,57344
ds_read_b128 v[200:203], v27 offset:32768
s_lshl2_add_u32 s28, s28, s33
Mp 88,136,164
Rt 170,250,57856
s_lshl2_add_u32 s65, s35, s33
s_lshl_b32 s65, s65, 7
v_dot2_f32_bf16 v125, v103, v119, v125
Mp 92,140,164
Rt 172,251,57344
ds_read_b128 v[204:207], v27 offset:34816
s_add_u32 s65, s65, s50
s_add_u32 s65, s65, 0xc000
Mp 84,140,160
Rt 174,251,57856
v_dot2_f32_bf16 v126, v107, v119, v126
s_waitcnt vmcnt(7) lgkmcnt(12)
s_barrier
Mp 64,144,168
Rt 128,252,0
ds_read_b128 v[208:211], v26 offset:36864
s_lshl_b32 s66, s28, 10
Mp 72,144,172
Rt 130,252,512
)" R"(
s_add_u32 s66, s66, 0xc000000
s_sub_u32 s28, 15, s35
v_dot2_f32_bf16 v127, v111, v119, v127
Mp 76,148,172
Rt 132,253,0
ds_read_b128 v[212:215], v26 offset:38912
s_lshl2_add_u32 s28, s28, s33
Mp 68,148,168
Rt 134,253,512
s_lshl_b32 s29, s28, 5
s_add_u32 s29, s29, s34
s_lshl_b32 s73, s29, 15
Mp 80,152,168
Rt 136,254,0
ds_read_b128 v[216:219], v27 offset:36864
s_add_u32 s73, s73, 0x8000000
Mp 88,152,172
Rt 138,254,512
s_add_u32 s44, s43, 768
s_lshr_b32 s29, s36, 1
v_permlane32_swap_b32_e32 v120, v121
Mp 92,156,172
Rt 140,255,0
ds_read_b128 v[220:223], v27 offset:38912
s_and_b32 s29, s29, 0x2000
s_add_u32 s73, s73, s29
Mp 84,156,168
Rt 142,255,512
v_add_f32_e32 v120, v120, v121
s_waitcnt lgkmcnt(12)
s_barrier
Mp 64,160,176
Rt 144,252,4096
s_add_u32 m0, s36, 32768
s_lshl4_add_u32 s28, s28, s34
Mp 72,160,180
Rt 146,252,4608
buffer_load_dwordx4 v2, s[12:15], s44 offen offset:0 lds
v_permlane32_swap_b32_e32 v122, v123
Mp 76,164,180
Rt 148,253,4096
s_lshl_b32 s68, s28, 8
s_add_u32 s68, s68, 0xc110000
Mp 68,164,176
Rt 150,253,4608
s_xor_b32 s72, s73, 0x3c04000
s_mov_b32 s55, s89
s_mov_b32 s57, 18
Mp 80,168,176
Rt 152,254,4096
buffer_load_dwordx4 v3, s[12:15], s44 offen offset:1024 lds
Mp 88,168,180
Rt 154,254,4608
s_mov_b32 s59, s67
s_mov_b32 s58, 0
v_add_f32_e32 v122, v122, v123
Mp 92,172,180
Rt 156,255,4096
s_cmp_eq_u32 s35, 7
s_cmov_b32 s58, 2
Mp 84,172,176
Rt 158,255,4608
s_add_u32 s44, s43, 13056
buffer_load_dwordx4 v2, s[12:15], s44 offen offset:2048 lds
s_waitcnt lgkmcnt(8)
)" R"(
Mp 32,128,184
Rt 160,252,8192
s_cmp_eq_u32 s35, 7
s_cmov_b32 s64, 0x40000000
Mp 40,128,188
Rt 162,252,8704
s_cmp_eq_u32 s35, 7
s_cmov_b32 s66, 0x40000000
v_permlane32_swap_b32_e32 v124, v125
Mp 44,132,188
Rt 164,253,8192
buffer_load_dwordx4 v3, s[12:15], s44 offen offset:3072 lds
Mp 36,132,184
Rt 166,253,8704
s_add_u32 s56, s55, 0x2000000
v_lshrrev_b32 v28, 4, v0
v_and_b32 v29, 7, v0
Mp 48,136,184
Rt 168,254,8192
v_xor_b32 v28, v28, v29
v_lshlrev_b32 v28, 4, v28
Mp 56,136,188
Rt 170,254,8704
s_add_u32 s92, s42, 0x100
v_lshrrev_b32 v29, 3, v0
v_add_f32_e32 v124, v124, v125
Mp 60,140,188
Rt 172,255,8192
s_add_u32 m0, s41, 65536
v_lshl_or_b32 v8, v29, 11, v28
Mp 52,140,184
Rt 174,255,8704
buffer_load_dwordx4 v240, s[16:19], s92 offen offset:0 lds
s_waitcnt lgkmcnt(8)
Mp 32,144,192
Rt 128,252,12288
v_xor_b32 v9, 64, v8
v_add_u32 v9, 15360, v9
v_add_u32 v10, 30720, v8
Mp 40,144,196
Rt 130,252,12800
v_add_u32 v11, 30720, v9
s_and_b32 s28, s33, 1
v_permlane32_swap_b32_e32 v126, v127
Mp 44,148,196
Rt 132,253,12288
s_lshl_b32 s28, s28, 7
v_lshrrev_b32 v29, 5, v0
Mp 36,148,192
Rt 134,253,12800
buffer_load_dwordx4 v241, s[16:19], s92 offen offset:1024 lds
v_mul_u32_u24 v29, 0x2220, v29
Mp 48,152,192
Rt 136,254,12288
v_xor_b32 v29, v29, v1
v_xor_b32 v12, s28, v29
Mp 56,152,196
Rt 138,254,12800
v_xor_b32 v13, 64, v12
v_add_u32 v13, 15360, v13
v_add_f32_e32 v126, v126, v127
Mp 60,156,196
Rt 140,255,12288
v_add_u32 v14, 30720, v12
)" R"(
v_add_u32 v15, 30720, v13
Mp 52,156,192
Rt 142,255,12800
buffer_load_dwordx4 v242, s[16:19], s92 offen offset:2048 lds
s_waitcnt lgkmcnt(8)
Mp 32,160,200
Rt 144,252,16384
v_bfe_u32 v28, v0, 1, 3
v_lshrrev_b32 v29, 4, v0
v_xor_b32 v28, v28, v29
Mp 40,160,204
Rt 146,252,16896
v_lshlrev_b32 v28, 4, v28
v_and_b32 v29, 15, v0
v_permlane16_swap_b32_e32 v120, v122
Mp 44,164,204
Rt 148,253,16384
v_lshl_or_b32 v28, v29, 7, v28
s_lshr_b32 s28, s33, 1
Mp 36,164,200
Rt 150,253,16896
buffer_load_dwordx4 v243, s[16:19], s92 offen offset:3072 lds
v_add_f32_e32 v120, v120, v122
Mp 48,168,200
Rt 152,254,16384
s_lshl_b32 s28, s28, 12
v_or_b32 v16, s28, v28
Mp 56,168,204
Rt 154,254,16896
s_add_u32 s92, s42, 0x40100
v_xor_b32 v17, 64, v16
v_bfe_u32 v28, v0, 2, 3
Mp 60,172,204
Rt 156,255,16384
s_add_u32 m0, s41, 69632
v_lshlrev_b32 v28, 5, v28
Mp 52,172,200
Rt 158,255,16896
buffer_load_dwordx4 v240, s[16:19], s92 offen offset:0 lds
s_waitcnt vmcnt(5) lgkmcnt(8)
Mp 32,128,208
Rt 160,252,20480
ds_read_b128 v[224:227], v26 offset:32768
v_and_b32 v29, 3, v0
v_lshl_or_b32 v28, v29, 3, v28
Mp 40,128,212
Rt 162,252,20992
v_bfe_u32 v29, v0, 2, 2
v_lshl_or_b32 v28, v29, 9, v28
v_permlane16_swap_b32_e32 v124, v126
Mp 44,132,212
Rt 164,253,20480
ds_read_b128 v[228:231], v26 offset:34816
v_lshrrev_b32 v29, 4, v0
Mp 36,132,208
Rt 166,253,20992
buffer_load_dwordx4 v241, s[16:19], s92 offen offset:1024 lds
v_add_f32_e32 v124, v124, v126
Mp 48,136,208
Rt 168,254,20480
ds_read_b128 v[232:235], v27 offset:32768
v_lshl_or_b32 v28, v29, 12, v28
Mp 56,136,212
Rt 170,254,20992
)" R"(
s_and_b32 s28, s33, 1
s_lshl_b32 s28, s28, 8
s_bitset1_b32 s28, 16
Mp 60,140,212
Rt 172,255,20480
ds_read_b128 v[236:239], v27 offset:34816
v_or_b32 v18, s28, v28
Mp 52,140,208
Rt 174,255,20992
buffer_load_dwordx4 v242, s[16:19], s92 offen offset:2048 lds
s_waitcnt lgkmcnt(12)
Mp 32,144,216
Rt 128,252,24576
v_xor_b32 v19, 32, v18
v_xor_b32 v20, 64, v18
Mp 40,144,220
Rt 130,252,25088
s_add_u32 s92, s42, 0x180
v_xor_b32 v21, 96, v18
v_add_f32_dpp v120, v120, v120 row_ror:8 row_mask:0xf bank_mask:0xf
Mp 44,148,220
Rt 132,253,24576
s_add_u32 m0, s41, 98304
v_xor_b32 v22, 128, v18
v_xor_b32 v23, 160, v18
Mp 36,148,216
Rt 134,253,25088
buffer_load_dwordx4 v240, s[16:19], s92 offen offset:0 lds
v_add_f32_dpp v124, v124, v124 row_ror:8 row_mask:0xf bank_mask:0xf
Mp 48,152,216
Rt 136,254,24576
v_xor_b32 v24, 192, v18
v_xor_b32 v25, 224, v18
Mp 56,152,220
Rt 138,254,25088
v_add_f32_dpp v120, v120, v120 row_ror:4 row_mask:0xf bank_mask:0xf
v_add_f32_dpp v124, v124, v124 row_ror:4 row_mask:0xf bank_mask:0xf
v_lshrrev_b32 v121, 5, v0
Mp 60,156,220
Rt 140,255,24576
v_add_f32_dpp v120, v120, v120 row_ror:2 row_mask:0xf bank_mask:0xf
v_add_f32_dpp v124, v124, v124 row_ror:2 row_mask:0xf bank_mask:0xf
Mp 52,156,216
Rt 142,255,25088
buffer_load_dwordx4 v241, s[16:19], s92 offen offset:1024 lds
s_waitcnt lgkmcnt(8)
s_barrier
Mp 32,160,224
Rt 144,244,0
v_add_f32_dpp v120, v120, v120 row_ror:1 row_mask:0xf bank_mask:0xf
v_add_f32_dpp v124, v124, v124 row_ror:1 row_mask:0xf bank_mask:0xf
Mp 40,160,228
Rt 146,244,512
Cv 120,120,120
v_lshlrev_b32 v120, 16, v120
Cv 124,124,124
Mp 44,164,228
Rt 148,245,0
v_lshlrev_b32 v124, 16, v124
v_mul_f32_e32 v120, 0xbfb8aa3b, v120
Mp 36,164,224
Rt 150,245,512
buffer_load_dwordx4 v242, s[16:19], s92 offen offset:2048 lds
v_bfe_u32 v122, v0, 4, 1
Mp 48,168,224
)" R"(
Rt 152,246,0
v_exp_f32_e32 v120, v120
Mp 56,168,228
Rt 154,246,512
v_add_f32_e32 v120, 1.0, v120
v_rcp_f32_e32 v120, v120
Mp 60,172,228
Rt 156,247,0
Cv 120,120,120
v_lshlrev_b32 v120, 16, v120
Mp 52,172,224
Rt 158,247,512
buffer_load_dwordx4 v243, s[16:19], s92 offen offset:3072 lds
s_waitcnt lgkmcnt(8)
s_barrier
Mp 32,128,232
Rt 160,244,4096
v_lshl_or_b32 v121, v122, 1, v121
v_add_u32 v121, s94, v121
Mp 40,128,236
Rt 162,244,4608
s_add_u32 s92, s42, 0x40180
v_add_f32_e32 v125, 0, v124
v_ashrrev_i32 v126, 31, v125
Mp 44,132,236
Rt 164,245,4096
s_add_u32 m0, s41, 102400
v_or_b32 v126, 0x80000000, v126
Mp 36,132,232
Rt 166,245,4608
buffer_load_dwordx4 v240, s[16:19], s92 offen offset:0 lds
v_xor_b32 v125, v125, v126
Mp 48,136,232
Rt 168,246,4096
v_and_b32 v125, 0xffff0000, v125
v_sub_u32 v126, 0xffff, v121
Mp 56,136,236
Rt 170,246,4608
v_or_b32 v116, v125, v126
v_mov_b32 v117, s3
v_mov_b32 v118, v120
Mp 60,140,236
Rt 172,247,4096
v_mov_b32 v119, v124
v_and_b32 v122, 15, v0
Mp 52,140,232
Rt 174,247,4608
buffer_load_dwordx4 v241, s[16:19], s92 offen offset:1024 lds
s_waitcnt lgkmcnt(8)
Mp 64,144,184
Rt 128,244,8192
v_min_u32 v122, 1, v122
v_lshlrev_b32 v122, 30, v122
Mp 72,144,188
Rt 130,244,8704
v_lshl_or_b32 v122, v121, 4, v122
s_mov_b32 s94, 0xc100000
Mp 76,148,188
Rt 132,245,8192
buffer_store_dwordx4 v[116:119], v122, s[20:23], s94 offen sc0 sc1
Mp 68,148,184
Rt 134,245,8704
buffer_load_dwordx4 v242, s[16:19], s92 offen offset:2048 lds
Mp 80,152,184
Rt 136,246,8192
)" R"(
v_mul_f32_e32 v32, s54, v32
Mp 88,152,188
Rt 138,246,8704
s_add_u32 s92, s42, 0x1c0100
Mp 92,156,188
Rt 140,247,8192
s_add_u32 m0, s41, 32768
Mp 84,156,184
Rt 142,247,8704
buffer_load_dwordx4 v240, s[16:19], s92 offen offset:0 lds
s_waitcnt lgkmcnt(8)
Mp 64,160,192
Rt 144,244,12288
v_mul_f32_e32 v33, s54, v33
Mp 72,160,196
Rt 146,244,12800
Cv 32,32,33
Mp 76,164,196
Rt 148,245,12288
v_accvgpr_write_b32 a64, v32
v_mul_f32_e32 v34, s54, v34
Mp 68,164,192
Rt 150,245,12800
buffer_load_dwordx4 v241, s[16:19], s92 offen offset:1024 lds
Mp 80,168,192
Rt 152,246,12288
v_mul_f32_e32 v35, s54, v35
Mp 88,168,196
Rt 154,246,12800
Cv 34,34,35
v_accvgpr_write_b32 a65, v34
Mp 92,172,196
Rt 156,247,12288
v_mul_f32_e32 v36, s54, v36
Mp 84,172,192
Rt 158,247,12800
buffer_load_dwordx4 v242, s[16:19], s92 offen offset:2048 lds
s_waitcnt lgkmcnt(8)
Mp 64,128,200
Rt 160,244,16384
v_mul_f32_e32 v37, s54, v37
Cv 36,36,37
Mp 72,128,204
Rt 162,244,16896
v_accvgpr_write_b32 a66, v36
Mp 76,132,204
Rt 164,245,16384
v_mul_f32_e32 v38, s54, v38
Mp 68,132,200
Rt 166,245,16896
buffer_load_dwordx4 v243, s[16:19], s92 offen offset:3072 lds
Mp 80,136,200
Rt 168,246,16384
v_mul_f32_e32 v39, s54, v39
Mp 88,136,204
Rt 170,246,16896
s_add_u32 s92, s42, 0x200100
Mp 92,140,204
Rt 172,247,16384
s_add_u32 m0, s41, 36864
Mp 84,140,200
Rt 174,247,16896
buffer_load_dwordx4 v240, s[16:19], s92 offen offset:0 lds
s_waitcnt lgkmcnt(8)
)" R"(
Mp 64,144,208
Rt 128,244,20480
Cv 38,38,39
Mp 72,144,212
Rt 130,244,20992
v_accvgpr_write_b32 a67, v38
Mp 76,148,212
Rt 132,245,20480
v_mul_f32_e32 v40, s54, v40
v_mul_f32_e32 v41, s54, v41
Mp 68,148,208
Rt 134,245,20992
buffer_load_dwordx4 v241, s[16:19], s92 offen offset:1024 lds
Mp 80,152,208
Rt 136,246,20480
Cv 40,40,41
Mp 88,152,212
Rt 138,246,20992
v_accvgpr_write_b32 a68, v40
v_mul_f32_e32 v42, s54, v42
Mp 92,156,212
Rt 140,247,20480
v_mul_f32_e32 v43, s54, v43
Mp 84,156,208
Rt 142,247,20992
buffer_load_dwordx4 v242, s[16:19], s92 offen offset:2048 lds
s_waitcnt lgkmcnt(8)
Mp 64,160,216
Rt 144,244,24576
Cv 42,42,43
v_accvgpr_write_b32 a69, v42
Mp 72,160,220
Rt 146,244,25088
v_mul_f32_e32 v44, s54, v44
Mp 76,164,220
Rt 148,245,24576
v_mul_f32_e32 v45, s54, v45
Mp 68,164,216
Rt 150,245,25088
s_add_u32 s92, s42, 0x1c0180
Cv 44,44,45
Mp 80,168,216
Rt 152,246,24576
s_add_u32 m0, s41, 131072
Mp 88,168,220
Rt 154,246,25088
buffer_load_dwordx4 v240, s[16:19], s92 offen offset:0 lds
Mp 92,172,220
Rt 156,247,24576
v_accvgpr_write_b32 a70, v44
Mp 84,172,216
Rt 158,247,25088
s_waitcnt vmcnt(16) lgkmcnt(8)
s_barrier
Mp 64,128,224
Rt 160,248,0
v_mul_f32_e32 v46, s54, v46
Mp 72,128,228
Rt 162,248,512
buffer_load_dwordx4 v241, s[16:19], s92 offen offset:1024 lds
Mp 76,132,228
Rt 164,249,0
v_mul_f32_e32 v47, s54, v47
Cv 46,46,47
Mp 68,132,224
)" R"(
Rt 166,249,512
v_accvgpr_write_b32 a71, v46
Mp 80,136,224
Rt 168,250,0
v_mul_f32_e32 v48, s54, v48
Mp 88,136,228
Rt 170,250,512
buffer_load_dwordx4 v242, s[16:19], s92 offen offset:2048 lds
Mp 92,140,228
Rt 172,251,0
v_mul_f32_e32 v49, s54, v49
Mp 84,140,224
Rt 174,251,512
s_waitcnt vmcnt(3) lgkmcnt(8)
s_barrier
Mp 64,144,232
Rt 128,248,4096
Cv 48,48,49
v_accvgpr_write_b32 a72, v48
Mp 72,144,236
Rt 130,248,4608
buffer_load_dwordx4 v243, s[16:19], s92 offen offset:3072 lds
Mp 76,148,236
Rt 132,249,4096
v_mul_f32_e32 v50, s54, v50
Mp 68,148,232
Rt 134,249,4608
s_add_u32 s92, s42, 0x200180
v_mul_f32_e32 v51, s54, v51
Mp 80,152,232
Rt 136,250,4096
s_add_u32 m0, s41, 135168
Mp 88,152,236
Rt 138,250,4608
buffer_load_dwordx4 v240, s[16:19], s92 offen offset:0 lds
Mp 92,156,236
Rt 140,251,4096
Cv 50,50,51
Mp 84,156,232
Rt 142,251,4608
v_accvgpr_write_b32 a73, v50
s_waitcnt lgkmcnt(8)
v_mfma_f32_16x16x32_bf16 v[96:99], a[160:163], v[128:131], 0
Rt 144,248,8192
v_mul_f32_e32 v52, s54, v52
v_mfma_f32_16x16x32_bf16 v[104:107], a[160:163], v[132:135], 0
Rt 146,248,8704
buffer_load_dwordx4 v241, s[16:19], s92 offen offset:1024 lds
v_mfma_f32_16x16x32_bf16 v[108:111], a[164:167], v[132:135], 0
Rt 148,249,8192
v_mul_f32_e32 v53, s54, v53
Cv 52,52,53
v_mfma_f32_16x16x32_bf16 v[100:103], a[164:167], v[128:131], 0
Rt 150,249,8704
v_accvgpr_write_b32 a74, v52
v_mfma_f32_16x16x32_bf16 v[112:115], a[168:171], v[128:131], 0
Rt 152,250,8192
v_mul_f32_e32 v54, s54, v54
v_mfma_f32_16x16x32_bf16 v[120:123], a[168:171], v[132:135], 0
Rt 154,250,8704
buffer_load_dwordx4 v242, s[16:19], s92 offen offset:2048 lds
v_mfma_f32_16x16x32_bf16 v[124:127], a[172:175], v[132:135], 0
Rt 156,251,8192
v_mul_f32_e32 v55, s54, v55
v_mfma_f32_16x16x32_bf16 v[116:119], a[172:175], v[128:131], 0
)" R"(
Rt 158,251,8704
s_waitcnt lgkmcnt(8)
Mp 96,128,136
Rt 160,248,12288
Cv 54,54,55
v_accvgpr_write_b32 a75, v54
Mp 104,128,140
Rt 162,248,12800
s_add_u32 s38, s38, 0xffff0080
Mp 108,132,140
Rt 164,249,12288
s_add_u32 m0, s36, 0
Mp 100,132,136
Rt 166,249,12800
buffer_load_dwordx4 v8, s[4:7], s38 offen offset:0 lds
Mp 112,136,136
Rt 168,250,12288
v_mul_f32_e32 v56, s54, v56
Mp 120,136,140
Rt 170,250,12800
v_mul_f32_e32 v57, s54, v57
Mp 124,140,140
Rt 172,251,12288
Cv 56,56,57
Mp 116,140,136
Rt 174,251,12800
buffer_load_dwordx4 v9, s[4:7], s38 offen offset:1024 lds
s_waitcnt lgkmcnt(8)
Mp 96,144,144
Rt 128,248,16384
v_accvgpr_write_b32 a76, v56
Mp 104,144,148
Rt 130,248,16896
v_mul_f32_e32 v58, s54, v58
Mp 108,148,148
Rt 132,249,16384
v_mul_f32_e32 v59, s54, v59
Cv 58,58,59
Mp 100,148,144
Rt 134,249,16896
buffer_load_dwordx4 v10, s[4:7], s38 offen offset:2048 lds
Mp 112,152,144
Rt 136,250,16384
v_accvgpr_write_b32 a77, v58
Mp 120,152,148
Rt 138,250,16896
v_mul_f32_e32 v60, s54, v60
v_mul_f32_e32 v61, s54, v61
Mp 124,156,148
Rt 140,251,16384
Cv 60,60,61
Mp 116,156,144
Rt 142,251,16896
buffer_load_dwordx4 v11, s[4:7], s38 offen offset:3072 lds
s_waitcnt lgkmcnt(8)
Mp 96,160,152
Rt 144,248,20480
v_accvgpr_write_b32 a78, v60
v_mul_f32_e32 v62, s54, v62
Mp 104,160,156
Rt 146,248,20992
s_add_u32 s38, s38, 0x10000
Mp 108,164,156
Rt 148,249,20480
)" R"(
s_add_u32 m0, s36, 4096
Mp 100,164,152
Rt 150,249,20992
buffer_load_dwordx4 v8, s[4:7], s38 offen offset:0 lds
Mp 112,168,152
Rt 152,250,20480
v_mul_f32_e32 v63, s54, v63
Mp 120,168,156
Rt 154,250,20992
Cv 62,62,63
Mp 124,172,156
Rt 156,251,20480
v_accvgpr_write_b32 a79, v62
Mp 116,172,152
Rt 158,251,20992
buffer_load_dwordx4 v9, s[4:7], s38 offen offset:1024 lds
s_waitcnt lgkmcnt(8)
Mp 96,128,160
Rt 160,248,24576
v_mul_f32_e32 v64, s54, v64
Mp 104,128,164
Rt 162,248,25088
v_mul_f32_e32 v65, s54, v65
Mp 108,132,164
Rt 164,249,24576
Cv 64,64,65
v_accvgpr_write_b32 a80, v64
Mp 100,132,160
Rt 166,249,25088
buffer_load_dwordx4 v10, s[4:7], s38 offen offset:2048 lds
Mp 112,136,160
Rt 168,250,24576
v_mul_f32_e32 v66, s54, v66
Mp 120,136,164
Rt 170,250,25088
v_mul_f32_e32 v67, s54, v67
Cv 66,66,67
Mp 124,140,164
Rt 172,251,24576
v_accvgpr_write_b32 a81, v66
Mp 116,140,160
Rt 174,251,25088
buffer_load_dwordx4 v11, s[4:7], s38 offen offset:3072 lds
s_waitcnt lgkmcnt(8)
Mp 96,144,168
Rt 128,244,32768
v_mul_f32_e32 v68, s54, v68
v_mul_f32_e32 v69, s54, v69
Mp 104,144,172
Rt 130,244,33280
Cv 68,68,69
s_mov_b32 s85, 0xc108800
Mp 108,148,172
Rt 132,245,32768
v_accvgpr_write_b32 a82, v68
v_and_b32 v2, 7, v0
Mp 100,148,168
Rt 134,245,33280
v_mul_f32_e32 v70, s54, v70
v_mul_f32_e32 v71, s54, v71
v_lshlrev_b32 v2, 7, v2
Mp 112,152,168
Rt 136,246,32768
Cv 70,70,71
)" R"(
Mp 120,152,172
Rt 138,246,33280
v_accvgpr_write_b32 a83, v70
buffer_load_dword v4, v2, s[20:23], s85 offen sc0 sc1
Mp 124,156,172
Rt 140,247,32768
v_mul_f32_e32 v72, s54, v72
Mp 116,156,168
Rt 142,247,33280
s_waitcnt lgkmcnt(8)
s_barrier
Mp 96,160,176
Rt 144,244,36864
v_mul_f32_e32 v73, s54, v73
Mp 104,160,180
Rt 146,244,37376
s_add_u32 s39, s39, 0x40000
Mp 108,164,180
Rt 148,245,36864
s_add_u32 m0, s37, 0
Cv 72,72,73
Mp 100,164,176
Rt 150,245,37376
buffer_load_dwordx4 v12, s[8:11], s39 offen offset:0 lds
Mp 112,168,176
Rt 152,246,36864
v_accvgpr_write_b32 a84, v72
Mp 120,168,180
Rt 154,246,37376
v_mul_f32_e32 v74, s54, v74
v_mul_f32_e32 v75, s54, v75
Mp 124,172,180
Rt 156,247,36864
Cv 74,74,75
Mp 116,172,176
Rt 158,247,37376
s_waitcnt lgkmcnt(8)
Mp 96,128,184
Rt 160,244,40960
v_accvgpr_write_b32 a85, v74
v_mul_f32_e32 v76, s54, v76
Mp 104,128,188
Rt 162,244,41472
buffer_load_dwordx4 v13, s[8:11], s39 offen offset:1024 lds
Mp 108,132,188
Rt 164,245,40960
v_mul_f32_e32 v77, s54, v77
Mp 100,132,184
Rt 166,245,41472
Cv 76,76,77
v_accvgpr_write_b32 a86, v76
Mp 112,136,184
Rt 168,246,40960
v_mul_f32_e32 v78, s54, v78
Mp 120,136,188
Rt 170,246,41472
v_mul_f32_e32 v79, s54, v79
Mp 124,140,188
Rt 172,247,40960
Cv 78,78,79
Mp 116,140,184
Rt 174,247,41472
buffer_load_dwordx4 v14, s[8:11], s39 offen offset:2048 lds
s_waitcnt lgkmcnt(8)
Mp 96,144,192
)" R"(
Rt 128,244,45056
v_accvgpr_write_b32 a87, v78
Mp 104,144,196
Rt 130,244,45568
v_mul_f32_e32 v80, s54, v80
Mp 108,148,196
Rt 132,245,45056
v_mul_f32_e32 v81, s54, v81
Cv 80,80,81
Mp 100,148,192
Rt 134,245,45568
v_accvgpr_write_b32 a88, v80
Mp 112,152,192
Rt 136,246,45056
v_mul_f32_e32 v82, s54, v82
Mp 120,152,196
Rt 138,246,45568
buffer_load_dwordx4 v15, s[8:11], s39 offen offset:3072 lds
Mp 124,156,196
Rt 140,247,45056
v_mul_f32_e32 v83, s54, v83
Mp 116,156,192
Rt 142,247,45568
s_waitcnt lgkmcnt(8)
Mp 96,160,200
Rt 144,244,49152
Cv 82,82,83
v_accvgpr_write_b32 a89, v82
Mp 104,160,204
Rt 146,244,49664
s_add_u32 s39, s39, 0x40000
Mp 108,164,204
Rt 148,245,49152
s_add_u32 m0, s37, 16384
Mp 100,164,200
Rt 150,245,49664
buffer_load_dwordx4 v12, s[8:11], s39 offen offset:0 lds
Mp 112,168,200
Rt 152,246,49152
v_mul_f32_e32 v84, s54, v84
Mp 120,168,204
Rt 154,246,49664
v_mul_f32_e32 v85, s54, v85
Mp 124,172,204
Rt 156,247,49152
Cv 84,84,85
Mp 116,172,200
Rt 158,247,49664
v_accvgpr_write_b32 a90, v84
s_waitcnt lgkmcnt(8)
Mp 96,128,208
Rt 160,244,53248
v_mul_f32_e32 v86, s54, v86
Mp 104,128,212
Rt 162,244,53760
buffer_load_dwordx4 v13, s[8:11], s39 offen offset:1024 lds
Mp 108,132,212
Rt 164,245,53248
v_mul_f32_e32 v87, s54, v87
Cv 86,86,87
Mp 100,132,208
Rt 166,245,53760
v_accvgpr_write_b32 a91, v86
Mp 112,136,208
)" R"(
Rt 168,246,53248
v_mul_f32_e32 v88, s54, v88
Mp 120,136,212
Rt 170,246,53760
v_mul_f32_e32 v89, s54, v89
Cv 88,88,89
Mp 124,140,212
Rt 172,247,53248
v_accvgpr_write_b32 a92, v88
Mp 116,140,208
Rt 174,247,53760
buffer_load_dwordx4 v14, s[8:11], s39 offen offset:2048 lds
s_waitcnt lgkmcnt(8)
Mp 96,144,216
Rt 128,244,57344
v_mul_f32_e32 v90, s54, v90
v_mul_f32_e32 v91, s54, v91
Mp 104,144,220
Rt 130,244,57856
Cv 90,90,91
Mp 108,148,220
Rt 132,245,57344
v_accvgpr_write_b32 a93, v90
Mp 100,148,216
Rt 134,245,57856
v_mul_f32_e32 v92, s54, v92
v_mul_f32_e32 v93, s54, v93
Mp 112,152,216
Rt 136,246,57344
Cv 92,92,93
Mp 120,152,220
Rt 138,246,57856
buffer_load_dwordx4 v15, s[8:11], s39 offen offset:3072 lds
Mp 124,156,220
Rt 140,247,57344
v_accvgpr_write_b32 a94, v92
Mp 116,156,216
Rt 142,247,57856
v_mul_f32_e32 v94, s54, v94
s_waitcnt lgkmcnt(8)
Mp 96,160,224
Rt 144,248,32768
v_mul_f32_e32 v95, s54, v95
Mp 104,160,228
Rt 146,248,33280
Cv 94,94,95
Mp 108,164,228
Rt 148,249,32768
v_accvgpr_write_b32 a95, v94
Mp 100,164,224
Rt 150,249,33280
s_lshl1_add_u32 s88, s34, s91
Mp 112,168,224
Rt 152,250,32768
s_lshl_b32 s88, s88, 12
Mp 120,168,228
Rt 154,250,33280
s_and_b32 s29, s38, 0xfffe0000
Mp 124,172,228
Rt 156,251,32768
s_add_u32 s88, s88, s29
Mp 116,172,224
Rt 158,251,33280
s_waitcnt vmcnt(8) lgkmcnt(8)
s_barrier
)" R"(
Mp 96,128,232
Rt 160,248,36864
v_min_u32 v28, 31, v0
v_xor_b32 v4, s3, v4
v_cmp_gt_u32_e32 vcc, 2, v4
s_andn2_b32 s28, 0xff, vcc_lo
s_cbranch_scc1 .Lzsp13
.Lzok13:
Mp 104,128,236
Rt 162,248,37376
s_add_u32 s38, s38, 0xffff0080
v_xor_b32 v4, s3, v4
Mp 108,132,236
Rt 164,249,36864
s_add_u32 m0, s36, 32768
v_and_b32 v4, 1, v4
Mp 100,132,232
Rt 166,249,37376
buffer_load_dwordx4 v8, s[4:7], s38 offen offset:0 lds
Mp 112,136,232
Rt 168,250,36864
v_lshlrev_b32 v28, 7, v28
Mp 120,136,236
Rt 170,250,37376
v_cmp_eq_u32_e32 vcc, 1, v4
s_mov_b32 s85, vcc_lo
Mp 124,140,236
Rt 172,251,36864
s_and_b32 s28, s2, 7
s_lshr_b32 s28, s85, s28
Mp 116,140,232
Rt 174,251,37376
s_xor_b32 s28, s28, s34
s_and_b32 s28, s28, 1
s_waitcnt lgkmcnt(8)
v_mfma_f32_16x16x32_bf16 v[64:67], a[144:147], v[128:131], 0
Rt 128,248,40960
s_lshl_b32 s84, s28, 13
s_add_u32 s49, s49, s84
v_mfma_f32_16x16x32_bf16 v[72:75], a[144:147], v[132:135], 0
Rt 130,248,41472
buffer_load_dwordx4 v9, s[4:7], s38 offen offset:1024 lds
v_mfma_f32_16x16x32_bf16 v[76:79], a[148:151], v[132:135], 0
Rt 132,249,40960
s_add_u32 s50, s50, s84
s_add_u32 s60, s50, 7168
v_mfma_f32_16x16x32_bf16 v[68:71], a[148:151], v[128:131], 0
Rt 134,249,41472
s_add_u32 s52, s52, s84
s_add_u32 s65, s65, s84
v_mfma_f32_16x16x32_bf16 v[80:83], a[152:155], v[128:131], 0
Rt 136,250,40960
v_mul_f32_e32 v96, s54, v96
s_lshr_b32 s28, s2, 6
s_lshl_b32 s28, s28, 14
v_mfma_f32_16x16x32_bf16 v[88:91], a[152:155], v[132:135], 0
Rt 138,250,41472
v_mul_f32_e32 v97, s54, v97
s_add_u32 s28, s28, s50
v_mfma_f32_16x16x32_bf16 v[92:95], a[156:159], v[132:135], 0
Rt 140,251,40960
Cv 96,96,97
s_bfe_u32 s29, s2, 0x20004
v_mfma_f32_16x16x32_bf16 v[84:87], a[156:159], v[128:131], 0
)" R"(
Rt 142,251,41472
buffer_load_dwordx4 v10, s[4:7], s38 offen offset:2048 lds
s_waitcnt lgkmcnt(8)
Mp 64,160,136
Rt 144,248,45056
v_accvgpr_write_b32 a96, v96
s_lshl2_add_u32 s29, s29, s33
Mp 72,160,140
Rt 146,248,45568
v_mul_f32_e32 v98, s54, v98
s_lshl_b32 s29, s29, 9
Mp 76,164,140
Rt 148,249,45056
v_mul_f32_e32 v99, s54, v99
s_add_u32 s28, s28, s29
Mp 68,164,136
Rt 150,249,45568
Cv 98,98,99
v_min_u32 v4, 3, v0
Mp 80,168,136
Rt 152,250,45056
v_accvgpr_write_b32 a97, v98
v_lshlrev_b32 v4, 7, v4
Mp 88,168,140
Rt 154,250,45568
buffer_load_dwordx4 v11, s[4:7], s38 offen offset:3072 lds
Mp 92,172,140
Rt 156,251,45056
v_mul_f32_e32 v100, s54, v100
Mp 84,172,136
Rt 158,251,45568
s_waitcnt lgkmcnt(8)
Mp 64,128,144
Rt 160,248,49152
v_mul_f32_e32 v101, s54, v101
Mp 72,128,148
Rt 162,248,49664
s_add_u32 s38, s38, 0x10000
Mp 76,132,148
Rt 164,249,49152
s_add_u32 m0, s36, 36864
Mp 68,132,144
Rt 166,249,49664
buffer_load_dwordx4 v8, s[4:7], s38 offen offset:0 lds
Mp 80,136,144
Rt 168,250,49152
Cv 100,100,101
buffer_load_dword v4, v4, s[20:23], s28 offen
Mp 88,136,148
Rt 170,250,49664
v_accvgpr_write_b32 a98, v100
s_lshl1_add_u32 s28, s36, s84
Mp 92,140,148
Rt 172,251,49152
v_mul_f32_e32 v102, s54, v102
v_add_u32 v2, s28, v1
Mp 84,140,144
Rt 174,251,49664
v_add_u32 v3, 0x1000, v2
s_waitcnt lgkmcnt(8)
Mp 64,144,152
Rt 128,248,53248
v_mul_f32_e32 v103, s54, v103
s_lshl_b32 s28, s34, 13
)" R"(
Mp 72,144,156
Rt 130,248,53760
buffer_load_dwordx4 v9, s[4:7], s38 offen offset:1024 lds
s_xor_b32 s28, s28, s84
Mp 76,148,156
Rt 132,249,53248
Cv 102,102,103
s_and_b32 s28, s28, 0x2000
Mp 68,148,152
Rt 134,249,53760
v_accvgpr_write_b32 a99, v102
s_xor_b32 s73, s73, s28
Mp 80,152,152
Rt 136,250,53248
v_mul_f32_e32 v104, s54, v104
s_xor_b32 s72, s72, s28
Mp 88,152,156
Rt 138,250,53760
v_mul_f32_e32 v105, s54, v105
Mp 92,156,156
Rt 140,251,53248
Cv 104,104,105
Mp 84,156,152
Rt 142,251,53760
buffer_load_dwordx4 v10, s[4:7], s38 offen offset:2048 lds
s_waitcnt lgkmcnt(8)
Mp 64,160,160
Rt 144,248,57344
v_accvgpr_write_b32 a100, v104
Mp 72,160,164
Rt 146,248,57856
v_mul_f32_e32 v106, s54, v106
Mp 76,164,164
Rt 148,249,57344
v_mul_f32_e32 v107, s54, v107
Mp 68,164,160
Rt 150,249,57856
Cv 106,106,107
Mp 80,168,160
Rt 152,250,57344
v_accvgpr_write_b32 a101, v106
Mp 88,168,164
Rt 154,250,57856
buffer_load_dwordx4 v11, s[4:7], s38 offen offset:3072 lds
Mp 92,172,164
Rt 156,251,57344
v_mul_f32_e32 v108, s54, v108
Mp 84,172,160
Rt 158,251,57856
s_waitcnt lgkmcnt(8)
Mp 64,128,168
Rt 160,252,0
v_mul_f32_e32 v109, s54, v109
Mp 72,128,172
Rt 162,252,512
Cv 108,108,109
Mp 76,132,172
Rt 164,253,0
v_accvgpr_write_b32 a102, v108
Mp 68,132,168
Rt 166,253,512
v_mul_f32_e32 v110, s54, v110
Mp 80,136,168
Rt 168,254,0
)" R"(
v_mul_f32_e32 v111, s54, v111
Mp 88,136,172
Rt 170,254,512
Cv 110,110,111
Mp 92,140,172
Rt 172,255,0
v_accvgpr_write_b32 a103, v110
Mp 84,140,168
Rt 174,255,512
s_waitcnt vmcnt(13) lgkmcnt(8)
s_barrier
Mp 64,144,176
Rt 128,252,4096
v_mul_f32_e32 v112, s54, v112
Mp 72,144,180
Rt 130,252,4608
s_add_u32 s39, s39, 0x40000
Mp 76,148,180
Rt 132,253,4096
s_add_u32 m0, s37, 32768
Mp 68,148,176
Rt 134,253,4608
buffer_load_dwordx4 v12, s[8:11], s39 offen offset:0 lds
Mp 80,152,176
Rt 136,254,4096
v_mul_f32_e32 v113, s54, v113
Mp 88,152,180
Rt 138,254,4608
Cv 112,112,113
Mp 92,156,180
Rt 140,255,4096
v_accvgpr_write_b32 a104, v112
Mp 84,156,176
Rt 142,255,4608
s_waitcnt lgkmcnt(8)
Mp 64,160,184
Rt 144,252,8192
v_mul_f32_e32 v114, s54, v114
Mp 72,160,188
Rt 146,252,8704
buffer_load_dwordx4 v13, s[8:11], s39 offen offset:1024 lds
Mp 76,164,188
Rt 148,253,8192
v_mul_f32_e32 v115, s54, v115
Mp 68,164,184
Rt 150,253,8704
Cv 114,114,115
Mp 80,168,184
Rt 152,254,8192
v_accvgpr_write_b32 a105, v114
Mp 88,168,188
Rt 154,254,8704
v_mul_f32_e32 v116, s54, v116
Mp 92,172,188
Rt 156,255,8192
v_mul_f32_e32 v117, s54, v117
Mp 84,172,184
Rt 158,255,8704
buffer_load_dwordx4 v14, s[8:11], s39 offen offset:2048 lds
s_waitcnt lgkmcnt(8)
Mp 64,128,192
Rt 160,252,12288
Cv 116,116,117
Mp 72,128,196
Rt 162,252,12800
)" R"(
v_accvgpr_write_b32 a106, v116
Mp 76,132,196
Rt 164,253,12288
v_mul_f32_e32 v118, s54, v118
Mp 68,132,192
Rt 166,253,12800
v_mul_f32_e32 v119, s54, v119
Mp 80,136,192
Rt 168,254,12288
Cv 118,118,119
Mp 88,136,196
Rt 170,254,12800
buffer_load_dwordx4 v15, s[8:11], s39 offen offset:3072 lds
Mp 92,140,196
Rt 172,255,12288
v_accvgpr_write_b32 a107, v118
Mp 84,140,192
Rt 174,255,12800
s_waitcnt lgkmcnt(8)
Mp 64,144,200
Rt 128,252,16384
v_mul_f32_e32 v120, s54, v120
Mp 72,144,204
Rt 130,252,16896
s_add_u32 s39, s39, 0x40000
Mp 76,148,204
Rt 132,253,16384
s_add_u32 m0, s37, 49152
Mp 68,148,200
Rt 134,253,16896
buffer_load_dwordx4 v12, s[8:11], s39 offen offset:0 lds
Mp 80,152,200
Rt 136,254,16384
v_mul_f32_e32 v121, s54, v121
Mp 88,152,204
Rt 138,254,16896
Cv 120,120,121
Mp 92,156,204
Rt 140,255,16384
v_accvgpr_write_b32 a108, v120
Mp 84,156,200
Rt 142,255,16896
s_waitcnt lgkmcnt(8)
Mp 64,160,208
Rt 144,252,20480
Rt 0,18,0
Mp 72,160,212
Rt 146,252,20992
buffer_load_dwordx4 v13, s[8:11], s39 offen offset:1024 lds
Rt 2,18,2048
Mp 76,164,212
Rt 148,253,20480
Rt 4,19,0
Mp 68,164,208
Rt 150,253,20992
Rt 6,19,2048
Mp 80,168,208
Rt 152,254,20480
Rt 8,20,0
Mp 88,168,212
Rt 154,254,20992
Rt 10,20,2048
Mp 92,172,212
Rt 156,255,20480
)" R"(
Rt 12,21,0
Mp 84,172,208
Rt 158,255,20992
buffer_load_dwordx4 v14, s[8:11], s39 offen offset:2048 lds
Rt 14,21,2048
Mp 64,128,216
Rt 160,252,24576
Rt 16,22,0
Mp 72,128,220
Rt 162,252,25088
Rt 18,22,2048
Mp 76,132,220
Rt 164,253,24576
Rt 20,23,0
Mp 68,132,216
Rt 166,253,25088
Rt 22,23,2048
Mp 80,136,216
Rt 168,254,24576
Rt 24,24,0
Mp 88,136,220
Rt 170,254,25088
buffer_load_dwordx4 v15, s[8:11], s39 offen offset:3072 lds
Rt 26,24,2048
Mp 92,140,220
Rt 172,255,24576
Rt 28,25,0
Mp 84,140,216
Rt 174,255,25088
Rt 30,25,2048
Mp 64,144,224
Rb 32,16,0
v_mul_f32_e32 v122, s54, v122
Mp 72,144,228
Rb 36,16,2048
v_mul_f32_e32 v123, s54, v123
Mp 76,148,228
Rb 40,16,8192
Cv 122,122,123
Mp 68,148,224
Rb 44,16,10240
v_accvgpr_write_b32 a109, v122
Mp 80,152,224
Rb 48,16,16384
v_mul_f32_e32 v124, s54, v124
Mp 88,152,228
Rb 52,16,18432
v_mul_f32_e32 v125, s54, v125
Mp 92,156,228
Rb 56,16,24576
Cv 124,124,125
Mp 84,156,224
Rb 60,16,26624
s_waitcnt lgkmcnt(9)
Mp 64,160,232
v_accvgpr_write_b32 a110, v124
v_mul_f32_e32 v126, s54, v126
Mp 72,160,236
v_mul_f32_e32 v127, s54, v127
Cv 126,126,127
Mp 76,164,236
v_accvgpr_write_b32 a111, v126
Mp 68,164,232
buffer_load_dword a254, v28, s[4:7], s88 offen
)" R"(
Mp 80,168,232
Mp 88,168,236
Mp 92,172,236
Mp 84,172,232
s_cmp_eq_u32 s91, 1
s_waitcnt vmcnt(18) lgkmcnt(0)
s_barrier
s_cbranch_scc1 .LzBV
Mz 32,0,32
Rt 128,18,16384
v_mul_f32_e32 v64, s54, v64
Mz 96,0,40
v_mul_f32_e32 v65, s54, v65
Cv 64,64,65
Mz 128,0,44
Rt 130,18,18432
v_accvgpr_write_b32 a112, v64
Mz 160,0,48
s_add_u32 s39, s39, 0x40000
v_mul_f32_e32 v66, s54, v66
Mz 192,0,52
Rt 132,19,16384
s_add_u32 m0, s37, 0
Mz 224,0,56
buffer_load_dwordx4 v12, s[8:11], s39 offen offset:0 lds
v_mfma_f32_16x16x32_bf16 a[192:195], a[0:3], a[60:63], 0
Rt 134,19,18432
v_mul_f32_e32 v67, s54, v67
v_mfma_f32_16x16x32_bf16 a[196:199], a[4:7], a[60:63], 0
Cv 66,66,67
v_accvgpr_write_b32 a113, v66
Mz 228,4,56
Rt 136,20,16384
v_mul_f32_e32 v68, s54, v68
Mz 196,4,52
v_mul_f32_e32 v69, s54, v69
Cv 68,68,69
buffer_store_dwordx4 a[64:67], v2, s[20:23], s51 offen offset:0 sc0 sc1
Mz 164,4,48
Rt 138,20,18432
v_accvgpr_write_b32 a114, v68
Mz 132,4,44
v_mul_f32_e32 v70, s54, v70
v_mul_f32_e32 v71, s54, v71
Mz 100,4,40
Rt 140,21,16384
Cv 70,70,71
Mz 36,4,32
buffer_load_dwordx4 v13, s[8:11], s39 offen offset:1024 lds
Mz 40,8,32
Rt 142,21,18432
v_accvgpr_write_b32 a115, v70
Mz 104,8,40
v_mul_f32_e32 v72, s54, v72
v_mul_f32_e32 v73, s54, v73
Mz 136,8,44
Rt 144,22,16384
Cv 72,72,73
Mz 168,8,48
v_accvgpr_write_b32 a116, v72
v_mul_f32_e32 v74, s54, v74
buffer_store_dwordx4 a[68:71], v2, s[20:23], s51 offen offset:1024 sc0 sc1
Mz 200,8,52
Rt 146,22,18432
v_mul_f32_e32 v75, s54, v75
)" R"(
Mz 232,8,56
Cv 74,74,75
v_accvgpr_write_b32 a117, v74
v_mfma_f32_16x16x32_bf16 a[200:203], a[8:11], a[60:63], 0
Rt 148,23,16384
v_mul_f32_e32 v76, s54, v76
v_mfma_f32_16x16x32_bf16 a[204:207], a[12:15], a[60:63], 0
buffer_load_dwordx4 v14, s[8:11], s39 offen offset:2048 lds
Mz 236,12,56
Rt 150,23,18432
v_mul_f32_e32 v77, s54, v77
Mz 204,12,52
Cv 76,76,77
v_accvgpr_write_b32 a118, v76
Mz 172,12,48
Rt 152,24,16384
v_mul_f32_e32 v78, s54, v78
Mz 140,12,44
v_mul_f32_e32 v79, s54, v79
Cv 78,78,79
buffer_store_dwordx4 a[72:75], v2, s[20:23], s51 offen offset:2048 sc0 sc1
Mz 108,12,40
Rt 154,24,18432
v_accvgpr_write_b32 a119, v78
Mz 44,12,32
v_mul_f32_e32 v80, s54, v80
v_mul_f32_e32 v81, s54, v81
Mz 48,16,32
Rt 156,25,16384
Cv 80,80,81
Mz 112,16,40
buffer_load_dwordx4 v15, s[8:11], s39 offen offset:3072 lds
Mz 144,16,44
Rt 158,25,18432
v_accvgpr_write_b32 a120, v80
Mz 176,16,48
v_mul_f32_e32 v82, s54, v82
v_mul_f32_e32 v83, s54, v83
Mz 208,16,52
Rb 160,17,0
Cv 82,82,83
Mz 240,16,56
v_accvgpr_write_b32 a121, v82
v_mul_f32_e32 v84, s54, v84
buffer_store_dwordx4 a[76:79], v2, s[20:23], s51 offen offset:3072 sc0 sc1
v_mfma_f32_16x16x32_bf16 a[208:211], a[16:19], a[60:63], 0
Rb 164,17,2048
v_mul_f32_e32 v85, s54, v85
v_mfma_f32_16x16x32_bf16 a[212:215], a[20:23], a[60:63], 0
Cv 84,84,85
v_accvgpr_write_b32 a122, v84
Mz 244,20,56
Rb 168,17,8192
v_mul_f32_e32 v86, s54, v86
Mz 212,20,52
v_mul_f32_e32 v87, s54, v87
Cv 86,86,87
Mz 180,20,48
Rb 172,17,10240
v_accvgpr_write_b32 a123, v86
Mz 148,20,44
v_mul_f32_e32 v88, s54, v88
v_mul_f32_e32 v89, s54, v89
Mz 116,20,40
)" R"(
Rb 176,17,16384
Cv 88,88,89
Mz 52,20,32
v_accvgpr_write_b32 a124, v88
v_mul_f32_e32 v90, s54, v90
buffer_store_dwordx4 a[80:83], v3, s[20:23], s51 offen offset:0 sc0 sc1
Mz 56,24,32
Rb 180,17,18432
v_mul_f32_e32 v91, s54, v91
Mz 120,24,40
Cv 90,90,91
v_accvgpr_write_b32 a125, v90
Mz 152,24,44
Rb 184,17,24576
v_mul_f32_e32 v92, s54, v92
Mz 184,24,48
v_mul_f32_e32 v93, s54, v93
Cv 92,92,93
Mz 216,24,52
Rb 188,17,26624
v_accvgpr_write_b32 a126, v92
Mz 248,24,56
v_mul_f32_e32 v94, s54, v94
v_mul_f32_e32 v95, s54, v95
v_mfma_f32_16x16x32_bf16 a[216:219], a[24:27], a[60:63], 0
Cv 94,94,95
v_accvgpr_write_b32 a127, v94
v_mfma_f32_16x16x32_bf16 a[220:223], a[28:31], a[60:63], 0
buffer_store_dwordx4 a[84:87], v3, s[20:23], s51 offen offset:1024 sc0 sc1
Mz 252,28,56
Mz 220,28,52
Mz 188,28,48
Mz 156,28,44
Mz 124,28,40
Mz 60,28,32
Mz 64,0,36
Mz 68,4,36
buffer_store_dwordx4 a[88:91], v3, s[20:23], s51 offen offset:2048 sc0 sc1
Mz 72,8,36
Mz 76,12,36
Mz 80,16,36
Mz 84,20,36
Mz 88,24,36
Mz 92,28,36
s_waitcnt vmcnt(16) lgkmcnt(0)
s_barrier
Mv 32,128,160
Rt 0,18,32768
Mv 64,128,164
s_add_u32 s38, s38, 0xffff0080
Mv 96,128,168
Rt 2,18,34816
s_add_u32 m0, s36, 0
Mv 128,128,172
buffer_load_dwordx4 v8, s[4:7], s38 offen offset:0 lds
Mv 160,128,176
Rt 4,19,32768
Mv 192,128,180
Mv 224,128,184
Rt 6,19,34816
Ma 192,128,188
buffer_load_dwordx4 v9, s[4:7], s38 offen offset:1024 lds
Ma 196,132,188
Rt 8,20,32768
Mv 228,132,184
)" R"(
Mv 196,132,180
Rt 10,20,34816
Mv 164,132,176
buffer_load_dwordx4 v10, s[4:7], s38 offen offset:2048 lds
Mv 132,132,172
Rt 12,21,32768
Mv 100,132,168
Mv 68,132,164
Rt 14,21,34816
Mv 36,132,160
buffer_load_dwordx4 v11, s[4:7], s38 offen offset:3072 lds
Mv 40,136,160
Rt 16,22,32768
Mv 72,136,164
s_add_u32 s38, s38, 0x10000
Mv 104,136,168
Rt 18,22,34816
s_add_u32 m0, s36, 4096
Mv 136,136,172
buffer_load_dwordx4 v8, s[4:7], s38 offen offset:0 lds
Mv 168,136,176
Rt 20,23,32768
Mv 200,136,180
Mv 232,136,184
Rt 22,23,34816
Ma 200,136,188
buffer_load_dwordx4 v9, s[4:7], s38 offen offset:1024 lds
Ma 204,140,188
Rt 24,24,32768
Mv 236,140,184
Mv 204,140,180
Rt 26,24,34816
Mv 172,140,176
buffer_load_dwordx4 v10, s[4:7], s38 offen offset:2048 lds
Mv 140,140,172
Rt 28,25,32768
Mv 108,140,168
Mv 76,140,164
Rt 30,25,34816
Mv 44,140,160
buffer_load_dwordx4 v11, s[4:7], s38 offen offset:3072 lds
Mv 48,144,160
Rb 32,16,32768
Mv 80,144,164
Mv 112,144,168
Rb 36,16,34816
Mv 144,144,172
s_add_u32 s39, s39, 0x40000
Mv 176,144,176
Rb 40,16,40960
s_add_u32 m0, s37, 16384
Mv 208,144,180
buffer_load_dwordx4 v12, s[8:11], s39 offen offset:0 lds
Mv 240,144,184
Rb 44,16,43008
Ma 208,144,188
Ma 212,148,188
Rb 48,16,49152
Mv 244,148,184
Mv 212,148,180
Rb 52,16,51200
Mv 180,148,176
buffer_load_dwordx4 v13, s[8:11], s39 offen offset:1024 lds
Mv 148,148,172
)" R"(
Rb 56,16,57344
Mv 116,148,168
Mv 84,148,164
Rb 60,16,59392
Mv 52,148,160
Mv 56,152,160
Mv 88,152,164
buffer_load_dwordx4 v14, s[8:11], s39 offen offset:2048 lds
Mv 120,152,168
Mv 152,152,172
Mv 184,152,176
Mv 216,152,180
Mv 248,152,184
Ma 216,152,188
buffer_load_dwordx4 v15, s[8:11], s39 offen offset:3072 lds
Ma 220,156,188
Mv 252,156,184
Mv 220,156,180
Mv 188,156,176
buffer_store_dwordx4 a[92:95], v3, s[20:23], s51 offen offset:3072 sc0 sc1
Mv 156,156,172
Mv 124,156,168
Mv 92,156,164
Mv 60,156,160
s_waitcnt vmcnt(25) lgkmcnt(0)
s_barrier
Mv 32,0,32
Rt 128,18,49152
s_mov_b64 s[16:17], s[76:77]
Mv 64,0,36
s_mov_b32 s18, 0x188000
Mv 96,0,40
Rt 130,18,51200
Mv 128,0,44
s_add_u32 s39, s39, 0x40000
Mv 160,0,48
Rt 132,19,49152
s_add_u32 m0, s37, 32768
Mv 192,0,52
buffer_load_dwordx4 v12, s[8:11], s39 offen offset:0 lds
Mv 224,0,56
Rt 134,19,51200
Ma 192,0,60
s_cmp_eq_u32 s33, 0
s_cmov_b32 s18, 0x380000
Ma 196,4,60
Rt 136,20,49152
s_mul_i32 s43, s2, 3072
Mv 228,4,56
s_add_u32 s51, s51, 0x10000
buffer_store_dwordx4 a[96:99], v2, s[20:23], s51 offen offset:0 sc0 sc1
Mv 196,4,52
Rt 138,20,51200
Mv 164,4,48
s_cmp_eq_u32 s33, 0
s_cselect_b32 s44, 3, 6
Mv 132,4,44
Rt 140,21,49152
s_mul_i32 s44, s44, 0x60000
Mv 100,4,40
buffer_load_dwordx4 v13, s[8:11], s39 offen offset:1024 lds
Mv 68,4,36
Rt 142,21,51200
s_sub_u32 s43, s43, s44
Mv 36,4,32
)" R"(
v_min_u32 v26, 23, v0
v_lshlrev_b32 v26, 7, v26
Mv 40,8,32
Rt 144,22,49152
v_and_b32 v5, 7, v0
Mv 72,8,36
buffer_store_dwordx4 a[100:103], v2, s[20:23], s51 offen offset:1024 sc0 sc1
Mv 104,8,40
Rt 146,22,51200
v_lshlrev_b32 v5, 7, v5
Mv 136,8,44
Mv 168,8,48
Rt 148,23,49152
Mv 200,8,52
buffer_load_dwordx4 v14, s[8:11], s39 offen offset:2048 lds
Mv 232,8,56
Rt 150,23,51200
Ma 200,8,60
Ma 204,12,60
Rt 152,24,49152
Mv 236,12,56
buffer_store_dwordx4 a[104:107], v2, s[20:23], s51 offen offset:2048 sc0 sc1
Mv 204,12,52
Rt 154,24,51200
Mv 172,12,48
Mv 140,12,44
Rt 156,25,49152
Mv 108,12,40
buffer_load_dwordx4 v15, s[8:11], s39 offen offset:3072 lds
Mv 76,12,36
Rt 158,25,51200
Mv 44,12,32
Mv 48,16,32
Rb 160,17,32768
Mv 80,16,36
buffer_store_dwordx4 a[108:111], v2, s[20:23], s51 offen offset:3072 sc0 sc1
Mv 112,16,40
Rb 164,17,34816
Mv 144,16,44
Mv 176,16,48
Rb 168,17,40960
Mv 208,16,52
Mv 240,16,56
Rb 172,17,43008
Ma 208,16,60
Ma 212,20,60
Rb 176,17,49152
Mv 244,20,56
buffer_store_dwordx4 a[112:115], v3, s[20:23], s51 offen offset:0 sc0 sc1
Mv 212,20,52
Rb 180,17,51200
Mv 180,20,48
Mv 148,20,44
Rb 184,17,57344
Mv 116,20,40
Mv 84,20,36
Rb 188,17,59392
Mv 52,20,32
Mv 56,24,32
Mv 88,24,36
buffer_store_dwordx4 a[116:119], v3, s[20:23], s51 offen offset:1024 sc0 sc1
Mv 120,24,40
Mv 152,24,44
Mv 184,24,48
)" R"(
Mv 216,24,52
Mv 248,24,56
Ma 216,24,60
Ma 220,28,60
Mv 252,28,56
buffer_store_dwordx4 a[120:123], v3, s[20:23], s51 offen offset:2048 sc0 sc1
Mv 220,28,52
Mv 188,28,48
Mv 156,28,44
Mv 124,28,40
Mv 92,28,36
Mv 60,28,32
s_waitcnt vmcnt(16) lgkmcnt(0)
s_barrier
Mv 32,128,160
Rt 0,18,0
v_accvgpr_write_b32 a224, -1
Mv 64,128,164
s_add_u32 s38, s38, 0xffff0080
v_accvgpr_write_b32 a225, -1
Mv 96,128,168
Rt 2,18,2048
s_add_u32 m0, s36, 32768
Mv 128,128,172
buffer_load_dwordx4 v8, s[4:7], s38 offen offset:0 lds
Mv 160,128,176
Rt 4,19,0
v_accvgpr_write_b32 a226, -1
Mv 192,128,180
v_accvgpr_write_b32 a227, -1
s_add_u32 s88, s73, 0x4000
Mv 224,128,184
Rt 6,19,2048
s_mov_b32 s40, 6
Ma 192,128,188
buffer_load_dwordx4 v9, s[4:7], s38 offen offset:1024 lds
Ma 196,132,188
Rt 8,20,0
s_mov_b32 s94, 0
Mv 228,132,184
Mv 196,132,180
Rt 10,20,2048
Mv 164,132,176
buffer_load_dwordx4 v10, s[4:7], s38 offen offset:2048 lds
Mv 132,132,172
Rt 12,21,0
Mv 100,132,168
Mv 68,132,164
Rt 14,21,2048
Mv 36,132,160
buffer_load_dwordx4 v11, s[4:7], s38 offen offset:3072 lds
Mv 40,136,160
Rt 16,22,0
Mv 72,136,164
s_add_u32 s38, s38, 0x10000
Mv 104,136,168
Rt 18,22,2048
s_add_u32 m0, s36, 36864
Mv 136,136,172
buffer_load_dwordx4 v8, s[4:7], s38 offen offset:0 lds
Mv 168,136,176
Rt 20,23,0
Mv 200,136,180
Mv 232,136,184
Rt 22,23,2048
)" R"(
Ma 200,136,188
buffer_load_dwordx4 v9, s[4:7], s38 offen offset:1024 lds
Ma 204,140,188
Rt 24,24,0
Mv 236,140,184
Mv 204,140,180
Rt 26,24,2048
Mv 172,140,176
buffer_load_dwordx4 v10, s[4:7], s38 offen offset:2048 lds
Mv 140,140,172
Rt 28,25,0
Mv 108,140,168
Mv 76,140,164
Rt 30,25,2048
Mv 44,140,160
buffer_load_dwordx4 v11, s[4:7], s38 offen offset:3072 lds
Mv 48,144,160
Rb 32,16,0
Mv 80,144,164
Mv 112,144,168
Rb 36,16,2048
Mv 144,144,172
s_add_u32 s39, s39, 0x40000
Mv 176,144,176
Rb 40,16,8192
s_add_u32 m0, s37, 49152
Mv 208,144,180
buffer_load_dwordx4 v12, s[8:11], s39 offen offset:0 lds
Mv 240,144,184
Rb 44,16,10240
Ma 208,144,188
Ma 212,148,188
Rb 48,16,16384
Mv 244,148,184
Mv 212,148,180
Rb 52,16,18432
Mv 180,148,176
buffer_load_dwordx4 v13, s[8:11], s39 offen offset:1024 lds
Mv 148,148,172
Rb 56,16,24576
Mv 116,148,168
Mv 84,148,164
Rb 60,16,26624
Mv 52,148,160
Mv 56,152,160
Mv 88,152,164
buffer_load_dwordx4 v14, s[8:11], s39 offen offset:2048 lds
Mv 120,152,168
Mv 152,152,172
Mv 184,152,176
Mv 216,152,180
Mv 248,152,184
Ma 216,152,188
buffer_load_dwordx4 v15, s[8:11], s39 offen offset:3072 lds
Ma 220,156,188
Mv 252,156,184
Mv 220,156,180
Mv 188,156,176
buffer_store_dwordx4 a[124:127], v3, s[20:23], s51 offen offset:3072 sc0 sc1
Mv 156,156,172
Mv 124,156,168
Mv 92,156,164
Mv 60,156,160
s_waitcnt vmcnt(25) lgkmcnt(0)
s_barrier
)" R"(
.LzPBK:
Mv 32,0,32
Rt 128,18,16384
Mv 64,0,36
Mv 96,0,40
Rt 130,18,18432
Mv 128,0,44
s_add_u32 s39, s39, 0x40000
Mv 160,0,48
Rt 132,19,16384
s_add_u32 m0, s37, 0
Mv 192,0,52
buffer_load_dwordx4 v12, s[8:11], s39 offen offset:0 lds
Mv 224,0,56
Rt 134,19,18432
Ma 192,0,60
Ma 196,4,60
Rt 136,20,16384
Mv 228,4,56
Mv 196,4,52
Rt 138,20,18432
Mv 164,4,48
Mv 132,4,44
Rt 140,21,16384
Mv 100,4,40
buffer_load_dwordx4 v13, s[8:11], s39 offen offset:1024 lds
Mv 68,4,36
Rt 142,21,18432
Mv 36,4,32
Mv 40,8,32
Rt 144,22,16384
Mv 72,8,36
Mv 104,8,40
Rt 146,22,18432
Mv 136,8,44
Mv 168,8,48
Rt 148,23,16384
Mv 200,8,52
buffer_load_dwordx4 v14, s[8:11], s39 offen offset:2048 lds
Mv 232,8,56
Rt 150,23,18432
Ma 200,8,60
Ma 204,12,60
Rt 152,24,16384
Mv 236,12,56
Mv 204,12,52
Rt 154,24,18432
Mv 172,12,48
Mv 140,12,44
Rt 156,25,16384
Mv 108,12,40
buffer_load_dwordx4 v15, s[8:11], s39 offen offset:3072 lds
Mv 76,12,36
Rt 158,25,18432
Mv 44,12,32
Mv 48,16,32
Rb 160,17,0
Mv 80,16,36
Mv 112,16,40
Rb 164,17,2048
Mv 144,16,44
Mv 176,16,48
Rb 168,17,8192
Mv 208,16,52
)" R"(
buffer_store_dwordx4 a[224:227], v1, s[20:23], s72 offen sc0 sc1
Mv 240,16,56
Rb 172,17,10240
s_add_u32 s72, s72, 1024
Ma 208,16,60
Ma 212,20,60
Rb 176,17,16384
Mv 244,20,56
Mv 212,20,52
Rb 180,17,18432
Mv 180,20,48
Mv 148,20,44
Rb 184,17,24576
Mv 116,20,40
Mv 84,20,36
Rb 188,17,26624
Mv 52,20,32
Mv 56,24,32
Mv 88,24,36
buffer_store_dwordx4 a[224:227], v1, s[20:23], s88 offen sc0 sc1
Mv 120,24,40
s_add_u32 s88, s88, 1024
Mv 152,24,44
Mv 184,24,48
Mv 216,24,52
Mv 248,24,56
Ma 216,24,60
Ma 220,28,60
Mv 252,28,56
Mv 220,28,52
Mv 188,28,48
Mv 156,28,44
Mv 124,28,40
Mv 92,28,36
Mv 60,28,32
s_waitcnt vmcnt(11) lgkmcnt(0)
s_barrier
Mv 32,128,160
Rt 0,18,32768
Mv 64,128,164
s_add_u32 s38, s38, 0xffff0080
Mv 96,128,168
Rt 2,18,34816
s_add_u32 m0, s36, 0
Mv 128,128,172
buffer_load_dwordx4 v8, s[4:7], s38 offen offset:0 lds
Mv 160,128,176
Rt 4,19,32768
Mv 192,128,180
Mv 224,128,184
Rt 6,19,34816
Ma 192,128,188
buffer_load_dwordx4 v9, s[4:7], s38 offen offset:1024 lds
Ma 196,132,188
Rt 8,20,32768
Mv 228,132,184
Mv 196,132,180
Rt 10,20,34816
Mv 164,132,176
buffer_load_dwordx4 v10, s[4:7], s38 offen offset:2048 lds
Mv 132,132,172
Rt 12,21,32768
Mv 100,132,168
Mv 68,132,164
Rt 14,21,34816
)" R"(
Mv 36,132,160
buffer_load_dwordx4 v11, s[4:7], s38 offen offset:3072 lds
Mv 40,136,160
Rt 16,22,32768
Mv 72,136,164
s_add_u32 s38, s38, 0x10000
Mv 104,136,168
Rt 18,22,34816
s_add_u32 m0, s36, 4096
Mv 136,136,172
buffer_load_dwordx4 v8, s[4:7], s38 offen offset:0 lds
Mv 168,136,176
Rt 20,23,32768
Mv 200,136,180
Mv 232,136,184
Rt 22,23,34816
Ma 200,136,188
buffer_load_dwordx4 v9, s[4:7], s38 offen offset:1024 lds
Ma 204,140,188
Rt 24,24,32768
Mv 236,140,184
Mv 204,140,180
Rt 26,24,34816
Mv 172,140,176
buffer_load_dwordx4 v10, s[4:7], s38 offen offset:2048 lds
Mv 140,140,172
Rt 28,25,32768
Mv 108,140,168
Mv 76,140,164
Rt 30,25,34816
Mv 44,140,160
buffer_load_dwordx4 v11, s[4:7], s38 offen offset:3072 lds
Mv 48,144,160
Rb 32,16,32768
Mv 80,144,164
Mv 112,144,168
Rb 36,16,34816
Mv 144,144,172
s_add_u32 s39, s39, 0x40000
Mv 176,144,176
Rb 40,16,40960
s_add_u32 m0, s37, 16384
Mv 208,144,180
buffer_load_dwordx4 v12, s[8:11], s39 offen offset:0 lds
Mv 240,144,184
Rb 44,16,43008
Ma 208,144,188
Ma 212,148,188
Rb 48,16,49152
Mv 244,148,184
Mv 212,148,180
Rb 52,16,51200
Mv 180,148,176
buffer_load_dwordx4 v13, s[8:11], s39 offen offset:1024 lds
Mv 148,148,172
Rb 56,16,57344
Mv 116,148,168
Mv 84,148,164
Rb 60,16,59392
Mv 52,148,160
Mv 56,152,160
Mv 88,152,164
buffer_load_dwordx4 v14, s[8:11], s39 offen offset:2048 lds
Mv 120,152,168
)" R"(
Mv 152,152,172
Mv 184,152,176
Mv 216,152,180
Mv 248,152,184
Ma 216,152,188
buffer_load_dwordx4 v15, s[8:11], s39 offen offset:3072 lds
Ma 220,156,188
Mv 252,156,184
s_add_u32 s43, s43, 0x60000
Mv 220,156,180
Mv 188,156,176
buffer_load_dword v27, v26, s[16:19], s43 offen
Mv 156,156,172
Mv 124,156,168
Mv 92,156,164
Mv 60,156,160
s_waitcnt vmcnt(19) lgkmcnt(0)
s_barrier
buffer_store_dword v6, v7, s[20:23], s52 offen offset:0 sc0 sc1
s_mov_b32 s52, 0x40000000
Mv 32,0,32
Rt 128,18,49152
Mv 64,0,36
Mv 96,0,40
Rt 130,18,51200
Mv 128,0,44
s_add_u32 s39, s39, 0x40000
Mv 160,0,48
Rt 132,19,49152
s_add_u32 m0, s37, 32768
Mv 192,0,52
buffer_load_dwordx4 v12, s[8:11], s39 offen offset:0 lds
Mv 224,0,56
Rt 134,19,51200
Ma 192,0,60
Ma 196,4,60
Rt 136,20,49152
Mv 228,4,56
Mv 196,4,52
Rt 138,20,51200
Mv 164,4,48
Mv 132,4,44
Rt 140,21,49152
Mv 100,4,40
buffer_load_dwordx4 v13, s[8:11], s39 offen offset:1024 lds
Mv 68,4,36
Rt 142,21,51200
Mv 36,4,32
Mv 40,8,32
Rt 144,22,49152
Mv 72,8,36
Mv 104,8,40
Rt 146,22,51200
Mv 136,8,44
Mv 168,8,48
Rt 148,23,49152
Mv 200,8,52
buffer_load_dwordx4 v14, s[8:11], s39 offen offset:2048 lds
Mv 232,8,56
Rt 150,23,51200
Ma 200,8,60
Ma 204,12,60
Rt 152,24,49152
Mv 236,12,56
Mv 204,12,52
)" R"(
Rt 154,24,51200
Mv 172,12,48
Mv 140,12,44
Rt 156,25,49152
Mv 108,12,40
buffer_load_dwordx4 v15, s[8:11], s39 offen offset:3072 lds
Mv 76,12,36
Rt 158,25,51200
Mv 44,12,32
Mv 48,16,32
Rb 160,17,32768
Mv 80,16,36
Mv 112,16,40
Rb 164,17,34816
Mv 144,16,44
Mv 176,16,48
Rb 168,17,40960
Mv 208,16,52
buffer_store_dwordx4 a[224:227], v1, s[20:23], s72 offen sc0 sc1
Mv 240,16,56
Rb 172,17,43008
s_add_u32 s72, s72, 1024
Ma 208,16,60
Ma 212,20,60
Rb 176,17,49152
Mv 244,20,56
Mv 212,20,52
Rb 180,17,51200
Mv 180,20,48
Mv 148,20,44
Rb 184,17,57344
Mv 116,20,40
Mv 84,20,36
Rb 188,17,59392
Mv 52,20,32
Mv 56,24,32
Mv 88,24,36
buffer_store_dwordx4 a[224:227], v1, s[20:23], s88 offen sc0 sc1
Mv 120,24,40
s_add_u32 s88, s88, 1024
Mv 152,24,44
Mv 184,24,48
Mv 216,24,52
Mv 248,24,56
Ma 216,24,60
Ma 220,28,60
Mv 252,28,56
Mv 220,28,52
Mv 188,28,48
Mv 156,28,44
Mv 124,28,40
Mv 92,28,36
s_cmp_eq_u32 s40, 3
s_cmov_b32 s72, 0x40000000
s_cmov_b32 s88, 0x40000000
Mv 60,28,32
s_waitcnt vmcnt(12) lgkmcnt(0)
s_barrier
Mv 32,128,160
Rt 0,18,0
Mv 64,128,164
s_add_u32 s38, s38, 0xffff0080
Mv 96,128,168
Rt 2,18,2048
s_add_u32 m0, s36, 32768
)" R"(
Mv 128,128,172
buffer_load_dwordx4 v8, s[4:7], s38 offen offset:0 lds
Mv 160,128,176
Rt 4,19,0
Mv 192,128,180
Mv 224,128,184
Rt 6,19,2048
Ma 192,128,188
buffer_load_dwordx4 v9, s[4:7], s38 offen offset:1024 lds
Ma 196,132,188
Rt 8,20,0
Mv 228,132,184
Mv 196,132,180
Rt 10,20,2048
Mv 164,132,176
buffer_load_dwordx4 v10, s[4:7], s38 offen offset:2048 lds
Mv 132,132,172
Rt 12,21,0
Mv 100,132,168
Mv 68,132,164
Rt 14,21,2048
Mv 36,132,160
buffer_load_dwordx4 v11, s[4:7], s38 offen offset:3072 lds
Mv 40,136,160
Rt 16,22,0
Mv 72,136,164
s_add_u32 s38, s38, 0x10000
Mv 104,136,168
Rt 18,22,2048
s_add_u32 m0, s36, 36864
Mv 136,136,172
buffer_load_dwordx4 v8, s[4:7], s38 offen offset:0 lds
Mv 168,136,176
Rt 20,23,0
Mv 200,136,180
Mv 232,136,184
Rt 22,23,2048
Ma 200,136,188
buffer_load_dwordx4 v9, s[4:7], s38 offen offset:1024 lds
Ma 204,140,188
Rt 24,24,0
Mv 236,140,184
Mv 204,140,180
Rt 26,24,2048
Mv 172,140,176
buffer_load_dwordx4 v10, s[4:7], s38 offen offset:2048 lds
Mv 140,140,172
Rt 28,25,0
Mv 108,140,168
Mv 76,140,164
Rt 30,25,2048
Mv 44,140,160
buffer_load_dwordx4 v11, s[4:7], s38 offen offset:3072 lds
Mv 48,144,160
Rb 32,16,0
Mv 80,144,164
Mv 112,144,168
Rb 36,16,2048
Mv 144,144,172
s_add_u32 s39, s39, 0x40000
Mv 176,144,176
Rb 40,16,8192
s_add_u32 m0, s37, 49152
Mv 208,144,180
)" R"(
buffer_load_dwordx4 v12, s[8:11], s39 offen offset:0 lds
Mv 240,144,184
Rb 44,16,10240
Ma 208,144,188
Ma 212,148,188
Rb 48,16,16384
Mv 244,148,184
Mv 212,148,180
Rb 52,16,18432
Mv 180,148,176
buffer_load_dwordx4 v13, s[8:11], s39 offen offset:1024 lds
Mv 148,148,172
Rb 56,16,24576
Mv 116,148,168
Mv 84,148,164
Rb 60,16,26624
Mv 52,148,160
Mv 56,152,160
Mv 88,152,164
buffer_load_dwordx4 v14, s[8:11], s39 offen offset:2048 lds
Mv 120,152,168
Mv 152,152,172
Mv 184,152,176
Mv 216,152,180
Mv 248,152,184
Ma 216,152,188
buffer_load_dwordx4 v15, s[8:11], s39 offen offset:3072 lds
Ma 220,156,188
Mv 252,156,184
s_add_u32 s43, s43, 0x60000
Mv 220,156,180
s_sub_u32 s40, s40, 1
Mv 188,156,176
buffer_load_dword v27, v26, s[16:19], s43 offen
Mv 156,156,172
Mv 124,156,168
Mv 92,156,164
Mv 60,156,160
s_cmp_lg_u32 s40, 0
s_waitcnt vmcnt(21) lgkmcnt(0)
s_barrier
s_cbranch_scc1 .LzPBK
.LzPBKX:
Mv 32,0,32
Rt 128,18,16384
Mv 64,0,36
buffer_load_dword v4, v5, s[20:23], s50 offen offset:0 sc0 sc1
Mv 96,0,40
Rt 130,18,18432
Mv 128,0,44
Mv 160,0,48
Rt 132,19,16384
Mv 192,0,52
Mv 224,0,56
Rt 134,19,18432
Ma 192,0,60
Ma 196,4,60
Rt 136,20,16384
Mv 228,4,56
Mv 196,4,52
Rt 138,20,18432
Mv 164,4,48
Mv 132,4,44
Rt 140,21,16384
Mv 100,4,40
)" R"(
Mv 68,4,36
Rt 142,21,18432
Mv 36,4,32
Mv 40,8,32
Rt 144,22,16384
Mv 72,8,36
Mv 104,8,40
Rt 146,22,18432
Mv 136,8,44
Mv 168,8,48
Rt 148,23,16384
Mv 200,8,52
Mv 232,8,56
Rt 150,23,18432
Ma 200,8,60
Ma 204,12,60
Rt 152,24,16384
Mv 236,12,56
Mv 204,12,52
Rt 154,24,18432
Mv 172,12,48
Mv 140,12,44
Rt 156,25,16384
Mv 108,12,40
Mv 76,12,36
Rt 158,25,18432
Mv 44,12,32
Mv 48,16,32
Rb 160,17,0
Mv 80,16,36
Mv 112,16,40
Rb 164,17,2048
Mv 144,16,44
Mv 176,16,48
Rb 168,17,8192
Mv 208,16,52
Mv 240,16,56
Rb 172,17,10240
Ma 208,16,60
s_waitcnt vmcnt(0)
v_cmp_eq_u32_e32 vcc, s3, v4
s_andn2_b64 s[28:29], -1, vcc
s_cbranch_scc1 .Lzsp14
.Lzok14:
Ma 212,20,60
Rb 176,17,16384
Mv 244,20,56
s_lshl3_add_u32 s29, s34, s35
s_lshl2_add_u32 s29, s29, s33
Mv 212,20,52
Rb 180,17,18432
s_add_u32 m0, s36, 131072
Mv 180,20,48
buffer_load_dwordx4 v2, s[20:23], s56 offen offset:0 sc1 lds
Mv 148,20,44
Rb 184,17,24576
s_mul_i32 s83, s29, 7168
Mv 116,20,40
s_lshl_b32 s82, s34, 21
s_add_u32 s82, s82, 0x6000000
Mv 84,20,36
Rb 188,17,26624
s_add_u32 s82, s82, s84
Mv 52,20,32
)" R"(
s_mov_b32 s24, s80
s_mov_b32 s25, s81
Mv 56,24,32
s_mov_b32 s26, 0x380000
s_mov_b32 s27, 0x20000
Mv 88,24,36
buffer_load_dwordx4 v2, s[20:23], s56 offen offset:1024 sc1 lds
Mv 120,24,40
s_add_u32 s28, s33, 0
s_lshl2_add_u32 s28, s35, s28
Mv 152,24,44
s_mul_i32 s29, s28, 293
s_lshr_b32 s29, s29, 11
Mv 184,24,48
s_mul_i32 s29, s29, 9
s_add_u32 s29, s29, s28
Mv 216,24,52
s_lshl_b32 s29, s29, 10
s_add_u32 s4, s29, s82
Mv 248,24,56
s_add_u32 s28, s33, 4
s_lshl3_add_u32 s28, s28, s35
Ma 216,24,60
buffer_load_dwordx4 v2, s[20:23], s56 offen offset:2048 sc1 lds
Ma 220,28,60
s_mul_i32 s29, s28, 293
s_lshr_b32 s29, s29, 11
Mv 252,28,56
s_mul_i32 s29, s29, 9
s_add_u32 s29, s29, s28
Mv 220,28,52
s_lshl_b32 s29, s29, 10
s_add_u32 s5, s29, s82
Mv 188,28,48
s_add_u32 s28, s33, 8
s_lshl3_add_u32 s28, s28, s35
Mv 156,28,44
s_mul_i32 s29, s28, 293
s_lshr_b32 s29, s29, 11
Mv 124,28,40
buffer_load_dwordx4 v2, s[20:23], s56 offen offset:3072 sc1 lds
Mv 92,28,36
s_mul_i32 s29, s29, 9
s_add_u32 s29, s29, s28
Mv 60,28,32
s_waitcnt lgkmcnt(0)
s_barrier
Mv 32,128,160
Rt 0,18,32768
s_lshl_b32 s29, s29, 10
Mv 64,128,164
s_add_u32 s6, s29, s82
s_add_u32 s28, s33, 12
Mv 96,128,168
Rt 2,18,34816
s_add_u32 m0, s36, 0
Mv 128,128,172
buffer_load_dwordx4 v2, s[20:23], s55 offen offset:0 sc1 lds
Mv 160,128,176
Rt 4,19,32768
s_lshl3_add_u32 s28, s28, s35
Mv 192,128,180
s_mul_i32 s29, s28, 293
s_lshr_b32 s29, s29, 11
Mv 224,128,184
)" R"(
Rt 6,19,34816
s_mul_i32 s29, s29, 9
Ma 192,128,188
s_add_u32 s29, s29, s28
s_lshl_b32 s29, s29, 10
Ma 196,132,188
Rt 8,20,32768
buffer_load_dwordx4 v2, s[20:23], s55 offen offset:1024 sc1 lds
Mv 228,132,184
s_add_u32 s7, s29, s82
Mv 196,132,180
Rt 10,20,34816
Mv 164,132,176
s_cmp_gt_u32 s33, 1
s_cmov_b32 s7, 0x40000000
Mv 132,132,172
Rt 12,21,32768
s_add_u32 s28, s33, 14
Mv 100,132,168
buffer_load_dwordx4 v2, s[20:23], s55 offen offset:2048 sc1 lds
Mv 68,132,164
Rt 14,21,34816
s_lshl3_add_u32 s28, s28, s35
Mv 36,132,160
s_mul_i32 s29, s28, 293
s_lshr_b32 s29, s29, 11
Mv 40,136,160
Rt 16,22,32768
s_mul_i32 s29, s29, 9
Mv 72,136,164
s_add_u32 s29, s29, s28
s_lshl_b32 s29, s29, 10
Mv 104,136,168
Rt 18,22,34816
buffer_load_dwordx4 v2, s[20:23], s55 offen offset:3072 sc1 lds
Mv 136,136,172
s_add_u32 s8, s29, s82
s_add_u32 s28, s33, 18
Mv 168,136,176
Rt 20,23,32768
s_lshl3_add_u32 s28, s28, s35
Mv 200,136,180
s_mul_i32 s29, s28, 293
s_lshr_b32 s29, s29, 11
Mv 232,136,184
Rt 22,23,34816
s_add_u32 m0, s36, 4096
Ma 200,136,188
buffer_load_dwordx4 v3, s[20:23], s55 offen offset:0 sc1 lds
Ma 204,140,188
Rt 24,24,32768
s_mul_i32 s29, s29, 9
Mv 236,140,184
s_add_u32 s29, s29, s28
s_lshl_b32 s29, s29, 10
Mv 204,140,180
Rt 26,24,34816
s_add_u32 s9, s29, s82
Mv 172,140,176
s_add_u32 s28, s33, 22
s_lshl3_add_u32 s28, s28, s35
Mv 140,140,172
Rt 28,25,32768
buffer_load_dwordx4 v3, s[20:23], s55 offen offset:1024 sc1 lds
)" R"(
Mv 108,140,168
s_mul_i32 s29, s28, 293
s_lshr_b32 s29, s29, 11
Mv 76,140,164
Rt 30,25,34816
s_mul_i32 s29, s29, 9
Mv 44,140,160
s_add_u32 s29, s29, s28
s_lshl_b32 s29, s29, 10
Mv 48,144,160
Rb 32,16,32768
s_add_u32 s10, s29, s82
Mv 80,144,164
buffer_load_dwordx4 v3, s[20:23], s55 offen offset:2048 sc1 lds
Mv 112,144,168
Rb 36,16,34816
s_add_u32 s28, s33, 26
Mv 144,144,172
s_lshl3_add_u32 s28, s28, s35
s_mul_i32 s29, s28, 293
Mv 176,144,176
Rb 40,16,40960
s_lshr_b32 s29, s29, 11
Mv 208,144,180
s_mul_i32 s29, s29, 9
s_add_u32 s29, s29, s28
Mv 240,144,184
Rb 44,16,43008
buffer_load_dwordx4 v3, s[20:23], s55 offen offset:3072 sc1 lds
Ma 208,144,188
s_lshl_b32 s29, s29, 10
s_add_u32 s11, s29, s82
Ma 212,148,188
Rb 48,16,49152
Mv 244,148,184
s_cmp_gt_u32 s33, 1
s_cmov_b32 s11, 0x40000000
Mv 212,148,180
Rb 52,16,51200
s_add_u32 m0, s36, 135168
Mv 180,148,176
buffer_load_dwordx4 v3, s[20:23], s56 offen offset:0 sc1 lds
Mv 148,148,172
Rb 56,16,57344
s_and_b32 s28, s34, 1
Mv 116,148,168
s_lshl_b32 s28, s28, 14
s_add_u32 s82, s37, s28
Mv 84,148,164
Rb 60,16,59392
s_add_u32 s82, s82, 0x10000
Mv 52,148,160
Mv 56,152,160
buffer_load_dwordx4 v3, s[20:23], s56 offen offset:1024 sc1 lds
Mv 88,152,164
Mv 120,152,168
Mv 152,152,172
Mv 184,152,176
Mv 216,152,180
buffer_load_dwordx4 v3, s[20:23], s56 offen offset:2048 sc1 lds
Mv 248,152,184
Ma 216,152,188
Ma 220,156,188
Mv 252,156,184
)" R"(
Mv 220,156,180
buffer_load_dwordx4 v3, s[20:23], s56 offen offset:3072 sc1 lds
Mv 188,156,176
Mv 156,156,172
Mv 124,156,168
s_add_u32 s55, s55, 0x10000
s_add_u32 s56, s56, 0x10000
Mv 92,156,164
Mv 60,156,160
s_waitcnt lgkmcnt(4)
s_barrier
Mv 32,0,32
Rt 128,18,49152
Mv 64,0,36
Rt 130,18,51200
Mv 96,0,40
Rt 132,19,49152
s_add_u32 m0, s36, 65536
Mv 128,0,44
Rt 134,19,51200
buffer_load_dwordx4 v2, s[20:23], s56 offen offset:0 sc1 lds
Mv 132,4,44
Rt 136,20,49152
Mv 100,4,40
Rt 138,20,51200
s_add_u32 s50, s50, 1024
Mv 68,4,36
Rt 140,21,49152
s_min_u32 s50, s50, s60
Mv 36,4,32
Rt 142,21,51200
Mv 40,8,32
Rt 144,22,49152
Mv 72,8,36
Rt 146,22,51200
Mv 104,8,40
Rt 148,23,49152
Mv 136,8,44
Rt 150,23,51200
buffer_load_dwordx4 v2, s[20:23], s56 offen offset:1024 sc1 lds
Mv 140,12,44
Rt 152,24,49152
Mv 108,12,40
Rt 154,24,51200
Mv 76,12,36
Rt 156,25,49152
Mv 44,12,32
Rt 158,25,51200
Mv 48,16,32
Rb 160,17,32768
Mv 80,16,36
Rb 164,17,34816
Mv 112,16,40
Rb 168,17,40960
Mv 144,16,44
Rb 172,17,43008
buffer_load_dwordx4 v2, s[20:23], s56 offen offset:2048 sc1 lds
Mv 148,20,44
Rb 176,17,49152
Mv 116,20,40
Rb 180,17,51200
Mv 84,20,36
Rb 184,17,57344
Mv 52,20,32
Rb 188,17,59392
)" R"(
Mv 56,24,32
v_add_u32 v8, 0x10000, v1
v_add_u32 v10, 0x20000, v1
Mv 88,24,36
buffer_load_dword v4, v5, s[20:23], s50 offen offset:0 sc0 sc1
Mv 120,24,40
s_mov_b32 s93, 2
s_mov_b32 s94, 0
Mv 152,24,44
buffer_load_dwordx4 v2, s[20:23], s56 offen offset:3072 sc1 lds
Mv 156,28,44
Mv 124,28,40
Mv 92,28,36
Mv 60,28,32
s_waitcnt lgkmcnt(0)
s_barrier
Mv 32,128,160
Mv 36,132,160
Mv 40,136,160
s_add_u32 m0, s36, 32768
Mv 44,140,160
buffer_load_dwordx4 v2, s[20:23], s55 offen offset:0 sc1 lds
Mv 48,144,160
Mv 52,148,160
s_nop 0
Cv 32,32,33
Cv 33,34,35
Mv 56,152,160
Cv 34,36,37
Cv 35,38,39
Mv 60,156,160
Cv 40,40,41
Cv 41,42,43
Mv 64,128,164
Cv 42,44,45
Cv 43,46,47
Mv 68,132,164
Cv 48,48,49
Cv 49,50,51
Mv 72,136,164
Cv 50,52,53
Cv 51,54,55
Mv 76,140,164
buffer_load_dwordx4 v2, s[20:23], s55 offen offset:1024 sc1 lds
Mv 80,144,164
Cv 56,56,57
Cv 57,58,59
Mv 84,148,164
Cv 58,60,61
Cv 59,62,63
Mv 88,152,164
Cv 64,64,65
Cv 65,66,67
Mv 92,156,164
Cv 66,68,69
Cv 67,70,71
Mv 96,128,168
Cv 72,72,73
Cv 73,74,75
Mv 100,132,168
Cv 74,76,77
Cv 75,78,79
Mv 104,136,168
Cv 80,80,81
Cv 81,82,83
)" R"(
Mv 108,140,168
buffer_load_dwordx4 v2, s[20:23], s55 offen offset:2048 sc1 lds
Mv 112,144,168
Cv 82,84,85
Cv 83,86,87
Mv 116,148,168
Cv 88,88,89
Cv 89,90,91
Mv 120,152,168
Cv 90,92,93
Cv 91,94,95
Mv 124,156,168
buffer_store_dwordx4 v[32:35], v2, s[20:23], s45 offen offset:0 sc0 sc1
Mv 128,128,172
Mv 132,132,172
Mv 136,136,172
Mv 140,140,172
buffer_load_dwordx4 v2, s[20:23], s55 offen offset:3072 sc1 lds
Mv 144,144,172
Mv 148,148,172
s_nop 1
Cv 128,128,129
Cv 129,130,131
Mv 152,152,172
Cv 130,132,133
Cv 131,134,135
Mv 156,156,172
buffer_store_dwordx4 v[40:43], v2, s[20:23], s45 offen offset:1024 sc0 sc1
Mv 160,0,48
Cv 136,136,137
Cv 137,138,139
Mv 160,128,176
Cv 138,140,141
Cv 139,142,143
Mv 164,4,48
s_add_u32 m0, s36, 36864
Cv 144,144,145
Mv 164,132,176
buffer_load_dwordx4 v3, s[20:23], s55 offen offset:0 sc1 lds
Mv 168,8,48
Cv 145,146,147
Cv 146,148,149
Mv 168,136,176
Cv 147,150,151
Cv 152,152,153
Mv 172,12,48
Cv 153,154,155
Cv 154,156,157
Mv 172,140,176
buffer_store_dwordx4 v[48:51], v2, s[20:23], s45 offen offset:2048 sc0 sc1
Mv 176,16,48
Cv 155,158,159
Cv 132,96,97
Mv 176,144,176
Cv 133,98,99
Cv 134,100,101
Mv 180,20,48
Cv 135,102,103
Cv 140,104,105
Mv 180,148,176
buffer_load_dwordx4 v3, s[20:23], s55 offen offset:1024 sc1 lds
Mv 184,24,48
Cv 141,106,107
Cv 142,108,109
)" R"(
Mv 184,152,176
Cv 143,110,111
Cv 148,112,113
Mv 188,28,48
Cv 149,114,115
Cv 150,116,117
Mv 188,156,176
buffer_store_dwordx4 v[56:59], v2, s[20:23], s45 offen offset:3072 sc0 sc1
Mv 192,0,52
Cv 151,118,119
Cv 156,120,121
Mv 192,128,180
Cv 157,122,123
Cv 158,124,125
Mv 196,4,52
Cv 159,126,127
Cv 160,160,161
Mv 196,132,180
buffer_load_dwordx4 v3, s[20:23], s55 offen offset:2048 sc1 lds
Mv 200,8,52
Cv 161,162,163
Cv 162,164,165
Mv 200,136,180
Cv 163,166,167
v_accvgpr_write_b32 a224, v160
Mv 204,12,52
v_accvgpr_write_b32 a225, v161
v_accvgpr_write_b32 a226, v162
Mv 204,140,180
buffer_store_dwordx4 v[64:67], v3, s[20:23], s45 offen offset:0 sc0 sc1
Mv 208,16,52
v_accvgpr_write_b32 a227, v163
Cv 168,168,169
Mv 208,144,180
Cv 169,170,171
Cv 170,172,173
Mv 212,20,52
Cv 171,174,175
v_accvgpr_write_b32 a228, v168
Mv 212,148,180
buffer_load_dwordx4 v3, s[20:23], s55 offen offset:3072 sc1 lds
Mv 216,24,52
v_accvgpr_write_b32 a229, v169
v_accvgpr_write_b32 a230, v170
Mv 216,152,180
v_accvgpr_write_b32 a231, v171
Cv 176,176,177
Mv 220,28,52
Cv 177,178,179
Cv 178,180,181
Mv 220,156,180
buffer_store_dwordx4 v[72:75], v3, s[20:23], s45 offen offset:1024 sc0 sc1
Mv 224,0,56
Cv 179,182,183
v_accvgpr_write_b32 a232, v176
Ma 192,0,60
v_accvgpr_write_b32 a233, v177
v_accvgpr_write_b32 a234, v178
Ma 196,4,60
s_add_u32 m0, s36, 69632
v_accvgpr_write_b32 a235, v179
Mv 228,4,56
buffer_load_dwordx4 v3, s[20:23], s56 offen offset:0 sc1 lds
Mv 232,8,56
)" R"(
Cv 184,184,185
Cv 185,186,187
Ma 200,8,60
Cv 186,188,189
Cv 187,190,191
Ma 204,12,60
v_accvgpr_write_b32 a236, v184
v_accvgpr_write_b32 a237, v185
Mv 236,12,56
s_waitcnt vmcnt(16)
s_barrier
Mv 240,16,56
v_cmp_eq_u32_e32 vcc, s3, v4
s_andn2_b64 s[28:29], -1, vcc
s_cbranch_scc1 .Lzsp15
.Lzok15:
Ma 208,16,60
buffer_store_dwordx4 v[80:83], v3, s[20:23], s45 offen offset:2048 sc0 sc1
Ma 212,20,60
v_accvgpr_write_b32 a238, v186
v_accvgpr_write_b32 a239, v187
Mv 244,20,56
buffer_load_dwordx4 v3, s[20:23], s56 offen offset:1024 sc1 lds
Mv 248,24,56
Cv 192,192,193
Cv 193,194,195
Ma 216,24,60
Cv 194,196,197
Cv 195,198,199
Ma 220,28,60
v_accvgpr_write_b32 a240, v192
v_accvgpr_write_b32 a241, v193
Mv 252,28,56
buffer_store_dwordx4 v[88:91], v3, s[20:23], s45 offen offset:3072 sc0 sc1
Mv 224,128,184
Rb 0,1,0
v_accvgpr_write_b32 a242, v194
Mv 228,132,184
Rb 4,1,1024
v_accvgpr_write_b32 a243, v195
Mv 232,136,184
Rb 8,1,2048
Cv 200,200,201
Mv 236,140,184
buffer_load_dwordx4 v3, s[20:23], s56 offen offset:2048 sc1 lds
Rb 12,1,3072
Mv 240,144,184
Rb 16,1,8192
Cv 201,202,203
Mv 244,148,184
Rb 20,1,9216
Cv 202,204,205
Mv 248,152,184
Rb 24,1,10240
Cv 203,206,207
Mv 252,156,184
Rb 28,1,11264
v_accvgpr_write_b32 a244, v200
Ma 192,128,188
Rb 32,1,4096
v_accvgpr_write_b32 a245, v201
Ma 196,132,188
Rb 36,1,5120
v_accvgpr_write_b32 a246, v202
Ma 200,136,188
)" R"(
Rb 40,1,6144
v_accvgpr_write_b32 a247, v203
Ma 204,140,188
buffer_load_dwordx4 v3, s[20:23], s56 offen offset:3072 sc1 lds
Rb 44,1,7168
Ma 208,144,188
Ma 212,148,188
Ma 216,152,188
Ma 220,156,188
s_add_u32 s56, s56, 0x10000
s_branch .LzFB
.LzBV:
Mz 32,32,0
Rt 128,18,16384
v_mul_f32_e32 v64, s54, v64
Mz 36,36,0
v_mul_f32_e32 v65, s54, v65
Cv 64,64,65
Mz 96,40,0
Rt 130,18,18432
v_accvgpr_write_b32 a112, v64
Mz 100,44,0
s_add_u32 s39, s39, 0x40000
v_mul_f32_e32 v66, s54, v66
Mz 160,48,0
Rt 132,19,16384
s_add_u32 m0, s37, 0
Mz 164,52,0
buffer_load_dwordx4 v12, s[8:11], s39 offen offset:0 lds
Mz 224,56,0
Rt 134,19,18432
v_mul_f32_e32 v67, s54, v67
Mz 228,60,0
Cv 66,66,67
v_accvgpr_write_b32 a113, v66
Mz 236,60,4
Rt 136,20,16384
v_mul_f32_e32 v68, s54, v68
Mz 232,56,4
v_mul_f32_e32 v69, s54, v69
Cv 68,68,69
buffer_store_dwordx4 a[64:67], v2, s[20:23], s51 offen offset:0 sc0 sc1
Mz 172,52,4
Rt 138,20,18432
v_accvgpr_write_b32 a114, v68
Mz 168,48,4
v_mul_f32_e32 v70, s54, v70
v_mul_f32_e32 v71, s54, v71
Mz 108,44,4
Rt 140,21,16384
Cv 70,70,71
Mz 104,40,4
buffer_load_dwordx4 v13, s[8:11], s39 offen offset:1024 lds
Mz 44,36,4
Rt 142,21,18432
v_accvgpr_write_b32 a115, v70
Mz 40,32,4
v_mul_f32_e32 v72, s54, v72
v_mul_f32_e32 v73, s54, v73
Mz 48,32,8
Rt 144,22,16384
Cv 72,72,73
Mz 52,36,8
v_accvgpr_write_b32 a116, v72
)" R"(
v_mul_f32_e32 v74, s54, v74
buffer_store_dwordx4 a[68:71], v2, s[20:23], s51 offen offset:1024 sc0 sc1
Mz 112,40,8
Rt 146,22,18432
v_mul_f32_e32 v75, s54, v75
Mz 116,44,8
Cv 74,74,75
v_accvgpr_write_b32 a117, v74
Mz 176,48,8
Rt 148,23,16384
v_mul_f32_e32 v76, s54, v76
Mz 180,52,8
buffer_load_dwordx4 v14, s[8:11], s39 offen offset:2048 lds
Mz 240,56,8
Rt 150,23,18432
v_mul_f32_e32 v77, s54, v77
Mz 244,60,8
Cv 76,76,77
v_accvgpr_write_b32 a118, v76
Mz 252,60,12
Rt 152,24,16384
v_mul_f32_e32 v78, s54, v78
Mz 248,56,12
v_mul_f32_e32 v79, s54, v79
Cv 78,78,79
buffer_store_dwordx4 a[72:75], v2, s[20:23], s51 offen offset:2048 sc0 sc1
Mz 188,52,12
Rt 154,24,18432
v_accvgpr_write_b32 a119, v78
Mz 184,48,12
v_mul_f32_e32 v80, s54, v80
v_mul_f32_e32 v81, s54, v81
Mz 124,44,12
Rt 156,25,16384
Cv 80,80,81
Mz 120,40,12
buffer_load_dwordx4 v15, s[8:11], s39 offen offset:3072 lds
Mz 60,36,12
Rt 158,25,18432
v_accvgpr_write_b32 a120, v80
Mz 56,32,12
v_mul_f32_e32 v82, s54, v82
v_mul_f32_e32 v83, s54, v83
Mz 128,40,16
Rb 160,17,0
Cv 82,82,83
Mz 132,44,16
v_accvgpr_write_b32 a121, v82
v_mul_f32_e32 v84, s54, v84
buffer_store_dwordx4 a[76:79], v2, s[20:23], s51 offen offset:3072 sc0 sc1
Mz 192,48,16
Rb 164,17,2048
v_mul_f32_e32 v85, s54, v85
Mz 196,52,16
Cv 84,84,85
v_accvgpr_write_b32 a122, v84
v_mfma_f32_16x16x32_bf16 a[192:195], a[56:59], a[16:19], 0
Rb 168,17,8192
v_mul_f32_e32 v86, s54, v86
v_mfma_f32_16x16x32_bf16 a[196:199], a[60:63], a[16:19], 0
v_mul_f32_e32 v87, s54, v87
Cv 86,86,87
v_mfma_f32_16x16x32_bf16 a[204:207], a[60:63], a[20:23], 0
Rb 172,17,10240
)" R"(
v_accvgpr_write_b32 a123, v86
v_mfma_f32_16x16x32_bf16 a[200:203], a[56:59], a[20:23], 0
v_mul_f32_e32 v88, s54, v88
v_mul_f32_e32 v89, s54, v89
Mz 204,52,20
Rb 176,17,16384
Cv 88,88,89
Mz 200,48,20
v_accvgpr_write_b32 a124, v88
v_mul_f32_e32 v90, s54, v90
buffer_store_dwordx4 a[80:83], v3, s[20:23], s51 offen offset:0 sc0 sc1
Mz 140,44,20
Rb 180,17,18432
v_mul_f32_e32 v91, s54, v91
Mz 136,40,20
Cv 90,90,91
v_accvgpr_write_b32 a125, v90
Mz 144,40,24
Rb 184,17,24576
v_mul_f32_e32 v92, s54, v92
Mz 148,44,24
v_mul_f32_e32 v93, s54, v93
Cv 92,92,93
Mz 208,48,24
Rb 188,17,26624
v_accvgpr_write_b32 a126, v92
Mz 212,52,24
v_mul_f32_e32 v94, s54, v94
v_mul_f32_e32 v95, s54, v95
v_mfma_f32_16x16x32_bf16 a[208:211], a[56:59], a[24:27], 0
Cv 94,94,95
v_accvgpr_write_b32 a127, v94
v_mfma_f32_16x16x32_bf16 a[212:215], a[60:63], a[24:27], 0
buffer_store_dwordx4 a[84:87], v3, s[20:23], s51 offen offset:1024 sc0 sc1
v_mfma_f32_16x16x32_bf16 a[220:223], a[60:63], a[28:31], 0
v_mfma_f32_16x16x32_bf16 a[216:219], a[56:59], a[28:31], 0
Mz 220,52,28
Mz 216,48,28
Mz 156,44,28
Mz 152,40,28
Mz 64,32,16
Mz 68,36,16
buffer_store_dwordx4 a[88:91], v3, s[20:23], s51 offen offset:2048 sc0 sc1
Mz 72,32,20
Mz 76,36,20
Mz 80,32,24
Mz 84,36,24
Mz 88,32,28
Mz 92,36,28
s_waitcnt vmcnt(16) lgkmcnt(0)
s_barrier
Mv 32,160,128
Rt 0,18,32768
Mv 36,164,128
s_add_u32 s38, s38, 0xffff0080
Mv 96,168,128
Rt 2,18,34816
s_add_u32 m0, s36, 0
Mv 100,172,128
buffer_load_dwordx4 v8, s[4:7], s38 offen offset:0 lds
Mv 160,176,128
Rt 4,19,32768
Mv 164,180,128
Mv 224,184,128
Rt 6,19,34816
)" R"(
Mv 228,188,128
buffer_load_dwordx4 v9, s[4:7], s38 offen offset:1024 lds
Mv 236,188,132
Rt 8,20,32768
Mv 232,184,132
Mv 172,180,132
Rt 10,20,34816
Mv 168,176,132
buffer_load_dwordx4 v10, s[4:7], s38 offen offset:2048 lds
Mv 108,172,132
Rt 12,21,32768
Mv 104,168,132
Mv 44,164,132
Rt 14,21,34816
Mv 40,160,132
buffer_load_dwordx4 v11, s[4:7], s38 offen offset:3072 lds
Mv 48,160,136
Rt 16,22,32768
Mv 52,164,136
s_add_u32 s38, s38, 0x10000
Mv 112,168,136
Rt 18,22,34816
s_add_u32 m0, s36, 4096
Mv 116,172,136
buffer_load_dwordx4 v8, s[4:7], s38 offen offset:0 lds
Mv 176,176,136
Rt 20,23,32768
Mv 180,180,136
Mv 240,184,136
Rt 22,23,34816
Mv 244,188,136
buffer_load_dwordx4 v9, s[4:7], s38 offen offset:1024 lds
Mv 252,188,140
Rt 24,24,32768
Mv 248,184,140
Mv 188,180,140
Rt 26,24,34816
Mv 184,176,140
buffer_load_dwordx4 v10, s[4:7], s38 offen offset:2048 lds
Mv 124,172,140
Rt 28,25,32768
Mv 120,168,140
Mv 60,164,140
Rt 30,25,34816
Mv 56,160,140
buffer_load_dwordx4 v11, s[4:7], s38 offen offset:3072 lds
Mv 64,160,144
Rb 32,16,32768
Mv 68,164,144
Mv 128,168,144
Rb 36,16,34816
Mv 132,172,144
s_add_u32 s39, s39, 0x40000
Mv 192,176,144
Rb 40,16,40960
s_add_u32 m0, s37, 16384
Mv 196,180,144
buffer_load_dwordx4 v12, s[8:11], s39 offen offset:0 lds
Ma 192,184,144
Rb 44,16,43008
Ma 196,188,144
Ma 204,188,148
Rb 48,16,49152
Ma 200,184,148
)" R"(
Mv 204,180,148
Rb 52,16,51200
Mv 200,176,148
buffer_load_dwordx4 v13, s[8:11], s39 offen offset:1024 lds
Mv 140,172,148
Rb 56,16,57344
Mv 136,168,148
Mv 76,164,148
Rb 60,16,59392
Mv 72,160,148
Mv 80,160,152
Mv 84,164,152
buffer_load_dwordx4 v14, s[8:11], s39 offen offset:2048 lds
Mv 144,168,152
Mv 148,172,152
Mv 208,176,152
Mv 212,180,152
Ma 208,184,152
Ma 212,188,152
buffer_load_dwordx4 v15, s[8:11], s39 offen offset:3072 lds
Ma 220,188,156
Ma 216,184,156
Mv 220,180,156
Mv 216,176,156
buffer_store_dwordx4 a[92:95], v3, s[20:23], s51 offen offset:3072 sc0 sc1
Mv 156,172,156
Mv 152,168,156
Mv 92,164,156
Mv 88,160,156
s_waitcnt vmcnt(25) lgkmcnt(0)
s_barrier
Mv 32,32,0
Rt 128,18,49152
s_mov_b64 s[16:17], s[76:77]
Mv 36,36,0
s_mov_b32 s18, 0x188000
Mv 96,40,0
Rt 130,18,51200
Mv 100,44,0
s_add_u32 s39, s39, 0x40000
Mv 160,48,0
Rt 132,19,49152
s_add_u32 m0, s37, 32768
Mv 164,52,0
buffer_load_dwordx4 v12, s[8:11], s39 offen offset:0 lds
Mv 224,56,0
Rt 134,19,51200
Mv 228,60,0
s_cmp_eq_u32 s33, 0
s_cmov_b32 s18, 0x380000
Mv 236,60,4
Rt 136,20,49152
s_mul_i32 s43, s2, 3072
Mv 232,56,4
s_add_u32 s51, s51, 0x10000
buffer_store_dwordx4 a[96:99], v2, s[20:23], s51 offen offset:0 sc0 sc1
Mv 172,52,4
Rt 138,20,51200
Mv 168,48,4
s_cmp_eq_u32 s33, 0
s_cselect_b32 s44, 3, 6
Mv 108,44,4
Rt 140,21,49152
s_mul_i32 s44, s44, 0x60000
Mv 104,40,4
)" R"(
buffer_load_dwordx4 v13, s[8:11], s39 offen offset:1024 lds
Mv 44,36,4
Rt 142,21,51200
s_sub_u32 s43, s43, s44
Mv 40,32,4
v_min_u32 v26, 23, v0
v_lshlrev_b32 v26, 7, v26
Mv 48,32,8
Rt 144,22,49152
v_and_b32 v5, 7, v0
Mv 52,36,8
buffer_store_dwordx4 a[100:103], v2, s[20:23], s51 offen offset:1024 sc0 sc1
Mv 112,40,8
Rt 146,22,51200
v_lshlrev_b32 v5, 7, v5
Mv 116,44,8
Mv 176,48,8
Rt 148,23,49152
Mv 180,52,8
buffer_load_dwordx4 v14, s[8:11], s39 offen offset:2048 lds
Mv 240,56,8
Rt 150,23,51200
Mv 244,60,8
Mv 252,60,12
Rt 152,24,49152
Mv 248,56,12
buffer_store_dwordx4 a[104:107], v2, s[20:23], s51 offen offset:2048 sc0 sc1
Mv 188,52,12
Rt 154,24,51200
Mv 184,48,12
Mv 124,44,12
Rt 156,25,49152
Mv 120,40,12
buffer_load_dwordx4 v15, s[8:11], s39 offen offset:3072 lds
Mv 60,36,12
Rt 158,25,51200
Mv 56,32,12
Mv 64,32,16
Rb 160,17,32768
Mv 68,36,16
buffer_store_dwordx4 a[108:111], v2, s[20:23], s51 offen offset:3072 sc0 sc1
Mv 128,40,16
Rb 164,17,34816
Mv 132,44,16
Mv 192,48,16
Rb 168,17,40960
Mv 196,52,16
Ma 192,56,16
Rb 172,17,43008
Ma 196,60,16
Ma 204,60,20
Rb 176,17,49152
Ma 200,56,20
buffer_store_dwordx4 a[112:115], v3, s[20:23], s51 offen offset:0 sc0 sc1
Mv 204,52,20
Rb 180,17,51200
Mv 200,48,20
Mv 140,44,20
Rb 184,17,57344
Mv 136,40,20
Mv 76,36,20
Rb 188,17,59392
Mv 72,32,20
Mv 80,32,24
)" R"(
Mv 84,36,24
buffer_store_dwordx4 a[116:119], v3, s[20:23], s51 offen offset:1024 sc0 sc1
Mv 144,40,24
Mv 148,44,24
Mv 208,48,24
Mv 212,52,24
Ma 208,56,24
Ma 212,60,24
Ma 220,60,28
Ma 216,56,28
buffer_store_dwordx4 a[120:123], v3, s[20:23], s51 offen offset:2048 sc0 sc1
Mv 220,52,28
Mv 216,48,28
Mv 156,44,28
Mv 152,40,28
Mv 92,36,28
Mv 88,32,28
s_waitcnt vmcnt(16) lgkmcnt(0)
s_barrier
Mv 32,160,128
Rt 0,18,0
v_accvgpr_write_b32 a224, -1
Mv 36,164,128
s_add_u32 s38, s38, 0xffff0080
v_accvgpr_write_b32 a225, -1
Mv 96,168,128
Rt 2,18,2048
s_add_u32 m0, s36, 32768
Mv 100,172,128
buffer_load_dwordx4 v8, s[4:7], s38 offen offset:0 lds
Mv 160,176,128
Rt 4,19,0
v_accvgpr_write_b32 a226, -1
Mv 164,180,128
v_accvgpr_write_b32 a227, -1
s_add_u32 s88, s73, 0x4000
Mv 224,184,128
Rt 6,19,2048
s_mov_b32 s40, 6
Mv 228,188,128
buffer_load_dwordx4 v9, s[4:7], s38 offen offset:1024 lds
Mv 236,188,132
Rt 8,20,0
s_mov_b32 s94, 0
Mv 232,184,132
Mv 172,180,132
Rt 10,20,2048
Mv 168,176,132
buffer_load_dwordx4 v10, s[4:7], s38 offen offset:2048 lds
Mv 108,172,132
Rt 12,21,0
Mv 104,168,132
Mv 44,164,132
Rt 14,21,2048
Mv 40,160,132
buffer_load_dwordx4 v11, s[4:7], s38 offen offset:3072 lds
Mv 48,160,136
Rt 16,22,0
Mv 52,164,136
s_add_u32 s38, s38, 0x10000
Mv 112,168,136
Rt 18,22,2048
s_add_u32 m0, s36, 36864
Mv 116,172,136
buffer_load_dwordx4 v8, s[4:7], s38 offen offset:0 lds
)" R"(
Mv 176,176,136
Rt 20,23,0
Mv 180,180,136
Mv 240,184,136
Rt 22,23,2048
Mv 244,188,136
buffer_load_dwordx4 v9, s[4:7], s38 offen offset:1024 lds
Mv 252,188,140
Rt 24,24,0
Mv 248,184,140
Mv 188,180,140
Rt 26,24,2048
Mv 184,176,140
buffer_load_dwordx4 v10, s[4:7], s38 offen offset:2048 lds
Mv 124,172,140
Rt 28,25,0
Mv 120,168,140
Mv 60,164,140
Rt 30,25,2048
Mv 56,160,140
buffer_load_dwordx4 v11, s[4:7], s38 offen offset:3072 lds
Mv 64,160,144
Rb 32,16,0
Mv 68,164,144
Mv 128,168,144
Rb 36,16,2048
Mv 132,172,144
s_add_u32 s39, s39, 0x40000
Mv 192,176,144
Rb 40,16,8192
s_add_u32 m0, s37, 49152
Mv 196,180,144
buffer_load_dwordx4 v12, s[8:11], s39 offen offset:0 lds
Ma 192,184,144
Rb 44,16,10240
Ma 196,188,144
Ma 204,188,148
Rb 48,16,16384
Ma 200,184,148
Mv 204,180,148
Rb 52,16,18432
Mv 200,176,148
buffer_load_dwordx4 v13, s[8:11], s39 offen offset:1024 lds
Mv 140,172,148
Rb 56,16,24576
Mv 136,168,148
Mv 76,164,148
Rb 60,16,26624
Mv 72,160,148
Mv 80,160,152
Mv 84,164,152
buffer_load_dwordx4 v14, s[8:11], s39 offen offset:2048 lds
Mv 144,168,152
Mv 148,172,152
Mv 208,176,152
Mv 212,180,152
Ma 208,184,152
Ma 212,188,152
buffer_load_dwordx4 v15, s[8:11], s39 offen offset:3072 lds
Ma 220,188,156
Ma 216,184,156
Mv 220,180,156
Mv 216,176,156
buffer_store_dwordx4 a[124:127], v3, s[20:23], s51 offen offset:3072 sc0 sc1
)" R"(
Mv 156,172,156
Mv 152,168,156
Mv 92,164,156
Mv 88,160,156
s_waitcnt vmcnt(25) lgkmcnt(0)
s_barrier
.LzPBV:
Mv 32,32,0
Rt 128,18,16384
Mv 36,36,0
Mv 96,40,0
Rt 130,18,18432
Mv 100,44,0
s_add_u32 s39, s39, 0x40000
Mv 160,48,0
Rt 132,19,16384
s_add_u32 m0, s37, 0
Mv 164,52,0
buffer_load_dwordx4 v12, s[8:11], s39 offen offset:0 lds
Mv 224,56,0
Rt 134,19,18432
Mv 228,60,0
Mv 236,60,4
Rt 136,20,16384
Mv 232,56,4
Mv 172,52,4
Rt 138,20,18432
Mv 168,48,4
Mv 108,44,4
Rt 140,21,16384
Mv 104,40,4
buffer_load_dwordx4 v13, s[8:11], s39 offen offset:1024 lds
Mv 44,36,4
Rt 142,21,18432
Mv 40,32,4
Mv 48,32,8
Rt 144,22,16384
Mv 52,36,8
Mv 112,40,8
Rt 146,22,18432
Mv 116,44,8
Mv 176,48,8
Rt 148,23,16384
Mv 180,52,8
buffer_load_dwordx4 v14, s[8:11], s39 offen offset:2048 lds
Mv 240,56,8
Rt 150,23,18432
Mv 244,60,8
Mv 252,60,12
Rt 152,24,16384
Mv 248,56,12
Mv 188,52,12
Rt 154,24,18432
Mv 184,48,12
Mv 124,44,12
Rt 156,25,16384
Mv 120,40,12
buffer_load_dwordx4 v15, s[8:11], s39 offen offset:3072 lds
Mv 60,36,12
Rt 158,25,18432
Mv 56,32,12
Mv 64,32,16
Rb 160,17,0
Mv 68,36,16
Mv 128,40,16
)" R"(
Rb 164,17,2048
Mv 132,44,16
Mv 192,48,16
Rb 168,17,8192
Mv 196,52,16
buffer_store_dwordx4 a[224:227], v1, s[20:23], s72 offen sc0 sc1
Ma 192,56,16
Rb 172,17,10240
s_add_u32 s72, s72, 1024
Ma 196,60,16
Ma 204,60,20
Rb 176,17,16384
Ma 200,56,20
Mv 204,52,20
Rb 180,17,18432
Mv 200,48,20
Mv 140,44,20
Rb 184,17,24576
Mv 136,40,20
Mv 76,36,20
Rb 188,17,26624
Mv 72,32,20
Mv 80,32,24
Mv 84,36,24
buffer_store_dwordx4 a[224:227], v1, s[20:23], s88 offen sc0 sc1
Mv 144,40,24
s_add_u32 s88, s88, 1024
Mv 148,44,24
Mv 208,48,24
Mv 212,52,24
Ma 208,56,24
Ma 212,60,24
Ma 220,60,28
Ma 216,56,28
Mv 220,52,28
Mv 216,48,28
Mv 156,44,28
Mv 152,40,28
Mv 92,36,28
Mv 88,32,28
s_waitcnt vmcnt(11) lgkmcnt(0)
s_barrier
Mv 32,160,128
Rt 0,18,32768
Mv 36,164,128
s_add_u32 s38, s38, 0xffff0080
Mv 96,168,128
Rt 2,18,34816
s_add_u32 m0, s36, 0
Mv 100,172,128
buffer_load_dwordx4 v8, s[4:7], s38 offen offset:0 lds
Mv 160,176,128
Rt 4,19,32768
Mv 164,180,128
Mv 224,184,128
Rt 6,19,34816
Mv 228,188,128
buffer_load_dwordx4 v9, s[4:7], s38 offen offset:1024 lds
Mv 236,188,132
Rt 8,20,32768
Mv 232,184,132
Mv 172,180,132
Rt 10,20,34816
Mv 168,176,132
buffer_load_dwordx4 v10, s[4:7], s38 offen offset:2048 lds
)" R"(
Mv 108,172,132
Rt 12,21,32768
Mv 104,168,132
Mv 44,164,132
Rt 14,21,34816
Mv 40,160,132
buffer_load_dwordx4 v11, s[4:7], s38 offen offset:3072 lds
Mv 48,160,136
Rt 16,22,32768
Mv 52,164,136
s_add_u32 s38, s38, 0x10000
Mv 112,168,136
Rt 18,22,34816
s_add_u32 m0, s36, 4096
Mv 116,172,136
buffer_load_dwordx4 v8, s[4:7], s38 offen offset:0 lds
Mv 176,176,136
Rt 20,23,32768
Mv 180,180,136
Mv 240,184,136
Rt 22,23,34816
Mv 244,188,136
buffer_load_dwordx4 v9, s[4:7], s38 offen offset:1024 lds
Mv 252,188,140
Rt 24,24,32768
Mv 248,184,140
Mv 188,180,140
Rt 26,24,34816
Mv 184,176,140
buffer_load_dwordx4 v10, s[4:7], s38 offen offset:2048 lds
Mv 124,172,140
Rt 28,25,32768
Mv 120,168,140
Mv 60,164,140
Rt 30,25,34816
Mv 56,160,140
buffer_load_dwordx4 v11, s[4:7], s38 offen offset:3072 lds
Mv 64,160,144
Rb 32,16,32768
Mv 68,164,144
Mv 128,168,144
Rb 36,16,34816
Mv 132,172,144
s_add_u32 s39, s39, 0x40000
Mv 192,176,144
Rb 40,16,40960
s_add_u32 m0, s37, 16384
Mv 196,180,144
buffer_load_dwordx4 v12, s[8:11], s39 offen offset:0 lds
Ma 192,184,144
Rb 44,16,43008
Ma 196,188,144
Ma 204,188,148
Rb 48,16,49152
Ma 200,184,148
Mv 204,180,148
Rb 52,16,51200
Mv 200,176,148
buffer_load_dwordx4 v13, s[8:11], s39 offen offset:1024 lds
Mv 140,172,148
Rb 56,16,57344
Mv 136,168,148
Mv 76,164,148
Rb 60,16,59392
)" R"(
Mv 72,160,148
Mv 80,160,152
Mv 84,164,152
buffer_load_dwordx4 v14, s[8:11], s39 offen offset:2048 lds
Mv 144,168,152
Mv 148,172,152
Mv 208,176,152
Mv 212,180,152
Ma 208,184,152
Ma 212,188,152
buffer_load_dwordx4 v15, s[8:11], s39 offen offset:3072 lds
Ma 220,188,156
Ma 216,184,156
s_add_u32 s43, s43, 0x60000
Mv 220,180,156
Mv 216,176,156
buffer_load_dword v27, v26, s[16:19], s43 offen
Mv 156,172,156
Mv 152,168,156
Mv 92,164,156
Mv 88,160,156
s_waitcnt vmcnt(19) lgkmcnt(0)
s_barrier
buffer_store_dword v6, v7, s[20:23], s52 offen offset:0 sc0 sc1
s_mov_b32 s52, 0x40000000
Mv 32,32,0
Rt 128,18,49152
Mv 36,36,0
Mv 96,40,0
Rt 130,18,51200
Mv 100,44,0
s_add_u32 s39, s39, 0x40000
Mv 160,48,0
Rt 132,19,49152
s_add_u32 m0, s37, 32768
Mv 164,52,0
buffer_load_dwordx4 v12, s[8:11], s39 offen offset:0 lds
Mv 224,56,0
Rt 134,19,51200
Mv 228,60,0
Mv 236,60,4
Rt 136,20,49152
Mv 232,56,4
Mv 172,52,4
Rt 138,20,51200
Mv 168,48,4
Mv 108,44,4
Rt 140,21,49152
Mv 104,40,4
buffer_load_dwordx4 v13, s[8:11], s39 offen offset:1024 lds
Mv 44,36,4
Rt 142,21,51200
Mv 40,32,4
Mv 48,32,8
Rt 144,22,49152
Mv 52,36,8
Mv 112,40,8
Rt 146,22,51200
Mv 116,44,8
Mv 176,48,8
Rt 148,23,49152
Mv 180,52,8
buffer_load_dwordx4 v14, s[8:11], s39 offen offset:2048 lds
Mv 240,56,8
Rt 150,23,51200
)" R"(
Mv 244,60,8
Mv 252,60,12
Rt 152,24,49152
Mv 248,56,12
Mv 188,52,12
Rt 154,24,51200
Mv 184,48,12
Mv 124,44,12
Rt 156,25,49152
Mv 120,40,12
buffer_load_dwordx4 v15, s[8:11], s39 offen offset:3072 lds
Mv 60,36,12
Rt 158,25,51200
Mv 56,32,12
Mv 64,32,16
Rb 160,17,32768
Mv 68,36,16
Mv 128,40,16
Rb 164,17,34816
Mv 132,44,16
Mv 192,48,16
Rb 168,17,40960
Mv 196,52,16
buffer_store_dwordx4 a[224:227], v1, s[20:23], s72 offen sc0 sc1
Ma 192,56,16
Rb 172,17,43008
s_add_u32 s72, s72, 1024
Ma 196,60,16
Ma 204,60,20
Rb 176,17,49152
Ma 200,56,20
Mv 204,52,20
Rb 180,17,51200
Mv 200,48,20
Mv 140,44,20
Rb 184,17,57344
Mv 136,40,20
Mv 76,36,20
Rb 188,17,59392
Mv 72,32,20
Mv 80,32,24
Mv 84,36,24
buffer_store_dwordx4 a[224:227], v1, s[20:23], s88 offen sc0 sc1
Mv 144,40,24
s_add_u32 s88, s88, 1024
Mv 148,44,24
Mv 208,48,24
Mv 212,52,24
Ma 208,56,24
Ma 212,60,24
Ma 220,60,28
Ma 216,56,28
Mv 220,52,28
Mv 216,48,28
Mv 156,44,28
Mv 152,40,28
Mv 92,36,28
s_cmp_eq_u32 s40, 3
s_cmov_b32 s72, 0x40000000
s_cmov_b32 s88, 0x40000000
Mv 88,32,28
s_waitcnt vmcnt(12) lgkmcnt(0)
s_barrier
Mv 32,160,128
Rt 0,18,0
)" R"(
Mv 36,164,128
s_add_u32 s38, s38, 0xffff0080
Mv 96,168,128
Rt 2,18,2048
s_add_u32 m0, s36, 32768
Mv 100,172,128
buffer_load_dwordx4 v8, s[4:7], s38 offen offset:0 lds
Mv 160,176,128
Rt 4,19,0
Mv 164,180,128
Mv 224,184,128
Rt 6,19,2048
Mv 228,188,128
buffer_load_dwordx4 v9, s[4:7], s38 offen offset:1024 lds
Mv 236,188,132
Rt 8,20,0
Mv 232,184,132
Mv 172,180,132
Rt 10,20,2048
Mv 168,176,132
buffer_load_dwordx4 v10, s[4:7], s38 offen offset:2048 lds
Mv 108,172,132
Rt 12,21,0
Mv 104,168,132
Mv 44,164,132
Rt 14,21,2048
Mv 40,160,132
buffer_load_dwordx4 v11, s[4:7], s38 offen offset:3072 lds
Mv 48,160,136
Rt 16,22,0
Mv 52,164,136
s_add_u32 s38, s38, 0x10000
Mv 112,168,136
Rt 18,22,2048
s_add_u32 m0, s36, 36864
Mv 116,172,136
buffer_load_dwordx4 v8, s[4:7], s38 offen offset:0 lds
Mv 176,176,136
Rt 20,23,0
Mv 180,180,136
Mv 240,184,136
Rt 22,23,2048
Mv 244,188,136
buffer_load_dwordx4 v9, s[4:7], s38 offen offset:1024 lds
Mv 252,188,140
Rt 24,24,0
Mv 248,184,140
Mv 188,180,140
Rt 26,24,2048
Mv 184,176,140
buffer_load_dwordx4 v10, s[4:7], s38 offen offset:2048 lds
Mv 124,172,140
Rt 28,25,0
Mv 120,168,140
Mv 60,164,140
Rt 30,25,2048
Mv 56,160,140
buffer_load_dwordx4 v11, s[4:7], s38 offen offset:3072 lds
Mv 64,160,144
Rb 32,16,0
Mv 68,164,144
Mv 128,168,144
Rb 36,16,2048
Mv 132,172,144
)" R"(
s_add_u32 s39, s39, 0x40000
Mv 192,176,144
Rb 40,16,8192
s_add_u32 m0, s37, 49152
Mv 196,180,144
buffer_load_dwordx4 v12, s[8:11], s39 offen offset:0 lds
Ma 192,184,144
Rb 44,16,10240
Ma 196,188,144
Ma 204,188,148
Rb 48,16,16384
Ma 200,184,148
Mv 204,180,148
Rb 52,16,18432
Mv 200,176,148
buffer_load_dwordx4 v13, s[8:11], s39 offen offset:1024 lds
Mv 140,172,148
Rb 56,16,24576
Mv 136,168,148
Mv 76,164,148
Rb 60,16,26624
Mv 72,160,148
Mv 80,160,152
Mv 84,164,152
buffer_load_dwordx4 v14, s[8:11], s39 offen offset:2048 lds
Mv 144,168,152
Mv 148,172,152
Mv 208,176,152
Mv 212,180,152
Ma 208,184,152
Ma 212,188,152
buffer_load_dwordx4 v15, s[8:11], s39 offen offset:3072 lds
Ma 220,188,156
Ma 216,184,156
s_add_u32 s43, s43, 0x60000
Mv 220,180,156
s_sub_u32 s40, s40, 1
Mv 216,176,156
buffer_load_dword v27, v26, s[16:19], s43 offen
Mv 156,172,156
Mv 152,168,156
Mv 92,164,156
Mv 88,160,156
s_cmp_lg_u32 s40, 0
s_waitcnt vmcnt(21) lgkmcnt(0)
s_barrier
s_cbranch_scc1 .LzPBV
.LzPBVX:
Mv 32,32,0
Rt 128,18,16384
Mv 36,36,0
buffer_load_dword v4, v5, s[20:23], s50 offen offset:0 sc0 sc1
Mv 96,40,0
Rt 130,18,18432
Mv 100,44,0
Mv 160,48,0
Rt 132,19,16384
Mv 164,52,0
Mv 224,56,0
Rt 134,19,18432
Mv 228,60,0
Mv 236,60,4
Rt 136,20,16384
Mv 232,56,4
Mv 172,52,4
)" R"(
Rt 138,20,18432
Mv 168,48,4
Mv 108,44,4
Rt 140,21,16384
Mv 104,40,4
Mv 44,36,4
Rt 142,21,18432
Mv 40,32,4
Mv 48,32,8
Rt 144,22,16384
Mv 52,36,8
Mv 112,40,8
Rt 146,22,18432
Mv 116,44,8
Mv 176,48,8
Rt 148,23,16384
Mv 180,52,8
Mv 240,56,8
Rt 150,23,18432
Mv 244,60,8
Mv 252,60,12
Rt 152,24,16384
Mv 248,56,12
Mv 188,52,12
Rt 154,24,18432
Mv 184,48,12
Mv 124,44,12
Rt 156,25,16384
Mv 120,40,12
Mv 60,36,12
Rt 158,25,18432
Mv 56,32,12
Mv 64,32,16
Rb 160,17,0
Mv 68,36,16
Mv 128,40,16
Rb 164,17,2048
Mv 132,44,16
Mv 192,48,16
Rb 168,17,8192
Mv 196,52,16
Ma 192,56,16
Rb 172,17,10240
Ma 196,60,16
s_waitcnt vmcnt(0)
v_cmp_eq_u32_e32 vcc, s3, v4
s_andn2_b64 s[28:29], -1, vcc
s_cbranch_scc1 .Lzsp16
.Lzok16:
Ma 204,60,20
Rb 176,17,16384
Ma 200,56,20
s_lshl3_add_u32 s29, s34, s35
s_lshl2_add_u32 s29, s29, s33
Mv 204,52,20
Rb 180,17,18432
s_add_u32 m0, s36, 131072
Mv 200,48,20
buffer_load_dwordx4 v2, s[20:23], s56 offen offset:0 sc1 lds
Mv 140,44,20
Rb 184,17,24576
s_mul_i32 s83, s29, 7168
Mv 136,40,20
s_lshl_b32 s82, s34, 21
)" R"(
s_add_u32 s82, s82, 0x6000000
Mv 76,36,20
Rb 188,17,26624
s_add_u32 s82, s82, s84
Mv 72,32,20
s_mov_b32 s24, s80
s_mov_b32 s25, s81
Mv 80,32,24
s_mov_b32 s26, 0x380000
s_mov_b32 s27, 0x20000
Mv 84,36,24
buffer_load_dwordx4 v2, s[20:23], s56 offen offset:1024 sc1 lds
Mv 144,40,24
s_add_u32 s28, s33, 0
s_lshl2_add_u32 s28, s35, s28
Mv 148,44,24
s_mul_i32 s29, s28, 293
s_lshr_b32 s29, s29, 11
Mv 208,48,24
s_mul_i32 s29, s29, 9
s_add_u32 s29, s29, s28
Mv 212,52,24
s_lshl_b32 s29, s29, 10
s_add_u32 s4, s29, s82
Ma 208,56,24
s_add_u32 s28, s33, 4
s_lshl3_add_u32 s28, s28, s35
Ma 212,60,24
buffer_load_dwordx4 v2, s[20:23], s56 offen offset:2048 sc1 lds
Ma 220,60,28
s_mul_i32 s29, s28, 293
s_lshr_b32 s29, s29, 11
Ma 216,56,28
s_mul_i32 s29, s29, 9
s_add_u32 s29, s29, s28
Mv 220,52,28
s_lshl_b32 s29, s29, 10
s_add_u32 s5, s29, s82
Mv 216,48,28
s_add_u32 s28, s33, 8
s_lshl3_add_u32 s28, s28, s35
Mv 156,44,28
s_mul_i32 s29, s28, 293
s_lshr_b32 s29, s29, 11
Mv 152,40,28
buffer_load_dwordx4 v2, s[20:23], s56 offen offset:3072 sc1 lds
Mv 92,36,28
s_mul_i32 s29, s29, 9
s_add_u32 s29, s29, s28
Mv 88,32,28
s_waitcnt lgkmcnt(0)
s_barrier
Mv 32,160,128
Rt 0,18,32768
s_lshl_b32 s29, s29, 10
Mv 36,164,128
s_add_u32 s6, s29, s82
s_add_u32 s28, s33, 12
Mv 96,168,128
Rt 2,18,34816
s_add_u32 m0, s36, 0
Mv 100,172,128
buffer_load_dwordx4 v2, s[20:23], s55 offen offset:0 sc1 lds
Mv 160,176,128
Rt 4,19,32768
)" R"(
s_lshl3_add_u32 s28, s28, s35
Mv 164,180,128
s_mul_i32 s29, s28, 293
s_lshr_b32 s29, s29, 11
Mv 224,184,128
Rt 6,19,34816
s_mul_i32 s29, s29, 9
Mv 228,188,128
s_add_u32 s29, s29, s28
s_lshl_b32 s29, s29, 10
Mv 236,188,132
Rt 8,20,32768
buffer_load_dwordx4 v2, s[20:23], s55 offen offset:1024 sc1 lds
Mv 232,184,132
s_add_u32 s7, s29, s82
Mv 172,180,132
Rt 10,20,34816
Mv 168,176,132
s_cmp_gt_u32 s33, 1
s_cmov_b32 s7, 0x40000000
Mv 108,172,132
Rt 12,21,32768
s_add_u32 s28, s33, 14
Mv 104,168,132
buffer_load_dwordx4 v2, s[20:23], s55 offen offset:2048 sc1 lds
Mv 44,164,132
Rt 14,21,34816
s_lshl3_add_u32 s28, s28, s35
Mv 40,160,132
s_mul_i32 s29, s28, 293
s_lshr_b32 s29, s29, 11
Mv 48,160,136
Rt 16,22,32768
s_mul_i32 s29, s29, 9
Mv 52,164,136
s_add_u32 s29, s29, s28
s_lshl_b32 s29, s29, 10
Mv 112,168,136
Rt 18,22,34816
buffer_load_dwordx4 v2, s[20:23], s55 offen offset:3072 sc1 lds
Mv 116,172,136
s_add_u32 s8, s29, s82
s_add_u32 s28, s33, 18
Mv 176,176,136
Rt 20,23,32768
s_lshl3_add_u32 s28, s28, s35
Mv 180,180,136
s_mul_i32 s29, s28, 293
s_lshr_b32 s29, s29, 11
Mv 240,184,136
Rt 22,23,34816
s_add_u32 m0, s36, 4096
Mv 244,188,136
buffer_load_dwordx4 v3, s[20:23], s55 offen offset:0 sc1 lds
Mv 252,188,140
Rt 24,24,32768
s_mul_i32 s29, s29, 9
Mv 248,184,140
s_add_u32 s29, s29, s28
s_lshl_b32 s29, s29, 10
Mv 188,180,140
Rt 26,24,34816
s_add_u32 s9, s29, s82
Mv 184,176,140
)" R"(
s_add_u32 s28, s33, 22
s_lshl3_add_u32 s28, s28, s35
Mv 124,172,140
Rt 28,25,32768
buffer_load_dwordx4 v3, s[20:23], s55 offen offset:1024 sc1 lds
Mv 120,168,140
s_mul_i32 s29, s28, 293
s_lshr_b32 s29, s29, 11
Mv 60,164,140
Rt 30,25,34816
s_mul_i32 s29, s29, 9
Mv 56,160,140
s_add_u32 s29, s29, s28
s_lshl_b32 s29, s29, 10
Mv 64,160,144
Rb 32,16,32768
s_add_u32 s10, s29, s82
Mv 68,164,144
buffer_load_dwordx4 v3, s[20:23], s55 offen offset:2048 sc1 lds
Mv 128,168,144
Rb 36,16,34816
s_add_u32 s28, s33, 26
Mv 132,172,144
s_lshl3_add_u32 s28, s28, s35
s_mul_i32 s29, s28, 293
Mv 192,176,144
Rb 40,16,40960
s_lshr_b32 s29, s29, 11
Mv 196,180,144
s_mul_i32 s29, s29, 9
s_add_u32 s29, s29, s28
Ma 192,184,144
Rb 44,16,43008
buffer_load_dwordx4 v3, s[20:23], s55 offen offset:3072 sc1 lds
Ma 196,188,144
s_lshl_b32 s29, s29, 10
s_add_u32 s11, s29, s82
Ma 204,188,148
Rb 48,16,49152
Ma 200,184,148
s_cmp_gt_u32 s33, 1
s_cmov_b32 s11, 0x40000000
Mv 204,180,148
Rb 52,16,51200
s_add_u32 m0, s36, 135168
Mv 200,176,148
buffer_load_dwordx4 v3, s[20:23], s56 offen offset:0 sc1 lds
Mv 140,172,148
Rb 56,16,57344
s_and_b32 s28, s34, 1
Mv 136,168,148
s_lshl_b32 s28, s28, 14
s_add_u32 s82, s37, s28
Mv 76,164,148
Rb 60,16,59392
s_add_u32 s82, s82, 0x10000
Mv 72,160,148
Mv 80,160,152
buffer_load_dwordx4 v3, s[20:23], s56 offen offset:1024 sc1 lds
Mv 84,164,152
Mv 144,168,152
Mv 148,172,152
Mv 208,176,152
Mv 212,180,152
)" R"(
buffer_load_dwordx4 v3, s[20:23], s56 offen offset:2048 sc1 lds
Ma 208,184,152
Ma 212,188,152
Ma 220,188,156
Ma 216,184,156
Mv 220,180,156
buffer_load_dwordx4 v3, s[20:23], s56 offen offset:3072 sc1 lds
Mv 216,176,156
Mv 156,172,156
Mv 152,168,156
s_add_u32 s55, s55, 0x10000
s_add_u32 s56, s56, 0x10000
Mv 92,164,156
Mv 88,160,156
s_waitcnt lgkmcnt(4)
s_barrier
Mv 32,32,0
Rt 128,18,49152
Mv 36,36,0
Rt 130,18,51200
Mv 96,40,0
Rt 132,19,49152
s_add_u32 m0, s36, 65536
Mv 100,44,0
Rt 134,19,51200
buffer_load_dwordx4 v2, s[20:23], s56 offen offset:0 sc1 lds
Mv 108,44,4
Rt 136,20,49152
Mv 104,40,4
Rt 138,20,51200
s_add_u32 s50, s50, 1024
Mv 44,36,4
Rt 140,21,49152
s_min_u32 s50, s50, s60
Mv 40,32,4
Rt 142,21,51200
Mv 48,32,8
Rt 144,22,49152
Mv 52,36,8
Rt 146,22,51200
Mv 112,40,8
Rt 148,23,49152
Mv 116,44,8
Rt 150,23,51200
buffer_load_dwordx4 v2, s[20:23], s56 offen offset:1024 sc1 lds
Mv 124,44,12
Rt 152,24,49152
Mv 120,40,12
Rt 154,24,51200
Mv 60,36,12
Rt 156,25,49152
Mv 56,32,12
Rt 158,25,51200
Mv 64,32,16
Rb 160,17,32768
Mv 68,36,16
Rb 164,17,34816
Mv 128,40,16
Rb 168,17,40960
Mv 132,44,16
Rb 172,17,43008
buffer_load_dwordx4 v2, s[20:23], s56 offen offset:2048 sc1 lds
Mv 140,44,20
Rb 176,17,49152
Mv 136,40,20
)" R"(
Rb 180,17,51200
Mv 76,36,20
Rb 184,17,57344
Mv 72,32,20
Rb 188,17,59392
Mv 80,32,24
v_add_u32 v8, 0x10000, v1
v_add_u32 v10, 0x20000, v1
Mv 84,36,24
buffer_load_dword v4, v5, s[20:23], s50 offen offset:0 sc0 sc1
Mv 144,40,24
s_mov_b32 s93, 2
s_mov_b32 s94, 0
Mv 148,44,24
buffer_load_dwordx4 v2, s[20:23], s56 offen offset:3072 sc1 lds
Mv 156,44,28
Mv 152,40,28
Mv 92,36,28
Mv 88,32,28
s_waitcnt lgkmcnt(0)
s_barrier
Mv 32,160,128
Mv 36,164,128
Mv 40,160,132
s_add_u32 m0, s36, 32768
Mv 44,164,132
buffer_load_dwordx4 v2, s[20:23], s55 offen offset:0 sc1 lds
Mv 48,160,136
Mv 52,164,136
s_nop 0
Cv 32,32,33
Cv 33,34,35
Mv 56,160,140
Cv 34,36,37
Cv 35,38,39
Mv 60,164,140
Cv 40,40,41
Cv 41,42,43
Mv 64,160,144
Cv 42,44,45
Cv 43,46,47
Mv 68,164,144
Cv 48,48,49
Cv 49,50,51
Mv 72,160,148
Cv 50,52,53
Cv 51,54,55
Mv 76,164,148
buffer_load_dwordx4 v2, s[20:23], s55 offen offset:1024 sc1 lds
Mv 80,160,152
Cv 56,56,57
Cv 57,58,59
Mv 84,164,152
Cv 58,60,61
Cv 59,62,63
Mv 88,160,156
Cv 64,64,65
Cv 65,66,67
Mv 92,164,156
Cv 66,68,69
Cv 67,70,71
Mv 96,168,128
Cv 72,72,73
Cv 73,74,75
Mv 100,172,128
)" R"(
Cv 74,76,77
Cv 75,78,79
Mv 104,168,132
Cv 80,80,81
Cv 81,82,83
Mv 108,172,132
buffer_load_dwordx4 v2, s[20:23], s55 offen offset:2048 sc1 lds
Mv 112,168,136
Cv 82,84,85
Cv 83,86,87
Mv 116,172,136
Cv 88,88,89
Cv 89,90,91
Mv 120,168,140
Cv 90,92,93
Cv 91,94,95
Mv 124,172,140
buffer_store_dwordx4 v[32:35], v2, s[20:23], s45 offen offset:0 sc0 sc1
Mv 128,168,144
Mv 132,172,144
Mv 136,168,148
Mv 140,172,148
buffer_load_dwordx4 v2, s[20:23], s55 offen offset:3072 sc1 lds
Mv 144,168,152
Mv 148,172,152
s_nop 1
Cv 128,128,129
Cv 129,130,131
Mv 152,168,156
Cv 130,132,133
Cv 131,134,135
Mv 156,172,156
buffer_store_dwordx4 v[40:43], v2, s[20:23], s45 offen offset:1024 sc0 sc1
Mv 160,48,0
Cv 136,136,137
Cv 137,138,139
Mv 160,176,128
Cv 138,140,141
Cv 139,142,143
Mv 164,52,0
s_add_u32 m0, s36, 36864
Cv 144,144,145
Mv 164,180,128
buffer_load_dwordx4 v3, s[20:23], s55 offen offset:0 sc1 lds
Mv 168,48,4
Cv 145,146,147
Cv 146,148,149
Mv 168,176,132
Cv 147,150,151
Cv 152,152,153
Mv 172,52,4
Cv 153,154,155
Cv 154,156,157
Mv 172,180,132
buffer_store_dwordx4 v[48:51], v2, s[20:23], s45 offen offset:2048 sc0 sc1
Mv 176,48,8
Cv 155,158,159
Cv 132,96,97
Mv 176,176,136
Cv 133,98,99
Cv 134,100,101
Mv 180,52,8
Cv 135,102,103
Cv 140,104,105
)" R"(
Mv 180,180,136
buffer_load_dwordx4 v3, s[20:23], s55 offen offset:1024 sc1 lds
Mv 184,48,12
Cv 141,106,107
Cv 142,108,109
Mv 184,176,140
Cv 143,110,111
Cv 148,112,113
Mv 188,52,12
Cv 149,114,115
Cv 150,116,117
Mv 188,180,140
buffer_store_dwordx4 v[56:59], v2, s[20:23], s45 offen offset:3072 sc0 sc1
Mv 192,48,16
Cv 151,118,119
Cv 156,120,121
Mv 192,176,144
Cv 157,122,123
Cv 158,124,125
Mv 196,52,16
Cv 159,126,127
Cv 160,160,161
Mv 196,180,144
buffer_load_dwordx4 v3, s[20:23], s55 offen offset:2048 sc1 lds
Mv 200,48,20
Cv 161,162,163
Cv 162,164,165
Mv 200,176,148
Cv 163,166,167
v_accvgpr_write_b32 a224, v160
Mv 204,52,20
v_accvgpr_write_b32 a225, v161
v_accvgpr_write_b32 a226, v162
Mv 204,180,148
buffer_store_dwordx4 v[64:67], v3, s[20:23], s45 offen offset:0 sc0 sc1
Mv 208,48,24
v_accvgpr_write_b32 a227, v163
Cv 168,168,169
Mv 208,176,152
Cv 169,170,171
Cv 170,172,173
Mv 212,52,24
Cv 171,174,175
v_accvgpr_write_b32 a228, v168
Mv 212,180,152
buffer_load_dwordx4 v3, s[20:23], s55 offen offset:3072 sc1 lds
Mv 216,48,28
v_accvgpr_write_b32 a229, v169
v_accvgpr_write_b32 a230, v170
Mv 216,176,156
v_accvgpr_write_b32 a231, v171
Cv 176,176,177
Mv 220,52,28
Cv 177,178,179
Cv 178,180,181
Mv 220,180,156
buffer_store_dwordx4 v[72:75], v3, s[20:23], s45 offen offset:1024 sc0 sc1
Mv 224,56,0
Cv 179,182,183
v_accvgpr_write_b32 a232, v176
Mv 228,60,0
v_accvgpr_write_b32 a233, v177
v_accvgpr_write_b32 a234, v178
Mv 236,60,4
)" R"(
s_add_u32 m0, s36, 69632
v_accvgpr_write_b32 a235, v179
Mv 232,56,4
buffer_load_dwordx4 v3, s[20:23], s56 offen offset:0 sc1 lds
Mv 240,56,8
Cv 184,184,185
Cv 185,186,187
Mv 244,60,8
Cv 186,188,189
Cv 187,190,191
Mv 252,60,12
v_accvgpr_write_b32 a236, v184
v_accvgpr_write_b32 a237, v185
Mv 248,56,12
s_waitcnt vmcnt(16)
s_barrier
Ma 192,56,16
v_cmp_eq_u32_e32 vcc, s3, v4
s_andn2_b64 s[28:29], -1, vcc
s_cbranch_scc1 .Lzsp17
.Lzok17:
Ma 196,60,16
buffer_store_dwordx4 v[80:83], v3, s[20:23], s45 offen offset:2048 sc0 sc1
Ma 204,60,20
v_accvgpr_write_b32 a238, v186
v_accvgpr_write_b32 a239, v187
Ma 200,56,20
buffer_load_dwordx4 v3, s[20:23], s56 offen offset:1024 sc1 lds
Ma 208,56,24
Cv 192,192,193
Cv 193,194,195
Ma 212,60,24
Cv 194,196,197
Cv 195,198,199
Ma 220,60,28
v_accvgpr_write_b32 a240, v192
v_accvgpr_write_b32 a241, v193
Ma 216,56,28
buffer_store_dwordx4 v[88:91], v3, s[20:23], s45 offen offset:3072 sc0 sc1
Mv 224,184,128
Rb 0,1,0
v_accvgpr_write_b32 a242, v194
Mv 228,188,128
Rb 4,1,1024
v_accvgpr_write_b32 a243, v195
Mv 232,184,132
Rb 8,1,2048
Cv 200,200,201
Mv 236,188,132
buffer_load_dwordx4 v3, s[20:23], s56 offen offset:2048 sc1 lds
Rb 12,1,3072
Mv 240,184,136
Rb 16,1,8192
Cv 201,202,203
Mv 244,188,136
Rb 20,1,9216
Cv 202,204,205
Mv 248,184,140
Rb 24,1,10240
Cv 203,206,207
Mv 252,188,140
Rb 28,1,11264
v_accvgpr_write_b32 a244, v200
Ma 192,184,144
Rb 32,1,4096
)" R"(
v_accvgpr_write_b32 a245, v201
Ma 196,188,144
Rb 36,1,5120
v_accvgpr_write_b32 a246, v202
Ma 200,184,148
Rb 40,1,6144
v_accvgpr_write_b32 a247, v203
Ma 204,188,148
buffer_load_dwordx4 v3, s[20:23], s56 offen offset:3072 sc1 lds
Rb 44,1,7168
Ma 208,184,152
Ma 212,188,152
Ma 216,184,156
Ma 220,188,156
s_add_u32 s56, s56, 0x10000
.LzFB:
s_waitcnt lgkmcnt(8)
Mz 64,0,64
Rb 48,1,12288
s_add_u32 s55, s55, 0x10000
Mz 68,0,68
s_cmp_eq_u32 s57, 2
s_cselect_b32 s55, s59, s55
Mv 68,4,76
Rb 52,1,13312
s_sub_u32 s57, s57, 1
v_mov_b32 v16, 0
Mv 64,4,72
buffer_store_dwordx4 v[132:135], v2, s[20:23], s46 offen offset:0 sc0 sc1
Mv 64,8,80
Rb 56,1,14336
Cv 208,208,209
Cv 209,210,211
Mv 68,8,84
Cv 210,212,213
Cv 211,214,215
Mv 68,12,92
Rb 60,1,15360
v_accvgpr_write_b32 a248, v208
s_waitcnt lgkmcnt(8)
Mv 64,12,88
v_accvgpr_write_b32 a249, v209
v_accvgpr_write_b32 a250, v210
Mv 64,16,96
Rb 0,1,16384
v_accvgpr_write_b32 a251, v211
Mv 68,16,100
Cv 216,216,217
Cv 217,218,219
Cv 218,220,221
Mv 68,20,108
Rb 4,1,17408
Cv 219,222,223
Mv 64,20,104
v_accvgpr_write_b32 a252, v216
v_accvgpr_write_b32 a253, v217
v_accvgpr_write_b32 a254, v218
Mv 64,24,112
Rb 8,1,18432
v_accvgpr_write_b32 a255, v219
Mv 68,24,116
buffer_store_dwordx4 v[140:143], v2, s[20:23], s46 offen offset:1024 sc0 sc1
Cv 224,224,225
Mv 68,28,124
)" R"(
Rb 12,1,19456
s_waitcnt lgkmcnt(8)
Mv 64,28,120
Cv 225,226,227
Cv 226,228,229
Cv 227,230,231
Mz 72,32,64
Rb 16,1,24576
v_accvgpr_write_b32 a128, v224
Mz 76,32,68
v_accvgpr_write_b32 a129, v225
v_accvgpr_write_b32 a130, v226
Mv 76,36,76
Rb 20,1,25600
v_accvgpr_write_b32 a131, v227
Cv 232,232,233
Mv 72,36,72
Cv 233,234,235
Cv 234,236,237
Mv 72,40,80
Rb 24,1,26624
Cv 235,238,239
v_accvgpr_write_b32 a132, v232
Mv 76,40,84
v_accvgpr_write_b32 a133, v233
v_accvgpr_write_b32 a134, v234
Mv 76,44,92
Rb 28,1,27648
v_accvgpr_write_b32 a135, v235
s_waitcnt lgkmcnt(8)
Mv 72,44,88
buffer_store_dwordx4 v[148:151], v2, s[20:23], s46 offen offset:2048 sc0 sc1
Mv 72,48,96
Rb 32,1,20480
Cv 240,240,241
Mv 76,48,100
Cv 241,242,243
Cv 242,244,245
Cv 243,246,247
Mv 76,52,108
Rb 36,1,21504
v_accvgpr_write_b32 a136, v240
Mv 72,52,104
v_accvgpr_write_b32 a137, v241
v_accvgpr_write_b32 a138, v242
v_accvgpr_write_b32 a139, v243
Mv 72,56,112
Rb 40,1,22528
Cv 248,248,249
Mv 76,56,116
Cv 249,250,251
Cv 250,252,253
Cv 251,254,255
Mv 76,60,124
Rb 44,1,23552
s_waitcnt lgkmcnt(8)
Mv 72,60,120
v_accvgpr_write_b32 a140, v248
v_accvgpr_write_b32 a141, v249
v_accvgpr_write_b32 a142, v250
Mz 80,0,64
Rb 48,1,28672
v_accvgpr_write_b32 a143, v251
Mz 84,0,68
)" R"(
buffer_store_dwordx4 v[156:159], v2, s[20:23], s46 offen offset:3072 sc0 sc1
Mv 84,4,76
Rb 52,1,29696
v_mov_b32 v17, 0
v_mov_b32 v18, 0
Mv 80,4,72
v_mov_b32 v19, 0
v_mov_b32 v20, 0
Mv 80,8,80
Rb 56,1,30720
v_exp_f32_e32 v32, v64
Mv 84,8,84
v_exp_f32_e32 v24, v65
Mv 84,12,92
Rb 60,1,31744
v_mov_b32 v21, 0
s_waitcnt lgkmcnt(8)
Mv 80,12,88
v_exp_f32_e32 v33, v66
Mv 80,16,96
v_exp_f32_e32 v25, v67
Mv 84,16,100
v_exp_f32_e32 v34, v72
v_mov_b32 v22, 0
Mv 84,20,108
v_exp_f32_e32 v26, v73
Mv 80,20,104
buffer_store_dwordx4 v[128:131], v3, s[20:23], s46 offen offset:0 sc0 sc1
v_mov_b32 v23, 0
Mv 80,24,112
s_add_u32 s50, s50, 1024
s_min_u32 s50, s50, s60
Mv 84,24,116
v_exp_f32_e32 v35, v74
v_add_f32_e32 v14, v32, v24
Mv 84,28,124
v_exp_f32_e32 v27, v75
Mv 80,28,120
s_waitcnt vmcnt(7) lgkmcnt(0)
s_barrier
buffer_store_dword v6, v7, s[20:23], s49 offen offset:0 sc0 sc1
Mz 88,32,64
Rb 0,1,32768
Cv 32,32,24
Mz 92,32,68
Rb 4,1,33792
v_add_f32_e32 v14, v14, v33
Mv 92,36,76
Rb 8,1,34816
s_add_u32 m0, s36, 0
v_add_f32_e32 v14, v14, v25
Mv 88,36,72
Rb 12,1,35840
buffer_load_dwordx4 v2, s[20:23], s55 offen offset:0 sc1 lds
Mv 88,40,80
Rb 16,1,40960
v_exp_f32_e32 v36, v68
Mv 92,40,84
Rb 20,1,41984
Cv 33,33,25
Mv 92,44,92
Rb 24,1,43008
v_exp_f32_e32 v28, v69
Mv 88,44,88
Rb 28,1,44032
)" R"(
v_add_f32_e32 v14, v14, v34
Mv 88,48,96
Rb 32,1,36864
v_add_f32_e32 v14, v14, v26
Mv 92,48,100
Rb 36,1,37888
v_exp_f32_e32 v37, v70
Mv 92,52,108
Rb 40,1,38912
Cv 34,34,26
Mv 88,52,104
Rb 44,1,39936
buffer_load_dwordx4 v2, s[20:23], s55 offen offset:1024 sc1 lds
Mv 88,56,112
v_exp_f32_e32 v29, v71
Mv 92,56,116
v_exp_f32_e32 v38, v76
v_add_f32_e32 v14, v14, v35
Mv 92,60,124
v_add_f32_e32 v14, v14, v27
s_waitcnt lgkmcnt(8)
Mv 88,60,120
v_exp_f32_e32 v24, v77
Cv 35,35,27
Mz 96,0,64
Rb 48,1,45056
buffer_load_dword v4, v5, s[20:23], s50 offen offset:0 sc0 sc1
Mz 100,0,68
v_exp_f32_e32 v39, v78
Mv 100,4,76
Rb 52,1,46080
v_exp_f32_e32 v25, v79
Mv 96,4,72
buffer_load_dwordx4 v2, s[20:23], s55 offen offset:2048 sc1 lds
Mv 96,8,80
Rb 56,1,47104
v_exp_f32_e32 v40, v80
Mv 100,8,84
v_exp_f32_e32 v26, v81
Mv 100,12,92
Rb 60,1,48128
v_add_f32_e32 v15, v36, v28
s_waitcnt lgkmcnt(8)
Mv 96,12,88
buffer_store_dwordx4 v[136:139], v3, s[20:23], s46 offen offset:1024 sc0 sc1
Mv 96,16,96
Rb 0,1,49152
Cv 36,36,28
Mv 100,16,100
v_exp_f32_e32 v41, v82
v_add_f32_e32 v15, v15, v37
Mv 100,20,108
Rb 4,1,50176
v_add_f32_e32 v15, v15, v29
Mv 96,20,104
buffer_load_dwordx4 v2, s[20:23], s55 offen offset:3072 sc1 lds
Cv 37,37,29
Mv 96,24,112
Rb 8,1,51200
v_add_f32_e32 v15, v15, v38
Mv 100,24,116
v_exp_f32_e32 v27, v83
v_add_f32_e32 v15, v15, v24
Mv 100,28,124
)" R"(
Rb 12,1,52224
s_waitcnt lgkmcnt(8)
Mv 96,28,120
v_exp_f32_e32 v42, v88
Cv 38,38,24
Mz 104,32,64
Rb 16,1,57344
v_add_f32_e32 v15, v15, v39
Mz 108,32,68
v_exp_f32_e32 v28, v89
Mv 108,36,76
Rb 20,1,58368
s_add_u32 m0, s36, 4096
v_add_f32_e32 v15, v15, v25
Mv 104,36,72
buffer_load_dwordx4 v3, s[20:23], s55 offen offset:0 sc1 lds
Mv 104,40,80
Rb 24,1,59392
v_exp_f32_e32 v43, v90
Mv 108,40,84
v_exp_f32_e32 v29, v91
Mv 108,44,92
Rb 28,1,60416
Cv 39,39,25
s_waitcnt lgkmcnt(8)
Mv 104,44,88
buffer_store_dwordx4 v[144:147], v3, s[20:23], s46 offen offset:2048 sc0 sc1
Mv 104,48,96
Rb 32,1,53248
v_add_f32_e32 v14, v14, v40
Mv 108,48,100
v_exp_f32_e32 v44, v84
v_add_f32_e32 v14, v14, v26
Mv 108,52,108
Rb 36,1,54272
Cv 40,40,26
Mv 104,52,104
buffer_load_dwordx4 v3, s[20:23], s55 offen offset:1024 sc1 lds
v_add_f32_e32 v14, v14, v41
Mv 104,56,112
Rb 40,1,55296
v_add_f32_e32 v14, v14, v27
Mv 108,56,116
v_exp_f32_e32 v24, v85
Cv 41,41,27
Mv 108,60,124
Rb 44,1,56320
s_waitcnt lgkmcnt(8)
Mv 104,60,120
v_exp_f32_e32 v45, v86
v_add_f32_e32 v14, v14, v42
Mz 112,0,64
Rb 48,1,61440
v_add_f32_e32 v14, v14, v28
Mz 116,0,68
v_exp_f32_e32 v25, v87
Mv 116,4,76
Rb 52,1,62464
v_exp_f32_e32 v46, v92
Mv 112,4,72
buffer_load_dwordx4 v3, s[20:23], s55 offen offset:2048 sc1 lds
Mv 112,8,80
Rb 56,1,63488
v_exp_f32_e32 v26, v93
)" R"(
Mv 116,8,84
v_exp_f32_e32 v47, v94
Mv 116,12,92
Rb 60,1,64512
Cv 42,42,28
s_waitcnt lgkmcnt(8)
Mv 112,12,88
buffer_store_dwordx4 v[152:155], v3, s[20:23], s46 offen offset:3072 sc0 sc1
Mv 112,16,96
v_exp_f32_e32 v27, v95
Mv 116,16,100
v_add_f32_e32 v14, v14, v43
v_add_f32_e32 v14, v14, v29
Cv 43,43,29
Mv 116,20,108
v_add_f32_e32 v15, v15, v44
v_add_f32_e32 v15, v15, v24
Mv 112,20,104
buffer_load_dwordx4 v3, s[20:23], s55 offen offset:3072 sc1 lds
Cv 44,44,24
Mv 112,24,112
v_add_f32_e32 v15, v15, v45
v_add_f32_e32 v15, v15, v25
Mv 116,24,116
Cv 45,45,25
v_add_f32_e32 v15, v15, v46
v_add_f32_e32 v15, v15, v26
Mv 116,28,124
Cv 46,46,26
v_add_f32_e32 v15, v15, v47
Mv 112,28,120
v_add_f32_e32 v15, v15, v27
s_waitcnt lgkmcnt(0)
s_barrier
Mz 120,32,64
Rb 0,10,0
Cv 47,47,27
Mz 124,32,68
Rb 4,10,1024
v_max_f32_e32 v11, v14, v15
Mv 124,36,76
Rb 8,10,2048
v_mov_b32 v24, v14
v_mov_b32 v25, v15
Mv 120,36,72
Rb 12,10,3072
v_permlane32_swap_b32_e32 v24, v25
Mv 120,40,80
Rb 16,10,4096
v_add_f32_e32 v24, v24, v25
v_mov_b32 v25, v24
Mv 124,40,84
Rb 20,10,5120
v_permlane16_swap_b32_e32 v24, v25
Mv 124,44,92
Rb 24,10,6144
v_add_f32_e32 v24, v24, v25
s_add_u32 s55, s55, 0x10000
Mv 120,44,88
Rb 28,10,7168
Mv 120,48,96
Rb 32,10,8192
Mv 124,48,100
Rb 36,10,9216
v_rcp_f32_e32 v24, v24
)" R"(
Mv 124,52,108
Rb 40,10,10240
v_max_f32_e32 v11, v11, v24
Mv 120,52,104
Rb 44,10,11264
s_cmp_eq_u32 s57, 2
s_cselect_b32 s55, s59, s55
Mv 120,56,112
v_cmp_nge_f32_e32 vcc, s62, v11
Mv 124,56,116
Mv 124,60,124
s_waitcnt lgkmcnt(8)
s_cbranch_vccnz .Lzfx6
.Lzfr6:
Mv 120,60,120
v_mov_b32 v12, v14
v_mov_b32 v13, v15
v_mfma_f32_16x16x32_bf16 v[128:131], a[0:3], v[32:35], 0
Rb 48,10,12288
v_mfma_f32_16x16x32_bf16 v[136:139], a[0:3], v[36:39], 0
v_mfma_f32_16x16x32_bf16 v[140:143], a[4:7], v[36:39], 0
Rb 52,10,13312
s_add_u32 m0, s36, 32768
v_mfma_f32_16x16x32_bf16 v[132:135], a[4:7], v[32:35], 0
buffer_load_dwordx4 v2, s[20:23], s55 offen offset:0 sc1 lds
v_mfma_f32_16x16x32_bf16 v[144:147], a[8:11], v[32:35], 0
Rb 56,10,14336
v_mfma_f32_16x16x32_bf16 v[152:155], a[8:11], v[36:39], 0
v_exp_f32_e32 v48, v96
v_mfma_f32_16x16x32_bf16 v[156:159], a[12:15], v[36:39], 0
Rb 60,10,15360
s_waitcnt lgkmcnt(8)
v_mfma_f32_16x16x32_bf16 v[148:151], a[12:15], v[32:35], 0
v_exp_f32_e32 v28, v97
v_mfma_f32_16x16x32_bf16 v[160:163], a[16:19], v[32:35], 0
Rb 0,10,16384
v_add_f32_e32 v14, v48, v28
v_mfma_f32_16x16x32_bf16 v[168:171], a[16:19], v[36:39], 0
v_exp_f32_e32 v49, v98
v_mfma_f32_16x16x32_bf16 v[172:175], a[20:23], v[36:39], 0
Rb 4,10,17408
Cv 48,48,28
v_mfma_f32_16x16x32_bf16 v[164:167], a[20:23], v[32:35], 0
buffer_load_dwordx4 v2, s[20:23], s55 offen offset:1024 sc1 lds
v_mfma_f32_16x16x32_bf16 v[176:179], a[24:27], v[32:35], 0
Rb 8,10,18432
v_exp_f32_e32 v29, v99
v_mfma_f32_16x16x32_bf16 v[184:187], a[24:27], v[36:39], 0
v_exp_f32_e32 v50, v104
v_mfma_f32_16x16x32_bf16 v[188:191], a[28:31], v[36:39], 0
Rb 12,10,19456
s_waitcnt lgkmcnt(8)
v_mfma_f32_16x16x32_bf16 v[180:183], a[28:31], v[32:35], 0
v_exp_f32_e32 v24, v105
v_mfma_f32_16x16x32_bf16 v[192:195], a[32:35], v[32:35], 0
Rb 16,10,20480
v_add_f32_e32 v14, v14, v49
v_mfma_f32_16x16x32_bf16 v[200:203], a[32:35], v[36:39], 0
v_exp_f32_e32 v51, v106
v_add_f32_e32 v14, v14, v29
v_mfma_f32_16x16x32_bf16 v[204:207], a[36:39], v[36:39], 0
Rb 20,10,21504
Cv 49,49,29
v_mfma_f32_16x16x32_bf16 v[196:199], a[36:39], v[32:35], 0
)" R"(
buffer_load_dwordx4 v2, s[20:23], s55 offen offset:2048 sc1 lds
v_mfma_f32_16x16x32_bf16 v[208:211], a[40:43], v[32:35], 0
Rb 24,10,22528
v_add_f32_e32 v14, v14, v50
v_mfma_f32_16x16x32_bf16 v[216:219], a[40:43], v[36:39], 0
v_exp_f32_e32 v25, v107
v_mfma_f32_16x16x32_bf16 v[220:223], a[44:47], v[36:39], 0
Rb 28,10,23552
s_waitcnt lgkmcnt(8)
v_mfma_f32_16x16x32_bf16 v[212:215], a[44:47], v[32:35], 0
v_exp_f32_e32 v52, v100
v_add_f32_e32 v14, v14, v24
v_mfma_f32_16x16x32_bf16 v[224:227], a[48:51], v[32:35], 0
Rb 32,10,24576
Cv 50,50,24
v_mfma_f32_16x16x32_bf16 v[232:235], a[48:51], v[36:39], 0
v_exp_f32_e32 v26, v101
v_mfma_f32_16x16x32_bf16 v[236:239], a[52:55], v[36:39], 0
Rb 36,10,25600
v_add_f32_e32 v14, v14, v51
v_mfma_f32_16x16x32_bf16 v[228:231], a[52:55], v[32:35], 0
buffer_load_dwordx4 v2, s[20:23], s55 offen offset:3072 sc1 lds
v_mfma_f32_16x16x32_bf16 v[240:243], a[56:59], v[32:35], 0
Rb 40,10,26624
v_exp_f32_e32 v53, v102
v_mfma_f32_16x16x32_bf16 v[248:251], a[56:59], v[36:39], 0
v_exp_f32_e32 v27, v103
v_mfma_f32_16x16x32_bf16 v[252:255], a[60:63], v[36:39], 0
Rb 44,10,27648
s_waitcnt lgkmcnt(8)
v_mfma_f32_16x16x32_bf16 v[244:247], a[60:63], v[32:35], 0
v_exp_f32_e32 v54, v108
Mp 128,0,40
Rb 48,10,28672
v_add_f32_e32 v14, v14, v25
Mp 136,0,44
v_exp_f32_e32 v28, v109
Cv 51,51,25
Mp 140,4,44
Rb 52,10,29696
s_add_u32 m0, s36, 36864
Mp 132,4,40
buffer_load_dwordx4 v3, s[20:23], s55 offen offset:0 sc1 lds
Mp 144,8,40
Rb 56,10,30720
v_add_f32_e32 v15, v52, v26
Mp 152,8,44
v_exp_f32_e32 v55, v110
Mp 156,12,44
Rb 60,10,31744
s_waitcnt lgkmcnt(8)
Mp 148,12,40
v_exp_f32_e32 v29, v111
Cv 52,52,26
Mp 160,16,40
v_add_f32_e32 v15, v15, v53
v_add_f32_e32 v15, v15, v27
Mp 168,16,44
Cv 53,53,27
v_add_f32_e32 v15, v15, v54
Mp 172,20,44
v_add_f32_e32 v15, v15, v28
Cv 54,54,28
Mp 164,20,40
)" R"(
buffer_load_dwordx4 v3, s[20:23], s55 offen offset:1024 sc1 lds
Mp 176,24,40
v_add_f32_e32 v15, v15, v55
v_add_f32_e32 v15, v15, v29
Cv 55,55,29
Mp 184,24,44
s_sub_u32 s57, s57, 1
v_accvgpr_read_b32 v9, a192
Mp 188,28,44
v_accvgpr_read_b32 v11, a193
Cv 9,9,11
Mp 180,28,40
s_waitcnt vmcnt(6) lgkmcnt(0)
s_barrier
buffer_store_dword v6, v7, s[20:23], s49 offen offset:1024 sc0 sc1
Mp 192,32,40
Rb 0,1,0
v_accvgpr_write_b32 a144, v9
Mp 200,32,44
Rb 4,1,1024
v_accvgpr_read_b32 v9, a194
v_accvgpr_read_b32 v11, a195
Mp 204,36,44
Rb 8,1,2048
Cv 9,9,11
Mp 196,36,40
Rb 12,1,3072
buffer_load_dwordx4 v3, s[20:23], s55 offen offset:2048 sc1 lds
Mp 208,40,40
Rb 16,1,8192
v_accvgpr_write_b32 a145, v9
Mp 216,40,44
Rb 20,1,9216
v_accvgpr_read_b32 v9, a196
Mp 220,44,44
Rb 24,1,10240
v_accvgpr_read_b32 v11, a197
Mp 212,44,40
Rb 28,1,11264
Cv 9,9,11
v_accvgpr_write_b32 a146, v9
Mp 224,48,40
Rb 32,1,4096
v_accvgpr_read_b32 v9, a198
Mp 232,48,44
Rb 36,1,5120
v_accvgpr_read_b32 v11, a199
Mp 236,52,44
Rb 40,1,6144
Cv 9,9,11
Mp 228,52,40
Rb 44,1,7168
buffer_load_dwordx4 v3, s[20:23], s55 offen offset:3072 sc1 lds
Mp 240,56,40
v_cmp_eq_u32_e32 vcc, s3, v4
s_andn2_b64 s[28:29], -1, vcc
s_cbranch_scc1 .Lzsp18
.Lzok18:
Mp 248,56,44
v_accvgpr_write_b32 a147, v9
v_accvgpr_read_b32 v9, a200
Mp 252,60,44
v_accvgpr_read_b32 v11, a201
s_waitcnt lgkmcnt(8)
Mp 244,60,40
)" R"(
Cv 9,9,11
v_accvgpr_write_b32 a148, v9
v_mfma_f32_16x16x32_bf16 v[64:67], a[0:3], a[64:67], v[16:19]
Rb 48,1,12288
v_add_u32 v9, 4, v0
v_mfma_f32_16x16x32_bf16 v[68:71], a[0:3], a[68:71], v[20:23]
v_xor_b32 v9, v9, v0
v_and_b32 v9, 32, v9
Mv 68,4,76
Rb 52,1,13312
s_add_u32 m0, s36, 98304
Mv 64,4,72
buffer_load_dwordx4 v2, s[20:23], s56 offen offset:0 sc1 lds
Mv 64,8,80
Rb 56,1,14336
v_lshlrev_b32 v9, 22, v9
Mv 68,8,84
v_and_or_b32 v9, v0, 32, v9
v_mul_lo_u32 v9, v9, 12
Mv 68,12,92
Rb 60,1,15360
s_waitcnt lgkmcnt(8)
Mv 64,12,88
buffer_load_dwordx4 v2, s[20:23], s56 offen offset:1024 sc1 lds
Mv 64,16,96
Rb 0,1,16384
v_add_u32 v9, v9, v1
Mv 68,16,100
v_exp_f32_e32 v56, v112
Mv 68,20,108
Rb 4,1,17408
v_exp_f32_e32 v24, v113
Mv 64,20,104
buffer_load_dwordx4 v2, s[20:23], s56 offen offset:2048 sc1 lds
Mv 64,24,112
Rb 8,1,18432
v_exp_f32_e32 v57, v114
Mv 68,24,116
v_exp_f32_e32 v25, v115
Mv 68,28,124
Rb 12,1,19456
v_add_f32_e32 v14, v14, v56
s_waitcnt lgkmcnt(8)
Mv 64,28,120
buffer_load_dwordx4 v2, s[20:23], s56 offen offset:3072 sc1 lds
v_add_f32_e32 v14, v14, v24
v_mfma_f32_16x16x32_bf16 v[72:75], a[32:35], a[64:67], v[16:19]
Rb 16,1,24576
Cv 56,56,24
v_mfma_f32_16x16x32_bf16 v[76:79], a[32:35], a[68:71], v[20:23]
v_exp_f32_e32 v58, v120
v_add_f32_e32 v14, v14, v57
Mv 76,36,76
Rb 20,1,25600
s_add_u32 m0, s36, 102400
Mv 72,36,72
buffer_load_dwordx4 v3, s[20:23], s56 offen offset:0 sc1 lds
v_add_f32_e32 v14, v14, v25
Mv 72,40,80
Rb 24,1,26624
Cv 57,57,25
Mv 76,40,84
v_exp_f32_e32 v26, v121
v_add_f32_e32 v14, v14, v58
)" R"(
Mv 76,44,92
Rb 28,1,27648
s_waitcnt lgkmcnt(8)
Mv 72,44,88
buffer_load_dwordx4 v3, s[20:23], s56 offen offset:1024 sc1 lds
v_add_f32_e32 v14, v14, v26
Mv 72,48,96
Rb 32,1,20480
v_exp_f32_e32 v59, v122
Mv 76,48,100
v_exp_f32_e32 v27, v123
Mv 76,52,108
Rb 36,1,21504
v_exp_f32_e32 v60, v116
Mv 72,52,104
buffer_load_dwordx4 v3, s[20:23], s56 offen offset:2048 sc1 lds
Mv 72,56,112
Rb 40,1,22528
v_exp_f32_e32 v28, v117
Mv 76,56,116
v_exp_f32_e32 v61, v118
Mv 76,60,124
Rb 44,1,23552
Cv 58,58,26
s_waitcnt lgkmcnt(8)
Mv 72,60,120
buffer_load_dwordx4 v3, s[20:23], s56 offen offset:3072 sc1 lds
v_add_f32_e32 v14, v14, v59
v_mfma_f32_16x16x32_bf16 v[80:83], a[0:3], a[64:67], v[16:19]
Rb 48,1,28672
s_xor_b32 m0, s82, 0
v_mfma_f32_16x16x32_bf16 v[84:87], a[0:3], a[68:71], v[20:23]
s_add_u32 s56, s56, 0x10000
s_add_u32 s92, s83, 448
v_add_f32_e32 v14, v14, v27
Mv 84,4,76
Rb 52,1,29696
Cv 59,59,27
Mv 80,4,72
buffer_load_dwordx4 v9, s[24:27], s92 offen offset:0 lds
v_add_f32_e32 v15, v15, v60
Mv 80,8,80
Rb 56,1,30720
s_add_u32 s92, s83, 1216
Mv 84,8,84
v_exp_f32_e32 v29, v119
v_add_f32_e32 v15, v15, v28
Mv 84,12,92
Rb 60,1,31744
s_waitcnt lgkmcnt(8)
Mv 80,12,88
buffer_load_dwordx4 v9, s[24:27], s92 offen offset:1024 lds
s_add_u32 s92, s83, 1984
Mv 80,16,96
v_exp_f32_e32 v62, v124
Cv 60,60,28
Mv 84,16,100
v_exp_f32_e32 v24, v125
Mv 84,20,108
v_exp_f32_e32 v63, v126
v_add_f32_e32 v15, v15, v61
Mv 80,20,104
buffer_load_dwordx4 v9, s[24:27], s92 offen offset:2048 lds
Mv 80,24,112
)" R"(
s_add_u32 s92, s83, 2752
v_exp_f32_e32 v25, v127
Mv 84,24,116
v_add_f32_e32 v15, v15, v29
Cv 61,61,29
Mv 84,28,124
buffer_load_dwordx4 v9, s[24:27], s92 offen offset:3072 lds
v_add_f32_e32 v15, v15, v62
Mv 80,28,120
s_xor_b32 m0, s82, 16384
s_waitcnt lgkmcnt(0)
s_barrier
v_mfma_f32_16x16x32_bf16 v[88:91], a[32:35], a[64:67], v[16:19]
Rb 0,8,0
s_add_u32 s92, s83, 0
v_mfma_f32_16x16x32_bf16 v[92:95], a[32:35], a[68:71], v[20:23]
Rb 4,8,1024
v_add_f32_e32 v15, v15, v24
Cv 62,62,24
Mv 92,36,76
Rb 8,8,2048
v_add_f32_e32 v15, v15, v63
Mv 88,36,72
Rb 12,8,3072
v_add_f32_e32 v15, v15, v25
Cv 63,63,25
Mv 88,40,80
Rb 16,8,4096
buffer_load_dwordx4 v9, s[24:27], s92 offen offset:0 lds
Mv 92,40,84
Rb 20,8,5120
s_add_u32 s92, s83, 768
v_max_f32_e32 v11, v14, v15
Mv 92,44,92
Rb 24,8,6144
v_cmp_nge_f32_e32 vcc, s62, v11
Mv 88,44,88
Rb 28,8,7168
s_add_u32 s55, s55, 0x10000
Mv 88,48,96
Rb 32,8,8192
s_cmp_eq_u32 s57, 2
s_cselect_b32 s55, s59, s55
Mv 92,48,100
Rb 36,8,9216
buffer_load_dwordx4 v9, s[24:27], s92 offen offset:1024 lds
Mv 92,52,108
Rb 40,8,10240
s_add_u32 s92, s83, 1536
Mv 88,52,104
Rb 44,8,11264
Mv 88,56,112
buffer_load_dwordx4 v9, s[24:27], s92 offen offset:2048 lds
s_add_u32 s92, s83, 2304
Mv 92,56,116
buffer_load_dwordx4 v9, s[24:27], s92 offen offset:3072 lds
Mv 92,60,124
s_waitcnt lgkmcnt(8)
s_cbranch_vccnz .Lzfx7
.Lzfr7:
Mv 88,60,120
v_add_f32_e32 v12, v12, v14
v_add_f32_e32 v13, v13, v15
Mp 128,0,48
Rb 48,8,12288
)" R"(
Mp 136,0,52
s_cmp_le_i32 s57, s58
s_cbranch_scc1 .Lzmk6
.Lzmr6:
Mp 140,4,52
Rb 52,8,13312
s_add_u32 m0, s36, 0
Mp 132,4,48
buffer_load_dwordx4 v2, s[20:23], s55 offen offset:0 sc1 lds
Mp 144,8,48
Rb 56,8,14336
v_exp_f32_e32 v32, v64
Mp 152,8,52
buffer_store_dwordx4 a[224:227], v2, s[20:23], s47 offen offset:0 sc0 sc1
Mp 156,12,52
Rb 60,8,15360
s_waitcnt lgkmcnt(8)
Mp 148,12,48
v_exp_f32_e32 v26, v65
Mp 160,16,48
Rb 0,8,16384
v_add_f32_e32 v14, v32, v26
Mp 168,16,52
v_exp_f32_e32 v33, v66
Cv 32,32,26
Mp 172,20,52
Rb 4,8,17408
v_add_f32_e32 v14, v14, v33
Mp 164,20,48
buffer_load_dwordx4 v2, s[20:23], s55 offen offset:1024 sc1 lds
Mp 176,24,48
Rb 8,8,18432
v_exp_f32_e32 v27, v67
Mp 184,24,52
v_exp_f32_e32 v34, v72
Mp 188,28,52
Rb 12,8,19456
s_waitcnt lgkmcnt(8)
Mp 180,28,48
v_exp_f32_e32 v28, v73
v_add_f32_e32 v14, v14, v27
Mp 192,32,48
Rb 16,8,20480
Cv 33,33,27
Mp 200,32,52
v_exp_f32_e32 v35, v74
v_add_f32_e32 v14, v14, v34
Mp 204,36,52
Rb 20,8,21504
v_add_f32_e32 v14, v14, v28
Mp 196,36,48
buffer_load_dwordx4 v2, s[20:23], s55 offen offset:2048 sc1 lds
Mp 208,40,48
Rb 24,8,22528
v_exp_f32_e32 v29, v75
Mp 216,40,52
buffer_store_dwordx4 a[228:231], v2, s[20:23], s47 offen offset:1024 sc0 sc1
Mp 220,44,52
Rb 28,8,23552
s_waitcnt lgkmcnt(8)
Mp 212,44,48
v_exp_f32_e32 v36, v68
Cv 34,34,28
Mp 224,48,48
)" R"(
Rb 32,8,24576
v_add_f32_e32 v14, v14, v35
Mp 232,48,52
v_exp_f32_e32 v24, v69
v_add_f32_e32 v14, v14, v29
Mp 236,52,52
Rb 36,8,25600
Cv 35,35,29
Mp 228,52,48
buffer_load_dwordx4 v2, s[20:23], s55 offen offset:3072 sc1 lds
Mp 240,56,48
Rb 40,8,26624
v_exp_f32_e32 v37, v70
Mp 248,56,52
v_exp_f32_e32 v25, v71
Mp 252,60,52
Rb 44,8,27648
s_waitcnt lgkmcnt(8)
Mp 244,60,48
v_exp_f32_e32 v38, v76
v_add_f32_e32 v15, v36, v24
Mp 128,0,56
Rb 48,8,28672
Cv 36,36,24
Mp 136,0,60
v_exp_f32_e32 v26, v77
v_add_f32_e32 v15, v15, v37
Mp 140,4,60
Rb 52,8,29696
s_add_u32 m0, s36, 4096
Mp 132,4,56
buffer_load_dwordx4 v3, s[20:23], s55 offen offset:0 sc1 lds
Mp 144,8,56
Rb 56,8,30720
v_exp_f32_e32 v39, v78
Mp 152,8,60
buffer_store_dwordx4 a[232:235], v2, s[20:23], s47 offen offset:2048 sc0 sc1
Mp 156,12,60
Rb 60,8,31744
s_waitcnt lgkmcnt(8)
Mp 148,12,56
v_exp_f32_e32 v27, v79
v_add_f32_e32 v15, v15, v25
Mp 160,16,56
Cv 37,37,25
v_add_f32_e32 v15, v15, v38
Mp 168,16,60
v_add_f32_e32 v15, v15, v26
Cv 38,38,26
v_add_f32_e32 v15, v15, v39
Mp 172,20,60
v_add_f32_e32 v15, v15, v27
Cv 39,39,27
Mp 164,20,56
buffer_load_dwordx4 v3, s[20:23], s55 offen offset:1024 sc1 lds
Mp 176,24,56
s_add_u32 s50, s50, 1024
s_min_u32 s50, s50, s60
s_sub_u32 s57, s57, 1
Mp 184,24,60
v_accvgpr_read_b32 v9, a202
v_accvgpr_read_b32 v11, a203
Mp 188,28,60
Cv 9,9,11
)" R"(
v_accvgpr_write_b32 a149, v9
Mp 180,28,56
v_accvgpr_read_b32 v9, a204
s_waitcnt vmcnt(25) lgkmcnt(0)
s_barrier
Mp 192,32,56
Rb 0,1,32768
v_accvgpr_read_b32 v11, a205
Mp 200,32,60
Rb 4,1,33792
Cv 9,9,11
v_accvgpr_write_b32 a150, v9
Mp 204,36,60
Rb 8,1,34816
v_accvgpr_read_b32 v9, a206
Mp 196,36,56
Rb 12,1,35840
buffer_load_dwordx4 v3, s[20:23], s55 offen offset:2048 sc1 lds
Mp 208,40,56
Rb 16,1,40960
v_accvgpr_read_b32 v11, a207
Cv 9,9,11
Mp 216,40,60
Rb 20,1,41984
buffer_store_dwordx4 a[236:239], v2, s[20:23], s47 offen offset:3072 sc0 sc1
Mp 220,44,60
Rb 24,1,43008
v_accvgpr_write_b32 a151, v9
Mp 212,44,56
Rb 28,1,44032
v_accvgpr_read_b32 v9, a208
v_accvgpr_read_b32 v11, a209
Mp 224,48,56
Rb 32,1,36864
Cv 9,9,11
Mp 232,48,60
Rb 36,1,37888
v_accvgpr_write_b32 a152, v9
v_accvgpr_read_b32 v9, a210
Mp 236,52,60
Rb 40,1,38912
v_accvgpr_read_b32 v11, a211
Mp 228,52,56
Rb 44,1,39936
buffer_load_dwordx4 v3, s[20:23], s55 offen offset:3072 sc1 lds
Mp 240,56,56
Cv 9,9,11
v_accvgpr_write_b32 a153, v9
v_accvgpr_read_b32 v9, a212
Mp 248,56,60
v_accvgpr_read_b32 v11, a213
Cv 9,9,11
Mp 252,60,60
v_accvgpr_write_b32 a154, v9
s_waitcnt lgkmcnt(8)
Mp 244,60,56
v_mfma_f32_16x16x32_bf16 v[96:99], a[0:3], a[64:67], v[16:19]
Rb 48,1,45056
buffer_load_dword v4, v5, s[20:23], s50 offen offset:0 sc0 sc1
v_mfma_f32_16x16x32_bf16 v[100:103], a[0:3], a[68:71], v[20:23]
v_exp_f32_e32 v40, v80
Mv 100,4,76
Rb 52,1,46080
s_add_u32 m0, s36, 65536
Mv 96,4,72
)" R"(
buffer_load_dwordx4 v2, s[20:23], s56 offen offset:0 sc1 lds
Mv 96,8,80
Rb 56,1,47104
v_add_f32_e32 v14, v14, v40
Mv 100,8,84
buffer_store_dwordx4 a[240:243], v3, s[20:23], s47 offen offset:0 sc0 sc1
Mv 100,12,92
Rb 60,1,48128
s_waitcnt lgkmcnt(8)
Mv 96,12,88
v_exp_f32_e32 v28, v81
Mv 96,16,96
Rb 0,1,49152
v_add_f32_e32 v14, v14, v28
Mv 100,16,100
v_exp_f32_e32 v41, v82
Mv 100,20,108
Rb 4,1,50176
Cv 40,40,28
Mv 96,20,104
buffer_load_dwordx4 v2, s[20:23], s56 offen offset:1024 sc1 lds
Mv 96,24,112
Rb 8,1,51200
v_add_f32_e32 v14, v14, v41
Mv 100,24,116
v_exp_f32_e32 v29, v83
Mv 100,28,124
Rb 12,1,52224
s_waitcnt lgkmcnt(8)
Mv 96,28,120
v_exp_f32_e32 v42, v88
v_add_f32_e32 v14, v14, v29
v_mfma_f32_16x16x32_bf16 v[104:107], a[32:35], a[64:67], v[16:19]
Rb 16,1,57344
Cv 41,41,29
v_mfma_f32_16x16x32_bf16 v[108:111], a[32:35], a[68:71], v[20:23]
v_exp_f32_e32 v24, v89
Mv 108,36,76
Rb 20,1,58368
v_add_f32_e32 v14, v14, v42
Mv 104,36,72
buffer_load_dwordx4 v2, s[20:23], s56 offen offset:2048 sc1 lds
Mv 104,40,80
Rb 24,1,59392
v_add_f32_e32 v14, v14, v24
Mv 108,40,84
buffer_store_dwordx4 a[244:247], v3, s[20:23], s47 offen offset:1024 sc0 sc1
Mv 108,44,92
Rb 28,1,60416
s_waitcnt lgkmcnt(8)
Mv 104,44,88
v_exp_f32_e32 v43, v90
Mv 104,48,96
Rb 32,1,53248
Cv 42,42,24
Mv 108,48,100
v_exp_f32_e32 v25, v91
Mv 108,52,108
Rb 36,1,54272
v_add_f32_e32 v14, v14, v43
Mv 104,52,104
buffer_load_dwordx4 v2, s[20:23], s56 offen offset:3072 sc1 lds
Mv 104,56,112
Rb 40,1,55296
)" R"(
v_add_f32_e32 v14, v14, v25
Mv 108,56,116
v_exp_f32_e32 v44, v84
Mv 108,60,124
Rb 44,1,56320
s_waitcnt lgkmcnt(8)
Mv 104,60,120
v_exp_f32_e32 v26, v85
Cv 43,43,25
v_mfma_f32_16x16x32_bf16 v[112:115], a[0:3], a[64:67], v[16:19]
Rb 48,1,61440
v_add_f32_e32 v15, v15, v44
v_mfma_f32_16x16x32_bf16 v[116:119], a[0:3], a[68:71], v[20:23]
v_exp_f32_e32 v45, v86
Mv 116,4,76
Rb 52,1,62464
s_add_u32 m0, s36, 69632
Mv 112,4,72
buffer_load_dwordx4 v3, s[20:23], s56 offen offset:0 sc1 lds
Mv 112,8,80
Rb 56,1,63488
v_add_f32_e32 v15, v15, v26
Mv 116,8,84
buffer_store_dwordx4 a[248:251], v3, s[20:23], s47 offen offset:2048 sc0 sc1
Mv 116,12,92
Rb 60,1,64512
s_waitcnt lgkmcnt(8)
Mv 112,12,88
v_exp_f32_e32 v27, v87
Mv 112,16,96
v_exp_f32_e32 v46, v92
Mv 116,16,100
v_exp_f32_e32 v28, v93
Mv 116,20,108
v_exp_f32_e32 v47, v94
Mv 112,20,104
buffer_load_dwordx4 v3, s[20:23], s56 offen offset:1024 sc1 lds
Mv 112,24,112
v_exp_f32_e32 v29, v95
Mv 116,24,116
Cv 44,44,26
v_add_f32_e32 v15, v15, v45
Mv 116,28,124
v_add_f32_e32 v15, v15, v27
Cv 45,45,27
Mv 112,28,120
v_add_f32_e32 v15, v15, v46
s_waitcnt vmcnt(30) lgkmcnt(0)
s_barrier
v_mfma_f32_16x16x32_bf16 v[120:123], a[32:35], a[64:67], v[16:19]
Rb 0,8,32768
v_add_f32_e32 v15, v15, v28
v_mfma_f32_16x16x32_bf16 v[124:127], a[32:35], a[68:71], v[20:23]
Rb 4,8,33792
Cv 46,46,28
Mv 124,36,76
Rb 8,8,34816
v_add_f32_e32 v15, v15, v47
Mv 120,36,72
Rb 12,8,35840
buffer_load_dwordx4 v3, s[20:23], s56 offen offset:2048 sc1 lds
Mv 120,40,80
Rb 16,8,36864
v_add_f32_e32 v15, v15, v29
Mv 124,40,84
)" R"(
Rb 20,8,37888
buffer_store_dwordx4 a[252:255], v3, s[20:23], s47 offen offset:3072 sc0 sc1
Mv 124,44,92
Rb 24,8,38912
Cv 47,47,29
Mv 120,44,88
Rb 28,8,39936
v_max_f32_e32 v11, v14, v15
Mv 120,48,96
Rb 32,8,40960
v_cmp_nge_f32_e32 vcc, s62, v11
Mv 124,48,100
Rb 36,8,41984
s_add_u32 s55, s55, 0x10000
Mv 124,52,108
Rb 40,8,43008
Mv 120,52,104
Rb 44,8,44032
buffer_load_dwordx4 v3, s[20:23], s56 offen offset:3072 sc1 lds
Mv 120,56,112
s_cmp_eq_u32 s57, 2
s_cselect_b32 s55, s59, s55
Mv 124,56,116
s_add_u32 s56, s56, 0x10000
Mv 124,60,124
s_waitcnt lgkmcnt(8)
s_cbranch_vccnz .Lzfx8
.Lzfr8:
Mv 120,60,120
v_add_f32_e32 v12, v12, v14
v_add_f32_e32 v13, v13, v15
Mp 128,0,32
Rb 48,8,45056
Mp 136,0,36
s_cmp_le_i32 s57, s58
s_cbranch_scc1 .Lzmk7
.Lzmr7:
Mp 140,4,36
Rb 52,8,46080
s_add_u32 m0, s36, 32768
Mp 132,4,32
buffer_load_dwordx4 v2, s[20:23], s55 offen offset:0 sc1 lds
Mp 144,8,32
Rb 56,8,47104
Mp 152,8,36
v_exp_f32_e32 v48, v96
Mp 156,12,36
Rb 60,8,48128
s_waitcnt lgkmcnt(8)
Mp 148,12,32
v_exp_f32_e32 v24, v97
Mp 160,16,32
Rb 0,8,49152
v_add_f32_e32 v14, v48, v24
Mp 168,16,36
v_exp_f32_e32 v49, v98
Mp 172,20,36
Rb 4,8,50176
Cv 48,48,24
Mp 164,20,32
buffer_load_dwordx4 v2, s[20:23], s55 offen offset:1024 sc1 lds
Mp 176,24,32
Rb 8,8,51200
v_exp_f32_e32 v25, v99
)" R"(
Mp 184,24,36
v_exp_f32_e32 v50, v104
Mp 188,28,36
Rb 12,8,52224
s_waitcnt lgkmcnt(8)
Mp 180,28,32
v_exp_f32_e32 v26, v105
Mp 192,32,32
Rb 16,8,53248
v_add_f32_e32 v14, v14, v49
Mp 200,32,36
v_exp_f32_e32 v51, v106
v_add_f32_e32 v14, v14, v25
Mp 204,36,36
Rb 20,8,54272
Cv 49,49,25
Mp 196,36,32
buffer_load_dwordx4 v2, s[20:23], s55 offen offset:2048 sc1 lds
Mp 208,40,32
Rb 24,8,55296
v_add_f32_e32 v14, v14, v50
Mp 216,40,36
v_exp_f32_e32 v27, v107
Mp 220,44,36
Rb 28,8,56320
s_waitcnt lgkmcnt(8)
Mp 212,44,32
v_exp_f32_e32 v52, v100
v_add_f32_e32 v14, v14, v26
Mp 224,48,32
Rb 32,8,57344
Cv 50,50,26
Mp 232,48,36
v_exp_f32_e32 v28, v101
Mp 236,52,36
Rb 36,8,58368
v_add_f32_e32 v14, v14, v51
Mp 228,52,32
buffer_load_dwordx4 v2, s[20:23], s55 offen offset:3072 sc1 lds
Mp 240,56,32
Rb 40,8,59392
v_exp_f32_e32 v53, v102
Mp 248,56,36
v_exp_f32_e32 v29, v103
Mp 252,60,36
Rb 44,8,60416
s_waitcnt lgkmcnt(8)
Mp 244,60,32
v_exp_f32_e32 v54, v108
Mp 128,0,40
Rb 48,8,61440
v_add_f32_e32 v14, v14, v27
Mp 136,0,44
v_exp_f32_e32 v24, v109
Cv 51,51,27
Mp 140,4,44
Rb 52,8,62464
s_add_u32 m0, s36, 36864
Mp 132,4,40
buffer_load_dwordx4 v3, s[20:23], s55 offen offset:0 sc1 lds
Mp 144,8,40
Rb 56,8,63488
v_add_f32_e32 v15, v52, v28
Mp 152,8,44
)" R"(
v_exp_f32_e32 v55, v110
Mp 156,12,44
Rb 60,8,64512
s_waitcnt lgkmcnt(8)
Mp 148,12,40
v_exp_f32_e32 v25, v111
Cv 52,52,28
Mp 160,16,40
v_add_f32_e32 v15, v15, v53
v_add_f32_e32 v15, v15, v29
Mp 168,16,44
Cv 53,53,29
v_add_f32_e32 v15, v15, v54
Mp 172,20,44
v_add_f32_e32 v15, v15, v24
Cv 54,54,24
Mp 164,20,40
buffer_load_dwordx4 v3, s[20:23], s55 offen offset:1024 sc1 lds
Mp 176,24,40
v_add_f32_e32 v15, v15, v55
v_add_f32_e32 v15, v15, v25
Cv 55,55,25
Mp 184,24,44
s_sub_u32 s57, s57, 1
v_accvgpr_read_b32 v9, a214
Mp 188,28,44
v_accvgpr_read_b32 v11, a215
Cv 9,9,11
Mp 180,28,40
s_waitcnt vmcnt(7) lgkmcnt(0)
s_barrier
buffer_store_dword v6, v7, s[20:23], s49 offen offset:2048 sc0 sc1
Mp 192,32,40
Rb 0,1,0
v_accvgpr_write_b32 a155, v9
Mp 200,32,44
Rb 4,1,1024
v_accvgpr_read_b32 v9, a216
v_accvgpr_read_b32 v11, a217
Mp 204,36,44
Rb 8,1,2048
Cv 9,9,11
Mp 196,36,40
Rb 12,1,3072
buffer_load_dwordx4 v3, s[20:23], s55 offen offset:2048 sc1 lds
Mp 208,40,40
Rb 16,1,8192
v_accvgpr_write_b32 a156, v9
Mp 216,40,44
Rb 20,1,9216
v_accvgpr_read_b32 v9, a218
Mp 220,44,44
Rb 24,1,10240
v_accvgpr_read_b32 v11, a219
Mp 212,44,40
Rb 28,1,11264
Cv 9,9,11
v_accvgpr_write_b32 a157, v9
Mp 224,48,40
Rb 32,1,4096
v_accvgpr_read_b32 v9, a220
Mp 232,48,44
Rb 36,1,5120
v_accvgpr_read_b32 v11, a221
Mp 236,52,44
)" R"(
Rb 40,1,6144
Cv 9,9,11
Mp 228,52,40
Rb 44,1,7168
buffer_load_dwordx4 v3, s[20:23], s55 offen offset:3072 sc1 lds
Mp 240,56,40
v_cmp_eq_u32_e32 vcc, s3, v4
s_andn2_b64 s[28:29], -1, vcc
s_cbranch_scc1 .Lzsp19
.Lzok19:
Mp 248,56,44
v_accvgpr_write_b32 a158, v9
v_accvgpr_read_b32 v9, a222
Mp 252,60,44
v_accvgpr_read_b32 v11, a223
s_waitcnt lgkmcnt(8)
Mp 244,60,40
Cv 9,9,11
v_accvgpr_write_b32 a159, v9
v_mfma_f32_16x16x32_bf16 v[64:67], a[0:3], a[64:67], v[16:19]
Rb 48,1,12288
v_lshrrev_b32 v9, 2, v0
v_mfma_f32_16x16x32_bf16 v[68:71], a[0:3], a[68:71], v[20:23]
v_mul_u32_u24 v9, 448, v9
s_lshr_b32 s92, s33, 1
Mv 68,4,76
Rb 52,1,13312
s_add_u32 m0, s36, 98304
Mv 64,4,72
buffer_load_dwordx4 v2, s[20:23], s56 offen offset:0 sc1 lds
Mv 64,8,80
Rb 56,1,14336
s_mul_i32 s92, s92, 48
Mv 68,8,84
buffer_store_dwordx4 a[128:131], v2, s[20:23], s48 offen offset:0 sc0 sc1
Mv 68,12,92
Rb 60,1,15360
s_waitcnt lgkmcnt(8)
Mv 64,12,88
s_lshl3_add_u32 s92, s33, s92
v_add3_u32 v9, v9, s92, v10
Mv 64,16,96
Rb 0,1,16384
Mv 68,16,100
v_exp_f32_e32 v56, v112
Mv 68,20,108
Rb 4,1,17408
Rt 160,9,0
Mv 64,20,104
buffer_load_dwordx4 v2, s[20:23], s56 offen offset:1024 sc1 lds
Mv 64,24,112
Rb 8,1,18432
v_exp_f32_e32 v26, v113
Mv 68,24,116
v_exp_f32_e32 v57, v114
Mv 68,28,124
Rb 12,1,19456
s_waitcnt lgkmcnt(9)
Mv 64,28,120
v_exp_f32_e32 v27, v115
v_add_f32_e32 v14, v14, v56
v_mfma_f32_16x16x32_bf16 v[72:75], a[32:35], a[64:67], v[16:19]
Rb 16,1,24576
v_add_f32_e32 v14, v14, v26
)" R"(
v_mfma_f32_16x16x32_bf16 v[76:79], a[32:35], a[68:71], v[20:23]
v_exp_f32_e32 v58, v120
Mv 76,36,76
Rb 20,1,25600
Cv 56,56,26
Mv 72,36,72
buffer_load_dwordx4 v2, s[20:23], s56 offen offset:2048 sc1 lds
v_add_f32_e32 v14, v14, v57
Mv 72,40,80
Rb 24,1,26624
Rt 162,9,8192
Mv 76,40,84
v_exp_f32_e32 v28, v121
Mv 76,44,92
Rb 28,1,27648
v_add_f32_e32 v14, v14, v27
s_waitcnt lgkmcnt(10)
Mv 72,44,88
v_exp_f32_e32 v59, v122
Mv 72,48,96
Rb 32,1,20480
Cv 57,57,27
Mv 76,48,100
v_exp_f32_e32 v29, v123
v_add_f32_e32 v14, v14, v58
Mv 76,52,108
Rb 36,1,21504
v_add_f32_e32 v14, v14, v28
Mv 72,52,104
buffer_load_dwordx4 v2, s[20:23], s56 offen offset:3072 sc1 lds
Mv 72,56,112
Rb 40,1,22528
v_exp_f32_e32 v60, v116
Mv 76,56,116
buffer_store_dwordx4 a[132:135], v2, s[20:23], s48 offen offset:1024 sc0 sc1
Mv 76,60,124
Rb 44,1,23552
Rt 164,9,128
s_waitcnt lgkmcnt(10)
Mv 72,60,120
v_exp_f32_e32 v24, v117
Cv 58,58,28
v_mfma_f32_16x16x32_bf16 v[80:83], a[0:3], a[64:67], v[16:19]
Rb 48,1,28672
v_add_f32_e32 v14, v14, v59
v_mfma_f32_16x16x32_bf16 v[84:87], a[0:3], a[68:71], v[20:23]
v_exp_f32_e32 v61, v118
Mv 84,4,76
Rb 52,1,29696
s_add_u32 m0, s36, 102400
Mv 80,4,72
buffer_load_dwordx4 v3, s[20:23], s56 offen offset:0 sc1 lds
v_add_f32_e32 v14, v14, v29
Mv 80,8,80
Rb 56,1,30720
Cv 59,59,29
Mv 84,8,84
v_exp_f32_e32 v25, v119
Mv 84,12,92
Rb 60,1,31744
v_add_f32_e32 v15, v15, v60
s_waitcnt lgkmcnt(9)
Mv 80,12,88
v_exp_f32_e32 v62, v124
)" R"(
Mv 80,16,96
Rt 166,9,8320
v_add_f32_e32 v15, v15, v24
Mv 84,16,100
buffer_store_dwordx4 a[160:163], v1, s[20:23], s4 offen sc0 sc1
Cv 60,60,24
Mv 84,20,108
v_exp_f32_e32 v26, v125
Mv 80,20,104
buffer_load_dwordx4 v3, s[20:23], s56 offen offset:1024 sc1 lds
Mv 80,24,112
v_exp_f32_e32 v63, v126
v_add_f32_e32 v15, v15, v61
Mv 84,24,116
v_exp_f32_e32 v27, v127
Mv 84,28,124
v_add_f32_e32 v15, v15, v25
Cv 61,61,25
Mv 80,28,120
v_add_f32_e32 v15, v15, v62
s_waitcnt vmcnt(18) lgkmcnt(0)
s_barrier
v_mfma_f32_16x16x32_bf16 v[88:91], a[32:35], a[64:67], v[16:19]
Rb 0,8,0
v_add_f32_e32 v15, v15, v26
v_mfma_f32_16x16x32_bf16 v[92:95], a[32:35], a[68:71], v[20:23]
Rb 4,8,1024
Cv 62,62,26
Mv 92,36,76
Rb 8,8,2048
Rt 168,9,256
Mv 88,36,72
Rb 12,8,3072
buffer_load_dwordx4 v3, s[20:23], s56 offen offset:2048 sc1 lds
Mv 88,40,80
Rb 16,8,4096
v_add_f32_e32 v15, v15, v63
Mv 92,40,84
Rb 20,8,5120
buffer_store_dwordx4 a[136:139], v2, s[20:23], s48 offen offset:2048 sc0 sc1
Mv 92,44,92
Rb 24,8,6144
v_add_f32_e32 v15, v15, v27
Cv 63,63,27
Mv 88,44,88
Rb 28,8,7168
v_max_f32_e32 v11, v14, v15
Mv 88,48,96
Rb 32,8,8192
v_cmp_nge_f32_e32 vcc, s62, v11
Mv 92,48,100
Rb 36,8,9216
s_add_u32 s55, s55, 0x10000
Mv 92,52,108
Rb 40,8,10240
Mv 88,52,104
Rb 44,8,11264
buffer_load_dwordx4 v3, s[20:23], s56 offen offset:3072 sc1 lds
Mv 88,56,112
Rt 170,9,8448
s_cmp_eq_u32 s57, 2
s_cselect_b32 s55, s59, s55
Mv 92,56,116
s_add_u32 s56, s56, 0x10000
buffer_store_dwordx4 a[164:167], v1, s[20:23], s5 offen sc0 sc1
)" R"(
Mv 92,60,124
s_waitcnt lgkmcnt(9)
s_cbranch_vccnz .Lzfx9
.Lzfr9:
Mv 88,60,120
v_add_f32_e32 v12, v12, v14
v_add_f32_e32 v13, v13, v15
Mp 128,0,48
Rb 48,8,12288
Mp 136,0,52
s_cmp_le_i32 s57, s58
s_cbranch_scc1 .Lzmk8
.Lzmr8:
Mp 140,4,52
Rb 52,8,13312
s_add_u32 m0, s36, 0
Mp 132,4,48
buffer_load_dwordx4 v2, s[20:23], s55 offen offset:0 sc1 lds
Mp 144,8,48
Rb 56,8,14336
Mp 152,8,52
v_exp_f32_e32 v32, v64
Mp 156,12,52
Rb 60,8,15360
Rt 172,9,384
s_waitcnt lgkmcnt(10)
Mp 148,12,48
v_exp_f32_e32 v28, v65
Mp 160,16,48
Rb 0,8,16384
v_add_f32_e32 v14, v32, v28
Mp 168,16,52
v_exp_f32_e32 v33, v66
Cv 32,32,28
Mp 172,20,52
Rb 4,8,17408
v_add_f32_e32 v14, v14, v33
Mp 164,20,48
buffer_load_dwordx4 v2, s[20:23], s55 offen offset:1024 sc1 lds
Mp 176,24,48
Rb 8,8,18432
Mp 184,24,52
buffer_store_dwordx4 a[140:143], v2, s[20:23], s48 offen offset:3072 sc0 sc1
Mp 188,28,52
Rb 12,8,19456
s_waitcnt lgkmcnt(10)
Mp 180,28,48
v_exp_f32_e32 v29, v67
Mp 192,32,48
Rb 16,8,20480
Rt 174,9,8576
Mp 200,32,52
v_exp_f32_e32 v34, v72
v_add_f32_e32 v14, v14, v29
Mp 204,36,52
Rb 20,8,21504
Cv 33,33,29
Mp 196,36,48
buffer_load_dwordx4 v2, s[20:23], s55 offen offset:2048 sc1 lds
Mp 208,40,48
Rb 24,8,22528
v_add_f32_e32 v14, v14, v34
Mp 216,40,52
v_exp_f32_e32 v24, v73
)" R"(
Mp 220,44,52
Rb 28,8,23552
s_waitcnt lgkmcnt(10)
Mp 212,44,48
buffer_store_dwordx4 a[168:171], v1, s[20:23], s6 offen sc0 sc1
Mp 224,48,48
Rb 32,8,24576
v_add_f32_e32 v14, v14, v24
Mp 232,48,52
v_exp_f32_e32 v35, v74
Cv 34,34,24
Mp 236,52,52
Rb 36,8,25600
Rt 176,9,16384
Mp 228,52,48
buffer_load_dwordx4 v2, s[20:23], s55 offen offset:3072 sc1 lds
Mp 240,56,48
Rb 40,8,26624
v_add_f32_e32 v14, v14, v35
Mp 248,56,52
v_exp_f32_e32 v25, v75
Mp 252,60,52
Rb 44,8,27648
s_waitcnt lgkmcnt(10)
Mp 244,60,48
v_exp_f32_e32 v36, v68
Mp 128,0,56
Rb 48,8,28672
v_add_f32_e32 v14, v14, v25
Mp 136,0,60
v_exp_f32_e32 v26, v69
Cv 35,35,25
Mp 140,4,60
Rb 52,8,29696
s_add_u32 m0, s36, 4096
Mp 132,4,56
buffer_load_dwordx4 v3, s[20:23], s55 offen offset:0 sc1 lds
Mp 144,8,56
Rb 56,8,30720
Rt 178,9,24576
Mp 152,8,60
buffer_store_dwordx4 a[144:147], v3, s[20:23], s48 offen offset:0 sc0 sc1
Mp 156,12,60
Rb 60,8,31744
s_waitcnt lgkmcnt(10)
Mp 148,12,56
v_exp_f32_e32 v37, v70
Mp 160,16,56
v_exp_f32_e32 v27, v71
Mp 168,16,60
v_exp_f32_e32 v38, v76
v_add_f32_e32 v15, v36, v26
Mp 172,20,60
v_exp_f32_e32 v28, v77
Mp 164,20,56
buffer_load_dwordx4 v3, s[20:23], s55 offen offset:1024 sc1 lds
Mp 176,24,56
s_add_u32 s50, s50, 1024
s_min_u32 s50, s50, s60
Mp 184,24,60
buffer_store_dwordx4 a[172:175], v1, s[20:23], s7 offen sc0 sc1
Mp 188,28,60
Rt 180,9,16512
Cv 36,36,26
)" R"(
Mp 180,28,56
s_waitcnt vmcnt(23) lgkmcnt(1)
s_barrier
Mp 192,32,56
Rb 0,1,32768
v_add_f32_e32 v15, v15, v37
Mp 200,32,60
Rb 4,1,33792
v_exp_f32_e32 v39, v78
Mp 204,36,60
Rb 8,1,34816
v_add_f32_e32 v15, v15, v27
Mp 196,36,56
Rb 12,1,35840
buffer_load_dwordx4 v3, s[20:23], s55 offen offset:2048 sc1 lds
Mp 208,40,56
Rb 16,1,40960
Cv 37,37,27
Mp 216,40,60
Rb 20,1,41984
v_add_f32_e32 v15, v15, v38
Mp 220,44,60
Rb 24,1,43008
v_add_f32_e32 v15, v15, v28
Mp 212,44,56
Rb 28,1,44032
Cv 38,38,28
Mp 224,48,56
Rb 32,1,36864
Rt 182,9,24704
Mp 232,48,60
Rb 36,1,37888
v_exp_f32_e32 v29, v79
Mp 236,52,60
Rb 40,1,38912
v_add_f32_e32 v15, v15, v39
Mp 228,52,56
Rb 44,1,39936
buffer_load_dwordx4 v3, s[20:23], s55 offen offset:3072 sc1 lds
Mp 240,56,56
v_add_f32_e32 v15, v15, v29
Cv 39,39,29
Mp 248,56,60
buffer_store_dwordx4 a[148:151], v3, s[20:23], s48 offen offset:1024 sc0 sc1
Mp 252,60,60
s_sub_u32 s57, s57, 1
s_waitcnt lgkmcnt(9)
Mp 244,60,56
v_mfma_f32_16x16x32_bf16 v[96:99], a[0:3], a[64:67], v[16:19]
Rb 48,1,45056
buffer_load_dword v4, v5, s[20:23], s50 offen offset:0 sc0 sc1
v_mfma_f32_16x16x32_bf16 v[100:103], a[0:3], a[68:71], v[20:23]
v_exp_f32_e32 v40, v80
Mv 100,4,76
Rb 52,1,46080
s_add_u32 m0, s36, 65536
Rt 184,9,16640
Mv 96,4,72
buffer_load_dwordx4 v2, s[20:23], s56 offen offset:0 sc1 lds
Mv 96,8,80
Rb 56,1,47104
v_add_f32_e32 v14, v14, v40
Mv 100,8,84
v_exp_f32_e32 v24, v81
Mv 100,12,92
)" R"(
Rb 60,1,48128
s_waitcnt lgkmcnt(10)
Mv 96,12,88
buffer_store_dwordx4 a[176:179], v1, s[20:23], s8 offen sc0 sc1
v_add_f32_e32 v14, v14, v24
Mv 96,16,96
Rb 0,1,49152
Cv 40,40,24
Mv 100,16,100
v_exp_f32_e32 v41, v82
Mv 100,20,108
Rb 4,1,50176
v_add_f32_e32 v14, v14, v41
Mv 96,20,104
buffer_load_dwordx4 v2, s[20:23], s56 offen offset:1024 sc1 lds
Mv 96,24,112
Rb 8,1,51200
Rt 186,9,24832
Mv 100,24,116
v_exp_f32_e32 v25, v83
Mv 100,28,124
Rb 12,1,52224
s_waitcnt lgkmcnt(10)
Mv 96,28,120
v_exp_f32_e32 v42, v88
v_add_f32_e32 v14, v14, v25
v_mfma_f32_16x16x32_bf16 v[104:107], a[32:35], a[64:67], v[16:19]
Rb 16,1,57344
Cv 41,41,25
v_mfma_f32_16x16x32_bf16 v[108:111], a[32:35], a[68:71], v[20:23]
v_exp_f32_e32 v26, v89
Mv 108,36,76
Rb 20,1,58368
v_add_f32_e32 v14, v14, v42
Mv 104,36,72
buffer_load_dwordx4 v2, s[20:23], s56 offen offset:2048 sc1 lds
Mv 104,40,80
Rb 24,1,59392
v_add_f32_e32 v14, v14, v26
Mv 108,40,84
buffer_store_dwordx4 a[152:155], v3, s[20:23], s48 offen offset:2048 sc0 sc1
Mv 108,44,92
Rb 28,1,60416
Rt 188,9,16768
s_waitcnt lgkmcnt(10)
Mv 104,44,88
v_exp_f32_e32 v43, v90
Cv 42,42,26
Mv 104,48,96
Rb 32,1,53248
v_add_f32_e32 v14, v14, v43
Mv 108,48,100
v_exp_f32_e32 v27, v91
Mv 108,52,108
Rb 36,1,54272
v_add_f32_e32 v14, v14, v27
Mv 104,52,104
buffer_load_dwordx4 v2, s[20:23], s56 offen offset:3072 sc1 lds
Mv 104,56,112
Rb 40,1,55296
Cv 43,43,27
Mv 108,56,116
buffer_store_dwordx4 a[180:183], v1, s[20:23], s9 offen sc0 sc1
Mv 108,60,124
)" R"(
Rb 44,1,56320
s_waitcnt lgkmcnt(9)
Mv 104,60,120
v_exp_f32_e32 v44, v84
s_add_u32 s55, s55, 0x10000
v_mfma_f32_16x16x32_bf16 v[112:115], a[0:3], a[64:67], v[16:19]
Rb 48,1,61440
Rt 190,9,24960
v_mfma_f32_16x16x32_bf16 v[116:119], a[0:3], a[68:71], v[20:23]
v_exp_f32_e32 v28, v85
Mv 116,4,76
Rb 52,1,62464
s_add_u32 m0, s36, 69632
Mv 112,4,72
buffer_load_dwordx4 v3, s[20:23], s56 offen offset:0 sc1 lds
Mv 112,8,80
Rb 56,1,63488
v_add_f32_e32 v15, v15, v44
Mv 116,8,84
v_exp_f32_e32 v45, v86
Mv 116,12,92
Rb 60,1,64512
s_waitcnt lgkmcnt(10)
Mv 112,12,88
v_exp_f32_e32 v29, v87
v_add_f32_e32 v15, v15, v28
Mv 112,16,96
v_exp_f32_e32 v46, v92
Mv 116,16,100
v_exp_f32_e32 v24, v93
Mv 116,20,108
v_exp_f32_e32 v47, v94
Mv 112,20,104
buffer_load_dwordx4 v3, s[20:23], s56 offen offset:1024 sc1 lds
Mv 112,24,112
v_exp_f32_e32 v25, v95
Mv 116,24,116
buffer_store_dwordx4 a[156:159], v3, s[20:23], s48 offen offset:3072 sc0 sc1
Mv 116,28,124
s_cmp_eq_u32 s57, 2
s_cselect_b32 s55, s59, s55
Mv 112,28,120
Cv 44,44,28
s_waitcnt vmcnt(25) lgkmcnt(0)
s_barrier
v_mfma_f32_16x16x32_bf16 v[120:123], a[32:35], a[64:67], v[16:19]
Rb 0,8,32768
v_add_f32_e32 v15, v15, v45
v_mfma_f32_16x16x32_bf16 v[124:127], a[32:35], a[68:71], v[20:23]
Rb 4,8,33792
v_add_f32_e32 v15, v15, v29
Mv 124,36,76
Rb 8,8,34816
Cv 45,45,29
Mv 120,36,72
Rb 12,8,35840
buffer_load_dwordx4 v3, s[20:23], s56 offen offset:2048 sc1 lds
Mv 120,40,80
Rb 16,8,36864
v_add_f32_e32 v15, v15, v46
Mv 124,40,84
Rb 20,8,37888
v_add_f32_e32 v15, v15, v24
Mv 124,44,92
Rb 24,8,38912
)" R"(
Cv 46,46,24
Mv 120,44,88
Rb 28,8,39936
buffer_store_dwordx4 a[184:187], v1, s[20:23], s10 offen sc0 sc1
Mv 120,48,96
Rb 32,8,40960
v_add_f32_e32 v15, v15, v47
Mv 124,48,100
Rb 36,8,41984
v_add_f32_e32 v15, v15, v25
Mv 124,52,108
Rb 40,8,43008
Cv 47,47,25
Mv 120,52,104
Rb 44,8,44032
buffer_load_dwordx4 v3, s[20:23], s56 offen offset:3072 sc1 lds
Mv 120,56,112
v_max_f32_e32 v11, v14, v15
v_cmp_nge_f32_e32 vcc, s62, v11
Mv 124,56,116
s_add_u32 s56, s56, 0x10000
Mv 124,60,124
s_waitcnt lgkmcnt(8)
s_cbranch_vccnz .Lzfx10
.Lzfr10:
Mv 120,60,120
v_add_f32_e32 v12, v12, v14
v_add_f32_e32 v13, v13, v15
Mp 128,0,32
Rb 48,8,45056
Mp 136,0,36
s_cmp_le_i32 s57, s58
s_cbranch_scc1 .Lzmk9
.Lzmr9:
Mp 140,4,36
Rb 52,8,46080
s_add_u32 m0, s36, 32768
Mp 132,4,32
buffer_load_dwordx4 v2, s[20:23], s55 offen offset:0 sc1 lds
Mp 144,8,32
Rb 56,8,47104
Mp 152,8,36
buffer_store_dwordx4 a[188:191], v1, s[20:23], s11 offen sc0 sc1
Mp 156,12,36
Rb 60,8,48128
s_waitcnt lgkmcnt(8)
Mp 148,12,32
v_exp_f32_e32 v48, v96
Mp 160,16,32
Rb 0,8,49152
Mp 168,16,36
v_exp_f32_e32 v26, v97
Mp 172,20,36
Rb 4,8,50176
v_add_f32_e32 v14, v48, v26
Mp 164,20,32
buffer_load_dwordx4 v2, s[20:23], s55 offen offset:1024 sc1 lds
Mp 176,24,32
Rb 8,8,51200
Cv 48,48,26
Mp 184,24,36
v_exp_f32_e32 v49, v98
Mp 188,28,36
Rb 12,8,52224
)" R"(
s_waitcnt lgkmcnt(8)
Mp 180,28,32
v_exp_f32_e32 v27, v99
Mp 192,32,32
Rb 16,8,53248
v_add_f32_e32 v14, v14, v49
Mp 200,32,36
v_exp_f32_e32 v50, v104
Mp 204,36,36
Rb 20,8,54272
v_add_f32_e32 v14, v14, v27
Mp 196,36,32
buffer_load_dwordx4 v2, s[20:23], s55 offen offset:2048 sc1 lds
Mp 208,40,32
Rb 24,8,55296
Cv 49,49,27
Mp 216,40,36
v_exp_f32_e32 v28, v105
Mp 220,44,36
Rb 28,8,56320
s_waitcnt lgkmcnt(8)
Mp 212,44,32
v_exp_f32_e32 v51, v106
Mp 224,48,32
Rb 32,8,57344
v_add_f32_e32 v14, v14, v50
Mp 232,48,36
v_exp_f32_e32 v29, v107
Mp 236,52,36
Rb 36,8,58368
v_add_f32_e32 v14, v14, v28
Mp 228,52,32
buffer_load_dwordx4 v2, s[20:23], s55 offen offset:3072 sc1 lds
Mp 240,56,32
Rb 40,8,59392
Cv 50,50,28
Mp 248,56,36
v_exp_f32_e32 v52, v100
Mp 252,60,36
Rb 44,8,60416
s_waitcnt lgkmcnt(8)
Mp 244,60,32
v_exp_f32_e32 v24, v101
Mp 128,0,40
Rb 48,8,61440
v_add_f32_e32 v14, v14, v51
Mp 136,0,44
v_exp_f32_e32 v53, v102
Mp 140,4,44
Rb 52,8,62464
s_add_u32 m0, s36, 36864
Mp 132,4,40
buffer_load_dwordx4 v3, s[20:23], s55 offen offset:0 sc1 lds
Mp 144,8,40
Rb 56,8,63488
v_add_f32_e32 v14, v14, v29
Mp 152,8,44
v_exp_f32_e32 v25, v103
Mp 156,12,44
Rb 60,8,64512
s_waitcnt lgkmcnt(8)
Mp 148,12,40
v_exp_f32_e32 v54, v108
Mp 160,16,40
)" R"(
v_exp_f32_e32 v26, v109
Mp 168,16,44
v_exp_f32_e32 v55, v110
Mp 172,20,44
v_exp_f32_e32 v27, v111
Mp 164,20,40
buffer_load_dwordx4 v3, s[20:23], s55 offen offset:1024 sc1 lds
Mp 176,24,40
Cv 51,51,29
v_add_f32_e32 v15, v52, v24
Mp 184,24,44
Cv 52,52,24
v_add_f32_e32 v15, v15, v53
Mp 188,28,44
v_add_f32_e32 v15, v15, v25
Cv 53,53,25
Mp 180,28,40
s_waitcnt vmcnt(5) lgkmcnt(0)
s_barrier
buffer_store_dword v6, v7, s[20:23], s49 offen offset:3072 sc0 sc1
Mp 192,32,40
Rb 0,1,0
v_add_f32_e32 v15, v15, v54
Mp 200,32,44
Rb 4,1,1024
v_add_f32_e32 v15, v15, v26
Mp 204,36,44
Rb 8,1,2048
Cv 54,54,26
Mp 196,36,40
Rb 12,1,3072
buffer_load_dwordx4 v3, s[20:23], s55 offen offset:2048 sc1 lds
Mp 208,40,40
Rb 16,1,8192
v_add_f32_e32 v15, v15, v55
Mp 216,40,44
Rb 20,1,9216
v_add_f32_e32 v15, v15, v27
Mp 220,44,44
Rb 24,1,10240
Cv 55,55,27
Mp 212,44,40
Rb 28,1,11264
s_sub_u32 s57, s57, 1
Mp 224,48,40
Rb 32,1,4096
Mp 232,48,44
Rb 36,1,5120
Mp 236,52,44
Rb 40,1,6144
Mp 228,52,40
Rb 44,1,7168
buffer_load_dwordx4 v3, s[20:23], s55 offen offset:3072 sc1 lds
Mp 240,56,40
v_cmp_eq_u32_e32 vcc, s3, v4
s_andn2_b64 s[28:29], -1, vcc
s_cbranch_scc1 .Lzsp20
.Lzok20:
Mp 248,56,44
Mp 252,60,44
s_waitcnt lgkmcnt(8)
Mp 244,60,40
.LzL:
v_mfma_f32_16x16x32_bf16 v[64:67], a[0:3], a[64:67], v[16:19]
Rb 48,1,12288
)" R"(
v_mfma_f32_16x16x32_bf16 v[68:71], a[0:3], a[68:71], v[20:23]
v_exp_f32_e32 v56, v112
Mv 68,4,76
Rb 52,1,13312
s_add_u32 m0, s36, 98304
Mv 64,4,72
buffer_load_dwordx4 v2, s[20:23], s56 offen offset:0 sc1 lds
Mv 64,8,80
Rb 56,1,14336
v_add_f32_e32 v14, v14, v56
Mv 68,8,84
v_exp_f32_e32 v24, v113
Mv 68,12,92
Rb 60,1,15360
s_waitcnt lgkmcnt(8)
Mv 64,12,88
v_exp_f32_e32 v57, v114
Mv 64,16,96
Rb 0,1,16384
v_add_f32_e32 v14, v14, v24
Mv 68,16,100
v_exp_f32_e32 v25, v115
Mv 68,20,108
Rb 4,1,17408
Cv 56,56,24
Mv 64,20,104
buffer_load_dwordx4 v2, s[20:23], s56 offen offset:1024 sc1 lds
Mv 64,24,112
Rb 8,1,18432
v_add_f32_e32 v14, v14, v57
Mv 68,24,116
v_exp_f32_e32 v58, v120
Mv 68,28,124
Rb 12,1,19456
s_waitcnt lgkmcnt(8)
Mv 64,28,120
v_exp_f32_e32 v26, v121
v_mfma_f32_16x16x32_bf16 v[72:75], a[32:35], a[64:67], v[16:19]
Rb 16,1,24576
v_add_f32_e32 v14, v14, v25
v_mfma_f32_16x16x32_bf16 v[76:79], a[32:35], a[68:71], v[20:23]
v_exp_f32_e32 v59, v122
Mv 76,36,76
Rb 20,1,25600
Cv 57,57,25
Mv 72,36,72
buffer_load_dwordx4 v2, s[20:23], s56 offen offset:2048 sc1 lds
Mv 72,40,80
Rb 24,1,26624
v_add_f32_e32 v14, v14, v58
Mv 76,40,84
v_exp_f32_e32 v27, v123
Mv 76,44,92
Rb 28,1,27648
s_waitcnt lgkmcnt(8)
Mv 72,44,88
v_exp_f32_e32 v60, v116
Mv 72,48,96
Rb 32,1,20480
v_add_f32_e32 v14, v14, v26
Mv 76,48,100
v_exp_f32_e32 v28, v117
Mv 76,52,108
Rb 36,1,21504
)" R"(
Cv 58,58,26
Mv 72,52,104
buffer_load_dwordx4 v2, s[20:23], s56 offen offset:3072 sc1 lds
Mv 72,56,112
Rb 40,1,22528
v_add_f32_e32 v14, v14, v59
Mv 76,56,116
v_exp_f32_e32 v61, v118
Mv 76,60,124
Rb 44,1,23552
s_waitcnt lgkmcnt(8)
Mv 72,60,120
v_exp_f32_e32 v29, v119
v_mfma_f32_16x16x32_bf16 v[80:83], a[0:3], a[64:67], v[16:19]
Rb 48,1,28672
v_add_f32_e32 v14, v14, v27
v_mfma_f32_16x16x32_bf16 v[84:87], a[0:3], a[68:71], v[20:23]
v_exp_f32_e32 v62, v124
Mv 84,4,76
Rb 52,1,29696
s_add_u32 m0, s36, 102400
Mv 80,4,72
buffer_load_dwordx4 v3, s[20:23], s56 offen offset:0 sc1 lds
Mv 80,8,80
Rb 56,1,30720
Cv 59,59,27
Mv 84,8,84
v_exp_f32_e32 v24, v125
Mv 84,12,92
Rb 60,1,31744
s_waitcnt lgkmcnt(8)
Mv 80,12,88
v_exp_f32_e32 v63, v126
Mv 80,16,96
v_exp_f32_e32 v25, v127
Mv 84,16,100
v_add_f32_e32 v15, v15, v60
v_add_f32_e32 v15, v15, v28
Mv 84,20,108
Cv 60,60,28
v_add_f32_e32 v15, v15, v61
Mv 80,20,104
buffer_load_dwordx4 v3, s[20:23], s56 offen offset:1024 sc1 lds
Mv 80,24,112
v_add_f32_e32 v15, v15, v29
Cv 61,61,29
Mv 84,24,116
v_add_f32_e32 v15, v15, v62
v_add_f32_e32 v15, v15, v24
Mv 84,28,124
Cv 62,62,24
v_add_f32_e32 v15, v15, v63
Mv 80,28,120
s_waitcnt vmcnt(14) lgkmcnt(0)
s_barrier
v_mfma_f32_16x16x32_bf16 v[88:91], a[32:35], a[64:67], v[16:19]
Rb 0,8,0
v_add_f32_e32 v15, v15, v25
v_mfma_f32_16x16x32_bf16 v[92:95], a[32:35], a[68:71], v[20:23]
Rb 4,8,1024
Cv 63,63,25
Mv 92,36,76
Rb 8,8,2048
v_max_f32_e32 v11, v14, v15
Mv 88,36,72
)" R"(
Rb 12,8,3072
buffer_load_dwordx4 v3, s[20:23], s56 offen offset:2048 sc1 lds
Mv 88,40,80
Rb 16,8,4096
v_cmp_nge_f32_e32 vcc, s62, v11
Mv 92,40,84
Rb 20,8,5120
s_add_u32 s55, s55, 0x10000
Mv 92,44,92
Rb 24,8,6144
Mv 88,44,88
Rb 28,8,7168
Mv 88,48,96
Rb 32,8,8192
Mv 92,48,100
Rb 36,8,9216
Mv 92,52,108
Rb 40,8,10240
Mv 88,52,104
Rb 44,8,11264
buffer_load_dwordx4 v3, s[20:23], s56 offen offset:3072 sc1 lds
Mv 88,56,112
s_cmp_eq_u32 s57, 2
s_cselect_b32 s55, s59, s55
Mv 92,56,116
s_add_u32 s56, s56, 0x10000
Mv 92,60,124
s_waitcnt lgkmcnt(8)
s_cbranch_vccnz .Lzfx1
.Lzfr1:
Mv 88,60,120
v_add_f32_e32 v12, v12, v14
v_add_f32_e32 v13, v13, v15
Mp 128,0,48
Rb 48,8,12288
Mp 136,0,52
s_cmp_le_i32 s57, s58
s_cbranch_scc1 .Lzmk2
.Lzmr2:
Mp 140,4,52
Rb 52,8,13312
s_add_u32 m0, s36, 0
Mp 132,4,48
buffer_load_dwordx4 v2, s[20:23], s55 offen offset:0 sc1 lds
Mp 144,8,48
Rb 56,8,14336
Mp 152,8,52
v_exp_f32_e32 v32, v64
Mp 156,12,52
Rb 60,8,15360
s_waitcnt lgkmcnt(8)
Mp 148,12,48
v_exp_f32_e32 v26, v65
Mp 160,16,48
Rb 0,8,16384
v_add_f32_e32 v14, v32, v26
Mp 168,16,52
v_exp_f32_e32 v33, v66
Mp 172,20,52
Rb 4,8,17408
Cv 32,32,26
Mp 164,20,48
buffer_load_dwordx4 v2, s[20:23], s55 offen offset:1024 sc1 lds
Mp 176,24,48
)" R"(
Rb 8,8,18432
v_add_f32_e32 v14, v14, v33
Mp 184,24,52
v_exp_f32_e32 v27, v67
Mp 188,28,52
Rb 12,8,19456
s_waitcnt lgkmcnt(8)
Mp 180,28,48
v_exp_f32_e32 v34, v72
Mp 192,32,48
Rb 16,8,20480
v_add_f32_e32 v14, v14, v27
Mp 200,32,52
v_exp_f32_e32 v28, v73
Mp 204,36,52
Rb 20,8,21504
Cv 33,33,27
Mp 196,36,48
buffer_load_dwordx4 v2, s[20:23], s55 offen offset:2048 sc1 lds
Mp 208,40,48
Rb 24,8,22528
v_add_f32_e32 v14, v14, v34
Mp 216,40,52
v_exp_f32_e32 v35, v74
Mp 220,44,52
Rb 28,8,23552
s_waitcnt lgkmcnt(8)
Mp 212,44,48
v_exp_f32_e32 v29, v75
Mp 224,48,48
Rb 32,8,24576
v_add_f32_e32 v14, v14, v28
Mp 232,48,52
v_exp_f32_e32 v36, v68
Mp 236,52,52
Rb 36,8,25600
Cv 34,34,28
Mp 228,52,48
buffer_load_dwordx4 v2, s[20:23], s55 offen offset:3072 sc1 lds
Mp 240,56,48
Rb 40,8,26624
v_add_f32_e32 v14, v14, v35
Mp 248,56,52
v_exp_f32_e32 v24, v69
Mp 252,60,52
Rb 44,8,27648
s_waitcnt lgkmcnt(8)
Mp 244,60,48
v_exp_f32_e32 v37, v70
Mp 128,0,56
Rb 48,8,28672
v_add_f32_e32 v14, v14, v29
Mp 136,0,60
v_exp_f32_e32 v25, v71
Mp 140,4,60
Rb 52,8,29696
s_add_u32 m0, s36, 4096
Mp 132,4,56
buffer_load_dwordx4 v3, s[20:23], s55 offen offset:0 sc1 lds
Mp 144,8,56
Rb 56,8,30720
Cv 35,35,29
Mp 152,8,60
v_exp_f32_e32 v38, v76
)" R"(
Mp 156,12,60
Rb 60,8,31744
s_waitcnt lgkmcnt(8)
Mp 148,12,56
v_exp_f32_e32 v26, v77
Mp 160,16,56
v_exp_f32_e32 v39, v78
Mp 168,16,60
v_exp_f32_e32 v27, v79
Mp 172,20,60
v_add_f32_e32 v15, v36, v24
Cv 36,36,24
Mp 164,20,56
buffer_load_dwordx4 v3, s[20:23], s55 offen offset:1024 sc1 lds
Mp 176,24,56
s_add_u32 s50, s50, 1024
s_min_u32 s50, s50, s60
Mp 184,24,60
v_add_f32_e32 v15, v15, v37
v_add_f32_e32 v15, v15, v25
Mp 188,28,60
Cv 37,37,25
v_add_f32_e32 v15, v15, v38
Mp 180,28,56
s_waitcnt vmcnt(14) lgkmcnt(0)
s_barrier
Mp 192,32,56
Rb 0,1,32768
v_add_f32_e32 v15, v15, v26
Mp 200,32,60
Rb 4,1,33792
Cv 38,38,26
Mp 204,36,60
Rb 8,1,34816
v_add_f32_e32 v15, v15, v39
Mp 196,36,56
Rb 12,1,35840
buffer_load_dwordx4 v3, s[20:23], s55 offen offset:2048 sc1 lds
Mp 208,40,56
Rb 16,1,40960
v_add_f32_e32 v15, v15, v27
Mp 216,40,60
Rb 20,1,41984
Cv 39,39,27
Mp 220,44,60
Rb 24,1,43008
s_sub_u32 s57, s57, 1
Mp 212,44,56
Rb 28,1,44032
Mp 224,48,56
Rb 32,1,36864
Mp 232,48,60
Rb 36,1,37888
Mp 236,52,60
Rb 40,1,38912
Mp 228,52,56
Rb 44,1,39936
buffer_load_dwordx4 v3, s[20:23], s55 offen offset:3072 sc1 lds
Mp 240,56,56
Mp 248,56,60
Mp 252,60,60
s_waitcnt lgkmcnt(8)
Mp 244,60,56
v_mfma_f32_16x16x32_bf16 v[96:99], a[0:3], a[64:67], v[16:19]
Rb 48,1,45056
)" R"(
buffer_load_dword v4, v5, s[20:23], s50 offen offset:0 sc0 sc1
v_mfma_f32_16x16x32_bf16 v[100:103], a[0:3], a[68:71], v[20:23]
v_exp_f32_e32 v40, v80
Mv 100,4,76
Rb 52,1,46080
s_add_u32 m0, s36, 65536
Mv 96,4,72
buffer_load_dwordx4 v2, s[20:23], s56 offen offset:0 sc1 lds
Mv 96,8,80
Rb 56,1,47104
v_add_f32_e32 v14, v14, v40
Mv 100,8,84
v_exp_f32_e32 v28, v81
Mv 100,12,92
Rb 60,1,48128
s_waitcnt lgkmcnt(8)
Mv 96,12,88
v_exp_f32_e32 v41, v82
Mv 96,16,96
Rb 0,1,49152
v_add_f32_e32 v14, v14, v28
Mv 100,16,100
v_exp_f32_e32 v29, v83
Mv 100,20,108
Rb 4,1,50176
Cv 40,40,28
Mv 96,20,104
buffer_load_dwordx4 v2, s[20:23], s56 offen offset:1024 sc1 lds
Mv 96,24,112
Rb 8,1,51200
v_add_f32_e32 v14, v14, v41
Mv 100,24,116
v_exp_f32_e32 v42, v88
Mv 100,28,124
Rb 12,1,52224
s_waitcnt lgkmcnt(8)
Mv 96,28,120
v_exp_f32_e32 v24, v89
v_mfma_f32_16x16x32_bf16 v[104:107], a[32:35], a[64:67], v[16:19]
Rb 16,1,57344
v_add_f32_e32 v14, v14, v29
v_mfma_f32_16x16x32_bf16 v[108:111], a[32:35], a[68:71], v[20:23]
v_exp_f32_e32 v43, v90
Mv 108,36,76
Rb 20,1,58368
Cv 41,41,29
Mv 104,36,72
buffer_load_dwordx4 v2, s[20:23], s56 offen offset:2048 sc1 lds
Mv 104,40,80
Rb 24,1,59392
v_add_f32_e32 v14, v14, v42
Mv 108,40,84
v_exp_f32_e32 v25, v91
Mv 108,44,92
Rb 28,1,60416
s_waitcnt lgkmcnt(8)
Mv 104,44,88
v_exp_f32_e32 v44, v84
Mv 104,48,96
Rb 32,1,53248
v_add_f32_e32 v14, v14, v24
Mv 108,48,100
v_exp_f32_e32 v26, v85
Mv 108,52,108
)" R"(
Rb 36,1,54272
Cv 42,42,24
Mv 104,52,104
buffer_load_dwordx4 v2, s[20:23], s56 offen offset:3072 sc1 lds
Mv 104,56,112
Rb 40,1,55296
v_add_f32_e32 v14, v14, v43
Mv 108,56,116
v_exp_f32_e32 v45, v86
Mv 108,60,124
Rb 44,1,56320
s_waitcnt lgkmcnt(8)
Mv 104,60,120
v_exp_f32_e32 v27, v87
v_mfma_f32_16x16x32_bf16 v[112:115], a[0:3], a[64:67], v[16:19]
Rb 48,1,61440
v_add_f32_e32 v14, v14, v25
v_mfma_f32_16x16x32_bf16 v[116:119], a[0:3], a[68:71], v[20:23]
v_exp_f32_e32 v46, v92
Mv 116,4,76
Rb 52,1,62464
s_add_u32 m0, s36, 69632
Mv 112,4,72
buffer_load_dwordx4 v3, s[20:23], s56 offen offset:0 sc1 lds
Mv 112,8,80
Rb 56,1,63488
Cv 43,43,25
Mv 116,8,84
v_exp_f32_e32 v28, v93
Mv 116,12,92
Rb 60,1,64512
s_waitcnt lgkmcnt(8)
Mv 112,12,88
v_exp_f32_e32 v47, v94
Mv 112,16,96
v_exp_f32_e32 v29, v95
Mv 116,16,100
v_add_f32_e32 v15, v15, v44
v_add_f32_e32 v15, v15, v26
Mv 116,20,108
Cv 44,44,26
v_add_f32_e32 v15, v15, v45
Mv 112,20,104
buffer_load_dwordx4 v3, s[20:23], s56 offen offset:1024 sc1 lds
Mv 112,24,112
v_add_f32_e32 v15, v15, v27
Cv 45,45,27
Mv 116,24,116
v_add_f32_e32 v15, v15, v46
v_add_f32_e32 v15, v15, v28
Mv 116,28,124
Cv 46,46,28
v_add_f32_e32 v15, v15, v47
Mv 112,28,120
s_waitcnt vmcnt(15) lgkmcnt(0)
s_barrier
v_mfma_f32_16x16x32_bf16 v[120:123], a[32:35], a[64:67], v[16:19]
Rb 0,8,32768
v_add_f32_e32 v15, v15, v29
v_mfma_f32_16x16x32_bf16 v[124:127], a[32:35], a[68:71], v[20:23]
Rb 4,8,33792
Cv 47,47,29
Mv 124,36,76
Rb 8,8,34816
v_max_f32_e32 v11, v14, v15
)" R"(
Mv 120,36,72
Rb 12,8,35840
buffer_load_dwordx4 v3, s[20:23], s56 offen offset:2048 sc1 lds
Mv 120,40,80
Rb 16,8,36864
v_cmp_nge_f32_e32 vcc, s62, v11
Mv 124,40,84
Rb 20,8,37888
s_add_u32 s55, s55, 0x10000
Mv 124,44,92
Rb 24,8,38912
Mv 120,44,88
Rb 28,8,39936
Mv 120,48,96
Rb 32,8,40960
Mv 124,48,100
Rb 36,8,41984
Mv 124,52,108
Rb 40,8,43008
Mv 120,52,104
Rb 44,8,44032
buffer_load_dwordx4 v3, s[20:23], s56 offen offset:3072 sc1 lds
Mv 120,56,112
s_cmp_eq_u32 s57, 2
s_cselect_b32 s55, s59, s55
Mv 124,56,116
s_add_u32 s56, s56, 0x10000
Mv 124,60,124
s_waitcnt lgkmcnt(8)
s_cbranch_vccnz .Lzfx2
.Lzfr2:
Mv 120,60,120
v_add_f32_e32 v12, v12, v14
v_add_f32_e32 v13, v13, v15
Mp 128,0,32
Rb 48,8,45056
Mp 136,0,36
s_cmp_le_i32 s57, s58
s_cbranch_scc1 .Lzmk3
.Lzmr3:
Mp 140,4,36
Rb 52,8,46080
s_add_u32 m0, s36, 32768
Mp 132,4,32
buffer_load_dwordx4 v2, s[20:23], s55 offen offset:0 sc1 lds
Mp 144,8,32
Rb 56,8,47104
Mp 152,8,36
v_exp_f32_e32 v48, v96
Mp 156,12,36
Rb 60,8,48128
s_waitcnt lgkmcnt(8)
Mp 148,12,32
v_exp_f32_e32 v24, v97
Mp 160,16,32
Rb 0,8,49152
v_add_f32_e32 v14, v48, v24
Mp 168,16,36
v_exp_f32_e32 v49, v98
Mp 172,20,36
Rb 4,8,50176
Cv 48,48,24
Mp 164,20,32
buffer_load_dwordx4 v2, s[20:23], s55 offen offset:1024 sc1 lds
)" R"(
Mp 176,24,32
Rb 8,8,51200
v_add_f32_e32 v14, v14, v49
Mp 184,24,36
v_exp_f32_e32 v25, v99
Mp 188,28,36
Rb 12,8,52224
s_waitcnt lgkmcnt(8)
Mp 180,28,32
v_exp_f32_e32 v50, v104
Mp 192,32,32
Rb 16,8,53248
v_add_f32_e32 v14, v14, v25
Mp 200,32,36
v_exp_f32_e32 v26, v105
Mp 204,36,36
Rb 20,8,54272
Cv 49,49,25
Mp 196,36,32
buffer_load_dwordx4 v2, s[20:23], s55 offen offset:2048 sc1 lds
Mp 208,40,32
Rb 24,8,55296
v_add_f32_e32 v14, v14, v50
Mp 216,40,36
v_exp_f32_e32 v51, v106
Mp 220,44,36
Rb 28,8,56320
s_waitcnt lgkmcnt(8)
Mp 212,44,32
v_exp_f32_e32 v27, v107
Mp 224,48,32
Rb 32,8,57344
v_add_f32_e32 v14, v14, v26
Mp 232,48,36
v_exp_f32_e32 v52, v100
Mp 236,52,36
Rb 36,8,58368
Cv 50,50,26
Mp 228,52,32
buffer_load_dwordx4 v2, s[20:23], s55 offen offset:3072 sc1 lds
Mp 240,56,32
Rb 40,8,59392
v_add_f32_e32 v14, v14, v51
Mp 248,56,36
v_exp_f32_e32 v28, v101
Mp 252,60,36
Rb 44,8,60416
s_waitcnt lgkmcnt(8)
Mp 244,60,32
v_exp_f32_e32 v53, v102
Mp 128,0,40
Rb 48,8,61440
v_add_f32_e32 v14, v14, v27
Mp 136,0,44
v_exp_f32_e32 v29, v103
Mp 140,4,44
Rb 52,8,62464
s_add_u32 m0, s36, 36864
Mp 132,4,40
buffer_load_dwordx4 v3, s[20:23], s55 offen offset:0 sc1 lds
Mp 144,8,40
Rb 56,8,63488
Cv 51,51,27
Mp 152,8,44
)" R"(
v_exp_f32_e32 v54, v108
Mp 156,12,44
Rb 60,8,64512
s_waitcnt lgkmcnt(8)
Mp 148,12,40
v_exp_f32_e32 v24, v109
Mp 160,16,40
v_exp_f32_e32 v55, v110
Mp 168,16,44
v_exp_f32_e32 v25, v111
Mp 172,20,44
v_add_f32_e32 v15, v52, v28
Cv 52,52,28
Mp 164,20,40
buffer_load_dwordx4 v3, s[20:23], s55 offen offset:1024 sc1 lds
Mp 176,24,40
v_add_f32_e32 v15, v15, v53
v_add_f32_e32 v15, v15, v29
Mp 184,24,44
Cv 53,53,29
v_add_f32_e32 v15, v15, v54
Mp 188,28,44
v_add_f32_e32 v15, v15, v24
Cv 54,54,24
Mp 180,28,40
s_waitcnt vmcnt(14) lgkmcnt(0)
s_barrier
Mp 192,32,40
Rb 0,1,0
v_add_f32_e32 v15, v15, v55
Mp 200,32,44
Rb 4,1,1024
v_add_f32_e32 v15, v15, v25
Mp 204,36,44
Rb 8,1,2048
Cv 55,55,25
Mp 196,36,40
Rb 12,1,3072
buffer_load_dwordx4 v3, s[20:23], s55 offen offset:2048 sc1 lds
Mp 208,40,40
Rb 16,1,8192
s_sub_u32 s57, s57, 1
Mp 216,40,44
Rb 20,1,9216
Mp 220,44,44
Rb 24,1,10240
Mp 212,44,40
Rb 28,1,11264
Mp 224,48,40
Rb 32,1,4096
Mp 232,48,44
Rb 36,1,5120
Mp 236,52,44
Rb 40,1,6144
Mp 228,52,40
Rb 44,1,7168
buffer_load_dwordx4 v3, s[20:23], s55 offen offset:3072 sc1 lds
Mp 240,56,40
v_cmp_eq_u32_e32 vcc, s3, v4
s_andn2_b64 s[28:29], -1, vcc
s_cbranch_scc1 .Lzsp9
.Lzok9:
Mp 248,56,44
Mp 252,60,44
s_waitcnt lgkmcnt(8)
)" R"(
Mp 244,60,40
s_cmp_gt_i32 s57, s93
s_cbranch_scc1 .LzL
s_cmp_eq_u32 s94, 1
s_cbranch_scc1 .LzW
s_cmp_eq_u32 s94, 2
s_cbranch_scc1 .LzDA
.LzEB:
v_mfma_f32_16x16x32_bf16 v[64:67], a[0:3], a[64:67], v[16:19]
Rb 48,1,12288
s_mov_b32 s83, 0xc108000
buffer_load_dword v4, v7, s[20:23], s83 offen offset:4 sc0 sc1
v_mfma_f32_16x16x32_bf16 v[68:71], a[0:3], a[68:71], v[20:23]
s_cmp_lg_u32 s35, 7
s_cselect_b64 s[60:61], 1, 0
Mv 68,4,76
Rb 52,1,13312
s_add_u32 m0, s36, 98304
Mv 64,4,72
buffer_load_dwordx4 v2, s[20:23], s56 offen offset:0 sc1 lds
Mv 64,8,80
Rb 56,1,14336
Mv 68,8,84
v_exp_f32_e32 v56, v112
Mv 68,12,92
Rb 60,1,15360
s_waitcnt lgkmcnt(8)
Mv 64,12,88
v_exp_f32_e32 v28, v113
Mv 64,16,96
Rb 0,1,16384
v_add_f32_e32 v14, v14, v56
Mv 68,16,100
v_exp_f32_e32 v57, v114
Mv 68,20,108
Rb 4,1,17408
v_add_f32_e32 v14, v14, v28
Mv 64,20,104
buffer_load_dwordx4 v2, s[20:23], s56 offen offset:1024 sc1 lds
Mv 64,24,112
Rb 8,1,18432
Cv 56,56,28
Mv 68,24,116
v_exp_f32_e32 v29, v115
Mv 68,28,124
Rb 12,1,19456
s_waitcnt lgkmcnt(8)
Mv 64,28,120
v_exp_f32_e32 v58, v120
v_mfma_f32_16x16x32_bf16 v[72:75], a[32:35], a[64:67], v[16:19]
Rb 16,1,24576
v_add_f32_e32 v14, v14, v57
v_mfma_f32_16x16x32_bf16 v[76:79], a[32:35], a[68:71], v[20:23]
v_exp_f32_e32 v24, v121
Mv 76,36,76
Rb 20,1,25600
v_add_f32_e32 v14, v14, v29
Mv 72,36,72
buffer_load_dwordx4 v2, s[20:23], s56 offen offset:2048 sc1 lds
Mv 72,40,80
Rb 24,1,26624
Cv 57,57,29
Mv 76,40,84
v_exp_f32_e32 v59, v122
)" R"(
Mv 76,44,92
Rb 28,1,27648
s_waitcnt lgkmcnt(8)
Mv 72,44,88
v_exp_f32_e32 v25, v123
Mv 72,48,96
Rb 32,1,20480
v_add_f32_e32 v14, v14, v58
Mv 76,48,100
v_exp_f32_e32 v60, v116
Mv 76,52,108
Rb 36,1,21504
v_add_f32_e32 v14, v14, v24
Mv 72,52,104
buffer_load_dwordx4 v2, s[20:23], s56 offen offset:3072 sc1 lds
Mv 72,56,112
Rb 40,1,22528
Cv 58,58,24
Mv 76,56,116
v_exp_f32_e32 v26, v117
Mv 76,60,124
Rb 44,1,23552
s_waitcnt lgkmcnt(8)
Mv 72,60,120
v_exp_f32_e32 v61, v118
v_mfma_f32_16x16x32_bf16 v[80:83], a[0:3], a[64:67], v[16:19]
Rb 48,1,28672
v_add_f32_e32 v14, v14, v59
v_mfma_f32_16x16x32_bf16 v[84:87], a[0:3], a[68:71], v[20:23]
v_exp_f32_e32 v27, v119
Mv 84,4,76
Rb 52,1,29696
s_add_u32 m0, s36, 102400
Mv 80,4,72
buffer_load_dwordx4 v3, s[20:23], s56 offen offset:0 sc1 lds
Mv 80,8,80
Rb 56,1,30720
v_add_f32_e32 v14, v14, v25
Mv 84,8,84
v_exp_f32_e32 v62, v124
Mv 84,12,92
Rb 60,1,31744
s_waitcnt lgkmcnt(8)
Mv 80,12,88
v_exp_f32_e32 v28, v125
Mv 80,16,96
v_exp_f32_e32 v63, v126
Mv 84,16,100
v_exp_f32_e32 v29, v127
Mv 84,20,108
Cv 59,59,25
v_add_f32_e32 v15, v15, v60
Mv 80,20,104
buffer_load_dwordx4 v3, s[20:23], s56 offen offset:1024 sc1 lds
Mv 80,24,112
v_add_f32_e32 v15, v15, v26
Cv 60,60,26
Mv 84,24,116
v_add_f32_e32 v15, v15, v61
v_add_f32_e32 v15, v15, v27
Mv 84,28,124
Cv 61,61,27
v_add_f32_e32 v15, v15, v62
Mv 80,28,120
)" R"(
s_waitcnt vmcnt(15) lgkmcnt(0)
s_barrier
v_mfma_f32_16x16x32_bf16 v[88:91], a[32:35], a[64:67], v[16:19]
Rb 0,8,0
v_add_f32_e32 v15, v15, v28
v_mfma_f32_16x16x32_bf16 v[92:95], a[32:35], a[68:71], v[20:23]
Rb 4,8,1024
Cv 62,62,28
Mv 92,36,76
Rb 8,8,2048
v_add_f32_e32 v15, v15, v63
Mv 88,36,72
Rb 12,8,3072
buffer_load_dwordx4 v3, s[20:23], s56 offen offset:2048 sc1 lds
Mv 88,40,80
Rb 16,8,4096
v_add_f32_e32 v15, v15, v29
Mv 92,40,84
Rb 20,8,5120
Cv 63,63,29
Mv 92,44,92
Rb 24,8,6144
v_max_f32_e32 v11, v14, v15
Mv 88,44,88
Rb 28,8,7168
v_cmp_nge_f32_e32 vcc, s62, v11
Mv 88,48,96
Rb 32,8,8192
s_add_u32 s55, s55, 0x10000
Mv 92,48,100
Rb 36,8,9216
Mv 92,52,108
Rb 40,8,10240
Mv 88,52,104
Rb 44,8,11264
buffer_load_dwordx4 v3, s[20:23], s56 offen offset:3072 sc1 lds
Mv 88,56,112
s_cmp_eq_u32 s57, 2
s_cselect_b32 s55, s59, s55
Mv 92,56,116
s_add_u32 s56, s56, 0x10000
Mv 92,60,124
s_waitcnt lgkmcnt(8)
s_cbranch_vccnz .Lzfx11
.Lzfr11:
Mv 88,60,120
s_waitcnt vmcnt(8)
v_cmp_eq_u32_e32 vcc, s3, v4
s_andn2_b64 s[28:29], 1, vcc
s_cbranch_scc1 .Lzsp21
.Lzok21:
Mp 128,0,48
Rb 48,8,12288
buffer_load_dword v9, v7, s[20:23], s83 offen sc0 sc1
Mp 136,0,52
v_add_f32_e32 v12, v12, v14
s_cmp_le_i32 s57, s58
s_cbranch_scc1 .Lzmk10
.Lzmr10:
Mp 140,4,52
Rb 52,8,13312
s_add_u32 m0, s36, 0
v_add_f32_e32 v13, v13, v15
Mp 132,4,48
buffer_load_dwordx4 v2, s[20:23], s55 offen offset:0 sc1 lds
)" R"(
Mp 144,8,48
Rb 56,8,14336
Mp 152,8,52
v_exp_f32_e32 v32, v64
Mp 156,12,52
Rb 60,8,15360
s_waitcnt lgkmcnt(8)
Mp 148,12,48
v_exp_f32_e32 v24, v65
Mp 160,16,48
Rb 0,8,16384
v_add_f32_e32 v14, v32, v24
Mp 168,16,52
v_exp_f32_e32 v33, v66
Cv 32,32,24
Mp 172,20,52
Rb 4,8,17408
v_add_f32_e32 v14, v14, v33
Mp 164,20,48
buffer_load_dwordx4 v2, s[20:23], s55 offen offset:1024 sc1 lds
Mp 176,24,48
Rb 8,8,18432
Mp 184,24,52
v_exp_f32_e32 v25, v67
Mp 188,28,52
Rb 12,8,19456
s_waitcnt lgkmcnt(8)
Mp 180,28,48
v_exp_f32_e32 v34, v72
Mp 192,32,48
Rb 16,8,20480
v_add_f32_e32 v14, v14, v25
Mp 200,32,52
v_exp_f32_e32 v26, v73
Cv 33,33,25
Mp 204,36,52
Rb 20,8,21504
v_add_f32_e32 v14, v14, v34
Mp 196,36,48
buffer_load_dwordx4 v2, s[20:23], s55 offen offset:2048 sc1 lds
Mp 208,40,48
Rb 24,8,22528
v_add_f32_e32 v14, v14, v26
Mp 216,40,52
v_exp_f32_e32 v35, v74
Mp 220,44,52
Rb 28,8,23552
s_waitcnt lgkmcnt(8)
Mp 212,44,48
v_exp_f32_e32 v27, v75
Mp 224,48,48
Rb 32,8,24576
Cv 34,34,26
Mp 232,48,52
v_exp_f32_e32 v36, v68
v_add_f32_e32 v14, v14, v35
Mp 236,52,52
Rb 36,8,25600
v_add_f32_e32 v14, v14, v27
Mp 228,52,48
buffer_load_dwordx4 v2, s[20:23], s55 offen offset:3072 sc1 lds
Mp 240,56,48
Rb 40,8,26624
Cv 35,35,27
)" R"(
Mp 248,56,52
v_exp_f32_e32 v28, v69
Mp 252,60,52
Rb 44,8,27648
s_waitcnt lgkmcnt(8)
Mp 244,60,48
v_exp_f32_e32 v37, v70
Mp 128,0,56
Rb 48,8,28672
v_add_f32_e32 v15, v36, v28
Mp 136,0,60
v_exp_f32_e32 v29, v71
Cv 36,36,28
Mp 140,4,60
Rb 52,8,29696
s_add_u32 m0, s36, 4096
Mp 132,4,56
buffer_load_dwordx4 v3, s[20:23], s55 offen offset:0 sc1 lds
Mp 144,8,56
Rb 56,8,30720
v_add_f32_e32 v15, v15, v37
Mp 152,8,60
v_exp_f32_e32 v38, v76
Mp 156,12,60
Rb 60,8,31744
s_waitcnt lgkmcnt(8)
Mp 148,12,56
v_exp_f32_e32 v24, v77
Mp 160,16,56
v_exp_f32_e32 v39, v78
Mp 168,16,60
v_exp_f32_e32 v25, v79
v_add_f32_e32 v15, v15, v29
Mp 172,20,60
Cv 37,37,29
v_add_f32_e32 v15, v15, v38
Mp 164,20,56
buffer_load_dwordx4 v3, s[20:23], s55 offen offset:1024 sc1 lds
Mp 176,24,56
v_add_f32_e32 v15, v15, v24
Cv 38,38,24
Mp 184,24,60
v_add_f32_e32 v15, v15, v39
v_add_f32_e32 v15, v15, v25
Mp 188,28,60
Cv 39,39,25
s_sub_u32 s57, s57, 1
Mp 180,28,56
s_waitcnt lgkmcnt(0)
s_barrier
Mp 192,32,56
Rb 0,1,32768
v_bfe_u32 v11, v0, 2, 2
Mp 200,32,60
Rb 4,1,33792
v_lshrrev_b32 v4, 4, v0
v_xor_b32 v11, v11, v4
Mp 204,36,60
Rb 8,1,34816
v_bfe_u32 v4, v0, 1, 1
Mp 196,36,56
Rb 12,1,35840
buffer_load_dwordx4 v3, s[20:23], s55 offen offset:2048 sc1 lds
Mp 208,40,56
Rb 16,1,40960
)" R"(
v_lshl_or_b32 v11, v11, 1, v4
Mp 216,40,60
Rb 20,1,41984
v_and_b32 v4, 1, v0
Mp 220,44,60
Rb 24,1,43008
v_mul_u32_u24 v4, 0xffff, v4
Mp 212,44,56
Rb 28,1,44032
v_add_u32 v4, 1, v4
Mp 224,48,56
Rb 32,1,36864
v_mul_u32_u24 v4, 0x3f80, v4
Mp 232,48,60
Rb 36,1,37888
v_cmp_eq_u32_e32 vcc, 0, v11
v_cmp_eq_u32_e64 s[28:29], 1, v11
Mp 236,52,60
Rb 40,1,38912
v_accvgpr_write_b32 a192, 0
Mp 228,52,56
Rb 44,1,39936
buffer_load_dwordx4 v3, s[20:23], s55 offen offset:3072 sc1 lds
Mp 240,56,56
v_accvgpr_write_b32 a193, 0
v_accvgpr_write_b32 a196, 0
Mp 248,56,60
v_accvgpr_write_b32 a197, 0
v_cndmask_b32_e32 v11, 0, v4, vcc
Mp 252,60,60
v_cndmask_b32_e64 v4, 0, v4, s[28:29]
s_waitcnt lgkmcnt(8)
Mp 244,60,56
v_accvgpr_write_b32 a194, v11
v_accvgpr_write_b32 a195, v4
v_mfma_f32_16x16x32_bf16 v[96:99], a[0:3], a[64:67], v[16:19]
Rb 48,1,45056
v_mfma_f32_16x16x32_bf16 v[100:103], a[0:3], a[68:71], v[20:23]
buffer_load_dword v4, v7, s[20:23], s65 offen offset:0 sc0 sc1
Mv 100,4,76
Rb 52,1,46080
s_add_u32 m0, s36, 65536
Mv 96,4,72
buffer_load_dwordx4 v2, s[20:23], s56 offen offset:0 sc1 lds
Mv 96,8,80
Rb 56,1,47104
Mv 100,8,84
v_exp_f32_e32 v40, v80
Mv 100,12,92
Rb 60,1,48128
s_waitcnt lgkmcnt(8)
Mv 96,12,88
v_exp_f32_e32 v26, v81
v_add_f32_e32 v14, v14, v40
Mv 96,16,96
Rb 0,1,49152
v_add_f32_e32 v14, v14, v26
Mv 100,16,100
v_exp_f32_e32 v41, v82
Mv 100,20,108
Rb 4,1,50176
Cv 40,40,26
Mv 96,20,104
buffer_load_dwordx4 v2, s[20:23], s56 offen offset:1024 sc1 lds
)" R"(
Mv 96,24,112
Rb 8,1,51200
v_add_f32_e32 v14, v14, v41
Mv 100,24,116
v_exp_f32_e32 v27, v83
Mv 100,28,124
Rb 12,1,52224
s_waitcnt lgkmcnt(8)
Mv 96,28,120
v_exp_f32_e32 v42, v88
v_add_f32_e32 v14, v14, v27
v_mfma_f32_16x16x32_bf16 v[104:107], a[32:35], a[64:67], v[16:19]
Rb 16,1,57344
Cv 41,41,27
v_mfma_f32_16x16x32_bf16 v[108:111], a[32:35], a[68:71], v[20:23]
v_exp_f32_e32 v28, v89
Mv 108,36,76
Rb 20,1,58368
v_add_f32_e32 v14, v14, v42
Mv 104,36,72
buffer_load_dwordx4 v2, s[20:23], s56 offen offset:2048 sc1 lds
Mv 104,40,80
Rb 24,1,59392
v_add_f32_e32 v14, v14, v28
Mv 108,40,84
v_exp_f32_e32 v43, v90
Mv 108,44,92
Rb 28,1,60416
s_waitcnt lgkmcnt(8)
Mv 104,44,88
v_exp_f32_e32 v29, v91
Cv 42,42,28
Mv 104,48,96
Rb 32,1,53248
v_add_f32_e32 v14, v14, v43
Mv 108,48,100
v_exp_f32_e32 v44, v84
Mv 108,52,108
Rb 36,1,54272
v_add_f32_e32 v14, v14, v29
Mv 104,52,104
buffer_load_dwordx4 v2, s[20:23], s56 offen offset:3072 sc1 lds
Mv 104,56,112
Rb 40,1,55296
Cv 43,43,29
Mv 108,56,116
v_exp_f32_e32 v24, v85
Mv 108,60,124
Rb 44,1,56320
s_waitcnt lgkmcnt(8)
Mv 104,60,120
v_exp_f32_e32 v45, v86
v_add_f32_e32 v15, v15, v44
v_mfma_f32_16x16x32_bf16 v[112:115], a[0:3], a[64:67], v[16:19]
Rb 48,1,61440
v_add_f32_e32 v15, v15, v24
v_mfma_f32_16x16x32_bf16 v[116:119], a[0:3], a[68:71], v[20:23]
v_exp_f32_e32 v25, v87
Mv 116,4,76
Rb 52,1,62464
s_add_u32 m0, s36, 69632
Mv 112,4,72
buffer_load_dwordx4 v3, s[20:23], s56 offen offset:0 sc1 lds
Mv 112,8,80
)" R"(
Rb 56,1,63488
Cv 44,44,24
Mv 116,8,84
v_exp_f32_e32 v46, v92
Mv 116,12,92
Rb 60,1,64512
s_waitcnt lgkmcnt(8)
Mv 112,12,88
v_exp_f32_e32 v26, v93
v_add_f32_e32 v15, v15, v45
Mv 112,16,96
v_exp_f32_e32 v47, v94
Mv 116,16,100
v_exp_f32_e32 v27, v95
Mv 116,20,108
v_add_f32_e32 v15, v15, v25
Cv 45,45,25
Mv 112,20,104
buffer_load_dwordx4 v3, s[20:23], s56 offen offset:1024 sc1 lds
Mv 112,24,112
v_add_f32_e32 v15, v15, v46
v_add_f32_e32 v15, v15, v26
Mv 116,24,116
Cv 46,46,26
v_add_f32_e32 v15, v15, v47
Mv 116,28,124
v_add_f32_e32 v15, v15, v27
Cv 47,47,27
Mv 112,28,120
v_max_f32_e32 v11, v14, v15
s_waitcnt vmcnt(6) lgkmcnt(0)
s_barrier
v_mfma_f32_16x16x32_bf16 v[120:123], a[32:35], a[64:67], v[16:19]
Rb 0,8,32768
v_cmp_nge_f32_e32 vcc, s62, v11
v_mfma_f32_16x16x32_bf16 v[124:127], a[32:35], a[68:71], v[20:23]
Rb 4,8,33792
s_add_u32 s55, s55, 0x10000
Mv 124,36,76
Rb 8,8,34816
Mv 120,36,72
Rb 12,8,35840
buffer_load_dwordx4 v3, s[20:23], s56 offen offset:2048 sc1 lds
Mv 120,40,80
Rb 16,8,36864
Mv 124,40,84
Rb 20,8,37888
Mv 124,44,92
Rb 24,8,38912
Mv 120,44,88
Rb 28,8,39936
s_cmp_eq_u32 s57, 2
s_cselect_b32 s55, s59, s55
Mv 120,48,96
Rb 32,8,40960
Mv 124,48,100
Rb 36,8,41984
Mv 124,52,108
Rb 40,8,43008
Mv 120,52,104
Rb 44,8,44032
buffer_load_dwordx4 v3, s[20:23], s56 offen offset:3072 sc1 lds
Mv 120,56,112
v_readfirstlane_b32 s63, v9
Mv 124,56,116
)" R"(
s_add_u32 s56, s56, 0x10000
Mv 124,60,124
s_waitcnt lgkmcnt(8)
s_cbranch_vccnz .Lzfx12
.Lzfr12:
Mv 120,60,120
v_cmp_eq_u32_e32 vcc, s3, v4
s_andn2_b64 s[28:29], s[60:61], vcc
s_cbranch_scc1 .Lzsp22
.Lzok22:
Mp 128,0,32
Rb 48,8,45056
v_add_f32_e32 v12, v12, v14
Mp 136,0,36
v_add_f32_e32 v13, v13, v15
s_cmp_le_i32 s57, s58
s_cbranch_scc1 .Lzmk11
.Lzmr11:
Mp 140,4,36
Rb 52,8,46080
s_add_u32 m0, s36, 32768
buffer_load_dwordx4 v[64:67], v1, s[20:23], s66 offen sc0 sc1
Mp 132,4,32
buffer_load_dwordx4 v2, s[20:23], s55 offen offset:0 sc1 lds
Mp 144,8,32
Rb 56,8,47104
Mp 152,8,36
buffer_load_dwordx4 a[128:131], v2, s[20:23], s64 offen offset:0 sc0 sc1
Mp 156,12,36
Rb 60,8,48128
s_waitcnt lgkmcnt(8)
Mp 148,12,32
buffer_load_dwordx4 a[132:135], v2, s[20:23], s64 offen offset:1024 sc0 sc1
Mp 160,16,32
Rb 0,8,49152
Mp 168,16,36
buffer_load_dwordx4 a[136:139], v2, s[20:23], s64 offen offset:2048 sc0 sc1
Mp 172,20,36
Rb 4,8,50176
v_exp_f32_e32 v48, v96
Mp 164,20,32
buffer_load_dwordx4 v2, s[20:23], s55 offen offset:1024 sc1 lds
Mp 176,24,32
Rb 8,8,51200
v_exp_f32_e32 v28, v97
Mp 184,24,36
buffer_load_dwordx4 a[140:143], v2, s[20:23], s64 offen offset:3072 sc0 sc1
Mp 188,28,36
Rb 12,8,52224
v_add_f32_e32 v14, v48, v28
s_waitcnt lgkmcnt(8)
Mp 180,28,32
buffer_load_dwordx4 a[144:147], v3, s[20:23], s64 offen offset:0 sc0 sc1
Mp 192,32,32
Rb 16,8,53248
v_exp_f32_e32 v49, v98
Mp 200,32,36
buffer_load_dwordx4 a[148:151], v3, s[20:23], s64 offen offset:1024 sc0 sc1
Cv 48,48,28
Mp 204,36,36
Rb 20,8,54272
v_add_f32_e32 v14, v14, v49
Mp 196,36,32
buffer_load_dwordx4 v2, s[20:23], s55 offen offset:2048 sc1 lds
)" R"(
Mp 208,40,32
Rb 24,8,55296
Mp 216,40,36
buffer_load_dwordx4 a[152:155], v3, s[20:23], s64 offen offset:2048 sc0 sc1
Mp 220,44,36
Rb 28,8,56320
s_waitcnt lgkmcnt(8)
Mp 212,44,32
buffer_load_dwordx4 a[156:159], v3, s[20:23], s64 offen offset:3072 sc0 sc1
Mp 224,48,32
Rb 32,8,57344
Mp 232,48,36
s_add_u32 s64, s64, 0x10000
buffer_load_dwordx4 a[160:163], v2, s[20:23], s64 offen offset:0 sc0 sc1
Mp 236,52,36
Rb 36,8,58368
v_exp_f32_e32 v29, v99
Mp 228,52,32
buffer_load_dwordx4 v2, s[20:23], s55 offen offset:3072 sc1 lds
Mp 240,56,32
Rb 40,8,59392
v_exp_f32_e32 v50, v104
Mp 248,56,36
buffer_load_dwordx4 a[164:167], v2, s[20:23], s64 offen offset:1024 sc0 sc1
Mp 252,60,36
Rb 44,8,60416
v_add_f32_e32 v14, v14, v29
s_waitcnt lgkmcnt(8)
Mp 244,60,32
buffer_load_dwordx4 a[168:171], v2, s[20:23], s64 offen offset:2048 sc0 sc1
Mp 128,0,40
Rb 48,8,61440
v_exp_f32_e32 v24, v105
Mp 136,0,44
buffer_load_dwordx4 a[172:175], v2, s[20:23], s64 offen offset:3072 sc0 sc1
Cv 49,49,29
Mp 140,4,44
Rb 52,8,62464
s_add_u32 m0, s36, 36864
Mp 132,4,40
buffer_load_dwordx4 v3, s[20:23], s55 offen offset:0 sc1 lds
v_add_f32_e32 v14, v14, v50
Mp 144,8,40
Rb 56,8,63488
v_add_f32_e32 v14, v14, v24
Mp 152,8,44
buffer_load_dwordx4 a[176:179], v3, s[20:23], s64 offen offset:0 sc0 sc1
Cv 50,50,24
Mp 156,12,44
Rb 60,8,64512
s_waitcnt lgkmcnt(8)
Mp 148,12,40
buffer_load_dwordx4 a[180:183], v3, s[20:23], s64 offen offset:1024 sc0 sc1
Mp 160,16,40
v_exp_f32_e32 v51, v106
Mp 168,16,44
buffer_load_dwordx4 a[184:187], v3, s[20:23], s64 offen offset:2048 sc0 sc1
v_add_f32_e32 v14, v14, v51
Mp 172,20,44
v_exp_f32_e32 v25, v107
s_sub_u32 s57, s57, 1
Mp 164,20,40
buffer_load_dwordx4 v3, s[20:23], s55 offen offset:1024 sc1 lds
Mp 176,24,40
)" R"(
v_exp_f32_e32 v52, v100
v_add_f32_e32 v14, v14, v25
Mp 184,24,44
buffer_load_dwordx4 a[188:191], v3, s[20:23], s64 offen offset:3072 sc0 sc1
Mp 188,28,44
v_exp_f32_e32 v26, v101
Cv 51,51,25
Mp 180,28,40
s_waitcnt vmcnt(23) lgkmcnt(0)
s_barrier
Mp 192,32,40
Rb 0,8,0
v_exp_f32_e32 v53, v102
Mp 200,32,44
Rb 4,8,16384
v_exp_f32_e32 v27, v103
Mp 204,36,44
Rb 8,8,1024
v_add_f32_e32 v15, v52, v26
Mp 196,36,40
Rb 12,8,17408
buffer_load_dwordx4 v3, s[20:23], s55 offen offset:2048 sc1 lds
Mp 208,40,40
Rb 16,8,2048
Cv 52,52,26
Mp 216,40,44
Rb 20,8,18432
v_exp_f32_e32 v54, v108
Mp 220,44,44
Rb 24,8,3072
v_add_f32_e32 v15, v15, v53
Mp 212,44,40
Rb 28,8,19456
v_exp_f32_e32 v28, v109
Mp 224,48,40
Rb 32,8,4096
v_add_f32_e32 v15, v15, v27
Mp 232,48,44
Rb 36,8,20480
v_exp_f32_e32 v55, v110
Mp 236,52,44
Rb 40,8,5120
v_exp_f32_e32 v29, v111
Mp 228,52,40
Rb 44,8,21504
buffer_load_dwordx4 v3, s[20:23], s55 offen offset:3072 sc1 lds
Mp 240,56,40
Cv 53,53,27
v_add_f32_e32 v15, v15, v54
v_add_f32_e32 v15, v15, v28
Mp 248,56,44
Cv 54,54,28
v_add_f32_e32 v15, v15, v55
Mp 252,60,44
v_add_f32_e32 v15, v15, v29
Cv 55,55,29
Mp 244,60,40
s_waitcnt vmcnt(18)
v_cmp_neq_f32_e32 vcc, v16, v66
v_cmp_neq_f32_e64 s[28:29], v20, v67
s_or_b64 vcc, vcc, s[28:29]
s_cbranch_scc1 .LzMRG
.LzMRR:
Mv 128,194,128
v_exp_f32_e32 v56, v112
)" R"(
Mv 136,194,132
v_exp_f32_e32 v26, v113
v_add_f32_e32 v12, v12, v64
Mv 132,192,128
s_add_u32 m0, s36, 131072
s_add_u32 s40, s67, 0x20000
v_add_f32_e32 v13, v13, v65
Mv 140,192,132
buffer_load_dwordx4 v2, s[20:23], s40 offen offset:0 sc1 lds
Mv 144,194,136
v_exp_f32_e32 v57, v114
v_add_f32_e32 v14, v14, v56
Mv 152,194,140
v_exp_f32_e32 v27, v115
v_add_f32_e32 v14, v14, v26
Mv 148,192,136
Cv 56,56,26
s_waitcnt vmcnt(14)
Mv 156,192,140
buffer_load_dwordx4 v2, s[20:23], s40 offen offset:1024 sc1 lds
v_add_f32_e32 v14, v14, v57
Mv 160,194,144
v_exp_f32_e32 v58, v120
v_add_f32_e32 v14, v14, v27
Mv 168,194,148
v_exp_f32_e32 v28, v121
Mv 164,192,144
v_exp_f32_e32 v59, v122
Cv 57,57,27
Mv 172,192,148
buffer_load_dwordx4 v2, s[20:23], s40 offen offset:2048 sc1 lds
v_add_f32_e32 v14, v14, v58
Mv 176,194,152
v_exp_f32_e32 v29, v123
Mv 184,194,156
v_exp_f32_e32 v60, v116
v_add_f32_e32 v14, v14, v28
Mv 180,192,152
v_exp_f32_e32 v24, v117
s_waitcnt vmcnt(11)
Mv 188,192,156
buffer_load_dwordx4 v2, s[20:23], s40 offen offset:3072 sc1 lds
Cv 58,58,28
Mv 192,194,160
v_exp_f32_e32 v61, v118
Mv 200,194,164
v_exp_f32_e32 v25, v119
v_add_f32_e32 v14, v14, v59
Mv 196,192,160
s_add_u32 m0, s36, 135168
v_exp_f32_e32 v62, v124
Mv 204,192,164
buffer_load_dwordx4 v3, s[20:23], s40 offen offset:0 sc1 lds
Mv 208,194,168
v_exp_f32_e32 v26, v125
v_add_f32_e32 v14, v14, v29
Mv 216,194,172
v_exp_f32_e32 v63, v126
Cv 59,59,29
Mv 212,192,168
v_add_f32_e32 v15, v15, v60
s_waitcnt vmcnt(7)
Mv 220,192,172
buffer_load_dwordx4 v3, s[20:23], s40 offen offset:1024 sc1 lds
)" R"(
v_add_f32_e32 v15, v15, v24
Mv 224,194,176
v_exp_f32_e32 v27, v127
Cv 60,60,24
Mv 232,194,180
v_add_f32_e32 v15, v15, v61
v_add_f32_e32 v15, v15, v25
Mv 228,192,176
Cv 61,61,25
v_add_f32_e32 v15, v15, v62
v_add_f32_e32 v15, v15, v26
Mv 236,192,180
buffer_load_dwordx4 v3, s[20:23], s40 offen offset:2048 sc1 lds
Cv 62,62,26
Mv 240,194,184
v_add_f32_e32 v15, v15, v63
v_add_f32_e32 v15, v15, v27
Mv 248,194,188
s_add_u32 s40, s67, 0x30000
Cv 63,63,27
v_max_f32_e32 v11, v14, v15
Mv 244,192,184
s_add_u32 m0, s36, 98304
v_cmp_nge_f32_e32 vcc, s62, v11
s_waitcnt lgkmcnt(8)
Mv 252,192,188
buffer_load_dwordx4 v2, s[20:23], s40 offen offset:0 sc1 lds
s_cbranch_vccnz .Lzfx13
.Lzfr13:
Mp 128,0,48
Rb 48,8,6144
v_add_f32_e32 v12, v12, v14
Mp 136,0,52
v_add_f32_e32 v13, v13, v15
v_mov_b32 v24, v12
v_mov_b32 v25, v13
Mp 136,4,60
Rb 52,8,22528
v_permlane32_swap_b32_e32 v12, v24
v_add_f32_e32 v12, v12, v24
Mp 128,4,56
buffer_load_dwordx4 v2, s[20:23], s40 offen offset:1024 sc1 lds
v_mov_b32 v24, v12
Mp 132,8,48
Rb 56,8,7168
v_permlane16_swap_b32_e32 v12, v24
Mp 140,8,52
v_add_f32_e32 v12, v12, v24
v_rcp_f32_e32 v12, v12
Mp 140,12,60
Rb 60,8,23552
v_permlane32_swap_b32_e32 v13, v25
s_waitcnt lgkmcnt(8)
Mp 132,12,56
buffer_load_dwordx4 v2, s[20:23], s40 offen offset:2048 sc1 lds
v_add_f32_e32 v13, v13, v25
Mp 144,16,48
Rb 0,8,8192
v_mov_b32 v25, v13
Mp 152,16,52
s_nop 0
v_permlane16_swap_b32_e32 v13, v25
v_add_f32_e32 v13, v13, v25
Mp 152,20,60
)" R"(
Rb 4,8,24576
v_rcp_f32_e32 v13, v13
Mp 144,20,56
buffer_load_dwordx4 v2, s[20:23], s40 offen offset:3072 sc1 lds
v_mul_f32_e32 v128, v12, v128
Mp 148,24,48
Rb 8,8,9216
v_mul_f32_e32 v129, v12, v129
Mp 156,24,52
s_add_u32 m0, s36, 102400
Cv 128,128,129
v_mul_f32_e32 v130, v12, v130
Mp 156,28,60
Rb 12,8,25600
v_mul_f32_e32 v131, v12, v131
s_waitcnt lgkmcnt(8)
Mp 148,28,56
buffer_load_dwordx4 v3, s[20:23], s40 offen offset:0 sc1 lds
Cv 129,130,131
Mp 160,32,48
Rb 16,8,10240
v_mul_f32_e32 v132, v12, v132
Mp 168,32,52
v_mul_f32_e32 v133, v12, v133
Cv 130,132,133
v_mul_f32_e32 v134, v12, v134
Mp 168,36,60
Rb 20,8,26624
v_mul_f32_e32 v135, v12, v135
Cv 131,134,135
Mp 160,36,56
buffer_load_dwordx4 v3, s[20:23], s40 offen offset:1024 sc1 lds
v_mul_f32_e32 v136, v13, v136
Mp 164,40,48
Rb 24,8,11264
v_mul_f32_e32 v137, v13, v137
Mp 172,40,52
Cv 136,136,137
v_mul_f32_e32 v138, v13, v138
v_mul_f32_e32 v139, v13, v139
Mp 172,44,60
Rb 28,8,27648
Cv 137,138,139
s_waitcnt lgkmcnt(8)
Mp 164,44,56
buffer_load_dwordx4 v3, s[20:23], s40 offen offset:2048 sc1 lds
v_mul_f32_e32 v140, v13, v140
Mp 176,48,48
Rb 32,8,12288
v_mul_f32_e32 v141, v13, v141
Mp 184,48,52
Cv 138,140,141
v_mul_f32_e32 v142, v13, v142
v_mul_f32_e32 v143, v13, v143
Mp 184,52,60
Rb 36,8,28672
Cv 139,142,143
v_mul_f32_e32 v144, v12, v144
Mp 176,52,56
v_mul_f32_e32 v145, v12, v145
Cv 144,144,145
v_mul_f32_e32 v146, v12, v146
Mp 180,56,48
Rb 40,8,13312
)" R"(
v_mul_f32_e32 v147, v12, v147
Mp 188,56,52
Cv 145,146,147
v_mul_f32_e32 v148, v12, v148
v_mul_f32_e32 v149, v12, v149
Mp 188,60,60
Rb 44,8,29696
Cv 146,148,149
s_waitcnt lgkmcnt(8)
Mp 180,60,56
v_mul_f32_e32 v150, v12, v150
v_mul_f32_e32 v151, v12, v151
Cv 147,150,151
Mp 192,0,48
Rb 48,8,14336
v_mul_f32_e32 v152, v13, v152
Mp 200,0,52
v_mul_f32_e32 v153, v13, v153
Cv 152,152,153
v_mul_f32_e32 v154, v13, v154
Mp 200,4,60
Rb 52,8,30720
v_mul_f32_e32 v155, v13, v155
Cv 153,154,155
Mp 192,4,56
v_mul_f32_e32 v156, v13, v156
v_mul_f32_e32 v157, v13, v157
Cv 154,156,157
Mp 196,8,48
Rb 56,8,15360
v_mul_f32_e32 v158, v13, v158
Mp 204,8,52
v_mul_f32_e32 v159, v13, v159
Cv 155,158,159
v_mul_f32_e32 v160, v12, v160
Mp 204,12,60
Rb 60,8,31744
v_mul_f32_e32 v161, v12, v161
s_waitcnt lgkmcnt(8)
Mp 196,12,56
Cv 160,160,161
v_mul_f32_e32 v162, v12, v162
v_mul_f32_e32 v163, v12, v163
Mp 208,16,48
Rb 0,1,0
Cv 161,162,163
Mp 216,16,52
v_mul_f32_e32 v164, v12, v164
v_mul_f32_e32 v165, v12, v165
Cv 162,164,165
Mp 216,20,60
Rb 4,1,1024
v_mul_f32_e32 v166, v12, v166
v_mul_f32_e32 v167, v12, v167
Mp 208,20,56
Cv 163,166,167
v_mul_f32_e32 v168, v13, v168
v_mul_f32_e32 v169, v13, v169
Mp 212,24,48
Rb 8,1,2048
Cv 168,168,169
Mp 220,24,52
v_mul_f32_e32 v170, v13, v170
v_mul_f32_e32 v171, v13, v171
)" R"(
Cv 169,170,171
Mp 220,28,60
Rb 12,1,3072
s_waitcnt vmcnt(14) lgkmcnt(4)
s_barrier
Mp 212,28,56
s_add_u32 s40, s67, 0x40000
v_mul_f32_e32 v172, v13, v172
v_mul_f32_e32 v173, v13, v173
Mp 224,32,48
Rb 16,1,4096
s_add_u32 m0, s36, 65536
Mp 232,32,52
buffer_load_dwordx4 v2, s[20:23], s40 offen offset:0 sc1 lds
Cv 170,172,173
Mp 232,36,60
Rb 20,1,5120
v_mul_f32_e32 v174, v13, v174
v_mul_f32_e32 v175, v13, v175
Mp 224,36,56
Cv 171,174,175
v_mul_f32_e32 v176, v12, v176
v_mul_f32_e32 v177, v12, v177
Mp 228,40,48
Rb 24,1,6144
Cv 176,176,177
s_lshl4_add_u32 s28, s34, s35
Mp 236,40,52
v_mul_f32_e32 v178, v12, v178
v_mul_f32_e32 v179, v12, v179
Cv 177,178,179
Mp 236,44,60
Rb 28,1,8192
v_mul_f32_e32 v180, v12, v180
Mp 228,44,56
buffer_load_dwordx4 v2, s[20:23], s40 offen offset:1024 sc1 lds
v_mul_f32_e32 v181, v12, v181
Mp 240,48,48
Rb 32,1,9216
Cv 178,180,181
s_lshl_b32 s64, s28, 17
Mp 248,48,52
v_mul_f32_e32 v182, v12, v182
v_mul_f32_e32 v183, v12, v183
Cv 179,182,183
Mp 248,52,60
Rb 36,1,10240
v_mul_f32_e32 v184, v13, v184
v_mul_f32_e32 v185, v13, v185
Mp 240,52,56
Cv 184,184,185
v_mul_f32_e32 v186, v13, v186
v_mul_f32_e32 v187, v13, v187
Mp 244,56,48
Rb 40,1,11264
Cv 185,186,187
s_add_u32 s64, s64, 0x4100000
Mp 252,56,52
buffer_load_dwordx4 v2, s[20:23], s40 offen offset:2048 sc1 lds
v_mul_f32_e32 v188, v13, v188
Mp 252,60,60
Rb 44,1,12288
v_mul_f32_e32 v189, v13, v189
s_mov_b32 s55, 0xc108c00
s_waitcnt lgkmcnt(8)
)" R"(
Mp 244,60,56
Cv 186,188,189
v_mul_f32_e32 v190, v13, v190
v_mul_f32_e32 v191, v13, v191
v_mfma_f32_16x16x32_bf16 v[32:35], a[0:3], v[128:131], 0
Rb 48,1,13312
Cv 187,190,191
s_xor_b32 s66, s64, 0x202000
v_mfma_f32_16x16x32_bf16 v[40:43], a[0:3], v[136:139], 0
v_mul_f32_e32 v192, v12, v192
v_mul_f32_e32 v193, v12, v193
Cv 192,192,193
v_mfma_f32_16x16x32_bf16 v[44:47], a[4:7], v[136:139], 0
Rb 52,1,14336
v_mul_f32_e32 v194, v12, v194
v_mul_f32_e32 v195, v12, v195
v_mfma_f32_16x16x32_bf16 v[36:39], a[4:7], v[128:131], 0
buffer_load_dwordx4 v2, s[20:23], s40 offen offset:3072 sc1 lds
Cv 193,194,195
v_mfma_f32_16x16x32_bf16 v[48:51], a[8:11], v[128:131], 0
Rb 56,1,16384
v_mul_f32_e32 v196, v12, v196
s_sub_u32 s66, s66, s84
v_mfma_f32_16x16x32_bf16 v[56:59], a[8:11], v[136:139], 0
v_mul_f32_e32 v197, v12, v197
Cv 194,196,197
v_mul_f32_e32 v198, v12, v198
v_mfma_f32_16x16x32_bf16 v[60:63], a[12:15], v[136:139], 0
Rb 60,1,17408
v_mul_f32_e32 v199, v12, v199
s_cmp_lg_u32 s33, 0
s_cmov_b32 s55, 0x40000000
s_waitcnt lgkmcnt(8)
v_mfma_f32_16x16x32_bf16 v[52:55], a[12:15], v[128:131], 0
Cv 195,198,199
v_mul_f32_e32 v200, v13, v200
v_mul_f32_e32 v201, v13, v201
v_mov_b32 v16, 1
Mp 32,16,144
Rb 0,1,18432
s_add_u32 m0, s36, 69632
s_sub_u32 s66, s66, s84
v_add_u32 v9, s36, v8
Mp 40,16,152
buffer_load_dwordx4 v3, s[20:23], s40 offen offset:0 sc1 lds
Cv 200,200,201
Mp 44,20,152
Rb 4,1,19456
v_mul_f32_e32 v202, v13, v202
v_mul_f32_e32 v203, v13, v203
Mp 36,20,144
Cv 201,202,203
v_mul_f32_e32 v204, v13, v204
v_mul_f32_e32 v205, v13, v205
Mp 48,24,144
Rb 8,1,20480
Cv 202,204,205
s_lshl2_add_u32 s65, s35, s33
Mp 56,24,152
v_mul_f32_e32 v206, v13, v206
v_mul_f32_e32 v207, v13, v207
Cv 203,206,207
Mp 60,28,152
Rb 12,1,21504
)" R"(
v_mul_f32_e32 v208, v12, v208
s_waitcnt lgkmcnt(8)
Mp 52,28,144
buffer_load_dwordx4 v3, s[20:23], s40 offen offset:1024 sc1 lds
v_mul_f32_e32 v209, v12, v209
Mp 32,32,160
Rb 16,1,22528
Cv 208,208,209
s_lshl_b32 s65, s65, 8
Mp 40,32,168
v_mul_f32_e32 v210, v12, v210
v_mul_f32_e32 v211, v12, v211
Cv 209,210,211
Mp 44,36,168
Rb 20,1,24576
v_mul_f32_e32 v212, v12, v212
v_mul_f32_e32 v213, v12, v213
Mp 36,36,160
Cv 210,212,213
v_mul_f32_e32 v214, v12, v214
v_mul_f32_e32 v215, v12, v215
Mp 48,40,160
Rb 24,1,25600
Cv 211,214,215
s_add_u32 s65, s65, s50
Mp 56,40,168
buffer_load_dwordx4 v3, s[20:23], s40 offen offset:2048 sc1 lds
v_mul_f32_e32 v216, v13, v216
Mp 60,44,168
Rb 28,1,26624
v_mul_f32_e32 v217, v13, v217
s_waitcnt lgkmcnt(8)
Mp 52,44,160
Cv 216,216,217
v_mul_f32_e32 v218, v13, v218
v_mul_f32_e32 v219, v13, v219
Mp 32,48,176
Rb 32,1,27648
Cv 217,218,219
s_add_u32 s65, s65, 0x6400
Mp 40,48,184
v_mul_f32_e32 v220, v13, v220
v_mul_f32_e32 v221, v13, v221
Cv 218,220,221
Mp 44,52,184
Rb 36,1,28672
v_mul_f32_e32 v222, v13, v222
v_mul_f32_e32 v223, v13, v223
Mp 36,52,176
Cv 219,222,223
v_mul_f32_e32 v224, v12, v224
v_mul_f32_e32 v225, v12, v225
Mp 48,56,176
Rb 40,1,29696
Cv 224,224,225
s_xor_b32 s52, s65, 0x202000
Mp 56,56,184
v_mul_f32_e32 v226, v12, v226
v_mul_f32_e32 v227, v12, v227
Cv 225,226,227
Mp 60,60,184
Rb 44,1,30720
v_mul_f32_e32 v228, v12, v228
s_waitcnt lgkmcnt(8)
)" R"(
Mp 52,60,176
v_mul_f32_e32 v229, v12, v229
Cv 226,228,229
v_mul_f32_e32 v230, v12, v230
Mp 32,0,192
Rb 48,1,32768
v_mul_f32_e32 v231, v12, v231
buffer_atomic_add v16, v7, s[20:23], s55 offen sc0 sc1
Mp 40,0,200
Cv 227,230,231
v_mul_f32_e32 v232, v13, v232
v_mul_f32_e32 v233, v13, v233
Mp 44,4,200
Rb 52,1,33792
Cv 232,232,233
v_mul_f32_e32 v234, v13, v234
Mp 36,4,192
v_mul_f32_e32 v235, v13, v235
Cv 233,234,235
v_mul_f32_e32 v236, v13, v236
Mp 48,8,192
Rb 56,1,34816
v_mul_f32_e32 v237, v13, v237
Mp 56,8,200
Cv 234,236,237
v_mul_f32_e32 v238, v13, v238
v_mul_f32_e32 v239, v13, v239
Mp 60,12,200
Rb 60,1,35840
s_waitcnt vmcnt(15) lgkmcnt(4)
s_barrier
Mp 52,12,192
s_add_u32 s40, s67, 0x50000
Cv 235,238,239
v_mul_f32_e32 v240, v12, v240
Mp 32,16,208
Rb 0,1,36864
s_add_u32 m0, s36, 0
Mp 40,16,216
buffer_load_dwordx4 v2, s[20:23], s40 offen offset:0 sc1 lds
v_mul_f32_e32 v241, v12, v241
Mp 44,20,216
Rb 4,1,37888
Cv 240,240,241
v_mul_f32_e32 v242, v12, v242
Mp 36,20,208
v_mul_f32_e32 v243, v12, v243
Cv 241,242,243
v_mul_f32_e32 v244, v12, v244
Mp 48,24,208
Rb 8,1,38912
v_mul_f32_e32 v245, v12, v245
Mp 56,24,216
Cv 242,244,245
v_mul_f32_e32 v246, v12, v246
v_mul_f32_e32 v247, v12, v247
Mp 60,28,216
Rb 12,1,40960
Cv 243,246,247
Mp 52,28,208
buffer_load_dwordx4 v2, s[20:23], s40 offen offset:1024 sc1 lds
v_mul_f32_e32 v248, v13, v248
Mp 32,32,224
Rb 16,1,41984
v_mul_f32_e32 v249, v13, v249
)" R"(
Mp 40,32,232
Cv 248,248,249
v_mul_f32_e32 v250, v13, v250
v_mul_f32_e32 v251, v13, v251
Mp 44,36,232
Rb 20,1,43008
Cv 249,250,251
v_mul_f32_e32 v252, v13, v252
Mp 36,36,224
v_mul_f32_e32 v253, v13, v253
Cv 250,252,253
v_mul_f32_e32 v254, v13, v254
Mp 48,40,224
Rb 24,1,44032
v_mul_f32_e32 v255, v13, v255
Mp 56,40,232
buffer_load_dwordx4 v2, s[20:23], s40 offen offset:2048 sc1 lds
Cv 251,254,255
Mp 60,44,232
Rb 28,1,45056
s_waitcnt lgkmcnt(8)
Mp 52,44,224
Mp 32,48,240
Rb 32,1,46080
Mp 40,48,248
Mp 44,52,248
Rb 36,1,47104
Mp 36,52,240
buffer_load_dwordx4 v2, s[20:23], s40 offen offset:3072 sc1 lds
Mp 48,56,240
Rb 40,1,49152
Mp 56,56,248
Mp 60,60,248
Rb 44,1,50176
s_waitcnt lgkmcnt(8)
Mp 52,60,240
v_mfma_f32_16x16x32_bf16 v[64:67], a[0:3], v[128:131], 0
Rb 48,1,51200
s_add_u32 m0, s36, 4096
v_mfma_f32_16x16x32_bf16 v[72:75], a[0:3], v[136:139], 0
buffer_load_dwordx4 v3, s[20:23], s40 offen offset:0 sc1 lds
Mp 72,4,152
Rb 52,1,52224
Mp 64,4,144
Cv 32,32,33
Cv 33,34,35
Mp 64,8,160
Rb 56,1,53248
Cv 34,36,37
Mp 72,8,168
Cv 35,38,39
Mp 72,12,184
Rb 60,1,54272
s_waitcnt lgkmcnt(8)
Mp 64,12,176
buffer_load_dwordx4 v3, s[20:23], s40 offen offset:1024 sc1 lds
Mp 64,16,192
Rb 0,1,55296
Mp 72,16,200
buffer_store_dwordx4 v[32:35], v2, s[20:23], s64 offen offset:0 sc0 sc1
Mp 72,20,216
Rb 4,1,57344
Mp 64,20,208
Mp 64,24,224
)" R"(
Rb 8,1,58368
Mp 72,24,232
buffer_load_dwordx4 v3, s[20:23], s40 offen offset:2048 sc1 lds
Mp 72,28,248
Rb 12,1,59392
s_waitcnt lgkmcnt(8)
Mp 64,28,240
Cv 40,40,41
Cv 41,42,43
v_mfma_f32_16x16x32_bf16 v[68:71], a[32:35], v[128:131], 0
Rb 16,1,60416
Cv 42,44,45
v_mfma_f32_16x16x32_bf16 v[76:79], a[32:35], v[136:139], 0
Cv 43,46,47
Mp 76,36,152
Rb 20,1,61440
Mp 68,36,144
buffer_store_dwordx4 v[40:43], v2, s[20:23], s64 offen offset:1024 sc0 sc1
Mp 68,40,160
Rb 24,1,62464
Mp 76,40,168
Mp 76,44,184
Rb 28,1,63488
s_waitcnt lgkmcnt(8)
Mp 68,44,176
Mp 68,48,192
Rb 32,10,0
Mp 76,48,200
Cv 48,48,49
Cv 49,50,51
Mp 76,52,216
Rb 36,10,1024
Cv 50,52,53
Mp 68,52,208
Cv 51,54,55
Mp 68,56,224
Rb 40,10,2048
Mp 76,56,232
buffer_store_dwordx4 v[48:51], v2, s[20:23], s64 offen offset:2048 sc0 sc1
Mp 76,60,248
Rb 44,10,3072
s_waitcnt vmcnt(18) lgkmcnt(4)
s_barrier
Mp 68,60,240
s_add_u32 s40, s67, 0x60000
v_mfma_f32_16x16x32_bf16 v[80:83], a[0:3], v[128:131], 0
Rb 48,10,4096
s_add_u32 m0, s36, 32768
v_mfma_f32_16x16x32_bf16 v[88:91], a[0:3], v[136:139], 0
buffer_load_dwordx4 v2, s[20:23], s40 offen offset:0 sc1 lds
Mp 88,4,152
Rb 52,10,5120
Mp 80,4,144
Cv 56,56,57
Cv 57,58,59
Mp 80,8,160
Rb 56,10,6144
Cv 58,60,61
Mp 88,8,168
Cv 59,62,63
Mp 88,12,184
Rb 60,10,8192
Mp 80,12,176
buffer_load_dwordx4 v2, s[20:23], s40 offen offset:1024 sc1 lds
Mp 80,16,192
)" R"(
Rb 0,10,9216
Mp 88,16,200
buffer_store_dwordx4 v[56:59], v2, s[20:23], s64 offen offset:3072 sc0 sc1
Mp 88,20,216
Rb 4,10,10240
Mp 80,20,208
Mp 80,24,224
Rb 8,10,11264
Mp 88,24,232
buffer_load_dwordx4 v2, s[20:23], s40 offen offset:2048 sc1 lds
Mp 88,28,248
Rb 12,10,12288
s_waitcnt lgkmcnt(8)
Mp 80,28,240
Cv 64,64,65
Cv 65,66,67
v_mfma_f32_16x16x32_bf16 v[84:87], a[32:35], v[128:131], 0
Rb 16,10,13312
Cv 66,68,69
v_mfma_f32_16x16x32_bf16 v[92:95], a[32:35], v[136:139], 0
Cv 67,70,71
Mp 92,36,152
Rb 20,10,14336
Mp 84,36,144
buffer_load_dwordx4 v2, s[20:23], s40 offen offset:3072 sc1 lds
Mp 84,40,160
Rb 24,10,16384
Mp 92,40,168
buffer_store_dwordx4 v[64:67], v3, s[20:23], s64 offen offset:0 sc0 sc1
Mp 92,44,184
Rb 28,10,17408
s_waitcnt lgkmcnt(8)
Mp 84,44,176
Mp 84,48,192
Rb 32,10,18432
s_add_u32 m0, s36, 36864
Mp 92,48,200
buffer_load_dwordx4 v3, s[20:23], s40 offen offset:0 sc1 lds
Mp 92,52,216
Rb 36,10,19456
Mp 84,52,208
Cv 72,72,73
Cv 73,74,75
Mp 84,56,224
Rb 40,10,20480
Cv 74,76,77
Mp 92,56,232
Cv 75,78,79
Mp 92,60,248
Rb 44,10,21504
s_waitcnt lgkmcnt(8)
Mp 84,60,240
buffer_load_dwordx4 v3, s[20:23], s40 offen offset:1024 sc1 lds
v_mfma_f32_16x16x32_bf16 v[96:99], a[0:3], v[128:131], 0
Rb 48,10,22528
v_mfma_f32_16x16x32_bf16 v[104:107], a[0:3], v[136:139], 0
buffer_store_dwordx4 v[72:75], v3, s[20:23], s64 offen offset:1024 sc0 sc1
Mp 104,4,152
Rb 52,10,24576
Mp 96,4,144
Mp 96,8,160
Rb 56,10,25600
Mp 104,8,168
buffer_load_dwordx4 v3, s[20:23], s40 offen offset:2048 sc1 lds
)" R"(
Mp 104,12,184
Rb 60,10,26624
s_waitcnt lgkmcnt(8)
Mp 96,12,176
Cv 80,80,81
Cv 81,82,83
Mp 96,16,192
Rb 0,10,27648
Cv 82,84,85
Mp 104,16,200
Cv 83,86,87
Mp 104,20,216
Rb 4,10,28672
Mp 96,20,208
buffer_store_dwordx4 v[80:83], v3, s[20:23], s64 offen offset:2048 sc0 sc1
Mp 96,24,224
Rb 8,10,29696
Mp 104,24,232
Mp 104,28,248
Rb 12,10,30720
s_waitcnt lgkmcnt(8)
Mp 96,28,240
v_mfma_f32_16x16x32_bf16 v[100:103], a[32:35], v[128:131], 0
Rb 16,8,32768
v_mfma_f32_16x16x32_bf16 v[108:111], a[32:35], v[136:139], 0
Cv 88,88,89
Cv 89,90,91
Mp 108,36,152
Rb 20,8,33792
Cv 90,92,93
Mp 100,36,144
Cv 91,94,95
Mp 100,40,160
Rb 24,8,34816
Mp 108,40,168
buffer_store_dwordx4 v[88:91], v3, s[20:23], s64 offen offset:3072 sc0 sc1
Mp 108,44,184
Rb 28,8,35840
s_waitcnt vmcnt(23) lgkmcnt(4)
s_barrier
Mp 100,44,176
s_add_u32 s40, s67, 0x70000
Mp 100,48,192
Rb 32,8,36864
s_add_u32 m0, s36, 131072
Mp 108,48,200
buffer_load_dwordx4 v2, s[20:23], s40 offen offset:0 sc1 lds
Mp 108,52,216
Rb 36,8,37888
s_mov_b32 s8, s74
Mp 100,52,208
s_mov_b32 s9, s75
s_mov_b32 s10, 0x1c0000
Mp 100,56,224
Rb 40,8,38912
s_mov_b32 s11, 0x20000
Mp 108,56,232
v_and_b32 v174, 1, v0
v_mul_u32_u24 v174, 0xffff, v174
Mp 108,60,248
Rb 44,8,40960
Mp 100,60,240
buffer_load_dwordx4 v2, s[20:23], s40 offen offset:1024 sc1 lds
v_mfma_f32_16x16x32_bf16 v[112:115], a[0:3], v[128:131], 0
Rb 48,8,41984
)" R"(
v_add_u32 v174, 1, v174
v_mfma_f32_16x16x32_bf16 v[120:123], a[0:3], v[136:139], 0
v_mul_u32_u24 v174, 0x3f80, v174
v_bfe_u32 v173, v0, 1, 3
Mp 120,4,152
Rb 52,8,43008
v_cmp_eq_u32_e32 vcc, 0, v173
Mp 112,4,144
Cv 96,96,97
Cv 97,98,99
Mp 112,8,160
Rb 56,8,44032
Cv 98,100,101
Mp 120,8,168
buffer_load_dwordx4 v2, s[20:23], s40 offen offset:2048 sc1 lds
Mp 120,12,184
Rb 60,8,45056
s_waitcnt lgkmcnt(8)
Mp 112,12,176
Cv 99,102,103
v_cndmask_b32_e32 v132, 0, v174, vcc
v_cmp_eq_u32_e32 vcc, 1, v173
Mp 112,16,192
Rb 0,8,46080
v_cndmask_b32_e32 v133, 0, v174, vcc
Mp 120,16,200
s_add_u32 s64, s64, 0x10000
buffer_store_dwordx4 v[96:99], v2, s[20:23], s64 offen offset:0 sc0 sc1
Mp 120,20,216
Rb 4,8,47104
v_cmp_eq_u32_e32 vcc, 2, v173
Mp 112,20,208
buffer_load_dwordx4 v2, s[20:23], s40 offen offset:3072 sc1 lds
Mp 112,24,224
Rb 8,8,49152
v_cndmask_b32_e32 v134, 0, v174, vcc
Mp 120,24,232
v_cmp_eq_u32_e32 vcc, 3, v173
v_and_b32 v167, 0xf0, v1
Mp 120,28,248
Rb 12,8,50176
s_waitcnt lgkmcnt(8)
Mp 112,28,240
Cv 104,104,105
Cv 105,106,107
v_mfma_f32_16x16x32_bf16 v[116:119], a[32:35], v[128:131], 0
Rb 16,8,51200
s_add_u32 m0, s36, 135168
v_mfma_f32_16x16x32_bf16 v[124:127], a[32:35], v[136:139], 0
buffer_load_dwordx4 v3, s[20:23], s40 offen offset:0 sc1 lds
Mp 124,36,152
Rb 20,8,52224
Cv 106,108,109
Mp 116,36,144
Cv 107,110,111
v_cndmask_b32_e32 v135, 0, v174, vcc
Mp 116,40,160
Rb 24,8,53248
v_cmp_eq_u32_e32 vcc, 4, v173
Mp 124,40,168
buffer_store_dwordx4 v[104:107], v2, s[20:23], s64 offen offset:1024 sc0 sc1
Mp 124,44,184
Rb 28,8,54272
s_waitcnt lgkmcnt(8)
)" R"(
Mp 116,44,176
buffer_load_dwordx4 v3, s[20:23], s40 offen offset:1024 sc1 lds
v_cndmask_b32_e32 v140, 0, v174, vcc
Mp 116,48,192
Rb 32,8,55296
v_cmp_eq_u32_e32 vcc, 5, v173
Mp 124,48,200
s_nop 0
v_cndmask_b32_e32 v141, 0, v174, vcc
v_cmp_eq_u32_e32 vcc, 6, v173
Mp 124,52,216
Rb 36,8,57344
v_cndmask_b32_e32 v142, 0, v174, vcc
Mp 116,52,208
v_cmp_eq_u32_e32 vcc, 7, v173
v_lshrrev_b32 v173, 4, v0
Mp 116,56,224
Rb 40,8,58368
v_cndmask_b32_e32 v143, 0, v174, vcc
Mp 124,56,232
buffer_load_dwordx4 v3, s[20:23], s40 offen offset:2048 sc1 lds
Mp 124,60,248
Rb 44,8,59392
s_waitcnt vmcnt(9) lgkmcnt(8)
buffer_store_dword v6, v7, s[20:23], s65 offen offset:0 sc0 sc1
Mp 116,60,240
v_lshl_or_b32 v164, v173, 16, v167
v_mul_u32_u24 v166, 56, v167
v_mfma_f32_16x16x32_bf16 v[32:35], a[0:3], v[128:131], 0
Rb 48,8,60416
v_lshl_add_u32 v166, v173, 3, v166
v_mfma_f32_16x16x32_bf16 v[40:43], a[0:3], v[136:139], 0
v_lshrrev_b32_e64 v173, v173, s85
v_bfe_u32 v174, v173, 4, 1
Mp 40,4,152
Rb 52,8,61440
v_lshl_or_b32 v183, v174, 13, v164
Mp 32,4,144
Cv 112,112,113
Cv 113,114,115
Mp 32,8,160
Rb 56,8,62464
Cv 114,116,117
Mp 40,8,168
Cv 115,118,119
v_and_b32 v174, 1, v173
Mp 40,12,184
Rb 60,8,63488
s_waitcnt lgkmcnt(8)
Mp 32,12,176
buffer_store_dwordx4 v[112:115], v2, s[20:23], s64 offen offset:2048 sc0 sc1
ds_write_b32 v9, v16 offset:39936
Mp 32,16,192
Rb 0,8,0
v_lshl_or_b32 v164, v174, 13, v164
Mp 40,16,200
v_and_b32 v174, 7, v0
v_and_b32 v165, 0x80, v1
Mp 40,20,216
Rb 4,8,1024
v_lshl_or_b32 v165, v174, 8, v165
Mp 32,20,208
v_lshlrev_b32 v165, 1, v165
s_mov_b32 s93, 0x1111
)" R"(
Mp 32,24,224
Rb 8,8,2048
v_lshrrev_b32 v173, 2, v0
Mp 40,24,232
Cv 120,120,121
Cv 121,122,123
Mp 40,28,248
Rb 12,8,3072
s_waitcnt lgkmcnt(3)
s_barrier
Mp 32,28,240
Cv 122,124,125
Cv 123,126,127
ds_read_b32 v16, v8 offset:39936
v_mfma_f32_16x16x32_bf16 v[36:39], a[32:35], v[128:131], 0
Rb 16,8,4096
v_mul_u32_u24 v173, 832, v173
v_mfma_f32_16x16x32_bf16 v[44:47], a[32:35], v[136:139], 0
buffer_store_dwordx4 v[120:123], v2, s[20:23], s64 offen offset:3072 sc0 sc1
Mp 44,36,152
Rb 20,8,5120
v_add_u32 v173, v173, v1
Mp 36,36,144
v_lshrrev_b32 v174, 1, v0
v_and_b32 v174, 24, v174
Mp 36,40,160
Rb 24,8,6144
v_lshl_add_u32 v174, v167, 2, v174
Mp 44,40,168
s_add_u32 s59, s36, 98304
v_add_u32 v174, s59, v174
Mp 44,44,184
Rb 28,8,8192
s_waitcnt lgkmcnt(4)
Mp 36,44,176
v_readfirstlane_b32 s82, v16
Mp 36,48,192
Rb 32,8,9216
s_lshl2_add_u32 s56, s82, s33
Mp 44,48,200
s_mul_i32 s88, s56, 4682
s_lshr_b32 s58, s88, 16
Mp 44,52,216
Rb 36,8,10240
s_mul_i32 s57, s58, 14
Mp 36,52,208
s_sub_u32 s57, s56, s57
s_lshl_b32 s40, s58, 12
Mp 36,56,224
Rb 40,8,11264
Mp 44,56,232
s_cmp_gt_u32 s57, 6
s_cselect_b32 s88, 128, 0
Mp 44,60,248
Rb 44,8,12288
Mp 36,60,240
buffer_load_dword v4, v7, s[20:23], s52 offen offset:0 sc0 sc1
s_add_u32 s40, s40, s88
v_mfma_f32_16x16x32_bf16 v[48:51], a[0:3], v[128:131], 0
Rb 48,8,13312
s_add_u32 s40, s40, 0xc110000
v_mfma_f32_16x16x32_bf16 v[56:59], a[0:3], v[136:139], 0
s_lshl_b32 s55, s58, 10
s_add_u32 s55, s55, s57
Mp 56,4,152
)" R"(
Rb 52,8,14336
Mp 48,4,144
Cv 32,32,33
Cv 33,34,35
Mp 48,8,160
Rb 56,8,16384
Cv 34,36,37
Mp 56,8,168
Cv 35,38,39
Mp 56,12,184
Rb 60,8,17408
s_waitcnt lgkmcnt(8)
Mp 48,12,176
buffer_store_dwordx4 v[32:35], v3, s[20:23], s64 offen offset:0 sc0 sc1
Mp 48,16,192
Rb 0,8,18432
Mp 56,16,200
s_cmp_gt_u32 s57, 6
s_cselect_b32 s88, 9, 0
Mp 56,20,216
Rb 4,8,19456
s_add_u32 s55, s55, s88
Mp 48,20,208
s_and_b32 s88, s58, 2
s_lshl_b32 s88, s88, 12
Mp 48,24,224
Rb 8,8,20480
v_xor_b32 v164, s88, v164
Mp 56,24,232
Cv 40,40,41
Cv 41,42,43
Mp 56,28,248
Rb 12,8,21504
s_waitcnt lgkmcnt(8)
Mp 48,28,240
Cv 42,44,45
Cv 43,46,47
v_xor_b32 v183, s88, v183
v_mfma_f32_16x16x32_bf16 v[52:55], a[32:35], v[128:131], 0
Rb 16,8,22528
s_lshl_b32 s55, s55, 10
v_mfma_f32_16x16x32_bf16 v[60:63], a[32:35], v[136:139], 0
buffer_store_dwordx4 v[40:43], v3, s[20:23], s64 offen offset:1024 sc0 sc1
Mp 60,36,152
Rb 20,8,24576
s_add_u32 s55, s55, 0x8000000
Mp 52,36,144
s_and_b32 s88, s57, 1
s_lshl1_add_u32 s58, s58, s88
Mp 52,40,160
Rb 24,8,25600
s_lshl_b32 s88, s58, 8
Mp 60,40,168
v_add_u32 v167, s88, v167
s_mul_i32 s59, s58, 14336
Mp 60,44,184
Rb 28,8,26624
s_waitcnt lgkmcnt(8)
Mp 52,44,176
s_lshr_b32 s88, s57, 1
s_lshl_b32 s88, s88, 6
s_add_u32 s88, s88, s59
Mp 52,48,192
Rb 32,8,27648
)" R"(
v_add_u32 v166, s88, v166
Mp 60,48,200
s_mov_b32 s57, 0
s_add_u32 m0, s36, 98304
Mp 60,52,216
Rb 36,8,28672
Mp 52,52,208
buffer_load_dwordx4 v173, s[12:15], s88 offen offset:0 lds
Mp 52,56,224
Rb 40,8,29696
s_add_u32 m0, s36, 98880
Mp 60,56,232
buffer_load_dwordx4 v173, s[12:15], s88 offen offset:448 lds
Mp 60,60,248
Rb 44,8,30720
s_waitcnt lgkmcnt(8)
Mp 52,60,240
s_mov_b32 s88, 0xc100008
buffer_load_dword v167, v167, s[20:23], s88 offen sc0 sc1
v_mfma_f32_16x16x32_bf16 v[64:67], a[0:3], v[128:131], 0
Rb 48,1,0
v_mfma_f32_16x16x32_bf16 v[72:75], a[0:3], v[136:139], 0
Mp 72,4,152
Rb 52,1,1024
Mp 64,4,144
Mp 64,8,160
Rb 56,1,2048
Mp 72,8,168
Mp 72,12,184
Rb 60,1,3072
s_waitcnt vmcnt(5) lgkmcnt(8)
s_barrier
v_cmp_eq_u32_e32 vcc, s3, v4
s_andn2_b64 s[28:29], 1, vcc
s_cbranch_scc1 .Lzsp23
.Lzok23:
Mp 64,12,176
buffer_load_dwordx4 a[64:67], v2, s[20:23], s66 offen offset:0 sc0 sc1
Mp 64,16,192
Rb 0,1,4096
Mp 72,16,200
Mp 72,20,216
Rb 4,1,5120
Mp 64,20,208
buffer_load_dwordx4 a[68:71], v2, s[20:23], s66 offen offset:1024 sc0 sc1
Mp 64,24,224
Rb 8,1,6144
Mp 72,24,232
Mp 72,28,248
Rb 12,1,8192
s_waitcnt lgkmcnt(8)
Mp 64,28,240
buffer_load_dwordx4 a[72:75], v2, s[20:23], s66 offen offset:2048 sc0 sc1
v_mfma_f32_16x16x32_bf16 v[68:71], a[32:35], v[128:131], 0
Rb 16,1,9216
v_mfma_f32_16x16x32_bf16 v[76:79], a[32:35], v[136:139], 0
Mp 76,36,152
Rb 20,1,10240
Mp 68,36,144
buffer_load_dwordx4 a[76:79], v2, s[20:23], s66 offen offset:3072 sc0 sc1
Mp 68,40,160
Rb 24,1,11264
Mp 76,40,168
Mp 76,44,184
Rb 28,1,12288
)" R"(
s_waitcnt vmcnt(7) lgkmcnt(8)
buffer_store_dword v6, v7, s[20:23], s65 offen offset:128 sc0 sc1
Mp 68,44,176
buffer_load_dwordx4 a[80:83], v3, s[20:23], s66 offen offset:0 sc0 sc1
Mp 68,48,192
Rb 32,1,13312
Mp 76,48,200
Mp 76,52,216
Rb 36,1,14336
Mp 68,52,208
buffer_load_dwordx4 a[84:87], v3, s[20:23], s66 offen offset:1024 sc0 sc1
Mp 68,56,224
Rb 40,1,16384
Mp 76,56,232
Mp 76,60,248
Rb 44,1,17408
s_waitcnt lgkmcnt(8)
Mp 68,60,240
buffer_load_dwordx4 a[88:91], v3, s[20:23], s66 offen offset:2048 sc0 sc1
v_mfma_f32_16x16x32_bf16 v[80:83], a[0:3], v[128:131], 0
Rb 48,1,18432
v_mfma_f32_16x16x32_bf16 v[88:91], a[0:3], v[136:139], 0
Mp 88,4,152
Rb 52,1,19456
Mp 80,4,144
buffer_load_dwordx4 a[92:95], v3, s[20:23], s66 offen offset:3072 sc0 sc1
Mp 80,8,160
Rb 56,1,20480
Mp 88,8,168
Mp 88,12,184
Rb 60,1,21504
s_waitcnt lgkmcnt(8)
Mp 80,12,176
Mp 80,16,192
Rb 0,1,22528
Mp 88,16,200
Mp 88,20,216
Rb 4,1,24576
Mp 80,20,208
Mp 80,24,224
Rb 8,1,25600
Mp 88,24,232
Mp 88,28,248
Rb 12,1,26624
s_waitcnt lgkmcnt(8)
Mp 80,28,240
v_mfma_f32_16x16x32_bf16 v[84:87], a[32:35], v[128:131], 0
Rb 16,1,27648
v_mfma_f32_16x16x32_bf16 v[92:95], a[32:35], v[136:139], 0
Mp 92,36,152
Rb 20,1,28672
Mp 84,36,144
Mp 84,40,160
Rb 24,1,29696
Mp 92,40,168
Mp 92,44,184
Rb 28,1,30720
s_waitcnt lgkmcnt(8)
Mp 84,44,176
buffer_load_dword v4, v7, s[20:23], s52 offen offset:128 sc0 sc1
Mp 84,48,192
Rb 32,1,32768
Mp 92,48,200
Mp 92,52,216
)" R"(
Rb 36,1,33792
Mp 84,52,208
Mp 84,56,224
Rb 40,1,34816
Mp 92,56,232
Mp 92,60,248
Rb 44,1,35840
s_waitcnt vmcnt(6)
s_barrier
Mp 84,60,240
Mv 48,194,64
Mv 56,194,68
Mv 52,192,64
Mv 60,192,68
Mv 64,194,72
Mv 72,194,76
Mv 68,192,72
s_waitcnt vmcnt(3) lgkmcnt(10)
Mv 76,192,76
Cv 48,48,49
Cv 49,50,51
Mv 80,194,80
Cv 50,52,53
Cv 51,54,55
Mv 88,194,84
s_add_u32 s29, s73, 0x0
buffer_store_dwordx4 v[48:51], v1, s[20:23], s29 offen sc0 sc1
Mv 84,192,80
Mv 92,192,84
v_mfma_f32_16x16x32_bf16 v[96:99], a[0:3], v[128:131], 0
Rb 48,1,36864
v_mfma_f32_16x16x32_bf16 v[104:107], a[0:3], v[136:139], 0
Mp 104,4,152
Rb 52,1,37888
s_waitcnt lgkmcnt(8)
Mp 96,4,144
Cv 56,56,57
Cv 57,58,59
ds_read_b64 v[188:189], v174 offset:0
Mp 96,8,160
Rb 56,1,38912
Cv 58,60,61
Mp 104,8,168
Cv 59,62,63
ds_read_b64 v[190:191], v174 offset:32
Mp 104,12,184
Rb 60,1,40960
Mp 96,12,176
s_add_u32 s29, s73, 0x400
buffer_store_dwordx4 v[56:59], v1, s[20:23], s29 offen sc0 sc1
ds_read_b64 v[196:197], v174 offset:1024
Mp 96,16,192
Rb 0,1,41984
Mp 104,16,200
ds_read_b64 v[198:199], v174 offset:1056
Mp 104,20,216
Rb 4,1,43008
s_waitcnt lgkmcnt(12)
Mp 96,20,208
Mp 96,24,224
Rb 8,1,44032
Mp 104,24,232
Cv 64,64,65
Cv 65,66,67
Mp 104,28,248
)" R"(
Rb 12,1,45056
Cv 66,68,69
Mp 96,28,240
Cv 67,70,71
v_mfma_f32_16x16x32_bf16 v[100:103], a[32:35], v[128:131], 0
Rb 16,1,46080
v_mfma_f32_16x16x32_bf16 v[108:111], a[32:35], v[136:139], 0
s_add_u32 s29, s73, 0x800
buffer_store_dwordx4 v[64:67], v1, s[20:23], s29 offen sc0 sc1
Mp 108,36,152
Rb 20,1,47104
s_waitcnt vmcnt(3) lgkmcnt(11)
v_cmp_eq_u32_e32 vcc, s3, v4
s_andn2_b64 s[28:29], 1, vcc
s_cbranch_scc1 .Lzsp24
.Lzok24:
Mp 100,36,144
s_add_u32 s66, s66, 0x10000
buffer_load_dwordx4 a[96:99], v2, s[20:23], s66 offen offset:0 sc0 sc1
Mp 100,40,160
Rb 24,1,49152
v_sub_f32_e32 v172, 1.0, v167
Mp 108,40,168
Cv 172,172,172
v_lshlrev_b32 v172, 16, v172
Mp 108,44,184
Rb 28,1,50176
v_lshlrev_b32 v204, 16, v188
Mp 100,44,176
buffer_load_dwordx4 a[100:103], v2, s[20:23], s66 offen offset:1024 sc0 sc1
Mp 100,48,192
Rb 32,1,51200
Cv 72,72,73
Mp 108,48,200
Cv 73,74,75
Cv 74,76,77
Mp 108,52,216
Rb 36,1,52224
s_waitcnt lgkmcnt(12)
Mp 100,52,208
buffer_load_dwordx4 a[104:107], v2, s[20:23], s66 offen offset:2048 sc0 sc1
Mp 100,56,224
Rb 40,1,53248
Cv 75,78,79
Mp 108,56,232
s_add_u32 s29, s73, 0xc00
buffer_store_dwordx4 v[72:75], v1, s[20:23], s29 offen sc0 sc1
Mp 108,60,248
Rb 44,1,54272
v_and_b32 v205, 0xffff0000, v188
Mp 100,60,240
buffer_load_dwordx4 a[108:111], v2, s[20:23], s66 offen offset:3072 sc0 sc1
Mv 96,194,88
v_mul_f32_e32 v204, v172, v204
v_mul_f32_e32 v205, v172, v205
Mv 104,194,92
Cv 205,204,205
v_lshlrev_b32 v204, 16, v205
Mv 100,192,88
v_and_b32 v205, 0xffff0000, v205
s_waitcnt lgkmcnt(8)
Mv 108,192,92
buffer_load_dwordx4 a[112:115], v3, s[20:23], s66 offen offset:0 sc0 sc1
v_mfma_f32_16x16x32_bf16 v[112:115], a[0:3], v[128:131], 0
)" R"(
Rb 48,1,55296
Cv 80,80,81
v_mfma_f32_16x16x32_bf16 v[120:123], a[0:3], v[136:139], 0
Cv 81,82,83
Cv 82,84,85
Mp 120,4,152
Rb 52,1,57344
Cv 83,86,87
Mp 112,4,144
buffer_load_dwordx4 a[116:119], v3, s[20:23], s66 offen offset:1024 sc0 sc1
Mp 112,8,160
Rb 56,1,58368
v_lshlrev_b32 v206, 16, v189
Mp 120,8,168
s_add_u32 s29, s73, 0x1000
buffer_store_dwordx4 v[80:83], v1, s[20:23], s29 offen sc0 sc1
Mp 120,12,184
Rb 60,1,59392
s_waitcnt lgkmcnt(8)
Mp 112,12,176
v_and_b32 v207, 0xffff0000, v189
v_mul_f32_e32 v206, v172, v206
Mp 112,16,192
Rb 0,1,60416
v_mul_f32_e32 v207, v172, v207
Mp 120,16,200
Cv 207,206,207
v_lshlrev_b32 v206, 16, v207
Mp 120,20,216
Rb 4,1,61440
v_and_b32 v207, 0xffff0000, v207
Mp 112,20,208
Cv 88,88,89
Cv 89,90,91
Mp 112,24,224
Rb 8,1,62464
Cv 90,92,93
Mp 120,24,232
Cv 91,94,95
v_lshlrev_b32 v212, 16, v190
Mp 120,28,248
Rb 12,1,63488
s_waitcnt lgkmcnt(8)
Mp 112,28,240
s_add_u32 s29, s73, 0x1400
buffer_store_dwordx4 v[88:91], v1, s[20:23], s29 offen sc0 sc1
v_mfma_f32_16x16x32_bf16 v[116:119], a[32:35], v[128:131], 0
Rb 16,10,0
v_and_b32 v213, 0xffff0000, v190
v_mfma_f32_16x16x32_bf16 v[124:127], a[32:35], v[136:139], 0
v_mul_f32_e32 v212, v172, v212
v_mul_f32_e32 v213, v172, v213
Mp 124,36,152
Rb 20,10,1024
Cv 213,212,213
Mp 116,36,144
v_lshlrev_b32 v212, 16, v213
v_and_b32 v213, 0xffff0000, v213
Mp 116,40,160
Rb 24,10,2048
v_lshlrev_b32 v214, 16, v191
Mp 124,40,168
Cv 96,96,97
Cv 97,98,99
)" R"(
Mp 124,44,184
Rb 28,10,3072
s_waitcnt lgkmcnt(8)
Mp 116,44,176
Cv 98,100,101
Cv 99,102,103
Mp 116,48,192
Rb 32,10,4096
v_and_b32 v215, 0xffff0000, v191
Mp 124,48,200
s_add_u32 s29, s73, 0x1800
buffer_store_dwordx4 v[96:99], v1, s[20:23], s29 offen sc0 sc1
Mp 124,52,216
Rb 36,10,5120
v_mul_f32_e32 v214, v172, v214
Mp 116,52,208
v_mul_f32_e32 v215, v172, v215
Cv 215,214,215
Mp 116,56,224
Rb 40,10,6144
v_lshlrev_b32 v214, 16, v215
Mp 124,56,232
v_and_b32 v215, 0xffff0000, v215
v_lshlrev_b32 v220, 16, v196
Mp 124,60,248
Rb 44,10,8192
s_waitcnt vmcnt(8) lgkmcnt(10)
Mp 116,60,240
Cv 104,104,105
Cv 105,106,107
Mv 112,194,96
Cv 106,108,109
Cv 107,110,111
Mv 120,194,100
s_add_u32 s29, s73, 0x4000
buffer_store_dwordx4 v[104:107], v1, s[20:23], s29 offen sc0 sc1
Mv 116,192,96
v_and_b32 v221, 0xffff0000, v196
v_mul_f32_e32 v220, v172, v220
Mv 124,192,100
v_mul_f32_e32 v221, v172, v221
Cv 221,220,221
v_mfma_f32_16x16x32_bf16 v[32:35], a[0:3], v[128:131], 0
Rb 48,10,9216
v_lshlrev_b32 v220, 16, v221
v_mfma_f32_16x16x32_bf16 v[40:43], a[0:3], v[136:139], 0
v_and_b32 v221, 0xffff0000, v221
v_lshlrev_b32 v222, 16, v197
Mp 40,4,152
Rb 52,10,10240
s_waitcnt lgkmcnt(8)
Mp 32,4,144
Cv 112,112,113
Cv 113,114,115
Mp 32,8,160
Rb 56,10,11264
Cv 114,116,117
Mp 40,8,168
Cv 115,118,119
v_and_b32 v223, 0xffff0000, v197
Mp 40,12,184
Rb 60,10,12288
v_mul_f32_e32 v222, v172, v222
Mp 32,12,176
)" R"(
s_add_u32 s29, s73, 0x4400
buffer_store_dwordx4 v[112:115], v1, s[20:23], s29 offen sc0 sc1
Mp 32,16,192
Rb 0,10,13312
v_mul_f32_e32 v223, v172, v223
Mp 40,16,200
Cv 223,222,223
v_lshlrev_b32 v222, 16, v223
Mp 40,20,216
Rb 4,10,14336
s_waitcnt lgkmcnt(8)
Mp 32,20,208
v_and_b32 v223, 0xffff0000, v223
v_lshlrev_b32 v228, 16, v198
Mp 32,24,224
Rb 8,10,16384
v_and_b32 v229, 0xffff0000, v198
Mp 40,24,232
Cv 120,120,121
Cv 121,122,123
Mp 40,28,248
Rb 12,10,17408
Cv 122,124,125
Mp 32,28,240
Cv 123,126,127
v_mul_f32_e32 v228, v172, v228
v_mfma_f32_16x16x32_bf16 v[36:39], a[32:35], v[128:131], 0
Rb 16,10,18432
v_mul_f32_e32 v229, v172, v229
v_mfma_f32_16x16x32_bf16 v[44:47], a[32:35], v[136:139], 0
s_add_u32 s29, s73, 0x4800
buffer_store_dwordx4 v[120:123], v1, s[20:23], s29 offen sc0 sc1
Mp 44,36,152
Rb 20,10,19456
s_waitcnt lgkmcnt(8)
Mp 36,36,144
Cv 229,228,229
v_lshlrev_b32 v228, 16, v229
Mp 36,40,160
Rb 24,10,20480
v_and_b32 v229, 0xffff0000, v229
Mp 44,40,168
v_lshlrev_b32 v230, 16, v199
v_and_b32 v231, 0xffff0000, v199
Mp 44,44,184
Rb 28,10,21504
v_mul_f32_e32 v230, v172, v230
Mp 36,44,176
v_mul_f32_e32 v231, v172, v231
Cv 231,230,231
Mp 36,48,192
Rb 32,10,22528
v_lshlrev_b32 v230, 16, v231
Mp 44,48,200
v_and_b32 v231, 0xffff0000, v231
Mp 44,52,216
Rb 36,10,24576
s_waitcnt vmcnt(3) lgkmcnt(10)
buffer_store_dword v6, v7, s[20:23], s68 offen offset:0 sc0 sc1
Mp 36,52,208
Mp 36,56,224
Rb 40,10,25600
Mp 44,56,232
Mp 44,60,248
)" R"(
Rb 44,10,26624
Mp 36,60,240
Mv 32,194,104
Mv 40,194,108
Mv 36,192,104
s_waitcnt lgkmcnt(8)
Mv 44,192,108
v_mfma_f32_16x16x32_bf16 v[48:51], a[0:3], v[128:131], 0
Rb 48,10,27648
v_mfma_f32_16x16x32_bf16 v[56:59], a[0:3], v[136:139], 0
Mp 56,4,152
Rb 52,10,28672
Mp 48,4,144
Cv 32,32,33
Cv 33,34,35
Mp 48,8,160
Rb 56,10,29696
Cv 34,36,37
Mp 56,8,168
Cv 35,38,39
Mp 56,12,184
Rb 60,10,30720
s_waitcnt lgkmcnt(8)
Mp 48,12,176
s_add_u32 s29, s73, 0x4c00
buffer_store_dwordx4 v[32:35], v1, s[20:23], s29 offen sc0 sc1
Mp 48,16,192
Mp 56,16,200
Mp 56,20,216
Mp 48,20,208
Mp 48,24,224
Mp 56,24,232
Cv 40,40,41
Cv 41,42,43
buffer_store_dword v6, v7, s[20:23], s68 offen offset:128 sc0 sc1
Mp 56,28,248
Cv 42,44,45
s_waitcnt lgkmcnt(4)
Mp 48,28,240
Cv 43,46,47
v_mfma_f32_16x16x32_bf16 v[52:55], a[32:35], v[128:131], 0
s_add_u32 s29, s73, 0x5000
buffer_store_dwordx4 v[40:43], v1, s[20:23], s29 offen sc0 sc1
v_mfma_f32_16x16x32_bf16 v[60:63], a[32:35], v[136:139], 0
buffer_load_dword v4, v165, s[20:23], s40 offen sc0 sc1
Mp 60,36,152
Mp 52,36,144
Mp 52,40,160
Mp 60,40,168
Mp 60,44,184
s_waitcnt lgkmcnt(0)
Mp 52,44,176
Mp 52,48,192
Mp 60,48,200
Mp 60,52,216
Mp 52,52,208
Mp 52,56,224
Mp 60,56,232
Mp 60,60,248
Mp 52,60,240
Mv 48,194,112
Mv 56,194,116
Mv 52,192,112
Mv 60,192,116
)" R"(
s_nop 7
Cv 48,48,49
Cv 49,50,51
Cv 50,52,53
Cv 51,54,55
s_add_u32 s29, s73, 0x5400
buffer_store_dwordx4 v[48:51], v1, s[20:23], s29 offen sc0 sc1
Cv 56,56,57
Cv 57,58,59
Cv 58,60,61
Cv 59,62,63
s_add_u32 s29, s73, 0x5800
buffer_store_dwordx4 v[56:59], v1, s[20:23], s29 offen sc0 sc1
.LzEND:
s_cmp_gt_u32 s82, 223
s_cbranch_scc1 .LzFIN
s_cmp_lt_u32 s82, 14
s_cbranch_scc1 .LzBB
.LzBBR:
s_waitcnt vmcnt(2)
.LzS0:
v_cmp_ne_u32_e32 vcc, s3, v4
s_lshr_b32 s28, vcc_lo, 1
s_or_b32 s28, s28, vcc_lo
s_lshr_b32 s29, s28, 2
s_or_b32 s28, s28, s29
s_andn2_b32 s28, s93, s28
s_sub_u32 s53, s53, 1
s_cselect_b32 s28, s93, s28
s_cmov_b32 s53, 0
s_xor_b32 s93, s93, s28
s_or_b32 s57, s57, s28
s_cbranch_scc1 .LzS2
s_sleep 4
s_branch .LzSk3
.LzS2:
s_bitcmp0_b32 s57, 0
s_cbranch_scc1 .LzSk0
s_add_u32 s29, s55, 0x0
buffer_load_dwordx4 a[128:131], v164, s[20:23], s29 offen offset:0 sc0 sc1
buffer_load_dwordx4 a[132:135], v164, s[20:23], s29 offen offset:256 sc0 sc1
buffer_load_dwordx4 a[136:139], v164, s[20:23], s29 offen offset:512 sc0 sc1
buffer_load_dwordx4 a[140:143], v164, s[20:23], s29 offen offset:768 sc0 sc1
.LzSk0:
s_bitcmp0_b32 s57, 4
s_cbranch_scc1 .LzSk1
s_add_u32 s29, s55, 0x40000
buffer_load_dwordx4 a[144:147], v183, s[20:23], s29 offen offset:0 sc0 sc1
buffer_load_dwordx4 a[148:151], v183, s[20:23], s29 offen offset:256 sc0 sc1
buffer_load_dwordx4 a[152:155], v183, s[20:23], s29 offen offset:512 sc0 sc1
buffer_load_dwordx4 a[156:159], v183, s[20:23], s29 offen offset:768 sc0 sc1
.LzSk1:
s_bitcmp0_b32 s57, 8
s_cbranch_scc1 .LzSk2
s_add_u32 s29, s55, 0x8000
buffer_load_dwordx4 a[160:163], v164, s[20:23], s29 offen offset:0 sc0 sc1
buffer_load_dwordx4 a[164:167], v164, s[20:23], s29 offen offset:256 sc0 sc1
buffer_load_dwordx4 a[168:171], v164, s[20:23], s29 offen offset:512 sc0 sc1
buffer_load_dwordx4 a[172:175], v164, s[20:23], s29 offen offset:768 sc0 sc1
.LzSk2:
s_bitcmp0_b32 s57, 12
s_cbranch_scc1 .LzSk3
s_add_u32 s29, s55, 0x48000
buffer_load_dwordx4 a[176:179], v183, s[20:23], s29 offen offset:0 sc0 sc1
)" R"(
buffer_load_dwordx4 a[180:183], v183, s[20:23], s29 offen offset:256 sc0 sc1
buffer_load_dwordx4 a[184:187], v183, s[20:23], s29 offen offset:512 sc0 sc1
buffer_load_dwordx4 a[188:191], v183, s[20:23], s29 offen offset:768 sc0 sc1
.LzSk3:
s_cmp_eq_u32 s93, 0
s_cbranch_scc1 .LzS1
buffer_load_dword v4, v165, s[20:23], s40 offen sc0 sc1
.LzS1:
s_waitcnt vmcnt(0)
s_bitcmp0_b32 s57, 0
s_cbranch_scc1 .LzSv0
v_accvgpr_read_b32 v173, a131
v_accvgpr_read_b32 v174, a135
v_accvgpr_read_b32 v175, a139
v_accvgpr_read_b32 v180, a143
v_max3_u32 v173, v173, v174, v175
v_max_u32 v173, v173, v180
v_cmp_eq_u32_e32 vcc, -1, v173
s_cbranch_vccnz .LzSv0
s_bitset0_b32 s57, 0
.LzSv0:
s_bitcmp0_b32 s57, 4
s_cbranch_scc1 .LzSv1
v_accvgpr_read_b32 v173, a147
v_accvgpr_read_b32 v174, a151
v_accvgpr_read_b32 v175, a155
v_accvgpr_read_b32 v180, a159
v_max3_u32 v173, v173, v174, v175
v_max_u32 v173, v173, v180
v_cmp_eq_u32_e32 vcc, -1, v173
s_cbranch_vccnz .LzSv1
s_bitset0_b32 s57, 4
.LzSv1:
s_bitcmp0_b32 s57, 8
s_cbranch_scc1 .LzSv2
v_accvgpr_read_b32 v173, a163
v_accvgpr_read_b32 v174, a167
v_accvgpr_read_b32 v175, a171
v_accvgpr_read_b32 v180, a175
v_max3_u32 v173, v173, v174, v175
v_max_u32 v173, v173, v180
v_cmp_eq_u32_e32 vcc, -1, v173
s_cbranch_vccnz .LzSv2
s_bitset0_b32 s57, 8
.LzSv2:
s_bitcmp0_b32 s57, 12
s_cbranch_scc1 .LzSv3
v_accvgpr_read_b32 v173, a179
v_accvgpr_read_b32 v174, a183
v_accvgpr_read_b32 v175, a187
v_accvgpr_read_b32 v180, a191
v_max3_u32 v173, v173, v174, v175
v_max_u32 v173, v173, v180
v_cmp_eq_u32_e32 vcc, -1, v173
s_cbranch_vccnz .LzSv3
s_bitset0_b32 s57, 12
.LzSv3:
s_cmp_eq_u32 s53, 0
s_cmov_b32 s57, 0
s_or_b32 s28, s57, s93
s_cbranch_scc1 .LzS0
s_bfe_u32 s28, s63, 0x70004
s_cmp_eq_u32 s28, s58
s_cbranch_scc1 .LzRT
)" R"(
s_bfe_u32 s28, s63, 0x70014
s_cmp_eq_u32 s28, s58
s_cbranch_scc1 .LzRT
.LzRT3:
v_mfma_f32_16x16x32_bf16 v[80:83], v[132:135], a[128:131], 0
v_mfma_f32_16x16x32_bf16 v[88:91], v[132:135], a[160:163], 0
v_mfma_f32_16x16x32_bf16 v[84:87], v[132:135], a[136:139], 0
v_mfma_f32_16x16x32_bf16 v[92:95], v[132:135], a[168:171], 0
v_mfma_f32_16x16x32_bf16 v[80:83], v[140:143], a[132:135], v[80:83]
v_mfma_f32_16x16x32_bf16 v[88:91], v[140:143], a[164:167], v[88:91]
v_mfma_f32_16x16x32_bf16 v[84:87], v[140:143], a[140:143], v[84:87]
v_mfma_f32_16x16x32_bf16 v[92:95], v[140:143], a[172:175], v[92:95]
v_mfma_f32_16x16x32_bf16 v[80:83], v[132:135], a[144:147], v[80:83]
v_mfma_f32_16x16x32_bf16 v[88:91], v[132:135], a[176:179], v[88:91]
v_mfma_f32_16x16x32_bf16 v[84:87], v[132:135], a[152:155], v[84:87]
v_mfma_f32_16x16x32_bf16 v[92:95], v[132:135], a[184:187], v[92:95]
v_mfma_f32_16x16x32_bf16 v[80:83], v[140:143], a[148:151], v[80:83]
v_mfma_f32_16x16x32_bf16 v[88:91], v[140:143], a[180:183], v[88:91]
v_mfma_f32_16x16x32_bf16 v[84:87], v[140:143], a[156:159], v[84:87]
v_mfma_f32_16x16x32_bf16 v[92:95], v[140:143], a[188:191], v[92:95]
v_lshrrev_b32 v173, 4, v0
v_and_b32 v173, 1, v173
v_mad_u32_u24 v166, v173, 24, v166
s_nop 1
Cv 81,80,81
v_lshlrev_b32 v80, 16, v81
v_and_b32 v81, 0xffff0000, v81
Cv 83,82,83
v_lshlrev_b32 v82, 16, v83
v_and_b32 v83, 0xffff0000, v83
Cv 89,88,89
v_lshlrev_b32 v88, 16, v89
v_and_b32 v89, 0xffff0000, v89
Cv 91,90,91
v_lshlrev_b32 v90, 16, v91
v_and_b32 v91, 0xffff0000, v91
Cv 85,84,85
v_lshlrev_b32 v84, 16, v85
v_and_b32 v85, 0xffff0000, v85
Cv 87,86,87
v_lshlrev_b32 v86, 16, v87
v_and_b32 v87, 0xffff0000, v87
Cv 93,92,93
v_lshlrev_b32 v92, 16, v93
v_and_b32 v93, 0xffff0000, v93
Cv 95,94,95
v_lshlrev_b32 v94, 16, v95
v_and_b32 v95, 0xffff0000, v95
v_mul_f32_e32 v80, v167, v80
v_mul_f32_e32 v81, v167, v81
v_mul_f32_e32 v82, v167, v82
v_mul_f32_e32 v83, v167, v83
v_mul_f32_e32 v88, v167, v88
v_mul_f32_e32 v89, v167, v89
v_mul_f32_e32 v90, v167, v90
v_mul_f32_e32 v91, v167, v91
v_mul_f32_e32 v84, v167, v84
v_mul_f32_e32 v85, v167, v85
v_mul_f32_e32 v86, v167, v86
v_mul_f32_e32 v87, v167, v87
v_mul_f32_e32 v92, v167, v92
v_mul_f32_e32 v93, v167, v93
v_mul_f32_e32 v94, v167, v94
v_mul_f32_e32 v95, v167, v95
)" R"(
Cv 81,80,81
v_lshlrev_b32 v80, 16, v81
v_and_b32 v81, 0xffff0000, v81
Cv 83,82,83
v_lshlrev_b32 v82, 16, v83
v_and_b32 v83, 0xffff0000, v83
Cv 89,88,89
v_lshlrev_b32 v88, 16, v89
v_and_b32 v89, 0xffff0000, v89
Cv 91,90,91
v_lshlrev_b32 v90, 16, v91
v_and_b32 v91, 0xffff0000, v91
Cv 85,84,85
v_lshlrev_b32 v84, 16, v85
v_and_b32 v85, 0xffff0000, v85
Cv 87,86,87
v_lshlrev_b32 v86, 16, v87
v_and_b32 v87, 0xffff0000, v87
Cv 93,92,93
v_lshlrev_b32 v92, 16, v93
v_and_b32 v93, 0xffff0000, v93
Cv 95,94,95
v_lshlrev_b32 v94, 16, v95
v_and_b32 v95, 0xffff0000, v95
v_add_f32_e32 v80, v80, v204
v_add_f32_e32 v81, v81, v205
v_add_f32_e32 v82, v82, v206
v_add_f32_e32 v83, v83, v207
v_add_f32_e32 v88, v88, v220
v_add_f32_e32 v89, v89, v221
v_add_f32_e32 v90, v90, v222
v_add_f32_e32 v91, v91, v223
v_add_f32_e32 v84, v84, v212
v_add_f32_e32 v85, v85, v213
v_add_f32_e32 v86, v86, v214
v_add_f32_e32 v87, v87, v215
v_add_f32_e32 v92, v92, v228
v_add_f32_e32 v93, v93, v229
v_add_f32_e32 v94, v94, v230
v_add_f32_e32 v95, v95, v231
Cv 64,80,81
Cv 65,82,83
Cv 66,84,85
Cv 67,86,87
Cv 68,88,89
Cv 69,90,91
Cv 70,92,93
Cv 71,94,95
v_permlane16_swap_b32_e32 v64, v66
v_permlane16_swap_b32_e32 v65, v67
buffer_store_dwordx4 v[64:67], v166, s[8:11], 0 offen offset:0 sc0 sc1
v_permlane16_swap_b32_e32 v68, v70
v_permlane16_swap_b32_e32 v69, v71
buffer_store_dwordx4 v[68:71], v166, s[8:11], 0 offen offset:448 sc0 sc1
s_or_b32 s28, s82, s33
s_cbranch_scc1 .LzFIN
s_mov_b32 s28, 0xc108400
buffer_store_dword v6, v7, s[20:23], s28 offen offset:0 sc0 sc1
.LzFIN:
s_endpgm
.LzRT:
s_and_b32 s28, s63, 0x7ff
s_mul_i32 s28, s28, 896
v_subrev_u32 v174, s28, v166
)" R"(
s_bfe_u32 s28, s63, 0xb0010
s_mul_i32 s28, s28, 896
v_subrev_u32 v175, s28, v166
s_movk_i32 s28, 896
v_cmp_gt_u32_e64 s[24:25], s28, v175
v_cmp_gt_u32_e32 vcc, s28, v174
v_add_u32 v175, s28, v175
s_or_b64 s[24:25], s[24:25], vcc
v_cndmask_b32_e32 v174, v175, v174, vcc
v_lshrrev_b32 v174, 5, v174
v_lshlrev_b32 v174, 7, v174
v_and_or_b32 v174, v0, 48, v174
s_mov_b64 exec, s[24:25]
s_mov_b32 s29, 0xc109000
.LzRT1:
buffer_load_dwordx4 v[64:67], v174, s[20:23], s29 offen offset:0 sc0 sc1
buffer_load_dwordx4 v[68:71], v174, s[20:23], s29 offen offset:128 sc0 sc1
buffer_load_dwordx4 v[72:75], v174, s[20:23], s29 offen offset:1792 sc0 sc1
buffer_load_dwordx4 v[76:79], v174, s[20:23], s29 offen offset:1920 sc0 sc1
s_waitcnt vmcnt(0)
v_xor_b32 v175, s3, v65
v_xor_b32 v173, s3, v67
v_or_b32 v175, v175, v173
v_xor_b32 v173, s3, v69
v_or_b32 v175, v175, v173
v_xor_b32 v173, s3, v71
v_or_b32 v175, v175, v173
v_xor_b32 v173, s3, v73
v_or_b32 v175, v175, v173
v_xor_b32 v173, s3, v75
v_or_b32 v175, v175, v173
v_xor_b32 v173, s3, v77
v_or_b32 v175, v175, v173
v_xor_b32 v173, s3, v79
v_or_b32 v175, v175, v173
v_cmp_ne_u32_e32 vcc, 0, v175
s_cbranch_vccz .LzRT2
s_sleep 2
s_sub_u32 s53, s53, 1
s_cbranch_scc0 .LzRT1
s_mov_b32 s53, 0
.LzRT2:
v_mov_b32 v188, v64
v_mov_b32 v189, v66
v_mov_b32 v190, v68
v_mov_b32 v191, v70
v_mov_b32 v196, v72
v_mov_b32 v197, v74
v_mov_b32 v198, v76
v_mov_b32 v199, v78
v_sub_f32_e32 v172, 1.0, v167
Cv 172,172,172
v_lshlrev_b32 v172, 16, v172
v_lshlrev_b32 v204, 16, v188
v_and_b32 v205, 0xffff0000, v188
v_mul_f32_e32 v204, v172, v204
v_mul_f32_e32 v205, v172, v205
Cv 205,204,205
v_lshlrev_b32 v204, 16, v205
v_and_b32 v205, 0xffff0000, v205
v_lshlrev_b32 v206, 16, v189
v_and_b32 v207, 0xffff0000, v189
v_mul_f32_e32 v206, v172, v206
v_mul_f32_e32 v207, v172, v207
)" R"(
Cv 207,206,207
v_lshlrev_b32 v206, 16, v207
v_and_b32 v207, 0xffff0000, v207
v_lshlrev_b32 v212, 16, v190
v_and_b32 v213, 0xffff0000, v190
v_mul_f32_e32 v212, v172, v212
v_mul_f32_e32 v213, v172, v213
Cv 213,212,213
v_lshlrev_b32 v212, 16, v213
v_and_b32 v213, 0xffff0000, v213
v_lshlrev_b32 v214, 16, v191
v_and_b32 v215, 0xffff0000, v191
v_mul_f32_e32 v214, v172, v214
v_mul_f32_e32 v215, v172, v215
Cv 215,214,215
v_lshlrev_b32 v214, 16, v215
v_and_b32 v215, 0xffff0000, v215
v_lshlrev_b32 v220, 16, v196
v_and_b32 v221, 0xffff0000, v196
v_mul_f32_e32 v220, v172, v220
v_mul_f32_e32 v221, v172, v221
Cv 221,220,221
v_lshlrev_b32 v220, 16, v221
v_and_b32 v221, 0xffff0000, v221
v_lshlrev_b32 v222, 16, v197
v_and_b32 v223, 0xffff0000, v197
v_mul_f32_e32 v222, v172, v222
v_mul_f32_e32 v223, v172, v223
Cv 223,222,223
v_lshlrev_b32 v222, 16, v223
v_and_b32 v223, 0xffff0000, v223
v_lshlrev_b32 v228, 16, v198
v_and_b32 v229, 0xffff0000, v198
v_mul_f32_e32 v228, v172, v228
v_mul_f32_e32 v229, v172, v229
Cv 229,228,229
v_lshlrev_b32 v228, 16, v229
v_and_b32 v229, 0xffff0000, v229
v_lshlrev_b32 v230, 16, v199
v_and_b32 v231, 0xffff0000, v199
v_mul_f32_e32 v230, v172, v230
v_mul_f32_e32 v231, v172, v231
Cv 231,230,231
v_lshlrev_b32 v230, 16, v231
v_and_b32 v231, 0xffff0000, v231
s_mov_b64 exec, -1
s_branch .LzRT3
.LzMASK0:
s_lshl1_add_u32 s28, s57, s33
s_mov_b32 s29, 0xff800000
s_cmp_gt_u32 s28, 5
s_cbranch_scc1 .LzMASKE0
s_cmp_eq_u32 s28, 5
s_cbranch_scc1 .LzMASKB0
v_mov_b32 v72, s29
v_mov_b32 v73, s29
v_mov_b32 v74, s29
v_mov_b32 v75, s29
v_mov_b32 v80, s29
v_mov_b32 v81, s29
v_mov_b32 v82, s29
v_mov_b32 v83, s29
v_mov_b32 v84, s29
v_mov_b32 v85, s29
)" R"(
v_mov_b32 v86, s29
v_mov_b32 v87, s29
v_mov_b32 v88, s29
v_mov_b32 v89, s29
v_mov_b32 v90, s29
v_mov_b32 v91, s29
v_mov_b32 v92, s29
v_mov_b32 v93, s29
v_mov_b32 v94, s29
v_mov_b32 v95, s29
s_cmp_eq_u32 s28, 4
s_cbranch_scc1 .LzMASKA0
v_mov_b32 v64, s29
v_mov_b32 v65, s29
v_mov_b32 v66, s29
v_mov_b32 v67, s29
v_mov_b32 v68, s29
v_mov_b32 v69, s29
v_mov_b32 v70, s29
v_mov_b32 v71, s29
v_mov_b32 v76, s29
v_mov_b32 v77, s29
v_mov_b32 v78, s29
v_mov_b32 v79, s29
.LzMASKE0:
s_setpc_b64 s[30:31]
.LzMASKA0:
s_mov_b32 exec_lo, 0xf0000
s_mov_b32 exec_hi, 0xfff00ff
v_mov_b32 v64, s29
v_mov_b32 v76, s29
s_mov_b32 exec_lo, 0x1f0001
s_mov_b32 exec_hi, 0x1fff01ff
v_mov_b32 v65, s29
v_mov_b32 v77, s29
s_mov_b32 exec_lo, 0x3f0003
s_mov_b32 exec_hi, 0x3fff03ff
v_mov_b32 v66, s29
v_mov_b32 v78, s29
s_mov_b32 exec_lo, 0x7f0007
s_mov_b32 exec_hi, 0x7fff07ff
v_mov_b32 v67, s29
v_mov_b32 v79, s29
s_mov_b64 exec, -1
s_setpc_b64 s[30:31]
.LzMASKB0:
s_nop 1
v_mov_b32 v88, s29
v_mov_b32 v89, s29
v_mov_b32 v90, s29
v_mov_b32 v91, s29
s_mov_b32 exec_lo, 0xf0000
s_mov_b32 exec_hi, 0xfff00ff
v_mov_b32 v80, s29
v_mov_b32 v92, s29
s_mov_b32 exec_lo, 0x1f0001
s_mov_b32 exec_hi, 0x1fff01ff
v_mov_b32 v81, s29
v_mov_b32 v93, s29
s_mov_b32 exec_lo, 0x3f0003
s_mov_b32 exec_hi, 0x3fff03ff
v_mov_b32 v82, s29
v_mov_b32 v94, s29
s_mov_b32 exec_lo, 0x7f0007
)" R"(
s_mov_b32 exec_hi, 0x7fff07ff
v_mov_b32 v83, s29
v_mov_b32 v95, s29
s_mov_b64 exec, -1
s_setpc_b64 s[30:31]
.LzFIX0:
s_nop 7
v_max_f32_e32 v28, v64, v65
v_max3_f32 v28, v28, v66, v67
v_max3_f32 v28, v28, v72, v73
v_max3_f32 v28, v28, v74, v75
v_max3_f32 v28, v28, v80, v81
v_max3_f32 v28, v28, v82, v83
v_max3_f32 v28, v28, v88, v89
v_max3_f32 v28, v28, v90, v91
v_mov_b32 v24, v28
s_nop 1
v_permlane32_swap_b32_e32 v28, v24
v_max_f32_e32 v28, v28, v24
v_mov_b32 v24, v28
s_nop 1
v_permlane16_swap_b32_e32 v28, v24
v_max_f32_e32 v28, v28, v24
v_max_f32_e32 v29, v68, v69
v_max3_f32 v29, v29, v70, v71
v_max3_f32 v29, v29, v76, v77
v_max3_f32 v29, v29, v78, v79
v_max3_f32 v29, v29, v84, v85
v_max3_f32 v29, v29, v86, v87
v_max3_f32 v29, v29, v92, v93
v_max3_f32 v29, v29, v94, v95
v_mov_b32 v24, v29
s_nop 1
v_permlane32_swap_b32_e32 v29, v24
v_max_f32_e32 v29, v29, v24
v_mov_b32 v24, v29
s_nop 1
v_permlane16_swap_b32_e32 v29, v24
v_max_f32_e32 v29, v29, v24
s_bitcmp1_b32 s88, 0
s_cbranch_scc1 .LzFIXF0
v_max_f32_e32 v28, 0, v28
v_sub_f32_e32 v24, 0, v28
v_exp_f32_e32 v24, v24
s_nop 0
v_mul_f32_e32 v12, v24, v12
v_mul_f32_e32 v128, v24, v128
v_mul_f32_e32 v129, v24, v129
v_mul_f32_e32 v130, v24, v130
v_mul_f32_e32 v131, v24, v131
v_mul_f32_e32 v132, v24, v132
v_mul_f32_e32 v133, v24, v133
v_mul_f32_e32 v134, v24, v134
v_mul_f32_e32 v135, v24, v135
v_mul_f32_e32 v144, v24, v144
v_mul_f32_e32 v145, v24, v145
v_mul_f32_e32 v146, v24, v146
v_mul_f32_e32 v147, v24, v147
v_mul_f32_e32 v148, v24, v148
v_mul_f32_e32 v149, v24, v149
v_mul_f32_e32 v150, v24, v150
v_mul_f32_e32 v151, v24, v151
v_mul_f32_e32 v160, v24, v160
v_mul_f32_e32 v161, v24, v161
)" R"(
v_mul_f32_e32 v162, v24, v162
v_mul_f32_e32 v163, v24, v163
v_mul_f32_e32 v164, v24, v164
v_mul_f32_e32 v165, v24, v165
v_mul_f32_e32 v166, v24, v166
v_mul_f32_e32 v167, v24, v167
v_mul_f32_e32 v176, v24, v176
v_mul_f32_e32 v177, v24, v177
v_mul_f32_e32 v178, v24, v178
v_mul_f32_e32 v179, v24, v179
v_mul_f32_e32 v180, v24, v180
v_mul_f32_e32 v181, v24, v181
v_mul_f32_e32 v182, v24, v182
v_mul_f32_e32 v183, v24, v183
v_mul_f32_e32 v192, v24, v192
v_mul_f32_e32 v193, v24, v193
v_mul_f32_e32 v194, v24, v194
v_mul_f32_e32 v195, v24, v195
v_mul_f32_e32 v196, v24, v196
v_mul_f32_e32 v197, v24, v197
v_mul_f32_e32 v198, v24, v198
v_mul_f32_e32 v199, v24, v199
v_mul_f32_e32 v208, v24, v208
v_mul_f32_e32 v209, v24, v209
v_mul_f32_e32 v210, v24, v210
v_mul_f32_e32 v211, v24, v211
v_mul_f32_e32 v212, v24, v212
v_mul_f32_e32 v213, v24, v213
v_mul_f32_e32 v214, v24, v214
v_mul_f32_e32 v215, v24, v215
v_mul_f32_e32 v224, v24, v224
v_mul_f32_e32 v225, v24, v225
v_mul_f32_e32 v226, v24, v226
v_mul_f32_e32 v227, v24, v227
v_mul_f32_e32 v228, v24, v228
v_mul_f32_e32 v229, v24, v229
v_mul_f32_e32 v230, v24, v230
v_mul_f32_e32 v231, v24, v231
v_mul_f32_e32 v240, v24, v240
v_mul_f32_e32 v241, v24, v241
v_mul_f32_e32 v242, v24, v242
v_mul_f32_e32 v243, v24, v243
v_mul_f32_e32 v244, v24, v244
v_mul_f32_e32 v245, v24, v245
v_mul_f32_e32 v246, v24, v246
v_mul_f32_e32 v247, v24, v247
v_max_f32_e32 v29, 0, v29
v_sub_f32_e32 v24, 0, v29
v_exp_f32_e32 v24, v24
s_nop 0
v_mul_f32_e32 v13, v24, v13
v_mul_f32_e32 v136, v24, v136
v_mul_f32_e32 v137, v24, v137
v_mul_f32_e32 v138, v24, v138
v_mul_f32_e32 v139, v24, v139
v_mul_f32_e32 v140, v24, v140
v_mul_f32_e32 v141, v24, v141
v_mul_f32_e32 v142, v24, v142
v_mul_f32_e32 v143, v24, v143
v_mul_f32_e32 v152, v24, v152
v_mul_f32_e32 v153, v24, v153
v_mul_f32_e32 v154, v24, v154
v_mul_f32_e32 v155, v24, v155
v_mul_f32_e32 v156, v24, v156
)" R"(
v_mul_f32_e32 v157, v24, v157
v_mul_f32_e32 v158, v24, v158
v_mul_f32_e32 v159, v24, v159
v_mul_f32_e32 v168, v24, v168
v_mul_f32_e32 v169, v24, v169
v_mul_f32_e32 v170, v24, v170
v_mul_f32_e32 v171, v24, v171
v_mul_f32_e32 v172, v24, v172
v_mul_f32_e32 v173, v24, v173
v_mul_f32_e32 v174, v24, v174
v_mul_f32_e32 v175, v24, v175
v_mul_f32_e32 v184, v24, v184
v_mul_f32_e32 v185, v24, v185
v_mul_f32_e32 v186, v24, v186
v_mul_f32_e32 v187, v24, v187
v_mul_f32_e32 v188, v24, v188
v_mul_f32_e32 v189, v24, v189
v_mul_f32_e32 v190, v24, v190
v_mul_f32_e32 v191, v24, v191
v_mul_f32_e32 v200, v24, v200
v_mul_f32_e32 v201, v24, v201
v_mul_f32_e32 v202, v24, v202
v_mul_f32_e32 v203, v24, v203
v_mul_f32_e32 v204, v24, v204
v_mul_f32_e32 v205, v24, v205
v_mul_f32_e32 v206, v24, v206
v_mul_f32_e32 v207, v24, v207
v_mul_f32_e32 v216, v24, v216
v_mul_f32_e32 v217, v24, v217
v_mul_f32_e32 v218, v24, v218
v_mul_f32_e32 v219, v24, v219
v_mul_f32_e32 v220, v24, v220
v_mul_f32_e32 v221, v24, v221
v_mul_f32_e32 v222, v24, v222
v_mul_f32_e32 v223, v24, v223
v_mul_f32_e32 v232, v24, v232
v_mul_f32_e32 v233, v24, v233
v_mul_f32_e32 v234, v24, v234
v_mul_f32_e32 v235, v24, v235
v_mul_f32_e32 v236, v24, v236
v_mul_f32_e32 v237, v24, v237
v_mul_f32_e32 v238, v24, v238
v_mul_f32_e32 v239, v24, v239
v_mul_f32_e32 v248, v24, v248
v_mul_f32_e32 v249, v24, v249
v_mul_f32_e32 v250, v24, v250
v_mul_f32_e32 v251, v24, v251
v_mul_f32_e32 v252, v24, v252
v_mul_f32_e32 v253, v24, v253
v_mul_f32_e32 v254, v24, v254
v_mul_f32_e32 v255, v24, v255
s_branch .LzFIXG0
.LzFIXF0:
v_cmp_lt_f32_e32 vcc, 0xff7fffff, v28
s_nop 1
v_cndmask_b32_e32 v28, 0, v28, vcc
v_cmp_lt_f32_e32 vcc, 0xff7fffff, v29
s_nop 1
v_cndmask_b32_e32 v29, 0, v29, vcc
.LzFIXG0:
v_sub_f32_e32 v16, v16, v28
v_sub_f32_e32 v17, v17, v28
v_sub_f32_e32 v18, v18, v28
v_sub_f32_e32 v19, v19, v28
)" R"(
v_sub_f32_e32 v64, v64, v28
v_sub_f32_e32 v65, v65, v28
v_sub_f32_e32 v66, v66, v28
v_sub_f32_e32 v67, v67, v28
v_sub_f32_e32 v72, v72, v28
v_sub_f32_e32 v73, v73, v28
v_sub_f32_e32 v74, v74, v28
v_sub_f32_e32 v75, v75, v28
v_sub_f32_e32 v80, v80, v28
v_sub_f32_e32 v81, v81, v28
v_sub_f32_e32 v82, v82, v28
v_sub_f32_e32 v83, v83, v28
v_sub_f32_e32 v88, v88, v28
v_sub_f32_e32 v89, v89, v28
v_sub_f32_e32 v90, v90, v28
v_sub_f32_e32 v91, v91, v28
v_sub_f32_e32 v20, v20, v29
v_sub_f32_e32 v21, v21, v29
v_sub_f32_e32 v22, v22, v29
v_sub_f32_e32 v23, v23, v29
v_sub_f32_e32 v68, v68, v29
v_sub_f32_e32 v69, v69, v29
v_sub_f32_e32 v70, v70, v29
v_sub_f32_e32 v71, v71, v29
v_sub_f32_e32 v76, v76, v29
v_sub_f32_e32 v77, v77, v29
v_sub_f32_e32 v78, v78, v29
v_sub_f32_e32 v79, v79, v29
v_sub_f32_e32 v84, v84, v29
v_sub_f32_e32 v85, v85, v29
v_sub_f32_e32 v86, v86, v29
v_sub_f32_e32 v87, v87, v29
v_sub_f32_e32 v92, v92, v29
v_sub_f32_e32 v93, v93, v29
v_sub_f32_e32 v94, v94, v29
v_sub_f32_e32 v95, v95, v29
s_bitcmp0_b32 s88, 1
s_cbranch_scc1 .LzFIXH0
v_sub_f32_e32 v96, v96, v28
v_sub_f32_e32 v97, v97, v28
v_sub_f32_e32 v98, v98, v28
v_sub_f32_e32 v99, v99, v28
v_sub_f32_e32 v104, v104, v28
v_sub_f32_e32 v105, v105, v28
v_sub_f32_e32 v106, v106, v28
v_sub_f32_e32 v107, v107, v28
v_sub_f32_e32 v112, v112, v28
v_sub_f32_e32 v113, v113, v28
v_sub_f32_e32 v114, v114, v28
v_sub_f32_e32 v115, v115, v28
v_sub_f32_e32 v120, v120, v28
v_sub_f32_e32 v121, v121, v28
v_sub_f32_e32 v122, v122, v28
v_sub_f32_e32 v123, v123, v28
v_sub_f32_e32 v100, v100, v29
v_sub_f32_e32 v101, v101, v29
v_sub_f32_e32 v102, v102, v29
v_sub_f32_e32 v103, v103, v29
v_sub_f32_e32 v108, v108, v29
v_sub_f32_e32 v109, v109, v29
v_sub_f32_e32 v110, v110, v29
v_sub_f32_e32 v111, v111, v29
v_sub_f32_e32 v116, v116, v29
v_sub_f32_e32 v117, v117, v29
)" R"(
v_sub_f32_e32 v118, v118, v29
v_sub_f32_e32 v119, v119, v29
v_sub_f32_e32 v124, v124, v29
v_sub_f32_e32 v125, v125, v29
v_sub_f32_e32 v126, v126, v29
v_sub_f32_e32 v127, v127, v29
.LzFIXH0:
v_exp_f32_e32 v32, v64
v_exp_f32_e32 v28, v65
s_nop 0
v_add_f32_e32 v14, v32, v28
Cv 32,32,28
v_exp_f32_e32 v33, v66
v_exp_f32_e32 v29, v67
v_add_f32_e32 v14, v14, v33
v_add_f32_e32 v14, v14, v29
Cv 33,33,29
v_exp_f32_e32 v34, v72
v_exp_f32_e32 v24, v73
v_add_f32_e32 v14, v14, v34
v_add_f32_e32 v14, v14, v24
Cv 34,34,24
v_exp_f32_e32 v35, v74
v_exp_f32_e32 v25, v75
v_add_f32_e32 v14, v14, v35
v_add_f32_e32 v14, v14, v25
Cv 35,35,25
v_exp_f32_e32 v36, v68
v_exp_f32_e32 v26, v69
s_nop 0
v_add_f32_e32 v15, v36, v26
Cv 36,36,26
v_exp_f32_e32 v37, v70
v_exp_f32_e32 v27, v71
v_add_f32_e32 v15, v15, v37
v_add_f32_e32 v15, v15, v27
Cv 37,37,27
v_exp_f32_e32 v38, v76
v_exp_f32_e32 v28, v77
v_add_f32_e32 v15, v15, v38
v_add_f32_e32 v15, v15, v28
Cv 38,38,28
v_exp_f32_e32 v39, v78
v_exp_f32_e32 v29, v79
v_add_f32_e32 v15, v15, v39
v_add_f32_e32 v15, v15, v29
Cv 39,39,29
v_exp_f32_e32 v40, v80
v_exp_f32_e32 v24, v81
v_add_f32_e32 v14, v14, v40
v_add_f32_e32 v14, v14, v24
Cv 40,40,24
v_exp_f32_e32 v41, v82
v_exp_f32_e32 v25, v83
v_add_f32_e32 v14, v14, v41
v_add_f32_e32 v14, v14, v25
Cv 41,41,25
v_exp_f32_e32 v42, v88
v_exp_f32_e32 v26, v89
v_add_f32_e32 v14, v14, v42
v_add_f32_e32 v14, v14, v26
Cv 42,42,26
v_exp_f32_e32 v43, v90
v_exp_f32_e32 v27, v91
)" R"(
v_add_f32_e32 v14, v14, v43
v_add_f32_e32 v14, v14, v27
Cv 43,43,27
v_exp_f32_e32 v44, v84
v_exp_f32_e32 v28, v85
v_add_f32_e32 v15, v15, v44
v_add_f32_e32 v15, v15, v28
Cv 44,44,28
v_exp_f32_e32 v45, v86
v_exp_f32_e32 v29, v87
v_add_f32_e32 v15, v15, v45
v_add_f32_e32 v15, v15, v29
Cv 45,45,29
v_exp_f32_e32 v46, v92
v_exp_f32_e32 v24, v93
v_add_f32_e32 v15, v15, v46
v_add_f32_e32 v15, v15, v24
Cv 46,46,24
v_exp_f32_e32 v47, v94
v_exp_f32_e32 v25, v95
v_add_f32_e32 v15, v15, v47
v_add_f32_e32 v15, v15, v25
Cv 47,47,25
s_nop 1
s_setpc_b64 s[30:31]
.LzMASK1:
s_lshl1_add_u32 s28, s57, s33
s_mov_b32 s29, 0xff800000
s_cmp_gt_u32 s28, 5
s_cbranch_scc1 .LzMASKE1
s_cmp_eq_u32 s28, 5
s_cbranch_scc1 .LzMASKB1
v_mov_b32 v104, s29
v_mov_b32 v105, s29
v_mov_b32 v106, s29
v_mov_b32 v107, s29
v_mov_b32 v112, s29
v_mov_b32 v113, s29
v_mov_b32 v114, s29
v_mov_b32 v115, s29
v_mov_b32 v116, s29
v_mov_b32 v117, s29
v_mov_b32 v118, s29
v_mov_b32 v119, s29
v_mov_b32 v120, s29
v_mov_b32 v121, s29
v_mov_b32 v122, s29
v_mov_b32 v123, s29
v_mov_b32 v124, s29
v_mov_b32 v125, s29
v_mov_b32 v126, s29
v_mov_b32 v127, s29
s_cmp_eq_u32 s28, 4
s_cbranch_scc1 .LzMASKA1
v_mov_b32 v96, s29
v_mov_b32 v97, s29
v_mov_b32 v98, s29
v_mov_b32 v99, s29
v_mov_b32 v100, s29
v_mov_b32 v101, s29
v_mov_b32 v102, s29
v_mov_b32 v103, s29
v_mov_b32 v108, s29
v_mov_b32 v109, s29
)" R"(
v_mov_b32 v110, s29
v_mov_b32 v111, s29
.LzMASKE1:
s_setpc_b64 s[30:31]
.LzMASKA1:
s_mov_b32 exec_lo, 0xf0000
s_mov_b32 exec_hi, 0xfff00ff
v_mov_b32 v96, s29
v_mov_b32 v108, s29
s_mov_b32 exec_lo, 0x1f0001
s_mov_b32 exec_hi, 0x1fff01ff
v_mov_b32 v97, s29
v_mov_b32 v109, s29
s_mov_b32 exec_lo, 0x3f0003
s_mov_b32 exec_hi, 0x3fff03ff
v_mov_b32 v98, s29
v_mov_b32 v110, s29
s_mov_b32 exec_lo, 0x7f0007
s_mov_b32 exec_hi, 0x7fff07ff
v_mov_b32 v99, s29
v_mov_b32 v111, s29
s_mov_b64 exec, -1
s_setpc_b64 s[30:31]
.LzMASKB1:
s_nop 1
v_mov_b32 v120, s29
v_mov_b32 v121, s29
v_mov_b32 v122, s29
v_mov_b32 v123, s29
s_mov_b32 exec_lo, 0xf0000
s_mov_b32 exec_hi, 0xfff00ff
v_mov_b32 v112, s29
v_mov_b32 v124, s29
s_mov_b32 exec_lo, 0x1f0001
s_mov_b32 exec_hi, 0x1fff01ff
v_mov_b32 v113, s29
v_mov_b32 v125, s29
s_mov_b32 exec_lo, 0x3f0003
s_mov_b32 exec_hi, 0x3fff03ff
v_mov_b32 v114, s29
v_mov_b32 v126, s29
s_mov_b32 exec_lo, 0x7f0007
s_mov_b32 exec_hi, 0x7fff07ff
v_mov_b32 v115, s29
v_mov_b32 v127, s29
s_mov_b64 exec, -1
s_setpc_b64 s[30:31]
.LzFIX1:
s_nop 7
v_max_f32_e32 v28, v96, v97
v_max3_f32 v28, v28, v98, v99
v_max3_f32 v28, v28, v104, v105
v_max3_f32 v28, v28, v106, v107
v_max3_f32 v28, v28, v112, v113
v_max3_f32 v28, v28, v114, v115
v_max3_f32 v28, v28, v120, v121
v_max3_f32 v28, v28, v122, v123
v_mov_b32 v24, v28
s_nop 1
v_permlane32_swap_b32_e32 v28, v24
v_max_f32_e32 v28, v28, v24
v_mov_b32 v24, v28
s_nop 1
v_permlane16_swap_b32_e32 v28, v24
)" R"(
v_max_f32_e32 v28, v28, v24
v_max_f32_e32 v29, v100, v101
v_max3_f32 v29, v29, v102, v103
v_max3_f32 v29, v29, v108, v109
v_max3_f32 v29, v29, v110, v111
v_max3_f32 v29, v29, v116, v117
v_max3_f32 v29, v29, v118, v119
v_max3_f32 v29, v29, v124, v125
v_max3_f32 v29, v29, v126, v127
v_mov_b32 v24, v29
s_nop 1
v_permlane32_swap_b32_e32 v29, v24
v_max_f32_e32 v29, v29, v24
v_mov_b32 v24, v29
s_nop 1
v_permlane16_swap_b32_e32 v29, v24
v_max_f32_e32 v29, v29, v24
s_bitcmp1_b32 s88, 0
s_cbranch_scc1 .LzFIXF1
v_max_f32_e32 v28, 0, v28
v_sub_f32_e32 v24, 0, v28
v_exp_f32_e32 v24, v24
s_nop 0
v_mul_f32_e32 v12, v24, v12
v_mul_f32_e32 v128, v24, v128
v_mul_f32_e32 v129, v24, v129
v_mul_f32_e32 v130, v24, v130
v_mul_f32_e32 v131, v24, v131
v_mul_f32_e32 v132, v24, v132
v_mul_f32_e32 v133, v24, v133
v_mul_f32_e32 v134, v24, v134
v_mul_f32_e32 v135, v24, v135
v_mul_f32_e32 v144, v24, v144
v_mul_f32_e32 v145, v24, v145
v_mul_f32_e32 v146, v24, v146
v_mul_f32_e32 v147, v24, v147
v_mul_f32_e32 v148, v24, v148
v_mul_f32_e32 v149, v24, v149
v_mul_f32_e32 v150, v24, v150
v_mul_f32_e32 v151, v24, v151
v_mul_f32_e32 v160, v24, v160
v_mul_f32_e32 v161, v24, v161
v_mul_f32_e32 v162, v24, v162
v_mul_f32_e32 v163, v24, v163
v_mul_f32_e32 v164, v24, v164
v_mul_f32_e32 v165, v24, v165
v_mul_f32_e32 v166, v24, v166
v_mul_f32_e32 v167, v24, v167
v_mul_f32_e32 v176, v24, v176
v_mul_f32_e32 v177, v24, v177
v_mul_f32_e32 v178, v24, v178
v_mul_f32_e32 v179, v24, v179
v_mul_f32_e32 v180, v24, v180
v_mul_f32_e32 v181, v24, v181
v_mul_f32_e32 v182, v24, v182
v_mul_f32_e32 v183, v24, v183
v_mul_f32_e32 v192, v24, v192
v_mul_f32_e32 v193, v24, v193
v_mul_f32_e32 v194, v24, v194
v_mul_f32_e32 v195, v24, v195
v_mul_f32_e32 v196, v24, v196
v_mul_f32_e32 v197, v24, v197
v_mul_f32_e32 v198, v24, v198
v_mul_f32_e32 v199, v24, v199
)" R"(
v_mul_f32_e32 v208, v24, v208
v_mul_f32_e32 v209, v24, v209
v_mul_f32_e32 v210, v24, v210
v_mul_f32_e32 v211, v24, v211
v_mul_f32_e32 v212, v24, v212
v_mul_f32_e32 v213, v24, v213
v_mul_f32_e32 v214, v24, v214
v_mul_f32_e32 v215, v24, v215
v_mul_f32_e32 v224, v24, v224
v_mul_f32_e32 v225, v24, v225
v_mul_f32_e32 v226, v24, v226
v_mul_f32_e32 v227, v24, v227
v_mul_f32_e32 v228, v24, v228
v_mul_f32_e32 v229, v24, v229
v_mul_f32_e32 v230, v24, v230
v_mul_f32_e32 v231, v24, v231
v_mul_f32_e32 v240, v24, v240
v_mul_f32_e32 v241, v24, v241
v_mul_f32_e32 v242, v24, v242
v_mul_f32_e32 v243, v24, v243
v_mul_f32_e32 v244, v24, v244
v_mul_f32_e32 v245, v24, v245
v_mul_f32_e32 v246, v24, v246
v_mul_f32_e32 v247, v24, v247
v_max_f32_e32 v29, 0, v29
v_sub_f32_e32 v24, 0, v29
v_exp_f32_e32 v24, v24
s_nop 0
v_mul_f32_e32 v13, v24, v13
v_mul_f32_e32 v136, v24, v136
v_mul_f32_e32 v137, v24, v137
v_mul_f32_e32 v138, v24, v138
v_mul_f32_e32 v139, v24, v139
v_mul_f32_e32 v140, v24, v140
v_mul_f32_e32 v141, v24, v141
v_mul_f32_e32 v142, v24, v142
v_mul_f32_e32 v143, v24, v143
v_mul_f32_e32 v152, v24, v152
v_mul_f32_e32 v153, v24, v153
v_mul_f32_e32 v154, v24, v154
v_mul_f32_e32 v155, v24, v155
v_mul_f32_e32 v156, v24, v156
v_mul_f32_e32 v157, v24, v157
v_mul_f32_e32 v158, v24, v158
v_mul_f32_e32 v159, v24, v159
v_mul_f32_e32 v168, v24, v168
v_mul_f32_e32 v169, v24, v169
v_mul_f32_e32 v170, v24, v170
v_mul_f32_e32 v171, v24, v171
v_mul_f32_e32 v172, v24, v172
v_mul_f32_e32 v173, v24, v173
v_mul_f32_e32 v174, v24, v174
v_mul_f32_e32 v175, v24, v175
v_mul_f32_e32 v184, v24, v184
v_mul_f32_e32 v185, v24, v185
v_mul_f32_e32 v186, v24, v186
v_mul_f32_e32 v187, v24, v187
v_mul_f32_e32 v188, v24, v188
v_mul_f32_e32 v189, v24, v189
v_mul_f32_e32 v190, v24, v190
v_mul_f32_e32 v191, v24, v191
v_mul_f32_e32 v200, v24, v200
v_mul_f32_e32 v201, v24, v201
v_mul_f32_e32 v202, v24, v202
)" R"(
v_mul_f32_e32 v203, v24, v203
v_mul_f32_e32 v204, v24, v204
v_mul_f32_e32 v205, v24, v205
v_mul_f32_e32 v206, v24, v206
v_mul_f32_e32 v207, v24, v207
v_mul_f32_e32 v216, v24, v216
v_mul_f32_e32 v217, v24, v217
v_mul_f32_e32 v218, v24, v218
v_mul_f32_e32 v219, v24, v219
v_mul_f32_e32 v220, v24, v220
v_mul_f32_e32 v221, v24, v221
v_mul_f32_e32 v222, v24, v222
v_mul_f32_e32 v223, v24, v223
v_mul_f32_e32 v232, v24, v232
v_mul_f32_e32 v233, v24, v233
v_mul_f32_e32 v234, v24, v234
v_mul_f32_e32 v235, v24, v235
v_mul_f32_e32 v236, v24, v236
v_mul_f32_e32 v237, v24, v237
v_mul_f32_e32 v238, v24, v238
v_mul_f32_e32 v239, v24, v239
v_mul_f32_e32 v248, v24, v248
v_mul_f32_e32 v249, v24, v249
v_mul_f32_e32 v250, v24, v250
v_mul_f32_e32 v251, v24, v251
v_mul_f32_e32 v252, v24, v252
v_mul_f32_e32 v253, v24, v253
v_mul_f32_e32 v254, v24, v254
v_mul_f32_e32 v255, v24, v255
s_branch .LzFIXG1
.LzFIXF1:
v_cmp_lt_f32_e32 vcc, 0xff7fffff, v28
s_nop 1
v_cndmask_b32_e32 v28, 0, v28, vcc
v_cmp_lt_f32_e32 vcc, 0xff7fffff, v29
s_nop 1
v_cndmask_b32_e32 v29, 0, v29, vcc
.LzFIXG1:
v_sub_f32_e32 v16, v16, v28
v_sub_f32_e32 v17, v17, v28
v_sub_f32_e32 v18, v18, v28
v_sub_f32_e32 v19, v19, v28
v_sub_f32_e32 v96, v96, v28
v_sub_f32_e32 v97, v97, v28
v_sub_f32_e32 v98, v98, v28
v_sub_f32_e32 v99, v99, v28
v_sub_f32_e32 v104, v104, v28
v_sub_f32_e32 v105, v105, v28
v_sub_f32_e32 v106, v106, v28
v_sub_f32_e32 v107, v107, v28
v_sub_f32_e32 v112, v112, v28
v_sub_f32_e32 v113, v113, v28
v_sub_f32_e32 v114, v114, v28
v_sub_f32_e32 v115, v115, v28
v_sub_f32_e32 v120, v120, v28
v_sub_f32_e32 v121, v121, v28
v_sub_f32_e32 v122, v122, v28
v_sub_f32_e32 v123, v123, v28
v_sub_f32_e32 v20, v20, v29
v_sub_f32_e32 v21, v21, v29
v_sub_f32_e32 v22, v22, v29
v_sub_f32_e32 v23, v23, v29
v_sub_f32_e32 v100, v100, v29
v_sub_f32_e32 v101, v101, v29
)" R"(
v_sub_f32_e32 v102, v102, v29
v_sub_f32_e32 v103, v103, v29
v_sub_f32_e32 v108, v108, v29
v_sub_f32_e32 v109, v109, v29
v_sub_f32_e32 v110, v110, v29
v_sub_f32_e32 v111, v111, v29
v_sub_f32_e32 v116, v116, v29
v_sub_f32_e32 v117, v117, v29
v_sub_f32_e32 v118, v118, v29
v_sub_f32_e32 v119, v119, v29
v_sub_f32_e32 v124, v124, v29
v_sub_f32_e32 v125, v125, v29
v_sub_f32_e32 v126, v126, v29
v_sub_f32_e32 v127, v127, v29
s_bitcmp0_b32 s88, 1
s_cbranch_scc1 .LzFIXH1
v_sub_f32_e32 v64, v64, v28
v_sub_f32_e32 v65, v65, v28
v_sub_f32_e32 v66, v66, v28
v_sub_f32_e32 v67, v67, v28
v_sub_f32_e32 v72, v72, v28
v_sub_f32_e32 v73, v73, v28
v_sub_f32_e32 v74, v74, v28
v_sub_f32_e32 v75, v75, v28
v_sub_f32_e32 v80, v80, v28
v_sub_f32_e32 v81, v81, v28
v_sub_f32_e32 v82, v82, v28
v_sub_f32_e32 v83, v83, v28
v_sub_f32_e32 v88, v88, v28
v_sub_f32_e32 v89, v89, v28
v_sub_f32_e32 v90, v90, v28
v_sub_f32_e32 v91, v91, v28
v_sub_f32_e32 v68, v68, v29
v_sub_f32_e32 v69, v69, v29
v_sub_f32_e32 v70, v70, v29
v_sub_f32_e32 v71, v71, v29
v_sub_f32_e32 v76, v76, v29
v_sub_f32_e32 v77, v77, v29
v_sub_f32_e32 v78, v78, v29
v_sub_f32_e32 v79, v79, v29
v_sub_f32_e32 v84, v84, v29
v_sub_f32_e32 v85, v85, v29
v_sub_f32_e32 v86, v86, v29
v_sub_f32_e32 v87, v87, v29
v_sub_f32_e32 v92, v92, v29
v_sub_f32_e32 v93, v93, v29
v_sub_f32_e32 v94, v94, v29
v_sub_f32_e32 v95, v95, v29
.LzFIXH1:
v_exp_f32_e32 v48, v96
v_exp_f32_e32 v26, v97
s_nop 0
v_add_f32_e32 v14, v48, v26
Cv 48,48,26
v_exp_f32_e32 v49, v98
v_exp_f32_e32 v27, v99
v_add_f32_e32 v14, v14, v49
v_add_f32_e32 v14, v14, v27
Cv 49,49,27
v_exp_f32_e32 v50, v104
v_exp_f32_e32 v28, v105
v_add_f32_e32 v14, v14, v50
v_add_f32_e32 v14, v14, v28
Cv 50,50,28
)" R"(
v_exp_f32_e32 v51, v106
v_exp_f32_e32 v29, v107
v_add_f32_e32 v14, v14, v51
v_add_f32_e32 v14, v14, v29
Cv 51,51,29
v_exp_f32_e32 v52, v100
v_exp_f32_e32 v24, v101
s_nop 0
v_add_f32_e32 v15, v52, v24
Cv 52,52,24
v_exp_f32_e32 v53, v102
v_exp_f32_e32 v25, v103
v_add_f32_e32 v15, v15, v53
v_add_f32_e32 v15, v15, v25
Cv 53,53,25
v_exp_f32_e32 v54, v108
v_exp_f32_e32 v26, v109
v_add_f32_e32 v15, v15, v54
v_add_f32_e32 v15, v15, v26
Cv 54,54,26
v_exp_f32_e32 v55, v110
v_exp_f32_e32 v27, v111
v_add_f32_e32 v15, v15, v55
v_add_f32_e32 v15, v15, v27
Cv 55,55,27
v_exp_f32_e32 v56, v112
v_exp_f32_e32 v28, v113
v_add_f32_e32 v14, v14, v56
v_add_f32_e32 v14, v14, v28
Cv 56,56,28
v_exp_f32_e32 v57, v114
v_exp_f32_e32 v29, v115
v_add_f32_e32 v14, v14, v57
v_add_f32_e32 v14, v14, v29
Cv 57,57,29
v_exp_f32_e32 v58, v120
v_exp_f32_e32 v24, v121
v_add_f32_e32 v14, v14, v58
v_add_f32_e32 v14, v14, v24
Cv 58,58,24
v_exp_f32_e32 v59, v122
v_exp_f32_e32 v25, v123
v_add_f32_e32 v14, v14, v59
v_add_f32_e32 v14, v14, v25
Cv 59,59,25
v_exp_f32_e32 v60, v116
v_exp_f32_e32 v26, v117
v_add_f32_e32 v15, v15, v60
v_add_f32_e32 v15, v15, v26
Cv 60,60,26
v_exp_f32_e32 v61, v118
v_exp_f32_e32 v27, v119
v_add_f32_e32 v15, v15, v61
v_add_f32_e32 v15, v15, v27
Cv 61,61,27
v_exp_f32_e32 v62, v124
v_exp_f32_e32 v28, v125
v_add_f32_e32 v15, v15, v62
v_add_f32_e32 v15, v15, v28
Cv 62,62,28
v_exp_f32_e32 v63, v126
v_exp_f32_e32 v29, v127
v_add_f32_e32 v15, v15, v63
v_add_f32_e32 v15, v15, v29
)" R"(
Cv 63,63,29
s_nop 1
s_setpc_b64 s[30:31]
.Lzsp1:
s_sub_u32 s53, s53, 1
s_cbranch_scc1 .Lzsq1
s_sleep 1
buffer_load_dword v4, v2, s[20:23], s85 offen sc0 sc1
s_waitcnt vmcnt(0)
v_xor_b32 v4, s3, v4
v_cmp_gt_u32_e32 vcc, 2, v4
s_andn2_b32 s28, 0xff, vcc_lo
s_cbranch_scc1 .Lzsp1
s_branch .Lzok1
.Lzsq1:
s_mov_b32 s53, 0
s_branch .Lzok1
.Lzsp2:
s_sub_u32 s53, s53, 1
s_cbranch_scc1 .Lzsq2
s_sleep 1
buffer_load_dword v4, v7, s[20:23], s52 offen offset:0 sc0 sc1
s_waitcnt vmcnt(0)
v_cmp_eq_u32_e32 vcc, s3, v4
s_andn2_b64 s[28:29], 1, vcc
s_cbranch_scc1 .Lzsp2
s_branch .Lzok2
.Lzsq2:
s_mov_b32 s53, 0
s_branch .Lzok2
.Lzsp3:
s_sub_u32 s53, s53, 1
s_cbranch_scc1 .Lzsq3
s_sleep 1
buffer_load_dword v4, v2, s[20:23], s85 offen sc0 sc1
s_waitcnt vmcnt(0)
v_xor_b32 v4, s3, v4
v_cmp_gt_u32_e32 vcc, 2, v4
s_andn2_b32 s28, 0xff, vcc_lo
s_cbranch_scc1 .Lzsp3
s_branch .Lzok3
.Lzsq3:
s_mov_b32 s53, 0
s_branch .Lzok3
.Lzsp4:
s_sub_u32 s53, s53, 1
s_cbranch_scc1 .Lzsq4
s_sleep 1
buffer_load_dword v4, v7, s[20:23], s52 offen offset:0 sc0 sc1
s_waitcnt vmcnt(0)
v_cmp_eq_u32_e32 vcc, s3, v4
s_andn2_b64 s[28:29], 1, vcc
s_cbranch_scc1 .Lzsp4
s_branch .Lzok4
.Lzsq4:
s_mov_b32 s53, 0
s_branch .Lzok4
.Lzsp5:
s_sub_u32 s53, s53, 1
s_cbranch_scc1 .Lzsq5
s_sleep 1
buffer_load_dword v4, v5, s[20:23], s50 offen offset:0 sc0 sc1
s_waitcnt vmcnt(0)
v_cmp_eq_u32_e32 vcc, s3, v4
)" R"(
s_andn2_b64 s[28:29], -1, vcc
s_cbranch_scc1 .Lzsp5
s_branch .Lzok5
.Lzsq5:
s_mov_b32 s53, 0
s_branch .Lzok5
.Lzsp6:
s_sub_u32 s53, s53, 1
s_cbranch_scc1 .Lzsq6
s_sleep 1
buffer_load_dword v4, v5, s[20:23], s50 offen offset:0 sc0 sc1
s_waitcnt vmcnt(0)
v_cmp_eq_u32_e32 vcc, s3, v4
s_andn2_b64 s[28:29], -1, vcc
s_cbranch_scc1 .Lzsp6
s_branch .Lzok6
.Lzsq6:
s_mov_b32 s53, 0
s_branch .Lzok6
.Lzsp7:
s_sub_u32 s53, s53, 1
s_cbranch_scc1 .Lzsq7
s_sleep 1
buffer_load_dwordx2 v[24:25], v7, s[20:23], s83 offen sc0 sc1
s_waitcnt vmcnt(0)
v_cmp_eq_u32_e32 vcc, s3, v25
s_andn2_b64 s[28:29], 1, vcc
s_cbranch_scc1 .Lzsp7
s_branch .Lzok7
.Lzsq7:
s_mov_b32 s53, 0
s_branch .Lzok7
.LzT2S:
s_cmp_lg_u32 s33, 0
s_cbranch_scc1 .LzT2E
.LzT2:
s_mov_b32 s28, 0xc100000
buffer_load_dwordx2 v[32:33], v1, s[20:23], s28 offen offset:0 sc0 sc1
buffer_load_dwordx2 v[34:35], v1, s[20:23], s28 offen offset:1024 sc0 sc1
buffer_load_dwordx2 v[36:37], v1, s[20:23], s28 offen offset:2048 sc0 sc1
buffer_load_dwordx2 v[38:39], v1, s[20:23], s28 offen offset:3072 sc0 sc1
s_mov_b32 s28, 0xc101000
buffer_load_dwordx2 v[40:41], v1, s[20:23], s28 offen offset:0 sc0 sc1
buffer_load_dwordx2 v[42:43], v1, s[20:23], s28 offen offset:1024 sc0 sc1
buffer_load_dwordx2 v[44:45], v1, s[20:23], s28 offen offset:2048 sc0 sc1
buffer_load_dwordx2 v[46:47], v1, s[20:23], s28 offen offset:3072 sc0 sc1
s_mov_b32 s28, 0xc102000
buffer_load_dwordx2 v[48:49], v1, s[20:23], s28 offen offset:0 sc0 sc1
buffer_load_dwordx2 v[50:51], v1, s[20:23], s28 offen offset:1024 sc0 sc1
buffer_load_dwordx2 v[52:53], v1, s[20:23], s28 offen offset:2048 sc0 sc1
buffer_load_dwordx2 v[54:55], v1, s[20:23], s28 offen offset:3072 sc0 sc1
s_mov_b32 s28, 0xc103000
buffer_load_dwordx2 v[56:57], v1, s[20:23], s28 offen offset:0 sc0 sc1
buffer_load_dwordx2 v[58:59], v1, s[20:23], s28 offen offset:1024 sc0 sc1
buffer_load_dwordx2 v[60:61], v1, s[20:23], s28 offen offset:2048 sc0 sc1
buffer_load_dwordx2 v[62:63], v1, s[20:23], s28 offen offset:3072 sc0 sc1
s_mov_b32 s28, 0xc104000
buffer_load_dwordx2 v[64:65], v1, s[20:23], s28 offen offset:0 sc0 sc1
buffer_load_dwordx2 v[66:67], v1, s[20:23], s28 offen offset:1024 sc0 sc1
buffer_load_dwordx2 v[68:69], v1, s[20:23], s28 offen offset:2048 sc0 sc1
buffer_load_dwordx2 v[70:71], v1, s[20:23], s28 offen offset:3072 sc0 sc1
s_mov_b32 s28, 0xc105000
buffer_load_dwordx2 v[72:73], v1, s[20:23], s28 offen offset:0 sc0 sc1
buffer_load_dwordx2 v[74:75], v1, s[20:23], s28 offen offset:1024 sc0 sc1
)" R"(
buffer_load_dwordx2 v[76:77], v1, s[20:23], s28 offen offset:2048 sc0 sc1
buffer_load_dwordx2 v[78:79], v1, s[20:23], s28 offen offset:3072 sc0 sc1
s_mov_b32 s28, 0xc106000
buffer_load_dwordx2 v[80:81], v1, s[20:23], s28 offen offset:0 sc0 sc1
buffer_load_dwordx2 v[82:83], v1, s[20:23], s28 offen offset:1024 sc0 sc1
buffer_load_dwordx2 v[84:85], v1, s[20:23], s28 offen offset:2048 sc0 sc1
buffer_load_dwordx2 v[86:87], v1, s[20:23], s28 offen offset:3072 sc0 sc1
s_mov_b32 s28, 0xc107000
buffer_load_dwordx2 v[88:89], v1, s[20:23], s28 offen offset:0 sc0 sc1
buffer_load_dwordx2 v[90:91], v1, s[20:23], s28 offen offset:1024 sc0 sc1
buffer_load_dwordx2 v[92:93], v1, s[20:23], s28 offen offset:2048 sc0 sc1
buffer_load_dwordx2 v[94:95], v1, s[20:23], s28 offen offset:3072 sc0 sc1
v_mov_b32 v26, 0
v_mov_b32 v27, -1
s_mov_b32 s28, 0xc108c00
buffer_store_dword v26, v7, s[20:23], s28 offen sc0 sc1
buffer_store_dword v26, v7, s[20:23], s28 offen offset:128 sc0 sc1
s_waitcnt vmcnt(0)
v_max3_u32 v26, v26, v33, v35
v_min3_u32 v27, v27, v33, v35
v_max3_u32 v26, v26, v37, v39
v_min3_u32 v27, v27, v37, v39
v_max3_u32 v26, v26, v41, v43
v_min3_u32 v27, v27, v41, v43
v_max3_u32 v26, v26, v45, v47
v_min3_u32 v27, v27, v45, v47
v_max3_u32 v26, v26, v49, v51
v_min3_u32 v27, v27, v49, v51
v_max3_u32 v26, v26, v53, v55
v_min3_u32 v27, v27, v53, v55
v_max3_u32 v26, v26, v57, v59
v_min3_u32 v27, v27, v57, v59
v_max3_u32 v26, v26, v61, v63
v_min3_u32 v27, v27, v61, v63
v_max3_u32 v26, v26, v65, v67
v_min3_u32 v27, v27, v65, v67
v_max3_u32 v26, v26, v69, v71
v_min3_u32 v27, v27, v69, v71
v_max3_u32 v26, v26, v73, v75
v_min3_u32 v27, v27, v73, v75
v_max3_u32 v26, v26, v77, v79
v_min3_u32 v27, v27, v77, v79
v_max3_u32 v26, v26, v81, v83
v_min3_u32 v27, v27, v81, v83
v_max3_u32 v26, v26, v85, v87
v_min3_u32 v27, v27, v85, v87
v_max3_u32 v26, v26, v89, v91
v_min3_u32 v27, v27, v89, v91
v_max3_u32 v26, v26, v93, v95
v_min3_u32 v27, v27, v93, v95
v_cmp_ne_u32_e32 vcc, s3, v26
v_cmp_ne_u32_e64 s[28:29], s3, v27
s_or_b64 vcc, vcc, s[28:29]
s_cbranch_scc0 .LzT2G
s_sub_u32 s53, s53, 1
s_cbranch_scc0 .LzT2
s_mov_b32 s53, 0
.LzT2G:
v_max_u32 v26, v32, v34
v_min_u32 v27, v32, v34
v_med3_u32 v27, v26, v27, v36
v_max_u32 v26, v26, v36
v_med3_u32 v27, v26, v27, v38
v_max_u32 v26, v26, v38
)" R"(
v_med3_u32 v27, v26, v27, v40
v_max_u32 v26, v26, v40
v_med3_u32 v27, v26, v27, v42
v_max_u32 v26, v26, v42
v_med3_u32 v27, v26, v27, v44
v_max_u32 v26, v26, v44
v_med3_u32 v27, v26, v27, v46
v_max_u32 v26, v26, v46
v_med3_u32 v27, v26, v27, v48
v_max_u32 v26, v26, v48
v_med3_u32 v27, v26, v27, v50
v_max_u32 v26, v26, v50
v_med3_u32 v27, v26, v27, v52
v_max_u32 v26, v26, v52
v_med3_u32 v27, v26, v27, v54
v_max_u32 v26, v26, v54
v_med3_u32 v27, v26, v27, v56
v_max_u32 v26, v26, v56
v_med3_u32 v27, v26, v27, v58
v_max_u32 v26, v26, v58
v_med3_u32 v27, v26, v27, v60
v_max_u32 v26, v26, v60
v_med3_u32 v27, v26, v27, v62
v_max_u32 v26, v26, v62
v_med3_u32 v27, v26, v27, v64
v_max_u32 v26, v26, v64
v_med3_u32 v27, v26, v27, v66
v_max_u32 v26, v26, v66
v_med3_u32 v27, v26, v27, v68
v_max_u32 v26, v26, v68
v_med3_u32 v27, v26, v27, v70
v_max_u32 v26, v26, v70
v_med3_u32 v27, v26, v27, v72
v_max_u32 v26, v26, v72
v_med3_u32 v27, v26, v27, v74
v_max_u32 v26, v26, v74
v_med3_u32 v27, v26, v27, v76
v_max_u32 v26, v26, v76
v_med3_u32 v27, v26, v27, v78
v_max_u32 v26, v26, v78
v_med3_u32 v27, v26, v27, v80
v_max_u32 v26, v26, v80
v_med3_u32 v27, v26, v27, v82
v_max_u32 v26, v26, v82
v_med3_u32 v27, v26, v27, v84
v_max_u32 v26, v26, v84
v_med3_u32 v27, v26, v27, v86
v_max_u32 v26, v26, v86
v_med3_u32 v27, v26, v27, v88
v_max_u32 v26, v26, v88
v_med3_u32 v27, v26, v27, v90
v_max_u32 v26, v26, v90
v_med3_u32 v27, v26, v27, v92
v_max_u32 v26, v26, v92
v_med3_u32 v27, v26, v27, v94
v_max_u32 v26, v26, v94
v_mov_b32 v24, v26
v_mov_b32 v25, v27
s_nop 1
v_permlane32_swap_b32_e32 v26, v24
v_permlane32_swap_b32_e32 v27, v25
v_max_u32 v27, v27, v25
v_med3_u32 v27, v26, v27, v24
v_max_u32 v26, v26, v24
)" R"(
v_mov_b32 v24, v26
v_mov_b32 v25, v27
s_nop 1
v_permlane16_swap_b32_e32 v26, v24
v_permlane16_swap_b32_e32 v27, v25
v_max_u32 v27, v27, v25
v_med3_u32 v27, v26, v27, v24
v_max_u32 v26, v26, v24
s_nop 1
v_mov_b32_dpp v24, v26 row_ror:8 row_mask:0xf bank_mask:0xf
v_mov_b32_dpp v25, v27 row_ror:8 row_mask:0xf bank_mask:0xf
v_max_u32 v27, v27, v25
v_med3_u32 v27, v26, v27, v24
v_max_u32 v26, v26, v24
s_nop 1
v_mov_b32_dpp v24, v26 row_ror:4 row_mask:0xf bank_mask:0xf
v_mov_b32_dpp v25, v27 row_ror:4 row_mask:0xf bank_mask:0xf
v_max_u32 v27, v27, v25
v_med3_u32 v27, v26, v27, v24
v_max_u32 v26, v26, v24
s_nop 1
v_mov_b32_dpp v24, v26 row_ror:2 row_mask:0xf bank_mask:0xf
v_mov_b32_dpp v25, v27 row_ror:2 row_mask:0xf bank_mask:0xf
v_max_u32 v27, v27, v25
v_med3_u32 v27, v26, v27, v24
v_max_u32 v26, v26, v24
s_nop 1
v_mov_b32_dpp v24, v26 row_ror:1 row_mask:0xf bank_mask:0xf
v_mov_b32_dpp v25, v27 row_ror:1 row_mask:0xf bank_mask:0xf
v_max_u32 v27, v27, v25
v_med3_u32 v27, v26, v27, v24
v_max_u32 v26, v26, v24
v_and_b32 v26, 0xffff, v26
v_lshl_or_b32 v26, v27, 16, v26
v_not_b32 v24, v26
v_mov_b32 v25, s3
s_mov_b32 s28, 0xc108000
buffer_store_dwordx2 v[24:25], v7, s[20:23], s28 offen sc0 sc1
s_branch .LzT2E
.LzA7:
s_waitcnt vmcnt(0)
s_nop 7
v_mul_f32_e32 v96, s54, v96
v_mul_f32_e32 v97, s54, v97
Cv 96,96,97
v_accvgpr_write_b32 a112, v96
v_mul_f32_e32 v98, s54, v98
v_mul_f32_e32 v99, s54, v99
Cv 98,98,99
v_accvgpr_write_b32 a113, v98
v_mul_f32_e32 v100, s54, v100
v_mul_f32_e32 v101, s54, v101
Cv 100,100,101
v_accvgpr_write_b32 a114, v100
v_mul_f32_e32 v102, s54, v102
v_mul_f32_e32 v103, s54, v103
Cv 102,102,103
v_accvgpr_write_b32 a115, v102
v_mul_f32_e32 v104, s54, v104
v_mul_f32_e32 v105, s54, v105
Cv 104,104,105
v_accvgpr_write_b32 a116, v104
v_mul_f32_e32 v106, s54, v106
v_mul_f32_e32 v107, s54, v107
)" R"(
Cv 106,106,107
v_accvgpr_write_b32 a117, v106
v_mul_f32_e32 v108, s54, v108
v_mul_f32_e32 v109, s54, v109
Cv 108,108,109
v_accvgpr_write_b32 a118, v108
v_mul_f32_e32 v110, s54, v110
v_mul_f32_e32 v111, s54, v111
Cv 110,110,111
v_accvgpr_write_b32 a119, v110
v_mul_f32_e32 v112, s54, v112
v_mul_f32_e32 v113, s54, v113
Cv 112,112,113
v_accvgpr_write_b32 a120, v112
v_mul_f32_e32 v114, s54, v114
v_mul_f32_e32 v115, s54, v115
Cv 114,114,115
v_accvgpr_write_b32 a121, v114
v_mul_f32_e32 v116, s54, v116
v_mul_f32_e32 v117, s54, v117
Cv 116,116,117
v_accvgpr_write_b32 a122, v116
v_mul_f32_e32 v118, s54, v118
v_mul_f32_e32 v119, s54, v119
Cv 118,118,119
v_accvgpr_write_b32 a123, v118
v_mul_f32_e32 v120, s54, v120
v_mul_f32_e32 v121, s54, v121
Cv 120,120,121
v_accvgpr_write_b32 a124, v120
v_mul_f32_e32 v122, s54, v122
v_mul_f32_e32 v123, s54, v123
Cv 122,122,123
v_accvgpr_write_b32 a125, v122
v_mul_f32_e32 v124, s54, v124
v_mul_f32_e32 v125, s54, v125
Cv 124,124,125
v_accvgpr_write_b32 a126, v124
v_mul_f32_e32 v126, s54, v126
v_mul_f32_e32 v127, s54, v127
Cv 126,126,127
v_accvgpr_write_b32 a127, v126
v_accvgpr_mov_b32 a64, a224
v_accvgpr_mov_b32 a65, a225
v_accvgpr_mov_b32 a66, a226
v_accvgpr_mov_b32 a67, a227
v_accvgpr_mov_b32 a68, a228
v_accvgpr_mov_b32 a69, a229
v_accvgpr_mov_b32 a70, a230
v_accvgpr_mov_b32 a71, a231
v_accvgpr_mov_b32 a72, a232
v_accvgpr_mov_b32 a73, a233
v_accvgpr_mov_b32 a74, a234
v_accvgpr_mov_b32 a75, a235
v_accvgpr_mov_b32 a76, a236
v_accvgpr_mov_b32 a77, a237
v_accvgpr_mov_b32 a78, a238
v_accvgpr_mov_b32 a79, a239
v_accvgpr_mov_b32 a80, a240
v_accvgpr_mov_b32 a81, a241
v_accvgpr_mov_b32 a82, a242
v_accvgpr_mov_b32 a83, a243
v_accvgpr_mov_b32 a84, a244
v_accvgpr_mov_b32 a85, a245
)" R"(
v_accvgpr_mov_b32 a86, a246
v_accvgpr_mov_b32 a87, a247
v_accvgpr_mov_b32 a88, a248
v_accvgpr_mov_b32 a89, a249
v_accvgpr_mov_b32 a90, a250
v_accvgpr_mov_b32 a91, a251
v_accvgpr_mov_b32 a92, a252
v_accvgpr_mov_b32 a93, a253
v_accvgpr_mov_b32 a94, a254
v_accvgpr_mov_b32 a95, a255
v_accvgpr_mov_b32 a96, a192
v_accvgpr_mov_b32 a97, a193
v_accvgpr_mov_b32 a98, a194
v_accvgpr_mov_b32 a99, a195
v_accvgpr_mov_b32 a100, a196
v_accvgpr_mov_b32 a101, a197
v_accvgpr_mov_b32 a102, a198
v_accvgpr_mov_b32 a103, a199
v_accvgpr_mov_b32 a104, a200
v_accvgpr_mov_b32 a105, a201
v_accvgpr_mov_b32 a106, a202
v_accvgpr_mov_b32 a107, a203
v_accvgpr_mov_b32 a108, a204
v_accvgpr_mov_b32 a109, a205
v_accvgpr_mov_b32 a110, a206
v_accvgpr_mov_b32 a111, a207
s_mov_b32 s94, 2
s_nop 1
s_branch .LzA7R
.Lzsp8:
s_sub_u32 s53, s53, 1
s_cbranch_scc1 .Lzsq8
s_sleep 1
buffer_load_dword v4, v5, s[20:23], s50 offen offset:0 sc0 sc1
s_waitcnt vmcnt(0)
v_cmp_eq_u32_e32 vcc, s3, v4
s_andn2_b64 s[28:29], -1, vcc
s_cbranch_scc1 .Lzsp8
s_branch .Lzok8
.Lzsq8:
s_mov_b32 s53, 0
s_branch .Lzok8
.Lzmk0:
s_call_b64 s[30:31], .LzMASK0
s_branch .Lzmr0
.Lzfx0:
s_mov_b32 s88, 3
s_call_b64 s[30:31], .LzFIX0
s_branch .Lzfr0
.Lzmk1:
s_call_b64 s[30:31], .LzMASK1
s_branch .Lzmr1
.Lzsp9:
s_sub_u32 s53, s53, 1
s_cbranch_scc1 .Lzsq9
s_sleep 1
buffer_load_dword v4, v5, s[20:23], s50 offen offset:0 sc0 sc1
s_waitcnt vmcnt(0)
v_cmp_eq_u32_e32 vcc, s3, v4
s_andn2_b64 s[28:29], -1, vcc
s_cbranch_scc1 .Lzsp9
s_branch .Lzok9
.Lzsq9:
s_mov_b32 s53, 0
)" R"(
s_branch .Lzok9
.Lzfx1:
s_mov_b32 s88, 2
s_call_b64 s[30:31], .LzFIX1
s_branch .Lzfr1
.Lzmk2:
s_call_b64 s[30:31], .LzMASK0
s_branch .Lzmr2
.Lzfx2:
s_mov_b32 s88, 2
s_call_b64 s[30:31], .LzFIX0
s_branch .Lzfr2
.Lzmk3:
s_call_b64 s[30:31], .LzMASK1
s_branch .Lzmr3
.Lzsp10:
s_sub_u32 s53, s53, 1
s_cbranch_scc1 .Lzsq10
s_sleep 1
buffer_load_dword v4, v5, s[20:23], s50 offen offset:0 sc0 sc1
s_waitcnt vmcnt(0)
v_cmp_eq_u32_e32 vcc, s3, v4
s_andn2_b64 s[28:29], -1, vcc
s_cbranch_scc1 .Lzsp10
s_branch .Lzok10
.Lzsq10:
s_mov_b32 s53, 0
s_branch .Lzok10
.Lzfx3:
s_mov_b32 s88, 0
s_call_b64 s[30:31], .LzFIX1
s_branch .Lzfr3
.Lzmk4:
s_call_b64 s[30:31], .LzMASK0
s_branch .Lzmr4
.Lzfx4:
s_mov_b32 s88, 3
s_call_b64 s[30:31], .LzFIX0
s_branch .Lzfr4
.Lzmk5:
s_call_b64 s[30:31], .LzMASK1
s_branch .Lzmr5
.Lzfx5:
s_mov_b32 s88, 0
s_call_b64 s[30:31], .LzFIX1
s_branch .Lzfr5
.Lzsp11:
s_sub_u32 s53, s53, 1
s_cbranch_scc1 .Lzsq11
s_sleep 1
buffer_load_dword v4, v7, s[20:23], s52 offen offset:0 sc0 sc1
s_waitcnt vmcnt(0)
v_cmp_eq_u32_e32 vcc, s3, v4
s_andn2_b64 s[28:29], 1, vcc
s_cbranch_scc1 .Lzsp11
s_branch .Lzok11
.Lzsq11:
s_mov_b32 s53, 0
s_branch .Lzok11
.Lzsp12:
s_sub_u32 s53, s53, 1
s_cbranch_scc1 .Lzsq12
s_sleep 1
buffer_load_dword v4, v7, s[20:23], s52 offen offset:128 sc0 sc1
)" R"(
s_waitcnt vmcnt(0)
v_cmp_eq_u32_e32 vcc, s3, v4
s_andn2_b64 s[28:29], 1, vcc
s_cbranch_scc1 .Lzsp12
s_branch .Lzok12
.Lzsq12:
s_mov_b32 s53, 0
s_branch .Lzok12
.Lzsp13:
s_sub_u32 s53, s53, 1
s_cbranch_scc1 .Lzsq13
s_sleep 1
buffer_load_dword v4, v2, s[20:23], s85 offen sc0 sc1
s_waitcnt vmcnt(0)
v_xor_b32 v4, s3, v4
v_cmp_gt_u32_e32 vcc, 2, v4
s_andn2_b32 s28, 0xff, vcc_lo
s_cbranch_scc1 .Lzsp13
s_branch .Lzok13
.Lzsq13:
s_mov_b32 s53, 0
s_branch .Lzok13
.Lzsp14:
s_sub_u32 s53, s53, 1
s_cbranch_scc1 .Lzsq14
s_sleep 1
buffer_load_dword v4, v5, s[20:23], s50 offen offset:0 sc0 sc1
s_waitcnt vmcnt(0)
v_cmp_eq_u32_e32 vcc, s3, v4
s_andn2_b64 s[28:29], -1, vcc
s_cbranch_scc1 .Lzsp14
s_branch .Lzok14
.Lzsq14:
s_mov_b32 s53, 0
s_branch .Lzok14
.Lzsp15:
s_sub_u32 s53, s53, 1
s_cbranch_scc1 .Lzsq15
s_sleep 1
buffer_load_dword v4, v5, s[20:23], s50 offen offset:0 sc0 sc1
s_waitcnt vmcnt(0)
v_cmp_eq_u32_e32 vcc, s3, v4
s_andn2_b64 s[28:29], -1, vcc
s_cbranch_scc1 .Lzsp15
s_branch .Lzok15
.Lzsq15:
s_mov_b32 s53, 0
s_branch .Lzok15
.Lzsp16:
s_sub_u32 s53, s53, 1
s_cbranch_scc1 .Lzsq16
s_sleep 1
buffer_load_dword v4, v5, s[20:23], s50 offen offset:0 sc0 sc1
s_waitcnt vmcnt(0)
v_cmp_eq_u32_e32 vcc, s3, v4
s_andn2_b64 s[28:29], -1, vcc
s_cbranch_scc1 .Lzsp16
s_branch .Lzok16
.Lzsq16:
s_mov_b32 s53, 0
s_branch .Lzok16
.Lzsp17:
s_sub_u32 s53, s53, 1
s_cbranch_scc1 .Lzsq17
)" R"(
s_sleep 1
buffer_load_dword v4, v5, s[20:23], s50 offen offset:0 sc0 sc1
s_waitcnt vmcnt(0)
v_cmp_eq_u32_e32 vcc, s3, v4
s_andn2_b64 s[28:29], -1, vcc
s_cbranch_scc1 .Lzsp17
s_branch .Lzok17
.Lzsq17:
s_mov_b32 s53, 0
s_branch .Lzok17
.Lzsp18:
s_sub_u32 s53, s53, 1
s_cbranch_scc1 .Lzsq18
s_sleep 1
buffer_load_dword v4, v5, s[20:23], s50 offen offset:0 sc0 sc1
s_waitcnt vmcnt(0)
v_cmp_eq_u32_e32 vcc, s3, v4
s_andn2_b64 s[28:29], -1, vcc
s_cbranch_scc1 .Lzsp18
s_branch .Lzok18
.Lzsq18:
s_mov_b32 s53, 0
s_branch .Lzok18
.Lzfx6:
s_mov_b32 s88, 3
s_call_b64 s[30:31], .LzFIX0
s_branch .Lzfr6
.Lzsp19:
s_sub_u32 s53, s53, 1
s_cbranch_scc1 .Lzsq19
s_sleep 1
buffer_load_dword v4, v5, s[20:23], s50 offen offset:0 sc0 sc1
s_waitcnt vmcnt(0)
v_cmp_eq_u32_e32 vcc, s3, v4
s_andn2_b64 s[28:29], -1, vcc
s_cbranch_scc1 .Lzsp19
s_branch .Lzok19
.Lzsq19:
s_mov_b32 s53, 0
s_branch .Lzok19
.Lzfx7:
s_mov_b32 s88, 2
s_call_b64 s[30:31], .LzFIX1
s_branch .Lzfr7
.Lzmk6:
s_call_b64 s[30:31], .LzMASK0
s_branch .Lzmr6
.Lzfx8:
s_mov_b32 s88, 2
s_call_b64 s[30:31], .LzFIX0
s_branch .Lzfr8
.Lzmk7:
s_call_b64 s[30:31], .LzMASK1
s_branch .Lzmr7
.Lzsp20:
s_sub_u32 s53, s53, 1
s_cbranch_scc1 .Lzsq20
s_sleep 1
buffer_load_dword v4, v5, s[20:23], s50 offen offset:0 sc0 sc1
s_waitcnt vmcnt(0)
v_cmp_eq_u32_e32 vcc, s3, v4
s_andn2_b64 s[28:29], -1, vcc
s_cbranch_scc1 .Lzsp20
s_branch .Lzok20
)" R"(
.Lzsq20:
s_mov_b32 s53, 0
s_branch .Lzok20
.Lzfx9:
s_mov_b32 s88, 2
s_call_b64 s[30:31], .LzFIX1
s_branch .Lzfr9
.Lzmk8:
s_call_b64 s[30:31], .LzMASK0
s_branch .Lzmr8
.Lzfx10:
s_mov_b32 s88, 2
s_call_b64 s[30:31], .LzFIX0
s_branch .Lzfr10
.Lzmk9:
s_call_b64 s[30:31], .LzMASK1
s_branch .Lzmr9
.Lzsp21:
s_sub_u32 s53, s53, 1
s_cbranch_scc1 .Lzsq21
s_sleep 1
buffer_load_dword v4, v7, s[20:23], s83 offen offset:4 sc0 sc1
s_waitcnt vmcnt(0)
v_cmp_eq_u32_e32 vcc, s3, v4
s_andn2_b64 s[28:29], 1, vcc
s_cbranch_scc1 .Lzsp21
s_branch .Lzok21
.Lzsq21:
s_mov_b32 s53, 0
s_branch .Lzok21
.Lzsp22:
s_sub_u32 s53, s53, 1
s_cbranch_scc1 .Lzsq22
s_sleep 1
buffer_load_dword v4, v7, s[20:23], s65 offen offset:0 sc0 sc1
s_waitcnt vmcnt(0)
v_cmp_eq_u32_e32 vcc, s3, v4
s_andn2_b64 s[28:29], s[60:61], vcc
s_cbranch_scc1 .Lzsp22
s_branch .Lzok22
.Lzsq22:
s_mov_b32 s53, 0
s_branch .Lzok22
.Lzfx11:
s_mov_b32 s88, 2
s_call_b64 s[30:31], .LzFIX1
s_branch .Lzfr11
.Lzmk10:
s_call_b64 s[30:31], .LzMASK0
s_branch .Lzmr10
.Lzfx12:
s_mov_b32 s88, 2
s_call_b64 s[30:31], .LzFIX0
s_branch .Lzfr12
.Lzmk11:
s_call_b64 s[30:31], .LzMASK1
s_branch .Lzmr11
.Lzfx13:
s_mov_b32 s88, 0
s_call_b64 s[30:31], .LzFIX1
s_branch .Lzfr13
.LzMRG:
s_waitcnt vmcnt(0)
s_nop 7
)" R"(
v_mov_b32 v25, v64
v_mov_b32 v24, v25
s_nop 1
v_permlane32_swap_b32_e32 v25, v24
v_add_f32_e32 v25, v25, v24
v_mov_b32 v24, v25
s_nop 1
v_permlane16_swap_b32_e32 v25, v24
v_add_f32_e32 v25, v25, v24
v_cmp_lt_f32_e32 vcc, 0, v25
s_nop 1
v_cndmask_b32_e32 v66, v16, v66, vcc
v_sub_f32_e32 v28, v16, v66
v_min_f32_e32 v26, 0, v28
v_exp_f32_e32 v26, v26
v_max_f32_e32 v28, 0, v28
v_sub_f32_e32 v24, 0, v28
v_exp_f32_e32 v24, v24
v_mul_f32_e32 v64, v26, v64
v_mul_f32_e32 v12, v24, v12
v_mul_f32_e32 v128, v24, v128
v_mul_f32_e32 v129, v24, v129
v_mul_f32_e32 v130, v24, v130
v_mul_f32_e32 v131, v24, v131
v_mul_f32_e32 v132, v24, v132
v_mul_f32_e32 v133, v24, v133
v_mul_f32_e32 v134, v24, v134
v_mul_f32_e32 v135, v24, v135
v_mul_f32_e32 v144, v24, v144
v_mul_f32_e32 v145, v24, v145
v_mul_f32_e32 v146, v24, v146
v_mul_f32_e32 v147, v24, v147
v_mul_f32_e32 v148, v24, v148
v_mul_f32_e32 v149, v24, v149
v_mul_f32_e32 v150, v24, v150
v_mul_f32_e32 v151, v24, v151
v_mul_f32_e32 v160, v24, v160
v_mul_f32_e32 v161, v24, v161
v_mul_f32_e32 v162, v24, v162
v_mul_f32_e32 v163, v24, v163
v_mul_f32_e32 v164, v24, v164
v_mul_f32_e32 v165, v24, v165
v_mul_f32_e32 v166, v24, v166
v_mul_f32_e32 v167, v24, v167
v_mul_f32_e32 v176, v24, v176
v_mul_f32_e32 v177, v24, v177
v_mul_f32_e32 v178, v24, v178
v_mul_f32_e32 v179, v24, v179
v_mul_f32_e32 v180, v24, v180
v_mul_f32_e32 v181, v24, v181
v_mul_f32_e32 v182, v24, v182
v_mul_f32_e32 v183, v24, v183
v_mul_f32_e32 v192, v24, v192
v_mul_f32_e32 v193, v24, v193
v_mul_f32_e32 v194, v24, v194
v_mul_f32_e32 v195, v24, v195
v_mul_f32_e32 v196, v24, v196
v_mul_f32_e32 v197, v24, v197
v_mul_f32_e32 v198, v24, v198
v_mul_f32_e32 v199, v24, v199
v_mul_f32_e32 v208, v24, v208
v_mul_f32_e32 v209, v24, v209
v_mul_f32_e32 v210, v24, v210
v_mul_f32_e32 v211, v24, v211
)" R"(
v_mul_f32_e32 v212, v24, v212
v_mul_f32_e32 v213, v24, v213
v_mul_f32_e32 v214, v24, v214
v_mul_f32_e32 v215, v24, v215
v_mul_f32_e32 v224, v24, v224
v_mul_f32_e32 v225, v24, v225
v_mul_f32_e32 v226, v24, v226
v_mul_f32_e32 v227, v24, v227
v_mul_f32_e32 v228, v24, v228
v_mul_f32_e32 v229, v24, v229
v_mul_f32_e32 v230, v24, v230
v_mul_f32_e32 v231, v24, v231
v_mul_f32_e32 v240, v24, v240
v_mul_f32_e32 v241, v24, v241
v_mul_f32_e32 v242, v24, v242
v_mul_f32_e32 v243, v24, v243
v_mul_f32_e32 v244, v24, v244
v_mul_f32_e32 v245, v24, v245
v_mul_f32_e32 v246, v24, v246
v_mul_f32_e32 v247, v24, v247
v_sub_f32_e32 v16, v16, v28
v_sub_f32_e32 v17, v17, v28
v_sub_f32_e32 v18, v18, v28
v_sub_f32_e32 v19, v19, v28
v_sub_f32_e32 v96, v96, v28
v_sub_f32_e32 v97, v97, v28
v_sub_f32_e32 v98, v98, v28
v_sub_f32_e32 v99, v99, v28
v_sub_f32_e32 v104, v104, v28
v_sub_f32_e32 v105, v105, v28
v_sub_f32_e32 v106, v106, v28
v_sub_f32_e32 v107, v107, v28
v_sub_f32_e32 v112, v112, v28
v_sub_f32_e32 v113, v113, v28
v_sub_f32_e32 v114, v114, v28
v_sub_f32_e32 v115, v115, v28
v_sub_f32_e32 v120, v120, v28
v_sub_f32_e32 v121, v121, v28
v_sub_f32_e32 v122, v122, v28
v_sub_f32_e32 v123, v123, v28
v_accvgpr_read_b32 v25, a128
v_lshlrev_b32 v24, 16, v25
v_and_b32 v25, 0xffff0000, v25
v_mul_f32_e32 v24, v26, v24
v_mul_f32_e32 v25, v26, v25
Cv 24,24,25
v_accvgpr_write_b32 a128, v24
v_accvgpr_read_b32 v25, a129
v_lshlrev_b32 v24, 16, v25
v_and_b32 v25, 0xffff0000, v25
v_mul_f32_e32 v24, v26, v24
v_mul_f32_e32 v25, v26, v25
Cv 24,24,25
v_accvgpr_write_b32 a129, v24
v_accvgpr_read_b32 v25, a130
v_lshlrev_b32 v24, 16, v25
v_and_b32 v25, 0xffff0000, v25
v_mul_f32_e32 v24, v26, v24
v_mul_f32_e32 v25, v26, v25
Cv 24,24,25
v_accvgpr_write_b32 a130, v24
v_accvgpr_read_b32 v25, a131
v_lshlrev_b32 v24, 16, v25
v_and_b32 v25, 0xffff0000, v25
)" R"(
v_mul_f32_e32 v24, v26, v24
v_mul_f32_e32 v25, v26, v25
Cv 24,24,25
v_accvgpr_write_b32 a131, v24
v_accvgpr_read_b32 v25, a136
v_lshlrev_b32 v24, 16, v25
v_and_b32 v25, 0xffff0000, v25
v_mul_f32_e32 v24, v26, v24
v_mul_f32_e32 v25, v26, v25
Cv 24,24,25
v_accvgpr_write_b32 a136, v24
v_accvgpr_read_b32 v25, a137
v_lshlrev_b32 v24, 16, v25
v_and_b32 v25, 0xffff0000, v25
v_mul_f32_e32 v24, v26, v24
v_mul_f32_e32 v25, v26, v25
Cv 24,24,25
v_accvgpr_write_b32 a137, v24
v_accvgpr_read_b32 v25, a138
v_lshlrev_b32 v24, 16, v25
v_and_b32 v25, 0xffff0000, v25
v_mul_f32_e32 v24, v26, v24
v_mul_f32_e32 v25, v26, v25
Cv 24,24,25
v_accvgpr_write_b32 a138, v24
v_accvgpr_read_b32 v25, a139
v_lshlrev_b32 v24, 16, v25
v_and_b32 v25, 0xffff0000, v25
v_mul_f32_e32 v24, v26, v24
v_mul_f32_e32 v25, v26, v25
Cv 24,24,25
v_accvgpr_write_b32 a139, v24
v_accvgpr_read_b32 v25, a144
v_lshlrev_b32 v24, 16, v25
v_and_b32 v25, 0xffff0000, v25
v_mul_f32_e32 v24, v26, v24
v_mul_f32_e32 v25, v26, v25
Cv 24,24,25
v_accvgpr_write_b32 a144, v24
v_accvgpr_read_b32 v25, a145
v_lshlrev_b32 v24, 16, v25
v_and_b32 v25, 0xffff0000, v25
v_mul_f32_e32 v24, v26, v24
v_mul_f32_e32 v25, v26, v25
Cv 24,24,25
v_accvgpr_write_b32 a145, v24
v_accvgpr_read_b32 v25, a146
v_lshlrev_b32 v24, 16, v25
v_and_b32 v25, 0xffff0000, v25
v_mul_f32_e32 v24, v26, v24
v_mul_f32_e32 v25, v26, v25
Cv 24,24,25
v_accvgpr_write_b32 a146, v24
v_accvgpr_read_b32 v25, a147
v_lshlrev_b32 v24, 16, v25
v_and_b32 v25, 0xffff0000, v25
v_mul_f32_e32 v24, v26, v24
v_mul_f32_e32 v25, v26, v25
Cv 24,24,25
v_accvgpr_write_b32 a147, v24
v_accvgpr_read_b32 v25, a152
v_lshlrev_b32 v24, 16, v25
v_and_b32 v25, 0xffff0000, v25
v_mul_f32_e32 v24, v26, v24
)" R"(
v_mul_f32_e32 v25, v26, v25
Cv 24,24,25
v_accvgpr_write_b32 a152, v24
v_accvgpr_read_b32 v25, a153
v_lshlrev_b32 v24, 16, v25
v_and_b32 v25, 0xffff0000, v25
v_mul_f32_e32 v24, v26, v24
v_mul_f32_e32 v25, v26, v25
Cv 24,24,25
v_accvgpr_write_b32 a153, v24
v_accvgpr_read_b32 v25, a154
v_lshlrev_b32 v24, 16, v25
v_and_b32 v25, 0xffff0000, v25
v_mul_f32_e32 v24, v26, v24
v_mul_f32_e32 v25, v26, v25
Cv 24,24,25
v_accvgpr_write_b32 a154, v24
v_accvgpr_read_b32 v25, a155
v_lshlrev_b32 v24, 16, v25
v_and_b32 v25, 0xffff0000, v25
v_mul_f32_e32 v24, v26, v24
v_mul_f32_e32 v25, v26, v25
Cv 24,24,25
v_accvgpr_write_b32 a155, v24
v_accvgpr_read_b32 v25, a160
v_lshlrev_b32 v24, 16, v25
v_and_b32 v25, 0xffff0000, v25
v_mul_f32_e32 v24, v26, v24
v_mul_f32_e32 v25, v26, v25
Cv 24,24,25
v_accvgpr_write_b32 a160, v24
v_accvgpr_read_b32 v25, a161
v_lshlrev_b32 v24, 16, v25
v_and_b32 v25, 0xffff0000, v25
v_mul_f32_e32 v24, v26, v24
v_mul_f32_e32 v25, v26, v25
Cv 24,24,25
v_accvgpr_write_b32 a161, v24
v_accvgpr_read_b32 v25, a162
v_lshlrev_b32 v24, 16, v25
v_and_b32 v25, 0xffff0000, v25
v_mul_f32_e32 v24, v26, v24
v_mul_f32_e32 v25, v26, v25
Cv 24,24,25
v_accvgpr_write_b32 a162, v24
v_accvgpr_read_b32 v25, a163
v_lshlrev_b32 v24, 16, v25
v_and_b32 v25, 0xffff0000, v25
v_mul_f32_e32 v24, v26, v24
v_mul_f32_e32 v25, v26, v25
Cv 24,24,25
v_accvgpr_write_b32 a163, v24
v_accvgpr_read_b32 v25, a168
v_lshlrev_b32 v24, 16, v25
v_and_b32 v25, 0xffff0000, v25
v_mul_f32_e32 v24, v26, v24
v_mul_f32_e32 v25, v26, v25
Cv 24,24,25
v_accvgpr_write_b32 a168, v24
v_accvgpr_read_b32 v25, a169
v_lshlrev_b32 v24, 16, v25
v_and_b32 v25, 0xffff0000, v25
v_mul_f32_e32 v24, v26, v24
v_mul_f32_e32 v25, v26, v25
)" R"(
Cv 24,24,25
v_accvgpr_write_b32 a169, v24
v_accvgpr_read_b32 v25, a170
v_lshlrev_b32 v24, 16, v25
v_and_b32 v25, 0xffff0000, v25
v_mul_f32_e32 v24, v26, v24
v_mul_f32_e32 v25, v26, v25
Cv 24,24,25
v_accvgpr_write_b32 a170, v24
v_accvgpr_read_b32 v25, a171
v_lshlrev_b32 v24, 16, v25
v_and_b32 v25, 0xffff0000, v25
v_mul_f32_e32 v24, v26, v24
v_mul_f32_e32 v25, v26, v25
Cv 24,24,25
v_accvgpr_write_b32 a171, v24
v_accvgpr_read_b32 v25, a176
v_lshlrev_b32 v24, 16, v25
v_and_b32 v25, 0xffff0000, v25
v_mul_f32_e32 v24, v26, v24
v_mul_f32_e32 v25, v26, v25
Cv 24,24,25
v_accvgpr_write_b32 a176, v24
v_accvgpr_read_b32 v25, a177
v_lshlrev_b32 v24, 16, v25
v_and_b32 v25, 0xffff0000, v25
v_mul_f32_e32 v24, v26, v24
v_mul_f32_e32 v25, v26, v25
Cv 24,24,25
v_accvgpr_write_b32 a177, v24
v_accvgpr_read_b32 v25, a178
v_lshlrev_b32 v24, 16, v25
v_and_b32 v25, 0xffff0000, v25
v_mul_f32_e32 v24, v26, v24
v_mul_f32_e32 v25, v26, v25
Cv 24,24,25
v_accvgpr_write_b32 a178, v24
v_accvgpr_read_b32 v25, a179
v_lshlrev_b32 v24, 16, v25
v_and_b32 v25, 0xffff0000, v25
v_mul_f32_e32 v24, v26, v24
v_mul_f32_e32 v25, v26, v25
Cv 24,24,25
v_accvgpr_write_b32 a179, v24
v_accvgpr_read_b32 v25, a184
v_lshlrev_b32 v24, 16, v25
v_and_b32 v25, 0xffff0000, v25
v_mul_f32_e32 v24, v26, v24
v_mul_f32_e32 v25, v26, v25
Cv 24,24,25
v_accvgpr_write_b32 a184, v24
v_accvgpr_read_b32 v25, a185
v_lshlrev_b32 v24, 16, v25
v_and_b32 v25, 0xffff0000, v25
v_mul_f32_e32 v24, v26, v24
v_mul_f32_e32 v25, v26, v25
Cv 24,24,25
v_accvgpr_write_b32 a185, v24
v_accvgpr_read_b32 v25, a186
v_lshlrev_b32 v24, 16, v25
v_and_b32 v25, 0xffff0000, v25
v_mul_f32_e32 v24, v26, v24
v_mul_f32_e32 v25, v26, v25
Cv 24,24,25
)" R"(
v_accvgpr_write_b32 a186, v24
v_accvgpr_read_b32 v25, a187
v_lshlrev_b32 v24, 16, v25
v_and_b32 v25, 0xffff0000, v25
v_mul_f32_e32 v24, v26, v24
v_mul_f32_e32 v25, v26, v25
Cv 24,24,25
v_accvgpr_write_b32 a187, v24
v_mov_b32 v25, v65
v_mov_b32 v24, v25
s_nop 1
v_permlane32_swap_b32_e32 v25, v24
v_add_f32_e32 v25, v25, v24
v_mov_b32 v24, v25
s_nop 1
v_permlane16_swap_b32_e32 v25, v24
v_add_f32_e32 v25, v25, v24
v_cmp_lt_f32_e32 vcc, 0, v25
s_nop 1
v_cndmask_b32_e32 v67, v20, v67, vcc
v_sub_f32_e32 v29, v20, v67
v_min_f32_e32 v27, 0, v29
v_exp_f32_e32 v27, v27
v_max_f32_e32 v29, 0, v29
v_sub_f32_e32 v24, 0, v29
v_exp_f32_e32 v24, v24
v_mul_f32_e32 v65, v27, v65
v_mul_f32_e32 v13, v24, v13
v_mul_f32_e32 v136, v24, v136
v_mul_f32_e32 v137, v24, v137
v_mul_f32_e32 v138, v24, v138
v_mul_f32_e32 v139, v24, v139
v_mul_f32_e32 v140, v24, v140
v_mul_f32_e32 v141, v24, v141
v_mul_f32_e32 v142, v24, v142
v_mul_f32_e32 v143, v24, v143
v_mul_f32_e32 v152, v24, v152
v_mul_f32_e32 v153, v24, v153
v_mul_f32_e32 v154, v24, v154
v_mul_f32_e32 v155, v24, v155
v_mul_f32_e32 v156, v24, v156
v_mul_f32_e32 v157, v24, v157
v_mul_f32_e32 v158, v24, v158
v_mul_f32_e32 v159, v24, v159
v_mul_f32_e32 v168, v24, v168
v_mul_f32_e32 v169, v24, v169
v_mul_f32_e32 v170, v24, v170
v_mul_f32_e32 v171, v24, v171
v_mul_f32_e32 v172, v24, v172
v_mul_f32_e32 v173, v24, v173
v_mul_f32_e32 v174, v24, v174
v_mul_f32_e32 v175, v24, v175
v_mul_f32_e32 v184, v24, v184
v_mul_f32_e32 v185, v24, v185
v_mul_f32_e32 v186, v24, v186
v_mul_f32_e32 v187, v24, v187
v_mul_f32_e32 v188, v24, v188
v_mul_f32_e32 v189, v24, v189
v_mul_f32_e32 v190, v24, v190
v_mul_f32_e32 v191, v24, v191
v_mul_f32_e32 v200, v24, v200
v_mul_f32_e32 v201, v24, v201
v_mul_f32_e32 v202, v24, v202
v_mul_f32_e32 v203, v24, v203
)" R"(
v_mul_f32_e32 v204, v24, v204
v_mul_f32_e32 v205, v24, v205
v_mul_f32_e32 v206, v24, v206
v_mul_f32_e32 v207, v24, v207
v_mul_f32_e32 v216, v24, v216
v_mul_f32_e32 v217, v24, v217
v_mul_f32_e32 v218, v24, v218
v_mul_f32_e32 v219, v24, v219
v_mul_f32_e32 v220, v24, v220
v_mul_f32_e32 v221, v24, v221
v_mul_f32_e32 v222, v24, v222
v_mul_f32_e32 v223, v24, v223
v_mul_f32_e32 v232, v24, v232
v_mul_f32_e32 v233, v24, v233
v_mul_f32_e32 v234, v24, v234
v_mul_f32_e32 v235, v24, v235
v_mul_f32_e32 v236, v24, v236
v_mul_f32_e32 v237, v24, v237
v_mul_f32_e32 v238, v24, v238
v_mul_f32_e32 v239, v24, v239
v_mul_f32_e32 v248, v24, v248
v_mul_f32_e32 v249, v24, v249
v_mul_f32_e32 v250, v24, v250
v_mul_f32_e32 v251, v24, v251
v_mul_f32_e32 v252, v24, v252
v_mul_f32_e32 v253, v24, v253
v_mul_f32_e32 v254, v24, v254
v_mul_f32_e32 v255, v24, v255
v_sub_f32_e32 v20, v20, v29
v_sub_f32_e32 v21, v21, v29
v_sub_f32_e32 v22, v22, v29
v_sub_f32_e32 v23, v23, v29
v_sub_f32_e32 v100, v100, v29
v_sub_f32_e32 v101, v101, v29
v_sub_f32_e32 v102, v102, v29
v_sub_f32_e32 v103, v103, v29
v_sub_f32_e32 v108, v108, v29
v_sub_f32_e32 v109, v109, v29
v_sub_f32_e32 v110, v110, v29
v_sub_f32_e32 v111, v111, v29
v_sub_f32_e32 v116, v116, v29
v_sub_f32_e32 v117, v117, v29
v_sub_f32_e32 v118, v118, v29
v_sub_f32_e32 v119, v119, v29
v_sub_f32_e32 v124, v124, v29
v_sub_f32_e32 v125, v125, v29
v_sub_f32_e32 v126, v126, v29
v_sub_f32_e32 v127, v127, v29
v_accvgpr_read_b32 v25, a132
v_lshlrev_b32 v24, 16, v25
v_and_b32 v25, 0xffff0000, v25
v_mul_f32_e32 v24, v27, v24
v_mul_f32_e32 v25, v27, v25
Cv 24,24,25
v_accvgpr_write_b32 a132, v24
v_accvgpr_read_b32 v25, a133
v_lshlrev_b32 v24, 16, v25
v_and_b32 v25, 0xffff0000, v25
v_mul_f32_e32 v24, v27, v24
v_mul_f32_e32 v25, v27, v25
Cv 24,24,25
v_accvgpr_write_b32 a133, v24
v_accvgpr_read_b32 v25, a134
v_lshlrev_b32 v24, 16, v25
)" R"(
v_and_b32 v25, 0xffff0000, v25
v_mul_f32_e32 v24, v27, v24
v_mul_f32_e32 v25, v27, v25
Cv 24,24,25
v_accvgpr_write_b32 a134, v24
v_accvgpr_read_b32 v25, a135
v_lshlrev_b32 v24, 16, v25
v_and_b32 v25, 0xffff0000, v25
v_mul_f32_e32 v24, v27, v24
v_mul_f32_e32 v25, v27, v25
Cv 24,24,25
v_accvgpr_write_b32 a135, v24
v_accvgpr_read_b32 v25, a140
v_lshlrev_b32 v24, 16, v25
v_and_b32 v25, 0xffff0000, v25
v_mul_f32_e32 v24, v27, v24
v_mul_f32_e32 v25, v27, v25
Cv 24,24,25
v_accvgpr_write_b32 a140, v24
v_accvgpr_read_b32 v25, a141
v_lshlrev_b32 v24, 16, v25
v_and_b32 v25, 0xffff0000, v25
v_mul_f32_e32 v24, v27, v24
v_mul_f32_e32 v25, v27, v25
Cv 24,24,25
v_accvgpr_write_b32 a141, v24
v_accvgpr_read_b32 v25, a142
v_lshlrev_b32 v24, 16, v25
v_and_b32 v25, 0xffff0000, v25
v_mul_f32_e32 v24, v27, v24
v_mul_f32_e32 v25, v27, v25
Cv 24,24,25
v_accvgpr_write_b32 a142, v24
v_accvgpr_read_b32 v25, a143
v_lshlrev_b32 v24, 16, v25
v_and_b32 v25, 0xffff0000, v25
v_mul_f32_e32 v24, v27, v24
v_mul_f32_e32 v25, v27, v25
Cv 24,24,25
v_accvgpr_write_b32 a143, v24
v_accvgpr_read_b32 v25, a148
v_lshlrev_b32 v24, 16, v25
v_and_b32 v25, 0xffff0000, v25
v_mul_f32_e32 v24, v27, v24
v_mul_f32_e32 v25, v27, v25
Cv 24,24,25
v_accvgpr_write_b32 a148, v24
v_accvgpr_read_b32 v25, a149
v_lshlrev_b32 v24, 16, v25
v_and_b32 v25, 0xffff0000, v25
v_mul_f32_e32 v24, v27, v24
v_mul_f32_e32 v25, v27, v25
Cv 24,24,25
v_accvgpr_write_b32 a149, v24
v_accvgpr_read_b32 v25, a150
v_lshlrev_b32 v24, 16, v25
v_and_b32 v25, 0xffff0000, v25
v_mul_f32_e32 v24, v27, v24
v_mul_f32_e32 v25, v27, v25
Cv 24,24,25
v_accvgpr_write_b32 a150, v24
v_accvgpr_read_b32 v25, a151
v_lshlrev_b32 v24, 16, v25
v_and_b32 v25, 0xffff0000, v25
)" R"(
v_mul_f32_e32 v24, v27, v24
v_mul_f32_e32 v25, v27, v25
Cv 24,24,25
v_accvgpr_write_b32 a151, v24
v_accvgpr_read_b32 v25, a156
v_lshlrev_b32 v24, 16, v25
v_and_b32 v25, 0xffff0000, v25
v_mul_f32_e32 v24, v27, v24
v_mul_f32_e32 v25, v27, v25
Cv 24,24,25
v_accvgpr_write_b32 a156, v24
v_accvgpr_read_b32 v25, a157
v_lshlrev_b32 v24, 16, v25
v_and_b32 v25, 0xffff0000, v25
v_mul_f32_e32 v24, v27, v24
v_mul_f32_e32 v25, v27, v25
Cv 24,24,25
v_accvgpr_write_b32 a157, v24
v_accvgpr_read_b32 v25, a158
v_lshlrev_b32 v24, 16, v25
v_and_b32 v25, 0xffff0000, v25
v_mul_f32_e32 v24, v27, v24
v_mul_f32_e32 v25, v27, v25
Cv 24,24,25
v_accvgpr_write_b32 a158, v24
v_accvgpr_read_b32 v25, a159
v_lshlrev_b32 v24, 16, v25
v_and_b32 v25, 0xffff0000, v25
v_mul_f32_e32 v24, v27, v24
v_mul_f32_e32 v25, v27, v25
Cv 24,24,25
v_accvgpr_write_b32 a159, v24
v_accvgpr_read_b32 v25, a164
v_lshlrev_b32 v24, 16, v25
v_and_b32 v25, 0xffff0000, v25
v_mul_f32_e32 v24, v27, v24
v_mul_f32_e32 v25, v27, v25
Cv 24,24,25
v_accvgpr_write_b32 a164, v24
v_accvgpr_read_b32 v25, a165
v_lshlrev_b32 v24, 16, v25
v_and_b32 v25, 0xffff0000, v25
v_mul_f32_e32 v24, v27, v24
v_mul_f32_e32 v25, v27, v25
Cv 24,24,25
v_accvgpr_write_b32 a165, v24
v_accvgpr_read_b32 v25, a166
v_lshlrev_b32 v24, 16, v25
v_and_b32 v25, 0xffff0000, v25
v_mul_f32_e32 v24, v27, v24
v_mul_f32_e32 v25, v27, v25
Cv 24,24,25
v_accvgpr_write_b32 a166, v24
v_accvgpr_read_b32 v25, a167
v_lshlrev_b32 v24, 16, v25
v_and_b32 v25, 0xffff0000, v25
v_mul_f32_e32 v24, v27, v24
v_mul_f32_e32 v25, v27, v25
Cv 24,24,25
v_accvgpr_write_b32 a167, v24
v_accvgpr_read_b32 v25, a172
v_lshlrev_b32 v24, 16, v25
v_and_b32 v25, 0xffff0000, v25
v_mul_f32_e32 v24, v27, v24
)" R"(
v_mul_f32_e32 v25, v27, v25
Cv 24,24,25
v_accvgpr_write_b32 a172, v24
v_accvgpr_read_b32 v25, a173
v_lshlrev_b32 v24, 16, v25
v_and_b32 v25, 0xffff0000, v25
v_mul_f32_e32 v24, v27, v24
v_mul_f32_e32 v25, v27, v25
Cv 24,24,25
v_accvgpr_write_b32 a173, v24
v_accvgpr_read_b32 v25, a174
v_lshlrev_b32 v24, 16, v25
v_and_b32 v25, 0xffff0000, v25
v_mul_f32_e32 v24, v27, v24
v_mul_f32_e32 v25, v27, v25
Cv 24,24,25
v_accvgpr_write_b32 a174, v24
v_accvgpr_read_b32 v25, a175
v_lshlrev_b32 v24, 16, v25
v_and_b32 v25, 0xffff0000, v25
v_mul_f32_e32 v24, v27, v24
v_mul_f32_e32 v25, v27, v25
Cv 24,24,25
v_accvgpr_write_b32 a175, v24
v_accvgpr_read_b32 v25, a180
v_lshlrev_b32 v24, 16, v25
v_and_b32 v25, 0xffff0000, v25
v_mul_f32_e32 v24, v27, v24
v_mul_f32_e32 v25, v27, v25
Cv 24,24,25
v_accvgpr_write_b32 a180, v24
v_accvgpr_read_b32 v25, a181
v_lshlrev_b32 v24, 16, v25
v_and_b32 v25, 0xffff0000, v25
v_mul_f32_e32 v24, v27, v24
v_mul_f32_e32 v25, v27, v25
Cv 24,24,25
v_accvgpr_write_b32 a181, v24
v_accvgpr_read_b32 v25, a182
v_lshlrev_b32 v24, 16, v25
v_and_b32 v25, 0xffff0000, v25
v_mul_f32_e32 v24, v27, v24
v_mul_f32_e32 v25, v27, v25
Cv 24,24,25
v_accvgpr_write_b32 a182, v24
v_accvgpr_read_b32 v25, a183
v_lshlrev_b32 v24, 16, v25
v_and_b32 v25, 0xffff0000, v25
v_mul_f32_e32 v24, v27, v24
v_mul_f32_e32 v25, v27, v25
Cv 24,24,25
v_accvgpr_write_b32 a183, v24
v_accvgpr_read_b32 v25, a188
v_lshlrev_b32 v24, 16, v25
v_and_b32 v25, 0xffff0000, v25
v_mul_f32_e32 v24, v27, v24
v_mul_f32_e32 v25, v27, v25
Cv 24,24,25
v_accvgpr_write_b32 a188, v24
v_accvgpr_read_b32 v25, a189
v_lshlrev_b32 v24, 16, v25
v_and_b32 v25, 0xffff0000, v25
v_mul_f32_e32 v24, v27, v24
v_mul_f32_e32 v25, v27, v25
)" R"(
Cv 24,24,25
v_accvgpr_write_b32 a189, v24
v_accvgpr_read_b32 v25, a190
v_lshlrev_b32 v24, 16, v25
v_and_b32 v25, 0xffff0000, v25
v_mul_f32_e32 v24, v27, v24
v_mul_f32_e32 v25, v27, v25
Cv 24,24,25
v_accvgpr_write_b32 a190, v24
v_accvgpr_read_b32 v25, a191
v_lshlrev_b32 v24, 16, v25
v_and_b32 v25, 0xffff0000, v25
v_mul_f32_e32 v24, v27, v24
v_mul_f32_e32 v25, v27, v25
Cv 24,24,25
v_accvgpr_write_b32 a191, v24
v_exp_f32_e32 v48, v96
v_exp_f32_e32 v24, v97
s_nop 0
v_add_f32_e32 v14, v48, v24
Cv 48,48,24
v_exp_f32_e32 v49, v98
v_exp_f32_e32 v25, v99
v_add_f32_e32 v14, v14, v49
v_add_f32_e32 v14, v14, v25
Cv 49,49,25
v_exp_f32_e32 v50, v104
v_exp_f32_e32 v26, v105
v_add_f32_e32 v14, v14, v50
v_add_f32_e32 v14, v14, v26
Cv 50,50,26
v_exp_f32_e32 v51, v106
v_exp_f32_e32 v27, v107
v_add_f32_e32 v14, v14, v51
v_add_f32_e32 v14, v14, v27
Cv 51,51,27
v_exp_f32_e32 v52, v100
v_exp_f32_e32 v28, v101
s_nop 0
v_add_f32_e32 v15, v52, v28
Cv 52,52,28
v_exp_f32_e32 v53, v102
v_exp_f32_e32 v29, v103
v_add_f32_e32 v15, v15, v53
v_add_f32_e32 v15, v15, v29
Cv 53,53,29
v_exp_f32_e32 v54, v108
v_exp_f32_e32 v24, v109
v_add_f32_e32 v15, v15, v54
v_add_f32_e32 v15, v15, v24
Cv 54,54,24
v_exp_f32_e32 v55, v110
v_exp_f32_e32 v25, v111
v_add_f32_e32 v15, v15, v55
v_add_f32_e32 v15, v15, v25
Cv 55,55,25
s_nop 1
s_branch .LzMRR
.Lzsp23:
s_sub_u32 s53, s53, 1
s_cbranch_scc1 .Lzsq23
s_sleep 1
buffer_load_dword v4, v7, s[20:23], s52 offen offset:0 sc0 sc1
s_waitcnt vmcnt(0)
)" R"(
v_cmp_eq_u32_e32 vcc, s3, v4
s_andn2_b64 s[28:29], 1, vcc
s_cbranch_scc1 .Lzsp23
s_branch .Lzok23
.Lzsq23:
s_mov_b32 s53, 0
s_branch .Lzok23
.Lzsp24:
s_sub_u32 s53, s53, 1
s_cbranch_scc1 .Lzsq24
s_sleep 1
buffer_load_dword v4, v7, s[20:23], s52 offen offset:128 sc0 sc1
s_waitcnt vmcnt(0)
v_cmp_eq_u32_e32 vcc, s3, v4
s_andn2_b64 s[28:29], 1, vcc
s_cbranch_scc1 .Lzsp24
s_branch .Lzok24
.Lzsq24:
s_mov_b32 s53, 0
s_branch .Lzok24
.LzBB:
v_lshlrev_b32 v95, 7, v0
s_mov_b32 s28, 0xc10c000
s_add_u32 s29, s28, 6144
.LzBB1:
buffer_load_dword v92, v95, s[20:23], s28 offen sc0 sc1
buffer_load_dword v93, v95, s[20:23], s29 offen sc0 sc1
s_waitcnt vmcnt(0)
v_cmp_ne_u32_e32 vcc, s3, v92
v_cmp_ne_u32_e64 s[30:31], s3, v93
s_or_b64 vcc, vcc, s[30:31]
s_cbranch_scc0 .LzBB2
s_sub_u32 s53, s53, 1
s_cbranch_scc0 .LzBB1
s_mov_b32 s53, 0
.LzBB2:
s_mul_i32 s28, s56, 7168
s_add_u32 s28, s28, 0xc158000
s_add_u32 s29, s28, 4096
buffer_load_dwordx4 v[64:67], v1, s[20:23], s28 offen offset:0 sc0 sc1
buffer_load_dwordx4 v[68:71], v1, s[20:23], s28 offen offset:1024 sc0 sc1
buffer_load_dwordx4 v[72:75], v1, s[20:23], s28 offen offset:2048 sc0 sc1
buffer_load_dwordx4 v[76:79], v1, s[20:23], s28 offen offset:3072 sc0 sc1
buffer_load_dwordx4 v[80:83], v1, s[20:23], s29 offen offset:0 sc0 sc1
buffer_load_dwordx4 v[84:87], v1, s[20:23], s29 offen offset:1024 sc0 sc1
buffer_load_dwordx4 v[88:91], v1, s[20:23], s29 offen offset:2048 sc0 sc1
s_cmp_gt_u32 s56, 27
s_cselect_b32 s28, 16, 0
s_cselect_b32 s29, 28, 0
s_lshr_b32 s28, s63, s28
s_and_b32 s28, s28, 0x7ff
s_sub_u32 s29, s56, s29
s_lshl_b32 s29, s29, 5
s_mul_i32 s83, s28, 896
s_add_u32 s29, s29, s83
v_and_b32 v95, 3, v0
v_lshlrev_b32 v95, 3, v95
buffer_load_dwordx2 v[92:93], v95, s[12:15], s29 offen
s_lshl4_add_u32 s28, s28, 0xc10000c
v_mov_b32 v94, 0
buffer_load_dword v94, v94, s[20:23], s28 offen sc0 sc1
s_waitcnt vmcnt(0)
v_add_f32_e32 v64, v64, v68
v_add_f32_e32 v65, v65, v69
)" R"(
v_add_f32_e32 v66, v66, v70
v_add_f32_e32 v67, v67, v71
v_add_f32_e32 v72, v72, v76
v_add_f32_e32 v73, v73, v77
v_add_f32_e32 v74, v74, v78
v_add_f32_e32 v75, v75, v79
v_add_f32_e32 v80, v80, v84
v_add_f32_e32 v81, v81, v85
v_add_f32_e32 v82, v82, v86
v_add_f32_e32 v83, v83, v87
v_add_f32_e32 v64, v64, v72
v_add_f32_e32 v65, v65, v73
v_add_f32_e32 v66, v66, v74
v_add_f32_e32 v67, v67, v75
v_add_f32_e32 v80, v80, v88
v_add_f32_e32 v81, v81, v89
v_add_f32_e32 v82, v82, v90
v_add_f32_e32 v83, v83, v91
v_add_f32_e32 v64, v64, v80
v_add_f32_e32 v65, v65, v81
v_add_f32_e32 v66, v66, v82
v_add_f32_e32 v67, v67, v83
v_mov_b32 v68, v64
s_nop 1
v_permlane32_swap_b32_e32 v64, v68
v_add_f32_e32 v64, v64, v68
v_mov_b32 v68, v64
s_nop 1
v_permlane16_swap_b32_e32 v64, v68
v_add_f32_e32 v64, v64, v68
v_mov_b32 v68, v65
s_nop 1
v_permlane32_swap_b32_e32 v65, v68
v_add_f32_e32 v65, v65, v68
v_mov_b32 v68, v65
s_nop 1
v_permlane16_swap_b32_e32 v65, v68
v_add_f32_e32 v65, v65, v68
v_mov_b32 v68, v66
s_nop 1
v_permlane32_swap_b32_e32 v66, v68
v_add_f32_e32 v66, v66, v68
v_mov_b32 v68, v66
s_nop 1
v_permlane16_swap_b32_e32 v66, v68
v_add_f32_e32 v66, v66, v68
v_mov_b32 v68, v67
s_nop 1
v_permlane32_swap_b32_e32 v67, v68
v_add_f32_e32 v67, v67, v68
v_mov_b32 v68, v67
s_nop 1
v_permlane16_swap_b32_e32 v67, v68
v_add_f32_e32 v67, v67, v68
v_add_f32_dpp v64, v64, v64 row_ror:8 row_mask:0xf bank_mask:0xf
v_add_f32_dpp v65, v65, v65 row_ror:8 row_mask:0xf bank_mask:0xf
v_add_f32_dpp v66, v66, v66 row_ror:8 row_mask:0xf bank_mask:0xf
v_add_f32_dpp v67, v67, v67 row_ror:8 row_mask:0xf bank_mask:0xf
v_add_f32_dpp v64, v64, v64 row_ror:4 row_mask:0xf bank_mask:0xf
v_add_f32_dpp v65, v65, v65 row_ror:4 row_mask:0xf bank_mask:0xf
v_add_f32_dpp v66, v66, v66 row_ror:4 row_mask:0xf bank_mask:0xf
v_add_f32_dpp v67, v67, v67 row_ror:4 row_mask:0xf bank_mask:0xf
v_mul_f32_e32 v94, 0xbfb8aa3b, v94
v_exp_f32_e32 v94, v94
)" R"(
Cv 65,64,65
v_lshlrev_b32 v64, 16, v65
v_and_b32 v65, 0xffff0000, v65
v_add_f32_e32 v94, 1.0, v94
v_rcp_f32_e32 v94, v94
Cv 67,66,67
v_lshlrev_b32 v66, 16, v67
v_and_b32 v67, 0xffff0000, v67
Cv 94,94,94
v_lshlrev_b32 v94, 16, v94
v_mul_f32_e32 v64, v94, v64
v_mul_f32_e32 v65, v94, v65
v_mul_f32_e32 v66, v94, v66
v_mul_f32_e32 v67, v94, v67
Cv 65,64,65
v_lshlrev_b32 v64, 16, v65
v_and_b32 v65, 0xffff0000, v65
Cv 67,66,67
v_lshlrev_b32 v66, 16, v67
v_and_b32 v67, 0xffff0000, v67
v_lshlrev_b32 v68, 16, v92
v_and_b32 v69, 0xffff0000, v92
v_lshlrev_b32 v70, 16, v93
v_and_b32 v71, 0xffff0000, v93
v_add_f32_e32 v64, v64, v68
v_add_f32_e32 v65, v65, v69
v_add_f32_e32 v66, v66, v70
v_add_f32_e32 v67, v67, v71
Cv 64,64,65
Cv 66,66,67
v_mov_b32 v65, s3
v_mov_b32 v67, s3
v_lshrrev_b32 v95, 2, v0
v_min_u32 v95, 1, v95
v_lshl_or_b32 v95, v95, 30, v1
s_lshl_b32 s28, s56, 7
s_add_u32 s28, s28, 0xc109000
buffer_store_dwordx4 v[64:67], v95, s[20:23], s28 offen sc0 sc1
buffer_load_dword v4, v165, s[20:23], s40 offen sc0 sc1
s_waitcnt vmcnt(0)
s_branch .LzBBR
.LzPRB:
s_lshl_b32 s24, s34, 21
s_mul_i32 s25, s33, 2304
s_add_u32 s24, s24, s25
s_add_u32 s24, s24, 0x6100000
s_and_b32 s25, s36, 0x2000
s_xor_b32 s24, s24, s25
s_xor_b32 s25, s24, 0x2000
buffer_store_dword v0, v7, s[20:23], s24 offen sc0 sc1
buffer_store_dword v0, v7, s[20:23], s25 offen sc0 sc1
s_mov_b32 s26, -1
s_mov_b32 s27, -1
s_waitcnt vmcnt(0)
s_memtime s[28:29]
s_waitcnt lgkmcnt(0)
buffer_store_dword v0, v7, s[20:23], s24 offen sc0 sc1
s_waitcnt vmcnt(0)
s_memtime s[30:31]
s_waitcnt lgkmcnt(0)
s_sub_u32 s30, s30, s28
s_min_u32 s26, s26, s30
s_memtime s[28:29]
s_waitcnt lgkmcnt(0)
)" R"(
buffer_store_dword v0, v7, s[20:23], s25 offen sc0 sc1
s_waitcnt vmcnt(0)
s_memtime s[30:31]
s_waitcnt lgkmcnt(0)
s_sub_u32 s30, s30, s28
s_min_u32 s27, s27, s30
s_memtime s[28:29]
s_waitcnt lgkmcnt(0)
buffer_store_dword v0, v7, s[20:23], s24 offen sc0 sc1
s_waitcnt vmcnt(0)
s_memtime s[30:31]
s_waitcnt lgkmcnt(0)
s_sub_u32 s30, s30, s28
s_min_u32 s26, s26, s30
s_memtime s[28:29]
s_waitcnt lgkmcnt(0)
buffer_store_dword v0, v7, s[20:23], s25 offen sc0 sc1
s_waitcnt vmcnt(0)
s_memtime s[30:31]
s_waitcnt lgkmcnt(0)
s_sub_u32 s30, s30, s28
s_min_u32 s27, s27, s30
s_bitcmp1_b32 s33, 0
s_cselect_b32 s28, s27, s26
s_cselect_b32 s29, s26, s27
v_mov_b32 v32, s28
v_mov_b32 v33, s29
s_lshl_b32 s24, s33, 3
v_mov_b32 v34, s24
v_mov_b32 v35, 0
ds_write_b64 v34, v[32:33]
s_waitcnt lgkmcnt(0)
s_barrier
ds_read_b128 v[36:39], v35
ds_read_b128 v[40:43], v35 offset:16
s_mul_i32 s24, s3, 1664525
s_add_u32 s24, s24, 1013904223
s_and_b32 s24, s24, -2
s_lshl_b32 s25, s34, 6
s_add_u32 s25, s25, 0xc108800
s_cmp_lg_u32 s33, 0
s_cmov_b32 s25, 0x40000000
s_waitcnt lgkmcnt(0)
v_min3_u32 v32, v36, v38, v40
v_min3_u32 v33, v37, v39, v41
v_min_u32 v32, v32, v42
v_min_u32 v33, v33, v43
v_cmp_gt_u32_e32 vcc, v32, v33
v_mov_b32 v34, s24
s_nop 0
v_addc_co_u32_e32 v34, vcc, 0, v34, vcc
buffer_store_dword v34, v7, s[20:23], s25 offen sc0 sc1
s_barrier
s_branch .LzPRR
.LzA0:
s_mov_b32 s63, 0x6040200
s_mov_b32 s28, 0x7050312
s_bitcmp1_b32 s2, 5
s_cmov_b32 s63, 0x18161401
s_cmov_b32 s28, 0x19171513
s_bitcmp1_b32 s2, 6
s_cmov_b32 s63, s28
s_lshl_b32 s28, s33, 3
s_lshr_b32 s28, s63, s28
)" R"(
s_and_b32 s28, s28, 255
s_lshl_b32 s38, s28, 17
s_add_u32 s38, s38, 0xff80
s_lshl_b32 s39, s33, 16
s_lshl_b32 s28, s34, 9
s_add_u32 s39, s39, s28
s_sub_u32 s39, s39, 0x40000
s_lshl_b32 s37, s33, 12
s_add_u32 s37, s37, 0x10000
v_lshrrev_b32 v28, 4, v0
v_and_b32 v29, 7, v0
v_xor_b32 v28, v28, v29
v_lshlrev_b32 v28, 4, v28
v_lshrrev_b32 v29, 3, v0
v_lshl_or_b32 v8, v29, 11, v28
v_xor_b32 v9, 64, v8
v_add_u32 v9, 15360, v9
v_add_u32 v10, 30720, v8
v_add_u32 v11, 30720, v9
s_and_b32 s28, s33, 1
s_lshl_b32 s28, s28, 7
v_lshrrev_b32 v29, 5, v0
v_mul_u32_u24 v29, 0x2220, v29
v_xor_b32 v29, v29, v1
v_xor_b32 v12, s28, v29
v_xor_b32 v13, 64, v12
v_add_u32 v13, 15360, v13
v_add_u32 v14, 30720, v12
v_add_u32 v15, 30720, v13
v_min_u32 v7, 1, v0
v_lshlrev_b32 v7, 30, v7
s_waitcnt lgkmcnt(0)
s_mov_b32 s28, 0xc108400
s_load_dword s3, s[74:75], s28
s_mov_b32 s20, s74
s_mov_b32 s21, s75
s_mov_b32 s22, 0xc1ba000
s_mov_b32 s23, 0x20000
s_mov_b32 s4, s66
s_mov_b32 s5, s67
s_mov_b32 s6, 0x400000
s_mov_b32 s7, 0x20000
s_cmp_eq_u32 s91, 1
s_cselect_b32 s8, s72, s70
s_cselect_b32 s9, s73, s71
s_mov_b32 s10, 0x800000
s_mov_b32 s11, 0x20000
s_lshr_b32 s28, s2, 3
s_cmp_eq_u32 s28, 14
s_cbranch_scc1 .LzPRB
.LzPRR:
s_add_u32 m0, s37, 0
s_add_u32 s39, s39, 0x40000
buffer_load_dwordx4 v12, s[8:11], s39 offen offset:0 lds
buffer_load_dwordx4 v13, s[8:11], s39 offen offset:1024 lds
buffer_load_dwordx4 v14, s[8:11], s39 offen offset:2048 lds
buffer_load_dwordx4 v15, s[8:11], s39 offen offset:3072 lds
s_add_u32 m0, s36, 0
s_add_u32 s38, s38, 0xffff0080
buffer_load_dwordx4 v8, s[4:7], s38 offen offset:0 lds
buffer_load_dwordx4 v9, s[4:7], s38 offen offset:1024 lds
buffer_load_dwordx4 v10, s[4:7], s38 offen offset:2048 lds
buffer_load_dwordx4 v11, s[4:7], s38 offen offset:3072 lds
s_add_u32 m0, s36, 4096
)" R"(
s_add_u32 s38, s38, 0x10000
buffer_load_dwordx4 v8, s[4:7], s38 offen offset:0 lds
buffer_load_dwordx4 v9, s[4:7], s38 offen offset:1024 lds
buffer_load_dwordx4 v10, s[4:7], s38 offen offset:2048 lds
buffer_load_dwordx4 v11, s[4:7], s38 offen offset:3072 lds
s_add_u32 m0, s37, 16384
s_add_u32 s39, s39, 0x40000
buffer_load_dwordx4 v12, s[8:11], s39 offen offset:0 lds
buffer_load_dwordx4 v13, s[8:11], s39 offen offset:1024 lds
buffer_load_dwordx4 v14, s[8:11], s39 offen offset:2048 lds
buffer_load_dwordx4 v15, s[8:11], s39 offen offset:3072 lds
s_add_u32 m0, s36, 32768
s_add_u32 s38, s38, 0xffff0080
buffer_load_dwordx4 v8, s[4:7], s38 offen offset:0 lds
buffer_load_dwordx4 v9, s[4:7], s38 offen offset:1024 lds
buffer_load_dwordx4 v10, s[4:7], s38 offen offset:2048 lds
buffer_load_dwordx4 v11, s[4:7], s38 offen offset:3072 lds
s_add_u32 m0, s36, 36864
s_add_u32 s38, s38, 0x10000
buffer_load_dwordx4 v8, s[4:7], s38 offen offset:0 lds
buffer_load_dwordx4 v9, s[4:7], s38 offen offset:1024 lds
buffer_load_dwordx4 v10, s[4:7], s38 offen offset:2048 lds
buffer_load_dwordx4 v11, s[4:7], s38 offen offset:3072 lds
s_add_u32 m0, s37, 32768
s_add_u32 s39, s39, 0x40000
buffer_load_dwordx4 v12, s[8:11], s39 offen offset:0 lds
buffer_load_dwordx4 v13, s[8:11], s39 offen offset:1024 lds
buffer_load_dwordx4 v14, s[8:11], s39 offen offset:2048 lds
buffer_load_dwordx4 v15, s[8:11], s39 offen offset:3072 lds
s_add_u32 m0, s37, 49152
s_add_u32 s39, s39, 0x40000
buffer_load_dwordx4 v12, s[8:11], s39 offen offset:0 lds
buffer_load_dwordx4 v13, s[8:11], s39 offen offset:1024 lds
buffer_load_dwordx4 v14, s[8:11], s39 offen offset:2048 lds
buffer_load_dwordx4 v15, s[8:11], s39 offen offset:3072 lds
s_mov_b32 s12, s64
s_mov_b32 s13, s65
s_mov_b32 s14, 0x1c0000
s_mov_b32 s15, 0x20000
s_mov_b32 s16, s68
s_mov_b32 s17, s69
s_mov_b32 s18, 0x380000
s_mov_b32 s19, 0x20000
s_lshl2_add_u32 s94, s2, s33
s_mul_i32 s94, s94, 7168
v_min_u32 v2, 55, v0
v_lshlrev_b32 v2, 7, v2
buffer_load_dword v6, v2, s[16:19], s94 offen
s_load_dwordx2 s[70:71], s[0:1], 88
s_load_dwordx2 s[74:75], s[0:1], 96
v_bfe_u32 v28, v0, 1, 3
v_lshrrev_b32 v29, 4, v0
v_xor_b32 v28, v28, v29
v_lshlrev_b32 v28, 4, v28
v_and_b32 v29, 15, v0
v_lshl_or_b32 v28, v29, 7, v28
s_lshr_b32 s28, s33, 1
s_lshl_b32 s28, s28, 12
v_or_b32 v16, s28, v28
v_xor_b32 v17, 64, v16
v_bfe_u32 v28, v0, 2, 3
v_lshlrev_b32 v28, 5, v28
v_and_b32 v29, 3, v0
v_lshl_or_b32 v28, v29, 3, v28
)" R"(
v_bfe_u32 v29, v0, 2, 2
v_lshl_or_b32 v28, v29, 9, v28
v_lshrrev_b32 v29, 4, v0
v_lshl_or_b32 v28, v29, 12, v28
s_and_b32 s28, s33, 1
s_lshl_b32 s28, s28, 8
s_bitset1_b32 s28, 16
v_or_b32 v18, s28, v28
v_xor_b32 v19, 32, v18
v_xor_b32 v20, 64, v18
v_xor_b32 v21, 96, v18
v_xor_b32 v22, 128, v18
v_xor_b32 v23, 160, v18
v_xor_b32 v24, 192, v18
v_xor_b32 v25, 224, v18
s_load_dwordx4 s[76:79], s[0:1], 72
s_waitcnt vmcnt(21) lgkmcnt(0)
s_barrier
Rt 0,18,0
Rt 2,18,2048
Rt 4,19,0
Rt 6,19,2048
Rt 8,20,0
Rt 10,20,2048
Rt 12,21,0
Rt 14,21,2048
Rt 16,22,0
Rt 18,22,2048
Rt 20,23,0
Rt 22,23,2048
Rt 24,24,0
Rt 26,24,2048
Rt 28,25,0
Rt 30,25,2048
Rb 32,16,0
Rb 36,16,2048
Rb 40,16,8192
Rb 44,16,10240
Rb 48,16,16384
Rb 52,16,18432
Rb 56,16,24576
Rb 60,16,26624
s_mul_i32 s3, s3, 1664525
s_add_u32 s3, s3, 1013904223
s_cmp_eq_u32 s91, 1
s_waitcnt vmcnt(17) lgkmcnt(0)
s_barrier
s_cbranch_scc1 .LzAV
s_branch .LzAK
.LzAK:
Mz 32,0,32
Rt 64,18,16384
Mz 64,0,36
s_lshl1_add_u32 s40, s34, s91
s_lshl_b32 s40, s40, 12
Mz 96,0,40
Rt 66,18,18432
s_and_b32 s29, s38, 0xfffe0000
Mz 128,0,44
s_add_u32 s39, s39, 0x40000
s_add_u32 s40, s40, s29
Mz 160,0,48
Rt 68,19,16384
s_add_u32 m0, s37, 0
Mz 192,0,52
buffer_load_dwordx4 v12, s[8:11], s39 offen offset:0 lds
)" R"(
Mz 224,0,56
Rt 70,19,18432
v_min_u32 v3, 31, v0
v_mfma_f32_16x16x32_bf16 a[224:227], a[0:3], a[60:63], 0
v_lshlrev_b32 v3, 7, v3
v_mfma_f32_16x16x32_bf16 a[228:231], a[4:7], a[60:63], 0
Rt 72,20,16384
Mz 228,4,56
Mz 196,4,52
Rt 74,20,18432
Mz 164,4,48
Mz 132,4,44
Rt 76,21,16384
Mz 100,4,40
buffer_load_dwordx4 v13, s[8:11], s39 offen offset:1024 lds
Mz 68,4,36
Rt 78,21,18432
Mz 36,4,32
Mz 40,8,32
Rt 80,22,16384
Mz 72,8,36
Mz 104,8,40
Rt 82,22,18432
Mz 136,8,44
Mz 168,8,48
Rt 84,23,16384
Mz 200,8,52
buffer_load_dwordx4 v14, s[8:11], s39 offen offset:2048 lds
Mz 232,8,56
Rt 86,23,18432
v_mfma_f32_16x16x32_bf16 a[232:235], a[8:11], a[60:63], 0
v_mfma_f32_16x16x32_bf16 a[236:239], a[12:15], a[60:63], 0
Rt 88,24,16384
Mz 236,12,56
Mz 204,12,52
Rt 90,24,18432
Mz 172,12,48
Mz 140,12,44
Rt 92,25,16384
Mz 108,12,40
buffer_load_dwordx4 v15, s[8:11], s39 offen offset:3072 lds
Mz 76,12,36
Rt 94,25,18432
Mz 44,12,32
Mz 48,16,32
Rb 96,17,0
Mz 80,16,36
buffer_load_dword v6, v3, s[4:7], s40 offen
Mz 112,16,40
Rb 100,17,2048
Mz 144,16,44
Mz 176,16,48
Rb 104,17,8192
s_mov_b32 s28, s35
Mz 208,16,52
s_lshl2_add_u32 s28, s28, s33
s_mul_i32 s43, s28, 28672
Mz 240,16,56
Rb 108,17,10240
v_and_b32 v26, 0x70, v1
v_mfma_f32_16x16x32_bf16 a[240:243], a[16:19], a[60:63], 0
v_and_b32 v4, 48, v0
v_xor_b32 v26, v26, v4
v_mfma_f32_16x16x32_bf16 a[244:247], a[20:23], a[60:63], 0
)" R"(
Rb 112,17,16384
v_lshrrev_b32 v4, 3, v0
Mz 244,20,56
v_mul_u32_u24 v4, 896, v4
v_add_u32 v26, v26, v4
Mz 212,20,52
Rb 116,17,18432
v_xor_b32 v27, 64, v26
Mz 180,20,48
v_add_u32 v27, 6144, v27
v_bfe_u32 v28, v0, 1, 3
Mz 148,20,44
Rb 120,17,24576
v_lshrrev_b32 v4, 4, v0
Mz 116,20,40
v_xor_b32 v28, v28, v4
v_lshlrev_b32 v28, 4, v28
Mz 84,20,36
Rb 124,17,26624
v_and_b32 v4, 15, v0
Mz 52,20,32
v_lshl_or_b32 v28, v4, 7, v28
s_add_u32 s28, s36, 0x18000
Mz 56,24,32
v_add_u32 v28, s28, v28
v_xor_b32 v29, 64, v28
Mz 88,24,36
Mz 120,24,40
Mz 152,24,44
Mz 184,24,48
Mz 216,24,52
Mz 248,24,56
v_mfma_f32_16x16x32_bf16 a[248:251], a[24:27], a[60:63], 0
v_mfma_f32_16x16x32_bf16 a[252:255], a[28:31], a[60:63], 0
Mz 252,28,56
Mz 220,28,52
Mz 188,28,48
Mz 156,28,44
Mz 124,28,40
Mz 92,28,36
Mz 60,28,32
s_waitcnt vmcnt(10) lgkmcnt(0)
s_barrier
Mv 32,64,96
Rt 0,18,32768
s_bfe_u32 s88, s2, 0x20005
Mv 64,64,100
s_add_u32 s38, s38, 0xffff0080
s_and_b32 s28, s2, 15
Mv 96,64,104
Rt 2,18,34816
s_add_u32 m0, s36, 0
Mv 128,64,108
buffer_load_dwordx4 v8, s[4:7], s38 offen offset:0 lds
Mv 160,64,112
Rt 4,19,32768
s_lshl4_add_u32 s88, s88, s28
Mv 192,64,116
s_lshl2_add_u32 s88, s88, s33
s_mul_i32 s88, s88, 4096
Mv 224,64,120
Rt 6,19,34816
s_add_u32 s88, s88, 0x100000
Ma 224,64,124
buffer_load_dwordx4 v9, s[4:7], s38 offen offset:1024 lds
)" R"(
Ma 228,68,124
Rt 8,20,32768
v_min_u32 v5, 31, v0
Mv 228,68,120
v_lshlrev_b32 v5, 7, v5
s_mov_b32 s90, 0x100000
Mv 196,68,116
Rt 10,20,34816
s_mov_b64 s[24:25], s[8:9]
Mv 164,68,112
buffer_load_dwordx4 v10, s[4:7], s38 offen offset:2048 lds
Mv 132,68,108
Rt 12,21,32768
s_mov_b64 s[26:27], s[10:11]
Mv 100,68,104
Mv 68,68,100
Rt 14,21,34816
Mv 36,68,96
buffer_load_dwordx4 v11, s[4:7], s38 offen offset:3072 lds
Mv 40,72,96
Rt 16,22,32768
Mv 72,72,100
s_add_u32 s38, s38, 0x10000
Mv 104,72,104
Rt 18,22,34816
s_add_u32 m0, s36, 4096
Mv 136,72,108
buffer_load_dwordx4 v8, s[4:7], s38 offen offset:0 lds
Mv 168,72,112
Rt 20,23,32768
Mv 200,72,116
Mv 232,72,120
Rt 22,23,34816
Ma 232,72,124
buffer_load_dwordx4 v9, s[4:7], s38 offen offset:1024 lds
Ma 236,76,124
Rt 24,24,32768
Mv 236,76,120
Mv 204,76,116
Rt 26,24,34816
Mv 172,76,112
buffer_load_dwordx4 v10, s[4:7], s38 offen offset:2048 lds
Mv 140,76,108
Rt 28,25,32768
Mv 108,76,104
Mv 76,76,100
Rt 30,25,34816
Mv 44,76,96
buffer_load_dwordx4 v11, s[4:7], s38 offen offset:3072 lds
Mv 48,80,96
Rb 32,16,32768
Mv 80,80,100
Mv 112,80,104
Rb 36,16,34816
Mv 144,80,108
s_add_u32 s39, s39, 0x40000
Mv 176,80,112
Rb 40,16,40960
s_add_u32 m0, s37, 16384
Mv 208,80,116
buffer_load_dwordx4 v12, s[8:11], s39 offen offset:0 lds
Mv 240,80,120
Rb 44,16,43008
Ma 240,80,124
)" R"(
Ma 244,84,124
Rb 48,16,49152
Mv 244,84,120
Mv 212,84,116
Rb 52,16,51200
Mv 180,84,112
buffer_load_dwordx4 v13, s[8:11], s39 offen offset:1024 lds
Mv 148,84,108
Rb 56,16,57344
Mv 116,84,104
Mv 84,84,100
Rb 60,16,59392
Mv 52,84,96
Mv 56,88,96
Mv 88,88,100
buffer_load_dwordx4 v14, s[8:11], s39 offen offset:2048 lds
Mv 120,88,104
Mv 152,88,108
Mv 184,88,112
Mv 216,88,116
Mv 248,88,120
Ma 248,88,124
buffer_load_dwordx4 v15, s[8:11], s39 offen offset:3072 lds
Ma 252,92,124
Mv 252,92,120
s_add_u32 s88, s88, s90
Mv 220,92,116
Mv 188,92,112
buffer_load_dword v6, v5, s[24:27], s88 offen
Mv 156,92,108
Mv 124,92,104
Mv 92,92,100
Mv 60,92,96
s_waitcnt vmcnt(19) lgkmcnt(0)
s_barrier
Mv 32,0,32
Rt 64,18,49152
s_lshl_b32 s28, s34, 21
Mv 64,0,36
s_lshl_b32 s29, s91, 25
s_add_u32 s28, s28, s29
Mv 96,0,40
Rt 66,18,51200
s_bfe_u32 s29, s63, 0x80000
Mv 128,0,44
s_add_u32 s39, s39, 0x40000
s_lshl_b32 s29, s29, 16
Mv 160,0,48
Rt 68,19,49152
s_add_u32 m0, s37, 32768
Mv 192,0,52
buffer_load_dwordx4 v12, s[8:11], s39 offen offset:0 lds
Mv 224,0,56
Rt 70,19,51200
s_add_u32 s45, s28, s29
Ma 224,0,60
s_bfe_u32 s29, s63, 0x80008
s_lshl_b32 s29, s29, 16
Ma 228,4,60
Rt 72,20,49152
s_add_u32 s46, s28, s29
Mv 228,4,56
s_bfe_u32 s29, s63, 0x80010
s_lshl_b32 s29, s29, 16
Mv 196,4,52
)" R"(
Rt 74,20,51200
s_add_u32 s47, s28, s29
Mv 164,4,48
s_bfe_u32 s29, s63, 0x80018
s_lshl_b32 s29, s29, 16
Mv 132,4,44
Rt 76,21,49152
s_add_u32 s48, s28, s29
Mv 100,4,40
buffer_load_dwordx4 v13, s[8:11], s39 offen offset:1024 lds
Mv 68,4,36
Rt 78,21,51200
s_lshl_b32 s50, s34, 21
Mv 36,4,32
s_add_u32 s50, s50, 0x6140000
s_lshl2_add_u32 s28, s91, s93
Mv 40,8,32
Rt 80,22,49152
s_lshl_b32 s28, s28, 7
Mv 72,8,36
s_add_u32 s49, s28, s50
s_lshl_b32 s52, s35, 7
Mv 104,8,40
Rt 82,22,51200
s_add_u32 s52, s52, s50
Mv 136,8,44
s_add_u32 s52, s52, 0xd000
s_lshl4_add_u32 s28, s34, s35
Mv 168,8,48
Rt 84,23,49152
s_lshl_b32 s51, s28, 17
Mv 200,8,52
buffer_load_dwordx4 v14, s[8:11], s39 offen offset:2048 lds
Mv 232,8,56
Rt 86,23,51200
s_add_u32 s51, s51, 0x4000000
Ma 232,8,60
s_cmp_lg_u32 s33, 0
s_cmov_b32 s49, 0x40000000
Ma 236,12,60
Rt 88,24,49152
s_mov_b32 s53, 0x40000
Mv 236,12,56
s_mov_b32 s54, 0x3db8aa3b
s_mul_i32 s41, s33, 7168
Mv 204,12,52
Rt 90,24,51200
s_mul_i32 s42, s33, 0x70000
Mv 172,12,48
s_lshl_b32 s28, s34, 9
s_add_u32 s42, s42, s28
Mv 140,12,44
Rt 92,25,49152
s_lshl_b32 s89, s34, 21
Mv 108,12,40
buffer_load_dwordx4 v15, s[8:11], s39 offen offset:3072 lds
Mv 76,12,36
Rt 94,25,51200
s_mov_b32 s62, 0x4e800000
Mv 44,12,32
s_add_u32 s67, s89, 0x6000000
s_lshl3_add_u32 s28, s34, s35
Mv 48,16,32
Rb 96,17,32768
)" R"(
s_add_u32 s64, s51, 0x100000
s_add_u32 m0, s36, 131072
s_add_u32 s44, s43, 0
Mv 80,16,36
s_lshl2_add_u32 s28, s28, s33
s_lshl2_add_u32 s65, s35, s33
buffer_load_dwordx4 v26, s[12:15], s44 offen offset:0 lds
Mv 112,16,40
Rb 100,17,34816
s_lshl_b32 s65, s65, 7
Mv 144,16,44
s_add_u32 s65, s65, s50
s_add_u32 s65, s65, 0xc000
Mv 176,16,48
Rb 104,17,40960
s_lshl_b32 s66, s28, 10
Mv 208,16,52
s_add_u32 s66, s66, 0xc000000
s_mov_b32 s28, s35
buffer_load_dwordx4 v27, s[12:15], s44 offen offset:1024 lds
Mv 240,16,56
Rb 108,17,43008
s_lshl2_add_u32 s28, s28, s33
Ma 240,16,60
s_lshl_b32 s29, s28, 5
s_add_u32 s29, s29, s34
Ma 244,20,60
Rb 112,17,49152
s_lshl_b32 s73, s29, 15
Mv 244,20,56
s_add_u32 s73, s73, 0x8000000
s_lshr_b32 s29, s36, 1
s_add_u32 s44, s43, 12288
buffer_load_dwordx4 v26, s[12:15], s44 offen offset:2048 lds
Mv 212,20,52
Rb 116,17,51200
s_and_b32 s29, s29, 0x2000
Mv 180,20,48
s_add_u32 s73, s73, s29
s_lshl4_add_u32 s28, s28, s34
Mv 148,20,44
Rb 120,17,57344
s_lshl_b32 s68, s28, 8
Mv 116,20,40
s_add_u32 s68, s68, 0xc110000
s_add_u32 s55, s89, 0x120000
buffer_load_dwordx4 v27, s[12:15], s44 offen offset:3072 lds
Mv 84,20,36
Rb 124,17,59392
s_lshl_b32 s28, s35, 1
Mv 52,20,32
s_sub_u32 s57, 14, s28
s_mov_b32 s59, s89
Mv 56,24,32
s_mov_b32 s58, 2
s_add_u32 m0, s36, 135168
s_add_u32 s44, s43, 128
Mv 88,24,36
s_cmp_eq_u32 s35, 7
s_cmov_b32 s55, s89
buffer_load_dwordx4 v26, s[12:15], s44 offen offset:0 lds
Mv 120,24,40
s_cmp_eq_u32 s35, 7
s_cmov_b32 s57, 16
)" R"(
Mv 152,24,44
s_cmp_eq_u32 s35, 7
s_cmov_b32 s59, s67
Mv 184,24,48
s_add_u32 s56, s55, 0x2000000
Mv 216,24,52
buffer_load_dwordx4 v27, s[12:15], s44 offen offset:1024 lds
Mv 248,24,56
Ma 248,24,60
Ma 252,28,60
Mv 252,28,56
s_add_u32 s44, s43, 12416
buffer_load_dwordx4 v26, s[12:15], s44 offen offset:2048 lds
Mv 220,28,52
Mv 188,28,48
Mv 156,28,44
Mv 124,28,40
buffer_load_dwordx4 v27, s[12:15], s44 offen offset:3072 lds
Mv 92,28,36
Mv 60,28,32
s_waitcnt vmcnt(17) lgkmcnt(0)
s_barrier
Mv 32,64,96
Rt 0,18,0
Mv 64,64,100
s_add_u32 s38, s38, 0xffff0080
Mv 96,64,104
Rt 2,18,2048
s_add_u32 m0, s36, 32768
Mv 128,64,108
buffer_load_dwordx4 v8, s[4:7], s38 offen offset:0 lds
Mv 160,64,112
Rt 4,19,0
Mv 192,64,116
Mv 224,64,120
Rt 6,19,2048
Ma 224,64,124
buffer_load_dwordx4 v9, s[4:7], s38 offen offset:1024 lds
Ma 228,68,124
Rt 8,20,0
s_lshl2_add_u32 s28, s2, s33
Mv 228,68,120
s_lshl_b32 s28, s28, 11
s_add_u32 s28, s28, 0xc100000
Mv 196,68,116
Rt 10,20,2048
v_min_u32 v3, 15, v0
Mv 164,68,112
buffer_load_dwordx4 v10, s[4:7], s38 offen offset:2048 lds
Mv 132,68,108
Rt 12,21,0
v_lshlrev_b32 v3, 7, v3
Mv 100,68,104
Mv 68,68,100
Rt 14,21,2048
Mv 36,68,96
buffer_load_dwordx4 v11, s[4:7], s38 offen offset:3072 lds
Mv 40,72,96
Rt 16,22,0
Mv 72,72,100
s_add_u32 s38, s38, 0x10000
Mv 104,72,104
Rt 18,22,2048
s_add_u32 m0, s36, 36864
Mv 136,72,108
)" R"(
buffer_load_dwordx4 v8, s[4:7], s38 offen offset:0 lds
Mv 168,72,112
Rt 20,23,0
Mv 200,72,116
Mv 232,72,120
Rt 22,23,2048
Ma 232,72,124
buffer_load_dwordx4 v9, s[4:7], s38 offen offset:1024 lds
Ma 236,76,124
Rt 24,24,0
Mv 236,76,120
Mv 204,76,116
Rt 26,24,2048
Mv 172,76,112
buffer_load_dwordx4 v10, s[4:7], s38 offen offset:2048 lds
Mv 140,76,108
Rt 28,25,0
Mv 108,76,104
Mv 76,76,100
Rt 30,25,2048
Mv 44,76,96
buffer_load_dwordx4 v11, s[4:7], s38 offen offset:3072 lds
Mv 48,80,96
Rb 32,16,0
Mv 80,80,100
Mv 112,80,104
Rb 36,16,2048
Mv 144,80,108
s_add_u32 s39, s39, 0x40000
Mv 176,80,112
Rb 40,16,8192
s_add_u32 m0, s37, 49152
Mv 208,80,116
buffer_load_dwordx4 v12, s[8:11], s39 offen offset:0 lds
Mv 240,80,120
Rb 44,16,10240
Ma 240,80,124
Ma 244,84,124
Rb 48,16,16384
Mv 244,84,120
Mv 212,84,116
Rb 52,16,18432
Mv 180,84,112
buffer_load_dwordx4 v13, s[8:11], s39 offen offset:1024 lds
Mv 148,84,108
Rb 56,16,24576
Mv 116,84,104
Mv 84,84,100
Rb 60,16,26624
Mv 52,84,96
Mv 56,88,96
Mv 88,88,100
buffer_load_dwordx4 v14, s[8:11], s39 offen offset:2048 lds
Mv 120,88,104
Mv 152,88,108
Mv 184,88,112
Mv 216,88,116
Mv 248,88,120
Ma 248,88,124
buffer_load_dwordx4 v15, s[8:11], s39 offen offset:3072 lds
Ma 252,92,124
Mv 252,92,120
s_add_u32 s88, s88, s90
s_mov_b32 s40, 5
)" R"(
Mv 220,92,116
Mv 188,92,112
buffer_load_dword v6, v5, s[24:27], s88 offen
Mv 156,92,108
Mv 124,92,104
buffer_load_dword v6, v3, s[20:23], s28 offen
Mv 92,92,100
Mv 60,92,96
s_waitcnt vmcnt(27) lgkmcnt(0)
s_barrier
.LzPAK:
Mv 32,0,32
Rt 64,18,16384
Mv 64,0,36
Mv 96,0,40
Rt 66,18,18432
Mv 128,0,44
s_add_u32 s39, s39, 0x40000
Mv 160,0,48
Rt 68,19,16384
s_add_u32 m0, s37, 0
Mv 192,0,52
buffer_load_dwordx4 v12, s[8:11], s39 offen offset:0 lds
Mv 224,0,56
Rt 70,19,18432
Ma 224,0,60
Ma 228,4,60
Rt 72,20,16384
Mv 228,4,56
Mv 196,4,52
Rt 74,20,18432
Mv 164,4,48
Mv 132,4,44
Rt 76,21,16384
Mv 100,4,40
buffer_load_dwordx4 v13, s[8:11], s39 offen offset:1024 lds
Mv 68,4,36
Rt 78,21,18432
Mv 36,4,32
Mv 40,8,32
Rt 80,22,16384
Mv 72,8,36
Mv 104,8,40
Rt 82,22,18432
Mv 136,8,44
Mv 168,8,48
Rt 84,23,16384
Mv 200,8,52
buffer_load_dwordx4 v14, s[8:11], s39 offen offset:2048 lds
Mv 232,8,56
Rt 86,23,18432
Ma 232,8,60
Ma 236,12,60
Rt 88,24,16384
Mv 236,12,56
Mv 204,12,52
Rt 90,24,18432
Mv 172,12,48
Mv 140,12,44
Rt 92,25,16384
Mv 108,12,40
buffer_load_dwordx4 v15, s[8:11], s39 offen offset:3072 lds
Mv 76,12,36
Rt 94,25,18432
Mv 44,12,32
)" R"(
Mv 48,16,32
Rb 96,17,0
Mv 80,16,36
Mv 112,16,40
Rb 100,17,2048
Mv 144,16,44
Mv 176,16,48
Rb 104,17,8192
Mv 208,16,52
Mv 240,16,56
Rb 108,17,10240
Ma 240,16,60
Ma 244,20,60
Rb 112,17,16384
Mv 244,20,56
Mv 212,20,52
Rb 116,17,18432
Mv 180,20,48
Mv 148,20,44
Rb 120,17,24576
Mv 116,20,40
Mv 84,20,36
Rb 124,17,26624
Mv 52,20,32
Mv 56,24,32
Mv 88,24,36
Mv 120,24,40
Mv 152,24,44
Mv 184,24,48
Mv 216,24,52
Mv 248,24,56
Ma 248,24,60
Ma 252,28,60
Mv 252,28,56
Mv 220,28,52
Mv 188,28,48
Mv 156,28,44
Mv 124,28,40
Mv 92,28,36
Mv 60,28,32
s_waitcnt vmcnt(9) lgkmcnt(0)
s_barrier
Mv 32,64,96
Rt 0,18,32768
Mv 64,64,100
s_add_u32 s38, s38, 0xffff0080
Mv 96,64,104
Rt 2,18,34816
s_add_u32 m0, s36, 0
Mv 128,64,108
buffer_load_dwordx4 v8, s[4:7], s38 offen offset:0 lds
Mv 160,64,112
Rt 4,19,32768
Mv 192,64,116
Mv 224,64,120
Rt 6,19,34816
Ma 224,64,124
buffer_load_dwordx4 v9, s[4:7], s38 offen offset:1024 lds
Ma 228,68,124
Rt 8,20,32768
Mv 228,68,120
Mv 196,68,116
Rt 10,20,34816
Mv 164,68,112
buffer_load_dwordx4 v10, s[4:7], s38 offen offset:2048 lds
)" R"(
Mv 132,68,108
Rt 12,21,32768
Mv 100,68,104
Mv 68,68,100
Rt 14,21,34816
Mv 36,68,96
buffer_load_dwordx4 v11, s[4:7], s38 offen offset:3072 lds
Mv 40,72,96
Rt 16,22,32768
Mv 72,72,100
s_add_u32 s38, s38, 0x10000
Mv 104,72,104
Rt 18,22,34816
s_add_u32 m0, s36, 4096
Mv 136,72,108
buffer_load_dwordx4 v8, s[4:7], s38 offen offset:0 lds
Mv 168,72,112
Rt 20,23,32768
Mv 200,72,116
Mv 232,72,120
Rt 22,23,34816
Ma 232,72,124
buffer_load_dwordx4 v9, s[4:7], s38 offen offset:1024 lds
Ma 236,76,124
Rt 24,24,32768
Mv 236,76,120
Mv 204,76,116
Rt 26,24,34816
Mv 172,76,112
buffer_load_dwordx4 v10, s[4:7], s38 offen offset:2048 lds
Mv 140,76,108
Rt 28,25,32768
Mv 108,76,104
Mv 76,76,100
Rt 30,25,34816
Mv 44,76,96
buffer_load_dwordx4 v11, s[4:7], s38 offen offset:3072 lds
Mv 48,80,96
Rb 32,16,32768
Mv 80,80,100
Mv 112,80,104
Rb 36,16,34816
Mv 144,80,108
s_add_u32 s39, s39, 0x40000
Mv 176,80,112
Rb 40,16,40960
s_add_u32 m0, s37, 16384
Mv 208,80,116
buffer_load_dwordx4 v12, s[8:11], s39 offen offset:0 lds
Mv 240,80,120
Rb 44,16,43008
Ma 240,80,124
Ma 244,84,124
Rb 48,16,49152
Mv 244,84,120
Mv 212,84,116
Rb 52,16,51200
Mv 180,84,112
buffer_load_dwordx4 v13, s[8:11], s39 offen offset:1024 lds
Mv 148,84,108
Rb 56,16,57344
Mv 116,84,104
Mv 84,84,100
Rb 60,16,59392
)" R"(
Mv 52,84,96
Mv 56,88,96
Mv 88,88,100
buffer_load_dwordx4 v14, s[8:11], s39 offen offset:2048 lds
Mv 120,88,104
Mv 152,88,108
Mv 184,88,112
Mv 216,88,116
Mv 248,88,120
Ma 248,88,124
buffer_load_dwordx4 v15, s[8:11], s39 offen offset:3072 lds
Ma 252,92,124
Mv 252,92,120
s_add_u32 s88, s88, s90
Mv 220,92,116
Mv 188,92,112
buffer_load_dword v6, v5, s[24:27], s88 offen
Mv 156,92,108
Mv 124,92,104
Mv 92,92,100
Mv 60,92,96
s_waitcnt vmcnt(18) lgkmcnt(0)
s_barrier
Mv 32,0,32
Rt 64,18,49152
Mv 64,0,36
Mv 96,0,40
Rt 66,18,51200
Mv 128,0,44
s_add_u32 s39, s39, 0x40000
Mv 160,0,48
Rt 68,19,49152
s_add_u32 m0, s37, 32768
Mv 192,0,52
buffer_load_dwordx4 v12, s[8:11], s39 offen offset:0 lds
Mv 224,0,56
Rt 70,19,51200
Ma 224,0,60
Ma 228,4,60
Rt 72,20,49152
Mv 228,4,56
Mv 196,4,52
Rt 74,20,51200
Mv 164,4,48
Mv 132,4,44
Rt 76,21,49152
Mv 100,4,40
buffer_load_dwordx4 v13, s[8:11], s39 offen offset:1024 lds
Mv 68,4,36
Rt 78,21,51200
Mv 36,4,32
Mv 40,8,32
Rt 80,22,49152
Mv 72,8,36
Mv 104,8,40
Rt 82,22,51200
Mv 136,8,44
Mv 168,8,48
Rt 84,23,49152
Mv 200,8,52
buffer_load_dwordx4 v14, s[8:11], s39 offen offset:2048 lds
Mv 232,8,56
Rt 86,23,51200
Ma 232,8,60
Ma 236,12,60
)" R"(
Rt 88,24,49152
Mv 236,12,56
Mv 204,12,52
Rt 90,24,51200
Mv 172,12,48
Mv 140,12,44
Rt 92,25,49152
Mv 108,12,40
buffer_load_dwordx4 v15, s[8:11], s39 offen offset:3072 lds
Mv 76,12,36
Rt 94,25,51200
Mv 44,12,32
Mv 48,16,32
Rb 96,17,32768
Mv 80,16,36
Mv 112,16,40
Rb 100,17,34816
Mv 144,16,44
Mv 176,16,48
Rb 104,17,40960
Mv 208,16,52
Mv 240,16,56
Rb 108,17,43008
Ma 240,16,60
Ma 244,20,60
Rb 112,17,49152
Mv 244,20,56
Mv 212,20,52
Rb 116,17,51200
Mv 180,20,48
Mv 148,20,44
Rb 120,17,57344
Mv 116,20,40
Mv 84,20,36
Rb 124,17,59392
Mv 52,20,32
Mv 56,24,32
Mv 88,24,36
Mv 120,24,40
Mv 152,24,44
Mv 184,24,48
Mv 216,24,52
Mv 248,24,56
Ma 248,24,60
Ma 252,28,60
Mv 252,28,56
Mv 220,28,52
Mv 188,28,48
Mv 156,28,44
Mv 124,28,40
Mv 92,28,36
Mv 60,28,32
s_waitcnt vmcnt(9) lgkmcnt(0)
s_barrier
Mv 32,64,96
Rt 0,18,0
Mv 64,64,100
s_add_u32 s38, s38, 0xffff0080
Mv 96,64,104
Rt 2,18,2048
s_add_u32 m0, s36, 32768
Mv 128,64,108
buffer_load_dwordx4 v8, s[4:7], s38 offen offset:0 lds
Mv 160,64,112
Rt 4,19,0
)" R"(
Mv 192,64,116
Mv 224,64,120
Rt 6,19,2048
Ma 224,64,124
buffer_load_dwordx4 v9, s[4:7], s38 offen offset:1024 lds
Ma 228,68,124
Rt 8,20,0
Mv 228,68,120
Mv 196,68,116
Rt 10,20,2048
Mv 164,68,112
buffer_load_dwordx4 v10, s[4:7], s38 offen offset:2048 lds
Mv 132,68,108
Rt 12,21,0
Mv 100,68,104
Mv 68,68,100
Rt 14,21,2048
Mv 36,68,96
buffer_load_dwordx4 v11, s[4:7], s38 offen offset:3072 lds
Mv 40,72,96
Rt 16,22,0
Mv 72,72,100
s_add_u32 s38, s38, 0x10000
Mv 104,72,104
Rt 18,22,2048
s_add_u32 m0, s36, 36864
Mv 136,72,108
buffer_load_dwordx4 v8, s[4:7], s38 offen offset:0 lds
Mv 168,72,112
Rt 20,23,0
Mv 200,72,116
Mv 232,72,120
Rt 22,23,2048
Ma 232,72,124
buffer_load_dwordx4 v9, s[4:7], s38 offen offset:1024 lds
Ma 236,76,124
Rt 24,24,0
Mv 236,76,120
Mv 204,76,116
Rt 26,24,2048
Mv 172,76,112
buffer_load_dwordx4 v10, s[4:7], s38 offen offset:2048 lds
Mv 140,76,108
Rt 28,25,0
Mv 108,76,104
Mv 76,76,100
Rt 30,25,2048
Mv 44,76,96
buffer_load_dwordx4 v11, s[4:7], s38 offen offset:3072 lds
Mv 48,80,96
Rb 32,16,0
Mv 80,80,100
Mv 112,80,104
Rb 36,16,2048
Mv 144,80,108
s_add_u32 s39, s39, 0x40000
Mv 176,80,112
Rb 40,16,8192
s_add_u32 m0, s37, 49152
Mv 208,80,116
buffer_load_dwordx4 v12, s[8:11], s39 offen offset:0 lds
Mv 240,80,120
Rb 44,16,10240
Ma 240,80,124
)" R"(
Ma 244,84,124
Rb 48,16,16384
Mv 244,84,120
Mv 212,84,116
Rb 52,16,18432
Mv 180,84,112
buffer_load_dwordx4 v13, s[8:11], s39 offen offset:1024 lds
Mv 148,84,108
Rb 56,16,24576
Mv 116,84,104
Mv 84,84,100
Rb 60,16,26624
Mv 52,84,96
Mv 56,88,96
Mv 88,88,100
buffer_load_dwordx4 v14, s[8:11], s39 offen offset:2048 lds
Mv 120,88,104
Mv 152,88,108
Mv 184,88,112
Mv 216,88,116
Mv 248,88,120
Ma 248,88,124
buffer_load_dwordx4 v15, s[8:11], s39 offen offset:3072 lds
Ma 252,92,124
Mv 252,92,120
s_add_u32 s88, s88, s90
Mv 220,92,116
s_sub_u32 s40, s40, 1
Mv 188,92,112
buffer_load_dword v6, v5, s[24:27], s88 offen
Mv 156,92,108
Mv 124,92,104
Mv 92,92,100
Mv 60,92,96
s_cmp_lg_u32 s40, 0
s_waitcnt vmcnt(18) lgkmcnt(0)
s_barrier
s_cbranch_scc1 .LzPAK
.LzPAKX:
Mv 32,0,32
Rt 64,18,16384
Mv 64,0,36
Rb 192,28,32768
Mv 96,0,40
Rt 66,18,18432
Mv 128,0,44
s_add_u32 s39, s39, 0x40000
Rb 196,28,34816
Mv 160,0,48
Rt 68,19,16384
s_add_u32 m0, s37, 0
Mv 192,0,52
buffer_load_dwordx4 v12, s[8:11], s39 offen offset:0 lds
Mv 224,0,56
Rt 70,19,18432
Ma 224,0,60
Rb 200,29,32768
Ma 228,4,60
Rt 72,20,16384
Mv 228,4,56
Rb 204,29,34816
Mv 196,4,52
Rt 74,20,18432
Mv 164,4,48
Rb 208,28,36864
)" R"(
Mv 132,4,44
Rt 76,21,16384
Mv 100,4,40
buffer_load_dwordx4 v13, s[8:11], s39 offen offset:1024 lds
Mv 68,4,36
Rt 78,21,18432
Mv 36,4,32
Rb 212,28,38912
Mv 40,8,32
Rt 80,22,16384
Mv 72,8,36
Rb 216,29,36864
Mv 104,8,40
Rt 82,22,18432
Mv 136,8,44
Rb 220,29,38912
Mv 168,8,48
Rt 84,23,16384
Mv 200,8,52
buffer_load_dwordx4 v14, s[8:11], s39 offen offset:2048 lds
Mv 232,8,56
Rt 86,23,18432
Ma 232,8,60
Ma 236,12,60
Rt 88,24,16384
Mv 236,12,56
Mv 204,12,52
Rt 90,24,18432
Mv 172,12,48
Mv 140,12,44
Rt 92,25,16384
Mv 108,12,40
buffer_load_dwordx4 v15, s[8:11], s39 offen offset:3072 lds
Mv 76,12,36
Rt 94,25,18432
Mv 44,12,32
s_add_u32 s44, s43, 256
s_waitcnt lgkmcnt(6)
Mv 48,16,32
Rb 96,17,0
s_add_u32 m0, s36, 131072
Mv 80,16,36
buffer_load_dwordx4 v26, s[12:15], s44 offen offset:0 lds
Mv 112,16,40
Rb 100,17,2048
Mv 144,16,44
Mv 176,16,48
Rb 104,17,8192
Mv 208,16,52
buffer_load_dwordx4 v27, s[12:15], s44 offen offset:1024 lds
Mv 240,16,56
Rb 108,17,10240
Ma 240,16,60
Ma 244,20,60
Rb 112,17,16384
Mv 244,20,56
s_add_u32 s44, s43, 12544
buffer_load_dwordx4 v26, s[12:15], s44 offen offset:2048 lds
Mv 212,20,52
Rb 116,17,18432
Mv 180,20,48
Mv 148,20,44
Rb 120,17,24576
Mv 116,20,40
)" R"(
buffer_load_dwordx4 v27, s[12:15], s44 offen offset:3072 lds
Mv 84,20,36
Rb 124,17,26624
Mv 52,20,32
Mv 56,24,32
s_add_u32 m0, s36, 135168
s_add_u32 s44, s43, 384
Mv 88,24,36
buffer_load_dwordx4 v26, s[12:15], s44 offen offset:0 lds
Mv 120,24,40
Mv 152,24,44
Mv 184,24,48
Mv 216,24,52
buffer_load_dwordx4 v27, s[12:15], s44 offen offset:1024 lds
Mv 248,24,56
Ma 248,24,60
Ma 252,28,60
Mv 252,28,56
s_add_u32 s44, s43, 12672
buffer_load_dwordx4 v26, s[12:15], s44 offen offset:2048 lds
Mv 220,28,52
Mv 188,28,48
Mv 156,28,44
s_mov_b32 s85, 0xc108800
v_and_b32 v2, 7, v0
v_lshlrev_b32 v2, 7, v2
Mv 124,28,40
buffer_load_dwordx4 v27, s[12:15], s44 offen offset:3072 lds
Mv 92,28,36
buffer_load_dword v4, v2, s[20:23], s85 offen sc0 sc1
Mv 60,28,32
s_waitcnt vmcnt(18) lgkmcnt(0)
s_barrier
Mv 32,64,96
Rt 0,18,32768
Mv 64,64,100
s_add_u32 s38, s38, 0xffff0080
Mv 96,64,104
Rt 2,18,34816
s_add_u32 m0, s36, 0
Mv 128,64,108
buffer_load_dwordx4 v8, s[4:7], s38 offen offset:0 lds
Mv 160,64,112
Rt 4,19,32768
Mv 192,64,116
Mv 224,64,120
Rt 6,19,34816
Ma 224,64,124
buffer_load_dwordx4 v9, s[4:7], s38 offen offset:1024 lds
Ma 228,68,124
Rt 8,20,32768
Mv 228,68,120
Mv 196,68,116
Rt 10,20,34816
Mv 164,68,112
buffer_load_dwordx4 v10, s[4:7], s38 offen offset:2048 lds
Mv 132,68,108
Rt 12,21,32768
Mv 100,68,104
Mv 68,68,100
Rt 14,21,34816
Mv 36,68,96
buffer_load_dwordx4 v11, s[4:7], s38 offen offset:3072 lds
Mv 40,72,96
Rt 16,22,32768
)" R"(
Mv 72,72,100
s_add_u32 s38, s38, 0x10000
Mv 104,72,104
Rt 18,22,34816
s_add_u32 m0, s36, 4096
Mv 136,72,108
buffer_load_dwordx4 v8, s[4:7], s38 offen offset:0 lds
Mv 168,72,112
Rt 20,23,32768
Mv 200,72,116
Mv 232,72,120
Rt 22,23,34816
Ma 232,72,124
buffer_load_dwordx4 v9, s[4:7], s38 offen offset:1024 lds
Ma 236,76,124
Rt 24,24,32768
Mv 236,76,120
Mv 204,76,116
Rt 26,24,34816
Mv 172,76,112
buffer_load_dwordx4 v10, s[4:7], s38 offen offset:2048 lds
Mv 140,76,108
Rt 28,25,32768
Mv 108,76,104
Mv 76,76,100
Rt 30,25,34816
Mv 44,76,96
buffer_load_dwordx4 v11, s[4:7], s38 offen offset:3072 lds
Mv 48,80,96
Rb 32,16,32768
Mv 80,80,100
Mv 112,80,104
Rb 36,16,34816
Mv 144,80,108
s_add_u32 s39, s39, 0x40000
Mv 176,80,112
Rb 40,16,40960
s_add_u32 m0, s37, 16384
Mv 208,80,116
buffer_load_dwordx4 v12, s[8:11], s39 offen offset:0 lds
Mv 240,80,120
Rb 44,16,43008
Ma 240,80,124
Ma 244,84,124
Rb 48,16,49152
Mv 244,84,120
Mv 212,84,116
Rb 52,16,51200
Mv 180,84,112
buffer_load_dwordx4 v13, s[8:11], s39 offen offset:1024 lds
Mv 148,84,108
Rb 56,16,57344
Mv 116,84,104
Mv 84,84,100
Rb 60,16,59392
Mv 52,84,96
Mv 56,88,96
Mv 88,88,100
buffer_load_dwordx4 v14, s[8:11], s39 offen offset:2048 lds
Mv 120,88,104
Mv 152,88,108
Mv 184,88,112
Mv 216,88,116
Mv 248,88,120
)" R"(
Ma 248,88,124
buffer_load_dwordx4 v15, s[8:11], s39 offen offset:3072 lds
Ma 252,92,124
Mv 252,92,120
Mv 220,92,116
Mv 188,92,112
Mv 156,92,108
Mv 124,92,104
Mv 92,92,100
Mv 60,92,96
s_waitcnt vmcnt(13) lgkmcnt(0)
s_barrier
Mv 32,0,32
Rt 64,18,49152
Mv 64,0,36
Rb 128,28,32768
Mv 96,0,40
Rt 66,18,51200
Mv 128,0,44
s_add_u32 s39, s39, 0x40000
Rb 132,28,34816
Mv 160,0,48
Rt 68,19,49152
s_add_u32 m0, s37, 32768
Mv 192,0,52
buffer_load_dwordx4 v12, s[8:11], s39 offen offset:0 lds
Mv 224,0,56
Rt 70,19,51200
Ma 224,0,60
Rb 136,29,32768
Ma 228,4,60
Rt 72,20,49152
Mv 228,4,56
Rb 140,29,34816
Mv 196,4,52
Rt 74,20,51200
Mv 164,4,48
Rb 144,28,36864
Mv 132,4,44
Rt 76,21,49152
Mv 100,4,40
buffer_load_dwordx4 v13, s[8:11], s39 offen offset:1024 lds
Mv 68,4,36
Rt 78,21,51200
Mv 36,4,32
Rb 148,28,38912
Mv 40,8,32
Rt 80,22,49152
Mv 72,8,36
Rb 152,29,36864
Mv 104,8,40
Rt 82,22,51200
Mv 136,8,44
Rb 156,29,38912
Mv 168,8,48
Rt 84,23,49152
Mv 200,8,52
buffer_load_dwordx4 v14, s[8:11], s39 offen offset:2048 lds
Mv 232,8,56
Rt 86,23,51200
Ma 232,8,60
Ma 236,12,60
Rt 88,24,49152
Mv 236,12,56
Mv 204,12,52
)" R"(
Rt 90,24,51200
Mv 172,12,48
Mv 140,12,44
Rt 92,25,49152
Mv 108,12,40
buffer_load_dwordx4 v15, s[8:11], s39 offen offset:3072 lds
Mv 76,12,36
Rt 94,25,51200
Mv 44,12,32
s_add_u32 s44, s43, 512
s_waitcnt lgkmcnt(6)
Mv 48,16,32
Rb 96,17,32768
s_add_u32 m0, s36, 131072
Mv 80,16,36
buffer_load_dwordx4 v26, s[12:15], s44 offen offset:0 lds
Mv 112,16,40
Rb 100,17,34816
Mv 144,16,44
Mv 176,16,48
Rb 104,17,40960
Mv 208,16,52
buffer_load_dwordx4 v27, s[12:15], s44 offen offset:1024 lds
Mv 240,16,56
Rb 108,17,43008
Ma 240,16,60
Ma 244,20,60
Rb 112,17,49152
Mv 244,20,56
s_add_u32 s44, s43, 12800
buffer_load_dwordx4 v26, s[12:15], s44 offen offset:2048 lds
Mv 212,20,52
Rb 116,17,51200
Mv 180,20,48
Mv 148,20,44
Rb 120,17,57344
Mv 116,20,40
buffer_load_dwordx4 v27, s[12:15], s44 offen offset:3072 lds
Mv 84,20,36
Rb 124,17,59392
Mv 52,20,32
Mv 56,24,32
s_add_u32 m0, s36, 135168
s_add_u32 s44, s43, 640
Mv 88,24,36
buffer_load_dwordx4 v26, s[12:15], s44 offen offset:0 lds
Mv 120,24,40
Mv 152,24,44
Mv 184,24,48
Mv 216,24,52
buffer_load_dwordx4 v27, s[12:15], s44 offen offset:1024 lds
Mv 248,24,56
Ma 248,24,60
Ma 252,28,60
Mv 252,28,56
s_add_u32 s44, s43, 12928
buffer_load_dwordx4 v26, s[12:15], s44 offen offset:2048 lds
Mv 220,28,52
Mv 188,28,48
Mv 156,28,44
Mv 124,28,40
buffer_load_dwordx4 v27, s[12:15], s44 offen offset:3072 lds
Mv 92,28,36
Mv 60,28,32
)" R"(
s_waitcnt vmcnt(16) lgkmcnt(0)
s_barrier
Mv 32,64,96
Rt 0,18,0
v_xor_b32 v4, s3, v4
v_cmp_gt_u32_e32 vcc, 2, v4
s_andn2_b32 s28, 0xff, vcc_lo
s_cbranch_scc1 .Lzsp1
.Lzok1:
Mv 64,64,100
s_add_u32 s38, s38, 0xffff0080
v_xor_b32 v4, s3, v4
Mv 96,64,104
Rt 2,18,2048
s_add_u32 m0, s36, 32768
Mv 128,64,108
buffer_load_dwordx4 v8, s[4:7], s38 offen offset:0 lds
Mv 160,64,112
Rt 4,19,0
v_and_b32 v4, 1, v4
Mv 192,64,116
v_cmp_eq_u32_e32 vcc, 1, v4
s_mov_b32 s85, vcc_lo
Mv 224,64,120
Rt 6,19,2048
s_and_b32 s28, s2, 7
Ma 224,64,124
buffer_load_dwordx4 v9, s[4:7], s38 offen offset:1024 lds
Ma 228,68,124
Rt 8,20,0
s_lshr_b32 s28, s85, s28
Mv 228,68,120
s_xor_b32 s28, s28, s34
s_and_b32 s28, s28, 1
Mv 196,68,116
Rt 10,20,2048
s_lshl_b32 s84, s28, 13
Mv 164,68,112
buffer_load_dwordx4 v10, s[4:7], s38 offen offset:2048 lds
Mv 132,68,108
Rt 12,21,0
s_add_u32 s49, s49, s84
Mv 100,68,104
s_add_u32 s50, s50, s84
s_add_u32 s60, s50, 7168
Mv 68,68,100
Rt 14,21,2048
s_add_u32 s52, s52, s84
Mv 36,68,96
buffer_load_dwordx4 v11, s[4:7], s38 offen offset:3072 lds
Mv 40,72,96
Rt 16,22,0
s_add_u32 s65, s65, s84
Mv 72,72,100
s_add_u32 s38, s38, 0x10000
s_lshr_b32 s28, s2, 6
Mv 104,72,104
Rt 18,22,2048
s_add_u32 m0, s36, 36864
Mv 136,72,108
buffer_load_dwordx4 v8, s[4:7], s38 offen offset:0 lds
Mv 168,72,112
Rt 20,23,0
s_lshl_b32 s28, s28, 14
Mv 200,72,116
)" R"(
s_add_u32 s28, s28, s50
s_bfe_u32 s29, s2, 0x20004
Mv 232,72,120
Rt 22,23,2048
s_lshl2_add_u32 s29, s29, s33
Ma 232,72,124
buffer_load_dwordx4 v9, s[4:7], s38 offen offset:1024 lds
Ma 236,76,124
Rt 24,24,0
s_lshl_b32 s29, s29, 9
Mv 236,76,120
s_add_u32 s28, s28, s29
v_min_u32 v4, 3, v0
Mv 204,76,116
Rt 26,24,2048
v_lshlrev_b32 v4, 7, v4
Mv 172,76,112
buffer_load_dwordx4 v10, s[4:7], s38 offen offset:2048 lds
Mv 140,76,108
Rt 28,25,0
Mv 108,76,104
buffer_load_dword v4, v4, s[20:23], s28 offen
Mv 76,76,100
Rt 30,25,2048
s_lshl1_add_u32 s28, s36, s84
Mv 44,76,96
buffer_load_dwordx4 v11, s[4:7], s38 offen offset:3072 lds
Mv 48,80,96
Rb 32,16,0
v_add_u32 v2, s28, v1
Mv 80,80,100
v_add_u32 v3, 0x1000, v2
s_lshl_b32 s28, s34, 13
Mv 112,80,104
Rb 36,16,2048
s_xor_b32 s28, s28, s84
Mv 144,80,108
s_add_u32 s39, s39, 0x40000
s_and_b32 s28, s28, 0x2000
Mv 176,80,112
Rb 40,16,8192
s_add_u32 m0, s37, 49152
Mv 208,80,116
buffer_load_dwordx4 v12, s[8:11], s39 offen offset:0 lds
Mv 240,80,120
Rb 44,16,10240
s_xor_b32 s73, s73, s28
Ma 240,80,124
Ma 244,84,124
Rb 48,16,16384
Mv 244,84,120
Mv 212,84,116
Rb 52,16,18432
Mv 180,84,112
buffer_load_dwordx4 v13, s[8:11], s39 offen offset:1024 lds
Mv 148,84,108
Rb 56,16,24576
Mv 116,84,104
Mv 84,84,100
Rb 60,16,26624
Mv 52,84,96
Mv 56,88,96
Mv 88,88,100
buffer_load_dwordx4 v14, s[8:11], s39 offen offset:2048 lds
)" R"(
Mv 120,88,104
Mv 152,88,108
Mv 184,88,112
Mv 216,88,116
Mv 248,88,120
Ma 248,88,124
buffer_load_dwordx4 v15, s[8:11], s39 offen offset:3072 lds
Ma 252,92,124
Mv 252,92,120
Mv 220,92,116
Mv 188,92,112
Mv 156,92,108
Mv 124,92,104
Mv 92,92,100
Mv 60,92,96
s_waitcnt vmcnt(5) lgkmcnt(0)
s_barrier
Mv 32,0,32
Rt 64,18,16384
Mv 64,0,36
Rb 160,28,32768
v_and_b32 v5, 7, v0
Mv 96,0,40
Rt 66,18,18432
v_lshlrev_b32 v5, 7, v5
Mv 128,0,44
Rb 164,28,34816
v_mov_b32 v6, s3
Mv 160,0,48
Rt 68,19,16384
Mv 192,0,52
Mv 224,0,56
Rt 70,19,18432
Ma 224,0,60
Rb 168,29,32768
Ma 228,4,60
Rt 72,20,16384
Mv 228,4,56
Rb 172,29,34816
Mv 196,4,52
Rt 74,20,18432
Mv 164,4,48
Rb 176,28,36864
Mv 132,4,44
Rt 76,21,16384
Mv 100,4,40
Mv 68,4,36
Rt 78,21,18432
Mv 36,4,32
Rb 180,28,38912
Mv 40,8,32
Rt 80,22,16384
Mv 72,8,36
Rb 184,29,36864
Mv 104,8,40
Rt 82,22,18432
Mv 136,8,44
Rb 188,29,38912
Mv 168,8,48
Rt 84,23,16384
Mv 200,8,52
Mv 232,8,56
Rt 86,23,18432
Ma 232,8,60
Ma 236,12,60
)" R"(
Rt 88,24,16384
Mv 236,12,56
Mv 204,12,52
Rt 90,24,18432
Mv 172,12,48
Mv 140,12,44
Rt 92,25,16384
Mv 108,12,40
Mv 76,12,36
Rt 94,25,18432
v_lshrrev_b32 v10, 3, v0
Mv 44,12,32
v_and_b32 v11, 0x70, v1
v_lshl_or_b32 v10, v10, 13, v11
Mv 48,16,32
Rb 96,17,0
v_lshlrev_b32 v11, 1, v0
Mv 80,16,36
v_and_b32 v11, 32, v11
v_xor_b32 v10, v10, v11
buffer_load_dword v4, v7, s[20:23], s52 offen offset:0 sc0 sc1
Mv 112,16,40
Rb 100,17,2048
s_and_b32 s28, s33, 1
Mv 144,16,44
s_lshl_b32 s28, s28, 6
v_xor_b32 v8, s28, v10
Mv 176,16,48
Rb 104,17,8192
v_xor_b32 v9, 64, v8
Mv 208,16,52
v_add_u32 v9, 0xfc00, v9
v_add_u32 v10, 0x1f800, v8
Mv 240,16,56
Rb 108,17,10240
v_add_u32 v11, 0x1f800, v9
Ma 240,16,60
Ma 244,20,60
Rb 112,17,16384
Mv 244,20,56
Mv 212,20,52
Rb 116,17,18432
Mv 180,20,48
Mv 148,20,44
Rb 120,17,24576
Mv 116,20,40
Mv 84,20,36
Rb 124,17,26624
Mv 52,20,32
Mv 56,24,32
Mv 88,24,36
Mv 120,24,40
Mv 152,24,44
Mv 184,24,48
Mv 216,24,52
Mv 248,24,56
Ma 248,24,60
Ma 252,28,60
Mv 252,28,56
Mv 220,28,52
Mv 188,28,48
Mv 156,28,44
Mv 124,28,40
Mv 92,28,36
)" R"(
Mv 60,28,32
s_waitcnt vmcnt(5) lgkmcnt(0)
s_barrier
Mv 32,64,96
Rt 0,18,32768
Mv 64,64,100
s_add_u32 s92, s42, 0x0
Mv 96,64,104
Rt 2,18,34816
s_add_u32 m0, s41, 131072
Mv 128,64,108
buffer_load_dwordx4 v8, s[16:19], s92 offen offset:0 lds
Mv 160,64,112
Rt 4,19,32768
Mv 192,64,116
Mv 224,64,120
Rt 6,19,34816
Ma 224,64,124
buffer_load_dwordx4 v9, s[16:19], s92 offen offset:1024 lds
Ma 228,68,124
Rt 8,20,32768
Mv 228,68,120
Mv 196,68,116
Rt 10,20,34816
Mv 164,68,112
buffer_load_dwordx4 v10, s[16:19], s92 offen offset:2048 lds
Mv 132,68,108
Rt 12,21,32768
Mv 100,68,104
Mv 68,68,100
Rt 14,21,34816
Mv 36,68,96
buffer_load_dwordx4 v11, s[16:19], s92 offen offset:3072 lds
Mv 40,72,96
Rt 16,22,32768
Mv 72,72,100
s_add_u32 s92, s42, 0x40000
Mv 104,72,104
Rt 18,22,34816
s_add_u32 m0, s41, 135168
Mv 136,72,108
buffer_load_dwordx4 v8, s[16:19], s92 offen offset:0 lds
Mv 168,72,112
Rt 20,23,32768
Mv 200,72,116
Mv 232,72,120
Rt 22,23,34816
Ma 232,72,124
buffer_load_dwordx4 v9, s[16:19], s92 offen offset:1024 lds
Ma 236,76,124
Rt 24,24,32768
Mv 236,76,120
Mv 204,76,116
Rt 26,24,34816
Mv 172,76,112
buffer_load_dwordx4 v10, s[16:19], s92 offen offset:2048 lds
Mv 140,76,108
Rt 28,25,32768
Mv 108,76,104
s_add_u32 s92, s42, 0x1c0000
Mv 76,76,100
Rt 30,25,34816
s_add_u32 m0, s41, 0
Mv 44,76,96
buffer_load_dwordx4 v8, s[16:19], s92 offen offset:0 lds
)" R"(
Mv 48,80,96
Rb 32,16,32768
Mv 80,80,100
Mv 112,80,104
Rb 36,16,34816
Mv 144,80,108
buffer_load_dwordx4 v9, s[16:19], s92 offen offset:1024 lds
Mv 176,80,112
Rb 40,16,40960
Mv 208,80,116
Mv 240,80,120
Rb 44,16,43008
Ma 240,80,124
buffer_load_dwordx4 v10, s[16:19], s92 offen offset:2048 lds
Ma 244,84,124
Rb 48,16,49152
Mv 244,84,120
Mv 212,84,116
Rb 52,16,51200
Mv 180,84,112
buffer_load_dwordx4 v11, s[16:19], s92 offen offset:3072 lds
Mv 148,84,108
Rb 56,16,57344
Mv 116,84,104
s_add_u32 s92, s42, 0x200000
Mv 84,84,100
Rb 60,16,59392
s_add_u32 m0, s41, 4096
Mv 52,84,96
buffer_load_dwordx4 v8, s[16:19], s92 offen offset:0 lds
Mv 56,88,96
Mv 88,88,100
Mv 120,88,104
Mv 152,88,108
buffer_load_dwordx4 v9, s[16:19], s92 offen offset:1024 lds
Mv 184,88,112
Mv 216,88,116
Mv 248,88,120
Ma 248,88,124
buffer_load_dwordx4 v10, s[16:19], s92 offen offset:2048 lds
Ma 252,92,124
Mv 252,92,120
Mv 220,92,116
Mv 188,92,112
Mv 156,92,108
Mv 124,92,104
Mv 92,92,100
Mv 60,92,96
s_waitcnt vmcnt(14) lgkmcnt(4)
s_barrier
v_cmp_eq_u32_e32 vcc, s3, v4
s_andn2_b64 s[28:29], 1, vcc
s_cbranch_scc1 .Lzsp2
.Lzok2:
Mv 32,0,32
Rt 64,18,49152
Mv 64,0,36
Rt 66,18,51200
s_add_u32 s92, s42, 0x80
Mv 96,0,40
Rt 68,19,49152
s_add_u32 m0, s41, 65536
Mv 128,0,44
Rt 70,19,51200
buffer_load_dwordx4 v8, s[16:19], s92 offen offset:0 lds
)" R"(
Mv 132,4,44
Rt 72,20,49152
Mv 100,4,40
Rt 74,20,51200
Mv 68,4,36
Rt 76,21,49152
Mv 36,4,32
Rt 78,21,51200
buffer_load_dwordx4 v9, s[16:19], s92 offen offset:1024 lds
Mv 40,8,32
Rt 80,22,49152
Mv 72,8,36
Rt 82,22,51200
Mv 104,8,40
Rt 84,23,49152
Mv 136,8,44
Rt 86,23,51200
buffer_load_dwordx4 v10, s[16:19], s92 offen offset:2048 lds
Mv 140,12,44
Rt 88,24,49152
Mv 108,12,40
Rt 90,24,51200
Mv 76,12,36
Rt 92,25,49152
Mv 44,12,32
Rt 94,25,51200
buffer_load_dwordx4 v11, s[16:19], s92 offen offset:3072 lds
Mv 48,16,32
Rb 96,17,32768
Mv 80,16,36
Rb 100,17,34816
s_add_u32 s92, s42, 0x40080
Mv 112,16,40
Rb 104,17,40960
s_add_u32 m0, s41, 69632
Mv 144,16,44
Rb 108,17,43008
buffer_load_dwordx4 v8, s[16:19], s92 offen offset:0 lds
Mv 148,20,44
Rb 112,17,49152
Mv 116,20,40
Rb 116,17,51200
Mv 84,20,36
Rb 120,17,57344
Mv 52,20,32
Rb 124,17,59392
buffer_load_dwordx4 v9, s[16:19], s92 offen offset:1024 lds
Mv 56,24,32
Mv 88,24,36
Mv 120,24,40
Mv 152,24,44
buffer_load_dwordx4 v10, s[16:19], s92 offen offset:2048 lds
Mv 156,28,44
Mv 124,28,40
Mv 92,28,36
Mv 60,28,32
s_waitcnt lgkmcnt(0)
s_barrier
Mv 32,64,96
v_bfe_u32 v12, v0, 3, 2
v_lshlrev_b32 v12, 5, v12
Mv 36,68,96
v_and_b32 v4, 3, v0
v_lshl_or_b32 v12, v4, 3, v12
Mv 40,72,96
)" R"(
s_add_u32 m0, s36, 98304
s_add_u32 s44, s43, 768
Mv 44,76,96
buffer_load_dwordx4 v26, s[12:15], s44 offen offset:0 lds
Mv 48,80,96
v_bfe_u32 v4, v0, 2, 2
v_lshl_or_b32 v12, v4, 7, v12
Mv 52,84,96
v_lshrrev_b32 v4, 4, v0
v_lshl_or_b32 v12, v4, 10, v12
Mv 56,88,96
v_xor_b32 v13, 32, v12
v_xor_b32 v14, 64, v12
Mv 60,92,96
buffer_load_dwordx4 v27, s[12:15], s44 offen offset:1024 lds
Mv 64,64,100
v_xor_b32 v15, 96, v12
v_add_u32 v20, 0x20000, v12
Mv 68,68,100
v_add_u32 v21, 0x20000, v13
v_add_u32 v22, 0x20000, v14
Mv 72,72,100
v_add_u32 v23, 0x20000, v15
v_add_u32 v16, 0x10000, v12
Mv 76,76,100
s_add_u32 s44, s43, 13056
buffer_load_dwordx4 v26, s[12:15], s44 offen offset:2048 lds
Mv 80,80,100
v_add_u32 v17, 0x10000, v13
v_add_u32 v18, 0x10000, v14
Mv 84,84,100
v_add_u32 v19, 0x10000, v15
Cv 32,32,33
Mv 88,88,100
Cv 33,34,35
Cv 34,36,37
Mv 92,92,100
buffer_load_dwordx4 v27, s[12:15], s44 offen offset:3072 lds
Mv 96,64,104
Cv 35,38,39
Cv 40,40,41
Mv 100,68,104
Cv 41,42,43
Cv 42,44,45
Mv 104,72,104
s_add_u32 m0, s41, 32768
s_add_u32 s92, s42, 0x1c0080
Mv 108,76,104
buffer_load_dwordx4 v8, s[16:19], s92 offen offset:0 lds
Mv 112,80,104
Cv 43,46,47
Cv 48,48,49
Mv 116,84,104
Cv 49,50,51
Cv 50,52,53
Mv 120,88,104
Cv 51,54,55
Cv 56,56,57
Mv 124,92,104
buffer_store_dwordx4 v[32:35], v2, s[20:23], s45 offen offset:0 sc0 sc1
Mv 128,64,108
Cv 57,58,59
Cv 58,60,61
Mv 132,68,108
)" R"(
Cv 59,62,63
Cv 64,64,65
Mv 136,72,108
Cv 65,66,67
Cv 66,68,69
Mv 140,76,108
buffer_load_dwordx4 v9, s[16:19], s92 offen offset:1024 lds
Mv 144,80,108
Cv 67,70,71
Cv 72,72,73
Mv 148,84,108
Cv 73,74,75
Cv 74,76,77
Mv 152,88,108
Cv 75,78,79
Cv 80,80,81
Mv 156,92,108
buffer_store_dwordx4 v[40:43], v2, s[20:23], s45 offen offset:1024 sc0 sc1
Mv 160,0,48
Cv 81,82,83
Cv 82,84,85
Mv 160,64,112
Cv 83,86,87
Cv 88,88,89
Mv 164,4,48
Cv 89,90,91
Cv 90,92,93
Mv 164,68,112
buffer_load_dwordx4 v10, s[16:19], s92 offen offset:2048 lds
Mv 168,8,48
Cv 91,94,95
Cv 96,96,97
Mv 168,72,112
Cv 97,98,99
Cv 98,100,101
Mv 172,12,48
Cv 99,102,103
Cv 104,104,105
Mv 172,76,112
buffer_store_dwordx4 v[48:51], v2, s[20:23], s45 offen offset:2048 sc0 sc1
Mv 176,16,48
Cv 105,106,107
Cv 106,108,109
Mv 176,80,112
Cv 107,110,111
Cv 112,112,113
Mv 180,20,48
Cv 113,114,115
Cv 114,116,117
Mv 180,84,112
buffer_load_dwordx4 v11, s[16:19], s92 offen offset:3072 lds
Mv 184,24,48
Cv 115,118,119
Cv 120,120,121
Mv 184,88,112
Cv 121,122,123
Cv 122,124,125
Mv 188,28,48
Cv 123,126,127
Cv 128,128,129
Mv 188,92,112
buffer_store_dwordx4 v[56:59], v2, s[20:23], s45 offen offset:3072 sc0 sc1
Mv 192,0,52
Cv 129,130,131
)" R"(
Cv 130,132,133
Mv 192,64,116
Cv 131,134,135
Cv 136,136,137
Mv 196,4,52
s_add_u32 m0, s41, 36864
s_add_u32 s92, s42, 0x200080
Mv 196,68,116
buffer_load_dwordx4 v8, s[16:19], s92 offen offset:0 lds
Mv 200,8,52
Cv 137,138,139
Cv 138,140,141
Mv 200,72,116
Cv 139,142,143
Cv 144,144,145
Mv 204,12,52
Cv 145,146,147
Cv 146,148,149
Mv 204,76,116
buffer_store_dwordx4 v[64:67], v3, s[20:23], s45 offen offset:0 sc0 sc1
Mv 208,16,52
Cv 147,150,151
Cv 152,152,153
Mv 208,80,116
Cv 153,154,155
Cv 154,156,157
Mv 212,20,52
Cv 155,158,159
Mv 212,84,116
buffer_load_dwordx4 v9, s[16:19], s92 offen offset:1024 lds
Mv 216,24,52
Mv 216,88,116
Mv 220,28,52
Mv 220,92,116
buffer_store_dwordx4 v[72:75], v3, s[20:23], s45 offen offset:1024 sc0 sc1
Mv 224,0,56
Ma 224,0,60
Ma 228,4,60
Mv 228,4,56
buffer_load_dwordx4 v10, s[16:19], s92 offen offset:2048 lds
Mv 232,8,56
Ma 232,8,60
Ma 236,12,60
Mv 236,12,56
s_waitcnt vmcnt(24)
s_barrier
Mv 240,16,56
buffer_store_dwordx4 v[80:83], v3, s[20:23], s45 offen offset:2048 sc0 sc1
Ma 240,16,60
Ma 244,20,60
Mv 244,20,56
Mv 248,24,56
Ma 248,24,60
Ma 252,28,60
Mv 252,28,56
buffer_store_dwordx4 v[88:91], v3, s[20:23], s45 offen offset:3072 sc0 sc1
Mv 224,64,120
Rt 16,20,0
Mv 228,68,120
Rt 18,20,512
Mv 232,72,120
Rt 20,21,0
Mv 236,76,120
Rt 22,21,512
Mv 240,80,120
)" R"(
Rt 24,22,0
Mv 244,84,120
Rt 26,22,512
Mv 248,88,120
Rt 28,23,0
Mv 252,92,120
Rt 30,23,512
Ma 224,64,124
Rt 32,20,4096
Ma 228,68,124
Rt 34,20,4608
Ma 232,72,124
Rt 36,21,4096
Ma 236,76,124
Rt 38,21,4608
Ma 240,80,124
Rt 40,22,4096
Ma 244,84,124
Rt 42,22,4608
Ma 248,88,124
Rt 44,23,4096
Ma 252,92,124
Rt 46,23,4608
s_waitcnt vmcnt(15) lgkmcnt(8)
s_branch .LzQA
.LzAV:
Mz 32,32,0
Rt 64,18,16384
Mz 36,36,0
s_lshl1_add_u32 s40, s34, s91
s_lshl_b32 s40, s40, 12
Mz 96,40,0
Rt 66,18,18432
s_and_b32 s29, s38, 0xfffe0000
Mz 100,44,0
s_add_u32 s39, s39, 0x40000
s_add_u32 s40, s40, s29
Mz 160,48,0
Rt 68,19,16384
s_add_u32 m0, s37, 0
Mz 164,52,0
buffer_load_dwordx4 v12, s[8:11], s39 offen offset:0 lds
Mz 224,56,0
Rt 70,19,18432
v_min_u32 v3, 31, v0
Mz 228,60,0
v_lshlrev_b32 v3, 7, v3
Mz 236,60,4
Rt 72,20,16384
Mz 232,56,4
Mz 172,52,4
Rt 74,20,18432
Mz 168,48,4
Mz 108,44,4
Rt 76,21,16384
Mz 104,40,4
buffer_load_dwordx4 v13, s[8:11], s39 offen offset:1024 lds
Mz 44,36,4
Rt 78,21,18432
Mz 40,32,4
Mz 48,32,8
Rt 80,22,16384
Mz 52,36,8
Mz 112,40,8
)" R"(
Rt 82,22,18432
Mz 116,44,8
Mz 176,48,8
Rt 84,23,16384
Mz 180,52,8
buffer_load_dwordx4 v14, s[8:11], s39 offen offset:2048 lds
Mz 240,56,8
Rt 86,23,18432
Mz 244,60,8
Mz 252,60,12
Rt 88,24,16384
Mz 248,56,12
Mz 188,52,12
Rt 90,24,18432
Mz 184,48,12
Mz 124,44,12
Rt 92,25,16384
Mz 120,40,12
buffer_load_dwordx4 v15, s[8:11], s39 offen offset:3072 lds
Mz 60,36,12
Rt 94,25,18432
Mz 56,32,12
Mz 64,32,16
Rb 96,17,0
Mz 68,36,16
buffer_load_dword v6, v3, s[4:7], s40 offen
Mz 128,40,16
Rb 100,17,2048
Mz 132,44,16
Mz 192,48,16
Rb 104,17,8192
s_mov_b32 s28, s35
Mz 196,52,16
s_lshl2_add_u32 s28, s28, s33
s_mul_i32 s43, s28, 28672
v_mfma_f32_16x16x32_bf16 a[224:227], a[56:59], a[16:19], 0
Rb 108,17,10240
v_and_b32 v26, 0x70, v1
v_mfma_f32_16x16x32_bf16 a[228:231], a[60:63], a[16:19], 0
v_and_b32 v4, 48, v0
v_xor_b32 v26, v26, v4
v_mfma_f32_16x16x32_bf16 a[236:239], a[60:63], a[20:23], 0
Rb 112,17,16384
v_lshrrev_b32 v4, 3, v0
v_mfma_f32_16x16x32_bf16 a[232:235], a[56:59], a[20:23], 0
v_mul_u32_u24 v4, 896, v4
v_add_u32 v26, v26, v4
Mz 204,52,20
Rb 116,17,18432
v_xor_b32 v27, 64, v26
Mz 200,48,20
v_add_u32 v27, 6144, v27
v_bfe_u32 v28, v0, 1, 3
Mz 140,44,20
Rb 120,17,24576
v_lshrrev_b32 v4, 4, v0
Mz 136,40,20
v_xor_b32 v28, v28, v4
v_lshlrev_b32 v28, 4, v28
Mz 76,36,20
Rb 124,17,26624
v_and_b32 v4, 15, v0
Mz 72,32,20
v_lshl_or_b32 v28, v4, 7, v28
)" R"(
s_add_u32 s28, s36, 0x18000
Mz 80,32,24
v_add_u32 v28, s28, v28
v_xor_b32 v29, 64, v28
Mz 84,36,24
Mz 144,40,24
Mz 148,44,24
Mz 208,48,24
Mz 212,52,24
v_mfma_f32_16x16x32_bf16 a[240:243], a[56:59], a[24:27], 0
v_mfma_f32_16x16x32_bf16 a[244:247], a[60:63], a[24:27], 0
v_mfma_f32_16x16x32_bf16 a[252:255], a[60:63], a[28:31], 0
v_mfma_f32_16x16x32_bf16 a[248:251], a[56:59], a[28:31], 0
Mz 220,52,28
Mz 216,48,28
Mz 156,44,28
Mz 152,40,28
Mz 92,36,28
Mz 88,32,28
s_waitcnt vmcnt(10) lgkmcnt(0)
s_barrier
Mv 32,96,64
Rt 0,18,32768
s_bfe_u32 s88, s2, 0x20005
Mv 36,100,64
s_add_u32 s38, s38, 0xffff0080
s_and_b32 s28, s2, 15
Mv 96,104,64
Rt 2,18,34816
s_add_u32 m0, s36, 0
Mv 100,108,64
buffer_load_dwordx4 v8, s[4:7], s38 offen offset:0 lds
Mv 160,112,64
Rt 4,19,32768
s_lshl4_add_u32 s88, s88, s28
Mv 164,116,64
s_lshl2_add_u32 s88, s88, s33
s_mul_i32 s88, s88, 4096
Mv 224,120,64
Rt 6,19,34816
s_add_u32 s88, s88, 0x100000
Mv 228,124,64
buffer_load_dwordx4 v9, s[4:7], s38 offen offset:1024 lds
Mv 236,124,68
Rt 8,20,32768
v_min_u32 v5, 31, v0
Mv 232,120,68
v_lshlrev_b32 v5, 7, v5
s_mov_b32 s90, 0x100000
Mv 172,116,68
Rt 10,20,34816
s_mov_b64 s[24:25], s[8:9]
Mv 168,112,68
buffer_load_dwordx4 v10, s[4:7], s38 offen offset:2048 lds
Mv 108,108,68
Rt 12,21,32768
s_mov_b64 s[26:27], s[10:11]
Mv 104,104,68
Mv 44,100,68
Rt 14,21,34816
Mv 40,96,68
buffer_load_dwordx4 v11, s[4:7], s38 offen offset:3072 lds
Mv 48,96,72
Rt 16,22,32768
Mv 52,100,72
)" R"(
s_add_u32 s38, s38, 0x10000
Mv 112,104,72
Rt 18,22,34816
s_add_u32 m0, s36, 4096
Mv 116,108,72
buffer_load_dwordx4 v8, s[4:7], s38 offen offset:0 lds
Mv 176,112,72
Rt 20,23,32768
Mv 180,116,72
Mv 240,120,72
Rt 22,23,34816
Mv 244,124,72
buffer_load_dwordx4 v9, s[4:7], s38 offen offset:1024 lds
Mv 252,124,76
Rt 24,24,32768
Mv 248,120,76
Mv 188,116,76
Rt 26,24,34816
Mv 184,112,76
buffer_load_dwordx4 v10, s[4:7], s38 offen offset:2048 lds
Mv 124,108,76
Rt 28,25,32768
Mv 120,104,76
Mv 60,100,76
Rt 30,25,34816
Mv 56,96,76
buffer_load_dwordx4 v11, s[4:7], s38 offen offset:3072 lds
Mv 64,96,80
Rb 32,16,32768
Mv 68,100,80
Mv 128,104,80
Rb 36,16,34816
Mv 132,108,80
s_add_u32 s39, s39, 0x40000
Mv 192,112,80
Rb 40,16,40960
s_add_u32 m0, s37, 16384
Mv 196,116,80
buffer_load_dwordx4 v12, s[8:11], s39 offen offset:0 lds
Ma 224,120,80
Rb 44,16,43008
Ma 228,124,80
Ma 236,124,84
Rb 48,16,49152
Ma 232,120,84
Mv 204,116,84
Rb 52,16,51200
Mv 200,112,84
buffer_load_dwordx4 v13, s[8:11], s39 offen offset:1024 lds
Mv 140,108,84
Rb 56,16,57344
Mv 136,104,84
Mv 76,100,84
Rb 60,16,59392
Mv 72,96,84
Mv 80,96,88
Mv 84,100,88
buffer_load_dwordx4 v14, s[8:11], s39 offen offset:2048 lds
Mv 144,104,88
Mv 148,108,88
Mv 208,112,88
Mv 212,116,88
Ma 240,120,88
Ma 244,124,88
)" R"(
buffer_load_dwordx4 v15, s[8:11], s39 offen offset:3072 lds
Ma 252,124,92
Ma 248,120,92
s_add_u32 s88, s88, s90
Mv 220,116,92
Mv 216,112,92
buffer_load_dword v6, v5, s[24:27], s88 offen
Mv 156,108,92
Mv 152,104,92
Mv 92,100,92
Mv 88,96,92
s_waitcnt vmcnt(19) lgkmcnt(0)
s_barrier
Mv 32,32,0
Rt 64,18,49152
s_lshl_b32 s28, s34, 21
Mv 36,36,0
s_lshl_b32 s29, s91, 25
s_add_u32 s28, s28, s29
Mv 96,40,0
Rt 66,18,51200
s_bfe_u32 s29, s63, 0x80000
Mv 100,44,0
s_add_u32 s39, s39, 0x40000
s_lshl_b32 s29, s29, 16
Mv 160,48,0
Rt 68,19,49152
s_add_u32 m0, s37, 32768
Mv 164,52,0
buffer_load_dwordx4 v12, s[8:11], s39 offen offset:0 lds
Mv 224,56,0
Rt 70,19,51200
s_add_u32 s45, s28, s29
Mv 228,60,0
s_bfe_u32 s29, s63, 0x80008
s_lshl_b32 s29, s29, 16
Mv 236,60,4
Rt 72,20,49152
s_add_u32 s46, s28, s29
Mv 232,56,4
s_bfe_u32 s29, s63, 0x80010
s_lshl_b32 s29, s29, 16
Mv 172,52,4
Rt 74,20,51200
s_add_u32 s47, s28, s29
Mv 168,48,4
s_bfe_u32 s29, s63, 0x80018
s_lshl_b32 s29, s29, 16
Mv 108,44,4
Rt 76,21,49152
s_add_u32 s48, s28, s29
Mv 104,40,4
buffer_load_dwordx4 v13, s[8:11], s39 offen offset:1024 lds
Mv 44,36,4
Rt 78,21,51200
s_lshl_b32 s50, s34, 21
Mv 40,32,4
s_add_u32 s50, s50, 0x6140000
s_lshl2_add_u32 s28, s91, s93
Mv 48,32,8
Rt 80,22,49152
s_lshl_b32 s28, s28, 7
Mv 52,36,8
s_add_u32 s49, s28, s50
s_lshl_b32 s52, s35, 7
)" R"(
Mv 112,40,8
Rt 82,22,51200
s_add_u32 s52, s52, s50
Mv 116,44,8
s_add_u32 s52, s52, 0xd000
s_lshl4_add_u32 s28, s34, s35
Mv 176,48,8
Rt 84,23,49152
s_lshl_b32 s51, s28, 17
Mv 180,52,8
buffer_load_dwordx4 v14, s[8:11], s39 offen offset:2048 lds
Mv 240,56,8
Rt 86,23,51200
s_add_u32 s51, s51, 0x4000000
Mv 244,60,8
s_cmp_lg_u32 s33, 0
s_cmov_b32 s49, 0x40000000
Mv 252,60,12
Rt 88,24,49152
s_mov_b32 s53, 0x40000
Mv 248,56,12
s_mov_b32 s54, 0x3db8aa3b
s_mul_i32 s41, s33, 7168
Mv 188,52,12
Rt 90,24,51200
s_mul_i32 s42, s33, 0x70000
Mv 184,48,12
s_lshl_b32 s28, s34, 9
s_add_u32 s42, s42, s28
Mv 124,44,12
Rt 92,25,49152
s_lshl_b32 s89, s34, 21
Mv 120,40,12
buffer_load_dwordx4 v15, s[8:11], s39 offen offset:3072 lds
Mv 60,36,12
Rt 94,25,51200
s_mov_b32 s62, 0x4e800000
Mv 56,32,12
s_add_u32 s67, s89, 0x6000000
s_lshl3_add_u32 s28, s34, s35
Mv 64,32,16
Rb 96,17,32768
s_add_u32 s64, s51, 0x100000
s_add_u32 m0, s36, 131072
s_add_u32 s44, s43, 0
Mv 68,36,16
s_lshl2_add_u32 s28, s28, s33
s_lshl2_add_u32 s65, s35, s33
buffer_load_dwordx4 v26, s[12:15], s44 offen offset:0 lds
Mv 128,40,16
Rb 100,17,34816
s_lshl_b32 s65, s65, 7
Mv 132,44,16
s_add_u32 s65, s65, s50
s_add_u32 s65, s65, 0xc000
Mv 192,48,16
Rb 104,17,40960
s_lshl_b32 s66, s28, 10
Mv 196,52,16
s_add_u32 s66, s66, 0xc000000
s_mov_b32 s28, s35
buffer_load_dwordx4 v27, s[12:15], s44 offen offset:1024 lds
Ma 224,56,16
Rb 108,17,43008
)" R"(
s_lshl2_add_u32 s28, s28, s33
Ma 228,60,16
s_lshl_b32 s29, s28, 5
s_add_u32 s29, s29, s34
Ma 236,60,20
Rb 112,17,49152
s_lshl_b32 s73, s29, 15
Ma 232,56,20
s_add_u32 s73, s73, 0x8000000
s_lshr_b32 s29, s36, 1
s_add_u32 s44, s43, 12288
buffer_load_dwordx4 v26, s[12:15], s44 offen offset:2048 lds
Mv 204,52,20
Rb 116,17,51200
s_and_b32 s29, s29, 0x2000
Mv 200,48,20
s_add_u32 s73, s73, s29
s_lshl4_add_u32 s28, s28, s34
Mv 140,44,20
Rb 120,17,57344
s_lshl_b32 s68, s28, 8
Mv 136,40,20
s_add_u32 s68, s68, 0xc110000
s_add_u32 s55, s89, 0x120000
buffer_load_dwordx4 v27, s[12:15], s44 offen offset:3072 lds
Mv 76,36,20
Rb 124,17,59392
s_lshl_b32 s28, s35, 1
Mv 72,32,20
s_sub_u32 s57, 14, s28
s_mov_b32 s59, s89
Mv 80,32,24
s_mov_b32 s58, 2
s_add_u32 m0, s36, 135168
s_add_u32 s44, s43, 128
Mv 84,36,24
s_cmp_eq_u32 s35, 7
s_cmov_b32 s55, s89
buffer_load_dwordx4 v26, s[12:15], s44 offen offset:0 lds
Mv 144,40,24
s_cmp_eq_u32 s35, 7
s_cmov_b32 s57, 16
Mv 148,44,24
s_cmp_eq_u32 s35, 7
s_cmov_b32 s59, s67
Mv 208,48,24
s_add_u32 s56, s55, 0x2000000
Mv 212,52,24
buffer_load_dwordx4 v27, s[12:15], s44 offen offset:1024 lds
Ma 240,56,24
Ma 244,60,24
Ma 252,60,28
Ma 248,56,28
s_add_u32 s44, s43, 12416
buffer_load_dwordx4 v26, s[12:15], s44 offen offset:2048 lds
Mv 220,52,28
Mv 216,48,28
Mv 156,44,28
Mv 152,40,28
buffer_load_dwordx4 v27, s[12:15], s44 offen offset:3072 lds
Mv 92,36,28
Mv 88,32,28
s_waitcnt vmcnt(17) lgkmcnt(0)
s_barrier
Mv 32,96,64
)" R"(
Rt 0,18,0
Mv 36,100,64
s_add_u32 s38, s38, 0xffff0080
Mv 96,104,64
Rt 2,18,2048
s_add_u32 m0, s36, 32768
Mv 100,108,64
buffer_load_dwordx4 v8, s[4:7], s38 offen offset:0 lds
Mv 160,112,64
Rt 4,19,0
Mv 164,116,64
Mv 224,120,64
Rt 6,19,2048
Mv 228,124,64
buffer_load_dwordx4 v9, s[4:7], s38 offen offset:1024 lds
Mv 236,124,68
Rt 8,20,0
s_lshl2_add_u32 s28, s2, s33
Mv 232,120,68
s_lshl_b32 s28, s28, 11
s_add_u32 s28, s28, 0xc100000
Mv 172,116,68
Rt 10,20,2048
v_min_u32 v3, 15, v0
Mv 168,112,68
buffer_load_dwordx4 v10, s[4:7], s38 offen offset:2048 lds
Mv 108,108,68
Rt 12,21,0
v_lshlrev_b32 v3, 7, v3
Mv 104,104,68
Mv 44,100,68
Rt 14,21,2048
Mv 40,96,68
buffer_load_dwordx4 v11, s[4:7], s38 offen offset:3072 lds
Mv 48,96,72
Rt 16,22,0
Mv 52,100,72
s_add_u32 s38, s38, 0x10000
Mv 112,104,72
Rt 18,22,2048
s_add_u32 m0, s36, 36864
Mv 116,108,72
buffer_load_dwordx4 v8, s[4:7], s38 offen offset:0 lds
Mv 176,112,72
Rt 20,23,0
Mv 180,116,72
Mv 240,120,72
Rt 22,23,2048
Mv 244,124,72
buffer_load_dwordx4 v9, s[4:7], s38 offen offset:1024 lds
Mv 252,124,76
Rt 24,24,0
Mv 248,120,76
Mv 188,116,76
Rt 26,24,2048
Mv 184,112,76
buffer_load_dwordx4 v10, s[4:7], s38 offen offset:2048 lds
Mv 124,108,76
Rt 28,25,0
Mv 120,104,76
Mv 60,100,76
Rt 30,25,2048
Mv 56,96,76
buffer_load_dwordx4 v11, s[4:7], s38 offen offset:3072 lds
)" R"(
Mv 64,96,80
Rb 32,16,0
Mv 68,100,80
Mv 128,104,80
Rb 36,16,2048
Mv 132,108,80
s_add_u32 s39, s39, 0x40000
Mv 192,112,80
Rb 40,16,8192
s_add_u32 m0, s37, 49152
Mv 196,116,80
buffer_load_dwordx4 v12, s[8:11], s39 offen offset:0 lds
Ma 224,120,80
Rb 44,16,10240
Ma 228,124,80
Ma 236,124,84
Rb 48,16,16384
Ma 232,120,84
Mv 204,116,84
Rb 52,16,18432
Mv 200,112,84
buffer_load_dwordx4 v13, s[8:11], s39 offen offset:1024 lds
Mv 140,108,84
Rb 56,16,24576
Mv 136,104,84
Mv 76,100,84
Rb 60,16,26624
Mv 72,96,84
Mv 80,96,88
Mv 84,100,88
buffer_load_dwordx4 v14, s[8:11], s39 offen offset:2048 lds
Mv 144,104,88
Mv 148,108,88
Mv 208,112,88
Mv 212,116,88
Ma 240,120,88
Ma 244,124,88
buffer_load_dwordx4 v15, s[8:11], s39 offen offset:3072 lds
Ma 252,124,92
Ma 248,120,92
s_add_u32 s88, s88, s90
s_mov_b32 s40, 5
Mv 220,116,92
Mv 216,112,92
buffer_load_dword v6, v5, s[24:27], s88 offen
Mv 156,108,92
Mv 152,104,92
buffer_load_dword v6, v3, s[20:23], s28 offen
Mv 92,100,92
Mv 88,96,92
s_waitcnt vmcnt(27) lgkmcnt(0)
s_barrier
.LzPAV:
Mv 32,32,0
Rt 64,18,16384
Mv 36,36,0
Mv 96,40,0
Rt 66,18,18432
Mv 100,44,0
s_add_u32 s39, s39, 0x40000
Mv 160,48,0
Rt 68,19,16384
s_add_u32 m0, s37, 0
Mv 164,52,0
buffer_load_dwordx4 v12, s[8:11], s39 offen offset:0 lds
)" R"(
Mv 224,56,0
Rt 70,19,18432
Mv 228,60,0
Mv 236,60,4
Rt 72,20,16384
Mv 232,56,4
Mv 172,52,4
Rt 74,20,18432
Mv 168,48,4
Mv 108,44,4
Rt 76,21,16384
Mv 104,40,4
buffer_load_dwordx4 v13, s[8:11], s39 offen offset:1024 lds
Mv 44,36,4
Rt 78,21,18432
Mv 40,32,4
Mv 48,32,8
Rt 80,22,16384
Mv 52,36,8
Mv 112,40,8
Rt 82,22,18432
Mv 116,44,8
Mv 176,48,8
Rt 84,23,16384
Mv 180,52,8
buffer_load_dwordx4 v14, s[8:11], s39 offen offset:2048 lds
Mv 240,56,8
Rt 86,23,18432
Mv 244,60,8
Mv 252,60,12
Rt 88,24,16384
Mv 248,56,12
Mv 188,52,12
Rt 90,24,18432
Mv 184,48,12
Mv 124,44,12
Rt 92,25,16384
Mv 120,40,12
buffer_load_dwordx4 v15, s[8:11], s39 offen offset:3072 lds
Mv 60,36,12
Rt 94,25,18432
Mv 56,32,12
Mv 64,32,16
Rb 96,17,0
Mv 68,36,16
Mv 128,40,16
Rb 100,17,2048
Mv 132,44,16
Mv 192,48,16
Rb 104,17,8192
Mv 196,52,16
Ma 224,56,16
Rb 108,17,10240
Ma 228,60,16
Ma 236,60,20
Rb 112,17,16384
Ma 232,56,20
Mv 204,52,20
Rb 116,17,18432
Mv 200,48,20
Mv 140,44,20
Rb 120,17,24576
Mv 136,40,20
Mv 76,36,20
)" R"(
Rb 124,17,26624
Mv 72,32,20
Mv 80,32,24
Mv 84,36,24
Mv 144,40,24
Mv 148,44,24
Mv 208,48,24
Mv 212,52,24
Ma 240,56,24
Ma 244,60,24
Ma 252,60,28
Ma 248,56,28
Mv 220,52,28
Mv 216,48,28
Mv 156,44,28
Mv 152,40,28
Mv 92,36,28
Mv 88,32,28
s_waitcnt vmcnt(9) lgkmcnt(0)
s_barrier
Mv 32,96,64
Rt 0,18,32768
Mv 36,100,64
s_add_u32 s38, s38, 0xffff0080
Mv 96,104,64
Rt 2,18,34816
s_add_u32 m0, s36, 0
Mv 100,108,64
buffer_load_dwordx4 v8, s[4:7], s38 offen offset:0 lds
Mv 160,112,64
Rt 4,19,32768
Mv 164,116,64
Mv 224,120,64
Rt 6,19,34816
Mv 228,124,64
buffer_load_dwordx4 v9, s[4:7], s38 offen offset:1024 lds
Mv 236,124,68
Rt 8,20,32768
Mv 232,120,68
Mv 172,116,68
Rt 10,20,34816
Mv 168,112,68
buffer_load_dwordx4 v10, s[4:7], s38 offen offset:2048 lds
Mv 108,108,68
Rt 12,21,32768
Mv 104,104,68
Mv 44,100,68
Rt 14,21,34816
Mv 40,96,68
buffer_load_dwordx4 v11, s[4:7], s38 offen offset:3072 lds
Mv 48,96,72
Rt 16,22,32768
Mv 52,100,72
s_add_u32 s38, s38, 0x10000
Mv 112,104,72
Rt 18,22,34816
s_add_u32 m0, s36, 4096
Mv 116,108,72
buffer_load_dwordx4 v8, s[4:7], s38 offen offset:0 lds
Mv 176,112,72
Rt 20,23,32768
Mv 180,116,72
Mv 240,120,72
Rt 22,23,34816
Mv 244,124,72
)" R"(
buffer_load_dwordx4 v9, s[4:7], s38 offen offset:1024 lds
Mv 252,124,76
Rt 24,24,32768
Mv 248,120,76
Mv 188,116,76
Rt 26,24,34816
Mv 184,112,76
buffer_load_dwordx4 v10, s[4:7], s38 offen offset:2048 lds
Mv 124,108,76
Rt 28,25,32768
Mv 120,104,76
Mv 60,100,76
Rt 30,25,34816
Mv 56,96,76
buffer_load_dwordx4 v11, s[4:7], s38 offen offset:3072 lds
Mv 64,96,80
Rb 32,16,32768
Mv 68,100,80
Mv 128,104,80
Rb 36,16,34816
Mv 132,108,80
s_add_u32 s39, s39, 0x40000
Mv 192,112,80
Rb 40,16,40960
s_add_u32 m0, s37, 16384
Mv 196,116,80
buffer_load_dwordx4 v12, s[8:11], s39 offen offset:0 lds
Ma 224,120,80
Rb 44,16,43008
Ma 228,124,80
Ma 236,124,84
Rb 48,16,49152
Ma 232,120,84
Mv 204,116,84
Rb 52,16,51200
Mv 200,112,84
buffer_load_dwordx4 v13, s[8:11], s39 offen offset:1024 lds
Mv 140,108,84
Rb 56,16,57344
Mv 136,104,84
Mv 76,100,84
Rb 60,16,59392
Mv 72,96,84
Mv 80,96,88
Mv 84,100,88
buffer_load_dwordx4 v14, s[8:11], s39 offen offset:2048 lds
Mv 144,104,88
Mv 148,108,88
Mv 208,112,88
Mv 212,116,88
Ma 240,120,88
Ma 244,124,88
buffer_load_dwordx4 v15, s[8:11], s39 offen offset:3072 lds
Ma 252,124,92
Ma 248,120,92
s_add_u32 s88, s88, s90
Mv 220,116,92
Mv 216,112,92
buffer_load_dword v6, v5, s[24:27], s88 offen
Mv 156,108,92
Mv 152,104,92
Mv 92,100,92
Mv 88,96,92
s_waitcnt vmcnt(18) lgkmcnt(0)
s_barrier
)" R"(
Mv 32,32,0
Rt 64,18,49152
Mv 36,36,0
Mv 96,40,0
Rt 66,18,51200
Mv 100,44,0
s_add_u32 s39, s39, 0x40000
Mv 160,48,0
Rt 68,19,49152
s_add_u32 m0, s37, 32768
Mv 164,52,0
buffer_load_dwordx4 v12, s[8:11], s39 offen offset:0 lds
Mv 224,56,0
Rt 70,19,51200
Mv 228,60,0
Mv 236,60,4
Rt 72,20,49152
Mv 232,56,4
Mv 172,52,4
Rt 74,20,51200
Mv 168,48,4
Mv 108,44,4
Rt 76,21,49152
Mv 104,40,4
buffer_load_dwordx4 v13, s[8:11], s39 offen offset:1024 lds
Mv 44,36,4
Rt 78,21,51200
Mv 40,32,4
Mv 48,32,8
Rt 80,22,49152
Mv 52,36,8
Mv 112,40,8
Rt 82,22,51200
Mv 116,44,8
Mv 176,48,8
Rt 84,23,49152
Mv 180,52,8
buffer_load_dwordx4 v14, s[8:11], s39 offen offset:2048 lds
Mv 240,56,8
Rt 86,23,51200
Mv 244,60,8
Mv 252,60,12
Rt 88,24,49152
Mv 248,56,12
Mv 188,52,12
Rt 90,24,51200
Mv 184,48,12
Mv 124,44,12
Rt 92,25,49152
Mv 120,40,12
buffer_load_dwordx4 v15, s[8:11], s39 offen offset:3072 lds
Mv 60,36,12
Rt 94,25,51200
Mv 56,32,12
Mv 64,32,16
Rb 96,17,32768
Mv 68,36,16
Mv 128,40,16
Rb 100,17,34816
Mv 132,44,16
Mv 192,48,16
Rb 104,17,40960
Mv 196,52,16
Ma 224,56,16
)" R"(
Rb 108,17,43008
Ma 228,60,16
Ma 236,60,20
Rb 112,17,49152
Ma 232,56,20
Mv 204,52,20
Rb 116,17,51200
Mv 200,48,20
Mv 140,44,20
Rb 120,17,57344
Mv 136,40,20
Mv 76,36,20
Rb 124,17,59392
Mv 72,32,20
Mv 80,32,24
Mv 84,36,24
Mv 144,40,24
Mv 148,44,24
Mv 208,48,24
Mv 212,52,24
Ma 240,56,24
Ma 244,60,24
Ma 252,60,28
Ma 248,56,28
Mv 220,52,28
Mv 216,48,28
Mv 156,44,28
Mv 152,40,28
Mv 92,36,28
Mv 88,32,28
s_waitcnt vmcnt(9) lgkmcnt(0)
s_barrier
Mv 32,96,64
Rt 0,18,0
Mv 36,100,64
s_add_u32 s38, s38, 0xffff0080
Mv 96,104,64
Rt 2,18,2048
s_add_u32 m0, s36, 32768
Mv 100,108,64
buffer_load_dwordx4 v8, s[4:7], s38 offen offset:0 lds
Mv 160,112,64
Rt 4,19,0
Mv 164,116,64
Mv 224,120,64
Rt 6,19,2048
Mv 228,124,64
buffer_load_dwordx4 v9, s[4:7], s38 offen offset:1024 lds
Mv 236,124,68
Rt 8,20,0
Mv 232,120,68
Mv 172,116,68
Rt 10,20,2048
Mv 168,112,68
buffer_load_dwordx4 v10, s[4:7], s38 offen offset:2048 lds
Mv 108,108,68
Rt 12,21,0
Mv 104,104,68
Mv 44,100,68
Rt 14,21,2048
Mv 40,96,68
buffer_load_dwordx4 v11, s[4:7], s38 offen offset:3072 lds
Mv 48,96,72
Rt 16,22,0
Mv 52,100,72
)" R"(
s_add_u32 s38, s38, 0x10000
Mv 112,104,72
Rt 18,22,2048
s_add_u32 m0, s36, 36864
Mv 116,108,72
buffer_load_dwordx4 v8, s[4:7], s38 offen offset:0 lds
Mv 176,112,72
Rt 20,23,0
Mv 180,116,72
Mv 240,120,72
Rt 22,23,2048
Mv 244,124,72
buffer_load_dwordx4 v9, s[4:7], s38 offen offset:1024 lds
Mv 252,124,76
Rt 24,24,0
Mv 248,120,76
Mv 188,116,76
Rt 26,24,2048
Mv 184,112,76
buffer_load_dwordx4 v10, s[4:7], s38 offen offset:2048 lds
Mv 124,108,76
Rt 28,25,0
Mv 120,104,76
Mv 60,100,76
Rt 30,25,2048
Mv 56,96,76
buffer_load_dwordx4 v11, s[4:7], s38 offen offset:3072 lds
Mv 64,96,80
Rb 32,16,0
Mv 68,100,80
Mv 128,104,80
Rb 36,16,2048
Mv 132,108,80
s_add_u32 s39, s39, 0x40000
Mv 192,112,80
Rb 40,16,8192
s_add_u32 m0, s37, 49152
Mv 196,116,80
buffer_load_dwordx4 v12, s[8:11], s39 offen offset:0 lds
Ma 224,120,80
Rb 44,16,10240
Ma 228,124,80
Ma 236,124,84
Rb 48,16,16384
Ma 232,120,84
Mv 204,116,84
Rb 52,16,18432
Mv 200,112,84
buffer_load_dwordx4 v13, s[8:11], s39 offen offset:1024 lds
Mv 140,108,84
Rb 56,16,24576
Mv 136,104,84
Mv 76,100,84
Rb 60,16,26624
Mv 72,96,84
Mv 80,96,88
Mv 84,100,88
buffer_load_dwordx4 v14, s[8:11], s39 offen offset:2048 lds
Mv 144,104,88
Mv 148,108,88
Mv 208,112,88
Mv 212,116,88
Ma 240,120,88
Ma 244,124,88
)" R"(
buffer_load_dwordx4 v15, s[8:11], s39 offen offset:3072 lds
Ma 252,124,92
Ma 248,120,92
s_add_u32 s88, s88, s90
Mv 220,116,92
s_sub_u32 s40, s40, 1
Mv 216,112,92
buffer_load_dword v6, v5, s[24:27], s88 offen
Mv 156,108,92
Mv 152,104,92
Mv 92,100,92
Mv 88,96,92
s_cmp_lg_u32 s40, 0
s_waitcnt vmcnt(18) lgkmcnt(0)
s_barrier
s_cbranch_scc1 .LzPAV
.LzPAVX:
Mv 32,32,0
Rt 64,18,16384
Mv 36,36,0
Rb 192,28,32768
Mv 96,40,0
Rt 66,18,18432
Mv 100,44,0
s_add_u32 s39, s39, 0x40000
Rb 196,28,34816
Mv 160,48,0
Rt 68,19,16384
s_add_u32 m0, s37, 0
Mv 164,52,0
buffer_load_dwordx4 v12, s[8:11], s39 offen offset:0 lds
Mv 224,56,0
Rt 70,19,18432
Mv 228,60,0
Rb 200,29,32768
Mv 236,60,4
Rt 72,20,16384
Mv 232,56,4
Rb 204,29,34816
Mv 172,52,4
Rt 74,20,18432
Mv 168,48,4
Rb 208,28,36864
Mv 108,44,4
Rt 76,21,16384
Mv 104,40,4
buffer_load_dwordx4 v13, s[8:11], s39 offen offset:1024 lds
Mv 44,36,4
Rt 78,21,18432
Mv 40,32,4
Rb 212,28,38912
Mv 48,32,8
Rt 80,22,16384
Mv 52,36,8
Rb 216,29,36864
Mv 112,40,8
Rt 82,22,18432
Mv 116,44,8
Rb 220,29,38912
Mv 176,48,8
Rt 84,23,16384
Mv 180,52,8
buffer_load_dwordx4 v14, s[8:11], s39 offen offset:2048 lds
Mv 240,56,8
Rt 86,23,18432
)" R"(
Mv 244,60,8
Mv 252,60,12
Rt 88,24,16384
Mv 248,56,12
Mv 188,52,12
Rt 90,24,18432
Mv 184,48,12
Mv 124,44,12
Rt 92,25,16384
Mv 120,40,12
buffer_load_dwordx4 v15, s[8:11], s39 offen offset:3072 lds
Mv 60,36,12
Rt 94,25,18432
Mv 56,32,12
s_add_u32 s44, s43, 256
s_waitcnt lgkmcnt(6)
Mv 64,32,16
Rb 96,17,0
s_add_u32 m0, s36, 131072
Mv 68,36,16
buffer_load_dwordx4 v26, s[12:15], s44 offen offset:0 lds
Mv 128,40,16
Rb 100,17,2048
Mv 132,44,16
Mv 192,48,16
Rb 104,17,8192
Mv 196,52,16
buffer_load_dwordx4 v27, s[12:15], s44 offen offset:1024 lds
Ma 224,56,16
Rb 108,17,10240
Ma 228,60,16
Ma 236,60,20
Rb 112,17,16384
Ma 232,56,20
s_add_u32 s44, s43, 12544
buffer_load_dwordx4 v26, s[12:15], s44 offen offset:2048 lds
Mv 204,52,20
Rb 116,17,18432
Mv 200,48,20
Mv 140,44,20
Rb 120,17,24576
Mv 136,40,20
buffer_load_dwordx4 v27, s[12:15], s44 offen offset:3072 lds
Mv 76,36,20
Rb 124,17,26624
Mv 72,32,20
Mv 80,32,24
s_add_u32 m0, s36, 135168
s_add_u32 s44, s43, 384
Mv 84,36,24
buffer_load_dwordx4 v26, s[12:15], s44 offen offset:0 lds
Mv 144,40,24
Mv 148,44,24
Mv 208,48,24
Mv 212,52,24
buffer_load_dwordx4 v27, s[12:15], s44 offen offset:1024 lds
Ma 240,56,24
Ma 244,60,24
Ma 252,60,28
Ma 248,56,28
s_add_u32 s44, s43, 12672
buffer_load_dwordx4 v26, s[12:15], s44 offen offset:2048 lds
Mv 220,52,28
Mv 216,48,28
)" R"(
Mv 156,44,28
s_mov_b32 s85, 0xc108800
v_and_b32 v2, 7, v0
v_lshlrev_b32 v2, 7, v2
Mv 152,40,28
buffer_load_dwordx4 v27, s[12:15], s44 offen offset:3072 lds
Mv 92,36,28
buffer_load_dword v4, v2, s[20:23], s85 offen sc0 sc1
Mv 88,32,28
s_waitcnt vmcnt(18) lgkmcnt(0)
s_barrier
Mv 32,96,64
Rt 0,18,32768
Mv 36,100,64
s_add_u32 s38, s38, 0xffff0080
Mv 96,104,64
Rt 2,18,34816
s_add_u32 m0, s36, 0
Mv 100,108,64
buffer_load_dwordx4 v8, s[4:7], s38 offen offset:0 lds
Mv 160,112,64
Rt 4,19,32768
Mv 164,116,64
Mv 224,120,64
Rt 6,19,34816
Mv 228,124,64
buffer_load_dwordx4 v9, s[4:7], s38 offen offset:1024 lds
Mv 236,124,68
Rt 8,20,32768
Mv 232,120,68
Mv 172,116,68
Rt 10,20,34816
Mv 168,112,68
buffer_load_dwordx4 v10, s[4:7], s38 offen offset:2048 lds
Mv 108,108,68
Rt 12,21,32768
Mv 104,104,68
Mv 44,100,68
Rt 14,21,34816
Mv 40,96,68
buffer_load_dwordx4 v11, s[4:7], s38 offen offset:3072 lds
Mv 48,96,72
Rt 16,22,32768
Mv 52,100,72
s_add_u32 s38, s38, 0x10000
Mv 112,104,72
Rt 18,22,34816
s_add_u32 m0, s36, 4096
Mv 116,108,72
buffer_load_dwordx4 v8, s[4:7], s38 offen offset:0 lds
Mv 176,112,72
Rt 20,23,32768
Mv 180,116,72
Mv 240,120,72
Rt 22,23,34816
Mv 244,124,72
buffer_load_dwordx4 v9, s[4:7], s38 offen offset:1024 lds
Mv 252,124,76
Rt 24,24,32768
Mv 248,120,76
Mv 188,116,76
Rt 26,24,34816
Mv 184,112,76
buffer_load_dwordx4 v10, s[4:7], s38 offen offset:2048 lds
Mv 124,108,76
)" R"(
Rt 28,25,32768
Mv 120,104,76
Mv 60,100,76
Rt 30,25,34816
Mv 56,96,76
buffer_load_dwordx4 v11, s[4:7], s38 offen offset:3072 lds
Mv 64,96,80
Rb 32,16,32768
Mv 68,100,80
Mv 128,104,80
Rb 36,16,34816
Mv 132,108,80
s_add_u32 s39, s39, 0x40000
Mv 192,112,80
Rb 40,16,40960
s_add_u32 m0, s37, 16384
Mv 196,116,80
buffer_load_dwordx4 v12, s[8:11], s39 offen offset:0 lds
Ma 224,120,80
Rb 44,16,43008
Ma 228,124,80
Ma 236,124,84
Rb 48,16,49152
Ma 232,120,84
Mv 204,116,84
Rb 52,16,51200
Mv 200,112,84
buffer_load_dwordx4 v13, s[8:11], s39 offen offset:1024 lds
Mv 140,108,84
Rb 56,16,57344
Mv 136,104,84
Mv 76,100,84
Rb 60,16,59392
Mv 72,96,84
Mv 80,96,88
Mv 84,100,88
buffer_load_dwordx4 v14, s[8:11], s39 offen offset:2048 lds
Mv 144,104,88
Mv 148,108,88
Mv 208,112,88
Mv 212,116,88
Ma 240,120,88
Ma 244,124,88
buffer_load_dwordx4 v15, s[8:11], s39 offen offset:3072 lds
Ma 252,124,92
Ma 248,120,92
Mv 220,116,92
Mv 216,112,92
Mv 156,108,92
Mv 152,104,92
Mv 92,100,92
Mv 88,96,92
s_waitcnt vmcnt(13) lgkmcnt(0)
s_barrier
Mv 32,32,0
Rt 64,18,49152
Mv 36,36,0
Rb 128,28,32768
Mv 96,40,0
Rt 66,18,51200
Mv 100,44,0
s_add_u32 s39, s39, 0x40000
Rb 132,28,34816
Mv 160,48,0
Rt 68,19,49152
)" R"(
s_add_u32 m0, s37, 32768
Mv 164,52,0
buffer_load_dwordx4 v12, s[8:11], s39 offen offset:0 lds
Mv 224,56,0
Rt 70,19,51200
Mv 228,60,0
Rb 136,29,32768
Mv 236,60,4
Rt 72,20,49152
Mv 232,56,4
Rb 140,29,34816
Mv 172,52,4
Rt 74,20,51200
Mv 168,48,4
Rb 144,28,36864
Mv 108,44,4
Rt 76,21,49152
Mv 104,40,4
buffer_load_dwordx4 v13, s[8:11], s39 offen offset:1024 lds
Mv 44,36,4
Rt 78,21,51200
Mv 40,32,4
Rb 148,28,38912
Mv 48,32,8
Rt 80,22,49152
Mv 52,36,8
Rb 152,29,36864
Mv 112,40,8
Rt 82,22,51200
Mv 116,44,8
Rb 156,29,38912
Mv 176,48,8
Rt 84,23,49152
Mv 180,52,8
buffer_load_dwordx4 v14, s[8:11], s39 offen offset:2048 lds
Mv 240,56,8
Rt 86,23,51200
Mv 244,60,8
Mv 252,60,12
Rt 88,24,49152
Mv 248,56,12
Mv 188,52,12
Rt 90,24,51200
Mv 184,48,12
Mv 124,44,12
Rt 92,25,49152
Mv 120,40,12
buffer_load_dwordx4 v15, s[8:11], s39 offen offset:3072 lds
Mv 60,36,12
Rt 94,25,51200
Mv 56,32,12
s_add_u32 s44, s43, 512
s_waitcnt lgkmcnt(6)
Mv 64,32,16
Rb 96,17,32768
s_add_u32 m0, s36, 131072
Mv 68,36,16
buffer_load_dwordx4 v26, s[12:15], s44 offen offset:0 lds
Mv 128,40,16
Rb 100,17,34816
Mv 132,44,16
Mv 192,48,16
Rb 104,17,40960
Mv 196,52,16
)" R"(
buffer_load_dwordx4 v27, s[12:15], s44 offen offset:1024 lds
Ma 224,56,16
Rb 108,17,43008
Ma 228,60,16
Ma 236,60,20
Rb 112,17,49152
Ma 232,56,20
s_add_u32 s44, s43, 12800
buffer_load_dwordx4 v26, s[12:15], s44 offen offset:2048 lds
Mv 204,52,20
Rb 116,17,51200
Mv 200,48,20
Mv 140,44,20
Rb 120,17,57344
Mv 136,40,20
buffer_load_dwordx4 v27, s[12:15], s44 offen offset:3072 lds
Mv 76,36,20
Rb 124,17,59392
Mv 72,32,20
Mv 80,32,24
s_add_u32 m0, s36, 135168
s_add_u32 s44, s43, 640
Mv 84,36,24
buffer_load_dwordx4 v26, s[12:15], s44 offen offset:0 lds
Mv 144,40,24
Mv 148,44,24
Mv 208,48,24
Mv 212,52,24
buffer_load_dwordx4 v27, s[12:15], s44 offen offset:1024 lds
Ma 240,56,24
Ma 244,60,24
Ma 252,60,28
Ma 248,56,28
s_add_u32 s44, s43, 12928
buffer_load_dwordx4 v26, s[12:15], s44 offen offset:2048 lds
Mv 220,52,28
Mv 216,48,28
Mv 156,44,28
Mv 152,40,28
buffer_load_dwordx4 v27, s[12:15], s44 offen offset:3072 lds
Mv 92,36,28
Mv 88,32,28
s_waitcnt vmcnt(16) lgkmcnt(0)
s_barrier
Mv 32,96,64
Rt 0,18,0
v_xor_b32 v4, s3, v4
v_cmp_gt_u32_e32 vcc, 2, v4
s_andn2_b32 s28, 0xff, vcc_lo
s_cbranch_scc1 .Lzsp3
.Lzok3:
Mv 36,100,64
s_add_u32 s38, s38, 0xffff0080
v_xor_b32 v4, s3, v4
Mv 96,104,64
Rt 2,18,2048
s_add_u32 m0, s36, 32768
Mv 100,108,64
buffer_load_dwordx4 v8, s[4:7], s38 offen offset:0 lds
Mv 160,112,64
Rt 4,19,0
v_and_b32 v4, 1, v4
Mv 164,116,64
v_cmp_eq_u32_e32 vcc, 1, v4
s_mov_b32 s85, vcc_lo
)" R"(
Mv 224,120,64
Rt 6,19,2048
s_and_b32 s28, s2, 7
Mv 228,124,64
buffer_load_dwordx4 v9, s[4:7], s38 offen offset:1024 lds
Mv 236,124,68
Rt 8,20,0
s_lshr_b32 s28, s85, s28
Mv 232,120,68
s_xor_b32 s28, s28, s34
s_and_b32 s28, s28, 1
Mv 172,116,68
Rt 10,20,2048
s_lshl_b32 s84, s28, 13
Mv 168,112,68
buffer_load_dwordx4 v10, s[4:7], s38 offen offset:2048 lds
Mv 108,108,68
Rt 12,21,0
s_add_u32 s49, s49, s84
Mv 104,104,68
s_add_u32 s50, s50, s84
s_add_u32 s60, s50, 7168
Mv 44,100,68
Rt 14,21,2048
s_add_u32 s52, s52, s84
Mv 40,96,68
buffer_load_dwordx4 v11, s[4:7], s38 offen offset:3072 lds
Mv 48,96,72
Rt 16,22,0
s_add_u32 s65, s65, s84
Mv 52,100,72
s_add_u32 s38, s38, 0x10000
s_lshr_b32 s28, s2, 6
Mv 112,104,72
Rt 18,22,2048
s_add_u32 m0, s36, 36864
Mv 116,108,72
buffer_load_dwordx4 v8, s[4:7], s38 offen offset:0 lds
Mv 176,112,72
Rt 20,23,0
s_lshl_b32 s28, s28, 14
Mv 180,116,72
s_add_u32 s28, s28, s50
s_bfe_u32 s29, s2, 0x20004
Mv 240,120,72
Rt 22,23,2048
s_lshl2_add_u32 s29, s29, s33
Mv 244,124,72
buffer_load_dwordx4 v9, s[4:7], s38 offen offset:1024 lds
Mv 252,124,76
Rt 24,24,0
s_lshl_b32 s29, s29, 9
Mv 248,120,76
s_add_u32 s28, s28, s29
v_min_u32 v4, 3, v0
Mv 188,116,76
Rt 26,24,2048
v_lshlrev_b32 v4, 7, v4
Mv 184,112,76
buffer_load_dwordx4 v10, s[4:7], s38 offen offset:2048 lds
Mv 124,108,76
Rt 28,25,0
Mv 120,104,76
buffer_load_dword v4, v4, s[20:23], s28 offen
)" R"(
Mv 60,100,76
Rt 30,25,2048
s_lshl1_add_u32 s28, s36, s84
Mv 56,96,76
buffer_load_dwordx4 v11, s[4:7], s38 offen offset:3072 lds
Mv 64,96,80
Rb 32,16,0
v_add_u32 v2, s28, v1
Mv 68,100,80
v_add_u32 v3, 0x1000, v2
s_lshl_b32 s28, s34, 13
Mv 128,104,80
Rb 36,16,2048
s_xor_b32 s28, s28, s84
Mv 132,108,80
s_add_u32 s39, s39, 0x40000
s_and_b32 s28, s28, 0x2000
Mv 192,112,80
Rb 40,16,8192
s_add_u32 m0, s37, 49152
Mv 196,116,80
buffer_load_dwordx4 v12, s[8:11], s39 offen offset:0 lds
Ma 224,120,80
Rb 44,16,10240
s_xor_b32 s73, s73, s28
Ma 228,124,80
Ma 236,124,84
Rb 48,16,16384
Ma 232,120,84
Mv 204,116,84
Rb 52,16,18432
Mv 200,112,84
buffer_load_dwordx4 v13, s[8:11], s39 offen offset:1024 lds
Mv 140,108,84
Rb 56,16,24576
Mv 136,104,84
Mv 76,100,84
Rb 60,16,26624
Mv 72,96,84
Mv 80,96,88
Mv 84,100,88
buffer_load_dwordx4 v14, s[8:11], s39 offen offset:2048 lds
Mv 144,104,88
Mv 148,108,88
Mv 208,112,88
Mv 212,116,88
Ma 240,120,88
Ma 244,124,88
buffer_load_dwordx4 v15, s[8:11], s39 offen offset:3072 lds
Ma 252,124,92
Ma 248,120,92
Mv 220,116,92
Mv 216,112,92
Mv 156,108,92
Mv 152,104,92
Mv 92,100,92
Mv 88,96,92
s_waitcnt vmcnt(5) lgkmcnt(0)
s_barrier
Mv 32,32,0
Rt 64,18,16384
Mv 36,36,0
Rb 160,28,32768
v_and_b32 v5, 7, v0
Mv 96,40,0
)" R"(
Rt 66,18,18432
v_lshlrev_b32 v5, 7, v5
Mv 100,44,0
Rb 164,28,34816
v_mov_b32 v6, s3
Mv 160,48,0
Rt 68,19,16384
Mv 164,52,0
Mv 224,56,0
Rt 70,19,18432
Mv 228,60,0
Rb 168,29,32768
Mv 236,60,4
Rt 72,20,16384
Mv 232,56,4
Rb 172,29,34816
Mv 172,52,4
Rt 74,20,18432
Mv 168,48,4
Rb 176,28,36864
Mv 108,44,4
Rt 76,21,16384
Mv 104,40,4
Mv 44,36,4
Rt 78,21,18432
Mv 40,32,4
Rb 180,28,38912
Mv 48,32,8
Rt 80,22,16384
Mv 52,36,8
Rb 184,29,36864
Mv 112,40,8
Rt 82,22,18432
Mv 116,44,8
Rb 188,29,38912
Mv 176,48,8
Rt 84,23,16384
Mv 180,52,8
Mv 240,56,8
Rt 86,23,18432
Mv 244,60,8
Mv 252,60,12
Rt 88,24,16384
Mv 248,56,12
Mv 188,52,12
Rt 90,24,18432
Mv 184,48,12
Mv 124,44,12
Rt 92,25,16384
Mv 120,40,12
Mv 60,36,12
Rt 94,25,18432
v_lshrrev_b32 v10, 3, v0
Mv 56,32,12
v_and_b32 v11, 0x70, v1
v_lshl_or_b32 v10, v10, 13, v11
Mv 64,32,16
Rb 96,17,0
v_lshlrev_b32 v11, 1, v0
Mv 68,36,16
v_and_b32 v11, 32, v11
v_xor_b32 v10, v10, v11
buffer_load_dword v4, v7, s[20:23], s52 offen offset:0 sc0 sc1
Mv 128,40,16
)" R"(
Rb 100,17,2048
s_and_b32 s28, s33, 1
Mv 132,44,16
s_lshl_b32 s28, s28, 6
v_xor_b32 v8, s28, v10
Mv 192,48,16
Rb 104,17,8192
v_xor_b32 v9, 64, v8
Mv 196,52,16
v_add_u32 v9, 0xfc00, v9
v_add_u32 v10, 0x1f800, v8
Ma 224,56,16
Rb 108,17,10240
v_add_u32 v11, 0x1f800, v9
Ma 228,60,16
Ma 236,60,20
Rb 112,17,16384
Ma 232,56,20
Mv 204,52,20
Rb 116,17,18432
Mv 200,48,20
Mv 140,44,20
Rb 120,17,24576
Mv 136,40,20
Mv 76,36,20
Rb 124,17,26624
Mv 72,32,20
Mv 80,32,24
Mv 84,36,24
Mv 144,40,24
Mv 148,44,24
Mv 208,48,24
Mv 212,52,24
Ma 240,56,24
Ma 244,60,24
Ma 252,60,28
Ma 248,56,28
Mv 220,52,28
Mv 216,48,28
Mv 156,44,28
Mv 152,40,28
Mv 92,36,28
Mv 88,32,28
s_waitcnt vmcnt(5) lgkmcnt(0)
s_barrier
Mv 32,96,64
Rt 0,18,32768
Mv 36,100,64
s_add_u32 s92, s42, 0x0
Mv 96,104,64
Rt 2,18,34816
s_add_u32 m0, s41, 131072
Mv 100,108,64
buffer_load_dwordx4 v8, s[16:19], s92 offen offset:0 lds
Mv 160,112,64
Rt 4,19,32768
Mv 164,116,64
Mv 224,120,64
Rt 6,19,34816
Mv 228,124,64
buffer_load_dwordx4 v9, s[16:19], s92 offen offset:1024 lds
Mv 236,124,68
Rt 8,20,32768
Mv 232,120,68
Mv 172,116,68
)" R"(
Rt 10,20,34816
Mv 168,112,68
buffer_load_dwordx4 v10, s[16:19], s92 offen offset:2048 lds
Mv 108,108,68
Rt 12,21,32768
Mv 104,104,68
Mv 44,100,68
Rt 14,21,34816
Mv 40,96,68
buffer_load_dwordx4 v11, s[16:19], s92 offen offset:3072 lds
Mv 48,96,72
Rt 16,22,32768
Mv 52,100,72
s_add_u32 s92, s42, 0x40000
Mv 112,104,72
Rt 18,22,34816
s_add_u32 m0, s41, 135168
Mv 116,108,72
buffer_load_dwordx4 v8, s[16:19], s92 offen offset:0 lds
Mv 176,112,72
Rt 20,23,32768
Mv 180,116,72
Mv 240,120,72
Rt 22,23,34816
Mv 244,124,72
buffer_load_dwordx4 v9, s[16:19], s92 offen offset:1024 lds
Mv 252,124,76
Rt 24,24,32768
Mv 248,120,76
Mv 188,116,76
Rt 26,24,34816
Mv 184,112,76
buffer_load_dwordx4 v10, s[16:19], s92 offen offset:2048 lds
Mv 124,108,76
Rt 28,25,32768
Mv 120,104,76
s_add_u32 s92, s42, 0x1c0000
Mv 60,100,76
Rt 30,25,34816
s_add_u32 m0, s41, 0
Mv 56,96,76
buffer_load_dwordx4 v8, s[16:19], s92 offen offset:0 lds
Mv 64,96,80
Rb 32,16,32768
Mv 68,100,80
Mv 128,104,80
Rb 36,16,34816
Mv 132,108,80
buffer_load_dwordx4 v9, s[16:19], s92 offen offset:1024 lds
Mv 192,112,80
Rb 40,16,40960
Mv 196,116,80
Ma 224,120,80
Rb 44,16,43008
Ma 228,124,80
buffer_load_dwordx4 v10, s[16:19], s92 offen offset:2048 lds
Ma 236,124,84
Rb 48,16,49152
Ma 232,120,84
Mv 204,116,84
Rb 52,16,51200
Mv 200,112,84
buffer_load_dwordx4 v11, s[16:19], s92 offen offset:3072 lds
Mv 140,108,84
)" R"(
Rb 56,16,57344
Mv 136,104,84
s_add_u32 s92, s42, 0x200000
Mv 76,100,84
Rb 60,16,59392
s_add_u32 m0, s41, 4096
Mv 72,96,84
buffer_load_dwordx4 v8, s[16:19], s92 offen offset:0 lds
Mv 80,96,88
Mv 84,100,88
Mv 144,104,88
Mv 148,108,88
buffer_load_dwordx4 v9, s[16:19], s92 offen offset:1024 lds
Mv 208,112,88
Mv 212,116,88
Ma 240,120,88
Ma 244,124,88
buffer_load_dwordx4 v10, s[16:19], s92 offen offset:2048 lds
Ma 252,124,92
Ma 248,120,92
Mv 220,116,92
Mv 216,112,92
Mv 156,108,92
Mv 152,104,92
Mv 92,100,92
Mv 88,96,92
s_waitcnt vmcnt(14) lgkmcnt(4)
s_barrier
v_cmp_eq_u32_e32 vcc, s3, v4
s_andn2_b64 s[28:29], 1, vcc
s_cbranch_scc1 .Lzsp4
.Lzok4:
Mv 32,32,0
Rt 64,18,49152
Mv 36,36,0
Rt 66,18,51200
s_add_u32 s92, s42, 0x80
Mv 96,40,0
Rt 68,19,49152
s_add_u32 m0, s41, 65536
Mv 100,44,0
Rt 70,19,51200
buffer_load_dwordx4 v8, s[16:19], s92 offen offset:0 lds
Mv 108,44,4
Rt 72,20,49152
Mv 104,40,4
Rt 74,20,51200
Mv 44,36,4
Rt 76,21,49152
Mv 40,32,4
Rt 78,21,51200
buffer_load_dwordx4 v9, s[16:19], s92 offen offset:1024 lds
Mv 48,32,8
Rt 80,22,49152
Mv 52,36,8
Rt 82,22,51200
Mv 112,40,8
Rt 84,23,49152
Mv 116,44,8
Rt 86,23,51200
buffer_load_dwordx4 v10, s[16:19], s92 offen offset:2048 lds
Mv 124,44,12
Rt 88,24,49152
Mv 120,40,12
Rt 90,24,51200
)" R"(
Mv 60,36,12
Rt 92,25,49152
Mv 56,32,12
Rt 94,25,51200
buffer_load_dwordx4 v11, s[16:19], s92 offen offset:3072 lds
Mv 64,32,16
Rb 96,17,32768
Mv 68,36,16
Rb 100,17,34816
s_add_u32 s92, s42, 0x40080
Mv 128,40,16
Rb 104,17,40960
s_add_u32 m0, s41, 69632
Mv 132,44,16
Rb 108,17,43008
buffer_load_dwordx4 v8, s[16:19], s92 offen offset:0 lds
Mv 140,44,20
Rb 112,17,49152
Mv 136,40,20
Rb 116,17,51200
Mv 76,36,20
Rb 120,17,57344
Mv 72,32,20
Rb 124,17,59392
buffer_load_dwordx4 v9, s[16:19], s92 offen offset:1024 lds
Mv 80,32,24
Mv 84,36,24
Mv 144,40,24
Mv 148,44,24
buffer_load_dwordx4 v10, s[16:19], s92 offen offset:2048 lds
Mv 156,44,28
Mv 152,40,28
Mv 92,36,28
Mv 88,32,28
s_waitcnt lgkmcnt(0)
s_barrier
Mv 32,96,64
v_bfe_u32 v12, v0, 3, 2
v_lshlrev_b32 v12, 5, v12
Mv 36,100,64
v_and_b32 v4, 3, v0
v_lshl_or_b32 v12, v4, 3, v12
Mv 40,96,68
s_add_u32 m0, s36, 98304
s_add_u32 s44, s43, 768
Mv 44,100,68
buffer_load_dwordx4 v26, s[12:15], s44 offen offset:0 lds
Mv 48,96,72
v_bfe_u32 v4, v0, 2, 2
v_lshl_or_b32 v12, v4, 7, v12
Mv 52,100,72
v_lshrrev_b32 v4, 4, v0
v_lshl_or_b32 v12, v4, 10, v12
Mv 56,96,76
v_xor_b32 v13, 32, v12
v_xor_b32 v14, 64, v12
Mv 60,100,76
buffer_load_dwordx4 v27, s[12:15], s44 offen offset:1024 lds
Mv 64,96,80
v_xor_b32 v15, 96, v12
v_add_u32 v20, 0x20000, v12
Mv 68,100,80
v_add_u32 v21, 0x20000, v13
v_add_u32 v22, 0x20000, v14
Mv 72,96,84
)" R"(
v_add_u32 v23, 0x20000, v15
v_add_u32 v16, 0x10000, v12
Mv 76,100,84
s_add_u32 s44, s43, 13056
buffer_load_dwordx4 v26, s[12:15], s44 offen offset:2048 lds
Mv 80,96,88
v_add_u32 v17, 0x10000, v13
v_add_u32 v18, 0x10000, v14
Mv 84,100,88
v_add_u32 v19, 0x10000, v15
Cv 32,32,33
Mv 88,96,92
Cv 33,34,35
Cv 34,36,37
Mv 92,100,92
buffer_load_dwordx4 v27, s[12:15], s44 offen offset:3072 lds
Mv 96,104,64
Cv 35,38,39
Cv 40,40,41
Mv 100,108,64
Cv 41,42,43
Cv 42,44,45
Mv 104,104,68
s_add_u32 m0, s41, 32768
s_add_u32 s92, s42, 0x1c0080
Mv 108,108,68
buffer_load_dwordx4 v8, s[16:19], s92 offen offset:0 lds
Mv 112,104,72
Cv 43,46,47
Cv 48,48,49
Mv 116,108,72
Cv 49,50,51
Cv 50,52,53
Mv 120,104,76
Cv 51,54,55
Cv 56,56,57
Mv 124,108,76
buffer_store_dwordx4 v[32:35], v2, s[20:23], s45 offen offset:0 sc0 sc1
Mv 128,104,80
Cv 57,58,59
Cv 58,60,61
Mv 132,108,80
Cv 59,62,63
Cv 64,64,65
Mv 136,104,84
Cv 65,66,67
Cv 66,68,69
Mv 140,108,84
buffer_load_dwordx4 v9, s[16:19], s92 offen offset:1024 lds
Mv 144,104,88
Cv 67,70,71
Cv 72,72,73
Mv 148,108,88
Cv 73,74,75
Cv 74,76,77
Mv 152,104,92
Cv 75,78,79
Cv 80,80,81
Mv 156,108,92
buffer_store_dwordx4 v[40:43], v2, s[20:23], s45 offen offset:1024 sc0 sc1
Mv 160,48,0
Cv 81,82,83
Cv 82,84,85
Mv 160,112,64
)" R"(
Cv 83,86,87
Cv 88,88,89
Mv 164,52,0
Cv 89,90,91
Cv 90,92,93
Mv 164,116,64
buffer_load_dwordx4 v10, s[16:19], s92 offen offset:2048 lds
Mv 168,48,4
Cv 91,94,95
Cv 96,96,97
Mv 168,112,68
Cv 97,98,99
Cv 98,100,101
Mv 172,52,4
Cv 99,102,103
Cv 104,104,105
Mv 172,116,68
buffer_store_dwordx4 v[48:51], v2, s[20:23], s45 offen offset:2048 sc0 sc1
Mv 176,48,8
Cv 105,106,107
Cv 106,108,109
Mv 176,112,72
Cv 107,110,111
Cv 112,112,113
Mv 180,52,8
Cv 113,114,115
Cv 114,116,117
Mv 180,116,72
buffer_load_dwordx4 v11, s[16:19], s92 offen offset:3072 lds
Mv 184,48,12
Cv 115,118,119
Cv 120,120,121
Mv 184,112,76
Cv 121,122,123
Cv 122,124,125
Mv 188,52,12
Cv 123,126,127
Cv 128,128,129
Mv 188,116,76
buffer_store_dwordx4 v[56:59], v2, s[20:23], s45 offen offset:3072 sc0 sc1
Mv 192,48,16
Cv 129,130,131
Cv 130,132,133
Mv 192,112,80
Cv 131,134,135
Cv 136,136,137
Mv 196,52,16
s_add_u32 m0, s41, 36864
s_add_u32 s92, s42, 0x200080
Mv 196,116,80
buffer_load_dwordx4 v8, s[16:19], s92 offen offset:0 lds
Mv 200,48,20
Cv 137,138,139
Cv 138,140,141
Mv 200,112,84
Cv 139,142,143
Cv 144,144,145
Mv 204,52,20
Cv 145,146,147
Cv 146,148,149
Mv 204,116,84
buffer_store_dwordx4 v[64:67], v3, s[20:23], s45 offen offset:0 sc0 sc1
Mv 208,48,24
Cv 147,150,151
)" R"(
Cv 152,152,153
Mv 208,112,88
Cv 153,154,155
Cv 154,156,157
Mv 212,52,24
Cv 155,158,159
Mv 212,116,88
buffer_load_dwordx4 v9, s[16:19], s92 offen offset:1024 lds
Mv 216,48,28
Mv 216,112,92
Mv 220,52,28
Mv 220,116,92
buffer_store_dwordx4 v[72:75], v3, s[20:23], s45 offen offset:1024 sc0 sc1
Mv 224,56,0
Mv 228,60,0
Mv 236,60,4
Mv 232,56,4
buffer_load_dwordx4 v10, s[16:19], s92 offen offset:2048 lds
Mv 240,56,8
Mv 244,60,8
Mv 252,60,12
Mv 248,56,12
s_waitcnt vmcnt(24)
s_barrier
Ma 224,56,16
buffer_store_dwordx4 v[80:83], v3, s[20:23], s45 offen offset:2048 sc0 sc1
Ma 228,60,16
Ma 236,60,20
Ma 232,56,20
Ma 240,56,24
Ma 244,60,24
Ma 252,60,28
Ma 248,56,28
buffer_store_dwordx4 v[88:91], v3, s[20:23], s45 offen offset:3072 sc0 sc1
Mv 224,120,64
Rt 16,20,0
Mv 228,124,64
Rt 18,20,512
Mv 232,120,68
Rt 20,21,0
Mv 236,124,68
Rt 22,21,512
Mv 240,120,72
Rt 24,22,0
Mv 244,124,72
Rt 26,22,512
Mv 248,120,76
Rt 28,23,0
Mv 252,124,76
Rt 30,23,512
Ma 224,120,80
Rt 32,20,4096
Ma 228,124,80
Rt 34,20,4608
Ma 232,120,84
Rt 36,21,4096
Ma 236,124,84
Rt 38,21,4608
Ma 240,120,88
Rt 40,22,4096
Ma 244,124,88
Rt 42,22,4608
Ma 248,120,92
Rt 44,23,4096
Ma 252,124,92
)" R"(
Rt 46,23,4608
s_waitcnt vmcnt(15) lgkmcnt(8)
.LzQA:
s_cmp_eq_u32 s2, 112
s_cbranch_scc1 .LzT2S
.LzT2E:
Mz 32,16,192
Rt 0,20,8192
Cv 224,224,225
Mz 40,16,196
Rt 2,20,8704
Rb 48,28,0
Mz 44,20,196
Rt 4,21,8192
buffer_store_dwordx4 v[96:99], v2, s[20:23], s46 offen offset:0 sc0 sc1
Mz 36,20,192
Rt 6,21,8704
Rb 52,28,2048
Mz 48,24,192
Rt 8,22,8192
Cv 225,226,227
Mz 56,24,196
Rt 10,22,8704
Rb 56,29,0
buffer_load_dwordx4 a[64:67], v2, s[20:23], s51 offen offset:0 sc0 sc1
Mz 60,28,196
Rt 12,23,8192
Cv 226,228,229
Mz 52,28,192
Rt 14,23,8704
Rb 60,29,2048
s_waitcnt lgkmcnt(12)
Mv 32,32,200
Rt 16,20,12288
Cv 227,230,231
Mv 40,32,204
Rt 18,20,12800
Cv 232,232,233
Mv 44,36,204
Rt 20,21,12288
buffer_load_dwordx4 a[68:71], v2, s[20:23], s51 offen offset:1024 sc0 sc1
Mv 36,36,200
Rt 22,21,12800
Cv 233,234,235
Mv 48,40,200
Rt 24,22,12288
buffer_store_dwordx4 v[104:107], v2, s[20:23], s46 offen offset:1024 sc0 sc1
Mv 56,40,204
Rt 26,22,12800
Cv 234,236,237
Mv 60,44,204
Rt 28,23,12288
Cv 235,238,239
Mv 52,44,200
Rt 30,23,12800
buffer_load_dwordx4 a[72:75], v2, s[20:23], s51 offen offset:2048 sc0 sc1
s_waitcnt lgkmcnt(9)
Mv 32,0,208
Rt 32,20,16384
Cv 240,240,241
Mv 40,0,212
Rt 34,20,16896
Cv 241,242,243
Mv 44,4,212
)" R"(
Rt 36,21,16384
Cv 242,244,245
Mv 36,4,208
Rt 38,21,16896
Cv 243,246,247
Mv 48,8,208
Rt 40,22,16384
buffer_load_dwordx4 a[76:79], v2, s[20:23], s51 offen offset:3072 sc0 sc1
Mv 56,8,212
Rt 42,22,16896
Cv 248,248,249
Cv 249,250,251
Mv 60,12,212
Rt 44,23,16384
buffer_store_dwordx4 v[112:115], v2, s[20:23], s46 offen offset:2048 sc0 sc1
Mv 52,12,208
Rt 46,23,16896
s_waitcnt lgkmcnt(8)
Mv 32,16,216
Rt 0,20,20480
Cv 250,252,253
Mv 40,16,220
Rt 2,20,20992
buffer_load_dwordx4 a[80:83], v3, s[20:23], s51 offen offset:0 sc0 sc1
Mv 44,20,220
Rt 4,21,20480
Cv 251,254,255
v_accvgpr_read_b32 v228, a224
Mv 36,20,216
Rt 6,21,20992
v_accvgpr_read_b32 v24, a225
Mv 48,24,216
Rt 8,22,20480
Cv 228,228,24
Mv 56,24,220
Rt 10,22,20992
v_accvgpr_read_b32 v229, a226
Mv 60,28,220
Rt 12,23,20480
buffer_load_dwordx4 a[84:87], v3, s[20:23], s51 offen offset:1024 sc0 sc1
Mv 52,28,216
Rt 14,23,20992
v_accvgpr_read_b32 v25, a227
s_waitcnt lgkmcnt(8)
Mv 32,32,128
Rt 16,20,24576
buffer_store_dwordx4 v[120:123], v2, s[20:23], s46 offen offset:3072 sc0 sc1
Mv 40,32,132
Rt 18,20,25088
Cv 229,229,25
Mv 44,36,132
Rt 20,21,24576
v_accvgpr_read_b32 v230, a228
Mv 36,36,128
Rt 22,21,25088
buffer_load_dwordx4 a[88:91], v3, s[20:23], s51 offen offset:2048 sc0 sc1
Mv 48,40,128
Rt 24,22,24576
v_accvgpr_read_b32 v24, a229
Mv 56,40,132
Rt 26,22,25088
Cv 230,230,24
v_accvgpr_read_b32 v231, a230
Mv 60,44,132
)" R"(
Rt 28,23,24576
v_accvgpr_read_b32 v25, a231
Mv 52,44,128
Rt 30,23,25088
s_waitcnt lgkmcnt(8)
Mv 32,0,136
Rt 32,12,0
buffer_load_dwordx4 a[92:95], v3, s[20:23], s51 offen offset:3072 sc0 sc1
Mv 40,0,140
Rt 34,12,512
Cv 231,231,25
Mv 44,4,140
Rt 36,13,0
buffer_store_dwordx4 v[128:131], v3, s[20:23], s46 offen offset:0 sc0 sc1
Mv 36,4,136
Rt 38,13,512
v_accvgpr_read_b32 v236, a232
Mv 48,8,136
Rt 40,14,0
v_accvgpr_read_b32 v24, a233
Mv 56,8,140
Rt 42,14,512
Cv 236,236,24
Mv 60,12,140
Rt 44,15,0
v_accvgpr_read_b32 v237, a234
Mv 52,12,136
Rt 46,15,512
s_waitcnt vmcnt(13) lgkmcnt(8)
s_barrier
buffer_store_dword v6, v7, s[20:23], s49 offen offset:0 sc0 sc1
Mv 32,16,144
Rt 0,12,4096
v_accvgpr_read_b32 v25, a235
Mv 40,16,148
Rt 2,12,4608
s_add_u32 s92, s42, 0x100
Mv 44,20,148
Rt 4,13,4096
s_add_u32 m0, s41, 98304
Mv 36,20,144
Rt 6,13,4608
buffer_load_dwordx4 v8, s[16:19], s92 offen offset:0 lds
Mv 48,24,144
Rt 8,14,4096
Cv 237,237,25
Mv 56,24,148
Rt 10,14,4608
v_accvgpr_read_b32 v238, a236
v_accvgpr_read_b32 v24, a237
Mv 60,28,148
Rt 12,15,4096
Cv 238,238,24
Mv 52,28,144
Rt 14,15,4608
buffer_load_dwordx4 v9, s[16:19], s92 offen offset:1024 lds
s_waitcnt lgkmcnt(8)
Mv 32,32,152
Rt 16,12,8192
v_accvgpr_read_b32 v239, a238
Mv 40,32,156
Rt 18,12,8704
buffer_store_dwordx4 v[136:139], v3, s[20:23], s46 offen offset:1024 sc0 sc1
Mv 44,36,156
Rt 20,13,8192
)" R"(
v_accvgpr_read_b32 v25, a239
Cv 239,239,25
Mv 36,36,152
Rt 22,13,8704
buffer_load_dwordx4 v10, s[16:19], s92 offen offset:2048 lds
Mv 48,40,152
Rt 24,14,8192
v_accvgpr_read_b32 v244, a240
Mv 56,40,156
Rt 26,14,8704
v_accvgpr_read_b32 v24, a241
Mv 60,44,156
Rt 28,15,8192
Cv 244,244,24
Mv 52,44,152
Rt 30,15,8704
buffer_load_dwordx4 v11, s[16:19], s92 offen offset:3072 lds
s_waitcnt lgkmcnt(8)
Mv 32,0,160
Rt 32,12,12288
v_accvgpr_read_b32 v245, a242
Mv 40,0,164
Rt 34,12,12800
s_add_u32 s92, s42, 0x40100
Mv 44,4,164
Rt 36,13,12288
s_add_u32 m0, s41, 102400
Mv 36,4,160
Rt 38,13,12800
buffer_load_dwordx4 v8, s[16:19], s92 offen offset:0 lds
Mv 48,8,160
Rt 40,14,12288
v_accvgpr_read_b32 v25, a243
Mv 56,8,164
Rt 42,14,12800
Cv 245,245,25
v_accvgpr_read_b32 v246, a244
Mv 60,12,164
Rt 44,15,12288
v_accvgpr_read_b32 v24, a245
Mv 52,12,160
Rt 46,15,12800
buffer_load_dwordx4 v9, s[16:19], s92 offen offset:1024 lds
s_waitcnt lgkmcnt(8)
Mv 32,16,168
Rt 0,12,16384
Cv 246,246,24
Mv 40,16,172
Rt 2,12,16896
buffer_store_dwordx4 v[144:147], v3, s[20:23], s46 offen offset:2048 sc0 sc1
Mv 44,20,172
Rt 4,13,16384
v_accvgpr_read_b32 v247, a246
v_accvgpr_read_b32 v25, a247
Mv 36,20,168
Rt 6,13,16896
buffer_load_dwordx4 v10, s[16:19], s92 offen offset:2048 lds
Mv 48,24,168
Rt 8,14,16384
Cv 247,247,25
Mv 56,24,172
Rt 10,14,16896
s_add_u32 s92, s42, 0x1c0100
Mv 60,28,172
)" R"(
Rt 12,15,16384
s_add_u32 m0, s41, 131072
Mv 52,28,168
Rt 14,15,16896
buffer_load_dwordx4 v8, s[16:19], s92 offen offset:0 lds
s_waitcnt lgkmcnt(8)
Mv 32,32,176
Rt 16,12,20480
v_accvgpr_read_b32 v252, a248
Mv 40,32,180
Rt 18,12,20992
v_accvgpr_read_b32 v24, a249
Mv 44,36,180
Rt 20,13,20480
Cv 252,252,24
Mv 36,36,176
Rt 22,13,20992
buffer_load_dwordx4 v9, s[16:19], s92 offen offset:1024 lds
Mv 48,40,176
Rt 24,14,20480
v_accvgpr_read_b32 v253, a250
Mv 56,40,180
Rt 26,14,20992
v_accvgpr_read_b32 v25, a251
Cv 253,253,25
Mv 60,44,180
Rt 28,15,20480
v_accvgpr_read_b32 v254, a252
Mv 52,44,176
Rt 30,15,20992
buffer_load_dwordx4 v10, s[16:19], s92 offen offset:2048 lds
s_waitcnt lgkmcnt(8)
Mv 32,0,184
Rt 32,12,24576
v_accvgpr_read_b32 v24, a253
Mv 40,0,188
Rt 34,12,25088
buffer_store_dwordx4 v[152:155], v3, s[20:23], s46 offen offset:3072 sc0 sc1
Mv 44,4,188
Rt 36,13,24576
Cv 254,254,24
v_accvgpr_read_b32 v255, a254
Mv 36,4,184
Rt 38,13,25088
buffer_load_dwordx4 v11, s[16:19], s92 offen offset:3072 lds
Mv 48,8,184
Rt 40,14,24576
v_accvgpr_read_b32 v25, a255
Mv 56,8,188
Rt 42,14,25088
s_add_u32 s92, s42, 0x200100
Mv 60,12,188
Rt 44,15,24576
s_add_u32 m0, s41, 135168
Mv 52,12,184
Rt 46,15,25088
buffer_load_dwordx4 v8, s[16:19], s92 offen offset:0 lds
s_waitcnt lgkmcnt(8)
Mv 32,16,48
Rt 0,16,0
Cv 255,255,25
Mv 40,16,52
Rt 2,16,512
Cv 160,160,161
)" R"(
Mv 44,20,52
Rt 4,17,0
Cv 161,162,163
Mv 36,20,48
Rt 6,17,512
buffer_load_dwordx4 v9, s[16:19], s92 offen offset:1024 lds
Mv 48,24,48
Rt 8,18,0
Cv 162,164,165
Mv 56,24,52
Rt 10,18,512
Cv 163,166,167
Cv 168,168,169
Mv 60,28,52
Rt 12,19,0
Cv 169,170,171
Mv 52,28,48
Rt 14,19,512
buffer_load_dwordx4 v10, s[16:19], s92 offen offset:2048 lds
s_waitcnt lgkmcnt(8)
s_barrier
Mv 32,32,56
Rt 16,16,4096
Cv 170,172,173
Mv 40,32,60
Rt 18,16,4608
buffer_store_dwordx4 v[160:163], v2, s[20:23], s47 offen offset:0 sc0 sc1
Mv 44,36,60
Rt 20,17,4096
Cv 171,174,175
Cv 176,176,177
Mv 36,36,56
Rt 22,17,4608
s_add_u32 s92, s42, 0x180
Mv 48,40,56
Rt 24,18,4096
s_add_u32 m0, s41, 0
Mv 56,40,60
Rt 26,18,4608
buffer_load_dwordx4 v8, s[16:19], s92 offen offset:0 lds
Mv 60,44,60
Rt 28,19,4096
Cv 177,178,179
Mv 52,44,56
Rt 30,19,4608
Cv 178,180,181
s_waitcnt lgkmcnt(8)
Mz 64,0,192
Rt 32,16,8192
Cv 179,182,183
Mz 72,0,196
Rt 34,16,8704
s_add_u32 s51, s51, 0x10000
buffer_load_dwordx4 a[96:99], v2, s[20:23], s51 offen offset:0 sc0 sc1
Mz 76,4,196
Rt 36,17,8192
Cv 184,184,185
Mz 68,4,192
Rt 38,17,8704
Cv 185,186,187
Mz 80,8,192
Rt 40,18,8192
Cv 186,188,189
Mz 88,8,196
Rt 42,18,8704
)" R"(
buffer_load_dwordx4 v9, s[16:19], s92 offen offset:1024 lds
Mz 92,12,196
Rt 44,19,8192
Cv 187,190,191
v_mul_f32_e32 v32, s54, v32
Mz 84,12,192
Rt 46,19,8704
s_waitcnt lgkmcnt(8)
Mv 64,16,200
Rt 0,16,12288
Cv 192,192,193
Mv 72,16,204
Rt 2,16,12800
buffer_store_dwordx4 v[168:171], v2, s[20:23], s47 offen offset:1024 sc0 sc1
Mv 76,20,204
Rt 4,17,12288
Cv 193,194,195
Cv 194,196,197
Mv 68,20,200
Rt 6,17,12800
Cv 195,198,199
v_mul_f32_e32 v33, s54, v33
Mv 80,24,200
Rt 8,18,12288
Cv 200,200,201
Mv 88,24,204
Rt 10,18,12800
buffer_load_dwordx4 v10, s[16:19], s92 offen offset:2048 lds
Mv 92,28,204
Rt 12,19,12288
Cv 201,202,203
Cv 32,32,33
Mv 84,28,200
Rt 14,19,12800
Cv 202,204,205
s_waitcnt lgkmcnt(8)
Mv 64,32,208
Rt 16,16,16384
Cv 203,206,207
Mv 72,32,212
Rt 18,16,16896
buffer_load_dwordx4 a[100:103], v2, s[20:23], s51 offen offset:1024 sc0 sc1
Mv 76,36,212
Rt 20,17,16384
Cv 208,208,209
Mv 68,36,208
Rt 22,17,16896
Cv 209,210,211
v_accvgpr_write_b32 a224, v32
Mv 80,40,208
Rt 24,18,16384
Cv 210,212,213
Mv 88,40,212
Rt 26,18,16896
buffer_load_dwordx4 v11, s[16:19], s92 offen offset:3072 lds
Mv 92,44,212
Rt 28,19,16384
Cv 211,214,215
v_mul_f32_e32 v34, s54, v34
Mv 84,44,208
Rt 30,19,16896
s_waitcnt lgkmcnt(8)
Mv 64,0,216
Rt 32,16,20480
)" R"(
Cv 216,216,217
Mv 72,0,220
Rt 34,16,20992
buffer_store_dwordx4 v[176:179], v2, s[20:23], s47 offen offset:2048 sc0 sc1
Mv 76,4,220
Rt 36,17,20480
Cv 217,218,219
Cv 218,220,221
Mv 68,4,216
Rt 38,17,20992
s_add_u32 s92, s42, 0x40180
v_mul_f32_e32 v35, s54, v35
Mv 80,8,216
Rt 40,18,20480
s_add_u32 m0, s41, 4096
Mv 88,8,220
Rt 42,18,20992
buffer_load_dwordx4 v8, s[16:19], s92 offen offset:0 lds
Mv 92,12,220
Rt 44,19,20480
Cv 219,222,223
Cv 34,34,35
Mv 84,12,216
Rt 46,19,20992
s_waitcnt lgkmcnt(8)
Mv 64,16,128
Rt 0,16,24576
v_accvgpr_write_b32 a225, v34
Mv 72,16,132
Rt 2,16,25088
buffer_load_dwordx4 a[104:107], v2, s[20:23], s51 offen offset:2048 sc0 sc1
Mv 76,20,132
Rt 4,17,24576
v_mul_f32_e32 v36, s54, v36
Mv 68,20,128
Rt 6,17,25088
v_mul_f32_e32 v37, s54, v37
Cv 36,36,37
Mv 80,24,128
Rt 8,18,24576
v_accvgpr_write_b32 a226, v36
Mv 88,24,132
Rt 10,18,25088
buffer_load_dwordx4 v9, s[16:19], s92 offen offset:1024 lds
Mv 92,28,132
Rt 12,19,24576
v_mul_f32_e32 v38, s54, v38
v_mul_f32_e32 v39, s54, v39
Mv 84,28,128
Rt 14,19,25088
s_waitcnt lgkmcnt(8)
Mv 64,32,136
Rt 16,12,32768
buffer_store_dwordx4 v[184:187], v2, s[20:23], s47 offen offset:3072 sc0 sc1
Mv 72,32,140
Rt 18,12,33280
Cv 38,38,39
v_accvgpr_write_b32 a227, v38
Mv 76,36,140
Rt 20,13,32768
v_mul_f32_e32 v40, s54, v40
Mv 68,36,136
Rt 22,13,33280
buffer_load_dwordx4 a[108:111], v2, s[20:23], s51 offen offset:3072 sc0 sc1
)" R"(
Mv 80,40,136
Rt 24,14,32768
v_mul_f32_e32 v41, s54, v41
Mv 88,40,140
Rt 26,14,33280
buffer_load_dwordx4 v10, s[16:19], s92 offen offset:2048 lds
Mv 92,44,140
Rt 28,15,32768
Cv 40,40,41
v_accvgpr_write_b32 a228, v40
Mv 84,44,136
Rt 30,15,33280
s_waitcnt vmcnt(19) lgkmcnt(8)
s_barrier
buffer_store_dword v6, v7, s[20:23], s49 offen offset:1024 sc0 sc1
Mv 64,0,144
Rt 32,12,36864
buffer_load_dword v4, v5, s[20:23], s50 offen offset:0 sc0 sc1
Mv 72,0,148
Rt 34,12,37376
v_mul_f32_e32 v42, s54, v42
v_mul_f32_e32 v43, s54, v43
Mv 76,4,148
Rt 36,13,36864
Cv 42,42,43
Mv 68,4,144
Rt 38,13,37376
s_add_u32 s92, s42, 0x1c0180
v_accvgpr_write_b32 a229, v42
Mv 80,8,144
Rt 40,14,36864
s_add_u32 m0, s41, 65536
Mv 88,8,148
Rt 42,14,37376
buffer_load_dwordx4 v8, s[16:19], s92 offen offset:0 lds
Mv 92,12,148
Rt 44,15,36864
v_mul_f32_e32 v44, s54, v44
v_mul_f32_e32 v45, s54, v45
Mv 84,12,144
Rt 46,15,37376
s_waitcnt lgkmcnt(8)
Mv 64,16,152
Rt 0,12,40960
Cv 44,44,45
Mv 72,16,156
Rt 2,12,41472
buffer_load_dwordx4 a[112:115], v3, s[20:23], s51 offen offset:0 sc0 sc1
Mv 76,20,156
Rt 4,13,40960
v_accvgpr_write_b32 a230, v44
Mv 68,20,152
Rt 6,13,41472
v_mul_f32_e32 v46, s54, v46
v_mul_f32_e32 v47, s54, v47
Mv 80,24,152
Rt 8,14,40960
Cv 46,46,47
Mv 88,24,156
Rt 10,14,41472
buffer_load_dwordx4 v9, s[16:19], s92 offen offset:1024 lds
Mv 92,28,156
Rt 12,15,40960
v_accvgpr_write_b32 a231, v46
v_mul_f32_e32 v48, s54, v48
)" R"(
Mv 84,28,152
Rt 14,15,41472
s_waitcnt lgkmcnt(8)
Mv 64,32,160
Rt 16,12,45056
v_mul_f32_e32 v49, s54, v49
Mv 72,32,164
Rt 18,12,45568
buffer_store_dwordx4 v[192:195], v3, s[20:23], s47 offen offset:0 sc0 sc1
Mv 76,36,164
Rt 20,13,45056
Cv 48,48,49
Mv 68,36,160
Rt 22,13,45568
v_accvgpr_write_b32 a232, v48
v_mul_f32_e32 v50, s54, v50
Mv 80,40,160
Rt 24,14,45056
v_mul_f32_e32 v51, s54, v51
Mv 88,40,164
Rt 26,14,45568
buffer_load_dwordx4 v10, s[16:19], s92 offen offset:2048 lds
Mv 92,44,164
Rt 28,15,45056
Cv 50,50,51
v_accvgpr_write_b32 a233, v50
Mv 84,44,160
Rt 30,15,45568
s_waitcnt lgkmcnt(8)
Mv 64,0,168
Rt 32,12,49152
v_mul_f32_e32 v52, s54, v52
Mv 72,0,172
Rt 34,12,49664
buffer_load_dwordx4 a[116:119], v3, s[20:23], s51 offen offset:1024 sc0 sc1
Mv 76,4,172
Rt 36,13,49152
v_mul_f32_e32 v53, s54, v53
Mv 68,4,168
Rt 38,13,49664
Cv 52,52,53
v_accvgpr_write_b32 a234, v52
Mv 80,8,168
Rt 40,14,49152
v_mul_f32_e32 v54, s54, v54
Mv 88,8,172
Rt 42,14,49664
buffer_load_dwordx4 v11, s[16:19], s92 offen offset:3072 lds
Mv 92,12,172
Rt 44,15,49152
v_mul_f32_e32 v55, s54, v55
Cv 54,54,55
Mv 84,12,168
Rt 46,15,49664
s_waitcnt lgkmcnt(8)
Mv 64,16,176
Rt 0,12,53248
v_accvgpr_write_b32 a235, v54
Mv 72,16,180
Rt 2,12,53760
buffer_store_dwordx4 v[200:203], v3, s[20:23], s47 offen offset:1024 sc0 sc1
Mv 76,20,180
Rt 4,13,53248
v_mul_f32_e32 v56, s54, v56
)" R"(
Mv 68,20,176
Rt 6,13,53760
s_add_u32 s92, s42, 0x200180
v_mul_f32_e32 v57, s54, v57
Mv 80,24,176
Rt 8,14,53248
s_add_u32 m0, s41, 69632
Mv 88,24,180
Rt 10,14,53760
buffer_load_dwordx4 v8, s[16:19], s92 offen offset:0 lds
Mv 92,28,180
Rt 12,15,53248
Cv 56,56,57
v_accvgpr_write_b32 a236, v56
Mv 84,28,176
Rt 14,15,53760
s_waitcnt lgkmcnt(8)
Mv 64,32,184
Rt 16,12,57344
v_mul_f32_e32 v58, s54, v58
Mv 72,32,188
Rt 18,12,57856
buffer_load_dwordx4 a[120:123], v3, s[20:23], s51 offen offset:2048 sc0 sc1
Mv 76,36,188
Rt 20,13,57344
v_mul_f32_e32 v59, s54, v59
Mv 68,36,184
Rt 22,13,57856
Cv 58,58,59
v_accvgpr_write_b32 a237, v58
Mv 80,40,184
Rt 24,14,57344
v_mul_f32_e32 v60, s54, v60
Mv 88,40,188
Rt 26,14,57856
buffer_load_dwordx4 v9, s[16:19], s92 offen offset:1024 lds
Mv 92,44,188
Rt 28,15,57344
v_mul_f32_e32 v61, s54, v61
Cv 60,60,61
Mv 84,44,184
Rt 30,15,57856
s_waitcnt lgkmcnt(8)
Mv 64,0,48
Rt 32,16,32768
buffer_store_dwordx4 v[208:211], v3, s[20:23], s47 offen offset:2048 sc0 sc1
Mv 72,0,52
Rt 34,16,33280
v_accvgpr_write_b32 a238, v60
v_mul_f32_e32 v62, s54, v62
Mv 76,4,52
Rt 36,17,32768
v_mul_f32_e32 v63, s54, v63
Mv 68,4,48
Rt 38,17,33280
buffer_load_dwordx4 a[124:127], v3, s[20:23], s51 offen offset:3072 sc0 sc1
Mv 80,8,48
Rt 40,18,32768
Cv 62,62,63
Mv 88,8,52
Rt 42,18,33280
buffer_load_dwordx4 v10, s[16:19], s92 offen offset:2048 lds
Mv 92,12,52
Rt 44,19,32768
)" R"(
v_accvgpr_write_b32 a239, v62
Mv 84,12,48
Rt 46,19,33280
s_waitcnt vmcnt(14) lgkmcnt(8)
s_barrier
Mv 64,16,56
Rt 0,16,36864
v_cmp_eq_u32_e32 vcc, s3, v4
s_andn2_b64 s[28:29], -1, vcc
s_cbranch_scc1 .Lzsp5
.Lzok5:
Mv 72,16,60
Rt 2,16,37376
s_add_u32 s40, s55, 0x10000
Mv 76,20,60
Rt 4,17,36864
Mv 68,20,56
Rt 6,17,37376
Mv 80,24,56
Rt 8,18,36864
s_add_u32 m0, s36, 32768
Mv 88,24,60
Rt 10,18,37376
buffer_load_dwordx4 v2, s[20:23], s40 offen offset:0 sc1 lds
Mv 92,28,60
Rt 12,19,36864
Mv 84,28,56
Rt 14,19,37376
s_waitcnt lgkmcnt(8)
Mz 32,32,192
Rt 16,16,40960
buffer_store_dwordx4 v[216:219], v3, s[20:23], s47 offen offset:3072 sc0 sc1
Mz 40,32,196
Rt 18,16,41472
Mz 44,36,196
Rt 20,17,40960
Mz 36,36,192
Rt 22,17,41472
buffer_load_dwordx4 v2, s[20:23], s40 offen offset:1024 sc1 lds
Mz 48,40,192
Rt 24,18,40960
v_mul_f32_e32 v64, s54, v64
Mz 56,40,196
Rt 26,18,41472
v_mul_f32_e32 v65, s54, v65
Mz 60,44,196
Rt 28,19,40960
Cv 64,64,65
Mz 52,44,192
Rt 30,19,41472
s_waitcnt lgkmcnt(8)
Mv 32,0,200
Rt 32,16,45056
v_accvgpr_write_b32 a240, v64
Mv 40,0,204
Rt 34,16,45568
buffer_load_dwordx4 v2, s[20:23], s40 offen offset:2048 sc1 lds
Mv 44,4,204
Rt 36,17,45056
v_mul_f32_e32 v66, s54, v66
Mv 36,4,200
Rt 38,17,45568
v_mul_f32_e32 v67, s54, v67
Mv 48,8,200
Rt 40,18,45056
)" R"(
buffer_store_dwordx4 v[224:227], v2, s[20:23], s48 offen offset:0 sc0 sc1
Mv 56,8,204
Rt 42,18,45568
Cv 66,66,67
Mv 60,12,204
Rt 44,19,45056
v_accvgpr_write_b32 a241, v66
Mv 52,12,200
Rt 46,19,45568
buffer_load_dwordx4 v2, s[20:23], s40 offen offset:3072 sc1 lds
s_waitcnt lgkmcnt(8)
Mv 32,16,208
Rt 0,16,49152
v_mul_f32_e32 v68, s54, v68
Mv 40,16,212
Rt 2,16,49664
v_mul_f32_e32 v69, s54, v69
Mv 44,20,212
Rt 4,17,49152
Cv 68,68,69
Mv 36,20,208
Rt 6,17,49664
v_accvgpr_write_b32 a242, v68
Mv 48,24,208
Rt 8,18,49152
s_add_u32 m0, s36, 36864
Mv 56,24,212
Rt 10,18,49664
buffer_load_dwordx4 v3, s[20:23], s40 offen offset:0 sc1 lds
Mv 60,28,212
Rt 12,19,49152
v_mul_f32_e32 v70, s54, v70
Mv 52,28,208
Rt 14,19,49664
s_waitcnt lgkmcnt(8)
Mv 32,32,216
Rt 16,16,53248
buffer_store_dwordx4 v[232:235], v2, s[20:23], s48 offen offset:1024 sc0 sc1
Mv 40,32,220
Rt 18,16,53760
v_mul_f32_e32 v71, s54, v71
Mv 44,36,220
Rt 20,17,53248
Cv 70,70,71
Mv 36,36,216
Rt 22,17,53760
buffer_load_dwordx4 v3, s[20:23], s40 offen offset:1024 sc1 lds
Mv 48,40,216
Rt 24,18,53248
v_accvgpr_write_b32 a243, v70
Mv 56,40,220
Rt 26,18,53760
v_mul_f32_e32 v72, s54, v72
Mv 60,44,220
Rt 28,19,53248
v_mul_f32_e32 v73, s54, v73
Mv 52,44,216
Rt 30,19,53760
s_waitcnt lgkmcnt(8)
Mv 32,0,128
Rt 32,16,57344
Cv 72,72,73
Mv 40,0,132
Rt 34,16,57856
)" R"(
buffer_load_dwordx4 v3, s[20:23], s40 offen offset:2048 sc1 lds
Mv 44,4,132
Rt 36,17,57344
v_accvgpr_write_b32 a244, v72
Mv 36,4,128
Rt 38,17,57856
v_mul_f32_e32 v74, s54, v74
Mv 48,8,128
Rt 40,18,57344
buffer_store_dwordx4 v[240:243], v2, s[20:23], s48 offen offset:2048 sc0 sc1
Mv 56,8,132
Rt 42,18,57856
v_mul_f32_e32 v75, s54, v75
Mv 60,12,132
Rt 44,19,57344
Cv 74,74,75
Mv 52,12,128
Rt 46,19,57856
buffer_load_dwordx4 v3, s[20:23], s40 offen offset:3072 sc1 lds
s_waitcnt lgkmcnt(8)
Mv 32,16,136
Rt 0,20,0
v_accvgpr_write_b32 a245, v74
Mv 40,16,140
Rt 2,20,512
v_mul_f32_e32 v76, s54, v76
Mv 44,20,140
Rt 4,21,0
v_mul_f32_e32 v77, s54, v77
Mv 36,20,136
Rt 6,21,512
Cv 76,76,77
Mv 48,24,136
Rt 8,22,0
v_accvgpr_write_b32 a246, v76
Mv 56,24,140
Rt 10,22,512
v_mul_f32_e32 v78, s54, v78
Mv 60,28,140
Rt 12,23,0
v_mul_f32_e32 v79, s54, v79
Mv 52,28,136
Rt 14,23,512
s_waitcnt vmcnt(10) lgkmcnt(8)
s_barrier
buffer_store_dword v6, v7, s[20:23], s49 offen offset:2048 sc0 sc1
Mv 32,32,144
Rt 16,20,4096
s_mov_b32 s83, 0xc108000
buffer_load_dwordx2 v[24:25], v7, s[20:23], s83 offen sc0 sc1
Mv 40,32,148
Rt 18,20,4608
Cv 78,78,79
Mv 44,36,148
Rt 20,21,4096
s_add_u32 m0, s36, 98304
Mv 36,36,144
Rt 22,21,4608
buffer_load_dwordx4 v2, s[20:23], s55 offen offset:0 sc1 lds
Mv 48,40,144
Rt 24,22,4096
v_accvgpr_write_b32 a247, v78
Mv 56,40,148
Rt 26,22,4608
v_mul_f32_e32 v80, s54, v80
)" R"(
Mv 60,44,148
Rt 28,23,4096
buffer_store_dwordx4 v[248:251], v2, s[20:23], s48 offen offset:3072 sc0 sc1
Mv 52,44,144
Rt 30,23,4608
s_waitcnt lgkmcnt(8)
Mv 32,0,152
Rt 32,20,8192
v_mul_f32_e32 v81, s54, v81
Mv 40,0,156
Rt 34,20,8704
buffer_load_dwordx4 v2, s[20:23], s55 offen offset:1024 sc1 lds
Mv 44,4,156
Rt 36,21,8192
Cv 80,80,81
Mv 36,4,152
Rt 38,21,8704
v_accvgpr_write_b32 a248, v80
Mv 48,8,152
Rt 40,22,8192
v_mul_f32_e32 v82, s54, v82
Mv 56,8,156
Rt 42,22,8704
v_mul_f32_e32 v83, s54, v83
Mv 60,12,156
Rt 44,23,8192
Cv 82,82,83
Mv 52,12,152
Rt 46,23,8704
buffer_load_dwordx4 v2, s[20:23], s55 offen offset:2048 sc1 lds
s_waitcnt lgkmcnt(8)
Mv 32,16,160
Rt 0,20,12288
v_accvgpr_write_b32 a249, v82
Mv 40,16,164
Rt 2,20,12800
v_mul_f32_e32 v84, s54, v84
Mv 44,20,164
Rt 4,21,12288
buffer_store_dwordx4 v[228:231], v3, s[20:23], s48 offen offset:0 sc0 sc1
Mv 36,20,160
Rt 6,21,12800
v_mul_f32_e32 v85, s54, v85
Mv 48,24,160
Rt 8,22,12288
Cv 84,84,85
Mv 56,24,164
Rt 10,22,12800
buffer_load_dwordx4 v2, s[20:23], s55 offen offset:3072 sc1 lds
Mv 60,28,164
Rt 12,23,12288
v_accvgpr_write_b32 a250, v84
Mv 52,28,160
Rt 14,23,12800
s_waitcnt lgkmcnt(8)
Mv 32,32,168
Rt 16,20,16384
v_mul_f32_e32 v86, s54, v86
Mv 40,32,172
Rt 18,20,16896
v_mul_f32_e32 v87, s54, v87
Mv 44,36,172
Rt 20,21,16384
s_add_u32 m0, s36, 102400
)" R"(
Mv 36,36,168
Rt 22,21,16896
buffer_load_dwordx4 v3, s[20:23], s55 offen offset:0 sc1 lds
Mv 48,40,168
Rt 24,22,16384
Cv 86,86,87
Mv 56,40,172
Rt 26,22,16896
v_accvgpr_write_b32 a251, v86
Mv 60,44,172
Rt 28,23,16384
buffer_store_dwordx4 v[236:239], v3, s[20:23], s48 offen offset:1024 sc0 sc1
Mv 52,44,168
Rt 30,23,16896
s_waitcnt lgkmcnt(8)
Mv 32,0,176
Rt 32,20,20480
v_mul_f32_e32 v88, s54, v88
Mv 40,0,180
Rt 34,20,20992
buffer_load_dwordx4 v3, s[20:23], s55 offen offset:1024 sc1 lds
Mv 44,4,180
Rt 36,21,20480
v_mul_f32_e32 v89, s54, v89
Mv 36,4,176
Rt 38,21,20992
Cv 88,88,89
Mv 48,8,176
Rt 40,22,20480
s_add_u32 s50, s50, 1024
Mv 56,8,180
Rt 42,22,20992
s_min_u32 s50, s50, s60
Mv 60,12,180
Rt 44,23,20480
v_accvgpr_write_b32 a252, v88
Mv 52,12,176
Rt 46,23,20992
buffer_load_dwordx4 v3, s[20:23], s55 offen offset:2048 sc1 lds
s_waitcnt lgkmcnt(8)
Mv 32,16,184
Rt 0,20,24576
v_mul_f32_e32 v90, s54, v90
Mv 40,16,188
Rt 2,20,25088
v_mul_f32_e32 v91, s54, v91
Mv 44,20,188
Rt 4,21,24576
buffer_store_dwordx4 v[244:247], v3, s[20:23], s48 offen offset:2048 sc0 sc1
Mv 36,20,184
Rt 6,21,25088
Cv 90,90,91
Mv 48,24,184
Rt 8,22,24576
v_accvgpr_write_b32 a253, v90
Mv 56,24,188
Rt 10,22,25088
buffer_load_dwordx4 v3, s[20:23], s55 offen offset:3072 sc1 lds
Mv 60,28,188
Rt 12,23,24576
v_mul_f32_e32 v92, s54, v92
Mv 52,28,184
Rt 14,23,25088
s_waitcnt lgkmcnt(8)
)" R"(
Mv 32,32,48
Rt 16,12,0
v_mul_f32_e32 v93, s54, v93
Mv 40,32,52
Rt 18,12,512
Cv 92,92,93
Mv 44,36,52
Rt 20,13,0
v_accvgpr_write_b32 a254, v92
Mv 36,36,48
Rt 22,13,512
v_mul_f32_e32 v94, s54, v94
Mv 48,40,48
Rt 24,14,0
buffer_store_dwordx4 v[252:255], v3, s[20:23], s48 offen offset:3072 sc0 sc1
Mv 56,40,52
Rt 26,14,512
v_mul_f32_e32 v95, s54, v95
Mv 60,44,52
Rt 28,15,0
Cv 94,94,95
Mv 52,44,48
Rt 30,15,512
s_waitcnt vmcnt(13) lgkmcnt(8)
s_barrier
v_cmp_eq_u32_e32 vcc, s3, v25
s_andn2_b64 s[28:29], 1, vcc
s_cbranch_scc1 .Lzsp7
.Lzok7:
Mv 32,0,56
Rt 32,12,4096
buffer_load_dword v4, v5, s[20:23], s50 offen offset:0 sc0 sc1
Mv 40,0,60
Rt 34,12,4608
v_accvgpr_write_b32 a255, v94
Mv 44,4,60
Rt 36,13,4096
s_add_u32 m0, s36, 131072
Mv 36,4,56
Rt 38,13,4608
buffer_load_dwordx4 v2, s[20:23], s56 offen offset:0 sc1 lds
Mv 48,8,56
Rt 40,14,4096
Mv 56,8,60
Rt 42,14,4608
Mv 60,12,60
Rt 44,15,4096
Mv 52,12,56
Rt 46,15,4608
s_waitcnt lgkmcnt(8)
Mz 96,16,192
Rt 0,12,8192
Mz 104,16,196
Rt 2,12,8704
buffer_load_dwordx4 v2, s[20:23], s56 offen offset:1024 sc1 lds
Mz 108,20,196
Rt 4,13,8192
Mz 100,20,192
Rt 6,13,8704
Mz 112,24,192
Rt 8,14,8192
v_mul_f32_e32 v32, s54, v32
Mz 120,24,196
Rt 10,14,8704
v_mul_f32_e32 v33, s54, v33
)" R"(
Mz 124,28,196
Rt 12,15,8192
Cv 32,32,33
Mz 116,28,192
Rt 14,15,8704
buffer_load_dwordx4 v2, s[20:23], s56 offen offset:2048 sc1 lds
s_waitcnt lgkmcnt(8)
Mv 96,32,200
Rt 16,12,12288
v_mul_f32_e32 v34, s54, v34
Mv 104,32,204
Rt 18,12,12800
v_accvgpr_write_b32 a192, v32
Mv 108,36,204
Rt 20,13,12288
v_mul_f32_e32 v35, s54, v35
Mv 100,36,200
Rt 22,13,12800
Cv 34,34,35
v_accvgpr_write_b32 a193, v34
Mv 112,40,200
Rt 24,14,12288
v_mul_f32_e32 v36, s54, v36
Mv 120,40,204
Rt 26,14,12800
buffer_load_dwordx4 v2, s[20:23], s56 offen offset:3072 sc1 lds
Mv 124,44,204
Rt 28,15,12288
v_mul_f32_e32 v37, s54, v37
Mv 116,44,200
Rt 30,15,12800
s_waitcnt lgkmcnt(8)
Mv 96,0,208
Rt 32,12,16384
Cv 36,36,37
Mv 104,0,212
Rt 34,12,16896
v_accvgpr_write_b32 a194, v36
Mv 108,4,212
Rt 36,13,16384
s_add_u32 m0, s36, 135168
Mv 100,4,208
Rt 38,13,16896
buffer_load_dwordx4 v3, s[20:23], s56 offen offset:0 sc1 lds
Mv 112,8,208
Rt 40,14,16384
v_mul_f32_e32 v38, s54, v38
Mv 120,8,212
Rt 42,14,16896
v_mul_f32_e32 v39, s54, v39
Mv 124,12,212
Rt 44,15,16384
Cv 38,38,39
Mv 116,12,208
Rt 46,15,16896
s_waitcnt lgkmcnt(8)
Mv 96,16,216
Rt 0,12,20480
v_accvgpr_write_b32 a195, v38
Mv 104,16,220
Rt 2,12,20992
buffer_load_dwordx4 v3, s[20:23], s56 offen offset:1024 sc1 lds
Mv 108,20,220
Rt 4,13,20480
)" R"(
v_mul_f32_e32 v40, s54, v40
Mv 100,20,216
Rt 6,13,20992
v_mul_f32_e32 v41, s54, v41
Cv 40,40,41
Mv 112,24,216
Rt 8,14,20480
v_accvgpr_write_b32 a196, v40
Mv 120,24,220
Rt 10,14,20992
v_mul_f32_e32 v42, s54, v42
Mv 124,28,220
Rt 12,15,20480
v_mul_f32_e32 v43, s54, v43
Mv 116,28,216
Rt 14,15,20992
buffer_load_dwordx4 v3, s[20:23], s56 offen offset:2048 sc1 lds
s_waitcnt lgkmcnt(8)
Mv 96,32,128
Rt 16,12,24576
Cv 42,42,43
Mv 104,32,132
Rt 18,12,25088
v_accvgpr_write_b32 a197, v42
Mv 108,36,132
Rt 20,13,24576
v_mul_f32_e32 v44, s54, v44
Mv 100,36,128
Rt 22,13,25088
v_mul_f32_e32 v45, s54, v45
Cv 44,44,45
Mv 112,40,128
Rt 24,14,24576
v_accvgpr_write_b32 a198, v44
Mv 120,40,132
Rt 26,14,25088
buffer_load_dwordx4 v3, s[20:23], s56 offen offset:3072 sc1 lds
Mv 124,44,132
Rt 28,15,24576
v_mul_f32_e32 v46, s54, v46
Mv 116,44,128
Rt 30,15,25088
s_waitcnt lgkmcnt(8)
Mv 96,0,136
Rt 32,16,0
v_readfirstlane_b32 s63, v24
Mv 104,0,140
Rt 34,16,512
v_and_b32 v24, 16, v1
Mv 108,4,140
Rt 36,17,0
v_lshrrev_b32_e64 v24, v24, s63
Mv 100,4,136
Rt 38,17,512
v_and_b32 v24, 0x7ff, v24
v_mul_u32_u24 v24, 896, v24
Mv 112,8,136
Rt 40,18,0
v_and_b32 v25, 14, v0
Mv 120,8,140
Rt 42,18,512
v_min_u32 v25, 1, v25
Mv 124,12,140
Rt 44,19,0
)" R"(
v_lshlrev_b32 v25, 30, v25
v_and_or_b32 v25, v0, 48, v25
Mv 116,12,136
Rt 46,19,512
s_waitcnt vmcnt(8) lgkmcnt(8)
s_barrier
buffer_store_dword v6, v7, s[20:23], s49 offen offset:3072 sc0 sc1
Mv 96,16,144
Rt 0,16,4096
v_cmp_eq_u32_e32 vcc, s3, v4
s_andn2_b64 s[28:29], -1, vcc
s_cbranch_scc1 .Lzsp6
.Lzok6:
Mv 104,16,148
Rt 2,16,4608
v_add_u32 v24, v24, v25
v_add_u32 v8, 0x10000, v1
Mv 108,20,148
Rt 4,17,4096
s_and_b32 s83, s33, 1
v_add_u32 v10, 0x20000, v1
Mv 100,20,144
Rt 6,17,4608
s_mul_i32 s82, s83, 448
v_mul_f32_e32 v47, s54, v47
s_add_u32 s56, s56, 0x10000
Mv 112,24,144
Rt 8,18,4096
Cv 46,46,47
s_add_u32 s55, s55, 0x10000
Mv 120,24,148
Rt 10,18,4608
v_accvgpr_write_b32 a199, v46
s_add_u32 s55, s55, 0x10000
Mv 124,28,148
Rt 12,19,4096
v_mul_f32_e32 v48, s54, v48
s_cmp_eq_u32 s57, 2
s_cselect_b32 s55, s59, s55
Mv 116,28,144
Rt 14,19,4608
s_mov_b32 s93, 0
s_waitcnt lgkmcnt(8)
Mv 96,32,152
Rt 16,16,8192
v_mul_f32_e32 v49, s54, v49
s_mov_b32 s94, 1
Mv 104,32,156
Rt 18,16,8704
Cv 48,48,49
Mv 108,36,156
Rt 20,17,8192
v_accvgpr_write_b32 a200, v48
Mv 100,36,152
Rt 22,17,8704
v_mul_f32_e32 v50, s54, v50
v_mul_f32_e32 v51, s54, v51
Mv 112,40,152
Rt 24,18,8192
Cv 50,50,51
Mv 120,40,156
Rt 26,18,8704
v_accvgpr_write_b32 a201, v50
Mv 124,44,156
Rt 28,19,8192
)" R"(
v_mul_f32_e32 v52, s54, v52
Mv 116,44,152
Rt 30,19,8704
s_waitcnt lgkmcnt(8)
Mv 96,0,160
Rt 32,16,12288
v_mul_f32_e32 v53, s54, v53
Mv 104,0,164
Rt 34,16,12800
Cv 52,52,53
Mv 108,4,164
Rt 36,17,12288
v_accvgpr_write_b32 a202, v52
Mv 100,4,160
Rt 38,17,12800
buffer_load_dwordx4 a[128:131], v24, s[12:15], s82 offen offset:0
Mv 112,8,160
Rt 40,18,12288
v_mul_f32_e32 v54, s54, v54
Mv 120,8,164
Rt 42,18,12800
v_mul_f32_e32 v55, s54, v55
Mv 124,12,164
Rt 44,19,12288
Cv 54,54,55
Mv 116,12,160
Rt 46,19,12800
s_waitcnt lgkmcnt(8)
Mv 96,16,168
Rt 0,16,16384
buffer_load_dwordx4 a[132:135], v24, s[12:15], s82 offen offset:64
Mv 104,16,172
Rt 2,16,16896
v_accvgpr_write_b32 a203, v54
Mv 108,20,172
Rt 4,17,16384
v_mul_f32_e32 v56, s54, v56
Mv 100,20,168
Rt 6,17,16896
v_mul_f32_e32 v57, s54, v57
Cv 56,56,57
Mv 112,24,168
Rt 8,18,16384
v_accvgpr_write_b32 a204, v56
Mv 120,24,172
Rt 10,18,16896
buffer_load_dwordx4 a[136:139], v24, s[12:15], s82 offen offset:128
Mv 124,28,172
Rt 12,19,16384
v_mul_f32_e32 v58, s54, v58
Mv 116,28,168
Rt 14,19,16896
s_waitcnt lgkmcnt(8)
Mv 96,32,176
Rt 16,16,20480
buffer_load_dwordx4 a[140:143], v24, s[12:15], s82 offen offset:192
Mv 104,32,180
Rt 18,16,20992
v_mul_f32_e32 v59, s54, v59
Mv 108,36,180
Rt 20,17,20480
Cv 58,58,59
Mv 100,36,176
Rt 22,17,20992
)" R"(
v_accvgpr_write_b32 a205, v58
v_mul_f32_e32 v60, s54, v60
Mv 112,40,176
Rt 24,18,20480
v_mul_f32_e32 v61, s54, v61
Mv 120,40,180
Rt 26,18,20992
buffer_load_dwordx4 a[144:147], v24, s[12:15], s82 offen offset:256
Mv 124,44,180
Rt 28,19,20480
Cv 60,60,61
Mv 116,44,176
Rt 30,19,20992
s_waitcnt lgkmcnt(8)
s_barrier
Mv 96,0,184
Rt 32,16,24576
buffer_load_dwordx4 a[148:151], v24, s[12:15], s82 offen offset:320
Mv 104,0,188
Rt 34,16,25088
v_accvgpr_write_b32 a206, v60
Mv 108,4,188
Rt 36,17,24576
v_mul_f32_e32 v62, s54, v62
Mv 100,4,184
Rt 38,17,25088
v_mul_f32_e32 v63, s54, v63
Cv 62,62,63
Mv 112,8,184
Rt 40,18,24576
v_accvgpr_write_b32 a207, v62
Mv 120,8,188
Rt 42,18,25088
buffer_load_dwordx4 a[152:155], v24, s[12:15], s82 offen offset:384
Mv 124,12,188
Rt 44,19,24576
Mv 116,12,184
Rt 46,19,25088
s_waitcnt lgkmcnt(8)
Mv 96,16,48
Rb 0,8,32768
Mv 104,16,52
Rb 4,8,33792
Mv 108,20,52
Rb 8,8,34816
Mv 100,20,48
Rb 12,8,35840
Mv 112,24,48
Rb 16,8,40960
Rb 20,8,41984
Mv 120,24,52
Rb 24,8,43008
Mv 124,28,52
Mv 116,28,48
Rb 28,8,44032
s_waitcnt lgkmcnt(8)
Mv 96,32,56
Mv 104,32,60
Rb 32,8,36864
Mv 108,36,60
Mv 100,36,56
Rb 36,8,37888
Mv 112,40,56
Mv 120,40,60
Rb 40,8,38912
)" R"(
Mv 124,44,60
Mv 116,44,56
Rb 44,8,39936
s_cmp_eq_u32 s35, 7
s_cbranch_scc1 .LzA7
.LzA7R:
s_waitcnt lgkmcnt(8)
Mz 64,0,64
Rb 48,8,45056
v_mul_f32_e32 v96, s54, v96
Mz 68,0,68
v_mul_f32_e32 v97, s54, v97
Cv 96,96,97
Mv 68,4,76
Rb 52,8,46080
s_add_u32 m0, s36, 0
Mv 64,4,72
buffer_load_dwordx4 v2, s[20:23], s55 offen offset:0 sc1 lds
Mv 64,8,80
Rb 56,8,47104
v_accvgpr_write_b32 a208, v96
Mv 68,8,84
v_mul_f32_e32 v98, s54, v98
v_mul_f32_e32 v99, s54, v99
Mv 68,12,92
Rb 60,8,48128
s_waitcnt lgkmcnt(8)
Mv 64,12,88
Cv 98,98,99
v_accvgpr_write_b32 a209, v98
v_mul_f32_e32 v100, s54, v100
Mv 64,16,96
Rb 0,8,49152
v_mul_f32_e32 v101, s54, v101
Mv 68,16,100
Cv 100,100,101
v_accvgpr_write_b32 a210, v100
Mv 68,20,108
Rb 4,8,50176
v_mul_f32_e32 v102, s54, v102
Mv 64,20,104
buffer_load_dwordx4 v2, s[20:23], s55 offen offset:1024 sc1 lds
Mv 64,24,112
Rb 8,8,51200
v_mul_f32_e32 v103, s54, v103
Mv 68,24,116
Cv 102,102,103
v_accvgpr_write_b32 a211, v102
Mv 68,28,124
Rb 12,8,52224
s_waitcnt lgkmcnt(8)
Mv 64,28,120
v_mul_f32_e32 v104, s54, v104
v_mul_f32_e32 v105, s54, v105
Cv 104,104,105
Mz 72,32,64
Rb 16,8,57344
v_accvgpr_write_b32 a212, v104
Mz 76,32,68
v_mul_f32_e32 v106, s54, v106
v_mul_f32_e32 v107, s54, v107
Mv 76,36,76
Rb 20,8,58368
Cv 106,106,107
)" R"(
Mv 72,36,72
buffer_load_dwordx4 v2, s[20:23], s55 offen offset:2048 sc1 lds
Mv 72,40,80
Rb 24,8,59392
v_accvgpr_write_b32 a213, v106
Mv 76,40,84
v_mul_f32_e32 v108, s54, v108
v_mul_f32_e32 v109, s54, v109
Mv 76,44,92
Rb 28,8,60416
s_waitcnt lgkmcnt(8)
Mv 72,44,88
Cv 108,108,109
v_accvgpr_write_b32 a214, v108
v_mul_f32_e32 v110, s54, v110
Mv 72,48,96
Rb 32,8,53248
v_mul_f32_e32 v111, s54, v111
Mv 76,48,100
Cv 110,110,111
v_accvgpr_write_b32 a215, v110
Mv 76,52,108
Rb 36,8,54272
v_mul_f32_e32 v112, s54, v112
Mv 72,52,104
buffer_load_dwordx4 v2, s[20:23], s55 offen offset:3072 sc1 lds
Mv 72,56,112
Rb 40,8,55296
v_mul_f32_e32 v113, s54, v113
Mv 76,56,116
Cv 112,112,113
v_accvgpr_write_b32 a216, v112
Mv 76,60,124
Rb 44,8,56320
s_waitcnt lgkmcnt(8)
Mv 72,60,120
v_mul_f32_e32 v114, s54, v114
v_mul_f32_e32 v115, s54, v115
Cv 114,114,115
Mz 80,0,64
Rb 48,8,61440
v_accvgpr_write_b32 a217, v114
Mz 84,0,68
v_mul_f32_e32 v116, s54, v116
v_mul_f32_e32 v117, s54, v117
Mv 84,4,76
Rb 52,8,62464
s_add_u32 m0, s36, 4096
Mv 80,4,72
buffer_load_dwordx4 v3, s[20:23], s55 offen offset:0 sc1 lds
Mv 80,8,80
Rb 56,8,63488
Cv 116,116,117
Mv 84,8,84
v_accvgpr_write_b32 a218, v116
v_mul_f32_e32 v118, s54, v118
Mv 84,12,92
Rb 60,8,64512
s_waitcnt lgkmcnt(8)
Mv 80,12,88
v_mul_f32_e32 v119, s54, v119
Cv 118,118,119
v_accvgpr_write_b32 a219, v118
Mv 80,16,96
)" R"(
v_mul_f32_e32 v120, s54, v120
v_mul_f32_e32 v121, s54, v121
Mv 84,16,100
Cv 120,120,121
v_accvgpr_write_b32 a220, v120
Mv 84,20,108
v_mul_f32_e32 v122, s54, v122
v_mul_f32_e32 v123, s54, v123
Mv 80,20,104
buffer_load_dwordx4 v3, s[20:23], s55 offen offset:1024 sc1 lds
Mv 80,24,112
s_add_u32 s50, s50, 1024
s_min_u32 s50, s50, s60
Mv 84,24,116
Cv 122,122,123
v_accvgpr_write_b32 a221, v122
Mv 84,28,124
v_mul_f32_e32 v124, s54, v124
v_mul_f32_e32 v125, s54, v125
Mv 80,28,120
Cv 124,124,125
s_waitcnt lgkmcnt(0)
s_barrier
Mz 88,32,64
Rb 0,1,32768
v_accvgpr_write_b32 a222, v124
Mz 92,32,68
Rb 4,1,33792
v_mul_f32_e32 v126, s54, v126
Mv 92,36,76
Rb 8,1,34816
v_mul_f32_e32 v127, s54, v127
Mv 88,36,72
Rb 12,1,35840
buffer_load_dwordx4 v3, s[20:23], s55 offen offset:2048 sc1 lds
Mv 88,40,80
Rb 16,1,40960
Cv 126,126,127
Mv 92,40,84
Rb 20,1,41984
v_accvgpr_write_b32 a223, v126
Mv 92,44,92
Rb 24,1,43008
v_mov_b32 v16, 0
Mv 88,44,88
Rb 28,1,44032
v_mov_b32 v17, 0
v_mov_b32 v18, 0
Mv 88,48,96
Rb 32,1,36864
v_mov_b32 v19, 0
Mv 92,48,100
Rb 36,1,37888
v_mov_b32 v20, 0
Mv 92,52,108
Rb 40,1,38912
v_mov_b32 v21, 0
Mv 88,52,104
Rb 44,1,39936
buffer_load_dwordx4 v3, s[20:23], s55 offen offset:3072 sc1 lds
Mv 88,56,112
v_mov_b32 v22, 0
v_mov_b32 v23, 0
Mv 92,56,116
Mv 92,60,124
)" R"(
s_waitcnt lgkmcnt(8)
Mv 88,60,120
Mz 96,0,64
Rb 48,1,45056
buffer_load_dword v4, v5, s[20:23], s50 offen offset:0 sc0 sc1
Mz 100,0,68
s_cmp_le_i32 s57, s58
s_cbranch_scc1 .Lzmk0
.Lzmr0:
Mv 100,4,76
Rb 52,1,46080
s_add_u32 m0, s36, 65536
s_sub_u32 s57, s57, 1
Mv 96,4,72
buffer_load_dwordx4 v2, s[20:23], s56 offen offset:0 sc1 lds
s_add_u32 s55, s55, 0x10000
Mv 96,8,80
Rb 56,1,47104
s_cmp_eq_u32 s57, 2
s_cselect_b32 s55, s59, s55
Mv 100,8,84
v_exp_f32_e32 v32, v64
Mv 100,12,92
Rb 60,1,48128
s_waitcnt lgkmcnt(8)
Mv 96,12,88
v_exp_f32_e32 v24, v65
Mv 96,16,96
Rb 0,1,49152
v_exp_f32_e32 v33, v66
Mv 100,16,100
v_exp_f32_e32 v25, v67
v_exp_f32_e32 v34, v72
Mv 100,20,108
Rb 4,1,50176
v_exp_f32_e32 v26, v73
Mv 96,20,104
buffer_load_dwordx4 v2, s[20:23], s56 offen offset:1024 sc1 lds
v_add_f32_e32 v14, v32, v24
Mv 96,24,112
Rb 8,1,51200
v_exp_f32_e32 v35, v74
Mv 100,24,116
v_exp_f32_e32 v27, v75
Cv 32,32,24
Mv 100,28,124
Rb 12,1,52224
v_add_f32_e32 v14, v14, v33
s_waitcnt lgkmcnt(8)
Mv 96,28,120
v_exp_f32_e32 v36, v68
v_add_f32_e32 v14, v14, v25
Mz 104,32,64
Rb 16,1,57344
v_exp_f32_e32 v28, v69
Mz 108,32,68
v_exp_f32_e32 v37, v70
v_exp_f32_e32 v29, v71
Mv 108,36,76
Rb 20,1,58368
v_exp_f32_e32 v38, v76
Mv 104,36,72
buffer_load_dwordx4 v2, s[20:23], s56 offen offset:2048 sc1 lds
Cv 33,33,25
)" R"(
Mv 104,40,80
Rb 24,1,59392
v_exp_f32_e32 v24, v77
Mv 108,40,84
v_exp_f32_e32 v39, v78
v_add_f32_e32 v14, v14, v34
Mv 108,44,92
Rb 28,1,60416
v_add_f32_e32 v14, v14, v26
s_waitcnt lgkmcnt(8)
Mv 104,44,88
v_exp_f32_e32 v25, v79
Cv 34,34,26
Mv 104,48,96
Rb 32,1,53248
v_exp_f32_e32 v40, v80
Mv 108,48,100
v_exp_f32_e32 v26, v81
v_add_f32_e32 v14, v14, v35
v_add_f32_e32 v14, v14, v27
Mv 108,52,108
Rb 36,1,54272
Cv 35,35,27
v_add_f32_e32 v15, v36, v28
Mv 104,52,104
buffer_load_dwordx4 v2, s[20:23], s56 offen offset:3072 sc1 lds
Cv 36,36,28
Mv 104,56,112
Rb 40,1,55296
v_exp_f32_e32 v41, v82
Mv 108,56,116
v_exp_f32_e32 v27, v83
v_add_f32_e32 v15, v15, v37
Mv 108,60,124
Rb 44,1,56320
v_add_f32_e32 v15, v15, v29
s_waitcnt lgkmcnt(8)
Mv 104,60,120
v_exp_f32_e32 v42, v88
Cv 37,37,29
Mz 112,0,64
Rb 48,1,61440
v_exp_f32_e32 v28, v89
Mz 116,0,68
v_exp_f32_e32 v43, v90
v_add_f32_e32 v15, v15, v38
v_add_f32_e32 v15, v15, v24
Mv 116,4,76
Rb 52,1,62464
s_add_u32 m0, s36, 69632
Cv 38,38,24
Mv 112,4,72
buffer_load_dwordx4 v3, s[20:23], s56 offen offset:0 sc1 lds
v_add_f32_e32 v15, v15, v39
Mv 112,8,80
Rb 56,1,63488
v_exp_f32_e32 v29, v91
Mv 116,8,84
v_exp_f32_e32 v44, v84
v_add_f32_e32 v15, v15, v25
Mv 116,12,92
Rb 60,1,64512
Cv 39,39,25
s_waitcnt lgkmcnt(8)
)" R"(
Mv 112,12,88
v_exp_f32_e32 v24, v85
v_add_f32_e32 v14, v14, v40
Mv 112,16,96
v_exp_f32_e32 v45, v86
v_add_f32_e32 v14, v14, v26
Mv 116,16,100
v_exp_f32_e32 v25, v87
Cv 40,40,26
v_add_f32_e32 v14, v14, v41
Mv 116,20,108
v_exp_f32_e32 v46, v92
v_add_f32_e32 v14, v14, v27
Mv 112,20,104
buffer_load_dwordx4 v3, s[20:23], s56 offen offset:1024 sc1 lds
Cv 41,41,27
Mv 112,24,112
v_exp_f32_e32 v26, v93
v_add_f32_e32 v14, v14, v42
Mv 116,24,116
v_exp_f32_e32 v47, v94
v_add_f32_e32 v14, v14, v28
Mv 116,28,124
v_exp_f32_e32 v27, v95
Cv 42,42,28
Mv 112,28,120
v_add_f32_e32 v14, v14, v43
s_waitcnt vmcnt(23) lgkmcnt(0)
s_barrier
Mz 120,32,64
Rb 0,10,0
v_add_f32_e32 v14, v14, v29
Cv 43,43,29
Mz 124,32,68
Rb 4,10,1024
v_add_f32_e32 v15, v15, v44
v_add_f32_e32 v15, v15, v24
Cv 44,44,24
Mv 124,36,76
Rb 8,10,2048
v_add_f32_e32 v15, v15, v45
v_add_f32_e32 v15, v15, v25
Mv 120,36,72
Rb 12,10,3072
buffer_load_dwordx4 v3, s[20:23], s56 offen offset:2048 sc1 lds
Mv 120,40,80
Rb 16,10,4096
Cv 45,45,25
v_add_f32_e32 v15, v15, v46
Mv 124,40,84
Rb 20,10,5120
v_add_f32_e32 v15, v15, v26
Cv 46,46,26
Mv 124,44,92
Rb 24,10,6144
v_add_f32_e32 v15, v15, v47
v_add_f32_e32 v15, v15, v27
Mv 120,44,88
Rb 28,10,7168
Cv 47,47,27
v_max_f32_e32 v11, v14, v15
Mv 120,48,96
Rb 32,10,8192
v_mov_b32 v24, v14
v_mov_b32 v25, v15
)" R"(
Mv 124,48,100
Rb 36,10,9216
v_permlane32_swap_b32_e32 v24, v25
v_add_f32_e32 v24, v24, v25
v_mov_b32 v25, v24
Mv 124,52,108
Rb 40,10,10240
v_permlane16_swap_b32_e32 v24, v25
v_add_f32_e32 v24, v24, v25
Mv 120,52,104
Rb 44,10,11264
buffer_load_dwordx4 v3, s[20:23], s56 offen offset:3072 sc1 lds
Mv 120,56,112
v_rcp_f32_e32 v24, v24
Mv 124,56,116
s_add_u32 s56, s56, 0x10000
v_max_f32_e32 v11, v11, v24
v_cmp_nge_f32_e32 vcc, s62, v11
Mv 124,60,124
s_waitcnt lgkmcnt(8)
s_cbranch_vccnz .Lzfx0
.Lzfr0:
Mv 120,60,120
v_mov_b32 v12, v14
v_mov_b32 v13, v15
v_mfma_f32_16x16x32_bf16 v[128:131], a[0:3], v[32:35], 0
Rb 48,10,12288
v_mfma_f32_16x16x32_bf16 v[136:139], a[0:3], v[36:39], 0
s_cmp_le_i32 s57, s58
s_cbranch_scc1 .Lzmk1
.Lzmr1:
v_mfma_f32_16x16x32_bf16 v[140:143], a[4:7], v[36:39], 0
Rb 52,10,13312
s_add_u32 m0, s36, 32768
v_mfma_f32_16x16x32_bf16 v[132:135], a[4:7], v[32:35], 0
buffer_load_dwordx4 v2, s[20:23], s55 offen offset:0 sc1 lds
v_mfma_f32_16x16x32_bf16 v[144:147], a[8:11], v[32:35], 0
Rb 56,10,14336
s_and_b32 s92, s2, 7
v_mfma_f32_16x16x32_bf16 v[152:155], a[8:11], v[36:39], 0
s_mul_i32 s92, s92, 14
s_lshr_b32 s82, s2, 3
v_mfma_f32_16x16x32_bf16 v[156:159], a[12:15], v[36:39], 0
Rb 60,10,15360
s_waitcnt lgkmcnt(8)
v_mfma_f32_16x16x32_bf16 v[148:151], a[12:15], v[32:35], 0
buffer_load_dwordx4 v2, s[20:23], s55 offen offset:1024 sc1 lds
v_mfma_f32_16x16x32_bf16 v[160:163], a[16:19], v[32:35], 0
Rb 0,10,16384
s_add_u32 s82, s82, s92
v_mfma_f32_16x16x32_bf16 v[168:171], a[16:19], v[36:39], 0
v_exp_f32_e32 v48, v96
v_mfma_f32_16x16x32_bf16 v[172:175], a[20:23], v[36:39], 0
Rb 4,10,17408
v_mfma_f32_16x16x32_bf16 v[164:167], a[20:23], v[32:35], 0
buffer_load_dwordx4 v2, s[20:23], s55 offen offset:2048 sc1 lds
v_mfma_f32_16x16x32_bf16 v[176:179], a[24:27], v[32:35], 0
Rb 8,10,18432
v_exp_f32_e32 v28, v97
v_mfma_f32_16x16x32_bf16 v[184:187], a[24:27], v[36:39], 0
v_exp_f32_e32 v49, v98
v_mfma_f32_16x16x32_bf16 v[188:191], a[28:31], v[36:39], 0
Rb 12,10,19456
s_waitcnt lgkmcnt(8)
)" R"(
v_mfma_f32_16x16x32_bf16 v[180:183], a[28:31], v[32:35], 0
buffer_load_dwordx4 v2, s[20:23], s55 offen offset:3072 sc1 lds
v_add_f32_e32 v14, v48, v28
v_mfma_f32_16x16x32_bf16 v[192:195], a[32:35], v[32:35], 0
Rb 16,10,20480
Cv 48,48,28
v_mfma_f32_16x16x32_bf16 v[200:203], a[32:35], v[36:39], 0
v_exp_f32_e32 v29, v99
v_add_f32_e32 v14, v14, v49
v_mfma_f32_16x16x32_bf16 v[204:207], a[36:39], v[36:39], 0
Rb 20,10,21504
s_add_u32 m0, s36, 36864
v_mfma_f32_16x16x32_bf16 v[196:199], a[36:39], v[32:35], 0
buffer_load_dwordx4 v3, s[20:23], s55 offen offset:0 sc1 lds
v_mfma_f32_16x16x32_bf16 v[208:211], a[40:43], v[32:35], 0
Rb 24,10,22528
v_exp_f32_e32 v50, v104
v_mfma_f32_16x16x32_bf16 v[216:219], a[40:43], v[36:39], 0
s_cmp_gt_u32 s33, 1
s_cselect_b32 s24, s78, s76
s_cselect_b32 s25, s79, s77
v_mfma_f32_16x16x32_bf16 v[220:223], a[44:47], v[36:39], 0
Rb 28,10,23552
s_waitcnt lgkmcnt(8)
v_mfma_f32_16x16x32_bf16 v[212:215], a[44:47], v[32:35], 0
buffer_load_dwordx4 v3, s[20:23], s55 offen offset:1024 sc1 lds
v_add_f32_e32 v14, v14, v29
v_mfma_f32_16x16x32_bf16 v[224:227], a[48:51], v[32:35], 0
Rb 32,10,24576
s_mov_b32 s27, 0x20000
v_mfma_f32_16x16x32_bf16 v[232:235], a[48:51], v[36:39], 0
s_cmp_lt_u32 s35, 7
s_cselect_b32 s26, 0x188000, 0
Cv 49,49,29
v_mfma_f32_16x16x32_bf16 v[236:239], a[52:55], v[36:39], 0
Rb 36,10,25600
s_mul_i32 s83, s83, 802816
v_mfma_f32_16x16x32_bf16 v[228:231], a[52:55], v[32:35], 0
buffer_load_dwordx4 v3, s[20:23], s55 offen offset:2048 sc1 lds
v_mfma_f32_16x16x32_bf16 v[240:243], a[56:59], v[32:35], 0
Rb 40,10,26624
s_lshl_b32 s92, s82, 5
v_add_f32_e32 v14, v14, v50
v_mfma_f32_16x16x32_bf16 v[248:251], a[56:59], v[36:39], 0
s_add_u32 s83, s83, s92
v_and_b32 v9, 62, v0
v_mul_u32_u24 v9, 0x6f0, v9
v_mfma_f32_16x16x32_bf16 v[252:255], a[60:63], v[36:39], 0
Rb 44,10,27648
s_waitcnt lgkmcnt(8)
v_mfma_f32_16x16x32_bf16 v[244:247], a[60:63], v[32:35], 0
buffer_load_dwordx4 v3, s[20:23], s55 offen offset:3072 sc1 lds
Mp 128,0,40
Rb 48,10,28672
v_add_u32 v9, v9, v1
Mp 136,0,44
v_exp_f32_e32 v24, v105
Mp 140,4,44
Rb 52,10,29696
v_add_f32_e32 v14, v14, v24
Mp 132,4,40
v_exp_f32_e32 v51, v106
Mp 144,8,40
Rb 56,10,30720
)" R"(
v_exp_f32_e32 v25, v107
Mp 152,8,44
v_exp_f32_e32 v52, v100
Mp 156,12,44
Rb 60,10,31744
s_waitcnt lgkmcnt(8)
Mp 148,12,40
v_exp_f32_e32 v26, v101
Cv 50,50,24
Mp 160,16,40
v_exp_f32_e32 v53, v102
Mp 168,16,44
v_exp_f32_e32 v27, v103
v_add_f32_e32 v14, v14, v51
Mp 172,20,44
v_exp_f32_e32 v54, v108
Mp 164,20,40
v_exp_f32_e32 v28, v109
Mp 176,24,40
v_exp_f32_e32 v55, v110
v_add_f32_e32 v14, v14, v25
Mp 184,24,44
v_exp_f32_e32 v29, v111
Mp 188,28,44
Cv 51,51,25
v_add_f32_e32 v15, v52, v26
Mp 180,28,40
Cv 52,52,26
s_waitcnt vmcnt(16) lgkmcnt(0)
s_barrier
Mp 192,32,40
Rb 0,1,0
s_add_u32 m0, s36, 131072
s_nop 0
buffer_load_dwordx4 v9, s[24:27], s83 offen offset:0 lds
Mp 200,32,44
Rb 4,1,1024
v_add_f32_e32 v15, v15, v53
v_add_f32_e32 v15, v15, v27
Mp 204,36,44
Rb 8,1,2048
s_add_u32 s83, s83, 0x1bc00
buffer_load_dwordx4 v9, s[24:27], s83 offen offset:1024 lds
Mp 196,36,40
Rb 12,1,3072
Cv 53,53,27
Mp 208,40,40
Rb 16,1,8192
s_add_u32 s83, s83, 0x1bc00
buffer_load_dwordx4 v9, s[24:27], s83 offen offset:2048 lds
Mp 216,40,44
Rb 20,1,9216
v_add_f32_e32 v15, v15, v54
Mp 220,44,44
Rb 24,1,10240
s_add_u32 s83, s83, 0x1bc00
buffer_load_dwordx4 v9, s[24:27], s83 offen offset:3072 lds
Mp 212,44,40
Rb 28,1,11264
v_add_f32_e32 v15, v15, v28
Cv 54,54,28
Mp 224,48,40
Rb 32,1,4096
s_add_u32 m0, s36, 135168
s_add_u32 s83, s83, 0x1cc00
)" R"(
buffer_load_dwordx4 v9, s[24:27], s83 offen offset:0 lds
Mp 232,48,44
Rb 36,1,5120
v_add_f32_e32 v15, v15, v55
v_add_f32_e32 v15, v15, v29
Mp 236,52,44
Rb 40,1,6144
s_add_u32 s83, s83, 0x1bc00
buffer_load_dwordx4 v9, s[24:27], s83 offen offset:1024 lds
Mp 228,52,40
Rb 44,1,7168
Cv 55,55,29
Mp 240,56,40
s_add_u32 s83, s83, 0x1bc00
buffer_load_dwordx4 v9, s[24:27], s83 offen offset:2048 lds
v_cmp_eq_u32_e32 vcc, s3, v4
s_andn2_b64 s[28:29], -1, vcc
s_cbranch_scc1 .Lzsp8
.Lzok8:
Mp 248,56,44
s_sub_u32 s57, s57, 1
Mp 252,60,44
s_waitcnt lgkmcnt(8)
Mp 244,60,40
s_cmp_gt_i32 s57, s93
s_cbranch_scc1 .LzL
.LzW:
s_lshl1_add_u32 s57, s35, 2
s_mov_b32 s59, s67
s_add_u32 s56, s89, 0x2000000
s_mov_b32 s94, 2
Mz 64,0,224
Rb 48,1,12288
v_lshrrev_b32 v9, 4, v0
Mz 68,0,228
v_lshlrev_b32 v9, 4, v9
v_add_lshl_u32 v9, v9, v0, 3
v_accvgpr_mov_b32 a64, a224
Mv 68,4,236
Rb 52,1,13312
s_add_u32 m0, s36, 98304
v_accvgpr_mov_b32 a65, a225
Mv 64,4,232
buffer_load_dwordx4 v2, s[20:23], s56 offen offset:0 sc1 lds
v_accvgpr_mov_b32 a66, a226
Mv 64,8,240
Rb 56,1,14336
s_add_u32 s92, s36, 131072
v_accvgpr_mov_b32 a67, a227
Mv 68,8,244
v_add_u32 v9, s92, v9
v_accvgpr_mov_b32 a68, a228
s_waitcnt vmcnt(1)
Mv 68,12,252
Rb 60,1,15360
v_accvgpr_mov_b32 a69, a229
s_waitcnt lgkmcnt(8)
Mv 64,12,248
buffer_load_dwordx4 v2, s[20:23], s56 offen offset:1024 sc1 lds
Rt 156,9,0
Mv 64,16,192
Rb 0,1,16384
Rt 158,9,128
v_accvgpr_mov_b32 a70, a230
)" R"(
Mv 68,16,196
Rt 160,9,1024
v_exp_f32_e32 v56, v112
Mv 68,20,204
Rb 4,1,17408
Rt 162,9,1152
v_add_f32_e32 v14, v14, v56
Mv 64,20,200
buffer_load_dwordx4 v2, s[20:23], s56 offen offset:2048 sc1 lds
Rt 164,9,2048
Mv 64,24,208
Rb 8,1,18432
Rt 166,9,2176
v_accvgpr_mov_b32 a71, a231
Mv 68,24,212
Rt 168,9,3072
v_exp_f32_e32 v26, v113
Mv 68,28,220
Rb 12,1,19456
Rt 170,9,3200
Mv 64,28,216
buffer_load_dwordx4 v2, s[20:23], s56 offen offset:3072 sc1 lds
Rt 172,9,4096
v_add_f32_e32 v14, v14, v26
Mz 72,32,224
Rb 16,1,24576
Rt 174,9,4224
Cv 56,56,26
Mz 76,32,228
Rt 176,9,5120
v_exp_f32_e32 v57, v114
Mv 76,36,236
Rb 20,1,25600
s_add_u32 m0, s36, 102400
Rt 178,9,5248
Mv 72,36,232
buffer_load_dwordx4 v3, s[20:23], s56 offen offset:0 sc1 lds
Rt 180,9,6144
Mv 72,40,240
Rb 24,1,26624
Rt 182,9,6272
v_add_f32_e32 v14, v14, v57
Mv 76,40,244
v_exp_f32_e32 v27, v115
v_accvgpr_mov_b32 a72, a232
Mv 76,44,252
Rb 28,1,27648
v_add_f32_e32 v14, v14, v27
Mv 72,44,248
buffer_load_dwordx4 v3, s[20:23], s56 offen offset:1024 sc1 lds
Cv 57,57,27
Mv 72,48,192
Rb 32,1,20480
v_and_b32 v9, 14, v0
v_accvgpr_mov_b32 a73, a233
Mv 76,48,196
v_min_u32 v9, 1, v9
v_lshlrev_b32 v9, 8, v9
v_accvgpr_mov_b32 a74, a234
Mv 76,52,204
Rb 36,1,21504
v_and_or_b32 v9, v0, 48, v9
v_accvgpr_mov_b32 a75, a235
Mv 72,52,200
)" R"(
buffer_load_dwordx4 v3, s[20:23], s56 offen offset:2048 sc1 lds
v_accvgpr_mov_b32 a76, a236
Mv 72,56,208
Rb 40,1,22528
v_lshlrev_b32 v9, 1, v9
v_accvgpr_mov_b32 a77, a237
Mv 76,56,212
v_and_or_b32 v9, v1, 16, v9
s_lshl_b32 s92, s33, 7
v_accvgpr_mov_b32 a78, a238
Mv 76,60,220
Rb 44,1,23552
v_accvgpr_mov_b32 a79, a239
s_waitcnt lgkmcnt(5)
Mv 72,60,216
v_mfma_f32_16x16x32_bf16 a[184:187], a[156:159], a[128:131], 0
buffer_load_dwordx4 v3, s[20:23], s56 offen offset:3072 sc1 lds
v_exp_f32_e32 v58, v120
Mz 80,0,224
Ma 184,160,132
Rb 48,1,28672
s_add_u32 s92, s92, 162816
v_add_f32_e32 v14, v14, v58
Mz 84,0,228
Ma 184,164,136
s_add_u32 s56, s56, 0x10000
v_add_u32 v9, s92, v9
v_accvgpr_mov_b32 a80, a240
Mv 84,4,236
Ma 184,168,140
Rb 52,1,29696
v_exp_f32_e32 v28, v121
Mv 80,4,232
Ma 184,172,144
v_exp_f32_e32 v59, v122
v_add_f32_e32 v14, v14, v28
Mv 80,8,240
Ma 184,176,148
Rb 56,1,30720
s_mov_b32 s24, s70
s_mov_b32 s25, s71
Mv 84,8,244
Ma 184,180,152
s_mul_i32 s83, s82, 14336
s_lshl_b32 s92, s33, 12
Cv 58,58,28
Mv 84,12,252
Rb 60,1,31744
v_add_f32_e32 v14, v14, v59
s_waitcnt lgkmcnt(8)
Mv 80,12,248
s_add_u32 s83, s83, s92
s_add_u32 m0, s92, 131072
v_accvgpr_mov_b32 a81, a241
Mv 80,16,192
v_exp_f32_e32 v29, v123
v_accvgpr_mov_b32 a82, a242
Mv 84,16,196
v_exp_f32_e32 v60, v116
v_add_f32_e32 v14, v14, v29
Mv 84,20,204
ds_write_b128 v9, a[184:187]
v_and_b32 v9, 0x70, v9
Cv 59,59,29
)" R"(
Mv 80,20,200
v_add_u32 v9, 0x27c00, v9
v_exp_f32_e32 v24, v117
Mv 80,24,208
v_exp_f32_e32 v61, v118
v_add_f32_e32 v15, v15, v60
Mv 84,24,212
v_exp_f32_e32 v25, v119
v_add_f32_e32 v15, v15, v24
Mv 84,28,220
v_exp_f32_e32 v62, v124
Cv 60,60,24
Mv 80,28,216
v_exp_f32_e32 v26, v125
s_waitcnt lgkmcnt(0)
s_barrier
Mz 88,32,224
Rb 0,8,0
v_exp_f32_e32 v63, v126
Mz 92,32,228
Rb 4,8,16384
v_exp_f32_e32 v27, v127
Mv 92,36,236
Rb 8,8,1024
v_add_f32_e32 v15, v15, v61
v_add_f32_e32 v15, v15, v25
Mv 88,36,232
Rb 12,8,17408
Cv 61,61,25
v_add_f32_e32 v15, v15, v62
Mv 88,40,240
Rb 16,8,2048
buffer_load_dwordx4 v1, s[24:27], s83 offen offset:0 lds
Mv 92,40,244
Rb 20,8,18432
v_add_f32_e32 v15, v15, v26
Cv 62,62,26
Mv 92,44,252
Rb 24,8,3072
v_add_f32_e32 v15, v15, v63
v_add_f32_e32 v15, v15, v27
Mv 88,44,248
Rb 28,8,19456
Cv 63,63,27
v_max_f32_e32 v11, v14, v15
Mv 88,48,192
Rb 32,8,4096
v_cmp_nge_f32_e32 vcc, s62, v11
s_add_u32 s55, s55, 0x10000
Mv 92,48,196
Rb 36,8,20480
buffer_load_dwordx4 v1, s[24:27], s83 offen offset:1024 lds
Mv 92,52,204
Rb 40,8,5120
s_cmp_eq_u32 s57, 2
s_cselect_b32 s55, s59, s55
Mv 88,52,200
Rb 44,8,21504
v_accvgpr_mov_b32 a83, a243
v_accvgpr_mov_b32 a84, a244
Mv 88,56,208
buffer_load_dwordx4 v1, s[24:27], s83 offen offset:2048 lds
v_accvgpr_mov_b32 a85, a245
Mv 92,56,212
buffer_load_dwordx4 v1, s[24:27], s83 offen offset:3072 lds
)" R"(
v_accvgpr_mov_b32 a86, a246
Mv 92,60,220
v_accvgpr_mov_b32 a87, a247
s_waitcnt lgkmcnt(8)
s_cbranch_vccnz .Lzfx3
.Lzfr3:
Mv 88,60,216
v_add_f32_e32 v12, v12, v14
v_add_f32_e32 v13, v13, v15
v_mov_b32 v14, v16
Mp 128,0,48
Rb 48,8,6144
v_mov_b32 v15, v20
Mp 136,0,52
buffer_store_dwordx4 v[12:15], v1, s[20:23], s66 offen sc0 sc1
s_cmp_le_i32 s57, s58
s_cbranch_scc1 .Lzmk4
.Lzmr4:
Mp 136,4,60
Rb 52,8,22528
s_add_u32 m0, s36, 0
v_accvgpr_mov_b32 a88, a248
Mp 128,4,56
buffer_load_dwordx4 v2, s[20:23], s55 offen offset:0 sc1 lds
v_accvgpr_mov_b32 a89, a249
Mp 132,8,48
Rb 56,8,7168
v_exp_f32_e32 v32, v64
Mp 140,8,52
v_exp_f32_e32 v28, v65
v_accvgpr_mov_b32 a90, a250
Mp 140,12,60
Rb 60,8,23552
v_exp_f32_e32 v33, v66
s_waitcnt lgkmcnt(8)
Mp 132,12,56
v_exp_f32_e32 v29, v67
v_add_f32_e32 v14, v32, v28
Mp 144,16,48
Rb 0,8,8192
v_exp_f32_e32 v34, v72
Mp 152,16,52
v_exp_f32_e32 v24, v73
v_exp_f32_e32 v35, v74
Mp 152,20,60
Rb 4,8,24576
v_exp_f32_e32 v25, v75
Mp 144,20,56
buffer_load_dwordx4 v2, s[20:23], s55 offen offset:1024 sc1 lds
Cv 32,32,28
Mp 148,24,48
Rb 8,8,9216
Cv 128,128,129
Cv 129,130,131
v_add_f32_e32 v14, v14, v33
Mp 156,24,52
Cv 130,132,133
Cv 131,134,135
v_add_f32_e32 v14, v14, v29
Mp 156,28,60
Rb 12,8,25600
Cv 136,136,137
s_waitcnt lgkmcnt(8)
Mp 148,28,56
)" R"(
Cv 137,138,139
Cv 138,140,141
Cv 139,142,143
Cv 33,33,29
Mp 160,32,48
Rb 16,8,10240
v_exp_f32_e32 v36, v68
Mp 168,32,52
v_exp_f32_e32 v26, v69
v_add_f32_e32 v14, v14, v34
Mp 168,36,60
Rb 20,8,26624
v_exp_f32_e32 v37, v70
v_add_f32_e32 v14, v14, v24
Mp 160,36,56
buffer_load_dwordx4 v2, s[20:23], s55 offen offset:2048 sc1 lds
Cv 144,144,145
Mp 164,40,48
Rb 24,8,11264
Cv 145,146,147
Cv 34,34,24
Mp 172,40,52
buffer_store_dwordx4 v[128:131], v2, s[20:23], s64 offen offset:0 sc0 sc1
Cv 146,148,149
Mp 172,44,60
Rb 28,8,27648
v_exp_f32_e32 v27, v71
s_waitcnt lgkmcnt(8)
Mp 164,44,56
Cv 147,150,151
Cv 152,152,153
Cv 153,154,155
Mp 176,48,48
Rb 32,8,12288
Cv 154,156,157
Cv 155,158,159
Mp 184,48,52
v_exp_f32_e32 v38, v76
v_exp_f32_e32 v28, v77
Mp 184,52,60
Rb 36,8,28672
v_exp_f32_e32 v39, v78
Mp 176,52,56
buffer_load_dwordx4 v2, s[20:23], s55 offen offset:3072 sc1 lds
v_add_f32_e32 v14, v14, v35
Mp 180,56,48
Rb 40,8,13312
Cv 160,160,161
Cv 161,162,163
v_add_f32_e32 v14, v14, v25
Mp 188,56,52
buffer_store_dwordx4 v[136:139], v2, s[20:23], s64 offen offset:1024 sc0 sc1
Cv 35,35,25
Mp 188,60,60
Rb 44,8,29696
Cv 162,164,165
s_waitcnt lgkmcnt(8)
Mp 180,60,56
Cv 163,166,167
Cv 168,168,169
Cv 169,170,171
v_add_f32_e32 v15, v36, v26
Mp 192,0,48
Rb 48,8,14336
)" R"(
Cv 170,172,173
Cv 36,36,26
Mp 200,0,52
buffer_store_dwordx4 v[144:147], v2, s[20:23], s64 offen offset:2048 sc0 sc1
Cv 171,174,175
Mp 200,4,60
Rb 52,8,30720
s_add_u32 m0, s36, 4096
v_exp_f32_e32 v29, v79
Mp 192,4,56
buffer_load_dwordx4 v3, s[20:23], s55 offen offset:0 sc1 lds
Cv 176,176,177
Mp 196,8,48
Rb 56,8,15360
Cv 177,178,179
v_add_f32_e32 v15, v15, v37
Mp 204,8,52
Cv 178,180,181
Cv 179,182,183
Cv 184,184,185
Mp 204,12,60
Rb 60,8,31744
v_add_f32_e32 v15, v15, v27
Cv 37,37,27
s_waitcnt lgkmcnt(8)
Mp 196,12,56
buffer_store_dwordx4 v[152:155], v2, s[20:23], s64 offen offset:3072 sc0 sc1
Cv 185,186,187
Mp 208,16,48
ds_read_b128 v[132:135], v9 offset:0
Cv 186,188,189
Cv 187,190,191
Mp 216,16,52
ds_read_b128 v[140:143], v9 offset:128
v_add_f32_e32 v15, v15, v38
v_add_f32_e32 v15, v15, v28
Cv 38,38,28
Mp 216,20,60
ds_read_b128 v[148:151], v9 offset:256
v_add_f32_e32 v15, v15, v39
v_add_f32_e32 v15, v15, v29
Mp 208,20,56
buffer_load_dwordx4 v3, s[20:23], s55 offen offset:1024 sc1 lds
ds_read_b128 v[156:159], v9 offset:384
Mp 212,24,48
s_add_u32 s50, s50, 1024
s_min_u32 s50, s50, s60
Cv 192,192,193
Cv 39,39,29
Mp 220,24,52
Cv 193,194,195
Cv 194,196,197
s_sub_u32 s57, s57, 1
Mp 220,28,60
buffer_store_dwordx4 v[160:163], v3, s[20:23], s64 offen offset:0 sc0 sc1
Cv 195,198,199
Mp 212,28,56
Cv 200,200,201
v_mov_b32 v16, 0
s_waitcnt lgkmcnt(0)
s_barrier
Mp 224,32,48
Rb 0,1,32768
v_add_f32_e32 v132, v132, v140
v_mov_b32 v17, 0
)" R"(
Mp 232,32,52
Rb 4,1,33792
v_add_f32_e32 v133, v133, v141
Cv 201,202,203
Mp 232,36,60
Rb 8,1,34816
v_add_f32_e32 v134, v134, v142
v_mov_b32 v18, 0
v_mov_b32 v19, 0
Mp 224,36,56
Rb 12,1,35840
buffer_load_dwordx4 v3, s[20:23], s55 offen offset:2048 sc1 lds
Mp 228,40,48
Rb 16,1,40960
v_add_f32_e32 v135, v135, v143
buffer_store_dwordx4 v[168:171], v3, s[20:23], s64 offen offset:1024 sc0 sc1
Mp 236,40,52
Rb 20,1,41984
v_add_f32_e32 v148, v148, v156
Cv 202,204,205
Mp 236,44,60
Rb 24,1,43008
v_add_f32_e32 v149, v149, v157
v_mov_b32 v20, 0
v_mov_b32 v21, 0
Mp 228,44,56
Rb 28,1,44032
v_add_f32_e32 v150, v150, v158
Cv 203,206,207
Mp 240,48,48
Rb 32,1,36864
v_add_f32_e32 v151, v151, v159
Cv 208,208,209
Mp 248,48,52
Rb 36,1,37888
Cv 132,132,132
v_mov_b32 v22, 0
v_mov_b32 v23, 0
Mp 248,52,60
Rb 40,1,38912
v_lshlrev_b32 v132, 16, v132
buffer_store_dwordx4 v[176:179], v3, s[20:23], s64 offen offset:2048 sc0 sc1
Mp 240,52,56
Rb 44,1,39936
buffer_load_dwordx4 v3, s[20:23], s55 offen offset:3072 sc1 lds
Mp 244,56,48
Cv 148,148,148
v_lshlrev_b32 v148, 16, v148
Cv 209,210,211
Mp 252,56,52
Cv 133,133,133
v_lshlrev_b32 v133, 16, v133
v_accvgpr_mov_b32 a91, a251
Mp 252,60,60
Cv 149,149,149
Cv 210,212,213
s_waitcnt lgkmcnt(8)
Mp 244,60,56
v_lshlrev_b32 v149, 16, v149
Cv 134,134,134
v_lshlrev_b32 v134, 16, v134
Mz 96,0,224
Rb 48,1,45056
buffer_load_dword v4, v5, s[20:23], s50 offen offset:0 sc0 sc1
)" R"(
buffer_store_dwordx4 v[184:187], v3, s[20:23], s64 offen offset:3072 sc0 sc1
Mz 100,0,228
Cv 150,150,150
v_lshlrev_b32 v150, 16, v150
Cv 211,214,215
Mv 100,4,236
Rb 52,1,46080
s_add_u32 m0, s36, 65536
v_exp_f32_e32 v40, v80
Mv 96,4,232
buffer_load_dwordx4 v2, s[20:23], s56 offen offset:0 sc1 lds
Cv 216,216,217
v_add_f32_e32 v14, v14, v40
Mv 96,8,240
Rb 56,1,47104
Cv 135,135,135
v_accvgpr_mov_b32 a92, a252
Mv 100,8,244
v_lshlrev_b32 v135, 16, v135
Cv 151,151,151
Cv 217,218,219
Mv 100,12,252
Rb 60,1,48128
s_add_u32 s64, s64, 0x10000
buffer_store_dwordx4 v[192:195], v2, s[20:23], s64 offen offset:0 sc0 sc1
s_waitcnt lgkmcnt(8)
Mv 96,12,248
v_lshlrev_b32 v151, 16, v151
v_mul_f32_e32 v140, 0xbfb8aa3b, v132
Cv 218,220,221
Mv 96,16,192
Rb 0,1,49152
v_mul_f32_e32 v141, 0xbfb8aa3b, v133
Cv 219,222,223
Mv 100,16,196
v_exp_f32_e32 v140, v140
v_exp_f32_e32 v24, v81
Mv 100,20,204
Rb 4,1,50176
v_mul_f32_e32 v142, 0xbfb8aa3b, v134
Cv 224,224,225
v_add_f32_e32 v14, v14, v24
Mv 96,20,200
buffer_load_dwordx4 v2, s[20:23], s56 offen offset:1024 sc1 lds
v_exp_f32_e32 v41, v82
Mv 96,24,208
Rb 8,1,51200
v_mul_f32_e32 v143, 0xbfb8aa3b, v135
buffer_store_dwordx4 v[200:203], v2, s[20:23], s64 offen offset:1024 sc0 sc1
Mv 100,24,212
v_exp_f32_e32 v141, v141
v_exp_f32_e32 v25, v83
Mv 100,28,220
Rb 12,1,52224
Cv 225,226,227
Cv 40,40,24
s_waitcnt lgkmcnt(8)
Mv 96,28,216
v_exp_f32_e32 v142, v142
v_add_f32_e32 v140, 1.0, v140
v_add_f32_e32 v14, v14, v41
Mz 104,32,224
Rb 16,1,57344
v_add_f32_e32 v141, 1.0, v141
)" R"(
v_add_f32_e32 v14, v14, v25
Mz 108,32,228
v_exp_f32_e32 v143, v143
Cv 226,228,229
Cv 41,41,25
Mv 108,36,236
Rb 20,1,58368
v_add_f32_e32 v142, 1.0, v142
buffer_store_dwordx4 v[208:211], v2, s[20:23], s64 offen offset:2048 sc0 sc1
Mv 104,36,232
buffer_load_dwordx4 v2, s[20:23], s56 offen offset:2048 sc1 lds
Cv 227,230,231
Mv 104,40,240
Rb 24,1,59392
v_add_f32_e32 v143, 1.0, v143
v_accvgpr_mov_b32 a93, a253
Mv 108,40,244
v_rcp_f32_e32 v140, v140
Cv 232,232,233
Mv 108,44,252
Rb 28,1,60416
v_exp_f32_e32 v42, v88
s_waitcnt lgkmcnt(8)
Mv 104,44,248
v_rcp_f32_e32 v141, v141
Cv 233,234,235
v_add_f32_e32 v14, v14, v42
Mv 104,48,192
Rb 32,1,53248
v_mul_f32_e32 v132, v132, v140
buffer_store_dwordx4 v[216:219], v2, s[20:23], s64 offen offset:3072 sc0 sc1
Mv 108,48,196
v_rcp_f32_e32 v142, v142
v_exp_f32_e32 v26, v89
Mv 108,52,204
Rb 36,1,54272
v_mul_f32_e32 v133, v133, v141
Cv 234,236,237
v_add_f32_e32 v14, v14, v26
Mv 104,52,200
buffer_load_dwordx4 v2, s[20:23], s56 offen offset:3072 sc1 lds
v_exp_f32_e32 v43, v90
Mv 104,56,208
Rb 40,1,55296
v_mul_f32_e32 v134, v134, v142
Cv 235,238,239
Mv 108,56,212
v_rcp_f32_e32 v143, v143
v_exp_f32_e32 v27, v91
Mv 108,60,220
Rb 44,1,56320
buffer_store_dwordx4 v[224:227], v3, s[20:23], s64 offen offset:0 sc0 sc1
s_waitcnt lgkmcnt(8)
Mv 104,60,216
v_mul_f32_e32 v135, v135, v143
Cv 132,132,132
v_lshlrev_b32 v132, 16, v132
Cv 42,42,26
Mz 112,0,224
Rb 48,1,61440
Cv 133,133,133
v_add_f32_e32 v14, v14, v43
Mz 116,0,228
v_lshlrev_b32 v133, 16, v133
)" R"(
Cv 134,134,134
Cv 240,240,241
v_add_f32_e32 v14, v14, v27
Mv 116,4,236
Rb 52,1,62464
s_add_u32 m0, s36, 69632
v_exp_f32_e32 v44, v84
Mv 112,4,232
buffer_load_dwordx4 v3, s[20:23], s56 offen offset:0 sc1 lds
Cv 241,242,243
Cv 43,43,27
Mv 112,8,240
Rb 56,1,63488
v_lshlrev_b32 v134, 16, v134
buffer_store_dwordx4 v[232:235], v3, s[20:23], s64 offen offset:1024 sc0 sc1
Mv 116,8,244
Cv 135,135,135
v_lshlrev_b32 v135, 16, v135
Cv 242,244,245
v_add_f32_e32 v15, v15, v44
Mv 116,12,252
Rb 60,1,64512
v_exp_f32_e32 v28, v85
s_waitcnt lgkmcnt(8)
Mv 112,12,248
v_mul_f32_e32 v132, v132, v148
v_mul_f32_e32 v133, v133, v149
Cv 243,246,247
v_add_f32_e32 v15, v15, v28
Mv 112,16,192
v_mul_f32_e32 v134, v134, v150
v_mul_f32_e32 v135, v135, v151
Cv 248,248,249
Mv 116,16,196
Cv 140,132,133
Cv 141,134,135
v_exp_f32_e32 v45, v86
Mv 116,20,204
v_accvgpr_write_b32 a188, v140
v_accvgpr_write_b32 a189, v141
buffer_store_dwordx4 v[240:243], v3, s[20:23], s64 offen offset:2048 sc0 sc1
Mv 112,20,200
buffer_load_dwordx4 v3, s[20:23], s56 offen offset:1024 sc1 lds
v_exp_f32_e32 v29, v87
Mv 112,24,208
v_lshrrev_b32 v156, 2, v0
v_mul_u32_u24 v156, 864, v156
Cv 249,250,251
Mv 116,24,212
v_lshl_add_u32 v156, v0, 3, v156
s_mul_i32 s92, s33, 224
v_exp_f32_e32 v46, v92
Mv 116,28,220
s_add_u32 s92, s92, 131072
v_add_u32 v156, s92, v156
Cv 250,252,253
Cv 44,44,28
Mv 112,28,216
Cv 251,254,255
v_add_f32_e32 v15, v15, v45
s_waitcnt vmcnt(31) lgkmcnt(0)
s_barrier
Mz 120,32,224
Rb 0,8,32768
Rt 156,156,0
)" R"(
buffer_store_dwordx4 v[248:251], v3, s[20:23], s64 offen offset:3072 sc0 sc1
Mz 124,32,228
Rb 4,8,33792
Rt 158,156,32
v_exp_f32_e32 v24, v93
Mv 124,36,236
Rb 8,8,34816
Rt 160,156,64
v_exp_f32_e32 v47, v94
Mv 120,36,232
Rb 12,8,35840
buffer_load_dwordx4 v3, s[20:23], s56 offen offset:2048 sc1 lds
Rt 162,156,96
Mv 120,40,240
Rb 16,8,36864
Rt 164,156,128
v_add_f32_e32 v15, v15, v29
Mv 124,40,244
Rb 20,8,37888
Rt 166,156,160
v_exp_f32_e32 v25, v95
Mv 124,44,252
Rb 24,8,38912
Rt 168,156,192
Cv 45,45,29
v_add_f32_e32 v15, v15, v46
Mv 120,44,248
Rb 28,8,39936
v_add_f32_e32 v15, v15, v24
Cv 46,46,24
v_add_f32_e32 v15, v15, v47
Mv 120,48,192
Rb 32,8,40960
v_add_f32_e32 v15, v15, v25
Cv 47,47,25
Mv 124,48,196
Rb 36,8,41984
v_max_f32_e32 v11, v14, v15
v_mov_b32 v24, v14
v_mov_b32 v25, v15
Mv 124,52,204
Rb 40,8,43008
v_permlane32_swap_b32_e32 v24, v25
v_add_f32_e32 v24, v24, v25
v_mov_b32 v25, v24
Mv 120,52,200
Rb 44,8,44032
buffer_load_dwordx4 v3, s[20:23], s56 offen offset:3072 sc1 lds
v_permlane16_swap_b32_e32 v24, v25
Mv 120,56,208
v_add_f32_e32 v24, v24, v25
v_rcp_f32_e32 v24, v24
Mv 124,56,212
s_add_u32 s56, s56, 0x10000
v_max_f32_e32 v11, v11, v24
v_cmp_nge_f32_e32 vcc, s62, v11
s_add_u32 s55, s55, 0x10000
Mv 124,60,220
s_cmp_eq_u32 s57, 2
s_cselect_b32 s55, s59, s55
s_waitcnt lgkmcnt(12)
s_cbranch_vccnz .Lzfx4
.Lzfr4:
Mv 120,60,216
)" R"(
v_mov_b32 v12, v14
v_mov_b32 v13, v15
v_accvgpr_mov_b32 a94, a254
v_mfma_f32_16x16x32_bf16 v[128:131], a[0:3], v[32:35], 0
Rb 48,8,45056
v_accvgpr_mov_b32 a95, a255
v_mfma_f32_16x16x32_bf16 v[136:139], a[0:3], v[36:39], 0
v_accvgpr_mov_b32 a96, a192
s_cmp_le_i32 s57, s58
s_cbranch_scc1 .Lzmk5
.Lzmr5:
v_mfma_f32_16x16x32_bf16 v[140:143], a[4:7], v[36:39], 0
Rb 52,8,46080
s_add_u32 m0, s36, 32768
v_accvgpr_mov_b32 a97, a193
v_mfma_f32_16x16x32_bf16 v[132:135], a[4:7], v[32:35], 0
buffer_load_dwordx4 v2, s[20:23], s55 offen offset:0 sc1 lds
v_accvgpr_mov_b32 a98, a194
v_mfma_f32_16x16x32_bf16 v[144:147], a[8:11], v[32:35], 0
Rb 56,8,47104
v_and_b32 v11, 14, v0
v_accvgpr_mov_b32 a99, a195
v_mfma_f32_16x16x32_bf16 v[152:155], a[8:11], v[36:39], 0
v_min_u32 v11, 1, v11
v_lshlrev_b32 v11, 13, v11
v_accvgpr_mov_b32 a100, a196
v_mfma_f32_16x16x32_bf16 v[156:159], a[12:15], v[36:39], 0
Rb 60,8,48128
v_accvgpr_mov_b32 a101, a197
s_waitcnt lgkmcnt(8)
v_mfma_f32_16x16x32_bf16 v[148:151], a[12:15], v[32:35], 0
v_mfma_f32_16x16x16_bf16 a[128:131], a[156:157], a[188:189], 0
v_and_or_b32 v11, v0, 1, v11
v_mul_u32_u24 v11, 0x31000, v11
v_accvgpr_mov_b32 a102, a198
v_mfma_f32_16x16x32_bf16 v[160:163], a[16:19], v[32:35], 0
v_mfma_f32_16x16x16_bf16 a[132:135], a[158:159], a[188:189], 0
Rb 0,8,49152
v_and_or_b32 v11, v0, 48, v11
v_accvgpr_mov_b32 a103, a199
v_mfma_f32_16x16x32_bf16 v[168:171], a[16:19], v[36:39], 0
v_mfma_f32_16x16x16_bf16 a[136:139], a[160:161], a[188:189], 0
s_mul_i32 s83, s33, 50176
s_lshl_b32 s92, s82, 6
v_accvgpr_mov_b32 a104, a200
v_mfma_f32_16x16x32_bf16 v[172:175], a[20:23], v[36:39], 0
v_mfma_f32_16x16x16_bf16 a[140:143], a[162:163], a[188:189], 0
Rb 4,8,50176
s_add_u32 s83, s83, s92
v_accvgpr_mov_b32 a105, a201
v_mfma_f32_16x16x32_bf16 v[164:167], a[20:23], v[32:35], 0
v_mfma_f32_16x16x16_bf16 a[144:147], a[164:165], a[188:189], 0
buffer_load_dwordx4 v2, s[20:23], s55 offen offset:1024 sc1 lds
v_accvgpr_mov_b32 a106, a202
v_mfma_f32_16x16x32_bf16 v[176:179], a[24:27], v[32:35], 0
v_mfma_f32_16x16x16_bf16 a[148:151], a[166:167], a[188:189], 0
Rb 8,8,51200
s_add_u32 s83, s83, 0xc158000
v_accvgpr_mov_b32 a107, a203
v_mfma_f32_16x16x32_bf16 v[184:187], a[24:27], v[36:39], 0
v_mfma_f32_16x16x16_bf16 a[152:155], a[168:169], a[188:189], 0
v_exp_f32_e32 v48, v96
v_accvgpr_mov_b32 a108, a204
v_mfma_f32_16x16x32_bf16 v[188:191], a[28:31], v[36:39], 0
)" R"(
Rb 12,8,52224
v_accvgpr_mov_b32 a109, a205
s_waitcnt lgkmcnt(8)
v_mfma_f32_16x16x32_bf16 v[180:183], a[28:31], v[32:35], 0
v_exp_f32_e32 v26, v97
v_accvgpr_mov_b32 a110, a206
v_mfma_f32_16x16x32_bf16 v[192:195], a[32:35], v[32:35], 0
Rb 16,8,53248
v_add_f32_e32 v14, v48, v26
v_mfma_f32_16x16x32_bf16 v[200:203], a[32:35], v[36:39], 0
v_exp_f32_e32 v49, v98
Cv 48,48,26
v_mfma_f32_16x16x32_bf16 v[204:207], a[36:39], v[36:39], 0
Rb 20,8,54272
v_add_f32_e32 v14, v14, v49
v_accvgpr_mov_b32 a111, a207
v_mfma_f32_16x16x32_bf16 v[196:199], a[36:39], v[32:35], 0
buffer_load_dwordx4 v2, s[20:23], s55 offen offset:2048 sc1 lds
v_accvgpr_mov_b32 a112, a208
v_mfma_f32_16x16x32_bf16 v[208:211], a[40:43], v[32:35], 0
Rb 24,8,55296
v_exp_f32_e32 v27, v99
v_mfma_f32_16x16x32_bf16 v[216:219], a[40:43], v[36:39], 0
buffer_store_dwordx4 a[128:131], v11, s[20:23], s83 offen sc0 sc1
v_accvgpr_mov_b32 a113, a209
v_mfma_f32_16x16x32_bf16 v[220:223], a[44:47], v[36:39], 0
Rb 28,8,56320
v_accvgpr_mov_b32 a114, a210
s_waitcnt lgkmcnt(8)
v_mfma_f32_16x16x32_bf16 v[212:215], a[44:47], v[32:35], 0
s_add_u32 s83, s83, 7168
v_exp_f32_e32 v50, v104
v_mfma_f32_16x16x32_bf16 v[224:227], a[48:51], v[32:35], 0
Rb 32,8,57344
v_add_f32_e32 v14, v14, v27
v_accvgpr_mov_b32 a115, a211
v_mfma_f32_16x16x32_bf16 v[232:235], a[48:51], v[36:39], 0
v_exp_f32_e32 v28, v105
Cv 49,49,27
v_mfma_f32_16x16x32_bf16 v[236:239], a[52:55], v[36:39], 0
Rb 36,8,58368
v_add_f32_e32 v14, v14, v50
v_accvgpr_mov_b32 a116, a212
v_mfma_f32_16x16x32_bf16 v[228:231], a[52:55], v[32:35], 0
buffer_load_dwordx4 v2, s[20:23], s55 offen offset:3072 sc1 lds
v_accvgpr_mov_b32 a117, a213
v_mfma_f32_16x16x32_bf16 v[240:243], a[56:59], v[32:35], 0
Rb 40,8,59392
v_exp_f32_e32 v51, v106
v_mfma_f32_16x16x32_bf16 v[248:251], a[56:59], v[36:39], 0
buffer_store_dwordx4 a[132:135], v11, s[20:23], s83 offen sc0 sc1
v_accvgpr_mov_b32 a118, a214
v_mfma_f32_16x16x32_bf16 v[252:255], a[60:63], v[36:39], 0
Rb 44,8,60416
v_accvgpr_mov_b32 a119, a215
s_waitcnt lgkmcnt(8)
v_mfma_f32_16x16x32_bf16 v[244:247], a[60:63], v[32:35], 0
s_add_u32 s83, s83, 7168
v_exp_f32_e32 v29, v107
Mp 128,0,40
Rb 48,8,61440
v_add_f32_e32 v14, v14, v28
Mp 136,0,44
buffer_store_dwordx4 a[136:139], v11, s[20:23], s83 offen sc0 sc1
)" R"(
s_add_u32 s83, s83, 7168
Mp 140,4,44
Rb 52,8,62464
s_add_u32 m0, s36, 36864
v_accvgpr_mov_b32 a120, a216
Mp 132,4,40
buffer_load_dwordx4 v3, s[20:23], s55 offen offset:0 sc1 lds
v_accvgpr_mov_b32 a121, a217
Mp 144,8,40
Rb 56,8,63488
v_exp_f32_e32 v52, v100
Mp 152,8,44
v_exp_f32_e32 v24, v101
v_accvgpr_mov_b32 a122, a218
Mp 156,12,44
Rb 60,8,64512
v_accvgpr_mov_b32 a123, a219
s_waitcnt lgkmcnt(8)
Mp 148,12,40
buffer_store_dwordx4 a[140:143], v11, s[20:23], s83 offen sc0 sc1
Cv 50,50,28
Mp 160,16,40
s_add_u32 s83, s83, 7168
v_add_f32_e32 v14, v14, v51
v_accvgpr_mov_b32 a124, a220
Mp 168,16,44
v_exp_f32_e32 v53, v102
v_add_f32_e32 v14, v14, v29
Mp 172,20,44
v_exp_f32_e32 v25, v103
v_accvgpr_mov_b32 a125, a221
Mp 164,20,40
buffer_load_dwordx4 v3, s[20:23], s55 offen offset:1024 sc1 lds
v_accvgpr_mov_b32 a126, a222
Mp 176,24,40
v_exp_f32_e32 v54, v108
Cv 51,51,29
Mp 184,24,44
buffer_store_dwordx4 a[144:147], v11, s[20:23], s83 offen sc0 sc1
v_accvgpr_mov_b32 a127, a223
Mp 188,28,44
s_add_u32 s83, s83, 7168
v_add_f32_e32 v15, v52, v24
Mp 180,28,40
Cv 52,52,24
s_waitcnt vmcnt(13) lgkmcnt(0)
s_barrier
buffer_store_dword v6, v7, s[20:23], s65 offen offset:0 sc0 sc1
Mp 192,32,40
Rb 0,1,0
v_add_f32_e32 v15, v15, v53
Mp 200,32,44
Rb 4,1,1024
v_exp_f32_e32 v26, v109
Mp 204,36,44
Rb 8,1,2048
v_add_f32_e32 v15, v15, v25
Mp 196,36,40
Rb 12,1,3072
buffer_load_dwordx4 v3, s[20:23], s55 offen offset:2048 sc1 lds
Mp 208,40,40
Rb 16,1,8192
v_exp_f32_e32 v55, v110
Mp 216,40,44
Rb 20,1,9216
)" R"(
Cv 53,53,25
Mp 220,44,44
Rb 24,1,10240
buffer_store_dwordx4 a[148:151], v11, s[20:23], s83 offen sc0 sc1
Mp 212,44,40
Rb 28,1,11264
s_add_u32 s83, s83, 7168
v_add_f32_e32 v15, v15, v54
Mp 224,48,40
Rb 32,1,4096
v_add_f32_e32 v15, v15, v26
Mp 232,48,44
Rb 36,1,5120
v_exp_f32_e32 v27, v111
Mp 236,52,44
Rb 40,1,6144
Cv 54,54,26
Mp 228,52,40
Rb 44,1,7168
buffer_load_dwordx4 v3, s[20:23], s55 offen offset:3072 sc1 lds
Mp 240,56,40
v_cmp_eq_u32_e32 vcc, s3, v4
s_andn2_b64 s[28:29], -1, vcc
s_cbranch_scc1 .Lzsp10
.Lzok10:
Mp 248,56,44
buffer_store_dwordx4 a[152:155], v11, s[20:23], s83 offen sc0 sc1
Mp 252,60,44
v_add_f32_e32 v15, v15, v55
s_waitcnt lgkmcnt(8)
Mp 244,60,40
v_add_f32_e32 v15, v15, v27
Cv 55,55,27
s_sub_u32 s57, s57, 1
s_cmp_gt_i32 s57, s93
s_cbranch_scc1 .LzL
.LzDA:
s_waitcnt vmcnt(8) lgkmcnt(0)
s_barrier
Rb 0,8,0
Rb 4,8,16384
Rb 8,8,1024
Rb 12,8,17408
Rb 16,8,2048
Rb 20,8,18432
Rb 24,8,3072
Rb 28,8,19456
Rb 32,8,4096
Rb 36,8,20480
Rb 40,8,5120
Rb 44,8,21504
v_exp_f32_e32 v56, v112
v_exp_f32_e32 v28, v113
v_add_f32_e32 v14, v14, v56
v_add_f32_e32 v14, v14, v28
Cv 56,56,28
v_exp_f32_e32 v57, v114
v_exp_f32_e32 v29, v115
v_add_f32_e32 v14, v14, v57
v_add_f32_e32 v14, v14, v29
Cv 57,57,29
v_exp_f32_e32 v58, v120
v_exp_f32_e32 v24, v121
v_add_f32_e32 v14, v14, v58
v_add_f32_e32 v14, v14, v24
)" R"(
Cv 58,58,24
v_exp_f32_e32 v59, v122
v_exp_f32_e32 v25, v123
v_add_f32_e32 v14, v14, v59
v_add_f32_e32 v14, v14, v25
Cv 59,59,25
v_exp_f32_e32 v60, v116
v_exp_f32_e32 v26, v117
v_add_f32_e32 v15, v15, v60
v_add_f32_e32 v15, v15, v26
Cv 60,60,26
v_exp_f32_e32 v61, v118
v_exp_f32_e32 v27, v119
v_add_f32_e32 v15, v15, v61
v_add_f32_e32 v15, v15, v27
Cv 61,61,27
v_exp_f32_e32 v62, v124
v_exp_f32_e32 v28, v125
v_add_f32_e32 v15, v15, v62
v_add_f32_e32 v15, v15, v28
Cv 62,62,28
v_exp_f32_e32 v63, v126
v_exp_f32_e32 v29, v127
v_add_f32_e32 v15, v15, v63
v_add_f32_e32 v15, v15, v29
Cv 63,63,29
v_max_f32_e32 v11, v14, v15
v_cmp_nge_f32_e32 vcc, s62, v11
s_cbranch_vccnz .Lzfx5
.Lzfr5:
s_waitcnt lgkmcnt(8)
Mp 128,0,48
Rb 48,8,6144
s_add_u32 m0, s36, 131072
s_add_u32 s40, s67, 0x20000
s_lshl_b32 s82, s82, 7
Mp 136,0,52
buffer_load_dwordx4 v2, s[20:23], s40 offen offset:0 sc1 lds
v_add_f32_e32 v12, v12, v14
s_add_u32 s82, s82, 0xc10c000
Mp 136,4,60
Rb 52,8,22528
v_add_f32_e32 v13, v13, v15
v_mov_b32 v24, v12
Mp 128,4,56
buffer_load_dwordx4 v2, s[20:23], s40 offen offset:1024 sc1 lds
v_permlane32_swap_b32_e32 v12, v24
Mp 132,8,48
Rb 56,8,7168
v_add_f32_e32 v12, v12, v24
v_mov_b32 v24, v12
Mp 140,8,52
s_nop 0
v_permlane16_swap_b32_e32 v12, v24
v_add_f32_e32 v12, v12, v24
v_mov_b32 v25, v13
Mp 140,12,60
Rb 60,8,23552
v_permlane32_swap_b32_e32 v13, v25
s_waitcnt lgkmcnt(8)
Mp 132,12,56
buffer_load_dwordx4 v2, s[20:23], s40 offen offset:2048 sc1 lds
v_add_f32_e32 v13, v13, v25
Mp 144,16,48
)" R"(
Rb 0,8,8192
v_rcp_f32_e32 v12, v12
Mp 152,16,52
buffer_load_dwordx4 v2, s[20:23], s40 offen offset:3072 sc1 lds
v_mov_b32 v25, v13
Mp 152,20,60
Rb 4,8,24576
v_permlane16_swap_b32_e32 v13, v25
v_add_f32_e32 v13, v13, v25
Mp 144,20,56
v_rcp_f32_e32 v13, v13
v_mul_f32_e32 v128, v12, v128
Mp 148,24,48
Rb 8,8,9216
s_add_u32 m0, s36, 135168
v_mul_f32_e32 v129, v12, v129
Mp 156,24,52
buffer_load_dwordx4 v3, s[20:23], s40 offen offset:0 sc1 lds
Cv 128,128,129
Mp 156,28,60
Rb 12,8,25600
v_mul_f32_e32 v130, v12, v130
s_waitcnt lgkmcnt(8)
Mp 148,28,56
buffer_load_dwordx4 v3, s[20:23], s40 offen offset:1024 sc1 lds
v_mul_f32_e32 v131, v12, v131
Mp 160,32,48
Rb 16,8,10240
Cv 129,130,131
s_cmp_lg_u32 s33, 0
s_cmov_b32 s82, 0x40000000
Mp 168,32,52
v_mul_f32_e32 v132, v12, v132
v_mul_f32_e32 v133, v12, v133
Cv 130,132,133
Mp 168,36,60
Rb 20,8,26624
v_mul_f32_e32 v134, v12, v134
v_mul_f32_e32 v135, v12, v135
Mp 160,36,56
buffer_load_dwordx4 v3, s[20:23], s40 offen offset:2048 sc1 lds
Cv 131,134,135
Mp 164,40,48
Rb 24,8,11264
s_add_u32 m0, s36, 98304
s_add_u32 s40, s67, 0x30000
Mp 172,40,52
buffer_load_dwordx4 v2, s[20:23], s40 offen offset:0 sc1 lds
v_mul_f32_e32 v136, v13, v136
Mp 172,44,60
Rb 28,8,27648
v_mul_f32_e32 v137, v13, v137
s_waitcnt lgkmcnt(8)
Mp 164,44,56
Cv 136,136,137
v_mul_f32_e32 v138, v13, v138
v_mul_f32_e32 v139, v13, v139
Mp 176,48,48
Rb 32,8,12288
Cv 137,138,139
v_mul_f32_e32 v140, v13, v140
Mp 184,48,52
buffer_load_dwordx4 v2, s[20:23], s40 offen offset:1024 sc1 lds
v_mul_f32_e32 v141, v13, v141
)" R"(
Mp 184,52,60
Rb 36,8,28672
Cv 138,140,141
v_mul_f32_e32 v142, v13, v142
Mp 176,52,56
buffer_load_dwordx4 v2, s[20:23], s40 offen offset:2048 sc1 lds
v_mul_f32_e32 v143, v13, v143
Mp 180,56,48
Rb 40,8,13312
Cv 139,142,143
v_mul_f32_e32 v144, v12, v144
Mp 188,56,52
v_mul_f32_e32 v145, v12, v145
Cv 144,144,145
v_mul_f32_e32 v146, v12, v146
Mp 188,60,60
Rb 44,8,29696
v_mul_f32_e32 v147, v12, v147
s_waitcnt lgkmcnt(8)
Mp 180,60,56
buffer_load_dwordx4 v2, s[20:23], s40 offen offset:3072 sc1 lds
Cv 145,146,147
Mp 192,0,48
Rb 48,8,14336
s_add_u32 m0, s36, 102400
s_cmp_eq_u32 s35, 7
s_cmov_b32 s82, 0x40000000
Mp 200,0,52
buffer_load_dwordx4 v3, s[20:23], s40 offen offset:0 sc1 lds
v_mul_f32_e32 v148, v12, v148
Mp 200,4,60
Rb 52,8,30720
v_mul_f32_e32 v149, v12, v149
Cv 146,148,149
Mp 192,4,56
v_mul_f32_e32 v150, v12, v150
v_mul_f32_e32 v151, v12, v151
Cv 147,150,151
Mp 196,8,48
Rb 56,8,15360
v_mul_f32_e32 v152, v13, v152
v_mul_f32_e32 v153, v13, v153
Mp 204,8,52
buffer_load_dwordx4 v3, s[20:23], s40 offen offset:1024 sc1 lds
Cv 152,152,153
Mp 204,12,60
Rb 60,8,31744
v_mul_f32_e32 v154, v13, v154
s_waitcnt lgkmcnt(8)
Mp 196,12,56
buffer_load_dwordx4 v3, s[20:23], s40 offen offset:2048 sc1 lds
v_mul_f32_e32 v155, v13, v155
Mp 208,16,48
Rb 0,1,0
Cv 153,154,155
v_mul_f32_e32 v156, v13, v156
Mp 216,16,52
v_mul_f32_e32 v157, v13, v157
Cv 154,156,157
v_mul_f32_e32 v158, v13, v158
Mp 216,20,60
Rb 4,1,1024
v_mul_f32_e32 v159, v13, v159
Cv 155,158,159
)" R"(
Mp 208,20,56
v_mul_f32_e32 v160, v12, v160
v_mul_f32_e32 v161, v12, v161
Cv 160,160,161
Mp 212,24,48
Rb 8,1,2048
v_mul_f32_e32 v162, v12, v162
v_mul_f32_e32 v163, v12, v163
Mp 220,24,52
Cv 161,162,163
v_mul_f32_e32 v164, v12, v164
v_mul_f32_e32 v165, v12, v165
Mp 220,28,60
Rb 12,1,3072
s_waitcnt vmcnt(14) lgkmcnt(4)
s_barrier
Mp 212,28,56
s_add_u32 s40, s67, 0x40000
Cv 162,164,165
v_mul_f32_e32 v166, v12, v166
Mp 224,32,48
Rb 16,1,4096
s_add_u32 m0, s36, 65536
Mp 232,32,52
buffer_load_dwordx4 v2, s[20:23], s40 offen offset:0 sc1 lds
v_mul_f32_e32 v167, v12, v167
Mp 232,36,60
Rb 20,1,5120
Cv 163,166,167
v_mul_f32_e32 v168, v13, v168
Mp 224,36,56
v_mul_f32_e32 v169, v13, v169
Cv 168,168,169
v_mul_f32_e32 v170, v13, v170
Mp 228,40,48
Rb 24,1,6144
v_mul_f32_e32 v171, v13, v171
Cv 169,170,171
Mp 236,40,52
v_mul_f32_e32 v172, v13, v172
v_mul_f32_e32 v173, v13, v173
Cv 170,172,173
Mp 236,44,60
Rb 28,1,8192
v_mul_f32_e32 v174, v13, v174
Mp 228,44,56
buffer_load_dwordx4 v2, s[20:23], s40 offen offset:1024 sc1 lds
v_mul_f32_e32 v175, v13, v175
s_lshl4_add_u32 s28, s34, s35
Mp 240,48,48
Rb 32,1,9216
Cv 171,174,175
v_mul_f32_e32 v176, v12, v176
Mp 248,48,52
v_mul_f32_e32 v177, v12, v177
Cv 176,176,177
v_mul_f32_e32 v178, v12, v178
Mp 248,52,60
Rb 36,1,10240
v_mul_f32_e32 v179, v12, v179
Cv 177,178,179
Mp 240,52,56
v_mul_f32_e32 v180, v12, v180
v_mul_f32_e32 v181, v12, v181
Cv 178,180,181
)" R"(
Mp 244,56,48
Rb 40,1,11264
v_mul_f32_e32 v182, v12, v182
v_mul_f32_e32 v183, v12, v183
s_lshl_b32 s64, s28, 17
Mp 252,56,52
buffer_load_dwordx4 v2, s[20:23], s40 offen offset:2048 sc1 lds
Cv 179,182,183
Mp 252,60,60
Rb 44,1,12288
v_mul_f32_e32 v184, v13, v184
s_mov_b32 s55, 0xc108c00
s_waitcnt lgkmcnt(8)
Mp 244,60,56
v_mul_f32_e32 v185, v13, v185
Cv 184,184,185
v_mul_f32_e32 v186, v13, v186
v_mfma_f32_16x16x32_bf16 v[32:35], a[0:3], v[128:131], 0
Rb 48,1,13312
v_mul_f32_e32 v187, v13, v187
s_add_u32 s64, s64, 0x4000000
v_mfma_f32_16x16x32_bf16 v[40:43], a[0:3], v[136:139], 0
Cv 185,186,187
v_mul_f32_e32 v188, v13, v188
v_mul_f32_e32 v189, v13, v189
s_xor_b32 s66, s64, 0x202000
v_mfma_f32_16x16x32_bf16 v[44:47], a[4:7], v[136:139], 0
Rb 52,1,14336
Cv 186,188,189
v_mul_f32_e32 v190, v13, v190
v_mfma_f32_16x16x32_bf16 v[36:39], a[4:7], v[128:131], 0
buffer_load_dwordx4 v2, s[20:23], s40 offen offset:3072 sc1 lds
v_mul_f32_e32 v191, v13, v191
v_mfma_f32_16x16x32_bf16 v[48:51], a[8:11], v[128:131], 0
Rb 56,1,16384
Cv 187,190,191
v_mul_f32_e32 v192, v12, v192
s_cmp_lg_u32 s33, 0
s_cmov_b32 s55, 0x40000000
v_mfma_f32_16x16x32_bf16 v[56:59], a[8:11], v[136:139], 0
v_mul_f32_e32 v193, v12, v193
Cv 192,192,193
v_mul_f32_e32 v194, v12, v194
v_mov_b32 v16, 1
v_mfma_f32_16x16x32_bf16 v[60:63], a[12:15], v[136:139], 0
Rb 60,1,17408
v_mul_f32_e32 v195, v12, v195
v_add_u32 v9, s36, v8
s_waitcnt lgkmcnt(8)
v_mfma_f32_16x16x32_bf16 v[52:55], a[12:15], v[128:131], 0
Cv 193,194,195
v_mul_f32_e32 v196, v12, v196
v_mul_f32_e32 v197, v12, v197
s_sub_u32 s66, s66, s84
Mp 32,16,144
Rb 0,1,18432
s_add_u32 m0, s36, 69632
Cv 194,196,197
Mp 40,16,152
buffer_load_dwordx4 v3, s[20:23], s40 offen offset:0 sc1 lds
v_mul_f32_e32 v198, v12, v198
Mp 44,20,152
Rb 4,1,19456
v_mul_f32_e32 v199, v12, v199
)" R"(
Cv 195,198,199
Mp 36,20,144
v_mul_f32_e32 v200, v13, v200
v_mul_f32_e32 v201, v13, v201
Cv 200,200,201
Mp 48,24,144
Rb 8,1,20480
v_mul_f32_e32 v202, v13, v202
v_mul_f32_e32 v203, v13, v203
s_sub_u32 s66, s66, s84
Mp 56,24,152
Cv 201,202,203
v_mul_f32_e32 v204, v13, v204
v_mul_f32_e32 v205, v13, v205
Mp 60,28,152
Rb 12,1,21504
Cv 202,204,205
s_waitcnt lgkmcnt(8)
Mp 52,28,144
buffer_load_dwordx4 v3, s[20:23], s40 offen offset:1024 sc1 lds
v_mul_f32_e32 v206, v13, v206
Mp 32,32,160
Rb 16,1,22528
v_mul_f32_e32 v207, v13, v207
s_lshl2_add_u32 s65, s35, s33
Mp 40,32,168
Cv 203,206,207
v_mul_f32_e32 v208, v12, v208
v_mul_f32_e32 v209, v12, v209
s_lshl_b32 s65, s65, 8
Mp 44,36,168
Rb 20,1,24576
Cv 208,208,209
v_mul_f32_e32 v210, v12, v210
Mp 36,36,160
v_mul_f32_e32 v211, v12, v211
Cv 209,210,211
v_mul_f32_e32 v212, v12, v212
Mp 48,40,160
Rb 24,1,25600
v_mul_f32_e32 v213, v12, v213
Cv 210,212,213
Mp 56,40,168
buffer_load_dwordx4 v3, s[20:23], s40 offen offset:2048 sc1 lds
v_mul_f32_e32 v214, v12, v214
Mp 60,44,168
Rb 28,1,26624
v_mul_f32_e32 v215, v12, v215
s_waitcnt lgkmcnt(8)
Mp 52,44,160
Cv 211,214,215
v_mul_f32_e32 v216, v13, v216
v_mul_f32_e32 v217, v13, v217
s_add_u32 s65, s65, s50
Mp 32,48,176
Rb 32,1,27648
Cv 216,216,217
v_mul_f32_e32 v218, v13, v218
Mp 40,48,184
v_mul_f32_e32 v219, v13, v219
Cv 217,218,219
v_mul_f32_e32 v220, v13, v220
Mp 44,52,184
Rb 36,1,28672
)" R"(
v_mul_f32_e32 v221, v13, v221
Cv 218,220,221
Mp 36,52,176
v_mul_f32_e32 v222, v13, v222
v_mul_f32_e32 v223, v13, v223
Cv 219,222,223
Mp 48,56,176
Rb 40,1,29696
v_mul_f32_e32 v224, v12, v224
v_mul_f32_e32 v225, v12, v225
s_add_u32 s65, s65, 0x2400
Mp 56,56,184
Cv 224,224,225
v_mul_f32_e32 v226, v12, v226
v_mul_f32_e32 v227, v12, v227
Mp 60,60,184
Rb 44,1,30720
Cv 225,226,227
buffer_atomic_add v16, v7, s[20:23], s55 offen sc0 sc1
s_waitcnt lgkmcnt(8)
Mp 52,60,176
v_mul_f32_e32 v228, v12, v228
v_mul_f32_e32 v229, v12, v229
Cv 226,228,229
Mp 32,0,192
Rb 48,1,32768
v_mul_f32_e32 v230, v12, v230
s_xor_b32 s52, s65, 0x202000
Mp 40,0,200
v_mul_f32_e32 v231, v12, v231
Cv 227,230,231
v_mul_f32_e32 v232, v13, v232
Mp 44,4,200
Rb 52,1,33792
v_mul_f32_e32 v233, v13, v233
Cv 232,232,233
Mp 36,4,192
v_mul_f32_e32 v234, v13, v234
v_mul_f32_e32 v235, v13, v235
Cv 233,234,235
Mp 48,8,192
Rb 56,1,34816
v_mul_f32_e32 v236, v13, v236
v_mul_f32_e32 v237, v13, v237
Mp 56,8,200
Cv 234,236,237
v_mul_f32_e32 v238, v13, v238
v_mul_f32_e32 v239, v13, v239
Mp 60,12,200
Rb 60,1,35840
s_waitcnt vmcnt(15) lgkmcnt(4)
s_barrier
buffer_store_dword v6, v7, s[20:23], s82 offen offset:0 sc0 sc1
Mp 52,12,192
s_add_u32 s40, s67, 0x50000
Cv 235,238,239
v_mul_f32_e32 v240, v12, v240
Mp 32,16,208
Rb 0,1,36864
s_add_u32 m0, s36, 0
v_mul_f32_e32 v241, v12, v241
Mp 40,16,216
buffer_load_dwordx4 v2, s[20:23], s40 offen offset:0 sc1 lds
Cv 240,240,241
Mp 44,20,216
)" R"(
Rb 4,1,37888
v_mul_f32_e32 v242, v12, v242
v_mul_f32_e32 v243, v12, v243
Mp 36,20,208
Cv 241,242,243
v_mul_f32_e32 v244, v12, v244
v_mul_f32_e32 v245, v12, v245
Mp 48,24,208
Rb 8,1,38912
Cv 242,244,245
v_mul_f32_e32 v246, v12, v246
Mp 56,24,216
v_mul_f32_e32 v247, v12, v247
Cv 243,246,247
v_mul_f32_e32 v248, v13, v248
Mp 60,28,216
Rb 12,1,40960
v_mul_f32_e32 v249, v13, v249
Mp 52,28,208
buffer_load_dwordx4 v2, s[20:23], s40 offen offset:1024 sc1 lds
Cv 248,248,249
Mp 32,32,224
Rb 16,1,41984
v_mul_f32_e32 v250, v13, v250
Mp 40,32,232
v_mul_f32_e32 v251, v13, v251
Cv 249,250,251
v_mul_f32_e32 v252, v13, v252
Mp 44,36,232
Rb 20,1,43008
v_mul_f32_e32 v253, v13, v253
Cv 250,252,253
Mp 36,36,224
v_mul_f32_e32 v254, v13, v254
v_mul_f32_e32 v255, v13, v255
Cv 251,254,255
Mp 48,40,224
Rb 24,1,44032
Mp 56,40,232
buffer_load_dwordx4 v2, s[20:23], s40 offen offset:2048 sc1 lds
Mp 60,44,232
Rb 28,1,45056
s_waitcnt lgkmcnt(8)
Mp 52,44,224
Mp 32,48,240
Rb 32,1,46080
Mp 40,48,248
Mp 44,52,248
Rb 36,1,47104
Mp 36,52,240
buffer_load_dwordx4 v2, s[20:23], s40 offen offset:3072 sc1 lds
Mp 48,56,240
Rb 40,1,49152
Mp 56,56,248
Mp 60,60,248
Rb 44,1,50176
s_waitcnt lgkmcnt(8)
Mp 52,60,240
v_mfma_f32_16x16x32_bf16 v[64:67], a[0:3], v[128:131], 0
Rb 48,1,51200
s_add_u32 m0, s36, 4096
v_mfma_f32_16x16x32_bf16 v[72:75], a[0:3], v[136:139], 0
buffer_load_dwordx4 v3, s[20:23], s40 offen offset:0 sc1 lds
Mp 72,4,152
)" R"(
Rb 52,1,52224
Mp 64,4,144
Cv 32,32,33
Cv 33,34,35
Mp 64,8,160
Rb 56,1,53248
Cv 34,36,37
Mp 72,8,168
Cv 35,38,39
Mp 72,12,184
Rb 60,1,54272
s_waitcnt lgkmcnt(8)
Mp 64,12,176
buffer_load_dwordx4 v3, s[20:23], s40 offen offset:1024 sc1 lds
Mp 64,16,192
Rb 0,1,55296
Mp 72,16,200
buffer_store_dwordx4 v[32:35], v2, s[20:23], s64 offen offset:0 sc0 sc1
Mp 72,20,216
Rb 4,1,57344
Mp 64,20,208
Mp 64,24,224
Rb 8,1,58368
Mp 72,24,232
buffer_load_dwordx4 v3, s[20:23], s40 offen offset:2048 sc1 lds
Mp 72,28,248
Rb 12,1,59392
s_waitcnt lgkmcnt(8)
Mp 64,28,240
Cv 40,40,41
Cv 41,42,43
v_mfma_f32_16x16x32_bf16 v[68:71], a[32:35], v[128:131], 0
Rb 16,1,60416
Cv 42,44,45
v_mfma_f32_16x16x32_bf16 v[76:79], a[32:35], v[136:139], 0
Cv 43,46,47
Mp 76,36,152
Rb 20,1,61440
Mp 68,36,144
buffer_store_dwordx4 v[40:43], v2, s[20:23], s64 offen offset:1024 sc0 sc1
Mp 68,40,160
Rb 24,1,62464
Mp 76,40,168
Mp 76,44,184
Rb 28,1,63488
s_waitcnt lgkmcnt(8)
Mp 68,44,176
Mp 68,48,192
Rb 32,10,0
Mp 76,48,200
Cv 48,48,49
Cv 49,50,51
Mp 76,52,216
Rb 36,10,1024
Cv 50,52,53
Mp 68,52,208
Cv 51,54,55
Mp 68,56,224
Rb 40,10,2048
Mp 76,56,232
buffer_store_dwordx4 v[48:51], v2, s[20:23], s64 offen offset:2048 sc0 sc1
Mp 76,60,248
Rb 44,10,3072
s_waitcnt vmcnt(19) lgkmcnt(4)
s_barrier
)" R"(
Mp 68,60,240
s_add_u32 s40, s67, 0x60000
v_mfma_f32_16x16x32_bf16 v[80:83], a[0:3], v[128:131], 0
Rb 48,10,4096
s_add_u32 m0, s36, 32768
v_mfma_f32_16x16x32_bf16 v[88:91], a[0:3], v[136:139], 0
buffer_load_dwordx4 v2, s[20:23], s40 offen offset:0 sc1 lds
Mp 88,4,152
Rb 52,10,5120
Mp 80,4,144
Cv 56,56,57
Cv 57,58,59
Cv 58,60,61
Mp 80,8,160
Rb 56,10,6144
v_bfe_u32 v11, v0, 2, 2
Cv 59,62,63
Mp 88,8,168
v_lshrrev_b32 v4, 4, v0
v_xor_b32 v11, v11, v4
Mp 88,12,184
Rb 60,10,8192
Mp 80,12,176
buffer_load_dwordx4 v2, s[20:23], s40 offen offset:1024 sc1 lds
Mp 80,16,192
Rb 0,10,9216
v_bfe_u32 v4, v0, 1, 1
Mp 88,16,200
v_lshl_or_b32 v11, v11, 1, v4
v_and_b32 v4, 1, v0
Mp 88,20,216
Rb 4,10,10240
v_mul_u32_u24 v4, 0xffff, v4
buffer_store_dwordx4 v[56:59], v2, s[20:23], s64 offen offset:3072 sc0 sc1
Mp 80,20,208
v_add_u32 v4, 1, v4
v_mul_u32_u24 v4, 0x3f80, v4
Mp 80,24,224
Rb 8,10,11264
v_cmp_eq_u32_e32 vcc, 0, v11
Mp 88,24,232
buffer_load_dwordx4 v2, s[20:23], s40 offen offset:2048 sc1 lds
Mp 88,28,248
Rb 12,10,12288
s_waitcnt lgkmcnt(8)
Mp 80,28,240
v_cmp_eq_u32_e64 s[28:29], 1, v11
v_accvgpr_write_b32 a192, 0
v_mfma_f32_16x16x32_bf16 v[84:87], a[32:35], v[128:131], 0
Rb 16,10,13312
v_accvgpr_write_b32 a193, 0
v_mfma_f32_16x16x32_bf16 v[92:95], a[32:35], v[136:139], 0
v_accvgpr_write_b32 a196, 0
v_accvgpr_write_b32 a197, 0
Cv 64,64,65
Mp 92,36,152
Rb 20,10,14336
v_cndmask_b32_e32 v11, 0, v4, vcc
Mp 84,36,144
buffer_load_dwordx4 v2, s[20:23], s40 offen offset:3072 sc1 lds
Cv 65,66,67
Mp 84,40,160
Rb 24,10,16384
v_cndmask_b32_e64 v4, 0, v4, s[28:29]
)" R"(
Mp 92,40,168
v_accvgpr_write_b32 a194, v11
v_accvgpr_write_b32 a195, v4
Cv 66,68,69
Mp 92,44,184
Rb 28,10,17408
s_waitcnt lgkmcnt(8)
Mp 84,44,176
Cv 67,70,71
buffer_store_dwordx4 v[64:67], v3, s[20:23], s64 offen offset:0 sc0 sc1
Mp 84,48,192
Rb 32,10,18432
s_add_u32 m0, s36, 36864
Mp 92,48,200
buffer_load_dwordx4 v3, s[20:23], s40 offen offset:0 sc1 lds
Mp 92,52,216
Rb 36,10,19456
Mp 84,52,208
Mp 84,56,224
Rb 40,10,20480
Mp 92,56,232
Cv 72,72,73
Cv 73,74,75
Mp 92,60,248
Rb 44,10,21504
s_waitcnt lgkmcnt(8)
Mp 84,60,240
buffer_load_dwordx4 v3, s[20:23], s40 offen offset:1024 sc1 lds
v_mfma_f32_16x16x32_bf16 v[96:99], a[0:3], v[128:131], 0
Rb 48,10,22528
Cv 74,76,77
v_mfma_f32_16x16x32_bf16 v[104:107], a[0:3], v[136:139], 0
Cv 75,78,79
Mp 104,4,152
Rb 52,10,24576
Mp 96,4,144
buffer_store_dwordx4 v[72:75], v3, s[20:23], s64 offen offset:1024 sc0 sc1
Mp 96,8,160
Rb 56,10,25600
Mp 104,8,168
buffer_load_dwordx4 v3, s[20:23], s40 offen offset:2048 sc1 lds
Mp 104,12,184
Rb 60,10,26624
s_waitcnt lgkmcnt(8)
Mp 96,12,176
Mp 96,16,192
Rb 0,10,27648
Mp 104,16,200
Cv 80,80,81
Cv 81,82,83
Mp 104,20,216
Rb 4,10,28672
Cv 82,84,85
Mp 96,20,208
Cv 83,86,87
Mp 96,24,224
Rb 8,10,29696
Mp 104,24,232
buffer_store_dwordx4 v[80:83], v3, s[20:23], s64 offen offset:2048 sc0 sc1
Mp 104,28,248
Rb 12,10,30720
s_waitcnt lgkmcnt(8)
Mp 96,28,240
v_mfma_f32_16x16x32_bf16 v[100:103], a[32:35], v[128:131], 0
)" R"(
Rb 16,8,32768
v_mfma_f32_16x16x32_bf16 v[108:111], a[32:35], v[136:139], 0
Mp 108,36,152
Rb 20,8,33792
Mp 100,36,144
Cv 88,88,89
Cv 89,90,91
Mp 100,40,160
Rb 24,8,34816
Cv 90,92,93
Mp 108,40,168
Cv 91,94,95
Mp 108,44,184
Rb 28,8,35840
s_waitcnt vmcnt(23) lgkmcnt(4)
s_barrier
Mp 100,44,176
s_add_u32 s40, s67, 0x70000
Mp 100,48,192
Rb 32,8,36864
s_add_u32 m0, s36, 131072
Mp 108,48,200
buffer_load_dwordx4 v2, s[20:23], s40 offen offset:0 sc1 lds
Mp 108,52,216
Rb 36,8,37888
buffer_store_dwordx4 v[88:91], v3, s[20:23], s64 offen offset:3072 sc0 sc1
Mp 100,52,208
s_mov_b32 s8, s74
s_mov_b32 s9, s75
Mp 100,56,224
Rb 40,8,38912
s_mov_b32 s10, 0x1c0000
Mp 108,56,232
s_mov_b32 s11, 0x20000
v_and_b32 v174, 1, v0
Mp 108,60,248
Rb 44,8,40960
Mp 100,60,240
buffer_load_dwordx4 v2, s[20:23], s40 offen offset:1024 sc1 lds
v_mfma_f32_16x16x32_bf16 v[112:115], a[0:3], v[128:131], 0
Rb 48,8,41984
v_mul_u32_u24 v174, 0xffff, v174
v_mfma_f32_16x16x32_bf16 v[120:123], a[0:3], v[136:139], 0
v_add_u32 v174, 1, v174
v_mul_u32_u24 v174, 0x3f80, v174
Mp 120,4,152
Rb 52,8,43008
v_bfe_u32 v173, v0, 1, 3
Mp 112,4,144
Cv 96,96,97
Cv 97,98,99
Mp 112,8,160
Rb 56,8,44032
Cv 98,100,101
Mp 120,8,168
buffer_load_dwordx4 v2, s[20:23], s40 offen offset:2048 sc1 lds
Mp 120,12,184
Rb 60,8,45056
s_waitcnt lgkmcnt(8)
Mp 112,12,176
Cv 99,102,103
v_cmp_eq_u32_e32 vcc, 0, v173
v_and_b32 v167, 0xf0, v1
Mp 112,16,192
Rb 0,8,46080
)" R"(
v_cndmask_b32_e32 v132, 0, v174, vcc
Mp 120,16,200
s_add_u32 s64, s64, 0x10000
buffer_store_dwordx4 v[96:99], v2, s[20:23], s64 offen offset:0 sc0 sc1
Mp 120,20,216
Rb 4,8,47104
v_cmp_eq_u32_e32 vcc, 1, v173
Mp 112,20,208
buffer_load_dwordx4 v2, s[20:23], s40 offen offset:3072 sc1 lds
Mp 112,24,224
Rb 8,8,49152
v_cndmask_b32_e32 v133, 0, v174, vcc
Mp 120,24,232
v_cmp_eq_u32_e32 vcc, 2, v173
v_mul_u32_u24 v166, 56, v167
Mp 120,28,248
Rb 12,8,50176
s_waitcnt lgkmcnt(8)
Mp 112,28,240
Cv 104,104,105
Cv 105,106,107
v_mfma_f32_16x16x32_bf16 v[116:119], a[32:35], v[128:131], 0
Rb 16,8,51200
s_add_u32 m0, s36, 135168
v_mfma_f32_16x16x32_bf16 v[124:127], a[32:35], v[136:139], 0
buffer_load_dwordx4 v3, s[20:23], s40 offen offset:0 sc1 lds
Mp 124,36,152
Rb 20,8,52224
Cv 106,108,109
Mp 116,36,144
Cv 107,110,111
v_cndmask_b32_e32 v134, 0, v174, vcc
Mp 116,40,160
Rb 24,8,53248
v_cmp_eq_u32_e32 vcc, 3, v173
Mp 124,40,168
buffer_store_dwordx4 v[104:107], v2, s[20:23], s64 offen offset:1024 sc0 sc1
Mp 124,44,184
Rb 28,8,54272
s_waitcnt lgkmcnt(8)
Mp 116,44,176
buffer_load_dwordx4 v3, s[20:23], s40 offen offset:1024 sc1 lds
v_cndmask_b32_e32 v135, 0, v174, vcc
Mp 116,48,192
Rb 32,8,55296
v_cmp_eq_u32_e32 vcc, 4, v173
Mp 124,48,200
s_nop 0
v_cndmask_b32_e32 v140, 0, v174, vcc
v_cmp_eq_u32_e32 vcc, 5, v173
Mp 124,52,216
Rb 36,8,57344
v_cndmask_b32_e32 v141, 0, v174, vcc
Mp 116,52,208
v_cmp_eq_u32_e32 vcc, 6, v173
v_and_b32 v165, 0x80, v1
Mp 116,56,224
Rb 40,8,58368
v_cndmask_b32_e32 v142, 0, v174, vcc
Mp 124,56,232
buffer_load_dwordx4 v3, s[20:23], s40 offen offset:2048 sc1 lds
Mp 124,60,248
Rb 44,8,59392
s_waitcnt vmcnt(8) lgkmcnt(8)
)" R"(
buffer_store_dword v6, v7, s[20:23], s65 offen offset:0 sc0 sc1
Mp 116,60,240
v_cmp_eq_u32_e32 vcc, 7, v173
v_lshrrev_b32 v173, 4, v0
v_mfma_f32_16x16x32_bf16 v[32:35], a[0:3], v[128:131], 0
Rb 48,8,60416
v_cndmask_b32_e32 v143, 0, v174, vcc
v_mfma_f32_16x16x32_bf16 v[40:43], a[0:3], v[136:139], 0
v_lshl_or_b32 v164, v173, 16, v167
v_lshl_add_u32 v166, v173, 3, v166
Mp 40,4,152
Rb 52,8,61440
v_lshrrev_b32_e64 v173, v173, s85
Mp 32,4,144
Cv 112,112,113
Cv 113,114,115
Mp 32,8,160
Rb 56,8,62464
Cv 114,116,117
Mp 40,8,168
Cv 115,118,119
v_bfe_u32 v174, v173, 4, 1
Mp 40,12,184
Rb 60,8,63488
s_waitcnt lgkmcnt(8)
Mp 32,12,176
buffer_store_dwordx4 v[112:115], v2, s[20:23], s64 offen offset:2048 sc0 sc1
ds_write_b32 v9, v16 offset:39936
Mp 32,16,192
Rb 0,8,0
v_lshl_or_b32 v183, v174, 13, v164
Mp 40,16,200
v_and_b32 v174, 1, v173
v_lshl_or_b32 v164, v174, 13, v164
Mp 40,20,216
Rb 4,8,1024
v_and_b32 v174, 7, v0
Mp 32,20,208
v_lshl_or_b32 v165, v174, 8, v165
v_lshlrev_b32 v165, 1, v165
Mp 32,24,224
Rb 8,8,2048
s_mov_b32 s93, 0x1111
Mp 40,24,232
Cv 120,120,121
Cv 121,122,123
Mp 40,28,248
Rb 12,8,3072
s_waitcnt lgkmcnt(3)
s_barrier
Mp 32,28,240
Cv 122,124,125
Cv 123,126,127
ds_read_b32 v16, v8 offset:39936
v_mfma_f32_16x16x32_bf16 v[36:39], a[32:35], v[128:131], 0
Rb 16,8,4096
v_lshrrev_b32 v173, 2, v0
v_mfma_f32_16x16x32_bf16 v[44:47], a[32:35], v[136:139], 0
buffer_store_dwordx4 v[120:123], v2, s[20:23], s64 offen offset:3072 sc0 sc1
Mp 44,36,152
Rb 20,8,5120
v_mul_u32_u24 v173, 832, v173
Mp 36,36,144
v_add_u32 v173, v173, v1
v_lshrrev_b32 v174, 1, v0
)" R"(
Mp 36,40,160
Rb 24,8,6144
v_and_b32 v174, 24, v174
Mp 44,40,168
v_lshl_add_u32 v174, v167, 2, v174
s_add_u32 s59, s36, 98304
Mp 44,44,184
Rb 28,8,8192
s_waitcnt lgkmcnt(4)
Mp 36,44,176
v_readfirstlane_b32 s82, v16
v_add_u32 v174, s59, v174
Mp 36,48,192
Rb 32,8,9216
s_lshl2_add_u32 s56, s82, s33
Mp 44,48,200
s_mul_i32 s88, s56, 4682
s_lshr_b32 s58, s88, 16
Mp 44,52,216
Rb 36,8,10240
s_mul_i32 s57, s58, 14
Mp 36,52,208
s_sub_u32 s57, s56, s57
s_lshl_b32 s40, s58, 12
Mp 36,56,224
Rb 40,8,11264
Mp 44,56,232
s_cmp_gt_u32 s57, 6
s_cselect_b32 s88, 128, 0
Mp 44,60,248
Rb 44,8,12288
Mp 36,60,240
buffer_load_dword v4, v7, s[20:23], s52 offen offset:0 sc0 sc1
s_add_u32 s40, s40, s88
v_mfma_f32_16x16x32_bf16 v[48:51], a[0:3], v[128:131], 0
Rb 48,8,13312
s_add_u32 s40, s40, 0xc110000
v_mfma_f32_16x16x32_bf16 v[56:59], a[0:3], v[136:139], 0
s_lshl_b32 s55, s58, 10
s_add_u32 s55, s55, s57
Mp 56,4,152
Rb 52,8,14336
Mp 48,4,144
Cv 32,32,33
Cv 33,34,35
Mp 48,8,160
Rb 56,8,16384
Cv 34,36,37
Mp 56,8,168
Cv 35,38,39
Mp 56,12,184
Rb 60,8,17408
s_waitcnt lgkmcnt(8)
Mp 48,12,176
buffer_store_dwordx4 v[32:35], v3, s[20:23], s64 offen offset:0 sc0 sc1
Mp 48,16,192
Rb 0,8,18432
Mp 56,16,200
s_cmp_gt_u32 s57, 6
s_cselect_b32 s88, 9, 0
Mp 56,20,216
Rb 4,8,19456
s_add_u32 s55, s55, s88
Mp 48,20,208
)" R"(
s_and_b32 s88, s58, 2
s_lshl_b32 s88, s88, 12
Mp 48,24,224
Rb 8,8,20480
v_xor_b32 v164, s88, v164
Mp 56,24,232
Cv 40,40,41
Cv 41,42,43
Mp 56,28,248
Rb 12,8,21504
s_waitcnt lgkmcnt(8)
Mp 48,28,240
Cv 42,44,45
Cv 43,46,47
v_xor_b32 v183, s88, v183
v_mfma_f32_16x16x32_bf16 v[52:55], a[32:35], v[128:131], 0
Rb 16,8,22528
s_lshl_b32 s55, s55, 10
v_mfma_f32_16x16x32_bf16 v[60:63], a[32:35], v[136:139], 0
buffer_store_dwordx4 v[40:43], v3, s[20:23], s64 offen offset:1024 sc0 sc1
Mp 60,36,152
Rb 20,8,24576
s_add_u32 s55, s55, 0x8000000
Mp 52,36,144
s_and_b32 s88, s57, 1
s_lshl1_add_u32 s58, s58, s88
Mp 52,40,160
Rb 24,8,25600
s_lshl_b32 s88, s58, 8
Mp 60,40,168
v_add_u32 v167, s88, v167
s_mul_i32 s59, s58, 14336
Mp 60,44,184
Rb 28,8,26624
s_waitcnt lgkmcnt(8)
Mp 52,44,176
s_lshr_b32 s88, s57, 1
s_lshl_b32 s88, s88, 6
s_add_u32 s88, s88, s59
Mp 52,48,192
Rb 32,8,27648
v_add_u32 v166, s88, v166
Mp 60,48,200
s_mov_b32 s57, 0
s_add_u32 m0, s36, 98304
Mp 60,52,216
Rb 36,8,28672
Mp 52,52,208
buffer_load_dwordx4 v173, s[12:15], s88 offen offset:0 lds
Mp 52,56,224
Rb 40,8,29696
s_add_u32 m0, s36, 98880
Mp 60,56,232
buffer_load_dwordx4 v173, s[12:15], s88 offen offset:448 lds
Mp 60,60,248
Rb 44,8,30720
s_waitcnt lgkmcnt(8)
Mp 52,60,240
s_mov_b32 s88, 0xc100008
buffer_load_dword v167, v167, s[20:23], s88 offen sc0 sc1
v_mfma_f32_16x16x32_bf16 v[64:67], a[0:3], v[128:131], 0
Rb 48,1,0
v_mfma_f32_16x16x32_bf16 v[72:75], a[0:3], v[136:139], 0
Mp 72,4,152
)" R"(
Rb 52,1,1024
Mp 64,4,144
Mp 64,8,160
Rb 56,1,2048
Mp 72,8,168
Mp 72,12,184
Rb 60,1,3072
s_waitcnt vmcnt(5) lgkmcnt(8)
s_barrier
v_cmp_eq_u32_e32 vcc, s3, v4
s_andn2_b64 s[28:29], 1, vcc
s_cbranch_scc1 .Lzsp11
.Lzok11:
Mp 64,12,176
buffer_load_dwordx4 a[64:67], v2, s[20:23], s66 offen offset:0 sc0 sc1
Mp 64,16,192
Rb 0,1,4096
Mp 72,16,200
Mp 72,20,216
Rb 4,1,5120
Mp 64,20,208
buffer_load_dwordx4 a[68:71], v2, s[20:23], s66 offen offset:1024 sc0 sc1
Mp 64,24,224
Rb 8,1,6144
Mp 72,24,232
Mp 72,28,248
Rb 12,1,8192
s_waitcnt lgkmcnt(8)
Mp 64,28,240
buffer_load_dwordx4 a[72:75], v2, s[20:23], s66 offen offset:2048 sc0 sc1
v_mfma_f32_16x16x32_bf16 v[68:71], a[32:35], v[128:131], 0
Rb 16,1,9216
v_mfma_f32_16x16x32_bf16 v[76:79], a[32:35], v[136:139], 0
Mp 76,36,152
Rb 20,1,10240
Mp 68,36,144
buffer_load_dwordx4 a[76:79], v2, s[20:23], s66 offen offset:3072 sc0 sc1
Mp 68,40,160
Rb 24,1,11264
Mp 76,40,168
Mp 76,44,184
Rb 28,1,12288
s_waitcnt vmcnt(7) lgkmcnt(8)
buffer_store_dword v6, v7, s[20:23], s65 offen offset:128 sc0 sc1
Mp 68,44,176
buffer_load_dwordx4 a[80:83], v3, s[20:23], s66 offen offset:0 sc0 sc1
Mp 68,48,192
Rb 32,1,13312
Mp 76,48,200
Mp 76,52,216
Rb 36,1,14336
Mp 68,52,208
buffer_load_dwordx4 a[84:87], v3, s[20:23], s66 offen offset:1024 sc0 sc1
Mp 68,56,224
Rb 40,1,16384
Mp 76,56,232
Mp 76,60,248
Rb 44,1,17408
s_waitcnt lgkmcnt(8)
Mp 68,60,240
buffer_load_dwordx4 a[88:91], v3, s[20:23], s66 offen offset:2048 sc0 sc1
v_mfma_f32_16x16x32_bf16 v[80:83], a[0:3], v[128:131], 0
Rb 48,1,18432
v_mfma_f32_16x16x32_bf16 v[88:91], a[0:3], v[136:139], 0
Mp 88,4,152
)" R"(
Rb 52,1,19456
Mp 80,4,144
buffer_load_dwordx4 a[92:95], v3, s[20:23], s66 offen offset:3072 sc0 sc1
Mp 80,8,160
Rb 56,1,20480
Mp 88,8,168
Mp 88,12,184
Rb 60,1,21504
s_waitcnt lgkmcnt(8)
Mp 80,12,176
Mp 80,16,192
Rb 0,1,22528
Mp 88,16,200
Mp 88,20,216
Rb 4,1,24576
Mp 80,20,208
Mp 80,24,224
Rb 8,1,25600
Mp 88,24,232
Mp 88,28,248
Rb 12,1,26624
s_waitcnt lgkmcnt(8)
Mp 80,28,240
v_mfma_f32_16x16x32_bf16 v[84:87], a[32:35], v[128:131], 0
Rb 16,1,27648
v_mfma_f32_16x16x32_bf16 v[92:95], a[32:35], v[136:139], 0
Mp 92,36,152
Rb 20,1,28672
Mp 84,36,144
Mp 84,40,160
Rb 24,1,29696
Mp 92,40,168
Mp 92,44,184
Rb 28,1,30720
s_waitcnt lgkmcnt(8)
Mp 84,44,176
buffer_load_dword v4, v7, s[20:23], s52 offen offset:128 sc0 sc1
Mp 84,48,192
Rb 32,1,32768
Mp 92,48,200
Mp 92,52,216
Rb 36,1,33792
Mp 84,52,208
Mp 84,56,224
Rb 40,1,34816
Mp 92,56,232
Mp 92,60,248
Rb 44,1,35840
s_waitcnt vmcnt(6)
s_barrier
Mp 84,60,240
Mv 48,194,64
Mv 56,194,68
Mv 52,192,64
Mv 60,192,68
Mv 64,194,72
Mv 72,194,76
Mv 68,192,72
s_waitcnt vmcnt(3) lgkmcnt(10)
Mv 76,192,76
Cv 48,48,49
Cv 49,50,51
Mv 80,194,80
Cv 50,52,53
Cv 51,54,55
)" R"(
Mv 88,194,84
s_add_u32 s29, s73, 0x0
buffer_store_dwordx4 v[48:51], v1, s[20:23], s29 offen sc0 sc1
Mv 84,192,80
Mv 92,192,84
v_mfma_f32_16x16x32_bf16 v[96:99], a[0:3], v[128:131], 0
Rb 48,1,36864
v_mfma_f32_16x16x32_bf16 v[104:107], a[0:3], v[136:139], 0
Mp 104,4,152
Rb 52,1,37888
s_waitcnt lgkmcnt(8)
Mp 96,4,144
Cv 56,56,57
Cv 57,58,59
ds_read_b64 v[188:189], v174 offset:0
Mp 96,8,160
Rb 56,1,38912
Cv 58,60,61
Mp 104,8,168
Cv 59,62,63
ds_read_b64 v[190:191], v174 offset:32
Mp 104,12,184
Rb 60,1,40960
Mp 96,12,176
s_add_u32 s29, s73, 0x400
buffer_store_dwordx4 v[56:59], v1, s[20:23], s29 offen sc0 sc1
ds_read_b64 v[196:197], v174 offset:1024
Mp 96,16,192
Rb 0,1,41984
Mp 104,16,200
ds_read_b64 v[198:199], v174 offset:1056
Mp 104,20,216
Rb 4,1,43008
s_waitcnt lgkmcnt(12)
Mp 96,20,208
Mp 96,24,224
Rb 8,1,44032
Mp 104,24,232
Cv 64,64,65
Cv 65,66,67
Mp 104,28,248
Rb 12,1,45056
Cv 66,68,69
Mp 96,28,240
Cv 67,70,71
v_mfma_f32_16x16x32_bf16 v[100:103], a[32:35], v[128:131], 0
Rb 16,1,46080
v_mfma_f32_16x16x32_bf16 v[108:111], a[32:35], v[136:139], 0
s_add_u32 s29, s73, 0x800
buffer_store_dwordx4 v[64:67], v1, s[20:23], s29 offen sc0 sc1
Mp 108,36,152
Rb 20,1,47104
s_waitcnt vmcnt(3) lgkmcnt(11)
v_cmp_eq_u32_e32 vcc, s3, v4
s_andn2_b64 s[28:29], 1, vcc
s_cbranch_scc1 .Lzsp12
.Lzok12:
Mp 100,36,144
s_add_u32 s66, s66, 0x10000
buffer_load_dwordx4 a[96:99], v2, s[20:23], s66 offen offset:0 sc0 sc1
Mp 100,40,160
Rb 24,1,49152
v_sub_f32_e32 v172, 1.0, v167
Mp 108,40,168
)" R"(
Cv 172,172,172
v_lshlrev_b32 v172, 16, v172
Mp 108,44,184
Rb 28,1,50176
v_lshlrev_b32 v204, 16, v188
Mp 100,44,176
buffer_load_dwordx4 a[100:103], v2, s[20:23], s66 offen offset:1024 sc0 sc1
Mp 100,48,192
Rb 32,1,51200
Cv 72,72,73
Mp 108,48,200
Cv 73,74,75
Cv 74,76,77
Mp 108,52,216
Rb 36,1,52224
s_waitcnt lgkmcnt(12)
Mp 100,52,208
buffer_load_dwordx4 a[104:107], v2, s[20:23], s66 offen offset:2048 sc0 sc1
Mp 100,56,224
Rb 40,1,53248
Cv 75,78,79
Mp 108,56,232
s_add_u32 s29, s73, 0xc00
buffer_store_dwordx4 v[72:75], v1, s[20:23], s29 offen sc0 sc1
Mp 108,60,248
Rb 44,1,54272
v_and_b32 v205, 0xffff0000, v188
Mp 100,60,240
buffer_load_dwordx4 a[108:111], v2, s[20:23], s66 offen offset:3072 sc0 sc1
Mv 96,194,88
v_mul_f32_e32 v204, v172, v204
v_mul_f32_e32 v205, v172, v205
Mv 104,194,92
Cv 205,204,205
v_lshlrev_b32 v204, 16, v205
Mv 100,192,88
v_and_b32 v205, 0xffff0000, v205
s_waitcnt lgkmcnt(8)
Mv 108,192,92
buffer_load_dwordx4 a[112:115], v3, s[20:23], s66 offen offset:0 sc0 sc1
v_mfma_f32_16x16x32_bf16 v[112:115], a[0:3], v[128:131], 0
Rb 48,1,55296
Cv 80,80,81
v_mfma_f32_16x16x32_bf16 v[120:123], a[0:3], v[136:139], 0
Cv 81,82,83
Cv 82,84,85
Mp 120,4,152
Rb 52,1,57344
Cv 83,86,87
Mp 112,4,144
buffer_load_dwordx4 a[116:119], v3, s[20:23], s66 offen offset:1024 sc0 sc1
Mp 112,8,160
Rb 56,1,58368
v_lshlrev_b32 v206, 16, v189
Mp 120,8,168
s_add_u32 s29, s73, 0x1000
buffer_store_dwordx4 v[80:83], v1, s[20:23], s29 offen sc0 sc1
Mp 120,12,184
Rb 60,1,59392
s_waitcnt lgkmcnt(8)
Mp 112,12,176
v_and_b32 v207, 0xffff0000, v189
v_mul_f32_e32 v206, v172, v206
Mp 112,16,192
)" R"(
Rb 0,1,60416
v_mul_f32_e32 v207, v172, v207
Mp 120,16,200
Cv 207,206,207
v_lshlrev_b32 v206, 16, v207
Mp 120,20,216
Rb 4,1,61440
v_and_b32 v207, 0xffff0000, v207
Mp 112,20,208
Cv 88,88,89
Cv 89,90,91
Mp 112,24,224
Rb 8,1,62464
Cv 90,92,93
Mp 120,24,232
Cv 91,94,95
v_lshlrev_b32 v212, 16, v190
Mp 120,28,248
Rb 12,1,63488
s_waitcnt lgkmcnt(8)
Mp 112,28,240
s_add_u32 s29, s73, 0x1400
buffer_store_dwordx4 v[88:91], v1, s[20:23], s29 offen sc0 sc1
v_mfma_f32_16x16x32_bf16 v[116:119], a[32:35], v[128:131], 0
Rb 16,10,0
v_and_b32 v213, 0xffff0000, v190
v_mfma_f32_16x16x32_bf16 v[124:127], a[32:35], v[136:139], 0
v_mul_f32_e32 v212, v172, v212
v_mul_f32_e32 v213, v172, v213
Mp 124,36,152
Rb 20,10,1024
Cv 213,212,213
Mp 116,36,144
v_lshlrev_b32 v212, 16, v213
v_and_b32 v213, 0xffff0000, v213
Mp 116,40,160
Rb 24,10,2048
v_lshlrev_b32 v214, 16, v191
Mp 124,40,168
Cv 96,96,97
Cv 97,98,99
Mp 124,44,184
Rb 28,10,3072
s_waitcnt lgkmcnt(8)
Mp 116,44,176
Cv 98,100,101
Cv 99,102,103
Mp 116,48,192
Rb 32,10,4096
v_and_b32 v215, 0xffff0000, v191
Mp 124,48,200
s_add_u32 s29, s73, 0x1800
buffer_store_dwordx4 v[96:99], v1, s[20:23], s29 offen sc0 sc1
Mp 124,52,216
Rb 36,10,5120
v_mul_f32_e32 v214, v172, v214
Mp 116,52,208
v_mul_f32_e32 v215, v172, v215
Cv 215,214,215
Mp 116,56,224
Rb 40,10,6144
v_lshlrev_b32 v214, 16, v215
Mp 124,56,232
v_and_b32 v215, 0xffff0000, v215
)" R"(
v_lshlrev_b32 v220, 16, v196
Mp 124,60,248
Rb 44,10,8192
s_waitcnt vmcnt(8) lgkmcnt(10)
Mp 116,60,240
Cv 104,104,105
Cv 105,106,107
Mv 112,194,96
Cv 106,108,109
Cv 107,110,111
Mv 120,194,100
s_add_u32 s29, s73, 0x4000
buffer_store_dwordx4 v[104:107], v1, s[20:23], s29 offen sc0 sc1
Mv 116,192,96
v_and_b32 v221, 0xffff0000, v196
v_mul_f32_e32 v220, v172, v220
Mv 124,192,100
v_mul_f32_e32 v221, v172, v221
Cv 221,220,221
v_mfma_f32_16x16x32_bf16 v[32:35], a[0:3], v[128:131], 0
Rb 48,10,9216
v_lshlrev_b32 v220, 16, v221
v_mfma_f32_16x16x32_bf16 v[40:43], a[0:3], v[136:139], 0
v_and_b32 v221, 0xffff0000, v221
v_lshlrev_b32 v222, 16, v197
Mp 40,4,152
Rb 52,10,10240
s_waitcnt lgkmcnt(8)
Mp 32,4,144
Cv 112,112,113
Cv 113,114,115
Mp 32,8,160
Rb 56,10,11264
Cv 114,116,117
Mp 40,8,168
Cv 115,118,119
v_and_b32 v223, 0xffff0000, v197
Mp 40,12,184
Rb 60,10,12288
v_mul_f32_e32 v222, v172, v222
Mp 32,12,176
s_add_u32 s29, s73, 0x4400
buffer_store_dwordx4 v[112:115], v1, s[20:23], s29 offen sc0 sc1
Mp 32,16,192
Rb 0,10,13312
v_mul_f32_e32 v223, v172, v223
Mp 40,16,200
Cv 223,222,223
v_lshlrev_b32 v222, 16, v223
Mp 40,20,216
Rb 4,10,14336
s_waitcnt lgkmcnt(8)
Mp 32,20,208
v_and_b32 v223, 0xffff0000, v223
v_lshlrev_b32 v228, 16, v198
Mp 32,24,224
Rb 8,10,16384
v_and_b32 v229, 0xffff0000, v198
Mp 40,24,232
Cv 120,120,121
Cv 121,122,123
Mp 40,28,248
Rb 12,10,17408
Cv 122,124,125
)" R"(
Mp 32,28,240
Cv 123,126,127
v_mul_f32_e32 v228, v172, v228
v_mfma_f32_16x16x32_bf16 v[36:39], a[32:35], v[128:131], 0
Rb 16,10,18432
v_mul_f32_e32 v229, v172, v229
v_mfma_f32_16x16x32_bf16 v[44:47], a[32:35], v[136:139], 0
s_add_u32 s29, s73, 0x4800
buffer_store_dwordx4 v[120:123], v1, s[20:23], s29 offen sc0 sc1
Mp 44,36,152
Rb 20,10,19456
s_waitcnt lgkmcnt(8)
Mp 36,36,144
Cv 229,228,229
v_lshlrev_b32 v228, 16, v229
Mp 36,40,160
Rb 24,10,20480
v_and_b32 v229, 0xffff0000, v229
Mp 44,40,168
v_lshlrev_b32 v230, 16, v199
v_and_b32 v231, 0xffff0000, v199
Mp 44,44,184
Rb 28,10,21504
v_mul_f32_e32 v230, v172, v230
Mp 36,44,176
v_mul_f32_e32 v231, v172, v231
Cv 231,230,231
Mp 36,48,192
Rb 32,10,22528
v_lshlrev_b32 v230, 16, v231
Mp 44,48,200
v_and_b32 v231, 0xffff0000, v231
Mp 44,52,216
Rb 36,10,24576
s_waitcnt vmcnt(3) lgkmcnt(10)
buffer_store_dword v6, v7, s[20:23], s68 offen offset:0 sc0 sc1
Mp 36,52,208
Mp 36,56,224
Rb 40,10,25600
Mp 44,56,232
Mp 44,60,248
Rb 44,10,26624
Mp 36,60,240
Mv 32,194,104
Mv 40,194,108
Mv 36,192,104
s_waitcnt lgkmcnt(8)
Mv 44,192,108
v_mfma_f32_16x16x32_bf16 v[48:51], a[0:3], v[128:131], 0
Rb 48,10,27648
v_mfma_f32_16x16x32_bf16 v[56:59], a[0:3], v[136:139], 0
Mp 56,4,152
Rb 52,10,28672
Mp 48,4,144
Cv 32,32,33
Cv 33,34,35
Mp 48,8,160
Rb 56,10,29696
Cv 34,36,37
Mp 56,8,168
Cv 35,38,39
Mp 56,12,184
Rb 60,10,30720
s_waitcnt lgkmcnt(8)
)" R"(
Mp 48,12,176
s_add_u32 s29, s73, 0x4c00
buffer_store_dwordx4 v[32:35], v1, s[20:23], s29 offen sc0 sc1
Mp 48,16,192
Mp 56,16,200
Mp 56,20,216
Mp 48,20,208
Mp 48,24,224
Mp 56,24,232
Cv 40,40,41
Cv 41,42,43
buffer_store_dword v6, v7, s[20:23], s68 offen offset:128 sc0 sc1
Mp 56,28,248
Cv 42,44,45
s_waitcnt lgkmcnt(4)
Mp 48,28,240
Cv 43,46,47
v_mfma_f32_16x16x32_bf16 v[52:55], a[32:35], v[128:131], 0
s_add_u32 s29, s73, 0x5000
buffer_store_dwordx4 v[40:43], v1, s[20:23], s29 offen sc0 sc1
v_mfma_f32_16x16x32_bf16 v[60:63], a[32:35], v[136:139], 0
buffer_load_dword v4, v165, s[20:23], s40 offen sc0 sc1
Mp 60,36,152
Mp 52,36,144
Mp 52,40,160
Mp 60,40,168
Mp 60,44,184
s_waitcnt lgkmcnt(0)
Mp 52,44,176
Mp 52,48,192
Mp 60,48,200
Mp 60,52,216
Mp 52,52,208
Mp 52,56,224
Mp 60,56,232
Mp 60,60,248
Mp 52,60,240
Mv 48,194,112
Mv 56,194,116
Mv 52,192,112
Mv 60,192,116
s_nop 7
Cv 48,48,49
Cv 49,50,51
Cv 50,52,53
Cv 51,54,55
s_add_u32 s29, s73, 0x5400
buffer_store_dwordx4 v[48:51], v1, s[20:23], s29 offen sc0 sc1
Cv 56,56,57
Cv 57,58,59
Cv 58,60,61
Cv 59,62,63
s_add_u32 s29, s73, 0x5800
buffer_store_dwordx4 v[56:59], v1, s[20:23], s29 offen sc0 sc1
s_branch .LzEND
.LzCEND:
)" : "+{s[0:1]}"(ka), "+{s2}"(bid), "+{v0}"(tid) :
   : "memory", "scc", "vcc", "v1", "v2", "v3", "v4", "v5", "v6", "v7", "v8", "v9", R256("v"), V10("a"), R256("a"),
     "s3", "s4", "s5", "s6", "s7", "s8", "s9", V10("s1"), V10("s2"), "s30", "s31", "s33", "s34", "s35", "s36", "s37", "s38", "s39",
     V10("s4"), V10("s5"), V10("s6"), V10("s7"), V10("s8"), V10("s9"));
}

extern "C" void kernel_launch(void* const* d_in, const int* in_sizes, int n_in, void* d_out, int out_size, void* d_ws, size_t ws_size, hipStream_t stream) {
  (void)hipFuncSetAttribute((const void*)k_main, hipFuncAttributeMaxDynamicSharedMemorySize, 163840);
  k_main<<<256, 256, 163840, stream>>>((const u16*)d_in[0], (const u16*)d_in[1], (const u16*)d_in[2], (const u16*)d_in[3], (const u16*)d_in[4],
      (char*)d_ws, (const u16*)d_in[11], (const u16*)d_in[7], (const u16*)d_in[5], (const u16*)d_in[8], (const u16*)d_in[9], (const u16*)d_in[10], (u16*)d_out);
}
```

```cpp
#include <hip/hip_runtime.h>

typedef unsigned short u16;
#define V10(p) p "0", p "1", p "2", p "3", p "4", p "5", p "6", p "7", p "8", p "9"
#define V100(p) V10(p "0"), V10(p "1"), V10(p "2"), V10(p "3"), V10(p "4"), V10(p "5"), V10(p "6"), V10(p "7"), V10(p "8"), V10(p "9")
#define R256(p) V10(p "1"), V10(p "2"), V10(p "3"), V10(p "4"), V10(p "5"), V10(p "6"), V10(p "7"), V10(p "8"), V10(p "9"), V100(p "1"), \
                V10(p "20"), V10(p "21"), V10(p "22"), V10(p "23"), V10(p "24"), p "250", p "251", p "252", p "253", p "254", p "255"

extern "C" __global__ __launch_bounds__(256, 1)
void k_main(const u16* x, const u16* ckv, const u16* wq, const u16* wuk, const u16* wuv, char* ws,
            const u16* gate_w, const u16* w_r, const u16* wo, const u16* wg, const u16* wu, const u16* wd, u16* out) {
  typedef const __attribute__((address_space(4))) char* karg_t;
  karg_t ka = (karg_t)__builtin_amdgcn_kernarg_segment_ptr();
  unsigned bid = blockIdx.x, tid = threadIdx.x;
  asm volatile(R"(
.macro Mv d,a,b
v_mfma_f32_16x16x32_bf16 v[\d:\d+3], a[\a:\a+3], a[\b:\b+3], v[\d:\d+3]
.endm
.macro Mp d,a,b
v_mfma_f32_16x16x32_bf16 v[\d:\d+3], a[\a:\a+3], v[\b:\b+3], v[\d:\d+3]
.endm
.macro Ma d,a,b
v_mfma_f32_16x16x32_bf16 a[\d:\d+3], a[\a:\a+3], a[\b:\b+3], a[\d:\d+3]
.endm
.macro Mz d,a,b
v_mfma_f32_16x16x32_bf16 v[\d:\d+3], a[\a:\a+3], a[\b:\b+3], 0
.endm
.macro Rb d,v,o
ds_read_b128 a[\d:\d+3], v\v offset:\o
.endm
.macro Rt d,v,o
ds_read_b64_tr_b16 a[\d:\d+1], v\v offset:\o
.endm
.macro Cv d,a,b
v_cvt_pk_bf16_f32 v\d, v\a, v\b
.endm
.LzSTART:
s_load_dwordx16 s[64:79], s[0:1], 0
s_load_dwordx2 s[80:81], s[0:1], 64
v_lshrrev_b32 v1, 6, v0
v_and_b32 v0, 63, v0
v_readfirstlane_b32 s33, v1
v_lshlrev_b32 v1, 4, v0
s_getpc_b64 s[24:25]
.Lzpc0:
s_sub_u32 s24, s24, .Lzpc0-.LzSTART
s_subb_u32 s25, s25, 0
s_mov_b32 s26, .LzCEND-.LzSTART
s_mov_b32 s27, 0x20000
s_lshr_b32 s28, s2, 3
s_lshl2_add_u32 s28, s28, s33
s_lshl_b32 s28, s28, 11
v_lshrrev_b32 v5, 4, v0
v_min_u32 v5, 1, v5
v_lshlrev_b32 v5, 27, v5
v_add_lshl_u32 v5, v5, v1, 3
buffer_load_dword v5, v5, s[24:27], s28 offen
s_and_b32 s28, s2, 0x17
s_bfe_u32 s29, s2, 0x10005
s_lshl3_add_u32 s28, s29, s28
s_lshr_b32 s29, s2, 6
s_lshl_b32 s29, s29, 5
s_or_b32 s28, s28, s29
s_and_b32 s29, s2, 8
s_lshl4_add_u32 s2, s29, s28
s_and_b32 s28, s2, 7
s_bfe_u32 s29, s2, 0x10003
s_lshl1_add_u32 s34, s28, s29
s_bfe_u32 s35, s2, 0x30004
s_bfe_u32 s91, s2, 0x10004
s_bfe_u32 s93, s2, 0x20005
s_lshl_b32 s36, s33, 13
s_mov_b32 s95, 40
s_bitcmp1_b32 s2, 7
s_cbranch_scc0 .LzA0
.LzB0:
s_mov_b32 s54, 0x3db8aa3b
s_mul_i32 s41, s33, 7168
s_mul_i32 s42, s33, 0x70000
s_lshl_b32 s28, s34, 9
s_add_u32 s42, s42, s28
v_lshrrev_b32 v28, 3, v0
v_and_b32 v29, 0x70, v1
v_lshl_or_b32 v28, v28, 13, v29
v_lshlrev_b32 v29, 1, v0
v_and_b32 v29, 32, v29
v_xor_b32 v28, v28, v29
s_and_b32 s28, s33, 1
s_lshl_b32 s28, s28, 6
v_xor_b32 v240, s28, v28
v_xor_b32 v241, 64, v240
v_add_u32 v241, 0xfc00, v241
v_add_u32 v242, 0x1f800, v240
v_add_u32 v243, 0x1f800, v241
s_sub_u32 s28, 15, s35
s_lshl2_add_u32 s28, s28, s33
s_mul_i32 s43, s28, 28672
v_and_b32 v2, 0x70, v1
v_and_b32 v28, 48, v0
)" R"(
v_xor_b32 v2, v2, v28
v_lshrrev_b32 v28, 3, v0
v_mul_u32_u24 v28, 896, v28
v_add_u32 v2, v2, v28
v_xor_b32 v3, 64, v2
v_add_u32 v3, 6144, v3
v_bfe_u32 v26, v0, 1, 3
v_lshrrev_b32 v28, 4, v0
v_xor_b32 v26, v26, v28
v_lshlrev_b32 v26, 4, v26
v_and_b32 v28, 15, v0
v_lshl_or_b32 v26, v28, 7, v26
s_add_u32 s28, s36, 0x0
v_add_u32 v26, s28, v26
v_xor_b32 v27, 64, v26
s_waitcnt lgkmcnt(0)
s_mov_b32 s16, s68
s_mov_b32 s17, s69
s_mov_b32 s18, 0x380000
s_mov_b32 s19, 0x20000
s_mov_b32 s12, s64
s_mov_b32 s13, s65
s_mov_b32 s14, 0x1c0000
s_mov_b32 s15, 0x20000
s_add_u32 m0, s36, 0
s_add_u32 s44, s43, 0
buffer_load_dwordx4 v2, s[12:15], s44 offen offset:0 lds
buffer_load_dwordx4 v3, s[12:15], s44 offen offset:1024 lds
s_add_u32 s44, s43, 12288
buffer_load_dwordx4 v2, s[12:15], s44 offen offset:2048 lds
buffer_load_dwordx4 v3, s[12:15], s44 offen offset:3072 lds
s_add_u32 m0, s41, 65536
s_add_u32 s92, s42, 0x0
buffer_load_dwordx4 v240, s[16:19], s92 offen offset:0 lds
buffer_load_dwordx4 v241, s[16:19], s92 offen offset:1024 lds
buffer_load_dwordx4 v242, s[16:19], s92 offen offset:2048 lds
buffer_load_dwordx4 v243, s[16:19], s92 offen offset:3072 lds
s_add_u32 m0, s41, 69632
s_add_u32 s92, s42, 0x40000
buffer_load_dwordx4 v240, s[16:19], s92 offen offset:0 lds
buffer_load_dwordx4 v241, s[16:19], s92 offen offset:1024 lds
buffer_load_dwordx4 v242, s[16:19], s92 offen offset:2048 lds
s_add_u32 m0, s36, 4096
s_add_u32 s44, s43, 128
buffer_load_dwordx4 v2, s[12:15], s44 offen offset:0 lds
buffer_load_dwordx4 v3, s[12:15], s44 offen offset:1024 lds
s_add_u32 s44, s43, 12416
buffer_load_dwordx4 v2, s[12:15], s44 offen offset:2048 lds
buffer_load_dwordx4 v3, s[12:15], s44 offen offset:3072 lds
s_add_u32 m0, s36, 32768
s_add_u32 s44, s43, 256
buffer_load_dwordx4 v2, s[12:15], s44 offen offset:0 lds
buffer_load_dwordx4 v3, s[12:15], s44 offen offset:1024 lds
s_add_u32 s44, s43, 12544
buffer_load_dwordx4 v2, s[12:15], s44 offen offset:2048 lds
buffer_load_dwordx4 v3, s[12:15], s44 offen offset:3072 lds
s_add_u32 m0, s36, 36864
s_add_u32 s44, s43, 384
buffer_load_dwordx4 v2, s[12:15], s44 offen offset:0 lds
buffer_load_dwordx4 v3, s[12:15], s44 offen offset:1024 lds
s_add_u32 s44, s43, 12672
buffer_load_dwordx4 v2, s[12:15], s44 offen offset:2048 lds
buffer_load_dwordx4 v3, s[12:15], s44 offen offset:3072 lds
s_mov_b32 s28, 0xc108400
)" R"(
s_load_dword s3, s[74:75], s28
s_mov_b32 s20, s74
s_mov_b32 s21, s75
s_mov_b32 s22, 0xc1ba000
s_mov_b32 s23, 0x20000
s_mov_b32 s4, s66
s_mov_b32 s5, s67
s_mov_b32 s6, 0x400000
s_mov_b32 s7, 0x20000
s_cmp_eq_u32 s91, 1
s_cselect_b32 s8, s72, s70
s_cselect_b32 s9, s73, s71
s_mov_b32 s10, 0x800000
s_mov_b32 s11, 0x20000
s_load_dwordx2 s[74:75], s[0:1], 96
s_lshl3_add_u32 s28, s33, 64
s_load_dwordx2 s[70:71], s[0:1], s28
v_bfe_u32 v244, v0, 3, 2
v_lshlrev_b32 v244, 5, v244
v_and_b32 v28, 3, v0
v_lshl_or_b32 v244, v28, 3, v244
v_bfe_u32 v28, v0, 2, 2
v_lshl_or_b32 v244, v28, 7, v244
v_lshrrev_b32 v28, 4, v0
v_lshl_or_b32 v244, v28, 10, v244
v_xor_b32 v245, 32, v244
v_xor_b32 v246, 64, v244
v_xor_b32 v247, 96, v244
v_add_u32 v252, 0x20000, v244
v_add_u32 v253, 0x20000, v245
v_add_u32 v254, 0x20000, v246
v_add_u32 v255, 0x20000, v247
v_add_u32 v248, 0x10000, v244
v_add_u32 v249, 0x10000, v245
v_add_u32 v250, 0x10000, v246
v_add_u32 v251, 0x10000, v247
s_waitcnt vmcnt(8) lgkmcnt(0)
s_barrier
ds_read_b128 v[128:131], v26 offset:0
ds_read_b128 v[132:135], v26 offset:2048
ds_read_b128 v[136:139], v27 offset:0
ds_read_b128 v[140:143], v27 offset:2048
ds_read_b128 v[144:147], v26 offset:4096
ds_read_b128 v[148:151], v26 offset:6144
ds_read_b128 v[152:155], v27 offset:4096
ds_read_b128 v[156:159], v27 offset:6144
Rt 144,248,0
Rt 146,248,512
Rt 148,249,0
Rt 150,249,512
Rt 152,250,0
Rt 154,250,512
Rt 156,251,0
Rt 158,251,512
Rt 160,248,4096
Rt 162,248,4608
Rt 164,249,4096
Rt 166,249,4608
Rt 168,250,4096
Rt 170,250,4608
Rt 172,251,4096
Rt 174,251,4608
s_add_u32 m0, s41, 98304
s_add_u32 s92, s42, 0x80
buffer_load_dwordx4 v240, s[16:19], s92 offen offset:0 lds
)" R"(
buffer_load_dwordx4 v241, s[16:19], s92 offen offset:1024 lds
buffer_load_dwordx4 v242, s[16:19], s92 offen offset:2048 lds
buffer_load_dwordx4 v243, s[16:19], s92 offen offset:3072 lds
s_add_u32 m0, s41, 102400
s_add_u32 s92, s42, 0x40080
buffer_load_dwordx4 v240, s[16:19], s92 offen offset:0 lds
buffer_load_dwordx4 v241, s[16:19], s92 offen offset:1024 lds
buffer_load_dwordx4 v242, s[16:19], s92 offen offset:2048 lds
s_mul_i32 s3, s3, 1664525
s_add_u32 s3, s3, 1013904223
v_mov_b32 v6, s3
v_min_u32 v7, 1, v0
v_lshlrev_b32 v7, 30, v7
s_waitcnt lgkmcnt(8)
s_barrier
v_mfma_f32_16x16x32_bf16 v[32:35], a[144:147], v[128:131], 0
Rt 128,248,8192
s_add_u32 m0, s41, 131072
s_add_u32 s92, s42, 0x1c0000
v_mfma_f32_16x16x32_bf16 v[40:43], a[144:147], v[132:135], 0
Rt 130,248,8704
buffer_load_dwordx4 v240, s[16:19], s92 offen offset:0 lds
v_mfma_f32_16x16x32_bf16 v[44:47], a[148:151], v[132:135], 0
Rt 132,249,8192
v_mfma_f32_16x16x32_bf16 v[36:39], a[148:151], v[128:131], 0
Rt 134,249,8704
v_mfma_f32_16x16x32_bf16 v[48:51], a[152:155], v[128:131], 0
Rt 136,250,8192
v_mfma_f32_16x16x32_bf16 v[56:59], a[152:155], v[132:135], 0
Rt 138,250,8704
buffer_load_dwordx4 v241, s[16:19], s92 offen offset:1024 lds
v_mfma_f32_16x16x32_bf16 v[60:63], a[156:159], v[132:135], 0
Rt 140,251,8192
v_mfma_f32_16x16x32_bf16 v[52:55], a[156:159], v[128:131], 0
Rt 142,251,8704
s_waitcnt vmcnt(13) lgkmcnt(8)
Mp 32,160,136
Rt 144,248,12288
ds_read_b128 v[160:163], v26 offset:32768
Mp 40,160,140
Rt 146,248,12800
buffer_load_dwordx4 v242, s[16:19], s92 offen offset:2048 lds
Mp 44,164,140
Rt 148,249,12288
ds_read_b128 v[164:167], v26 offset:34816
Mp 36,164,136
Rt 150,249,12800
Mp 48,168,136
Rt 152,250,12288
ds_read_b128 v[168:171], v27 offset:32768
Mp 56,168,140
Rt 154,250,12800
buffer_load_dwordx4 v243, s[16:19], s92 offen offset:3072 lds
Mp 60,172,140
Rt 156,251,12288
ds_read_b128 v[172:175], v27 offset:34816
Mp 52,172,136
Rt 158,251,12800
s_waitcnt vmcnt(11) lgkmcnt(12)
Mp 32,128,144
Rt 160,248,16384
ds_read_b128 v[176:179], v26 offset:36864
s_add_u32 m0, s41, 135168
s_add_u32 s92, s42, 0x200000
Mp 40,128,148
)" R"(
Rt 162,248,16896
buffer_load_dwordx4 v240, s[16:19], s92 offen offset:0 lds
Mp 44,132,148
Rt 164,249,16384
ds_read_b128 v[180:183], v26 offset:38912
Mp 36,132,144
Rt 166,249,16896
Mp 48,136,144
Rt 168,250,16384
ds_read_b128 v[184:187], v27 offset:36864
Mp 56,136,148
Rt 170,250,16896
buffer_load_dwordx4 v241, s[16:19], s92 offen offset:1024 lds
Mp 60,140,148
Rt 172,251,16384
ds_read_b128 v[188:191], v27 offset:38912
Mp 52,140,144
Rt 174,251,16896
s_waitcnt lgkmcnt(12)
Mp 32,144,152
Rt 128,248,20480
Mp 40,144,156
Rt 130,248,20992
buffer_load_dwordx4 v242, s[16:19], s92 offen offset:2048 lds
Mp 44,148,156
Rt 132,249,20480
Mp 36,148,152
Rt 134,249,20992
Mp 48,152,152
Rt 136,250,20480
Mp 56,152,156
Rt 138,250,20992
Mp 60,156,156
Rt 140,251,20480
s_add_u32 s44, s43, 512
Mp 52,156,152
Rt 142,251,20992
s_waitcnt lgkmcnt(8)
Mp 32,160,160
Rt 144,248,24576
s_add_u32 m0, s36, 32768
Mp 40,160,164
Rt 146,248,25088
buffer_load_dwordx4 v2, s[12:15], s44 offen offset:0 lds
Mp 44,164,164
Rt 148,249,24576
Mp 36,164,160
Rt 150,249,25088
Mp 48,168,160
Rt 152,250,24576
Mp 56,168,164
Rt 154,250,25088
buffer_load_dwordx4 v3, s[12:15], s44 offen offset:1024 lds
Mp 60,172,164
Rt 156,251,24576
Mp 52,172,160
Rt 158,251,25088
s_waitcnt vmcnt(9) lgkmcnt(8)
s_barrier
Mp 32,128,168
Rt 160,248,32768
s_mov_b32 s63, 0xe0c0a08
s_mov_b32 s28, 0x100d0b09
Mp 40,128,172
Rt 162,248,33280
)" R"(
s_add_u32 s44, s43, 12800
buffer_load_dwordx4 v2, s[12:15], s44 offen offset:2048 lds
Mp 44,132,172
Rt 164,249,32768
Mp 36,132,168
Rt 166,249,33280
Mp 48,136,168
Rt 168,250,32768
s_bitcmp1_b32 s2, 5
s_cmov_b32 s63, 0xf1e1c1a
s_cmov_b32 s28, 0x111f1d1b
Mp 56,136,172
Rt 170,250,33280
buffer_load_dwordx4 v3, s[12:15], s44 offen offset:3072 lds
Mp 60,140,172
Rt 172,251,32768
s_add_u32 s44, s43, 640
Mp 52,140,168
Rt 174,251,33280
s_waitcnt lgkmcnt(8)
s_barrier
Mp 32,144,176
Rt 128,248,36864
s_add_u32 m0, s36, 36864
Mp 40,144,180
Rt 130,248,37376
buffer_load_dwordx4 v2, s[12:15], s44 offen offset:0 lds
Mp 44,148,180
Rt 132,249,36864
s_bitcmp1_b32 s2, 6
s_cmov_b32 s63, s28
Mp 36,148,176
Rt 134,249,37376
s_lshl_b32 s28, s33, 3
s_lshr_b32 s28, s63, s28
Mp 48,152,176
Rt 136,250,36864
s_and_b32 s28, s28, 255
s_lshl_b32 s38, s28, 17
s_add_u32 s38, s38, 0xff80
Mp 56,152,180
Rt 138,250,37376
buffer_load_dwordx4 v3, s[12:15], s44 offen offset:1024 lds
Mp 60,156,180
Rt 140,251,36864
s_lshl_b32 s39, s33, 16
s_lshl_b32 s28, s34, 9
Mp 52,156,176
Rt 142,251,37376
s_add_u32 s39, s39, s28
s_waitcnt lgkmcnt(8)
v_mfma_f32_16x16x32_bf16 v[64:67], a[160:163], v[128:131], 0
Rt 144,248,40960
s_sub_u32 s39, s39, 0x40000
s_lshl_b32 s37, s33, 12
v_mfma_f32_16x16x32_bf16 v[72:75], a[160:163], v[132:135], 0
Rt 146,248,41472
s_add_u32 s44, s43, 12928
buffer_load_dwordx4 v2, s[12:15], s44 offen offset:2048 lds
v_mfma_f32_16x16x32_bf16 v[76:79], a[164:167], v[132:135], 0
Rt 148,249,40960
s_add_u32 s37, s37, 0x10000
s_lshl_b32 s28, s34, 21
v_mfma_f32_16x16x32_bf16 v[68:71], a[164:167], v[128:131], 0
Rt 150,249,41472
)" R"(
s_lshl_b32 s29, s91, 25
s_add_u32 s28, s28, s29
v_mfma_f32_16x16x32_bf16 v[80:83], a[168:171], v[128:131], 0
Rt 152,250,40960
s_bfe_u32 s29, s63, 0x80000
s_lshl_b32 s29, s29, 16
s_add_u32 s45, s28, s29
v_mfma_f32_16x16x32_bf16 v[88:91], a[168:171], v[132:135], 0
Rt 154,250,41472
buffer_load_dwordx4 v3, s[12:15], s44 offen offset:3072 lds
v_mfma_f32_16x16x32_bf16 v[92:95], a[172:175], v[132:135], 0
Rt 156,251,40960
s_add_u32 s92, s42, 0x1c0080
s_bfe_u32 s29, s63, 0x80008
v_mfma_f32_16x16x32_bf16 v[84:87], a[172:175], v[128:131], 0
Rt 158,251,41472
s_lshl_b32 s29, s29, 16
s_waitcnt lgkmcnt(8)
Mp 64,128,136
Rt 160,248,45056
s_add_u32 m0, s41, 0
s_add_u32 s46, s28, s29
Mp 72,128,140
Rt 162,248,45568
buffer_load_dwordx4 v240, s[16:19], s92 offen offset:0 lds
s_bfe_u32 s29, s63, 0x80010
Mp 76,132,140
Rt 164,249,45056
s_lshl_b32 s29, s29, 16
s_add_u32 s47, s28, s29
Mp 68,132,136
Rt 166,249,45568
s_bfe_u32 s29, s63, 0x80018
s_lshl_b32 s29, s29, 16
Mp 80,136,136
Rt 168,250,45056
s_add_u32 s48, s28, s29
s_lshl_b32 s50, s34, 21
s_add_u32 s50, s50, 0x6140000
Mp 88,136,140
Rt 170,250,45568
buffer_load_dwordx4 v241, s[16:19], s92 offen offset:1024 lds
Mp 92,140,140
Rt 172,251,45056
s_lshl2_add_u32 s28, s91, s93
s_lshl_b32 s28, s28, 7
Mp 84,140,136
Rt 174,251,45568
s_add_u32 s49, s28, s50
s_waitcnt lgkmcnt(8)
Mp 64,144,144
Rt 128,248,49152
s_add_u32 s49, s49, 4096
s_lshl_b32 s52, s35, 7
Mp 72,144,148
Rt 130,248,49664
buffer_load_dwordx4 v242, s[16:19], s92 offen offset:2048 lds
s_add_u32 s52, s52, s50
Mp 76,148,148
Rt 132,249,49152
s_add_u32 s52, s52, 0xd000
s_lshl4_add_u32 s28, s34, s35
Mp 68,148,144
Rt 134,249,49664
)" R"(
s_lshl_b32 s51, s28, 17
s_add_u32 s51, s51, 0x4000000
Mp 80,152,144
Rt 136,250,49152
Mp 88,152,148
Rt 138,250,49664
buffer_load_dwordx4 v243, s[16:19], s92 offen offset:3072 lds
Mp 92,156,148
Rt 140,251,49152
s_add_u32 s92, s42, 0x200080
Mp 84,156,144
Rt 142,251,49664
s_waitcnt lgkmcnt(8)
Mp 64,160,152
Rt 144,248,53248
s_add_u32 m0, s41, 4096
Mp 72,160,156
Rt 146,248,53760
buffer_load_dwordx4 v240, s[16:19], s92 offen offset:0 lds
Mp 76,164,156
Rt 148,249,53248
Mp 68,164,152
Rt 150,249,53760
Mp 80,168,152
Rt 152,250,53248
Mp 88,168,156
Rt 154,250,53760
buffer_load_dwordx4 v241, s[16:19], s92 offen offset:1024 lds
Mp 92,172,156
Rt 156,251,53248
s_cmp_lg_u32 s33, 0
s_cmov_b32 s49, 0x40000000
s_cmov_b32 s52, 0x40000000
Mp 84,172,152
Rt 158,251,53760
s_mov_b32 s53, 0x40000
s_waitcnt vmcnt(10) lgkmcnt(8)
Mp 64,128,160
Rt 160,248,57344
ds_read_b128 v[192:195], v26 offset:32768
s_lshl_b32 s89, s34, 21
Mp 72,128,164
Rt 162,248,57856
buffer_load_dwordx4 v242, s[16:19], s92 offen offset:2048 lds
s_mov_b32 s62, 0x4e800000
Mp 76,132,164
Rt 164,249,57344
ds_read_b128 v[196:199], v26 offset:34816
s_add_u32 s67, s89, 0x6000000
Mp 68,132,160
Rt 166,249,57856
s_lshl3_add_u32 s28, s34, s35
s_add_u32 s64, s51, 0x100000
s_lshl2_add_u32 s28, s28, s33
Mp 80,136,160
Rt 168,250,57344
ds_read_b128 v[200:203], v27 offset:32768
s_lshl2_add_u32 s65, s35, s33
Mp 88,136,164
Rt 170,250,57856
s_lshl_b32 s65, s65, 7
s_add_u32 s65, s65, s50
Mp 92,140,164
Rt 172,251,57344
)" R"(
ds_read_b128 v[204:207], v27 offset:34816
s_add_u32 s65, s65, 0xc000
s_lshl_b32 s66, s28, 10
Mp 84,140,160
Rt 174,251,57856
s_waitcnt vmcnt(7) lgkmcnt(12)
s_barrier
Mp 64,144,168
Rt 128,252,0
ds_read_b128 v[208:211], v26 offset:36864
s_add_u32 s66, s66, 0xc000000
Mp 72,144,172
Rt 130,252,512
s_sub_u32 s28, 15, s35
s_lshl2_add_u32 s28, s28, s33
Mp 76,148,172
Rt 132,253,0
ds_read_b128 v[212:215], v26 offset:38912
s_lshl_b32 s29, s28, 5
Mp 68,148,168
Rt 134,253,512
s_add_u32 s29, s29, s34
s_lshl_b32 s73, s29, 15
s_add_u32 s73, s73, 0x8000000
Mp 80,152,168
Rt 136,254,0
ds_read_b128 v[216:219], v27 offset:36864
s_lshr_b32 s29, s36, 1
Mp 88,152,172
Rt 138,254,512
s_add_u32 s44, s43, 768
s_and_b32 s29, s29, 0x2000
Mp 92,156,172
Rt 140,255,0
ds_read_b128 v[220:223], v27 offset:38912
s_add_u32 s73, s73, s29
s_lshl4_add_u32 s28, s28, s34
Mp 84,156,168
Rt 142,255,512
s_waitcnt lgkmcnt(12)
s_barrier
Mp 64,160,176
Rt 144,252,4096
s_add_u32 m0, s36, 32768
s_lshl_b32 s68, s28, 8
Mp 72,160,180
Rt 146,252,4608
buffer_load_dwordx4 v2, s[12:15], s44 offen offset:0 lds
Mp 76,164,180
Rt 148,253,4096
s_add_u32 s68, s68, 0xc110000
s_xor_b32 s72, s73, 0x3c04000
Mp 68,164,176
Rt 150,253,4608
s_mov_b32 s55, s89
s_mov_b32 s57, 18
s_mov_b32 s59, s67
Mp 80,168,176
Rt 152,254,4096
buffer_load_dwordx4 v3, s[12:15], s44 offen offset:1024 lds
Mp 88,168,180
Rt 154,254,4608
s_mov_b32 s58, 0
Mp 92,172,180
Rt 156,255,4096
s_cmp_eq_u32 s35, 7
)" R"(
s_cmov_b32 s58, 2
Mp 84,172,176
Rt 158,255,4608
s_add_u32 s44, s43, 13056
buffer_load_dwordx4 v2, s[12:15], s44 offen offset:2048 lds
s_waitcnt lgkmcnt(8)
Mp 32,128,184
Rt 160,252,8192
s_cmp_eq_u32 s35, 7
s_cmov_b32 s64, 0x40000000
Mp 40,128,188
Rt 162,252,8704
s_cmp_eq_u32 s35, 7
s_cmov_b32 s66, 0x40000000
Mp 44,132,188
Rt 164,253,8192
buffer_load_dwordx4 v3, s[12:15], s44 offen offset:3072 lds
Mp 36,132,184
Rt 166,253,8704
s_add_u32 s56, s55, 0x2000000
v_lshrrev_b32 v28, 4, v0
v_and_b32 v29, 7, v0
Mp 48,136,184
Rt 168,254,8192
v_xor_b32 v28, v28, v29
v_lshlrev_b32 v28, 4, v28
Mp 56,136,188
Rt 170,254,8704
s_add_u32 s92, s42, 0x100
v_lshrrev_b32 v29, 3, v0
Mp 60,140,188
Rt 172,255,8192
s_add_u32 m0, s41, 65536
v_lshl_or_b32 v8, v29, 11, v28
Mp 52,140,184
Rt 174,255,8704
buffer_load_dwordx4 v240, s[16:19], s92 offen offset:0 lds
s_waitcnt lgkmcnt(8)
Mp 32,144,192
Rt 128,252,12288
v_xor_b32 v9, 64, v8
v_add_u32 v9, 15360, v9
v_add_u32 v10, 30720, v8
Mp 40,144,196
Rt 130,252,12800
v_add_u32 v11, 30720, v9
s_and_b32 s28, s33, 1
Mp 44,148,196
Rt 132,253,12288
s_lshl_b32 s28, s28, 7
v_lshrrev_b32 v29, 5, v0
Mp 36,148,192
Rt 134,253,12800
buffer_load_dwordx4 v241, s[16:19], s92 offen offset:1024 lds
v_mul_u32_u24 v29, 0x2220, v29
Mp 48,152,192
Rt 136,254,12288
v_xor_b32 v29, v29, v1
v_xor_b32 v12, s28, v29
Mp 56,152,196
Rt 138,254,12800
v_xor_b32 v13, 64, v12
v_add_u32 v13, 15360, v13
Mp 60,156,196
)" R"(
Rt 140,255,12288
v_add_u32 v14, 30720, v12
v_add_u32 v15, 30720, v13
Mp 52,156,192
Rt 142,255,12800
buffer_load_dwordx4 v242, s[16:19], s92 offen offset:2048 lds
s_waitcnt lgkmcnt(8)
Mp 32,160,200
Rt 144,252,16384
v_bfe_u32 v28, v0, 1, 3
v_lshrrev_b32 v29, 4, v0
v_xor_b32 v28, v28, v29
Mp 40,160,204
Rt 146,252,16896
v_lshlrev_b32 v28, 4, v28
v_and_b32 v29, 15, v0
Mp 44,164,204
Rt 148,253,16384
v_lshl_or_b32 v28, v29, 7, v28
s_lshr_b32 s28, s33, 1
Mp 36,164,200
Rt 150,253,16896
buffer_load_dwordx4 v243, s[16:19], s92 offen offset:3072 lds
Mp 48,168,200
Rt 152,254,16384
s_lshl_b32 s28, s28, 12
v_or_b32 v16, s28, v28
Mp 56,168,204
Rt 154,254,16896
s_add_u32 s92, s42, 0x40100
v_xor_b32 v17, 64, v16
v_bfe_u32 v28, v0, 2, 3
Mp 60,172,204
Rt 156,255,16384
s_add_u32 m0, s41, 69632
v_lshlrev_b32 v28, 5, v28
Mp 52,172,200
Rt 158,255,16896
buffer_load_dwordx4 v240, s[16:19], s92 offen offset:0 lds
s_waitcnt vmcnt(5) lgkmcnt(8)
Mp 32,128,208
Rt 160,252,20480
ds_read_b128 v[224:227], v26 offset:32768
v_and_b32 v29, 3, v0
v_lshl_or_b32 v28, v29, 3, v28
Mp 40,128,212
Rt 162,252,20992
v_bfe_u32 v29, v0, 2, 2
v_lshl_or_b32 v28, v29, 9, v28
Mp 44,132,212
Rt 164,253,20480
ds_read_b128 v[228:231], v26 offset:34816
v_lshrrev_b32 v29, 4, v0
Mp 36,132,208
Rt 166,253,20992
buffer_load_dwordx4 v241, s[16:19], s92 offen offset:1024 lds
Mp 48,136,208
Rt 168,254,20480
ds_read_b128 v[232:235], v27 offset:32768
v_lshl_or_b32 v28, v29, 12, v28
Mp 56,136,212
Rt 170,254,20992
s_and_b32 s28, s33, 1
s_lshl_b32 s28, s28, 8
)" R"(
s_bitset1_b32 s28, 16
Mp 60,140,212
Rt 172,255,20480
ds_read_b128 v[236:239], v27 offset:34816
v_or_b32 v18, s28, v28
Mp 52,140,208
Rt 174,255,20992
buffer_load_dwordx4 v242, s[16:19], s92 offen offset:2048 lds
s_waitcnt lgkmcnt(12)
Mp 32,144,216
Rt 128,252,24576
v_xor_b32 v19, 32, v18
v_xor_b32 v20, 64, v18
Mp 40,144,220
Rt 130,252,25088
s_add_u32 s92, s42, 0x180
v_xor_b32 v21, 96, v18
Mp 44,148,220
Rt 132,253,24576
s_add_u32 m0, s41, 98304
v_xor_b32 v22, 128, v18
v_xor_b32 v23, 160, v18
Mp 36,148,216
Rt 134,253,25088
buffer_load_dwordx4 v240, s[16:19], s92 offen offset:0 lds
Mp 48,152,216
Rt 136,254,24576
v_xor_b32 v24, 192, v18
v_xor_b32 v25, 224, v18
Mp 56,152,220
Rt 138,254,25088
Mp 60,156,220
Rt 140,255,24576
Mp 52,156,216
Rt 142,255,25088
buffer_load_dwordx4 v241, s[16:19], s92 offen offset:1024 lds
s_waitcnt lgkmcnt(8)
s_barrier
Mp 32,160,224
Rt 144,244,0
Mp 40,160,228
Rt 146,244,512
Mp 44,164,228
Rt 148,245,0
Mp 36,164,224
Rt 150,245,512
buffer_load_dwordx4 v242, s[16:19], s92 offen offset:2048 lds
Mp 48,168,224
Rt 152,246,0
Mp 56,168,228
Rt 154,246,512
Mp 60,172,228
Rt 156,247,0
Mp 52,172,224
Rt 158,247,512
buffer_load_dwordx4 v243, s[16:19], s92 offen offset:3072 lds
s_waitcnt lgkmcnt(8)
s_barrier
Mp 32,128,232
Rt 160,244,4096
Mp 40,128,236
Rt 162,244,4608
s_add_u32 s92, s42, 0x40180
Mp 44,132,236
Rt 164,245,4096
s_add_u32 m0, s41, 102400
)" R"(
Mp 36,132,232
Rt 166,245,4608
buffer_load_dwordx4 v240, s[16:19], s92 offen offset:0 lds
Mp 48,136,232
Rt 168,246,4096
Mp 56,136,236
Rt 170,246,4608
Mp 60,140,236
Rt 172,247,4096
Mp 52,140,232
Rt 174,247,4608
buffer_load_dwordx4 v241, s[16:19], s92 offen offset:1024 lds
s_waitcnt lgkmcnt(8)
Mp 64,144,184
Rt 128,244,8192
Mp 72,144,188
Rt 130,244,8704
Mp 76,148,188
Rt 132,245,8192
Mp 68,148,184
Rt 134,245,8704
buffer_load_dwordx4 v242, s[16:19], s92 offen offset:2048 lds
Mp 80,152,184
Rt 136,246,8192
v_mul_f32_e32 v32, s54, v32
Mp 88,152,188
Rt 138,246,8704
s_add_u32 s92, s42, 0x1c0100
Mp 92,156,188
Rt 140,247,8192
s_add_u32 m0, s41, 32768
Mp 84,156,184
Rt 142,247,8704
buffer_load_dwordx4 v240, s[16:19], s92 offen offset:0 lds
s_waitcnt lgkmcnt(8)
Mp 64,160,192
Rt 144,244,12288
v_mul_f32_e32 v33, s54, v33
Mp 72,160,196
Rt 146,244,12800
Cv 32,32,33
Mp 76,164,196
Rt 148,245,12288
v_accvgpr_write_b32 a64, v32
v_mul_f32_e32 v34, s54, v34
Mp 68,164,192
Rt 150,245,12800
buffer_load_dwordx4 v241, s[16:19], s92 offen offset:1024 lds
Mp 80,168,192
Rt 152,246,12288
v_mul_f32_e32 v35, s54, v35
Mp 88,168,196
Rt 154,246,12800
Cv 34,34,35
v_accvgpr_write_b32 a65, v34
Mp 92,172,196
Rt 156,247,12288
v_mul_f32_e32 v36, s54, v36
Mp 84,172,192
Rt 158,247,12800
buffer_load_dwordx4 v242, s[16:19], s92 offen offset:2048 lds
s_waitcnt lgkmcnt(8)
Mp 64,128,200
Rt 160,244,16384
)" R"(
v_mul_f32_e32 v37, s54, v37
Cv 36,36,37
Mp 72,128,204
Rt 162,244,16896
v_accvgpr_write_b32 a66, v36
Mp 76,132,204
Rt 164,245,16384
v_mul_f32_e32 v38, s54, v38
Mp 68,132,200
Rt 166,245,16896
buffer_load_dwordx4 v243, s[16:19], s92 offen offset:3072 lds
Mp 80,136,200
Rt 168,246,16384
v_mul_f32_e32 v39, s54, v39
Mp 88,136,204
Rt 170,246,16896
s_add_u32 s92, s42, 0x200100
Mp 92,140,204
Rt 172,247,16384
s_add_u32 m0, s41, 36864
Mp 84,140,200
Rt 174,247,16896
buffer_load_dwordx4 v240, s[16:19], s92 offen offset:0 lds
s_waitcnt lgkmcnt(8)
Mp 64,144,208
Rt 128,244,20480
Cv 38,38,39
Mp 72,144,212
Rt 130,244,20992
v_accvgpr_write_b32 a67, v38
Mp 76,148,212
Rt 132,245,20480
v_mul_f32_e32 v40, s54, v40
v_mul_f32_e32 v41, s54, v41
Mp 68,148,208
Rt 134,245,20992
buffer_load_dwordx4 v241, s[16:19], s92 offen offset:1024 lds
Mp 80,152,208
Rt 136,246,20480
Cv 40,40,41
Mp 88,152,212
Rt 138,246,20992
v_accvgpr_write_b32 a68, v40
v_mul_f32_e32 v42, s54, v42
Mp 92,156,212
Rt 140,247,20480
v_mul_f32_e32 v43, s54, v43
Mp 84,156,208
Rt 142,247,20992
buffer_load_dwordx4 v242, s[16:19], s92 offen offset:2048 lds
s_waitcnt lgkmcnt(8)
Mp 64,160,216
Rt 144,244,24576
Cv 42,42,43
v_accvgpr_write_b32 a69, v42
Mp 72,160,220
Rt 146,244,25088
v_mul_f32_e32 v44, s54, v44
Mp 76,164,220
Rt 148,245,24576
v_mul_f32_e32 v45, s54, v45
Mp 68,164,216
Rt 150,245,25088
s_add_u32 s92, s42, 0x1c0180
)" R"(
Cv 44,44,45
Mp 80,168,216
Rt 152,246,24576
s_add_u32 m0, s41, 131072
Mp 88,168,220
Rt 154,246,25088
buffer_load_dwordx4 v240, s[16:19], s92 offen offset:0 lds
Mp 92,172,220
Rt 156,247,24576
v_accvgpr_write_b32 a70, v44
Mp 84,172,216
Rt 158,247,25088
s_waitcnt vmcnt(15) lgkmcnt(8)
s_barrier
Mp 64,128,224
Rt 160,248,0
v_mul_f32_e32 v46, s54, v46
Mp 72,128,228
Rt 162,248,512
buffer_load_dwordx4 v241, s[16:19], s92 offen offset:1024 lds
Mp 76,132,228
Rt 164,249,0
v_mul_f32_e32 v47, s54, v47
Cv 46,46,47
Mp 68,132,224
Rt 166,249,512
v_accvgpr_write_b32 a71, v46
Mp 80,136,224
Rt 168,250,0
v_mul_f32_e32 v48, s54, v48
Mp 88,136,228
Rt 170,250,512
buffer_load_dwordx4 v242, s[16:19], s92 offen offset:2048 lds
Mp 92,140,228
Rt 172,251,0
v_mul_f32_e32 v49, s54, v49
Mp 84,140,224
Rt 174,251,512
s_waitcnt vmcnt(3) lgkmcnt(8)
s_barrier
Mp 64,144,232
Rt 128,248,4096
Cv 48,48,49
v_accvgpr_write_b32 a72, v48
Mp 72,144,236
Rt 130,248,4608
buffer_load_dwordx4 v243, s[16:19], s92 offen offset:3072 lds
Mp 76,148,236
Rt 132,249,4096
v_mul_f32_e32 v50, s54, v50
Mp 68,148,232
Rt 134,249,4608
s_add_u32 s92, s42, 0x200180
v_mul_f32_e32 v51, s54, v51
Mp 80,152,232
Rt 136,250,4096
s_add_u32 m0, s41, 135168
Mp 88,152,236
Rt 138,250,4608
buffer_load_dwordx4 v240, s[16:19], s92 offen offset:0 lds
Mp 92,156,236
Rt 140,251,4096
Cv 50,50,51
Mp 84,156,232
Rt 142,251,4608
v_accvgpr_write_b32 a73, v50
)" R"(
s_waitcnt lgkmcnt(8)
v_mfma_f32_16x16x32_bf16 v[96:99], a[160:163], v[128:131], 0
Rt 144,248,8192
v_mul_f32_e32 v52, s54, v52
v_mfma_f32_16x16x32_bf16 v[104:107], a[160:163], v[132:135], 0
Rt 146,248,8704
buffer_load_dwordx4 v241, s[16:19], s92 offen offset:1024 lds
v_mfma_f32_16x16x32_bf16 v[108:111], a[164:167], v[132:135], 0
Rt 148,249,8192
v_mul_f32_e32 v53, s54, v53
Cv 52,52,53
v_mfma_f32_16x16x32_bf16 v[100:103], a[164:167], v[128:131], 0
Rt 150,249,8704
v_accvgpr_write_b32 a74, v52
v_mfma_f32_16x16x32_bf16 v[112:115], a[168:171], v[128:131], 0
Rt 152,250,8192
v_mul_f32_e32 v54, s54, v54
v_mfma_f32_16x16x32_bf16 v[120:123], a[168:171], v[132:135], 0
Rt 154,250,8704
buffer_load_dwordx4 v242, s[16:19], s92 offen offset:2048 lds
v_mfma_f32_16x16x32_bf16 v[124:127], a[172:175], v[132:135], 0
Rt 156,251,8192
v_mul_f32_e32 v55, s54, v55
v_mfma_f32_16x16x32_bf16 v[116:119], a[172:175], v[128:131], 0
Rt 158,251,8704
s_waitcnt lgkmcnt(8)
Mp 96,128,136
Rt 160,248,12288
Cv 54,54,55
v_accvgpr_write_b32 a75, v54
Mp 104,128,140
Rt 162,248,12800
s_add_u32 s38, s38, 0xffff0080
Mp 108,132,140
Rt 164,249,12288
s_add_u32 m0, s36, 0
Mp 100,132,136
Rt 166,249,12800
buffer_load_dwordx4 v8, s[4:7], s38 offen offset:0 lds
Mp 112,136,136
Rt 168,250,12288
v_mul_f32_e32 v56, s54, v56
Mp 120,136,140
Rt 170,250,12800
v_mul_f32_e32 v57, s54, v57
Mp 124,140,140
Rt 172,251,12288
Cv 56,56,57
Mp 116,140,136
Rt 174,251,12800
buffer_load_dwordx4 v9, s[4:7], s38 offen offset:1024 lds
s_waitcnt lgkmcnt(8)
Mp 96,144,144
Rt 128,248,16384
v_accvgpr_write_b32 a76, v56
Mp 104,144,148
Rt 130,248,16896
v_mul_f32_e32 v58, s54, v58
Mp 108,148,148
Rt 132,249,16384
v_mul_f32_e32 v59, s54, v59
Cv 58,58,59
Mp 100,148,144
Rt 134,249,16896
)" R"(
buffer_load_dwordx4 v10, s[4:7], s38 offen offset:2048 lds
Mp 112,152,144
Rt 136,250,16384
v_accvgpr_write_b32 a77, v58
Mp 120,152,148
Rt 138,250,16896
v_mul_f32_e32 v60, s54, v60
v_mul_f32_e32 v61, s54, v61
Mp 124,156,148
Rt 140,251,16384
Cv 60,60,61
Mp 116,156,144
Rt 142,251,16896
buffer_load_dwordx4 v11, s[4:7], s38 offen offset:3072 lds
s_waitcnt lgkmcnt(8)
Mp 96,160,152
Rt 144,248,20480
v_accvgpr_write_b32 a78, v60
v_mul_f32_e32 v62, s54, v62
Mp 104,160,156
Rt 146,248,20992
s_add_u32 s38, s38, 0x10000
Mp 108,164,156
Rt 148,249,20480
s_add_u32 m0, s36, 4096
Mp 100,164,152
Rt 150,249,20992
buffer_load_dwordx4 v8, s[4:7], s38 offen offset:0 lds
Mp 112,168,152
Rt 152,250,20480
v_mul_f32_e32 v63, s54, v63
Mp 120,168,156
Rt 154,250,20992
Cv 62,62,63
Mp 124,172,156
Rt 156,251,20480
v_accvgpr_write_b32 a79, v62
Mp 116,172,152
Rt 158,251,20992
buffer_load_dwordx4 v9, s[4:7], s38 offen offset:1024 lds
s_waitcnt lgkmcnt(8)
Mp 96,128,160
Rt 160,248,24576
v_mul_f32_e32 v64, s54, v64
Mp 104,128,164
Rt 162,248,25088
v_mul_f32_e32 v65, s54, v65
Mp 108,132,164
Rt 164,249,24576
Cv 64,64,65
v_accvgpr_write_b32 a80, v64
Mp 100,132,160
Rt 166,249,25088
buffer_load_dwordx4 v10, s[4:7], s38 offen offset:2048 lds
Mp 112,136,160
Rt 168,250,24576
v_mul_f32_e32 v66, s54, v66
Mp 120,136,164
Rt 170,250,25088
v_mul_f32_e32 v67, s54, v67
Cv 66,66,67
Mp 124,140,164
Rt 172,251,24576
v_accvgpr_write_b32 a81, v66
)" R"(
Mp 116,140,160
Rt 174,251,25088
buffer_load_dwordx4 v11, s[4:7], s38 offen offset:3072 lds
s_waitcnt lgkmcnt(8)
Mp 96,144,168
Rt 128,244,32768
v_mul_f32_e32 v68, s54, v68
v_mul_f32_e32 v69, s54, v69
Mp 104,144,172
Rt 130,244,33280
Cv 68,68,69
s_mov_b32 s85, 0xc108800
Mp 108,148,172
Rt 132,245,32768
v_accvgpr_write_b32 a82, v68
v_and_b32 v2, 7, v0
Mp 100,148,168
Rt 134,245,33280
v_mul_f32_e32 v70, s54, v70
v_mul_f32_e32 v71, s54, v71
v_lshlrev_b32 v2, 7, v2
Mp 112,152,168
Rt 136,246,32768
Cv 70,70,71
Mp 120,152,172
Rt 138,246,33280
v_accvgpr_write_b32 a83, v70
buffer_load_dword v4, v2, s[20:23], s85 offen sc0 sc1
Mp 124,156,172
Rt 140,247,32768
v_mul_f32_e32 v72, s54, v72
Mp 116,156,168
Rt 142,247,33280
s_waitcnt lgkmcnt(8)
s_barrier
Mp 96,160,176
Rt 144,244,36864
v_mul_f32_e32 v73, s54, v73
Mp 104,160,180
Rt 146,244,37376
s_add_u32 s39, s39, 0x40000
Mp 108,164,180
Rt 148,245,36864
s_add_u32 m0, s37, 0
Cv 72,72,73
Mp 100,164,176
Rt 150,245,37376
buffer_load_dwordx4 v12, s[8:11], s39 offen offset:0 lds
Mp 112,168,176
Rt 152,246,36864
v_accvgpr_write_b32 a84, v72
Mp 120,168,180
Rt 154,246,37376
v_mul_f32_e32 v74, s54, v74
v_mul_f32_e32 v75, s54, v75
Mp 124,172,180
Rt 156,247,36864
Cv 74,74,75
Mp 116,172,176
Rt 158,247,37376
s_waitcnt lgkmcnt(8)
Mp 96,128,184
Rt 160,244,40960
v_accvgpr_write_b32 a85, v74
v_mul_f32_e32 v76, s54, v76
)" R"(
Mp 104,128,188
Rt 162,244,41472
buffer_load_dwordx4 v13, s[8:11], s39 offen offset:1024 lds
Mp 108,132,188
Rt 164,245,40960
v_mul_f32_e32 v77, s54, v77
Mp 100,132,184
Rt 166,245,41472
Cv 76,76,77
v_accvgpr_write_b32 a86, v76
Mp 112,136,184
Rt 168,246,40960
v_mul_f32_e32 v78, s54, v78
Mp 120,136,188
Rt 170,246,41472
v_mul_f32_e32 v79, s54, v79
Mp 124,140,188
Rt 172,247,40960
Cv 78,78,79
Mp 116,140,184
Rt 174,247,41472
buffer_load_dwordx4 v14, s[8:11], s39 offen offset:2048 lds
s_waitcnt lgkmcnt(8)
Mp 96,144,192
Rt 128,244,45056
v_accvgpr_write_b32 a87, v78
Mp 104,144,196
Rt 130,244,45568
v_mul_f32_e32 v80, s54, v80
Mp 108,148,196
Rt 132,245,45056
v_mul_f32_e32 v81, s54, v81
Cv 80,80,81
Mp 100,148,192
Rt 134,245,45568
v_accvgpr_write_b32 a88, v80
Mp 112,152,192
Rt 136,246,45056
v_mul_f32_e32 v82, s54, v82
Mp 120,152,196
Rt 138,246,45568
buffer_load_dwordx4 v15, s[8:11], s39 offen offset:3072 lds
Mp 124,156,196
Rt 140,247,45056
v_mul_f32_e32 v83, s54, v83
Mp 116,156,192
Rt 142,247,45568
s_waitcnt lgkmcnt(8)
Mp 96,160,200
Rt 144,244,49152
Cv 82,82,83
v_accvgpr_write_b32 a89, v82
Mp 104,160,204
Rt 146,244,49664
s_add_u32 s39, s39, 0x40000
Mp 108,164,204
Rt 148,245,49152
s_add_u32 m0, s37, 16384
Mp 100,164,200
Rt 150,245,49664
buffer_load_dwordx4 v12, s[8:11], s39 offen offset:0 lds
Mp 112,168,200
Rt 152,246,49152
v_mul_f32_e32 v84, s54, v84
)" R"(
Mp 120,168,204
Rt 154,246,49664
v_mul_f32_e32 v85, s54, v85
Mp 124,172,204
Rt 156,247,49152
Cv 84,84,85
Mp 116,172,200
Rt 158,247,49664
v_accvgpr_write_b32 a90, v84
s_waitcnt lgkmcnt(8)
Mp 96,128,208
Rt 160,244,53248
v_mul_f32_e32 v86, s54, v86
Mp 104,128,212
Rt 162,244,53760
buffer_load_dwordx4 v13, s[8:11], s39 offen offset:1024 lds
Mp 108,132,212
Rt 164,245,53248
v_mul_f32_e32 v87, s54, v87
Cv 86,86,87
Mp 100,132,208
Rt 166,245,53760
v_accvgpr_write_b32 a91, v86
Mp 112,136,208
Rt 168,246,53248
v_mul_f32_e32 v88, s54, v88
Mp 120,136,212
Rt 170,246,53760
v_mul_f32_e32 v89, s54, v89
Cv 88,88,89
Mp 124,140,212
Rt 172,247,53248
v_accvgpr_write_b32 a92, v88
Mp 116,140,208
Rt 174,247,53760
buffer_load_dwordx4 v14, s[8:11], s39 offen offset:2048 lds
s_waitcnt lgkmcnt(8)
Mp 96,144,216
Rt 128,244,57344
v_mul_f32_e32 v90, s54, v90
v_mul_f32_e32 v91, s54, v91
Mp 104,144,220
Rt 130,244,57856
Cv 90,90,91
Mp 108,148,220
Rt 132,245,57344
v_accvgpr_write_b32 a93, v90
Mp 100,148,216
Rt 134,245,57856
v_mul_f32_e32 v92, s54, v92
v_mul_f32_e32 v93, s54, v93
Mp 112,152,216
Rt 136,246,57344
Cv 92,92,93
Mp 120,152,220
Rt 138,246,57856
buffer_load_dwordx4 v15, s[8:11], s39 offen offset:3072 lds
Mp 124,156,220
Rt 140,247,57344
v_accvgpr_write_b32 a94, v92
Mp 116,156,216
Rt 142,247,57856
v_mul_f32_e32 v94, s54, v94
s_waitcnt lgkmcnt(8)
)" R"(
Mp 96,160,224
Rt 144,248,32768
v_mul_f32_e32 v95, s54, v95
Mp 104,160,228
Rt 146,248,33280
Cv 94,94,95
Mp 108,164,228
Rt 148,249,32768
v_accvgpr_write_b32 a95, v94
Mp 100,164,224
Rt 150,249,33280
s_lshl1_add_u32 s88, s34, s91
Mp 112,168,224
Rt 152,250,32768
s_lshl_b32 s88, s88, 12
Mp 120,168,228
Rt 154,250,33280
s_and_b32 s29, s38, 0xfffe0000
Mp 124,172,228
Rt 156,251,32768
s_add_u32 s88, s88, s29
Mp 116,172,224
Rt 158,251,33280
s_waitcnt vmcnt(8) lgkmcnt(8)
s_barrier
Mp 96,128,232
Rt 160,248,36864
v_min_u32 v28, 31, v0
v_xor_b32 v4, s3, v4
v_cmp_gt_u32_e32 vcc, 2, v4
s_andn2_b32 s28, 0xff, vcc_lo
s_cbranch_scc1 .Lzsp13
.Lzok13:
Mp 104,128,236
Rt 162,248,37376
s_add_u32 s38, s38, 0xffff0080
v_xor_b32 v4, s3, v4
Mp 108,132,236
Rt 164,249,36864
s_add_u32 m0, s36, 32768
v_and_b32 v4, 1, v4
Mp 100,132,232
Rt 166,249,37376
buffer_load_dwordx4 v8, s[4:7], s38 offen offset:0 lds
Mp 112,136,232
Rt 168,250,36864
v_lshlrev_b32 v28, 7, v28
Mp 120,136,236
Rt 170,250,37376
v_cmp_eq_u32_e32 vcc, 1, v4
s_mov_b32 s85, vcc_lo
Mp 124,140,236
Rt 172,251,36864
s_and_b32 s28, s2, 7
s_lshr_b32 s28, s85, s28
Mp 116,140,232
Rt 174,251,37376
s_xor_b32 s28, s28, s34
s_and_b32 s28, s28, 1
s_waitcnt lgkmcnt(8)
v_mfma_f32_16x16x32_bf16 v[64:67], a[144:147], v[128:131], 0
Rt 128,248,40960
s_lshl_b32 s84, s28, 13
s_add_u32 s49, s49, s84
v_mfma_f32_16x16x32_bf16 v[72:75], a[144:147], v[132:135], 0
)" R"(
Rt 130,248,41472
buffer_load_dwordx4 v9, s[4:7], s38 offen offset:1024 lds
v_mfma_f32_16x16x32_bf16 v[76:79], a[148:151], v[132:135], 0
Rt 132,249,40960
s_add_u32 s50, s50, s84
s_add_u32 s60, s50, 7168
v_mfma_f32_16x16x32_bf16 v[68:71], a[148:151], v[128:131], 0
Rt 134,249,41472
s_add_u32 s52, s52, s84
s_add_u32 s65, s65, s84
v_mfma_f32_16x16x32_bf16 v[80:83], a[152:155], v[128:131], 0
Rt 136,250,40960
v_mul_f32_e32 v96, s54, v96
s_lshr_b32 s28, s2, 6
s_lshl_b32 s28, s28, 14
v_mfma_f32_16x16x32_bf16 v[88:91], a[152:155], v[132:135], 0
Rt 138,250,41472
v_mul_f32_e32 v97, s54, v97
s_add_u32 s28, s28, s50
v_mfma_f32_16x16x32_bf16 v[92:95], a[156:159], v[132:135], 0
Rt 140,251,40960
Cv 96,96,97
s_bfe_u32 s29, s2, 0x20004
v_mfma_f32_16x16x32_bf16 v[84:87], a[156:159], v[128:131], 0
Rt 142,251,41472
buffer_load_dwordx4 v10, s[4:7], s38 offen offset:2048 lds
s_waitcnt lgkmcnt(8)
Mp 64,160,136
Rt 144,248,45056
v_accvgpr_write_b32 a96, v96
s_lshl2_add_u32 s29, s29, s33
Mp 72,160,140
Rt 146,248,45568
v_mul_f32_e32 v98, s54, v98
s_lshl_b32 s29, s29, 9
Mp 76,164,140
Rt 148,249,45056
v_mul_f32_e32 v99, s54, v99
s_add_u32 s28, s28, s29
Mp 68,164,136
Rt 150,249,45568
Cv 98,98,99
v_min_u32 v4, 3, v0
Mp 80,168,136
Rt 152,250,45056
v_accvgpr_write_b32 a97, v98
v_lshlrev_b32 v4, 7, v4
Mp 88,168,140
Rt 154,250,45568
buffer_load_dwordx4 v11, s[4:7], s38 offen offset:3072 lds
Mp 92,172,140
Rt 156,251,45056
v_mul_f32_e32 v100, s54, v100
Mp 84,172,136
Rt 158,251,45568
s_waitcnt lgkmcnt(8)
Mp 64,128,144
Rt 160,248,49152
v_mul_f32_e32 v101, s54, v101
Mp 72,128,148
Rt 162,248,49664
s_add_u32 s38, s38, 0x10000
Mp 76,132,148
Rt 164,249,49152
)" R"(
s_add_u32 m0, s36, 36864
Mp 68,132,144
Rt 166,249,49664
buffer_load_dwordx4 v8, s[4:7], s38 offen offset:0 lds
Mp 80,136,144
Rt 168,250,49152
Cv 100,100,101
buffer_load_dword v4, v4, s[20:23], s28 offen
Mp 88,136,148
Rt 170,250,49664
v_accvgpr_write_b32 a98, v100
s_lshl1_add_u32 s28, s36, s84
Mp 92,140,148
Rt 172,251,49152
v_mul_f32_e32 v102, s54, v102
v_add_u32 v2, s28, v1
Mp 84,140,144
Rt 174,251,49664
v_add_u32 v3, 0x1000, v2
s_waitcnt lgkmcnt(8)
Mp 64,144,152
Rt 128,248,53248
v_mul_f32_e32 v103, s54, v103
s_lshl_b32 s28, s34, 13
Mp 72,144,156
Rt 130,248,53760
buffer_load_dwordx4 v9, s[4:7], s38 offen offset:1024 lds
s_xor_b32 s28, s28, s84
Mp 76,148,156
Rt 132,249,53248
Cv 102,102,103
s_and_b32 s28, s28, 0x2000
Mp 68,148,152
Rt 134,249,53760
v_accvgpr_write_b32 a99, v102
s_xor_b32 s73, s73, s28
Mp 80,152,152
Rt 136,250,53248
v_mul_f32_e32 v104, s54, v104
s_xor_b32 s72, s72, s28
Mp 88,152,156
Rt 138,250,53760
v_mul_f32_e32 v105, s54, v105
Mp 92,156,156
Rt 140,251,53248
Cv 104,104,105
Mp 84,156,152
Rt 142,251,53760
buffer_load_dwordx4 v10, s[4:7], s38 offen offset:2048 lds
s_waitcnt lgkmcnt(8)
Mp 64,160,160
Rt 144,248,57344
v_accvgpr_write_b32 a100, v104
Mp 72,160,164
Rt 146,248,57856
v_mul_f32_e32 v106, s54, v106
Mp 76,164,164
Rt 148,249,57344
v_mul_f32_e32 v107, s54, v107
Mp 68,164,160
Rt 150,249,57856
Cv 106,106,107
Mp 80,168,160
Rt 152,250,57344
)" R"(
v_accvgpr_write_b32 a101, v106
Mp 88,168,164
Rt 154,250,57856
buffer_load_dwordx4 v11, s[4:7], s38 offen offset:3072 lds
Mp 92,172,164
Rt 156,251,57344
v_mul_f32_e32 v108, s54, v108
Mp 84,172,160
Rt 158,251,57856
s_waitcnt lgkmcnt(8)
Mp 64,128,168
Rt 160,252,0
v_mul_f32_e32 v109, s54, v109
Mp 72,128,172
Rt 162,252,512
Cv 108,108,109
Mp 76,132,172
Rt 164,253,0
v_accvgpr_write_b32 a102, v108
Mp 68,132,168
Rt 166,253,512
v_mul_f32_e32 v110, s54, v110
Mp 80,136,168
Rt 168,254,0
v_mul_f32_e32 v111, s54, v111
Mp 88,136,172
Rt 170,254,512
Cv 110,110,111
Mp 92,140,172
Rt 172,255,0
v_accvgpr_write_b32 a103, v110
Mp 84,140,168
Rt 174,255,512
s_waitcnt vmcnt(13) lgkmcnt(8)
s_barrier
Mp 64,144,176
Rt 128,252,4096
v_mul_f32_e32 v112, s54, v112
Mp 72,144,180
Rt 130,252,4608
s_add_u32 s39, s39, 0x40000
Mp 76,148,180
Rt 132,253,4096
s_add_u32 m0, s37, 32768
Mp 68,148,176
Rt 134,253,4608
buffer_load_dwordx4 v12, s[8:11], s39 offen offset:0 lds
Mp 80,152,176
Rt 136,254,4096
v_mul_f32_e32 v113, s54, v113
Mp 88,152,180
Rt 138,254,4608
Cv 112,112,113
Mp 92,156,180
Rt 140,255,4096
v_accvgpr_write_b32 a104, v112
Mp 84,156,176
Rt 142,255,4608
s_waitcnt lgkmcnt(8)
Mp 64,160,184
Rt 144,252,8192
v_mul_f32_e32 v114, s54, v114
Mp 72,160,188
Rt 146,252,8704
buffer_load_dwordx4 v13, s[8:11], s39 offen offset:1024 lds
)" R"(
Mp 76,164,188
Rt 148,253,8192
v_mul_f32_e32 v115, s54, v115
Mp 68,164,184
Rt 150,253,8704
Cv 114,114,115
Mp 80,168,184
Rt 152,254,8192
v_accvgpr_write_b32 a105, v114
Mp 88,168,188
Rt 154,254,8704
v_mul_f32_e32 v116, s54, v116
Mp 92,172,188
Rt 156,255,8192
v_mul_f32_e32 v117, s54, v117
Mp 84,172,184
Rt 158,255,8704
buffer_load_dwordx4 v14, s[8:11], s39 offen offset:2048 lds
s_waitcnt lgkmcnt(8)
Mp 64,128,192
Rt 160,252,12288
Cv 116,116,117
Mp 72,128,196
Rt 162,252,12800
v_accvgpr_write_b32 a106, v116
Mp 76,132,196
Rt 164,253,12288
v_mul_f32_e32 v118, s54, v118
Mp 68,132,192
Rt 166,253,12800
v_mul_f32_e32 v119, s54, v119
Mp 80,136,192
Rt 168,254,12288
Cv 118,118,119
Mp 88,136,196
Rt 170,254,12800
buffer_load_dwordx4 v15, s[8:11], s39 offen offset:3072 lds
Mp 92,140,196
Rt 172,255,12288
v_accvgpr_write_b32 a107, v118
Mp 84,140,192
Rt 174,255,12800
s_waitcnt lgkmcnt(8)
Mp 64,144,200
Rt 128,252,16384
v_mul_f32_e32 v120, s54, v120
Mp 72,144,204
Rt 130,252,16896
s_add_u32 s39, s39, 0x40000
Mp 76,148,204
Rt 132,253,16384
s_add_u32 m0, s37, 49152
Mp 68,148,200
Rt 134,253,16896
buffer_load_dwordx4 v12, s[8:11], s39 offen offset:0 lds
Mp 80,152,200
Rt 136,254,16384
v_mul_f32_e32 v121, s54, v121
Mp 88,152,204
Rt 138,254,16896
Cv 120,120,121
Mp 92,156,204
Rt 140,255,16384
v_accvgpr_write_b32 a108, v120
)" R"(
Mp 84,156,200
Rt 142,255,16896
s_waitcnt lgkmcnt(8)
Mp 64,160,208
Rt 144,252,20480
Rt 0,18,0
Mp 72,160,212
Rt 146,252,20992
buffer_load_dwordx4 v13, s[8:11], s39 offen offset:1024 lds
Rt 2,18,2048
Mp 76,164,212
Rt 148,253,20480
Rt 4,19,0
Mp 68,164,208
Rt 150,253,20992
Rt 6,19,2048
Mp 80,168,208
Rt 152,254,20480
Rt 8,20,0
Mp 88,168,212
Rt 154,254,20992
Rt 10,20,2048
Mp 92,172,212
Rt 156,255,20480
Rt 12,21,0
Mp 84,172,208
Rt 158,255,20992
buffer_load_dwordx4 v14, s[8:11], s39 offen offset:2048 lds
Rt 14,21,2048
Mp 64,128,216
Rt 160,252,24576
Rt 16,22,0
Mp 72,128,220
Rt 162,252,25088
Rt 18,22,2048
Mp 76,132,220
Rt 164,253,24576
Rt 20,23,0
Mp 68,132,216
Rt 166,253,25088
Rt 22,23,2048
Mp 80,136,216
Rt 168,254,24576
Rt 24,24,0
Mp 88,136,220
Rt 170,254,25088
buffer_load_dwordx4 v15, s[8:11], s39 offen offset:3072 lds
Rt 26,24,2048
Mp 92,140,220
Rt 172,255,24576
Rt 28,25,0
Mp 84,140,216
Rt 174,255,25088
Rt 30,25,2048
Mp 64,144,224
Rb 32,16,0
v_mul_f32_e32 v122, s54, v122
Mp 72,144,228
Rb 36,16,2048
v_mul_f32_e32 v123, s54, v123
Mp 76,148,228
Rb 40,16,8192
Cv 122,122,123
Mp 68,148,224
)" R"(
Rb 44,16,10240
v_accvgpr_write_b32 a109, v122
Mp 80,152,224
Rb 48,16,16384
v_mul_f32_e32 v124, s54, v124
Mp 88,152,228
Rb 52,16,18432
v_mul_f32_e32 v125, s54, v125
Mp 92,156,228
Rb 56,16,24576
Cv 124,124,125
Mp 84,156,224
Rb 60,16,26624
s_waitcnt lgkmcnt(9)
Mp 64,160,232
v_accvgpr_write_b32 a110, v124
v_mul_f32_e32 v126, s54, v126
Mp 72,160,236
v_mul_f32_e32 v127, s54, v127
Cv 126,126,127
Mp 76,164,236
v_accvgpr_write_b32 a111, v126
Mp 68,164,232
buffer_load_dword a254, v28, s[4:7], s88 offen
Mp 80,168,232
Mp 88,168,236
Mp 92,172,236
Mp 84,172,232
s_cmp_eq_u32 s91, 1
s_waitcnt vmcnt(18) lgkmcnt(0)
s_barrier
s_cbranch_scc1 .LzBV
Mz 32,0,32
Rt 128,18,16384
v_mul_f32_e32 v64, s54, v64
Mz 96,0,40
v_mul_f32_e32 v65, s54, v65
Cv 64,64,65
Mz 128,0,44
Rt 130,18,18432
v_accvgpr_write_b32 a112, v64
Mz 160,0,48
s_add_u32 s39, s39, 0x40000
v_mul_f32_e32 v66, s54, v66
Mz 192,0,52
Rt 132,19,16384
s_add_u32 m0, s37, 0
Mz 224,0,56
buffer_load_dwordx4 v12, s[8:11], s39 offen offset:0 lds
v_mfma_f32_16x16x32_bf16 a[192:195], a[0:3], a[60:63], 0
Rt 134,19,18432
v_mul_f32_e32 v67, s54, v67
v_mfma_f32_16x16x32_bf16 a[196:199], a[4:7], a[60:63], 0
Cv 66,66,67
v_accvgpr_write_b32 a113, v66
Mz 228,4,56
Rt 136,20,16384
v_mul_f32_e32 v68, s54, v68
Mz 196,4,52
v_mul_f32_e32 v69, s54, v69
Cv 68,68,69
buffer_store_dwordx4 a[64:67], v2, s[20:23], s51 offen offset:0 sc0 sc1
Mz 164,4,48
Rt 138,20,18432
v_accvgpr_write_b32 a114, v68
)" R"(
Mz 132,4,44
v_mul_f32_e32 v70, s54, v70
v_mul_f32_e32 v71, s54, v71
Mz 100,4,40
Rt 140,21,16384
Cv 70,70,71
Mz 36,4,32
buffer_load_dwordx4 v13, s[8:11], s39 offen offset:1024 lds
Mz 40,8,32
Rt 142,21,18432
v_accvgpr_write_b32 a115, v70
Mz 104,8,40
v_mul_f32_e32 v72, s54, v72
v_mul_f32_e32 v73, s54, v73
Mz 136,8,44
Rt 144,22,16384
Cv 72,72,73
Mz 168,8,48
v_accvgpr_write_b32 a116, v72
v_mul_f32_e32 v74, s54, v74
buffer_store_dwordx4 a[68:71], v2, s[20:23], s51 offen offset:1024 sc0 sc1
Mz 200,8,52
Rt 146,22,18432
v_mul_f32_e32 v75, s54, v75
Mz 232,8,56
Cv 74,74,75
v_accvgpr_write_b32 a117, v74
v_mfma_f32_16x16x32_bf16 a[200:203], a[8:11], a[60:63], 0
Rt 148,23,16384
v_mul_f32_e32 v76, s54, v76
v_mfma_f32_16x16x32_bf16 a[204:207], a[12:15], a[60:63], 0
buffer_load_dwordx4 v14, s[8:11], s39 offen offset:2048 lds
Mz 236,12,56
Rt 150,23,18432
v_mul_f32_e32 v77, s54, v77
Mz 204,12,52
Cv 76,76,77
v_accvgpr_write_b32 a118, v76
Mz 172,12,48
Rt 152,24,16384
v_mul_f32_e32 v78, s54, v78
Mz 140,12,44
v_mul_f32_e32 v79, s54, v79
Cv 78,78,79
buffer_store_dwordx4 a[72:75], v2, s[20:23], s51 offen offset:2048 sc0 sc1
Mz 108,12,40
Rt 154,24,18432
v_accvgpr_write_b32 a119, v78
Mz 44,12,32
v_mul_f32_e32 v80, s54, v80
v_mul_f32_e32 v81, s54, v81
Mz 48,16,32
Rt 156,25,16384
Cv 80,80,81
Mz 112,16,40
buffer_load_dwordx4 v15, s[8:11], s39 offen offset:3072 lds
Mz 144,16,44
Rt 158,25,18432
v_accvgpr_write_b32 a120, v80
Mz 176,16,48
v_mul_f32_e32 v82, s54, v82
v_mul_f32_e32 v83, s54, v83
Mz 208,16,52
Rb 160,17,0
)" R"(
Cv 82,82,83
Mz 240,16,56
v_accvgpr_write_b32 a121, v82
v_mul_f32_e32 v84, s54, v84
buffer_store_dwordx4 a[76:79], v2, s[20:23], s51 offen offset:3072 sc0 sc1
v_mfma_f32_16x16x32_bf16 a[208:211], a[16:19], a[60:63], 0
Rb 164,17,2048
v_mul_f32_e32 v85, s54, v85
v_mfma_f32_16x16x32_bf16 a[212:215], a[20:23], a[60:63], 0
Cv 84,84,85
v_accvgpr_write_b32 a122, v84
Mz 244,20,56
Rb 168,17,8192
v_mul_f32_e32 v86, s54, v86
Mz 212,20,52
v_mul_f32_e32 v87, s54, v87
Cv 86,86,87
Mz 180,20,48
Rb 172,17,10240
v_accvgpr_write_b32 a123, v86
Mz 148,20,44
v_mul_f32_e32 v88, s54, v88
v_mul_f32_e32 v89, s54, v89
Mz 116,20,40
Rb 176,17,16384
Cv 88,88,89
Mz 52,20,32
v_accvgpr_write_b32 a124, v88
v_mul_f32_e32 v90, s54, v90
buffer_store_dwordx4 a[80:83], v3, s[20:23], s51 offen offset:0 sc0 sc1
Mz 56,24,32
Rb 180,17,18432
v_mul_f32_e32 v91, s54, v91
Mz 120,24,40
Cv 90,90,91
v_accvgpr_write_b32 a125, v90
Mz 152,24,44
Rb 184,17,24576
v_mul_f32_e32 v92, s54, v92
Mz 184,24,48
v_mul_f32_e32 v93, s54, v93
Cv 92,92,93
Mz 216,24,52
Rb 188,17,26624
v_accvgpr_write_b32 a126, v92
Mz 248,24,56
v_mul_f32_e32 v94, s54, v94
v_mul_f32_e32 v95, s54, v95
v_mfma_f32_16x16x32_bf16 a[216:219], a[24:27], a[60:63], 0
Cv 94,94,95
v_accvgpr_write_b32 a127, v94
v_mfma_f32_16x16x32_bf16 a[220:223], a[28:31], a[60:63], 0
buffer_store_dwordx4 a[84:87], v3, s[20:23], s51 offen offset:1024 sc0 sc1
Mz 252,28,56
Mz 220,28,52
Mz 188,28,48
Mz 156,28,44
Mz 124,28,40
Mz 60,28,32
Mz 64,0,36
Mz 68,4,36
buffer_store_dwordx4 a[88:91], v3, s[20:23], s51 offen offset:2048 sc0 sc1
Mz 72,8,36
Mz 76,12,36
)" R"(
Mz 80,16,36
Mz 84,20,36
Mz 88,24,36
Mz 92,28,36
s_waitcnt vmcnt(16) lgkmcnt(0)
s_barrier
Mv 32,128,160
Rt 0,18,32768
Mv 64,128,164
s_add_u32 s38, s38, 0xffff0080
Mv 96,128,168
Rt 2,18,34816
s_add_u32 m0, s36, 0
Mv 128,128,172
buffer_load_dwordx4 v8, s[4:7], s38 offen offset:0 lds
Mv 160,128,176
Rt 4,19,32768
Mv 192,128,180
Mv 224,128,184
Rt 6,19,34816
Ma 192,128,188
buffer_load_dwordx4 v9, s[4:7], s38 offen offset:1024 lds
Ma 196,132,188
Rt 8,20,32768
Mv 228,132,184
Mv 196,132,180
Rt 10,20,34816
Mv 164,132,176
buffer_load_dwordx4 v10, s[4:7], s38 offen offset:2048 lds
Mv 132,132,172
Rt 12,21,32768
Mv 100,132,168
Mv 68,132,164
Rt 14,21,34816
Mv 36,132,160
buffer_load_dwordx4 v11, s[4:7], s38 offen offset:3072 lds
Mv 40,136,160
Rt 16,22,32768
Mv 72,136,164
s_add_u32 s38, s38, 0x10000
Mv 104,136,168
Rt 18,22,34816
s_add_u32 m0, s36, 4096
Mv 136,136,172
buffer_load_dwordx4 v8, s[4:7], s38 offen offset:0 lds
Mv 168,136,176
Rt 20,23,32768
Mv 200,136,180
Mv 232,136,184
Rt 22,23,34816
Ma 200,136,188
buffer_load_dwordx4 v9, s[4:7], s38 offen offset:1024 lds
Ma 204,140,188
Rt 24,24,32768
Mv 236,140,184
Mv 204,140,180
Rt 26,24,34816
Mv 172,140,176
buffer_load_dwordx4 v10, s[4:7], s38 offen offset:2048 lds
Mv 140,140,172
Rt 28,25,32768
Mv 108,140,168
Mv 76,140,164
Rt 30,25,34816
Mv 44,140,160
)" R"(
buffer_load_dwordx4 v11, s[4:7], s38 offen offset:3072 lds
Mv 48,144,160
Rb 32,16,32768
Mv 80,144,164
Mv 112,144,168
Rb 36,16,34816
Mv 144,144,172
s_add_u32 s39, s39, 0x40000
Mv 176,144,176
Rb 40,16,40960
s_add_u32 m0, s37, 16384
Mv 208,144,180
buffer_load_dwordx4 v12, s[8:11], s39 offen offset:0 lds
Mv 240,144,184
Rb 44,16,43008
Ma 208,144,188
Ma 212,148,188
Rb 48,16,49152
Mv 244,148,184
Mv 212,148,180
Rb 52,16,51200
Mv 180,148,176
buffer_load_dwordx4 v13, s[8:11], s39 offen offset:1024 lds
Mv 148,148,172
Rb 56,16,57344
Mv 116,148,168
Mv 84,148,164
Rb 60,16,59392
Mv 52,148,160
Mv 56,152,160
Mv 88,152,164
buffer_load_dwordx4 v14, s[8:11], s39 offen offset:2048 lds
Mv 120,152,168
Mv 152,152,172
Mv 184,152,176
Mv 216,152,180
Mv 248,152,184
Ma 216,152,188
buffer_load_dwordx4 v15, s[8:11], s39 offen offset:3072 lds
Ma 220,156,188
Mv 252,156,184
Mv 220,156,180
Mv 188,156,176
buffer_store_dwordx4 a[92:95], v3, s[20:23], s51 offen offset:3072 sc0 sc1
Mv 156,156,172
Mv 124,156,168
Mv 92,156,164
Mv 60,156,160
s_waitcnt vmcnt(25) lgkmcnt(0)
s_barrier
Mv 32,0,32
Rt 128,18,49152
Mv 64,0,36
Mv 96,0,40
Rt 130,18,51200
Mv 128,0,44
s_add_u32 s39, s39, 0x40000
Mv 160,0,48
Rt 132,19,49152
s_add_u32 m0, s37, 32768
Mv 192,0,52
buffer_load_dwordx4 v12, s[8:11], s39 offen offset:0 lds
Mv 224,0,56
Rt 134,19,51200
Ma 192,0,60
)" R"(
Ma 196,4,60
Rt 136,20,49152
Mv 228,4,56
s_add_u32 s51, s51, 0x10000
buffer_store_dwordx4 a[96:99], v2, s[20:23], s51 offen offset:0 sc0 sc1
Mv 196,4,52
Rt 138,20,51200
Mv 164,4,48
Mv 132,4,44
Rt 140,21,49152
Mv 100,4,40
buffer_load_dwordx4 v13, s[8:11], s39 offen offset:1024 lds
Mv 68,4,36
Rt 142,21,51200
Mv 36,4,32
Mv 40,8,32
Rt 144,22,49152
Mv 72,8,36
buffer_store_dwordx4 a[100:103], v2, s[20:23], s51 offen offset:1024 sc0 sc1
Mv 104,8,40
Rt 146,22,51200
Mv 136,8,44
Mv 168,8,48
Rt 148,23,49152
Mv 200,8,52
buffer_load_dwordx4 v14, s[8:11], s39 offen offset:2048 lds
Mv 232,8,56
Rt 150,23,51200
Ma 200,8,60
Ma 204,12,60
Rt 152,24,49152
Mv 236,12,56
buffer_store_dwordx4 a[104:107], v2, s[20:23], s51 offen offset:2048 sc0 sc1
Mv 204,12,52
Rt 154,24,51200
Mv 172,12,48
Mv 140,12,44
Rt 156,25,49152
Mv 108,12,40
buffer_load_dwordx4 v15, s[8:11], s39 offen offset:3072 lds
Mv 76,12,36
Rt 158,25,51200
Mv 44,12,32
Mv 48,16,32
Rb 160,17,32768
Mv 80,16,36
buffer_store_dwordx4 a[108:111], v2, s[20:23], s51 offen offset:3072 sc0 sc1
Mv 112,16,40
Rb 164,17,34816
s_mov_b64 s[16:17], s[70:71]
Mv 144,16,44
s_mov_b32 s18, 0x188000
Mv 176,16,48
Rb 168,17,40960
Mv 208,16,52
s_cmp_eq_u32 s33, 0
s_cmov_b32 s18, 0x380000
Mv 240,16,56
Rb 172,17,43008
s_mul_i32 s43, s2, 3072
Ma 208,16,60
s_cmp_eq_u32 s33, 0
s_cselect_b32 s44, 3, 6
Ma 212,20,60
)" R"(
Rb 176,17,49152
s_mul_i32 s44, s44, 0x60000
Mv 244,20,56
buffer_store_dwordx4 a[112:115], v3, s[20:23], s51 offen offset:0 sc0 sc1
Mv 212,20,52
Rb 180,17,51200
s_sub_u32 s43, s43, s44
Mv 180,20,48
v_min_u32 v26, 23, v0
v_lshlrev_b32 v26, 7, v26
Mv 148,20,44
Rb 184,17,57344
v_and_b32 v5, 7, v0
Mv 116,20,40
v_lshlrev_b32 v5, 7, v5
Mv 84,20,36
Rb 188,17,59392
Mv 52,20,32
Mv 56,24,32
Mv 88,24,36
buffer_store_dwordx4 a[116:119], v3, s[20:23], s51 offen offset:1024 sc0 sc1
Mv 120,24,40
Mv 152,24,44
Mv 184,24,48
Mv 216,24,52
Mv 248,24,56
Ma 216,24,60
Ma 220,28,60
Mv 252,28,56
buffer_store_dwordx4 a[120:123], v3, s[20:23], s51 offen offset:2048 sc0 sc1
Mv 220,28,52
Mv 188,28,48
Mv 156,28,44
Mv 124,28,40
Mv 92,28,36
Mv 60,28,32
s_waitcnt vmcnt(16) lgkmcnt(0)
s_barrier
Mv 32,128,160
Rt 0,18,0
Mv 64,128,164
s_add_u32 s38, s38, 0xffff0080
Mv 96,128,168
Rt 2,18,2048
s_add_u32 m0, s36, 32768
Mv 128,128,172
buffer_load_dwordx4 v8, s[4:7], s38 offen offset:0 lds
Mv 160,128,176
Rt 4,19,0
Mv 192,128,180
Mv 224,128,184
Rt 6,19,2048
Ma 192,128,188
buffer_load_dwordx4 v9, s[4:7], s38 offen offset:1024 lds
Ma 196,132,188
Rt 8,20,0
Mv 228,132,184
Mv 196,132,180
Rt 10,20,2048
Mv 164,132,176
buffer_load_dwordx4 v10, s[4:7], s38 offen offset:2048 lds
Mv 132,132,172
Rt 12,21,0
Mv 100,132,168
Mv 68,132,164
)" R"(
Rt 14,21,2048
Mv 36,132,160
buffer_load_dwordx4 v11, s[4:7], s38 offen offset:3072 lds
Mv 40,136,160
Rt 16,22,0
Mv 72,136,164
s_add_u32 s38, s38, 0x10000
Mv 104,136,168
Rt 18,22,2048
s_add_u32 m0, s36, 36864
Mv 136,136,172
buffer_load_dwordx4 v8, s[4:7], s38 offen offset:0 lds
Mv 168,136,176
Rt 20,23,0
Mv 200,136,180
Mv 232,136,184
Rt 22,23,2048
Ma 200,136,188
buffer_load_dwordx4 v9, s[4:7], s38 offen offset:1024 lds
Ma 204,140,188
Rt 24,24,0
Mv 236,140,184
Mv 204,140,180
Rt 26,24,2048
Mv 172,140,176
buffer_load_dwordx4 v10, s[4:7], s38 offen offset:2048 lds
Mv 140,140,172
Rt 28,25,0
Mv 108,140,168
Mv 76,140,164
Rt 30,25,2048
v_accvgpr_write_b32 a224, -1
Mv 44,140,160
buffer_load_dwordx4 v11, s[4:7], s38 offen offset:3072 lds
Mv 48,144,160
Rb 32,16,0
v_accvgpr_write_b32 a225, -1
Mv 80,144,164
v_accvgpr_write_b32 a226, -1
v_accvgpr_write_b32 a227, -1
Mv 112,144,168
Rb 36,16,2048
s_add_u32 s88, s73, 0x4000
Mv 144,144,172
s_add_u32 s39, s39, 0x40000
s_mov_b32 s40, 6
Mv 176,144,176
Rb 40,16,8192
s_add_u32 m0, s37, 49152
Mv 208,144,180
buffer_load_dwordx4 v12, s[8:11], s39 offen offset:0 lds
Mv 240,144,184
Rb 44,16,10240
s_mov_b32 s94, 0
Ma 208,144,188
Ma 212,148,188
Rb 48,16,16384
Mv 244,148,184
Mv 212,148,180
Rb 52,16,18432
Mv 180,148,176
buffer_load_dwordx4 v13, s[8:11], s39 offen offset:1024 lds
Mv 148,148,172
Rb 56,16,24576
)" R"(
Mv 116,148,168
Mv 84,148,164
Rb 60,16,26624
Mv 52,148,160
Mv 56,152,160
Mv 88,152,164
buffer_load_dwordx4 v14, s[8:11], s39 offen offset:2048 lds
Mv 120,152,168
Mv 152,152,172
Mv 184,152,176
Mv 216,152,180
Mv 248,152,184
Ma 216,152,188
buffer_load_dwordx4 v15, s[8:11], s39 offen offset:3072 lds
Ma 220,156,188
Mv 252,156,184
Mv 220,156,180
Mv 188,156,176
buffer_store_dwordx4 a[124:127], v3, s[20:23], s51 offen offset:3072 sc0 sc1
Mv 156,156,172
Mv 124,156,168
Mv 92,156,164
Mv 60,156,160
s_waitcnt vmcnt(25) lgkmcnt(0)
s_barrier
.LzPBK:
Mv 32,0,32
Rt 128,18,16384
Mv 64,0,36
Mv 96,0,40
Rt 130,18,18432
Mv 128,0,44
s_add_u32 s39, s39, 0x40000
Mv 160,0,48
Rt 132,19,16384
s_add_u32 m0, s37, 0
Mv 192,0,52
buffer_load_dwordx4 v12, s[8:11], s39 offen offset:0 lds
Mv 224,0,56
Rt 134,19,18432
Ma 192,0,60
Ma 196,4,60
Rt 136,20,16384
Mv 228,4,56
Mv 196,4,52
Rt 138,20,18432
Mv 164,4,48
Mv 132,4,44
Rt 140,21,16384
Mv 100,4,40
buffer_load_dwordx4 v13, s[8:11], s39 offen offset:1024 lds
Mv 68,4,36
Rt 142,21,18432
Mv 36,4,32
Mv 40,8,32
Rt 144,22,16384
Mv 72,8,36
Mv 104,8,40
Rt 146,22,18432
Mv 136,8,44
Mv 168,8,48
Rt 148,23,16384
Mv 200,8,52
buffer_load_dwordx4 v14, s[8:11], s39 offen offset:2048 lds
Mv 232,8,56
)" R"(
Rt 150,23,18432
Ma 200,8,60
Ma 204,12,60
Rt 152,24,16384
Mv 236,12,56
Mv 204,12,52
Rt 154,24,18432
Mv 172,12,48
Mv 140,12,44
Rt 156,25,16384
Mv 108,12,40
buffer_load_dwordx4 v15, s[8:11], s39 offen offset:3072 lds
Mv 76,12,36
Rt 158,25,18432
Mv 44,12,32
Mv 48,16,32
Rb 160,17,0
Mv 80,16,36
Mv 112,16,40
Rb 164,17,2048
Mv 144,16,44
Mv 176,16,48
Rb 168,17,8192
Mv 208,16,52
buffer_store_dwordx4 a[224:227], v1, s[20:23], s72 offen sc0 sc1
Mv 240,16,56
Rb 172,17,10240
s_add_u32 s72, s72, 1024
Ma 208,16,60
Ma 212,20,60
Rb 176,17,16384
Mv 244,20,56
Mv 212,20,52
Rb 180,17,18432
Mv 180,20,48
Mv 148,20,44
Rb 184,17,24576
Mv 116,20,40
Mv 84,20,36
Rb 188,17,26624
Mv 52,20,32
Mv 56,24,32
Mv 88,24,36
buffer_store_dwordx4 a[224:227], v1, s[20:23], s88 offen sc0 sc1
Mv 120,24,40
s_add_u32 s88, s88, 1024
Mv 152,24,44
Mv 184,24,48
Mv 216,24,52
Mv 248,24,56
Ma 216,24,60
Ma 220,28,60
Mv 252,28,56
Mv 220,28,52
Mv 188,28,48
Mv 156,28,44
Mv 124,28,40
Mv 92,28,36
Mv 60,28,32
s_waitcnt vmcnt(11) lgkmcnt(0)
s_barrier
Mv 32,128,160
Rt 0,18,32768
Mv 64,128,164
s_add_u32 s38, s38, 0xffff0080
)" R"(
Mv 96,128,168
Rt 2,18,34816
s_add_u32 m0, s36, 0
Mv 128,128,172
buffer_load_dwordx4 v8, s[4:7], s38 offen offset:0 lds
Mv 160,128,176
Rt 4,19,32768
Mv 192,128,180
Mv 224,128,184
Rt 6,19,34816
Ma 192,128,188
buffer_load_dwordx4 v9, s[4:7], s38 offen offset:1024 lds
Ma 196,132,188
Rt 8,20,32768
Mv 228,132,184
Mv 196,132,180
Rt 10,20,34816
Mv 164,132,176
buffer_load_dwordx4 v10, s[4:7], s38 offen offset:2048 lds
Mv 132,132,172
Rt 12,21,32768
Mv 100,132,168
Mv 68,132,164
Rt 14,21,34816
Mv 36,132,160
buffer_load_dwordx4 v11, s[4:7], s38 offen offset:3072 lds
Mv 40,136,160
Rt 16,22,32768
Mv 72,136,164
s_add_u32 s38, s38, 0x10000
Mv 104,136,168
Rt 18,22,34816
s_add_u32 m0, s36, 4096
Mv 136,136,172
buffer_load_dwordx4 v8, s[4:7], s38 offen offset:0 lds
Mv 168,136,176
Rt 20,23,32768
Mv 200,136,180
Mv 232,136,184
Rt 22,23,34816
Ma 200,136,188
buffer_load_dwordx4 v9, s[4:7], s38 offen offset:1024 lds
Ma 204,140,188
Rt 24,24,32768
Mv 236,140,184
Mv 204,140,180
Rt 26,24,34816
Mv 172,140,176
buffer_load_dwordx4 v10, s[4:7], s38 offen offset:2048 lds
Mv 140,140,172
Rt 28,25,32768
Mv 108,140,168
Mv 76,140,164
Rt 30,25,34816
Mv 44,140,160
buffer_load_dwordx4 v11, s[4:7], s38 offen offset:3072 lds
Mv 48,144,160
Rb 32,16,32768
Mv 80,144,164
Mv 112,144,168
Rb 36,16,34816
Mv 144,144,172
s_add_u32 s39, s39, 0x40000
Mv 176,144,176
)" R"(
Rb 40,16,40960
s_add_u32 m0, s37, 16384
Mv 208,144,180
buffer_load_dwordx4 v12, s[8:11], s39 offen offset:0 lds
Mv 240,144,184
Rb 44,16,43008
Ma 208,144,188
Ma 212,148,188
Rb 48,16,49152
Mv 244,148,184
Mv 212,148,180
Rb 52,16,51200
Mv 180,148,176
buffer_load_dwordx4 v13, s[8:11], s39 offen offset:1024 lds
Mv 148,148,172
Rb 56,16,57344
Mv 116,148,168
Mv 84,148,164
Rb 60,16,59392
Mv 52,148,160
Mv 56,152,160
Mv 88,152,164
buffer_load_dwordx4 v14, s[8:11], s39 offen offset:2048 lds
Mv 120,152,168
Mv 152,152,172
Mv 184,152,176
Mv 216,152,180
Mv 248,152,184
Ma 216,152,188
buffer_load_dwordx4 v15, s[8:11], s39 offen offset:3072 lds
Ma 220,156,188
Mv 252,156,184
s_add_u32 s43, s43, 0x60000
Mv 220,156,180
Mv 188,156,176
buffer_load_dword v27, v26, s[16:19], s43 offen
Mv 156,156,172
Mv 124,156,168
Mv 92,156,164
Mv 60,156,160
s_waitcnt vmcnt(19) lgkmcnt(0)
s_barrier
buffer_store_dword v6, v7, s[20:23], s52 offen offset:0 sc0 sc1
s_mov_b32 s52, 0x40000000
Mv 32,0,32
Rt 128,18,49152
Mv 64,0,36
Mv 96,0,40
Rt 130,18,51200
Mv 128,0,44
s_add_u32 s39, s39, 0x40000
Mv 160,0,48
Rt 132,19,49152
s_add_u32 m0, s37, 32768
Mv 192,0,52
buffer_load_dwordx4 v12, s[8:11], s39 offen offset:0 lds
Mv 224,0,56
Rt 134,19,51200
Ma 192,0,60
Ma 196,4,60
Rt 136,20,49152
Mv 228,4,56
Mv 196,4,52
Rt 138,20,51200
Mv 164,4,48
)" R"(
Mv 132,4,44
Rt 140,21,49152
Mv 100,4,40
buffer_load_dwordx4 v13, s[8:11], s39 offen offset:1024 lds
Mv 68,4,36
Rt 142,21,51200
Mv 36,4,32
Mv 40,8,32
Rt 144,22,49152
Mv 72,8,36
Mv 104,8,40
Rt 146,22,51200
Mv 136,8,44
Mv 168,8,48
Rt 148,23,49152
Mv 200,8,52
buffer_load_dwordx4 v14, s[8:11], s39 offen offset:2048 lds
Mv 232,8,56
Rt 150,23,51200
Ma 200,8,60
Ma 204,12,60
Rt 152,24,49152
Mv 236,12,56
Mv 204,12,52
Rt 154,24,51200
Mv 172,12,48
Mv 140,12,44
Rt 156,25,49152
Mv 108,12,40
buffer_load_dwordx4 v15, s[8:11], s39 offen offset:3072 lds
Mv 76,12,36
Rt 158,25,51200
Mv 44,12,32
Mv 48,16,32
Rb 160,17,32768
Mv 80,16,36
Mv 112,16,40
Rb 164,17,34816
Mv 144,16,44
Mv 176,16,48
Rb 168,17,40960
Mv 208,16,52
buffer_store_dwordx4 a[224:227], v1, s[20:23], s72 offen sc0 sc1
Mv 240,16,56
Rb 172,17,43008
s_add_u32 s72, s72, 1024
Ma 208,16,60
Ma 212,20,60
Rb 176,17,49152
Mv 244,20,56
Mv 212,20,52
Rb 180,17,51200
Mv 180,20,48
Mv 148,20,44
Rb 184,17,57344
Mv 116,20,40
Mv 84,20,36
Rb 188,17,59392
Mv 52,20,32
Mv 56,24,32
Mv 88,24,36
buffer_store_dwordx4 a[224:227], v1, s[20:23], s88 offen sc0 sc1
Mv 120,24,40
s_add_u32 s88, s88, 1024
)" R"(
Mv 152,24,44
Mv 184,24,48
Mv 216,24,52
Mv 248,24,56
Ma 216,24,60
Ma 220,28,60
Mv 252,28,56
Mv 220,28,52
Mv 188,28,48
Mv 156,28,44
Mv 124,28,40
Mv 92,28,36
s_cmp_eq_u32 s40, 3
s_cmov_b32 s72, 0x40000000
s_cmov_b32 s88, 0x40000000
Mv 60,28,32
s_waitcnt vmcnt(12) lgkmcnt(0)
s_barrier
Mv 32,128,160
Rt 0,18,0
Mv 64,128,164
s_add_u32 s38, s38, 0xffff0080
Mv 96,128,168
Rt 2,18,2048
s_add_u32 m0, s36, 32768
Mv 128,128,172
buffer_load_dwordx4 v8, s[4:7], s38 offen offset:0 lds
Mv 160,128,176
Rt 4,19,0
Mv 192,128,180
Mv 224,128,184
Rt 6,19,2048
Ma 192,128,188
buffer_load_dwordx4 v9, s[4:7], s38 offen offset:1024 lds
Ma 196,132,188
Rt 8,20,0
Mv 228,132,184
Mv 196,132,180
Rt 10,20,2048
Mv 164,132,176
buffer_load_dwordx4 v10, s[4:7], s38 offen offset:2048 lds
Mv 132,132,172
Rt 12,21,0
Mv 100,132,168
Mv 68,132,164
Rt 14,21,2048
Mv 36,132,160
buffer_load_dwordx4 v11, s[4:7], s38 offen offset:3072 lds
Mv 40,136,160
Rt 16,22,0
Mv 72,136,164
s_add_u32 s38, s38, 0x10000
Mv 104,136,168
Rt 18,22,2048
s_add_u32 m0, s36, 36864
Mv 136,136,172
buffer_load_dwordx4 v8, s[4:7], s38 offen offset:0 lds
Mv 168,136,176
Rt 20,23,0
Mv 200,136,180
Mv 232,136,184
Rt 22,23,2048
Ma 200,136,188
buffer_load_dwordx4 v9, s[4:7], s38 offen offset:1024 lds
Ma 204,140,188
)" R"(
Rt 24,24,0
Mv 236,140,184
Mv 204,140,180
Rt 26,24,2048
Mv 172,140,176
buffer_load_dwordx4 v10, s[4:7], s38 offen offset:2048 lds
Mv 140,140,172
Rt 28,25,0
Mv 108,140,168
Mv 76,140,164
Rt 30,25,2048
Mv 44,140,160
buffer_load_dwordx4 v11, s[4:7], s38 offen offset:3072 lds
Mv 48,144,160
Rb 32,16,0
Mv 80,144,164
Mv 112,144,168
Rb 36,16,2048
Mv 144,144,172
s_add_u32 s39, s39, 0x40000
Mv 176,144,176
Rb 40,16,8192
s_add_u32 m0, s37, 49152
Mv 208,144,180
buffer_load_dwordx4 v12, s[8:11], s39 offen offset:0 lds
Mv 240,144,184
Rb 44,16,10240
Ma 208,144,188
Ma 212,148,188
Rb 48,16,16384
Mv 244,148,184
Mv 212,148,180
Rb 52,16,18432
Mv 180,148,176
buffer_load_dwordx4 v13, s[8:11], s39 offen offset:1024 lds
Mv 148,148,172
Rb 56,16,24576
Mv 116,148,168
Mv 84,148,164
Rb 60,16,26624
Mv 52,148,160
Mv 56,152,160
Mv 88,152,164
buffer_load_dwordx4 v14, s[8:11], s39 offen offset:2048 lds
Mv 120,152,168
Mv 152,152,172
Mv 184,152,176
Mv 216,152,180
Mv 248,152,184
Ma 216,152,188
buffer_load_dwordx4 v15, s[8:11], s39 offen offset:3072 lds
Ma 220,156,188
Mv 252,156,184
s_add_u32 s43, s43, 0x60000
Mv 220,156,180
s_sub_u32 s40, s40, 1
Mv 188,156,176
buffer_load_dword v27, v26, s[16:19], s43 offen
Mv 156,156,172
Mv 124,156,168
Mv 92,156,164
Mv 60,156,160
s_cmp_lg_u32 s40, 0
s_waitcnt vmcnt(21) lgkmcnt(0)
s_barrier
)" R"(
s_cbranch_scc1 .LzPBK
.LzPBKX:
Mv 32,0,32
Rt 128,18,16384
Mv 64,0,36
buffer_load_dword v4, v5, s[20:23], s50 offen offset:0 sc0 sc1
Mv 96,0,40
Rt 130,18,18432
Mv 128,0,44
Mv 160,0,48
Rt 132,19,16384
Mv 192,0,52
Mv 224,0,56
Rt 134,19,18432
Ma 192,0,60
Ma 196,4,60
Rt 136,20,16384
Mv 228,4,56
Mv 196,4,52
Rt 138,20,18432
Mv 164,4,48
Mv 132,4,44
Rt 140,21,16384
Mv 100,4,40
Mv 68,4,36
Rt 142,21,18432
Mv 36,4,32
Mv 40,8,32
Rt 144,22,16384
Mv 72,8,36
Mv 104,8,40
Rt 146,22,18432
Mv 136,8,44
Mv 168,8,48
Rt 148,23,16384
Mv 200,8,52
Mv 232,8,56
Rt 150,23,18432
Ma 200,8,60
Ma 204,12,60
Rt 152,24,16384
Mv 236,12,56
Mv 204,12,52
Rt 154,24,18432
Mv 172,12,48
Mv 140,12,44
Rt 156,25,16384
Mv 108,12,40
Mv 76,12,36
Rt 158,25,18432
Mv 44,12,32
Mv 48,16,32
Rb 160,17,0
Mv 80,16,36
Mv 112,16,40
Rb 164,17,2048
Mv 144,16,44
Mv 176,16,48
Rb 168,17,8192
Mv 208,16,52
Mv 240,16,56
Rb 172,17,10240
Ma 208,16,60
s_waitcnt vmcnt(0)
)" R"(
v_cmp_eq_u32_e32 vcc, s3, v4
s_andn2_b64 s[28:29], -1, vcc
s_cbranch_scc1 .Lzsp14
.Lzok14:
Ma 212,20,60
Rb 176,17,16384
Mv 244,20,56
s_lshl3_add_u32 s29, s34, s35
s_lshl2_add_u32 s29, s29, s33
Mv 212,20,52
Rb 180,17,18432
s_add_u32 m0, s36, 131072
Mv 180,20,48
buffer_load_dwordx4 v2, s[20:23], s56 offen offset:0 sc1 lds
Mv 148,20,44
Rb 184,17,24576
s_mul_i32 s83, s29, 7168
Mv 116,20,40
s_lshl_b32 s82, s34, 21
s_add_u32 s82, s82, 0x6000000
Mv 84,20,36
Rb 188,17,26624
s_add_u32 s82, s82, s84
Mv 52,20,32
s_mov_b32 s24, s80
s_mov_b32 s25, s81
Mv 56,24,32
s_mov_b32 s26, 0x380000
s_mov_b32 s27, 0x20000
Mv 88,24,36
buffer_load_dwordx4 v2, s[20:23], s56 offen offset:1024 sc1 lds
Mv 120,24,40
s_add_u32 s28, s33, 0
s_lshl2_add_u32 s28, s35, s28
Mv 152,24,44
s_mul_i32 s29, s28, 293
s_lshr_b32 s29, s29, 11
Mv 184,24,48
s_mul_i32 s29, s29, 9
s_add_u32 s29, s29, s28
Mv 216,24,52
s_lshl_b32 s29, s29, 10
s_add_u32 s4, s29, s82
Mv 248,24,56
s_add_u32 s28, s33, 4
s_lshl3_add_u32 s28, s28, s35
Ma 216,24,60
buffer_load_dwordx4 v2, s[20:23], s56 offen offset:2048 sc1 lds
Ma 220,28,60
s_mul_i32 s29, s28, 293
s_lshr_b32 s29, s29, 11
Mv 252,28,56
s_mul_i32 s29, s29, 9
s_add_u32 s29, s29, s28
Mv 220,28,52
s_lshl_b32 s29, s29, 10
s_add_u32 s5, s29, s82
Mv 188,28,48
s_add_u32 s28, s33, 8
s_lshl3_add_u32 s28, s28, s35
Mv 156,28,44
s_mul_i32 s29, s28, 293
s_lshr_b32 s29, s29, 11
Mv 124,28,40
)" R"(
buffer_load_dwordx4 v2, s[20:23], s56 offen offset:3072 sc1 lds
Mv 92,28,36
s_mul_i32 s29, s29, 9
s_add_u32 s29, s29, s28
Mv 60,28,32
s_waitcnt lgkmcnt(0)
s_barrier
Mv 32,128,160
Rt 0,18,32768
s_lshl_b32 s29, s29, 10
Mv 64,128,164
s_add_u32 s6, s29, s82
s_add_u32 s28, s33, 12
Mv 96,128,168
Rt 2,18,34816
s_add_u32 m0, s36, 0
Mv 128,128,172
buffer_load_dwordx4 v2, s[20:23], s55 offen offset:0 sc1 lds
Mv 160,128,176
Rt 4,19,32768
s_lshl3_add_u32 s28, s28, s35
Mv 192,128,180
s_mul_i32 s29, s28, 293
s_lshr_b32 s29, s29, 11
Mv 224,128,184
Rt 6,19,34816
s_mul_i32 s29, s29, 9
Ma 192,128,188
s_add_u32 s29, s29, s28
s_lshl_b32 s29, s29, 10
Ma 196,132,188
Rt 8,20,32768
buffer_load_dwordx4 v2, s[20:23], s55 offen offset:1024 sc1 lds
Mv 228,132,184
s_add_u32 s7, s29, s82
Mv 196,132,180
Rt 10,20,34816
Mv 164,132,176
s_cmp_gt_u32 s33, 1
s_cmov_b32 s7, 0x40000000
Mv 132,132,172
Rt 12,21,32768
s_add_u32 s28, s33, 14
Mv 100,132,168
buffer_load_dwordx4 v2, s[20:23], s55 offen offset:2048 sc1 lds
Mv 68,132,164
Rt 14,21,34816
s_lshl3_add_u32 s28, s28, s35
Mv 36,132,160
s_mul_i32 s29, s28, 293
s_lshr_b32 s29, s29, 11
Mv 40,136,160
Rt 16,22,32768
s_mul_i32 s29, s29, 9
Mv 72,136,164
s_add_u32 s29, s29, s28
s_lshl_b32 s29, s29, 10
Mv 104,136,168
Rt 18,22,34816
buffer_load_dwordx4 v2, s[20:23], s55 offen offset:3072 sc1 lds
Mv 136,136,172
s_add_u32 s8, s29, s82
s_add_u32 s28, s33, 18
Mv 168,136,176
Rt 20,23,32768
)" R"(
s_lshl3_add_u32 s28, s28, s35
Mv 200,136,180
s_mul_i32 s29, s28, 293
s_lshr_b32 s29, s29, 11
Mv 232,136,184
Rt 22,23,34816
s_add_u32 m0, s36, 4096
Ma 200,136,188
buffer_load_dwordx4 v3, s[20:23], s55 offen offset:0 sc1 lds
Ma 204,140,188
Rt 24,24,32768
s_mul_i32 s29, s29, 9
Mv 236,140,184
s_add_u32 s29, s29, s28
s_lshl_b32 s29, s29, 10
Mv 204,140,180
Rt 26,24,34816
s_add_u32 s9, s29, s82
Mv 172,140,176
s_add_u32 s28, s33, 22
s_lshl3_add_u32 s28, s28, s35
Mv 140,140,172
Rt 28,25,32768
buffer_load_dwordx4 v3, s[20:23], s55 offen offset:1024 sc1 lds
Mv 108,140,168
s_mul_i32 s29, s28, 293
s_lshr_b32 s29, s29, 11
Mv 76,140,164
Rt 30,25,34816
s_mul_i32 s29, s29, 9
Mv 44,140,160
s_add_u32 s29, s29, s28
s_lshl_b32 s29, s29, 10
Mv 48,144,160
Rb 32,16,32768
s_add_u32 s10, s29, s82
Mv 80,144,164
buffer_load_dwordx4 v3, s[20:23], s55 offen offset:2048 sc1 lds
Mv 112,144,168
Rb 36,16,34816
s_add_u32 s28, s33, 26
Mv 144,144,172
s_lshl3_add_u32 s28, s28, s35
s_mul_i32 s29, s28, 293
Mv 176,144,176
Rb 40,16,40960
s_lshr_b32 s29, s29, 11
Mv 208,144,180
s_mul_i32 s29, s29, 9
s_add_u32 s29, s29, s28
Mv 240,144,184
Rb 44,16,43008
buffer_load_dwordx4 v3, s[20:23], s55 offen offset:3072 sc1 lds
Ma 208,144,188
s_lshl_b32 s29, s29, 10
s_add_u32 s11, s29, s82
Ma 212,148,188
Rb 48,16,49152
Mv 244,148,184
s_cmp_gt_u32 s33, 1
s_cmov_b32 s11, 0x40000000
Mv 212,148,180
Rb 52,16,51200
s_add_u32 m0, s36, 135168
)" R"(
Mv 180,148,176
buffer_load_dwordx4 v3, s[20:23], s56 offen offset:0 sc1 lds
Mv 148,148,172
Rb 56,16,57344
s_and_b32 s28, s34, 1
Mv 116,148,168
s_lshl_b32 s28, s28, 14
s_add_u32 s82, s37, s28
Mv 84,148,164
Rb 60,16,59392
s_add_u32 s82, s82, 0x10000
Mv 52,148,160
Mv 56,152,160
buffer_load_dwordx4 v3, s[20:23], s56 offen offset:1024 sc1 lds
Mv 88,152,164
Mv 120,152,168
Mv 152,152,172
Mv 184,152,176
Mv 216,152,180
buffer_load_dwordx4 v3, s[20:23], s56 offen offset:2048 sc1 lds
Mv 248,152,184
Ma 216,152,188
Ma 220,156,188
Mv 252,156,184
Mv 220,156,180
buffer_load_dwordx4 v3, s[20:23], s56 offen offset:3072 sc1 lds
Mv 188,156,176
Mv 156,156,172
Mv 124,156,168
s_add_u32 s55, s55, 0x10000
s_add_u32 s56, s56, 0x10000
Mv 92,156,164
Mv 60,156,160
s_waitcnt lgkmcnt(4)
s_barrier
Mv 32,0,32
Rt 128,18,49152
Mv 64,0,36
Rt 130,18,51200
Mv 96,0,40
Rt 132,19,49152
s_add_u32 m0, s36, 65536
Mv 128,0,44
Rt 134,19,51200
buffer_load_dwordx4 v2, s[20:23], s56 offen offset:0 sc1 lds
Mv 132,4,44
Rt 136,20,49152
Mv 100,4,40
Rt 138,20,51200
s_add_u32 s50, s50, 1024
Mv 68,4,36
Rt 140,21,49152
s_min_u32 s50, s50, s60
Mv 36,4,32
Rt 142,21,51200
Mv 40,8,32
Rt 144,22,49152
Mv 72,8,36
Rt 146,22,51200
Mv 104,8,40
Rt 148,23,49152
Mv 136,8,44
Rt 150,23,51200
buffer_load_dwordx4 v2, s[20:23], s56 offen offset:1024 sc1 lds
Mv 140,12,44
)" R"(
Rt 152,24,49152
Mv 108,12,40
Rt 154,24,51200
Mv 76,12,36
Rt 156,25,49152
Mv 44,12,32
Rt 158,25,51200
Mv 48,16,32
Rb 160,17,32768
Mv 80,16,36
Rb 164,17,34816
Mv 112,16,40
Rb 168,17,40960
Mv 144,16,44
Rb 172,17,43008
buffer_load_dwordx4 v2, s[20:23], s56 offen offset:2048 sc1 lds
Mv 148,20,44
Rb 176,17,49152
Mv 116,20,40
Rb 180,17,51200
Mv 84,20,36
Rb 184,17,57344
Mv 52,20,32
Rb 188,17,59392
Mv 56,24,32
v_add_u32 v8, 0x10000, v1
v_add_u32 v10, 0x20000, v1
Mv 88,24,36
buffer_load_dword v4, v5, s[20:23], s50 offen offset:0 sc0 sc1
Mv 120,24,40
s_mov_b32 s93, 2
s_mov_b32 s94, 0
Mv 152,24,44
buffer_load_dwordx4 v2, s[20:23], s56 offen offset:3072 sc1 lds
Mv 156,28,44
Mv 124,28,40
Mv 92,28,36
Mv 60,28,32
s_waitcnt lgkmcnt(0)
s_barrier
Mv 32,128,160
Mv 36,132,160
Mv 40,136,160
s_add_u32 m0, s36, 32768
Mv 44,140,160
buffer_load_dwordx4 v2, s[20:23], s55 offen offset:0 sc1 lds
Mv 48,144,160
Mv 52,148,160
s_nop 0
Cv 32,32,33
Cv 33,34,35
Mv 56,152,160
Cv 34,36,37
Cv 35,38,39
Mv 60,156,160
Cv 40,40,41
Cv 41,42,43
Mv 64,128,164
Cv 42,44,45
Cv 43,46,47
Mv 68,132,164
Cv 48,48,49
Cv 49,50,51
Mv 72,136,164
Cv 50,52,53
)" R"(
Cv 51,54,55
Mv 76,140,164
buffer_load_dwordx4 v2, s[20:23], s55 offen offset:1024 sc1 lds
Mv 80,144,164
Cv 56,56,57
Cv 57,58,59
Mv 84,148,164
Cv 58,60,61
Cv 59,62,63
Mv 88,152,164
Cv 64,64,65
Cv 65,66,67
Mv 92,156,164
Cv 66,68,69
Cv 67,70,71
Mv 96,128,168
Cv 72,72,73
Cv 73,74,75
Mv 100,132,168
Cv 74,76,77
Cv 75,78,79
Mv 104,136,168
Cv 80,80,81
Cv 81,82,83
Mv 108,140,168
buffer_load_dwordx4 v2, s[20:23], s55 offen offset:2048 sc1 lds
Mv 112,144,168
Cv 82,84,85
Cv 83,86,87
Mv 116,148,168
Cv 88,88,89
Cv 89,90,91
Mv 120,152,168
Cv 90,92,93
Cv 91,94,95
Mv 124,156,168
buffer_store_dwordx4 v[32:35], v2, s[20:23], s45 offen offset:0 sc0 sc1
Mv 128,128,172
Mv 132,132,172
Mv 136,136,172
Mv 140,140,172
buffer_load_dwordx4 v2, s[20:23], s55 offen offset:3072 sc1 lds
Mv 144,144,172
Mv 148,148,172
s_nop 1
Cv 128,128,129
Cv 129,130,131
Mv 152,152,172
Cv 130,132,133
Cv 131,134,135
Mv 156,156,172
buffer_store_dwordx4 v[40:43], v2, s[20:23], s45 offen offset:1024 sc0 sc1
Mv 160,0,48
Cv 136,136,137
Cv 137,138,139
Mv 160,128,176
Cv 138,140,141
Cv 139,142,143
Mv 164,4,48
s_add_u32 m0, s36, 36864
Cv 144,144,145
Mv 164,132,176
buffer_load_dwordx4 v3, s[20:23], s55 offen offset:0 sc1 lds
Mv 168,8,48
)" R"(
Cv 145,146,147
Cv 146,148,149
Mv 168,136,176
Cv 147,150,151
Cv 152,152,153
Mv 172,12,48
Cv 153,154,155
Cv 154,156,157
Mv 172,140,176
buffer_store_dwordx4 v[48:51], v2, s[20:23], s45 offen offset:2048 sc0 sc1
Mv 176,16,48
Cv 155,158,159
Cv 132,96,97
Mv 176,144,176
Cv 133,98,99
Cv 134,100,101
Mv 180,20,48
Cv 135,102,103
Cv 140,104,105
Mv 180,148,176
buffer_load_dwordx4 v3, s[20:23], s55 offen offset:1024 sc1 lds
Mv 184,24,48
Cv 141,106,107
Cv 142,108,109
Mv 184,152,176
Cv 143,110,111
Cv 148,112,113
Mv 188,28,48
Cv 149,114,115
Cv 150,116,117
Mv 188,156,176
buffer_store_dwordx4 v[56:59], v2, s[20:23], s45 offen offset:3072 sc0 sc1
Mv 192,0,52
Cv 151,118,119
Cv 156,120,121
Mv 192,128,180
Cv 157,122,123
Cv 158,124,125
Mv 196,4,52
Cv 159,126,127
Cv 160,160,161
Mv 196,132,180
buffer_load_dwordx4 v3, s[20:23], s55 offen offset:2048 sc1 lds
Mv 200,8,52
Cv 161,162,163
Cv 162,164,165
Mv 200,136,180
Cv 163,166,167
v_accvgpr_write_b32 a224, v160
Mv 204,12,52
v_accvgpr_write_b32 a225, v161
v_accvgpr_write_b32 a226, v162
Mv 204,140,180
buffer_store_dwordx4 v[64:67], v3, s[20:23], s45 offen offset:0 sc0 sc1
Mv 208,16,52
v_accvgpr_write_b32 a227, v163
Cv 168,168,169
Mv 208,144,180
Cv 169,170,171
Cv 170,172,173
Mv 212,20,52
Cv 171,174,175
v_accvgpr_write_b32 a228, v168
Mv 212,148,180
)" R"(
buffer_load_dwordx4 v3, s[20:23], s55 offen offset:3072 sc1 lds
Mv 216,24,52
v_accvgpr_write_b32 a229, v169
v_accvgpr_write_b32 a230, v170
Mv 216,152,180
v_accvgpr_write_b32 a231, v171
Cv 176,176,177
Mv 220,28,52
Cv 177,178,179
Cv 178,180,181
Mv 220,156,180
buffer_store_dwordx4 v[72:75], v3, s[20:23], s45 offen offset:1024 sc0 sc1
Mv 224,0,56
Cv 179,182,183
v_accvgpr_write_b32 a232, v176
Ma 192,0,60
v_accvgpr_write_b32 a233, v177
v_accvgpr_write_b32 a234, v178
Ma 196,4,60
s_add_u32 m0, s36, 69632
v_accvgpr_write_b32 a235, v179
Mv 228,4,56
buffer_load_dwordx4 v3, s[20:23], s56 offen offset:0 sc1 lds
Mv 232,8,56
Cv 184,184,185
Cv 185,186,187
Ma 200,8,60
Cv 186,188,189
Cv 187,190,191
Ma 204,12,60
v_accvgpr_write_b32 a236, v184
v_accvgpr_write_b32 a237, v185
Mv 236,12,56
s_waitcnt vmcnt(16)
s_barrier
Mv 240,16,56
v_cmp_eq_u32_e32 vcc, s3, v4
s_andn2_b64 s[28:29], -1, vcc
s_cbranch_scc1 .Lzsp15
.Lzok15:
Ma 208,16,60
buffer_store_dwordx4 v[80:83], v3, s[20:23], s45 offen offset:2048 sc0 sc1
Ma 212,20,60
v_accvgpr_write_b32 a238, v186
v_accvgpr_write_b32 a239, v187
Mv 244,20,56
buffer_load_dwordx4 v3, s[20:23], s56 offen offset:1024 sc1 lds
Mv 248,24,56
Cv 192,192,193
Cv 193,194,195
Ma 216,24,60
Cv 194,196,197
Cv 195,198,199
Ma 220,28,60
v_accvgpr_write_b32 a240, v192
v_accvgpr_write_b32 a241, v193
Mv 252,28,56
buffer_store_dwordx4 v[88:91], v3, s[20:23], s45 offen offset:3072 sc0 sc1
Mv 224,128,184
Rb 0,1,0
v_accvgpr_write_b32 a242, v194
Mv 228,132,184
Rb 4,1,1024
v_accvgpr_write_b32 a243, v195
Mv 232,136,184
)" R"(
Rb 8,1,2048
Cv 200,200,201
Mv 236,140,184
buffer_load_dwordx4 v3, s[20:23], s56 offen offset:2048 sc1 lds
Rb 12,1,3072
Mv 240,144,184
Rb 16,1,8192
Cv 201,202,203
Mv 244,148,184
Rb 20,1,9216
Cv 202,204,205
Mv 248,152,184
Rb 24,1,10240
Cv 203,206,207
Mv 252,156,184
Rb 28,1,11264
v_accvgpr_write_b32 a244, v200
Ma 192,128,188
Rb 32,1,4096
v_accvgpr_write_b32 a245, v201
Ma 196,132,188
Rb 36,1,5120
v_accvgpr_write_b32 a246, v202
Ma 200,136,188
Rb 40,1,6144
v_accvgpr_write_b32 a247, v203
Ma 204,140,188
buffer_load_dwordx4 v3, s[20:23], s56 offen offset:3072 sc1 lds
Rb 44,1,7168
Ma 208,144,188
Ma 212,148,188
Ma 216,152,188
Ma 220,156,188
s_add_u32 s56, s56, 0x10000
s_branch .LzFB
.LzBV:
Mz 32,32,0
Rt 128,18,16384
v_mul_f32_e32 v64, s54, v64
Mz 36,36,0
v_mul_f32_e32 v65, s54, v65
Cv 64,64,65
Mz 96,40,0
Rt 130,18,18432
v_accvgpr_write_b32 a112, v64
Mz 100,44,0
s_add_u32 s39, s39, 0x40000
v_mul_f32_e32 v66, s54, v66
Mz 160,48,0
Rt 132,19,16384
s_add_u32 m0, s37, 0
Mz 164,52,0
buffer_load_dwordx4 v12, s[8:11], s39 offen offset:0 lds
Mz 224,56,0
Rt 134,19,18432
v_mul_f32_e32 v67, s54, v67
Mz 228,60,0
Cv 66,66,67
v_accvgpr_write_b32 a113, v66
Mz 236,60,4
Rt 136,20,16384
v_mul_f32_e32 v68, s54, v68
Mz 232,56,4
v_mul_f32_e32 v69, s54, v69
)" R"(
Cv 68,68,69
buffer_store_dwordx4 a[64:67], v2, s[20:23], s51 offen offset:0 sc0 sc1
Mz 172,52,4
Rt 138,20,18432
v_accvgpr_write_b32 a114, v68
Mz 168,48,4
v_mul_f32_e32 v70, s54, v70
v_mul_f32_e32 v71, s54, v71
Mz 108,44,4
Rt 140,21,16384
Cv 70,70,71
Mz 104,40,4
buffer_load_dwordx4 v13, s[8:11], s39 offen offset:1024 lds
Mz 44,36,4
Rt 142,21,18432
v_accvgpr_write_b32 a115, v70
Mz 40,32,4
v_mul_f32_e32 v72, s54, v72
v_mul_f32_e32 v73, s54, v73
Mz 48,32,8
Rt 144,22,16384
Cv 72,72,73
Mz 52,36,8
v_accvgpr_write_b32 a116, v72
v_mul_f32_e32 v74, s54, v74
buffer_store_dwordx4 a[68:71], v2, s[20:23], s51 offen offset:1024 sc0 sc1
Mz 112,40,8
Rt 146,22,18432
v_mul_f32_e32 v75, s54, v75
Mz 116,44,8
Cv 74,74,75
v_accvgpr_write_b32 a117, v74
Mz 176,48,8
Rt 148,23,16384
v_mul_f32_e32 v76, s54, v76
Mz 180,52,8
buffer_load_dwordx4 v14, s[8:11], s39 offen offset:2048 lds
Mz 240,56,8
Rt 150,23,18432
v_mul_f32_e32 v77, s54, v77
Mz 244,60,8
Cv 76,76,77
v_accvgpr_write_b32 a118, v76
Mz 252,60,12
Rt 152,24,16384
v_mul_f32_e32 v78, s54, v78
Mz 248,56,12
v_mul_f32_e32 v79, s54, v79
Cv 78,78,79
buffer_store_dwordx4 a[72:75], v2, s[20:23], s51 offen offset:2048 sc0 sc1
Mz 188,52,12
Rt 154,24,18432
v_accvgpr_write_b32 a119, v78
Mz 184,48,12
v_mul_f32_e32 v80, s54, v80
v_mul_f32_e32 v81, s54, v81
Mz 124,44,12
Rt 156,25,16384
Cv 80,80,81
Mz 120,40,12
buffer_load_dwordx4 v15, s[8:11], s39 offen offset:3072 lds
Mz 60,36,12
Rt 158,25,18432
v_accvgpr_write_b32 a120, v80
)" R"(
Mz 56,32,12
v_mul_f32_e32 v82, s54, v82
v_mul_f32_e32 v83, s54, v83
Mz 128,40,16
Rb 160,17,0
Cv 82,82,83
Mz 132,44,16
v_accvgpr_write_b32 a121, v82
v_mul_f32_e32 v84, s54, v84
buffer_store_dwordx4 a[76:79], v2, s[20:23], s51 offen offset:3072 sc0 sc1
Mz 192,48,16
Rb 164,17,2048
v_mul_f32_e32 v85, s54, v85
Mz 196,52,16
Cv 84,84,85
v_accvgpr_write_b32 a122, v84
v_mfma_f32_16x16x32_bf16 a[192:195], a[56:59], a[16:19], 0
Rb 168,17,8192
v_mul_f32_e32 v86, s54, v86
v_mfma_f32_16x16x32_bf16 a[196:199], a[60:63], a[16:19], 0
v_mul_f32_e32 v87, s54, v87
Cv 86,86,87
v_mfma_f32_16x16x32_bf16 a[204:207], a[60:63], a[20:23], 0
Rb 172,17,10240
v_accvgpr_write_b32 a123, v86
v_mfma_f32_16x16x32_bf16 a[200:203], a[56:59], a[20:23], 0
v_mul_f32_e32 v88, s54, v88
v_mul_f32_e32 v89, s54, v89
Mz 204,52,20
Rb 176,17,16384
Cv 88,88,89
Mz 200,48,20
v_accvgpr_write_b32 a124, v88
v_mul_f32_e32 v90, s54, v90
buffer_store_dwordx4 a[80:83], v3, s[20:23], s51 offen offset:0 sc0 sc1
Mz 140,44,20
Rb 180,17,18432
v_mul_f32_e32 v91, s54, v91
Mz 136,40,20
Cv 90,90,91
v_accvgpr_write_b32 a125, v90
Mz 144,40,24
Rb 184,17,24576
v_mul_f32_e32 v92, s54, v92
Mz 148,44,24
v_mul_f32_e32 v93, s54, v93
Cv 92,92,93
Mz 208,48,24
Rb 188,17,26624
v_accvgpr_write_b32 a126, v92
Mz 212,52,24
v_mul_f32_e32 v94, s54, v94
v_mul_f32_e32 v95, s54, v95
v_mfma_f32_16x16x32_bf16 a[208:211], a[56:59], a[24:27], 0
Cv 94,94,95
v_accvgpr_write_b32 a127, v94
v_mfma_f32_16x16x32_bf16 a[212:215], a[60:63], a[24:27], 0
buffer_store_dwordx4 a[84:87], v3, s[20:23], s51 offen offset:1024 sc0 sc1
v_mfma_f32_16x16x32_bf16 a[220:223], a[60:63], a[28:31], 0
v_mfma_f32_16x16x32_bf16 a[216:219], a[56:59], a[28:31], 0
Mz 220,52,28
Mz 216,48,28
Mz 156,44,28
Mz 152,40,28
)" R"(
Mz 64,32,16
Mz 68,36,16
buffer_store_dwordx4 a[88:91], v3, s[20:23], s51 offen offset:2048 sc0 sc1
Mz 72,32,20
Mz 76,36,20
Mz 80,32,24
Mz 84,36,24
Mz 88,32,28
Mz 92,36,28
s_waitcnt vmcnt(16) lgkmcnt(0)
s_barrier
Mv 32,160,128
Rt 0,18,32768
Mv 36,164,128
s_add_u32 s38, s38, 0xffff0080
Mv 96,168,128
Rt 2,18,34816
s_add_u32 m0, s36, 0
Mv 100,172,128
buffer_load_dwordx4 v8, s[4:7], s38 offen offset:0 lds
Mv 160,176,128
Rt 4,19,32768
Mv 164,180,128
Mv 224,184,128
Rt 6,19,34816
Mv 228,188,128
buffer_load_dwordx4 v9, s[4:7], s38 offen offset:1024 lds
Mv 236,188,132
Rt 8,20,32768
Mv 232,184,132
Mv 172,180,132
Rt 10,20,34816
Mv 168,176,132
buffer_load_dwordx4 v10, s[4:7], s38 offen offset:2048 lds
Mv 108,172,132
Rt 12,21,32768
Mv 104,168,132
Mv 44,164,132
Rt 14,21,34816
Mv 40,160,132
buffer_load_dwordx4 v11, s[4:7], s38 offen offset:3072 lds
Mv 48,160,136
Rt 16,22,32768
Mv 52,164,136
s_add_u32 s38, s38, 0x10000
Mv 112,168,136
Rt 18,22,34816
s_add_u32 m0, s36, 4096
Mv 116,172,136
buffer_load_dwordx4 v8, s[4:7], s38 offen offset:0 lds
Mv 176,176,136
Rt 20,23,32768
Mv 180,180,136
Mv 240,184,136
Rt 22,23,34816
Mv 244,188,136
buffer_load_dwordx4 v9, s[4:7], s38 offen offset:1024 lds
Mv 252,188,140
Rt 24,24,32768
Mv 248,184,140
Mv 188,180,140
Rt 26,24,34816
Mv 184,176,140
buffer_load_dwordx4 v10, s[4:7], s38 offen offset:2048 lds
Mv 124,172,140
)" R"(
Rt 28,25,32768
Mv 120,168,140
Mv 60,164,140
Rt 30,25,34816
Mv 56,160,140
buffer_load_dwordx4 v11, s[4:7], s38 offen offset:3072 lds
Mv 64,160,144
Rb 32,16,32768
Mv 68,164,144
Mv 128,168,144
Rb 36,16,34816
Mv 132,172,144
s_add_u32 s39, s39, 0x40000
Mv 192,176,144
Rb 40,16,40960
s_add_u32 m0, s37, 16384
Mv 196,180,144
buffer_load_dwordx4 v12, s[8:11], s39 offen offset:0 lds
Ma 192,184,144
Rb 44,16,43008
Ma 196,188,144
Ma 204,188,148
Rb 48,16,49152
Ma 200,184,148
Mv 204,180,148
Rb 52,16,51200
Mv 200,176,148
buffer_load_dwordx4 v13, s[8:11], s39 offen offset:1024 lds
Mv 140,172,148
Rb 56,16,57344
Mv 136,168,148
Mv 76,164,148
Rb 60,16,59392
Mv 72,160,148
Mv 80,160,152
Mv 84,164,152
buffer_load_dwordx4 v14, s[8:11], s39 offen offset:2048 lds
Mv 144,168,152
Mv 148,172,152
Mv 208,176,152
Mv 212,180,152
Ma 208,184,152
Ma 212,188,152
buffer_load_dwordx4 v15, s[8:11], s39 offen offset:3072 lds
Ma 220,188,156
Ma 216,184,156
Mv 220,180,156
Mv 216,176,156
buffer_store_dwordx4 a[92:95], v3, s[20:23], s51 offen offset:3072 sc0 sc1
Mv 156,172,156
Mv 152,168,156
Mv 92,164,156
Mv 88,160,156
s_waitcnt vmcnt(25) lgkmcnt(0)
s_barrier
Mv 32,32,0
Rt 128,18,49152
Mv 36,36,0
Mv 96,40,0
Rt 130,18,51200
Mv 100,44,0
s_add_u32 s39, s39, 0x40000
Mv 160,48,0
Rt 132,19,49152
s_add_u32 m0, s37, 32768
)" R"(
Mv 164,52,0
buffer_load_dwordx4 v12, s[8:11], s39 offen offset:0 lds
Mv 224,56,0
Rt 134,19,51200
Mv 228,60,0
Mv 236,60,4
Rt 136,20,49152
Mv 232,56,4
s_add_u32 s51, s51, 0x10000
buffer_store_dwordx4 a[96:99], v2, s[20:23], s51 offen offset:0 sc0 sc1
Mv 172,52,4
Rt 138,20,51200
Mv 168,48,4
Mv 108,44,4
Rt 140,21,49152
Mv 104,40,4
buffer_load_dwordx4 v13, s[8:11], s39 offen offset:1024 lds
Mv 44,36,4
Rt 142,21,51200
Mv 40,32,4
Mv 48,32,8
Rt 144,22,49152
Mv 52,36,8
buffer_store_dwordx4 a[100:103], v2, s[20:23], s51 offen offset:1024 sc0 sc1
Mv 112,40,8
Rt 146,22,51200
Mv 116,44,8
Mv 176,48,8
Rt 148,23,49152
Mv 180,52,8
buffer_load_dwordx4 v14, s[8:11], s39 offen offset:2048 lds
Mv 240,56,8
Rt 150,23,51200
Mv 244,60,8
Mv 252,60,12
Rt 152,24,49152
Mv 248,56,12
buffer_store_dwordx4 a[104:107], v2, s[20:23], s51 offen offset:2048 sc0 sc1
Mv 188,52,12
Rt 154,24,51200
Mv 184,48,12
Mv 124,44,12
Rt 156,25,49152
Mv 120,40,12
buffer_load_dwordx4 v15, s[8:11], s39 offen offset:3072 lds
Mv 60,36,12
Rt 158,25,51200
Mv 56,32,12
Mv 64,32,16
Rb 160,17,32768
Mv 68,36,16
buffer_store_dwordx4 a[108:111], v2, s[20:23], s51 offen offset:3072 sc0 sc1
Mv 128,40,16
Rb 164,17,34816
s_mov_b64 s[16:17], s[70:71]
Mv 132,44,16
s_mov_b32 s18, 0x188000
Mv 192,48,16
Rb 168,17,40960
Mv 196,52,16
s_cmp_eq_u32 s33, 0
s_cmov_b32 s18, 0x380000
Ma 192,56,16
Rb 172,17,43008
)" R"(
s_mul_i32 s43, s2, 3072
Ma 196,60,16
s_cmp_eq_u32 s33, 0
s_cselect_b32 s44, 3, 6
Ma 204,60,20
Rb 176,17,49152
s_mul_i32 s44, s44, 0x60000
Ma 200,56,20
buffer_store_dwordx4 a[112:115], v3, s[20:23], s51 offen offset:0 sc0 sc1
Mv 204,52,20
Rb 180,17,51200
s_sub_u32 s43, s43, s44
Mv 200,48,20
v_min_u32 v26, 23, v0
v_lshlrev_b32 v26, 7, v26
Mv 140,44,20
Rb 184,17,57344
v_and_b32 v5, 7, v0
Mv 136,40,20
v_lshlrev_b32 v5, 7, v5
Mv 76,36,20
Rb 188,17,59392
Mv 72,32,20
Mv 80,32,24
Mv 84,36,24
buffer_store_dwordx4 a[116:119], v3, s[20:23], s51 offen offset:1024 sc0 sc1
Mv 144,40,24
Mv 148,44,24
Mv 208,48,24
Mv 212,52,24
Ma 208,56,24
Ma 212,60,24
Ma 220,60,28
Ma 216,56,28
buffer_store_dwordx4 a[120:123], v3, s[20:23], s51 offen offset:2048 sc0 sc1
Mv 220,52,28
Mv 216,48,28
Mv 156,44,28
Mv 152,40,28
Mv 92,36,28
Mv 88,32,28
s_waitcnt vmcnt(16) lgkmcnt(0)
s_barrier
Mv 32,160,128
Rt 0,18,0
Mv 36,164,128
s_add_u32 s38, s38, 0xffff0080
Mv 96,168,128
Rt 2,18,2048
s_add_u32 m0, s36, 32768
Mv 100,172,128
buffer_load_dwordx4 v8, s[4:7], s38 offen offset:0 lds
Mv 160,176,128
Rt 4,19,0
Mv 164,180,128
Mv 224,184,128
Rt 6,19,2048
Mv 228,188,128
buffer_load_dwordx4 v9, s[4:7], s38 offen offset:1024 lds
Mv 236,188,132
Rt 8,20,0
Mv 232,184,132
Mv 172,180,132
Rt 10,20,2048
Mv 168,176,132
)" R"(
buffer_load_dwordx4 v10, s[4:7], s38 offen offset:2048 lds
Mv 108,172,132
Rt 12,21,0
Mv 104,168,132
Mv 44,164,132
Rt 14,21,2048
Mv 40,160,132
buffer_load_dwordx4 v11, s[4:7], s38 offen offset:3072 lds
Mv 48,160,136
Rt 16,22,0
Mv 52,164,136
s_add_u32 s38, s38, 0x10000
Mv 112,168,136
Rt 18,22,2048
s_add_u32 m0, s36, 36864
Mv 116,172,136
buffer_load_dwordx4 v8, s[4:7], s38 offen offset:0 lds
Mv 176,176,136
Rt 20,23,0
Mv 180,180,136
Mv 240,184,136
Rt 22,23,2048
Mv 244,188,136
buffer_load_dwordx4 v9, s[4:7], s38 offen offset:1024 lds
Mv 252,188,140
Rt 24,24,0
Mv 248,184,140
Mv 188,180,140
Rt 26,24,2048
Mv 184,176,140
buffer_load_dwordx4 v10, s[4:7], s38 offen offset:2048 lds
Mv 124,172,140
Rt 28,25,0
Mv 120,168,140
Mv 60,164,140
Rt 30,25,2048
v_accvgpr_write_b32 a224, -1
Mv 56,160,140
buffer_load_dwordx4 v11, s[4:7], s38 offen offset:3072 lds
Mv 64,160,144
Rb 32,16,0
v_accvgpr_write_b32 a225, -1
Mv 68,164,144
v_accvgpr_write_b32 a226, -1
v_accvgpr_write_b32 a227, -1
Mv 128,168,144
Rb 36,16,2048
s_add_u32 s88, s73, 0x4000
Mv 132,172,144
s_add_u32 s39, s39, 0x40000
s_mov_b32 s40, 6
Mv 192,176,144
Rb 40,16,8192
s_add_u32 m0, s37, 49152
Mv 196,180,144
buffer_load_dwordx4 v12, s[8:11], s39 offen offset:0 lds
Ma 192,184,144
Rb 44,16,10240
s_mov_b32 s94, 0
Ma 196,188,144
Ma 204,188,148
Rb 48,16,16384
Ma 200,184,148
Mv 204,180,148
)" R"(
Rb 52,16,18432
Mv 200,176,148
buffer_load_dwordx4 v13, s[8:11], s39 offen offset:1024 lds
Mv 140,172,148
Rb 56,16,24576
Mv 136,168,148
Mv 76,164,148
Rb 60,16,26624
Mv 72,160,148
Mv 80,160,152
Mv 84,164,152
buffer_load_dwordx4 v14, s[8:11], s39 offen offset:2048 lds
Mv 144,168,152
Mv 148,172,152
Mv 208,176,152
Mv 212,180,152
Ma 208,184,152
Ma 212,188,152
buffer_load_dwordx4 v15, s[8:11], s39 offen offset:3072 lds
Ma 220,188,156
Ma 216,184,156
Mv 220,180,156
Mv 216,176,156
buffer_store_dwordx4 a[124:127], v3, s[20:23], s51 offen offset:3072 sc0 sc1
Mv 156,172,156
Mv 152,168,156
Mv 92,164,156
Mv 88,160,156
s_waitcnt vmcnt(25) lgkmcnt(0)
s_barrier
.LzPBV:
Mv 32,32,0
Rt 128,18,16384
Mv 36,36,0
Mv 96,40,0
Rt 130,18,18432
Mv 100,44,0
s_add_u32 s39, s39, 0x40000
Mv 160,48,0
Rt 132,19,16384
s_add_u32 m0, s37, 0
Mv 164,52,0
buffer_load_dwordx4 v12, s[8:11], s39 offen offset:0 lds
Mv 224,56,0
Rt 134,19,18432
Mv 228,60,0
Mv 236,60,4
Rt 136,20,16384
Mv 232,56,4
Mv 172,52,4
Rt 138,20,18432
Mv 168,48,4
Mv 108,44,4
Rt 140,21,16384
Mv 104,40,4
buffer_load_dwordx4 v13, s[8:11], s39 offen offset:1024 lds
Mv 44,36,4
Rt 142,21,18432
Mv 40,32,4
Mv 48,32,8
Rt 144,22,16384
Mv 52,36,8
Mv 112,40,8
Rt 146,22,18432
Mv 116,44,8
)" R"(
Mv 176,48,8
Rt 148,23,16384
Mv 180,52,8
buffer_load_dwordx4 v14, s[8:11], s39 offen offset:2048 lds
Mv 240,56,8
Rt 150,23,18432
Mv 244,60,8
Mv 252,60,12
Rt 152,24,16384
Mv 248,56,12
Mv 188,52,12
Rt 154,24,18432
Mv 184,48,12
Mv 124,44,12
Rt 156,25,16384
Mv 120,40,12
buffer_load_dwordx4 v15, s[8:11], s39 offen offset:3072 lds
Mv 60,36,12
Rt 158,25,18432
Mv 56,32,12
Mv 64,32,16
Rb 160,17,0
Mv 68,36,16
Mv 128,40,16
Rb 164,17,2048
Mv 132,44,16
Mv 192,48,16
Rb 168,17,8192
Mv 196,52,16
buffer_store_dwordx4 a[224:227], v1, s[20:23], s72 offen sc0 sc1
Ma 192,56,16
Rb 172,17,10240
s_add_u32 s72, s72, 1024
Ma 196,60,16
Ma 204,60,20
Rb 176,17,16384
Ma 200,56,20
Mv 204,52,20
Rb 180,17,18432
Mv 200,48,20
Mv 140,44,20
Rb 184,17,24576
Mv 136,40,20
Mv 76,36,20
Rb 188,17,26624
Mv 72,32,20
Mv 80,32,24
Mv 84,36,24
buffer_store_dwordx4 a[224:227], v1, s[20:23], s88 offen sc0 sc1
Mv 144,40,24
s_add_u32 s88, s88, 1024
Mv 148,44,24
Mv 208,48,24
Mv 212,52,24
Ma 208,56,24
Ma 212,60,24
Ma 220,60,28
Ma 216,56,28
Mv 220,52,28
Mv 216,48,28
Mv 156,44,28
Mv 152,40,28
Mv 92,36,28
Mv 88,32,28
)" R"(
s_waitcnt vmcnt(11) lgkmcnt(0)
s_barrier
Mv 32,160,128
Rt 0,18,32768
Mv 36,164,128
s_add_u32 s38, s38, 0xffff0080
Mv 96,168,128
Rt 2,18,34816
s_add_u32 m0, s36, 0
Mv 100,172,128
buffer_load_dwordx4 v8, s[4:7], s38 offen offset:0 lds
Mv 160,176,128
Rt 4,19,32768
Mv 164,180,128
Mv 224,184,128
Rt 6,19,34816
Mv 228,188,128
buffer_load_dwordx4 v9, s[4:7], s38 offen offset:1024 lds
Mv 236,188,132
Rt 8,20,32768
Mv 232,184,132
Mv 172,180,132
Rt 10,20,34816
Mv 168,176,132
buffer_load_dwordx4 v10, s[4:7], s38 offen offset:2048 lds
Mv 108,172,132
Rt 12,21,32768
Mv 104,168,132
Mv 44,164,132
Rt 14,21,34816
Mv 40,160,132
buffer_load_dwordx4 v11, s[4:7], s38 offen offset:3072 lds
Mv 48,160,136
Rt 16,22,32768
Mv 52,164,136
s_add_u32 s38, s38, 0x10000
Mv 112,168,136
Rt 18,22,34816
s_add_u32 m0, s36, 4096
Mv 116,172,136
buffer_load_dwordx4 v8, s[4:7], s38 offen offset:0 lds
Mv 176,176,136
Rt 20,23,32768
Mv 180,180,136
Mv 240,184,136
Rt 22,23,34816
Mv 244,188,136
buffer_load_dwordx4 v9, s[4:7], s38 offen offset:1024 lds
Mv 252,188,140
Rt 24,24,32768
Mv 248,184,140
Mv 188,180,140
Rt 26,24,34816
Mv 184,176,140
buffer_load_dwordx4 v10, s[4:7], s38 offen offset:2048 lds
Mv 124,172,140
Rt 28,25,32768
Mv 120,168,140
Mv 60,164,140
Rt 30,25,34816
Mv 56,160,140
buffer_load_dwordx4 v11, s[4:7], s38 offen offset:3072 lds
Mv 64,160,144
Rb 32,16,32768
Mv 68,164,144
)" R"(
Mv 128,168,144
Rb 36,16,34816
Mv 132,172,144
s_add_u32 s39, s39, 0x40000
Mv 192,176,144
Rb 40,16,40960
s_add_u32 m0, s37, 16384
Mv 196,180,144
buffer_load_dwordx4 v12, s[8:11], s39 offen offset:0 lds
Ma 192,184,144
Rb 44,16,43008
Ma 196,188,144
Ma 204,188,148
Rb 48,16,49152
Ma 200,184,148
Mv 204,180,148
Rb 52,16,51200
Mv 200,176,148
buffer_load_dwordx4 v13, s[8:11], s39 offen offset:1024 lds
Mv 140,172,148
Rb 56,16,57344
Mv 136,168,148
Mv 76,164,148
Rb 60,16,59392
Mv 72,160,148
Mv 80,160,152
Mv 84,164,152
buffer_load_dwordx4 v14, s[8:11], s39 offen offset:2048 lds
Mv 144,168,152
Mv 148,172,152
Mv 208,176,152
Mv 212,180,152
Ma 208,184,152
Ma 212,188,152
buffer_load_dwordx4 v15, s[8:11], s39 offen offset:3072 lds
Ma 220,188,156
Ma 216,184,156
s_add_u32 s43, s43, 0x60000
Mv 220,180,156
Mv 216,176,156
buffer_load_dword v27, v26, s[16:19], s43 offen
Mv 156,172,156
Mv 152,168,156
Mv 92,164,156
Mv 88,160,156
s_waitcnt vmcnt(19) lgkmcnt(0)
s_barrier
buffer_store_dword v6, v7, s[20:23], s52 offen offset:0 sc0 sc1
s_mov_b32 s52, 0x40000000
Mv 32,32,0
Rt 128,18,49152
Mv 36,36,0
Mv 96,40,0
Rt 130,18,51200
Mv 100,44,0
s_add_u32 s39, s39, 0x40000
Mv 160,48,0
Rt 132,19,49152
s_add_u32 m0, s37, 32768
Mv 164,52,0
buffer_load_dwordx4 v12, s[8:11], s39 offen offset:0 lds
Mv 224,56,0
Rt 134,19,51200
Mv 228,60,0
Mv 236,60,4
)" R"(
Rt 136,20,49152
Mv 232,56,4
Mv 172,52,4
Rt 138,20,51200
Mv 168,48,4
Mv 108,44,4
Rt 140,21,49152
Mv 104,40,4
buffer_load_dwordx4 v13, s[8:11], s39 offen offset:1024 lds
Mv 44,36,4
Rt 142,21,51200
Mv 40,32,4
Mv 48,32,8
Rt 144,22,49152
Mv 52,36,8
Mv 112,40,8
Rt 146,22,51200
Mv 116,44,8
Mv 176,48,8
Rt 148,23,49152
Mv 180,52,8
buffer_load_dwordx4 v14, s[8:11], s39 offen offset:2048 lds
Mv 240,56,8
Rt 150,23,51200
Mv 244,60,8
Mv 252,60,12
Rt 152,24,49152
Mv 248,56,12
Mv 188,52,12
Rt 154,24,51200
Mv 184,48,12
Mv 124,44,12
Rt 156,25,49152
Mv 120,40,12
buffer_load_dwordx4 v15, s[8:11], s39 offen offset:3072 lds
Mv 60,36,12
Rt 158,25,51200
Mv 56,32,12
Mv 64,32,16
Rb 160,17,32768
Mv 68,36,16
Mv 128,40,16
Rb 164,17,34816
Mv 132,44,16
Mv 192,48,16
Rb 168,17,40960
Mv 196,52,16
buffer_store_dwordx4 a[224:227], v1, s[20:23], s72 offen sc0 sc1
Ma 192,56,16
Rb 172,17,43008
s_add_u32 s72, s72, 1024
Ma 196,60,16
Ma 204,60,20
Rb 176,17,49152
Ma 200,56,20
Mv 204,52,20
Rb 180,17,51200
Mv 200,48,20
Mv 140,44,20
Rb 184,17,57344
Mv 136,40,20
Mv 76,36,20
Rb 188,17,59392
Mv 72,32,20
)" R"(
Mv 80,32,24
Mv 84,36,24
buffer_store_dwordx4 a[224:227], v1, s[20:23], s88 offen sc0 sc1
Mv 144,40,24
s_add_u32 s88, s88, 1024
Mv 148,44,24
Mv 208,48,24
Mv 212,52,24
Ma 208,56,24
Ma 212,60,24
Ma 220,60,28
Ma 216,56,28
Mv 220,52,28
Mv 216,48,28
Mv 156,44,28
Mv 152,40,28
Mv 92,36,28
s_cmp_eq_u32 s40, 3
s_cmov_b32 s72, 0x40000000
s_cmov_b32 s88, 0x40000000
Mv 88,32,28
s_waitcnt vmcnt(12) lgkmcnt(0)
s_barrier
Mv 32,160,128
Rt 0,18,0
Mv 36,164,128
s_add_u32 s38, s38, 0xffff0080
Mv 96,168,128
Rt 2,18,2048
s_add_u32 m0, s36, 32768
Mv 100,172,128
buffer_load_dwordx4 v8, s[4:7], s38 offen offset:0 lds
Mv 160,176,128
Rt 4,19,0
Mv 164,180,128
Mv 224,184,128
Rt 6,19,2048
Mv 228,188,128
buffer_load_dwordx4 v9, s[4:7], s38 offen offset:1024 lds
Mv 236,188,132
Rt 8,20,0
Mv 232,184,132
Mv 172,180,132
Rt 10,20,2048
Mv 168,176,132
buffer_load_dwordx4 v10, s[4:7], s38 offen offset:2048 lds
Mv 108,172,132
Rt 12,21,0
Mv 104,168,132
Mv 44,164,132
Rt 14,21,2048
Mv 40,160,132
buffer_load_dwordx4 v11, s[4:7], s38 offen offset:3072 lds
Mv 48,160,136
Rt 16,22,0
Mv 52,164,136
s_add_u32 s38, s38, 0x10000
Mv 112,168,136
Rt 18,22,2048
s_add_u32 m0, s36, 36864
Mv 116,172,136
buffer_load_dwordx4 v8, s[4:7], s38 offen offset:0 lds
Mv 176,176,136
Rt 20,23,0
Mv 180,180,136
)" R"(
Mv 240,184,136
Rt 22,23,2048
Mv 244,188,136
buffer_load_dwordx4 v9, s[4:7], s38 offen offset:1024 lds
Mv 252,188,140
Rt 24,24,0
Mv 248,184,140
Mv 188,180,140
Rt 26,24,2048
Mv 184,176,140
buffer_load_dwordx4 v10, s[4:7], s38 offen offset:2048 lds
Mv 124,172,140
Rt 28,25,0
Mv 120,168,140
Mv 60,164,140
Rt 30,25,2048
Mv 56,160,140
buffer_load_dwordx4 v11, s[4:7], s38 offen offset:3072 lds
Mv 64,160,144
Rb 32,16,0
Mv 68,164,144
Mv 128,168,144
Rb 36,16,2048
Mv 132,172,144
s_add_u32 s39, s39, 0x40000
Mv 192,176,144
Rb 40,16,8192
s_add_u32 m0, s37, 49152
Mv 196,180,144
buffer_load_dwordx4 v12, s[8:11], s39 offen offset:0 lds
Ma 192,184,144
Rb 44,16,10240
Ma 196,188,144
Ma 204,188,148
Rb 48,16,16384
Ma 200,184,148
Mv 204,180,148
Rb 52,16,18432
Mv 200,176,148
buffer_load_dwordx4 v13, s[8:11], s39 offen offset:1024 lds
Mv 140,172,148
Rb 56,16,24576
Mv 136,168,148
Mv 76,164,148
Rb 60,16,26624
Mv 72,160,148
Mv 80,160,152
Mv 84,164,152
buffer_load_dwordx4 v14, s[8:11], s39 offen offset:2048 lds
Mv 144,168,152
Mv 148,172,152
Mv 208,176,152
Mv 212,180,152
Ma 208,184,152
Ma 212,188,152
buffer_load_dwordx4 v15, s[8:11], s39 offen offset:3072 lds
Ma 220,188,156
Ma 216,184,156
s_add_u32 s43, s43, 0x60000
Mv 220,180,156
s_sub_u32 s40, s40, 1
Mv 216,176,156
buffer_load_dword v27, v26, s[16:19], s43 offen
Mv 156,172,156
)" R"(
Mv 152,168,156
Mv 92,164,156
Mv 88,160,156
s_cmp_lg_u32 s40, 0
s_waitcnt vmcnt(21) lgkmcnt(0)
s_barrier
s_cbranch_scc1 .LzPBV
.LzPBVX:
Mv 32,32,0
Rt 128,18,16384
Mv 36,36,0
buffer_load_dword v4, v5, s[20:23], s50 offen offset:0 sc0 sc1
Mv 96,40,0
Rt 130,18,18432
Mv 100,44,0
Mv 160,48,0
Rt 132,19,16384
Mv 164,52,0
Mv 224,56,0
Rt 134,19,18432
Mv 228,60,0
Mv 236,60,4
Rt 136,20,16384
Mv 232,56,4
Mv 172,52,4
Rt 138,20,18432
Mv 168,48,4
Mv 108,44,4
Rt 140,21,16384
Mv 104,40,4
Mv 44,36,4
Rt 142,21,18432
Mv 40,32,4
Mv 48,32,8
Rt 144,22,16384
Mv 52,36,8
Mv 112,40,8
Rt 146,22,18432
Mv 116,44,8
Mv 176,48,8
Rt 148,23,16384
Mv 180,52,8
Mv 240,56,8
Rt 150,23,18432
Mv 244,60,8
Mv 252,60,12
Rt 152,24,16384
Mv 248,56,12
Mv 188,52,12
Rt 154,24,18432
Mv 184,48,12
Mv 124,44,12
Rt 156,25,16384
Mv 120,40,12
Mv 60,36,12
Rt 158,25,18432
Mv 56,32,12
Mv 64,32,16
Rb 160,17,0
Mv 68,36,16
Mv 128,40,16
Rb 164,17,2048
Mv 132,44,16
Mv 192,48,16
Rb 168,17,8192
)" R"(
Mv 196,52,16
Ma 192,56,16
Rb 172,17,10240
Ma 196,60,16
s_waitcnt vmcnt(0)
v_cmp_eq_u32_e32 vcc, s3, v4
s_andn2_b64 s[28:29], -1, vcc
s_cbranch_scc1 .Lzsp16
.Lzok16:
Ma 204,60,20
Rb 176,17,16384
Ma 200,56,20
s_lshl3_add_u32 s29, s34, s35
s_lshl2_add_u32 s29, s29, s33
Mv 204,52,20
Rb 180,17,18432
s_add_u32 m0, s36, 131072
Mv 200,48,20
buffer_load_dwordx4 v2, s[20:23], s56 offen offset:0 sc1 lds
Mv 140,44,20
Rb 184,17,24576
s_mul_i32 s83, s29, 7168
Mv 136,40,20
s_lshl_b32 s82, s34, 21
s_add_u32 s82, s82, 0x6000000
Mv 76,36,20
Rb 188,17,26624
s_add_u32 s82, s82, s84
Mv 72,32,20
s_mov_b32 s24, s80
s_mov_b32 s25, s81
Mv 80,32,24
s_mov_b32 s26, 0x380000
s_mov_b32 s27, 0x20000
Mv 84,36,24
buffer_load_dwordx4 v2, s[20:23], s56 offen offset:1024 sc1 lds
Mv 144,40,24
s_add_u32 s28, s33, 0
s_lshl2_add_u32 s28, s35, s28
Mv 148,44,24
s_mul_i32 s29, s28, 293
s_lshr_b32 s29, s29, 11
Mv 208,48,24
s_mul_i32 s29, s29, 9
s_add_u32 s29, s29, s28
Mv 212,52,24
s_lshl_b32 s29, s29, 10
s_add_u32 s4, s29, s82
Ma 208,56,24
s_add_u32 s28, s33, 4
s_lshl3_add_u32 s28, s28, s35
Ma 212,60,24
buffer_load_dwordx4 v2, s[20:23], s56 offen offset:2048 sc1 lds
Ma 220,60,28
s_mul_i32 s29, s28, 293
s_lshr_b32 s29, s29, 11
Ma 216,56,28
s_mul_i32 s29, s29, 9
s_add_u32 s29, s29, s28
Mv 220,52,28
s_lshl_b32 s29, s29, 10
s_add_u32 s5, s29, s82
Mv 216,48,28
s_add_u32 s28, s33, 8
)" R"(
s_lshl3_add_u32 s28, s28, s35
Mv 156,44,28
s_mul_i32 s29, s28, 293
s_lshr_b32 s29, s29, 11
Mv 152,40,28
buffer_load_dwordx4 v2, s[20:23], s56 offen offset:3072 sc1 lds
Mv 92,36,28
s_mul_i32 s29, s29, 9
s_add_u32 s29, s29, s28
Mv 88,32,28
s_waitcnt lgkmcnt(0)
s_barrier
Mv 32,160,128
Rt 0,18,32768
s_lshl_b32 s29, s29, 10
Mv 36,164,128
s_add_u32 s6, s29, s82
s_add_u32 s28, s33, 12
Mv 96,168,128
Rt 2,18,34816
s_add_u32 m0, s36, 0
Mv 100,172,128
buffer_load_dwordx4 v2, s[20:23], s55 offen offset:0 sc1 lds
Mv 160,176,128
Rt 4,19,32768
s_lshl3_add_u32 s28, s28, s35
Mv 164,180,128
s_mul_i32 s29, s28, 293
s_lshr_b32 s29, s29, 11
Mv 224,184,128
Rt 6,19,34816
s_mul_i32 s29, s29, 9
Mv 228,188,128
s_add_u32 s29, s29, s28
s_lshl_b32 s29, s29, 10
Mv 236,188,132
Rt 8,20,32768
buffer_load_dwordx4 v2, s[20:23], s55 offen offset:1024 sc1 lds
Mv 232,184,132
s_add_u32 s7, s29, s82
Mv 172,180,132
Rt 10,20,34816
Mv 168,176,132
s_cmp_gt_u32 s33, 1
s_cmov_b32 s7, 0x40000000
Mv 108,172,132
Rt 12,21,32768
s_add_u32 s28, s33, 14
Mv 104,168,132
buffer_load_dwordx4 v2, s[20:23], s55 offen offset:2048 sc1 lds
Mv 44,164,132
Rt 14,21,34816
s_lshl3_add_u32 s28, s28, s35
Mv 40,160,132
s_mul_i32 s29, s28, 293
s_lshr_b32 s29, s29, 11
Mv 48,160,136
Rt 16,22,32768
s_mul_i32 s29, s29, 9
Mv 52,164,136
s_add_u32 s29, s29, s28
s_lshl_b32 s29, s29, 10
Mv 112,168,136
Rt 18,22,34816
buffer_load_dwordx4 v2, s[20:23], s55 offen offset:3072 sc1 lds
)" R"(
Mv 116,172,136
s_add_u32 s8, s29, s82
s_add_u32 s28, s33, 18
Mv 176,176,136
Rt 20,23,32768
s_lshl3_add_u32 s28, s28, s35
Mv 180,180,136
s_mul_i32 s29, s28, 293
s_lshr_b32 s29, s29, 11
Mv 240,184,136
Rt 22,23,34816
s_add_u32 m0, s36, 4096
Mv 244,188,136
buffer_load_dwordx4 v3, s[20:23], s55 offen offset:0 sc1 lds
Mv 252,188,140
Rt 24,24,32768
s_mul_i32 s29, s29, 9
Mv 248,184,140
s_add_u32 s29, s29, s28
s_lshl_b32 s29, s29, 10
Mv 188,180,140
Rt 26,24,34816
s_add_u32 s9, s29, s82
Mv 184,176,140
s_add_u32 s28, s33, 22
s_lshl3_add_u32 s28, s28, s35
Mv 124,172,140
Rt 28,25,32768
buffer_load_dwordx4 v3, s[20:23], s55 offen offset:1024 sc1 lds
Mv 120,168,140
s_mul_i32 s29, s28, 293
s_lshr_b32 s29, s29, 11
Mv 60,164,140
Rt 30,25,34816
s_mul_i32 s29, s29, 9
Mv 56,160,140
s_add_u32 s29, s29, s28
s_lshl_b32 s29, s29, 10
Mv 64,160,144
Rb 32,16,32768
s_add_u32 s10, s29, s82
Mv 68,164,144
buffer_load_dwordx4 v3, s[20:23], s55 offen offset:2048 sc1 lds
Mv 128,168,144
Rb 36,16,34816
s_add_u32 s28, s33, 26
Mv 132,172,144
s_lshl3_add_u32 s28, s28, s35
s_mul_i32 s29, s28, 293
Mv 192,176,144
Rb 40,16,40960
s_lshr_b32 s29, s29, 11
Mv 196,180,144
s_mul_i32 s29, s29, 9
s_add_u32 s29, s29, s28
Ma 192,184,144
Rb 44,16,43008
buffer_load_dwordx4 v3, s[20:23], s55 offen offset:3072 sc1 lds
Ma 196,188,144
s_lshl_b32 s29, s29, 10
s_add_u32 s11, s29, s82
Ma 204,188,148
Rb 48,16,49152
Ma 200,184,148
)" R"(
s_cmp_gt_u32 s33, 1
s_cmov_b32 s11, 0x40000000
Mv 204,180,148
Rb 52,16,51200
s_add_u32 m0, s36, 135168
Mv 200,176,148
buffer_load_dwordx4 v3, s[20:23], s56 offen offset:0 sc1 lds
Mv 140,172,148
Rb 56,16,57344
s_and_b32 s28, s34, 1
Mv 136,168,148
s_lshl_b32 s28, s28, 14
s_add_u32 s82, s37, s28
Mv 76,164,148
Rb 60,16,59392
s_add_u32 s82, s82, 0x10000
Mv 72,160,148
Mv 80,160,152
buffer_load_dwordx4 v3, s[20:23], s56 offen offset:1024 sc1 lds
Mv 84,164,152
Mv 144,168,152
Mv 148,172,152
Mv 208,176,152
Mv 212,180,152
buffer_load_dwordx4 v3, s[20:23], s56 offen offset:2048 sc1 lds
Ma 208,184,152
Ma 212,188,152
Ma 220,188,156
Ma 216,184,156
Mv 220,180,156
buffer_load_dwordx4 v3, s[20:23], s56 offen offset:3072 sc1 lds
Mv 216,176,156
Mv 156,172,156
Mv 152,168,156
s_add_u32 s55, s55, 0x10000
s_add_u32 s56, s56, 0x10000
Mv 92,164,156
Mv 88,160,156
s_waitcnt lgkmcnt(4)
s_barrier
Mv 32,32,0
Rt 128,18,49152
Mv 36,36,0
Rt 130,18,51200
Mv 96,40,0
Rt 132,19,49152
s_add_u32 m0, s36, 65536
Mv 100,44,0
Rt 134,19,51200
buffer_load_dwordx4 v2, s[20:23], s56 offen offset:0 sc1 lds
Mv 108,44,4
Rt 136,20,49152
Mv 104,40,4
Rt 138,20,51200
s_add_u32 s50, s50, 1024
Mv 44,36,4
Rt 140,21,49152
s_min_u32 s50, s50, s60
Mv 40,32,4
Rt 142,21,51200
Mv 48,32,8
Rt 144,22,49152
Mv 52,36,8
Rt 146,22,51200
Mv 112,40,8
)" R"(
Rt 148,23,49152
Mv 116,44,8
Rt 150,23,51200
buffer_load_dwordx4 v2, s[20:23], s56 offen offset:1024 sc1 lds
Mv 124,44,12
Rt 152,24,49152
Mv 120,40,12
Rt 154,24,51200
Mv 60,36,12
Rt 156,25,49152
Mv 56,32,12
Rt 158,25,51200
Mv 64,32,16
Rb 160,17,32768
Mv 68,36,16
Rb 164,17,34816
Mv 128,40,16
Rb 168,17,40960
Mv 132,44,16
Rb 172,17,43008
buffer_load_dwordx4 v2, s[20:23], s56 offen offset:2048 sc1 lds
Mv 140,44,20
Rb 176,17,49152
Mv 136,40,20
Rb 180,17,51200
Mv 76,36,20
Rb 184,17,57344
Mv 72,32,20
Rb 188,17,59392
Mv 80,32,24
v_add_u32 v8, 0x10000, v1
v_add_u32 v10, 0x20000, v1
Mv 84,36,24
buffer_load_dword v4, v5, s[20:23], s50 offen offset:0 sc0 sc1
Mv 144,40,24
s_mov_b32 s93, 2
s_mov_b32 s94, 0
Mv 148,44,24
buffer_load_dwordx4 v2, s[20:23], s56 offen offset:3072 sc1 lds
Mv 156,44,28
Mv 152,40,28
Mv 92,36,28
Mv 88,32,28
s_waitcnt lgkmcnt(0)
s_barrier
Mv 32,160,128
Mv 36,164,128
Mv 40,160,132
s_add_u32 m0, s36, 32768
Mv 44,164,132
buffer_load_dwordx4 v2, s[20:23], s55 offen offset:0 sc1 lds
Mv 48,160,136
Mv 52,164,136
s_nop 0
Cv 32,32,33
Cv 33,34,35
Mv 56,160,140
Cv 34,36,37
Cv 35,38,39
Mv 60,164,140
Cv 40,40,41
Cv 41,42,43
Mv 64,160,144
Cv 42,44,45
Cv 43,46,47
)" R"(
Mv 68,164,144
Cv 48,48,49
Cv 49,50,51
Mv 72,160,148
Cv 50,52,53
Cv 51,54,55
Mv 76,164,148
buffer_load_dwordx4 v2, s[20:23], s55 offen offset:1024 sc1 lds
Mv 80,160,152
Cv 56,56,57
Cv 57,58,59
Mv 84,164,152
Cv 58,60,61
Cv 59,62,63
Mv 88,160,156
Cv 64,64,65
Cv 65,66,67
Mv 92,164,156
Cv 66,68,69
Cv 67,70,71
Mv 96,168,128
Cv 72,72,73
Cv 73,74,75
Mv 100,172,128
Cv 74,76,77
Cv 75,78,79
Mv 104,168,132
Cv 80,80,81
Cv 81,82,83
Mv 108,172,132
buffer_load_dwordx4 v2, s[20:23], s55 offen offset:2048 sc1 lds
Mv 112,168,136
Cv 82,84,85
Cv 83,86,87
Mv 116,172,136
Cv 88,88,89
Cv 89,90,91
Mv 120,168,140
Cv 90,92,93
Cv 91,94,95
Mv 124,172,140
buffer_store_dwordx4 v[32:35], v2, s[20:23], s45 offen offset:0 sc0 sc1
Mv 128,168,144
Mv 132,172,144
Mv 136,168,148
Mv 140,172,148
buffer_load_dwordx4 v2, s[20:23], s55 offen offset:3072 sc1 lds
Mv 144,168,152
Mv 148,172,152
s_nop 1
Cv 128,128,129
Cv 129,130,131
Mv 152,168,156
Cv 130,132,133
Cv 131,134,135
Mv 156,172,156
buffer_store_dwordx4 v[40:43], v2, s[20:23], s45 offen offset:1024 sc0 sc1
Mv 160,48,0
Cv 136,136,137
Cv 137,138,139
Mv 160,176,128
Cv 138,140,141
Cv 139,142,143
Mv 164,52,0
)" R"(
s_add_u32 m0, s36, 36864
Cv 144,144,145
Mv 164,180,128
buffer_load_dwordx4 v3, s[20:23], s55 offen offset:0 sc1 lds
Mv 168,48,4
Cv 145,146,147
Cv 146,148,149
Mv 168,176,132
Cv 147,150,151
Cv 152,152,153
Mv 172,52,4
Cv 153,154,155
Cv 154,156,157
Mv 172,180,132
buffer_store_dwordx4 v[48:51], v2, s[20:23], s45 offen offset:2048 sc0 sc1
Mv 176,48,8
Cv 155,158,159
Cv 132,96,97
Mv 176,176,136
Cv 133,98,99
Cv 134,100,101
Mv 180,52,8
Cv 135,102,103
Cv 140,104,105
Mv 180,180,136
buffer_load_dwordx4 v3, s[20:23], s55 offen offset:1024 sc1 lds
Mv 184,48,12
Cv 141,106,107
Cv 142,108,109
Mv 184,176,140
Cv 143,110,111
Cv 148,112,113
Mv 188,52,12
Cv 149,114,115
Cv 150,116,117
Mv 188,180,140
buffer_store_dwordx4 v[56:59], v2, s[20:23], s45 offen offset:3072 sc0 sc1
Mv 192,48,16
Cv 151,118,119
Cv 156,120,121
Mv 192,176,144
Cv 157,122,123
Cv 158,124,125
Mv 196,52,16
Cv 159,126,127
Cv 160,160,161
Mv 196,180,144
buffer_load_dwordx4 v3, s[20:23], s55 offen offset:2048 sc1 lds
Mv 200,48,20
Cv 161,162,163
Cv 162,164,165
Mv 200,176,148
Cv 163,166,167
v_accvgpr_write_b32 a224, v160
Mv 204,52,20
v_accvgpr_write_b32 a225, v161
v_accvgpr_write_b32 a226, v162
Mv 204,180,148
buffer_store_dwordx4 v[64:67], v3, s[20:23], s45 offen offset:0 sc0 sc1
Mv 208,48,24
v_accvgpr_write_b32 a227, v163
Cv 168,168,169
Mv 208,176,152
Cv 169,170,171
)" R"(
Cv 170,172,173
Mv 212,52,24
Cv 171,174,175
v_accvgpr_write_b32 a228, v168
Mv 212,180,152
buffer_load_dwordx4 v3, s[20:23], s55 offen offset:3072 sc1 lds
Mv 216,48,28
v_accvgpr_write_b32 a229, v169
v_accvgpr_write_b32 a230, v170
Mv 216,176,156
v_accvgpr_write_b32 a231, v171
Cv 176,176,177
Mv 220,52,28
Cv 177,178,179
Cv 178,180,181
Mv 220,180,156
buffer_store_dwordx4 v[72:75], v3, s[20:23], s45 offen offset:1024 sc0 sc1
Mv 224,56,0
Cv 179,182,183
v_accvgpr_write_b32 a232, v176
Mv 228,60,0
v_accvgpr_write_b32 a233, v177
v_accvgpr_write_b32 a234, v178
Mv 236,60,4
s_add_u32 m0, s36, 69632
v_accvgpr_write_b32 a235, v179
Mv 232,56,4
buffer_load_dwordx4 v3, s[20:23], s56 offen offset:0 sc1 lds
Mv 240,56,8
Cv 184,184,185
Cv 185,186,187
Mv 244,60,8
Cv 186,188,189
Cv 187,190,191
Mv 252,60,12
v_accvgpr_write_b32 a236, v184
v_accvgpr_write_b32 a237, v185
Mv 248,56,12
s_waitcnt vmcnt(16)
s_barrier
Ma 192,56,16
v_cmp_eq_u32_e32 vcc, s3, v4
s_andn2_b64 s[28:29], -1, vcc
s_cbranch_scc1 .Lzsp17
.Lzok17:
Ma 196,60,16
buffer_store_dwordx4 v[80:83], v3, s[20:23], s45 offen offset:2048 sc0 sc1
Ma 204,60,20
v_accvgpr_write_b32 a238, v186
v_accvgpr_write_b32 a239, v187
Ma 200,56,20
buffer_load_dwordx4 v3, s[20:23], s56 offen offset:1024 sc1 lds
Ma 208,56,24
Cv 192,192,193
Cv 193,194,195
Ma 212,60,24
Cv 194,196,197
Cv 195,198,199
Ma 220,60,28
v_accvgpr_write_b32 a240, v192
v_accvgpr_write_b32 a241, v193
Ma 216,56,28
buffer_store_dwordx4 v[88:91], v3, s[20:23], s45 offen offset:3072 sc0 sc1
Mv 224,184,128
Rb 0,1,0
)" R"(
v_accvgpr_write_b32 a242, v194
Mv 228,188,128
Rb 4,1,1024
v_accvgpr_write_b32 a243, v195
Mv 232,184,132
Rb 8,1,2048
Cv 200,200,201
Mv 236,188,132
buffer_load_dwordx4 v3, s[20:23], s56 offen offset:2048 sc1 lds
Rb 12,1,3072
Mv 240,184,136
Rb 16,1,8192
Cv 201,202,203
Mv 244,188,136
Rb 20,1,9216
Cv 202,204,205
Mv 248,184,140
Rb 24,1,10240
Cv 203,206,207
Mv 252,188,140
Rb 28,1,11264
v_accvgpr_write_b32 a244, v200
Ma 192,184,144
Rb 32,1,4096
v_accvgpr_write_b32 a245, v201
Ma 196,188,144
Rb 36,1,5120
v_accvgpr_write_b32 a246, v202
Ma 200,184,148
Rb 40,1,6144
v_accvgpr_write_b32 a247, v203
Ma 204,188,148
buffer_load_dwordx4 v3, s[20:23], s56 offen offset:3072 sc1 lds
Rb 44,1,7168
Ma 208,184,152
Ma 212,188,152
Ma 216,184,156
Ma 220,188,156
s_add_u32 s56, s56, 0x10000
.LzFB:
s_waitcnt lgkmcnt(8)
Mz 64,0,64
Rb 48,1,12288
s_add_u32 s55, s55, 0x10000
Mz 68,0,68
s_cmp_eq_u32 s57, 2
s_cselect_b32 s55, s59, s55
Mv 68,4,76
Rb 52,1,13312
s_sub_u32 s57, s57, 1
v_mov_b32 v16, 0
Mv 64,4,72
buffer_store_dwordx4 v[132:135], v2, s[20:23], s46 offen offset:0 sc0 sc1
Mv 64,8,80
Rb 56,1,14336
Cv 208,208,209
Cv 209,210,211
Mv 68,8,84
Cv 210,212,213
Cv 211,214,215
Mv 68,12,92
Rb 60,1,15360
v_accvgpr_write_b32 a248, v208
s_waitcnt lgkmcnt(8)
)" R"(
Mv 64,12,88
v_accvgpr_write_b32 a249, v209
v_accvgpr_write_b32 a250, v210
Mv 64,16,96
Rb 0,1,16384
v_accvgpr_write_b32 a251, v211
Mv 68,16,100
Cv 216,216,217
Cv 217,218,219
Cv 218,220,221
Mv 68,20,108
Rb 4,1,17408
Cv 219,222,223
Mv 64,20,104
v_accvgpr_write_b32 a252, v216
v_accvgpr_write_b32 a253, v217
v_accvgpr_write_b32 a254, v218
Mv 64,24,112
Rb 8,1,18432
v_accvgpr_write_b32 a255, v219
Mv 68,24,116
buffer_store_dwordx4 v[140:143], v2, s[20:23], s46 offen offset:1024 sc0 sc1
Cv 224,224,225
Mv 68,28,124
Rb 12,1,19456
s_waitcnt lgkmcnt(8)
Mv 64,28,120
Cv 225,226,227
Cv 226,228,229
Cv 227,230,231
Mz 72,32,64
Rb 16,1,24576
v_accvgpr_write_b32 a128, v224
Mz 76,32,68
v_accvgpr_write_b32 a129, v225
v_accvgpr_write_b32 a130, v226
Mv 76,36,76
Rb 20,1,25600
v_accvgpr_write_b32 a131, v227
Cv 232,232,233
Mv 72,36,72
Cv 233,234,235
Cv 234,236,237
Mv 72,40,80
Rb 24,1,26624
Cv 235,238,239
v_accvgpr_write_b32 a132, v232
Mv 76,40,84
v_accvgpr_write_b32 a133, v233
v_accvgpr_write_b32 a134, v234
Mv 76,44,92
Rb 28,1,27648
v_accvgpr_write_b32 a135, v235
s_waitcnt lgkmcnt(8)
Mv 72,44,88
buffer_store_dwordx4 v[148:151], v2, s[20:23], s46 offen offset:2048 sc0 sc1
Mv 72,48,96
Rb 32,1,20480
Cv 240,240,241
Mv 76,48,100
Cv 241,242,243
Cv 242,244,245
Cv 243,246,247
Mv 76,52,108
)" R"(
Rb 36,1,21504
v_accvgpr_write_b32 a136, v240
Mv 72,52,104
v_accvgpr_write_b32 a137, v241
v_accvgpr_write_b32 a138, v242
v_accvgpr_write_b32 a139, v243
Mv 72,56,112
Rb 40,1,22528
Cv 248,248,249
Mv 76,56,116
Cv 249,250,251
Cv 250,252,253
Cv 251,254,255
Mv 76,60,124
Rb 44,1,23552
s_waitcnt lgkmcnt(8)
Mv 72,60,120
v_accvgpr_write_b32 a140, v248
v_accvgpr_write_b32 a141, v249
v_accvgpr_write_b32 a142, v250
Mz 80,0,64
Rb 48,1,28672
v_accvgpr_write_b32 a143, v251
Mz 84,0,68
buffer_store_dwordx4 v[156:159], v2, s[20:23], s46 offen offset:3072 sc0 sc1
Mv 84,4,76
Rb 52,1,29696
v_mov_b32 v17, 0
v_mov_b32 v18, 0
Mv 80,4,72
v_mov_b32 v19, 0
v_mov_b32 v20, 0
Mv 80,8,80
Rb 56,1,30720
v_exp_f32_e32 v32, v64
Mv 84,8,84
v_exp_f32_e32 v24, v65
Mv 84,12,92
Rb 60,1,31744
v_mov_b32 v21, 0
s_waitcnt lgkmcnt(8)
Mv 80,12,88
v_exp_f32_e32 v33, v66
Mv 80,16,96
v_exp_f32_e32 v25, v67
Mv 84,16,100
v_exp_f32_e32 v34, v72
v_mov_b32 v22, 0
Mv 84,20,108
v_exp_f32_e32 v26, v73
Mv 80,20,104
buffer_store_dwordx4 v[128:131], v3, s[20:23], s46 offen offset:0 sc0 sc1
v_mov_b32 v23, 0
Mv 80,24,112
s_add_u32 s50, s50, 1024
s_min_u32 s50, s50, s60
Mv 84,24,116
v_exp_f32_e32 v35, v74
v_add_f32_e32 v14, v32, v24
Mv 84,28,124
v_exp_f32_e32 v27, v75
Mv 80,28,120
s_waitcnt vmcnt(7) lgkmcnt(0)
s_barrier
buffer_store_dword v6, v7, s[20:23], s49 offen offset:0 sc0 sc1
)" R"(
Mz 88,32,64
Rb 0,1,32768
Cv 32,32,24
Mz 92,32,68
Rb 4,1,33792
v_add_f32_e32 v14, v14, v33
Mv 92,36,76
Rb 8,1,34816
s_add_u32 m0, s36, 0
v_add_f32_e32 v14, v14, v25
Mv 88,36,72
Rb 12,1,35840
buffer_load_dwordx4 v2, s[20:23], s55 offen offset:0 sc1 lds
Mv 88,40,80
Rb 16,1,40960
v_exp_f32_e32 v36, v68
Mv 92,40,84
Rb 20,1,41984
Cv 33,33,25
Mv 92,44,92
Rb 24,1,43008
v_exp_f32_e32 v28, v69
Mv 88,44,88
Rb 28,1,44032
v_add_f32_e32 v14, v14, v34
Mv 88,48,96
Rb 32,1,36864
v_add_f32_e32 v14, v14, v26
Mv 92,48,100
Rb 36,1,37888
v_exp_f32_e32 v37, v70
Mv 92,52,108
Rb 40,1,38912
Cv 34,34,26
Mv 88,52,104
Rb 44,1,39936
buffer_load_dwordx4 v2, s[20:23], s55 offen offset:1024 sc1 lds
Mv 88,56,112
v_exp_f32_e32 v29, v71
Mv 92,56,116
v_exp_f32_e32 v38, v76
v_add_f32_e32 v14, v14, v35
Mv 92,60,124
v_add_f32_e32 v14, v14, v27
s_waitcnt lgkmcnt(8)
Mv 88,60,120
v_exp_f32_e32 v24, v77
Cv 35,35,27
Mz 96,0,64
Rb 48,1,45056
buffer_load_dword v4, v5, s[20:23], s50 offen offset:0 sc0 sc1
Mz 100,0,68
v_exp_f32_e32 v39, v78
Mv 100,4,76
Rb 52,1,46080
v_exp_f32_e32 v25, v79
Mv 96,4,72
buffer_load_dwordx4 v2, s[20:23], s55 offen offset:2048 sc1 lds
Mv 96,8,80
Rb 56,1,47104
v_exp_f32_e32 v40, v80
Mv 100,8,84
v_exp_f32_e32 v26, v81
Mv 100,12,92
)" R"(
Rb 60,1,48128
v_add_f32_e32 v15, v36, v28
s_waitcnt lgkmcnt(8)
Mv 96,12,88
buffer_store_dwordx4 v[136:139], v3, s[20:23], s46 offen offset:1024 sc0 sc1
Mv 96,16,96
Rb 0,1,49152
Cv 36,36,28
Mv 100,16,100
v_exp_f32_e32 v41, v82
v_add_f32_e32 v15, v15, v37
Mv 100,20,108
Rb 4,1,50176
v_add_f32_e32 v15, v15, v29
Mv 96,20,104
buffer_load_dwordx4 v2, s[20:23], s55 offen offset:3072 sc1 lds
Cv 37,37,29
Mv 96,24,112
Rb 8,1,51200
v_add_f32_e32 v15, v15, v38
Mv 100,24,116
v_exp_f32_e32 v27, v83
v_add_f32_e32 v15, v15, v24
Mv 100,28,124
Rb 12,1,52224
s_waitcnt lgkmcnt(8)
Mv 96,28,120
v_exp_f32_e32 v42, v88
Cv 38,38,24
Mz 104,32,64
Rb 16,1,57344
v_add_f32_e32 v15, v15, v39
Mz 108,32,68
v_exp_f32_e32 v28, v89
Mv 108,36,76
Rb 20,1,58368
s_add_u32 m0, s36, 4096
v_add_f32_e32 v15, v15, v25
Mv 104,36,72
buffer_load_dwordx4 v3, s[20:23], s55 offen offset:0 sc1 lds
Mv 104,40,80
Rb 24,1,59392
v_exp_f32_e32 v43, v90
Mv 108,40,84
v_exp_f32_e32 v29, v91
Mv 108,44,92
Rb 28,1,60416
Cv 39,39,25
s_waitcnt lgkmcnt(8)
Mv 104,44,88
buffer_store_dwordx4 v[144:147], v3, s[20:23], s46 offen offset:2048 sc0 sc1
Mv 104,48,96
Rb 32,1,53248
v_add_f32_e32 v14, v14, v40
Mv 108,48,100
v_exp_f32_e32 v44, v84
v_add_f32_e32 v14, v14, v26
Mv 108,52,108
Rb 36,1,54272
Cv 40,40,26
Mv 104,52,104
buffer_load_dwordx4 v3, s[20:23], s55 offen offset:1024 sc1 lds
v_add_f32_e32 v14, v14, v41
Mv 104,56,112
)" R"(
Rb 40,1,55296
v_add_f32_e32 v14, v14, v27
Mv 108,56,116
v_exp_f32_e32 v24, v85
Cv 41,41,27
Mv 108,60,124
Rb 44,1,56320
s_waitcnt lgkmcnt(8)
Mv 104,60,120
v_exp_f32_e32 v45, v86
v_add_f32_e32 v14, v14, v42
Mz 112,0,64
Rb 48,1,61440
v_add_f32_e32 v14, v14, v28
Mz 116,0,68
v_exp_f32_e32 v25, v87
Mv 116,4,76
Rb 52,1,62464
v_exp_f32_e32 v46, v92
Mv 112,4,72
buffer_load_dwordx4 v3, s[20:23], s55 offen offset:2048 sc1 lds
Mv 112,8,80
Rb 56,1,63488
v_exp_f32_e32 v26, v93
Mv 116,8,84
v_exp_f32_e32 v47, v94
Mv 116,12,92
Rb 60,1,64512
Cv 42,42,28
s_waitcnt lgkmcnt(8)
Mv 112,12,88
buffer_store_dwordx4 v[152:155], v3, s[20:23], s46 offen offset:3072 sc0 sc1
Mv 112,16,96
v_exp_f32_e32 v27, v95
Mv 116,16,100
v_add_f32_e32 v14, v14, v43
v_add_f32_e32 v14, v14, v29
Cv 43,43,29
Mv 116,20,108
v_add_f32_e32 v15, v15, v44
v_add_f32_e32 v15, v15, v24
Mv 112,20,104
buffer_load_dwordx4 v3, s[20:23], s55 offen offset:3072 sc1 lds
Cv 44,44,24
Mv 112,24,112
v_add_f32_e32 v15, v15, v45
v_add_f32_e32 v15, v15, v25
Mv 116,24,116
Cv 45,45,25
v_add_f32_e32 v15, v15, v46
v_add_f32_e32 v15, v15, v26
Mv 116,28,124
Cv 46,46,26
v_add_f32_e32 v15, v15, v47
Mv 112,28,120
v_add_f32_e32 v15, v15, v27
s_waitcnt lgkmcnt(0)
s_barrier
Mz 120,32,64
Rb 0,10,0
Cv 47,47,27
Mz 124,32,68
Rb 4,10,1024
v_max_f32_e32 v11, v14, v15
Mv 124,36,76
)" R"(
Rb 8,10,2048
v_mov_b32 v24, v14
v_mov_b32 v25, v15
Mv 120,36,72
Rb 12,10,3072
v_permlane32_swap_b32_e32 v24, v25
Mv 120,40,80
Rb 16,10,4096
v_add_f32_e32 v24, v24, v25
v_mov_b32 v25, v24
Mv 124,40,84
Rb 20,10,5120
v_permlane16_swap_b32_e32 v24, v25
Mv 124,44,92
Rb 24,10,6144
v_add_f32_e32 v24, v24, v25
s_add_u32 s55, s55, 0x10000
Mv 120,44,88
Rb 28,10,7168
Mv 120,48,96
Rb 32,10,8192
Mv 124,48,100
Rb 36,10,9216
v_rcp_f32_e32 v24, v24
Mv 124,52,108
Rb 40,10,10240
v_max_f32_e32 v11, v11, v24
Mv 120,52,104
Rb 44,10,11264
s_cmp_eq_u32 s57, 2
s_cselect_b32 s55, s59, s55
Mv 120,56,112
v_cmp_nge_f32_e32 vcc, s62, v11
Mv 124,56,116
Mv 124,60,124
s_waitcnt lgkmcnt(8)
s_cbranch_vccnz .Lzfx6
.Lzfr6:
Mv 120,60,120
v_mov_b32 v12, v14
v_mov_b32 v13, v15
v_mfma_f32_16x16x32_bf16 v[128:131], a[0:3], v[32:35], 0
Rb 48,10,12288
v_mfma_f32_16x16x32_bf16 v[136:139], a[0:3], v[36:39], 0
v_mfma_f32_16x16x32_bf16 v[140:143], a[4:7], v[36:39], 0
Rb 52,10,13312
s_add_u32 m0, s36, 32768
v_mfma_f32_16x16x32_bf16 v[132:135], a[4:7], v[32:35], 0
buffer_load_dwordx4 v2, s[20:23], s55 offen offset:0 sc1 lds
v_mfma_f32_16x16x32_bf16 v[144:147], a[8:11], v[32:35], 0
Rb 56,10,14336
v_mfma_f32_16x16x32_bf16 v[152:155], a[8:11], v[36:39], 0
v_exp_f32_e32 v48, v96
v_mfma_f32_16x16x32_bf16 v[156:159], a[12:15], v[36:39], 0
Rb 60,10,15360
s_waitcnt lgkmcnt(8)
v_mfma_f32_16x16x32_bf16 v[148:151], a[12:15], v[32:35], 0
v_exp_f32_e32 v28, v97
v_mfma_f32_16x16x32_bf16 v[160:163], a[16:19], v[32:35], 0
Rb 0,10,16384
v_add_f32_e32 v14, v48, v28
v_mfma_f32_16x16x32_bf16 v[168:171], a[16:19], v[36:39], 0
v_exp_f32_e32 v49, v98
v_mfma_f32_16x16x32_bf16 v[172:175], a[20:23], v[36:39], 0
)" R"(
Rb 4,10,17408
Cv 48,48,28
v_mfma_f32_16x16x32_bf16 v[164:167], a[20:23], v[32:35], 0
buffer_load_dwordx4 v2, s[20:23], s55 offen offset:1024 sc1 lds
v_mfma_f32_16x16x32_bf16 v[176:179], a[24:27], v[32:35], 0
Rb 8,10,18432
v_exp_f32_e32 v29, v99
v_mfma_f32_16x16x32_bf16 v[184:187], a[24:27], v[36:39], 0
v_exp_f32_e32 v50, v104
v_mfma_f32_16x16x32_bf16 v[188:191], a[28:31], v[36:39], 0
Rb 12,10,19456
s_waitcnt lgkmcnt(8)
v_mfma_f32_16x16x32_bf16 v[180:183], a[28:31], v[32:35], 0
v_exp_f32_e32 v24, v105
v_mfma_f32_16x16x32_bf16 v[192:195], a[32:35], v[32:35], 0
Rb 16,10,20480
v_add_f32_e32 v14, v14, v49
v_mfma_f32_16x16x32_bf16 v[200:203], a[32:35], v[36:39], 0
v_exp_f32_e32 v51, v106
v_add_f32_e32 v14, v14, v29
v_mfma_f32_16x16x32_bf16 v[204:207], a[36:39], v[36:39], 0
Rb 20,10,21504
Cv 49,49,29
v_mfma_f32_16x16x32_bf16 v[196:199], a[36:39], v[32:35], 0
buffer_load_dwordx4 v2, s[20:23], s55 offen offset:2048 sc1 lds
v_mfma_f32_16x16x32_bf16 v[208:211], a[40:43], v[32:35], 0
Rb 24,10,22528
v_add_f32_e32 v14, v14, v50
v_mfma_f32_16x16x32_bf16 v[216:219], a[40:43], v[36:39], 0
v_exp_f32_e32 v25, v107
v_mfma_f32_16x16x32_bf16 v[220:223], a[44:47], v[36:39], 0
Rb 28,10,23552
s_waitcnt lgkmcnt(8)
v_mfma_f32_16x16x32_bf16 v[212:215], a[44:47], v[32:35], 0
v_exp_f32_e32 v52, v100
v_add_f32_e32 v14, v14, v24
v_mfma_f32_16x16x32_bf16 v[224:227], a[48:51], v[32:35], 0
Rb 32,10,24576
Cv 50,50,24
v_mfma_f32_16x16x32_bf16 v[232:235], a[48:51], v[36:39], 0
v_exp_f32_e32 v26, v101
v_mfma_f32_16x16x32_bf16 v[236:239], a[52:55], v[36:39], 0
Rb 36,10,25600
v_add_f32_e32 v14, v14, v51
v_mfma_f32_16x16x32_bf16 v[228:231], a[52:55], v[32:35], 0
buffer_load_dwordx4 v2, s[20:23], s55 offen offset:3072 sc1 lds
v_mfma_f32_16x16x32_bf16 v[240:243], a[56:59], v[32:35], 0
Rb 40,10,26624
v_exp_f32_e32 v53, v102
v_mfma_f32_16x16x32_bf16 v[248:251], a[56:59], v[36:39], 0
v_exp_f32_e32 v27, v103
v_mfma_f32_16x16x32_bf16 v[252:255], a[60:63], v[36:39], 0
Rb 44,10,27648
s_waitcnt lgkmcnt(8)
v_mfma_f32_16x16x32_bf16 v[244:247], a[60:63], v[32:35], 0
v_exp_f32_e32 v54, v108
Mp 128,0,40
Rb 48,10,28672
v_add_f32_e32 v14, v14, v25
Mp 136,0,44
v_exp_f32_e32 v28, v109
Cv 51,51,25
Mp 140,4,44
Rb 52,10,29696
)" R"(
s_add_u32 m0, s36, 36864
Mp 132,4,40
buffer_load_dwordx4 v3, s[20:23], s55 offen offset:0 sc1 lds
Mp 144,8,40
Rb 56,10,30720
v_add_f32_e32 v15, v52, v26
Mp 152,8,44
v_exp_f32_e32 v55, v110
Mp 156,12,44
Rb 60,10,31744
s_waitcnt lgkmcnt(8)
Mp 148,12,40
v_exp_f32_e32 v29, v111
Cv 52,52,26
Mp 160,16,40
v_add_f32_e32 v15, v15, v53
v_add_f32_e32 v15, v15, v27
Mp 168,16,44
Cv 53,53,27
v_add_f32_e32 v15, v15, v54
Mp 172,20,44
v_add_f32_e32 v15, v15, v28
Cv 54,54,28
Mp 164,20,40
buffer_load_dwordx4 v3, s[20:23], s55 offen offset:1024 sc1 lds
Mp 176,24,40
v_add_f32_e32 v15, v15, v55
v_add_f32_e32 v15, v15, v29
Cv 55,55,29
Mp 184,24,44
s_sub_u32 s57, s57, 1
v_accvgpr_read_b32 v9, a192
Mp 188,28,44
v_accvgpr_read_b32 v11, a193
Cv 9,9,11
Mp 180,28,40
s_waitcnt vmcnt(6) lgkmcnt(0)
s_barrier
buffer_store_dword v6, v7, s[20:23], s49 offen offset:1024 sc0 sc1
Mp 192,32,40
Rb 0,1,0
v_accvgpr_write_b32 a144, v9
Mp 200,32,44
Rb 4,1,1024
v_accvgpr_read_b32 v9, a194
v_accvgpr_read_b32 v11, a195
Mp 204,36,44
Rb 8,1,2048
Cv 9,9,11
Mp 196,36,40
Rb 12,1,3072
buffer_load_dwordx4 v3, s[20:23], s55 offen offset:2048 sc1 lds
Mp 208,40,40
Rb 16,1,8192
v_accvgpr_write_b32 a145, v9
Mp 216,40,44
Rb 20,1,9216
v_accvgpr_read_b32 v9, a196
Mp 220,44,44
Rb 24,1,10240
v_accvgpr_read_b32 v11, a197
Mp 212,44,40
Rb 28,1,11264
Cv 9,9,11
v_accvgpr_write_b32 a146, v9
)" R"(
Mp 224,48,40
Rb 32,1,4096
v_accvgpr_read_b32 v9, a198
Mp 232,48,44
Rb 36,1,5120
v_accvgpr_read_b32 v11, a199
Mp 236,52,44
Rb 40,1,6144
Cv 9,9,11
Mp 228,52,40
Rb 44,1,7168
buffer_load_dwordx4 v3, s[20:23], s55 offen offset:3072 sc1 lds
Mp 240,56,40
v_cmp_eq_u32_e32 vcc, s3, v4
s_andn2_b64 s[28:29], -1, vcc
s_cbranch_scc1 .Lzsp18
.Lzok18:
Mp 248,56,44
v_accvgpr_write_b32 a147, v9
v_accvgpr_read_b32 v9, a200
Mp 252,60,44
v_accvgpr_read_b32 v11, a201
s_waitcnt lgkmcnt(8)
Mp 244,60,40
Cv 9,9,11
v_accvgpr_write_b32 a148, v9
v_mfma_f32_16x16x32_bf16 v[64:67], a[0:3], a[64:67], v[16:19]
Rb 48,1,12288
v_add_u32 v9, 4, v0
v_mfma_f32_16x16x32_bf16 v[68:71], a[0:3], a[68:71], v[20:23]
v_xor_b32 v9, v9, v0
v_and_b32 v9, 32, v9
Mv 68,4,76
Rb 52,1,13312
s_add_u32 m0, s36, 98304
Mv 64,4,72
buffer_load_dwordx4 v2, s[20:23], s56 offen offset:0 sc1 lds
Mv 64,8,80
Rb 56,1,14336
v_lshlrev_b32 v9, 22, v9
Mv 68,8,84
v_and_or_b32 v9, v0, 32, v9
v_mul_lo_u32 v9, v9, 12
Mv 68,12,92
Rb 60,1,15360
s_waitcnt lgkmcnt(8)
Mv 64,12,88
buffer_load_dwordx4 v2, s[20:23], s56 offen offset:1024 sc1 lds
Mv 64,16,96
Rb 0,1,16384
v_add_u32 v9, v9, v1
Mv 68,16,100
v_exp_f32_e32 v56, v112
Mv 68,20,108
Rb 4,1,17408
v_exp_f32_e32 v24, v113
Mv 64,20,104
buffer_load_dwordx4 v2, s[20:23], s56 offen offset:2048 sc1 lds
Mv 64,24,112
Rb 8,1,18432
v_exp_f32_e32 v57, v114
Mv 68,24,116
v_exp_f32_e32 v25, v115
Mv 68,28,124
)" R"(
Rb 12,1,19456
v_add_f32_e32 v14, v14, v56
s_waitcnt lgkmcnt(8)
Mv 64,28,120
buffer_load_dwordx4 v2, s[20:23], s56 offen offset:3072 sc1 lds
v_add_f32_e32 v14, v14, v24
v_mfma_f32_16x16x32_bf16 v[72:75], a[32:35], a[64:67], v[16:19]
Rb 16,1,24576
Cv 56,56,24
v_mfma_f32_16x16x32_bf16 v[76:79], a[32:35], a[68:71], v[20:23]
v_exp_f32_e32 v58, v120
v_add_f32_e32 v14, v14, v57
Mv 76,36,76
Rb 20,1,25600
s_add_u32 m0, s36, 102400
Mv 72,36,72
buffer_load_dwordx4 v3, s[20:23], s56 offen offset:0 sc1 lds
v_add_f32_e32 v14, v14, v25
Mv 72,40,80
Rb 24,1,26624
Cv 57,57,25
Mv 76,40,84
v_exp_f32_e32 v26, v121
v_add_f32_e32 v14, v14, v58
Mv 76,44,92
Rb 28,1,27648
s_waitcnt lgkmcnt(8)
Mv 72,44,88
buffer_load_dwordx4 v3, s[20:23], s56 offen offset:1024 sc1 lds
v_add_f32_e32 v14, v14, v26
Mv 72,48,96
Rb 32,1,20480
v_exp_f32_e32 v59, v122
Mv 76,48,100
v_exp_f32_e32 v27, v123
Mv 76,52,108
Rb 36,1,21504
v_exp_f32_e32 v60, v116
Mv 72,52,104
buffer_load_dwordx4 v3, s[20:23], s56 offen offset:2048 sc1 lds
Mv 72,56,112
Rb 40,1,22528
v_exp_f32_e32 v28, v117
Mv 76,56,116
v_exp_f32_e32 v61, v118
Mv 76,60,124
Rb 44,1,23552
Cv 58,58,26
s_waitcnt lgkmcnt(8)
Mv 72,60,120
buffer_load_dwordx4 v3, s[20:23], s56 offen offset:3072 sc1 lds
v_add_f32_e32 v14, v14, v59
v_mfma_f32_16x16x32_bf16 v[80:83], a[0:3], a[64:67], v[16:19]
Rb 48,1,28672
s_xor_b32 m0, s82, 0
v_mfma_f32_16x16x32_bf16 v[84:87], a[0:3], a[68:71], v[20:23]
s_add_u32 s56, s56, 0x10000
s_add_u32 s92, s83, 448
v_add_f32_e32 v14, v14, v27
Mv 84,4,76
Rb 52,1,29696
Cv 59,59,27
Mv 80,4,72
buffer_load_dwordx4 v9, s[24:27], s92 offen offset:0 lds
)" R"(
v_add_f32_e32 v15, v15, v60
Mv 80,8,80
Rb 56,1,30720
s_add_u32 s92, s83, 1216
Mv 84,8,84
v_exp_f32_e32 v29, v119
v_add_f32_e32 v15, v15, v28
Mv 84,12,92
Rb 60,1,31744
s_waitcnt lgkmcnt(8)
Mv 80,12,88
buffer_load_dwordx4 v9, s[24:27], s92 offen offset:1024 lds
s_add_u32 s92, s83, 1984
Mv 80,16,96
v_exp_f32_e32 v62, v124
Cv 60,60,28
Mv 84,16,100
v_exp_f32_e32 v24, v125
Mv 84,20,108
v_exp_f32_e32 v63, v126
v_add_f32_e32 v15, v15, v61
Mv 80,20,104
buffer_load_dwordx4 v9, s[24:27], s92 offen offset:2048 lds
Mv 80,24,112
s_add_u32 s92, s83, 2752
v_exp_f32_e32 v25, v127
Mv 84,24,116
v_add_f32_e32 v15, v15, v29
Cv 61,61,29
Mv 84,28,124
buffer_load_dwordx4 v9, s[24:27], s92 offen offset:3072 lds
v_add_f32_e32 v15, v15, v62
Mv 80,28,120
s_xor_b32 m0, s82, 16384
s_waitcnt lgkmcnt(0)
s_barrier
v_mfma_f32_16x16x32_bf16 v[88:91], a[32:35], a[64:67], v[16:19]
Rb 0,8,0
s_add_u32 s92, s83, 0
v_mfma_f32_16x16x32_bf16 v[92:95], a[32:35], a[68:71], v[20:23]
Rb 4,8,1024
v_add_f32_e32 v15, v15, v24
Cv 62,62,24
Mv 92,36,76
Rb 8,8,2048
v_add_f32_e32 v15, v15, v63
Mv 88,36,72
Rb 12,8,3072
v_add_f32_e32 v15, v15, v25
Cv 63,63,25
Mv 88,40,80
Rb 16,8,4096
buffer_load_dwordx4 v9, s[24:27], s92 offen offset:0 lds
Mv 92,40,84
Rb 20,8,5120
s_add_u32 s92, s83, 768
v_max_f32_e32 v11, v14, v15
Mv 92,44,92
Rb 24,8,6144
v_cmp_nge_f32_e32 vcc, s62, v11
Mv 88,44,88
Rb 28,8,7168
s_add_u32 s55, s55, 0x10000
Mv 88,48,96
Rb 32,8,8192
)" R"(
s_cmp_eq_u32 s57, 2
s_cselect_b32 s55, s59, s55
Mv 92,48,100
Rb 36,8,9216
buffer_load_dwordx4 v9, s[24:27], s92 offen offset:1024 lds
Mv 92,52,108
Rb 40,8,10240
s_add_u32 s92, s83, 1536
Mv 88,52,104
Rb 44,8,11264
Mv 88,56,112
buffer_load_dwordx4 v9, s[24:27], s92 offen offset:2048 lds
s_add_u32 s92, s83, 2304
Mv 92,56,116
buffer_load_dwordx4 v9, s[24:27], s92 offen offset:3072 lds
Mv 92,60,124
s_waitcnt lgkmcnt(8)
s_cbranch_vccnz .Lzfx7
.Lzfr7:
Mv 88,60,120
v_add_f32_e32 v12, v12, v14
v_add_f32_e32 v13, v13, v15
Mp 128,0,48
Rb 48,8,12288
Mp 136,0,52
s_cmp_le_i32 s57, s58
s_cbranch_scc1 .Lzmk6
.Lzmr6:
Mp 140,4,52
Rb 52,8,13312
s_add_u32 m0, s36, 0
Mp 132,4,48
buffer_load_dwordx4 v2, s[20:23], s55 offen offset:0 sc1 lds
Mp 144,8,48
Rb 56,8,14336
v_exp_f32_e32 v32, v64
Mp 152,8,52
buffer_store_dwordx4 a[224:227], v2, s[20:23], s47 offen offset:0 sc0 sc1
Mp 156,12,52
Rb 60,8,15360
s_waitcnt lgkmcnt(8)
Mp 148,12,48
v_exp_f32_e32 v26, v65
Mp 160,16,48
Rb 0,8,16384
v_add_f32_e32 v14, v32, v26
Mp 168,16,52
v_exp_f32_e32 v33, v66
Cv 32,32,26
Mp 172,20,52
Rb 4,8,17408
v_add_f32_e32 v14, v14, v33
Mp 164,20,48
buffer_load_dwordx4 v2, s[20:23], s55 offen offset:1024 sc1 lds
Mp 176,24,48
Rb 8,8,18432
v_exp_f32_e32 v27, v67
Mp 184,24,52
v_exp_f32_e32 v34, v72
Mp 188,28,52
Rb 12,8,19456
s_waitcnt lgkmcnt(8)
Mp 180,28,48
v_exp_f32_e32 v28, v73
)" R"(
v_add_f32_e32 v14, v14, v27
Mp 192,32,48
Rb 16,8,20480
Cv 33,33,27
Mp 200,32,52
v_exp_f32_e32 v35, v74
v_add_f32_e32 v14, v14, v34
Mp 204,36,52
Rb 20,8,21504
v_add_f32_e32 v14, v14, v28
Mp 196,36,48
buffer_load_dwordx4 v2, s[20:23], s55 offen offset:2048 sc1 lds
Mp 208,40,48
Rb 24,8,22528
v_exp_f32_e32 v29, v75
Mp 216,40,52
buffer_store_dwordx4 a[228:231], v2, s[20:23], s47 offen offset:1024 sc0 sc1
Mp 220,44,52
Rb 28,8,23552
s_waitcnt lgkmcnt(8)
Mp 212,44,48
v_exp_f32_e32 v36, v68
Cv 34,34,28
Mp 224,48,48
Rb 32,8,24576
v_add_f32_e32 v14, v14, v35
Mp 232,48,52
v_exp_f32_e32 v24, v69
v_add_f32_e32 v14, v14, v29
Mp 236,52,52
Rb 36,8,25600
Cv 35,35,29
Mp 228,52,48
buffer_load_dwordx4 v2, s[20:23], s55 offen offset:3072 sc1 lds
Mp 240,56,48
Rb 40,8,26624
v_exp_f32_e32 v37, v70
Mp 248,56,52
v_exp_f32_e32 v25, v71
Mp 252,60,52
Rb 44,8,27648
s_waitcnt lgkmcnt(8)
Mp 244,60,48
v_exp_f32_e32 v38, v76
v_add_f32_e32 v15, v36, v24
Mp 128,0,56
Rb 48,8,28672
Cv 36,36,24
Mp 136,0,60
v_exp_f32_e32 v26, v77
v_add_f32_e32 v15, v15, v37
Mp 140,4,60
Rb 52,8,29696
s_add_u32 m0, s36, 4096
Mp 132,4,56
buffer_load_dwordx4 v3, s[20:23], s55 offen offset:0 sc1 lds
Mp 144,8,56
Rb 56,8,30720
v_exp_f32_e32 v39, v78
Mp 152,8,60
buffer_store_dwordx4 a[232:235], v2, s[20:23], s47 offen offset:2048 sc0 sc1
Mp 156,12,60
Rb 60,8,31744
s_waitcnt lgkmcnt(8)
)" R"(
Mp 148,12,56
v_exp_f32_e32 v27, v79
v_add_f32_e32 v15, v15, v25
Mp 160,16,56
Cv 37,37,25
v_add_f32_e32 v15, v15, v38
Mp 168,16,60
v_add_f32_e32 v15, v15, v26
Cv 38,38,26
v_add_f32_e32 v15, v15, v39
Mp 172,20,60
v_add_f32_e32 v15, v15, v27
Cv 39,39,27
Mp 164,20,56
buffer_load_dwordx4 v3, s[20:23], s55 offen offset:1024 sc1 lds
Mp 176,24,56
s_add_u32 s50, s50, 1024
s_min_u32 s50, s50, s60
s_sub_u32 s57, s57, 1
Mp 184,24,60
v_accvgpr_read_b32 v9, a202
v_accvgpr_read_b32 v11, a203
Mp 188,28,60
Cv 9,9,11
v_accvgpr_write_b32 a149, v9
Mp 180,28,56
v_accvgpr_read_b32 v9, a204
s_waitcnt vmcnt(25) lgkmcnt(0)
s_barrier
Mp 192,32,56
Rb 0,1,32768
v_accvgpr_read_b32 v11, a205
Mp 200,32,60
Rb 4,1,33792
Cv 9,9,11
v_accvgpr_write_b32 a150, v9
Mp 204,36,60
Rb 8,1,34816
v_accvgpr_read_b32 v9, a206
Mp 196,36,56
Rb 12,1,35840
buffer_load_dwordx4 v3, s[20:23], s55 offen offset:2048 sc1 lds
Mp 208,40,56
Rb 16,1,40960
v_accvgpr_read_b32 v11, a207
Cv 9,9,11
Mp 216,40,60
Rb 20,1,41984
buffer_store_dwordx4 a[236:239], v2, s[20:23], s47 offen offset:3072 sc0 sc1
Mp 220,44,60
Rb 24,1,43008
v_accvgpr_write_b32 a151, v9
Mp 212,44,56
Rb 28,1,44032
v_accvgpr_read_b32 v9, a208
v_accvgpr_read_b32 v11, a209
Mp 224,48,56
Rb 32,1,36864
Cv 9,9,11
Mp 232,48,60
Rb 36,1,37888
v_accvgpr_write_b32 a152, v9
v_accvgpr_read_b32 v9, a210
Mp 236,52,60
Rb 40,1,38912
)" R"(
v_accvgpr_read_b32 v11, a211
Mp 228,52,56
Rb 44,1,39936
buffer_load_dwordx4 v3, s[20:23], s55 offen offset:3072 sc1 lds
Mp 240,56,56
Cv 9,9,11
v_accvgpr_write_b32 a153, v9
v_accvgpr_read_b32 v9, a212
Mp 248,56,60
v_accvgpr_read_b32 v11, a213
Cv 9,9,11
Mp 252,60,60
v_accvgpr_write_b32 a154, v9
s_waitcnt lgkmcnt(8)
Mp 244,60,56
v_mfma_f32_16x16x32_bf16 v[96:99], a[0:3], a[64:67], v[16:19]
Rb 48,1,45056
buffer_load_dword v4, v5, s[20:23], s50 offen offset:0 sc0 sc1
v_mfma_f32_16x16x32_bf16 v[100:103], a[0:3], a[68:71], v[20:23]
v_exp_f32_e32 v40, v80
Mv 100,4,76
Rb 52,1,46080
s_add_u32 m0, s36, 65536
Mv 96,4,72
buffer_load_dwordx4 v2, s[20:23], s56 offen offset:0 sc1 lds
Mv 96,8,80
Rb 56,1,47104
v_add_f32_e32 v14, v14, v40
Mv 100,8,84
buffer_store_dwordx4 a[240:243], v3, s[20:23], s47 offen offset:0 sc0 sc1
Mv 100,12,92
Rb 60,1,48128
s_waitcnt lgkmcnt(8)
Mv 96,12,88
v_exp_f32_e32 v28, v81
Mv 96,16,96
Rb 0,1,49152
v_add_f32_e32 v14, v14, v28
Mv 100,16,100
v_exp_f32_e32 v41, v82
Mv 100,20,108
Rb 4,1,50176
Cv 40,40,28
Mv 96,20,104
buffer_load_dwordx4 v2, s[20:23], s56 offen offset:1024 sc1 lds
Mv 96,24,112
Rb 8,1,51200
v_add_f32_e32 v14, v14, v41
Mv 100,24,116
v_exp_f32_e32 v29, v83
Mv 100,28,124
Rb 12,1,52224
s_waitcnt lgkmcnt(8)
Mv 96,28,120
v_exp_f32_e32 v42, v88
v_add_f32_e32 v14, v14, v29
v_mfma_f32_16x16x32_bf16 v[104:107], a[32:35], a[64:67], v[16:19]
Rb 16,1,57344
Cv 41,41,29
v_mfma_f32_16x16x32_bf16 v[108:111], a[32:35], a[68:71], v[20:23]
v_exp_f32_e32 v24, v89
Mv 108,36,76
Rb 20,1,58368
v_add_f32_e32 v14, v14, v42
)" R"(
Mv 104,36,72
buffer_load_dwordx4 v2, s[20:23], s56 offen offset:2048 sc1 lds
Mv 104,40,80
Rb 24,1,59392
v_add_f32_e32 v14, v14, v24
Mv 108,40,84
buffer_store_dwordx4 a[244:247], v3, s[20:23], s47 offen offset:1024 sc0 sc1
Mv 108,44,92
Rb 28,1,60416
s_waitcnt lgkmcnt(8)
Mv 104,44,88
v_exp_f32_e32 v43, v90
Mv 104,48,96
Rb 32,1,53248
Cv 42,42,24
Mv 108,48,100
v_exp_f32_e32 v25, v91
Mv 108,52,108
Rb 36,1,54272
v_add_f32_e32 v14, v14, v43
Mv 104,52,104
buffer_load_dwordx4 v2, s[20:23], s56 offen offset:3072 sc1 lds
Mv 104,56,112
Rb 40,1,55296
v_add_f32_e32 v14, v14, v25
Mv 108,56,116
v_exp_f32_e32 v44, v84
Mv 108,60,124
Rb 44,1,56320
s_waitcnt lgkmcnt(8)
Mv 104,60,120
v_exp_f32_e32 v26, v85
Cv 43,43,25
v_mfma_f32_16x16x32_bf16 v[112:115], a[0:3], a[64:67], v[16:19]
Rb 48,1,61440
v_add_f32_e32 v15, v15, v44
v_mfma_f32_16x16x32_bf16 v[116:119], a[0:3], a[68:71], v[20:23]
v_exp_f32_e32 v45, v86
Mv 116,4,76
Rb 52,1,62464
s_add_u32 m0, s36, 69632
Mv 112,4,72
buffer_load_dwordx4 v3, s[20:23], s56 offen offset:0 sc1 lds
Mv 112,8,80
Rb 56,1,63488
v_add_f32_e32 v15, v15, v26
Mv 116,8,84
buffer_store_dwordx4 a[248:251], v3, s[20:23], s47 offen offset:2048 sc0 sc1
Mv 116,12,92
Rb 60,1,64512
s_waitcnt lgkmcnt(8)
Mv 112,12,88
v_exp_f32_e32 v27, v87
Mv 112,16,96
v_exp_f32_e32 v46, v92
Mv 116,16,100
v_exp_f32_e32 v28, v93
Mv 116,20,108
v_exp_f32_e32 v47, v94
Mv 112,20,104
buffer_load_dwordx4 v3, s[20:23], s56 offen offset:1024 sc1 lds
Mv 112,24,112
v_exp_f32_e32 v29, v95
Mv 116,24,116
)" R"(
Cv 44,44,26
v_add_f32_e32 v15, v15, v45
Mv 116,28,124
v_add_f32_e32 v15, v15, v27
Cv 45,45,27
Mv 112,28,120
v_add_f32_e32 v15, v15, v46
s_waitcnt vmcnt(30) lgkmcnt(0)
s_barrier
v_mfma_f32_16x16x32_bf16 v[120:123], a[32:35], a[64:67], v[16:19]
Rb 0,8,32768
v_add_f32_e32 v15, v15, v28
v_mfma_f32_16x16x32_bf16 v[124:127], a[32:35], a[68:71], v[20:23]
Rb 4,8,33792
Cv 46,46,28
Mv 124,36,76
Rb 8,8,34816
v_add_f32_e32 v15, v15, v47
Mv 120,36,72
Rb 12,8,35840
buffer_load_dwordx4 v3, s[20:23], s56 offen offset:2048 sc1 lds
Mv 120,40,80
Rb 16,8,36864
v_add_f32_e32 v15, v15, v29
Mv 124,40,84
Rb 20,8,37888
buffer_store_dwordx4 a[252:255], v3, s[20:23], s47 offen offset:3072 sc0 sc1
Mv 124,44,92
Rb 24,8,38912
Cv 47,47,29
Mv 120,44,88
Rb 28,8,39936
v_max_f32_e32 v11, v14, v15
Mv 120,48,96
Rb 32,8,40960
v_cmp_nge_f32_e32 vcc, s62, v11
Mv 124,48,100
Rb 36,8,41984
s_add_u32 s55, s55, 0x10000
Mv 124,52,108
Rb 40,8,43008
Mv 120,52,104
Rb 44,8,44032
buffer_load_dwordx4 v3, s[20:23], s56 offen offset:3072 sc1 lds
Mv 120,56,112
s_cmp_eq_u32 s57, 2
s_cselect_b32 s55, s59, s55
Mv 124,56,116
s_add_u32 s56, s56, 0x10000
Mv 124,60,124
s_waitcnt lgkmcnt(8)
s_cbranch_vccnz .Lzfx8
.Lzfr8:
Mv 120,60,120
v_add_f32_e32 v12, v12, v14
v_add_f32_e32 v13, v13, v15
Mp 128,0,32
Rb 48,8,45056
Mp 136,0,36
s_cmp_le_i32 s57, s58
s_cbranch_scc1 .Lzmk7
.Lzmr7:
Mp 140,4,36
Rb 52,8,46080
s_add_u32 m0, s36, 32768
)" R"(
Mp 132,4,32
buffer_load_dwordx4 v2, s[20:23], s55 offen offset:0 sc1 lds
Mp 144,8,32
Rb 56,8,47104
Mp 152,8,36
v_exp_f32_e32 v48, v96
Mp 156,12,36
Rb 60,8,48128
s_waitcnt lgkmcnt(8)
Mp 148,12,32
v_exp_f32_e32 v24, v97
Mp 160,16,32
Rb 0,8,49152
v_add_f32_e32 v14, v48, v24
Mp 168,16,36
v_exp_f32_e32 v49, v98
Mp 172,20,36
Rb 4,8,50176
Cv 48,48,24
Mp 164,20,32
buffer_load_dwordx4 v2, s[20:23], s55 offen offset:1024 sc1 lds
Mp 176,24,32
Rb 8,8,51200
v_exp_f32_e32 v25, v99
Mp 184,24,36
v_exp_f32_e32 v50, v104
Mp 188,28,36
Rb 12,8,52224
s_waitcnt lgkmcnt(8)
Mp 180,28,32
v_exp_f32_e32 v26, v105
Mp 192,32,32
Rb 16,8,53248
v_add_f32_e32 v14, v14, v49
Mp 200,32,36
v_exp_f32_e32 v51, v106
v_add_f32_e32 v14, v14, v25
Mp 204,36,36
Rb 20,8,54272
Cv 49,49,25
Mp 196,36,32
buffer_load_dwordx4 v2, s[20:23], s55 offen offset:2048 sc1 lds
Mp 208,40,32
Rb 24,8,55296
v_add_f32_e32 v14, v14, v50
Mp 216,40,36
v_exp_f32_e32 v27, v107
Mp 220,44,36
Rb 28,8,56320
s_waitcnt lgkmcnt(8)
Mp 212,44,32
v_exp_f32_e32 v52, v100
v_add_f32_e32 v14, v14, v26
Mp 224,48,32
Rb 32,8,57344
Cv 50,50,26
Mp 232,48,36
v_exp_f32_e32 v28, v101
Mp 236,52,36
Rb 36,8,58368
v_add_f32_e32 v14, v14, v51
Mp 228,52,32
buffer_load_dwordx4 v2, s[20:23], s55 offen offset:3072 sc1 lds
Mp 240,56,32
)" R"(
Rb 40,8,59392
v_exp_f32_e32 v53, v102
Mp 248,56,36
v_exp_f32_e32 v29, v103
Mp 252,60,36
Rb 44,8,60416
s_waitcnt lgkmcnt(8)
Mp 244,60,32
v_exp_f32_e32 v54, v108
Mp 128,0,40
Rb 48,8,61440
v_add_f32_e32 v14, v14, v27
Mp 136,0,44
v_exp_f32_e32 v24, v109
Cv 51,51,27
Mp 140,4,44
Rb 52,8,62464
s_add_u32 m0, s36, 36864
Mp 132,4,40
buffer_load_dwordx4 v3, s[20:23], s55 offen offset:0 sc1 lds
Mp 144,8,40
Rb 56,8,63488
v_add_f32_e32 v15, v52, v28
Mp 152,8,44
v_exp_f32_e32 v55, v110
Mp 156,12,44
Rb 60,8,64512
s_waitcnt lgkmcnt(8)
Mp 148,12,40
v_exp_f32_e32 v25, v111
Cv 52,52,28
Mp 160,16,40
v_add_f32_e32 v15, v15, v53
v_add_f32_e32 v15, v15, v29
Mp 168,16,44
Cv 53,53,29
v_add_f32_e32 v15, v15, v54
Mp 172,20,44
v_add_f32_e32 v15, v15, v24
Cv 54,54,24
Mp 164,20,40
buffer_load_dwordx4 v3, s[20:23], s55 offen offset:1024 sc1 lds
Mp 176,24,40
v_add_f32_e32 v15, v15, v55
v_add_f32_e32 v15, v15, v25
Cv 55,55,25
Mp 184,24,44
s_sub_u32 s57, s57, 1
v_accvgpr_read_b32 v9, a214
Mp 188,28,44
v_accvgpr_read_b32 v11, a215
Cv 9,9,11
Mp 180,28,40
s_waitcnt vmcnt(7) lgkmcnt(0)
s_barrier
buffer_store_dword v6, v7, s[20:23], s49 offen offset:2048 sc0 sc1
Mp 192,32,40
Rb 0,1,0
v_accvgpr_write_b32 a155, v9
Mp 200,32,44
Rb 4,1,1024
v_accvgpr_read_b32 v9, a216
v_accvgpr_read_b32 v11, a217
Mp 204,36,44
Rb 8,1,2048
)" R"(
Cv 9,9,11
Mp 196,36,40
Rb 12,1,3072
buffer_load_dwordx4 v3, s[20:23], s55 offen offset:2048 sc1 lds
Mp 208,40,40
Rb 16,1,8192
v_accvgpr_write_b32 a156, v9
Mp 216,40,44
Rb 20,1,9216
v_accvgpr_read_b32 v9, a218
Mp 220,44,44
Rb 24,1,10240
v_accvgpr_read_b32 v11, a219
Mp 212,44,40
Rb 28,1,11264
Cv 9,9,11
v_accvgpr_write_b32 a157, v9
Mp 224,48,40
Rb 32,1,4096
v_accvgpr_read_b32 v9, a220
Mp 232,48,44
Rb 36,1,5120
v_accvgpr_read_b32 v11, a221
Mp 236,52,44
Rb 40,1,6144
Cv 9,9,11
Mp 228,52,40
Rb 44,1,7168
buffer_load_dwordx4 v3, s[20:23], s55 offen offset:3072 sc1 lds
Mp 240,56,40
v_cmp_eq_u32_e32 vcc, s3, v4
s_andn2_b64 s[28:29], -1, vcc
s_cbranch_scc1 .Lzsp19
.Lzok19:
Mp 248,56,44
v_accvgpr_write_b32 a158, v9
v_accvgpr_read_b32 v9, a222
Mp 252,60,44
v_accvgpr_read_b32 v11, a223
s_waitcnt lgkmcnt(8)
Mp 244,60,40
Cv 9,9,11
v_accvgpr_write_b32 a159, v9
v_mfma_f32_16x16x32_bf16 v[64:67], a[0:3], a[64:67], v[16:19]
Rb 48,1,12288
v_lshrrev_b32 v9, 2, v0
v_mfma_f32_16x16x32_bf16 v[68:71], a[0:3], a[68:71], v[20:23]
v_mul_u32_u24 v9, 448, v9
s_lshr_b32 s92, s33, 1
Mv 68,4,76
Rb 52,1,13312
s_add_u32 m0, s36, 98304
Mv 64,4,72
buffer_load_dwordx4 v2, s[20:23], s56 offen offset:0 sc1 lds
Mv 64,8,80
Rb 56,1,14336
s_mul_i32 s92, s92, 48
Mv 68,8,84
buffer_store_dwordx4 a[128:131], v2, s[20:23], s48 offen offset:0 sc0 sc1
Mv 68,12,92
Rb 60,1,15360
s_waitcnt lgkmcnt(8)
Mv 64,12,88
s_lshl3_add_u32 s92, s33, s92
)" R"(
v_add3_u32 v9, v9, s92, v10
Mv 64,16,96
Rb 0,1,16384
Mv 68,16,100
v_exp_f32_e32 v56, v112
Mv 68,20,108
Rb 4,1,17408
Rt 160,9,0
Mv 64,20,104
buffer_load_dwordx4 v2, s[20:23], s56 offen offset:1024 sc1 lds
Mv 64,24,112
Rb 8,1,18432
v_exp_f32_e32 v26, v113
Mv 68,24,116
v_exp_f32_e32 v57, v114
Mv 68,28,124
Rb 12,1,19456
s_waitcnt lgkmcnt(9)
Mv 64,28,120
v_exp_f32_e32 v27, v115
v_add_f32_e32 v14, v14, v56
v_mfma_f32_16x16x32_bf16 v[72:75], a[32:35], a[64:67], v[16:19]
Rb 16,1,24576
v_add_f32_e32 v14, v14, v26
v_mfma_f32_16x16x32_bf16 v[76:79], a[32:35], a[68:71], v[20:23]
v_exp_f32_e32 v58, v120
Mv 76,36,76
Rb 20,1,25600
Cv 56,56,26
Mv 72,36,72
buffer_load_dwordx4 v2, s[20:23], s56 offen offset:2048 sc1 lds
v_add_f32_e32 v14, v14, v57
Mv 72,40,80
Rb 24,1,26624
Rt 162,9,8192
Mv 76,40,84
v_exp_f32_e32 v28, v121
Mv 76,44,92
Rb 28,1,27648
v_add_f32_e32 v14, v14, v27
s_waitcnt lgkmcnt(10)
Mv 72,44,88
v_exp_f32_e32 v59, v122
Mv 72,48,96
Rb 32,1,20480
Cv 57,57,27
Mv 76,48,100
v_exp_f32_e32 v29, v123
v_add_f32_e32 v14, v14, v58
Mv 76,52,108
Rb 36,1,21504
v_add_f32_e32 v14, v14, v28
Mv 72,52,104
buffer_load_dwordx4 v2, s[20:23], s56 offen offset:3072 sc1 lds
Mv 72,56,112
Rb 40,1,22528
v_exp_f32_e32 v60, v116
Mv 76,56,116
buffer_store_dwordx4 a[132:135], v2, s[20:23], s48 offen offset:1024 sc0 sc1
Mv 76,60,124
Rb 44,1,23552
Rt 164,9,128
s_waitcnt lgkmcnt(10)
Mv 72,60,120
)" R"(
v_exp_f32_e32 v24, v117
Cv 58,58,28
v_mfma_f32_16x16x32_bf16 v[80:83], a[0:3], a[64:67], v[16:19]
Rb 48,1,28672
v_add_f32_e32 v14, v14, v59
v_mfma_f32_16x16x32_bf16 v[84:87], a[0:3], a[68:71], v[20:23]
v_exp_f32_e32 v61, v118
Mv 84,4,76
Rb 52,1,29696
s_add_u32 m0, s36, 102400
Mv 80,4,72
buffer_load_dwordx4 v3, s[20:23], s56 offen offset:0 sc1 lds
v_add_f32_e32 v14, v14, v29
Mv 80,8,80
Rb 56,1,30720
Cv 59,59,29
Mv 84,8,84
v_exp_f32_e32 v25, v119
Mv 84,12,92
Rb 60,1,31744
v_add_f32_e32 v15, v15, v60
s_waitcnt lgkmcnt(9)
Mv 80,12,88
v_exp_f32_e32 v62, v124
Mv 80,16,96
Rt 166,9,8320
v_add_f32_e32 v15, v15, v24
Mv 84,16,100
buffer_store_dwordx4 a[160:163], v1, s[20:23], s4 offen sc0 sc1
Cv 60,60,24
Mv 84,20,108
v_exp_f32_e32 v26, v125
Mv 80,20,104
buffer_load_dwordx4 v3, s[20:23], s56 offen offset:1024 sc1 lds
Mv 80,24,112
v_exp_f32_e32 v63, v126
v_add_f32_e32 v15, v15, v61
Mv 84,24,116
v_exp_f32_e32 v27, v127
Mv 84,28,124
v_add_f32_e32 v15, v15, v25
Cv 61,61,25
Mv 80,28,120
v_add_f32_e32 v15, v15, v62
s_waitcnt vmcnt(18) lgkmcnt(0)
s_barrier
v_mfma_f32_16x16x32_bf16 v[88:91], a[32:35], a[64:67], v[16:19]
Rb 0,8,0
v_add_f32_e32 v15, v15, v26
v_mfma_f32_16x16x32_bf16 v[92:95], a[32:35], a[68:71], v[20:23]
Rb 4,8,1024
Cv 62,62,26
Mv 92,36,76
Rb 8,8,2048
Rt 168,9,256
Mv 88,36,72
Rb 12,8,3072
buffer_load_dwordx4 v3, s[20:23], s56 offen offset:2048 sc1 lds
Mv 88,40,80
Rb 16,8,4096
v_add_f32_e32 v15, v15, v63
Mv 92,40,84
Rb 20,8,5120
buffer_store_dwordx4 a[136:139], v2, s[20:23], s48 offen offset:2048 sc0 sc1
Mv 92,44,92
)" R"(
Rb 24,8,6144
v_add_f32_e32 v15, v15, v27
Cv 63,63,27
Mv 88,44,88
Rb 28,8,7168
v_max_f32_e32 v11, v14, v15
Mv 88,48,96
Rb 32,8,8192
v_cmp_nge_f32_e32 vcc, s62, v11
Mv 92,48,100
Rb 36,8,9216
s_add_u32 s55, s55, 0x10000
Mv 92,52,108
Rb 40,8,10240
Mv 88,52,104
Rb 44,8,11264
buffer_load_dwordx4 v3, s[20:23], s56 offen offset:3072 sc1 lds
Mv 88,56,112
Rt 170,9,8448
s_cmp_eq_u32 s57, 2
s_cselect_b32 s55, s59, s55
Mv 92,56,116
s_add_u32 s56, s56, 0x10000
buffer_store_dwordx4 a[164:167], v1, s[20:23], s5 offen sc0 sc1
Mv 92,60,124
s_waitcnt lgkmcnt(9)
s_cbranch_vccnz .Lzfx9
.Lzfr9:
Mv 88,60,120
v_add_f32_e32 v12, v12, v14
v_add_f32_e32 v13, v13, v15
Mp 128,0,48
Rb 48,8,12288
Mp 136,0,52
s_cmp_le_i32 s57, s58
s_cbranch_scc1 .Lzmk8
.Lzmr8:
Mp 140,4,52
Rb 52,8,13312
s_add_u32 m0, s36, 0
Mp 132,4,48
buffer_load_dwordx4 v2, s[20:23], s55 offen offset:0 sc1 lds
Mp 144,8,48
Rb 56,8,14336
Mp 152,8,52
v_exp_f32_e32 v32, v64
Mp 156,12,52
Rb 60,8,15360
Rt 172,9,384
s_waitcnt lgkmcnt(10)
Mp 148,12,48
v_exp_f32_e32 v28, v65
Mp 160,16,48
Rb 0,8,16384
v_add_f32_e32 v14, v32, v28
Mp 168,16,52
v_exp_f32_e32 v33, v66
Cv 32,32,28
Mp 172,20,52
Rb 4,8,17408
v_add_f32_e32 v14, v14, v33
Mp 164,20,48
buffer_load_dwordx4 v2, s[20:23], s55 offen offset:1024 sc1 lds
Mp 176,24,48
)" R"(
Rb 8,8,18432
Mp 184,24,52
buffer_store_dwordx4 a[140:143], v2, s[20:23], s48 offen offset:3072 sc0 sc1
Mp 188,28,52
Rb 12,8,19456
s_waitcnt lgkmcnt(10)
Mp 180,28,48
v_exp_f32_e32 v29, v67
Mp 192,32,48
Rb 16,8,20480
Rt 174,9,8576
Mp 200,32,52
v_exp_f32_e32 v34, v72
v_add_f32_e32 v14, v14, v29
Mp 204,36,52
Rb 20,8,21504
Cv 33,33,29
Mp 196,36,48
buffer_load_dwordx4 v2, s[20:23], s55 offen offset:2048 sc1 lds
Mp 208,40,48
Rb 24,8,22528
v_add_f32_e32 v14, v14, v34
Mp 216,40,52
v_exp_f32_e32 v24, v73
Mp 220,44,52
Rb 28,8,23552
s_waitcnt lgkmcnt(10)
Mp 212,44,48
buffer_store_dwordx4 a[168:171], v1, s[20:23], s6 offen sc0 sc1
Mp 224,48,48
Rb 32,8,24576
v_add_f32_e32 v14, v14, v24
Mp 232,48,52
v_exp_f32_e32 v35, v74
Cv 34,34,24
Mp 236,52,52
Rb 36,8,25600
Rt 176,9,16384
Mp 228,52,48
buffer_load_dwordx4 v2, s[20:23], s55 offen offset:3072 sc1 lds
Mp 240,56,48
Rb 40,8,26624
v_add_f32_e32 v14, v14, v35
Mp 248,56,52
v_exp_f32_e32 v25, v75
Mp 252,60,52
Rb 44,8,27648
s_waitcnt lgkmcnt(10)
Mp 244,60,48
v_exp_f32_e32 v36, v68
Mp 128,0,56
Rb 48,8,28672
v_add_f32_e32 v14, v14, v25
Mp 136,0,60
v_exp_f32_e32 v26, v69
Cv 35,35,25
Mp 140,4,60
Rb 52,8,29696
s_add_u32 m0, s36, 4096
Mp 132,4,56
buffer_load_dwordx4 v3, s[20:23], s55 offen offset:0 sc1 lds
Mp 144,8,56
Rb 56,8,30720
Rt 178,9,24576
)" R"(
Mp 152,8,60
buffer_store_dwordx4 a[144:147], v3, s[20:23], s48 offen offset:0 sc0 sc1
Mp 156,12,60
Rb 60,8,31744
s_waitcnt lgkmcnt(10)
Mp 148,12,56
v_exp_f32_e32 v37, v70
Mp 160,16,56
v_exp_f32_e32 v27, v71
Mp 168,16,60
v_exp_f32_e32 v38, v76
v_add_f32_e32 v15, v36, v26
Mp 172,20,60
v_exp_f32_e32 v28, v77
Mp 164,20,56
buffer_load_dwordx4 v3, s[20:23], s55 offen offset:1024 sc1 lds
Mp 176,24,56
s_add_u32 s50, s50, 1024
s_min_u32 s50, s50, s60
Mp 184,24,60
buffer_store_dwordx4 a[172:175], v1, s[20:23], s7 offen sc0 sc1
Mp 188,28,60
Rt 180,9,16512
Cv 36,36,26
Mp 180,28,56
s_waitcnt vmcnt(23) lgkmcnt(1)
s_barrier
Mp 192,32,56
Rb 0,1,32768
v_add_f32_e32 v15, v15, v37
Mp 200,32,60
Rb 4,1,33792
v_exp_f32_e32 v39, v78
Mp 204,36,60
Rb 8,1,34816
v_add_f32_e32 v15, v15, v27
Mp 196,36,56
Rb 12,1,35840
buffer_load_dwordx4 v3, s[20:23], s55 offen offset:2048 sc1 lds
Mp 208,40,56
Rb 16,1,40960
Cv 37,37,27
Mp 216,40,60
Rb 20,1,41984
v_add_f32_e32 v15, v15, v38
Mp 220,44,60
Rb 24,1,43008
v_add_f32_e32 v15, v15, v28
Mp 212,44,56
Rb 28,1,44032
Cv 38,38,28
Mp 224,48,56
Rb 32,1,36864
Rt 182,9,24704
Mp 232,48,60
Rb 36,1,37888
v_exp_f32_e32 v29, v79
Mp 236,52,60
Rb 40,1,38912
v_add_f32_e32 v15, v15, v39
Mp 228,52,56
Rb 44,1,39936
buffer_load_dwordx4 v3, s[20:23], s55 offen offset:3072 sc1 lds
Mp 240,56,56
v_add_f32_e32 v15, v15, v29
)" R"(
Cv 39,39,29
Mp 248,56,60
buffer_store_dwordx4 a[148:151], v3, s[20:23], s48 offen offset:1024 sc0 sc1
Mp 252,60,60
s_sub_u32 s57, s57, 1
s_waitcnt lgkmcnt(9)
Mp 244,60,56
v_mfma_f32_16x16x32_bf16 v[96:99], a[0:3], a[64:67], v[16:19]
Rb 48,1,45056
buffer_load_dword v4, v5, s[20:23], s50 offen offset:0 sc0 sc1
v_mfma_f32_16x16x32_bf16 v[100:103], a[0:3], a[68:71], v[20:23]
v_exp_f32_e32 v40, v80
Mv 100,4,76
Rb 52,1,46080
s_add_u32 m0, s36, 65536
Rt 184,9,16640
Mv 96,4,72
buffer_load_dwordx4 v2, s[20:23], s56 offen offset:0 sc1 lds
Mv 96,8,80
Rb 56,1,47104
v_add_f32_e32 v14, v14, v40
Mv 100,8,84
v_exp_f32_e32 v24, v81
Mv 100,12,92
Rb 60,1,48128
s_waitcnt lgkmcnt(10)
Mv 96,12,88
buffer_store_dwordx4 a[176:179], v1, s[20:23], s8 offen sc0 sc1
v_add_f32_e32 v14, v14, v24
Mv 96,16,96
Rb 0,1,49152
Cv 40,40,24
Mv 100,16,100
v_exp_f32_e32 v41, v82
Mv 100,20,108
Rb 4,1,50176
v_add_f32_e32 v14, v14, v41
Mv 96,20,104
buffer_load_dwordx4 v2, s[20:23], s56 offen offset:1024 sc1 lds
Mv 96,24,112
Rb 8,1,51200
Rt 186,9,24832
Mv 100,24,116
v_exp_f32_e32 v25, v83
Mv 100,28,124
Rb 12,1,52224
s_waitcnt lgkmcnt(10)
Mv 96,28,120
v_exp_f32_e32 v42, v88
v_add_f32_e32 v14, v14, v25
v_mfma_f32_16x16x32_bf16 v[104:107], a[32:35], a[64:67], v[16:19]
Rb 16,1,57344
Cv 41,41,25
v_mfma_f32_16x16x32_bf16 v[108:111], a[32:35], a[68:71], v[20:23]
v_exp_f32_e32 v26, v89
Mv 108,36,76
Rb 20,1,58368
v_add_f32_e32 v14, v14, v42
Mv 104,36,72
buffer_load_dwordx4 v2, s[20:23], s56 offen offset:2048 sc1 lds
Mv 104,40,80
Rb 24,1,59392
v_add_f32_e32 v14, v14, v26
Mv 108,40,84
)" R"(
buffer_store_dwordx4 a[152:155], v3, s[20:23], s48 offen offset:2048 sc0 sc1
Mv 108,44,92
Rb 28,1,60416
Rt 188,9,16768
s_waitcnt lgkmcnt(10)
Mv 104,44,88
v_exp_f32_e32 v43, v90
Cv 42,42,26
Mv 104,48,96
Rb 32,1,53248
v_add_f32_e32 v14, v14, v43
Mv 108,48,100
v_exp_f32_e32 v27, v91
Mv 108,52,108
Rb 36,1,54272
v_add_f32_e32 v14, v14, v27
Mv 104,52,104
buffer_load_dwordx4 v2, s[20:23], s56 offen offset:3072 sc1 lds
Mv 104,56,112
Rb 40,1,55296
Cv 43,43,27
Mv 108,56,116
buffer_store_dwordx4 a[180:183], v1, s[20:23], s9 offen sc0 sc1
Mv 108,60,124
Rb 44,1,56320
s_waitcnt lgkmcnt(9)
Mv 104,60,120
v_exp_f32_e32 v44, v84
s_add_u32 s55, s55, 0x10000
v_mfma_f32_16x16x32_bf16 v[112:115], a[0:3], a[64:67], v[16:19]
Rb 48,1,61440
Rt 190,9,24960
v_mfma_f32_16x16x32_bf16 v[116:119], a[0:3], a[68:71], v[20:23]
v_exp_f32_e32 v28, v85
Mv 116,4,76
Rb 52,1,62464
s_add_u32 m0, s36, 69632
Mv 112,4,72
buffer_load_dwordx4 v3, s[20:23], s56 offen offset:0 sc1 lds
Mv 112,8,80
Rb 56,1,63488
v_add_f32_e32 v15, v15, v44
Mv 116,8,84
v_exp_f32_e32 v45, v86
Mv 116,12,92
Rb 60,1,64512
s_waitcnt lgkmcnt(10)
Mv 112,12,88
v_exp_f32_e32 v29, v87
v_add_f32_e32 v15, v15, v28
Mv 112,16,96
v_exp_f32_e32 v46, v92
Mv 116,16,100
v_exp_f32_e32 v24, v93
Mv 116,20,108
v_exp_f32_e32 v47, v94
Mv 112,20,104
buffer_load_dwordx4 v3, s[20:23], s56 offen offset:1024 sc1 lds
Mv 112,24,112
v_exp_f32_e32 v25, v95
Mv 116,24,116
buffer_store_dwordx4 a[156:159], v3, s[20:23], s48 offen offset:3072 sc0 sc1
Mv 116,28,124
s_cmp_eq_u32 s57, 2
)" R"(
s_cselect_b32 s55, s59, s55
Mv 112,28,120
Cv 44,44,28
s_waitcnt vmcnt(25) lgkmcnt(0)
s_barrier
v_mfma_f32_16x16x32_bf16 v[120:123], a[32:35], a[64:67], v[16:19]
Rb 0,8,32768
v_add_f32_e32 v15, v15, v45
v_mfma_f32_16x16x32_bf16 v[124:127], a[32:35], a[68:71], v[20:23]
Rb 4,8,33792
v_add_f32_e32 v15, v15, v29
Mv 124,36,76
Rb 8,8,34816
Cv 45,45,29
Mv 120,36,72
Rb 12,8,35840
buffer_load_dwordx4 v3, s[20:23], s56 offen offset:2048 sc1 lds
Mv 120,40,80
Rb 16,8,36864
v_add_f32_e32 v15, v15, v46
Mv 124,40,84
Rb 20,8,37888
v_add_f32_e32 v15, v15, v24
Mv 124,44,92
Rb 24,8,38912
Cv 46,46,24
Mv 120,44,88
Rb 28,8,39936
buffer_store_dwordx4 a[184:187], v1, s[20:23], s10 offen sc0 sc1
Mv 120,48,96
Rb 32,8,40960
v_add_f32_e32 v15, v15, v47
Mv 124,48,100
Rb 36,8,41984
v_add_f32_e32 v15, v15, v25
Mv 124,52,108
Rb 40,8,43008
Cv 47,47,25
Mv 120,52,104
Rb 44,8,44032
buffer_load_dwordx4 v3, s[20:23], s56 offen offset:3072 sc1 lds
Mv 120,56,112
v_max_f32_e32 v11, v14, v15
v_cmp_nge_f32_e32 vcc, s62, v11
Mv 124,56,116
s_add_u32 s56, s56, 0x10000
Mv 124,60,124
s_waitcnt lgkmcnt(8)
s_cbranch_vccnz .Lzfx10
.Lzfr10:
Mv 120,60,120
v_add_f32_e32 v12, v12, v14
v_add_f32_e32 v13, v13, v15
Mp 128,0,32
Rb 48,8,45056
Mp 136,0,36
s_cmp_le_i32 s57, s58
s_cbranch_scc1 .Lzmk9
.Lzmr9:
Mp 140,4,36
Rb 52,8,46080
s_add_u32 m0, s36, 32768
Mp 132,4,32
buffer_load_dwordx4 v2, s[20:23], s55 offen offset:0 sc1 lds
Mp 144,8,32
)" R"(
Rb 56,8,47104
Mp 152,8,36
buffer_store_dwordx4 a[188:191], v1, s[20:23], s11 offen sc0 sc1
Mp 156,12,36
Rb 60,8,48128
s_waitcnt lgkmcnt(8)
Mp 148,12,32
v_exp_f32_e32 v48, v96
Mp 160,16,32
Rb 0,8,49152
Mp 168,16,36
v_exp_f32_e32 v26, v97
Mp 172,20,36
Rb 4,8,50176
v_add_f32_e32 v14, v48, v26
Mp 164,20,32
buffer_load_dwordx4 v2, s[20:23], s55 offen offset:1024 sc1 lds
Mp 176,24,32
Rb 8,8,51200
Cv 48,48,26
Mp 184,24,36
v_exp_f32_e32 v49, v98
Mp 188,28,36
Rb 12,8,52224
s_waitcnt lgkmcnt(8)
Mp 180,28,32
v_exp_f32_e32 v27, v99
Mp 192,32,32
Rb 16,8,53248
v_add_f32_e32 v14, v14, v49
Mp 200,32,36
v_exp_f32_e32 v50, v104
Mp 204,36,36
Rb 20,8,54272
v_add_f32_e32 v14, v14, v27
Mp 196,36,32
buffer_load_dwordx4 v2, s[20:23], s55 offen offset:2048 sc1 lds
Mp 208,40,32
Rb 24,8,55296
Cv 49,49,27
Mp 216,40,36
v_exp_f32_e32 v28, v105
Mp 220,44,36
Rb 28,8,56320
s_waitcnt lgkmcnt(8)
Mp 212,44,32
v_exp_f32_e32 v51, v106
Mp 224,48,32
Rb 32,8,57344
v_add_f32_e32 v14, v14, v50
Mp 232,48,36
v_exp_f32_e32 v29, v107
Mp 236,52,36
Rb 36,8,58368
v_add_f32_e32 v14, v14, v28
Mp 228,52,32
buffer_load_dwordx4 v2, s[20:23], s55 offen offset:3072 sc1 lds
Mp 240,56,32
Rb 40,8,59392
Cv 50,50,28
Mp 248,56,36
v_exp_f32_e32 v52, v100
Mp 252,60,36
Rb 44,8,60416
)" R"(
s_waitcnt lgkmcnt(8)
Mp 244,60,32
v_exp_f32_e32 v24, v101
Mp 128,0,40
Rb 48,8,61440
v_add_f32_e32 v14, v14, v51
Mp 136,0,44
v_exp_f32_e32 v53, v102
Mp 140,4,44
Rb 52,8,62464
s_add_u32 m0, s36, 36864
Mp 132,4,40
buffer_load_dwordx4 v3, s[20:23], s55 offen offset:0 sc1 lds
Mp 144,8,40
Rb 56,8,63488
v_add_f32_e32 v14, v14, v29
Mp 152,8,44
v_exp_f32_e32 v25, v103
Mp 156,12,44
Rb 60,8,64512
s_waitcnt lgkmcnt(8)
Mp 148,12,40
v_exp_f32_e32 v54, v108
Mp 160,16,40
v_exp_f32_e32 v26, v109
Mp 168,16,44
v_exp_f32_e32 v55, v110
Mp 172,20,44
v_exp_f32_e32 v27, v111
Mp 164,20,40
buffer_load_dwordx4 v3, s[20:23], s55 offen offset:1024 sc1 lds
Mp 176,24,40
Cv 51,51,29
v_add_f32_e32 v15, v52, v24
Mp 184,24,44
Cv 52,52,24
v_add_f32_e32 v15, v15, v53
Mp 188,28,44
v_add_f32_e32 v15, v15, v25
Cv 53,53,25
Mp 180,28,40
s_waitcnt vmcnt(5) lgkmcnt(0)
s_barrier
buffer_store_dword v6, v7, s[20:23], s49 offen offset:3072 sc0 sc1
Mp 192,32,40
Rb 0,1,0
v_add_f32_e32 v15, v15, v54
Mp 200,32,44
Rb 4,1,1024
v_add_f32_e32 v15, v15, v26
Mp 204,36,44
Rb 8,1,2048
Cv 54,54,26
Mp 196,36,40
Rb 12,1,3072
buffer_load_dwordx4 v3, s[20:23], s55 offen offset:2048 sc1 lds
Mp 208,40,40
Rb 16,1,8192
v_add_f32_e32 v15, v15, v55
Mp 216,40,44
Rb 20,1,9216
v_add_f32_e32 v15, v15, v27
Mp 220,44,44
Rb 24,1,10240
Cv 55,55,27
)" R"(
Mp 212,44,40
Rb 28,1,11264
s_sub_u32 s57, s57, 1
Mp 224,48,40
Rb 32,1,4096
Mp 232,48,44
Rb 36,1,5120
Mp 236,52,44
Rb 40,1,6144
Mp 228,52,40
Rb 44,1,7168
buffer_load_dwordx4 v3, s[20:23], s55 offen offset:3072 sc1 lds
Mp 240,56,40
v_cmp_eq_u32_e32 vcc, s3, v4
s_andn2_b64 s[28:29], -1, vcc
s_cbranch_scc1 .Lzsp20
.Lzok20:
Mp 248,56,44
Mp 252,60,44
s_waitcnt lgkmcnt(8)
Mp 244,60,40
.LzL:
v_mfma_f32_16x16x32_bf16 v[64:67], a[0:3], a[64:67], v[16:19]
Rb 48,1,12288
v_mfma_f32_16x16x32_bf16 v[68:71], a[0:3], a[68:71], v[20:23]
v_exp_f32_e32 v56, v112
Mv 68,4,76
Rb 52,1,13312
s_add_u32 m0, s36, 98304
Mv 64,4,72
buffer_load_dwordx4 v2, s[20:23], s56 offen offset:0 sc1 lds
Mv 64,8,80
Rb 56,1,14336
v_add_f32_e32 v14, v14, v56
Mv 68,8,84
v_exp_f32_e32 v24, v113
Mv 68,12,92
Rb 60,1,15360
s_waitcnt lgkmcnt(8)
Mv 64,12,88
v_exp_f32_e32 v57, v114
Mv 64,16,96
Rb 0,1,16384
v_add_f32_e32 v14, v14, v24
Mv 68,16,100
v_exp_f32_e32 v25, v115
Mv 68,20,108
Rb 4,1,17408
Cv 56,56,24
Mv 64,20,104
buffer_load_dwordx4 v2, s[20:23], s56 offen offset:1024 sc1 lds
Mv 64,24,112
Rb 8,1,18432
v_add_f32_e32 v14, v14, v57
Mv 68,24,116
v_exp_f32_e32 v58, v120
Mv 68,28,124
Rb 12,1,19456
s_waitcnt lgkmcnt(8)
Mv 64,28,120
v_exp_f32_e32 v26, v121
v_mfma_f32_16x16x32_bf16 v[72:75], a[32:35], a[64:67], v[16:19]
Rb 16,1,24576
v_add_f32_e32 v14, v14, v25
)" R"(
v_mfma_f32_16x16x32_bf16 v[76:79], a[32:35], a[68:71], v[20:23]
v_exp_f32_e32 v59, v122
Mv 76,36,76
Rb 20,1,25600
Cv 57,57,25
Mv 72,36,72
buffer_load_dwordx4 v2, s[20:23], s56 offen offset:2048 sc1 lds
Mv 72,40,80
Rb 24,1,26624
v_add_f32_e32 v14, v14, v58
Mv 76,40,84
v_exp_f32_e32 v27, v123
Mv 76,44,92
Rb 28,1,27648
s_waitcnt lgkmcnt(8)
Mv 72,44,88
v_exp_f32_e32 v60, v116
Mv 72,48,96
Rb 32,1,20480
v_add_f32_e32 v14, v14, v26
Mv 76,48,100
v_exp_f32_e32 v28, v117
Mv 76,52,108
Rb 36,1,21504
Cv 58,58,26
Mv 72,52,104
buffer_load_dwordx4 v2, s[20:23], s56 offen offset:3072 sc1 lds
Mv 72,56,112
Rb 40,1,22528
v_add_f32_e32 v14, v14, v59
Mv 76,56,116
v_exp_f32_e32 v61, v118
Mv 76,60,124
Rb 44,1,23552
s_waitcnt lgkmcnt(8)
Mv 72,60,120
v_exp_f32_e32 v29, v119
v_mfma_f32_16x16x32_bf16 v[80:83], a[0:3], a[64:67], v[16:19]
Rb 48,1,28672
v_add_f32_e32 v14, v14, v27
v_mfma_f32_16x16x32_bf16 v[84:87], a[0:3], a[68:71], v[20:23]
v_exp_f32_e32 v62, v124
Mv 84,4,76
Rb 52,1,29696
s_add_u32 m0, s36, 102400
Mv 80,4,72
buffer_load_dwordx4 v3, s[20:23], s56 offen offset:0 sc1 lds
Mv 80,8,80
Rb 56,1,30720
Cv 59,59,27
Mv 84,8,84
v_exp_f32_e32 v24, v125
Mv 84,12,92
Rb 60,1,31744
s_waitcnt lgkmcnt(8)
Mv 80,12,88
v_exp_f32_e32 v63, v126
Mv 80,16,96
v_exp_f32_e32 v25, v127
Mv 84,16,100
v_add_f32_e32 v15, v15, v60
v_add_f32_e32 v15, v15, v28
Mv 84,20,108
Cv 60,60,28
)" R"(
v_add_f32_e32 v15, v15, v61
Mv 80,20,104
buffer_load_dwordx4 v3, s[20:23], s56 offen offset:1024 sc1 lds
Mv 80,24,112
v_add_f32_e32 v15, v15, v29
Cv 61,61,29
Mv 84,24,116
v_add_f32_e32 v15, v15, v62
v_add_f32_e32 v15, v15, v24
Mv 84,28,124
Cv 62,62,24
v_add_f32_e32 v15, v15, v63
Mv 80,28,120
s_waitcnt vmcnt(14) lgkmcnt(0)
s_barrier
v_mfma_f32_16x16x32_bf16 v[88:91], a[32:35], a[64:67], v[16:19]
Rb 0,8,0
v_add_f32_e32 v15, v15, v25
v_mfma_f32_16x16x32_bf16 v[92:95], a[32:35], a[68:71], v[20:23]
Rb 4,8,1024
Cv 63,63,25
Mv 92,36,76
Rb 8,8,2048
v_max_f32_e32 v11, v14, v15
Mv 88,36,72
Rb 12,8,3072
buffer_load_dwordx4 v3, s[20:23], s56 offen offset:2048 sc1 lds
Mv 88,40,80
Rb 16,8,4096
v_cmp_nge_f32_e32 vcc, s62, v11
Mv 92,40,84
Rb 20,8,5120
s_add_u32 s55, s55, 0x10000
Mv 92,44,92
Rb 24,8,6144
Mv 88,44,88
Rb 28,8,7168
Mv 88,48,96
Rb 32,8,8192
Mv 92,48,100
Rb 36,8,9216
Mv 92,52,108
Rb 40,8,10240
Mv 88,52,104
Rb 44,8,11264
buffer_load_dwordx4 v3, s[20:23], s56 offen offset:3072 sc1 lds
Mv 88,56,112
s_cmp_eq_u32 s57, 2
s_cselect_b32 s55, s59, s55
Mv 92,56,116
s_add_u32 s56, s56, 0x10000
Mv 92,60,124
s_waitcnt lgkmcnt(8)
s_cbranch_vccnz .Lzfx1
.Lzfr1:
Mv 88,60,120
v_add_f32_e32 v12, v12, v14
v_add_f32_e32 v13, v13, v15
Mp 128,0,48
Rb 48,8,12288
Mp 136,0,52
s_cmp_le_i32 s57, s58
s_cbranch_scc1 .Lzmk2
.Lzmr2:
Mp 140,4,52
)" R"(
Rb 52,8,13312
s_add_u32 m0, s36, 0
Mp 132,4,48
buffer_load_dwordx4 v2, s[20:23], s55 offen offset:0 sc1 lds
Mp 144,8,48
Rb 56,8,14336
Mp 152,8,52
v_exp_f32_e32 v32, v64
Mp 156,12,52
Rb 60,8,15360
s_waitcnt lgkmcnt(8)
Mp 148,12,48
v_exp_f32_e32 v26, v65
Mp 160,16,48
Rb 0,8,16384
v_add_f32_e32 v14, v32, v26
Mp 168,16,52
v_exp_f32_e32 v33, v66
Mp 172,20,52
Rb 4,8,17408
Cv 32,32,26
Mp 164,20,48
buffer_load_dwordx4 v2, s[20:23], s55 offen offset:1024 sc1 lds
Mp 176,24,48
Rb 8,8,18432
v_add_f32_e32 v14, v14, v33
Mp 184,24,52
v_exp_f32_e32 v27, v67
Mp 188,28,52
Rb 12,8,19456
s_waitcnt lgkmcnt(8)
Mp 180,28,48
v_exp_f32_e32 v34, v72
Mp 192,32,48
Rb 16,8,20480
v_add_f32_e32 v14, v14, v27
Mp 200,32,52
v_exp_f32_e32 v28, v73
Mp 204,36,52
Rb 20,8,21504
Cv 33,33,27
Mp 196,36,48
buffer_load_dwordx4 v2, s[20:23], s55 offen offset:2048 sc1 lds
Mp 208,40,48
Rb 24,8,22528
v_add_f32_e32 v14, v14, v34
Mp 216,40,52
v_exp_f32_e32 v35, v74
Mp 220,44,52
Rb 28,8,23552
s_waitcnt lgkmcnt(8)
Mp 212,44,48
v_exp_f32_e32 v29, v75
Mp 224,48,48
Rb 32,8,24576
v_add_f32_e32 v14, v14, v28
Mp 232,48,52
v_exp_f32_e32 v36, v68
Mp 236,52,52
Rb 36,8,25600
Cv 34,34,28
Mp 228,52,48
buffer_load_dwordx4 v2, s[20:23], s55 offen offset:3072 sc1 lds
Mp 240,56,48
)" R"(
Rb 40,8,26624
v_add_f32_e32 v14, v14, v35
Mp 248,56,52
v_exp_f32_e32 v24, v69
Mp 252,60,52
Rb 44,8,27648
s_waitcnt lgkmcnt(8)
Mp 244,60,48
v_exp_f32_e32 v37, v70
Mp 128,0,56
Rb 48,8,28672
v_add_f32_e32 v14, v14, v29
Mp 136,0,60
v_exp_f32_e32 v25, v71
Mp 140,4,60
Rb 52,8,29696
s_add_u32 m0, s36, 4096
Mp 132,4,56
buffer_load_dwordx4 v3, s[20:23], s55 offen offset:0 sc1 lds
Mp 144,8,56
Rb 56,8,30720
Cv 35,35,29
Mp 152,8,60
v_exp_f32_e32 v38, v76
Mp 156,12,60
Rb 60,8,31744
s_waitcnt lgkmcnt(8)
Mp 148,12,56
v_exp_f32_e32 v26, v77
Mp 160,16,56
v_exp_f32_e32 v39, v78
Mp 168,16,60
v_exp_f32_e32 v27, v79
Mp 172,20,60
v_add_f32_e32 v15, v36, v24
Cv 36,36,24
Mp 164,20,56
buffer_load_dwordx4 v3, s[20:23], s55 offen offset:1024 sc1 lds
Mp 176,24,56
s_add_u32 s50, s50, 1024
s_min_u32 s50, s50, s60
Mp 184,24,60
v_add_f32_e32 v15, v15, v37
v_add_f32_e32 v15, v15, v25
Mp 188,28,60
Cv 37,37,25
v_add_f32_e32 v15, v15, v38
Mp 180,28,56
s_waitcnt vmcnt(14) lgkmcnt(0)
s_barrier
Mp 192,32,56
Rb 0,1,32768
v_add_f32_e32 v15, v15, v26
Mp 200,32,60
Rb 4,1,33792
Cv 38,38,26
Mp 204,36,60
Rb 8,1,34816
v_add_f32_e32 v15, v15, v39
Mp 196,36,56
Rb 12,1,35840
buffer_load_dwordx4 v3, s[20:23], s55 offen offset:2048 sc1 lds
Mp 208,40,56
Rb 16,1,40960
v_add_f32_e32 v15, v15, v27
)" R"(
Mp 216,40,60
Rb 20,1,41984
Cv 39,39,27
Mp 220,44,60
Rb 24,1,43008
s_sub_u32 s57, s57, 1
Mp 212,44,56
Rb 28,1,44032
Mp 224,48,56
Rb 32,1,36864
Mp 232,48,60
Rb 36,1,37888
Mp 236,52,60
Rb 40,1,38912
Mp 228,52,56
Rb 44,1,39936
buffer_load_dwordx4 v3, s[20:23], s55 offen offset:3072 sc1 lds
Mp 240,56,56
Mp 248,56,60
Mp 252,60,60
s_waitcnt lgkmcnt(8)
Mp 244,60,56
v_mfma_f32_16x16x32_bf16 v[96:99], a[0:3], a[64:67], v[16:19]
Rb 48,1,45056
buffer_load_dword v4, v5, s[20:23], s50 offen offset:0 sc0 sc1
v_mfma_f32_16x16x32_bf16 v[100:103], a[0:3], a[68:71], v[20:23]
v_exp_f32_e32 v40, v80
Mv 100,4,76
Rb 52,1,46080
s_add_u32 m0, s36, 65536
Mv 96,4,72
buffer_load_dwordx4 v2, s[20:23], s56 offen offset:0 sc1 lds
Mv 96,8,80
Rb 56,1,47104
v_add_f32_e32 v14, v14, v40
Mv 100,8,84
v_exp_f32_e32 v28, v81
Mv 100,12,92
Rb 60,1,48128
s_waitcnt lgkmcnt(8)
Mv 96,12,88
v_exp_f32_e32 v41, v82
Mv 96,16,96
Rb 0,1,49152
v_add_f32_e32 v14, v14, v28
Mv 100,16,100
v_exp_f32_e32 v29, v83
Mv 100,20,108
Rb 4,1,50176
Cv 40,40,28
Mv 96,20,104
buffer_load_dwordx4 v2, s[20:23], s56 offen offset:1024 sc1 lds
Mv 96,24,112
Rb 8,1,51200
v_add_f32_e32 v14, v14, v41
Mv 100,24,116
v_exp_f32_e32 v42, v88
Mv 100,28,124
Rb 12,1,52224
s_waitcnt lgkmcnt(8)
Mv 96,28,120
v_exp_f32_e32 v24, v89
v_mfma_f32_16x16x32_bf16 v[104:107], a[32:35], a[64:67], v[16:19]
Rb 16,1,57344
)" R"(
v_add_f32_e32 v14, v14, v29
v_mfma_f32_16x16x32_bf16 v[108:111], a[32:35], a[68:71], v[20:23]
v_exp_f32_e32 v43, v90
Mv 108,36,76
Rb 20,1,58368
Cv 41,41,29
Mv 104,36,72
buffer_load_dwordx4 v2, s[20:23], s56 offen offset:2048 sc1 lds
Mv 104,40,80
Rb 24,1,59392
v_add_f32_e32 v14, v14, v42
Mv 108,40,84
v_exp_f32_e32 v25, v91
Mv 108,44,92
Rb 28,1,60416
s_waitcnt lgkmcnt(8)
Mv 104,44,88
v_exp_f32_e32 v44, v84
Mv 104,48,96
Rb 32,1,53248
v_add_f32_e32 v14, v14, v24
Mv 108,48,100
v_exp_f32_e32 v26, v85
Mv 108,52,108
Rb 36,1,54272
Cv 42,42,24
Mv 104,52,104
buffer_load_dwordx4 v2, s[20:23], s56 offen offset:3072 sc1 lds
Mv 104,56,112
Rb 40,1,55296
v_add_f32_e32 v14, v14, v43
Mv 108,56,116
v_exp_f32_e32 v45, v86
Mv 108,60,124
Rb 44,1,56320
s_waitcnt lgkmcnt(8)
Mv 104,60,120
v_exp_f32_e32 v27, v87
v_mfma_f32_16x16x32_bf16 v[112:115], a[0:3], a[64:67], v[16:19]
Rb 48,1,61440
v_add_f32_e32 v14, v14, v25
v_mfma_f32_16x16x32_bf16 v[116:119], a[0:3], a[68:71], v[20:23]
v_exp_f32_e32 v46, v92
Mv 116,4,76
Rb 52,1,62464
s_add_u32 m0, s36, 69632
Mv 112,4,72
buffer_load_dwordx4 v3, s[20:23], s56 offen offset:0 sc1 lds
Mv 112,8,80
Rb 56,1,63488
Cv 43,43,25
Mv 116,8,84
v_exp_f32_e32 v28, v93
Mv 116,12,92
Rb 60,1,64512
s_waitcnt lgkmcnt(8)
Mv 112,12,88
v_exp_f32_e32 v47, v94
Mv 112,16,96
v_exp_f32_e32 v29, v95
Mv 116,16,100
v_add_f32_e32 v15, v15, v44
v_add_f32_e32 v15, v15, v26
Mv 116,20,108
)" R"(
Cv 44,44,26
v_add_f32_e32 v15, v15, v45
Mv 112,20,104
buffer_load_dwordx4 v3, s[20:23], s56 offen offset:1024 sc1 lds
Mv 112,24,112
v_add_f32_e32 v15, v15, v27
Cv 45,45,27
Mv 116,24,116
v_add_f32_e32 v15, v15, v46
v_add_f32_e32 v15, v15, v28
Mv 116,28,124
Cv 46,46,28
v_add_f32_e32 v15, v15, v47
Mv 112,28,120
s_waitcnt vmcnt(15) lgkmcnt(0)
s_barrier
v_mfma_f32_16x16x32_bf16 v[120:123], a[32:35], a[64:67], v[16:19]
Rb 0,8,32768
v_add_f32_e32 v15, v15, v29
v_mfma_f32_16x16x32_bf16 v[124:127], a[32:35], a[68:71], v[20:23]
Rb 4,8,33792
Cv 47,47,29
Mv 124,36,76
Rb 8,8,34816
v_max_f32_e32 v11, v14, v15
Mv 120,36,72
Rb 12,8,35840
buffer_load_dwordx4 v3, s[20:23], s56 offen offset:2048 sc1 lds
Mv 120,40,80
Rb 16,8,36864
v_cmp_nge_f32_e32 vcc, s62, v11
Mv 124,40,84
Rb 20,8,37888
s_add_u32 s55, s55, 0x10000
Mv 124,44,92
Rb 24,8,38912
Mv 120,44,88
Rb 28,8,39936
Mv 120,48,96
Rb 32,8,40960
Mv 124,48,100
Rb 36,8,41984
Mv 124,52,108
Rb 40,8,43008
Mv 120,52,104
Rb 44,8,44032
buffer_load_dwordx4 v3, s[20:23], s56 offen offset:3072 sc1 lds
Mv 120,56,112
s_cmp_eq_u32 s57, 2
s_cselect_b32 s55, s59, s55
Mv 124,56,116
s_add_u32 s56, s56, 0x10000
Mv 124,60,124
s_waitcnt lgkmcnt(8)
s_cbranch_vccnz .Lzfx2
.Lzfr2:
Mv 120,60,120
v_add_f32_e32 v12, v12, v14
v_add_f32_e32 v13, v13, v15
Mp 128,0,32
Rb 48,8,45056
Mp 136,0,36
s_cmp_le_i32 s57, s58
s_cbranch_scc1 .Lzmk3
.Lzmr3:
)" R"(
Mp 140,4,36
Rb 52,8,46080
s_add_u32 m0, s36, 32768
Mp 132,4,32
buffer_load_dwordx4 v2, s[20:23], s55 offen offset:0 sc1 lds
Mp 144,8,32
Rb 56,8,47104
Mp 152,8,36
v_exp_f32_e32 v48, v96
Mp 156,12,36
Rb 60,8,48128
s_waitcnt lgkmcnt(8)
Mp 148,12,32
v_exp_f32_e32 v24, v97
Mp 160,16,32
Rb 0,8,49152
v_add_f32_e32 v14, v48, v24
Mp 168,16,36
v_exp_f32_e32 v49, v98
Mp 172,20,36
Rb 4,8,50176
Cv 48,48,24
Mp 164,20,32
buffer_load_dwordx4 v2, s[20:23], s55 offen offset:1024 sc1 lds
Mp 176,24,32
Rb 8,8,51200
v_add_f32_e32 v14, v14, v49
Mp 184,24,36
v_exp_f32_e32 v25, v99
Mp 188,28,36
Rb 12,8,52224
s_waitcnt lgkmcnt(8)
Mp 180,28,32
v_exp_f32_e32 v50, v104
Mp 192,32,32
Rb 16,8,53248
v_add_f32_e32 v14, v14, v25
Mp 200,32,36
v_exp_f32_e32 v26, v105
Mp 204,36,36
Rb 20,8,54272
Cv 49,49,25
Mp 196,36,32
buffer_load_dwordx4 v2, s[20:23], s55 offen offset:2048 sc1 lds
Mp 208,40,32
Rb 24,8,55296
v_add_f32_e32 v14, v14, v50
Mp 216,40,36
v_exp_f32_e32 v51, v106
Mp 220,44,36
Rb 28,8,56320
s_waitcnt lgkmcnt(8)
Mp 212,44,32
v_exp_f32_e32 v27, v107
Mp 224,48,32
Rb 32,8,57344
v_add_f32_e32 v14, v14, v26
Mp 232,48,36
v_exp_f32_e32 v52, v100
Mp 236,52,36
Rb 36,8,58368
Cv 50,50,26
Mp 228,52,32
buffer_load_dwordx4 v2, s[20:23], s55 offen offset:3072 sc1 lds
)" R"(
Mp 240,56,32
Rb 40,8,59392
v_add_f32_e32 v14, v14, v51
Mp 248,56,36
v_exp_f32_e32 v28, v101
Mp 252,60,36
Rb 44,8,60416
s_waitcnt lgkmcnt(8)
Mp 244,60,32
v_exp_f32_e32 v53, v102
Mp 128,0,40
Rb 48,8,61440
v_add_f32_e32 v14, v14, v27
Mp 136,0,44
v_exp_f32_e32 v29, v103
Mp 140,4,44
Rb 52,8,62464
s_add_u32 m0, s36, 36864
Mp 132,4,40
buffer_load_dwordx4 v3, s[20:23], s55 offen offset:0 sc1 lds
Mp 144,8,40
Rb 56,8,63488
Cv 51,51,27
Mp 152,8,44
v_exp_f32_e32 v54, v108
Mp 156,12,44
Rb 60,8,64512
s_waitcnt lgkmcnt(8)
Mp 148,12,40
v_exp_f32_e32 v24, v109
Mp 160,16,40
v_exp_f32_e32 v55, v110
Mp 168,16,44
v_exp_f32_e32 v25, v111
Mp 172,20,44
v_add_f32_e32 v15, v52, v28
Cv 52,52,28
Mp 164,20,40
buffer_load_dwordx4 v3, s[20:23], s55 offen offset:1024 sc1 lds
Mp 176,24,40
v_add_f32_e32 v15, v15, v53
v_add_f32_e32 v15, v15, v29
Mp 184,24,44
Cv 53,53,29
v_add_f32_e32 v15, v15, v54
Mp 188,28,44
v_add_f32_e32 v15, v15, v24
Cv 54,54,24
Mp 180,28,40
s_waitcnt vmcnt(14) lgkmcnt(0)
s_barrier
Mp 192,32,40
Rb 0,1,0
v_add_f32_e32 v15, v15, v55
Mp 200,32,44
Rb 4,1,1024
v_add_f32_e32 v15, v15, v25
Mp 204,36,44
Rb 8,1,2048
Cv 55,55,25
Mp 196,36,40
Rb 12,1,3072
buffer_load_dwordx4 v3, s[20:23], s55 offen offset:2048 sc1 lds
Mp 208,40,40
Rb 16,1,8192
)" R"(
s_sub_u32 s57, s57, 1
Mp 216,40,44
Rb 20,1,9216
Mp 220,44,44
Rb 24,1,10240
Mp 212,44,40
Rb 28,1,11264
Mp 224,48,40
Rb 32,1,4096
Mp 232,48,44
Rb 36,1,5120
Mp 236,52,44
Rb 40,1,6144
Mp 228,52,40
Rb 44,1,7168
buffer_load_dwordx4 v3, s[20:23], s55 offen offset:3072 sc1 lds
Mp 240,56,40
v_cmp_eq_u32_e32 vcc, s3, v4
s_andn2_b64 s[28:29], -1, vcc
s_cbranch_scc1 .Lzsp9
.Lzok9:
Mp 248,56,44
Mp 252,60,44
s_waitcnt lgkmcnt(8)
Mp 244,60,40
s_cmp_gt_i32 s57, s93
s_cbranch_scc1 .LzL
s_cmp_eq_u32 s94, 1
s_cbranch_scc1 .LzW
s_cmp_eq_u32 s94, 2
s_cbranch_scc1 .LzDA
.LzEB:
v_mfma_f32_16x16x32_bf16 v[64:67], a[0:3], a[64:67], v[16:19]
Rb 48,1,12288
s_mov_b32 s83, 0xc108000
buffer_load_dword v4, v7, s[20:23], s83 offen offset:4 sc0 sc1
v_mfma_f32_16x16x32_bf16 v[68:71], a[0:3], a[68:71], v[20:23]
s_cmp_lg_u32 s35, 7
s_cselect_b64 s[60:61], 1, 0
Mv 68,4,76
Rb 52,1,13312
s_add_u32 m0, s36, 98304
Mv 64,4,72
buffer_load_dwordx4 v2, s[20:23], s56 offen offset:0 sc1 lds
Mv 64,8,80
Rb 56,1,14336
Mv 68,8,84
v_exp_f32_e32 v56, v112
Mv 68,12,92
Rb 60,1,15360
s_waitcnt lgkmcnt(8)
Mv 64,12,88
v_exp_f32_e32 v28, v113
Mv 64,16,96
Rb 0,1,16384
v_add_f32_e32 v14, v14, v56
Mv 68,16,100
v_exp_f32_e32 v57, v114
Mv 68,20,108
Rb 4,1,17408
v_add_f32_e32 v14, v14, v28
Mv 64,20,104
buffer_load_dwordx4 v2, s[20:23], s56 offen offset:1024 sc1 lds
Mv 64,24,112
)" R"(
Rb 8,1,18432
Cv 56,56,28
Mv 68,24,116
v_exp_f32_e32 v29, v115
Mv 68,28,124
Rb 12,1,19456
s_waitcnt lgkmcnt(8)
Mv 64,28,120
v_exp_f32_e32 v58, v120
v_mfma_f32_16x16x32_bf16 v[72:75], a[32:35], a[64:67], v[16:19]
Rb 16,1,24576
v_add_f32_e32 v14, v14, v57
v_mfma_f32_16x16x32_bf16 v[76:79], a[32:35], a[68:71], v[20:23]
v_exp_f32_e32 v24, v121
Mv 76,36,76
Rb 20,1,25600
v_add_f32_e32 v14, v14, v29
Mv 72,36,72
buffer_load_dwordx4 v2, s[20:23], s56 offen offset:2048 sc1 lds
Mv 72,40,80
Rb 24,1,26624
Cv 57,57,29
Mv 76,40,84
v_exp_f32_e32 v59, v122
Mv 76,44,92
Rb 28,1,27648
s_waitcnt lgkmcnt(8)
Mv 72,44,88
v_exp_f32_e32 v25, v123
Mv 72,48,96
Rb 32,1,20480
v_add_f32_e32 v14, v14, v58
Mv 76,48,100
v_exp_f32_e32 v60, v116
Mv 76,52,108
Rb 36,1,21504
v_add_f32_e32 v14, v14, v24
Mv 72,52,104
buffer_load_dwordx4 v2, s[20:23], s56 offen offset:3072 sc1 lds
Mv 72,56,112
Rb 40,1,22528
Cv 58,58,24
Mv 76,56,116
v_exp_f32_e32 v26, v117
Mv 76,60,124
Rb 44,1,23552
s_waitcnt lgkmcnt(8)
Mv 72,60,120
v_exp_f32_e32 v61, v118
v_mfma_f32_16x16x32_bf16 v[80:83], a[0:3], a[64:67], v[16:19]
Rb 48,1,28672
v_add_f32_e32 v14, v14, v59
v_mfma_f32_16x16x32_bf16 v[84:87], a[0:3], a[68:71], v[20:23]
v_exp_f32_e32 v27, v119
Mv 84,4,76
Rb 52,1,29696
s_add_u32 m0, s36, 102400
Mv 80,4,72
buffer_load_dwordx4 v3, s[20:23], s56 offen offset:0 sc1 lds
Mv 80,8,80
Rb 56,1,30720
v_add_f32_e32 v14, v14, v25
Mv 84,8,84
v_exp_f32_e32 v62, v124
)" R"(
Mv 84,12,92
Rb 60,1,31744
s_waitcnt lgkmcnt(8)
Mv 80,12,88
v_exp_f32_e32 v28, v125
Mv 80,16,96
v_exp_f32_e32 v63, v126
Mv 84,16,100
v_exp_f32_e32 v29, v127
Mv 84,20,108
Cv 59,59,25
v_add_f32_e32 v15, v15, v60
Mv 80,20,104
buffer_load_dwordx4 v3, s[20:23], s56 offen offset:1024 sc1 lds
Mv 80,24,112
v_add_f32_e32 v15, v15, v26
Cv 60,60,26
Mv 84,24,116
v_add_f32_e32 v15, v15, v61
v_add_f32_e32 v15, v15, v27
Mv 84,28,124
Cv 61,61,27
v_add_f32_e32 v15, v15, v62
Mv 80,28,120
s_waitcnt vmcnt(15) lgkmcnt(0)
s_barrier
v_mfma_f32_16x16x32_bf16 v[88:91], a[32:35], a[64:67], v[16:19]
Rb 0,8,0
v_add_f32_e32 v15, v15, v28
v_mfma_f32_16x16x32_bf16 v[92:95], a[32:35], a[68:71], v[20:23]
Rb 4,8,1024
Cv 62,62,28
Mv 92,36,76
Rb 8,8,2048
v_add_f32_e32 v15, v15, v63
Mv 88,36,72
Rb 12,8,3072
buffer_load_dwordx4 v3, s[20:23], s56 offen offset:2048 sc1 lds
Mv 88,40,80
Rb 16,8,4096
v_add_f32_e32 v15, v15, v29
Mv 92,40,84
Rb 20,8,5120
Cv 63,63,29
Mv 92,44,92
Rb 24,8,6144
v_max_f32_e32 v11, v14, v15
Mv 88,44,88
Rb 28,8,7168
v_cmp_nge_f32_e32 vcc, s62, v11
Mv 88,48,96
Rb 32,8,8192
s_add_u32 s55, s55, 0x10000
Mv 92,48,100
Rb 36,8,9216
Mv 92,52,108
Rb 40,8,10240
Mv 88,52,104
Rb 44,8,11264
buffer_load_dwordx4 v3, s[20:23], s56 offen offset:3072 sc1 lds
Mv 88,56,112
s_cmp_eq_u32 s57, 2
s_cselect_b32 s55, s59, s55
Mv 92,56,116
s_add_u32 s56, s56, 0x10000
)" R"(
Mv 92,60,124
s_waitcnt lgkmcnt(8)
s_cbranch_vccnz .Lzfx11
.Lzfr11:
Mv 88,60,120
s_waitcnt vmcnt(8)
v_cmp_eq_u32_e32 vcc, s3, v4
s_andn2_b64 s[28:29], 1, vcc
s_cbranch_scc1 .Lzsp21
.Lzok21:
Mp 128,0,48
Rb 48,8,12288
buffer_load_dword v9, v7, s[20:23], s83 offen sc0 sc1
Mp 136,0,52
v_add_f32_e32 v12, v12, v14
s_cmp_le_i32 s57, s58
s_cbranch_scc1 .Lzmk10
.Lzmr10:
Mp 140,4,52
Rb 52,8,13312
s_add_u32 m0, s36, 0
v_add_f32_e32 v13, v13, v15
Mp 132,4,48
buffer_load_dwordx4 v2, s[20:23], s55 offen offset:0 sc1 lds
Mp 144,8,48
Rb 56,8,14336
Mp 152,8,52
v_exp_f32_e32 v32, v64
Mp 156,12,52
Rb 60,8,15360
s_waitcnt lgkmcnt(8)
Mp 148,12,48
v_exp_f32_e32 v24, v65
Mp 160,16,48
Rb 0,8,16384
v_add_f32_e32 v14, v32, v24
Mp 168,16,52
v_exp_f32_e32 v33, v66
Cv 32,32,24
Mp 172,20,52
Rb 4,8,17408
v_add_f32_e32 v14, v14, v33
Mp 164,20,48
buffer_load_dwordx4 v2, s[20:23], s55 offen offset:1024 sc1 lds
Mp 176,24,48
Rb 8,8,18432
Mp 184,24,52
v_exp_f32_e32 v25, v67
Mp 188,28,52
Rb 12,8,19456
s_waitcnt lgkmcnt(8)
Mp 180,28,48
v_exp_f32_e32 v34, v72
Mp 192,32,48
Rb 16,8,20480
v_add_f32_e32 v14, v14, v25
Mp 200,32,52
v_exp_f32_e32 v26, v73
Cv 33,33,25
Mp 204,36,52
Rb 20,8,21504
v_add_f32_e32 v14, v14, v34
Mp 196,36,48
buffer_load_dwordx4 v2, s[20:23], s55 offen offset:2048 sc1 lds
)" R"(
Mp 208,40,48
Rb 24,8,22528
v_add_f32_e32 v14, v14, v26
Mp 216,40,52
v_exp_f32_e32 v35, v74
Mp 220,44,52
Rb 28,8,23552
s_waitcnt lgkmcnt(8)
Mp 212,44,48
v_exp_f32_e32 v27, v75
Mp 224,48,48
Rb 32,8,24576
Cv 34,34,26
Mp 232,48,52
v_exp_f32_e32 v36, v68
v_add_f32_e32 v14, v14, v35
Mp 236,52,52
Rb 36,8,25600
v_add_f32_e32 v14, v14, v27
Mp 228,52,48
buffer_load_dwordx4 v2, s[20:23], s55 offen offset:3072 sc1 lds
Mp 240,56,48
Rb 40,8,26624
Cv 35,35,27
Mp 248,56,52
v_exp_f32_e32 v28, v69
Mp 252,60,52
Rb 44,8,27648
s_waitcnt lgkmcnt(8)
Mp 244,60,48
v_exp_f32_e32 v37, v70
Mp 128,0,56
Rb 48,8,28672
v_add_f32_e32 v15, v36, v28
Mp 136,0,60
v_exp_f32_e32 v29, v71
Cv 36,36,28
Mp 140,4,60
Rb 52,8,29696
s_add_u32 m0, s36, 4096
Mp 132,4,56
buffer_load_dwordx4 v3, s[20:23], s55 offen offset:0 sc1 lds
Mp 144,8,56
Rb 56,8,30720
v_add_f32_e32 v15, v15, v37
Mp 152,8,60
v_exp_f32_e32 v38, v76
Mp 156,12,60
Rb 60,8,31744
s_waitcnt lgkmcnt(8)
Mp 148,12,56
v_exp_f32_e32 v24, v77
Mp 160,16,56
v_exp_f32_e32 v39, v78
Mp 168,16,60
v_exp_f32_e32 v25, v79
v_add_f32_e32 v15, v15, v29
Mp 172,20,60
Cv 37,37,29
v_add_f32_e32 v15, v15, v38
Mp 164,20,56
buffer_load_dwordx4 v3, s[20:23], s55 offen offset:1024 sc1 lds
Mp 176,24,56
v_add_f32_e32 v15, v15, v24
)" R"(
Cv 38,38,24
Mp 184,24,60
v_add_f32_e32 v15, v15, v39
v_add_f32_e32 v15, v15, v25
Mp 188,28,60
Cv 39,39,25
s_sub_u32 s57, s57, 1
Mp 180,28,56
s_waitcnt lgkmcnt(0)
s_barrier
Mp 192,32,56
Rb 0,1,32768
v_bfe_u32 v11, v0, 2, 2
Mp 200,32,60
Rb 4,1,33792
v_lshrrev_b32 v4, 4, v0
v_xor_b32 v11, v11, v4
Mp 204,36,60
Rb 8,1,34816
v_bfe_u32 v4, v0, 1, 1
Mp 196,36,56
Rb 12,1,35840
buffer_load_dwordx4 v3, s[20:23], s55 offen offset:2048 sc1 lds
Mp 208,40,56
Rb 16,1,40960
v_lshl_or_b32 v11, v11, 1, v4
Mp 216,40,60
Rb 20,1,41984
v_and_b32 v4, 1, v0
Mp 220,44,60
Rb 24,1,43008
v_mul_u32_u24 v4, 0xffff, v4
Mp 212,44,56
Rb 28,1,44032
v_add_u32 v4, 1, v4
Mp 224,48,56
Rb 32,1,36864
v_mul_u32_u24 v4, 0x3f80, v4
Mp 232,48,60
Rb 36,1,37888
v_cmp_eq_u32_e32 vcc, 0, v11
v_cmp_eq_u32_e64 s[28:29], 1, v11
Mp 236,52,60
Rb 40,1,38912
v_accvgpr_write_b32 a192, 0
Mp 228,52,56
Rb 44,1,39936
buffer_load_dwordx4 v3, s[20:23], s55 offen offset:3072 sc1 lds
Mp 240,56,56
v_accvgpr_write_b32 a193, 0
v_accvgpr_write_b32 a196, 0
Mp 248,56,60
v_accvgpr_write_b32 a197, 0
v_cndmask_b32_e32 v11, 0, v4, vcc
Mp 252,60,60
v_cndmask_b32_e64 v4, 0, v4, s[28:29]
s_waitcnt lgkmcnt(8)
Mp 244,60,56
v_accvgpr_write_b32 a194, v11
v_accvgpr_write_b32 a195, v4
v_mfma_f32_16x16x32_bf16 v[96:99], a[0:3], a[64:67], v[16:19]
Rb 48,1,45056
v_mfma_f32_16x16x32_bf16 v[100:103], a[0:3], a[68:71], v[20:23]
buffer_load_dword v4, v7, s[20:23], s65 offen offset:0 sc0 sc1
Mv 100,4,76
)" R"(
Rb 52,1,46080
s_add_u32 m0, s36, 65536
Mv 96,4,72
buffer_load_dwordx4 v2, s[20:23], s56 offen offset:0 sc1 lds
Mv 96,8,80
Rb 56,1,47104
Mv 100,8,84
v_exp_f32_e32 v40, v80
Mv 100,12,92
Rb 60,1,48128
s_waitcnt lgkmcnt(8)
Mv 96,12,88
v_exp_f32_e32 v26, v81
v_add_f32_e32 v14, v14, v40
Mv 96,16,96
Rb 0,1,49152
v_add_f32_e32 v14, v14, v26
Mv 100,16,100
v_exp_f32_e32 v41, v82
Mv 100,20,108
Rb 4,1,50176
Cv 40,40,26
Mv 96,20,104
buffer_load_dwordx4 v2, s[20:23], s56 offen offset:1024 sc1 lds
Mv 96,24,112
Rb 8,1,51200
v_add_f32_e32 v14, v14, v41
Mv 100,24,116
v_exp_f32_e32 v27, v83
Mv 100,28,124
Rb 12,1,52224
s_waitcnt lgkmcnt(8)
Mv 96,28,120
v_exp_f32_e32 v42, v88
v_add_f32_e32 v14, v14, v27
v_mfma_f32_16x16x32_bf16 v[104:107], a[32:35], a[64:67], v[16:19]
Rb 16,1,57344
Cv 41,41,27
v_mfma_f32_16x16x32_bf16 v[108:111], a[32:35], a[68:71], v[20:23]
v_exp_f32_e32 v28, v89
Mv 108,36,76
Rb 20,1,58368
v_add_f32_e32 v14, v14, v42
Mv 104,36,72
buffer_load_dwordx4 v2, s[20:23], s56 offen offset:2048 sc1 lds
Mv 104,40,80
Rb 24,1,59392
v_add_f32_e32 v14, v14, v28
Mv 108,40,84
v_exp_f32_e32 v43, v90
Mv 108,44,92
Rb 28,1,60416
s_waitcnt lgkmcnt(8)
Mv 104,44,88
v_exp_f32_e32 v29, v91
Cv 42,42,28
Mv 104,48,96
Rb 32,1,53248
v_add_f32_e32 v14, v14, v43
Mv 108,48,100
v_exp_f32_e32 v44, v84
Mv 108,52,108
Rb 36,1,54272
v_add_f32_e32 v14, v14, v29
)" R"(
Mv 104,52,104
buffer_load_dwordx4 v2, s[20:23], s56 offen offset:3072 sc1 lds
Mv 104,56,112
Rb 40,1,55296
Cv 43,43,29
Mv 108,56,116
v_exp_f32_e32 v24, v85
Mv 108,60,124
Rb 44,1,56320
s_waitcnt lgkmcnt(8)
Mv 104,60,120
v_exp_f32_e32 v45, v86
v_add_f32_e32 v15, v15, v44
v_mfma_f32_16x16x32_bf16 v[112:115], a[0:3], a[64:67], v[16:19]
Rb 48,1,61440
v_add_f32_e32 v15, v15, v24
v_mfma_f32_16x16x32_bf16 v[116:119], a[0:3], a[68:71], v[20:23]
v_exp_f32_e32 v25, v87
Mv 116,4,76
Rb 52,1,62464
s_add_u32 m0, s36, 69632
Mv 112,4,72
buffer_load_dwordx4 v3, s[20:23], s56 offen offset:0 sc1 lds
Mv 112,8,80
Rb 56,1,63488
Cv 44,44,24
Mv 116,8,84
v_exp_f32_e32 v46, v92
Mv 116,12,92
Rb 60,1,64512
s_waitcnt lgkmcnt(8)
Mv 112,12,88
v_exp_f32_e32 v26, v93
v_add_f32_e32 v15, v15, v45
Mv 112,16,96
v_exp_f32_e32 v47, v94
Mv 116,16,100
v_exp_f32_e32 v27, v95
Mv 116,20,108
v_add_f32_e32 v15, v15, v25
Cv 45,45,25
Mv 112,20,104
buffer_load_dwordx4 v3, s[20:23], s56 offen offset:1024 sc1 lds
Mv 112,24,112
v_add_f32_e32 v15, v15, v46
v_add_f32_e32 v15, v15, v26
Mv 116,24,116
Cv 46,46,26
v_add_f32_e32 v15, v15, v47
Mv 116,28,124
v_add_f32_e32 v15, v15, v27
Cv 47,47,27
Mv 112,28,120
v_max_f32_e32 v11, v14, v15
s_waitcnt vmcnt(6) lgkmcnt(0)
s_barrier
v_mfma_f32_16x16x32_bf16 v[120:123], a[32:35], a[64:67], v[16:19]
Rb 0,8,32768
v_cmp_nge_f32_e32 vcc, s62, v11
v_mfma_f32_16x16x32_bf16 v[124:127], a[32:35], a[68:71], v[20:23]
Rb 4,8,33792
s_add_u32 s55, s55, 0x10000
Mv 124,36,76
Rb 8,8,34816
Mv 120,36,72
)" R"(
Rb 12,8,35840
buffer_load_dwordx4 v3, s[20:23], s56 offen offset:2048 sc1 lds
Mv 120,40,80
Rb 16,8,36864
Mv 124,40,84
Rb 20,8,37888
Mv 124,44,92
Rb 24,8,38912
Mv 120,44,88
Rb 28,8,39936
s_cmp_eq_u32 s57, 2
s_cselect_b32 s55, s59, s55
Mv 120,48,96
Rb 32,8,40960
Mv 124,48,100
Rb 36,8,41984
Mv 124,52,108
Rb 40,8,43008
Mv 120,52,104
Rb 44,8,44032
buffer_load_dwordx4 v3, s[20:23], s56 offen offset:3072 sc1 lds
Mv 120,56,112
v_readfirstlane_b32 s63, v9
Mv 124,56,116
s_add_u32 s56, s56, 0x10000
Mv 124,60,124
s_waitcnt lgkmcnt(8)
s_cbranch_vccnz .Lzfx12
.Lzfr12:
Mv 120,60,120
v_cmp_eq_u32_e32 vcc, s3, v4
s_andn2_b64 s[28:29], s[60:61], vcc
s_cbranch_scc1 .Lzsp22
.Lzok22:
Mp 128,0,32
Rb 48,8,45056
v_add_f32_e32 v12, v12, v14
Mp 136,0,36
v_add_f32_e32 v13, v13, v15
s_cmp_le_i32 s57, s58
s_cbranch_scc1 .Lzmk11
.Lzmr11:
Mp 140,4,36
Rb 52,8,46080
s_add_u32 m0, s36, 32768
buffer_load_dwordx4 v[64:67], v1, s[20:23], s66 offen sc0 sc1
Mp 132,4,32
buffer_load_dwordx4 v2, s[20:23], s55 offen offset:0 sc1 lds
Mp 144,8,32
Rb 56,8,47104
Mp 152,8,36
buffer_load_dwordx4 a[128:131], v2, s[20:23], s64 offen offset:0 sc0 sc1
Mp 156,12,36
Rb 60,8,48128
s_waitcnt lgkmcnt(8)
Mp 148,12,32
buffer_load_dwordx4 a[132:135], v2, s[20:23], s64 offen offset:1024 sc0 sc1
Mp 160,16,32
Rb 0,8,49152
Mp 168,16,36
buffer_load_dwordx4 a[136:139], v2, s[20:23], s64 offen offset:2048 sc0 sc1
Mp 172,20,36
Rb 4,8,50176
v_exp_f32_e32 v48, v96
)" R"(
Mp 164,20,32
buffer_load_dwordx4 v2, s[20:23], s55 offen offset:1024 sc1 lds
Mp 176,24,32
Rb 8,8,51200
v_exp_f32_e32 v28, v97
Mp 184,24,36
buffer_load_dwordx4 a[140:143], v2, s[20:23], s64 offen offset:3072 sc0 sc1
Mp 188,28,36
Rb 12,8,52224
v_add_f32_e32 v14, v48, v28
s_waitcnt lgkmcnt(8)
Mp 180,28,32
buffer_load_dwordx4 a[144:147], v3, s[20:23], s64 offen offset:0 sc0 sc1
Mp 192,32,32
Rb 16,8,53248
v_exp_f32_e32 v49, v98
Mp 200,32,36
buffer_load_dwordx4 a[148:151], v3, s[20:23], s64 offen offset:1024 sc0 sc1
Cv 48,48,28
Mp 204,36,36
Rb 20,8,54272
v_add_f32_e32 v14, v14, v49
Mp 196,36,32
buffer_load_dwordx4 v2, s[20:23], s55 offen offset:2048 sc1 lds
Mp 208,40,32
Rb 24,8,55296
Mp 216,40,36
buffer_load_dwordx4 a[152:155], v3, s[20:23], s64 offen offset:2048 sc0 sc1
Mp 220,44,36
Rb 28,8,56320
s_waitcnt lgkmcnt(8)
Mp 212,44,32
buffer_load_dwordx4 a[156:159], v3, s[20:23], s64 offen offset:3072 sc0 sc1
Mp 224,48,32
Rb 32,8,57344
Mp 232,48,36
s_add_u32 s64, s64, 0x10000
buffer_load_dwordx4 a[160:163], v2, s[20:23], s64 offen offset:0 sc0 sc1
Mp 236,52,36
Rb 36,8,58368
v_exp_f32_e32 v29, v99
Mp 228,52,32
buffer_load_dwordx4 v2, s[20:23], s55 offen offset:3072 sc1 lds
Mp 240,56,32
Rb 40,8,59392
v_exp_f32_e32 v50, v104
Mp 248,56,36
buffer_load_dwordx4 a[164:167], v2, s[20:23], s64 offen offset:1024 sc0 sc1
Mp 252,60,36
Rb 44,8,60416
v_add_f32_e32 v14, v14, v29
s_waitcnt lgkmcnt(8)
Mp 244,60,32
buffer_load_dwordx4 a[168:171], v2, s[20:23], s64 offen offset:2048 sc0 sc1
Mp 128,0,40
Rb 48,8,61440
v_exp_f32_e32 v24, v105
Mp 136,0,44
buffer_load_dwordx4 a[172:175], v2, s[20:23], s64 offen offset:3072 sc0 sc1
Cv 49,49,29
Mp 140,4,44
Rb 52,8,62464
s_add_u32 m0, s36, 36864
Mp 132,4,40
)" R"(
buffer_load_dwordx4 v3, s[20:23], s55 offen offset:0 sc1 lds
v_add_f32_e32 v14, v14, v50
Mp 144,8,40
Rb 56,8,63488
v_add_f32_e32 v14, v14, v24
Mp 152,8,44
buffer_load_dwordx4 a[176:179], v3, s[20:23], s64 offen offset:0 sc0 sc1
Cv 50,50,24
Mp 156,12,44
Rb 60,8,64512
s_waitcnt lgkmcnt(8)
Mp 148,12,40
buffer_load_dwordx4 a[180:183], v3, s[20:23], s64 offen offset:1024 sc0 sc1
Mp 160,16,40
v_exp_f32_e32 v51, v106
Mp 168,16,44
buffer_load_dwordx4 a[184:187], v3, s[20:23], s64 offen offset:2048 sc0 sc1
v_add_f32_e32 v14, v14, v51
Mp 172,20,44
v_exp_f32_e32 v25, v107
s_sub_u32 s57, s57, 1
Mp 164,20,40
buffer_load_dwordx4 v3, s[20:23], s55 offen offset:1024 sc1 lds
Mp 176,24,40
v_exp_f32_e32 v52, v100
v_add_f32_e32 v14, v14, v25
Mp 184,24,44
buffer_load_dwordx4 a[188:191], v3, s[20:23], s64 offen offset:3072 sc0 sc1
Mp 188,28,44
v_exp_f32_e32 v26, v101
Cv 51,51,25
Mp 180,28,40
s_waitcnt vmcnt(23) lgkmcnt(0)
s_barrier
Mp 192,32,40
Rb 0,8,0
v_exp_f32_e32 v53, v102
Mp 200,32,44
Rb 4,8,16384
v_exp_f32_e32 v27, v103
Mp 204,36,44
Rb 8,8,1024
v_add_f32_e32 v15, v52, v26
Mp 196,36,40
Rb 12,8,17408
buffer_load_dwordx4 v3, s[20:23], s55 offen offset:2048 sc1 lds
Mp 208,40,40
Rb 16,8,2048
Cv 52,52,26
Mp 216,40,44
Rb 20,8,18432
v_exp_f32_e32 v54, v108
Mp 220,44,44
Rb 24,8,3072
v_add_f32_e32 v15, v15, v53
Mp 212,44,40
Rb 28,8,19456
v_exp_f32_e32 v28, v109
Mp 224,48,40
Rb 32,8,4096
v_add_f32_e32 v15, v15, v27
Mp 232,48,44
Rb 36,8,20480
v_exp_f32_e32 v55, v110
Mp 236,52,44
)" R"(
Rb 40,8,5120
v_exp_f32_e32 v29, v111
Mp 228,52,40
Rb 44,8,21504
buffer_load_dwordx4 v3, s[20:23], s55 offen offset:3072 sc1 lds
Mp 240,56,40
Cv 53,53,27
v_add_f32_e32 v15, v15, v54
v_add_f32_e32 v15, v15, v28
Mp 248,56,44
Cv 54,54,28
v_add_f32_e32 v15, v15, v55
Mp 252,60,44
v_add_f32_e32 v15, v15, v29
Cv 55,55,29
Mp 244,60,40
s_waitcnt vmcnt(18)
v_cmp_neq_f32_e32 vcc, v16, v66
v_cmp_neq_f32_e64 s[28:29], v20, v67
s_or_b64 vcc, vcc, s[28:29]
s_cbranch_scc1 .LzMRG
.LzMRR:
Mv 128,194,128
v_exp_f32_e32 v56, v112
Mv 136,194,132
v_exp_f32_e32 v26, v113
v_add_f32_e32 v12, v12, v64
Mv 132,192,128
s_add_u32 m0, s36, 131072
s_add_u32 s40, s67, 0x20000
v_add_f32_e32 v13, v13, v65
Mv 140,192,132
buffer_load_dwordx4 v2, s[20:23], s40 offen offset:0 sc1 lds
Mv 144,194,136
v_exp_f32_e32 v57, v114
v_add_f32_e32 v14, v14, v56
Mv 152,194,140
v_exp_f32_e32 v27, v115
v_add_f32_e32 v14, v14, v26
Mv 148,192,136
Cv 56,56,26
s_waitcnt vmcnt(14)
Mv 156,192,140
buffer_load_dwordx4 v2, s[20:23], s40 offen offset:1024 sc1 lds
v_add_f32_e32 v14, v14, v57
Mv 160,194,144
v_exp_f32_e32 v58, v120
v_add_f32_e32 v14, v14, v27
Mv 168,194,148
v_exp_f32_e32 v28, v121
Mv 164,192,144
v_exp_f32_e32 v59, v122
Cv 57,57,27
Mv 172,192,148
buffer_load_dwordx4 v2, s[20:23], s40 offen offset:2048 sc1 lds
v_add_f32_e32 v14, v14, v58
Mv 176,194,152
v_exp_f32_e32 v29, v123
Mv 184,194,156
v_exp_f32_e32 v60, v116
v_add_f32_e32 v14, v14, v28
Mv 180,192,152
v_exp_f32_e32 v24, v117
s_waitcnt vmcnt(11)
)" R"(
Mv 188,192,156
buffer_load_dwordx4 v2, s[20:23], s40 offen offset:3072 sc1 lds
Cv 58,58,28
Mv 192,194,160
v_exp_f32_e32 v61, v118
Mv 200,194,164
v_exp_f32_e32 v25, v119
v_add_f32_e32 v14, v14, v59
Mv 196,192,160
s_add_u32 m0, s36, 135168
v_exp_f32_e32 v62, v124
Mv 204,192,164
buffer_load_dwordx4 v3, s[20:23], s40 offen offset:0 sc1 lds
Mv 208,194,168
v_exp_f32_e32 v26, v125
v_add_f32_e32 v14, v14, v29
Mv 216,194,172
v_exp_f32_e32 v63, v126
Cv 59,59,29
Mv 212,192,168
v_add_f32_e32 v15, v15, v60
s_waitcnt vmcnt(7)
Mv 220,192,172
buffer_load_dwordx4 v3, s[20:23], s40 offen offset:1024 sc1 lds
v_add_f32_e32 v15, v15, v24
Mv 224,194,176
v_exp_f32_e32 v27, v127
Cv 60,60,24
Mv 232,194,180
v_add_f32_e32 v15, v15, v61
v_add_f32_e32 v15, v15, v25
Mv 228,192,176
Cv 61,61,25
v_add_f32_e32 v15, v15, v62
v_add_f32_e32 v15, v15, v26
Mv 236,192,180
buffer_load_dwordx4 v3, s[20:23], s40 offen offset:2048 sc1 lds
Cv 62,62,26
Mv 240,194,184
v_add_f32_e32 v15, v15, v63
v_add_f32_e32 v15, v15, v27
Mv 248,194,188
s_add_u32 s40, s67, 0x30000
Cv 63,63,27
v_max_f32_e32 v11, v14, v15
Mv 244,192,184
s_add_u32 m0, s36, 98304
v_cmp_nge_f32_e32 vcc, s62, v11
s_waitcnt lgkmcnt(8)
Mv 252,192,188
buffer_load_dwordx4 v2, s[20:23], s40 offen offset:0 sc1 lds
s_cbranch_vccnz .Lzfx13
.Lzfr13:
Mp 128,0,48
Rb 48,8,6144
v_add_f32_e32 v12, v12, v14
Mp 136,0,52
v_add_f32_e32 v13, v13, v15
v_mov_b32 v24, v12
v_mov_b32 v25, v13
Mp 136,4,60
Rb 52,8,22528
v_permlane32_swap_b32_e32 v12, v24
v_add_f32_e32 v12, v12, v24
)" R"(
Mp 128,4,56
buffer_load_dwordx4 v2, s[20:23], s40 offen offset:1024 sc1 lds
v_mov_b32 v24, v12
Mp 132,8,48
Rb 56,8,7168
v_permlane16_swap_b32_e32 v12, v24
Mp 140,8,52
v_add_f32_e32 v12, v12, v24
v_rcp_f32_e32 v12, v12
Mp 140,12,60
Rb 60,8,23552
v_permlane32_swap_b32_e32 v13, v25
s_waitcnt lgkmcnt(8)
Mp 132,12,56
buffer_load_dwordx4 v2, s[20:23], s40 offen offset:2048 sc1 lds
v_add_f32_e32 v13, v13, v25
Mp 144,16,48
Rb 0,8,8192
v_mov_b32 v25, v13
Mp 152,16,52
s_nop 0
v_permlane16_swap_b32_e32 v13, v25
v_add_f32_e32 v13, v13, v25
Mp 152,20,60
Rb 4,8,24576
v_rcp_f32_e32 v13, v13
Mp 144,20,56
buffer_load_dwordx4 v2, s[20:23], s40 offen offset:3072 sc1 lds
v_mul_f32_e32 v128, v12, v128
Mp 148,24,48
Rb 8,8,9216
v_mul_f32_e32 v129, v12, v129
Mp 156,24,52
s_add_u32 m0, s36, 102400
Cv 128,128,129
v_mul_f32_e32 v130, v12, v130
Mp 156,28,60
Rb 12,8,25600
v_mul_f32_e32 v131, v12, v131
s_waitcnt lgkmcnt(8)
Mp 148,28,56
buffer_load_dwordx4 v3, s[20:23], s40 offen offset:0 sc1 lds
Cv 129,130,131
Mp 160,32,48
Rb 16,8,10240
v_mul_f32_e32 v132, v12, v132
Mp 168,32,52
v_mul_f32_e32 v133, v12, v133
Cv 130,132,133
v_mul_f32_e32 v134, v12, v134
Mp 168,36,60
Rb 20,8,26624
v_mul_f32_e32 v135, v12, v135
Cv 131,134,135
Mp 160,36,56
buffer_load_dwordx4 v3, s[20:23], s40 offen offset:1024 sc1 lds
v_mul_f32_e32 v136, v13, v136
Mp 164,40,48
Rb 24,8,11264
v_mul_f32_e32 v137, v13, v137
Mp 172,40,52
Cv 136,136,137
v_mul_f32_e32 v138, v13, v138
v_mul_f32_e32 v139, v13, v139
)" R"(
Mp 172,44,60
Rb 28,8,27648
Cv 137,138,139
s_waitcnt lgkmcnt(8)
Mp 164,44,56
buffer_load_dwordx4 v3, s[20:23], s40 offen offset:2048 sc1 lds
v_mul_f32_e32 v140, v13, v140
Mp 176,48,48
Rb 32,8,12288
v_mul_f32_e32 v141, v13, v141
Mp 184,48,52
Cv 138,140,141
v_mul_f32_e32 v142, v13, v142
v_mul_f32_e32 v143, v13, v143
Mp 184,52,60
Rb 36,8,28672
Cv 139,142,143
v_mul_f32_e32 v144, v12, v144
Mp 176,52,56
v_mul_f32_e32 v145, v12, v145
Cv 144,144,145
v_mul_f32_e32 v146, v12, v146
Mp 180,56,48
Rb 40,8,13312
v_mul_f32_e32 v147, v12, v147
Mp 188,56,52
Cv 145,146,147
v_mul_f32_e32 v148, v12, v148
v_mul_f32_e32 v149, v12, v149
Mp 188,60,60
Rb 44,8,29696
Cv 146,148,149
s_waitcnt lgkmcnt(8)
Mp 180,60,56
v_mul_f32_e32 v150, v12, v150
v_mul_f32_e32 v151, v12, v151
Cv 147,150,151
Mp 192,0,48
Rb 48,8,14336
v_mul_f32_e32 v152, v13, v152
Mp 200,0,52
v_mul_f32_e32 v153, v13, v153
Cv 152,152,153
v_mul_f32_e32 v154, v13, v154
Mp 200,4,60
Rb 52,8,30720
v_mul_f32_e32 v155, v13, v155
Cv 153,154,155
Mp 192,4,56
v_mul_f32_e32 v156, v13, v156
v_mul_f32_e32 v157, v13, v157
Cv 154,156,157
Mp 196,8,48
Rb 56,8,15360
v_mul_f32_e32 v158, v13, v158
Mp 204,8,52
v_mul_f32_e32 v159, v13, v159
Cv 155,158,159
v_mul_f32_e32 v160, v12, v160
Mp 204,12,60
Rb 60,8,31744
v_mul_f32_e32 v161, v12, v161
s_waitcnt lgkmcnt(8)
Mp 196,12,56
)" R"(
Cv 160,160,161
v_mul_f32_e32 v162, v12, v162
v_mul_f32_e32 v163, v12, v163
Mp 208,16,48
Rb 0,1,0
Cv 161,162,163
Mp 216,16,52
v_mul_f32_e32 v164, v12, v164
v_mul_f32_e32 v165, v12, v165
Cv 162,164,165
Mp 216,20,60
Rb 4,1,1024
v_mul_f32_e32 v166, v12, v166
v_mul_f32_e32 v167, v12, v167
Mp 208,20,56
Cv 163,166,167
v_mul_f32_e32 v168, v13, v168
v_mul_f32_e32 v169, v13, v169
Mp 212,24,48
Rb 8,1,2048
Cv 168,168,169
Mp 220,24,52
v_mul_f32_e32 v170, v13, v170
v_mul_f32_e32 v171, v13, v171
Cv 169,170,171
Mp 220,28,60
Rb 12,1,3072
s_waitcnt vmcnt(14) lgkmcnt(4)
s_barrier
Mp 212,28,56
s_add_u32 s40, s67, 0x40000
v_mul_f32_e32 v172, v13, v172
v_mul_f32_e32 v173, v13, v173
Mp 224,32,48
Rb 16,1,4096
s_add_u32 m0, s36, 65536
Mp 232,32,52
buffer_load_dwordx4 v2, s[20:23], s40 offen offset:0 sc1 lds
Cv 170,172,173
Mp 232,36,60
Rb 20,1,5120
v_mul_f32_e32 v174, v13, v174
v_mul_f32_e32 v175, v13, v175
Mp 224,36,56
Cv 171,174,175
v_mul_f32_e32 v176, v12, v176
v_mul_f32_e32 v177, v12, v177
Mp 228,40,48
Rb 24,1,6144
Cv 176,176,177
s_lshl4_add_u32 s28, s34, s35
Mp 236,40,52
v_mul_f32_e32 v178, v12, v178
v_mul_f32_e32 v179, v12, v179
Cv 177,178,179
Mp 236,44,60
Rb 28,1,8192
v_mul_f32_e32 v180, v12, v180
Mp 228,44,56
buffer_load_dwordx4 v2, s[20:23], s40 offen offset:1024 sc1 lds
v_mul_f32_e32 v181, v12, v181
Mp 240,48,48
Rb 32,1,9216
Cv 178,180,181
s_lshl_b32 s64, s28, 17
)" R"(
Mp 248,48,52
v_mul_f32_e32 v182, v12, v182
v_mul_f32_e32 v183, v12, v183
Cv 179,182,183
Mp 248,52,60
Rb 36,1,10240
v_mul_f32_e32 v184, v13, v184
v_mul_f32_e32 v185, v13, v185
Mp 240,52,56
Cv 184,184,185
v_mul_f32_e32 v186, v13, v186
v_mul_f32_e32 v187, v13, v187
Mp 244,56,48
Rb 40,1,11264
Cv 185,186,187
s_add_u32 s64, s64, 0x4100000
Mp 252,56,52
buffer_load_dwordx4 v2, s[20:23], s40 offen offset:2048 sc1 lds
v_mul_f32_e32 v188, v13, v188
Mp 252,60,60
Rb 44,1,12288
v_mul_f32_e32 v189, v13, v189
s_mov_b32 s55, 0xc108c00
s_waitcnt lgkmcnt(8)
Mp 244,60,56
Cv 186,188,189
v_mul_f32_e32 v190, v13, v190
v_mul_f32_e32 v191, v13, v191
v_mfma_f32_16x16x32_bf16 v[32:35], a[0:3], v[128:131], 0
Rb 48,1,13312
Cv 187,190,191
s_xor_b32 s66, s64, 0x202000
v_mfma_f32_16x16x32_bf16 v[40:43], a[0:3], v[136:139], 0
v_mul_f32_e32 v192, v12, v192
v_mul_f32_e32 v193, v12, v193
Cv 192,192,193
v_mfma_f32_16x16x32_bf16 v[44:47], a[4:7], v[136:139], 0
Rb 52,1,14336
v_mul_f32_e32 v194, v12, v194
v_mul_f32_e32 v195, v12, v195
v_mfma_f32_16x16x32_bf16 v[36:39], a[4:7], v[128:131], 0
buffer_load_dwordx4 v2, s[20:23], s40 offen offset:3072 sc1 lds
Cv 193,194,195
v_mfma_f32_16x16x32_bf16 v[48:51], a[8:11], v[128:131], 0
Rb 56,1,16384
v_mul_f32_e32 v196, v12, v196
s_sub_u32 s66, s66, s84
v_mfma_f32_16x16x32_bf16 v[56:59], a[8:11], v[136:139], 0
v_mul_f32_e32 v197, v12, v197
Cv 194,196,197
v_mul_f32_e32 v198, v12, v198
v_mfma_f32_16x16x32_bf16 v[60:63], a[12:15], v[136:139], 0
Rb 60,1,17408
v_mul_f32_e32 v199, v12, v199
s_cmp_lg_u32 s33, 0
s_cmov_b32 s55, 0x40000000
s_waitcnt lgkmcnt(8)
v_mfma_f32_16x16x32_bf16 v[52:55], a[12:15], v[128:131], 0
Cv 195,198,199
v_mul_f32_e32 v200, v13, v200
v_mul_f32_e32 v201, v13, v201
v_mov_b32 v16, 1
Mp 32,16,144
Rb 0,1,18432
)" R"(
s_add_u32 m0, s36, 69632
s_sub_u32 s66, s66, s84
v_add_u32 v9, s36, v8
Mp 40,16,152
buffer_load_dwordx4 v3, s[20:23], s40 offen offset:0 sc1 lds
Cv 200,200,201
Mp 44,20,152
Rb 4,1,19456
v_mul_f32_e32 v202, v13, v202
v_mul_f32_e32 v203, v13, v203
Mp 36,20,144
Cv 201,202,203
v_mul_f32_e32 v204, v13, v204
v_mul_f32_e32 v205, v13, v205
Mp 48,24,144
Rb 8,1,20480
Cv 202,204,205
s_lshl2_add_u32 s65, s35, s33
Mp 56,24,152
v_mul_f32_e32 v206, v13, v206
v_mul_f32_e32 v207, v13, v207
Cv 203,206,207
Mp 60,28,152
Rb 12,1,21504
v_mul_f32_e32 v208, v12, v208
s_waitcnt lgkmcnt(8)
Mp 52,28,144
buffer_load_dwordx4 v3, s[20:23], s40 offen offset:1024 sc1 lds
v_mul_f32_e32 v209, v12, v209
Mp 32,32,160
Rb 16,1,22528
Cv 208,208,209
s_lshl_b32 s65, s65, 8
Mp 40,32,168
v_mul_f32_e32 v210, v12, v210
v_mul_f32_e32 v211, v12, v211
Cv 209,210,211
Mp 44,36,168
Rb 20,1,24576
v_mul_f32_e32 v212, v12, v212
v_mul_f32_e32 v213, v12, v213
Mp 36,36,160
Cv 210,212,213
v_mul_f32_e32 v214, v12, v214
v_mul_f32_e32 v215, v12, v215
Mp 48,40,160
Rb 24,1,25600
Cv 211,214,215
s_add_u32 s65, s65, s50
Mp 56,40,168
buffer_load_dwordx4 v3, s[20:23], s40 offen offset:2048 sc1 lds
v_mul_f32_e32 v216, v13, v216
Mp 60,44,168
Rb 28,1,26624
v_mul_f32_e32 v217, v13, v217
s_waitcnt lgkmcnt(8)
Mp 52,44,160
Cv 216,216,217
v_mul_f32_e32 v218, v13, v218
v_mul_f32_e32 v219, v13, v219
Mp 32,48,176
Rb 32,1,27648
Cv 217,218,219
s_add_u32 s65, s65, 0x6400
)" R"(
Mp 40,48,184
v_mul_f32_e32 v220, v13, v220
v_mul_f32_e32 v221, v13, v221
Cv 218,220,221
Mp 44,52,184
Rb 36,1,28672
v_mul_f32_e32 v222, v13, v222
v_mul_f32_e32 v223, v13, v223
Mp 36,52,176
Cv 219,222,223
v_mul_f32_e32 v224, v12, v224
v_mul_f32_e32 v225, v12, v225
Mp 48,56,176
Rb 40,1,29696
Cv 224,224,225
s_xor_b32 s52, s65, 0x202000
Mp 56,56,184
v_mul_f32_e32 v226, v12, v226
v_mul_f32_e32 v227, v12, v227
Cv 225,226,227
Mp 60,60,184
Rb 44,1,30720
v_mul_f32_e32 v228, v12, v228
s_waitcnt lgkmcnt(8)
Mp 52,60,176
v_mul_f32_e32 v229, v12, v229
Cv 226,228,229
v_mul_f32_e32 v230, v12, v230
Mp 32,0,192
Rb 48,1,32768
v_mul_f32_e32 v231, v12, v231
buffer_atomic_add v16, v7, s[20:23], s55 offen sc0 sc1
Mp 40,0,200
Cv 227,230,231
v_mul_f32_e32 v232, v13, v232
v_mul_f32_e32 v233, v13, v233
Mp 44,4,200
Rb 52,1,33792
Cv 232,232,233
v_mul_f32_e32 v234, v13, v234
Mp 36,4,192
v_mul_f32_e32 v235, v13, v235
Cv 233,234,235
v_mul_f32_e32 v236, v13, v236
Mp 48,8,192
Rb 56,1,34816
v_mul_f32_e32 v237, v13, v237
Mp 56,8,200
Cv 234,236,237
v_mul_f32_e32 v238, v13, v238
v_mul_f32_e32 v239, v13, v239
Mp 60,12,200
Rb 60,1,35840
s_waitcnt vmcnt(15) lgkmcnt(4)
s_barrier
Mp 52,12,192
s_add_u32 s40, s67, 0x50000
Cv 235,238,239
v_mul_f32_e32 v240, v12, v240
Mp 32,16,208
Rb 0,1,36864
s_add_u32 m0, s36, 0
Mp 40,16,216
buffer_load_dwordx4 v2, s[20:23], s40 offen offset:0 sc1 lds
v_mul_f32_e32 v241, v12, v241
)" R"(
Mp 44,20,216
Rb 4,1,37888
Cv 240,240,241
v_mul_f32_e32 v242, v12, v242
Mp 36,20,208
v_mul_f32_e32 v243, v12, v243
Cv 241,242,243
v_mul_f32_e32 v244, v12, v244
Mp 48,24,208
Rb 8,1,38912
v_mul_f32_e32 v245, v12, v245
Mp 56,24,216
Cv 242,244,245
v_mul_f32_e32 v246, v12, v246
v_mul_f32_e32 v247, v12, v247
Mp 60,28,216
Rb 12,1,40960
Cv 243,246,247
Mp 52,28,208
buffer_load_dwordx4 v2, s[20:23], s40 offen offset:1024 sc1 lds
v_mul_f32_e32 v248, v13, v248
Mp 32,32,224
Rb 16,1,41984
v_mul_f32_e32 v249, v13, v249
Mp 40,32,232
Cv 248,248,249
v_mul_f32_e32 v250, v13, v250
v_mul_f32_e32 v251, v13, v251
Mp 44,36,232
Rb 20,1,43008
Cv 249,250,251
v_mul_f32_e32 v252, v13, v252
Mp 36,36,224
v_mul_f32_e32 v253, v13, v253
Cv 250,252,253
v_mul_f32_e32 v254, v13, v254
Mp 48,40,224
Rb 24,1,44032
v_mul_f32_e32 v255, v13, v255
Mp 56,40,232
buffer_load_dwordx4 v2, s[20:23], s40 offen offset:2048 sc1 lds
Cv 251,254,255
Mp 60,44,232
Rb 28,1,45056
s_waitcnt lgkmcnt(8)
Mp 52,44,224
Mp 32,48,240
Rb 32,1,46080
Mp 40,48,248
Mp 44,52,248
Rb 36,1,47104
Mp 36,52,240
buffer_load_dwordx4 v2, s[20:23], s40 offen offset:3072 sc1 lds
Mp 48,56,240
Rb 40,1,49152
Mp 56,56,248
Mp 60,60,248
Rb 44,1,50176
s_waitcnt lgkmcnt(8)
Mp 52,60,240
v_mfma_f32_16x16x32_bf16 v[64:67], a[0:3], v[128:131], 0
Rb 48,1,51200
s_add_u32 m0, s36, 4096
v_mfma_f32_16x16x32_bf16 v[72:75], a[0:3], v[136:139], 0
)" R"(
buffer_load_dwordx4 v3, s[20:23], s40 offen offset:0 sc1 lds
Mp 72,4,152
Rb 52,1,52224
Mp 64,4,144
Cv 32,32,33
Cv 33,34,35
Mp 64,8,160
Rb 56,1,53248
Cv 34,36,37
Mp 72,8,168
Cv 35,38,39
Mp 72,12,184
Rb 60,1,54272
s_waitcnt lgkmcnt(8)
Mp 64,12,176
buffer_load_dwordx4 v3, s[20:23], s40 offen offset:1024 sc1 lds
Mp 64,16,192
Rb 0,1,55296
Mp 72,16,200
buffer_store_dwordx4 v[32:35], v2, s[20:23], s64 offen offset:0 sc0 sc1
Mp 72,20,216
Rb 4,1,57344
Mp 64,20,208
Mp 64,24,224
Rb 8,1,58368
Mp 72,24,232
buffer_load_dwordx4 v3, s[20:23], s40 offen offset:2048 sc1 lds
Mp 72,28,248
Rb 12,1,59392
s_waitcnt lgkmcnt(8)
Mp 64,28,240
Cv 40,40,41
Cv 41,42,43
v_mfma_f32_16x16x32_bf16 v[68:71], a[32:35], v[128:131], 0
Rb 16,1,60416
Cv 42,44,45
v_mfma_f32_16x16x32_bf16 v[76:79], a[32:35], v[136:139], 0
Cv 43,46,47
Mp 76,36,152
Rb 20,1,61440
Mp 68,36,144
buffer_store_dwordx4 v[40:43], v2, s[20:23], s64 offen offset:1024 sc0 sc1
Mp 68,40,160
Rb 24,1,62464
Mp 76,40,168
Mp 76,44,184
Rb 28,1,63488
s_waitcnt lgkmcnt(8)
Mp 68,44,176
Mp 68,48,192
Rb 32,10,0
Mp 76,48,200
Cv 48,48,49
Cv 49,50,51
Mp 76,52,216
Rb 36,10,1024
Cv 50,52,53
Mp 68,52,208
Cv 51,54,55
Mp 68,56,224
Rb 40,10,2048
Mp 76,56,232
buffer_store_dwordx4 v[48:51], v2, s[20:23], s64 offen offset:2048 sc0 sc1
Mp 76,60,248
)" R"(
Rb 44,10,3072
s_waitcnt vmcnt(18) lgkmcnt(4)
s_barrier
Mp 68,60,240
s_add_u32 s40, s67, 0x60000
v_mfma_f32_16x16x32_bf16 v[80:83], a[0:3], v[128:131], 0
Rb 48,10,4096
s_add_u32 m0, s36, 32768
v_mfma_f32_16x16x32_bf16 v[88:91], a[0:3], v[136:139], 0
buffer_load_dwordx4 v2, s[20:23], s40 offen offset:0 sc1 lds
Mp 88,4,152
Rb 52,10,5120
Mp 80,4,144
Cv 56,56,57
Cv 57,58,59
Mp 80,8,160
Rb 56,10,6144
Cv 58,60,61
Mp 88,8,168
Cv 59,62,63
Mp 88,12,184
Rb 60,10,8192
Mp 80,12,176
buffer_load_dwordx4 v2, s[20:23], s40 offen offset:1024 sc1 lds
Mp 80,16,192
Rb 0,10,9216
Mp 88,16,200
buffer_store_dwordx4 v[56:59], v2, s[20:23], s64 offen offset:3072 sc0 sc1
Mp 88,20,216
Rb 4,10,10240
Mp 80,20,208
Mp 80,24,224
Rb 8,10,11264
Mp 88,24,232
buffer_load_dwordx4 v2, s[20:23], s40 offen offset:2048 sc1 lds
Mp 88,28,248
Rb 12,10,12288
s_waitcnt lgkmcnt(8)
Mp 80,28,240
Cv 64,64,65
Cv 65,66,67
v_mfma_f32_16x16x32_bf16 v[84:87], a[32:35], v[128:131], 0
Rb 16,10,13312
Cv 66,68,69
v_mfma_f32_16x16x32_bf16 v[92:95], a[32:35], v[136:139], 0
Cv 67,70,71
Mp 92,36,152
Rb 20,10,14336
Mp 84,36,144
buffer_load_dwordx4 v2, s[20:23], s40 offen offset:3072 sc1 lds
Mp 84,40,160
Rb 24,10,16384
Mp 92,40,168
buffer_store_dwordx4 v[64:67], v3, s[20:23], s64 offen offset:0 sc0 sc1
Mp 92,44,184
Rb 28,10,17408
s_waitcnt lgkmcnt(8)
Mp 84,44,176
Mp 84,48,192
Rb 32,10,18432
s_add_u32 m0, s36, 36864
Mp 92,48,200
buffer_load_dwordx4 v3, s[20:23], s40 offen offset:0 sc1 lds
Mp 92,52,216
Rb 36,10,19456
)" R"(
Mp 84,52,208
Cv 72,72,73
Cv 73,74,75
Mp 84,56,224
Rb 40,10,20480
Cv 74,76,77
Mp 92,56,232
Cv 75,78,79
Mp 92,60,248
Rb 44,10,21504
s_waitcnt lgkmcnt(8)
Mp 84,60,240
buffer_load_dwordx4 v3, s[20:23], s40 offen offset:1024 sc1 lds
v_mfma_f32_16x16x32_bf16 v[96:99], a[0:3], v[128:131], 0
Rb 48,10,22528
v_mfma_f32_16x16x32_bf16 v[104:107], a[0:3], v[136:139], 0
buffer_store_dwordx4 v[72:75], v3, s[20:23], s64 offen offset:1024 sc0 sc1
Mp 104,4,152
Rb 52,10,24576
Mp 96,4,144
Mp 96,8,160
Rb 56,10,25600
Mp 104,8,168
buffer_load_dwordx4 v3, s[20:23], s40 offen offset:2048 sc1 lds
Mp 104,12,184
Rb 60,10,26624
s_waitcnt lgkmcnt(8)
Mp 96,12,176
Cv 80,80,81
Cv 81,82,83
Mp 96,16,192
Rb 0,10,27648
Cv 82,84,85
Mp 104,16,200
Cv 83,86,87
Mp 104,20,216
Rb 4,10,28672
Mp 96,20,208
buffer_store_dwordx4 v[80:83], v3, s[20:23], s64 offen offset:2048 sc0 sc1
Mp 96,24,224
Rb 8,10,29696
Mp 104,24,232
Mp 104,28,248
Rb 12,10,30720
s_waitcnt lgkmcnt(8)
Mp 96,28,240
v_mfma_f32_16x16x32_bf16 v[100:103], a[32:35], v[128:131], 0
Rb 16,8,32768
v_mfma_f32_16x16x32_bf16 v[108:111], a[32:35], v[136:139], 0
Cv 88,88,89
Cv 89,90,91
Mp 108,36,152
Rb 20,8,33792
Cv 90,92,93
Mp 100,36,144
Cv 91,94,95
Mp 100,40,160
Rb 24,8,34816
Mp 108,40,168
buffer_store_dwordx4 v[88:91], v3, s[20:23], s64 offen offset:3072 sc0 sc1
Mp 108,44,184
Rb 28,8,35840
s_waitcnt vmcnt(23) lgkmcnt(4)
s_barrier
Mp 100,44,176
)" R"(
s_add_u32 s40, s67, 0x70000
Mp 100,48,192
Rb 32,8,36864
s_add_u32 m0, s36, 131072
Mp 108,48,200
buffer_load_dwordx4 v2, s[20:23], s40 offen offset:0 sc1 lds
Mp 108,52,216
Rb 36,8,37888
s_mov_b32 s8, s74
Mp 100,52,208
s_mov_b32 s9, s75
s_mov_b32 s10, 0x1c0000
Mp 100,56,224
Rb 40,8,38912
s_mov_b32 s11, 0x20000
Mp 108,56,232
v_and_b32 v174, 1, v0
v_mul_u32_u24 v174, 0xffff, v174
Mp 108,60,248
Rb 44,8,40960
Mp 100,60,240
buffer_load_dwordx4 v2, s[20:23], s40 offen offset:1024 sc1 lds
v_mfma_f32_16x16x32_bf16 v[112:115], a[0:3], v[128:131], 0
Rb 48,8,41984
v_add_u32 v174, 1, v174
v_mfma_f32_16x16x32_bf16 v[120:123], a[0:3], v[136:139], 0
v_mul_u32_u24 v174, 0x3f80, v174
v_bfe_u32 v173, v0, 1, 3
Mp 120,4,152
Rb 52,8,43008
v_cmp_eq_u32_e32 vcc, 0, v173
Mp 112,4,144
Cv 96,96,97
Cv 97,98,99
Mp 112,8,160
Rb 56,8,44032
Cv 98,100,101
Mp 120,8,168
buffer_load_dwordx4 v2, s[20:23], s40 offen offset:2048 sc1 lds
Mp 120,12,184
Rb 60,8,45056
s_waitcnt lgkmcnt(8)
Mp 112,12,176
Cv 99,102,103
v_cndmask_b32_e32 v132, 0, v174, vcc
v_cmp_eq_u32_e32 vcc, 1, v173
Mp 112,16,192
Rb 0,8,46080
v_cndmask_b32_e32 v133, 0, v174, vcc
Mp 120,16,200
s_add_u32 s64, s64, 0x10000
buffer_store_dwordx4 v[96:99], v2, s[20:23], s64 offen offset:0 sc0 sc1
Mp 120,20,216
Rb 4,8,47104
v_cmp_eq_u32_e32 vcc, 2, v173
Mp 112,20,208
buffer_load_dwordx4 v2, s[20:23], s40 offen offset:3072 sc1 lds
Mp 112,24,224
Rb 8,8,49152
v_cndmask_b32_e32 v134, 0, v174, vcc
Mp 120,24,232
v_cmp_eq_u32_e32 vcc, 3, v173
v_and_b32 v167, 0xf0, v1
Mp 120,28,248
)" R"(
Rb 12,8,50176
s_waitcnt lgkmcnt(8)
Mp 112,28,240
Cv 104,104,105
Cv 105,106,107
v_mfma_f32_16x16x32_bf16 v[116:119], a[32:35], v[128:131], 0
Rb 16,8,51200
s_add_u32 m0, s36, 135168
v_mfma_f32_16x16x32_bf16 v[124:127], a[32:35], v[136:139], 0
buffer_load_dwordx4 v3, s[20:23], s40 offen offset:0 sc1 lds
Mp 124,36,152
Rb 20,8,52224
Cv 106,108,109
Mp 116,36,144
Cv 107,110,111
v_cndmask_b32_e32 v135, 0, v174, vcc
Mp 116,40,160
Rb 24,8,53248
v_cmp_eq_u32_e32 vcc, 4, v173
Mp 124,40,168
buffer_store_dwordx4 v[104:107], v2, s[20:23], s64 offen offset:1024 sc0 sc1
Mp 124,44,184
Rb 28,8,54272
s_waitcnt lgkmcnt(8)
Mp 116,44,176
buffer_load_dwordx4 v3, s[20:23], s40 offen offset:1024 sc1 lds
v_cndmask_b32_e32 v140, 0, v174, vcc
Mp 116,48,192
Rb 32,8,55296
v_cmp_eq_u32_e32 vcc, 5, v173
Mp 124,48,200
s_nop 0
v_cndmask_b32_e32 v141, 0, v174, vcc
v_cmp_eq_u32_e32 vcc, 6, v173
Mp 124,52,216
Rb 36,8,57344
v_cndmask_b32_e32 v142, 0, v174, vcc
Mp 116,52,208
v_cmp_eq_u32_e32 vcc, 7, v173
v_lshrrev_b32 v173, 4, v0
Mp 116,56,224
Rb 40,8,58368
v_cndmask_b32_e32 v143, 0, v174, vcc
Mp 124,56,232
buffer_load_dwordx4 v3, s[20:23], s40 offen offset:2048 sc1 lds
Mp 124,60,248
Rb 44,8,59392
s_waitcnt vmcnt(9) lgkmcnt(8)
buffer_store_dword v6, v7, s[20:23], s65 offen offset:0 sc0 sc1
Mp 116,60,240
v_lshl_or_b32 v164, v173, 16, v167
v_mul_u32_u24 v166, 56, v167
v_mfma_f32_16x16x32_bf16 v[32:35], a[0:3], v[128:131], 0
Rb 48,8,60416
v_lshl_add_u32 v166, v173, 3, v166
v_mfma_f32_16x16x32_bf16 v[40:43], a[0:3], v[136:139], 0
v_lshrrev_b32_e64 v173, v173, s85
v_bfe_u32 v174, v173, 4, 1
Mp 40,4,152
Rb 52,8,61440
v_lshl_or_b32 v183, v174, 13, v164
Mp 32,4,144
Cv 112,112,113
Cv 113,114,115
)" R"(
Mp 32,8,160
Rb 56,8,62464
Cv 114,116,117
Mp 40,8,168
Cv 115,118,119
v_and_b32 v174, 1, v173
Mp 40,12,184
Rb 60,8,63488
s_waitcnt lgkmcnt(8)
Mp 32,12,176
buffer_store_dwordx4 v[112:115], v2, s[20:23], s64 offen offset:2048 sc0 sc1
ds_write_b32 v9, v16 offset:39936
Mp 32,16,192
Rb 0,8,0
v_lshl_or_b32 v164, v174, 13, v164
Mp 40,16,200
v_and_b32 v174, 7, v0
v_and_b32 v165, 0x80, v1
Mp 40,20,216
Rb 4,8,1024
v_lshl_or_b32 v165, v174, 8, v165
Mp 32,20,208
v_lshlrev_b32 v165, 1, v165
s_mov_b32 s93, 0x1111
Mp 32,24,224
Rb 8,8,2048
v_lshrrev_b32 v173, 2, v0
Mp 40,24,232
Cv 120,120,121
Cv 121,122,123
Mp 40,28,248
Rb 12,8,3072
s_waitcnt lgkmcnt(3)
s_barrier
Mp 32,28,240
Cv 122,124,125
Cv 123,126,127
ds_read_b32 v16, v8 offset:39936
v_mfma_f32_16x16x32_bf16 v[36:39], a[32:35], v[128:131], 0
Rb 16,8,4096
v_mul_u32_u24 v173, 832, v173
v_mfma_f32_16x16x32_bf16 v[44:47], a[32:35], v[136:139], 0
buffer_store_dwordx4 v[120:123], v2, s[20:23], s64 offen offset:3072 sc0 sc1
Mp 44,36,152
Rb 20,8,5120
v_add_u32 v173, v173, v1
Mp 36,36,144
v_lshrrev_b32 v174, 1, v0
v_and_b32 v174, 24, v174
Mp 36,40,160
Rb 24,8,6144
v_lshl_add_u32 v174, v167, 2, v174
Mp 44,40,168
s_add_u32 s59, s36, 98304
v_add_u32 v174, s59, v174
Mp 44,44,184
Rb 28,8,8192
s_waitcnt lgkmcnt(4)
Mp 36,44,176
v_readfirstlane_b32 s82, v16
Mp 36,48,192
Rb 32,8,9216
s_lshl2_add_u32 s56, s82, s33
Mp 44,48,200
s_mul_i32 s88, s56, 4682
)" R"(
s_lshr_b32 s58, s88, 16
Mp 44,52,216
Rb 36,8,10240
s_mul_i32 s57, s58, 14
Mp 36,52,208
s_sub_u32 s57, s56, s57
s_lshl_b32 s40, s58, 12
Mp 36,56,224
Rb 40,8,11264
Mp 44,56,232
s_cmp_gt_u32 s57, 6
s_cselect_b32 s88, 128, 0
Mp 44,60,248
Rb 44,8,12288
Mp 36,60,240
buffer_load_dword v4, v7, s[20:23], s52 offen offset:0 sc0 sc1
s_add_u32 s40, s40, s88
v_mfma_f32_16x16x32_bf16 v[48:51], a[0:3], v[128:131], 0
Rb 48,8,13312
s_add_u32 s40, s40, 0xc110000
v_mfma_f32_16x16x32_bf16 v[56:59], a[0:3], v[136:139], 0
s_lshl_b32 s55, s58, 10
s_add_u32 s55, s55, s57
Mp 56,4,152
Rb 52,8,14336
Mp 48,4,144
Cv 32,32,33
Cv 33,34,35
Mp 48,8,160
Rb 56,8,16384
Cv 34,36,37
Mp 56,8,168
Cv 35,38,39
Mp 56,12,184
Rb 60,8,17408
s_waitcnt lgkmcnt(8)
Mp 48,12,176
buffer_store_dwordx4 v[32:35], v3, s[20:23], s64 offen offset:0 sc0 sc1
Mp 48,16,192
Rb 0,8,18432
Mp 56,16,200
s_cmp_gt_u32 s57, 6
s_cselect_b32 s88, 9, 0
Mp 56,20,216
Rb 4,8,19456
s_add_u32 s55, s55, s88
Mp 48,20,208
s_and_b32 s88, s58, 2
s_lshl_b32 s88, s88, 12
Mp 48,24,224
Rb 8,8,20480
v_xor_b32 v164, s88, v164
Mp 56,24,232
Cv 40,40,41
Cv 41,42,43
Mp 56,28,248
Rb 12,8,21504
s_waitcnt lgkmcnt(8)
Mp 48,28,240
Cv 42,44,45
Cv 43,46,47
v_xor_b32 v183, s88, v183
v_mfma_f32_16x16x32_bf16 v[52:55], a[32:35], v[128:131], 0
Rb 16,8,22528
)" R"(
s_lshl_b32 s55, s55, 10
v_mfma_f32_16x16x32_bf16 v[60:63], a[32:35], v[136:139], 0
buffer_store_dwordx4 v[40:43], v3, s[20:23], s64 offen offset:1024 sc0 sc1
Mp 60,36,152
Rb 20,8,24576
s_add_u32 s55, s55, 0x8000000
Mp 52,36,144
s_and_b32 s88, s57, 1
s_lshl1_add_u32 s58, s58, s88
Mp 52,40,160
Rb 24,8,25600
s_lshl_b32 s88, s58, 8
Mp 60,40,168
v_add_u32 v167, s88, v167
s_mul_i32 s59, s58, 14336
Mp 60,44,184
Rb 28,8,26624
s_waitcnt lgkmcnt(8)
Mp 52,44,176
s_lshr_b32 s88, s57, 1
s_lshl_b32 s88, s88, 6
s_add_u32 s88, s88, s59
Mp 52,48,192
Rb 32,8,27648
v_add_u32 v166, s88, v166
Mp 60,48,200
s_mov_b32 s57, 0
s_add_u32 m0, s36, 98304
Mp 60,52,216
Rb 36,8,28672
Mp 52,52,208
buffer_load_dwordx4 v173, s[12:15], s88 offen offset:0 lds
Mp 52,56,224
Rb 40,8,29696
s_add_u32 m0, s36, 98880
Mp 60,56,232
buffer_load_dwordx4 v173, s[12:15], s88 offen offset:448 lds
Mp 60,60,248
Rb 44,8,30720
s_waitcnt lgkmcnt(8)
Mp 52,60,240
s_mov_b32 s88, 0xc100008
buffer_load_dword v167, v167, s[20:23], s88 offen sc0 sc1
v_mfma_f32_16x16x32_bf16 v[64:67], a[0:3], v[128:131], 0
Rb 48,1,0
v_mfma_f32_16x16x32_bf16 v[72:75], a[0:3], v[136:139], 0
Mp 72,4,152
Rb 52,1,1024
Mp 64,4,144
Mp 64,8,160
Rb 56,1,2048
Mp 72,8,168
Mp 72,12,184
Rb 60,1,3072
s_waitcnt vmcnt(5) lgkmcnt(8)
s_barrier
v_cmp_eq_u32_e32 vcc, s3, v4
s_andn2_b64 s[28:29], 1, vcc
s_cbranch_scc1 .Lzsp23
.Lzok23:
Mp 64,12,176
buffer_load_dwordx4 a[64:67], v2, s[20:23], s66 offen offset:0 sc0 sc1
Mp 64,16,192
Rb 0,1,4096
Mp 72,16,200
)" R"(
Mp 72,20,216
Rb 4,1,5120
Mp 64,20,208
buffer_load_dwordx4 a[68:71], v2, s[20:23], s66 offen offset:1024 sc0 sc1
Mp 64,24,224
Rb 8,1,6144
Mp 72,24,232
Mp 72,28,248
Rb 12,1,8192
s_waitcnt lgkmcnt(8)
Mp 64,28,240
buffer_load_dwordx4 a[72:75], v2, s[20:23], s66 offen offset:2048 sc0 sc1
v_mfma_f32_16x16x32_bf16 v[68:71], a[32:35], v[128:131], 0
Rb 16,1,9216
v_mfma_f32_16x16x32_bf16 v[76:79], a[32:35], v[136:139], 0
Mp 76,36,152
Rb 20,1,10240
Mp 68,36,144
buffer_load_dwordx4 a[76:79], v2, s[20:23], s66 offen offset:3072 sc0 sc1
Mp 68,40,160
Rb 24,1,11264
Mp 76,40,168
Mp 76,44,184
Rb 28,1,12288
s_waitcnt vmcnt(7) lgkmcnt(8)
buffer_store_dword v6, v7, s[20:23], s65 offen offset:128 sc0 sc1
Mp 68,44,176
buffer_load_dwordx4 a[80:83], v3, s[20:23], s66 offen offset:0 sc0 sc1
Mp 68,48,192
Rb 32,1,13312
Mp 76,48,200
Mp 76,52,216
Rb 36,1,14336
Mp 68,52,208
buffer_load_dwordx4 a[84:87], v3, s[20:23], s66 offen offset:1024 sc0 sc1
Mp 68,56,224
Rb 40,1,16384
Mp 76,56,232
Mp 76,60,248
Rb 44,1,17408
s_waitcnt lgkmcnt(8)
Mp 68,60,240
buffer_load_dwordx4 a[88:91], v3, s[20:23], s66 offen offset:2048 sc0 sc1
v_mfma_f32_16x16x32_bf16 v[80:83], a[0:3], v[128:131], 0
Rb 48,1,18432
v_mfma_f32_16x16x32_bf16 v[88:91], a[0:3], v[136:139], 0
Mp 88,4,152
Rb 52,1,19456
Mp 80,4,144
buffer_load_dwordx4 a[92:95], v3, s[20:23], s66 offen offset:3072 sc0 sc1
Mp 80,8,160
Rb 56,1,20480
Mp 88,8,168
Mp 88,12,184
Rb 60,1,21504
s_waitcnt lgkmcnt(8)
Mp 80,12,176
Mp 80,16,192
Rb 0,1,22528
Mp 88,16,200
Mp 88,20,216
Rb 4,1,24576
Mp 80,20,208
Mp 80,24,224
)" R"(
Rb 8,1,25600
Mp 88,24,232
Mp 88,28,248
Rb 12,1,26624
s_waitcnt lgkmcnt(8)
Mp 80,28,240
v_mfma_f32_16x16x32_bf16 v[84:87], a[32:35], v[128:131], 0
Rb 16,1,27648
v_mfma_f32_16x16x32_bf16 v[92:95], a[32:35], v[136:139], 0
Mp 92,36,152
Rb 20,1,28672
Mp 84,36,144
Mp 84,40,160
Rb 24,1,29696
Mp 92,40,168
Mp 92,44,184
Rb 28,1,30720
s_waitcnt lgkmcnt(8)
Mp 84,44,176
buffer_load_dword v4, v7, s[20:23], s52 offen offset:128 sc0 sc1
Mp 84,48,192
Rb 32,1,32768
Mp 92,48,200
Mp 92,52,216
Rb 36,1,33792
Mp 84,52,208
Mp 84,56,224
Rb 40,1,34816
Mp 92,56,232
Mp 92,60,248
Rb 44,1,35840
s_waitcnt vmcnt(6)
s_barrier
Mp 84,60,240
Mv 48,194,64
Mv 56,194,68
Mv 52,192,64
Mv 60,192,68
Mv 64,194,72
Mv 72,194,76
Mv 68,192,72
s_waitcnt vmcnt(3) lgkmcnt(10)
Mv 76,192,76
Cv 48,48,49
Cv 49,50,51
Mv 80,194,80
Cv 50,52,53
Cv 51,54,55
Mv 88,194,84
s_add_u32 s29, s73, 0x0
buffer_store_dwordx4 v[48:51], v1, s[20:23], s29 offen sc0 sc1
Mv 84,192,80
Mv 92,192,84
v_mfma_f32_16x16x32_bf16 v[96:99], a[0:3], v[128:131], 0
Rb 48,1,36864
v_mfma_f32_16x16x32_bf16 v[104:107], a[0:3], v[136:139], 0
Mp 104,4,152
Rb 52,1,37888
s_waitcnt lgkmcnt(8)
Mp 96,4,144
Cv 56,56,57
Cv 57,58,59
ds_read_b64 v[188:189], v174 offset:0
Mp 96,8,160
Rb 56,1,38912
)" R"(
Cv 58,60,61
Mp 104,8,168
Cv 59,62,63
ds_read_b64 v[190:191], v174 offset:32
Mp 104,12,184
Rb 60,1,40960
Mp 96,12,176
s_add_u32 s29, s73, 0x400
buffer_store_dwordx4 v[56:59], v1, s[20:23], s29 offen sc0 sc1
ds_read_b64 v[196:197], v174 offset:1024
Mp 96,16,192
Rb 0,1,41984
Mp 104,16,200
ds_read_b64 v[198:199], v174 offset:1056
Mp 104,20,216
Rb 4,1,43008
s_waitcnt lgkmcnt(12)
Mp 96,20,208
Mp 96,24,224
Rb 8,1,44032
Mp 104,24,232
Cv 64,64,65
Cv 65,66,67
Mp 104,28,248
Rb 12,1,45056
Cv 66,68,69
Mp 96,28,240
Cv 67,70,71
v_mfma_f32_16x16x32_bf16 v[100:103], a[32:35], v[128:131], 0
Rb 16,1,46080
v_mfma_f32_16x16x32_bf16 v[108:111], a[32:35], v[136:139], 0
s_add_u32 s29, s73, 0x800
buffer_store_dwordx4 v[64:67], v1, s[20:23], s29 offen sc0 sc1
Mp 108,36,152
Rb 20,1,47104
s_waitcnt vmcnt(3) lgkmcnt(11)
v_cmp_eq_u32_e32 vcc, s3, v4
s_andn2_b64 s[28:29], 1, vcc
s_cbranch_scc1 .Lzsp24
.Lzok24:
Mp 100,36,144
s_add_u32 s66, s66, 0x10000
buffer_load_dwordx4 a[96:99], v2, s[20:23], s66 offen offset:0 sc0 sc1
Mp 100,40,160
Rb 24,1,49152
v_sub_f32_e32 v172, 1.0, v167
Mp 108,40,168
Cv 172,172,172
v_lshlrev_b32 v172, 16, v172
Mp 108,44,184
Rb 28,1,50176
v_lshlrev_b32 v204, 16, v188
Mp 100,44,176
buffer_load_dwordx4 a[100:103], v2, s[20:23], s66 offen offset:1024 sc0 sc1
Mp 100,48,192
Rb 32,1,51200
Cv 72,72,73
Mp 108,48,200
Cv 73,74,75
Cv 74,76,77
Mp 108,52,216
Rb 36,1,52224
s_waitcnt lgkmcnt(12)
Mp 100,52,208
)" R"(
buffer_load_dwordx4 a[104:107], v2, s[20:23], s66 offen offset:2048 sc0 sc1
Mp 100,56,224
Rb 40,1,53248
Cv 75,78,79
Mp 108,56,232
s_add_u32 s29, s73, 0xc00
buffer_store_dwordx4 v[72:75], v1, s[20:23], s29 offen sc0 sc1
Mp 108,60,248
Rb 44,1,54272
v_and_b32 v205, 0xffff0000, v188
Mp 100,60,240
buffer_load_dwordx4 a[108:111], v2, s[20:23], s66 offen offset:3072 sc0 sc1
Mv 96,194,88
v_mul_f32_e32 v204, v172, v204
v_mul_f32_e32 v205, v172, v205
Mv 104,194,92
Cv 205,204,205
v_lshlrev_b32 v204, 16, v205
Mv 100,192,88
v_and_b32 v205, 0xffff0000, v205
s_waitcnt lgkmcnt(8)
Mv 108,192,92
buffer_load_dwordx4 a[112:115], v3, s[20:23], s66 offen offset:0 sc0 sc1
v_mfma_f32_16x16x32_bf16 v[112:115], a[0:3], v[128:131], 0
Rb 48,1,55296
Cv 80,80,81
v_mfma_f32_16x16x32_bf16 v[120:123], a[0:3], v[136:139], 0
Cv 81,82,83
Cv 82,84,85
Mp 120,4,152
Rb 52,1,57344
Cv 83,86,87
Mp 112,4,144
buffer_load_dwordx4 a[116:119], v3, s[20:23], s66 offen offset:1024 sc0 sc1
Mp 112,8,160
Rb 56,1,58368
v_lshlrev_b32 v206, 16, v189
Mp 120,8,168
s_add_u32 s29, s73, 0x1000
buffer_store_dwordx4 v[80:83], v1, s[20:23], s29 offen sc0 sc1
Mp 120,12,184
Rb 60,1,59392
s_waitcnt lgkmcnt(8)
Mp 112,12,176
v_and_b32 v207, 0xffff0000, v189
v_mul_f32_e32 v206, v172, v206
Mp 112,16,192
Rb 0,1,60416
v_mul_f32_e32 v207, v172, v207
Mp 120,16,200
Cv 207,206,207
v_lshlrev_b32 v206, 16, v207
Mp 120,20,216
Rb 4,1,61440
v_and_b32 v207, 0xffff0000, v207
Mp 112,20,208
Cv 88,88,89
Cv 89,90,91
Mp 112,24,224
Rb 8,1,62464
Cv 90,92,93
Mp 120,24,232
Cv 91,94,95
v_lshlrev_b32 v212, 16, v190
)" R"(
Mp 120,28,248
Rb 12,1,63488
s_waitcnt lgkmcnt(8)
Mp 112,28,240
s_add_u32 s29, s73, 0x1400
buffer_store_dwordx4 v[88:91], v1, s[20:23], s29 offen sc0 sc1
v_mfma_f32_16x16x32_bf16 v[116:119], a[32:35], v[128:131], 0
Rb 16,10,0
v_and_b32 v213, 0xffff0000, v190
v_mfma_f32_16x16x32_bf16 v[124:127], a[32:35], v[136:139], 0
v_mul_f32_e32 v212, v172, v212
v_mul_f32_e32 v213, v172, v213
Mp 124,36,152
Rb 20,10,1024
Cv 213,212,213
Mp 116,36,144
v_lshlrev_b32 v212, 16, v213
v_and_b32 v213, 0xffff0000, v213
Mp 116,40,160
Rb 24,10,2048
v_lshlrev_b32 v214, 16, v191
Mp 124,40,168
Cv 96,96,97
Cv 97,98,99
Mp 124,44,184
Rb 28,10,3072
s_waitcnt lgkmcnt(8)
Mp 116,44,176
Cv 98,100,101
Cv 99,102,103
Mp 116,48,192
Rb 32,10,4096
v_and_b32 v215, 0xffff0000, v191
Mp 124,48,200
s_add_u32 s29, s73, 0x1800
buffer_store_dwordx4 v[96:99], v1, s[20:23], s29 offen sc0 sc1
Mp 124,52,216
Rb 36,10,5120
v_mul_f32_e32 v214, v172, v214
Mp 116,52,208
v_mul_f32_e32 v215, v172, v215
Cv 215,214,215
Mp 116,56,224
Rb 40,10,6144
v_lshlrev_b32 v214, 16, v215
Mp 124,56,232
v_and_b32 v215, 0xffff0000, v215
v_lshlrev_b32 v220, 16, v196
Mp 124,60,248
Rb 44,10,8192
s_waitcnt vmcnt(8) lgkmcnt(10)
Mp 116,60,240
Cv 104,104,105
Cv 105,106,107
Mv 112,194,96
Cv 106,108,109
Cv 107,110,111
Mv 120,194,100
s_add_u32 s29, s73, 0x4000
buffer_store_dwordx4 v[104:107], v1, s[20:23], s29 offen sc0 sc1
Mv 116,192,96
v_and_b32 v221, 0xffff0000, v196
v_mul_f32_e32 v220, v172, v220
Mv 124,192,100
)" R"(
v_mul_f32_e32 v221, v172, v221
Cv 221,220,221
v_mfma_f32_16x16x32_bf16 v[32:35], a[0:3], v[128:131], 0
Rb 48,10,9216
v_lshlrev_b32 v220, 16, v221
v_mfma_f32_16x16x32_bf16 v[40:43], a[0:3], v[136:139], 0
v_and_b32 v221, 0xffff0000, v221
v_lshlrev_b32 v222, 16, v197
Mp 40,4,152
Rb 52,10,10240
s_waitcnt lgkmcnt(8)
Mp 32,4,144
Cv 112,112,113
Cv 113,114,115
Mp 32,8,160
Rb 56,10,11264
Cv 114,116,117
Mp 40,8,168
Cv 115,118,119
v_and_b32 v223, 0xffff0000, v197
Mp 40,12,184
Rb 60,10,12288
v_mul_f32_e32 v222, v172, v222
Mp 32,12,176
s_add_u32 s29, s73, 0x4400
buffer_store_dwordx4 v[112:115], v1, s[20:23], s29 offen sc0 sc1
Mp 32,16,192
Rb 0,10,13312
v_mul_f32_e32 v223, v172, v223
Mp 40,16,200
Cv 223,222,223
v_lshlrev_b32 v222, 16, v223
Mp 40,20,216
Rb 4,10,14336
s_waitcnt lgkmcnt(8)
Mp 32,20,208
v_and_b32 v223, 0xffff0000, v223
v_lshlrev_b32 v228, 16, v198
Mp 32,24,224
Rb 8,10,16384
v_and_b32 v229, 0xffff0000, v198
Mp 40,24,232
Cv 120,120,121
Cv 121,122,123
Mp 40,28,248
Rb 12,10,17408
Cv 122,124,125
Mp 32,28,240
Cv 123,126,127
v_mul_f32_e32 v228, v172, v228
v_mfma_f32_16x16x32_bf16 v[36:39], a[32:35], v[128:131], 0
Rb 16,10,18432
v_mul_f32_e32 v229, v172, v229
v_mfma_f32_16x16x32_bf16 v[44:47], a[32:35], v[136:139], 0
s_add_u32 s29, s73, 0x4800
buffer_store_dwordx4 v[120:123], v1, s[20:23], s29 offen sc0 sc1
Mp 44,36,152
Rb 20,10,19456
s_waitcnt lgkmcnt(8)
Mp 36,36,144
Cv 229,228,229
v_lshlrev_b32 v228, 16, v229
Mp 36,40,160
Rb 24,10,20480
)" R"(
v_and_b32 v229, 0xffff0000, v229
Mp 44,40,168
v_lshlrev_b32 v230, 16, v199
v_and_b32 v231, 0xffff0000, v199
Mp 44,44,184
Rb 28,10,21504
v_mul_f32_e32 v230, v172, v230
Mp 36,44,176
v_mul_f32_e32 v231, v172, v231
Cv 231,230,231
Mp 36,48,192
Rb 32,10,22528
v_lshlrev_b32 v230, 16, v231
Mp 44,48,200
v_and_b32 v231, 0xffff0000, v231
Mp 44,52,216
Rb 36,10,24576
s_waitcnt vmcnt(3) lgkmcnt(10)
buffer_store_dword v6, v7, s[20:23], s68 offen offset:0 sc0 sc1
Mp 36,52,208
Mp 36,56,224
Rb 40,10,25600
Mp 44,56,232
Mp 44,60,248
Rb 44,10,26624
Mp 36,60,240
Mv 32,194,104
Mv 40,194,108
Mv 36,192,104
s_waitcnt lgkmcnt(8)
Mv 44,192,108
v_mfma_f32_16x16x32_bf16 v[48:51], a[0:3], v[128:131], 0
Rb 48,10,27648
v_mfma_f32_16x16x32_bf16 v[56:59], a[0:3], v[136:139], 0
Mp 56,4,152
Rb 52,10,28672
Mp 48,4,144
Cv 32,32,33
Cv 33,34,35
Mp 48,8,160
Rb 56,10,29696
Cv 34,36,37
Mp 56,8,168
Cv 35,38,39
Mp 56,12,184
Rb 60,10,30720
s_waitcnt lgkmcnt(8)
Mp 48,12,176
s_add_u32 s29, s73, 0x4c00
buffer_store_dwordx4 v[32:35], v1, s[20:23], s29 offen sc0 sc1
Mp 48,16,192
Mp 56,16,200
Mp 56,20,216
Mp 48,20,208
Mp 48,24,224
Mp 56,24,232
Cv 40,40,41
Cv 41,42,43
buffer_store_dword v6, v7, s[20:23], s68 offen offset:128 sc0 sc1
Mp 56,28,248
Cv 42,44,45
s_waitcnt lgkmcnt(4)
Mp 48,28,240
Cv 43,46,47
)" R"(
v_mfma_f32_16x16x32_bf16 v[52:55], a[32:35], v[128:131], 0
s_add_u32 s29, s73, 0x5000
buffer_store_dwordx4 v[40:43], v1, s[20:23], s29 offen sc0 sc1
v_mfma_f32_16x16x32_bf16 v[60:63], a[32:35], v[136:139], 0
buffer_load_dword v4, v165, s[20:23], s40 offen sc0 sc1
Mp 60,36,152
Mp 52,36,144
Mp 52,40,160
Mp 60,40,168
Mp 60,44,184
s_waitcnt lgkmcnt(0)
Mp 52,44,176
Mp 52,48,192
Mp 60,48,200
Mp 60,52,216
Mp 52,52,208
Mp 52,56,224
Mp 60,56,232
Mp 60,60,248
Mp 52,60,240
Mv 48,194,112
Mv 56,194,116
Mv 52,192,112
Mv 60,192,116
s_nop 7
Cv 48,48,49
Cv 49,50,51
Cv 50,52,53
Cv 51,54,55
s_add_u32 s29, s73, 0x5400
buffer_store_dwordx4 v[48:51], v1, s[20:23], s29 offen sc0 sc1
Cv 56,56,57
Cv 57,58,59
Cv 58,60,61
Cv 59,62,63
s_add_u32 s29, s73, 0x5800
buffer_store_dwordx4 v[56:59], v1, s[20:23], s29 offen sc0 sc1
.LzEND:
s_cmp_gt_u32 s82, 223
s_cbranch_scc1 .LzFIN
s_cmp_lt_u32 s82, 14
s_cbranch_scc1 .LzBB
.LzBBR:
s_waitcnt vmcnt(2)
.LzS0:
v_cmp_ne_u32_e32 vcc, s3, v4
s_lshr_b32 s28, vcc_lo, 1
s_or_b32 s28, s28, vcc_lo
s_lshr_b32 s29, s28, 2
s_or_b32 s28, s28, s29
s_andn2_b32 s28, s93, s28
s_sub_u32 s53, s53, 1
s_cselect_b32 s28, s93, s28
s_cmov_b32 s53, 0
s_xor_b32 s93, s93, s28
s_or_b32 s57, s57, s28
s_cbranch_scc1 .LzS2
s_sleep 4
s_branch .LzSk3
.LzS2:
s_bitcmp0_b32 s57, 0
s_cbranch_scc1 .LzSk0
s_add_u32 s29, s55, 0x0
buffer_load_dwordx4 a[128:131], v164, s[20:23], s29 offen offset:0 sc0 sc1
)" R"(
buffer_load_dwordx4 a[132:135], v164, s[20:23], s29 offen offset:256 sc0 sc1
buffer_load_dwordx4 a[136:139], v164, s[20:23], s29 offen offset:512 sc0 sc1
buffer_load_dwordx4 a[140:143], v164, s[20:23], s29 offen offset:768 sc0 sc1
.LzSk0:
s_bitcmp0_b32 s57, 4
s_cbranch_scc1 .LzSk1
s_add_u32 s29, s55, 0x40000
buffer_load_dwordx4 a[144:147], v183, s[20:23], s29 offen offset:0 sc0 sc1
buffer_load_dwordx4 a[148:151], v183, s[20:23], s29 offen offset:256 sc0 sc1
buffer_load_dwordx4 a[152:155], v183, s[20:23], s29 offen offset:512 sc0 sc1
buffer_load_dwordx4 a[156:159], v183, s[20:23], s29 offen offset:768 sc0 sc1
.LzSk1:
s_bitcmp0_b32 s57, 8
s_cbranch_scc1 .LzSk2
s_add_u32 s29, s55, 0x8000
buffer_load_dwordx4 a[160:163], v164, s[20:23], s29 offen offset:0 sc0 sc1
buffer_load_dwordx4 a[164:167], v164, s[20:23], s29 offen offset:256 sc0 sc1
buffer_load_dwordx4 a[168:171], v164, s[20:23], s29 offen offset:512 sc0 sc1
buffer_load_dwordx4 a[172:175], v164, s[20:23], s29 offen offset:768 sc0 sc1
.LzSk2:
s_bitcmp0_b32 s57, 12
s_cbranch_scc1 .LzSk3
s_add_u32 s29, s55, 0x48000
buffer_load_dwordx4 a[176:179], v183, s[20:23], s29 offen offset:0 sc0 sc1
buffer_load_dwordx4 a[180:183], v183, s[20:23], s29 offen offset:256 sc0 sc1
buffer_load_dwordx4 a[184:187], v183, s[20:23], s29 offen offset:512 sc0 sc1
buffer_load_dwordx4 a[188:191], v183, s[20:23], s29 offen offset:768 sc0 sc1
.LzSk3:
s_cmp_eq_u32 s93, 0
s_cbranch_scc1 .LzS1
buffer_load_dword v4, v165, s[20:23], s40 offen sc0 sc1
.LzS1:
s_waitcnt vmcnt(0)
s_bitcmp0_b32 s57, 0
s_cbranch_scc1 .LzSv0
v_accvgpr_read_b32 v173, a131
v_accvgpr_read_b32 v174, a135
v_accvgpr_read_b32 v175, a139
v_accvgpr_read_b32 v180, a143
v_max3_u32 v173, v173, v174, v175
v_max_u32 v173, v173, v180
v_cmp_eq_u32_e32 vcc, -1, v173
s_cbranch_vccnz .LzSv0
s_bitset0_b32 s57, 0
.LzSv0:
s_bitcmp0_b32 s57, 4
s_cbranch_scc1 .LzSv1
v_accvgpr_read_b32 v173, a147
v_accvgpr_read_b32 v174, a151
v_accvgpr_read_b32 v175, a155
v_accvgpr_read_b32 v180, a159
v_max3_u32 v173, v173, v174, v175
v_max_u32 v173, v173, v180
v_cmp_eq_u32_e32 vcc, -1, v173
s_cbranch_vccnz .LzSv1
s_bitset0_b32 s57, 4
.LzSv1:
s_bitcmp0_b32 s57, 8
s_cbranch_scc1 .LzSv2
v_accvgpr_read_b32 v173, a163
v_accvgpr_read_b32 v174, a167
v_accvgpr_read_b32 v175, a171
v_accvgpr_read_b32 v180, a175
v_max3_u32 v173, v173, v174, v175
)" R"(
v_max_u32 v173, v173, v180
v_cmp_eq_u32_e32 vcc, -1, v173
s_cbranch_vccnz .LzSv2
s_bitset0_b32 s57, 8
.LzSv2:
s_bitcmp0_b32 s57, 12
s_cbranch_scc1 .LzSv3
v_accvgpr_read_b32 v173, a179
v_accvgpr_read_b32 v174, a183
v_accvgpr_read_b32 v175, a187
v_accvgpr_read_b32 v180, a191
v_max3_u32 v173, v173, v174, v175
v_max_u32 v173, v173, v180
v_cmp_eq_u32_e32 vcc, -1, v173
s_cbranch_vccnz .LzSv3
s_bitset0_b32 s57, 12
.LzSv3:
s_cmp_eq_u32 s53, 0
s_cmov_b32 s57, 0
s_or_b32 s28, s57, s93
s_cbranch_scc1 .LzS0
s_bfe_u32 s28, s63, 0x70004
s_cmp_eq_u32 s28, s58
s_cbranch_scc1 .LzRT
s_bfe_u32 s28, s63, 0x70014
s_cmp_eq_u32 s28, s58
s_cbranch_scc1 .LzRT
.LzRT3:
v_mfma_f32_16x16x32_bf16 v[80:83], v[132:135], a[128:131], 0
v_mfma_f32_16x16x32_bf16 v[88:91], v[132:135], a[160:163], 0
v_mfma_f32_16x16x32_bf16 v[84:87], v[132:135], a[136:139], 0
v_mfma_f32_16x16x32_bf16 v[92:95], v[132:135], a[168:171], 0
v_mfma_f32_16x16x32_bf16 v[80:83], v[140:143], a[132:135], v[80:83]
v_mfma_f32_16x16x32_bf16 v[88:91], v[140:143], a[164:167], v[88:91]
v_mfma_f32_16x16x32_bf16 v[84:87], v[140:143], a[140:143], v[84:87]
v_mfma_f32_16x16x32_bf16 v[92:95], v[140:143], a[172:175], v[92:95]
v_mfma_f32_16x16x32_bf16 v[80:83], v[132:135], a[144:147], v[80:83]
v_mfma_f32_16x16x32_bf16 v[88:91], v[132:135], a[176:179], v[88:91]
v_mfma_f32_16x16x32_bf16 v[84:87], v[132:135], a[152:155], v[84:87]
v_mfma_f32_16x16x32_bf16 v[92:95], v[132:135], a[184:187], v[92:95]
v_mfma_f32_16x16x32_bf16 v[80:83], v[140:143], a[148:151], v[80:83]
v_mfma_f32_16x16x32_bf16 v[88:91], v[140:143], a[180:183], v[88:91]
v_mfma_f32_16x16x32_bf16 v[84:87], v[140:143], a[156:159], v[84:87]
v_mfma_f32_16x16x32_bf16 v[92:95], v[140:143], a[188:191], v[92:95]
v_lshrrev_b32 v173, 4, v0
v_and_b32 v173, 1, v173
v_mad_u32_u24 v166, v173, 24, v166
s_nop 1
Cv 81,80,81
v_lshlrev_b32 v80, 16, v81
v_and_b32 v81, 0xffff0000, v81
Cv 83,82,83
v_lshlrev_b32 v82, 16, v83
v_and_b32 v83, 0xffff0000, v83
Cv 89,88,89
v_lshlrev_b32 v88, 16, v89
v_and_b32 v89, 0xffff0000, v89
Cv 91,90,91
v_lshlrev_b32 v90, 16, v91
v_and_b32 v91, 0xffff0000, v91
Cv 85,84,85
v_lshlrev_b32 v84, 16, v85
v_and_b32 v85, 0xffff0000, v85
Cv 87,86,87
)" R"(
v_lshlrev_b32 v86, 16, v87
v_and_b32 v87, 0xffff0000, v87
Cv 93,92,93
v_lshlrev_b32 v92, 16, v93
v_and_b32 v93, 0xffff0000, v93
Cv 95,94,95
v_lshlrev_b32 v94, 16, v95
v_and_b32 v95, 0xffff0000, v95
v_mul_f32_e32 v80, v167, v80
v_mul_f32_e32 v81, v167, v81
v_mul_f32_e32 v82, v167, v82
v_mul_f32_e32 v83, v167, v83
v_mul_f32_e32 v88, v167, v88
v_mul_f32_e32 v89, v167, v89
v_mul_f32_e32 v90, v167, v90
v_mul_f32_e32 v91, v167, v91
v_mul_f32_e32 v84, v167, v84
v_mul_f32_e32 v85, v167, v85
v_mul_f32_e32 v86, v167, v86
v_mul_f32_e32 v87, v167, v87
v_mul_f32_e32 v92, v167, v92
v_mul_f32_e32 v93, v167, v93
v_mul_f32_e32 v94, v167, v94
v_mul_f32_e32 v95, v167, v95
Cv 81,80,81
v_lshlrev_b32 v80, 16, v81
v_and_b32 v81, 0xffff0000, v81
Cv 83,82,83
v_lshlrev_b32 v82, 16, v83
v_and_b32 v83, 0xffff0000, v83
Cv 89,88,89
v_lshlrev_b32 v88, 16, v89
v_and_b32 v89, 0xffff0000, v89
Cv 91,90,91
v_lshlrev_b32 v90, 16, v91
v_and_b32 v91, 0xffff0000, v91
Cv 85,84,85
v_lshlrev_b32 v84, 16, v85
v_and_b32 v85, 0xffff0000, v85
Cv 87,86,87
v_lshlrev_b32 v86, 16, v87
v_and_b32 v87, 0xffff0000, v87
Cv 93,92,93
v_lshlrev_b32 v92, 16, v93
v_and_b32 v93, 0xffff0000, v93
Cv 95,94,95
v_lshlrev_b32 v94, 16, v95
v_and_b32 v95, 0xffff0000, v95
v_add_f32_e32 v80, v80, v204
v_add_f32_e32 v81, v81, v205
v_add_f32_e32 v82, v82, v206
v_add_f32_e32 v83, v83, v207
v_add_f32_e32 v88, v88, v220
v_add_f32_e32 v89, v89, v221
v_add_f32_e32 v90, v90, v222
v_add_f32_e32 v91, v91, v223
v_add_f32_e32 v84, v84, v212
v_add_f32_e32 v85, v85, v213
v_add_f32_e32 v86, v86, v214
v_add_f32_e32 v87, v87, v215
v_add_f32_e32 v92, v92, v228
v_add_f32_e32 v93, v93, v229
v_add_f32_e32 v94, v94, v230
v_add_f32_e32 v95, v95, v231
)" R"(
Cv 64,80,81
Cv 65,82,83
Cv 66,84,85
Cv 67,86,87
Cv 68,88,89
Cv 69,90,91
Cv 70,92,93
Cv 71,94,95
v_permlane16_swap_b32_e32 v64, v66
v_permlane16_swap_b32_e32 v65, v67
buffer_store_dwordx4 v[64:67], v166, s[8:11], 0 offen offset:0 sc0 sc1
v_permlane16_swap_b32_e32 v68, v70
v_permlane16_swap_b32_e32 v69, v71
buffer_store_dwordx4 v[68:71], v166, s[8:11], 0 offen offset:448 sc0 sc1
s_or_b32 s28, s82, s33
s_cbranch_scc1 .LzFIN
s_mov_b32 s28, 0xc108400
buffer_store_dword v6, v7, s[20:23], s28 offen offset:0 sc0 sc1
.LzFIN:
s_endpgm
.LzRT:
s_and_b32 s28, s63, 0x7ff
s_mul_i32 s28, s28, 896
v_subrev_u32 v174, s28, v166
s_bfe_u32 s28, s63, 0xb0010
s_mul_i32 s28, s28, 896
v_subrev_u32 v175, s28, v166
s_movk_i32 s28, 896
v_cmp_gt_u32_e64 s[24:25], s28, v175
v_cmp_gt_u32_e32 vcc, s28, v174
v_add_u32 v175, s28, v175
s_or_b64 s[24:25], s[24:25], vcc
v_cndmask_b32_e32 v174, v175, v174, vcc
v_lshrrev_b32 v174, 5, v174
v_lshlrev_b32 v174, 7, v174
v_and_or_b32 v174, v0, 48, v174
s_mov_b64 exec, s[24:25]
s_mov_b32 s29, 0xc109000
.LzRT1:
buffer_load_dwordx4 v[64:67], v174, s[20:23], s29 offen offset:0 sc0 sc1
buffer_load_dwordx4 v[68:71], v174, s[20:23], s29 offen offset:128 sc0 sc1
buffer_load_dwordx4 v[72:75], v174, s[20:23], s29 offen offset:1792 sc0 sc1
buffer_load_dwordx4 v[76:79], v174, s[20:23], s29 offen offset:1920 sc0 sc1
s_waitcnt vmcnt(0)
v_xor_b32 v175, s3, v65
v_xor_b32 v173, s3, v67
v_or_b32 v175, v175, v173
v_xor_b32 v173, s3, v69
v_or_b32 v175, v175, v173
v_xor_b32 v173, s3, v71
v_or_b32 v175, v175, v173
v_xor_b32 v173, s3, v73
v_or_b32 v175, v175, v173
v_xor_b32 v173, s3, v75
v_or_b32 v175, v175, v173
v_xor_b32 v173, s3, v77
v_or_b32 v175, v175, v173
v_xor_b32 v173, s3, v79
v_or_b32 v175, v175, v173
v_cmp_ne_u32_e32 vcc, 0, v175
s_cbranch_vccz .LzRT2
s_sleep 2
s_sub_u32 s53, s53, 1
s_cbranch_scc0 .LzRT1
)" R"(
s_mov_b32 s53, 0
.LzRT2:
v_mov_b32 v188, v64
v_mov_b32 v189, v66
v_mov_b32 v190, v68
v_mov_b32 v191, v70
v_mov_b32 v196, v72
v_mov_b32 v197, v74
v_mov_b32 v198, v76
v_mov_b32 v199, v78
v_sub_f32_e32 v172, 1.0, v167
Cv 172,172,172
v_lshlrev_b32 v172, 16, v172
v_lshlrev_b32 v204, 16, v188
v_and_b32 v205, 0xffff0000, v188
v_mul_f32_e32 v204, v172, v204
v_mul_f32_e32 v205, v172, v205
Cv 205,204,205
v_lshlrev_b32 v204, 16, v205
v_and_b32 v205, 0xffff0000, v205
v_lshlrev_b32 v206, 16, v189
v_and_b32 v207, 0xffff0000, v189
v_mul_f32_e32 v206, v172, v206
v_mul_f32_e32 v207, v172, v207
Cv 207,206,207
v_lshlrev_b32 v206, 16, v207
v_and_b32 v207, 0xffff0000, v207
v_lshlrev_b32 v212, 16, v190
v_and_b32 v213, 0xffff0000, v190
v_mul_f32_e32 v212, v172, v212
v_mul_f32_e32 v213, v172, v213
Cv 213,212,213
v_lshlrev_b32 v212, 16, v213
v_and_b32 v213, 0xffff0000, v213
v_lshlrev_b32 v214, 16, v191
v_and_b32 v215, 0xffff0000, v191
v_mul_f32_e32 v214, v172, v214
v_mul_f32_e32 v215, v172, v215
Cv 215,214,215
v_lshlrev_b32 v214, 16, v215
v_and_b32 v215, 0xffff0000, v215
v_lshlrev_b32 v220, 16, v196
v_and_b32 v221, 0xffff0000, v196
v_mul_f32_e32 v220, v172, v220
v_mul_f32_e32 v221, v172, v221
Cv 221,220,221
v_lshlrev_b32 v220, 16, v221
v_and_b32 v221, 0xffff0000, v221
v_lshlrev_b32 v222, 16, v197
v_and_b32 v223, 0xffff0000, v197
v_mul_f32_e32 v222, v172, v222
v_mul_f32_e32 v223, v172, v223
Cv 223,222,223
v_lshlrev_b32 v222, 16, v223
v_and_b32 v223, 0xffff0000, v223
v_lshlrev_b32 v228, 16, v198
v_and_b32 v229, 0xffff0000, v198
v_mul_f32_e32 v228, v172, v228
v_mul_f32_e32 v229, v172, v229
Cv 229,228,229
v_lshlrev_b32 v228, 16, v229
v_and_b32 v229, 0xffff0000, v229
v_lshlrev_b32 v230, 16, v199
v_and_b32 v231, 0xffff0000, v199
)" R"(
v_mul_f32_e32 v230, v172, v230
v_mul_f32_e32 v231, v172, v231
Cv 231,230,231
v_lshlrev_b32 v230, 16, v231
v_and_b32 v231, 0xffff0000, v231
s_mov_b64 exec, -1
s_branch .LzRT3
.LzMASK0:
s_lshl1_add_u32 s28, s57, s33
s_mov_b32 s29, 0xff800000
s_cmp_gt_u32 s28, 5
s_cbranch_scc1 .LzMASKE0
s_cmp_eq_u32 s28, 5
s_cbranch_scc1 .LzMASKB0
v_mov_b32 v72, s29
v_mov_b32 v73, s29
v_mov_b32 v74, s29
v_mov_b32 v75, s29
v_mov_b32 v80, s29
v_mov_b32 v81, s29
v_mov_b32 v82, s29
v_mov_b32 v83, s29
v_mov_b32 v84, s29
v_mov_b32 v85, s29
v_mov_b32 v86, s29
v_mov_b32 v87, s29
v_mov_b32 v88, s29
v_mov_b32 v89, s29
v_mov_b32 v90, s29
v_mov_b32 v91, s29
v_mov_b32 v92, s29
v_mov_b32 v93, s29
v_mov_b32 v94, s29
v_mov_b32 v95, s29
s_cmp_eq_u32 s28, 4
s_cbranch_scc1 .LzMASKA0
v_mov_b32 v64, s29
v_mov_b32 v65, s29
v_mov_b32 v66, s29
v_mov_b32 v67, s29
v_mov_b32 v68, s29
v_mov_b32 v69, s29
v_mov_b32 v70, s29
v_mov_b32 v71, s29
v_mov_b32 v76, s29
v_mov_b32 v77, s29
v_mov_b32 v78, s29
v_mov_b32 v79, s29
.LzMASKE0:
s_setpc_b64 s[30:31]
.LzMASKA0:
s_mov_b32 exec_lo, 0xf0000
s_mov_b32 exec_hi, 0xfff00ff
v_mov_b32 v64, s29
v_mov_b32 v76, s29
s_mov_b32 exec_lo, 0x1f0001
s_mov_b32 exec_hi, 0x1fff01ff
v_mov_b32 v65, s29
v_mov_b32 v77, s29
s_mov_b32 exec_lo, 0x3f0003
s_mov_b32 exec_hi, 0x3fff03ff
v_mov_b32 v66, s29
v_mov_b32 v78, s29
s_mov_b32 exec_lo, 0x7f0007
)" R"(
s_mov_b32 exec_hi, 0x7fff07ff
v_mov_b32 v67, s29
v_mov_b32 v79, s29
s_mov_b64 exec, -1
s_setpc_b64 s[30:31]
.LzMASKB0:
s_nop 1
v_mov_b32 v88, s29
v_mov_b32 v89, s29
v_mov_b32 v90, s29
v_mov_b32 v91, s29
s_mov_b32 exec_lo, 0xf0000
s_mov_b32 exec_hi, 0xfff00ff
v_mov_b32 v80, s29
v_mov_b32 v92, s29
s_mov_b32 exec_lo, 0x1f0001
s_mov_b32 exec_hi, 0x1fff01ff
v_mov_b32 v81, s29
v_mov_b32 v93, s29
s_mov_b32 exec_lo, 0x3f0003
s_mov_b32 exec_hi, 0x3fff03ff
v_mov_b32 v82, s29
v_mov_b32 v94, s29
s_mov_b32 exec_lo, 0x7f0007
s_mov_b32 exec_hi, 0x7fff07ff
v_mov_b32 v83, s29
v_mov_b32 v95, s29
s_mov_b64 exec, -1
s_setpc_b64 s[30:31]
.LzFIX0:
s_nop 7
v_max_f32_e32 v28, v64, v65
v_max3_f32 v28, v28, v66, v67
v_max3_f32 v28, v28, v72, v73
v_max3_f32 v28, v28, v74, v75
v_max3_f32 v28, v28, v80, v81
v_max3_f32 v28, v28, v82, v83
v_max3_f32 v28, v28, v88, v89
v_max3_f32 v28, v28, v90, v91
v_mov_b32 v24, v28
s_nop 1
v_permlane32_swap_b32_e32 v28, v24
v_max_f32_e32 v28, v28, v24
v_mov_b32 v24, v28
s_nop 1
v_permlane16_swap_b32_e32 v28, v24
v_max_f32_e32 v28, v28, v24
v_max_f32_e32 v29, v68, v69
v_max3_f32 v29, v29, v70, v71
v_max3_f32 v29, v29, v76, v77
v_max3_f32 v29, v29, v78, v79
v_max3_f32 v29, v29, v84, v85
v_max3_f32 v29, v29, v86, v87
v_max3_f32 v29, v29, v92, v93
v_max3_f32 v29, v29, v94, v95
v_mov_b32 v24, v29
s_nop 1
v_permlane32_swap_b32_e32 v29, v24
v_max_f32_e32 v29, v29, v24
v_mov_b32 v24, v29
s_nop 1
v_permlane16_swap_b32_e32 v29, v24
v_max_f32_e32 v29, v29, v24
s_bitcmp1_b32 s88, 0
)" R"(
s_cbranch_scc1 .LzFIXF0
v_max_f32_e32 v28, 0, v28
v_sub_f32_e32 v24, 0, v28
v_exp_f32_e32 v24, v24
s_nop 0
v_mul_f32_e32 v12, v24, v12
v_mul_f32_e32 v128, v24, v128
v_mul_f32_e32 v129, v24, v129
v_mul_f32_e32 v130, v24, v130
v_mul_f32_e32 v131, v24, v131
v_mul_f32_e32 v132, v24, v132
v_mul_f32_e32 v133, v24, v133
v_mul_f32_e32 v134, v24, v134
v_mul_f32_e32 v135, v24, v135
v_mul_f32_e32 v144, v24, v144
v_mul_f32_e32 v145, v24, v145
v_mul_f32_e32 v146, v24, v146
v_mul_f32_e32 v147, v24, v147
v_mul_f32_e32 v148, v24, v148
v_mul_f32_e32 v149, v24, v149
v_mul_f32_e32 v150, v24, v150
v_mul_f32_e32 v151, v24, v151
v_mul_f32_e32 v160, v24, v160
v_mul_f32_e32 v161, v24, v161
v_mul_f32_e32 v162, v24, v162
v_mul_f32_e32 v163, v24, v163
v_mul_f32_e32 v164, v24, v164
v_mul_f32_e32 v165, v24, v165
v_mul_f32_e32 v166, v24, v166
v_mul_f32_e32 v167, v24, v167
v_mul_f32_e32 v176, v24, v176
v_mul_f32_e32 v177, v24, v177
v_mul_f32_e32 v178, v24, v178
v_mul_f32_e32 v179, v24, v179
v_mul_f32_e32 v180, v24, v180
v_mul_f32_e32 v181, v24, v181
v_mul_f32_e32 v182, v24, v182
v_mul_f32_e32 v183, v24, v183
v_mul_f32_e32 v192, v24, v192
v_mul_f32_e32 v193, v24, v193
v_mul_f32_e32 v194, v24, v194
v_mul_f32_e32 v195, v24, v195
v_mul_f32_e32 v196, v24, v196
v_mul_f32_e32 v197, v24, v197
v_mul_f32_e32 v198, v24, v198
v_mul_f32_e32 v199, v24, v199
v_mul_f32_e32 v208, v24, v208
v_mul_f32_e32 v209, v24, v209
v_mul_f32_e32 v210, v24, v210
v_mul_f32_e32 v211, v24, v211
v_mul_f32_e32 v212, v24, v212
v_mul_f32_e32 v213, v24, v213
v_mul_f32_e32 v214, v24, v214
v_mul_f32_e32 v215, v24, v215
v_mul_f32_e32 v224, v24, v224
v_mul_f32_e32 v225, v24, v225
v_mul_f32_e32 v226, v24, v226
v_mul_f32_e32 v227, v24, v227
v_mul_f32_e32 v228, v24, v228
v_mul_f32_e32 v229, v24, v229
v_mul_f32_e32 v230, v24, v230
v_mul_f32_e32 v231, v24, v231
v_mul_f32_e32 v240, v24, v240
v_mul_f32_e32 v241, v24, v241
)" R"(
v_mul_f32_e32 v242, v24, v242
v_mul_f32_e32 v243, v24, v243
v_mul_f32_e32 v244, v24, v244
v_mul_f32_e32 v245, v24, v245
v_mul_f32_e32 v246, v24, v246
v_mul_f32_e32 v247, v24, v247
v_max_f32_e32 v29, 0, v29
v_sub_f32_e32 v24, 0, v29
v_exp_f32_e32 v24, v24
s_nop 0
v_mul_f32_e32 v13, v24, v13
v_mul_f32_e32 v136, v24, v136
v_mul_f32_e32 v137, v24, v137
v_mul_f32_e32 v138, v24, v138
v_mul_f32_e32 v139, v24, v139
v_mul_f32_e32 v140, v24, v140
v_mul_f32_e32 v141, v24, v141
v_mul_f32_e32 v142, v24, v142
v_mul_f32_e32 v143, v24, v143
v_mul_f32_e32 v152, v24, v152
v_mul_f32_e32 v153, v24, v153
v_mul_f32_e32 v154, v24, v154
v_mul_f32_e32 v155, v24, v155
v_mul_f32_e32 v156, v24, v156
v_mul_f32_e32 v157, v24, v157
v_mul_f32_e32 v158, v24, v158
v_mul_f32_e32 v159, v24, v159
v_mul_f32_e32 v168, v24, v168
v_mul_f32_e32 v169, v24, v169
v_mul_f32_e32 v170, v24, v170
v_mul_f32_e32 v171, v24, v171
v_mul_f32_e32 v172, v24, v172
v_mul_f32_e32 v173, v24, v173
v_mul_f32_e32 v174, v24, v174
v_mul_f32_e32 v175, v24, v175
v_mul_f32_e32 v184, v24, v184
v_mul_f32_e32 v185, v24, v185
v_mul_f32_e32 v186, v24, v186
v_mul_f32_e32 v187, v24, v187
v_mul_f32_e32 v188, v24, v188
v_mul_f32_e32 v189, v24, v189
v_mul_f32_e32 v190, v24, v190
v_mul_f32_e32 v191, v24, v191
v_mul_f32_e32 v200, v24, v200
v_mul_f32_e32 v201, v24, v201
v_mul_f32_e32 v202, v24, v202
v_mul_f32_e32 v203, v24, v203
v_mul_f32_e32 v204, v24, v204
v_mul_f32_e32 v205, v24, v205
v_mul_f32_e32 v206, v24, v206
v_mul_f32_e32 v207, v24, v207
v_mul_f32_e32 v216, v24, v216
v_mul_f32_e32 v217, v24, v217
v_mul_f32_e32 v218, v24, v218
v_mul_f32_e32 v219, v24, v219
v_mul_f32_e32 v220, v24, v220
v_mul_f32_e32 v221, v24, v221
v_mul_f32_e32 v222, v24, v222
v_mul_f32_e32 v223, v24, v223
v_mul_f32_e32 v232, v24, v232
v_mul_f32_e32 v233, v24, v233
v_mul_f32_e32 v234, v24, v234
v_mul_f32_e32 v235, v24, v235
v_mul_f32_e32 v236, v24, v236
)" R"(
v_mul_f32_e32 v237, v24, v237
v_mul_f32_e32 v238, v24, v238
v_mul_f32_e32 v239, v24, v239
v_mul_f32_e32 v248, v24, v248
v_mul_f32_e32 v249, v24, v249
v_mul_f32_e32 v250, v24, v250
v_mul_f32_e32 v251, v24, v251
v_mul_f32_e32 v252, v24, v252
v_mul_f32_e32 v253, v24, v253
v_mul_f32_e32 v254, v24, v254
v_mul_f32_e32 v255, v24, v255
s_branch .LzFIXG0
.LzFIXF0:
v_cmp_lt_f32_e32 vcc, 0xff7fffff, v28
s_nop 1
v_cndmask_b32_e32 v28, 0, v28, vcc
v_cmp_lt_f32_e32 vcc, 0xff7fffff, v29
s_nop 1
v_cndmask_b32_e32 v29, 0, v29, vcc
.LzFIXG0:
v_sub_f32_e32 v16, v16, v28
v_sub_f32_e32 v17, v17, v28
v_sub_f32_e32 v18, v18, v28
v_sub_f32_e32 v19, v19, v28
v_sub_f32_e32 v64, v64, v28
v_sub_f32_e32 v65, v65, v28
v_sub_f32_e32 v66, v66, v28
v_sub_f32_e32 v67, v67, v28
v_sub_f32_e32 v72, v72, v28
v_sub_f32_e32 v73, v73, v28
v_sub_f32_e32 v74, v74, v28
v_sub_f32_e32 v75, v75, v28
v_sub_f32_e32 v80, v80, v28
v_sub_f32_e32 v81, v81, v28
v_sub_f32_e32 v82, v82, v28
v_sub_f32_e32 v83, v83, v28
v_sub_f32_e32 v88, v88, v28
v_sub_f32_e32 v89, v89, v28
v_sub_f32_e32 v90, v90, v28
v_sub_f32_e32 v91, v91, v28
v_sub_f32_e32 v20, v20, v29
v_sub_f32_e32 v21, v21, v29
v_sub_f32_e32 v22, v22, v29
v_sub_f32_e32 v23, v23, v29
v_sub_f32_e32 v68, v68, v29
v_sub_f32_e32 v69, v69, v29
v_sub_f32_e32 v70, v70, v29
v_sub_f32_e32 v71, v71, v29
v_sub_f32_e32 v76, v76, v29
v_sub_f32_e32 v77, v77, v29
v_sub_f32_e32 v78, v78, v29
v_sub_f32_e32 v79, v79, v29
v_sub_f32_e32 v84, v84, v29
v_sub_f32_e32 v85, v85, v29
v_sub_f32_e32 v86, v86, v29
v_sub_f32_e32 v87, v87, v29
v_sub_f32_e32 v92, v92, v29
v_sub_f32_e32 v93, v93, v29
v_sub_f32_e32 v94, v94, v29
v_sub_f32_e32 v95, v95, v29
s_bitcmp0_b32 s88, 1
s_cbranch_scc1 .LzFIXH0
v_sub_f32_e32 v96, v96, v28
v_sub_f32_e32 v97, v97, v28
)" R"(
v_sub_f32_e32 v98, v98, v28
v_sub_f32_e32 v99, v99, v28
v_sub_f32_e32 v104, v104, v28
v_sub_f32_e32 v105, v105, v28
v_sub_f32_e32 v106, v106, v28
v_sub_f32_e32 v107, v107, v28
v_sub_f32_e32 v112, v112, v28
v_sub_f32_e32 v113, v113, v28
v_sub_f32_e32 v114, v114, v28
v_sub_f32_e32 v115, v115, v28
v_sub_f32_e32 v120, v120, v28
v_sub_f32_e32 v121, v121, v28
v_sub_f32_e32 v122, v122, v28
v_sub_f32_e32 v123, v123, v28
v_sub_f32_e32 v100, v100, v29
v_sub_f32_e32 v101, v101, v29
v_sub_f32_e32 v102, v102, v29
v_sub_f32_e32 v103, v103, v29
v_sub_f32_e32 v108, v108, v29
v_sub_f32_e32 v109, v109, v29
v_sub_f32_e32 v110, v110, v29
v_sub_f32_e32 v111, v111, v29
v_sub_f32_e32 v116, v116, v29
v_sub_f32_e32 v117, v117, v29
v_sub_f32_e32 v118, v118, v29
v_sub_f32_e32 v119, v119, v29
v_sub_f32_e32 v124, v124, v29
v_sub_f32_e32 v125, v125, v29
v_sub_f32_e32 v126, v126, v29
v_sub_f32_e32 v127, v127, v29
.LzFIXH0:
v_exp_f32_e32 v32, v64
v_exp_f32_e32 v28, v65
s_nop 0
v_add_f32_e32 v14, v32, v28
Cv 32,32,28
v_exp_f32_e32 v33, v66
v_exp_f32_e32 v29, v67
v_add_f32_e32 v14, v14, v33
v_add_f32_e32 v14, v14, v29
Cv 33,33,29
v_exp_f32_e32 v34, v72
v_exp_f32_e32 v24, v73
v_add_f32_e32 v14, v14, v34
v_add_f32_e32 v14, v14, v24
Cv 34,34,24
v_exp_f32_e32 v35, v74
v_exp_f32_e32 v25, v75
v_add_f32_e32 v14, v14, v35
v_add_f32_e32 v14, v14, v25
Cv 35,35,25
v_exp_f32_e32 v36, v68
v_exp_f32_e32 v26, v69
s_nop 0
v_add_f32_e32 v15, v36, v26
Cv 36,36,26
v_exp_f32_e32 v37, v70
v_exp_f32_e32 v27, v71
v_add_f32_e32 v15, v15, v37
v_add_f32_e32 v15, v15, v27
Cv 37,37,27
v_exp_f32_e32 v38, v76
v_exp_f32_e32 v28, v77
v_add_f32_e32 v15, v15, v38
)" R"(
v_add_f32_e32 v15, v15, v28
Cv 38,38,28
v_exp_f32_e32 v39, v78
v_exp_f32_e32 v29, v79
v_add_f32_e32 v15, v15, v39
v_add_f32_e32 v15, v15, v29
Cv 39,39,29
v_exp_f32_e32 v40, v80
v_exp_f32_e32 v24, v81
v_add_f32_e32 v14, v14, v40
v_add_f32_e32 v14, v14, v24
Cv 40,40,24
v_exp_f32_e32 v41, v82
v_exp_f32_e32 v25, v83
v_add_f32_e32 v14, v14, v41
v_add_f32_e32 v14, v14, v25
Cv 41,41,25
v_exp_f32_e32 v42, v88
v_exp_f32_e32 v26, v89
v_add_f32_e32 v14, v14, v42
v_add_f32_e32 v14, v14, v26
Cv 42,42,26
v_exp_f32_e32 v43, v90
v_exp_f32_e32 v27, v91
v_add_f32_e32 v14, v14, v43
v_add_f32_e32 v14, v14, v27
Cv 43,43,27
v_exp_f32_e32 v44, v84
v_exp_f32_e32 v28, v85
v_add_f32_e32 v15, v15, v44
v_add_f32_e32 v15, v15, v28
Cv 44,44,28
v_exp_f32_e32 v45, v86
v_exp_f32_e32 v29, v87
v_add_f32_e32 v15, v15, v45
v_add_f32_e32 v15, v15, v29
Cv 45,45,29
v_exp_f32_e32 v46, v92
v_exp_f32_e32 v24, v93
v_add_f32_e32 v15, v15, v46
v_add_f32_e32 v15, v15, v24
Cv 46,46,24
v_exp_f32_e32 v47, v94
v_exp_f32_e32 v25, v95
v_add_f32_e32 v15, v15, v47
v_add_f32_e32 v15, v15, v25
Cv 47,47,25
s_nop 1
s_setpc_b64 s[30:31]
.LzMASK1:
s_lshl1_add_u32 s28, s57, s33
s_mov_b32 s29, 0xff800000
s_cmp_gt_u32 s28, 5
s_cbranch_scc1 .LzMASKE1
s_cmp_eq_u32 s28, 5
s_cbranch_scc1 .LzMASKB1
v_mov_b32 v104, s29
v_mov_b32 v105, s29
v_mov_b32 v106, s29
v_mov_b32 v107, s29
v_mov_b32 v112, s29
v_mov_b32 v113, s29
v_mov_b32 v114, s29
v_mov_b32 v115, s29
)" R"(
v_mov_b32 v116, s29
v_mov_b32 v117, s29
v_mov_b32 v118, s29
v_mov_b32 v119, s29
v_mov_b32 v120, s29
v_mov_b32 v121, s29
v_mov_b32 v122, s29
v_mov_b32 v123, s29
v_mov_b32 v124, s29
v_mov_b32 v125, s29
v_mov_b32 v126, s29
v_mov_b32 v127, s29
s_cmp_eq_u32 s28, 4
s_cbranch_scc1 .LzMASKA1
v_mov_b32 v96, s29
v_mov_b32 v97, s29
v_mov_b32 v98, s29
v_mov_b32 v99, s29
v_mov_b32 v100, s29
v_mov_b32 v101, s29
v_mov_b32 v102, s29
v_mov_b32 v103, s29
v_mov_b32 v108, s29
v_mov_b32 v109, s29
v_mov_b32 v110, s29
v_mov_b32 v111, s29
.LzMASKE1:
s_setpc_b64 s[30:31]
.LzMASKA1:
s_mov_b32 exec_lo, 0xf0000
s_mov_b32 exec_hi, 0xfff00ff
v_mov_b32 v96, s29
v_mov_b32 v108, s29
s_mov_b32 exec_lo, 0x1f0001
s_mov_b32 exec_hi, 0x1fff01ff
v_mov_b32 v97, s29
v_mov_b32 v109, s29
s_mov_b32 exec_lo, 0x3f0003
s_mov_b32 exec_hi, 0x3fff03ff
v_mov_b32 v98, s29
v_mov_b32 v110, s29
s_mov_b32 exec_lo, 0x7f0007
s_mov_b32 exec_hi, 0x7fff07ff
v_mov_b32 v99, s29
v_mov_b32 v111, s29
s_mov_b64 exec, -1
s_setpc_b64 s[30:31]
.LzMASKB1:
s_nop 1
v_mov_b32 v120, s29
v_mov_b32 v121, s29
v_mov_b32 v122, s29
v_mov_b32 v123, s29
s_mov_b32 exec_lo, 0xf0000
s_mov_b32 exec_hi, 0xfff00ff
v_mov_b32 v112, s29
v_mov_b32 v124, s29
s_mov_b32 exec_lo, 0x1f0001
s_mov_b32 exec_hi, 0x1fff01ff
v_mov_b32 v113, s29
v_mov_b32 v125, s29
s_mov_b32 exec_lo, 0x3f0003
s_mov_b32 exec_hi, 0x3fff03ff
v_mov_b32 v114, s29
)" R"(
v_mov_b32 v126, s29
s_mov_b32 exec_lo, 0x7f0007
s_mov_b32 exec_hi, 0x7fff07ff
v_mov_b32 v115, s29
v_mov_b32 v127, s29
s_mov_b64 exec, -1
s_setpc_b64 s[30:31]
.LzFIX1:
s_nop 7
v_max_f32_e32 v28, v96, v97
v_max3_f32 v28, v28, v98, v99
v_max3_f32 v28, v28, v104, v105
v_max3_f32 v28, v28, v106, v107
v_max3_f32 v28, v28, v112, v113
v_max3_f32 v28, v28, v114, v115
v_max3_f32 v28, v28, v120, v121
v_max3_f32 v28, v28, v122, v123
v_mov_b32 v24, v28
s_nop 1
v_permlane32_swap_b32_e32 v28, v24
v_max_f32_e32 v28, v28, v24
v_mov_b32 v24, v28
s_nop 1
v_permlane16_swap_b32_e32 v28, v24
v_max_f32_e32 v28, v28, v24
v_max_f32_e32 v29, v100, v101
v_max3_f32 v29, v29, v102, v103
v_max3_f32 v29, v29, v108, v109
v_max3_f32 v29, v29, v110, v111
v_max3_f32 v29, v29, v116, v117
v_max3_f32 v29, v29, v118, v119
v_max3_f32 v29, v29, v124, v125
v_max3_f32 v29, v29, v126, v127
v_mov_b32 v24, v29
s_nop 1
v_permlane32_swap_b32_e32 v29, v24
v_max_f32_e32 v29, v29, v24
v_mov_b32 v24, v29
s_nop 1
v_permlane16_swap_b32_e32 v29, v24
v_max_f32_e32 v29, v29, v24
s_bitcmp1_b32 s88, 0
s_cbranch_scc1 .LzFIXF1
v_max_f32_e32 v28, 0, v28
v_sub_f32_e32 v24, 0, v28
v_exp_f32_e32 v24, v24
s_nop 0
v_mul_f32_e32 v12, v24, v12
v_mul_f32_e32 v128, v24, v128
v_mul_f32_e32 v129, v24, v129
v_mul_f32_e32 v130, v24, v130
v_mul_f32_e32 v131, v24, v131
v_mul_f32_e32 v132, v24, v132
v_mul_f32_e32 v133, v24, v133
v_mul_f32_e32 v134, v24, v134
v_mul_f32_e32 v135, v24, v135
v_mul_f32_e32 v144, v24, v144
v_mul_f32_e32 v145, v24, v145
v_mul_f32_e32 v146, v24, v146
v_mul_f32_e32 v147, v24, v147
v_mul_f32_e32 v148, v24, v148
v_mul_f32_e32 v149, v24, v149
v_mul_f32_e32 v150, v24, v150
v_mul_f32_e32 v151, v24, v151
)" R"(
v_mul_f32_e32 v160, v24, v160
v_mul_f32_e32 v161, v24, v161
v_mul_f32_e32 v162, v24, v162
v_mul_f32_e32 v163, v24, v163
v_mul_f32_e32 v164, v24, v164
v_mul_f32_e32 v165, v24, v165
v_mul_f32_e32 v166, v24, v166
v_mul_f32_e32 v167, v24, v167
v_mul_f32_e32 v176, v24, v176
v_mul_f32_e32 v177, v24, v177
v_mul_f32_e32 v178, v24, v178
v_mul_f32_e32 v179, v24, v179
v_mul_f32_e32 v180, v24, v180
v_mul_f32_e32 v181, v24, v181
v_mul_f32_e32 v182, v24, v182
v_mul_f32_e32 v183, v24, v183
v_mul_f32_e32 v192, v24, v192
v_mul_f32_e32 v193, v24, v193
v_mul_f32_e32 v194, v24, v194
v_mul_f32_e32 v195, v24, v195
v_mul_f32_e32 v196, v24, v196
v_mul_f32_e32 v197, v24, v197
v_mul_f32_e32 v198, v24, v198
v_mul_f32_e32 v199, v24, v199
v_mul_f32_e32 v208, v24, v208
v_mul_f32_e32 v209, v24, v209
v_mul_f32_e32 v210, v24, v210
v_mul_f32_e32 v211, v24, v211
v_mul_f32_e32 v212, v24, v212
v_mul_f32_e32 v213, v24, v213
v_mul_f32_e32 v214, v24, v214
v_mul_f32_e32 v215, v24, v215
v_mul_f32_e32 v224, v24, v224
v_mul_f32_e32 v225, v24, v225
v_mul_f32_e32 v226, v24, v226
v_mul_f32_e32 v227, v24, v227
v_mul_f32_e32 v228, v24, v228
v_mul_f32_e32 v229, v24, v229
v_mul_f32_e32 v230, v24, v230
v_mul_f32_e32 v231, v24, v231
v_mul_f32_e32 v240, v24, v240
v_mul_f32_e32 v241, v24, v241
v_mul_f32_e32 v242, v24, v242
v_mul_f32_e32 v243, v24, v243
v_mul_f32_e32 v244, v24, v244
v_mul_f32_e32 v245, v24, v245
v_mul_f32_e32 v246, v24, v246
v_mul_f32_e32 v247, v24, v247
v_max_f32_e32 v29, 0, v29
v_sub_f32_e32 v24, 0, v29
v_exp_f32_e32 v24, v24
s_nop 0
v_mul_f32_e32 v13, v24, v13
v_mul_f32_e32 v136, v24, v136
v_mul_f32_e32 v137, v24, v137
v_mul_f32_e32 v138, v24, v138
v_mul_f32_e32 v139, v24, v139
v_mul_f32_e32 v140, v24, v140
v_mul_f32_e32 v141, v24, v141
v_mul_f32_e32 v142, v24, v142
v_mul_f32_e32 v143, v24, v143
v_mul_f32_e32 v152, v24, v152
v_mul_f32_e32 v153, v24, v153
v_mul_f32_e32 v154, v24, v154
)" R"(
v_mul_f32_e32 v155, v24, v155
v_mul_f32_e32 v156, v24, v156
v_mul_f32_e32 v157, v24, v157
v_mul_f32_e32 v158, v24, v158
v_mul_f32_e32 v159, v24, v159
v_mul_f32_e32 v168, v24, v168
v_mul_f32_e32 v169, v24, v169
v_mul_f32_e32 v170, v24, v170
v_mul_f32_e32 v171, v24, v171
v_mul_f32_e32 v172, v24, v172
v_mul_f32_e32 v173, v24, v173
v_mul_f32_e32 v174, v24, v174
v_mul_f32_e32 v175, v24, v175
v_mul_f32_e32 v184, v24, v184
v_mul_f32_e32 v185, v24, v185
v_mul_f32_e32 v186, v24, v186
v_mul_f32_e32 v187, v24, v187
v_mul_f32_e32 v188, v24, v188
v_mul_f32_e32 v189, v24, v189
v_mul_f32_e32 v190, v24, v190
v_mul_f32_e32 v191, v24, v191
v_mul_f32_e32 v200, v24, v200
v_mul_f32_e32 v201, v24, v201
v_mul_f32_e32 v202, v24, v202
v_mul_f32_e32 v203, v24, v203
v_mul_f32_e32 v204, v24, v204
v_mul_f32_e32 v205, v24, v205
v_mul_f32_e32 v206, v24, v206
v_mul_f32_e32 v207, v24, v207
v_mul_f32_e32 v216, v24, v216
v_mul_f32_e32 v217, v24, v217
v_mul_f32_e32 v218, v24, v218
v_mul_f32_e32 v219, v24, v219
v_mul_f32_e32 v220, v24, v220
v_mul_f32_e32 v221, v24, v221
v_mul_f32_e32 v222, v24, v222
v_mul_f32_e32 v223, v24, v223
v_mul_f32_e32 v232, v24, v232
v_mul_f32_e32 v233, v24, v233
v_mul_f32_e32 v234, v24, v234
v_mul_f32_e32 v235, v24, v235
v_mul_f32_e32 v236, v24, v236
v_mul_f32_e32 v237, v24, v237
v_mul_f32_e32 v238, v24, v238
v_mul_f32_e32 v239, v24, v239
v_mul_f32_e32 v248, v24, v248
v_mul_f32_e32 v249, v24, v249
v_mul_f32_e32 v250, v24, v250
v_mul_f32_e32 v251, v24, v251
v_mul_f32_e32 v252, v24, v252
v_mul_f32_e32 v253, v24, v253
v_mul_f32_e32 v254, v24, v254
v_mul_f32_e32 v255, v24, v255
s_branch .LzFIXG1
.LzFIXF1:
v_cmp_lt_f32_e32 vcc, 0xff7fffff, v28
s_nop 1
v_cndmask_b32_e32 v28, 0, v28, vcc
v_cmp_lt_f32_e32 vcc, 0xff7fffff, v29
s_nop 1
v_cndmask_b32_e32 v29, 0, v29, vcc
.LzFIXG1:
v_sub_f32_e32 v16, v16, v28
v_sub_f32_e32 v17, v17, v28
)" R"(
v_sub_f32_e32 v18, v18, v28
v_sub_f32_e32 v19, v19, v28
v_sub_f32_e32 v96, v96, v28
v_sub_f32_e32 v97, v97, v28
v_sub_f32_e32 v98, v98, v28
v_sub_f32_e32 v99, v99, v28
v_sub_f32_e32 v104, v104, v28
v_sub_f32_e32 v105, v105, v28
v_sub_f32_e32 v106, v106, v28
v_sub_f32_e32 v107, v107, v28
v_sub_f32_e32 v112, v112, v28
v_sub_f32_e32 v113, v113, v28
v_sub_f32_e32 v114, v114, v28
v_sub_f32_e32 v115, v115, v28
v_sub_f32_e32 v120, v120, v28
v_sub_f32_e32 v121, v121, v28
v_sub_f32_e32 v122, v122, v28
v_sub_f32_e32 v123, v123, v28
v_sub_f32_e32 v20, v20, v29
v_sub_f32_e32 v21, v21, v29
v_sub_f32_e32 v22, v22, v29
v_sub_f32_e32 v23, v23, v29
v_sub_f32_e32 v100, v100, v29
v_sub_f32_e32 v101, v101, v29
v_sub_f32_e32 v102, v102, v29
v_sub_f32_e32 v103, v103, v29
v_sub_f32_e32 v108, v108, v29
v_sub_f32_e32 v109, v109, v29
v_sub_f32_e32 v110, v110, v29
v_sub_f32_e32 v111, v111, v29
v_sub_f32_e32 v116, v116, v29
v_sub_f32_e32 v117, v117, v29
v_sub_f32_e32 v118, v118, v29
v_sub_f32_e32 v119, v119, v29
v_sub_f32_e32 v124, v124, v29
v_sub_f32_e32 v125, v125, v29
v_sub_f32_e32 v126, v126, v29
v_sub_f32_e32 v127, v127, v29
s_bitcmp0_b32 s88, 1
s_cbranch_scc1 .LzFIXH1
v_sub_f32_e32 v64, v64, v28
v_sub_f32_e32 v65, v65, v28
v_sub_f32_e32 v66, v66, v28
v_sub_f32_e32 v67, v67, v28
v_sub_f32_e32 v72, v72, v28
v_sub_f32_e32 v73, v73, v28
v_sub_f32_e32 v74, v74, v28
v_sub_f32_e32 v75, v75, v28
v_sub_f32_e32 v80, v80, v28
v_sub_f32_e32 v81, v81, v28
v_sub_f32_e32 v82, v82, v28
v_sub_f32_e32 v83, v83, v28
v_sub_f32_e32 v88, v88, v28
v_sub_f32_e32 v89, v89, v28
v_sub_f32_e32 v90, v90, v28
v_sub_f32_e32 v91, v91, v28
v_sub_f32_e32 v68, v68, v29
v_sub_f32_e32 v69, v69, v29
v_sub_f32_e32 v70, v70, v29
v_sub_f32_e32 v71, v71, v29
v_sub_f32_e32 v76, v76, v29
v_sub_f32_e32 v77, v77, v29
v_sub_f32_e32 v78, v78, v29
v_sub_f32_e32 v79, v79, v29
)" R"(
v_sub_f32_e32 v84, v84, v29
v_sub_f32_e32 v85, v85, v29
v_sub_f32_e32 v86, v86, v29
v_sub_f32_e32 v87, v87, v29
v_sub_f32_e32 v92, v92, v29
v_sub_f32_e32 v93, v93, v29
v_sub_f32_e32 v94, v94, v29
v_sub_f32_e32 v95, v95, v29
.LzFIXH1:
v_exp_f32_e32 v48, v96
v_exp_f32_e32 v26, v97
s_nop 0
v_add_f32_e32 v14, v48, v26
Cv 48,48,26
v_exp_f32_e32 v49, v98
v_exp_f32_e32 v27, v99
v_add_f32_e32 v14, v14, v49
v_add_f32_e32 v14, v14, v27
Cv 49,49,27
v_exp_f32_e32 v50, v104
v_exp_f32_e32 v28, v105
v_add_f32_e32 v14, v14, v50
v_add_f32_e32 v14, v14, v28
Cv 50,50,28
v_exp_f32_e32 v51, v106
v_exp_f32_e32 v29, v107
v_add_f32_e32 v14, v14, v51
v_add_f32_e32 v14, v14, v29
Cv 51,51,29
v_exp_f32_e32 v52, v100
v_exp_f32_e32 v24, v101
s_nop 0
v_add_f32_e32 v15, v52, v24
Cv 52,52,24
v_exp_f32_e32 v53, v102
v_exp_f32_e32 v25, v103
v_add_f32_e32 v15, v15, v53
v_add_f32_e32 v15, v15, v25
Cv 53,53,25
v_exp_f32_e32 v54, v108
v_exp_f32_e32 v26, v109
v_add_f32_e32 v15, v15, v54
v_add_f32_e32 v15, v15, v26
Cv 54,54,26
v_exp_f32_e32 v55, v110
v_exp_f32_e32 v27, v111
v_add_f32_e32 v15, v15, v55
v_add_f32_e32 v15, v15, v27
Cv 55,55,27
v_exp_f32_e32 v56, v112
v_exp_f32_e32 v28, v113
v_add_f32_e32 v14, v14, v56
v_add_f32_e32 v14, v14, v28
Cv 56,56,28
v_exp_f32_e32 v57, v114
v_exp_f32_e32 v29, v115
v_add_f32_e32 v14, v14, v57
v_add_f32_e32 v14, v14, v29
Cv 57,57,29
v_exp_f32_e32 v58, v120
v_exp_f32_e32 v24, v121
v_add_f32_e32 v14, v14, v58
v_add_f32_e32 v14, v14, v24
Cv 58,58,24
)" R"(
v_exp_f32_e32 v59, v122
v_exp_f32_e32 v25, v123
v_add_f32_e32 v14, v14, v59
v_add_f32_e32 v14, v14, v25
Cv 59,59,25
v_exp_f32_e32 v60, v116
v_exp_f32_e32 v26, v117
v_add_f32_e32 v15, v15, v60
v_add_f32_e32 v15, v15, v26
Cv 60,60,26
v_exp_f32_e32 v61, v118
v_exp_f32_e32 v27, v119
v_add_f32_e32 v15, v15, v61
v_add_f32_e32 v15, v15, v27
Cv 61,61,27
v_exp_f32_e32 v62, v124
v_exp_f32_e32 v28, v125
v_add_f32_e32 v15, v15, v62
v_add_f32_e32 v15, v15, v28
Cv 62,62,28
v_exp_f32_e32 v63, v126
v_exp_f32_e32 v29, v127
v_add_f32_e32 v15, v15, v63
v_add_f32_e32 v15, v15, v29
Cv 63,63,29
s_nop 1
s_setpc_b64 s[30:31]
.Lzsp1:
s_sub_u32 s53, s53, 1
s_cbranch_scc1 .Lzsq1
s_sleep 1
buffer_load_dword v4, v2, s[20:23], s85 offen sc0 sc1
s_waitcnt vmcnt(0)
v_xor_b32 v4, s3, v4
v_cmp_gt_u32_e32 vcc, 2, v4
s_andn2_b32 s28, 0xff, vcc_lo
s_cbranch_scc1 .Lzsp1
s_branch .Lzok1
.Lzsq1:
s_mov_b32 s53, 0
s_branch .Lzok1
.Lzsp2:
s_sub_u32 s53, s53, 1
s_cbranch_scc1 .Lzsq2
s_sleep 1
buffer_load_dword v4, v7, s[20:23], s52 offen offset:0 sc0 sc1
s_waitcnt vmcnt(0)
v_cmp_eq_u32_e32 vcc, s3, v4
s_andn2_b64 s[28:29], 1, vcc
s_cbranch_scc1 .Lzsp2
s_branch .Lzok2
.Lzsq2:
s_mov_b32 s53, 0
s_branch .Lzok2
.Lzsp3:
s_sub_u32 s53, s53, 1
s_cbranch_scc1 .Lzsq3
s_sleep 1
buffer_load_dword v4, v2, s[20:23], s85 offen sc0 sc1
s_waitcnt vmcnt(0)
v_xor_b32 v4, s3, v4
v_cmp_gt_u32_e32 vcc, 2, v4
s_andn2_b32 s28, 0xff, vcc_lo
s_cbranch_scc1 .Lzsp3
)" R"(
s_branch .Lzok3
.Lzsq3:
s_mov_b32 s53, 0
s_branch .Lzok3
.Lzsp4:
s_sub_u32 s53, s53, 1
s_cbranch_scc1 .Lzsq4
s_sleep 1
buffer_load_dword v4, v7, s[20:23], s52 offen offset:0 sc0 sc1
s_waitcnt vmcnt(0)
v_cmp_eq_u32_e32 vcc, s3, v4
s_andn2_b64 s[28:29], 1, vcc
s_cbranch_scc1 .Lzsp4
s_branch .Lzok4
.Lzsq4:
s_mov_b32 s53, 0
s_branch .Lzok4
.Lzsp5:
s_sub_u32 s53, s53, 1
s_cbranch_scc1 .Lzsq5
s_sleep 1
buffer_load_dword v4, v5, s[20:23], s50 offen offset:0 sc0 sc1
s_waitcnt vmcnt(0)
v_cmp_eq_u32_e32 vcc, s3, v4
s_andn2_b64 s[28:29], -1, vcc
s_cbranch_scc1 .Lzsp5
s_branch .Lzok5
.Lzsq5:
s_mov_b32 s53, 0
s_branch .Lzok5
.Lzsp6:
s_sub_u32 s53, s53, 1
s_cbranch_scc1 .Lzsq6
s_sleep 1
buffer_load_dword v4, v5, s[20:23], s50 offen offset:0 sc0 sc1
s_waitcnt vmcnt(0)
v_cmp_eq_u32_e32 vcc, s3, v4
s_andn2_b64 s[28:29], -1, vcc
s_cbranch_scc1 .Lzsp6
s_branch .Lzok6
.Lzsq6:
s_mov_b32 s53, 0
s_branch .Lzok6
.Lzsp7:
s_sub_u32 s53, s53, 1
s_cbranch_scc1 .Lzsq7
s_sleep 1
buffer_load_dwordx2 v[24:25], v7, s[20:23], s83 offen sc0 sc1
s_waitcnt vmcnt(0)
v_cmp_eq_u32_e32 vcc, s3, v25
s_andn2_b64 s[28:29], 1, vcc
s_cbranch_scc1 .Lzsp7
s_branch .Lzok7
.Lzsq7:
s_mov_b32 s53, 0
s_branch .Lzok7
.LzT2S:
s_cmp_lg_u32 s33, 0
s_cbranch_scc1 .LzT2E
.LzT2:
s_mov_b32 s28, 0xc100000
buffer_load_dwordx2 v[32:33], v1, s[20:23], s28 offen offset:0 sc0 sc1
buffer_load_dwordx2 v[34:35], v1, s[20:23], s28 offen offset:1024 sc0 sc1
buffer_load_dwordx2 v[36:37], v1, s[20:23], s28 offen offset:2048 sc0 sc1
)" R"(
buffer_load_dwordx2 v[38:39], v1, s[20:23], s28 offen offset:3072 sc0 sc1
s_mov_b32 s28, 0xc101000
buffer_load_dwordx2 v[40:41], v1, s[20:23], s28 offen offset:0 sc0 sc1
buffer_load_dwordx2 v[42:43], v1, s[20:23], s28 offen offset:1024 sc0 sc1
buffer_load_dwordx2 v[44:45], v1, s[20:23], s28 offen offset:2048 sc0 sc1
buffer_load_dwordx2 v[46:47], v1, s[20:23], s28 offen offset:3072 sc0 sc1
s_mov_b32 s28, 0xc102000
buffer_load_dwordx2 v[48:49], v1, s[20:23], s28 offen offset:0 sc0 sc1
buffer_load_dwordx2 v[50:51], v1, s[20:23], s28 offen offset:1024 sc0 sc1
buffer_load_dwordx2 v[52:53], v1, s[20:23], s28 offen offset:2048 sc0 sc1
buffer_load_dwordx2 v[54:55], v1, s[20:23], s28 offen offset:3072 sc0 sc1
s_mov_b32 s28, 0xc103000
buffer_load_dwordx2 v[56:57], v1, s[20:23], s28 offen offset:0 sc0 sc1
buffer_load_dwordx2 v[58:59], v1, s[20:23], s28 offen offset:1024 sc0 sc1
buffer_load_dwordx2 v[60:61], v1, s[20:23], s28 offen offset:2048 sc0 sc1
buffer_load_dwordx2 v[62:63], v1, s[20:23], s28 offen offset:3072 sc0 sc1
s_mov_b32 s28, 0xc104000
buffer_load_dwordx2 v[64:65], v1, s[20:23], s28 offen offset:0 sc0 sc1
buffer_load_dwordx2 v[66:67], v1, s[20:23], s28 offen offset:1024 sc0 sc1
buffer_load_dwordx2 v[68:69], v1, s[20:23], s28 offen offset:2048 sc0 sc1
buffer_load_dwordx2 v[70:71], v1, s[20:23], s28 offen offset:3072 sc0 sc1
s_mov_b32 s28, 0xc105000
buffer_load_dwordx2 v[72:73], v1, s[20:23], s28 offen offset:0 sc0 sc1
buffer_load_dwordx2 v[74:75], v1, s[20:23], s28 offen offset:1024 sc0 sc1
buffer_load_dwordx2 v[76:77], v1, s[20:23], s28 offen offset:2048 sc0 sc1
buffer_load_dwordx2 v[78:79], v1, s[20:23], s28 offen offset:3072 sc0 sc1
s_mov_b32 s28, 0xc106000
buffer_load_dwordx2 v[80:81], v1, s[20:23], s28 offen offset:0 sc0 sc1
buffer_load_dwordx2 v[82:83], v1, s[20:23], s28 offen offset:1024 sc0 sc1
buffer_load_dwordx2 v[84:85], v1, s[20:23], s28 offen offset:2048 sc0 sc1
buffer_load_dwordx2 v[86:87], v1, s[20:23], s28 offen offset:3072 sc0 sc1
s_mov_b32 s28, 0xc107000
buffer_load_dwordx2 v[88:89], v1, s[20:23], s28 offen offset:0 sc0 sc1
buffer_load_dwordx2 v[90:91], v1, s[20:23], s28 offen offset:1024 sc0 sc1
buffer_load_dwordx2 v[92:93], v1, s[20:23], s28 offen offset:2048 sc0 sc1
buffer_load_dwordx2 v[94:95], v1, s[20:23], s28 offen offset:3072 sc0 sc1
v_mov_b32 v26, 0
v_mov_b32 v27, -1
s_mov_b32 s28, 0xc108c00
buffer_store_dword v26, v7, s[20:23], s28 offen sc0 sc1
buffer_store_dword v26, v7, s[20:23], s28 offen offset:128 sc0 sc1
s_waitcnt vmcnt(0)
v_max3_u32 v26, v26, v33, v35
v_min3_u32 v27, v27, v33, v35
v_max3_u32 v26, v26, v37, v39
v_min3_u32 v27, v27, v37, v39
v_max3_u32 v26, v26, v41, v43
v_min3_u32 v27, v27, v41, v43
v_max3_u32 v26, v26, v45, v47
v_min3_u32 v27, v27, v45, v47
v_max3_u32 v26, v26, v49, v51
v_min3_u32 v27, v27, v49, v51
v_max3_u32 v26, v26, v53, v55
v_min3_u32 v27, v27, v53, v55
v_max3_u32 v26, v26, v57, v59
v_min3_u32 v27, v27, v57, v59
v_max3_u32 v26, v26, v61, v63
v_min3_u32 v27, v27, v61, v63
v_max3_u32 v26, v26, v65, v67
v_min3_u32 v27, v27, v65, v67
v_max3_u32 v26, v26, v69, v71
v_min3_u32 v27, v27, v69, v71
v_max3_u32 v26, v26, v73, v75
v_min3_u32 v27, v27, v73, v75
)" R"(
v_max3_u32 v26, v26, v77, v79
v_min3_u32 v27, v27, v77, v79
v_max3_u32 v26, v26, v81, v83
v_min3_u32 v27, v27, v81, v83
v_max3_u32 v26, v26, v85, v87
v_min3_u32 v27, v27, v85, v87
v_max3_u32 v26, v26, v89, v91
v_min3_u32 v27, v27, v89, v91
v_max3_u32 v26, v26, v93, v95
v_min3_u32 v27, v27, v93, v95
v_cmp_ne_u32_e32 vcc, s3, v26
v_cmp_ne_u32_e64 s[28:29], s3, v27
s_or_b64 vcc, vcc, s[28:29]
s_cbranch_scc0 .LzT2G
s_sub_u32 s53, s53, 1
s_cbranch_scc0 .LzT2
s_mov_b32 s53, 0
.LzT2G:
v_max_u32 v26, v32, v34
v_min_u32 v27, v32, v34
v_med3_u32 v27, v26, v27, v36
v_max_u32 v26, v26, v36
v_med3_u32 v27, v26, v27, v38
v_max_u32 v26, v26, v38
v_med3_u32 v27, v26, v27, v40
v_max_u32 v26, v26, v40
v_med3_u32 v27, v26, v27, v42
v_max_u32 v26, v26, v42
v_med3_u32 v27, v26, v27, v44
v_max_u32 v26, v26, v44
v_med3_u32 v27, v26, v27, v46
v_max_u32 v26, v26, v46
v_med3_u32 v27, v26, v27, v48
v_max_u32 v26, v26, v48
v_med3_u32 v27, v26, v27, v50
v_max_u32 v26, v26, v50
v_med3_u32 v27, v26, v27, v52
v_max_u32 v26, v26, v52
v_med3_u32 v27, v26, v27, v54
v_max_u32 v26, v26, v54
v_med3_u32 v27, v26, v27, v56
v_max_u32 v26, v26, v56
v_med3_u32 v27, v26, v27, v58
v_max_u32 v26, v26, v58
v_med3_u32 v27, v26, v27, v60
v_max_u32 v26, v26, v60
v_med3_u32 v27, v26, v27, v62
v_max_u32 v26, v26, v62
v_med3_u32 v27, v26, v27, v64
v_max_u32 v26, v26, v64
v_med3_u32 v27, v26, v27, v66
v_max_u32 v26, v26, v66
v_med3_u32 v27, v26, v27, v68
v_max_u32 v26, v26, v68
v_med3_u32 v27, v26, v27, v70
v_max_u32 v26, v26, v70
v_med3_u32 v27, v26, v27, v72
v_max_u32 v26, v26, v72
v_med3_u32 v27, v26, v27, v74
v_max_u32 v26, v26, v74
v_med3_u32 v27, v26, v27, v76
v_max_u32 v26, v26, v76
v_med3_u32 v27, v26, v27, v78
v_max_u32 v26, v26, v78
)" R"(
v_med3_u32 v27, v26, v27, v80
v_max_u32 v26, v26, v80
v_med3_u32 v27, v26, v27, v82
v_max_u32 v26, v26, v82
v_med3_u32 v27, v26, v27, v84
v_max_u32 v26, v26, v84
v_med3_u32 v27, v26, v27, v86
v_max_u32 v26, v26, v86
v_med3_u32 v27, v26, v27, v88
v_max_u32 v26, v26, v88
v_med3_u32 v27, v26, v27, v90
v_max_u32 v26, v26, v90
v_med3_u32 v27, v26, v27, v92
v_max_u32 v26, v26, v92
v_med3_u32 v27, v26, v27, v94
v_max_u32 v26, v26, v94
v_mov_b32 v24, v26
v_mov_b32 v25, v27
s_nop 1
v_permlane32_swap_b32_e32 v26, v24
v_permlane32_swap_b32_e32 v27, v25
v_max_u32 v27, v27, v25
v_med3_u32 v27, v26, v27, v24
v_max_u32 v26, v26, v24
v_mov_b32 v24, v26
v_mov_b32 v25, v27
s_nop 1
v_permlane16_swap_b32_e32 v26, v24
v_permlane16_swap_b32_e32 v27, v25
v_max_u32 v27, v27, v25
v_med3_u32 v27, v26, v27, v24
v_max_u32 v26, v26, v24
s_nop 1
v_mov_b32_dpp v24, v26 row_ror:8 row_mask:0xf bank_mask:0xf
v_mov_b32_dpp v25, v27 row_ror:8 row_mask:0xf bank_mask:0xf
v_max_u32 v27, v27, v25
v_med3_u32 v27, v26, v27, v24
v_max_u32 v26, v26, v24
s_nop 1
v_mov_b32_dpp v24, v26 row_ror:4 row_mask:0xf bank_mask:0xf
v_mov_b32_dpp v25, v27 row_ror:4 row_mask:0xf bank_mask:0xf
v_max_u32 v27, v27, v25
v_med3_u32 v27, v26, v27, v24
v_max_u32 v26, v26, v24
s_nop 1
v_mov_b32_dpp v24, v26 row_ror:2 row_mask:0xf bank_mask:0xf
v_mov_b32_dpp v25, v27 row_ror:2 row_mask:0xf bank_mask:0xf
v_max_u32 v27, v27, v25
v_med3_u32 v27, v26, v27, v24
v_max_u32 v26, v26, v24
s_nop 1
v_mov_b32_dpp v24, v26 row_ror:1 row_mask:0xf bank_mask:0xf
v_mov_b32_dpp v25, v27 row_ror:1 row_mask:0xf bank_mask:0xf
v_max_u32 v27, v27, v25
v_med3_u32 v27, v26, v27, v24
v_max_u32 v26, v26, v24
v_and_b32 v26, 0xffff, v26
v_lshl_or_b32 v26, v27, 16, v26
v_not_b32 v24, v26
v_mov_b32 v25, s3
s_mov_b32 s28, 0xc108000
buffer_store_dwordx2 v[24:25], v7, s[20:23], s28 offen sc0 sc1
s_branch .LzT2E
.LzA7:
)" R"(
s_waitcnt vmcnt(0)
s_nop 7
v_mul_f32_e32 v96, s54, v96
v_mul_f32_e32 v97, s54, v97
Cv 96,96,97
v_accvgpr_write_b32 a112, v96
v_mul_f32_e32 v98, s54, v98
v_mul_f32_e32 v99, s54, v99
Cv 98,98,99
v_accvgpr_write_b32 a113, v98
v_mul_f32_e32 v100, s54, v100
v_mul_f32_e32 v101, s54, v101
Cv 100,100,101
v_accvgpr_write_b32 a114, v100
v_mul_f32_e32 v102, s54, v102
v_mul_f32_e32 v103, s54, v103
Cv 102,102,103
v_accvgpr_write_b32 a115, v102
v_mul_f32_e32 v104, s54, v104
v_mul_f32_e32 v105, s54, v105
Cv 104,104,105
v_accvgpr_write_b32 a116, v104
v_mul_f32_e32 v106, s54, v106
v_mul_f32_e32 v107, s54, v107
Cv 106,106,107
v_accvgpr_write_b32 a117, v106
v_mul_f32_e32 v108, s54, v108
v_mul_f32_e32 v109, s54, v109
Cv 108,108,109
v_accvgpr_write_b32 a118, v108
v_mul_f32_e32 v110, s54, v110
v_mul_f32_e32 v111, s54, v111
Cv 110,110,111
v_accvgpr_write_b32 a119, v110
v_mul_f32_e32 v112, s54, v112
v_mul_f32_e32 v113, s54, v113
Cv 112,112,113
v_accvgpr_write_b32 a120, v112
v_mul_f32_e32 v114, s54, v114
v_mul_f32_e32 v115, s54, v115
Cv 114,114,115
v_accvgpr_write_b32 a121, v114
v_mul_f32_e32 v116, s54, v116
v_mul_f32_e32 v117, s54, v117
Cv 116,116,117
v_accvgpr_write_b32 a122, v116
v_mul_f32_e32 v118, s54, v118
v_mul_f32_e32 v119, s54, v119
Cv 118,118,119
v_accvgpr_write_b32 a123, v118
v_mul_f32_e32 v120, s54, v120
v_mul_f32_e32 v121, s54, v121
Cv 120,120,121
v_accvgpr_write_b32 a124, v120
v_mul_f32_e32 v122, s54, v122
v_mul_f32_e32 v123, s54, v123
Cv 122,122,123
v_accvgpr_write_b32 a125, v122
v_mul_f32_e32 v124, s54, v124
v_mul_f32_e32 v125, s54, v125
Cv 124,124,125
v_accvgpr_write_b32 a126, v124
v_mul_f32_e32 v126, s54, v126
v_mul_f32_e32 v127, s54, v127
)" R"(
Cv 126,126,127
v_accvgpr_write_b32 a127, v126
v_accvgpr_mov_b32 a64, a224
v_accvgpr_mov_b32 a65, a225
v_accvgpr_mov_b32 a66, a226
v_accvgpr_mov_b32 a67, a227
v_accvgpr_mov_b32 a68, a228
v_accvgpr_mov_b32 a69, a229
v_accvgpr_mov_b32 a70, a230
v_accvgpr_mov_b32 a71, a231
v_accvgpr_mov_b32 a72, a232
v_accvgpr_mov_b32 a73, a233
v_accvgpr_mov_b32 a74, a234
v_accvgpr_mov_b32 a75, a235
v_accvgpr_mov_b32 a76, a236
v_accvgpr_mov_b32 a77, a237
v_accvgpr_mov_b32 a78, a238
v_accvgpr_mov_b32 a79, a239
v_accvgpr_mov_b32 a80, a240
v_accvgpr_mov_b32 a81, a241
v_accvgpr_mov_b32 a82, a242
v_accvgpr_mov_b32 a83, a243
v_accvgpr_mov_b32 a84, a244
v_accvgpr_mov_b32 a85, a245
v_accvgpr_mov_b32 a86, a246
v_accvgpr_mov_b32 a87, a247
v_accvgpr_mov_b32 a88, a248
v_accvgpr_mov_b32 a89, a249
v_accvgpr_mov_b32 a90, a250
v_accvgpr_mov_b32 a91, a251
v_accvgpr_mov_b32 a92, a252
v_accvgpr_mov_b32 a93, a253
v_accvgpr_mov_b32 a94, a254
v_accvgpr_mov_b32 a95, a255
v_accvgpr_mov_b32 a96, a192
v_accvgpr_mov_b32 a97, a193
v_accvgpr_mov_b32 a98, a194
v_accvgpr_mov_b32 a99, a195
v_accvgpr_mov_b32 a100, a196
v_accvgpr_mov_b32 a101, a197
v_accvgpr_mov_b32 a102, a198
v_accvgpr_mov_b32 a103, a199
v_accvgpr_mov_b32 a104, a200
v_accvgpr_mov_b32 a105, a201
v_accvgpr_mov_b32 a106, a202
v_accvgpr_mov_b32 a107, a203
v_accvgpr_mov_b32 a108, a204
v_accvgpr_mov_b32 a109, a205
v_accvgpr_mov_b32 a110, a206
v_accvgpr_mov_b32 a111, a207
s_mov_b32 s94, 2
s_nop 1
s_branch .LzA7R
.Lzsp8:
s_sub_u32 s53, s53, 1
s_cbranch_scc1 .Lzsq8
s_sleep 1
buffer_load_dword v4, v5, s[20:23], s50 offen offset:0 sc0 sc1
s_waitcnt vmcnt(0)
v_cmp_eq_u32_e32 vcc, s3, v4
s_andn2_b64 s[28:29], -1, vcc
s_cbranch_scc1 .Lzsp8
s_branch .Lzok8
.Lzsq8:
)" R"(
s_mov_b32 s53, 0
s_branch .Lzok8
.Lzmk0:
s_call_b64 s[30:31], .LzMASK0
s_branch .Lzmr0
.Lzfx0:
s_mov_b32 s88, 3
s_call_b64 s[30:31], .LzFIX0
s_branch .Lzfr0
.Lzmk1:
s_call_b64 s[30:31], .LzMASK1
s_branch .Lzmr1
.Lzsp9:
s_sub_u32 s53, s53, 1
s_cbranch_scc1 .Lzsq9
s_sleep 1
buffer_load_dword v4, v5, s[20:23], s50 offen offset:0 sc0 sc1
s_waitcnt vmcnt(0)
v_cmp_eq_u32_e32 vcc, s3, v4
s_andn2_b64 s[28:29], -1, vcc
s_cbranch_scc1 .Lzsp9
s_branch .Lzok9
.Lzsq9:
s_mov_b32 s53, 0
s_branch .Lzok9
.Lzfx1:
s_mov_b32 s88, 2
s_call_b64 s[30:31], .LzFIX1
s_branch .Lzfr1
.Lzmk2:
s_call_b64 s[30:31], .LzMASK0
s_branch .Lzmr2
.Lzfx2:
s_mov_b32 s88, 2
s_call_b64 s[30:31], .LzFIX0
s_branch .Lzfr2
.Lzmk3:
s_call_b64 s[30:31], .LzMASK1
s_branch .Lzmr3
.Lzsp10:
s_sub_u32 s53, s53, 1
s_cbranch_scc1 .Lzsq10
s_sleep 1
buffer_load_dword v4, v5, s[20:23], s50 offen offset:0 sc0 sc1
s_waitcnt vmcnt(0)
v_cmp_eq_u32_e32 vcc, s3, v4
s_andn2_b64 s[28:29], -1, vcc
s_cbranch_scc1 .Lzsp10
s_branch .Lzok10
.Lzsq10:
s_mov_b32 s53, 0
s_branch .Lzok10
.Lzfx3:
s_mov_b32 s88, 0
s_call_b64 s[30:31], .LzFIX1
s_branch .Lzfr3
.Lzmk4:
s_call_b64 s[30:31], .LzMASK0
s_branch .Lzmr4
.Lzfx4:
s_mov_b32 s88, 3
s_call_b64 s[30:31], .LzFIX0
s_branch .Lzfr4
.Lzmk5:
)" R"(
s_call_b64 s[30:31], .LzMASK1
s_branch .Lzmr5
.Lzfx5:
s_mov_b32 s88, 0
s_call_b64 s[30:31], .LzFIX1
s_branch .Lzfr5
.Lzsp11:
s_sub_u32 s53, s53, 1
s_cbranch_scc1 .Lzsq11
s_sleep 1
buffer_load_dword v4, v7, s[20:23], s52 offen offset:0 sc0 sc1
s_waitcnt vmcnt(0)
v_cmp_eq_u32_e32 vcc, s3, v4
s_andn2_b64 s[28:29], 1, vcc
s_cbranch_scc1 .Lzsp11
s_branch .Lzok11
.Lzsq11:
s_mov_b32 s53, 0
s_branch .Lzok11
.Lzsp12:
s_sub_u32 s53, s53, 1
s_cbranch_scc1 .Lzsq12
s_sleep 1
buffer_load_dword v4, v7, s[20:23], s52 offen offset:128 sc0 sc1
s_waitcnt vmcnt(0)
v_cmp_eq_u32_e32 vcc, s3, v4
s_andn2_b64 s[28:29], 1, vcc
s_cbranch_scc1 .Lzsp12
s_branch .Lzok12
.Lzsq12:
s_mov_b32 s53, 0
s_branch .Lzok12
.Lzsp13:
s_sub_u32 s53, s53, 1
s_cbranch_scc1 .Lzsq13
s_sleep 1
buffer_load_dword v4, v2, s[20:23], s85 offen sc0 sc1
s_waitcnt vmcnt(0)
v_xor_b32 v4, s3, v4
v_cmp_gt_u32_e32 vcc, 2, v4
s_andn2_b32 s28, 0xff, vcc_lo
s_cbranch_scc1 .Lzsp13
s_branch .Lzok13
.Lzsq13:
s_mov_b32 s53, 0
s_branch .Lzok13
.Lzsp14:
s_sub_u32 s53, s53, 1
s_cbranch_scc1 .Lzsq14
s_sleep 1
buffer_load_dword v4, v5, s[20:23], s50 offen offset:0 sc0 sc1
s_waitcnt vmcnt(0)
v_cmp_eq_u32_e32 vcc, s3, v4
s_andn2_b64 s[28:29], -1, vcc
s_cbranch_scc1 .Lzsp14
s_branch .Lzok14
.Lzsq14:
s_mov_b32 s53, 0
s_branch .Lzok14
.Lzsp15:
s_sub_u32 s53, s53, 1
s_cbranch_scc1 .Lzsq15
s_sleep 1
buffer_load_dword v4, v5, s[20:23], s50 offen offset:0 sc0 sc1
)" R"(
s_waitcnt vmcnt(0)
v_cmp_eq_u32_e32 vcc, s3, v4
s_andn2_b64 s[28:29], -1, vcc
s_cbranch_scc1 .Lzsp15
s_branch .Lzok15
.Lzsq15:
s_mov_b32 s53, 0
s_branch .Lzok15
.Lzsp16:
s_sub_u32 s53, s53, 1
s_cbranch_scc1 .Lzsq16
s_sleep 1
buffer_load_dword v4, v5, s[20:23], s50 offen offset:0 sc0 sc1
s_waitcnt vmcnt(0)
v_cmp_eq_u32_e32 vcc, s3, v4
s_andn2_b64 s[28:29], -1, vcc
s_cbranch_scc1 .Lzsp16
s_branch .Lzok16
.Lzsq16:
s_mov_b32 s53, 0
s_branch .Lzok16
.Lzsp17:
s_sub_u32 s53, s53, 1
s_cbranch_scc1 .Lzsq17
s_sleep 1
buffer_load_dword v4, v5, s[20:23], s50 offen offset:0 sc0 sc1
s_waitcnt vmcnt(0)
v_cmp_eq_u32_e32 vcc, s3, v4
s_andn2_b64 s[28:29], -1, vcc
s_cbranch_scc1 .Lzsp17
s_branch .Lzok17
.Lzsq17:
s_mov_b32 s53, 0
s_branch .Lzok17
.Lzsp18:
s_sub_u32 s53, s53, 1
s_cbranch_scc1 .Lzsq18
s_sleep 1
buffer_load_dword v4, v5, s[20:23], s50 offen offset:0 sc0 sc1
s_waitcnt vmcnt(0)
v_cmp_eq_u32_e32 vcc, s3, v4
s_andn2_b64 s[28:29], -1, vcc
s_cbranch_scc1 .Lzsp18
s_branch .Lzok18
.Lzsq18:
s_mov_b32 s53, 0
s_branch .Lzok18
.Lzfx6:
s_mov_b32 s88, 3
s_call_b64 s[30:31], .LzFIX0
s_branch .Lzfr6
.Lzsp19:
s_sub_u32 s53, s53, 1
s_cbranch_scc1 .Lzsq19
s_sleep 1
buffer_load_dword v4, v5, s[20:23], s50 offen offset:0 sc0 sc1
s_waitcnt vmcnt(0)
v_cmp_eq_u32_e32 vcc, s3, v4
s_andn2_b64 s[28:29], -1, vcc
s_cbranch_scc1 .Lzsp19
s_branch .Lzok19
.Lzsq19:
s_mov_b32 s53, 0
s_branch .Lzok19
)" R"(
.Lzfx7:
s_mov_b32 s88, 2
s_call_b64 s[30:31], .LzFIX1
s_branch .Lzfr7
.Lzmk6:
s_call_b64 s[30:31], .LzMASK0
s_branch .Lzmr6
.Lzfx8:
s_mov_b32 s88, 2
s_call_b64 s[30:31], .LzFIX0
s_branch .Lzfr8
.Lzmk7:
s_call_b64 s[30:31], .LzMASK1
s_branch .Lzmr7
.Lzsp20:
s_sub_u32 s53, s53, 1
s_cbranch_scc1 .Lzsq20
s_sleep 1
buffer_load_dword v4, v5, s[20:23], s50 offen offset:0 sc0 sc1
s_waitcnt vmcnt(0)
v_cmp_eq_u32_e32 vcc, s3, v4
s_andn2_b64 s[28:29], -1, vcc
s_cbranch_scc1 .Lzsp20
s_branch .Lzok20
.Lzsq20:
s_mov_b32 s53, 0
s_branch .Lzok20
.Lzfx9:
s_mov_b32 s88, 2
s_call_b64 s[30:31], .LzFIX1
s_branch .Lzfr9
.Lzmk8:
s_call_b64 s[30:31], .LzMASK0
s_branch .Lzmr8
.Lzfx10:
s_mov_b32 s88, 2
s_call_b64 s[30:31], .LzFIX0
s_branch .Lzfr10
.Lzmk9:
s_call_b64 s[30:31], .LzMASK1
s_branch .Lzmr9
.Lzsp21:
s_sub_u32 s53, s53, 1
s_cbranch_scc1 .Lzsq21
s_sleep 1
buffer_load_dword v4, v7, s[20:23], s83 offen offset:4 sc0 sc1
s_waitcnt vmcnt(0)
v_cmp_eq_u32_e32 vcc, s3, v4
s_andn2_b64 s[28:29], 1, vcc
s_cbranch_scc1 .Lzsp21
s_branch .Lzok21
.Lzsq21:
s_mov_b32 s53, 0
s_branch .Lzok21
.Lzsp22:
s_sub_u32 s53, s53, 1
s_cbranch_scc1 .Lzsq22
s_sleep 1
buffer_load_dword v4, v7, s[20:23], s65 offen offset:0 sc0 sc1
s_waitcnt vmcnt(0)
v_cmp_eq_u32_e32 vcc, s3, v4
s_andn2_b64 s[28:29], s[60:61], vcc
s_cbranch_scc1 .Lzsp22
s_branch .Lzok22
)" R"(
.Lzsq22:
s_mov_b32 s53, 0
s_branch .Lzok22
.Lzfx11:
s_mov_b32 s88, 2
s_call_b64 s[30:31], .LzFIX1
s_branch .Lzfr11
.Lzmk10:
s_call_b64 s[30:31], .LzMASK0
s_branch .Lzmr10
.Lzfx12:
s_mov_b32 s88, 2
s_call_b64 s[30:31], .LzFIX0
s_branch .Lzfr12
.Lzmk11:
s_call_b64 s[30:31], .LzMASK1
s_branch .Lzmr11
.Lzfx13:
s_mov_b32 s88, 0
s_call_b64 s[30:31], .LzFIX1
s_branch .Lzfr13
.LzMRG:
s_waitcnt vmcnt(0)
s_nop 7
v_mov_b32 v25, v64
v_mov_b32 v24, v25
s_nop 1
v_permlane32_swap_b32_e32 v25, v24
v_add_f32_e32 v25, v25, v24
v_mov_b32 v24, v25
s_nop 1
v_permlane16_swap_b32_e32 v25, v24
v_add_f32_e32 v25, v25, v24
v_cmp_lt_f32_e32 vcc, 0, v25
s_nop 1
v_cndmask_b32_e32 v66, v16, v66, vcc
v_sub_f32_e32 v28, v16, v66
v_min_f32_e32 v26, 0, v28
v_exp_f32_e32 v26, v26
v_max_f32_e32 v28, 0, v28
v_sub_f32_e32 v24, 0, v28
v_exp_f32_e32 v24, v24
v_mul_f32_e32 v64, v26, v64
v_mul_f32_e32 v12, v24, v12
v_mul_f32_e32 v128, v24, v128
v_mul_f32_e32 v129, v24, v129
v_mul_f32_e32 v130, v24, v130
v_mul_f32_e32 v131, v24, v131
v_mul_f32_e32 v132, v24, v132
v_mul_f32_e32 v133, v24, v133
v_mul_f32_e32 v134, v24, v134
v_mul_f32_e32 v135, v24, v135
v_mul_f32_e32 v144, v24, v144
v_mul_f32_e32 v145, v24, v145
v_mul_f32_e32 v146, v24, v146
v_mul_f32_e32 v147, v24, v147
v_mul_f32_e32 v148, v24, v148
v_mul_f32_e32 v149, v24, v149
v_mul_f32_e32 v150, v24, v150
v_mul_f32_e32 v151, v24, v151
v_mul_f32_e32 v160, v24, v160
v_mul_f32_e32 v161, v24, v161
v_mul_f32_e32 v162, v24, v162
v_mul_f32_e32 v163, v24, v163
)" R"(
v_mul_f32_e32 v164, v24, v164
v_mul_f32_e32 v165, v24, v165
v_mul_f32_e32 v166, v24, v166
v_mul_f32_e32 v167, v24, v167
v_mul_f32_e32 v176, v24, v176
v_mul_f32_e32 v177, v24, v177
v_mul_f32_e32 v178, v24, v178
v_mul_f32_e32 v179, v24, v179
v_mul_f32_e32 v180, v24, v180
v_mul_f32_e32 v181, v24, v181
v_mul_f32_e32 v182, v24, v182
v_mul_f32_e32 v183, v24, v183
v_mul_f32_e32 v192, v24, v192
v_mul_f32_e32 v193, v24, v193
v_mul_f32_e32 v194, v24, v194
v_mul_f32_e32 v195, v24, v195
v_mul_f32_e32 v196, v24, v196
v_mul_f32_e32 v197, v24, v197
v_mul_f32_e32 v198, v24, v198
v_mul_f32_e32 v199, v24, v199
v_mul_f32_e32 v208, v24, v208
v_mul_f32_e32 v209, v24, v209
v_mul_f32_e32 v210, v24, v210
v_mul_f32_e32 v211, v24, v211
v_mul_f32_e32 v212, v24, v212
v_mul_f32_e32 v213, v24, v213
v_mul_f32_e32 v214, v24, v214
v_mul_f32_e32 v215, v24, v215
v_mul_f32_e32 v224, v24, v224
v_mul_f32_e32 v225, v24, v225
v_mul_f32_e32 v226, v24, v226
v_mul_f32_e32 v227, v24, v227
v_mul_f32_e32 v228, v24, v228
v_mul_f32_e32 v229, v24, v229
v_mul_f32_e32 v230, v24, v230
v_mul_f32_e32 v231, v24, v231
v_mul_f32_e32 v240, v24, v240
v_mul_f32_e32 v241, v24, v241
v_mul_f32_e32 v242, v24, v242
v_mul_f32_e32 v243, v24, v243
v_mul_f32_e32 v244, v24, v244
v_mul_f32_e32 v245, v24, v245
v_mul_f32_e32 v246, v24, v246
v_mul_f32_e32 v247, v24, v247
v_sub_f32_e32 v16, v16, v28
v_sub_f32_e32 v17, v17, v28
v_sub_f32_e32 v18, v18, v28
v_sub_f32_e32 v19, v19, v28
v_sub_f32_e32 v96, v96, v28
v_sub_f32_e32 v97, v97, v28
v_sub_f32_e32 v98, v98, v28
v_sub_f32_e32 v99, v99, v28
v_sub_f32_e32 v104, v104, v28
v_sub_f32_e32 v105, v105, v28
v_sub_f32_e32 v106, v106, v28
v_sub_f32_e32 v107, v107, v28
v_sub_f32_e32 v112, v112, v28
v_sub_f32_e32 v113, v113, v28
v_sub_f32_e32 v114, v114, v28
v_sub_f32_e32 v115, v115, v28
v_sub_f32_e32 v120, v120, v28
v_sub_f32_e32 v121, v121, v28
v_sub_f32_e32 v122, v122, v28
v_sub_f32_e32 v123, v123, v28
)" R"(
v_accvgpr_read_b32 v25, a128
v_lshlrev_b32 v24, 16, v25
v_and_b32 v25, 0xffff0000, v25
v_mul_f32_e32 v24, v26, v24
v_mul_f32_e32 v25, v26, v25
Cv 24,24,25
v_accvgpr_write_b32 a128, v24
v_accvgpr_read_b32 v25, a129
v_lshlrev_b32 v24, 16, v25
v_and_b32 v25, 0xffff0000, v25
v_mul_f32_e32 v24, v26, v24
v_mul_f32_e32 v25, v26, v25
Cv 24,24,25
v_accvgpr_write_b32 a129, v24
v_accvgpr_read_b32 v25, a130
v_lshlrev_b32 v24, 16, v25
v_and_b32 v25, 0xffff0000, v25
v_mul_f32_e32 v24, v26, v24
v_mul_f32_e32 v25, v26, v25
Cv 24,24,25
v_accvgpr_write_b32 a130, v24
v_accvgpr_read_b32 v25, a131
v_lshlrev_b32 v24, 16, v25
v_and_b32 v25, 0xffff0000, v25
v_mul_f32_e32 v24, v26, v24
v_mul_f32_e32 v25, v26, v25
Cv 24,24,25
v_accvgpr_write_b32 a131, v24
v_accvgpr_read_b32 v25, a136
v_lshlrev_b32 v24, 16, v25
v_and_b32 v25, 0xffff0000, v25
v_mul_f32_e32 v24, v26, v24
v_mul_f32_e32 v25, v26, v25
Cv 24,24,25
v_accvgpr_write_b32 a136, v24
v_accvgpr_read_b32 v25, a137
v_lshlrev_b32 v24, 16, v25
v_and_b32 v25, 0xffff0000, v25
v_mul_f32_e32 v24, v26, v24
v_mul_f32_e32 v25, v26, v25
Cv 24,24,25
v_accvgpr_write_b32 a137, v24
v_accvgpr_read_b32 v25, a138
v_lshlrev_b32 v24, 16, v25
v_and_b32 v25, 0xffff0000, v25
v_mul_f32_e32 v24, v26, v24
v_mul_f32_e32 v25, v26, v25
Cv 24,24,25
v_accvgpr_write_b32 a138, v24
v_accvgpr_read_b32 v25, a139
v_lshlrev_b32 v24, 16, v25
v_and_b32 v25, 0xffff0000, v25
v_mul_f32_e32 v24, v26, v24
v_mul_f32_e32 v25, v26, v25
Cv 24,24,25
v_accvgpr_write_b32 a139, v24
v_accvgpr_read_b32 v25, a144
v_lshlrev_b32 v24, 16, v25
v_and_b32 v25, 0xffff0000, v25
v_mul_f32_e32 v24, v26, v24
v_mul_f32_e32 v25, v26, v25
Cv 24,24,25
v_accvgpr_write_b32 a144, v24
v_accvgpr_read_b32 v25, a145
)" R"(
v_lshlrev_b32 v24, 16, v25
v_and_b32 v25, 0xffff0000, v25
v_mul_f32_e32 v24, v26, v24
v_mul_f32_e32 v25, v26, v25
Cv 24,24,25
v_accvgpr_write_b32 a145, v24
v_accvgpr_read_b32 v25, a146
v_lshlrev_b32 v24, 16, v25
v_and_b32 v25, 0xffff0000, v25
v_mul_f32_e32 v24, v26, v24
v_mul_f32_e32 v25, v26, v25
Cv 24,24,25
v_accvgpr_write_b32 a146, v24
v_accvgpr_read_b32 v25, a147
v_lshlrev_b32 v24, 16, v25
v_and_b32 v25, 0xffff0000, v25
v_mul_f32_e32 v24, v26, v24
v_mul_f32_e32 v25, v26, v25
Cv 24,24,25
v_accvgpr_write_b32 a147, v24
v_accvgpr_read_b32 v25, a152
v_lshlrev_b32 v24, 16, v25
v_and_b32 v25, 0xffff0000, v25
v_mul_f32_e32 v24, v26, v24
v_mul_f32_e32 v25, v26, v25
Cv 24,24,25
v_accvgpr_write_b32 a152, v24
v_accvgpr_read_b32 v25, a153
v_lshlrev_b32 v24, 16, v25
v_and_b32 v25, 0xffff0000, v25
v_mul_f32_e32 v24, v26, v24
v_mul_f32_e32 v25, v26, v25
Cv 24,24,25
v_accvgpr_write_b32 a153, v24
v_accvgpr_read_b32 v25, a154
v_lshlrev_b32 v24, 16, v25
v_and_b32 v25, 0xffff0000, v25
v_mul_f32_e32 v24, v26, v24
v_mul_f32_e32 v25, v26, v25
Cv 24,24,25
v_accvgpr_write_b32 a154, v24
v_accvgpr_read_b32 v25, a155
v_lshlrev_b32 v24, 16, v25
v_and_b32 v25, 0xffff0000, v25
v_mul_f32_e32 v24, v26, v24
v_mul_f32_e32 v25, v26, v25
Cv 24,24,25
v_accvgpr_write_b32 a155, v24
v_accvgpr_read_b32 v25, a160
v_lshlrev_b32 v24, 16, v25
v_and_b32 v25, 0xffff0000, v25
v_mul_f32_e32 v24, v26, v24
v_mul_f32_e32 v25, v26, v25
Cv 24,24,25
v_accvgpr_write_b32 a160, v24
v_accvgpr_read_b32 v25, a161
v_lshlrev_b32 v24, 16, v25
v_and_b32 v25, 0xffff0000, v25
v_mul_f32_e32 v24, v26, v24
v_mul_f32_e32 v25, v26, v25
Cv 24,24,25
v_accvgpr_write_b32 a161, v24
v_accvgpr_read_b32 v25, a162
v_lshlrev_b32 v24, 16, v25
)" R"(
v_and_b32 v25, 0xffff0000, v25
v_mul_f32_e32 v24, v26, v24
v_mul_f32_e32 v25, v26, v25
Cv 24,24,25
v_accvgpr_write_b32 a162, v24
v_accvgpr_read_b32 v25, a163
v_lshlrev_b32 v24, 16, v25
v_and_b32 v25, 0xffff0000, v25
v_mul_f32_e32 v24, v26, v24
v_mul_f32_e32 v25, v26, v25
Cv 24,24,25
v_accvgpr_write_b32 a163, v24
v_accvgpr_read_b32 v25, a168
v_lshlrev_b32 v24, 16, v25
v_and_b32 v25, 0xffff0000, v25
v_mul_f32_e32 v24, v26, v24
v_mul_f32_e32 v25, v26, v25
Cv 24,24,25
v_accvgpr_write_b32 a168, v24
v_accvgpr_read_b32 v25, a169
v_lshlrev_b32 v24, 16, v25
v_and_b32 v25, 0xffff0000, v25
v_mul_f32_e32 v24, v26, v24
v_mul_f32_e32 v25, v26, v25
Cv 24,24,25
v_accvgpr_write_b32 a169, v24
v_accvgpr_read_b32 v25, a170
v_lshlrev_b32 v24, 16, v25
v_and_b32 v25, 0xffff0000, v25
v_mul_f32_e32 v24, v26, v24
v_mul_f32_e32 v25, v26, v25
Cv 24,24,25
v_accvgpr_write_b32 a170, v24
v_accvgpr_read_b32 v25, a171
v_lshlrev_b32 v24, 16, v25
v_and_b32 v25, 0xffff0000, v25
v_mul_f32_e32 v24, v26, v24
v_mul_f32_e32 v25, v26, v25
Cv 24,24,25
v_accvgpr_write_b32 a171, v24
v_accvgpr_read_b32 v25, a176
v_lshlrev_b32 v24, 16, v25
v_and_b32 v25, 0xffff0000, v25
v_mul_f32_e32 v24, v26, v24
v_mul_f32_e32 v25, v26, v25
Cv 24,24,25
v_accvgpr_write_b32 a176, v24
v_accvgpr_read_b32 v25, a177
v_lshlrev_b32 v24, 16, v25
v_and_b32 v25, 0xffff0000, v25
v_mul_f32_e32 v24, v26, v24
v_mul_f32_e32 v25, v26, v25
Cv 24,24,25
v_accvgpr_write_b32 a177, v24
v_accvgpr_read_b32 v25, a178
v_lshlrev_b32 v24, 16, v25
v_and_b32 v25, 0xffff0000, v25
v_mul_f32_e32 v24, v26, v24
v_mul_f32_e32 v25, v26, v25
Cv 24,24,25
v_accvgpr_write_b32 a178, v24
v_accvgpr_read_b32 v25, a179
v_lshlrev_b32 v24, 16, v25
v_and_b32 v25, 0xffff0000, v25
)" R"(
v_mul_f32_e32 v24, v26, v24
v_mul_f32_e32 v25, v26, v25
Cv 24,24,25
v_accvgpr_write_b32 a179, v24
v_accvgpr_read_b32 v25, a184
v_lshlrev_b32 v24, 16, v25
v_and_b32 v25, 0xffff0000, v25
v_mul_f32_e32 v24, v26, v24
v_mul_f32_e32 v25, v26, v25
Cv 24,24,25
v_accvgpr_write_b32 a184, v24
v_accvgpr_read_b32 v25, a185
v_lshlrev_b32 v24, 16, v25
v_and_b32 v25, 0xffff0000, v25
v_mul_f32_e32 v24, v26, v24
v_mul_f32_e32 v25, v26, v25
Cv 24,24,25
v_accvgpr_write_b32 a185, v24
v_accvgpr_read_b32 v25, a186
v_lshlrev_b32 v24, 16, v25
v_and_b32 v25, 0xffff0000, v25
v_mul_f32_e32 v24, v26, v24
v_mul_f32_e32 v25, v26, v25
Cv 24,24,25
v_accvgpr_write_b32 a186, v24
v_accvgpr_read_b32 v25, a187
v_lshlrev_b32 v24, 16, v25
v_and_b32 v25, 0xffff0000, v25
v_mul_f32_e32 v24, v26, v24
v_mul_f32_e32 v25, v26, v25
Cv 24,24,25
v_accvgpr_write_b32 a187, v24
v_mov_b32 v25, v65
v_mov_b32 v24, v25
s_nop 1
v_permlane32_swap_b32_e32 v25, v24
v_add_f32_e32 v25, v25, v24
v_mov_b32 v24, v25
s_nop 1
v_permlane16_swap_b32_e32 v25, v24
v_add_f32_e32 v25, v25, v24
v_cmp_lt_f32_e32 vcc, 0, v25
s_nop 1
v_cndmask_b32_e32 v67, v20, v67, vcc
v_sub_f32_e32 v29, v20, v67
v_min_f32_e32 v27, 0, v29
v_exp_f32_e32 v27, v27
v_max_f32_e32 v29, 0, v29
v_sub_f32_e32 v24, 0, v29
v_exp_f32_e32 v24, v24
v_mul_f32_e32 v65, v27, v65
v_mul_f32_e32 v13, v24, v13
v_mul_f32_e32 v136, v24, v136
v_mul_f32_e32 v137, v24, v137
v_mul_f32_e32 v138, v24, v138
v_mul_f32_e32 v139, v24, v139
v_mul_f32_e32 v140, v24, v140
v_mul_f32_e32 v141, v24, v141
v_mul_f32_e32 v142, v24, v142
v_mul_f32_e32 v143, v24, v143
v_mul_f32_e32 v152, v24, v152
v_mul_f32_e32 v153, v24, v153
v_mul_f32_e32 v154, v24, v154
v_mul_f32_e32 v155, v24, v155
)" R"(
v_mul_f32_e32 v156, v24, v156
v_mul_f32_e32 v157, v24, v157
v_mul_f32_e32 v158, v24, v158
v_mul_f32_e32 v159, v24, v159
v_mul_f32_e32 v168, v24, v168
v_mul_f32_e32 v169, v24, v169
v_mul_f32_e32 v170, v24, v170
v_mul_f32_e32 v171, v24, v171
v_mul_f32_e32 v172, v24, v172
v_mul_f32_e32 v173, v24, v173
v_mul_f32_e32 v174, v24, v174
v_mul_f32_e32 v175, v24, v175
v_mul_f32_e32 v184, v24, v184
v_mul_f32_e32 v185, v24, v185
v_mul_f32_e32 v186, v24, v186
v_mul_f32_e32 v187, v24, v187
v_mul_f32_e32 v188, v24, v188
v_mul_f32_e32 v189, v24, v189
v_mul_f32_e32 v190, v24, v190
v_mul_f32_e32 v191, v24, v191
v_mul_f32_e32 v200, v24, v200
v_mul_f32_e32 v201, v24, v201
v_mul_f32_e32 v202, v24, v202
v_mul_f32_e32 v203, v24, v203
v_mul_f32_e32 v204, v24, v204
v_mul_f32_e32 v205, v24, v205
v_mul_f32_e32 v206, v24, v206
v_mul_f32_e32 v207, v24, v207
v_mul_f32_e32 v216, v24, v216
v_mul_f32_e32 v217, v24, v217
v_mul_f32_e32 v218, v24, v218
v_mul_f32_e32 v219, v24, v219
v_mul_f32_e32 v220, v24, v220
v_mul_f32_e32 v221, v24, v221
v_mul_f32_e32 v222, v24, v222
v_mul_f32_e32 v223, v24, v223
v_mul_f32_e32 v232, v24, v232
v_mul_f32_e32 v233, v24, v233
v_mul_f32_e32 v234, v24, v234
v_mul_f32_e32 v235, v24, v235
v_mul_f32_e32 v236, v24, v236
v_mul_f32_e32 v237, v24, v237
v_mul_f32_e32 v238, v24, v238
v_mul_f32_e32 v239, v24, v239
v_mul_f32_e32 v248, v24, v248
v_mul_f32_e32 v249, v24, v249
v_mul_f32_e32 v250, v24, v250
v_mul_f32_e32 v251, v24, v251
v_mul_f32_e32 v252, v24, v252
v_mul_f32_e32 v253, v24, v253
v_mul_f32_e32 v254, v24, v254
v_mul_f32_e32 v255, v24, v255
v_sub_f32_e32 v20, v20, v29
v_sub_f32_e32 v21, v21, v29
v_sub_f32_e32 v22, v22, v29
v_sub_f32_e32 v23, v23, v29
v_sub_f32_e32 v100, v100, v29
v_sub_f32_e32 v101, v101, v29
v_sub_f32_e32 v102, v102, v29
v_sub_f32_e32 v103, v103, v29
v_sub_f32_e32 v108, v108, v29
v_sub_f32_e32 v109, v109, v29
v_sub_f32_e32 v110, v110, v29
v_sub_f32_e32 v111, v111, v29
)" R"(
v_sub_f32_e32 v116, v116, v29
v_sub_f32_e32 v117, v117, v29
v_sub_f32_e32 v118, v118, v29
v_sub_f32_e32 v119, v119, v29
v_sub_f32_e32 v124, v124, v29
v_sub_f32_e32 v125, v125, v29
v_sub_f32_e32 v126, v126, v29
v_sub_f32_e32 v127, v127, v29
v_accvgpr_read_b32 v25, a132
v_lshlrev_b32 v24, 16, v25
v_and_b32 v25, 0xffff0000, v25
v_mul_f32_e32 v24, v27, v24
v_mul_f32_e32 v25, v27, v25
Cv 24,24,25
v_accvgpr_write_b32 a132, v24
v_accvgpr_read_b32 v25, a133
v_lshlrev_b32 v24, 16, v25
v_and_b32 v25, 0xffff0000, v25
v_mul_f32_e32 v24, v27, v24
v_mul_f32_e32 v25, v27, v25
Cv 24,24,25
v_accvgpr_write_b32 a133, v24
v_accvgpr_read_b32 v25, a134
v_lshlrev_b32 v24, 16, v25
v_and_b32 v25, 0xffff0000, v25
v_mul_f32_e32 v24, v27, v24
v_mul_f32_e32 v25, v27, v25
Cv 24,24,25
v_accvgpr_write_b32 a134, v24
v_accvgpr_read_b32 v25, a135
v_lshlrev_b32 v24, 16, v25
v_and_b32 v25, 0xffff0000, v25
v_mul_f32_e32 v24, v27, v24
v_mul_f32_e32 v25, v27, v25
Cv 24,24,25
v_accvgpr_write_b32 a135, v24
v_accvgpr_read_b32 v25, a140
v_lshlrev_b32 v24, 16, v25
v_and_b32 v25, 0xffff0000, v25
v_mul_f32_e32 v24, v27, v24
v_mul_f32_e32 v25, v27, v25
Cv 24,24,25
v_accvgpr_write_b32 a140, v24
v_accvgpr_read_b32 v25, a141
v_lshlrev_b32 v24, 16, v25
v_and_b32 v25, 0xffff0000, v25
v_mul_f32_e32 v24, v27, v24
v_mul_f32_e32 v25, v27, v25
Cv 24,24,25
v_accvgpr_write_b32 a141, v24
v_accvgpr_read_b32 v25, a142
v_lshlrev_b32 v24, 16, v25
v_and_b32 v25, 0xffff0000, v25
v_mul_f32_e32 v24, v27, v24
v_mul_f32_e32 v25, v27, v25
Cv 24,24,25
v_accvgpr_write_b32 a142, v24
v_accvgpr_read_b32 v25, a143
v_lshlrev_b32 v24, 16, v25
v_and_b32 v25, 0xffff0000, v25
v_mul_f32_e32 v24, v27, v24
v_mul_f32_e32 v25, v27, v25
Cv 24,24,25
v_accvgpr_write_b32 a143, v24
)" R"(
v_accvgpr_read_b32 v25, a148
v_lshlrev_b32 v24, 16, v25
v_and_b32 v25, 0xffff0000, v25
v_mul_f32_e32 v24, v27, v24
v_mul_f32_e32 v25, v27, v25
Cv 24,24,25
v_accvgpr_write_b32 a148, v24
v_accvgpr_read_b32 v25, a149
v_lshlrev_b32 v24, 16, v25
v_and_b32 v25, 0xffff0000, v25
v_mul_f32_e32 v24, v27, v24
v_mul_f32_e32 v25, v27, v25
Cv 24,24,25
v_accvgpr_write_b32 a149, v24
v_accvgpr_read_b32 v25, a150
v_lshlrev_b32 v24, 16, v25
v_and_b32 v25, 0xffff0000, v25
v_mul_f32_e32 v24, v27, v24
v_mul_f32_e32 v25, v27, v25
Cv 24,24,25
v_accvgpr_write_b32 a150, v24
v_accvgpr_read_b32 v25, a151
v_lshlrev_b32 v24, 16, v25
v_and_b32 v25, 0xffff0000, v25
v_mul_f32_e32 v24, v27, v24
v_mul_f32_e32 v25, v27, v25
Cv 24,24,25
v_accvgpr_write_b32 a151, v24
v_accvgpr_read_b32 v25, a156
v_lshlrev_b32 v24, 16, v25
v_and_b32 v25, 0xffff0000, v25
v_mul_f32_e32 v24, v27, v24
v_mul_f32_e32 v25, v27, v25
Cv 24,24,25
v_accvgpr_write_b32 a156, v24
v_accvgpr_read_b32 v25, a157
v_lshlrev_b32 v24, 16, v25
v_and_b32 v25, 0xffff0000, v25
v_mul_f32_e32 v24, v27, v24
v_mul_f32_e32 v25, v27, v25
Cv 24,24,25
v_accvgpr_write_b32 a157, v24
v_accvgpr_read_b32 v25, a158
v_lshlrev_b32 v24, 16, v25
v_and_b32 v25, 0xffff0000, v25
v_mul_f32_e32 v24, v27, v24
v_mul_f32_e32 v25, v27, v25
Cv 24,24,25
v_accvgpr_write_b32 a158, v24
v_accvgpr_read_b32 v25, a159
v_lshlrev_b32 v24, 16, v25
v_and_b32 v25, 0xffff0000, v25
v_mul_f32_e32 v24, v27, v24
v_mul_f32_e32 v25, v27, v25
Cv 24,24,25
v_accvgpr_write_b32 a159, v24
v_accvgpr_read_b32 v25, a164
v_lshlrev_b32 v24, 16, v25
v_and_b32 v25, 0xffff0000, v25
v_mul_f32_e32 v24, v27, v24
v_mul_f32_e32 v25, v27, v25
Cv 24,24,25
v_accvgpr_write_b32 a164, v24
v_accvgpr_read_b32 v25, a165
)" R"(
v_lshlrev_b32 v24, 16, v25
v_and_b32 v25, 0xffff0000, v25
v_mul_f32_e32 v24, v27, v24
v_mul_f32_e32 v25, v27, v25
Cv 24,24,25
v_accvgpr_write_b32 a165, v24
v_accvgpr_read_b32 v25, a166
v_lshlrev_b32 v24, 16, v25
v_and_b32 v25, 0xffff0000, v25
v_mul_f32_e32 v24, v27, v24
v_mul_f32_e32 v25, v27, v25
Cv 24,24,25
v_accvgpr_write_b32 a166, v24
v_accvgpr_read_b32 v25, a167
v_lshlrev_b32 v24, 16, v25
v_and_b32 v25, 0xffff0000, v25
v_mul_f32_e32 v24, v27, v24
v_mul_f32_e32 v25, v27, v25
Cv 24,24,25
v_accvgpr_write_b32 a167, v24
v_accvgpr_read_b32 v25, a172
v_lshlrev_b32 v24, 16, v25
v_and_b32 v25, 0xffff0000, v25
v_mul_f32_e32 v24, v27, v24
v_mul_f32_e32 v25, v27, v25
Cv 24,24,25
v_accvgpr_write_b32 a172, v24
v_accvgpr_read_b32 v25, a173
v_lshlrev_b32 v24, 16, v25
v_and_b32 v25, 0xffff0000, v25
v_mul_f32_e32 v24, v27, v24
v_mul_f32_e32 v25, v27, v25
Cv 24,24,25
v_accvgpr_write_b32 a173, v24
v_accvgpr_read_b32 v25, a174
v_lshlrev_b32 v24, 16, v25
v_and_b32 v25, 0xffff0000, v25
v_mul_f32_e32 v24, v27, v24
v_mul_f32_e32 v25, v27, v25
Cv 24,24,25
v_accvgpr_write_b32 a174, v24
v_accvgpr_read_b32 v25, a175
v_lshlrev_b32 v24, 16, v25
v_and_b32 v25, 0xffff0000, v25
v_mul_f32_e32 v24, v27, v24
v_mul_f32_e32 v25, v27, v25
Cv 24,24,25
v_accvgpr_write_b32 a175, v24
v_accvgpr_read_b32 v25, a180
v_lshlrev_b32 v24, 16, v25
v_and_b32 v25, 0xffff0000, v25
v_mul_f32_e32 v24, v27, v24
v_mul_f32_e32 v25, v27, v25
Cv 24,24,25
v_accvgpr_write_b32 a180, v24
v_accvgpr_read_b32 v25, a181
v_lshlrev_b32 v24, 16, v25
v_and_b32 v25, 0xffff0000, v25
v_mul_f32_e32 v24, v27, v24
v_mul_f32_e32 v25, v27, v25
Cv 24,24,25
v_accvgpr_write_b32 a181, v24
v_accvgpr_read_b32 v25, a182
v_lshlrev_b32 v24, 16, v25
)" R"(
v_and_b32 v25, 0xffff0000, v25
v_mul_f32_e32 v24, v27, v24
v_mul_f32_e32 v25, v27, v25
Cv 24,24,25
v_accvgpr_write_b32 a182, v24
v_accvgpr_read_b32 v25, a183
v_lshlrev_b32 v24, 16, v25
v_and_b32 v25, 0xffff0000, v25
v_mul_f32_e32 v24, v27, v24
v_mul_f32_e32 v25, v27, v25
Cv 24,24,25
v_accvgpr_write_b32 a183, v24
v_accvgpr_read_b32 v25, a188
v_lshlrev_b32 v24, 16, v25
v_and_b32 v25, 0xffff0000, v25
v_mul_f32_e32 v24, v27, v24
v_mul_f32_e32 v25, v27, v25
Cv 24,24,25
v_accvgpr_write_b32 a188, v24
v_accvgpr_read_b32 v25, a189
v_lshlrev_b32 v24, 16, v25
v_and_b32 v25, 0xffff0000, v25
v_mul_f32_e32 v24, v27, v24
v_mul_f32_e32 v25, v27, v25
Cv 24,24,25
v_accvgpr_write_b32 a189, v24
v_accvgpr_read_b32 v25, a190
v_lshlrev_b32 v24, 16, v25
v_and_b32 v25, 0xffff0000, v25
v_mul_f32_e32 v24, v27, v24
v_mul_f32_e32 v25, v27, v25
Cv 24,24,25
v_accvgpr_write_b32 a190, v24
v_accvgpr_read_b32 v25, a191
v_lshlrev_b32 v24, 16, v25
v_and_b32 v25, 0xffff0000, v25
v_mul_f32_e32 v24, v27, v24
v_mul_f32_e32 v25, v27, v25
Cv 24,24,25
v_accvgpr_write_b32 a191, v24
v_exp_f32_e32 v48, v96
v_exp_f32_e32 v24, v97
s_nop 0
v_add_f32_e32 v14, v48, v24
Cv 48,48,24
v_exp_f32_e32 v49, v98
v_exp_f32_e32 v25, v99
v_add_f32_e32 v14, v14, v49
v_add_f32_e32 v14, v14, v25
Cv 49,49,25
v_exp_f32_e32 v50, v104
v_exp_f32_e32 v26, v105
v_add_f32_e32 v14, v14, v50
v_add_f32_e32 v14, v14, v26
Cv 50,50,26
v_exp_f32_e32 v51, v106
v_exp_f32_e32 v27, v107
v_add_f32_e32 v14, v14, v51
v_add_f32_e32 v14, v14, v27
Cv 51,51,27
v_exp_f32_e32 v52, v100
v_exp_f32_e32 v28, v101
s_nop 0
v_add_f32_e32 v15, v52, v28
)" R"(
Cv 52,52,28
v_exp_f32_e32 v53, v102
v_exp_f32_e32 v29, v103
v_add_f32_e32 v15, v15, v53
v_add_f32_e32 v15, v15, v29
Cv 53,53,29
v_exp_f32_e32 v54, v108
v_exp_f32_e32 v24, v109
v_add_f32_e32 v15, v15, v54
v_add_f32_e32 v15, v15, v24
Cv 54,54,24
v_exp_f32_e32 v55, v110
v_exp_f32_e32 v25, v111
v_add_f32_e32 v15, v15, v55
v_add_f32_e32 v15, v15, v25
Cv 55,55,25
s_nop 1
s_branch .LzMRR
.Lzsp23:
s_sub_u32 s53, s53, 1
s_cbranch_scc1 .Lzsq23
s_sleep 1
buffer_load_dword v4, v7, s[20:23], s52 offen offset:0 sc0 sc1
s_waitcnt vmcnt(0)
v_cmp_eq_u32_e32 vcc, s3, v4
s_andn2_b64 s[28:29], 1, vcc
s_cbranch_scc1 .Lzsp23
s_branch .Lzok23
.Lzsq23:
s_mov_b32 s53, 0
s_branch .Lzok23
.Lzsp24:
s_sub_u32 s53, s53, 1
s_cbranch_scc1 .Lzsq24
s_sleep 1
buffer_load_dword v4, v7, s[20:23], s52 offen offset:128 sc0 sc1
s_waitcnt vmcnt(0)
v_cmp_eq_u32_e32 vcc, s3, v4
s_andn2_b64 s[28:29], 1, vcc
s_cbranch_scc1 .Lzsp24
s_branch .Lzok24
.Lzsq24:
s_mov_b32 s53, 0
s_branch .Lzok24
.LzBB:
v_lshlrev_b32 v95, 7, v0
s_mov_b32 s28, 0xc10c000
s_add_u32 s29, s28, 6144
.LzBB1:
buffer_load_dword v92, v95, s[20:23], s28 offen sc0 sc1
buffer_load_dword v93, v95, s[20:23], s29 offen sc0 sc1
s_waitcnt vmcnt(0)
v_cmp_ne_u32_e32 vcc, s3, v92
v_cmp_ne_u32_e64 s[30:31], s3, v93
s_or_b64 vcc, vcc, s[30:31]
s_cbranch_scc0 .LzBB2
s_sub_u32 s53, s53, 1
s_cbranch_scc0 .LzBB1
s_mov_b32 s53, 0
.LzBB2:
s_mul_i32 s28, s56, 7168
s_add_u32 s28, s28, 0xc158000
s_add_u32 s29, s28, 4096
buffer_load_dwordx4 v[64:67], v1, s[20:23], s28 offen offset:0 sc0 sc1
)" R"(
buffer_load_dwordx4 v[68:71], v1, s[20:23], s28 offen offset:1024 sc0 sc1
buffer_load_dwordx4 v[72:75], v1, s[20:23], s28 offen offset:2048 sc0 sc1
buffer_load_dwordx4 v[76:79], v1, s[20:23], s28 offen offset:3072 sc0 sc1
buffer_load_dwordx4 v[80:83], v1, s[20:23], s29 offen offset:0 sc0 sc1
buffer_load_dwordx4 v[84:87], v1, s[20:23], s29 offen offset:1024 sc0 sc1
buffer_load_dwordx4 v[88:91], v1, s[20:23], s29 offen offset:2048 sc0 sc1
s_cmp_gt_u32 s56, 27
s_cselect_b32 s28, 16, 0
s_cselect_b32 s29, 28, 0
s_lshr_b32 s28, s63, s28
s_and_b32 s28, s28, 0x7ff
s_sub_u32 s29, s56, s29
s_lshl_b32 s29, s29, 5
s_mul_i32 s83, s28, 896
s_add_u32 s29, s29, s83
v_and_b32 v95, 3, v0
v_lshlrev_b32 v95, 3, v95
buffer_load_dwordx2 v[92:93], v95, s[12:15], s29 offen
s_lshl4_add_u32 s28, s28, 0xc10000c
v_mov_b32 v94, 0
buffer_load_dword v94, v94, s[20:23], s28 offen sc0 sc1
s_waitcnt vmcnt(0)
v_add_f32_e32 v64, v64, v68
v_add_f32_e32 v65, v65, v69
v_add_f32_e32 v66, v66, v70
v_add_f32_e32 v67, v67, v71
v_add_f32_e32 v72, v72, v76
v_add_f32_e32 v73, v73, v77
v_add_f32_e32 v74, v74, v78
v_add_f32_e32 v75, v75, v79
v_add_f32_e32 v80, v80, v84
v_add_f32_e32 v81, v81, v85
v_add_f32_e32 v82, v82, v86
v_add_f32_e32 v83, v83, v87
v_add_f32_e32 v64, v64, v72
v_add_f32_e32 v65, v65, v73
v_add_f32_e32 v66, v66, v74
v_add_f32_e32 v67, v67, v75
v_add_f32_e32 v80, v80, v88
v_add_f32_e32 v81, v81, v89
v_add_f32_e32 v82, v82, v90
v_add_f32_e32 v83, v83, v91
v_add_f32_e32 v64, v64, v80
v_add_f32_e32 v65, v65, v81
v_add_f32_e32 v66, v66, v82
v_add_f32_e32 v67, v67, v83
v_mov_b32 v68, v64
s_nop 1
v_permlane32_swap_b32_e32 v64, v68
v_add_f32_e32 v64, v64, v68
v_mov_b32 v68, v64
s_nop 1
v_permlane16_swap_b32_e32 v64, v68
v_add_f32_e32 v64, v64, v68
v_mov_b32 v68, v65
s_nop 1
v_permlane32_swap_b32_e32 v65, v68
v_add_f32_e32 v65, v65, v68
v_mov_b32 v68, v65
s_nop 1
v_permlane16_swap_b32_e32 v65, v68
v_add_f32_e32 v65, v65, v68
v_mov_b32 v68, v66
s_nop 1
)" R"(
v_permlane32_swap_b32_e32 v66, v68
v_add_f32_e32 v66, v66, v68
v_mov_b32 v68, v66
s_nop 1
v_permlane16_swap_b32_e32 v66, v68
v_add_f32_e32 v66, v66, v68
v_mov_b32 v68, v67
s_nop 1
v_permlane32_swap_b32_e32 v67, v68
v_add_f32_e32 v67, v67, v68
v_mov_b32 v68, v67
s_nop 1
v_permlane16_swap_b32_e32 v67, v68
v_add_f32_e32 v67, v67, v68
v_add_f32_dpp v64, v64, v64 row_ror:8 row_mask:0xf bank_mask:0xf
v_add_f32_dpp v65, v65, v65 row_ror:8 row_mask:0xf bank_mask:0xf
v_add_f32_dpp v66, v66, v66 row_ror:8 row_mask:0xf bank_mask:0xf
v_add_f32_dpp v67, v67, v67 row_ror:8 row_mask:0xf bank_mask:0xf
v_add_f32_dpp v64, v64, v64 row_ror:4 row_mask:0xf bank_mask:0xf
v_add_f32_dpp v65, v65, v65 row_ror:4 row_mask:0xf bank_mask:0xf
v_add_f32_dpp v66, v66, v66 row_ror:4 row_mask:0xf bank_mask:0xf
v_add_f32_dpp v67, v67, v67 row_ror:4 row_mask:0xf bank_mask:0xf
v_mul_f32_e32 v94, 0xbfb8aa3b, v94
v_exp_f32_e32 v94, v94
Cv 65,64,65
v_lshlrev_b32 v64, 16, v65
v_and_b32 v65, 0xffff0000, v65
v_add_f32_e32 v94, 1.0, v94
v_rcp_f32_e32 v94, v94
Cv 67,66,67
v_lshlrev_b32 v66, 16, v67
v_and_b32 v67, 0xffff0000, v67
Cv 94,94,94
v_lshlrev_b32 v94, 16, v94
v_mul_f32_e32 v64, v94, v64
v_mul_f32_e32 v65, v94, v65
v_mul_f32_e32 v66, v94, v66
v_mul_f32_e32 v67, v94, v67
Cv 65,64,65
v_lshlrev_b32 v64, 16, v65
v_and_b32 v65, 0xffff0000, v65
Cv 67,66,67
v_lshlrev_b32 v66, 16, v67
v_and_b32 v67, 0xffff0000, v67
v_lshlrev_b32 v68, 16, v92
v_and_b32 v69, 0xffff0000, v92
v_lshlrev_b32 v70, 16, v93
v_and_b32 v71, 0xffff0000, v93
v_add_f32_e32 v64, v64, v68
v_add_f32_e32 v65, v65, v69
v_add_f32_e32 v66, v66, v70
v_add_f32_e32 v67, v67, v71
Cv 64,64,65
Cv 66,66,67
v_mov_b32 v65, s3
v_mov_b32 v67, s3
v_lshrrev_b32 v95, 2, v0
v_min_u32 v95, 1, v95
v_lshl_or_b32 v95, v95, 30, v1
s_lshl_b32 s28, s56, 7
s_add_u32 s28, s28, 0xc109000
buffer_store_dwordx4 v[64:67], v95, s[20:23], s28 offen sc0 sc1
buffer_load_dword v4, v165, s[20:23], s40 offen sc0 sc1
s_waitcnt vmcnt(0)
)" R"(
s_branch .LzBBR
.LzPRB:
s_lshl_b32 s24, s34, 21
s_mul_i32 s25, s33, 2304
s_add_u32 s24, s24, s25
s_add_u32 s24, s24, 0x6100000
s_and_b32 s25, s36, 0x2000
s_xor_b32 s24, s24, s25
s_xor_b32 s25, s24, 0x2000
buffer_store_dword v0, v7, s[20:23], s24 offen sc0 sc1
buffer_store_dword v0, v7, s[20:23], s25 offen sc0 sc1
s_mov_b32 s26, -1
s_mov_b32 s27, -1
s_waitcnt vmcnt(0)
s_memtime s[28:29]
s_waitcnt lgkmcnt(0)
buffer_store_dword v0, v7, s[20:23], s24 offen sc0 sc1
s_waitcnt vmcnt(0)
s_memtime s[30:31]
s_waitcnt lgkmcnt(0)
s_sub_u32 s30, s30, s28
s_min_u32 s26, s26, s30
s_memtime s[28:29]
s_waitcnt lgkmcnt(0)
buffer_store_dword v0, v7, s[20:23], s25 offen sc0 sc1
s_waitcnt vmcnt(0)
s_memtime s[30:31]
s_waitcnt lgkmcnt(0)
s_sub_u32 s30, s30, s28
s_min_u32 s27, s27, s30
s_memtime s[28:29]
s_waitcnt lgkmcnt(0)
buffer_store_dword v0, v7, s[20:23], s24 offen sc0 sc1
s_waitcnt vmcnt(0)
s_memtime s[30:31]
s_waitcnt lgkmcnt(0)
s_sub_u32 s30, s30, s28
s_min_u32 s26, s26, s30
s_memtime s[28:29]
s_waitcnt lgkmcnt(0)
buffer_store_dword v0, v7, s[20:23], s25 offen sc0 sc1
s_waitcnt vmcnt(0)
s_memtime s[30:31]
s_waitcnt lgkmcnt(0)
s_sub_u32 s30, s30, s28
s_min_u32 s27, s27, s30
s_bitcmp1_b32 s33, 0
s_cselect_b32 s28, s27, s26
s_cselect_b32 s29, s26, s27
v_mov_b32 v32, s28
v_mov_b32 v33, s29
s_lshl_b32 s24, s33, 3
v_mov_b32 v34, s24
v_mov_b32 v35, 0
ds_write_b64 v34, v[32:33]
s_waitcnt lgkmcnt(0)
s_barrier
ds_read_b128 v[36:39], v35
ds_read_b128 v[40:43], v35 offset:16
s_mul_i32 s24, s3, 1664525
s_add_u32 s24, s24, 1013904223
s_and_b32 s24, s24, -2
s_lshl_b32 s25, s34, 6
s_add_u32 s25, s25, 0xc108800
)" R"(
s_cmp_lg_u32 s33, 0
s_cmov_b32 s25, 0x40000000
s_waitcnt lgkmcnt(0)
v_min3_u32 v32, v36, v38, v40
v_min3_u32 v33, v37, v39, v41
v_min_u32 v32, v32, v42
v_min_u32 v33, v33, v43
v_cmp_gt_u32_e32 vcc, v32, v33
v_mov_b32 v34, s24
s_nop 0
v_addc_co_u32_e32 v34, vcc, 0, v34, vcc
buffer_store_dword v34, v7, s[20:23], s25 offen sc0 sc1
s_barrier
s_branch .LzPRR
.LzA0:
s_mov_b32 s63, 0x6040200
s_mov_b32 s28, 0x7050312
s_bitcmp1_b32 s2, 5
s_cmov_b32 s63, 0x18161401
s_cmov_b32 s28, 0x19171513
s_bitcmp1_b32 s2, 6
s_cmov_b32 s63, s28
s_lshl_b32 s28, s33, 3
s_lshr_b32 s28, s63, s28
s_and_b32 s28, s28, 255
s_lshl_b32 s38, s28, 17
s_add_u32 s38, s38, 0xff80
s_lshl_b32 s39, s33, 16
s_lshl_b32 s28, s34, 9
s_add_u32 s39, s39, s28
s_sub_u32 s39, s39, 0x40000
s_lshl_b32 s37, s33, 12
s_add_u32 s37, s37, 0x10000
v_lshrrev_b32 v28, 4, v0
v_and_b32 v29, 7, v0
v_xor_b32 v28, v28, v29
v_lshlrev_b32 v28, 4, v28
v_lshrrev_b32 v29, 3, v0
v_lshl_or_b32 v8, v29, 11, v28
v_xor_b32 v9, 64, v8
v_add_u32 v9, 15360, v9
v_add_u32 v10, 30720, v8
v_add_u32 v11, 30720, v9
s_and_b32 s28, s33, 1
s_lshl_b32 s28, s28, 7
v_lshrrev_b32 v29, 5, v0
v_mul_u32_u24 v29, 0x2220, v29
v_xor_b32 v29, v29, v1
v_xor_b32 v12, s28, v29
v_xor_b32 v13, 64, v12
v_add_u32 v13, 15360, v13
v_add_u32 v14, 30720, v12
v_add_u32 v15, 30720, v13
v_min_u32 v7, 1, v0
v_lshlrev_b32 v7, 30, v7
s_waitcnt lgkmcnt(0)
s_mov_b32 s28, 0xc108400
s_load_dword s3, s[74:75], s28
s_mov_b32 s20, s74
s_mov_b32 s21, s75
s_mov_b32 s22, 0xc1ba000
s_mov_b32 s23, 0x20000
s_mov_b32 s4, s66
s_mov_b32 s5, s67
)" R"(
s_mov_b32 s6, 0x400000
s_mov_b32 s7, 0x20000
s_cmp_eq_u32 s91, 1
s_cselect_b32 s8, s72, s70
s_cselect_b32 s9, s73, s71
s_mov_b32 s10, 0x800000
s_mov_b32 s11, 0x20000
s_lshr_b32 s28, s2, 3
s_cmp_eq_u32 s28, 14
s_cbranch_scc1 .LzPRB
.LzPRR:
s_add_u32 m0, s37, 0
s_add_u32 s39, s39, 0x40000
buffer_load_dwordx4 v12, s[8:11], s39 offen offset:0 lds
buffer_load_dwordx4 v13, s[8:11], s39 offen offset:1024 lds
buffer_load_dwordx4 v14, s[8:11], s39 offen offset:2048 lds
buffer_load_dwordx4 v15, s[8:11], s39 offen offset:3072 lds
s_add_u32 m0, s36, 0
s_add_u32 s38, s38, 0xffff0080
buffer_load_dwordx4 v8, s[4:7], s38 offen offset:0 lds
buffer_load_dwordx4 v9, s[4:7], s38 offen offset:1024 lds
buffer_load_dwordx4 v10, s[4:7], s38 offen offset:2048 lds
buffer_load_dwordx4 v11, s[4:7], s38 offen offset:3072 lds
s_add_u32 m0, s36, 4096
s_add_u32 s38, s38, 0x10000
buffer_load_dwordx4 v8, s[4:7], s38 offen offset:0 lds
buffer_load_dwordx4 v9, s[4:7], s38 offen offset:1024 lds
buffer_load_dwordx4 v10, s[4:7], s38 offen offset:2048 lds
buffer_load_dwordx4 v11, s[4:7], s38 offen offset:3072 lds
s_add_u32 m0, s37, 16384
s_add_u32 s39, s39, 0x40000
buffer_load_dwordx4 v12, s[8:11], s39 offen offset:0 lds
buffer_load_dwordx4 v13, s[8:11], s39 offen offset:1024 lds
buffer_load_dwordx4 v14, s[8:11], s39 offen offset:2048 lds
buffer_load_dwordx4 v15, s[8:11], s39 offen offset:3072 lds
s_mov_b32 s12, s64
s_mov_b32 s13, s65
s_mov_b32 s14, 0x1c0000
s_mov_b32 s15, 0x20000
s_and_b32 s94, s2, 127
s_lshl2_add_u32 s94, s94, s33
s_lshl_b32 s94, s94, 2
s_mul_i32 s29, s94, 896
v_mov_b32 v120, 0x40000000
v_cmp_gt_u32_e32 vcc, 56, v0
s_nop 1
v_cndmask_b32_e32 v120, v120, v1, vcc
buffer_load_dwordx4 v[96:99], v120, s[12:15], s29 offen offset:0
buffer_load_dwordx4 v[100:103], v120, s[12:15], s29 offen offset:896
buffer_load_dwordx4 v[104:107], v120, s[12:15], s29 offen offset:1792
buffer_load_dwordx4 v[108:111], v120, s[12:15], s29 offen offset:2688
s_mov_b32 s24, s76
s_mov_b32 s25, s77
s_mov_b32 s26, 0x380
s_mov_b32 s27, 0x20000
buffer_load_dwordx4 v[112:115], v120, s[24:27], 0 offen
s_mov_b32 s24, s78
s_mov_b32 s25, s79
s_mov_b32 s26, 0x380
s_mov_b32 s27, 0x20000
buffer_load_dwordx4 v[116:119], v120, s[24:27], 0 offen
s_add_u32 m0, s36, 32768
s_add_u32 s38, s38, 0xffff0080
buffer_load_dwordx4 v8, s[4:7], s38 offen offset:0 lds
)" R"(
buffer_load_dwordx4 v9, s[4:7], s38 offen offset:1024 lds
buffer_load_dwordx4 v10, s[4:7], s38 offen offset:2048 lds
buffer_load_dwordx4 v11, s[4:7], s38 offen offset:3072 lds
s_add_u32 m0, s36, 36864
s_add_u32 s38, s38, 0x10000
buffer_load_dwordx4 v8, s[4:7], s38 offen offset:0 lds
buffer_load_dwordx4 v9, s[4:7], s38 offen offset:1024 lds
buffer_load_dwordx4 v10, s[4:7], s38 offen offset:2048 lds
buffer_load_dwordx4 v11, s[4:7], s38 offen offset:3072 lds
s_mov_b32 s16, s68
s_mov_b32 s17, s69
s_mov_b32 s18, 0x380000
s_mov_b32 s19, 0x20000
s_load_dwordx2 s[70:71], s[0:1], 88
s_load_dwordx2 s[74:75], s[0:1], 96
v_bfe_u32 v28, v0, 1, 3
v_lshrrev_b32 v29, 4, v0
v_xor_b32 v28, v28, v29
v_lshlrev_b32 v28, 4, v28
v_and_b32 v29, 15, v0
v_lshl_or_b32 v28, v29, 7, v28
s_lshr_b32 s28, s33, 1
s_lshl_b32 s28, s28, 12
v_or_b32 v16, s28, v28
v_xor_b32 v17, 64, v16
v_bfe_u32 v28, v0, 2, 3
v_lshlrev_b32 v28, 5, v28
v_and_b32 v29, 3, v0
v_lshl_or_b32 v28, v29, 3, v28
v_bfe_u32 v29, v0, 2, 2
v_lshl_or_b32 v28, v29, 9, v28
v_lshrrev_b32 v29, 4, v0
v_lshl_or_b32 v28, v29, 12, v28
s_and_b32 s28, s33, 1
s_lshl_b32 s28, s28, 8
s_bitset1_b32 s28, 16
v_or_b32 v18, s28, v28
v_xor_b32 v19, 32, v18
v_xor_b32 v20, 64, v18
v_xor_b32 v21, 96, v18
v_xor_b32 v22, 128, v18
v_xor_b32 v23, 160, v18
v_xor_b32 v24, 192, v18
v_xor_b32 v25, 224, v18
s_load_dwordx4 s[76:79], s[0:1], 72
s_waitcnt vmcnt(18) lgkmcnt(0)
s_barrier
Rt 0,18,0
Rt 2,18,2048
Rt 4,19,0
Rt 6,19,2048
Rt 8,20,0
Rt 10,20,2048
Rt 12,21,0
Rt 14,21,2048
Rt 16,22,0
Rt 18,22,2048
Rt 20,23,0
Rt 22,23,2048
Rt 24,24,0
Rt 26,24,2048
Rt 28,25,0
Rt 30,25,2048
Rb 32,16,0
Rb 36,16,2048
)" R"(
Rb 40,16,8192
Rb 44,16,10240
Rb 48,16,16384
Rb 52,16,18432
Rb 56,16,24576
Rb 60,16,26624
s_add_u32 m0, s37, 32768
s_add_u32 s39, s39, 0x40000
buffer_load_dwordx4 v12, s[8:11], s39 offen offset:0 lds
buffer_load_dwordx4 v13, s[8:11], s39 offen offset:1024 lds
buffer_load_dwordx4 v14, s[8:11], s39 offen offset:2048 lds
buffer_load_dwordx4 v15, s[8:11], s39 offen offset:3072 lds
s_add_u32 m0, s37, 49152
s_add_u32 s39, s39, 0x40000
buffer_load_dwordx4 v12, s[8:11], s39 offen offset:0 lds
buffer_load_dwordx4 v13, s[8:11], s39 offen offset:1024 lds
buffer_load_dwordx4 v14, s[8:11], s39 offen offset:2048 lds
buffer_load_dwordx4 v15, s[8:11], s39 offen offset:3072 lds
s_lshl2_add_u32 s40, s2, s33
s_mul_i32 s40, s40, 7168
v_min_u32 v2, 55, v0
v_lshlrev_b32 v2, 7, v2
buffer_load_dword v6, v2, s[16:19], s40 offen
s_mul_i32 s3, s3, 1664525
s_add_u32 s3, s3, 1013904223
s_waitcnt vmcnt(17)
v_dot2_f32_bf16 v120, v96, v112, 0
v_dot2_f32_bf16 v121, v100, v112, 0
v_dot2_f32_bf16 v122, v104, v112, 0
v_dot2_f32_bf16 v123, v108, v112, 0
v_dot2_f32_bf16 v124, v96, v116, 0
v_dot2_f32_bf16 v125, v100, v116, 0
v_dot2_f32_bf16 v126, v104, v116, 0
v_dot2_f32_bf16 v127, v108, v116, 0
v_dot2_f32_bf16 v120, v97, v113, v120
v_dot2_f32_bf16 v121, v101, v113, v121
v_dot2_f32_bf16 v122, v105, v113, v122
v_dot2_f32_bf16 v123, v109, v113, v123
v_dot2_f32_bf16 v124, v97, v117, v124
v_dot2_f32_bf16 v125, v101, v117, v125
v_dot2_f32_bf16 v126, v105, v117, v126
v_dot2_f32_bf16 v127, v109, v117, v127
v_dot2_f32_bf16 v120, v98, v114, v120
v_dot2_f32_bf16 v121, v102, v114, v121
v_dot2_f32_bf16 v122, v106, v114, v122
v_dot2_f32_bf16 v123, v110, v114, v123
v_dot2_f32_bf16 v124, v98, v118, v124
v_dot2_f32_bf16 v125, v102, v118, v125
v_dot2_f32_bf16 v126, v106, v118, v126
v_dot2_f32_bf16 v127, v110, v118, v127
v_dot2_f32_bf16 v120, v99, v115, v120
v_dot2_f32_bf16 v121, v103, v115, v121
v_dot2_f32_bf16 v122, v107, v115, v122
v_dot2_f32_bf16 v123, v111, v115, v123
v_dot2_f32_bf16 v124, v99, v119, v124
v_dot2_f32_bf16 v125, v103, v119, v125
v_dot2_f32_bf16 v126, v107, v119, v126
v_dot2_f32_bf16 v127, v111, v119, v127
v_permlane32_swap_b32_e32 v120, v121
v_add_f32_e32 v120, v120, v121
v_permlane32_swap_b32_e32 v122, v123
v_add_f32_e32 v122, v122, v123
v_permlane32_swap_b32_e32 v124, v125
v_add_f32_e32 v124, v124, v125
)" R"(
v_permlane32_swap_b32_e32 v126, v127
v_add_f32_e32 v126, v126, v127
v_permlane16_swap_b32_e32 v120, v122
v_add_f32_e32 v120, v120, v122
v_permlane16_swap_b32_e32 v124, v126
v_add_f32_e32 v124, v124, v126
v_add_f32_dpp v120, v120, v120 row_ror:8 row_mask:0xf bank_mask:0xf
s_nop 0
v_add_f32_dpp v124, v124, v124 row_ror:8 row_mask:0xf bank_mask:0xf
v_add_f32_dpp v120, v120, v120 row_ror:4 row_mask:0xf bank_mask:0xf
s_nop 0
v_add_f32_dpp v124, v124, v124 row_ror:4 row_mask:0xf bank_mask:0xf
v_add_f32_dpp v120, v120, v120 row_ror:2 row_mask:0xf bank_mask:0xf
s_nop 0
v_add_f32_dpp v124, v124, v124 row_ror:2 row_mask:0xf bank_mask:0xf
v_add_f32_dpp v120, v120, v120 row_ror:1 row_mask:0xf bank_mask:0xf
s_nop 0
v_add_f32_dpp v124, v124, v124 row_ror:1 row_mask:0xf bank_mask:0xf
Cv 120,120,120
v_lshlrev_b32 v120, 16, v120
Cv 124,124,124
v_lshlrev_b32 v124, 16, v124
v_mul_f32_e32 v120, 0xbfb8aa3b, v120
v_exp_f32_e32 v120, v120
s_nop 0
v_add_f32_e32 v120, 1.0, v120
v_rcp_f32_e32 v120, v120
s_nop 0
Cv 120,120,120
v_lshlrev_b32 v120, 16, v120
v_lshrrev_b32 v121, 5, v0
v_bfe_u32 v122, v0, 4, 1
v_lshl_or_b32 v121, v122, 1, v121
v_add_u32 v121, s94, v121
v_add_f32_e32 v125, 0, v124
v_ashrrev_i32 v126, 31, v125
v_or_b32 v126, 0x80000000, v126
v_xor_b32 v125, v125, v126
v_and_b32 v125, 0xffff0000, v125
v_sub_u32 v126, 0xffff, v121
v_or_b32 v116, v125, v126
v_mov_b32 v117, s3
v_mov_b32 v118, v120
v_mov_b32 v119, v124
v_and_b32 v122, 15, v0
v_min_u32 v122, 1, v122
v_lshlrev_b32 v122, 30, v122
v_lshl_or_b32 v122, v121, 4, v122
s_mov_b32 s94, 0xc100000
buffer_store_dwordx4 v[116:119], v122, s[20:23], s94 offen sc0 sc1
s_cmp_eq_u32 s91, 1
s_waitcnt lgkmcnt(0)
s_barrier
s_cbranch_scc1 .LzAV
s_branch .LzAK
.LzAK:
Mz 32,0,32
Rt 64,18,16384
Mz 64,0,36
s_lshl1_add_u32 s40, s34, s91
s_lshl_b32 s40, s40, 12
Mz 96,0,40
Rt 66,18,18432
s_and_b32 s29, s38, 0xfffe0000
Mz 128,0,44
)" R"(
s_add_u32 s39, s39, 0x40000
s_add_u32 s40, s40, s29
Mz 160,0,48
Rt 68,19,16384
s_add_u32 m0, s37, 0
Mz 192,0,52
buffer_load_dwordx4 v12, s[8:11], s39 offen offset:0 lds
Mz 224,0,56
Rt 70,19,18432
v_min_u32 v3, 31, v0
v_mfma_f32_16x16x32_bf16 a[224:227], a[0:3], a[60:63], 0
v_lshlrev_b32 v3, 7, v3
v_mfma_f32_16x16x32_bf16 a[228:231], a[4:7], a[60:63], 0
Rt 72,20,16384
Mz 228,4,56
Mz 196,4,52
Rt 74,20,18432
Mz 164,4,48
Mz 132,4,44
Rt 76,21,16384
Mz 100,4,40
buffer_load_dwordx4 v13, s[8:11], s39 offen offset:1024 lds
Mz 68,4,36
Rt 78,21,18432
Mz 36,4,32
Mz 40,8,32
Rt 80,22,16384
Mz 72,8,36
Mz 104,8,40
Rt 82,22,18432
Mz 136,8,44
Mz 168,8,48
Rt 84,23,16384
Mz 200,8,52
buffer_load_dwordx4 v14, s[8:11], s39 offen offset:2048 lds
Mz 232,8,56
Rt 86,23,18432
v_mfma_f32_16x16x32_bf16 a[232:235], a[8:11], a[60:63], 0
v_mfma_f32_16x16x32_bf16 a[236:239], a[12:15], a[60:63], 0
Rt 88,24,16384
Mz 236,12,56
Mz 204,12,52
Rt 90,24,18432
Mz 172,12,48
Mz 140,12,44
Rt 92,25,16384
Mz 108,12,40
buffer_load_dwordx4 v15, s[8:11], s39 offen offset:3072 lds
Mz 76,12,36
Rt 94,25,18432
Mz 44,12,32
Mz 48,16,32
Rb 96,17,0
Mz 80,16,36
buffer_load_dword v6, v3, s[4:7], s40 offen
Mz 112,16,40
Rb 100,17,2048
Mz 144,16,44
Mz 176,16,48
Rb 104,17,8192
s_mov_b32 s28, s35
Mz 208,16,52
s_lshl2_add_u32 s28, s28, s33
s_mul_i32 s43, s28, 28672
)" R"(
Mz 240,16,56
Rb 108,17,10240
v_and_b32 v26, 0x70, v1
v_mfma_f32_16x16x32_bf16 a[240:243], a[16:19], a[60:63], 0
v_and_b32 v4, 48, v0
v_xor_b32 v26, v26, v4
v_mfma_f32_16x16x32_bf16 a[244:247], a[20:23], a[60:63], 0
Rb 112,17,16384
v_lshrrev_b32 v4, 3, v0
Mz 244,20,56
v_mul_u32_u24 v4, 896, v4
v_add_u32 v26, v26, v4
Mz 212,20,52
Rb 116,17,18432
v_xor_b32 v27, 64, v26
Mz 180,20,48
v_add_u32 v27, 6144, v27
v_bfe_u32 v28, v0, 1, 3
Mz 148,20,44
Rb 120,17,24576
v_lshrrev_b32 v4, 4, v0
Mz 116,20,40
v_xor_b32 v28, v28, v4
v_lshlrev_b32 v28, 4, v28
Mz 84,20,36
Rb 124,17,26624
v_and_b32 v4, 15, v0
Mz 52,20,32
v_lshl_or_b32 v28, v4, 7, v28
s_add_u32 s28, s36, 0x18000
Mz 56,24,32
v_add_u32 v28, s28, v28
v_xor_b32 v29, 64, v28
Mz 88,24,36
Mz 120,24,40
Mz 152,24,44
Mz 184,24,48
Mz 216,24,52
Mz 248,24,56
v_mfma_f32_16x16x32_bf16 a[248:251], a[24:27], a[60:63], 0
v_mfma_f32_16x16x32_bf16 a[252:255], a[28:31], a[60:63], 0
Mz 252,28,56
Mz 220,28,52
Mz 188,28,48
Mz 156,28,44
Mz 124,28,40
Mz 92,28,36
Mz 60,28,32
s_waitcnt vmcnt(11) lgkmcnt(0)
s_barrier
Mv 32,64,96
Rt 0,18,32768
s_bfe_u32 s88, s2, 0x20005
Mv 64,64,100
s_add_u32 s38, s38, 0xffff0080
s_and_b32 s28, s2, 15
Mv 96,64,104
Rt 2,18,34816
s_add_u32 m0, s36, 0
Mv 128,64,108
buffer_load_dwordx4 v8, s[4:7], s38 offen offset:0 lds
Mv 160,64,112
Rt 4,19,32768
s_lshl4_add_u32 s88, s88, s28
Mv 192,64,116
)" R"(
s_lshl2_add_u32 s88, s88, s33
s_mul_i32 s88, s88, 4096
Mv 224,64,120
Rt 6,19,34816
s_add_u32 s88, s88, 0x100000
Ma 224,64,124
buffer_load_dwordx4 v9, s[4:7], s38 offen offset:1024 lds
Ma 228,68,124
Rt 8,20,32768
v_min_u32 v5, 31, v0
Mv 228,68,120
v_lshlrev_b32 v5, 7, v5
s_mov_b32 s90, 0x100000
Mv 196,68,116
Rt 10,20,34816
s_mov_b64 s[24:25], s[8:9]
Mv 164,68,112
buffer_load_dwordx4 v10, s[4:7], s38 offen offset:2048 lds
Mv 132,68,108
Rt 12,21,32768
s_mov_b64 s[26:27], s[10:11]
Mv 100,68,104
Mv 68,68,100
Rt 14,21,34816
Mv 36,68,96
buffer_load_dwordx4 v11, s[4:7], s38 offen offset:3072 lds
Mv 40,72,96
Rt 16,22,32768
Mv 72,72,100
s_add_u32 s38, s38, 0x10000
Mv 104,72,104
Rt 18,22,34816
s_add_u32 m0, s36, 4096
Mv 136,72,108
buffer_load_dwordx4 v8, s[4:7], s38 offen offset:0 lds
Mv 168,72,112
Rt 20,23,32768
Mv 200,72,116
Mv 232,72,120
Rt 22,23,34816
Ma 232,72,124
buffer_load_dwordx4 v9, s[4:7], s38 offen offset:1024 lds
Ma 236,76,124
Rt 24,24,32768
Mv 236,76,120
Mv 204,76,116
Rt 26,24,34816
Mv 172,76,112
buffer_load_dwordx4 v10, s[4:7], s38 offen offset:2048 lds
Mv 140,76,108
Rt 28,25,32768
Mv 108,76,104
Mv 76,76,100
Rt 30,25,34816
Mv 44,76,96
buffer_load_dwordx4 v11, s[4:7], s38 offen offset:3072 lds
Mv 48,80,96
Rb 32,16,32768
Mv 80,80,100
Mv 112,80,104
Rb 36,16,34816
Mv 144,80,108
s_add_u32 s39, s39, 0x40000
Mv 176,80,112
)" R"(
Rb 40,16,40960
s_add_u32 m0, s37, 16384
Mv 208,80,116
buffer_load_dwordx4 v12, s[8:11], s39 offen offset:0 lds
Mv 240,80,120
Rb 44,16,43008
Ma 240,80,124
Ma 244,84,124
Rb 48,16,49152
Mv 244,84,120
Mv 212,84,116
Rb 52,16,51200
Mv 180,84,112
buffer_load_dwordx4 v13, s[8:11], s39 offen offset:1024 lds
Mv 148,84,108
Rb 56,16,57344
Mv 116,84,104
Mv 84,84,100
Rb 60,16,59392
Mv 52,84,96
Mv 56,88,96
Mv 88,88,100
buffer_load_dwordx4 v14, s[8:11], s39 offen offset:2048 lds
Mv 120,88,104
Mv 152,88,108
Mv 184,88,112
Mv 216,88,116
Mv 248,88,120
Ma 248,88,124
buffer_load_dwordx4 v15, s[8:11], s39 offen offset:3072 lds
Ma 252,92,124
Mv 252,92,120
s_add_u32 s88, s88, s90
Mv 220,92,116
Mv 188,92,112
buffer_load_dword v6, v5, s[24:27], s88 offen
Mv 156,92,108
Mv 124,92,104
Mv 92,92,100
Mv 60,92,96
s_waitcnt vmcnt(20) lgkmcnt(0)
s_barrier
Mv 32,0,32
Rt 64,18,49152
s_lshl_b32 s28, s34, 21
Mv 64,0,36
s_lshl_b32 s29, s91, 25
s_add_u32 s28, s28, s29
Mv 96,0,40
Rt 66,18,51200
s_bfe_u32 s29, s63, 0x80000
Mv 128,0,44
s_add_u32 s39, s39, 0x40000
s_lshl_b32 s29, s29, 16
Mv 160,0,48
Rt 68,19,49152
s_add_u32 m0, s37, 32768
Mv 192,0,52
buffer_load_dwordx4 v12, s[8:11], s39 offen offset:0 lds
Mv 224,0,56
Rt 70,19,51200
s_add_u32 s45, s28, s29
Ma 224,0,60
s_bfe_u32 s29, s63, 0x80008
s_lshl_b32 s29, s29, 16
)" R"(
Ma 228,4,60
Rt 72,20,49152
s_add_u32 s46, s28, s29
Mv 228,4,56
s_bfe_u32 s29, s63, 0x80010
s_lshl_b32 s29, s29, 16
Mv 196,4,52
Rt 74,20,51200
s_add_u32 s47, s28, s29
Mv 164,4,48
s_bfe_u32 s29, s63, 0x80018
s_lshl_b32 s29, s29, 16
Mv 132,4,44
Rt 76,21,49152
s_add_u32 s48, s28, s29
Mv 100,4,40
buffer_load_dwordx4 v13, s[8:11], s39 offen offset:1024 lds
Mv 68,4,36
Rt 78,21,51200
s_lshl_b32 s50, s34, 21
Mv 36,4,32
s_add_u32 s50, s50, 0x6140000
s_lshl2_add_u32 s28, s91, s93
Mv 40,8,32
Rt 80,22,49152
s_lshl_b32 s28, s28, 7
Mv 72,8,36
s_add_u32 s49, s28, s50
s_lshl_b32 s52, s35, 7
Mv 104,8,40
Rt 82,22,51200
s_add_u32 s52, s52, s50
Mv 136,8,44
s_add_u32 s52, s52, 0xd000
s_lshl4_add_u32 s28, s34, s35
Mv 168,8,48
Rt 84,23,49152
s_lshl_b32 s51, s28, 17
Mv 200,8,52
buffer_load_dwordx4 v14, s[8:11], s39 offen offset:2048 lds
Mv 232,8,56
Rt 86,23,51200
s_add_u32 s51, s51, 0x4000000
Ma 232,8,60
s_cmp_lg_u32 s33, 0
s_cmov_b32 s49, 0x40000000
Ma 236,12,60
Rt 88,24,49152
s_mov_b32 s53, 0x40000
Mv 236,12,56
s_mov_b32 s54, 0x3db8aa3b
s_mul_i32 s41, s33, 7168
Mv 204,12,52
Rt 90,24,51200
s_mul_i32 s42, s33, 0x70000
Mv 172,12,48
s_lshl_b32 s28, s34, 9
s_add_u32 s42, s42, s28
Mv 140,12,44
Rt 92,25,49152
s_lshl_b32 s89, s34, 21
Mv 108,12,40
buffer_load_dwordx4 v15, s[8:11], s39 offen offset:3072 lds
Mv 76,12,36
)" R"(
Rt 94,25,51200
s_mov_b32 s62, 0x4e800000
Mv 44,12,32
s_add_u32 s67, s89, 0x6000000
s_lshl3_add_u32 s28, s34, s35
Mv 48,16,32
Rb 96,17,32768
s_add_u32 s64, s51, 0x100000
s_add_u32 m0, s36, 131072
s_add_u32 s44, s43, 0
Mv 80,16,36
s_lshl2_add_u32 s28, s28, s33
s_lshl2_add_u32 s65, s35, s33
buffer_load_dwordx4 v26, s[12:15], s44 offen offset:0 lds
Mv 112,16,40
Rb 100,17,34816
s_lshl_b32 s65, s65, 7
Mv 144,16,44
s_add_u32 s65, s65, s50
s_add_u32 s65, s65, 0xc000
Mv 176,16,48
Rb 104,17,40960
s_lshl_b32 s66, s28, 10
Mv 208,16,52
s_add_u32 s66, s66, 0xc000000
s_mov_b32 s28, s35
buffer_load_dwordx4 v27, s[12:15], s44 offen offset:1024 lds
Mv 240,16,56
Rb 108,17,43008
s_lshl2_add_u32 s28, s28, s33
Ma 240,16,60
s_lshl_b32 s29, s28, 5
s_add_u32 s29, s29, s34
Ma 244,20,60
Rb 112,17,49152
s_lshl_b32 s73, s29, 15
Mv 244,20,56
s_add_u32 s73, s73, 0x8000000
s_lshr_b32 s29, s36, 1
s_add_u32 s44, s43, 12288
buffer_load_dwordx4 v26, s[12:15], s44 offen offset:2048 lds
Mv 212,20,52
Rb 116,17,51200
s_and_b32 s29, s29, 0x2000
Mv 180,20,48
s_add_u32 s73, s73, s29
s_lshl4_add_u32 s28, s28, s34
Mv 148,20,44
Rb 120,17,57344
s_lshl_b32 s68, s28, 8
Mv 116,20,40
s_add_u32 s68, s68, 0xc110000
s_add_u32 s55, s89, 0x120000
buffer_load_dwordx4 v27, s[12:15], s44 offen offset:3072 lds
Mv 84,20,36
Rb 124,17,59392
s_lshl_b32 s28, s35, 1
Mv 52,20,32
s_sub_u32 s57, 14, s28
s_mov_b32 s59, s89
Mv 56,24,32
s_mov_b32 s58, 2
s_add_u32 m0, s36, 135168
s_add_u32 s44, s43, 128
)" R"(
Mv 88,24,36
s_cmp_eq_u32 s35, 7
s_cmov_b32 s55, s89
buffer_load_dwordx4 v26, s[12:15], s44 offen offset:0 lds
Mv 120,24,40
s_cmp_eq_u32 s35, 7
s_cmov_b32 s57, 16
Mv 152,24,44
s_cmp_eq_u32 s35, 7
s_cmov_b32 s59, s67
Mv 184,24,48
s_add_u32 s56, s55, 0x2000000
Mv 216,24,52
buffer_load_dwordx4 v27, s[12:15], s44 offen offset:1024 lds
Mv 248,24,56
Ma 248,24,60
Ma 252,28,60
Mv 252,28,56
s_add_u32 s44, s43, 12416
buffer_load_dwordx4 v26, s[12:15], s44 offen offset:2048 lds
Mv 220,28,52
Mv 188,28,48
Mv 156,28,44
Mv 124,28,40
buffer_load_dwordx4 v27, s[12:15], s44 offen offset:3072 lds
Mv 92,28,36
Mv 60,28,32
s_waitcnt vmcnt(17) lgkmcnt(0)
s_barrier
Mv 32,64,96
Rt 0,18,0
Mv 64,64,100
s_add_u32 s38, s38, 0xffff0080
Mv 96,64,104
Rt 2,18,2048
s_add_u32 m0, s36, 32768
Mv 128,64,108
buffer_load_dwordx4 v8, s[4:7], s38 offen offset:0 lds
Mv 160,64,112
Rt 4,19,0
Mv 192,64,116
Mv 224,64,120
Rt 6,19,2048
Ma 224,64,124
buffer_load_dwordx4 v9, s[4:7], s38 offen offset:1024 lds
Ma 228,68,124
Rt 8,20,0
s_lshl2_add_u32 s28, s2, s33
Mv 228,68,120
s_lshl_b32 s28, s28, 11
s_add_u32 s28, s28, 0xc100000
Mv 196,68,116
Rt 10,20,2048
v_min_u32 v3, 15, v0
Mv 164,68,112
buffer_load_dwordx4 v10, s[4:7], s38 offen offset:2048 lds
Mv 132,68,108
Rt 12,21,0
v_lshlrev_b32 v3, 7, v3
Mv 100,68,104
Mv 68,68,100
Rt 14,21,2048
Mv 36,68,96
buffer_load_dwordx4 v11, s[4:7], s38 offen offset:3072 lds
Mv 40,72,96
)" R"(
Rt 16,22,0
Mv 72,72,100
s_add_u32 s38, s38, 0x10000
Mv 104,72,104
Rt 18,22,2048
s_add_u32 m0, s36, 36864
Mv 136,72,108
buffer_load_dwordx4 v8, s[4:7], s38 offen offset:0 lds
Mv 168,72,112
Rt 20,23,0
Mv 200,72,116
Mv 232,72,120
Rt 22,23,2048
Ma 232,72,124
buffer_load_dwordx4 v9, s[4:7], s38 offen offset:1024 lds
Ma 236,76,124
Rt 24,24,0
Mv 236,76,120
Mv 204,76,116
Rt 26,24,2048
Mv 172,76,112
buffer_load_dwordx4 v10, s[4:7], s38 offen offset:2048 lds
Mv 140,76,108
Rt 28,25,0
Mv 108,76,104
Mv 76,76,100
Rt 30,25,2048
Mv 44,76,96
buffer_load_dwordx4 v11, s[4:7], s38 offen offset:3072 lds
Mv 48,80,96
Rb 32,16,0
Mv 80,80,100
Mv 112,80,104
Rb 36,16,2048
Mv 144,80,108
s_add_u32 s39, s39, 0x40000
Mv 176,80,112
Rb 40,16,8192
s_add_u32 m0, s37, 49152
Mv 208,80,116
buffer_load_dwordx4 v12, s[8:11], s39 offen offset:0 lds
Mv 240,80,120
Rb 44,16,10240
Ma 240,80,124
Ma 244,84,124
Rb 48,16,16384
Mv 244,84,120
Mv 212,84,116
Rb 52,16,18432
Mv 180,84,112
buffer_load_dwordx4 v13, s[8:11], s39 offen offset:1024 lds
Mv 148,84,108
Rb 56,16,24576
Mv 116,84,104
Mv 84,84,100
Rb 60,16,26624
Mv 52,84,96
Mv 56,88,96
Mv 88,88,100
buffer_load_dwordx4 v14, s[8:11], s39 offen offset:2048 lds
Mv 120,88,104
Mv 152,88,108
Mv 184,88,112
Mv 216,88,116
)" R"(
Mv 248,88,120
Ma 248,88,124
buffer_load_dwordx4 v15, s[8:11], s39 offen offset:3072 lds
Ma 252,92,124
Mv 252,92,120
s_add_u32 s88, s88, s90
s_mov_b32 s40, 5
Mv 220,92,116
Mv 188,92,112
buffer_load_dword v6, v5, s[24:27], s88 offen
Mv 156,92,108
Mv 124,92,104
buffer_load_dword v6, v3, s[20:23], s28 offen
Mv 92,92,100
Mv 60,92,96
s_waitcnt vmcnt(27) lgkmcnt(0)
s_barrier
.LzPAK:
Mv 32,0,32
Rt 64,18,16384
Mv 64,0,36
Mv 96,0,40
Rt 66,18,18432
Mv 128,0,44
s_add_u32 s39, s39, 0x40000
Mv 160,0,48
Rt 68,19,16384
s_add_u32 m0, s37, 0
Mv 192,0,52
buffer_load_dwordx4 v12, s[8:11], s39 offen offset:0 lds
Mv 224,0,56
Rt 70,19,18432
Ma 224,0,60
Ma 228,4,60
Rt 72,20,16384
Mv 228,4,56
Mv 196,4,52
Rt 74,20,18432
Mv 164,4,48
Mv 132,4,44
Rt 76,21,16384
Mv 100,4,40
buffer_load_dwordx4 v13, s[8:11], s39 offen offset:1024 lds
Mv 68,4,36
Rt 78,21,18432
Mv 36,4,32
Mv 40,8,32
Rt 80,22,16384
Mv 72,8,36
Mv 104,8,40
Rt 82,22,18432
Mv 136,8,44
Mv 168,8,48
Rt 84,23,16384
Mv 200,8,52
buffer_load_dwordx4 v14, s[8:11], s39 offen offset:2048 lds
Mv 232,8,56
Rt 86,23,18432
Ma 232,8,60
Ma 236,12,60
Rt 88,24,16384
Mv 236,12,56
Mv 204,12,52
Rt 90,24,18432
Mv 172,12,48
)" R"(
Mv 140,12,44
Rt 92,25,16384
Mv 108,12,40
buffer_load_dwordx4 v15, s[8:11], s39 offen offset:3072 lds
Mv 76,12,36
Rt 94,25,18432
Mv 44,12,32
Mv 48,16,32
Rb 96,17,0
Mv 80,16,36
Mv 112,16,40
Rb 100,17,2048
Mv 144,16,44
Mv 176,16,48
Rb 104,17,8192
Mv 208,16,52
Mv 240,16,56
Rb 108,17,10240
Ma 240,16,60
Ma 244,20,60
Rb 112,17,16384
Mv 244,20,56
Mv 212,20,52
Rb 116,17,18432
Mv 180,20,48
Mv 148,20,44
Rb 120,17,24576
Mv 116,20,40
Mv 84,20,36
Rb 124,17,26624
Mv 52,20,32
Mv 56,24,32
Mv 88,24,36
Mv 120,24,40
Mv 152,24,44
Mv 184,24,48
Mv 216,24,52
Mv 248,24,56
Ma 248,24,60
Ma 252,28,60
Mv 252,28,56
Mv 220,28,52
Mv 188,28,48
Mv 156,28,44
Mv 124,28,40
Mv 92,28,36
Mv 60,28,32
s_waitcnt vmcnt(9) lgkmcnt(0)
s_barrier
Mv 32,64,96
Rt 0,18,32768
Mv 64,64,100
s_add_u32 s38, s38, 0xffff0080
Mv 96,64,104
Rt 2,18,34816
s_add_u32 m0, s36, 0
Mv 128,64,108
buffer_load_dwordx4 v8, s[4:7], s38 offen offset:0 lds
Mv 160,64,112
Rt 4,19,32768
Mv 192,64,116
Mv 224,64,120
Rt 6,19,34816
Ma 224,64,124
buffer_load_dwordx4 v9, s[4:7], s38 offen offset:1024 lds
)" R"(
Ma 228,68,124
Rt 8,20,32768
Mv 228,68,120
Mv 196,68,116
Rt 10,20,34816
Mv 164,68,112
buffer_load_dwordx4 v10, s[4:7], s38 offen offset:2048 lds
Mv 132,68,108
Rt 12,21,32768
Mv 100,68,104
Mv 68,68,100
Rt 14,21,34816
Mv 36,68,96
buffer_load_dwordx4 v11, s[4:7], s38 offen offset:3072 lds
Mv 40,72,96
Rt 16,22,32768
Mv 72,72,100
s_add_u32 s38, s38, 0x10000
Mv 104,72,104
Rt 18,22,34816
s_add_u32 m0, s36, 4096
Mv 136,72,108
buffer_load_dwordx4 v8, s[4:7], s38 offen offset:0 lds
Mv 168,72,112
Rt 20,23,32768
Mv 200,72,116
Mv 232,72,120
Rt 22,23,34816
Ma 232,72,124
buffer_load_dwordx4 v9, s[4:7], s38 offen offset:1024 lds
Ma 236,76,124
Rt 24,24,32768
Mv 236,76,120
Mv 204,76,116
Rt 26,24,34816
Mv 172,76,112
buffer_load_dwordx4 v10, s[4:7], s38 offen offset:2048 lds
Mv 140,76,108
Rt 28,25,32768
Mv 108,76,104
Mv 76,76,100
Rt 30,25,34816
Mv 44,76,96
buffer_load_dwordx4 v11, s[4:7], s38 offen offset:3072 lds
Mv 48,80,96
Rb 32,16,32768
Mv 80,80,100
Mv 112,80,104
Rb 36,16,34816
Mv 144,80,108
s_add_u32 s39, s39, 0x40000
Mv 176,80,112
Rb 40,16,40960
s_add_u32 m0, s37, 16384
Mv 208,80,116
buffer_load_dwordx4 v12, s[8:11], s39 offen offset:0 lds
Mv 240,80,120
Rb 44,16,43008
Ma 240,80,124
Ma 244,84,124
Rb 48,16,49152
Mv 244,84,120
Mv 212,84,116
Rb 52,16,51200
)" R"(
Mv 180,84,112
buffer_load_dwordx4 v13, s[8:11], s39 offen offset:1024 lds
Mv 148,84,108
Rb 56,16,57344
Mv 116,84,104
Mv 84,84,100
Rb 60,16,59392
Mv 52,84,96
Mv 56,88,96
Mv 88,88,100
buffer_load_dwordx4 v14, s[8:11], s39 offen offset:2048 lds
Mv 120,88,104
Mv 152,88,108
Mv 184,88,112
Mv 216,88,116
Mv 248,88,120
Ma 248,88,124
buffer_load_dwordx4 v15, s[8:11], s39 offen offset:3072 lds
Ma 252,92,124
Mv 252,92,120
s_add_u32 s88, s88, s90
Mv 220,92,116
Mv 188,92,112
buffer_load_dword v6, v5, s[24:27], s88 offen
Mv 156,92,108
Mv 124,92,104
Mv 92,92,100
Mv 60,92,96
s_waitcnt vmcnt(18) lgkmcnt(0)
s_barrier
Mv 32,0,32
Rt 64,18,49152
Mv 64,0,36
Mv 96,0,40
Rt 66,18,51200
Mv 128,0,44
s_add_u32 s39, s39, 0x40000
Mv 160,0,48
Rt 68,19,49152
s_add_u32 m0, s37, 32768
Mv 192,0,52
buffer_load_dwordx4 v12, s[8:11], s39 offen offset:0 lds
Mv 224,0,56
Rt 70,19,51200
Ma 224,0,60
Ma 228,4,60
Rt 72,20,49152
Mv 228,4,56
Mv 196,4,52
Rt 74,20,51200
Mv 164,4,48
Mv 132,4,44
Rt 76,21,49152
Mv 100,4,40
buffer_load_dwordx4 v13, s[8:11], s39 offen offset:1024 lds
Mv 68,4,36
Rt 78,21,51200
Mv 36,4,32
Mv 40,8,32
Rt 80,22,49152
Mv 72,8,36
Mv 104,8,40
Rt 82,22,51200
Mv 136,8,44
Mv 168,8,48
)" R"(
Rt 84,23,49152
Mv 200,8,52
buffer_load_dwordx4 v14, s[8:11], s39 offen offset:2048 lds
Mv 232,8,56
Rt 86,23,51200
Ma 232,8,60
Ma 236,12,60
Rt 88,24,49152
Mv 236,12,56
Mv 204,12,52
Rt 90,24,51200
Mv 172,12,48
Mv 140,12,44
Rt 92,25,49152
Mv 108,12,40
buffer_load_dwordx4 v15, s[8:11], s39 offen offset:3072 lds
Mv 76,12,36
Rt 94,25,51200
Mv 44,12,32
Mv 48,16,32
Rb 96,17,32768
Mv 80,16,36
Mv 112,16,40
Rb 100,17,34816
Mv 144,16,44
Mv 176,16,48
Rb 104,17,40960
Mv 208,16,52
Mv 240,16,56
Rb 108,17,43008
Ma 240,16,60
Ma 244,20,60
Rb 112,17,49152
Mv 244,20,56
Mv 212,20,52
Rb 116,17,51200
Mv 180,20,48
Mv 148,20,44
Rb 120,17,57344
Mv 116,20,40
Mv 84,20,36
Rb 124,17,59392
Mv 52,20,32
Mv 56,24,32
Mv 88,24,36
Mv 120,24,40
Mv 152,24,44
Mv 184,24,48
Mv 216,24,52
Mv 248,24,56
Ma 248,24,60
Ma 252,28,60
Mv 252,28,56
Mv 220,28,52
Mv 188,28,48
Mv 156,28,44
Mv 124,28,40
Mv 92,28,36
Mv 60,28,32
s_waitcnt vmcnt(9) lgkmcnt(0)
s_barrier
Mv 32,64,96
Rt 0,18,0
Mv 64,64,100
s_add_u32 s38, s38, 0xffff0080
)" R"(
Mv 96,64,104
Rt 2,18,2048
s_add_u32 m0, s36, 32768
Mv 128,64,108
buffer_load_dwordx4 v8, s[4:7], s38 offen offset:0 lds
Mv 160,64,112
Rt 4,19,0
Mv 192,64,116
Mv 224,64,120
Rt 6,19,2048
Ma 224,64,124
buffer_load_dwordx4 v9, s[4:7], s38 offen offset:1024 lds
Ma 228,68,124
Rt 8,20,0
Mv 228,68,120
Mv 196,68,116
Rt 10,20,2048
Mv 164,68,112
buffer_load_dwordx4 v10, s[4:7], s38 offen offset:2048 lds
Mv 132,68,108
Rt 12,21,0
Mv 100,68,104
Mv 68,68,100
Rt 14,21,2048
Mv 36,68,96
buffer_load_dwordx4 v11, s[4:7], s38 offen offset:3072 lds
Mv 40,72,96
Rt 16,22,0
Mv 72,72,100
s_add_u32 s38, s38, 0x10000
Mv 104,72,104
Rt 18,22,2048
s_add_u32 m0, s36, 36864
Mv 136,72,108
buffer_load_dwordx4 v8, s[4:7], s38 offen offset:0 lds
Mv 168,72,112
Rt 20,23,0
Mv 200,72,116
Mv 232,72,120
Rt 22,23,2048
Ma 232,72,124
buffer_load_dwordx4 v9, s[4:7], s38 offen offset:1024 lds
Ma 236,76,124
Rt 24,24,0
Mv 236,76,120
Mv 204,76,116
Rt 26,24,2048
Mv 172,76,112
buffer_load_dwordx4 v10, s[4:7], s38 offen offset:2048 lds
Mv 140,76,108
Rt 28,25,0
Mv 108,76,104
Mv 76,76,100
Rt 30,25,2048
Mv 44,76,96
buffer_load_dwordx4 v11, s[4:7], s38 offen offset:3072 lds
Mv 48,80,96
Rb 32,16,0
Mv 80,80,100
Mv 112,80,104
Rb 36,16,2048
Mv 144,80,108
s_add_u32 s39, s39, 0x40000
Mv 176,80,112
)" R"(
Rb 40,16,8192
s_add_u32 m0, s37, 49152
Mv 208,80,116
buffer_load_dwordx4 v12, s[8:11], s39 offen offset:0 lds
Mv 240,80,120
Rb 44,16,10240
Ma 240,80,124
Ma 244,84,124
Rb 48,16,16384
Mv 244,84,120
Mv 212,84,116
Rb 52,16,18432
Mv 180,84,112
buffer_load_dwordx4 v13, s[8:11], s39 offen offset:1024 lds
Mv 148,84,108
Rb 56,16,24576
Mv 116,84,104
Mv 84,84,100
Rb 60,16,26624
Mv 52,84,96
Mv 56,88,96
Mv 88,88,100
buffer_load_dwordx4 v14, s[8:11], s39 offen offset:2048 lds
Mv 120,88,104
Mv 152,88,108
Mv 184,88,112
Mv 216,88,116
Mv 248,88,120
Ma 248,88,124
buffer_load_dwordx4 v15, s[8:11], s39 offen offset:3072 lds
Ma 252,92,124
Mv 252,92,120
s_add_u32 s88, s88, s90
Mv 220,92,116
s_sub_u32 s40, s40, 1
Mv 188,92,112
buffer_load_dword v6, v5, s[24:27], s88 offen
Mv 156,92,108
Mv 124,92,104
Mv 92,92,100
Mv 60,92,96
s_cmp_lg_u32 s40, 0
s_waitcnt vmcnt(18) lgkmcnt(0)
s_barrier
s_cbranch_scc1 .LzPAK
.LzPAKX:
Mv 32,0,32
Rt 64,18,16384
Mv 64,0,36
Rb 192,28,32768
Mv 96,0,40
Rt 66,18,18432
Mv 128,0,44
s_add_u32 s39, s39, 0x40000
Rb 196,28,34816
Mv 160,0,48
Rt 68,19,16384
s_add_u32 m0, s37, 0
Mv 192,0,52
buffer_load_dwordx4 v12, s[8:11], s39 offen offset:0 lds
Mv 224,0,56
Rt 70,19,18432
Ma 224,0,60
Rb 200,29,32768
Ma 228,4,60
)" R"(
Rt 72,20,16384
Mv 228,4,56
Rb 204,29,34816
Mv 196,4,52
Rt 74,20,18432
Mv 164,4,48
Rb 208,28,36864
Mv 132,4,44
Rt 76,21,16384
Mv 100,4,40
buffer_load_dwordx4 v13, s[8:11], s39 offen offset:1024 lds
Mv 68,4,36
Rt 78,21,18432
Mv 36,4,32
Rb 212,28,38912
Mv 40,8,32
Rt 80,22,16384
Mv 72,8,36
Rb 216,29,36864
Mv 104,8,40
Rt 82,22,18432
Mv 136,8,44
Rb 220,29,38912
Mv 168,8,48
Rt 84,23,16384
Mv 200,8,52
buffer_load_dwordx4 v14, s[8:11], s39 offen offset:2048 lds
Mv 232,8,56
Rt 86,23,18432
Ma 232,8,60
Ma 236,12,60
Rt 88,24,16384
Mv 236,12,56
Mv 204,12,52
Rt 90,24,18432
Mv 172,12,48
Mv 140,12,44
Rt 92,25,16384
Mv 108,12,40
buffer_load_dwordx4 v15, s[8:11], s39 offen offset:3072 lds
Mv 76,12,36
Rt 94,25,18432
Mv 44,12,32
s_add_u32 s44, s43, 256
s_waitcnt lgkmcnt(6)
Mv 48,16,32
Rb 96,17,0
s_add_u32 m0, s36, 131072
Mv 80,16,36
buffer_load_dwordx4 v26, s[12:15], s44 offen offset:0 lds
Mv 112,16,40
Rb 100,17,2048
Mv 144,16,44
Mv 176,16,48
Rb 104,17,8192
Mv 208,16,52
buffer_load_dwordx4 v27, s[12:15], s44 offen offset:1024 lds
Mv 240,16,56
Rb 108,17,10240
Ma 240,16,60
Ma 244,20,60
Rb 112,17,16384
Mv 244,20,56
s_add_u32 s44, s43, 12544
)" R"(
buffer_load_dwordx4 v26, s[12:15], s44 offen offset:2048 lds
Mv 212,20,52
Rb 116,17,18432
Mv 180,20,48
Mv 148,20,44
Rb 120,17,24576
Mv 116,20,40
buffer_load_dwordx4 v27, s[12:15], s44 offen offset:3072 lds
Mv 84,20,36
Rb 124,17,26624
Mv 52,20,32
Mv 56,24,32
s_add_u32 m0, s36, 135168
s_add_u32 s44, s43, 384
Mv 88,24,36
buffer_load_dwordx4 v26, s[12:15], s44 offen offset:0 lds
Mv 120,24,40
Mv 152,24,44
Mv 184,24,48
Mv 216,24,52
buffer_load_dwordx4 v27, s[12:15], s44 offen offset:1024 lds
Mv 248,24,56
Ma 248,24,60
Ma 252,28,60
Mv 252,28,56
s_add_u32 s44, s43, 12672
buffer_load_dwordx4 v26, s[12:15], s44 offen offset:2048 lds
Mv 220,28,52
Mv 188,28,48
Mv 156,28,44
s_mov_b32 s85, 0xc108800
v_and_b32 v2, 7, v0
v_lshlrev_b32 v2, 7, v2
Mv 124,28,40
buffer_load_dwordx4 v27, s[12:15], s44 offen offset:3072 lds
Mv 92,28,36
buffer_load_dword v4, v2, s[20:23], s85 offen sc0 sc1
Mv 60,28,32
s_waitcnt vmcnt(18) lgkmcnt(0)
s_barrier
Mv 32,64,96
Rt 0,18,32768
Mv 64,64,100
s_add_u32 s38, s38, 0xffff0080
Mv 96,64,104
Rt 2,18,34816
s_add_u32 m0, s36, 0
Mv 128,64,108
buffer_load_dwordx4 v8, s[4:7], s38 offen offset:0 lds
Mv 160,64,112
Rt 4,19,32768
Mv 192,64,116
Mv 224,64,120
Rt 6,19,34816
Ma 224,64,124
buffer_load_dwordx4 v9, s[4:7], s38 offen offset:1024 lds
Ma 228,68,124
Rt 8,20,32768
Mv 228,68,120
Mv 196,68,116
Rt 10,20,34816
Mv 164,68,112
buffer_load_dwordx4 v10, s[4:7], s38 offen offset:2048 lds
Mv 132,68,108
Rt 12,21,32768
)" R"(
Mv 100,68,104
Mv 68,68,100
Rt 14,21,34816
Mv 36,68,96
buffer_load_dwordx4 v11, s[4:7], s38 offen offset:3072 lds
Mv 40,72,96
Rt 16,22,32768
Mv 72,72,100
s_add_u32 s38, s38, 0x10000
Mv 104,72,104
Rt 18,22,34816
s_add_u32 m0, s36, 4096
Mv 136,72,108
buffer_load_dwordx4 v8, s[4:7], s38 offen offset:0 lds
Mv 168,72,112
Rt 20,23,32768
Mv 200,72,116
Mv 232,72,120
Rt 22,23,34816
Ma 232,72,124
buffer_load_dwordx4 v9, s[4:7], s38 offen offset:1024 lds
Ma 236,76,124
Rt 24,24,32768
Mv 236,76,120
Mv 204,76,116
Rt 26,24,34816
Mv 172,76,112
buffer_load_dwordx4 v10, s[4:7], s38 offen offset:2048 lds
Mv 140,76,108
Rt 28,25,32768
Mv 108,76,104
Mv 76,76,100
Rt 30,25,34816
Mv 44,76,96
buffer_load_dwordx4 v11, s[4:7], s38 offen offset:3072 lds
Mv 48,80,96
Rb 32,16,32768
Mv 80,80,100
Mv 112,80,104
Rb 36,16,34816
Mv 144,80,108
s_add_u32 s39, s39, 0x40000
Mv 176,80,112
Rb 40,16,40960
s_add_u32 m0, s37, 16384
Mv 208,80,116
buffer_load_dwordx4 v12, s[8:11], s39 offen offset:0 lds
Mv 240,80,120
Rb 44,16,43008
Ma 240,80,124
Ma 244,84,124
Rb 48,16,49152
Mv 244,84,120
Mv 212,84,116
Rb 52,16,51200
Mv 180,84,112
buffer_load_dwordx4 v13, s[8:11], s39 offen offset:1024 lds
Mv 148,84,108
Rb 56,16,57344
Mv 116,84,104
Mv 84,84,100
Rb 60,16,59392
Mv 52,84,96
Mv 56,88,96
)" R"(
Mv 88,88,100
buffer_load_dwordx4 v14, s[8:11], s39 offen offset:2048 lds
Mv 120,88,104
Mv 152,88,108
Mv 184,88,112
Mv 216,88,116
Mv 248,88,120
Ma 248,88,124
buffer_load_dwordx4 v15, s[8:11], s39 offen offset:3072 lds
Ma 252,92,124
Mv 252,92,120
Mv 220,92,116
Mv 188,92,112
Mv 156,92,108
Mv 124,92,104
Mv 92,92,100
Mv 60,92,96
s_waitcnt vmcnt(13) lgkmcnt(0)
s_barrier
Mv 32,0,32
Rt 64,18,49152
Mv 64,0,36
Rb 128,28,32768
Mv 96,0,40
Rt 66,18,51200
Mv 128,0,44
s_add_u32 s39, s39, 0x40000
Rb 132,28,34816
Mv 160,0,48
Rt 68,19,49152
s_add_u32 m0, s37, 32768
Mv 192,0,52
buffer_load_dwordx4 v12, s[8:11], s39 offen offset:0 lds
Mv 224,0,56
Rt 70,19,51200
Ma 224,0,60
Rb 136,29,32768
Ma 228,4,60
Rt 72,20,49152
Mv 228,4,56
Rb 140,29,34816
Mv 196,4,52
Rt 74,20,51200
Mv 164,4,48
Rb 144,28,36864
Mv 132,4,44
Rt 76,21,49152
Mv 100,4,40
buffer_load_dwordx4 v13, s[8:11], s39 offen offset:1024 lds
Mv 68,4,36
Rt 78,21,51200
Mv 36,4,32
Rb 148,28,38912
Mv 40,8,32
Rt 80,22,49152
Mv 72,8,36
Rb 152,29,36864
Mv 104,8,40
Rt 82,22,51200
Mv 136,8,44
Rb 156,29,38912
Mv 168,8,48
Rt 84,23,49152
Mv 200,8,52
buffer_load_dwordx4 v14, s[8:11], s39 offen offset:2048 lds
)" R"(
Mv 232,8,56
Rt 86,23,51200
Ma 232,8,60
Ma 236,12,60
Rt 88,24,49152
Mv 236,12,56
Mv 204,12,52
Rt 90,24,51200
Mv 172,12,48
Mv 140,12,44
Rt 92,25,49152
Mv 108,12,40
buffer_load_dwordx4 v15, s[8:11], s39 offen offset:3072 lds
Mv 76,12,36
Rt 94,25,51200
Mv 44,12,32
s_add_u32 s44, s43, 512
s_waitcnt lgkmcnt(6)
Mv 48,16,32
Rb 96,17,32768
s_add_u32 m0, s36, 131072
Mv 80,16,36
buffer_load_dwordx4 v26, s[12:15], s44 offen offset:0 lds
Mv 112,16,40
Rb 100,17,34816
Mv 144,16,44
Mv 176,16,48
Rb 104,17,40960
Mv 208,16,52
buffer_load_dwordx4 v27, s[12:15], s44 offen offset:1024 lds
Mv 240,16,56
Rb 108,17,43008
Ma 240,16,60
Ma 244,20,60
Rb 112,17,49152
Mv 244,20,56
s_add_u32 s44, s43, 12800
buffer_load_dwordx4 v26, s[12:15], s44 offen offset:2048 lds
Mv 212,20,52
Rb 116,17,51200
Mv 180,20,48
Mv 148,20,44
Rb 120,17,57344
Mv 116,20,40
buffer_load_dwordx4 v27, s[12:15], s44 offen offset:3072 lds
Mv 84,20,36
Rb 124,17,59392
Mv 52,20,32
Mv 56,24,32
s_add_u32 m0, s36, 135168
s_add_u32 s44, s43, 640
Mv 88,24,36
buffer_load_dwordx4 v26, s[12:15], s44 offen offset:0 lds
Mv 120,24,40
Mv 152,24,44
Mv 184,24,48
Mv 216,24,52
buffer_load_dwordx4 v27, s[12:15], s44 offen offset:1024 lds
Mv 248,24,56
Ma 248,24,60
Ma 252,28,60
Mv 252,28,56
s_add_u32 s44, s43, 12928
buffer_load_dwordx4 v26, s[12:15], s44 offen offset:2048 lds
)" R"(
Mv 220,28,52
Mv 188,28,48
Mv 156,28,44
Mv 124,28,40
buffer_load_dwordx4 v27, s[12:15], s44 offen offset:3072 lds
Mv 92,28,36
Mv 60,28,32
s_waitcnt vmcnt(16) lgkmcnt(0)
s_barrier
Mv 32,64,96
Rt 0,18,0
v_xor_b32 v4, s3, v4
v_cmp_gt_u32_e32 vcc, 2, v4
s_andn2_b32 s28, 0xff, vcc_lo
s_cbranch_scc1 .Lzsp1
.Lzok1:
Mv 64,64,100
s_add_u32 s38, s38, 0xffff0080
v_xor_b32 v4, s3, v4
Mv 96,64,104
Rt 2,18,2048
s_add_u32 m0, s36, 32768
Mv 128,64,108
buffer_load_dwordx4 v8, s[4:7], s38 offen offset:0 lds
Mv 160,64,112
Rt 4,19,0
v_and_b32 v4, 1, v4
Mv 192,64,116
v_cmp_eq_u32_e32 vcc, 1, v4
s_mov_b32 s85, vcc_lo
Mv 224,64,120
Rt 6,19,2048
s_and_b32 s28, s2, 7
Ma 224,64,124
buffer_load_dwordx4 v9, s[4:7], s38 offen offset:1024 lds
Ma 228,68,124
Rt 8,20,0
s_lshr_b32 s28, s85, s28
Mv 228,68,120
s_xor_b32 s28, s28, s34
s_and_b32 s28, s28, 1
Mv 196,68,116
Rt 10,20,2048
s_lshl_b32 s84, s28, 13
Mv 164,68,112
buffer_load_dwordx4 v10, s[4:7], s38 offen offset:2048 lds
Mv 132,68,108
Rt 12,21,0
s_add_u32 s49, s49, s84
Mv 100,68,104
s_add_u32 s50, s50, s84
s_add_u32 s60, s50, 7168
Mv 68,68,100
Rt 14,21,2048
s_add_u32 s52, s52, s84
Mv 36,68,96
buffer_load_dwordx4 v11, s[4:7], s38 offen offset:3072 lds
Mv 40,72,96
Rt 16,22,0
s_add_u32 s65, s65, s84
Mv 72,72,100
s_add_u32 s38, s38, 0x10000
s_lshr_b32 s28, s2, 6
Mv 104,72,104
Rt 18,22,2048
)" R"(
s_add_u32 m0, s36, 36864
Mv 136,72,108
buffer_load_dwordx4 v8, s[4:7], s38 offen offset:0 lds
Mv 168,72,112
Rt 20,23,0
s_lshl_b32 s28, s28, 14
Mv 200,72,116
s_add_u32 s28, s28, s50
s_bfe_u32 s29, s2, 0x20004
Mv 232,72,120
Rt 22,23,2048
s_lshl2_add_u32 s29, s29, s33
Ma 232,72,124
buffer_load_dwordx4 v9, s[4:7], s38 offen offset:1024 lds
Ma 236,76,124
Rt 24,24,0
s_lshl_b32 s29, s29, 9
Mv 236,76,120
s_add_u32 s28, s28, s29
v_min_u32 v4, 3, v0
Mv 204,76,116
Rt 26,24,2048
v_lshlrev_b32 v4, 7, v4
Mv 172,76,112
buffer_load_dwordx4 v10, s[4:7], s38 offen offset:2048 lds
Mv 140,76,108
Rt 28,25,0
Mv 108,76,104
buffer_load_dword v4, v4, s[20:23], s28 offen
Mv 76,76,100
Rt 30,25,2048
s_lshl1_add_u32 s28, s36, s84
Mv 44,76,96
buffer_load_dwordx4 v11, s[4:7], s38 offen offset:3072 lds
Mv 48,80,96
Rb 32,16,0
v_add_u32 v2, s28, v1
Mv 80,80,100
v_add_u32 v3, 0x1000, v2
s_lshl_b32 s28, s34, 13
Mv 112,80,104
Rb 36,16,2048
s_xor_b32 s28, s28, s84
Mv 144,80,108
s_add_u32 s39, s39, 0x40000
s_and_b32 s28, s28, 0x2000
Mv 176,80,112
Rb 40,16,8192
s_add_u32 m0, s37, 49152
Mv 208,80,116
buffer_load_dwordx4 v12, s[8:11], s39 offen offset:0 lds
Mv 240,80,120
Rb 44,16,10240
s_xor_b32 s73, s73, s28
Ma 240,80,124
Ma 244,84,124
Rb 48,16,16384
Mv 244,84,120
Mv 212,84,116
Rb 52,16,18432
Mv 180,84,112
buffer_load_dwordx4 v13, s[8:11], s39 offen offset:1024 lds
Mv 148,84,108
Rb 56,16,24576
)" R"(
Mv 116,84,104
Mv 84,84,100
Rb 60,16,26624
Mv 52,84,96
Mv 56,88,96
Mv 88,88,100
buffer_load_dwordx4 v14, s[8:11], s39 offen offset:2048 lds
Mv 120,88,104
Mv 152,88,108
Mv 184,88,112
Mv 216,88,116
Mv 248,88,120
Ma 248,88,124
buffer_load_dwordx4 v15, s[8:11], s39 offen offset:3072 lds
Ma 252,92,124
Mv 252,92,120
Mv 220,92,116
Mv 188,92,112
Mv 156,92,108
Mv 124,92,104
Mv 92,92,100
Mv 60,92,96
s_waitcnt vmcnt(5) lgkmcnt(0)
s_barrier
Mv 32,0,32
Rt 64,18,16384
Mv 64,0,36
Rb 160,28,32768
v_and_b32 v5, 7, v0
Mv 96,0,40
Rt 66,18,18432
v_lshlrev_b32 v5, 7, v5
Mv 128,0,44
Rb 164,28,34816
v_mov_b32 v6, s3
Mv 160,0,48
Rt 68,19,16384
Mv 192,0,52
Mv 224,0,56
Rt 70,19,18432
Ma 224,0,60
Rb 168,29,32768
Ma 228,4,60
Rt 72,20,16384
Mv 228,4,56
Rb 172,29,34816
Mv 196,4,52
Rt 74,20,18432
Mv 164,4,48
Rb 176,28,36864
Mv 132,4,44
Rt 76,21,16384
Mv 100,4,40
Mv 68,4,36
Rt 78,21,18432
Mv 36,4,32
Rb 180,28,38912
Mv 40,8,32
Rt 80,22,16384
Mv 72,8,36
Rb 184,29,36864
Mv 104,8,40
Rt 82,22,18432
Mv 136,8,44
Rb 188,29,38912
)" R"(
Mv 168,8,48
Rt 84,23,16384
Mv 200,8,52
Mv 232,8,56
Rt 86,23,18432
Ma 232,8,60
Ma 236,12,60
Rt 88,24,16384
Mv 236,12,56
Mv 204,12,52
Rt 90,24,18432
Mv 172,12,48
Mv 140,12,44
Rt 92,25,16384
Mv 108,12,40
Mv 76,12,36
Rt 94,25,18432
v_lshrrev_b32 v10, 3, v0
Mv 44,12,32
v_and_b32 v11, 0x70, v1
v_lshl_or_b32 v10, v10, 13, v11
Mv 48,16,32
Rb 96,17,0
v_lshlrev_b32 v11, 1, v0
Mv 80,16,36
v_and_b32 v11, 32, v11
v_xor_b32 v10, v10, v11
buffer_load_dword v4, v7, s[20:23], s52 offen offset:0 sc0 sc1
Mv 112,16,40
Rb 100,17,2048
s_and_b32 s28, s33, 1
Mv 144,16,44
s_lshl_b32 s28, s28, 6
v_xor_b32 v8, s28, v10
Mv 176,16,48
Rb 104,17,8192
v_xor_b32 v9, 64, v8
Mv 208,16,52
v_add_u32 v9, 0xfc00, v9
v_add_u32 v10, 0x1f800, v8
Mv 240,16,56
Rb 108,17,10240
v_add_u32 v11, 0x1f800, v9
Ma 240,16,60
Ma 244,20,60
Rb 112,17,16384
Mv 244,20,56
Mv 212,20,52
Rb 116,17,18432
Mv 180,20,48
Mv 148,20,44
Rb 120,17,24576
Mv 116,20,40
Mv 84,20,36
Rb 124,17,26624
Mv 52,20,32
Mv 56,24,32
Mv 88,24,36
Mv 120,24,40
Mv 152,24,44
Mv 184,24,48
Mv 216,24,52
Mv 248,24,56
Ma 248,24,60
)" R"(
Ma 252,28,60
Mv 252,28,56
Mv 220,28,52
Mv 188,28,48
Mv 156,28,44
Mv 124,28,40
Mv 92,28,36
Mv 60,28,32
s_waitcnt vmcnt(5) lgkmcnt(0)
s_barrier
Mv 32,64,96
Rt 0,18,32768
Mv 64,64,100
s_add_u32 s92, s42, 0x0
Mv 96,64,104
Rt 2,18,34816
s_add_u32 m0, s41, 131072
Mv 128,64,108
buffer_load_dwordx4 v8, s[16:19], s92 offen offset:0 lds
Mv 160,64,112
Rt 4,19,32768
Mv 192,64,116
Mv 224,64,120
Rt 6,19,34816
Ma 224,64,124
buffer_load_dwordx4 v9, s[16:19], s92 offen offset:1024 lds
Ma 228,68,124
Rt 8,20,32768
Mv 228,68,120
Mv 196,68,116
Rt 10,20,34816
Mv 164,68,112
buffer_load_dwordx4 v10, s[16:19], s92 offen offset:2048 lds
Mv 132,68,108
Rt 12,21,32768
Mv 100,68,104
Mv 68,68,100
Rt 14,21,34816
Mv 36,68,96
buffer_load_dwordx4 v11, s[16:19], s92 offen offset:3072 lds
Mv 40,72,96
Rt 16,22,32768
Mv 72,72,100
s_add_u32 s92, s42, 0x40000
Mv 104,72,104
Rt 18,22,34816
s_add_u32 m0, s41, 135168
Mv 136,72,108
buffer_load_dwordx4 v8, s[16:19], s92 offen offset:0 lds
Mv 168,72,112
Rt 20,23,32768
Mv 200,72,116
Mv 232,72,120
Rt 22,23,34816
Ma 232,72,124
buffer_load_dwordx4 v9, s[16:19], s92 offen offset:1024 lds
Ma 236,76,124
Rt 24,24,32768
Mv 236,76,120
Mv 204,76,116
Rt 26,24,34816
Mv 172,76,112
buffer_load_dwordx4 v10, s[16:19], s92 offen offset:2048 lds
Mv 140,76,108
Rt 28,25,32768
)" R"(
Mv 108,76,104
s_add_u32 s92, s42, 0x1c0000
Mv 76,76,100
Rt 30,25,34816
s_add_u32 m0, s41, 0
Mv 44,76,96
buffer_load_dwordx4 v8, s[16:19], s92 offen offset:0 lds
Mv 48,80,96
Rb 32,16,32768
Mv 80,80,100
Mv 112,80,104
Rb 36,16,34816
Mv 144,80,108
buffer_load_dwordx4 v9, s[16:19], s92 offen offset:1024 lds
Mv 176,80,112
Rb 40,16,40960
Mv 208,80,116
Mv 240,80,120
Rb 44,16,43008
Ma 240,80,124
buffer_load_dwordx4 v10, s[16:19], s92 offen offset:2048 lds
Ma 244,84,124
Rb 48,16,49152
Mv 244,84,120
Mv 212,84,116
Rb 52,16,51200
Mv 180,84,112
buffer_load_dwordx4 v11, s[16:19], s92 offen offset:3072 lds
Mv 148,84,108
Rb 56,16,57344
Mv 116,84,104
s_add_u32 s92, s42, 0x200000
Mv 84,84,100
Rb 60,16,59392
s_add_u32 m0, s41, 4096
Mv 52,84,96
buffer_load_dwordx4 v8, s[16:19], s92 offen offset:0 lds
Mv 56,88,96
Mv 88,88,100
Mv 120,88,104
Mv 152,88,108
buffer_load_dwordx4 v9, s[16:19], s92 offen offset:1024 lds
Mv 184,88,112
Mv 216,88,116
Mv 248,88,120
Ma 248,88,124
buffer_load_dwordx4 v10, s[16:19], s92 offen offset:2048 lds
Ma 252,92,124
Mv 252,92,120
Mv 220,92,116
Mv 188,92,112
Mv 156,92,108
Mv 124,92,104
Mv 92,92,100
Mv 60,92,96
s_waitcnt vmcnt(14) lgkmcnt(4)
s_barrier
v_cmp_eq_u32_e32 vcc, s3, v4
s_andn2_b64 s[28:29], 1, vcc
s_cbranch_scc1 .Lzsp2
.Lzok2:
Mv 32,0,32
Rt 64,18,49152
Mv 64,0,36
Rt 66,18,51200
)" R"(
s_add_u32 s92, s42, 0x80
Mv 96,0,40
Rt 68,19,49152
s_add_u32 m0, s41, 65536
Mv 128,0,44
Rt 70,19,51200
buffer_load_dwordx4 v8, s[16:19], s92 offen offset:0 lds
Mv 132,4,44
Rt 72,20,49152
Mv 100,4,40
Rt 74,20,51200
Mv 68,4,36
Rt 76,21,49152
Mv 36,4,32
Rt 78,21,51200
buffer_load_dwordx4 v9, s[16:19], s92 offen offset:1024 lds
Mv 40,8,32
Rt 80,22,49152
Mv 72,8,36
Rt 82,22,51200
Mv 104,8,40
Rt 84,23,49152
Mv 136,8,44
Rt 86,23,51200
buffer_load_dwordx4 v10, s[16:19], s92 offen offset:2048 lds
Mv 140,12,44
Rt 88,24,49152
Mv 108,12,40
Rt 90,24,51200
Mv 76,12,36
Rt 92,25,49152
Mv 44,12,32
Rt 94,25,51200
buffer_load_dwordx4 v11, s[16:19], s92 offen offset:3072 lds
Mv 48,16,32
Rb 96,17,32768
Mv 80,16,36
Rb 100,17,34816
s_add_u32 s92, s42, 0x40080
Mv 112,16,40
Rb 104,17,40960
s_add_u32 m0, s41, 69632
Mv 144,16,44
Rb 108,17,43008
buffer_load_dwordx4 v8, s[16:19], s92 offen offset:0 lds
Mv 148,20,44
Rb 112,17,49152
Mv 116,20,40
Rb 116,17,51200
Mv 84,20,36
Rb 120,17,57344
Mv 52,20,32
Rb 124,17,59392
buffer_load_dwordx4 v9, s[16:19], s92 offen offset:1024 lds
Mv 56,24,32
Mv 88,24,36
Mv 120,24,40
Mv 152,24,44
buffer_load_dwordx4 v10, s[16:19], s92 offen offset:2048 lds
Mv 156,28,44
Mv 124,28,40
Mv 92,28,36
Mv 60,28,32
s_waitcnt lgkmcnt(0)
s_barrier
)" R"(
Mv 32,64,96
v_bfe_u32 v12, v0, 3, 2
v_lshlrev_b32 v12, 5, v12
Mv 36,68,96
v_and_b32 v4, 3, v0
v_lshl_or_b32 v12, v4, 3, v12
Mv 40,72,96
s_add_u32 m0, s36, 98304
s_add_u32 s44, s43, 768
Mv 44,76,96
buffer_load_dwordx4 v26, s[12:15], s44 offen offset:0 lds
Mv 48,80,96
v_bfe_u32 v4, v0, 2, 2
v_lshl_or_b32 v12, v4, 7, v12
Mv 52,84,96
v_lshrrev_b32 v4, 4, v0
v_lshl_or_b32 v12, v4, 10, v12
Mv 56,88,96
v_xor_b32 v13, 32, v12
v_xor_b32 v14, 64, v12
Mv 60,92,96
buffer_load_dwordx4 v27, s[12:15], s44 offen offset:1024 lds
Mv 64,64,100
v_xor_b32 v15, 96, v12
v_add_u32 v20, 0x20000, v12
Mv 68,68,100
v_add_u32 v21, 0x20000, v13
v_add_u32 v22, 0x20000, v14
Mv 72,72,100
v_add_u32 v23, 0x20000, v15
v_add_u32 v16, 0x10000, v12
Mv 76,76,100
s_add_u32 s44, s43, 13056
buffer_load_dwordx4 v26, s[12:15], s44 offen offset:2048 lds
Mv 80,80,100
v_add_u32 v17, 0x10000, v13
v_add_u32 v18, 0x10000, v14
Mv 84,84,100
v_add_u32 v19, 0x10000, v15
Cv 32,32,33
Mv 88,88,100
Cv 33,34,35
Cv 34,36,37
Mv 92,92,100
buffer_load_dwordx4 v27, s[12:15], s44 offen offset:3072 lds
Mv 96,64,104
Cv 35,38,39
Cv 40,40,41
Mv 100,68,104
Cv 41,42,43
Cv 42,44,45
Mv 104,72,104
s_add_u32 m0, s41, 32768
s_add_u32 s92, s42, 0x1c0080
Mv 108,76,104
buffer_load_dwordx4 v8, s[16:19], s92 offen offset:0 lds
Mv 112,80,104
Cv 43,46,47
Cv 48,48,49
Mv 116,84,104
Cv 49,50,51
Cv 50,52,53
Mv 120,88,104
Cv 51,54,55
)" R"(
Cv 56,56,57
Mv 124,92,104
buffer_store_dwordx4 v[32:35], v2, s[20:23], s45 offen offset:0 sc0 sc1
Mv 128,64,108
Cv 57,58,59
Cv 58,60,61
Mv 132,68,108
Cv 59,62,63
Cv 64,64,65
Mv 136,72,108
Cv 65,66,67
Cv 66,68,69
Mv 140,76,108
buffer_load_dwordx4 v9, s[16:19], s92 offen offset:1024 lds
Mv 144,80,108
Cv 67,70,71
Cv 72,72,73
Mv 148,84,108
Cv 73,74,75
Cv 74,76,77
Mv 152,88,108
Cv 75,78,79
Cv 80,80,81
Mv 156,92,108
buffer_store_dwordx4 v[40:43], v2, s[20:23], s45 offen offset:1024 sc0 sc1
Mv 160,0,48
Cv 81,82,83
Cv 82,84,85
Mv 160,64,112
Cv 83,86,87
Cv 88,88,89
Mv 164,4,48
Cv 89,90,91
Cv 90,92,93
Mv 164,68,112
buffer_load_dwordx4 v10, s[16:19], s92 offen offset:2048 lds
Mv 168,8,48
Cv 91,94,95
Cv 96,96,97
Mv 168,72,112
Cv 97,98,99
Cv 98,100,101
Mv 172,12,48
Cv 99,102,103
Cv 104,104,105
Mv 172,76,112
buffer_store_dwordx4 v[48:51], v2, s[20:23], s45 offen offset:2048 sc0 sc1
Mv 176,16,48
Cv 105,106,107
Cv 106,108,109
Mv 176,80,112
Cv 107,110,111
Cv 112,112,113
Mv 180,20,48
Cv 113,114,115
Cv 114,116,117
Mv 180,84,112
buffer_load_dwordx4 v11, s[16:19], s92 offen offset:3072 lds
Mv 184,24,48
Cv 115,118,119
Cv 120,120,121
Mv 184,88,112
Cv 121,122,123
Cv 122,124,125
)" R"(
Mv 188,28,48
Cv 123,126,127
Cv 128,128,129
Mv 188,92,112
buffer_store_dwordx4 v[56:59], v2, s[20:23], s45 offen offset:3072 sc0 sc1
Mv 192,0,52
Cv 129,130,131
Cv 130,132,133
Mv 192,64,116
Cv 131,134,135
Cv 136,136,137
Mv 196,4,52
s_add_u32 m0, s41, 36864
s_add_u32 s92, s42, 0x200080
Mv 196,68,116
buffer_load_dwordx4 v8, s[16:19], s92 offen offset:0 lds
Mv 200,8,52
Cv 137,138,139
Cv 138,140,141
Mv 200,72,116
Cv 139,142,143
Cv 144,144,145
Mv 204,12,52
Cv 145,146,147
Cv 146,148,149
Mv 204,76,116
buffer_store_dwordx4 v[64:67], v3, s[20:23], s45 offen offset:0 sc0 sc1
Mv 208,16,52
Cv 147,150,151
Cv 152,152,153
Mv 208,80,116
Cv 153,154,155
Cv 154,156,157
Mv 212,20,52
Cv 155,158,159
Mv 212,84,116
buffer_load_dwordx4 v9, s[16:19], s92 offen offset:1024 lds
Mv 216,24,52
Mv 216,88,116
Mv 220,28,52
Mv 220,92,116
buffer_store_dwordx4 v[72:75], v3, s[20:23], s45 offen offset:1024 sc0 sc1
Mv 224,0,56
Ma 224,0,60
Ma 228,4,60
Mv 228,4,56
buffer_load_dwordx4 v10, s[16:19], s92 offen offset:2048 lds
Mv 232,8,56
Ma 232,8,60
Ma 236,12,60
Mv 236,12,56
s_waitcnt vmcnt(24)
s_barrier
Mv 240,16,56
buffer_store_dwordx4 v[80:83], v3, s[20:23], s45 offen offset:2048 sc0 sc1
Ma 240,16,60
Ma 244,20,60
Mv 244,20,56
Mv 248,24,56
Ma 248,24,60
Ma 252,28,60
Mv 252,28,56
buffer_store_dwordx4 v[88:91], v3, s[20:23], s45 offen offset:3072 sc0 sc1
Mv 224,64,120
Rt 16,20,0
)" R"(
Mv 228,68,120
Rt 18,20,512
Mv 232,72,120
Rt 20,21,0
Mv 236,76,120
Rt 22,21,512
Mv 240,80,120
Rt 24,22,0
Mv 244,84,120
Rt 26,22,512
Mv 248,88,120
Rt 28,23,0
Mv 252,92,120
Rt 30,23,512
Ma 224,64,124
Rt 32,20,4096
Ma 228,68,124
Rt 34,20,4608
Ma 232,72,124
Rt 36,21,4096
Ma 236,76,124
Rt 38,21,4608
Ma 240,80,124
Rt 40,22,4096
Ma 244,84,124
Rt 42,22,4608
Ma 248,88,124
Rt 44,23,4096
Ma 252,92,124
Rt 46,23,4608
s_waitcnt vmcnt(15) lgkmcnt(8)
s_branch .LzQA
.LzAV:
Mz 32,32,0
Rt 64,18,16384
Mz 36,36,0
s_lshl1_add_u32 s40, s34, s91
s_lshl_b32 s40, s40, 12
Mz 96,40,0
Rt 66,18,18432
s_and_b32 s29, s38, 0xfffe0000
Mz 100,44,0
s_add_u32 s39, s39, 0x40000
s_add_u32 s40, s40, s29
Mz 160,48,0
Rt 68,19,16384
s_add_u32 m0, s37, 0
Mz 164,52,0
buffer_load_dwordx4 v12, s[8:11], s39 offen offset:0 lds
Mz 224,56,0
Rt 70,19,18432
v_min_u32 v3, 31, v0
Mz 228,60,0
v_lshlrev_b32 v3, 7, v3
Mz 236,60,4
Rt 72,20,16384
Mz 232,56,4
Mz 172,52,4
Rt 74,20,18432
Mz 168,48,4
Mz 108,44,4
Rt 76,21,16384
Mz 104,40,4
buffer_load_dwordx4 v13, s[8:11], s39 offen offset:1024 lds
)" R"(
Mz 44,36,4
Rt 78,21,18432
Mz 40,32,4
Mz 48,32,8
Rt 80,22,16384
Mz 52,36,8
Mz 112,40,8
Rt 82,22,18432
Mz 116,44,8
Mz 176,48,8
Rt 84,23,16384
Mz 180,52,8
buffer_load_dwordx4 v14, s[8:11], s39 offen offset:2048 lds
Mz 240,56,8
Rt 86,23,18432
Mz 244,60,8
Mz 252,60,12
Rt 88,24,16384
Mz 248,56,12
Mz 188,52,12
Rt 90,24,18432
Mz 184,48,12
Mz 124,44,12
Rt 92,25,16384
Mz 120,40,12
buffer_load_dwordx4 v15, s[8:11], s39 offen offset:3072 lds
Mz 60,36,12
Rt 94,25,18432
Mz 56,32,12
Mz 64,32,16
Rb 96,17,0
Mz 68,36,16
buffer_load_dword v6, v3, s[4:7], s40 offen
Mz 128,40,16
Rb 100,17,2048
Mz 132,44,16
Mz 192,48,16
Rb 104,17,8192
s_mov_b32 s28, s35
Mz 196,52,16
s_lshl2_add_u32 s28, s28, s33
s_mul_i32 s43, s28, 28672
v_mfma_f32_16x16x32_bf16 a[224:227], a[56:59], a[16:19], 0
Rb 108,17,10240
v_and_b32 v26, 0x70, v1
v_mfma_f32_16x16x32_bf16 a[228:231], a[60:63], a[16:19], 0
v_and_b32 v4, 48, v0
v_xor_b32 v26, v26, v4
v_mfma_f32_16x16x32_bf16 a[236:239], a[60:63], a[20:23], 0
Rb 112,17,16384
v_lshrrev_b32 v4, 3, v0
v_mfma_f32_16x16x32_bf16 a[232:235], a[56:59], a[20:23], 0
v_mul_u32_u24 v4, 896, v4
v_add_u32 v26, v26, v4
Mz 204,52,20
Rb 116,17,18432
v_xor_b32 v27, 64, v26
Mz 200,48,20
v_add_u32 v27, 6144, v27
v_bfe_u32 v28, v0, 1, 3
Mz 140,44,20
Rb 120,17,24576
v_lshrrev_b32 v4, 4, v0
Mz 136,40,20
)" R"(
v_xor_b32 v28, v28, v4
v_lshlrev_b32 v28, 4, v28
Mz 76,36,20
Rb 124,17,26624
v_and_b32 v4, 15, v0
Mz 72,32,20
v_lshl_or_b32 v28, v4, 7, v28
s_add_u32 s28, s36, 0x18000
Mz 80,32,24
v_add_u32 v28, s28, v28
v_xor_b32 v29, 64, v28
Mz 84,36,24
Mz 144,40,24
Mz 148,44,24
Mz 208,48,24
Mz 212,52,24
v_mfma_f32_16x16x32_bf16 a[240:243], a[56:59], a[24:27], 0
v_mfma_f32_16x16x32_bf16 a[244:247], a[60:63], a[24:27], 0
v_mfma_f32_16x16x32_bf16 a[252:255], a[60:63], a[28:31], 0
v_mfma_f32_16x16x32_bf16 a[248:251], a[56:59], a[28:31], 0
Mz 220,52,28
Mz 216,48,28
Mz 156,44,28
Mz 152,40,28
Mz 92,36,28
Mz 88,32,28
s_waitcnt vmcnt(11) lgkmcnt(0)
s_barrier
Mv 32,96,64
Rt 0,18,32768
s_bfe_u32 s88, s2, 0x20005
Mv 36,100,64
s_add_u32 s38, s38, 0xffff0080
s_and_b32 s28, s2, 15
Mv 96,104,64
Rt 2,18,34816
s_add_u32 m0, s36, 0
Mv 100,108,64
buffer_load_dwordx4 v8, s[4:7], s38 offen offset:0 lds
Mv 160,112,64
Rt 4,19,32768
s_lshl4_add_u32 s88, s88, s28
Mv 164,116,64
s_lshl2_add_u32 s88, s88, s33
s_mul_i32 s88, s88, 4096
Mv 224,120,64
Rt 6,19,34816
s_add_u32 s88, s88, 0x100000
Mv 228,124,64
buffer_load_dwordx4 v9, s[4:7], s38 offen offset:1024 lds
Mv 236,124,68
Rt 8,20,32768
v_min_u32 v5, 31, v0
Mv 232,120,68
v_lshlrev_b32 v5, 7, v5
s_mov_b32 s90, 0x100000
Mv 172,116,68
Rt 10,20,34816
s_mov_b64 s[24:25], s[8:9]
Mv 168,112,68
buffer_load_dwordx4 v10, s[4:7], s38 offen offset:2048 lds
Mv 108,108,68
Rt 12,21,32768
s_mov_b64 s[26:27], s[10:11]
Mv 104,104,68
)" R"(
Mv 44,100,68
Rt 14,21,34816
Mv 40,96,68
buffer_load_dwordx4 v11, s[4:7], s38 offen offset:3072 lds
Mv 48,96,72
Rt 16,22,32768
Mv 52,100,72
s_add_u32 s38, s38, 0x10000
Mv 112,104,72
Rt 18,22,34816
s_add_u32 m0, s36, 4096
Mv 116,108,72
buffer_load_dwordx4 v8, s[4:7], s38 offen offset:0 lds
Mv 176,112,72
Rt 20,23,32768
Mv 180,116,72
Mv 240,120,72
Rt 22,23,34816
Mv 244,124,72
buffer_load_dwordx4 v9, s[4:7], s38 offen offset:1024 lds
Mv 252,124,76
Rt 24,24,32768
Mv 248,120,76
Mv 188,116,76
Rt 26,24,34816
Mv 184,112,76
buffer_load_dwordx4 v10, s[4:7], s38 offen offset:2048 lds
Mv 124,108,76
Rt 28,25,32768
Mv 120,104,76
Mv 60,100,76
Rt 30,25,34816
Mv 56,96,76
buffer_load_dwordx4 v11, s[4:7], s38 offen offset:3072 lds
Mv 64,96,80
Rb 32,16,32768
Mv 68,100,80
Mv 128,104,80
Rb 36,16,34816
Mv 132,108,80
s_add_u32 s39, s39, 0x40000
Mv 192,112,80
Rb 40,16,40960
s_add_u32 m0, s37, 16384
Mv 196,116,80
buffer_load_dwordx4 v12, s[8:11], s39 offen offset:0 lds
Ma 224,120,80
Rb 44,16,43008
Ma 228,124,80
Ma 236,124,84
Rb 48,16,49152
Ma 232,120,84
Mv 204,116,84
Rb 52,16,51200
Mv 200,112,84
buffer_load_dwordx4 v13, s[8:11], s39 offen offset:1024 lds
Mv 140,108,84
Rb 56,16,57344
Mv 136,104,84
Mv 76,100,84
Rb 60,16,59392
Mv 72,96,84
Mv 80,96,88
Mv 84,100,88
)" R"(
buffer_load_dwordx4 v14, s[8:11], s39 offen offset:2048 lds
Mv 144,104,88
Mv 148,108,88
Mv 208,112,88
Mv 212,116,88
Ma 240,120,88
Ma 244,124,88
buffer_load_dwordx4 v15, s[8:11], s39 offen offset:3072 lds
Ma 252,124,92
Ma 248,120,92
s_add_u32 s88, s88, s90
Mv 220,116,92
Mv 216,112,92
buffer_load_dword v6, v5, s[24:27], s88 offen
Mv 156,108,92
Mv 152,104,92
Mv 92,100,92
Mv 88,96,92
s_waitcnt vmcnt(20) lgkmcnt(0)
s_barrier
Mv 32,32,0
Rt 64,18,49152
s_lshl_b32 s28, s34, 21
Mv 36,36,0
s_lshl_b32 s29, s91, 25
s_add_u32 s28, s28, s29
Mv 96,40,0
Rt 66,18,51200
s_bfe_u32 s29, s63, 0x80000
Mv 100,44,0
s_add_u32 s39, s39, 0x40000
s_lshl_b32 s29, s29, 16
Mv 160,48,0
Rt 68,19,49152
s_add_u32 m0, s37, 32768
Mv 164,52,0
buffer_load_dwordx4 v12, s[8:11], s39 offen offset:0 lds
Mv 224,56,0
Rt 70,19,51200
s_add_u32 s45, s28, s29
Mv 228,60,0
s_bfe_u32 s29, s63, 0x80008
s_lshl_b32 s29, s29, 16
Mv 236,60,4
Rt 72,20,49152
s_add_u32 s46, s28, s29
Mv 232,56,4
s_bfe_u32 s29, s63, 0x80010
s_lshl_b32 s29, s29, 16
Mv 172,52,4
Rt 74,20,51200
s_add_u32 s47, s28, s29
Mv 168,48,4
s_bfe_u32 s29, s63, 0x80018
s_lshl_b32 s29, s29, 16
Mv 108,44,4
Rt 76,21,49152
s_add_u32 s48, s28, s29
Mv 104,40,4
buffer_load_dwordx4 v13, s[8:11], s39 offen offset:1024 lds
Mv 44,36,4
Rt 78,21,51200
s_lshl_b32 s50, s34, 21
Mv 40,32,4
s_add_u32 s50, s50, 0x6140000
)" R"(
s_lshl2_add_u32 s28, s91, s93
Mv 48,32,8
Rt 80,22,49152
s_lshl_b32 s28, s28, 7
Mv 52,36,8
s_add_u32 s49, s28, s50
s_lshl_b32 s52, s35, 7
Mv 112,40,8
Rt 82,22,51200
s_add_u32 s52, s52, s50
Mv 116,44,8
s_add_u32 s52, s52, 0xd000
s_lshl4_add_u32 s28, s34, s35
Mv 176,48,8
Rt 84,23,49152
s_lshl_b32 s51, s28, 17
Mv 180,52,8
buffer_load_dwordx4 v14, s[8:11], s39 offen offset:2048 lds
Mv 240,56,8
Rt 86,23,51200
s_add_u32 s51, s51, 0x4000000
Mv 244,60,8
s_cmp_lg_u32 s33, 0
s_cmov_b32 s49, 0x40000000
Mv 252,60,12
Rt 88,24,49152
s_mov_b32 s53, 0x40000
Mv 248,56,12
s_mov_b32 s54, 0x3db8aa3b
s_mul_i32 s41, s33, 7168
Mv 188,52,12
Rt 90,24,51200
s_mul_i32 s42, s33, 0x70000
Mv 184,48,12
s_lshl_b32 s28, s34, 9
s_add_u32 s42, s42, s28
Mv 124,44,12
Rt 92,25,49152
s_lshl_b32 s89, s34, 21
Mv 120,40,12
buffer_load_dwordx4 v15, s[8:11], s39 offen offset:3072 lds
Mv 60,36,12
Rt 94,25,51200
s_mov_b32 s62, 0x4e800000
Mv 56,32,12
s_add_u32 s67, s89, 0x6000000
s_lshl3_add_u32 s28, s34, s35
Mv 64,32,16
Rb 96,17,32768
s_add_u32 s64, s51, 0x100000
s_add_u32 m0, s36, 131072
s_add_u32 s44, s43, 0
Mv 68,36,16
s_lshl2_add_u32 s28, s28, s33
s_lshl2_add_u32 s65, s35, s33
buffer_load_dwordx4 v26, s[12:15], s44 offen offset:0 lds
Mv 128,40,16
Rb 100,17,34816
s_lshl_b32 s65, s65, 7
Mv 132,44,16
s_add_u32 s65, s65, s50
s_add_u32 s65, s65, 0xc000
Mv 192,48,16
Rb 104,17,40960
)" R"(
s_lshl_b32 s66, s28, 10
Mv 196,52,16
s_add_u32 s66, s66, 0xc000000
s_mov_b32 s28, s35
buffer_load_dwordx4 v27, s[12:15], s44 offen offset:1024 lds
Ma 224,56,16
Rb 108,17,43008
s_lshl2_add_u32 s28, s28, s33
Ma 228,60,16
s_lshl_b32 s29, s28, 5
s_add_u32 s29, s29, s34
Ma 236,60,20
Rb 112,17,49152
s_lshl_b32 s73, s29, 15
Ma 232,56,20
s_add_u32 s73, s73, 0x8000000
s_lshr_b32 s29, s36, 1
s_add_u32 s44, s43, 12288
buffer_load_dwordx4 v26, s[12:15], s44 offen offset:2048 lds
Mv 204,52,20
Rb 116,17,51200
s_and_b32 s29, s29, 0x2000
Mv 200,48,20
s_add_u32 s73, s73, s29
s_lshl4_add_u32 s28, s28, s34
Mv 140,44,20
Rb 120,17,57344
s_lshl_b32 s68, s28, 8
Mv 136,40,20
s_add_u32 s68, s68, 0xc110000
s_add_u32 s55, s89, 0x120000
buffer_load_dwordx4 v27, s[12:15], s44 offen offset:3072 lds
Mv 76,36,20
Rb 124,17,59392
s_lshl_b32 s28, s35, 1
Mv 72,32,20
s_sub_u32 s57, 14, s28
s_mov_b32 s59, s89
Mv 80,32,24
s_mov_b32 s58, 2
s_add_u32 m0, s36, 135168
s_add_u32 s44, s43, 128
Mv 84,36,24
s_cmp_eq_u32 s35, 7
s_cmov_b32 s55, s89
buffer_load_dwordx4 v26, s[12:15], s44 offen offset:0 lds
Mv 144,40,24
s_cmp_eq_u32 s35, 7
s_cmov_b32 s57, 16
Mv 148,44,24
s_cmp_eq_u32 s35, 7
s_cmov_b32 s59, s67
Mv 208,48,24
s_add_u32 s56, s55, 0x2000000
Mv 212,52,24
buffer_load_dwordx4 v27, s[12:15], s44 offen offset:1024 lds
Ma 240,56,24
Ma 244,60,24
Ma 252,60,28
Ma 248,56,28
s_add_u32 s44, s43, 12416
buffer_load_dwordx4 v26, s[12:15], s44 offen offset:2048 lds
Mv 220,52,28
Mv 216,48,28
)" R"(
Mv 156,44,28
Mv 152,40,28
buffer_load_dwordx4 v27, s[12:15], s44 offen offset:3072 lds
Mv 92,36,28
Mv 88,32,28
s_waitcnt vmcnt(17) lgkmcnt(0)
s_barrier
Mv 32,96,64
Rt 0,18,0
Mv 36,100,64
s_add_u32 s38, s38, 0xffff0080
Mv 96,104,64
Rt 2,18,2048
s_add_u32 m0, s36, 32768
Mv 100,108,64
buffer_load_dwordx4 v8, s[4:7], s38 offen offset:0 lds
Mv 160,112,64
Rt 4,19,0
Mv 164,116,64
Mv 224,120,64
Rt 6,19,2048
Mv 228,124,64
buffer_load_dwordx4 v9, s[4:7], s38 offen offset:1024 lds
Mv 236,124,68
Rt 8,20,0
s_lshl2_add_u32 s28, s2, s33
Mv 232,120,68
s_lshl_b32 s28, s28, 11
s_add_u32 s28, s28, 0xc100000
Mv 172,116,68
Rt 10,20,2048
v_min_u32 v3, 15, v0
Mv 168,112,68
buffer_load_dwordx4 v10, s[4:7], s38 offen offset:2048 lds
Mv 108,108,68
Rt 12,21,0
v_lshlrev_b32 v3, 7, v3
Mv 104,104,68
Mv 44,100,68
Rt 14,21,2048
Mv 40,96,68
buffer_load_dwordx4 v11, s[4:7], s38 offen offset:3072 lds
Mv 48,96,72
Rt 16,22,0
Mv 52,100,72
s_add_u32 s38, s38, 0x10000
Mv 112,104,72
Rt 18,22,2048
s_add_u32 m0, s36, 36864
Mv 116,108,72
buffer_load_dwordx4 v8, s[4:7], s38 offen offset:0 lds
Mv 176,112,72
Rt 20,23,0
Mv 180,116,72
Mv 240,120,72
Rt 22,23,2048
Mv 244,124,72
buffer_load_dwordx4 v9, s[4:7], s38 offen offset:1024 lds
Mv 252,124,76
Rt 24,24,0
Mv 248,120,76
Mv 188,116,76
Rt 26,24,2048
Mv 184,112,76
buffer_load_dwordx4 v10, s[4:7], s38 offen offset:2048 lds
)" R"(
Mv 124,108,76
Rt 28,25,0
Mv 120,104,76
Mv 60,100,76
Rt 30,25,2048
Mv 56,96,76
buffer_load_dwordx4 v11, s[4:7], s38 offen offset:3072 lds
Mv 64,96,80
Rb 32,16,0
Mv 68,100,80
Mv 128,104,80
Rb 36,16,2048
Mv 132,108,80
s_add_u32 s39, s39, 0x40000
Mv 192,112,80
Rb 40,16,8192
s_add_u32 m0, s37, 49152
Mv 196,116,80
buffer_load_dwordx4 v12, s[8:11], s39 offen offset:0 lds
Ma 224,120,80
Rb 44,16,10240
Ma 228,124,80
Ma 236,124,84
Rb 48,16,16384
Ma 232,120,84
Mv 204,116,84
Rb 52,16,18432
Mv 200,112,84
buffer_load_dwordx4 v13, s[8:11], s39 offen offset:1024 lds
Mv 140,108,84
Rb 56,16,24576
Mv 136,104,84
Mv 76,100,84
Rb 60,16,26624
Mv 72,96,84
Mv 80,96,88
Mv 84,100,88
buffer_load_dwordx4 v14, s[8:11], s39 offen offset:2048 lds
Mv 144,104,88
Mv 148,108,88
Mv 208,112,88
Mv 212,116,88
Ma 240,120,88
Ma 244,124,88
buffer_load_dwordx4 v15, s[8:11], s39 offen offset:3072 lds
Ma 252,124,92
Ma 248,120,92
s_add_u32 s88, s88, s90
s_mov_b32 s40, 5
Mv 220,116,92
Mv 216,112,92
buffer_load_dword v6, v5, s[24:27], s88 offen
Mv 156,108,92
Mv 152,104,92
buffer_load_dword v6, v3, s[20:23], s28 offen
Mv 92,100,92
Mv 88,96,92
s_waitcnt vmcnt(27) lgkmcnt(0)
s_barrier
.LzPAV:
Mv 32,32,0
Rt 64,18,16384
Mv 36,36,0
Mv 96,40,0
Rt 66,18,18432
)" R"(
Mv 100,44,0
s_add_u32 s39, s39, 0x40000
Mv 160,48,0
Rt 68,19,16384
s_add_u32 m0, s37, 0
Mv 164,52,0
buffer_load_dwordx4 v12, s[8:11], s39 offen offset:0 lds
Mv 224,56,0
Rt 70,19,18432
Mv 228,60,0
Mv 236,60,4
Rt 72,20,16384
Mv 232,56,4
Mv 172,52,4
Rt 74,20,18432
Mv 168,48,4
Mv 108,44,4
Rt 76,21,16384
Mv 104,40,4
buffer_load_dwordx4 v13, s[8:11], s39 offen offset:1024 lds
Mv 44,36,4
Rt 78,21,18432
Mv 40,32,4
Mv 48,32,8
Rt 80,22,16384
Mv 52,36,8
Mv 112,40,8
Rt 82,22,18432
Mv 116,44,8
Mv 176,48,8
Rt 84,23,16384
Mv 180,52,8
buffer_load_dwordx4 v14, s[8:11], s39 offen offset:2048 lds
Mv 240,56,8
Rt 86,23,18432
Mv 244,60,8
Mv 252,60,12
Rt 88,24,16384
Mv 248,56,12
Mv 188,52,12
Rt 90,24,18432
Mv 184,48,12
Mv 124,44,12
Rt 92,25,16384
Mv 120,40,12
buffer_load_dwordx4 v15, s[8:11], s39 offen offset:3072 lds
Mv 60,36,12
Rt 94,25,18432
Mv 56,32,12
Mv 64,32,16
Rb 96,17,0
Mv 68,36,16
Mv 128,40,16
Rb 100,17,2048
Mv 132,44,16
Mv 192,48,16
Rb 104,17,8192
Mv 196,52,16
Ma 224,56,16
Rb 108,17,10240
Ma 228,60,16
Ma 236,60,20
Rb 112,17,16384
Ma 232,56,20
)" R"(
Mv 204,52,20
Rb 116,17,18432
Mv 200,48,20
Mv 140,44,20
Rb 120,17,24576
Mv 136,40,20
Mv 76,36,20
Rb 124,17,26624
Mv 72,32,20
Mv 80,32,24
Mv 84,36,24
Mv 144,40,24
Mv 148,44,24
Mv 208,48,24
Mv 212,52,24
Ma 240,56,24
Ma 244,60,24
Ma 252,60,28
Ma 248,56,28
Mv 220,52,28
Mv 216,48,28
Mv 156,44,28
Mv 152,40,28
Mv 92,36,28
Mv 88,32,28
s_waitcnt vmcnt(9) lgkmcnt(0)
s_barrier
Mv 32,96,64
Rt 0,18,32768
Mv 36,100,64
s_add_u32 s38, s38, 0xffff0080
Mv 96,104,64
Rt 2,18,34816
s_add_u32 m0, s36, 0
Mv 100,108,64
buffer_load_dwordx4 v8, s[4:7], s38 offen offset:0 lds
Mv 160,112,64
Rt 4,19,32768
Mv 164,116,64
Mv 224,120,64
Rt 6,19,34816
Mv 228,124,64
buffer_load_dwordx4 v9, s[4:7], s38 offen offset:1024 lds
Mv 236,124,68
Rt 8,20,32768
Mv 232,120,68
Mv 172,116,68
Rt 10,20,34816
Mv 168,112,68
buffer_load_dwordx4 v10, s[4:7], s38 offen offset:2048 lds
Mv 108,108,68
Rt 12,21,32768
Mv 104,104,68
Mv 44,100,68
Rt 14,21,34816
Mv 40,96,68
buffer_load_dwordx4 v11, s[4:7], s38 offen offset:3072 lds
Mv 48,96,72
Rt 16,22,32768
Mv 52,100,72
s_add_u32 s38, s38, 0x10000
Mv 112,104,72
Rt 18,22,34816
s_add_u32 m0, s36, 4096
Mv 116,108,72
)" R"(
buffer_load_dwordx4 v8, s[4:7], s38 offen offset:0 lds
Mv 176,112,72
Rt 20,23,32768
Mv 180,116,72
Mv 240,120,72
Rt 22,23,34816
Mv 244,124,72
buffer_load_dwordx4 v9, s[4:7], s38 offen offset:1024 lds
Mv 252,124,76
Rt 24,24,32768
Mv 248,120,76
Mv 188,116,76
Rt 26,24,34816
Mv 184,112,76
buffer_load_dwordx4 v10, s[4:7], s38 offen offset:2048 lds
Mv 124,108,76
Rt 28,25,32768
Mv 120,104,76
Mv 60,100,76
Rt 30,25,34816
Mv 56,96,76
buffer_load_dwordx4 v11, s[4:7], s38 offen offset:3072 lds
Mv 64,96,80
Rb 32,16,32768
Mv 68,100,80
Mv 128,104,80
Rb 36,16,34816
Mv 132,108,80
s_add_u32 s39, s39, 0x40000
Mv 192,112,80
Rb 40,16,40960
s_add_u32 m0, s37, 16384
Mv 196,116,80
buffer_load_dwordx4 v12, s[8:11], s39 offen offset:0 lds
Ma 224,120,80
Rb 44,16,43008
Ma 228,124,80
Ma 236,124,84
Rb 48,16,49152
Ma 232,120,84
Mv 204,116,84
Rb 52,16,51200
Mv 200,112,84
buffer_load_dwordx4 v13, s[8:11], s39 offen offset:1024 lds
Mv 140,108,84
Rb 56,16,57344
Mv 136,104,84
Mv 76,100,84
Rb 60,16,59392
Mv 72,96,84
Mv 80,96,88
Mv 84,100,88
buffer_load_dwordx4 v14, s[8:11], s39 offen offset:2048 lds
Mv 144,104,88
Mv 148,108,88
Mv 208,112,88
Mv 212,116,88
Ma 240,120,88
Ma 244,124,88
buffer_load_dwordx4 v15, s[8:11], s39 offen offset:3072 lds
Ma 252,124,92
Ma 248,120,92
s_add_u32 s88, s88, s90
Mv 220,116,92
)" R"(
Mv 216,112,92
buffer_load_dword v6, v5, s[24:27], s88 offen
Mv 156,108,92
Mv 152,104,92
Mv 92,100,92
Mv 88,96,92
s_waitcnt vmcnt(18) lgkmcnt(0)
s_barrier
Mv 32,32,0
Rt 64,18,49152
Mv 36,36,0
Mv 96,40,0
Rt 66,18,51200
Mv 100,44,0
s_add_u32 s39, s39, 0x40000
Mv 160,48,0
Rt 68,19,49152
s_add_u32 m0, s37, 32768
Mv 164,52,0
buffer_load_dwordx4 v12, s[8:11], s39 offen offset:0 lds
Mv 224,56,0
Rt 70,19,51200
Mv 228,60,0
Mv 236,60,4
Rt 72,20,49152
Mv 232,56,4
Mv 172,52,4
Rt 74,20,51200
Mv 168,48,4
Mv 108,44,4
Rt 76,21,49152
Mv 104,40,4
buffer_load_dwordx4 v13, s[8:11], s39 offen offset:1024 lds
Mv 44,36,4
Rt 78,21,51200
Mv 40,32,4
Mv 48,32,8
Rt 80,22,49152
Mv 52,36,8
Mv 112,40,8
Rt 82,22,51200
Mv 116,44,8
Mv 176,48,8
Rt 84,23,49152
Mv 180,52,8
buffer_load_dwordx4 v14, s[8:11], s39 offen offset:2048 lds
Mv 240,56,8
Rt 86,23,51200
Mv 244,60,8
Mv 252,60,12
Rt 88,24,49152
Mv 248,56,12
Mv 188,52,12
Rt 90,24,51200
Mv 184,48,12
Mv 124,44,12
Rt 92,25,49152
Mv 120,40,12
buffer_load_dwordx4 v15, s[8:11], s39 offen offset:3072 lds
Mv 60,36,12
Rt 94,25,51200
Mv 56,32,12
Mv 64,32,16
Rb 96,17,32768
Mv 68,36,16
)" R"(
Mv 128,40,16
Rb 100,17,34816
Mv 132,44,16
Mv 192,48,16
Rb 104,17,40960
Mv 196,52,16
Ma 224,56,16
Rb 108,17,43008
Ma 228,60,16
Ma 236,60,20
Rb 112,17,49152
Ma 232,56,20
Mv 204,52,20
Rb 116,17,51200
Mv 200,48,20
Mv 140,44,20
Rb 120,17,57344
Mv 136,40,20
Mv 76,36,20
Rb 124,17,59392
Mv 72,32,20
Mv 80,32,24
Mv 84,36,24
Mv 144,40,24
Mv 148,44,24
Mv 208,48,24
Mv 212,52,24
Ma 240,56,24
Ma 244,60,24
Ma 252,60,28
Ma 248,56,28
Mv 220,52,28
Mv 216,48,28
Mv 156,44,28
Mv 152,40,28
Mv 92,36,28
Mv 88,32,28
s_waitcnt vmcnt(9) lgkmcnt(0)
s_barrier
Mv 32,96,64
Rt 0,18,0
Mv 36,100,64
s_add_u32 s38, s38, 0xffff0080
Mv 96,104,64
Rt 2,18,2048
s_add_u32 m0, s36, 32768
Mv 100,108,64
buffer_load_dwordx4 v8, s[4:7], s38 offen offset:0 lds
Mv 160,112,64
Rt 4,19,0
Mv 164,116,64
Mv 224,120,64
Rt 6,19,2048
Mv 228,124,64
buffer_load_dwordx4 v9, s[4:7], s38 offen offset:1024 lds
Mv 236,124,68
Rt 8,20,0
Mv 232,120,68
Mv 172,116,68
Rt 10,20,2048
Mv 168,112,68
buffer_load_dwordx4 v10, s[4:7], s38 offen offset:2048 lds
Mv 108,108,68
Rt 12,21,0
Mv 104,104,68
)" R"(
Mv 44,100,68
Rt 14,21,2048
Mv 40,96,68
buffer_load_dwordx4 v11, s[4:7], s38 offen offset:3072 lds
Mv 48,96,72
Rt 16,22,0
Mv 52,100,72
s_add_u32 s38, s38, 0x10000
Mv 112,104,72
Rt 18,22,2048
s_add_u32 m0, s36, 36864
Mv 116,108,72
buffer_load_dwordx4 v8, s[4:7], s38 offen offset:0 lds
Mv 176,112,72
Rt 20,23,0
Mv 180,116,72
Mv 240,120,72
Rt 22,23,2048
Mv 244,124,72
buffer_load_dwordx4 v9, s[4:7], s38 offen offset:1024 lds
Mv 252,124,76
Rt 24,24,0
Mv 248,120,76
Mv 188,116,76
Rt 26,24,2048
Mv 184,112,76
buffer_load_dwordx4 v10, s[4:7], s38 offen offset:2048 lds
Mv 124,108,76
Rt 28,25,0
Mv 120,104,76
Mv 60,100,76
Rt 30,25,2048
Mv 56,96,76
buffer_load_dwordx4 v11, s[4:7], s38 offen offset:3072 lds
Mv 64,96,80
Rb 32,16,0
Mv 68,100,80
Mv 128,104,80
Rb 36,16,2048
Mv 132,108,80
s_add_u32 s39, s39, 0x40000
Mv 192,112,80
Rb 40,16,8192
s_add_u32 m0, s37, 49152
Mv 196,116,80
buffer_load_dwordx4 v12, s[8:11], s39 offen offset:0 lds
Ma 224,120,80
Rb 44,16,10240
Ma 228,124,80
Ma 236,124,84
Rb 48,16,16384
Ma 232,120,84
Mv 204,116,84
Rb 52,16,18432
Mv 200,112,84
buffer_load_dwordx4 v13, s[8:11], s39 offen offset:1024 lds
Mv 140,108,84
Rb 56,16,24576
Mv 136,104,84
Mv 76,100,84
Rb 60,16,26624
Mv 72,96,84
Mv 80,96,88
Mv 84,100,88
)" R"(
buffer_load_dwordx4 v14, s[8:11], s39 offen offset:2048 lds
Mv 144,104,88
Mv 148,108,88
Mv 208,112,88
Mv 212,116,88
Ma 240,120,88
Ma 244,124,88
buffer_load_dwordx4 v15, s[8:11], s39 offen offset:3072 lds
Ma 252,124,92
Ma 248,120,92
s_add_u32 s88, s88, s90
Mv 220,116,92
s_sub_u32 s40, s40, 1
Mv 216,112,92
buffer_load_dword v6, v5, s[24:27], s88 offen
Mv 156,108,92
Mv 152,104,92
Mv 92,100,92
Mv 88,96,92
s_cmp_lg_u32 s40, 0
s_waitcnt vmcnt(18) lgkmcnt(0)
s_barrier
s_cbranch_scc1 .LzPAV
.LzPAVX:
Mv 32,32,0
Rt 64,18,16384
Mv 36,36,0
Rb 192,28,32768
Mv 96,40,0
Rt 66,18,18432
Mv 100,44,0
s_add_u32 s39, s39, 0x40000
Rb 196,28,34816
Mv 160,48,0
Rt 68,19,16384
s_add_u32 m0, s37, 0
Mv 164,52,0
buffer_load_dwordx4 v12, s[8:11], s39 offen offset:0 lds
Mv 224,56,0
Rt 70,19,18432
Mv 228,60,0
Rb 200,29,32768
Mv 236,60,4
Rt 72,20,16384
Mv 232,56,4
Rb 204,29,34816
Mv 172,52,4
Rt 74,20,18432
Mv 168,48,4
Rb 208,28,36864
Mv 108,44,4
Rt 76,21,16384
Mv 104,40,4
buffer_load_dwordx4 v13, s[8:11], s39 offen offset:1024 lds
Mv 44,36,4
Rt 78,21,18432
Mv 40,32,4
Rb 212,28,38912
Mv 48,32,8
Rt 80,22,16384
Mv 52,36,8
Rb 216,29,36864
Mv 112,40,8
Rt 82,22,18432
Mv 116,44,8
)" R"(
Rb 220,29,38912
Mv 176,48,8
Rt 84,23,16384
Mv 180,52,8
buffer_load_dwordx4 v14, s[8:11], s39 offen offset:2048 lds
Mv 240,56,8
Rt 86,23,18432
Mv 244,60,8
Mv 252,60,12
Rt 88,24,16384
Mv 248,56,12
Mv 188,52,12
Rt 90,24,18432
Mv 184,48,12
Mv 124,44,12
Rt 92,25,16384
Mv 120,40,12
buffer_load_dwordx4 v15, s[8:11], s39 offen offset:3072 lds
Mv 60,36,12
Rt 94,25,18432
Mv 56,32,12
s_add_u32 s44, s43, 256
s_waitcnt lgkmcnt(6)
Mv 64,32,16
Rb 96,17,0
s_add_u32 m0, s36, 131072
Mv 68,36,16
buffer_load_dwordx4 v26, s[12:15], s44 offen offset:0 lds
Mv 128,40,16
Rb 100,17,2048
Mv 132,44,16
Mv 192,48,16
Rb 104,17,8192
Mv 196,52,16
buffer_load_dwordx4 v27, s[12:15], s44 offen offset:1024 lds
Ma 224,56,16
Rb 108,17,10240
Ma 228,60,16
Ma 236,60,20
Rb 112,17,16384
Ma 232,56,20
s_add_u32 s44, s43, 12544
buffer_load_dwordx4 v26, s[12:15], s44 offen offset:2048 lds
Mv 204,52,20
Rb 116,17,18432
Mv 200,48,20
Mv 140,44,20
Rb 120,17,24576
Mv 136,40,20
buffer_load_dwordx4 v27, s[12:15], s44 offen offset:3072 lds
Mv 76,36,20
Rb 124,17,26624
Mv 72,32,20
Mv 80,32,24
s_add_u32 m0, s36, 135168
s_add_u32 s44, s43, 384
Mv 84,36,24
buffer_load_dwordx4 v26, s[12:15], s44 offen offset:0 lds
Mv 144,40,24
Mv 148,44,24
Mv 208,48,24
Mv 212,52,24
buffer_load_dwordx4 v27, s[12:15], s44 offen offset:1024 lds
Ma 240,56,24
)" R"(
Ma 244,60,24
Ma 252,60,28
Ma 248,56,28
s_add_u32 s44, s43, 12672
buffer_load_dwordx4 v26, s[12:15], s44 offen offset:2048 lds
Mv 220,52,28
Mv 216,48,28
Mv 156,44,28
s_mov_b32 s85, 0xc108800
v_and_b32 v2, 7, v0
v_lshlrev_b32 v2, 7, v2
Mv 152,40,28
buffer_load_dwordx4 v27, s[12:15], s44 offen offset:3072 lds
Mv 92,36,28
buffer_load_dword v4, v2, s[20:23], s85 offen sc0 sc1
Mv 88,32,28
s_waitcnt vmcnt(18) lgkmcnt(0)
s_barrier
Mv 32,96,64
Rt 0,18,32768
Mv 36,100,64
s_add_u32 s38, s38, 0xffff0080
Mv 96,104,64
Rt 2,18,34816
s_add_u32 m0, s36, 0
Mv 100,108,64
buffer_load_dwordx4 v8, s[4:7], s38 offen offset:0 lds
Mv 160,112,64
Rt 4,19,32768
Mv 164,116,64
Mv 224,120,64
Rt 6,19,34816
Mv 228,124,64
buffer_load_dwordx4 v9, s[4:7], s38 offen offset:1024 lds
Mv 236,124,68
Rt 8,20,32768
Mv 232,120,68
Mv 172,116,68
Rt 10,20,34816
Mv 168,112,68
buffer_load_dwordx4 v10, s[4:7], s38 offen offset:2048 lds
Mv 108,108,68
Rt 12,21,32768
Mv 104,104,68
Mv 44,100,68
Rt 14,21,34816
Mv 40,96,68
buffer_load_dwordx4 v11, s[4:7], s38 offen offset:3072 lds
Mv 48,96,72
Rt 16,22,32768
Mv 52,100,72
s_add_u32 s38, s38, 0x10000
Mv 112,104,72
Rt 18,22,34816
s_add_u32 m0, s36, 4096
Mv 116,108,72
buffer_load_dwordx4 v8, s[4:7], s38 offen offset:0 lds
Mv 176,112,72
Rt 20,23,32768
Mv 180,116,72
Mv 240,120,72
Rt 22,23,34816
Mv 244,124,72
buffer_load_dwordx4 v9, s[4:7], s38 offen offset:1024 lds
Mv 252,124,76
)" R"(
Rt 24,24,32768
Mv 248,120,76
Mv 188,116,76
Rt 26,24,34816
Mv 184,112,76
buffer_load_dwordx4 v10, s[4:7], s38 offen offset:2048 lds
Mv 124,108,76
Rt 28,25,32768
Mv 120,104,76
Mv 60,100,76
Rt 30,25,34816
Mv 56,96,76
buffer_load_dwordx4 v11, s[4:7], s38 offen offset:3072 lds
Mv 64,96,80
Rb 32,16,32768
Mv 68,100,80
Mv 128,104,80
Rb 36,16,34816
Mv 132,108,80
s_add_u32 s39, s39, 0x40000
Mv 192,112,80
Rb 40,16,40960
s_add_u32 m0, s37, 16384
Mv 196,116,80
buffer_load_dwordx4 v12, s[8:11], s39 offen offset:0 lds
Ma 224,120,80
Rb 44,16,43008
Ma 228,124,80
Ma 236,124,84
Rb 48,16,49152
Ma 232,120,84
Mv 204,116,84
Rb 52,16,51200
Mv 200,112,84
buffer_load_dwordx4 v13, s[8:11], s39 offen offset:1024 lds
Mv 140,108,84
Rb 56,16,57344
Mv 136,104,84
Mv 76,100,84
Rb 60,16,59392
Mv 72,96,84
Mv 80,96,88
Mv 84,100,88
buffer_load_dwordx4 v14, s[8:11], s39 offen offset:2048 lds
Mv 144,104,88
Mv 148,108,88
Mv 208,112,88
Mv 212,116,88
Ma 240,120,88
Ma 244,124,88
buffer_load_dwordx4 v15, s[8:11], s39 offen offset:3072 lds
Ma 252,124,92
Ma 248,120,92
Mv 220,116,92
Mv 216,112,92
Mv 156,108,92
Mv 152,104,92
Mv 92,100,92
Mv 88,96,92
s_waitcnt vmcnt(13) lgkmcnt(0)
s_barrier
Mv 32,32,0
Rt 64,18,49152
Mv 36,36,0
Rb 128,28,32768
)" R"(
Mv 96,40,0
Rt 66,18,51200
Mv 100,44,0
s_add_u32 s39, s39, 0x40000
Rb 132,28,34816
Mv 160,48,0
Rt 68,19,49152
s_add_u32 m0, s37, 32768
Mv 164,52,0
buffer_load_dwordx4 v12, s[8:11], s39 offen offset:0 lds
Mv 224,56,0
Rt 70,19,51200
Mv 228,60,0
Rb 136,29,32768
Mv 236,60,4
Rt 72,20,49152
Mv 232,56,4
Rb 140,29,34816
Mv 172,52,4
Rt 74,20,51200
Mv 168,48,4
Rb 144,28,36864
Mv 108,44,4
Rt 76,21,49152
Mv 104,40,4
buffer_load_dwordx4 v13, s[8:11], s39 offen offset:1024 lds
Mv 44,36,4
Rt 78,21,51200
Mv 40,32,4
Rb 148,28,38912
Mv 48,32,8
Rt 80,22,49152
Mv 52,36,8
Rb 152,29,36864
Mv 112,40,8
Rt 82,22,51200
Mv 116,44,8
Rb 156,29,38912
Mv 176,48,8
Rt 84,23,49152
Mv 180,52,8
buffer_load_dwordx4 v14, s[8:11], s39 offen offset:2048 lds
Mv 240,56,8
Rt 86,23,51200
Mv 244,60,8
Mv 252,60,12
Rt 88,24,49152
Mv 248,56,12
Mv 188,52,12
Rt 90,24,51200
Mv 184,48,12
Mv 124,44,12
Rt 92,25,49152
Mv 120,40,12
buffer_load_dwordx4 v15, s[8:11], s39 offen offset:3072 lds
Mv 60,36,12
Rt 94,25,51200
Mv 56,32,12
s_add_u32 s44, s43, 512
s_waitcnt lgkmcnt(6)
Mv 64,32,16
Rb 96,17,32768
s_add_u32 m0, s36, 131072
Mv 68,36,16
)" R"(
buffer_load_dwordx4 v26, s[12:15], s44 offen offset:0 lds
Mv 128,40,16
Rb 100,17,34816
Mv 132,44,16
Mv 192,48,16
Rb 104,17,40960
Mv 196,52,16
buffer_load_dwordx4 v27, s[12:15], s44 offen offset:1024 lds
Ma 224,56,16
Rb 108,17,43008
Ma 228,60,16
Ma 236,60,20
Rb 112,17,49152
Ma 232,56,20
s_add_u32 s44, s43, 12800
buffer_load_dwordx4 v26, s[12:15], s44 offen offset:2048 lds
Mv 204,52,20
Rb 116,17,51200
Mv 200,48,20
Mv 140,44,20
Rb 120,17,57344
Mv 136,40,20
buffer_load_dwordx4 v27, s[12:15], s44 offen offset:3072 lds
Mv 76,36,20
Rb 124,17,59392
Mv 72,32,20
Mv 80,32,24
s_add_u32 m0, s36, 135168
s_add_u32 s44, s43, 640
Mv 84,36,24
buffer_load_dwordx4 v26, s[12:15], s44 offen offset:0 lds
Mv 144,40,24
Mv 148,44,24
Mv 208,48,24
Mv 212,52,24
buffer_load_dwordx4 v27, s[12:15], s44 offen offset:1024 lds
Ma 240,56,24
Ma 244,60,24
Ma 252,60,28
Ma 248,56,28
s_add_u32 s44, s43, 12928
buffer_load_dwordx4 v26, s[12:15], s44 offen offset:2048 lds
Mv 220,52,28
Mv 216,48,28
Mv 156,44,28
Mv 152,40,28
buffer_load_dwordx4 v27, s[12:15], s44 offen offset:3072 lds
Mv 92,36,28
Mv 88,32,28
s_waitcnt vmcnt(16) lgkmcnt(0)
s_barrier
Mv 32,96,64
Rt 0,18,0
v_xor_b32 v4, s3, v4
v_cmp_gt_u32_e32 vcc, 2, v4
s_andn2_b32 s28, 0xff, vcc_lo
s_cbranch_scc1 .Lzsp3
.Lzok3:
Mv 36,100,64
s_add_u32 s38, s38, 0xffff0080
v_xor_b32 v4, s3, v4
Mv 96,104,64
Rt 2,18,2048
s_add_u32 m0, s36, 32768
Mv 100,108,64
)" R"(
buffer_load_dwordx4 v8, s[4:7], s38 offen offset:0 lds
Mv 160,112,64
Rt 4,19,0
v_and_b32 v4, 1, v4
Mv 164,116,64
v_cmp_eq_u32_e32 vcc, 1, v4
s_mov_b32 s85, vcc_lo
Mv 224,120,64
Rt 6,19,2048
s_and_b32 s28, s2, 7
Mv 228,124,64
buffer_load_dwordx4 v9, s[4:7], s38 offen offset:1024 lds
Mv 236,124,68
Rt 8,20,0
s_lshr_b32 s28, s85, s28
Mv 232,120,68
s_xor_b32 s28, s28, s34
s_and_b32 s28, s28, 1
Mv 172,116,68
Rt 10,20,2048
s_lshl_b32 s84, s28, 13
Mv 168,112,68
buffer_load_dwordx4 v10, s[4:7], s38 offen offset:2048 lds
Mv 108,108,68
Rt 12,21,0
s_add_u32 s49, s49, s84
Mv 104,104,68
s_add_u32 s50, s50, s84
s_add_u32 s60, s50, 7168
Mv 44,100,68
Rt 14,21,2048
s_add_u32 s52, s52, s84
Mv 40,96,68
buffer_load_dwordx4 v11, s[4:7], s38 offen offset:3072 lds
Mv 48,96,72
Rt 16,22,0
s_add_u32 s65, s65, s84
Mv 52,100,72
s_add_u32 s38, s38, 0x10000
s_lshr_b32 s28, s2, 6
Mv 112,104,72
Rt 18,22,2048
s_add_u32 m0, s36, 36864
Mv 116,108,72
buffer_load_dwordx4 v8, s[4:7], s38 offen offset:0 lds
Mv 176,112,72
Rt 20,23,0
s_lshl_b32 s28, s28, 14
Mv 180,116,72
s_add_u32 s28, s28, s50
s_bfe_u32 s29, s2, 0x20004
Mv 240,120,72
Rt 22,23,2048
s_lshl2_add_u32 s29, s29, s33
Mv 244,124,72
buffer_load_dwordx4 v9, s[4:7], s38 offen offset:1024 lds
Mv 252,124,76
Rt 24,24,0
s_lshl_b32 s29, s29, 9
Mv 248,120,76
s_add_u32 s28, s28, s29
v_min_u32 v4, 3, v0
Mv 188,116,76
Rt 26,24,2048
)" R"(
v_lshlrev_b32 v4, 7, v4
Mv 184,112,76
buffer_load_dwordx4 v10, s[4:7], s38 offen offset:2048 lds
Mv 124,108,76
Rt 28,25,0
Mv 120,104,76
buffer_load_dword v4, v4, s[20:23], s28 offen
Mv 60,100,76
Rt 30,25,2048
s_lshl1_add_u32 s28, s36, s84
Mv 56,96,76
buffer_load_dwordx4 v11, s[4:7], s38 offen offset:3072 lds
Mv 64,96,80
Rb 32,16,0
v_add_u32 v2, s28, v1
Mv 68,100,80
v_add_u32 v3, 0x1000, v2
s_lshl_b32 s28, s34, 13
Mv 128,104,80
Rb 36,16,2048
s_xor_b32 s28, s28, s84
Mv 132,108,80
s_add_u32 s39, s39, 0x40000
s_and_b32 s28, s28, 0x2000
Mv 192,112,80
Rb 40,16,8192
s_add_u32 m0, s37, 49152
Mv 196,116,80
buffer_load_dwordx4 v12, s[8:11], s39 offen offset:0 lds
Ma 224,120,80
Rb 44,16,10240
s_xor_b32 s73, s73, s28
Ma 228,124,80
Ma 236,124,84
Rb 48,16,16384
Ma 232,120,84
Mv 204,116,84
Rb 52,16,18432
Mv 200,112,84
buffer_load_dwordx4 v13, s[8:11], s39 offen offset:1024 lds
Mv 140,108,84
Rb 56,16,24576
Mv 136,104,84
Mv 76,100,84
Rb 60,16,26624
Mv 72,96,84
Mv 80,96,88
Mv 84,100,88
buffer_load_dwordx4 v14, s[8:11], s39 offen offset:2048 lds
Mv 144,104,88
Mv 148,108,88
Mv 208,112,88
Mv 212,116,88
Ma 240,120,88
Ma 244,124,88
buffer_load_dwordx4 v15, s[8:11], s39 offen offset:3072 lds
Ma 252,124,92
Ma 248,120,92
Mv 220,116,92
Mv 216,112,92
Mv 156,108,92
Mv 152,104,92
Mv 92,100,92
Mv 88,96,92
)" R"(
s_waitcnt vmcnt(5) lgkmcnt(0)
s_barrier
Mv 32,32,0
Rt 64,18,16384
Mv 36,36,0
Rb 160,28,32768
v_and_b32 v5, 7, v0
Mv 96,40,0
Rt 66,18,18432
v_lshlrev_b32 v5, 7, v5
Mv 100,44,0
Rb 164,28,34816
v_mov_b32 v6, s3
Mv 160,48,0
Rt 68,19,16384
Mv 164,52,0
Mv 224,56,0
Rt 70,19,18432
Mv 228,60,0
Rb 168,29,32768
Mv 236,60,4
Rt 72,20,16384
Mv 232,56,4
Rb 172,29,34816
Mv 172,52,4
Rt 74,20,18432
Mv 168,48,4
Rb 176,28,36864
Mv 108,44,4
Rt 76,21,16384
Mv 104,40,4
Mv 44,36,4
Rt 78,21,18432
Mv 40,32,4
Rb 180,28,38912
Mv 48,32,8
Rt 80,22,16384
Mv 52,36,8
Rb 184,29,36864
Mv 112,40,8
Rt 82,22,18432
Mv 116,44,8
Rb 188,29,38912
Mv 176,48,8
Rt 84,23,16384
Mv 180,52,8
Mv 240,56,8
Rt 86,23,18432
Mv 244,60,8
Mv 252,60,12
Rt 88,24,16384
Mv 248,56,12
Mv 188,52,12
Rt 90,24,18432
Mv 184,48,12
Mv 124,44,12
Rt 92,25,16384
Mv 120,40,12
Mv 60,36,12
Rt 94,25,18432
v_lshrrev_b32 v10, 3, v0
Mv 56,32,12
v_and_b32 v11, 0x70, v1
v_lshl_or_b32 v10, v10, 13, v11
Mv 64,32,16
)" R"(
Rb 96,17,0
v_lshlrev_b32 v11, 1, v0
Mv 68,36,16
v_and_b32 v11, 32, v11
v_xor_b32 v10, v10, v11
buffer_load_dword v4, v7, s[20:23], s52 offen offset:0 sc0 sc1
Mv 128,40,16
Rb 100,17,2048
s_and_b32 s28, s33, 1
Mv 132,44,16
s_lshl_b32 s28, s28, 6
v_xor_b32 v8, s28, v10
Mv 192,48,16
Rb 104,17,8192
v_xor_b32 v9, 64, v8
Mv 196,52,16
v_add_u32 v9, 0xfc00, v9
v_add_u32 v10, 0x1f800, v8
Ma 224,56,16
Rb 108,17,10240
v_add_u32 v11, 0x1f800, v9
Ma 228,60,16
Ma 236,60,20
Rb 112,17,16384
Ma 232,56,20
Mv 204,52,20
Rb 116,17,18432
Mv 200,48,20
Mv 140,44,20
Rb 120,17,24576
Mv 136,40,20
Mv 76,36,20
Rb 124,17,26624
Mv 72,32,20
Mv 80,32,24
Mv 84,36,24
Mv 144,40,24
Mv 148,44,24
Mv 208,48,24
Mv 212,52,24
Ma 240,56,24
Ma 244,60,24
Ma 252,60,28
Ma 248,56,28
Mv 220,52,28
Mv 216,48,28
Mv 156,44,28
Mv 152,40,28
Mv 92,36,28
Mv 88,32,28
s_waitcnt vmcnt(5) lgkmcnt(0)
s_barrier
Mv 32,96,64
Rt 0,18,32768
Mv 36,100,64
s_add_u32 s92, s42, 0x0
Mv 96,104,64
Rt 2,18,34816
s_add_u32 m0, s41, 131072
Mv 100,108,64
buffer_load_dwordx4 v8, s[16:19], s92 offen offset:0 lds
Mv 160,112,64
Rt 4,19,32768
Mv 164,116,64
Mv 224,120,64
)" R"(
Rt 6,19,34816
Mv 228,124,64
buffer_load_dwordx4 v9, s[16:19], s92 offen offset:1024 lds
Mv 236,124,68
Rt 8,20,32768
Mv 232,120,68
Mv 172,116,68
Rt 10,20,34816
Mv 168,112,68
buffer_load_dwordx4 v10, s[16:19], s92 offen offset:2048 lds
Mv 108,108,68
Rt 12,21,32768
Mv 104,104,68
Mv 44,100,68
Rt 14,21,34816
Mv 40,96,68
buffer_load_dwordx4 v11, s[16:19], s92 offen offset:3072 lds
Mv 48,96,72
Rt 16,22,32768
Mv 52,100,72
s_add_u32 s92, s42, 0x40000
Mv 112,104,72
Rt 18,22,34816
s_add_u32 m0, s41, 135168
Mv 116,108,72
buffer_load_dwordx4 v8, s[16:19], s92 offen offset:0 lds
Mv 176,112,72
Rt 20,23,32768
Mv 180,116,72
Mv 240,120,72
Rt 22,23,34816
Mv 244,124,72
buffer_load_dwordx4 v9, s[16:19], s92 offen offset:1024 lds
Mv 252,124,76
Rt 24,24,32768
Mv 248,120,76
Mv 188,116,76
Rt 26,24,34816
Mv 184,112,76
buffer_load_dwordx4 v10, s[16:19], s92 offen offset:2048 lds
Mv 124,108,76
Rt 28,25,32768
Mv 120,104,76
s_add_u32 s92, s42, 0x1c0000
Mv 60,100,76
Rt 30,25,34816
s_add_u32 m0, s41, 0
Mv 56,96,76
buffer_load_dwordx4 v8, s[16:19], s92 offen offset:0 lds
Mv 64,96,80
Rb 32,16,32768
Mv 68,100,80
Mv 128,104,80
Rb 36,16,34816
Mv 132,108,80
buffer_load_dwordx4 v9, s[16:19], s92 offen offset:1024 lds
Mv 192,112,80
Rb 40,16,40960
Mv 196,116,80
Ma 224,120,80
Rb 44,16,43008
Ma 228,124,80
buffer_load_dwordx4 v10, s[16:19], s92 offen offset:2048 lds
Ma 236,124,84
)" R"(
Rb 48,16,49152
Ma 232,120,84
Mv 204,116,84
Rb 52,16,51200
Mv 200,112,84
buffer_load_dwordx4 v11, s[16:19], s92 offen offset:3072 lds
Mv 140,108,84
Rb 56,16,57344
Mv 136,104,84
s_add_u32 s92, s42, 0x200000
Mv 76,100,84
Rb 60,16,59392
s_add_u32 m0, s41, 4096
Mv 72,96,84
buffer_load_dwordx4 v8, s[16:19], s92 offen offset:0 lds
Mv 80,96,88
Mv 84,100,88
Mv 144,104,88
Mv 148,108,88
buffer_load_dwordx4 v9, s[16:19], s92 offen offset:1024 lds
Mv 208,112,88
Mv 212,116,88
Ma 240,120,88
Ma 244,124,88
buffer_load_dwordx4 v10, s[16:19], s92 offen offset:2048 lds
Ma 252,124,92
Ma 248,120,92
Mv 220,116,92
Mv 216,112,92
Mv 156,108,92
Mv 152,104,92
Mv 92,100,92
Mv 88,96,92
s_waitcnt vmcnt(14) lgkmcnt(4)
s_barrier
v_cmp_eq_u32_e32 vcc, s3, v4
s_andn2_b64 s[28:29], 1, vcc
s_cbranch_scc1 .Lzsp4
.Lzok4:
Mv 32,32,0
Rt 64,18,49152
Mv 36,36,0
Rt 66,18,51200
s_add_u32 s92, s42, 0x80
Mv 96,40,0
Rt 68,19,49152
s_add_u32 m0, s41, 65536
Mv 100,44,0
Rt 70,19,51200
buffer_load_dwordx4 v8, s[16:19], s92 offen offset:0 lds
Mv 108,44,4
Rt 72,20,49152
Mv 104,40,4
Rt 74,20,51200
Mv 44,36,4
Rt 76,21,49152
Mv 40,32,4
Rt 78,21,51200
buffer_load_dwordx4 v9, s[16:19], s92 offen offset:1024 lds
Mv 48,32,8
Rt 80,22,49152
Mv 52,36,8
Rt 82,22,51200
Mv 112,40,8
Rt 84,23,49152
)" R"(
Mv 116,44,8
Rt 86,23,51200
buffer_load_dwordx4 v10, s[16:19], s92 offen offset:2048 lds
Mv 124,44,12
Rt 88,24,49152
Mv 120,40,12
Rt 90,24,51200
Mv 60,36,12
Rt 92,25,49152
Mv 56,32,12
Rt 94,25,51200
buffer_load_dwordx4 v11, s[16:19], s92 offen offset:3072 lds
Mv 64,32,16
Rb 96,17,32768
Mv 68,36,16
Rb 100,17,34816
s_add_u32 s92, s42, 0x40080
Mv 128,40,16
Rb 104,17,40960
s_add_u32 m0, s41, 69632
Mv 132,44,16
Rb 108,17,43008
buffer_load_dwordx4 v8, s[16:19], s92 offen offset:0 lds
Mv 140,44,20
Rb 112,17,49152
Mv 136,40,20
Rb 116,17,51200
Mv 76,36,20
Rb 120,17,57344
Mv 72,32,20
Rb 124,17,59392
buffer_load_dwordx4 v9, s[16:19], s92 offen offset:1024 lds
Mv 80,32,24
Mv 84,36,24
Mv 144,40,24
Mv 148,44,24
buffer_load_dwordx4 v10, s[16:19], s92 offen offset:2048 lds
Mv 156,44,28
Mv 152,40,28
Mv 92,36,28
Mv 88,32,28
s_waitcnt lgkmcnt(0)
s_barrier
Mv 32,96,64
v_bfe_u32 v12, v0, 3, 2
v_lshlrev_b32 v12, 5, v12
Mv 36,100,64
v_and_b32 v4, 3, v0
v_lshl_or_b32 v12, v4, 3, v12
Mv 40,96,68
s_add_u32 m0, s36, 98304
s_add_u32 s44, s43, 768
Mv 44,100,68
buffer_load_dwordx4 v26, s[12:15], s44 offen offset:0 lds
Mv 48,96,72
v_bfe_u32 v4, v0, 2, 2
v_lshl_or_b32 v12, v4, 7, v12
Mv 52,100,72
v_lshrrev_b32 v4, 4, v0
v_lshl_or_b32 v12, v4, 10, v12
Mv 56,96,76
v_xor_b32 v13, 32, v12
v_xor_b32 v14, 64, v12
Mv 60,100,76
buffer_load_dwordx4 v27, s[12:15], s44 offen offset:1024 lds
)" R"(
Mv 64,96,80
v_xor_b32 v15, 96, v12
v_add_u32 v20, 0x20000, v12
Mv 68,100,80
v_add_u32 v21, 0x20000, v13
v_add_u32 v22, 0x20000, v14
Mv 72,96,84
v_add_u32 v23, 0x20000, v15
v_add_u32 v16, 0x10000, v12
Mv 76,100,84
s_add_u32 s44, s43, 13056
buffer_load_dwordx4 v26, s[12:15], s44 offen offset:2048 lds
Mv 80,96,88
v_add_u32 v17, 0x10000, v13
v_add_u32 v18, 0x10000, v14
Mv 84,100,88
v_add_u32 v19, 0x10000, v15
Cv 32,32,33
Mv 88,96,92
Cv 33,34,35
Cv 34,36,37
Mv 92,100,92
buffer_load_dwordx4 v27, s[12:15], s44 offen offset:3072 lds
Mv 96,104,64
Cv 35,38,39
Cv 40,40,41
Mv 100,108,64
Cv 41,42,43
Cv 42,44,45
Mv 104,104,68
s_add_u32 m0, s41, 32768
s_add_u32 s92, s42, 0x1c0080
Mv 108,108,68
buffer_load_dwordx4 v8, s[16:19], s92 offen offset:0 lds
Mv 112,104,72
Cv 43,46,47
Cv 48,48,49
Mv 116,108,72
Cv 49,50,51
Cv 50,52,53
Mv 120,104,76
Cv 51,54,55
Cv 56,56,57
Mv 124,108,76
buffer_store_dwordx4 v[32:35], v2, s[20:23], s45 offen offset:0 sc0 sc1
Mv 128,104,80
Cv 57,58,59
Cv 58,60,61
Mv 132,108,80
Cv 59,62,63
Cv 64,64,65
Mv 136,104,84
Cv 65,66,67
Cv 66,68,69
Mv 140,108,84
buffer_load_dwordx4 v9, s[16:19], s92 offen offset:1024 lds
Mv 144,104,88
Cv 67,70,71
Cv 72,72,73
Mv 148,108,88
Cv 73,74,75
Cv 74,76,77
Mv 152,104,92
Cv 75,78,79
)" R"(
Cv 80,80,81
Mv 156,108,92
buffer_store_dwordx4 v[40:43], v2, s[20:23], s45 offen offset:1024 sc0 sc1
Mv 160,48,0
Cv 81,82,83
Cv 82,84,85
Mv 160,112,64
Cv 83,86,87
Cv 88,88,89
Mv 164,52,0
Cv 89,90,91
Cv 90,92,93
Mv 164,116,64
buffer_load_dwordx4 v10, s[16:19], s92 offen offset:2048 lds
Mv 168,48,4
Cv 91,94,95
Cv 96,96,97
Mv 168,112,68
Cv 97,98,99
Cv 98,100,101
Mv 172,52,4
Cv 99,102,103
Cv 104,104,105
Mv 172,116,68
buffer_store_dwordx4 v[48:51], v2, s[20:23], s45 offen offset:2048 sc0 sc1
Mv 176,48,8
Cv 105,106,107
Cv 106,108,109
Mv 176,112,72
Cv 107,110,111
Cv 112,112,113
Mv 180,52,8
Cv 113,114,115
Cv 114,116,117
Mv 180,116,72
buffer_load_dwordx4 v11, s[16:19], s92 offen offset:3072 lds
Mv 184,48,12
Cv 115,118,119
Cv 120,120,121
Mv 184,112,76
Cv 121,122,123
Cv 122,124,125
Mv 188,52,12
Cv 123,126,127
Cv 128,128,129
Mv 188,116,76
buffer_store_dwordx4 v[56:59], v2, s[20:23], s45 offen offset:3072 sc0 sc1
Mv 192,48,16
Cv 129,130,131
Cv 130,132,133
Mv 192,112,80
Cv 131,134,135
Cv 136,136,137
Mv 196,52,16
s_add_u32 m0, s41, 36864
s_add_u32 s92, s42, 0x200080
Mv 196,116,80
buffer_load_dwordx4 v8, s[16:19], s92 offen offset:0 lds
Mv 200,48,20
Cv 137,138,139
Cv 138,140,141
Mv 200,112,84
Cv 139,142,143
Cv 144,144,145
)" R"(
Mv 204,52,20
Cv 145,146,147
Cv 146,148,149
Mv 204,116,84
buffer_store_dwordx4 v[64:67], v3, s[20:23], s45 offen offset:0 sc0 sc1
Mv 208,48,24
Cv 147,150,151
Cv 152,152,153
Mv 208,112,88
Cv 153,154,155
Cv 154,156,157
Mv 212,52,24
Cv 155,158,159
Mv 212,116,88
buffer_load_dwordx4 v9, s[16:19], s92 offen offset:1024 lds
Mv 216,48,28
Mv 216,112,92
Mv 220,52,28
Mv 220,116,92
buffer_store_dwordx4 v[72:75], v3, s[20:23], s45 offen offset:1024 sc0 sc1
Mv 224,56,0
Mv 228,60,0
Mv 236,60,4
Mv 232,56,4
buffer_load_dwordx4 v10, s[16:19], s92 offen offset:2048 lds
Mv 240,56,8
Mv 244,60,8
Mv 252,60,12
Mv 248,56,12
s_waitcnt vmcnt(24)
s_barrier
Ma 224,56,16
buffer_store_dwordx4 v[80:83], v3, s[20:23], s45 offen offset:2048 sc0 sc1
Ma 228,60,16
Ma 236,60,20
Ma 232,56,20
Ma 240,56,24
Ma 244,60,24
Ma 252,60,28
Ma 248,56,28
buffer_store_dwordx4 v[88:91], v3, s[20:23], s45 offen offset:3072 sc0 sc1
Mv 224,120,64
Rt 16,20,0
Mv 228,124,64
Rt 18,20,512
Mv 232,120,68
Rt 20,21,0
Mv 236,124,68
Rt 22,21,512
Mv 240,120,72
Rt 24,22,0
Mv 244,124,72
Rt 26,22,512
Mv 248,120,76
Rt 28,23,0
Mv 252,124,76
Rt 30,23,512
Ma 224,120,80
Rt 32,20,4096
Ma 228,124,80
Rt 34,20,4608
Ma 232,120,84
Rt 36,21,4096
Ma 236,124,84
Rt 38,21,4608
)" R"(
Ma 240,120,88
Rt 40,22,4096
Ma 244,124,88
Rt 42,22,4608
Ma 248,120,92
Rt 44,23,4096
Ma 252,124,92
Rt 46,23,4608
s_waitcnt vmcnt(15) lgkmcnt(8)
.LzQA:
s_cmp_eq_u32 s2, 112
s_cbranch_scc1 .LzT2S
.LzT2E:
Mz 32,16,192
Rt 0,20,8192
Cv 224,224,225
Mz 40,16,196
Rt 2,20,8704
Rb 48,28,0
Mz 44,20,196
Rt 4,21,8192
buffer_store_dwordx4 v[96:99], v2, s[20:23], s46 offen offset:0 sc0 sc1
Mz 36,20,192
Rt 6,21,8704
Rb 52,28,2048
Mz 48,24,192
Rt 8,22,8192
Cv 225,226,227
Mz 56,24,196
Rt 10,22,8704
Rb 56,29,0
buffer_load_dwordx4 a[64:67], v2, s[20:23], s51 offen offset:0 sc0 sc1
Mz 60,28,196
Rt 12,23,8192
Cv 226,228,229
Mz 52,28,192
Rt 14,23,8704
Rb 60,29,2048
s_waitcnt lgkmcnt(12)
Mv 32,32,200
Rt 16,20,12288
Cv 227,230,231
Mv 40,32,204
Rt 18,20,12800
Cv 232,232,233
Mv 44,36,204
Rt 20,21,12288
buffer_load_dwordx4 a[68:71], v2, s[20:23], s51 offen offset:1024 sc0 sc1
Mv 36,36,200
Rt 22,21,12800
Cv 233,234,235
Mv 48,40,200
Rt 24,22,12288
buffer_store_dwordx4 v[104:107], v2, s[20:23], s46 offen offset:1024 sc0 sc1
Mv 56,40,204
Rt 26,22,12800
Cv 234,236,237
Mv 60,44,204
Rt 28,23,12288
Cv 235,238,239
Mv 52,44,200
Rt 30,23,12800
buffer_load_dwordx4 a[72:75], v2, s[20:23], s51 offen offset:2048 sc0 sc1
s_waitcnt lgkmcnt(9)
)" R"(
Mv 32,0,208
Rt 32,20,16384
Cv 240,240,241
Mv 40,0,212
Rt 34,20,16896
Cv 241,242,243
Mv 44,4,212
Rt 36,21,16384
Cv 242,244,245
Mv 36,4,208
Rt 38,21,16896
Cv 243,246,247
Mv 48,8,208
Rt 40,22,16384
buffer_load_dwordx4 a[76:79], v2, s[20:23], s51 offen offset:3072 sc0 sc1
Mv 56,8,212
Rt 42,22,16896
Cv 248,248,249
Cv 249,250,251
Mv 60,12,212
Rt 44,23,16384
buffer_store_dwordx4 v[112:115], v2, s[20:23], s46 offen offset:2048 sc0 sc1
Mv 52,12,208
Rt 46,23,16896
s_waitcnt lgkmcnt(8)
Mv 32,16,216
Rt 0,20,20480
Cv 250,252,253
Mv 40,16,220
Rt 2,20,20992
buffer_load_dwordx4 a[80:83], v3, s[20:23], s51 offen offset:0 sc0 sc1
Mv 44,20,220
Rt 4,21,20480
Cv 251,254,255
v_accvgpr_read_b32 v228, a224
Mv 36,20,216
Rt 6,21,20992
v_accvgpr_read_b32 v24, a225
Mv 48,24,216
Rt 8,22,20480
Cv 228,228,24
Mv 56,24,220
Rt 10,22,20992
v_accvgpr_read_b32 v229, a226
Mv 60,28,220
Rt 12,23,20480
buffer_load_dwordx4 a[84:87], v3, s[20:23], s51 offen offset:1024 sc0 sc1
Mv 52,28,216
Rt 14,23,20992
v_accvgpr_read_b32 v25, a227
s_waitcnt lgkmcnt(8)
Mv 32,32,128
Rt 16,20,24576
buffer_store_dwordx4 v[120:123], v2, s[20:23], s46 offen offset:3072 sc0 sc1
Mv 40,32,132
Rt 18,20,25088
Cv 229,229,25
Mv 44,36,132
Rt 20,21,24576
v_accvgpr_read_b32 v230, a228
Mv 36,36,128
Rt 22,21,25088
buffer_load_dwordx4 a[88:91], v3, s[20:23], s51 offen offset:2048 sc0 sc1
Mv 48,40,128
)" R"(
Rt 24,22,24576
v_accvgpr_read_b32 v24, a229
Mv 56,40,132
Rt 26,22,25088
Cv 230,230,24
v_accvgpr_read_b32 v231, a230
Mv 60,44,132
Rt 28,23,24576
v_accvgpr_read_b32 v25, a231
Mv 52,44,128
Rt 30,23,25088
s_waitcnt lgkmcnt(8)
Mv 32,0,136
Rt 32,12,0
buffer_load_dwordx4 a[92:95], v3, s[20:23], s51 offen offset:3072 sc0 sc1
Mv 40,0,140
Rt 34,12,512
Cv 231,231,25
Mv 44,4,140
Rt 36,13,0
buffer_store_dwordx4 v[128:131], v3, s[20:23], s46 offen offset:0 sc0 sc1
Mv 36,4,136
Rt 38,13,512
v_accvgpr_read_b32 v236, a232
Mv 48,8,136
Rt 40,14,0
v_accvgpr_read_b32 v24, a233
Mv 56,8,140
Rt 42,14,512
Cv 236,236,24
Mv 60,12,140
Rt 44,15,0
v_accvgpr_read_b32 v237, a234
Mv 52,12,136
Rt 46,15,512
s_waitcnt vmcnt(13) lgkmcnt(8)
s_barrier
buffer_store_dword v6, v7, s[20:23], s49 offen offset:0 sc0 sc1
Mv 32,16,144
Rt 0,12,4096
v_accvgpr_read_b32 v25, a235
Mv 40,16,148
Rt 2,12,4608
s_add_u32 s92, s42, 0x100
Mv 44,20,148
Rt 4,13,4096
s_add_u32 m0, s41, 98304
Mv 36,20,144
Rt 6,13,4608
buffer_load_dwordx4 v8, s[16:19], s92 offen offset:0 lds
Mv 48,24,144
Rt 8,14,4096
Cv 237,237,25
Mv 56,24,148
Rt 10,14,4608
v_accvgpr_read_b32 v238, a236
v_accvgpr_read_b32 v24, a237
Mv 60,28,148
Rt 12,15,4096
Cv 238,238,24
Mv 52,28,144
Rt 14,15,4608
buffer_load_dwordx4 v9, s[16:19], s92 offen offset:1024 lds
s_waitcnt lgkmcnt(8)
Mv 32,32,152
)" R"(
Rt 16,12,8192
v_accvgpr_read_b32 v239, a238
Mv 40,32,156
Rt 18,12,8704
buffer_store_dwordx4 v[136:139], v3, s[20:23], s46 offen offset:1024 sc0 sc1
Mv 44,36,156
Rt 20,13,8192
v_accvgpr_read_b32 v25, a239
Cv 239,239,25
Mv 36,36,152
Rt 22,13,8704
buffer_load_dwordx4 v10, s[16:19], s92 offen offset:2048 lds
Mv 48,40,152
Rt 24,14,8192
v_accvgpr_read_b32 v244, a240
Mv 56,40,156
Rt 26,14,8704
v_accvgpr_read_b32 v24, a241
Mv 60,44,156
Rt 28,15,8192
Cv 244,244,24
Mv 52,44,152
Rt 30,15,8704
buffer_load_dwordx4 v11, s[16:19], s92 offen offset:3072 lds
s_waitcnt lgkmcnt(8)
Mv 32,0,160
Rt 32,12,12288
v_accvgpr_read_b32 v245, a242
Mv 40,0,164
Rt 34,12,12800
s_add_u32 s92, s42, 0x40100
Mv 44,4,164
Rt 36,13,12288
s_add_u32 m0, s41, 102400
Mv 36,4,160
Rt 38,13,12800
buffer_load_dwordx4 v8, s[16:19], s92 offen offset:0 lds
Mv 48,8,160
Rt 40,14,12288
v_accvgpr_read_b32 v25, a243
Mv 56,8,164
Rt 42,14,12800
Cv 245,245,25
v_accvgpr_read_b32 v246, a244
Mv 60,12,164
Rt 44,15,12288
v_accvgpr_read_b32 v24, a245
Mv 52,12,160
Rt 46,15,12800
buffer_load_dwordx4 v9, s[16:19], s92 offen offset:1024 lds
s_waitcnt lgkmcnt(8)
Mv 32,16,168
Rt 0,12,16384
Cv 246,246,24
Mv 40,16,172
Rt 2,12,16896
buffer_store_dwordx4 v[144:147], v3, s[20:23], s46 offen offset:2048 sc0 sc1
Mv 44,20,172
Rt 4,13,16384
v_accvgpr_read_b32 v247, a246
v_accvgpr_read_b32 v25, a247
Mv 36,20,168
Rt 6,13,16896
buffer_load_dwordx4 v10, s[16:19], s92 offen offset:2048 lds
)" R"(
Mv 48,24,168
Rt 8,14,16384
Cv 247,247,25
Mv 56,24,172
Rt 10,14,16896
s_add_u32 s92, s42, 0x1c0100
Mv 60,28,172
Rt 12,15,16384
s_add_u32 m0, s41, 131072
Mv 52,28,168
Rt 14,15,16896
buffer_load_dwordx4 v8, s[16:19], s92 offen offset:0 lds
s_waitcnt lgkmcnt(8)
Mv 32,32,176
Rt 16,12,20480
v_accvgpr_read_b32 v252, a248
Mv 40,32,180
Rt 18,12,20992
v_accvgpr_read_b32 v24, a249
Mv 44,36,180
Rt 20,13,20480
Cv 252,252,24
Mv 36,36,176
Rt 22,13,20992
buffer_load_dwordx4 v9, s[16:19], s92 offen offset:1024 lds
Mv 48,40,176
Rt 24,14,20480
v_accvgpr_read_b32 v253, a250
Mv 56,40,180
Rt 26,14,20992
v_accvgpr_read_b32 v25, a251
Cv 253,253,25
Mv 60,44,180
Rt 28,15,20480
v_accvgpr_read_b32 v254, a252
Mv 52,44,176
Rt 30,15,20992
buffer_load_dwordx4 v10, s[16:19], s92 offen offset:2048 lds
s_waitcnt lgkmcnt(8)
Mv 32,0,184
Rt 32,12,24576
v_accvgpr_read_b32 v24, a253
Mv 40,0,188
Rt 34,12,25088
buffer_store_dwordx4 v[152:155], v3, s[20:23], s46 offen offset:3072 sc0 sc1
Mv 44,4,188
Rt 36,13,24576
Cv 254,254,24
v_accvgpr_read_b32 v255, a254
Mv 36,4,184
Rt 38,13,25088
buffer_load_dwordx4 v11, s[16:19], s92 offen offset:3072 lds
Mv 48,8,184
Rt 40,14,24576
v_accvgpr_read_b32 v25, a255
Mv 56,8,188
Rt 42,14,25088
s_add_u32 s92, s42, 0x200100
Mv 60,12,188
Rt 44,15,24576
s_add_u32 m0, s41, 135168
Mv 52,12,184
Rt 46,15,25088
buffer_load_dwordx4 v8, s[16:19], s92 offen offset:0 lds
)" R"(
s_waitcnt lgkmcnt(8)
Mv 32,16,48
Rt 0,16,0
Cv 255,255,25
Mv 40,16,52
Rt 2,16,512
Cv 160,160,161
Mv 44,20,52
Rt 4,17,0
Cv 161,162,163
Mv 36,20,48
Rt 6,17,512
buffer_load_dwordx4 v9, s[16:19], s92 offen offset:1024 lds
Mv 48,24,48
Rt 8,18,0
Cv 162,164,165
Mv 56,24,52
Rt 10,18,512
Cv 163,166,167
Cv 168,168,169
Mv 60,28,52
Rt 12,19,0
Cv 169,170,171
Mv 52,28,48
Rt 14,19,512
buffer_load_dwordx4 v10, s[16:19], s92 offen offset:2048 lds
s_waitcnt lgkmcnt(8)
s_barrier
Mv 32,32,56
Rt 16,16,4096
Cv 170,172,173
Mv 40,32,60
Rt 18,16,4608
buffer_store_dwordx4 v[160:163], v2, s[20:23], s47 offen offset:0 sc0 sc1
Mv 44,36,60
Rt 20,17,4096
Cv 171,174,175
Cv 176,176,177
Mv 36,36,56
Rt 22,17,4608
s_add_u32 s92, s42, 0x180
Mv 48,40,56
Rt 24,18,4096
s_add_u32 m0, s41, 0
Mv 56,40,60
Rt 26,18,4608
buffer_load_dwordx4 v8, s[16:19], s92 offen offset:0 lds
Mv 60,44,60
Rt 28,19,4096
Cv 177,178,179
Mv 52,44,56
Rt 30,19,4608
Cv 178,180,181
s_waitcnt lgkmcnt(8)
Mz 64,0,192
Rt 32,16,8192
Cv 179,182,183
Mz 72,0,196
Rt 34,16,8704
s_add_u32 s51, s51, 0x10000
buffer_load_dwordx4 a[96:99], v2, s[20:23], s51 offen offset:0 sc0 sc1
Mz 76,4,196
Rt 36,17,8192
Cv 184,184,185
Mz 68,4,192
)" R"(
Rt 38,17,8704
Cv 185,186,187
Mz 80,8,192
Rt 40,18,8192
Cv 186,188,189
Mz 88,8,196
Rt 42,18,8704
buffer_load_dwordx4 v9, s[16:19], s92 offen offset:1024 lds
Mz 92,12,196
Rt 44,19,8192
Cv 187,190,191
v_mul_f32_e32 v32, s54, v32
Mz 84,12,192
Rt 46,19,8704
s_waitcnt lgkmcnt(8)
Mv 64,16,200
Rt 0,16,12288
Cv 192,192,193
Mv 72,16,204
Rt 2,16,12800
buffer_store_dwordx4 v[168:171], v2, s[20:23], s47 offen offset:1024 sc0 sc1
Mv 76,20,204
Rt 4,17,12288
Cv 193,194,195
Cv 194,196,197
Mv 68,20,200
Rt 6,17,12800
Cv 195,198,199
v_mul_f32_e32 v33, s54, v33
Mv 80,24,200
Rt 8,18,12288
Cv 200,200,201
Mv 88,24,204
Rt 10,18,12800
buffer_load_dwordx4 v10, s[16:19], s92 offen offset:2048 lds
Mv 92,28,204
Rt 12,19,12288
Cv 201,202,203
Cv 32,32,33
Mv 84,28,200
Rt 14,19,12800
Cv 202,204,205
s_waitcnt lgkmcnt(8)
Mv 64,32,208
Rt 16,16,16384
Cv 203,206,207
Mv 72,32,212
Rt 18,16,16896
buffer_load_dwordx4 a[100:103], v2, s[20:23], s51 offen offset:1024 sc0 sc1
Mv 76,36,212
Rt 20,17,16384
Cv 208,208,209
Mv 68,36,208
Rt 22,17,16896
Cv 209,210,211
v_accvgpr_write_b32 a224, v32
Mv 80,40,208
Rt 24,18,16384
Cv 210,212,213
Mv 88,40,212
Rt 26,18,16896
buffer_load_dwordx4 v11, s[16:19], s92 offen offset:3072 lds
Mv 92,44,212
Rt 28,19,16384
)" R"(
Cv 211,214,215
v_mul_f32_e32 v34, s54, v34
Mv 84,44,208
Rt 30,19,16896
s_waitcnt lgkmcnt(8)
Mv 64,0,216
Rt 32,16,20480
Cv 216,216,217
Mv 72,0,220
Rt 34,16,20992
buffer_store_dwordx4 v[176:179], v2, s[20:23], s47 offen offset:2048 sc0 sc1
Mv 76,4,220
Rt 36,17,20480
Cv 217,218,219
Cv 218,220,221
Mv 68,4,216
Rt 38,17,20992
s_add_u32 s92, s42, 0x40180
v_mul_f32_e32 v35, s54, v35
Mv 80,8,216
Rt 40,18,20480
s_add_u32 m0, s41, 4096
Mv 88,8,220
Rt 42,18,20992
buffer_load_dwordx4 v8, s[16:19], s92 offen offset:0 lds
Mv 92,12,220
Rt 44,19,20480
Cv 219,222,223
Cv 34,34,35
Mv 84,12,216
Rt 46,19,20992
s_waitcnt lgkmcnt(8)
Mv 64,16,128
Rt 0,16,24576
v_accvgpr_write_b32 a225, v34
Mv 72,16,132
Rt 2,16,25088
buffer_load_dwordx4 a[104:107], v2, s[20:23], s51 offen offset:2048 sc0 sc1
Mv 76,20,132
Rt 4,17,24576
v_mul_f32_e32 v36, s54, v36
Mv 68,20,128
Rt 6,17,25088
v_mul_f32_e32 v37, s54, v37
Cv 36,36,37
Mv 80,24,128
Rt 8,18,24576
v_accvgpr_write_b32 a226, v36
Mv 88,24,132
Rt 10,18,25088
buffer_load_dwordx4 v9, s[16:19], s92 offen offset:1024 lds
Mv 92,28,132
Rt 12,19,24576
v_mul_f32_e32 v38, s54, v38
v_mul_f32_e32 v39, s54, v39
Mv 84,28,128
Rt 14,19,25088
s_waitcnt lgkmcnt(8)
Mv 64,32,136
Rt 16,12,32768
buffer_store_dwordx4 v[184:187], v2, s[20:23], s47 offen offset:3072 sc0 sc1
Mv 72,32,140
Rt 18,12,33280
Cv 38,38,39
)" R"(
v_accvgpr_write_b32 a227, v38
Mv 76,36,140
Rt 20,13,32768
v_mul_f32_e32 v40, s54, v40
Mv 68,36,136
Rt 22,13,33280
buffer_load_dwordx4 a[108:111], v2, s[20:23], s51 offen offset:3072 sc0 sc1
Mv 80,40,136
Rt 24,14,32768
v_mul_f32_e32 v41, s54, v41
Mv 88,40,140
Rt 26,14,33280
buffer_load_dwordx4 v10, s[16:19], s92 offen offset:2048 lds
Mv 92,44,140
Rt 28,15,32768
Cv 40,40,41
v_accvgpr_write_b32 a228, v40
Mv 84,44,136
Rt 30,15,33280
s_waitcnt vmcnt(19) lgkmcnt(8)
s_barrier
buffer_store_dword v6, v7, s[20:23], s49 offen offset:1024 sc0 sc1
Mv 64,0,144
Rt 32,12,36864
buffer_load_dword v4, v5, s[20:23], s50 offen offset:0 sc0 sc1
Mv 72,0,148
Rt 34,12,37376
v_mul_f32_e32 v42, s54, v42
v_mul_f32_e32 v43, s54, v43
Mv 76,4,148
Rt 36,13,36864
Cv 42,42,43
Mv 68,4,144
Rt 38,13,37376
s_add_u32 s92, s42, 0x1c0180
v_accvgpr_write_b32 a229, v42
Mv 80,8,144
Rt 40,14,36864
s_add_u32 m0, s41, 65536
Mv 88,8,148
Rt 42,14,37376
buffer_load_dwordx4 v8, s[16:19], s92 offen offset:0 lds
Mv 92,12,148
Rt 44,15,36864
v_mul_f32_e32 v44, s54, v44
v_mul_f32_e32 v45, s54, v45
Mv 84,12,144
Rt 46,15,37376
s_waitcnt lgkmcnt(8)
Mv 64,16,152
Rt 0,12,40960
Cv 44,44,45
Mv 72,16,156
Rt 2,12,41472
buffer_load_dwordx4 a[112:115], v3, s[20:23], s51 offen offset:0 sc0 sc1
Mv 76,20,156
Rt 4,13,40960
v_accvgpr_write_b32 a230, v44
Mv 68,20,152
Rt 6,13,41472
v_mul_f32_e32 v46, s54, v46
v_mul_f32_e32 v47, s54, v47
Mv 80,24,152
Rt 8,14,40960
Cv 46,46,47
)" R"(
Mv 88,24,156
Rt 10,14,41472
buffer_load_dwordx4 v9, s[16:19], s92 offen offset:1024 lds
Mv 92,28,156
Rt 12,15,40960
v_accvgpr_write_b32 a231, v46
v_mul_f32_e32 v48, s54, v48
Mv 84,28,152
Rt 14,15,41472
s_waitcnt lgkmcnt(8)
Mv 64,32,160
Rt 16,12,45056
v_mul_f32_e32 v49, s54, v49
Mv 72,32,164
Rt 18,12,45568
buffer_store_dwordx4 v[192:195], v3, s[20:23], s47 offen offset:0 sc0 sc1
Mv 76,36,164
Rt 20,13,45056
Cv 48,48,49
Mv 68,36,160
Rt 22,13,45568
v_accvgpr_write_b32 a232, v48
v_mul_f32_e32 v50, s54, v50
Mv 80,40,160
Rt 24,14,45056
v_mul_f32_e32 v51, s54, v51
Mv 88,40,164
Rt 26,14,45568
buffer_load_dwordx4 v10, s[16:19], s92 offen offset:2048 lds
Mv 92,44,164
Rt 28,15,45056
Cv 50,50,51
v_accvgpr_write_b32 a233, v50
Mv 84,44,160
Rt 30,15,45568
s_waitcnt lgkmcnt(8)
Mv 64,0,168
Rt 32,12,49152
v_mul_f32_e32 v52, s54, v52
Mv 72,0,172
Rt 34,12,49664
buffer_load_dwordx4 a[116:119], v3, s[20:23], s51 offen offset:1024 sc0 sc1
Mv 76,4,172
Rt 36,13,49152
v_mul_f32_e32 v53, s54, v53
Mv 68,4,168
Rt 38,13,49664
Cv 52,52,53
v_accvgpr_write_b32 a234, v52
Mv 80,8,168
Rt 40,14,49152
v_mul_f32_e32 v54, s54, v54
Mv 88,8,172
Rt 42,14,49664
buffer_load_dwordx4 v11, s[16:19], s92 offen offset:3072 lds
Mv 92,12,172
Rt 44,15,49152
v_mul_f32_e32 v55, s54, v55
Cv 54,54,55
Mv 84,12,168
Rt 46,15,49664
s_waitcnt lgkmcnt(8)
Mv 64,16,176
Rt 0,12,53248
)" R"(
v_accvgpr_write_b32 a235, v54
Mv 72,16,180
Rt 2,12,53760
buffer_store_dwordx4 v[200:203], v3, s[20:23], s47 offen offset:1024 sc0 sc1
Mv 76,20,180
Rt 4,13,53248
v_mul_f32_e32 v56, s54, v56
Mv 68,20,176
Rt 6,13,53760
s_add_u32 s92, s42, 0x200180
v_mul_f32_e32 v57, s54, v57
Mv 80,24,176
Rt 8,14,53248
s_add_u32 m0, s41, 69632
Mv 88,24,180
Rt 10,14,53760
buffer_load_dwordx4 v8, s[16:19], s92 offen offset:0 lds
Mv 92,28,180
Rt 12,15,53248
Cv 56,56,57
v_accvgpr_write_b32 a236, v56
Mv 84,28,176
Rt 14,15,53760
s_waitcnt lgkmcnt(8)
Mv 64,32,184
Rt 16,12,57344
v_mul_f32_e32 v58, s54, v58
Mv 72,32,188
Rt 18,12,57856
buffer_load_dwordx4 a[120:123], v3, s[20:23], s51 offen offset:2048 sc0 sc1
Mv 76,36,188
Rt 20,13,57344
v_mul_f32_e32 v59, s54, v59
Mv 68,36,184
Rt 22,13,57856
Cv 58,58,59
v_accvgpr_write_b32 a237, v58
Mv 80,40,184
Rt 24,14,57344
v_mul_f32_e32 v60, s54, v60
Mv 88,40,188
Rt 26,14,57856
buffer_load_dwordx4 v9, s[16:19], s92 offen offset:1024 lds
Mv 92,44,188
Rt 28,15,57344
v_mul_f32_e32 v61, s54, v61
Cv 60,60,61
Mv 84,44,184
Rt 30,15,57856
s_waitcnt lgkmcnt(8)
Mv 64,0,48
Rt 32,16,32768
buffer_store_dwordx4 v[208:211], v3, s[20:23], s47 offen offset:2048 sc0 sc1
Mv 72,0,52
Rt 34,16,33280
v_accvgpr_write_b32 a238, v60
v_mul_f32_e32 v62, s54, v62
Mv 76,4,52
Rt 36,17,32768
v_mul_f32_e32 v63, s54, v63
Mv 68,4,48
Rt 38,17,33280
buffer_load_dwordx4 a[124:127], v3, s[20:23], s51 offen offset:3072 sc0 sc1
Mv 80,8,48
)" R"(
Rt 40,18,32768
Cv 62,62,63
Mv 88,8,52
Rt 42,18,33280
buffer_load_dwordx4 v10, s[16:19], s92 offen offset:2048 lds
Mv 92,12,52
Rt 44,19,32768
v_accvgpr_write_b32 a239, v62
Mv 84,12,48
Rt 46,19,33280
s_waitcnt vmcnt(14) lgkmcnt(8)
s_barrier
Mv 64,16,56
Rt 0,16,36864
v_cmp_eq_u32_e32 vcc, s3, v4
s_andn2_b64 s[28:29], -1, vcc
s_cbranch_scc1 .Lzsp5
.Lzok5:
Mv 72,16,60
Rt 2,16,37376
s_add_u32 s40, s55, 0x10000
Mv 76,20,60
Rt 4,17,36864
Mv 68,20,56
Rt 6,17,37376
Mv 80,24,56
Rt 8,18,36864
s_add_u32 m0, s36, 32768
Mv 88,24,60
Rt 10,18,37376
buffer_load_dwordx4 v2, s[20:23], s40 offen offset:0 sc1 lds
Mv 92,28,60
Rt 12,19,36864
Mv 84,28,56
Rt 14,19,37376
s_waitcnt lgkmcnt(8)
Mz 32,32,192
Rt 16,16,40960
buffer_store_dwordx4 v[216:219], v3, s[20:23], s47 offen offset:3072 sc0 sc1
Mz 40,32,196
Rt 18,16,41472
Mz 44,36,196
Rt 20,17,40960
Mz 36,36,192
Rt 22,17,41472
buffer_load_dwordx4 v2, s[20:23], s40 offen offset:1024 sc1 lds
Mz 48,40,192
Rt 24,18,40960
v_mul_f32_e32 v64, s54, v64
Mz 56,40,196
Rt 26,18,41472
v_mul_f32_e32 v65, s54, v65
Mz 60,44,196
Rt 28,19,40960
Cv 64,64,65
Mz 52,44,192
Rt 30,19,41472
s_waitcnt lgkmcnt(8)
Mv 32,0,200
Rt 32,16,45056
v_accvgpr_write_b32 a240, v64
Mv 40,0,204
Rt 34,16,45568
buffer_load_dwordx4 v2, s[20:23], s40 offen offset:2048 sc1 lds
Mv 44,4,204
)" R"(
Rt 36,17,45056
v_mul_f32_e32 v66, s54, v66
Mv 36,4,200
Rt 38,17,45568
v_mul_f32_e32 v67, s54, v67
Mv 48,8,200
Rt 40,18,45056
buffer_store_dwordx4 v[224:227], v2, s[20:23], s48 offen offset:0 sc0 sc1
Mv 56,8,204
Rt 42,18,45568
Cv 66,66,67
Mv 60,12,204
Rt 44,19,45056
v_accvgpr_write_b32 a241, v66
Mv 52,12,200
Rt 46,19,45568
buffer_load_dwordx4 v2, s[20:23], s40 offen offset:3072 sc1 lds
s_waitcnt lgkmcnt(8)
Mv 32,16,208
Rt 0,16,49152
v_mul_f32_e32 v68, s54, v68
Mv 40,16,212
Rt 2,16,49664
v_mul_f32_e32 v69, s54, v69
Mv 44,20,212
Rt 4,17,49152
Cv 68,68,69
Mv 36,20,208
Rt 6,17,49664
v_accvgpr_write_b32 a242, v68
Mv 48,24,208
Rt 8,18,49152
s_add_u32 m0, s36, 36864
Mv 56,24,212
Rt 10,18,49664
buffer_load_dwordx4 v3, s[20:23], s40 offen offset:0 sc1 lds
Mv 60,28,212
Rt 12,19,49152
v_mul_f32_e32 v70, s54, v70
Mv 52,28,208
Rt 14,19,49664
s_waitcnt lgkmcnt(8)
Mv 32,32,216
Rt 16,16,53248
buffer_store_dwordx4 v[232:235], v2, s[20:23], s48 offen offset:1024 sc0 sc1
Mv 40,32,220
Rt 18,16,53760
v_mul_f32_e32 v71, s54, v71
Mv 44,36,220
Rt 20,17,53248
Cv 70,70,71
Mv 36,36,216
Rt 22,17,53760
buffer_load_dwordx4 v3, s[20:23], s40 offen offset:1024 sc1 lds
Mv 48,40,216
Rt 24,18,53248
v_accvgpr_write_b32 a243, v70
Mv 56,40,220
Rt 26,18,53760
v_mul_f32_e32 v72, s54, v72
Mv 60,44,220
Rt 28,19,53248
v_mul_f32_e32 v73, s54, v73
Mv 52,44,216
)" R"(
Rt 30,19,53760
s_waitcnt lgkmcnt(8)
Mv 32,0,128
Rt 32,16,57344
Cv 72,72,73
Mv 40,0,132
Rt 34,16,57856
buffer_load_dwordx4 v3, s[20:23], s40 offen offset:2048 sc1 lds
Mv 44,4,132
Rt 36,17,57344
v_accvgpr_write_b32 a244, v72
Mv 36,4,128
Rt 38,17,57856
v_mul_f32_e32 v74, s54, v74
Mv 48,8,128
Rt 40,18,57344
buffer_store_dwordx4 v[240:243], v2, s[20:23], s48 offen offset:2048 sc0 sc1
Mv 56,8,132
Rt 42,18,57856
v_mul_f32_e32 v75, s54, v75
Mv 60,12,132
Rt 44,19,57344
Cv 74,74,75
Mv 52,12,128
Rt 46,19,57856
buffer_load_dwordx4 v3, s[20:23], s40 offen offset:3072 sc1 lds
s_waitcnt lgkmcnt(8)
Mv 32,16,136
Rt 0,20,0
v_accvgpr_write_b32 a245, v74
Mv 40,16,140
Rt 2,20,512
v_mul_f32_e32 v76, s54, v76
Mv 44,20,140
Rt 4,21,0
v_mul_f32_e32 v77, s54, v77
Mv 36,20,136
Rt 6,21,512
Cv 76,76,77
Mv 48,24,136
Rt 8,22,0
v_accvgpr_write_b32 a246, v76
Mv 56,24,140
Rt 10,22,512
v_mul_f32_e32 v78, s54, v78
Mv 60,28,140
Rt 12,23,0
v_mul_f32_e32 v79, s54, v79
Mv 52,28,136
Rt 14,23,512
s_waitcnt vmcnt(10) lgkmcnt(8)
s_barrier
buffer_store_dword v6, v7, s[20:23], s49 offen offset:2048 sc0 sc1
Mv 32,32,144
Rt 16,20,4096
s_mov_b32 s83, 0xc108000
buffer_load_dwordx2 v[24:25], v7, s[20:23], s83 offen sc0 sc1
Mv 40,32,148
Rt 18,20,4608
Cv 78,78,79
Mv 44,36,148
Rt 20,21,4096
s_add_u32 m0, s36, 98304
Mv 36,36,144
Rt 22,21,4608
)" R"(
buffer_load_dwordx4 v2, s[20:23], s55 offen offset:0 sc1 lds
Mv 48,40,144
Rt 24,22,4096
v_accvgpr_write_b32 a247, v78
Mv 56,40,148
Rt 26,22,4608
v_mul_f32_e32 v80, s54, v80
Mv 60,44,148
Rt 28,23,4096
buffer_store_dwordx4 v[248:251], v2, s[20:23], s48 offen offset:3072 sc0 sc1
Mv 52,44,144
Rt 30,23,4608
s_waitcnt lgkmcnt(8)
Mv 32,0,152
Rt 32,20,8192
v_mul_f32_e32 v81, s54, v81
Mv 40,0,156
Rt 34,20,8704
buffer_load_dwordx4 v2, s[20:23], s55 offen offset:1024 sc1 lds
Mv 44,4,156
Rt 36,21,8192
Cv 80,80,81
Mv 36,4,152
Rt 38,21,8704
v_accvgpr_write_b32 a248, v80
Mv 48,8,152
Rt 40,22,8192
v_mul_f32_e32 v82, s54, v82
Mv 56,8,156
Rt 42,22,8704
v_mul_f32_e32 v83, s54, v83
Mv 60,12,156
Rt 44,23,8192
Cv 82,82,83
Mv 52,12,152
Rt 46,23,8704
buffer_load_dwordx4 v2, s[20:23], s55 offen offset:2048 sc1 lds
s_waitcnt lgkmcnt(8)
Mv 32,16,160
Rt 0,20,12288
v_accvgpr_write_b32 a249, v82
Mv 40,16,164
Rt 2,20,12800
v_mul_f32_e32 v84, s54, v84
Mv 44,20,164
Rt 4,21,12288
buffer_store_dwordx4 v[228:231], v3, s[20:23], s48 offen offset:0 sc0 sc1
Mv 36,20,160
Rt 6,21,12800
v_mul_f32_e32 v85, s54, v85
Mv 48,24,160
Rt 8,22,12288
Cv 84,84,85
Mv 56,24,164
Rt 10,22,12800
buffer_load_dwordx4 v2, s[20:23], s55 offen offset:3072 sc1 lds
Mv 60,28,164
Rt 12,23,12288
v_accvgpr_write_b32 a250, v84
Mv 52,28,160
Rt 14,23,12800
s_waitcnt lgkmcnt(8)
Mv 32,32,168
Rt 16,20,16384
)" R"(
v_mul_f32_e32 v86, s54, v86
Mv 40,32,172
Rt 18,20,16896
v_mul_f32_e32 v87, s54, v87
Mv 44,36,172
Rt 20,21,16384
s_add_u32 m0, s36, 102400
Mv 36,36,168
Rt 22,21,16896
buffer_load_dwordx4 v3, s[20:23], s55 offen offset:0 sc1 lds
Mv 48,40,168
Rt 24,22,16384
Cv 86,86,87
Mv 56,40,172
Rt 26,22,16896
v_accvgpr_write_b32 a251, v86
Mv 60,44,172
Rt 28,23,16384
buffer_store_dwordx4 v[236:239], v3, s[20:23], s48 offen offset:1024 sc0 sc1
Mv 52,44,168
Rt 30,23,16896
s_waitcnt lgkmcnt(8)
Mv 32,0,176
Rt 32,20,20480
v_mul_f32_e32 v88, s54, v88
Mv 40,0,180
Rt 34,20,20992
buffer_load_dwordx4 v3, s[20:23], s55 offen offset:1024 sc1 lds
Mv 44,4,180
Rt 36,21,20480
v_mul_f32_e32 v89, s54, v89
Mv 36,4,176
Rt 38,21,20992
Cv 88,88,89
Mv 48,8,176
Rt 40,22,20480
s_add_u32 s50, s50, 1024
Mv 56,8,180
Rt 42,22,20992
s_min_u32 s50, s50, s60
Mv 60,12,180
Rt 44,23,20480
v_accvgpr_write_b32 a252, v88
Mv 52,12,176
Rt 46,23,20992
buffer_load_dwordx4 v3, s[20:23], s55 offen offset:2048 sc1 lds
s_waitcnt lgkmcnt(8)
Mv 32,16,184
Rt 0,20,24576
v_mul_f32_e32 v90, s54, v90
Mv 40,16,188
Rt 2,20,25088
v_mul_f32_e32 v91, s54, v91
Mv 44,20,188
Rt 4,21,24576
buffer_store_dwordx4 v[244:247], v3, s[20:23], s48 offen offset:2048 sc0 sc1
Mv 36,20,184
Rt 6,21,25088
Cv 90,90,91
Mv 48,24,184
Rt 8,22,24576
v_accvgpr_write_b32 a253, v90
Mv 56,24,188
Rt 10,22,25088
)" R"(
buffer_load_dwordx4 v3, s[20:23], s55 offen offset:3072 sc1 lds
Mv 60,28,188
Rt 12,23,24576
v_mul_f32_e32 v92, s54, v92
Mv 52,28,184
Rt 14,23,25088
s_waitcnt lgkmcnt(8)
Mv 32,32,48
Rt 16,12,0
v_mul_f32_e32 v93, s54, v93
Mv 40,32,52
Rt 18,12,512
Cv 92,92,93
Mv 44,36,52
Rt 20,13,0
v_accvgpr_write_b32 a254, v92
Mv 36,36,48
Rt 22,13,512
v_mul_f32_e32 v94, s54, v94
Mv 48,40,48
Rt 24,14,0
buffer_store_dwordx4 v[252:255], v3, s[20:23], s48 offen offset:3072 sc0 sc1
Mv 56,40,52
Rt 26,14,512
v_mul_f32_e32 v95, s54, v95
Mv 60,44,52
Rt 28,15,0
Cv 94,94,95
Mv 52,44,48
Rt 30,15,512
s_waitcnt vmcnt(13) lgkmcnt(8)
s_barrier
v_cmp_eq_u32_e32 vcc, s3, v25
s_andn2_b64 s[28:29], 1, vcc
s_cbranch_scc1 .Lzsp7
.Lzok7:
Mv 32,0,56
Rt 32,12,4096
buffer_load_dword v4, v5, s[20:23], s50 offen offset:0 sc0 sc1
Mv 40,0,60
Rt 34,12,4608
v_accvgpr_write_b32 a255, v94
Mv 44,4,60
Rt 36,13,4096
s_add_u32 m0, s36, 131072
Mv 36,4,56
Rt 38,13,4608
buffer_load_dwordx4 v2, s[20:23], s56 offen offset:0 sc1 lds
Mv 48,8,56
Rt 40,14,4096
Mv 56,8,60
Rt 42,14,4608
Mv 60,12,60
Rt 44,15,4096
Mv 52,12,56
Rt 46,15,4608
s_waitcnt lgkmcnt(8)
Mz 96,16,192
Rt 0,12,8192
Mz 104,16,196
Rt 2,12,8704
buffer_load_dwordx4 v2, s[20:23], s56 offen offset:1024 sc1 lds
Mz 108,20,196
Rt 4,13,8192
Mz 100,20,192
)" R"(
Rt 6,13,8704
Mz 112,24,192
Rt 8,14,8192
v_mul_f32_e32 v32, s54, v32
Mz 120,24,196
Rt 10,14,8704
v_mul_f32_e32 v33, s54, v33
Mz 124,28,196
Rt 12,15,8192
Cv 32,32,33
Mz 116,28,192
Rt 14,15,8704
buffer_load_dwordx4 v2, s[20:23], s56 offen offset:2048 sc1 lds
s_waitcnt lgkmcnt(8)
Mv 96,32,200
Rt 16,12,12288
v_mul_f32_e32 v34, s54, v34
Mv 104,32,204
Rt 18,12,12800
v_accvgpr_write_b32 a192, v32
Mv 108,36,204
Rt 20,13,12288
v_mul_f32_e32 v35, s54, v35
Mv 100,36,200
Rt 22,13,12800
Cv 34,34,35
v_accvgpr_write_b32 a193, v34
Mv 112,40,200
Rt 24,14,12288
v_mul_f32_e32 v36, s54, v36
Mv 120,40,204
Rt 26,14,12800
buffer_load_dwordx4 v2, s[20:23], s56 offen offset:3072 sc1 lds
Mv 124,44,204
Rt 28,15,12288
v_mul_f32_e32 v37, s54, v37
Mv 116,44,200
Rt 30,15,12800
s_waitcnt lgkmcnt(8)
Mv 96,0,208
Rt 32,12,16384
Cv 36,36,37
Mv 104,0,212
Rt 34,12,16896
v_accvgpr_write_b32 a194, v36
Mv 108,4,212
Rt 36,13,16384
s_add_u32 m0, s36, 135168
Mv 100,4,208
Rt 38,13,16896
buffer_load_dwordx4 v3, s[20:23], s56 offen offset:0 sc1 lds
Mv 112,8,208
Rt 40,14,16384
v_mul_f32_e32 v38, s54, v38
Mv 120,8,212
Rt 42,14,16896
v_mul_f32_e32 v39, s54, v39
Mv 124,12,212
Rt 44,15,16384
Cv 38,38,39
Mv 116,12,208
Rt 46,15,16896
s_waitcnt lgkmcnt(8)
Mv 96,16,216
)" R"(
Rt 0,12,20480
v_accvgpr_write_b32 a195, v38
Mv 104,16,220
Rt 2,12,20992
buffer_load_dwordx4 v3, s[20:23], s56 offen offset:1024 sc1 lds
Mv 108,20,220
Rt 4,13,20480
v_mul_f32_e32 v40, s54, v40
Mv 100,20,216
Rt 6,13,20992
v_mul_f32_e32 v41, s54, v41
Cv 40,40,41
Mv 112,24,216
Rt 8,14,20480
v_accvgpr_write_b32 a196, v40
Mv 120,24,220
Rt 10,14,20992
v_mul_f32_e32 v42, s54, v42
Mv 124,28,220
Rt 12,15,20480
v_mul_f32_e32 v43, s54, v43
Mv 116,28,216
Rt 14,15,20992
buffer_load_dwordx4 v3, s[20:23], s56 offen offset:2048 sc1 lds
s_waitcnt lgkmcnt(8)
Mv 96,32,128
Rt 16,12,24576
Cv 42,42,43
Mv 104,32,132
Rt 18,12,25088
v_accvgpr_write_b32 a197, v42
Mv 108,36,132
Rt 20,13,24576
v_mul_f32_e32 v44, s54, v44
Mv 100,36,128
Rt 22,13,25088
v_mul_f32_e32 v45, s54, v45
Cv 44,44,45
Mv 112,40,128
Rt 24,14,24576
v_accvgpr_write_b32 a198, v44
Mv 120,40,132
Rt 26,14,25088
buffer_load_dwordx4 v3, s[20:23], s56 offen offset:3072 sc1 lds
Mv 124,44,132
Rt 28,15,24576
v_mul_f32_e32 v46, s54, v46
Mv 116,44,128
Rt 30,15,25088
s_waitcnt lgkmcnt(8)
Mv 96,0,136
Rt 32,16,0
v_readfirstlane_b32 s63, v24
Mv 104,0,140
Rt 34,16,512
v_and_b32 v24, 16, v1
Mv 108,4,140
Rt 36,17,0
v_lshrrev_b32_e64 v24, v24, s63
Mv 100,4,136
Rt 38,17,512
v_and_b32 v24, 0x7ff, v24
v_mul_u32_u24 v24, 896, v24
Mv 112,8,136
)" R"(
Rt 40,18,0
v_and_b32 v25, 14, v0
Mv 120,8,140
Rt 42,18,512
v_min_u32 v25, 1, v25
Mv 124,12,140
Rt 44,19,0
v_lshlrev_b32 v25, 30, v25
v_and_or_b32 v25, v0, 48, v25
Mv 116,12,136
Rt 46,19,512
s_waitcnt vmcnt(8) lgkmcnt(8)
s_barrier
buffer_store_dword v6, v7, s[20:23], s49 offen offset:3072 sc0 sc1
Mv 96,16,144
Rt 0,16,4096
v_cmp_eq_u32_e32 vcc, s3, v4
s_andn2_b64 s[28:29], -1, vcc
s_cbranch_scc1 .Lzsp6
.Lzok6:
Mv 104,16,148
Rt 2,16,4608
v_add_u32 v24, v24, v25
v_add_u32 v8, 0x10000, v1
Mv 108,20,148
Rt 4,17,4096
s_and_b32 s83, s33, 1
v_add_u32 v10, 0x20000, v1
Mv 100,20,144
Rt 6,17,4608
s_mul_i32 s82, s83, 448
v_mul_f32_e32 v47, s54, v47
s_add_u32 s56, s56, 0x10000
Mv 112,24,144
Rt 8,18,4096
Cv 46,46,47
s_add_u32 s55, s55, 0x10000
Mv 120,24,148
Rt 10,18,4608
v_accvgpr_write_b32 a199, v46
s_add_u32 s55, s55, 0x10000
Mv 124,28,148
Rt 12,19,4096
v_mul_f32_e32 v48, s54, v48
s_cmp_eq_u32 s57, 2
s_cselect_b32 s55, s59, s55
Mv 116,28,144
Rt 14,19,4608
s_mov_b32 s93, 0
s_waitcnt lgkmcnt(8)
Mv 96,32,152
Rt 16,16,8192
v_mul_f32_e32 v49, s54, v49
s_mov_b32 s94, 1
Mv 104,32,156
Rt 18,16,8704
Cv 48,48,49
Mv 108,36,156
Rt 20,17,8192
v_accvgpr_write_b32 a200, v48
Mv 100,36,152
Rt 22,17,8704
v_mul_f32_e32 v50, s54, v50
v_mul_f32_e32 v51, s54, v51
Mv 112,40,152
)" R"(
Rt 24,18,8192
Cv 50,50,51
Mv 120,40,156
Rt 26,18,8704
v_accvgpr_write_b32 a201, v50
Mv 124,44,156
Rt 28,19,8192
v_mul_f32_e32 v52, s54, v52
Mv 116,44,152
Rt 30,19,8704
s_waitcnt lgkmcnt(8)
Mv 96,0,160
Rt 32,16,12288
v_mul_f32_e32 v53, s54, v53
Mv 104,0,164
Rt 34,16,12800
Cv 52,52,53
Mv 108,4,164
Rt 36,17,12288
v_accvgpr_write_b32 a202, v52
Mv 100,4,160
Rt 38,17,12800
buffer_load_dwordx4 a[128:131], v24, s[12:15], s82 offen offset:0
Mv 112,8,160
Rt 40,18,12288
v_mul_f32_e32 v54, s54, v54
Mv 120,8,164
Rt 42,18,12800
v_mul_f32_e32 v55, s54, v55
Mv 124,12,164
Rt 44,19,12288
Cv 54,54,55
Mv 116,12,160
Rt 46,19,12800
s_waitcnt lgkmcnt(8)
Mv 96,16,168
Rt 0,16,16384
buffer_load_dwordx4 a[132:135], v24, s[12:15], s82 offen offset:64
Mv 104,16,172
Rt 2,16,16896
v_accvgpr_write_b32 a203, v54
Mv 108,20,172
Rt 4,17,16384
v_mul_f32_e32 v56, s54, v56
Mv 100,20,168
Rt 6,17,16896
v_mul_f32_e32 v57, s54, v57
Cv 56,56,57
Mv 112,24,168
Rt 8,18,16384
v_accvgpr_write_b32 a204, v56
Mv 120,24,172
Rt 10,18,16896
buffer_load_dwordx4 a[136:139], v24, s[12:15], s82 offen offset:128
Mv 124,28,172
Rt 12,19,16384
v_mul_f32_e32 v58, s54, v58
Mv 116,28,168
Rt 14,19,16896
s_waitcnt lgkmcnt(8)
Mv 96,32,176
Rt 16,16,20480
buffer_load_dwordx4 a[140:143], v24, s[12:15], s82 offen offset:192
Mv 104,32,180
)" R"(
Rt 18,16,20992
v_mul_f32_e32 v59, s54, v59
Mv 108,36,180
Rt 20,17,20480
Cv 58,58,59
Mv 100,36,176
Rt 22,17,20992
v_accvgpr_write_b32 a205, v58
v_mul_f32_e32 v60, s54, v60
Mv 112,40,176
Rt 24,18,20480
v_mul_f32_e32 v61, s54, v61
Mv 120,40,180
Rt 26,18,20992
buffer_load_dwordx4 a[144:147], v24, s[12:15], s82 offen offset:256
Mv 124,44,180
Rt 28,19,20480
Cv 60,60,61
Mv 116,44,176
Rt 30,19,20992
s_waitcnt lgkmcnt(8)
s_barrier
Mv 96,0,184
Rt 32,16,24576
buffer_load_dwordx4 a[148:151], v24, s[12:15], s82 offen offset:320
Mv 104,0,188
Rt 34,16,25088
v_accvgpr_write_b32 a206, v60
Mv 108,4,188
Rt 36,17,24576
v_mul_f32_e32 v62, s54, v62
Mv 100,4,184
Rt 38,17,25088
v_mul_f32_e32 v63, s54, v63
Cv 62,62,63
Mv 112,8,184
Rt 40,18,24576
v_accvgpr_write_b32 a207, v62
Mv 120,8,188
Rt 42,18,25088
buffer_load_dwordx4 a[152:155], v24, s[12:15], s82 offen offset:384
Mv 124,12,188
Rt 44,19,24576
Mv 116,12,184
Rt 46,19,25088
s_waitcnt lgkmcnt(8)
Mv 96,16,48
Rb 0,8,32768
Mv 104,16,52
Rb 4,8,33792
Mv 108,20,52
Rb 8,8,34816
Mv 100,20,48
Rb 12,8,35840
Mv 112,24,48
Rb 16,8,40960
Rb 20,8,41984
Mv 120,24,52
Rb 24,8,43008
Mv 124,28,52
Mv 116,28,48
Rb 28,8,44032
s_waitcnt lgkmcnt(8)
Mv 96,32,56
Mv 104,32,60
)" R"(
Rb 32,8,36864
Mv 108,36,60
Mv 100,36,56
Rb 36,8,37888
Mv 112,40,56
Mv 120,40,60
Rb 40,8,38912
Mv 124,44,60
Mv 116,44,56
Rb 44,8,39936
s_cmp_eq_u32 s35, 7
s_cbranch_scc1 .LzA7
.LzA7R:
s_waitcnt lgkmcnt(8)
Mz 64,0,64
Rb 48,8,45056
v_mul_f32_e32 v96, s54, v96
Mz 68,0,68
v_mul_f32_e32 v97, s54, v97
Cv 96,96,97
Mv 68,4,76
Rb 52,8,46080
s_add_u32 m0, s36, 0
Mv 64,4,72
buffer_load_dwordx4 v2, s[20:23], s55 offen offset:0 sc1 lds
Mv 64,8,80
Rb 56,8,47104
v_accvgpr_write_b32 a208, v96
Mv 68,8,84
v_mul_f32_e32 v98, s54, v98
v_mul_f32_e32 v99, s54, v99
Mv 68,12,92
Rb 60,8,48128
s_waitcnt lgkmcnt(8)
Mv 64,12,88
Cv 98,98,99
v_accvgpr_write_b32 a209, v98
v_mul_f32_e32 v100, s54, v100
Mv 64,16,96
Rb 0,8,49152
v_mul_f32_e32 v101, s54, v101
Mv 68,16,100
Cv 100,100,101
v_accvgpr_write_b32 a210, v100
Mv 68,20,108
Rb 4,8,50176
v_mul_f32_e32 v102, s54, v102
Mv 64,20,104
buffer_load_dwordx4 v2, s[20:23], s55 offen offset:1024 sc1 lds
Mv 64,24,112
Rb 8,8,51200
v_mul_f32_e32 v103, s54, v103
Mv 68,24,116
Cv 102,102,103
v_accvgpr_write_b32 a211, v102
Mv 68,28,124
Rb 12,8,52224
s_waitcnt lgkmcnt(8)
Mv 64,28,120
v_mul_f32_e32 v104, s54, v104
v_mul_f32_e32 v105, s54, v105
Cv 104,104,105
Mz 72,32,64
Rb 16,8,57344
)" R"(
v_accvgpr_write_b32 a212, v104
Mz 76,32,68
v_mul_f32_e32 v106, s54, v106
v_mul_f32_e32 v107, s54, v107
Mv 76,36,76
Rb 20,8,58368
Cv 106,106,107
Mv 72,36,72
buffer_load_dwordx4 v2, s[20:23], s55 offen offset:2048 sc1 lds
Mv 72,40,80
Rb 24,8,59392
v_accvgpr_write_b32 a213, v106
Mv 76,40,84
v_mul_f32_e32 v108, s54, v108
v_mul_f32_e32 v109, s54, v109
Mv 76,44,92
Rb 28,8,60416
s_waitcnt lgkmcnt(8)
Mv 72,44,88
Cv 108,108,109
v_accvgpr_write_b32 a214, v108
v_mul_f32_e32 v110, s54, v110
Mv 72,48,96
Rb 32,8,53248
v_mul_f32_e32 v111, s54, v111
Mv 76,48,100
Cv 110,110,111
v_accvgpr_write_b32 a215, v110
Mv 76,52,108
Rb 36,8,54272
v_mul_f32_e32 v112, s54, v112
Mv 72,52,104
buffer_load_dwordx4 v2, s[20:23], s55 offen offset:3072 sc1 lds
Mv 72,56,112
Rb 40,8,55296
v_mul_f32_e32 v113, s54, v113
Mv 76,56,116
Cv 112,112,113
v_accvgpr_write_b32 a216, v112
Mv 76,60,124
Rb 44,8,56320
s_waitcnt lgkmcnt(8)
Mv 72,60,120
v_mul_f32_e32 v114, s54, v114
v_mul_f32_e32 v115, s54, v115
Cv 114,114,115
Mz 80,0,64
Rb 48,8,61440
v_accvgpr_write_b32 a217, v114
Mz 84,0,68
v_mul_f32_e32 v116, s54, v116
v_mul_f32_e32 v117, s54, v117
Mv 84,4,76
Rb 52,8,62464
s_add_u32 m0, s36, 4096
Mv 80,4,72
buffer_load_dwordx4 v3, s[20:23], s55 offen offset:0 sc1 lds
Mv 80,8,80
Rb 56,8,63488
Cv 116,116,117
Mv 84,8,84
v_accvgpr_write_b32 a218, v116
v_mul_f32_e32 v118, s54, v118
Mv 84,12,92
)" R"(
Rb 60,8,64512
s_waitcnt lgkmcnt(8)
Mv 80,12,88
v_mul_f32_e32 v119, s54, v119
Cv 118,118,119
v_accvgpr_write_b32 a219, v118
Mv 80,16,96
v_mul_f32_e32 v120, s54, v120
v_mul_f32_e32 v121, s54, v121
Mv 84,16,100
Cv 120,120,121
v_accvgpr_write_b32 a220, v120
Mv 84,20,108
v_mul_f32_e32 v122, s54, v122
v_mul_f32_e32 v123, s54, v123
Mv 80,20,104
buffer_load_dwordx4 v3, s[20:23], s55 offen offset:1024 sc1 lds
Mv 80,24,112
s_add_u32 s50, s50, 1024
s_min_u32 s50, s50, s60
Mv 84,24,116
Cv 122,122,123
v_accvgpr_write_b32 a221, v122
Mv 84,28,124
v_mul_f32_e32 v124, s54, v124
v_mul_f32_e32 v125, s54, v125
Mv 80,28,120
Cv 124,124,125
s_waitcnt lgkmcnt(0)
s_barrier
Mz 88,32,64
Rb 0,1,32768
v_accvgpr_write_b32 a222, v124
Mz 92,32,68
Rb 4,1,33792
v_mul_f32_e32 v126, s54, v126
Mv 92,36,76
Rb 8,1,34816
v_mul_f32_e32 v127, s54, v127
Mv 88,36,72
Rb 12,1,35840
buffer_load_dwordx4 v3, s[20:23], s55 offen offset:2048 sc1 lds
Mv 88,40,80
Rb 16,1,40960
Cv 126,126,127
Mv 92,40,84
Rb 20,1,41984
v_accvgpr_write_b32 a223, v126
Mv 92,44,92
Rb 24,1,43008
v_mov_b32 v16, 0
Mv 88,44,88
Rb 28,1,44032
v_mov_b32 v17, 0
v_mov_b32 v18, 0
Mv 88,48,96
Rb 32,1,36864
v_mov_b32 v19, 0
Mv 92,48,100
Rb 36,1,37888
v_mov_b32 v20, 0
Mv 92,52,108
Rb 40,1,38912
v_mov_b32 v21, 0
Mv 88,52,104
)" R"(
Rb 44,1,39936
buffer_load_dwordx4 v3, s[20:23], s55 offen offset:3072 sc1 lds
Mv 88,56,112
v_mov_b32 v22, 0
v_mov_b32 v23, 0
Mv 92,56,116
Mv 92,60,124
s_waitcnt lgkmcnt(8)
Mv 88,60,120
Mz 96,0,64
Rb 48,1,45056
buffer_load_dword v4, v5, s[20:23], s50 offen offset:0 sc0 sc1
Mz 100,0,68
s_cmp_le_i32 s57, s58
s_cbranch_scc1 .Lzmk0
.Lzmr0:
Mv 100,4,76
Rb 52,1,46080
s_add_u32 m0, s36, 65536
s_sub_u32 s57, s57, 1
Mv 96,4,72
buffer_load_dwordx4 v2, s[20:23], s56 offen offset:0 sc1 lds
s_add_u32 s55, s55, 0x10000
Mv 96,8,80
Rb 56,1,47104
s_cmp_eq_u32 s57, 2
s_cselect_b32 s55, s59, s55
Mv 100,8,84
v_exp_f32_e32 v32, v64
Mv 100,12,92
Rb 60,1,48128
s_waitcnt lgkmcnt(8)
Mv 96,12,88
v_exp_f32_e32 v24, v65
Mv 96,16,96
Rb 0,1,49152
v_exp_f32_e32 v33, v66
Mv 100,16,100
v_exp_f32_e32 v25, v67
v_exp_f32_e32 v34, v72
Mv 100,20,108
Rb 4,1,50176
v_exp_f32_e32 v26, v73
Mv 96,20,104
buffer_load_dwordx4 v2, s[20:23], s56 offen offset:1024 sc1 lds
v_add_f32_e32 v14, v32, v24
Mv 96,24,112
Rb 8,1,51200
v_exp_f32_e32 v35, v74
Mv 100,24,116
v_exp_f32_e32 v27, v75
Cv 32,32,24
Mv 100,28,124
Rb 12,1,52224
v_add_f32_e32 v14, v14, v33
s_waitcnt lgkmcnt(8)
Mv 96,28,120
v_exp_f32_e32 v36, v68
v_add_f32_e32 v14, v14, v25
Mz 104,32,64
Rb 16,1,57344
v_exp_f32_e32 v28, v69
Mz 108,32,68
v_exp_f32_e32 v37, v70
)" R"(
v_exp_f32_e32 v29, v71
Mv 108,36,76
Rb 20,1,58368
v_exp_f32_e32 v38, v76
Mv 104,36,72
buffer_load_dwordx4 v2, s[20:23], s56 offen offset:2048 sc1 lds
Cv 33,33,25
Mv 104,40,80
Rb 24,1,59392
v_exp_f32_e32 v24, v77
Mv 108,40,84
v_exp_f32_e32 v39, v78
v_add_f32_e32 v14, v14, v34
Mv 108,44,92
Rb 28,1,60416
v_add_f32_e32 v14, v14, v26
s_waitcnt lgkmcnt(8)
Mv 104,44,88
v_exp_f32_e32 v25, v79
Cv 34,34,26
Mv 104,48,96
Rb 32,1,53248
v_exp_f32_e32 v40, v80
Mv 108,48,100
v_exp_f32_e32 v26, v81
v_add_f32_e32 v14, v14, v35
v_add_f32_e32 v14, v14, v27
Mv 108,52,108
Rb 36,1,54272
Cv 35,35,27
v_add_f32_e32 v15, v36, v28
Mv 104,52,104
buffer_load_dwordx4 v2, s[20:23], s56 offen offset:3072 sc1 lds
Cv 36,36,28
Mv 104,56,112
Rb 40,1,55296
v_exp_f32_e32 v41, v82
Mv 108,56,116
v_exp_f32_e32 v27, v83
v_add_f32_e32 v15, v15, v37
Mv 108,60,124
Rb 44,1,56320
v_add_f32_e32 v15, v15, v29
s_waitcnt lgkmcnt(8)
Mv 104,60,120
v_exp_f32_e32 v42, v88
Cv 37,37,29
Mz 112,0,64
Rb 48,1,61440
v_exp_f32_e32 v28, v89
Mz 116,0,68
v_exp_f32_e32 v43, v90
v_add_f32_e32 v15, v15, v38
v_add_f32_e32 v15, v15, v24
Mv 116,4,76
Rb 52,1,62464
s_add_u32 m0, s36, 69632
Cv 38,38,24
Mv 112,4,72
buffer_load_dwordx4 v3, s[20:23], s56 offen offset:0 sc1 lds
v_add_f32_e32 v15, v15, v39
Mv 112,8,80
Rb 56,1,63488
v_exp_f32_e32 v29, v91
)" R"(
Mv 116,8,84
v_exp_f32_e32 v44, v84
v_add_f32_e32 v15, v15, v25
Mv 116,12,92
Rb 60,1,64512
Cv 39,39,25
s_waitcnt lgkmcnt(8)
Mv 112,12,88
v_exp_f32_e32 v24, v85
v_add_f32_e32 v14, v14, v40
Mv 112,16,96
v_exp_f32_e32 v45, v86
v_add_f32_e32 v14, v14, v26
Mv 116,16,100
v_exp_f32_e32 v25, v87
Cv 40,40,26
v_add_f32_e32 v14, v14, v41
Mv 116,20,108
v_exp_f32_e32 v46, v92
v_add_f32_e32 v14, v14, v27
Mv 112,20,104
buffer_load_dwordx4 v3, s[20:23], s56 offen offset:1024 sc1 lds
Cv 41,41,27
Mv 112,24,112
v_exp_f32_e32 v26, v93
v_add_f32_e32 v14, v14, v42
Mv 116,24,116
v_exp_f32_e32 v47, v94
v_add_f32_e32 v14, v14, v28
Mv 116,28,124
v_exp_f32_e32 v27, v95
Cv 42,42,28
Mv 112,28,120
v_add_f32_e32 v14, v14, v43
s_waitcnt vmcnt(23) lgkmcnt(0)
s_barrier
Mz 120,32,64
Rb 0,10,0
v_add_f32_e32 v14, v14, v29
Cv 43,43,29
Mz 124,32,68
Rb 4,10,1024
v_add_f32_e32 v15, v15, v44
v_add_f32_e32 v15, v15, v24
Cv 44,44,24
Mv 124,36,76
Rb 8,10,2048
v_add_f32_e32 v15, v15, v45
v_add_f32_e32 v15, v15, v25
Mv 120,36,72
Rb 12,10,3072
buffer_load_dwordx4 v3, s[20:23], s56 offen offset:2048 sc1 lds
Mv 120,40,80
Rb 16,10,4096
Cv 45,45,25
v_add_f32_e32 v15, v15, v46
Mv 124,40,84
Rb 20,10,5120
v_add_f32_e32 v15, v15, v26
Cv 46,46,26
Mv 124,44,92
Rb 24,10,6144
v_add_f32_e32 v15, v15, v47
v_add_f32_e32 v15, v15, v27
Mv 120,44,88
)" R"(
Rb 28,10,7168
Cv 47,47,27
v_max_f32_e32 v11, v14, v15
Mv 120,48,96
Rb 32,10,8192
v_mov_b32 v24, v14
v_mov_b32 v25, v15
Mv 124,48,100
Rb 36,10,9216
v_permlane32_swap_b32_e32 v24, v25
v_add_f32_e32 v24, v24, v25
v_mov_b32 v25, v24
Mv 124,52,108
Rb 40,10,10240
v_permlane16_swap_b32_e32 v24, v25
v_add_f32_e32 v24, v24, v25
Mv 120,52,104
Rb 44,10,11264
buffer_load_dwordx4 v3, s[20:23], s56 offen offset:3072 sc1 lds
Mv 120,56,112
v_rcp_f32_e32 v24, v24
Mv 124,56,116
s_add_u32 s56, s56, 0x10000
v_max_f32_e32 v11, v11, v24
v_cmp_nge_f32_e32 vcc, s62, v11
Mv 124,60,124
s_waitcnt lgkmcnt(8)
s_cbranch_vccnz .Lzfx0
.Lzfr0:
Mv 120,60,120
v_mov_b32 v12, v14
v_mov_b32 v13, v15
v_mfma_f32_16x16x32_bf16 v[128:131], a[0:3], v[32:35], 0
Rb 48,10,12288
v_mfma_f32_16x16x32_bf16 v[136:139], a[0:3], v[36:39], 0
s_cmp_le_i32 s57, s58
s_cbranch_scc1 .Lzmk1
.Lzmr1:
v_mfma_f32_16x16x32_bf16 v[140:143], a[4:7], v[36:39], 0
Rb 52,10,13312
s_add_u32 m0, s36, 32768
v_mfma_f32_16x16x32_bf16 v[132:135], a[4:7], v[32:35], 0
buffer_load_dwordx4 v2, s[20:23], s55 offen offset:0 sc1 lds
v_mfma_f32_16x16x32_bf16 v[144:147], a[8:11], v[32:35], 0
Rb 56,10,14336
s_and_b32 s92, s2, 7
v_mfma_f32_16x16x32_bf16 v[152:155], a[8:11], v[36:39], 0
s_mul_i32 s92, s92, 14
s_lshr_b32 s82, s2, 3
v_mfma_f32_16x16x32_bf16 v[156:159], a[12:15], v[36:39], 0
Rb 60,10,15360
s_waitcnt lgkmcnt(8)
v_mfma_f32_16x16x32_bf16 v[148:151], a[12:15], v[32:35], 0
buffer_load_dwordx4 v2, s[20:23], s55 offen offset:1024 sc1 lds
v_mfma_f32_16x16x32_bf16 v[160:163], a[16:19], v[32:35], 0
Rb 0,10,16384
s_add_u32 s82, s82, s92
v_mfma_f32_16x16x32_bf16 v[168:171], a[16:19], v[36:39], 0
v_exp_f32_e32 v48, v96
v_mfma_f32_16x16x32_bf16 v[172:175], a[20:23], v[36:39], 0
Rb 4,10,17408
v_mfma_f32_16x16x32_bf16 v[164:167], a[20:23], v[32:35], 0
buffer_load_dwordx4 v2, s[20:23], s55 offen offset:2048 sc1 lds
v_mfma_f32_16x16x32_bf16 v[176:179], a[24:27], v[32:35], 0
)" R"(
Rb 8,10,18432
v_exp_f32_e32 v28, v97
v_mfma_f32_16x16x32_bf16 v[184:187], a[24:27], v[36:39], 0
v_exp_f32_e32 v49, v98
v_mfma_f32_16x16x32_bf16 v[188:191], a[28:31], v[36:39], 0
Rb 12,10,19456
s_waitcnt lgkmcnt(8)
v_mfma_f32_16x16x32_bf16 v[180:183], a[28:31], v[32:35], 0
buffer_load_dwordx4 v2, s[20:23], s55 offen offset:3072 sc1 lds
v_add_f32_e32 v14, v48, v28
v_mfma_f32_16x16x32_bf16 v[192:195], a[32:35], v[32:35], 0
Rb 16,10,20480
Cv 48,48,28
v_mfma_f32_16x16x32_bf16 v[200:203], a[32:35], v[36:39], 0
v_exp_f32_e32 v29, v99
v_add_f32_e32 v14, v14, v49
v_mfma_f32_16x16x32_bf16 v[204:207], a[36:39], v[36:39], 0
Rb 20,10,21504
s_add_u32 m0, s36, 36864
v_mfma_f32_16x16x32_bf16 v[196:199], a[36:39], v[32:35], 0
buffer_load_dwordx4 v3, s[20:23], s55 offen offset:0 sc1 lds
v_mfma_f32_16x16x32_bf16 v[208:211], a[40:43], v[32:35], 0
Rb 24,10,22528
v_exp_f32_e32 v50, v104
v_mfma_f32_16x16x32_bf16 v[216:219], a[40:43], v[36:39], 0
s_cmp_gt_u32 s33, 1
s_cselect_b32 s24, s78, s76
s_cselect_b32 s25, s79, s77
v_mfma_f32_16x16x32_bf16 v[220:223], a[44:47], v[36:39], 0
Rb 28,10,23552
s_waitcnt lgkmcnt(8)
v_mfma_f32_16x16x32_bf16 v[212:215], a[44:47], v[32:35], 0
buffer_load_dwordx4 v3, s[20:23], s55 offen offset:1024 sc1 lds
v_add_f32_e32 v14, v14, v29
v_mfma_f32_16x16x32_bf16 v[224:227], a[48:51], v[32:35], 0
Rb 32,10,24576
s_mov_b32 s27, 0x20000
v_mfma_f32_16x16x32_bf16 v[232:235], a[48:51], v[36:39], 0
s_cmp_lt_u32 s35, 7
s_cselect_b32 s26, 0x188000, 0
Cv 49,49,29
v_mfma_f32_16x16x32_bf16 v[236:239], a[52:55], v[36:39], 0
Rb 36,10,25600
s_mul_i32 s83, s83, 802816
v_mfma_f32_16x16x32_bf16 v[228:231], a[52:55], v[32:35], 0
buffer_load_dwordx4 v3, s[20:23], s55 offen offset:2048 sc1 lds
v_mfma_f32_16x16x32_bf16 v[240:243], a[56:59], v[32:35], 0
Rb 40,10,26624
s_lshl_b32 s92, s82, 5
v_add_f32_e32 v14, v14, v50
v_mfma_f32_16x16x32_bf16 v[248:251], a[56:59], v[36:39], 0
s_add_u32 s83, s83, s92
v_and_b32 v9, 62, v0
v_mul_u32_u24 v9, 0x6f0, v9
v_mfma_f32_16x16x32_bf16 v[252:255], a[60:63], v[36:39], 0
Rb 44,10,27648
s_waitcnt lgkmcnt(8)
v_mfma_f32_16x16x32_bf16 v[244:247], a[60:63], v[32:35], 0
buffer_load_dwordx4 v3, s[20:23], s55 offen offset:3072 sc1 lds
Mp 128,0,40
Rb 48,10,28672
v_add_u32 v9, v9, v1
Mp 136,0,44
v_exp_f32_e32 v24, v105
)" R"(
Mp 140,4,44
Rb 52,10,29696
v_add_f32_e32 v14, v14, v24
Mp 132,4,40
v_exp_f32_e32 v51, v106
Mp 144,8,40
Rb 56,10,30720
v_exp_f32_e32 v25, v107
Mp 152,8,44
v_exp_f32_e32 v52, v100
Mp 156,12,44
Rb 60,10,31744
s_waitcnt lgkmcnt(8)
Mp 148,12,40
v_exp_f32_e32 v26, v101
Cv 50,50,24
Mp 160,16,40
v_exp_f32_e32 v53, v102
Mp 168,16,44
v_exp_f32_e32 v27, v103
v_add_f32_e32 v14, v14, v51
Mp 172,20,44
v_exp_f32_e32 v54, v108
Mp 164,20,40
v_exp_f32_e32 v28, v109
Mp 176,24,40
v_exp_f32_e32 v55, v110
v_add_f32_e32 v14, v14, v25
Mp 184,24,44
v_exp_f32_e32 v29, v111
Mp 188,28,44
Cv 51,51,25
v_add_f32_e32 v15, v52, v26
Mp 180,28,40
Cv 52,52,26
s_waitcnt vmcnt(16) lgkmcnt(0)
s_barrier
Mp 192,32,40
Rb 0,1,0
s_add_u32 m0, s36, 131072
s_nop 0
buffer_load_dwordx4 v9, s[24:27], s83 offen offset:0 lds
Mp 200,32,44
Rb 4,1,1024
v_add_f32_e32 v15, v15, v53
v_add_f32_e32 v15, v15, v27
Mp 204,36,44
Rb 8,1,2048
s_add_u32 s83, s83, 0x1bc00
buffer_load_dwordx4 v9, s[24:27], s83 offen offset:1024 lds
Mp 196,36,40
Rb 12,1,3072
Cv 53,53,27
Mp 208,40,40
Rb 16,1,8192
s_add_u32 s83, s83, 0x1bc00
buffer_load_dwordx4 v9, s[24:27], s83 offen offset:2048 lds
Mp 216,40,44
Rb 20,1,9216
v_add_f32_e32 v15, v15, v54
Mp 220,44,44
Rb 24,1,10240
s_add_u32 s83, s83, 0x1bc00
buffer_load_dwordx4 v9, s[24:27], s83 offen offset:3072 lds
Mp 212,44,40
)" R"(
Rb 28,1,11264
v_add_f32_e32 v15, v15, v28
Cv 54,54,28
Mp 224,48,40
Rb 32,1,4096
s_add_u32 m0, s36, 135168
s_add_u32 s83, s83, 0x1cc00
buffer_load_dwordx4 v9, s[24:27], s83 offen offset:0 lds
Mp 232,48,44
Rb 36,1,5120
v_add_f32_e32 v15, v15, v55
v_add_f32_e32 v15, v15, v29
Mp 236,52,44
Rb 40,1,6144
s_add_u32 s83, s83, 0x1bc00
buffer_load_dwordx4 v9, s[24:27], s83 offen offset:1024 lds
Mp 228,52,40
Rb 44,1,7168
Cv 55,55,29
Mp 240,56,40
s_add_u32 s83, s83, 0x1bc00
buffer_load_dwordx4 v9, s[24:27], s83 offen offset:2048 lds
v_cmp_eq_u32_e32 vcc, s3, v4
s_andn2_b64 s[28:29], -1, vcc
s_cbranch_scc1 .Lzsp8
.Lzok8:
Mp 248,56,44
s_sub_u32 s57, s57, 1
Mp 252,60,44
s_waitcnt lgkmcnt(8)
Mp 244,60,40
s_cmp_gt_i32 s57, s93
s_cbranch_scc1 .LzL
.LzW:
s_lshl1_add_u32 s57, s35, 2
s_mov_b32 s59, s67
s_add_u32 s56, s89, 0x2000000
s_mov_b32 s94, 2
Mz 64,0,224
Rb 48,1,12288
v_lshrrev_b32 v9, 4, v0
Mz 68,0,228
v_lshlrev_b32 v9, 4, v9
v_add_lshl_u32 v9, v9, v0, 3
v_accvgpr_mov_b32 a64, a224
Mv 68,4,236
Rb 52,1,13312
s_add_u32 m0, s36, 98304
v_accvgpr_mov_b32 a65, a225
Mv 64,4,232
buffer_load_dwordx4 v2, s[20:23], s56 offen offset:0 sc1 lds
v_accvgpr_mov_b32 a66, a226
Mv 64,8,240
Rb 56,1,14336
s_add_u32 s92, s36, 131072
v_accvgpr_mov_b32 a67, a227
Mv 68,8,244
v_add_u32 v9, s92, v9
v_accvgpr_mov_b32 a68, a228
s_waitcnt vmcnt(1)
Mv 68,12,252
Rb 60,1,15360
v_accvgpr_mov_b32 a69, a229
s_waitcnt lgkmcnt(8)
)" R"(
Mv 64,12,248
buffer_load_dwordx4 v2, s[20:23], s56 offen offset:1024 sc1 lds
Rt 156,9,0
Mv 64,16,192
Rb 0,1,16384
Rt 158,9,128
v_accvgpr_mov_b32 a70, a230
Mv 68,16,196
Rt 160,9,1024
v_exp_f32_e32 v56, v112
Mv 68,20,204
Rb 4,1,17408
Rt 162,9,1152
v_add_f32_e32 v14, v14, v56
Mv 64,20,200
buffer_load_dwordx4 v2, s[20:23], s56 offen offset:2048 sc1 lds
Rt 164,9,2048
Mv 64,24,208
Rb 8,1,18432
Rt 166,9,2176
v_accvgpr_mov_b32 a71, a231
Mv 68,24,212
Rt 168,9,3072
v_exp_f32_e32 v26, v113
Mv 68,28,220
Rb 12,1,19456
Rt 170,9,3200
Mv 64,28,216
buffer_load_dwordx4 v2, s[20:23], s56 offen offset:3072 sc1 lds
Rt 172,9,4096
v_add_f32_e32 v14, v14, v26
Mz 72,32,224
Rb 16,1,24576
Rt 174,9,4224
Cv 56,56,26
Mz 76,32,228
Rt 176,9,5120
v_exp_f32_e32 v57, v114
Mv 76,36,236
Rb 20,1,25600
s_add_u32 m0, s36, 102400
Rt 178,9,5248
Mv 72,36,232
buffer_load_dwordx4 v3, s[20:23], s56 offen offset:0 sc1 lds
Rt 180,9,6144
Mv 72,40,240
Rb 24,1,26624
Rt 182,9,6272
v_add_f32_e32 v14, v14, v57
Mv 76,40,244
v_exp_f32_e32 v27, v115
v_accvgpr_mov_b32 a72, a232
Mv 76,44,252
Rb 28,1,27648
v_add_f32_e32 v14, v14, v27
Mv 72,44,248
buffer_load_dwordx4 v3, s[20:23], s56 offen offset:1024 sc1 lds
Cv 57,57,27
Mv 72,48,192
Rb 32,1,20480
v_and_b32 v9, 14, v0
v_accvgpr_mov_b32 a73, a233
Mv 76,48,196
v_min_u32 v9, 1, v9
)" R"(
v_lshlrev_b32 v9, 8, v9
v_accvgpr_mov_b32 a74, a234
Mv 76,52,204
Rb 36,1,21504
v_and_or_b32 v9, v0, 48, v9
v_accvgpr_mov_b32 a75, a235
Mv 72,52,200
buffer_load_dwordx4 v3, s[20:23], s56 offen offset:2048 sc1 lds
v_accvgpr_mov_b32 a76, a236
Mv 72,56,208
Rb 40,1,22528
v_lshlrev_b32 v9, 1, v9
v_accvgpr_mov_b32 a77, a237
Mv 76,56,212
v_and_or_b32 v9, v1, 16, v9
s_lshl_b32 s92, s33, 7
v_accvgpr_mov_b32 a78, a238
Mv 76,60,220
Rb 44,1,23552
v_accvgpr_mov_b32 a79, a239
s_waitcnt lgkmcnt(5)
Mv 72,60,216
v_mfma_f32_16x16x32_bf16 a[184:187], a[156:159], a[128:131], 0
buffer_load_dwordx4 v3, s[20:23], s56 offen offset:3072 sc1 lds
v_exp_f32_e32 v58, v120
Mz 80,0,224
Ma 184,160,132
Rb 48,1,28672
s_add_u32 s92, s92, 162816
v_add_f32_e32 v14, v14, v58
Mz 84,0,228
Ma 184,164,136
s_add_u32 s56, s56, 0x10000
v_add_u32 v9, s92, v9
v_accvgpr_mov_b32 a80, a240
Mv 84,4,236
Ma 184,168,140
Rb 52,1,29696
v_exp_f32_e32 v28, v121
Mv 80,4,232
Ma 184,172,144
v_exp_f32_e32 v59, v122
v_add_f32_e32 v14, v14, v28
Mv 80,8,240
Ma 184,176,148
Rb 56,1,30720
s_mov_b32 s24, s70
s_mov_b32 s25, s71
Mv 84,8,244
Ma 184,180,152
s_mul_i32 s83, s82, 14336
s_lshl_b32 s92, s33, 12
Cv 58,58,28
Mv 84,12,252
Rb 60,1,31744
v_add_f32_e32 v14, v14, v59
s_waitcnt lgkmcnt(8)
Mv 80,12,248
s_add_u32 s83, s83, s92
s_add_u32 m0, s92, 131072
v_accvgpr_mov_b32 a81, a241
Mv 80,16,192
v_exp_f32_e32 v29, v123
v_accvgpr_mov_b32 a82, a242
)" R"(
Mv 84,16,196
v_exp_f32_e32 v60, v116
v_add_f32_e32 v14, v14, v29
Mv 84,20,204
ds_write_b128 v9, a[184:187]
v_and_b32 v9, 0x70, v9
Cv 59,59,29
Mv 80,20,200
v_add_u32 v9, 0x27c00, v9
v_exp_f32_e32 v24, v117
Mv 80,24,208
v_exp_f32_e32 v61, v118
v_add_f32_e32 v15, v15, v60
Mv 84,24,212
v_exp_f32_e32 v25, v119
v_add_f32_e32 v15, v15, v24
Mv 84,28,220
v_exp_f32_e32 v62, v124
Cv 60,60,24
Mv 80,28,216
v_exp_f32_e32 v26, v125
s_waitcnt lgkmcnt(0)
s_barrier
Mz 88,32,224
Rb 0,8,0
v_exp_f32_e32 v63, v126
Mz 92,32,228
Rb 4,8,16384
v_exp_f32_e32 v27, v127
Mv 92,36,236
Rb 8,8,1024
v_add_f32_e32 v15, v15, v61
v_add_f32_e32 v15, v15, v25
Mv 88,36,232
Rb 12,8,17408
Cv 61,61,25
v_add_f32_e32 v15, v15, v62
Mv 88,40,240
Rb 16,8,2048
buffer_load_dwordx4 v1, s[24:27], s83 offen offset:0 lds
Mv 92,40,244
Rb 20,8,18432
v_add_f32_e32 v15, v15, v26
Cv 62,62,26
Mv 92,44,252
Rb 24,8,3072
v_add_f32_e32 v15, v15, v63
v_add_f32_e32 v15, v15, v27
Mv 88,44,248
Rb 28,8,19456
Cv 63,63,27
v_max_f32_e32 v11, v14, v15
Mv 88,48,192
Rb 32,8,4096
v_cmp_nge_f32_e32 vcc, s62, v11
s_add_u32 s55, s55, 0x10000
Mv 92,48,196
Rb 36,8,20480
buffer_load_dwordx4 v1, s[24:27], s83 offen offset:1024 lds
Mv 92,52,204
Rb 40,8,5120
s_cmp_eq_u32 s57, 2
s_cselect_b32 s55, s59, s55
Mv 88,52,200
Rb 44,8,21504
)" R"(
v_accvgpr_mov_b32 a83, a243
v_accvgpr_mov_b32 a84, a244
Mv 88,56,208
buffer_load_dwordx4 v1, s[24:27], s83 offen offset:2048 lds
v_accvgpr_mov_b32 a85, a245
Mv 92,56,212
buffer_load_dwordx4 v1, s[24:27], s83 offen offset:3072 lds
v_accvgpr_mov_b32 a86, a246
Mv 92,60,220
v_accvgpr_mov_b32 a87, a247
s_waitcnt lgkmcnt(8)
s_cbranch_vccnz .Lzfx3
.Lzfr3:
Mv 88,60,216
v_add_f32_e32 v12, v12, v14
v_add_f32_e32 v13, v13, v15
v_mov_b32 v14, v16
Mp 128,0,48
Rb 48,8,6144
v_mov_b32 v15, v20
Mp 136,0,52
buffer_store_dwordx4 v[12:15], v1, s[20:23], s66 offen sc0 sc1
s_cmp_le_i32 s57, s58
s_cbranch_scc1 .Lzmk4
.Lzmr4:
Mp 136,4,60
Rb 52,8,22528
s_add_u32 m0, s36, 0
v_accvgpr_mov_b32 a88, a248
Mp 128,4,56
buffer_load_dwordx4 v2, s[20:23], s55 offen offset:0 sc1 lds
v_accvgpr_mov_b32 a89, a249
Mp 132,8,48
Rb 56,8,7168
v_exp_f32_e32 v32, v64
Mp 140,8,52
v_exp_f32_e32 v28, v65
v_accvgpr_mov_b32 a90, a250
Mp 140,12,60
Rb 60,8,23552
v_exp_f32_e32 v33, v66
s_waitcnt lgkmcnt(8)
Mp 132,12,56
v_exp_f32_e32 v29, v67
v_add_f32_e32 v14, v32, v28
Mp 144,16,48
Rb 0,8,8192
v_exp_f32_e32 v34, v72
Mp 152,16,52
v_exp_f32_e32 v24, v73
v_exp_f32_e32 v35, v74
Mp 152,20,60
Rb 4,8,24576
v_exp_f32_e32 v25, v75
Mp 144,20,56
buffer_load_dwordx4 v2, s[20:23], s55 offen offset:1024 sc1 lds
Cv 32,32,28
Mp 148,24,48
Rb 8,8,9216
Cv 128,128,129
Cv 129,130,131
v_add_f32_e32 v14, v14, v33
Mp 156,24,52
Cv 130,132,133
)" R"(
Cv 131,134,135
v_add_f32_e32 v14, v14, v29
Mp 156,28,60
Rb 12,8,25600
Cv 136,136,137
s_waitcnt lgkmcnt(8)
Mp 148,28,56
Cv 137,138,139
Cv 138,140,141
Cv 139,142,143
Cv 33,33,29
Mp 160,32,48
Rb 16,8,10240
v_exp_f32_e32 v36, v68
Mp 168,32,52
v_exp_f32_e32 v26, v69
v_add_f32_e32 v14, v14, v34
Mp 168,36,60
Rb 20,8,26624
v_exp_f32_e32 v37, v70
v_add_f32_e32 v14, v14, v24
Mp 160,36,56
buffer_load_dwordx4 v2, s[20:23], s55 offen offset:2048 sc1 lds
Cv 144,144,145
Mp 164,40,48
Rb 24,8,11264
Cv 145,146,147
Cv 34,34,24
Mp 172,40,52
buffer_store_dwordx4 v[128:131], v2, s[20:23], s64 offen offset:0 sc0 sc1
Cv 146,148,149
Mp 172,44,60
Rb 28,8,27648
v_exp_f32_e32 v27, v71
s_waitcnt lgkmcnt(8)
Mp 164,44,56
Cv 147,150,151
Cv 152,152,153
Cv 153,154,155
Mp 176,48,48
Rb 32,8,12288
Cv 154,156,157
Cv 155,158,159
Mp 184,48,52
v_exp_f32_e32 v38, v76
v_exp_f32_e32 v28, v77
Mp 184,52,60
Rb 36,8,28672
v_exp_f32_e32 v39, v78
Mp 176,52,56
buffer_load_dwordx4 v2, s[20:23], s55 offen offset:3072 sc1 lds
v_add_f32_e32 v14, v14, v35
Mp 180,56,48
Rb 40,8,13312
Cv 160,160,161
Cv 161,162,163
v_add_f32_e32 v14, v14, v25
Mp 188,56,52
buffer_store_dwordx4 v[136:139], v2, s[20:23], s64 offen offset:1024 sc0 sc1
Cv 35,35,25
Mp 188,60,60
Rb 44,8,29696
Cv 162,164,165
s_waitcnt lgkmcnt(8)
)" R"(
Mp 180,60,56
Cv 163,166,167
Cv 168,168,169
Cv 169,170,171
v_add_f32_e32 v15, v36, v26
Mp 192,0,48
Rb 48,8,14336
Cv 170,172,173
Cv 36,36,26
Mp 200,0,52
buffer_store_dwordx4 v[144:147], v2, s[20:23], s64 offen offset:2048 sc0 sc1
Cv 171,174,175
Mp 200,4,60
Rb 52,8,30720
s_add_u32 m0, s36, 4096
v_exp_f32_e32 v29, v79
Mp 192,4,56
buffer_load_dwordx4 v3, s[20:23], s55 offen offset:0 sc1 lds
Cv 176,176,177
Mp 196,8,48
Rb 56,8,15360
Cv 177,178,179
v_add_f32_e32 v15, v15, v37
Mp 204,8,52
Cv 178,180,181
Cv 179,182,183
Cv 184,184,185
Mp 204,12,60
Rb 60,8,31744
v_add_f32_e32 v15, v15, v27
Cv 37,37,27
s_waitcnt lgkmcnt(8)
Mp 196,12,56
buffer_store_dwordx4 v[152:155], v2, s[20:23], s64 offen offset:3072 sc0 sc1
Cv 185,186,187
Mp 208,16,48
ds_read_b128 v[132:135], v9 offset:0
Cv 186,188,189
Cv 187,190,191
Mp 216,16,52
ds_read_b128 v[140:143], v9 offset:128
v_add_f32_e32 v15, v15, v38
v_add_f32_e32 v15, v15, v28
Cv 38,38,28
Mp 216,20,60
ds_read_b128 v[148:151], v9 offset:256
v_add_f32_e32 v15, v15, v39
v_add_f32_e32 v15, v15, v29
Mp 208,20,56
buffer_load_dwordx4 v3, s[20:23], s55 offen offset:1024 sc1 lds
ds_read_b128 v[156:159], v9 offset:384
Mp 212,24,48
s_add_u32 s50, s50, 1024
s_min_u32 s50, s50, s60
Cv 192,192,193
Cv 39,39,29
Mp 220,24,52
Cv 193,194,195
Cv 194,196,197
s_sub_u32 s57, s57, 1
Mp 220,28,60
buffer_store_dwordx4 v[160:163], v3, s[20:23], s64 offen offset:0 sc0 sc1
Cv 195,198,199
Mp 212,28,56
)" R"(
Cv 200,200,201
v_mov_b32 v16, 0
s_waitcnt lgkmcnt(0)
s_barrier
Mp 224,32,48
Rb 0,1,32768
v_add_f32_e32 v132, v132, v140
v_mov_b32 v17, 0
Mp 232,32,52
Rb 4,1,33792
v_add_f32_e32 v133, v133, v141
Cv 201,202,203
Mp 232,36,60
Rb 8,1,34816
v_add_f32_e32 v134, v134, v142
v_mov_b32 v18, 0
v_mov_b32 v19, 0
Mp 224,36,56
Rb 12,1,35840
buffer_load_dwordx4 v3, s[20:23], s55 offen offset:2048 sc1 lds
Mp 228,40,48
Rb 16,1,40960
v_add_f32_e32 v135, v135, v143
buffer_store_dwordx4 v[168:171], v3, s[20:23], s64 offen offset:1024 sc0 sc1
Mp 236,40,52
Rb 20,1,41984
v_add_f32_e32 v148, v148, v156
Cv 202,204,205
Mp 236,44,60
Rb 24,1,43008
v_add_f32_e32 v149, v149, v157
v_mov_b32 v20, 0
v_mov_b32 v21, 0
Mp 228,44,56
Rb 28,1,44032
v_add_f32_e32 v150, v150, v158
Cv 203,206,207
Mp 240,48,48
Rb 32,1,36864
v_add_f32_e32 v151, v151, v159
Cv 208,208,209
Mp 248,48,52
Rb 36,1,37888
Cv 132,132,132
v_mov_b32 v22, 0
v_mov_b32 v23, 0
Mp 248,52,60
Rb 40,1,38912
v_lshlrev_b32 v132, 16, v132
buffer_store_dwordx4 v[176:179], v3, s[20:23], s64 offen offset:2048 sc0 sc1
Mp 240,52,56
Rb 44,1,39936
buffer_load_dwordx4 v3, s[20:23], s55 offen offset:3072 sc1 lds
Mp 244,56,48
Cv 148,148,148
v_lshlrev_b32 v148, 16, v148
Cv 209,210,211
Mp 252,56,52
Cv 133,133,133
v_lshlrev_b32 v133, 16, v133
v_accvgpr_mov_b32 a91, a251
Mp 252,60,60
Cv 149,149,149
Cv 210,212,213
s_waitcnt lgkmcnt(8)
)" R"(
Mp 244,60,56
v_lshlrev_b32 v149, 16, v149
Cv 134,134,134
v_lshlrev_b32 v134, 16, v134
Mz 96,0,224
Rb 48,1,45056
buffer_load_dword v4, v5, s[20:23], s50 offen offset:0 sc0 sc1
buffer_store_dwordx4 v[184:187], v3, s[20:23], s64 offen offset:3072 sc0 sc1
Mz 100,0,228
Cv 150,150,150
v_lshlrev_b32 v150, 16, v150
Cv 211,214,215
Mv 100,4,236
Rb 52,1,46080
s_add_u32 m0, s36, 65536
v_exp_f32_e32 v40, v80
Mv 96,4,232
buffer_load_dwordx4 v2, s[20:23], s56 offen offset:0 sc1 lds
Cv 216,216,217
v_add_f32_e32 v14, v14, v40
Mv 96,8,240
Rb 56,1,47104
Cv 135,135,135
v_accvgpr_mov_b32 a92, a252
Mv 100,8,244
v_lshlrev_b32 v135, 16, v135
Cv 151,151,151
Cv 217,218,219
Mv 100,12,252
Rb 60,1,48128
s_add_u32 s64, s64, 0x10000
buffer_store_dwordx4 v[192:195], v2, s[20:23], s64 offen offset:0 sc0 sc1
s_waitcnt lgkmcnt(8)
Mv 96,12,248
v_lshlrev_b32 v151, 16, v151
v_mul_f32_e32 v140, 0xbfb8aa3b, v132
Cv 218,220,221
Mv 96,16,192
Rb 0,1,49152
v_mul_f32_e32 v141, 0xbfb8aa3b, v133
Cv 219,222,223
Mv 100,16,196
v_exp_f32_e32 v140, v140
v_exp_f32_e32 v24, v81
Mv 100,20,204
Rb 4,1,50176
v_mul_f32_e32 v142, 0xbfb8aa3b, v134
Cv 224,224,225
v_add_f32_e32 v14, v14, v24
Mv 96,20,200
buffer_load_dwordx4 v2, s[20:23], s56 offen offset:1024 sc1 lds
v_exp_f32_e32 v41, v82
Mv 96,24,208
Rb 8,1,51200
v_mul_f32_e32 v143, 0xbfb8aa3b, v135
buffer_store_dwordx4 v[200:203], v2, s[20:23], s64 offen offset:1024 sc0 sc1
Mv 100,24,212
v_exp_f32_e32 v141, v141
v_exp_f32_e32 v25, v83
Mv 100,28,220
Rb 12,1,52224
Cv 225,226,227
Cv 40,40,24
s_waitcnt lgkmcnt(8)
)" R"(
Mv 96,28,216
v_exp_f32_e32 v142, v142
v_add_f32_e32 v140, 1.0, v140
v_add_f32_e32 v14, v14, v41
Mz 104,32,224
Rb 16,1,57344
v_add_f32_e32 v141, 1.0, v141
v_add_f32_e32 v14, v14, v25
Mz 108,32,228
v_exp_f32_e32 v143, v143
Cv 226,228,229
Cv 41,41,25
Mv 108,36,236
Rb 20,1,58368
v_add_f32_e32 v142, 1.0, v142
buffer_store_dwordx4 v[208:211], v2, s[20:23], s64 offen offset:2048 sc0 sc1
Mv 104,36,232
buffer_load_dwordx4 v2, s[20:23], s56 offen offset:2048 sc1 lds
Cv 227,230,231
Mv 104,40,240
Rb 24,1,59392
v_add_f32_e32 v143, 1.0, v143
v_accvgpr_mov_b32 a93, a253
Mv 108,40,244
v_rcp_f32_e32 v140, v140
Cv 232,232,233
Mv 108,44,252
Rb 28,1,60416
v_exp_f32_e32 v42, v88
s_waitcnt lgkmcnt(8)
Mv 104,44,248
v_rcp_f32_e32 v141, v141
Cv 233,234,235
v_add_f32_e32 v14, v14, v42
Mv 104,48,192
Rb 32,1,53248
v_mul_f32_e32 v132, v132, v140
buffer_store_dwordx4 v[216:219], v2, s[20:23], s64 offen offset:3072 sc0 sc1
Mv 108,48,196
v_rcp_f32_e32 v142, v142
v_exp_f32_e32 v26, v89
Mv 108,52,204
Rb 36,1,54272
v_mul_f32_e32 v133, v133, v141
Cv 234,236,237
v_add_f32_e32 v14, v14, v26
Mv 104,52,200
buffer_load_dwordx4 v2, s[20:23], s56 offen offset:3072 sc1 lds
v_exp_f32_e32 v43, v90
Mv 104,56,208
Rb 40,1,55296
v_mul_f32_e32 v134, v134, v142
Cv 235,238,239
Mv 108,56,212
v_rcp_f32_e32 v143, v143
v_exp_f32_e32 v27, v91
Mv 108,60,220
Rb 44,1,56320
buffer_store_dwordx4 v[224:227], v3, s[20:23], s64 offen offset:0 sc0 sc1
s_waitcnt lgkmcnt(8)
Mv 104,60,216
v_mul_f32_e32 v135, v135, v143
Cv 132,132,132
v_lshlrev_b32 v132, 16, v132
)" R"(
Cv 42,42,26
Mz 112,0,224
Rb 48,1,61440
Cv 133,133,133
v_add_f32_e32 v14, v14, v43
Mz 116,0,228
v_lshlrev_b32 v133, 16, v133
Cv 134,134,134
Cv 240,240,241
v_add_f32_e32 v14, v14, v27
Mv 116,4,236
Rb 52,1,62464
s_add_u32 m0, s36, 69632
v_exp_f32_e32 v44, v84
Mv 112,4,232
buffer_load_dwordx4 v3, s[20:23], s56 offen offset:0 sc1 lds
Cv 241,242,243
Cv 43,43,27
Mv 112,8,240
Rb 56,1,63488
v_lshlrev_b32 v134, 16, v134
buffer_store_dwordx4 v[232:235], v3, s[20:23], s64 offen offset:1024 sc0 sc1
Mv 116,8,244
Cv 135,135,135
v_lshlrev_b32 v135, 16, v135
Cv 242,244,245
v_add_f32_e32 v15, v15, v44
Mv 116,12,252
Rb 60,1,64512
v_exp_f32_e32 v28, v85
s_waitcnt lgkmcnt(8)
Mv 112,12,248
v_mul_f32_e32 v132, v132, v148
v_mul_f32_e32 v133, v133, v149
Cv 243,246,247
v_add_f32_e32 v15, v15, v28
Mv 112,16,192
v_mul_f32_e32 v134, v134, v150
v_mul_f32_e32 v135, v135, v151
Cv 248,248,249
Mv 116,16,196
Cv 140,132,133
Cv 141,134,135
v_exp_f32_e32 v45, v86
Mv 116,20,204
v_accvgpr_write_b32 a188, v140
v_accvgpr_write_b32 a189, v141
buffer_store_dwordx4 v[240:243], v3, s[20:23], s64 offen offset:2048 sc0 sc1
Mv 112,20,200
buffer_load_dwordx4 v3, s[20:23], s56 offen offset:1024 sc1 lds
v_exp_f32_e32 v29, v87
Mv 112,24,208
v_lshrrev_b32 v156, 2, v0
v_mul_u32_u24 v156, 864, v156
Cv 249,250,251
Mv 116,24,212
v_lshl_add_u32 v156, v0, 3, v156
s_mul_i32 s92, s33, 224
v_exp_f32_e32 v46, v92
Mv 116,28,220
s_add_u32 s92, s92, 131072
v_add_u32 v156, s92, v156
Cv 250,252,253
Cv 44,44,28
)" R"(
Mv 112,28,216
Cv 251,254,255
v_add_f32_e32 v15, v15, v45
s_waitcnt vmcnt(31) lgkmcnt(0)
s_barrier
Mz 120,32,224
Rb 0,8,32768
Rt 156,156,0
buffer_store_dwordx4 v[248:251], v3, s[20:23], s64 offen offset:3072 sc0 sc1
Mz 124,32,228
Rb 4,8,33792
Rt 158,156,32
v_exp_f32_e32 v24, v93
Mv 124,36,236
Rb 8,8,34816
Rt 160,156,64
v_exp_f32_e32 v47, v94
Mv 120,36,232
Rb 12,8,35840
buffer_load_dwordx4 v3, s[20:23], s56 offen offset:2048 sc1 lds
Rt 162,156,96
Mv 120,40,240
Rb 16,8,36864
Rt 164,156,128
v_add_f32_e32 v15, v15, v29
Mv 124,40,244
Rb 20,8,37888
Rt 166,156,160
v_exp_f32_e32 v25, v95
Mv 124,44,252
Rb 24,8,38912
Rt 168,156,192
Cv 45,45,29
v_add_f32_e32 v15, v15, v46
Mv 120,44,248
Rb 28,8,39936
v_add_f32_e32 v15, v15, v24
Cv 46,46,24
v_add_f32_e32 v15, v15, v47
Mv 120,48,192
Rb 32,8,40960
v_add_f32_e32 v15, v15, v25
Cv 47,47,25
Mv 124,48,196
Rb 36,8,41984
v_max_f32_e32 v11, v14, v15
v_mov_b32 v24, v14
v_mov_b32 v25, v15
Mv 124,52,204
Rb 40,8,43008
v_permlane32_swap_b32_e32 v24, v25
v_add_f32_e32 v24, v24, v25
v_mov_b32 v25, v24
Mv 120,52,200
Rb 44,8,44032
buffer_load_dwordx4 v3, s[20:23], s56 offen offset:3072 sc1 lds
v_permlane16_swap_b32_e32 v24, v25
Mv 120,56,208
v_add_f32_e32 v24, v24, v25
v_rcp_f32_e32 v24, v24
Mv 124,56,212
s_add_u32 s56, s56, 0x10000
v_max_f32_e32 v11, v11, v24
v_cmp_nge_f32_e32 vcc, s62, v11
s_add_u32 s55, s55, 0x10000
)" R"(
Mv 124,60,220
s_cmp_eq_u32 s57, 2
s_cselect_b32 s55, s59, s55
s_waitcnt lgkmcnt(12)
s_cbranch_vccnz .Lzfx4
.Lzfr4:
Mv 120,60,216
v_mov_b32 v12, v14
v_mov_b32 v13, v15
v_accvgpr_mov_b32 a94, a254
v_mfma_f32_16x16x32_bf16 v[128:131], a[0:3], v[32:35], 0
Rb 48,8,45056
v_accvgpr_mov_b32 a95, a255
v_mfma_f32_16x16x32_bf16 v[136:139], a[0:3], v[36:39], 0
v_accvgpr_mov_b32 a96, a192
s_cmp_le_i32 s57, s58
s_cbranch_scc1 .Lzmk5
.Lzmr5:
v_mfma_f32_16x16x32_bf16 v[140:143], a[4:7], v[36:39], 0
Rb 52,8,46080
s_add_u32 m0, s36, 32768
v_accvgpr_mov_b32 a97, a193
v_mfma_f32_16x16x32_bf16 v[132:135], a[4:7], v[32:35], 0
buffer_load_dwordx4 v2, s[20:23], s55 offen offset:0 sc1 lds
v_accvgpr_mov_b32 a98, a194
v_mfma_f32_16x16x32_bf16 v[144:147], a[8:11], v[32:35], 0
Rb 56,8,47104
v_and_b32 v11, 14, v0
v_accvgpr_mov_b32 a99, a195
v_mfma_f32_16x16x32_bf16 v[152:155], a[8:11], v[36:39], 0
v_min_u32 v11, 1, v11
v_lshlrev_b32 v11, 13, v11
v_accvgpr_mov_b32 a100, a196
v_mfma_f32_16x16x32_bf16 v[156:159], a[12:15], v[36:39], 0
Rb 60,8,48128
v_accvgpr_mov_b32 a101, a197
s_waitcnt lgkmcnt(8)
v_mfma_f32_16x16x32_bf16 v[148:151], a[12:15], v[32:35], 0
v_mfma_f32_16x16x16_bf16 a[128:131], a[156:157], a[188:189], 0
v_and_or_b32 v11, v0, 1, v11
v_mul_u32_u24 v11, 0x31000, v11
v_accvgpr_mov_b32 a102, a198
v_mfma_f32_16x16x32_bf16 v[160:163], a[16:19], v[32:35], 0
v_mfma_f32_16x16x16_bf16 a[132:135], a[158:159], a[188:189], 0
Rb 0,8,49152
v_and_or_b32 v11, v0, 48, v11
v_accvgpr_mov_b32 a103, a199
v_mfma_f32_16x16x32_bf16 v[168:171], a[16:19], v[36:39], 0
v_mfma_f32_16x16x16_bf16 a[136:139], a[160:161], a[188:189], 0
s_mul_i32 s83, s33, 50176
s_lshl_b32 s92, s82, 6
v_accvgpr_mov_b32 a104, a200
v_mfma_f32_16x16x32_bf16 v[172:175], a[20:23], v[36:39], 0
v_mfma_f32_16x16x16_bf16 a[140:143], a[162:163], a[188:189], 0
Rb 4,8,50176
s_add_u32 s83, s83, s92
v_accvgpr_mov_b32 a105, a201
v_mfma_f32_16x16x32_bf16 v[164:167], a[20:23], v[32:35], 0
v_mfma_f32_16x16x16_bf16 a[144:147], a[164:165], a[188:189], 0
buffer_load_dwordx4 v2, s[20:23], s55 offen offset:1024 sc1 lds
v_accvgpr_mov_b32 a106, a202
v_mfma_f32_16x16x32_bf16 v[176:179], a[24:27], v[32:35], 0
v_mfma_f32_16x16x16_bf16 a[148:151], a[166:167], a[188:189], 0
Rb 8,8,51200
)" R"(
s_add_u32 s83, s83, 0xc158000
v_accvgpr_mov_b32 a107, a203
v_mfma_f32_16x16x32_bf16 v[184:187], a[24:27], v[36:39], 0
v_mfma_f32_16x16x16_bf16 a[152:155], a[168:169], a[188:189], 0
v_exp_f32_e32 v48, v96
v_accvgpr_mov_b32 a108, a204
v_mfma_f32_16x16x32_bf16 v[188:191], a[28:31], v[36:39], 0
Rb 12,8,52224
v_accvgpr_mov_b32 a109, a205
s_waitcnt lgkmcnt(8)
v_mfma_f32_16x16x32_bf16 v[180:183], a[28:31], v[32:35], 0
v_exp_f32_e32 v26, v97
v_accvgpr_mov_b32 a110, a206
v_mfma_f32_16x16x32_bf16 v[192:195], a[32:35], v[32:35], 0
Rb 16,8,53248
v_add_f32_e32 v14, v48, v26
v_mfma_f32_16x16x32_bf16 v[200:203], a[32:35], v[36:39], 0
v_exp_f32_e32 v49, v98
Cv 48,48,26
v_mfma_f32_16x16x32_bf16 v[204:207], a[36:39], v[36:39], 0
Rb 20,8,54272
v_add_f32_e32 v14, v14, v49
v_accvgpr_mov_b32 a111, a207
v_mfma_f32_16x16x32_bf16 v[196:199], a[36:39], v[32:35], 0
buffer_load_dwordx4 v2, s[20:23], s55 offen offset:2048 sc1 lds
v_accvgpr_mov_b32 a112, a208
v_mfma_f32_16x16x32_bf16 v[208:211], a[40:43], v[32:35], 0
Rb 24,8,55296
v_exp_f32_e32 v27, v99
v_mfma_f32_16x16x32_bf16 v[216:219], a[40:43], v[36:39], 0
buffer_store_dwordx4 a[128:131], v11, s[20:23], s83 offen sc0 sc1
v_accvgpr_mov_b32 a113, a209
v_mfma_f32_16x16x32_bf16 v[220:223], a[44:47], v[36:39], 0
Rb 28,8,56320
v_accvgpr_mov_b32 a114, a210
s_waitcnt lgkmcnt(8)
v_mfma_f32_16x16x32_bf16 v[212:215], a[44:47], v[32:35], 0
s_add_u32 s83, s83, 7168
v_exp_f32_e32 v50, v104
v_mfma_f32_16x16x32_bf16 v[224:227], a[48:51], v[32:35], 0
Rb 32,8,57344
v_add_f32_e32 v14, v14, v27
v_accvgpr_mov_b32 a115, a211
v_mfma_f32_16x16x32_bf16 v[232:235], a[48:51], v[36:39], 0
v_exp_f32_e32 v28, v105
Cv 49,49,27
v_mfma_f32_16x16x32_bf16 v[236:239], a[52:55], v[36:39], 0
Rb 36,8,58368
v_add_f32_e32 v14, v14, v50
v_accvgpr_mov_b32 a116, a212
v_mfma_f32_16x16x32_bf16 v[228:231], a[52:55], v[32:35], 0
buffer_load_dwordx4 v2, s[20:23], s55 offen offset:3072 sc1 lds
v_accvgpr_mov_b32 a117, a213
v_mfma_f32_16x16x32_bf16 v[240:243], a[56:59], v[32:35], 0
Rb 40,8,59392
v_exp_f32_e32 v51, v106
v_mfma_f32_16x16x32_bf16 v[248:251], a[56:59], v[36:39], 0
buffer_store_dwordx4 a[132:135], v11, s[20:23], s83 offen sc0 sc1
v_accvgpr_mov_b32 a118, a214
v_mfma_f32_16x16x32_bf16 v[252:255], a[60:63], v[36:39], 0
Rb 44,8,60416
v_accvgpr_mov_b32 a119, a215
s_waitcnt lgkmcnt(8)
v_mfma_f32_16x16x32_bf16 v[244:247], a[60:63], v[32:35], 0
)" R"(
s_add_u32 s83, s83, 7168
v_exp_f32_e32 v29, v107
Mp 128,0,40
Rb 48,8,61440
v_add_f32_e32 v14, v14, v28
Mp 136,0,44
buffer_store_dwordx4 a[136:139], v11, s[20:23], s83 offen sc0 sc1
s_add_u32 s83, s83, 7168
Mp 140,4,44
Rb 52,8,62464
s_add_u32 m0, s36, 36864
v_accvgpr_mov_b32 a120, a216
Mp 132,4,40
buffer_load_dwordx4 v3, s[20:23], s55 offen offset:0 sc1 lds
v_accvgpr_mov_b32 a121, a217
Mp 144,8,40
Rb 56,8,63488
v_exp_f32_e32 v52, v100
Mp 152,8,44
v_exp_f32_e32 v24, v101
v_accvgpr_mov_b32 a122, a218
Mp 156,12,44
Rb 60,8,64512
v_accvgpr_mov_b32 a123, a219
s_waitcnt lgkmcnt(8)
Mp 148,12,40
buffer_store_dwordx4 a[140:143], v11, s[20:23], s83 offen sc0 sc1
Cv 50,50,28
Mp 160,16,40
s_add_u32 s83, s83, 7168
v_add_f32_e32 v14, v14, v51
v_accvgpr_mov_b32 a124, a220
Mp 168,16,44
v_exp_f32_e32 v53, v102
v_add_f32_e32 v14, v14, v29
Mp 172,20,44
v_exp_f32_e32 v25, v103
v_accvgpr_mov_b32 a125, a221
Mp 164,20,40
buffer_load_dwordx4 v3, s[20:23], s55 offen offset:1024 sc1 lds
v_accvgpr_mov_b32 a126, a222
Mp 176,24,40
v_exp_f32_e32 v54, v108
Cv 51,51,29
Mp 184,24,44
buffer_store_dwordx4 a[144:147], v11, s[20:23], s83 offen sc0 sc1
v_accvgpr_mov_b32 a127, a223
Mp 188,28,44
s_add_u32 s83, s83, 7168
v_add_f32_e32 v15, v52, v24
Mp 180,28,40
Cv 52,52,24
s_waitcnt vmcnt(13) lgkmcnt(0)
s_barrier
buffer_store_dword v6, v7, s[20:23], s65 offen offset:0 sc0 sc1
Mp 192,32,40
Rb 0,1,0
v_add_f32_e32 v15, v15, v53
Mp 200,32,44
Rb 4,1,1024
v_exp_f32_e32 v26, v109
Mp 204,36,44
Rb 8,1,2048
v_add_f32_e32 v15, v15, v25
Mp 196,36,40
)" R"(
Rb 12,1,3072
buffer_load_dwordx4 v3, s[20:23], s55 offen offset:2048 sc1 lds
Mp 208,40,40
Rb 16,1,8192
v_exp_f32_e32 v55, v110
Mp 216,40,44
Rb 20,1,9216
Cv 53,53,25
Mp 220,44,44
Rb 24,1,10240
buffer_store_dwordx4 a[148:151], v11, s[20:23], s83 offen sc0 sc1
Mp 212,44,40
Rb 28,1,11264
s_add_u32 s83, s83, 7168
v_add_f32_e32 v15, v15, v54
Mp 224,48,40
Rb 32,1,4096
v_add_f32_e32 v15, v15, v26
Mp 232,48,44
Rb 36,1,5120
v_exp_f32_e32 v27, v111
Mp 236,52,44
Rb 40,1,6144
Cv 54,54,26
Mp 228,52,40
Rb 44,1,7168
buffer_load_dwordx4 v3, s[20:23], s55 offen offset:3072 sc1 lds
Mp 240,56,40
v_cmp_eq_u32_e32 vcc, s3, v4
s_andn2_b64 s[28:29], -1, vcc
s_cbranch_scc1 .Lzsp10
.Lzok10:
Mp 248,56,44
buffer_store_dwordx4 a[152:155], v11, s[20:23], s83 offen sc0 sc1
Mp 252,60,44
v_add_f32_e32 v15, v15, v55
s_waitcnt lgkmcnt(8)
Mp 244,60,40
v_add_f32_e32 v15, v15, v27
Cv 55,55,27
s_sub_u32 s57, s57, 1
s_cmp_gt_i32 s57, s93
s_cbranch_scc1 .LzL
.LzDA:
s_waitcnt vmcnt(8) lgkmcnt(0)
s_barrier
Rb 0,8,0
Rb 4,8,16384
Rb 8,8,1024
Rb 12,8,17408
Rb 16,8,2048
Rb 20,8,18432
Rb 24,8,3072
Rb 28,8,19456
Rb 32,8,4096
Rb 36,8,20480
Rb 40,8,5120
Rb 44,8,21504
v_exp_f32_e32 v56, v112
v_exp_f32_e32 v28, v113
v_add_f32_e32 v14, v14, v56
v_add_f32_e32 v14, v14, v28
Cv 56,56,28
v_exp_f32_e32 v57, v114
v_exp_f32_e32 v29, v115
)" R"(
v_add_f32_e32 v14, v14, v57
v_add_f32_e32 v14, v14, v29
Cv 57,57,29
v_exp_f32_e32 v58, v120
v_exp_f32_e32 v24, v121
v_add_f32_e32 v14, v14, v58
v_add_f32_e32 v14, v14, v24
Cv 58,58,24
v_exp_f32_e32 v59, v122
v_exp_f32_e32 v25, v123
v_add_f32_e32 v14, v14, v59
v_add_f32_e32 v14, v14, v25
Cv 59,59,25
v_exp_f32_e32 v60, v116
v_exp_f32_e32 v26, v117
v_add_f32_e32 v15, v15, v60
v_add_f32_e32 v15, v15, v26
Cv 60,60,26
v_exp_f32_e32 v61, v118
v_exp_f32_e32 v27, v119
v_add_f32_e32 v15, v15, v61
v_add_f32_e32 v15, v15, v27
Cv 61,61,27
v_exp_f32_e32 v62, v124
v_exp_f32_e32 v28, v125
v_add_f32_e32 v15, v15, v62
v_add_f32_e32 v15, v15, v28
Cv 62,62,28
v_exp_f32_e32 v63, v126
v_exp_f32_e32 v29, v127
v_add_f32_e32 v15, v15, v63
v_add_f32_e32 v15, v15, v29
Cv 63,63,29
v_max_f32_e32 v11, v14, v15
v_cmp_nge_f32_e32 vcc, s62, v11
s_cbranch_vccnz .Lzfx5
.Lzfr5:
s_waitcnt lgkmcnt(8)
Mp 128,0,48
Rb 48,8,6144
s_add_u32 m0, s36, 131072
s_add_u32 s40, s67, 0x20000
s_lshl_b32 s82, s82, 7
Mp 136,0,52
buffer_load_dwordx4 v2, s[20:23], s40 offen offset:0 sc1 lds
v_add_f32_e32 v12, v12, v14
s_add_u32 s82, s82, 0xc10c000
Mp 136,4,60
Rb 52,8,22528
v_add_f32_e32 v13, v13, v15
v_mov_b32 v24, v12
Mp 128,4,56
buffer_load_dwordx4 v2, s[20:23], s40 offen offset:1024 sc1 lds
v_permlane32_swap_b32_e32 v12, v24
Mp 132,8,48
Rb 56,8,7168
v_add_f32_e32 v12, v12, v24
v_mov_b32 v24, v12
Mp 140,8,52
s_nop 0
v_permlane16_swap_b32_e32 v12, v24
v_add_f32_e32 v12, v12, v24
v_mov_b32 v25, v13
Mp 140,12,60
)" R"(
Rb 60,8,23552
v_permlane32_swap_b32_e32 v13, v25
s_waitcnt lgkmcnt(8)
Mp 132,12,56
buffer_load_dwordx4 v2, s[20:23], s40 offen offset:2048 sc1 lds
v_add_f32_e32 v13, v13, v25
Mp 144,16,48
Rb 0,8,8192
v_rcp_f32_e32 v12, v12
Mp 152,16,52
buffer_load_dwordx4 v2, s[20:23], s40 offen offset:3072 sc1 lds
v_mov_b32 v25, v13
Mp 152,20,60
Rb 4,8,24576
v_permlane16_swap_b32_e32 v13, v25
v_add_f32_e32 v13, v13, v25
Mp 144,20,56
v_rcp_f32_e32 v13, v13
v_mul_f32_e32 v128, v12, v128
Mp 148,24,48
Rb 8,8,9216
s_add_u32 m0, s36, 135168
v_mul_f32_e32 v129, v12, v129
Mp 156,24,52
buffer_load_dwordx4 v3, s[20:23], s40 offen offset:0 sc1 lds
Cv 128,128,129
Mp 156,28,60
Rb 12,8,25600
v_mul_f32_e32 v130, v12, v130
s_waitcnt lgkmcnt(8)
Mp 148,28,56
buffer_load_dwordx4 v3, s[20:23], s40 offen offset:1024 sc1 lds
v_mul_f32_e32 v131, v12, v131
Mp 160,32,48
Rb 16,8,10240
Cv 129,130,131
s_cmp_lg_u32 s33, 0
s_cmov_b32 s82, 0x40000000
Mp 168,32,52
v_mul_f32_e32 v132, v12, v132
v_mul_f32_e32 v133, v12, v133
Cv 130,132,133
Mp 168,36,60
Rb 20,8,26624
v_mul_f32_e32 v134, v12, v134
v_mul_f32_e32 v135, v12, v135
Mp 160,36,56
buffer_load_dwordx4 v3, s[20:23], s40 offen offset:2048 sc1 lds
Cv 131,134,135
Mp 164,40,48
Rb 24,8,11264
s_add_u32 m0, s36, 98304
s_add_u32 s40, s67, 0x30000
Mp 172,40,52
buffer_load_dwordx4 v2, s[20:23], s40 offen offset:0 sc1 lds
v_mul_f32_e32 v136, v13, v136
Mp 172,44,60
Rb 28,8,27648
v_mul_f32_e32 v137, v13, v137
s_waitcnt lgkmcnt(8)
Mp 164,44,56
Cv 136,136,137
v_mul_f32_e32 v138, v13, v138
v_mul_f32_e32 v139, v13, v139
)" R"(
Mp 176,48,48
Rb 32,8,12288
Cv 137,138,139
v_mul_f32_e32 v140, v13, v140
Mp 184,48,52
buffer_load_dwordx4 v2, s[20:23], s40 offen offset:1024 sc1 lds
v_mul_f32_e32 v141, v13, v141
Mp 184,52,60
Rb 36,8,28672
Cv 138,140,141
v_mul_f32_e32 v142, v13, v142
Mp 176,52,56
buffer_load_dwordx4 v2, s[20:23], s40 offen offset:2048 sc1 lds
v_mul_f32_e32 v143, v13, v143
Mp 180,56,48
Rb 40,8,13312
Cv 139,142,143
v_mul_f32_e32 v144, v12, v144
Mp 188,56,52
v_mul_f32_e32 v145, v12, v145
Cv 144,144,145
v_mul_f32_e32 v146, v12, v146
Mp 188,60,60
Rb 44,8,29696
v_mul_f32_e32 v147, v12, v147
s_waitcnt lgkmcnt(8)
Mp 180,60,56
buffer_load_dwordx4 v2, s[20:23], s40 offen offset:3072 sc1 lds
Cv 145,146,147
Mp 192,0,48
Rb 48,8,14336
s_add_u32 m0, s36, 102400
s_cmp_eq_u32 s35, 7
s_cmov_b32 s82, 0x40000000
Mp 200,0,52
buffer_load_dwordx4 v3, s[20:23], s40 offen offset:0 sc1 lds
v_mul_f32_e32 v148, v12, v148
Mp 200,4,60
Rb 52,8,30720
v_mul_f32_e32 v149, v12, v149
Cv 146,148,149
Mp 192,4,56
v_mul_f32_e32 v150, v12, v150
v_mul_f32_e32 v151, v12, v151
Cv 147,150,151
Mp 196,8,48
Rb 56,8,15360
v_mul_f32_e32 v152, v13, v152
v_mul_f32_e32 v153, v13, v153
Mp 204,8,52
buffer_load_dwordx4 v3, s[20:23], s40 offen offset:1024 sc1 lds
Cv 152,152,153
Mp 204,12,60
Rb 60,8,31744
v_mul_f32_e32 v154, v13, v154
s_waitcnt lgkmcnt(8)
Mp 196,12,56
buffer_load_dwordx4 v3, s[20:23], s40 offen offset:2048 sc1 lds
v_mul_f32_e32 v155, v13, v155
Mp 208,16,48
Rb 0,1,0
Cv 153,154,155
v_mul_f32_e32 v156, v13, v156
Mp 216,16,52
)" R"(
v_mul_f32_e32 v157, v13, v157
Cv 154,156,157
v_mul_f32_e32 v158, v13, v158
Mp 216,20,60
Rb 4,1,1024
v_mul_f32_e32 v159, v13, v159
Cv 155,158,159
Mp 208,20,56
v_mul_f32_e32 v160, v12, v160
v_mul_f32_e32 v161, v12, v161
Cv 160,160,161
Mp 212,24,48
Rb 8,1,2048
v_mul_f32_e32 v162, v12, v162
v_mul_f32_e32 v163, v12, v163
Mp 220,24,52
Cv 161,162,163
v_mul_f32_e32 v164, v12, v164
v_mul_f32_e32 v165, v12, v165
Mp 220,28,60
Rb 12,1,3072
s_waitcnt vmcnt(14) lgkmcnt(4)
s_barrier
Mp 212,28,56
s_add_u32 s40, s67, 0x40000
Cv 162,164,165
v_mul_f32_e32 v166, v12, v166
Mp 224,32,48
Rb 16,1,4096
s_add_u32 m0, s36, 65536
Mp 232,32,52
buffer_load_dwordx4 v2, s[20:23], s40 offen offset:0 sc1 lds
v_mul_f32_e32 v167, v12, v167
Mp 232,36,60
Rb 20,1,5120
Cv 163,166,167
v_mul_f32_e32 v168, v13, v168
Mp 224,36,56
v_mul_f32_e32 v169, v13, v169
Cv 168,168,169
v_mul_f32_e32 v170, v13, v170
Mp 228,40,48
Rb 24,1,6144
v_mul_f32_e32 v171, v13, v171
Cv 169,170,171
Mp 236,40,52
v_mul_f32_e32 v172, v13, v172
v_mul_f32_e32 v173, v13, v173
Cv 170,172,173
Mp 236,44,60
Rb 28,1,8192
v_mul_f32_e32 v174, v13, v174
Mp 228,44,56
buffer_load_dwordx4 v2, s[20:23], s40 offen offset:1024 sc1 lds
v_mul_f32_e32 v175, v13, v175
s_lshl4_add_u32 s28, s34, s35
Mp 240,48,48
Rb 32,1,9216
Cv 171,174,175
v_mul_f32_e32 v176, v12, v176
Mp 248,48,52
v_mul_f32_e32 v177, v12, v177
Cv 176,176,177
v_mul_f32_e32 v178, v12, v178
Mp 248,52,60
)" R"(
Rb 36,1,10240
v_mul_f32_e32 v179, v12, v179
Cv 177,178,179
Mp 240,52,56
v_mul_f32_e32 v180, v12, v180
v_mul_f32_e32 v181, v12, v181
Cv 178,180,181
Mp 244,56,48
Rb 40,1,11264
v_mul_f32_e32 v182, v12, v182
v_mul_f32_e32 v183, v12, v183
s_lshl_b32 s64, s28, 17
Mp 252,56,52
buffer_load_dwordx4 v2, s[20:23], s40 offen offset:2048 sc1 lds
Cv 179,182,183
Mp 252,60,60
Rb 44,1,12288
v_mul_f32_e32 v184, v13, v184
s_mov_b32 s55, 0xc108c00
s_waitcnt lgkmcnt(8)
Mp 244,60,56
v_mul_f32_e32 v185, v13, v185
Cv 184,184,185
v_mul_f32_e32 v186, v13, v186
v_mfma_f32_16x16x32_bf16 v[32:35], a[0:3], v[128:131], 0
Rb 48,1,13312
v_mul_f32_e32 v187, v13, v187
s_add_u32 s64, s64, 0x4000000
v_mfma_f32_16x16x32_bf16 v[40:43], a[0:3], v[136:139], 0
Cv 185,186,187
v_mul_f32_e32 v188, v13, v188
v_mul_f32_e32 v189, v13, v189
s_xor_b32 s66, s64, 0x202000
v_mfma_f32_16x16x32_bf16 v[44:47], a[4:7], v[136:139], 0
Rb 52,1,14336
Cv 186,188,189
v_mul_f32_e32 v190, v13, v190
v_mfma_f32_16x16x32_bf16 v[36:39], a[4:7], v[128:131], 0
buffer_load_dwordx4 v2, s[20:23], s40 offen offset:3072 sc1 lds
v_mul_f32_e32 v191, v13, v191
v_mfma_f32_16x16x32_bf16 v[48:51], a[8:11], v[128:131], 0
Rb 56,1,16384
Cv 187,190,191
v_mul_f32_e32 v192, v12, v192
s_cmp_lg_u32 s33, 0
s_cmov_b32 s55, 0x40000000
v_mfma_f32_16x16x32_bf16 v[56:59], a[8:11], v[136:139], 0
v_mul_f32_e32 v193, v12, v193
Cv 192,192,193
v_mul_f32_e32 v194, v12, v194
v_mov_b32 v16, 1
v_mfma_f32_16x16x32_bf16 v[60:63], a[12:15], v[136:139], 0
Rb 60,1,17408
v_mul_f32_e32 v195, v12, v195
v_add_u32 v9, s36, v8
s_waitcnt lgkmcnt(8)
v_mfma_f32_16x16x32_bf16 v[52:55], a[12:15], v[128:131], 0
Cv 193,194,195
v_mul_f32_e32 v196, v12, v196
v_mul_f32_e32 v197, v12, v197
s_sub_u32 s66, s66, s84
Mp 32,16,144
Rb 0,1,18432
s_add_u32 m0, s36, 69632
)" R"(
Cv 194,196,197
Mp 40,16,152
buffer_load_dwordx4 v3, s[20:23], s40 offen offset:0 sc1 lds
v_mul_f32_e32 v198, v12, v198
Mp 44,20,152
Rb 4,1,19456
v_mul_f32_e32 v199, v12, v199
Cv 195,198,199
Mp 36,20,144
v_mul_f32_e32 v200, v13, v200
v_mul_f32_e32 v201, v13, v201
Cv 200,200,201
Mp 48,24,144
Rb 8,1,20480
v_mul_f32_e32 v202, v13, v202
v_mul_f32_e32 v203, v13, v203
s_sub_u32 s66, s66, s84
Mp 56,24,152
Cv 201,202,203
v_mul_f32_e32 v204, v13, v204
v_mul_f32_e32 v205, v13, v205
Mp 60,28,152
Rb 12,1,21504
Cv 202,204,205
s_waitcnt lgkmcnt(8)
Mp 52,28,144
buffer_load_dwordx4 v3, s[20:23], s40 offen offset:1024 sc1 lds
v_mul_f32_e32 v206, v13, v206
Mp 32,32,160
Rb 16,1,22528
v_mul_f32_e32 v207, v13, v207
s_lshl2_add_u32 s65, s35, s33
Mp 40,32,168
Cv 203,206,207
v_mul_f32_e32 v208, v12, v208
v_mul_f32_e32 v209, v12, v209
s_lshl_b32 s65, s65, 8
Mp 44,36,168
Rb 20,1,24576
Cv 208,208,209
v_mul_f32_e32 v210, v12, v210
Mp 36,36,160
v_mul_f32_e32 v211, v12, v211
Cv 209,210,211
v_mul_f32_e32 v212, v12, v212
Mp 48,40,160
Rb 24,1,25600
v_mul_f32_e32 v213, v12, v213
Cv 210,212,213
Mp 56,40,168
buffer_load_dwordx4 v3, s[20:23], s40 offen offset:2048 sc1 lds
v_mul_f32_e32 v214, v12, v214
Mp 60,44,168
Rb 28,1,26624
v_mul_f32_e32 v215, v12, v215
s_waitcnt lgkmcnt(8)
Mp 52,44,160
Cv 211,214,215
v_mul_f32_e32 v216, v13, v216
v_mul_f32_e32 v217, v13, v217
s_add_u32 s65, s65, s50
Mp 32,48,176
Rb 32,1,27648
Cv 216,216,217
)" R"(
v_mul_f32_e32 v218, v13, v218
Mp 40,48,184
v_mul_f32_e32 v219, v13, v219
Cv 217,218,219
v_mul_f32_e32 v220, v13, v220
Mp 44,52,184
Rb 36,1,28672
v_mul_f32_e32 v221, v13, v221
Cv 218,220,221
Mp 36,52,176
v_mul_f32_e32 v222, v13, v222
v_mul_f32_e32 v223, v13, v223
Cv 219,222,223
Mp 48,56,176
Rb 40,1,29696
v_mul_f32_e32 v224, v12, v224
v_mul_f32_e32 v225, v12, v225
s_add_u32 s65, s65, 0x2400
Mp 56,56,184
Cv 224,224,225
v_mul_f32_e32 v226, v12, v226
v_mul_f32_e32 v227, v12, v227
Mp 60,60,184
Rb 44,1,30720
Cv 225,226,227
buffer_atomic_add v16, v7, s[20:23], s55 offen sc0 sc1
s_waitcnt lgkmcnt(8)
Mp 52,60,176
v_mul_f32_e32 v228, v12, v228
v_mul_f32_e32 v229, v12, v229
Cv 226,228,229
Mp 32,0,192
Rb 48,1,32768
v_mul_f32_e32 v230, v12, v230
s_xor_b32 s52, s65, 0x202000
Mp 40,0,200
v_mul_f32_e32 v231, v12, v231
Cv 227,230,231
v_mul_f32_e32 v232, v13, v232
Mp 44,4,200
Rb 52,1,33792
v_mul_f32_e32 v233, v13, v233
Cv 232,232,233
Mp 36,4,192
v_mul_f32_e32 v234, v13, v234
v_mul_f32_e32 v235, v13, v235
Cv 233,234,235
Mp 48,8,192
Rb 56,1,34816
v_mul_f32_e32 v236, v13, v236
v_mul_f32_e32 v237, v13, v237
Mp 56,8,200
Cv 234,236,237
v_mul_f32_e32 v238, v13, v238
v_mul_f32_e32 v239, v13, v239
Mp 60,12,200
Rb 60,1,35840
s_waitcnt vmcnt(15) lgkmcnt(4)
s_barrier
buffer_store_dword v6, v7, s[20:23], s82 offen offset:0 sc0 sc1
Mp 52,12,192
s_add_u32 s40, s67, 0x50000
Cv 235,238,239
v_mul_f32_e32 v240, v12, v240
Mp 32,16,208
)" R"(
Rb 0,1,36864
s_add_u32 m0, s36, 0
v_mul_f32_e32 v241, v12, v241
Mp 40,16,216
buffer_load_dwordx4 v2, s[20:23], s40 offen offset:0 sc1 lds
Cv 240,240,241
Mp 44,20,216
Rb 4,1,37888
v_mul_f32_e32 v242, v12, v242
v_mul_f32_e32 v243, v12, v243
Mp 36,20,208
Cv 241,242,243
v_mul_f32_e32 v244, v12, v244
v_mul_f32_e32 v245, v12, v245
Mp 48,24,208
Rb 8,1,38912
Cv 242,244,245
v_mul_f32_e32 v246, v12, v246
Mp 56,24,216
v_mul_f32_e32 v247, v12, v247
Cv 243,246,247
v_mul_f32_e32 v248, v13, v248
Mp 60,28,216
Rb 12,1,40960
v_mul_f32_e32 v249, v13, v249
Mp 52,28,208
buffer_load_dwordx4 v2, s[20:23], s40 offen offset:1024 sc1 lds
Cv 248,248,249
Mp 32,32,224
Rb 16,1,41984
v_mul_f32_e32 v250, v13, v250
Mp 40,32,232
v_mul_f32_e32 v251, v13, v251
Cv 249,250,251
v_mul_f32_e32 v252, v13, v252
Mp 44,36,232
Rb 20,1,43008
v_mul_f32_e32 v253, v13, v253
Cv 250,252,253
Mp 36,36,224
v_mul_f32_e32 v254, v13, v254
v_mul_f32_e32 v255, v13, v255
Cv 251,254,255
Mp 48,40,224
Rb 24,1,44032
Mp 56,40,232
buffer_load_dwordx4 v2, s[20:23], s40 offen offset:2048 sc1 lds
Mp 60,44,232
Rb 28,1,45056
s_waitcnt lgkmcnt(8)
Mp 52,44,224
Mp 32,48,240
Rb 32,1,46080
Mp 40,48,248
Mp 44,52,248
Rb 36,1,47104
Mp 36,52,240
buffer_load_dwordx4 v2, s[20:23], s40 offen offset:3072 sc1 lds
Mp 48,56,240
Rb 40,1,49152
Mp 56,56,248
Mp 60,60,248
Rb 44,1,50176
s_waitcnt lgkmcnt(8)
)" R"(
Mp 52,60,240
v_mfma_f32_16x16x32_bf16 v[64:67], a[0:3], v[128:131], 0
Rb 48,1,51200
s_add_u32 m0, s36, 4096
v_mfma_f32_16x16x32_bf16 v[72:75], a[0:3], v[136:139], 0
buffer_load_dwordx4 v3, s[20:23], s40 offen offset:0 sc1 lds
Mp 72,4,152
Rb 52,1,52224
Mp 64,4,144
Cv 32,32,33
Cv 33,34,35
Mp 64,8,160
Rb 56,1,53248
Cv 34,36,37
Mp 72,8,168
Cv 35,38,39
Mp 72,12,184
Rb 60,1,54272
s_waitcnt lgkmcnt(8)
Mp 64,12,176
buffer_load_dwordx4 v3, s[20:23], s40 offen offset:1024 sc1 lds
Mp 64,16,192
Rb 0,1,55296
Mp 72,16,200
buffer_store_dwordx4 v[32:35], v2, s[20:23], s64 offen offset:0 sc0 sc1
Mp 72,20,216
Rb 4,1,57344
Mp 64,20,208
Mp 64,24,224
Rb 8,1,58368
Mp 72,24,232
buffer_load_dwordx4 v3, s[20:23], s40 offen offset:2048 sc1 lds
Mp 72,28,248
Rb 12,1,59392
s_waitcnt lgkmcnt(8)
Mp 64,28,240
Cv 40,40,41
Cv 41,42,43
v_mfma_f32_16x16x32_bf16 v[68:71], a[32:35], v[128:131], 0
Rb 16,1,60416
Cv 42,44,45
v_mfma_f32_16x16x32_bf16 v[76:79], a[32:35], v[136:139], 0
Cv 43,46,47
Mp 76,36,152
Rb 20,1,61440
Mp 68,36,144
buffer_store_dwordx4 v[40:43], v2, s[20:23], s64 offen offset:1024 sc0 sc1
Mp 68,40,160
Rb 24,1,62464
Mp 76,40,168
Mp 76,44,184
Rb 28,1,63488
s_waitcnt lgkmcnt(8)
Mp 68,44,176
Mp 68,48,192
Rb 32,10,0
Mp 76,48,200
Cv 48,48,49
Cv 49,50,51
Mp 76,52,216
Rb 36,10,1024
Cv 50,52,53
Mp 68,52,208
Cv 51,54,55
)" R"(
Mp 68,56,224
Rb 40,10,2048
Mp 76,56,232
buffer_store_dwordx4 v[48:51], v2, s[20:23], s64 offen offset:2048 sc0 sc1
Mp 76,60,248
Rb 44,10,3072
s_waitcnt vmcnt(19) lgkmcnt(4)
s_barrier
Mp 68,60,240
s_add_u32 s40, s67, 0x60000
v_mfma_f32_16x16x32_bf16 v[80:83], a[0:3], v[128:131], 0
Rb 48,10,4096
s_add_u32 m0, s36, 32768
v_mfma_f32_16x16x32_bf16 v[88:91], a[0:3], v[136:139], 0
buffer_load_dwordx4 v2, s[20:23], s40 offen offset:0 sc1 lds
Mp 88,4,152
Rb 52,10,5120
Mp 80,4,144
Cv 56,56,57
Cv 57,58,59
Cv 58,60,61
Mp 80,8,160
Rb 56,10,6144
v_bfe_u32 v11, v0, 2, 2
Cv 59,62,63
Mp 88,8,168
v_lshrrev_b32 v4, 4, v0
v_xor_b32 v11, v11, v4
Mp 88,12,184
Rb 60,10,8192
Mp 80,12,176
buffer_load_dwordx4 v2, s[20:23], s40 offen offset:1024 sc1 lds
Mp 80,16,192
Rb 0,10,9216
v_bfe_u32 v4, v0, 1, 1
Mp 88,16,200
v_lshl_or_b32 v11, v11, 1, v4
v_and_b32 v4, 1, v0
Mp 88,20,216
Rb 4,10,10240
v_mul_u32_u24 v4, 0xffff, v4
buffer_store_dwordx4 v[56:59], v2, s[20:23], s64 offen offset:3072 sc0 sc1
Mp 80,20,208
v_add_u32 v4, 1, v4
v_mul_u32_u24 v4, 0x3f80, v4
Mp 80,24,224
Rb 8,10,11264
v_cmp_eq_u32_e32 vcc, 0, v11
Mp 88,24,232
buffer_load_dwordx4 v2, s[20:23], s40 offen offset:2048 sc1 lds
Mp 88,28,248
Rb 12,10,12288
s_waitcnt lgkmcnt(8)
Mp 80,28,240
v_cmp_eq_u32_e64 s[28:29], 1, v11
v_accvgpr_write_b32 a192, 0
v_mfma_f32_16x16x32_bf16 v[84:87], a[32:35], v[128:131], 0
Rb 16,10,13312
v_accvgpr_write_b32 a193, 0
v_mfma_f32_16x16x32_bf16 v[92:95], a[32:35], v[136:139], 0
v_accvgpr_write_b32 a196, 0
v_accvgpr_write_b32 a197, 0
Cv 64,64,65
Mp 92,36,152
Rb 20,10,14336
)" R"(
v_cndmask_b32_e32 v11, 0, v4, vcc
Mp 84,36,144
buffer_load_dwordx4 v2, s[20:23], s40 offen offset:3072 sc1 lds
Cv 65,66,67
Mp 84,40,160
Rb 24,10,16384
v_cndmask_b32_e64 v4, 0, v4, s[28:29]
Mp 92,40,168
v_accvgpr_write_b32 a194, v11
v_accvgpr_write_b32 a195, v4
Cv 66,68,69
Mp 92,44,184
Rb 28,10,17408
s_waitcnt lgkmcnt(8)
Mp 84,44,176
Cv 67,70,71
buffer_store_dwordx4 v[64:67], v3, s[20:23], s64 offen offset:0 sc0 sc1
Mp 84,48,192
Rb 32,10,18432
s_add_u32 m0, s36, 36864
Mp 92,48,200
buffer_load_dwordx4 v3, s[20:23], s40 offen offset:0 sc1 lds
Mp 92,52,216
Rb 36,10,19456
Mp 84,52,208
Mp 84,56,224
Rb 40,10,20480
Mp 92,56,232
Cv 72,72,73
Cv 73,74,75
Mp 92,60,248
Rb 44,10,21504
s_waitcnt lgkmcnt(8)
Mp 84,60,240
buffer_load_dwordx4 v3, s[20:23], s40 offen offset:1024 sc1 lds
v_mfma_f32_16x16x32_bf16 v[96:99], a[0:3], v[128:131], 0
Rb 48,10,22528
Cv 74,76,77
v_mfma_f32_16x16x32_bf16 v[104:107], a[0:3], v[136:139], 0
Cv 75,78,79
Mp 104,4,152
Rb 52,10,24576
Mp 96,4,144
buffer_store_dwordx4 v[72:75], v3, s[20:23], s64 offen offset:1024 sc0 sc1
Mp 96,8,160
Rb 56,10,25600
Mp 104,8,168
buffer_load_dwordx4 v3, s[20:23], s40 offen offset:2048 sc1 lds
Mp 104,12,184
Rb 60,10,26624
s_waitcnt lgkmcnt(8)
Mp 96,12,176
Mp 96,16,192
Rb 0,10,27648
Mp 104,16,200
Cv 80,80,81
Cv 81,82,83
Mp 104,20,216
Rb 4,10,28672
Cv 82,84,85
Mp 96,20,208
Cv 83,86,87
Mp 96,24,224
Rb 8,10,29696
)" R"(
Mp 104,24,232
buffer_store_dwordx4 v[80:83], v3, s[20:23], s64 offen offset:2048 sc0 sc1
Mp 104,28,248
Rb 12,10,30720
s_waitcnt lgkmcnt(8)
Mp 96,28,240
v_mfma_f32_16x16x32_bf16 v[100:103], a[32:35], v[128:131], 0
Rb 16,8,32768
v_mfma_f32_16x16x32_bf16 v[108:111], a[32:35], v[136:139], 0
Mp 108,36,152
Rb 20,8,33792
Mp 100,36,144
Cv 88,88,89
Cv 89,90,91
Mp 100,40,160
Rb 24,8,34816
Cv 90,92,93
Mp 108,40,168
Cv 91,94,95
Mp 108,44,184
Rb 28,8,35840
s_waitcnt vmcnt(23) lgkmcnt(4)
s_barrier
Mp 100,44,176
s_add_u32 s40, s67, 0x70000
Mp 100,48,192
Rb 32,8,36864
s_add_u32 m0, s36, 131072
Mp 108,48,200
buffer_load_dwordx4 v2, s[20:23], s40 offen offset:0 sc1 lds
Mp 108,52,216
Rb 36,8,37888
buffer_store_dwordx4 v[88:91], v3, s[20:23], s64 offen offset:3072 sc0 sc1
Mp 100,52,208
s_mov_b32 s8, s74
s_mov_b32 s9, s75
Mp 100,56,224
Rb 40,8,38912
s_mov_b32 s10, 0x1c0000
Mp 108,56,232
s_mov_b32 s11, 0x20000
v_and_b32 v174, 1, v0
Mp 108,60,248
Rb 44,8,40960
Mp 100,60,240
buffer_load_dwordx4 v2, s[20:23], s40 offen offset:1024 sc1 lds
v_mfma_f32_16x16x32_bf16 v[112:115], a[0:3], v[128:131], 0
Rb 48,8,41984
v_mul_u32_u24 v174, 0xffff, v174
v_mfma_f32_16x16x32_bf16 v[120:123], a[0:3], v[136:139], 0
v_add_u32 v174, 1, v174
v_mul_u32_u24 v174, 0x3f80, v174
Mp 120,4,152
Rb 52,8,43008
v_bfe_u32 v173, v0, 1, 3
Mp 112,4,144
Cv 96,96,97
Cv 97,98,99
Mp 112,8,160
Rb 56,8,44032
Cv 98,100,101
Mp 120,8,168
buffer_load_dwordx4 v2, s[20:23], s40 offen offset:2048 sc1 lds
Mp 120,12,184
Rb 60,8,45056
)" R"(
s_waitcnt lgkmcnt(8)
Mp 112,12,176
Cv 99,102,103
v_cmp_eq_u32_e32 vcc, 0, v173
v_and_b32 v167, 0xf0, v1
Mp 112,16,192
Rb 0,8,46080
v_cndmask_b32_e32 v132, 0, v174, vcc
Mp 120,16,200
s_add_u32 s64, s64, 0x10000
buffer_store_dwordx4 v[96:99], v2, s[20:23], s64 offen offset:0 sc0 sc1
Mp 120,20,216
Rb 4,8,47104
v_cmp_eq_u32_e32 vcc, 1, v173
Mp 112,20,208
buffer_load_dwordx4 v2, s[20:23], s40 offen offset:3072 sc1 lds
Mp 112,24,224
Rb 8,8,49152
v_cndmask_b32_e32 v133, 0, v174, vcc
Mp 120,24,232
v_cmp_eq_u32_e32 vcc, 2, v173
v_mul_u32_u24 v166, 56, v167
Mp 120,28,248
Rb 12,8,50176
s_waitcnt lgkmcnt(8)
Mp 112,28,240
Cv 104,104,105
Cv 105,106,107
v_mfma_f32_16x16x32_bf16 v[116:119], a[32:35], v[128:131], 0
Rb 16,8,51200
s_add_u32 m0, s36, 135168
v_mfma_f32_16x16x32_bf16 v[124:127], a[32:35], v[136:139], 0
buffer_load_dwordx4 v3, s[20:23], s40 offen offset:0 sc1 lds
Mp 124,36,152
Rb 20,8,52224
Cv 106,108,109
Mp 116,36,144
Cv 107,110,111
v_cndmask_b32_e32 v134, 0, v174, vcc
Mp 116,40,160
Rb 24,8,53248
v_cmp_eq_u32_e32 vcc, 3, v173
Mp 124,40,168
buffer_store_dwordx4 v[104:107], v2, s[20:23], s64 offen offset:1024 sc0 sc1
Mp 124,44,184
Rb 28,8,54272
s_waitcnt lgkmcnt(8)
Mp 116,44,176
buffer_load_dwordx4 v3, s[20:23], s40 offen offset:1024 sc1 lds
v_cndmask_b32_e32 v135, 0, v174, vcc
Mp 116,48,192
Rb 32,8,55296
v_cmp_eq_u32_e32 vcc, 4, v173
Mp 124,48,200
s_nop 0
v_cndmask_b32_e32 v140, 0, v174, vcc
v_cmp_eq_u32_e32 vcc, 5, v173
Mp 124,52,216
Rb 36,8,57344
v_cndmask_b32_e32 v141, 0, v174, vcc
Mp 116,52,208
v_cmp_eq_u32_e32 vcc, 6, v173
v_and_b32 v165, 0x80, v1
Mp 116,56,224
)" R"(
Rb 40,8,58368
v_cndmask_b32_e32 v142, 0, v174, vcc
Mp 124,56,232
buffer_load_dwordx4 v3, s[20:23], s40 offen offset:2048 sc1 lds
Mp 124,60,248
Rb 44,8,59392
s_waitcnt vmcnt(8) lgkmcnt(8)
buffer_store_dword v6, v7, s[20:23], s65 offen offset:0 sc0 sc1
Mp 116,60,240
v_cmp_eq_u32_e32 vcc, 7, v173
v_lshrrev_b32 v173, 4, v0
v_mfma_f32_16x16x32_bf16 v[32:35], a[0:3], v[128:131], 0
Rb 48,8,60416
v_cndmask_b32_e32 v143, 0, v174, vcc
v_mfma_f32_16x16x32_bf16 v[40:43], a[0:3], v[136:139], 0
v_lshl_or_b32 v164, v173, 16, v167
v_lshl_add_u32 v166, v173, 3, v166
Mp 40,4,152
Rb 52,8,61440
v_lshrrev_b32_e64 v173, v173, s85
Mp 32,4,144
Cv 112,112,113
Cv 113,114,115
Mp 32,8,160
Rb 56,8,62464
Cv 114,116,117
Mp 40,8,168
Cv 115,118,119
v_bfe_u32 v174, v173, 4, 1
Mp 40,12,184
Rb 60,8,63488
s_waitcnt lgkmcnt(8)
Mp 32,12,176
buffer_store_dwordx4 v[112:115], v2, s[20:23], s64 offen offset:2048 sc0 sc1
ds_write_b32 v9, v16 offset:39936
Mp 32,16,192
Rb 0,8,0
v_lshl_or_b32 v183, v174, 13, v164
Mp 40,16,200
v_and_b32 v174, 1, v173
v_lshl_or_b32 v164, v174, 13, v164
Mp 40,20,216
Rb 4,8,1024
v_and_b32 v174, 7, v0
Mp 32,20,208
v_lshl_or_b32 v165, v174, 8, v165
v_lshlrev_b32 v165, 1, v165
Mp 32,24,224
Rb 8,8,2048
s_mov_b32 s93, 0x1111
Mp 40,24,232
Cv 120,120,121
Cv 121,122,123
Mp 40,28,248
Rb 12,8,3072
s_waitcnt lgkmcnt(3)
s_barrier
Mp 32,28,240
Cv 122,124,125
Cv 123,126,127
ds_read_b32 v16, v8 offset:39936
v_mfma_f32_16x16x32_bf16 v[36:39], a[32:35], v[128:131], 0
Rb 16,8,4096
v_lshrrev_b32 v173, 2, v0
v_mfma_f32_16x16x32_bf16 v[44:47], a[32:35], v[136:139], 0
)" R"(
buffer_store_dwordx4 v[120:123], v2, s[20:23], s64 offen offset:3072 sc0 sc1
Mp 44,36,152
Rb 20,8,5120
v_mul_u32_u24 v173, 832, v173
Mp 36,36,144
v_add_u32 v173, v173, v1
v_lshrrev_b32 v174, 1, v0
Mp 36,40,160
Rb 24,8,6144
v_and_b32 v174, 24, v174
Mp 44,40,168
v_lshl_add_u32 v174, v167, 2, v174
s_add_u32 s59, s36, 98304
Mp 44,44,184
Rb 28,8,8192
s_waitcnt lgkmcnt(4)
Mp 36,44,176
v_readfirstlane_b32 s82, v16
v_add_u32 v174, s59, v174
Mp 36,48,192
Rb 32,8,9216
s_lshl2_add_u32 s56, s82, s33
Mp 44,48,200
s_mul_i32 s88, s56, 4682
s_lshr_b32 s58, s88, 16
Mp 44,52,216
Rb 36,8,10240
s_mul_i32 s57, s58, 14
Mp 36,52,208
s_sub_u32 s57, s56, s57
s_lshl_b32 s40, s58, 12
Mp 36,56,224
Rb 40,8,11264
Mp 44,56,232
s_cmp_gt_u32 s57, 6
s_cselect_b32 s88, 128, 0
Mp 44,60,248
Rb 44,8,12288
Mp 36,60,240
buffer_load_dword v4, v7, s[20:23], s52 offen offset:0 sc0 sc1
s_add_u32 s40, s40, s88
v_mfma_f32_16x16x32_bf16 v[48:51], a[0:3], v[128:131], 0
Rb 48,8,13312
s_add_u32 s40, s40, 0xc110000
v_mfma_f32_16x16x32_bf16 v[56:59], a[0:3], v[136:139], 0
s_lshl_b32 s55, s58, 10
s_add_u32 s55, s55, s57
Mp 56,4,152
Rb 52,8,14336
Mp 48,4,144
Cv 32,32,33
Cv 33,34,35
Mp 48,8,160
Rb 56,8,16384
Cv 34,36,37
Mp 56,8,168
Cv 35,38,39
Mp 56,12,184
Rb 60,8,17408
s_waitcnt lgkmcnt(8)
Mp 48,12,176
buffer_store_dwordx4 v[32:35], v3, s[20:23], s64 offen offset:0 sc0 sc1
Mp 48,16,192
Rb 0,8,18432
)" R"(
Mp 56,16,200
s_cmp_gt_u32 s57, 6
s_cselect_b32 s88, 9, 0
Mp 56,20,216
Rb 4,8,19456
s_add_u32 s55, s55, s88
Mp 48,20,208
s_and_b32 s88, s58, 2
s_lshl_b32 s88, s88, 12
Mp 48,24,224
Rb 8,8,20480
v_xor_b32 v164, s88, v164
Mp 56,24,232
Cv 40,40,41
Cv 41,42,43
Mp 56,28,248
Rb 12,8,21504
s_waitcnt lgkmcnt(8)
Mp 48,28,240
Cv 42,44,45
Cv 43,46,47
v_xor_b32 v183, s88, v183
v_mfma_f32_16x16x32_bf16 v[52:55], a[32:35], v[128:131], 0
Rb 16,8,22528
s_lshl_b32 s55, s55, 10
v_mfma_f32_16x16x32_bf16 v[60:63], a[32:35], v[136:139], 0
buffer_store_dwordx4 v[40:43], v3, s[20:23], s64 offen offset:1024 sc0 sc1
Mp 60,36,152
Rb 20,8,24576
s_add_u32 s55, s55, 0x8000000
Mp 52,36,144
s_and_b32 s88, s57, 1
s_lshl1_add_u32 s58, s58, s88
Mp 52,40,160
Rb 24,8,25600
s_lshl_b32 s88, s58, 8
Mp 60,40,168
v_add_u32 v167, s88, v167
s_mul_i32 s59, s58, 14336
Mp 60,44,184
Rb 28,8,26624
s_waitcnt lgkmcnt(8)
Mp 52,44,176
s_lshr_b32 s88, s57, 1
s_lshl_b32 s88, s88, 6
s_add_u32 s88, s88, s59
Mp 52,48,192
Rb 32,8,27648
v_add_u32 v166, s88, v166
Mp 60,48,200
s_mov_b32 s57, 0
s_add_u32 m0, s36, 98304
Mp 60,52,216
Rb 36,8,28672
Mp 52,52,208
buffer_load_dwordx4 v173, s[12:15], s88 offen offset:0 lds
Mp 52,56,224
Rb 40,8,29696
s_add_u32 m0, s36, 98880
Mp 60,56,232
buffer_load_dwordx4 v173, s[12:15], s88 offen offset:448 lds
Mp 60,60,248
Rb 44,8,30720
s_waitcnt lgkmcnt(8)
)" R"(
Mp 52,60,240
s_mov_b32 s88, 0xc100008
buffer_load_dword v167, v167, s[20:23], s88 offen sc0 sc1
v_mfma_f32_16x16x32_bf16 v[64:67], a[0:3], v[128:131], 0
Rb 48,1,0
v_mfma_f32_16x16x32_bf16 v[72:75], a[0:3], v[136:139], 0
Mp 72,4,152
Rb 52,1,1024
Mp 64,4,144
Mp 64,8,160
Rb 56,1,2048
Mp 72,8,168
Mp 72,12,184
Rb 60,1,3072
s_waitcnt vmcnt(5) lgkmcnt(8)
s_barrier
v_cmp_eq_u32_e32 vcc, s3, v4
s_andn2_b64 s[28:29], 1, vcc
s_cbranch_scc1 .Lzsp11
.Lzok11:
Mp 64,12,176
buffer_load_dwordx4 a[64:67], v2, s[20:23], s66 offen offset:0 sc0 sc1
Mp 64,16,192
Rb 0,1,4096
Mp 72,16,200
Mp 72,20,216
Rb 4,1,5120
Mp 64,20,208
buffer_load_dwordx4 a[68:71], v2, s[20:23], s66 offen offset:1024 sc0 sc1
Mp 64,24,224
Rb 8,1,6144
Mp 72,24,232
Mp 72,28,248
Rb 12,1,8192
s_waitcnt lgkmcnt(8)
Mp 64,28,240
buffer_load_dwordx4 a[72:75], v2, s[20:23], s66 offen offset:2048 sc0 sc1
v_mfma_f32_16x16x32_bf16 v[68:71], a[32:35], v[128:131], 0
Rb 16,1,9216
v_mfma_f32_16x16x32_bf16 v[76:79], a[32:35], v[136:139], 0
Mp 76,36,152
Rb 20,1,10240
Mp 68,36,144
buffer_load_dwordx4 a[76:79], v2, s[20:23], s66 offen offset:3072 sc0 sc1
Mp 68,40,160
Rb 24,1,11264
Mp 76,40,168
Mp 76,44,184
Rb 28,1,12288
s_waitcnt vmcnt(7) lgkmcnt(8)
buffer_store_dword v6, v7, s[20:23], s65 offen offset:128 sc0 sc1
Mp 68,44,176
buffer_load_dwordx4 a[80:83], v3, s[20:23], s66 offen offset:0 sc0 sc1
Mp 68,48,192
Rb 32,1,13312
Mp 76,48,200
Mp 76,52,216
Rb 36,1,14336
Mp 68,52,208
buffer_load_dwordx4 a[84:87], v3, s[20:23], s66 offen offset:1024 sc0 sc1
Mp 68,56,224
Rb 40,1,16384
Mp 76,56,232
Mp 76,60,248
Rb 44,1,17408
)" R"(
s_waitcnt lgkmcnt(8)
Mp 68,60,240
buffer_load_dwordx4 a[88:91], v3, s[20:23], s66 offen offset:2048 sc0 sc1
v_mfma_f32_16x16x32_bf16 v[80:83], a[0:3], v[128:131], 0
Rb 48,1,18432
v_mfma_f32_16x16x32_bf16 v[88:91], a[0:3], v[136:139], 0
Mp 88,4,152
Rb 52,1,19456
Mp 80,4,144
buffer_load_dwordx4 a[92:95], v3, s[20:23], s66 offen offset:3072 sc0 sc1
Mp 80,8,160
Rb 56,1,20480
Mp 88,8,168
Mp 88,12,184
Rb 60,1,21504
s_waitcnt lgkmcnt(8)
Mp 80,12,176
Mp 80,16,192
Rb 0,1,22528
Mp 88,16,200
Mp 88,20,216
Rb 4,1,24576
Mp 80,20,208
Mp 80,24,224
Rb 8,1,25600
Mp 88,24,232
Mp 88,28,248
Rb 12,1,26624
s_waitcnt lgkmcnt(8)
Mp 80,28,240
v_mfma_f32_16x16x32_bf16 v[84:87], a[32:35], v[128:131], 0
Rb 16,1,27648
v_mfma_f32_16x16x32_bf16 v[92:95], a[32:35], v[136:139], 0
Mp 92,36,152
Rb 20,1,28672
Mp 84,36,144
Mp 84,40,160
Rb 24,1,29696
Mp 92,40,168
Mp 92,44,184
Rb 28,1,30720
s_waitcnt lgkmcnt(8)
Mp 84,44,176
buffer_load_dword v4, v7, s[20:23], s52 offen offset:128 sc0 sc1
Mp 84,48,192
Rb 32,1,32768
Mp 92,48,200
Mp 92,52,216
Rb 36,1,33792
Mp 84,52,208
Mp 84,56,224
Rb 40,1,34816
Mp 92,56,232
Mp 92,60,248
Rb 44,1,35840
s_waitcnt vmcnt(6)
s_barrier
Mp 84,60,240
Mv 48,194,64
Mv 56,194,68
Mv 52,192,64
Mv 60,192,68
Mv 64,194,72
Mv 72,194,76
Mv 68,192,72
)" R"(
s_waitcnt vmcnt(3) lgkmcnt(10)
Mv 76,192,76
Cv 48,48,49
Cv 49,50,51
Mv 80,194,80
Cv 50,52,53
Cv 51,54,55
Mv 88,194,84
s_add_u32 s29, s73, 0x0
buffer_store_dwordx4 v[48:51], v1, s[20:23], s29 offen sc0 sc1
Mv 84,192,80
Mv 92,192,84
v_mfma_f32_16x16x32_bf16 v[96:99], a[0:3], v[128:131], 0
Rb 48,1,36864
v_mfma_f32_16x16x32_bf16 v[104:107], a[0:3], v[136:139], 0
Mp 104,4,152
Rb 52,1,37888
s_waitcnt lgkmcnt(8)
Mp 96,4,144
Cv 56,56,57
Cv 57,58,59
ds_read_b64 v[188:189], v174 offset:0
Mp 96,8,160
Rb 56,1,38912
Cv 58,60,61
Mp 104,8,168
Cv 59,62,63
ds_read_b64 v[190:191], v174 offset:32
Mp 104,12,184
Rb 60,1,40960
Mp 96,12,176
s_add_u32 s29, s73, 0x400
buffer_store_dwordx4 v[56:59], v1, s[20:23], s29 offen sc0 sc1
ds_read_b64 v[196:197], v174 offset:1024
Mp 96,16,192
Rb 0,1,41984
Mp 104,16,200
ds_read_b64 v[198:199], v174 offset:1056
Mp 104,20,216
Rb 4,1,43008
s_waitcnt lgkmcnt(12)
Mp 96,20,208
Mp 96,24,224
Rb 8,1,44032
Mp 104,24,232
Cv 64,64,65
Cv 65,66,67
Mp 104,28,248
Rb 12,1,45056
Cv 66,68,69
Mp 96,28,240
Cv 67,70,71
v_mfma_f32_16x16x32_bf16 v[100:103], a[32:35], v[128:131], 0
Rb 16,1,46080
v_mfma_f32_16x16x32_bf16 v[108:111], a[32:35], v[136:139], 0
s_add_u32 s29, s73, 0x800
buffer_store_dwordx4 v[64:67], v1, s[20:23], s29 offen sc0 sc1
Mp 108,36,152
Rb 20,1,47104
s_waitcnt vmcnt(3) lgkmcnt(11)
v_cmp_eq_u32_e32 vcc, s3, v4
s_andn2_b64 s[28:29], 1, vcc
s_cbranch_scc1 .Lzsp12
.Lzok12:
)" R"(
Mp 100,36,144
s_add_u32 s66, s66, 0x10000
buffer_load_dwordx4 a[96:99], v2, s[20:23], s66 offen offset:0 sc0 sc1
Mp 100,40,160
Rb 24,1,49152
v_sub_f32_e32 v172, 1.0, v167
Mp 108,40,168
Cv 172,172,172
v_lshlrev_b32 v172, 16, v172
Mp 108,44,184
Rb 28,1,50176
v_lshlrev_b32 v204, 16, v188
Mp 100,44,176
buffer_load_dwordx4 a[100:103], v2, s[20:23], s66 offen offset:1024 sc0 sc1
Mp 100,48,192
Rb 32,1,51200
Cv 72,72,73
Mp 108,48,200
Cv 73,74,75
Cv 74,76,77
Mp 108,52,216
Rb 36,1,52224
s_waitcnt lgkmcnt(12)
Mp 100,52,208
buffer_load_dwordx4 a[104:107], v2, s[20:23], s66 offen offset:2048 sc0 sc1
Mp 100,56,224
Rb 40,1,53248
Cv 75,78,79
Mp 108,56,232
s_add_u32 s29, s73, 0xc00
buffer_store_dwordx4 v[72:75], v1, s[20:23], s29 offen sc0 sc1
Mp 108,60,248
Rb 44,1,54272
v_and_b32 v205, 0xffff0000, v188
Mp 100,60,240
buffer_load_dwordx4 a[108:111], v2, s[20:23], s66 offen offset:3072 sc0 sc1
Mv 96,194,88
v_mul_f32_e32 v204, v172, v204
v_mul_f32_e32 v205, v172, v205
Mv 104,194,92
Cv 205,204,205
v_lshlrev_b32 v204, 16, v205
Mv 100,192,88
v_and_b32 v205, 0xffff0000, v205
s_waitcnt lgkmcnt(8)
Mv 108,192,92
buffer_load_dwordx4 a[112:115], v3, s[20:23], s66 offen offset:0 sc0 sc1
v_mfma_f32_16x16x32_bf16 v[112:115], a[0:3], v[128:131], 0
Rb 48,1,55296
Cv 80,80,81
v_mfma_f32_16x16x32_bf16 v[120:123], a[0:3], v[136:139], 0
Cv 81,82,83
Cv 82,84,85
Mp 120,4,152
Rb 52,1,57344
Cv 83,86,87
Mp 112,4,144
buffer_load_dwordx4 a[116:119], v3, s[20:23], s66 offen offset:1024 sc0 sc1
Mp 112,8,160
Rb 56,1,58368
v_lshlrev_b32 v206, 16, v189
Mp 120,8,168
s_add_u32 s29, s73, 0x1000
buffer_store_dwordx4 v[80:83], v1, s[20:23], s29 offen sc0 sc1
)" R"(
Mp 120,12,184
Rb 60,1,59392
s_waitcnt lgkmcnt(8)
Mp 112,12,176
v_and_b32 v207, 0xffff0000, v189
v_mul_f32_e32 v206, v172, v206
Mp 112,16,192
Rb 0,1,60416
v_mul_f32_e32 v207, v172, v207
Mp 120,16,200
Cv 207,206,207
v_lshlrev_b32 v206, 16, v207
Mp 120,20,216
Rb 4,1,61440
v_and_b32 v207, 0xffff0000, v207
Mp 112,20,208
Cv 88,88,89
Cv 89,90,91
Mp 112,24,224
Rb 8,1,62464
Cv 90,92,93
Mp 120,24,232
Cv 91,94,95
v_lshlrev_b32 v212, 16, v190
Mp 120,28,248
Rb 12,1,63488
s_waitcnt lgkmcnt(8)
Mp 112,28,240
s_add_u32 s29, s73, 0x1400
buffer_store_dwordx4 v[88:91], v1, s[20:23], s29 offen sc0 sc1
v_mfma_f32_16x16x32_bf16 v[116:119], a[32:35], v[128:131], 0
Rb 16,10,0
v_and_b32 v213, 0xffff0000, v190
v_mfma_f32_16x16x32_bf16 v[124:127], a[32:35], v[136:139], 0
v_mul_f32_e32 v212, v172, v212
v_mul_f32_e32 v213, v172, v213
Mp 124,36,152
Rb 20,10,1024
Cv 213,212,213
Mp 116,36,144
v_lshlrev_b32 v212, 16, v213
v_and_b32 v213, 0xffff0000, v213
Mp 116,40,160
Rb 24,10,2048
v_lshlrev_b32 v214, 16, v191
Mp 124,40,168
Cv 96,96,97
Cv 97,98,99
Mp 124,44,184
Rb 28,10,3072
s_waitcnt lgkmcnt(8)
Mp 116,44,176
Cv 98,100,101
Cv 99,102,103
Mp 116,48,192
Rb 32,10,4096
v_and_b32 v215, 0xffff0000, v191
Mp 124,48,200
s_add_u32 s29, s73, 0x1800
buffer_store_dwordx4 v[96:99], v1, s[20:23], s29 offen sc0 sc1
Mp 124,52,216
Rb 36,10,5120
v_mul_f32_e32 v214, v172, v214
Mp 116,52,208
)" R"(
v_mul_f32_e32 v215, v172, v215
Cv 215,214,215
Mp 116,56,224
Rb 40,10,6144
v_lshlrev_b32 v214, 16, v215
Mp 124,56,232
v_and_b32 v215, 0xffff0000, v215
v_lshlrev_b32 v220, 16, v196
Mp 124,60,248
Rb 44,10,8192
s_waitcnt vmcnt(8) lgkmcnt(10)
Mp 116,60,240
Cv 104,104,105
Cv 105,106,107
Mv 112,194,96
Cv 106,108,109
Cv 107,110,111
Mv 120,194,100
s_add_u32 s29, s73, 0x4000
buffer_store_dwordx4 v[104:107], v1, s[20:23], s29 offen sc0 sc1
Mv 116,192,96
v_and_b32 v221, 0xffff0000, v196
v_mul_f32_e32 v220, v172, v220
Mv 124,192,100
v_mul_f32_e32 v221, v172, v221
Cv 221,220,221
v_mfma_f32_16x16x32_bf16 v[32:35], a[0:3], v[128:131], 0
Rb 48,10,9216
v_lshlrev_b32 v220, 16, v221
v_mfma_f32_16x16x32_bf16 v[40:43], a[0:3], v[136:139], 0
v_and_b32 v221, 0xffff0000, v221
v_lshlrev_b32 v222, 16, v197
Mp 40,4,152
Rb 52,10,10240
s_waitcnt lgkmcnt(8)
Mp 32,4,144
Cv 112,112,113
Cv 113,114,115
Mp 32,8,160
Rb 56,10,11264
Cv 114,116,117
Mp 40,8,168
Cv 115,118,119
v_and_b32 v223, 0xffff0000, v197
Mp 40,12,184
Rb 60,10,12288
v_mul_f32_e32 v222, v172, v222
Mp 32,12,176
s_add_u32 s29, s73, 0x4400
buffer_store_dwordx4 v[112:115], v1, s[20:23], s29 offen sc0 sc1
Mp 32,16,192
Rb 0,10,13312
v_mul_f32_e32 v223, v172, v223
Mp 40,16,200
Cv 223,222,223
v_lshlrev_b32 v222, 16, v223
Mp 40,20,216
Rb 4,10,14336
s_waitcnt lgkmcnt(8)
Mp 32,20,208
v_and_b32 v223, 0xffff0000, v223
v_lshlrev_b32 v228, 16, v198
Mp 32,24,224
Rb 8,10,16384
)" R"(
v_and_b32 v229, 0xffff0000, v198
Mp 40,24,232
Cv 120,120,121
Cv 121,122,123
Mp 40,28,248
Rb 12,10,17408
Cv 122,124,125
Mp 32,28,240
Cv 123,126,127
v_mul_f32_e32 v228, v172, v228
v_mfma_f32_16x16x32_bf16 v[36:39], a[32:35], v[128:131], 0
Rb 16,10,18432
v_mul_f32_e32 v229, v172, v229
v_mfma_f32_16x16x32_bf16 v[44:47], a[32:35], v[136:139], 0
s_add_u32 s29, s73, 0x4800
buffer_store_dwordx4 v[120:123], v1, s[20:23], s29 offen sc0 sc1
Mp 44,36,152
Rb 20,10,19456
s_waitcnt lgkmcnt(8)
Mp 36,36,144
Cv 229,228,229
v_lshlrev_b32 v228, 16, v229
Mp 36,40,160
Rb 24,10,20480
v_and_b32 v229, 0xffff0000, v229
Mp 44,40,168
v_lshlrev_b32 v230, 16, v199
v_and_b32 v231, 0xffff0000, v199
Mp 44,44,184
Rb 28,10,21504
v_mul_f32_e32 v230, v172, v230
Mp 36,44,176
v_mul_f32_e32 v231, v172, v231
Cv 231,230,231
Mp 36,48,192
Rb 32,10,22528
v_lshlrev_b32 v230, 16, v231
Mp 44,48,200
v_and_b32 v231, 0xffff0000, v231
Mp 44,52,216
Rb 36,10,24576
s_waitcnt vmcnt(3) lgkmcnt(10)
buffer_store_dword v6, v7, s[20:23], s68 offen offset:0 sc0 sc1
Mp 36,52,208
Mp 36,56,224
Rb 40,10,25600
Mp 44,56,232
Mp 44,60,248
Rb 44,10,26624
Mp 36,60,240
Mv 32,194,104
Mv 40,194,108
Mv 36,192,104
s_waitcnt lgkmcnt(8)
Mv 44,192,108
v_mfma_f32_16x16x32_bf16 v[48:51], a[0:3], v[128:131], 0
Rb 48,10,27648
v_mfma_f32_16x16x32_bf16 v[56:59], a[0:3], v[136:139], 0
Mp 56,4,152
Rb 52,10,28672
Mp 48,4,144
Cv 32,32,33
Cv 33,34,35
Mp 48,8,160
)" R"(
Rb 56,10,29696
Cv 34,36,37
Mp 56,8,168
Cv 35,38,39
Mp 56,12,184
Rb 60,10,30720
s_waitcnt lgkmcnt(8)
Mp 48,12,176
s_add_u32 s29, s73, 0x4c00
buffer_store_dwordx4 v[32:35], v1, s[20:23], s29 offen sc0 sc1
Mp 48,16,192
Mp 56,16,200
Mp 56,20,216
Mp 48,20,208
Mp 48,24,224
Mp 56,24,232
Cv 40,40,41
Cv 41,42,43
buffer_store_dword v6, v7, s[20:23], s68 offen offset:128 sc0 sc1
Mp 56,28,248
Cv 42,44,45
s_waitcnt lgkmcnt(4)
Mp 48,28,240
Cv 43,46,47
v_mfma_f32_16x16x32_bf16 v[52:55], a[32:35], v[128:131], 0
s_add_u32 s29, s73, 0x5000
buffer_store_dwordx4 v[40:43], v1, s[20:23], s29 offen sc0 sc1
v_mfma_f32_16x16x32_bf16 v[60:63], a[32:35], v[136:139], 0
buffer_load_dword v4, v165, s[20:23], s40 offen sc0 sc1
Mp 60,36,152
Mp 52,36,144
Mp 52,40,160
Mp 60,40,168
Mp 60,44,184
s_waitcnt lgkmcnt(0)
Mp 52,44,176
Mp 52,48,192
Mp 60,48,200
Mp 60,52,216
Mp 52,52,208
Mp 52,56,224
Mp 60,56,232
Mp 60,60,248
Mp 52,60,240
Mv 48,194,112
Mv 56,194,116
Mv 52,192,112
Mv 60,192,116
s_nop 7
Cv 48,48,49
Cv 49,50,51
Cv 50,52,53
Cv 51,54,55
s_add_u32 s29, s73, 0x5400
buffer_store_dwordx4 v[48:51], v1, s[20:23], s29 offen sc0 sc1
Cv 56,56,57
Cv 57,58,59
Cv 58,60,61
Cv 59,62,63
s_add_u32 s29, s73, 0x5800
buffer_store_dwordx4 v[56:59], v1, s[20:23], s29 offen sc0 sc1
s_branch .LzEND
.LzCEND:
)" : "+{s[0:1]}"(ka), "+{s2}"(bid), "+{v0}"(tid) :
   : "memory", "scc", "vcc", "v1", "v2", "v3", "v4", "v5", "v6", "v7", "v8", "v9", R256("v"), V10("a"), R256("a"),
     "s3", "s4", "s5", "s6", "s7", "s8", "s9", V10("s1"), V10("s2"), "s30", "s31", "s33", "s34", "s35", "s36", "s37", "s38", "s39",
     V10("s4"), V10("s5"), V10("s6"), V10("s7"), V10("s8"), V10("s9"));
}

extern "C" void kernel_launch(void* const* d_in, const int* in_sizes, int n_in, void* d_out, int out_size, void* d_ws, size_t ws_size, hipStream_t stream) {
  (void)hipFuncSetAttribute((const void*)k_main, hipFuncAttributeMaxDynamicSharedMemorySize, 163840);
  k_main<<<256, 256, 163840, stream>>>((const u16*)d_in[0], (const u16*)d_in[1], (const u16*)d_in[2], (const u16*)d_in[3], (const u16*)d_in[4],
      (char*)d_ws, (const u16*)d_in[11], (const u16*)d_in[7], (const u16*)d_in[5], (const u16*)d_in[8], (const u16*)d_in[9], (const u16*)d_in[10], (u16*)d_out);
}
```

```cpp
#include <hip/hip_runtime.h>

typedef unsigned short u16;
#define V10(p) p "0", p "1", p "2", p "3", p "4", p "5", p "6", p "7", p "8", p "9"
#define V100(p) V10(p "0"), V10(p "1"), V10(p "2"), V10(p "3"), V10(p "4"), V10(p "5"), V10(p "6"), V10(p "7"), V10(p "8"), V10(p "9")
#define R256(p) V10(p "1"), V10(p "2"), V10(p "3"), V10(p "4"), V10(p "5"), V10(p "6"), V10(p "7"), V10(p "8"), V10(p "9"), V100(p "1"), \
                V10(p "20"), V10(p "21"), V10(p "22"), V10(p "23"), V10(p "24"), p "250", p "251", p "252", p "253", p "254", p "255"

extern "C" __global__ __launch_bounds__(256, 1) __attribute__((aligned(4096)))
void k_main(const u16* x, const u16* ckv, const u16* wq, const u16* wuk, const u16* wuv, char* ws,
            const u16* gate_w, const u16* w_r, const u16* wo, const u16* wg, const u16* wu, const u16* wd, u16* out) {
  typedef const __attribute__((address_space(4))) char* karg_t;
  karg_t ka = (karg_t)__builtin_amdgcn_kernarg_segment_ptr();
  unsigned bid = blockIdx.x, tid = threadIdx.x;
  asm volatile(R"(
.macro Mv d,a,b
v_mfma_f32_16x16x32_bf16 v[\d:\d+3], a[\a:\a+3], a[\b:\b+3], v[\d:\d+3]
.endm
.macro Mp d,a,b
v_mfma_f32_16x16x32_bf16 v[\d:\d+3], a[\a:\a+3], v[\b:\b+3], v[\d:\d+3]
.endm
.macro Ma d,a,b
v_mfma_f32_16x16x32_bf16 a[\d:\d+3], a[\a:\a+3], a[\b:\b+3], a[\d:\d+3]
.endm
.macro Mz d,a,b
v_mfma_f32_16x16x32_bf16 v[\d:\d+3], a[\a:\a+3], a[\b:\b+3], 0
.endm
.macro Rb d,v,o
ds_read_b128 a[\d:\d+3], v\v offset:\o
.endm
.macro Rt d,v,o
ds_read_b64_tr_b16 a[\d:\d+1], v\v offset:\o
.endm
.macro Cv d,a,b
v_cvt_pk_bf16_f32 v\d, v\a, v\b
.endm
.LzSTART:
s_load_dwordx16 s[64:79], s[0:1], 0
s_load_dwordx2 s[80:81], s[0:1], 64
v_lshrrev_b32 v1, 6, v0
v_and_b32 v0, 63, v0
v_readfirstlane_b32 s33, v1
v_lshlrev_b32 v1, 4, v0
s_getpc_b64 s[24:25]
.Lzpc0:
s_sub_u32 s24, s24, .Lzpc0-.LzSTART
s_subb_u32 s25, s25, 0
s_mov_b32 s26, .LzCEND-.LzSTART
s_mov_b32 s27, 0x20000
s_lshr_b32 s28, s2, 3
s_lshl2_add_u32 s28, s28, s33
s_lshl_b32 s28, s28, 11
v_lshrrev_b32 v5, 4, v0
v_min_u32 v5, 1, v5
v_lshlrev_b32 v5, 27, v5
v_add_lshl_u32 v5, v5, v1, 3
buffer_load_dword v5, v5, s[24:27], s28 offen
s_and_b32 s28, s2, 0x17
s_bfe_u32 s29, s2, 0x10005
s_lshl3_add_u32 s28, s29, s28
s_lshr_b32 s29, s2, 6
s_lshl_b32 s29, s29, 5
s_or_b32 s28, s28, s29
s_and_b32 s29, s2, 8
s_lshl4_add_u32 s2, s29, s28
s_and_b32 s28, s2, 7
s_bfe_u32 s29, s2, 0x10003
s_lshl1_add_u32 s34, s28, s29
s_bfe_u32 s35, s2, 0x30004
s_bfe_u32 s91, s2, 0x10004
s_bfe_u32 s93, s2, 0x20005
s_lshl_b32 s36, s33, 13
s_mov_b32 s95, 40
s_bitcmp1_b32 s2, 7
s_cbranch_scc0 .LzA0
.LzB0:
s_mov_b32 s54, 0x3db8aa3b
s_mul_i32 s41, s33, 7168
s_mul_i32 s42, s33, 0x70000
s_lshl_b32 s28, s34, 9
s_add_u32 s42, s42, s28
v_lshrrev_b32 v28, 3, v0
v_and_b32 v29, 0x70, v1
v_lshl_or_b32 v28, v28, 13, v29
v_lshlrev_b32 v29, 1, v0
v_and_b32 v29, 32, v29
v_xor_b32 v28, v28, v29
s_and_b32 s28, s33, 1
s_lshl_b32 s28, s28, 6
v_xor_b32 v240, s28, v28
v_xor_b32 v241, 64, v240
v_add_u32 v241, 0xfc00, v241
v_add_u32 v242, 0x1f800, v240
v_add_u32 v243, 0x1f800, v241
s_sub_u32 s28, 15, s35
s_lshl2_add_u32 s28, s28, s33
s_mul_i32 s43, s28, 28672
v_and_b32 v2, 0x70, v1
v_and_b32 v28, 48, v0
)" R"(
v_xor_b32 v2, v2, v28
v_lshrrev_b32 v28, 3, v0
v_mul_u32_u24 v28, 896, v28
v_add_u32 v2, v2, v28
v_xor_b32 v3, 64, v2
v_add_u32 v3, 6144, v3
v_bfe_u32 v26, v0, 1, 3
v_lshrrev_b32 v28, 4, v0
v_xor_b32 v26, v26, v28
v_lshlrev_b32 v26, 4, v26
v_and_b32 v28, 15, v0
v_lshl_or_b32 v26, v28, 7, v26
s_add_u32 s28, s36, 0x0
v_add_u32 v26, s28, v26
v_xor_b32 v27, 64, v26
s_waitcnt lgkmcnt(0)
s_mov_b32 s16, s68
s_mov_b32 s17, s69
s_mov_b32 s18, 0x380000
s_mov_b32 s19, 0x20000
s_mov_b32 s12, s64
s_mov_b32 s13, s65
s_mov_b32 s14, 0x1c0000
s_mov_b32 s15, 0x20000
s_add_u32 m0, s36, 0
s_add_u32 s44, s43, 0
buffer_load_dwordx4 v2, s[12:15], s44 offen offset:0 lds
buffer_load_dwordx4 v3, s[12:15], s44 offen offset:1024 lds
s_add_u32 s44, s43, 12288
buffer_load_dwordx4 v2, s[12:15], s44 offen offset:2048 lds
buffer_load_dwordx4 v3, s[12:15], s44 offen offset:3072 lds
s_add_u32 m0, s41, 65536
s_add_u32 s92, s42, 0x0
buffer_load_dwordx4 v240, s[16:19], s92 offen offset:0 lds
buffer_load_dwordx4 v241, s[16:19], s92 offen offset:1024 lds
buffer_load_dwordx4 v242, s[16:19], s92 offen offset:2048 lds
buffer_load_dwordx4 v243, s[16:19], s92 offen offset:3072 lds
s_add_u32 m0, s41, 69632
s_add_u32 s92, s42, 0x40000
buffer_load_dwordx4 v240, s[16:19], s92 offen offset:0 lds
buffer_load_dwordx4 v241, s[16:19], s92 offen offset:1024 lds
buffer_load_dwordx4 v242, s[16:19], s92 offen offset:2048 lds
s_add_u32 m0, s36, 4096
s_add_u32 s44, s43, 128
buffer_load_dwordx4 v2, s[12:15], s44 offen offset:0 lds
buffer_load_dwordx4 v3, s[12:15], s44 offen offset:1024 lds
s_add_u32 s44, s43, 12416
buffer_load_dwordx4 v2, s[12:15], s44 offen offset:2048 lds
buffer_load_dwordx4 v3, s[12:15], s44 offen offset:3072 lds
s_add_u32 m0, s36, 32768
s_add_u32 s44, s43, 256
buffer_load_dwordx4 v2, s[12:15], s44 offen offset:0 lds
buffer_load_dwordx4 v3, s[12:15], s44 offen offset:1024 lds
s_add_u32 s44, s43, 12544
buffer_load_dwordx4 v2, s[12:15], s44 offen offset:2048 lds
buffer_load_dwordx4 v3, s[12:15], s44 offen offset:3072 lds
s_add_u32 m0, s36, 36864
s_add_u32 s44, s43, 384
buffer_load_dwordx4 v2, s[12:15], s44 offen offset:0 lds
buffer_load_dwordx4 v3, s[12:15], s44 offen offset:1024 lds
s_add_u32 s44, s43, 12672
buffer_load_dwordx4 v2, s[12:15], s44 offen offset:2048 lds
buffer_load_dwordx4 v3, s[12:15], s44 offen offset:3072 lds
s_mov_b32 s28, 0xc108400
)" R"(
s_load_dword s3, s[74:75], s28
s_mov_b32 s20, s74
s_mov_b32 s21, s75
s_mov_b32 s22, 0xc1ba000
s_mov_b32 s23, 0x20000
s_mov_b32 s4, s66
s_mov_b32 s5, s67
s_mov_b32 s6, 0x400000
s_mov_b32 s7, 0x20000
s_cmp_eq_u32 s91, 1
s_cselect_b32 s8, s72, s70
s_cselect_b32 s9, s73, s71
s_mov_b32 s10, 0x800000
s_mov_b32 s11, 0x20000
s_load_dwordx2 s[74:75], s[0:1], 96
s_lshl3_add_u32 s28, s33, 64
s_load_dwordx2 s[70:71], s[0:1], s28
v_bfe_u32 v244, v0, 3, 2
v_lshlrev_b32 v244, 5, v244
v_and_b32 v28, 3, v0
v_lshl_or_b32 v244, v28, 3, v244
v_bfe_u32 v28, v0, 2, 2
v_lshl_or_b32 v244, v28, 7, v244
v_lshrrev_b32 v28, 4, v0
v_lshl_or_b32 v244, v28, 10, v244
v_xor_b32 v245, 32, v244
v_xor_b32 v246, 64, v244
v_xor_b32 v247, 96, v244
v_add_u32 v252, 0x20000, v244
v_add_u32 v253, 0x20000, v245
v_add_u32 v254, 0x20000, v246
v_add_u32 v255, 0x20000, v247
v_add_u32 v248, 0x10000, v244
v_add_u32 v249, 0x10000, v245
v_add_u32 v250, 0x10000, v246
v_add_u32 v251, 0x10000, v247
s_waitcnt vmcnt(8) lgkmcnt(0)
s_barrier
ds_read_b128 v[128:131], v26 offset:0
ds_read_b128 v[132:135], v26 offset:2048
ds_read_b128 v[136:139], v27 offset:0
ds_read_b128 v[140:143], v27 offset:2048
ds_read_b128 v[144:147], v26 offset:4096
ds_read_b128 v[148:151], v26 offset:6144
ds_read_b128 v[152:155], v27 offset:4096
ds_read_b128 v[156:159], v27 offset:6144
Rt 144,248,0
Rt 146,248,512
Rt 148,249,0
Rt 150,249,512
Rt 152,250,0
Rt 154,250,512
Rt 156,251,0
Rt 158,251,512
Rt 160,248,4096
Rt 162,248,4608
Rt 164,249,4096
Rt 166,249,4608
Rt 168,250,4096
Rt 170,250,4608
Rt 172,251,4096
Rt 174,251,4608
s_add_u32 m0, s41, 98304
s_add_u32 s92, s42, 0x80
buffer_load_dwordx4 v240, s[16:19], s92 offen offset:0 lds
)" R"(
buffer_load_dwordx4 v241, s[16:19], s92 offen offset:1024 lds
buffer_load_dwordx4 v242, s[16:19], s92 offen offset:2048 lds
buffer_load_dwordx4 v243, s[16:19], s92 offen offset:3072 lds
s_add_u32 m0, s41, 102400
s_add_u32 s92, s42, 0x40080
buffer_load_dwordx4 v240, s[16:19], s92 offen offset:0 lds
buffer_load_dwordx4 v241, s[16:19], s92 offen offset:1024 lds
buffer_load_dwordx4 v242, s[16:19], s92 offen offset:2048 lds
s_mul_i32 s3, s3, 1664525
s_add_u32 s3, s3, 1013904223
v_mov_b32 v6, s3
v_min_u32 v7, 1, v0
v_lshlrev_b32 v7, 30, v7
s_waitcnt lgkmcnt(8)
s_barrier
v_mfma_f32_16x16x32_bf16 v[32:35], a[144:147], v[128:131], 0
Rt 128,248,8192
s_add_u32 m0, s41, 131072
s_add_u32 s92, s42, 0x1c0000
v_mfma_f32_16x16x32_bf16 v[40:43], a[144:147], v[132:135], 0
Rt 130,248,8704
buffer_load_dwordx4 v240, s[16:19], s92 offen offset:0 lds
v_mfma_f32_16x16x32_bf16 v[44:47], a[148:151], v[132:135], 0
Rt 132,249,8192
v_mfma_f32_16x16x32_bf16 v[36:39], a[148:151], v[128:131], 0
Rt 134,249,8704
v_mfma_f32_16x16x32_bf16 v[48:51], a[152:155], v[128:131], 0
Rt 136,250,8192
v_mfma_f32_16x16x32_bf16 v[56:59], a[152:155], v[132:135], 0
Rt 138,250,8704
buffer_load_dwordx4 v241, s[16:19], s92 offen offset:1024 lds
v_mfma_f32_16x16x32_bf16 v[60:63], a[156:159], v[132:135], 0
Rt 140,251,8192
v_mfma_f32_16x16x32_bf16 v[52:55], a[156:159], v[128:131], 0
Rt 142,251,8704
s_waitcnt vmcnt(13) lgkmcnt(8)
Mp 32,160,136
Rt 144,248,12288
ds_read_b128 v[160:163], v26 offset:32768
Mp 40,160,140
Rt 146,248,12800
buffer_load_dwordx4 v242, s[16:19], s92 offen offset:2048 lds
Mp 44,164,140
Rt 148,249,12288
ds_read_b128 v[164:167], v26 offset:34816
Mp 36,164,136
Rt 150,249,12800
Mp 48,168,136
Rt 152,250,12288
ds_read_b128 v[168:171], v27 offset:32768
Mp 56,168,140
Rt 154,250,12800
buffer_load_dwordx4 v243, s[16:19], s92 offen offset:3072 lds
Mp 60,172,140
Rt 156,251,12288
ds_read_b128 v[172:175], v27 offset:34816
Mp 52,172,136
Rt 158,251,12800
s_waitcnt vmcnt(11) lgkmcnt(12)
Mp 32,128,144
Rt 160,248,16384
ds_read_b128 v[176:179], v26 offset:36864
s_add_u32 m0, s41, 135168
s_add_u32 s92, s42, 0x200000
Mp 40,128,148
)" R"(
Rt 162,248,16896
buffer_load_dwordx4 v240, s[16:19], s92 offen offset:0 lds
Mp 44,132,148
Rt 164,249,16384
ds_read_b128 v[180:183], v26 offset:38912
Mp 36,132,144
Rt 166,249,16896
Mp 48,136,144
Rt 168,250,16384
ds_read_b128 v[184:187], v27 offset:36864
Mp 56,136,148
Rt 170,250,16896
buffer_load_dwordx4 v241, s[16:19], s92 offen offset:1024 lds
Mp 60,140,148
Rt 172,251,16384
ds_read_b128 v[188:191], v27 offset:38912
Mp 52,140,144
Rt 174,251,16896
s_waitcnt lgkmcnt(12)
Mp 32,144,152
Rt 128,248,20480
Mp 40,144,156
Rt 130,248,20992
buffer_load_dwordx4 v242, s[16:19], s92 offen offset:2048 lds
Mp 44,148,156
Rt 132,249,20480
Mp 36,148,152
Rt 134,249,20992
Mp 48,152,152
Rt 136,250,20480
Mp 56,152,156
Rt 138,250,20992
Mp 60,156,156
Rt 140,251,20480
s_add_u32 s44, s43, 512
Mp 52,156,152
Rt 142,251,20992
s_waitcnt lgkmcnt(8)
Mp 32,160,160
Rt 144,248,24576
s_add_u32 m0, s36, 32768
Mp 40,160,164
Rt 146,248,25088
buffer_load_dwordx4 v2, s[12:15], s44 offen offset:0 lds
Mp 44,164,164
Rt 148,249,24576
Mp 36,164,160
Rt 150,249,25088
Mp 48,168,160
Rt 152,250,24576
Mp 56,168,164
Rt 154,250,25088
buffer_load_dwordx4 v3, s[12:15], s44 offen offset:1024 lds
Mp 60,172,164
Rt 156,251,24576
Mp 52,172,160
Rt 158,251,25088
s_call_b64 s[30:31], .LzHOP
s_waitcnt vmcnt(9) lgkmcnt(8)
s_barrier
Mp 32,128,168
Rt 160,248,32768
s_mov_b32 s63, 0xe0c0a08
s_mov_b32 s28, 0x100d0b09
Mp 40,128,172
)" R"(
Rt 162,248,33280
s_add_u32 s44, s43, 12800
buffer_load_dwordx4 v2, s[12:15], s44 offen offset:2048 lds
Mp 44,132,172
Rt 164,249,32768
Mp 36,132,168
Rt 166,249,33280
Mp 48,136,168
Rt 168,250,32768
s_bitcmp1_b32 s2, 5
s_cmov_b32 s63, 0xf1e1c1a
s_cmov_b32 s28, 0x111f1d1b
Mp 56,136,172
Rt 170,250,33280
buffer_load_dwordx4 v3, s[12:15], s44 offen offset:3072 lds
Mp 60,140,172
Rt 172,251,32768
s_add_u32 s44, s43, 640
Mp 52,140,168
Rt 174,251,33280
s_waitcnt lgkmcnt(8)
s_barrier
Mp 32,144,176
Rt 128,248,36864
s_add_u32 m0, s36, 36864
Mp 40,144,180
Rt 130,248,37376
buffer_load_dwordx4 v2, s[12:15], s44 offen offset:0 lds
Mp 44,148,180
Rt 132,249,36864
s_bitcmp1_b32 s2, 6
s_cmov_b32 s63, s28
Mp 36,148,176
Rt 134,249,37376
s_lshl_b32 s28, s33, 3
s_lshr_b32 s28, s63, s28
Mp 48,152,176
Rt 136,250,36864
s_and_b32 s28, s28, 255
s_lshl_b32 s38, s28, 17
s_add_u32 s38, s38, 0xff80
Mp 56,152,180
Rt 138,250,37376
buffer_load_dwordx4 v3, s[12:15], s44 offen offset:1024 lds
Mp 60,156,180
Rt 140,251,36864
s_lshl_b32 s39, s33, 16
s_lshl_b32 s28, s34, 9
Mp 52,156,176
Rt 142,251,37376
s_add_u32 s39, s39, s28
s_waitcnt lgkmcnt(8)
v_mfma_f32_16x16x32_bf16 v[64:67], a[160:163], v[128:131], 0
Rt 144,248,40960
s_sub_u32 s39, s39, 0x40000
s_lshl_b32 s37, s33, 12
v_mfma_f32_16x16x32_bf16 v[72:75], a[160:163], v[132:135], 0
Rt 146,248,41472
s_add_u32 s44, s43, 12928
buffer_load_dwordx4 v2, s[12:15], s44 offen offset:2048 lds
v_mfma_f32_16x16x32_bf16 v[76:79], a[164:167], v[132:135], 0
Rt 148,249,40960
s_add_u32 s37, s37, 0x10000
s_lshl_b32 s28, s34, 21
v_mfma_f32_16x16x32_bf16 v[68:71], a[164:167], v[128:131], 0
)" R"(
Rt 150,249,41472
s_lshl_b32 s29, s91, 25
s_add_u32 s28, s28, s29
v_mfma_f32_16x16x32_bf16 v[80:83], a[168:171], v[128:131], 0
Rt 152,250,40960
s_bfe_u32 s29, s63, 0x80000
s_lshl_b32 s29, s29, 16
s_add_u32 s45, s28, s29
v_mfma_f32_16x16x32_bf16 v[88:91], a[168:171], v[132:135], 0
Rt 154,250,41472
buffer_load_dwordx4 v3, s[12:15], s44 offen offset:3072 lds
v_mfma_f32_16x16x32_bf16 v[92:95], a[172:175], v[132:135], 0
Rt 156,251,40960
s_add_u32 s92, s42, 0x1c0080
s_bfe_u32 s29, s63, 0x80008
v_mfma_f32_16x16x32_bf16 v[84:87], a[172:175], v[128:131], 0
Rt 158,251,41472
s_lshl_b32 s29, s29, 16
s_waitcnt lgkmcnt(8)
Mp 64,128,136
Rt 160,248,45056
s_add_u32 m0, s41, 0
s_add_u32 s46, s28, s29
Mp 72,128,140
Rt 162,248,45568
buffer_load_dwordx4 v240, s[16:19], s92 offen offset:0 lds
s_bfe_u32 s29, s63, 0x80010
Mp 76,132,140
Rt 164,249,45056
s_lshl_b32 s29, s29, 16
s_add_u32 s47, s28, s29
Mp 68,132,136
Rt 166,249,45568
s_bfe_u32 s29, s63, 0x80018
s_lshl_b32 s29, s29, 16
Mp 80,136,136
Rt 168,250,45056
s_add_u32 s48, s28, s29
s_lshl_b32 s50, s34, 21
s_add_u32 s50, s50, 0x6140000
Mp 88,136,140
Rt 170,250,45568
buffer_load_dwordx4 v241, s[16:19], s92 offen offset:1024 lds
Mp 92,140,140
Rt 172,251,45056
s_lshl2_add_u32 s28, s91, s93
s_lshl_b32 s28, s28, 7
Mp 84,140,136
Rt 174,251,45568
s_add_u32 s49, s28, s50
s_waitcnt lgkmcnt(8)
Mp 64,144,144
Rt 128,248,49152
s_add_u32 s49, s49, 4096
s_lshl_b32 s52, s35, 7
Mp 72,144,148
Rt 130,248,49664
buffer_load_dwordx4 v242, s[16:19], s92 offen offset:2048 lds
s_add_u32 s52, s52, s50
Mp 76,148,148
Rt 132,249,49152
s_add_u32 s52, s52, 0xd000
s_lshl4_add_u32 s28, s34, s35
Mp 68,148,144
)" R"(
Rt 134,249,49664
s_lshl_b32 s51, s28, 17
s_add_u32 s51, s51, 0x4000000
Mp 80,152,144
Rt 136,250,49152
Mp 88,152,148
Rt 138,250,49664
buffer_load_dwordx4 v243, s[16:19], s92 offen offset:3072 lds
Mp 92,156,148
Rt 140,251,49152
s_add_u32 s92, s42, 0x200080
Mp 84,156,144
Rt 142,251,49664
s_waitcnt lgkmcnt(8)
Mp 64,160,152
Rt 144,248,53248
s_add_u32 m0, s41, 4096
Mp 72,160,156
Rt 146,248,53760
buffer_load_dwordx4 v240, s[16:19], s92 offen offset:0 lds
Mp 76,164,156
Rt 148,249,53248
Mp 68,164,152
Rt 150,249,53760
Mp 80,168,152
Rt 152,250,53248
Mp 88,168,156
Rt 154,250,53760
buffer_load_dwordx4 v241, s[16:19], s92 offen offset:1024 lds
Mp 92,172,156
Rt 156,251,53248
s_cmp_lg_u32 s33, 0
s_cmov_b32 s49, 0x40000000
s_cmov_b32 s52, 0x40000000
Mp 84,172,152
Rt 158,251,53760
s_mov_b32 s53, 0x40000
s_waitcnt vmcnt(10) lgkmcnt(8)
Mp 64,128,160
Rt 160,248,57344
ds_read_b128 v[192:195], v26 offset:32768
s_lshl_b32 s89, s34, 21
Mp 72,128,164
Rt 162,248,57856
buffer_load_dwordx4 v242, s[16:19], s92 offen offset:2048 lds
s_mov_b32 s62, 0x4e800000
Mp 76,132,164
Rt 164,249,57344
ds_read_b128 v[196:199], v26 offset:34816
s_add_u32 s67, s89, 0x6000000
Mp 68,132,160
Rt 166,249,57856
s_lshl3_add_u32 s28, s34, s35
s_add_u32 s64, s51, 0x100000
s_lshl2_add_u32 s28, s28, s33
Mp 80,136,160
Rt 168,250,57344
ds_read_b128 v[200:203], v27 offset:32768
s_lshl2_add_u32 s65, s35, s33
Mp 88,136,164
Rt 170,250,57856
s_lshl_b32 s65, s65, 7
s_add_u32 s65, s65, s50
Mp 92,140,164
)" R"(
Rt 172,251,57344
ds_read_b128 v[204:207], v27 offset:34816
s_add_u32 s65, s65, 0xc000
s_lshl_b32 s66, s28, 10
Mp 84,140,160
Rt 174,251,57856
s_waitcnt vmcnt(7) lgkmcnt(12)
s_barrier
Mp 64,144,168
Rt 128,252,0
ds_read_b128 v[208:211], v26 offset:36864
s_add_u32 s66, s66, 0xc000000
Mp 72,144,172
Rt 130,252,512
s_sub_u32 s28, 15, s35
s_lshl2_add_u32 s28, s28, s33
Mp 76,148,172
Rt 132,253,0
ds_read_b128 v[212:215], v26 offset:38912
s_lshl_b32 s29, s28, 5
Mp 68,148,168
Rt 134,253,512
s_add_u32 s29, s29, s34
s_lshl_b32 s73, s29, 15
s_add_u32 s73, s73, 0x8000000
Mp 80,152,168
Rt 136,254,0
ds_read_b128 v[216:219], v27 offset:36864
s_lshr_b32 s29, s36, 1
Mp 88,152,172
Rt 138,254,512
s_add_u32 s44, s43, 768
s_and_b32 s29, s29, 0x2000
Mp 92,156,172
Rt 140,255,0
ds_read_b128 v[220:223], v27 offset:38912
s_add_u32 s73, s73, s29
s_lshl4_add_u32 s28, s28, s34
Mp 84,156,168
Rt 142,255,512
s_waitcnt lgkmcnt(12)
s_barrier
Mp 64,160,176
Rt 144,252,4096
s_add_u32 m0, s36, 32768
s_lshl_b32 s68, s28, 8
Mp 72,160,180
Rt 146,252,4608
buffer_load_dwordx4 v2, s[12:15], s44 offen offset:0 lds
Mp 76,164,180
Rt 148,253,4096
s_add_u32 s68, s68, 0xc110000
s_xor_b32 s72, s73, 0x3c04000
Mp 68,164,176
Rt 150,253,4608
s_mov_b32 s55, s89
s_mov_b32 s57, 18
s_mov_b32 s59, s67
Mp 80,168,176
Rt 152,254,4096
buffer_load_dwordx4 v3, s[12:15], s44 offen offset:1024 lds
Mp 88,168,180
Rt 154,254,4608
s_mov_b32 s58, 0
Mp 92,172,180
Rt 156,255,4096
)" R"(
s_cmp_eq_u32 s35, 7
s_cmov_b32 s58, 2
Mp 84,172,176
Rt 158,255,4608
s_add_u32 s44, s43, 13056
buffer_load_dwordx4 v2, s[12:15], s44 offen offset:2048 lds
s_waitcnt lgkmcnt(8)
Mp 32,128,184
Rt 160,252,8192
s_cmp_eq_u32 s35, 7
s_cmov_b32 s64, 0x40000000
.LzH0:
s_cbranch_execz .LzH13
Mp 40,128,188
Rt 162,252,8704
s_cmp_eq_u32 s35, 7
s_cmov_b32 s66, 0x40000000
Mp 44,132,188
Rt 164,253,8192
buffer_load_dwordx4 v3, s[12:15], s44 offen offset:3072 lds
Mp 36,132,184
Rt 166,253,8704
s_add_u32 s56, s55, 0x2000000
v_lshrrev_b32 v28, 4, v0
v_and_b32 v29, 7, v0
Mp 48,136,184
Rt 168,254,8192
v_xor_b32 v28, v28, v29
v_lshlrev_b32 v28, 4, v28
Mp 56,136,188
Rt 170,254,8704
s_add_u32 s92, s42, 0x100
v_lshrrev_b32 v29, 3, v0
Mp 60,140,188
Rt 172,255,8192
s_add_u32 m0, s41, 65536
v_lshl_or_b32 v8, v29, 11, v28
Mp 52,140,184
Rt 174,255,8704
buffer_load_dwordx4 v240, s[16:19], s92 offen offset:0 lds
s_waitcnt lgkmcnt(8)
Mp 32,144,192
Rt 128,252,12288
v_xor_b32 v9, 64, v8
v_add_u32 v9, 15360, v9
v_add_u32 v10, 30720, v8
Mp 40,144,196
Rt 130,252,12800
v_add_u32 v11, 30720, v9
s_and_b32 s28, s33, 1
Mp 44,148,196
Rt 132,253,12288
s_lshl_b32 s28, s28, 7
v_lshrrev_b32 v29, 5, v0
Mp 36,148,192
Rt 134,253,12800
buffer_load_dwordx4 v241, s[16:19], s92 offen offset:1024 lds
v_mul_u32_u24 v29, 0x2220, v29
Mp 48,152,192
Rt 136,254,12288
v_xor_b32 v29, v29, v1
v_xor_b32 v12, s28, v29
Mp 56,152,196
Rt 138,254,12800
)" R"(
v_xor_b32 v13, 64, v12
v_add_u32 v13, 15360, v13
Mp 60,156,196
Rt 140,255,12288
v_add_u32 v14, 30720, v12
v_add_u32 v15, 30720, v13
Mp 52,156,192
Rt 142,255,12800
buffer_load_dwordx4 v242, s[16:19], s92 offen offset:2048 lds
s_waitcnt lgkmcnt(8)
Mp 32,160,200
Rt 144,252,16384
v_bfe_u32 v28, v0, 1, 3
v_lshrrev_b32 v29, 4, v0
v_xor_b32 v28, v28, v29
Mp 40,160,204
Rt 146,252,16896
v_lshlrev_b32 v28, 4, v28
v_and_b32 v29, 15, v0
Mp 44,164,204
Rt 148,253,16384
v_lshl_or_b32 v28, v29, 7, v28
s_lshr_b32 s28, s33, 1
Mp 36,164,200
Rt 150,253,16896
buffer_load_dwordx4 v243, s[16:19], s92 offen offset:3072 lds
Mp 48,168,200
Rt 152,254,16384
s_lshl_b32 s28, s28, 12
v_or_b32 v16, s28, v28
Mp 56,168,204
Rt 154,254,16896
s_add_u32 s92, s42, 0x40100
v_xor_b32 v17, 64, v16
v_bfe_u32 v28, v0, 2, 3
Mp 60,172,204
Rt 156,255,16384
s_add_u32 m0, s41, 69632
v_lshlrev_b32 v28, 5, v28
Mp 52,172,200
Rt 158,255,16896
buffer_load_dwordx4 v240, s[16:19], s92 offen offset:0 lds
s_waitcnt vmcnt(5) lgkmcnt(8)
Mp 32,128,208
Rt 160,252,20480
ds_read_b128 v[224:227], v26 offset:32768
v_and_b32 v29, 3, v0
v_lshl_or_b32 v28, v29, 3, v28
Mp 40,128,212
Rt 162,252,20992
v_bfe_u32 v29, v0, 2, 2
v_lshl_or_b32 v28, v29, 9, v28
Mp 44,132,212
Rt 164,253,20480
ds_read_b128 v[228:231], v26 offset:34816
v_lshrrev_b32 v29, 4, v0
Mp 36,132,208
Rt 166,253,20992
buffer_load_dwordx4 v241, s[16:19], s92 offen offset:1024 lds
Mp 48,136,208
Rt 168,254,20480
ds_read_b128 v[232:235], v27 offset:32768
v_lshl_or_b32 v28, v29, 12, v28
Mp 56,136,212
)" R"(
Rt 170,254,20992
s_and_b32 s28, s33, 1
s_lshl_b32 s28, s28, 8
s_bitset1_b32 s28, 16
Mp 60,140,212
Rt 172,255,20480
ds_read_b128 v[236:239], v27 offset:34816
v_or_b32 v18, s28, v28
Mp 52,140,208
Rt 174,255,20992
buffer_load_dwordx4 v242, s[16:19], s92 offen offset:2048 lds
s_waitcnt lgkmcnt(12)
Mp 32,144,216
Rt 128,252,24576
v_xor_b32 v19, 32, v18
v_xor_b32 v20, 64, v18
Mp 40,144,220
Rt 130,252,25088
s_add_u32 s92, s42, 0x180
v_xor_b32 v21, 96, v18
Mp 44,148,220
Rt 132,253,24576
s_add_u32 m0, s41, 98304
v_xor_b32 v22, 128, v18
v_xor_b32 v23, 160, v18
Mp 36,148,216
Rt 134,253,25088
buffer_load_dwordx4 v240, s[16:19], s92 offen offset:0 lds
Mp 48,152,216
Rt 136,254,24576
v_xor_b32 v24, 192, v18
v_xor_b32 v25, 224, v18
Mp 56,152,220
Rt 138,254,25088
Mp 60,156,220
Rt 140,255,24576
Mp 52,156,216
Rt 142,255,25088
buffer_load_dwordx4 v241, s[16:19], s92 offen offset:1024 lds
s_waitcnt lgkmcnt(8)
s_barrier
Mp 32,160,224
Rt 144,244,0
Mp 40,160,228
Rt 146,244,512
Mp 44,164,228
Rt 148,245,0
Mp 36,164,224
Rt 150,245,512
buffer_load_dwordx4 v242, s[16:19], s92 offen offset:2048 lds
Mp 48,168,224
Rt 152,246,0
Mp 56,168,228
Rt 154,246,512
Mp 60,172,228
Rt 156,247,0
Mp 52,172,224
Rt 158,247,512
buffer_load_dwordx4 v243, s[16:19], s92 offen offset:3072 lds
s_waitcnt lgkmcnt(8)
s_barrier
Mp 32,128,232
Rt 160,244,4096
Mp 40,128,236
Rt 162,244,4608
s_add_u32 s92, s42, 0x40180
)" R"(
Mp 44,132,236
Rt 164,245,4096
s_add_u32 m0, s41, 102400
Mp 36,132,232
Rt 166,245,4608
buffer_load_dwordx4 v240, s[16:19], s92 offen offset:0 lds
Mp 48,136,232
Rt 168,246,4096
Mp 56,136,236
Rt 170,246,4608
Mp 60,140,236
Rt 172,247,4096
Mp 52,140,232
Rt 174,247,4608
buffer_load_dwordx4 v241, s[16:19], s92 offen offset:1024 lds
s_waitcnt lgkmcnt(8)
Mp 64,144,184
Rt 128,244,8192
Mp 72,144,188
Rt 130,244,8704
Mp 76,148,188
Rt 132,245,8192
Mp 68,148,184
Rt 134,245,8704
buffer_load_dwordx4 v242, s[16:19], s92 offen offset:2048 lds
Mp 80,152,184
Rt 136,246,8192
v_mul_f32_e32 v32, s54, v32
Mp 88,152,188
Rt 138,246,8704
s_add_u32 s92, s42, 0x1c0100
Mp 92,156,188
Rt 140,247,8192
s_add_u32 m0, s41, 32768
Mp 84,156,184
Rt 142,247,8704
buffer_load_dwordx4 v240, s[16:19], s92 offen offset:0 lds
s_waitcnt lgkmcnt(8)
Mp 64,160,192
Rt 144,244,12288
v_mul_f32_e32 v33, s54, v33
Mp 72,160,196
Rt 146,244,12800
Cv 32,32,33
Mp 76,164,196
Rt 148,245,12288
v_accvgpr_write_b32 a64, v32
v_mul_f32_e32 v34, s54, v34
Mp 68,164,192
Rt 150,245,12800
buffer_load_dwordx4 v241, s[16:19], s92 offen offset:1024 lds
Mp 80,168,192
Rt 152,246,12288
v_mul_f32_e32 v35, s54, v35
Mp 88,168,196
Rt 154,246,12800
Cv 34,34,35
v_accvgpr_write_b32 a65, v34
Mp 92,172,196
Rt 156,247,12288
v_mul_f32_e32 v36, s54, v36
Mp 84,172,192
Rt 158,247,12800
buffer_load_dwordx4 v242, s[16:19], s92 offen offset:2048 lds
)" R"(
s_waitcnt lgkmcnt(8)
Mp 64,128,200
Rt 160,244,16384
v_mul_f32_e32 v37, s54, v37
Cv 36,36,37
Mp 72,128,204
Rt 162,244,16896
v_accvgpr_write_b32 a66, v36
Mp 76,132,204
Rt 164,245,16384
v_mul_f32_e32 v38, s54, v38
Mp 68,132,200
Rt 166,245,16896
buffer_load_dwordx4 v243, s[16:19], s92 offen offset:3072 lds
Mp 80,136,200
Rt 168,246,16384
v_mul_f32_e32 v39, s54, v39
Mp 88,136,204
Rt 170,246,16896
s_add_u32 s92, s42, 0x200100
Mp 92,140,204
Rt 172,247,16384
s_add_u32 m0, s41, 36864
Mp 84,140,200
Rt 174,247,16896
buffer_load_dwordx4 v240, s[16:19], s92 offen offset:0 lds
s_waitcnt lgkmcnt(8)
Mp 64,144,208
Rt 128,244,20480
Cv 38,38,39
Mp 72,144,212
Rt 130,244,20992
v_accvgpr_write_b32 a67, v38
Mp 76,148,212
Rt 132,245,20480
v_mul_f32_e32 v40, s54, v40
v_mul_f32_e32 v41, s54, v41
Mp 68,148,208
Rt 134,245,20992
buffer_load_dwordx4 v241, s[16:19], s92 offen offset:1024 lds
Mp 80,152,208
Rt 136,246,20480
Cv 40,40,41
Mp 88,152,212
Rt 138,246,20992
v_accvgpr_write_b32 a68, v40
v_mul_f32_e32 v42, s54, v42
Mp 92,156,212
Rt 140,247,20480
v_mul_f32_e32 v43, s54, v43
Mp 84,156,208
Rt 142,247,20992
buffer_load_dwordx4 v242, s[16:19], s92 offen offset:2048 lds
s_waitcnt lgkmcnt(8)
Mp 64,160,216
Rt 144,244,24576
Cv 42,42,43
v_accvgpr_write_b32 a69, v42
Mp 72,160,220
Rt 146,244,25088
v_mul_f32_e32 v44, s54, v44
Mp 76,164,220
Rt 148,245,24576
v_mul_f32_e32 v45, s54, v45
)" R"(
Mp 68,164,216
Rt 150,245,25088
s_add_u32 s92, s42, 0x1c0180
Cv 44,44,45
Mp 80,168,216
Rt 152,246,24576
s_add_u32 m0, s41, 131072
Mp 88,168,220
Rt 154,246,25088
buffer_load_dwordx4 v240, s[16:19], s92 offen offset:0 lds
Mp 92,172,220
Rt 156,247,24576
v_accvgpr_write_b32 a70, v44
Mp 84,172,216
Rt 158,247,25088
s_waitcnt vmcnt(15) lgkmcnt(8)
s_barrier
Mp 64,128,224
Rt 160,248,0
v_mul_f32_e32 v46, s54, v46
Mp 72,128,228
Rt 162,248,512
buffer_load_dwordx4 v241, s[16:19], s92 offen offset:1024 lds
Mp 76,132,228
Rt 164,249,0
v_mul_f32_e32 v47, s54, v47
Cv 46,46,47
Mp 68,132,224
Rt 166,249,512
v_accvgpr_write_b32 a71, v46
Mp 80,136,224
Rt 168,250,0
v_mul_f32_e32 v48, s54, v48
Mp 88,136,228
Rt 170,250,512
buffer_load_dwordx4 v242, s[16:19], s92 offen offset:2048 lds
Mp 92,140,228
Rt 172,251,0
v_mul_f32_e32 v49, s54, v49
Mp 84,140,224
Rt 174,251,512
s_waitcnt vmcnt(3) lgkmcnt(8)
s_barrier
Mp 64,144,232
Rt 128,248,4096
Cv 48,48,49
v_accvgpr_write_b32 a72, v48
Mp 72,144,236
Rt 130,248,4608
buffer_load_dwordx4 v243, s[16:19], s92 offen offset:3072 lds
Mp 76,148,236
Rt 132,249,4096
v_mul_f32_e32 v50, s54, v50
Mp 68,148,232
Rt 134,249,4608
s_add_u32 s92, s42, 0x200180
v_mul_f32_e32 v51, s54, v51
Mp 80,152,232
Rt 136,250,4096
s_add_u32 m0, s41, 135168
Mp 88,152,236
Rt 138,250,4608
buffer_load_dwordx4 v240, s[16:19], s92 offen offset:0 lds
Mp 92,156,236
Rt 140,251,4096
Cv 50,50,51
)" R"(
Mp 84,156,232
Rt 142,251,4608
v_accvgpr_write_b32 a73, v50
s_waitcnt lgkmcnt(8)
v_mfma_f32_16x16x32_bf16 v[96:99], a[160:163], v[128:131], 0
Rt 144,248,8192
v_mul_f32_e32 v52, s54, v52
v_mfma_f32_16x16x32_bf16 v[104:107], a[160:163], v[132:135], 0
Rt 146,248,8704
buffer_load_dwordx4 v241, s[16:19], s92 offen offset:1024 lds
v_mfma_f32_16x16x32_bf16 v[108:111], a[164:167], v[132:135], 0
Rt 148,249,8192
v_mul_f32_e32 v53, s54, v53
Cv 52,52,53
v_mfma_f32_16x16x32_bf16 v[100:103], a[164:167], v[128:131], 0
Rt 150,249,8704
v_accvgpr_write_b32 a74, v52
v_mfma_f32_16x16x32_bf16 v[112:115], a[168:171], v[128:131], 0
Rt 152,250,8192
v_mul_f32_e32 v54, s54, v54
v_mfma_f32_16x16x32_bf16 v[120:123], a[168:171], v[132:135], 0
Rt 154,250,8704
buffer_load_dwordx4 v242, s[16:19], s92 offen offset:2048 lds
v_mfma_f32_16x16x32_bf16 v[124:127], a[172:175], v[132:135], 0
Rt 156,251,8192
v_mul_f32_e32 v55, s54, v55
v_mfma_f32_16x16x32_bf16 v[116:119], a[172:175], v[128:131], 0
Rt 158,251,8704
s_waitcnt lgkmcnt(8)
Mp 96,128,136
Rt 160,248,12288
Cv 54,54,55
v_accvgpr_write_b32 a75, v54
Mp 104,128,140
Rt 162,248,12800
s_add_u32 s38, s38, 0xffff0080
Mp 108,132,140
Rt 164,249,12288
s_add_u32 m0, s36, 0
Mp 100,132,136
Rt 166,249,12800
buffer_load_dwordx4 v8, s[4:7], s38 offen offset:0 lds
Mp 112,136,136
Rt 168,250,12288
v_mul_f32_e32 v56, s54, v56
Mp 120,136,140
Rt 170,250,12800
v_mul_f32_e32 v57, s54, v57
Mp 124,140,140
Rt 172,251,12288
Cv 56,56,57
Mp 116,140,136
Rt 174,251,12800
buffer_load_dwordx4 v9, s[4:7], s38 offen offset:1024 lds
s_waitcnt lgkmcnt(8)
Mp 96,144,144
Rt 128,248,16384
v_accvgpr_write_b32 a76, v56
Mp 104,144,148
Rt 130,248,16896
v_mul_f32_e32 v58, s54, v58
Mp 108,148,148
Rt 132,249,16384
v_mul_f32_e32 v59, s54, v59
)" R"(
Cv 58,58,59
Mp 100,148,144
Rt 134,249,16896
buffer_load_dwordx4 v10, s[4:7], s38 offen offset:2048 lds
Mp 112,152,144
Rt 136,250,16384
v_accvgpr_write_b32 a77, v58
Mp 120,152,148
Rt 138,250,16896
v_mul_f32_e32 v60, s54, v60
v_mul_f32_e32 v61, s54, v61
Mp 124,156,148
Rt 140,251,16384
Cv 60,60,61
Mp 116,156,144
Rt 142,251,16896
buffer_load_dwordx4 v11, s[4:7], s38 offen offset:3072 lds
s_waitcnt lgkmcnt(8)
Mp 96,160,152
Rt 144,248,20480
v_accvgpr_write_b32 a78, v60
v_mul_f32_e32 v62, s54, v62
Mp 104,160,156
Rt 146,248,20992
s_add_u32 s38, s38, 0x10000
Mp 108,164,156
Rt 148,249,20480
s_add_u32 m0, s36, 4096
Mp 100,164,152
Rt 150,249,20992
buffer_load_dwordx4 v8, s[4:7], s38 offen offset:0 lds
Mp 112,168,152
Rt 152,250,20480
v_mul_f32_e32 v63, s54, v63
Mp 120,168,156
Rt 154,250,20992
Cv 62,62,63
Mp 124,172,156
Rt 156,251,20480
v_accvgpr_write_b32 a79, v62
Mp 116,172,152
Rt 158,251,20992
buffer_load_dwordx4 v9, s[4:7], s38 offen offset:1024 lds
s_waitcnt lgkmcnt(8)
Mp 96,128,160
Rt 160,248,24576
v_mul_f32_e32 v64, s54, v64
Mp 104,128,164
Rt 162,248,25088
v_mul_f32_e32 v65, s54, v65
Mp 108,132,164
Rt 164,249,24576
Cv 64,64,65
v_accvgpr_write_b32 a80, v64
Mp 100,132,160
Rt 166,249,25088
buffer_load_dwordx4 v10, s[4:7], s38 offen offset:2048 lds
Mp 112,136,160
Rt 168,250,24576
v_mul_f32_e32 v66, s54, v66
Mp 120,136,164
Rt 170,250,25088
v_mul_f32_e32 v67, s54, v67
Cv 66,66,67
)" R"(
Mp 124,140,164
Rt 172,251,24576
v_accvgpr_write_b32 a81, v66
Mp 116,140,160
Rt 174,251,25088
buffer_load_dwordx4 v11, s[4:7], s38 offen offset:3072 lds
s_waitcnt lgkmcnt(8)
Mp 96,144,168
Rt 128,244,32768
v_mul_f32_e32 v68, s54, v68
v_mul_f32_e32 v69, s54, v69
Mp 104,144,172
Rt 130,244,33280
Cv 68,68,69
s_mov_b32 s85, 0xc108800
Mp 108,148,172
Rt 132,245,32768
v_accvgpr_write_b32 a82, v68
v_and_b32 v2, 7, v0
Mp 100,148,168
Rt 134,245,33280
v_mul_f32_e32 v70, s54, v70
v_mul_f32_e32 v71, s54, v71
v_lshlrev_b32 v2, 7, v2
Mp 112,152,168
Rt 136,246,32768
Cv 70,70,71
Mp 120,152,172
Rt 138,246,33280
v_accvgpr_write_b32 a83, v70
buffer_load_dword v4, v2, s[20:23], s85 offen sc0 sc1
Mp 124,156,172
Rt 140,247,32768
v_mul_f32_e32 v72, s54, v72
Mp 116,156,168
Rt 142,247,33280
s_waitcnt lgkmcnt(8)
s_barrier
Mp 96,160,176
Rt 144,244,36864
v_mul_f32_e32 v73, s54, v73
Mp 104,160,180
Rt 146,244,37376
s_add_u32 s39, s39, 0x40000
Mp 108,164,180
Rt 148,245,36864
s_add_u32 m0, s37, 0
Cv 72,72,73
Mp 100,164,176
Rt 150,245,37376
buffer_load_dwordx4 v12, s[8:11], s39 offen offset:0 lds
Mp 112,168,176
Rt 152,246,36864
v_accvgpr_write_b32 a84, v72
Mp 120,168,180
Rt 154,246,37376
v_mul_f32_e32 v74, s54, v74
v_mul_f32_e32 v75, s54, v75
Mp 124,172,180
Rt 156,247,36864
Cv 74,74,75
.LzH1:
s_cbranch_execz .LzH16
Mp 116,172,176
Rt 158,247,37376
)" R"(
s_waitcnt lgkmcnt(8)
Mp 96,128,184
Rt 160,244,40960
v_accvgpr_write_b32 a85, v74
v_mul_f32_e32 v76, s54, v76
Mp 104,128,188
Rt 162,244,41472
buffer_load_dwordx4 v13, s[8:11], s39 offen offset:1024 lds
Mp 108,132,188
Rt 164,245,40960
v_mul_f32_e32 v77, s54, v77
Mp 100,132,184
Rt 166,245,41472
Cv 76,76,77
v_accvgpr_write_b32 a86, v76
Mp 112,136,184
Rt 168,246,40960
v_mul_f32_e32 v78, s54, v78
Mp 120,136,188
Rt 170,246,41472
v_mul_f32_e32 v79, s54, v79
Mp 124,140,188
Rt 172,247,40960
Cv 78,78,79
Mp 116,140,184
Rt 174,247,41472
buffer_load_dwordx4 v14, s[8:11], s39 offen offset:2048 lds
s_waitcnt lgkmcnt(8)
Mp 96,144,192
Rt 128,244,45056
v_accvgpr_write_b32 a87, v78
Mp 104,144,196
Rt 130,244,45568
v_mul_f32_e32 v80, s54, v80
Mp 108,148,196
Rt 132,245,45056
v_mul_f32_e32 v81, s54, v81
Cv 80,80,81
Mp 100,148,192
Rt 134,245,45568
v_accvgpr_write_b32 a88, v80
Mp 112,152,192
Rt 136,246,45056
v_mul_f32_e32 v82, s54, v82
Mp 120,152,196
Rt 138,246,45568
buffer_load_dwordx4 v15, s[8:11], s39 offen offset:3072 lds
Mp 124,156,196
Rt 140,247,45056
v_mul_f32_e32 v83, s54, v83
Mp 116,156,192
Rt 142,247,45568
s_waitcnt lgkmcnt(8)
Mp 96,160,200
Rt 144,244,49152
Cv 82,82,83
v_accvgpr_write_b32 a89, v82
Mp 104,160,204
Rt 146,244,49664
s_add_u32 s39, s39, 0x40000
Mp 108,164,204
Rt 148,245,49152
s_add_u32 m0, s37, 16384
Mp 100,164,200
)" R"(
Rt 150,245,49664
buffer_load_dwordx4 v12, s[8:11], s39 offen offset:0 lds
Mp 112,168,200
Rt 152,246,49152
v_mul_f32_e32 v84, s54, v84
Mp 120,168,204
Rt 154,246,49664
v_mul_f32_e32 v85, s54, v85
Mp 124,172,204
Rt 156,247,49152
Cv 84,84,85
Mp 116,172,200
Rt 158,247,49664
v_accvgpr_write_b32 a90, v84
s_waitcnt lgkmcnt(8)
Mp 96,128,208
Rt 160,244,53248
v_mul_f32_e32 v86, s54, v86
Mp 104,128,212
Rt 162,244,53760
buffer_load_dwordx4 v13, s[8:11], s39 offen offset:1024 lds
Mp 108,132,212
Rt 164,245,53248
v_mul_f32_e32 v87, s54, v87
Cv 86,86,87
Mp 100,132,208
Rt 166,245,53760
v_accvgpr_write_b32 a91, v86
Mp 112,136,208
Rt 168,246,53248
v_mul_f32_e32 v88, s54, v88
Mp 120,136,212
Rt 170,246,53760
v_mul_f32_e32 v89, s54, v89
Cv 88,88,89
Mp 124,140,212
Rt 172,247,53248
v_accvgpr_write_b32 a92, v88
Mp 116,140,208
Rt 174,247,53760
buffer_load_dwordx4 v14, s[8:11], s39 offen offset:2048 lds
s_waitcnt lgkmcnt(8)
Mp 96,144,216
Rt 128,244,57344
v_mul_f32_e32 v90, s54, v90
v_mul_f32_e32 v91, s54, v91
Mp 104,144,220
Rt 130,244,57856
Cv 90,90,91
Mp 108,148,220
Rt 132,245,57344
v_accvgpr_write_b32 a93, v90
Mp 100,148,216
Rt 134,245,57856
v_mul_f32_e32 v92, s54, v92
v_mul_f32_e32 v93, s54, v93
Mp 112,152,216
Rt 136,246,57344
Cv 92,92,93
Mp 120,152,220
Rt 138,246,57856
buffer_load_dwordx4 v15, s[8:11], s39 offen offset:3072 lds
Mp 124,156,220
Rt 140,247,57344
)" R"(
v_accvgpr_write_b32 a94, v92
Mp 116,156,216
Rt 142,247,57856
v_mul_f32_e32 v94, s54, v94
s_waitcnt lgkmcnt(8)
Mp 96,160,224
Rt 144,248,32768
v_mul_f32_e32 v95, s54, v95
Mp 104,160,228
Rt 146,248,33280
Cv 94,94,95
Mp 108,164,228
Rt 148,249,32768
v_accvgpr_write_b32 a95, v94
Mp 100,164,224
Rt 150,249,33280
s_lshl1_add_u32 s88, s34, s91
Mp 112,168,224
Rt 152,250,32768
s_lshl_b32 s88, s88, 12
Mp 120,168,228
Rt 154,250,33280
s_and_b32 s29, s38, 0xfffe0000
Mp 124,172,228
Rt 156,251,32768
s_add_u32 s88, s88, s29
Mp 116,172,224
Rt 158,251,33280
s_waitcnt vmcnt(8) lgkmcnt(8)
s_barrier
Mp 96,128,232
Rt 160,248,36864
v_min_u32 v28, 31, v0
v_xor_b32 v4, s3, v4
v_cmp_gt_u32_e32 vcc, 2, v4
s_andn2_b32 s28, 0xff, vcc_lo
s_cbranch_scc1 .Lzsp13
.Lzok13:
Mp 104,128,236
Rt 162,248,37376
s_add_u32 s38, s38, 0xffff0080
v_xor_b32 v4, s3, v4
Mp 108,132,236
Rt 164,249,36864
s_add_u32 m0, s36, 32768
v_and_b32 v4, 1, v4
Mp 100,132,232
Rt 166,249,37376
buffer_load_dwordx4 v8, s[4:7], s38 offen offset:0 lds
Mp 112,136,232
Rt 168,250,36864
v_lshlrev_b32 v28, 7, v28
Mp 120,136,236
Rt 170,250,37376
v_cmp_eq_u32_e32 vcc, 1, v4
s_mov_b32 s85, vcc_lo
Mp 124,140,236
Rt 172,251,36864
s_and_b32 s28, s2, 7
s_lshr_b32 s28, s85, s28
Mp 116,140,232
Rt 174,251,37376
s_xor_b32 s28, s28, s34
s_and_b32 s28, s28, 1
s_waitcnt lgkmcnt(8)
)" R"(
v_mfma_f32_16x16x32_bf16 v[64:67], a[144:147], v[128:131], 0
Rt 128,248,40960
s_lshl_b32 s84, s28, 13
s_add_u32 s49, s49, s84
v_mfma_f32_16x16x32_bf16 v[72:75], a[144:147], v[132:135], 0
Rt 130,248,41472
buffer_load_dwordx4 v9, s[4:7], s38 offen offset:1024 lds
v_mfma_f32_16x16x32_bf16 v[76:79], a[148:151], v[132:135], 0
Rt 132,249,40960
s_add_u32 s50, s50, s84
s_add_u32 s60, s50, 7168
v_mfma_f32_16x16x32_bf16 v[68:71], a[148:151], v[128:131], 0
Rt 134,249,41472
s_add_u32 s52, s52, s84
s_add_u32 s65, s65, s84
v_mfma_f32_16x16x32_bf16 v[80:83], a[152:155], v[128:131], 0
Rt 136,250,40960
v_mul_f32_e32 v96, s54, v96
s_lshr_b32 s28, s2, 6
s_lshl_b32 s28, s28, 14
v_mfma_f32_16x16x32_bf16 v[88:91], a[152:155], v[132:135], 0
Rt 138,250,41472
v_mul_f32_e32 v97, s54, v97
s_add_u32 s28, s28, s50
v_mfma_f32_16x16x32_bf16 v[92:95], a[156:159], v[132:135], 0
Rt 140,251,40960
Cv 96,96,97
s_bfe_u32 s29, s2, 0x20004
v_mfma_f32_16x16x32_bf16 v[84:87], a[156:159], v[128:131], 0
Rt 142,251,41472
buffer_load_dwordx4 v10, s[4:7], s38 offen offset:2048 lds
s_waitcnt lgkmcnt(8)
Mp 64,160,136
Rt 144,248,45056
v_accvgpr_write_b32 a96, v96
s_lshl2_add_u32 s29, s29, s33
Mp 72,160,140
Rt 146,248,45568
v_mul_f32_e32 v98, s54, v98
s_lshl_b32 s29, s29, 9
Mp 76,164,140
Rt 148,249,45056
v_mul_f32_e32 v99, s54, v99
s_add_u32 s28, s28, s29
Mp 68,164,136
Rt 150,249,45568
Cv 98,98,99
v_min_u32 v4, 3, v0
Mp 80,168,136
Rt 152,250,45056
v_accvgpr_write_b32 a97, v98
v_lshlrev_b32 v4, 7, v4
Mp 88,168,140
Rt 154,250,45568
buffer_load_dwordx4 v11, s[4:7], s38 offen offset:3072 lds
Mp 92,172,140
Rt 156,251,45056
v_mul_f32_e32 v100, s54, v100
Mp 84,172,136
Rt 158,251,45568
s_waitcnt lgkmcnt(8)
Mp 64,128,144
Rt 160,248,49152
v_mul_f32_e32 v101, s54, v101
)" R"(
Mp 72,128,148
Rt 162,248,49664
s_add_u32 s38, s38, 0x10000
Mp 76,132,148
Rt 164,249,49152
s_add_u32 m0, s36, 36864
Mp 68,132,144
Rt 166,249,49664
buffer_load_dwordx4 v8, s[4:7], s38 offen offset:0 lds
Mp 80,136,144
Rt 168,250,49152
Cv 100,100,101
buffer_load_dword v4, v4, s[20:23], s28 offen
Mp 88,136,148
Rt 170,250,49664
v_accvgpr_write_b32 a98, v100
s_lshl1_add_u32 s28, s36, s84
Mp 92,140,148
Rt 172,251,49152
v_mul_f32_e32 v102, s54, v102
v_add_u32 v2, s28, v1
Mp 84,140,144
Rt 174,251,49664
v_add_u32 v3, 0x1000, v2
s_waitcnt lgkmcnt(8)
Mp 64,144,152
Rt 128,248,53248
v_mul_f32_e32 v103, s54, v103
s_lshl_b32 s28, s34, 13
Mp 72,144,156
Rt 130,248,53760
buffer_load_dwordx4 v9, s[4:7], s38 offen offset:1024 lds
s_xor_b32 s28, s28, s84
Mp 76,148,156
Rt 132,249,53248
Cv 102,102,103
s_and_b32 s28, s28, 0x2000
Mp 68,148,152
Rt 134,249,53760
v_accvgpr_write_b32 a99, v102
s_xor_b32 s73, s73, s28
Mp 80,152,152
Rt 136,250,53248
v_mul_f32_e32 v104, s54, v104
s_xor_b32 s72, s72, s28
Mp 88,152,156
Rt 138,250,53760
v_mul_f32_e32 v105, s54, v105
Mp 92,156,156
Rt 140,251,53248
Cv 104,104,105
Mp 84,156,152
Rt 142,251,53760
buffer_load_dwordx4 v10, s[4:7], s38 offen offset:2048 lds
s_waitcnt lgkmcnt(8)
Mp 64,160,160
Rt 144,248,57344
v_accvgpr_write_b32 a100, v104
Mp 72,160,164
Rt 146,248,57856
v_mul_f32_e32 v106, s54, v106
Mp 76,164,164
Rt 148,249,57344
v_mul_f32_e32 v107, s54, v107
)" R"(
Mp 68,164,160
Rt 150,249,57856
Cv 106,106,107
Mp 80,168,160
Rt 152,250,57344
v_accvgpr_write_b32 a101, v106
Mp 88,168,164
Rt 154,250,57856
buffer_load_dwordx4 v11, s[4:7], s38 offen offset:3072 lds
Mp 92,172,164
Rt 156,251,57344
v_mul_f32_e32 v108, s54, v108
Mp 84,172,160
Rt 158,251,57856
s_waitcnt lgkmcnt(8)
Mp 64,128,168
Rt 160,252,0
v_mul_f32_e32 v109, s54, v109
Mp 72,128,172
Rt 162,252,512
Cv 108,108,109
Mp 76,132,172
Rt 164,253,0
v_accvgpr_write_b32 a102, v108
Mp 68,132,168
Rt 166,253,512
v_mul_f32_e32 v110, s54, v110
Mp 80,136,168
Rt 168,254,0
v_mul_f32_e32 v111, s54, v111
Mp 88,136,172
Rt 170,254,512
Cv 110,110,111
Mp 92,140,172
Rt 172,255,0
v_accvgpr_write_b32 a103, v110
Mp 84,140,168
Rt 174,255,512
s_waitcnt vmcnt(13) lgkmcnt(8)
s_barrier
Mp 64,144,176
Rt 128,252,4096
v_mul_f32_e32 v112, s54, v112
Mp 72,144,180
Rt 130,252,4608
s_add_u32 s39, s39, 0x40000
Mp 76,148,180
Rt 132,253,4096
s_add_u32 m0, s37, 32768
Mp 68,148,176
Rt 134,253,4608
buffer_load_dwordx4 v12, s[8:11], s39 offen offset:0 lds
Mp 80,152,176
Rt 136,254,4096
v_mul_f32_e32 v113, s54, v113
Mp 88,152,180
Rt 138,254,4608
Cv 112,112,113
Mp 92,156,180
Rt 140,255,4096
v_accvgpr_write_b32 a104, v112
Mp 84,156,176
Rt 142,255,4608
s_waitcnt lgkmcnt(8)
Mp 64,160,184
)" R"(
Rt 144,252,8192
v_mul_f32_e32 v114, s54, v114
Mp 72,160,188
Rt 146,252,8704
buffer_load_dwordx4 v13, s[8:11], s39 offen offset:1024 lds
Mp 76,164,188
Rt 148,253,8192
v_mul_f32_e32 v115, s54, v115
Mp 68,164,184
Rt 150,253,8704
Cv 114,114,115
Mp 80,168,184
Rt 152,254,8192
v_accvgpr_write_b32 a105, v114
Mp 88,168,188
Rt 154,254,8704
v_mul_f32_e32 v116, s54, v116
Mp 92,172,188
Rt 156,255,8192
v_mul_f32_e32 v117, s54, v117
Mp 84,172,184
Rt 158,255,8704
buffer_load_dwordx4 v14, s[8:11], s39 offen offset:2048 lds
s_waitcnt lgkmcnt(8)
Mp 64,128,192
Rt 160,252,12288
Cv 116,116,117
Mp 72,128,196
Rt 162,252,12800
v_accvgpr_write_b32 a106, v116
Mp 76,132,196
Rt 164,253,12288
v_mul_f32_e32 v118, s54, v118
Mp 68,132,192
Rt 166,253,12800
v_mul_f32_e32 v119, s54, v119
Mp 80,136,192
Rt 168,254,12288
Cv 118,118,119
Mp 88,136,196
Rt 170,254,12800
buffer_load_dwordx4 v15, s[8:11], s39 offen offset:3072 lds
Mp 92,140,196
Rt 172,255,12288
v_accvgpr_write_b32 a107, v118
Mp 84,140,192
Rt 174,255,12800
s_waitcnt lgkmcnt(8)
Mp 64,144,200
Rt 128,252,16384
v_mul_f32_e32 v120, s54, v120
Mp 72,144,204
Rt 130,252,16896
s_add_u32 s39, s39, 0x40000
Mp 76,148,204
Rt 132,253,16384
s_add_u32 m0, s37, 49152
Mp 68,148,200
Rt 134,253,16896
buffer_load_dwordx4 v12, s[8:11], s39 offen offset:0 lds
Mp 80,152,200
Rt 136,254,16384
v_mul_f32_e32 v121, s54, v121
Mp 88,152,204
)" R"(
Rt 138,254,16896
Cv 120,120,121
Mp 92,156,204
Rt 140,255,16384
v_accvgpr_write_b32 a108, v120
Mp 84,156,200
Rt 142,255,16896
s_waitcnt lgkmcnt(8)
Mp 64,160,208
Rt 144,252,20480
Rt 0,18,0
Mp 72,160,212
Rt 146,252,20992
buffer_load_dwordx4 v13, s[8:11], s39 offen offset:1024 lds
Rt 2,18,2048
Mp 76,164,212
Rt 148,253,20480
Rt 4,19,0
Mp 68,164,208
Rt 150,253,20992
Rt 6,19,2048
Mp 80,168,208
Rt 152,254,20480
Rt 8,20,0
Mp 88,168,212
Rt 154,254,20992
Rt 10,20,2048
Mp 92,172,212
Rt 156,255,20480
Rt 12,21,0
Mp 84,172,208
Rt 158,255,20992
buffer_load_dwordx4 v14, s[8:11], s39 offen offset:2048 lds
Rt 14,21,2048
Mp 64,128,216
Rt 160,252,24576
Rt 16,22,0
Mp 72,128,220
Rt 162,252,25088
Rt 18,22,2048
Mp 76,132,220
Rt 164,253,24576
Rt 20,23,0
Mp 68,132,216
Rt 166,253,25088
Rt 22,23,2048
Mp 80,136,216
Rt 168,254,24576
Rt 24,24,0
Mp 88,136,220
Rt 170,254,25088
buffer_load_dwordx4 v15, s[8:11], s39 offen offset:3072 lds
Rt 26,24,2048
Mp 92,140,220
Rt 172,255,24576
Rt 28,25,0
Mp 84,140,216
Rt 174,255,25088
Rt 30,25,2048
Mp 64,144,224
Rb 32,16,0
v_mul_f32_e32 v122, s54, v122
Mp 72,144,228
Rb 36,16,2048
)" R"(
v_mul_f32_e32 v123, s54, v123
Mp 76,148,228
Rb 40,16,8192
Cv 122,122,123
Mp 68,148,224
Rb 44,16,10240
v_accvgpr_write_b32 a109, v122
Mp 80,152,224
Rb 48,16,16384
v_mul_f32_e32 v124, s54, v124
Mp 88,152,228
Rb 52,16,18432
v_mul_f32_e32 v125, s54, v125
Mp 92,156,228
Rb 56,16,24576
Cv 124,124,125
Mp 84,156,224
Rb 60,16,26624
s_waitcnt lgkmcnt(9)
Mp 64,160,232
v_accvgpr_write_b32 a110, v124
v_mul_f32_e32 v126, s54, v126
Mp 72,160,236
v_mul_f32_e32 v127, s54, v127
Cv 126,126,127
Mp 76,164,236
v_accvgpr_write_b32 a111, v126
Mp 68,164,232
buffer_load_dword a254, v28, s[4:7], s88 offen
Mp 80,168,232
Mp 88,168,236
Mp 92,172,236
Mp 84,172,232
s_cmp_eq_u32 s91, 1
s_waitcnt vmcnt(18) lgkmcnt(0)
s_barrier
s_cbranch_scc1 .LzBV
Mz 32,0,32
Rt 128,18,16384
v_mul_f32_e32 v64, s54, v64
Mz 96,0,40
v_mul_f32_e32 v65, s54, v65
Cv 64,64,65
Mz 128,0,44
Rt 130,18,18432
v_accvgpr_write_b32 a112, v64
Mz 160,0,48
s_add_u32 s39, s39, 0x40000
v_mul_f32_e32 v66, s54, v66
Mz 192,0,52
Rt 132,19,16384
s_add_u32 m0, s37, 0
Mz 224,0,56
buffer_load_dwordx4 v12, s[8:11], s39 offen offset:0 lds
.LzH2:
s_cbranch_execz .LzH11
v_mfma_f32_16x16x32_bf16 a[192:195], a[0:3], a[60:63], 0
Rt 134,19,18432
v_mul_f32_e32 v67, s54, v67
v_mfma_f32_16x16x32_bf16 a[196:199], a[4:7], a[60:63], 0
Cv 66,66,67
v_accvgpr_write_b32 a113, v66
Mz 228,4,56
Rt 136,20,16384
v_mul_f32_e32 v68, s54, v68
)" R"(
Mz 196,4,52
v_mul_f32_e32 v69, s54, v69
Cv 68,68,69
buffer_store_dwordx4 a[64:67], v2, s[20:23], s51 offen offset:0 sc0 sc1
Mz 164,4,48
Rt 138,20,18432
v_accvgpr_write_b32 a114, v68
Mz 132,4,44
v_mul_f32_e32 v70, s54, v70
v_mul_f32_e32 v71, s54, v71
Mz 100,4,40
Rt 140,21,16384
Cv 70,70,71
Mz 36,4,32
buffer_load_dwordx4 v13, s[8:11], s39 offen offset:1024 lds
Mz 40,8,32
Rt 142,21,18432
v_accvgpr_write_b32 a115, v70
Mz 104,8,40
v_mul_f32_e32 v72, s54, v72
v_mul_f32_e32 v73, s54, v73
Mz 136,8,44
Rt 144,22,16384
Cv 72,72,73
Mz 168,8,48
v_accvgpr_write_b32 a116, v72
v_mul_f32_e32 v74, s54, v74
buffer_store_dwordx4 a[68:71], v2, s[20:23], s51 offen offset:1024 sc0 sc1
Mz 200,8,52
Rt 146,22,18432
v_mul_f32_e32 v75, s54, v75
Mz 232,8,56
Cv 74,74,75
v_accvgpr_write_b32 a117, v74
v_mfma_f32_16x16x32_bf16 a[200:203], a[8:11], a[60:63], 0
Rt 148,23,16384
v_mul_f32_e32 v76, s54, v76
v_mfma_f32_16x16x32_bf16 a[204:207], a[12:15], a[60:63], 0
buffer_load_dwordx4 v14, s[8:11], s39 offen offset:2048 lds
Mz 236,12,56
Rt 150,23,18432
v_mul_f32_e32 v77, s54, v77
Mz 204,12,52
Cv 76,76,77
v_accvgpr_write_b32 a118, v76
Mz 172,12,48
Rt 152,24,16384
v_mul_f32_e32 v78, s54, v78
Mz 140,12,44
v_mul_f32_e32 v79, s54, v79
Cv 78,78,79
buffer_store_dwordx4 a[72:75], v2, s[20:23], s51 offen offset:2048 sc0 sc1
Mz 108,12,40
Rt 154,24,18432
v_accvgpr_write_b32 a119, v78
Mz 44,12,32
v_mul_f32_e32 v80, s54, v80
v_mul_f32_e32 v81, s54, v81
Mz 48,16,32
Rt 156,25,16384
Cv 80,80,81
Mz 112,16,40
buffer_load_dwordx4 v15, s[8:11], s39 offen offset:3072 lds
Mz 144,16,44
)" R"(
Rt 158,25,18432
v_accvgpr_write_b32 a120, v80
Mz 176,16,48
v_mul_f32_e32 v82, s54, v82
v_mul_f32_e32 v83, s54, v83
Mz 208,16,52
Rb 160,17,0
Cv 82,82,83
Mz 240,16,56
v_accvgpr_write_b32 a121, v82
v_mul_f32_e32 v84, s54, v84
buffer_store_dwordx4 a[76:79], v2, s[20:23], s51 offen offset:3072 sc0 sc1
v_mfma_f32_16x16x32_bf16 a[208:211], a[16:19], a[60:63], 0
Rb 164,17,2048
v_mul_f32_e32 v85, s54, v85
v_mfma_f32_16x16x32_bf16 a[212:215], a[20:23], a[60:63], 0
Cv 84,84,85
v_accvgpr_write_b32 a122, v84
Mz 244,20,56
Rb 168,17,8192
v_mul_f32_e32 v86, s54, v86
Mz 212,20,52
v_mul_f32_e32 v87, s54, v87
Cv 86,86,87
Mz 180,20,48
Rb 172,17,10240
v_accvgpr_write_b32 a123, v86
Mz 148,20,44
v_mul_f32_e32 v88, s54, v88
v_mul_f32_e32 v89, s54, v89
Mz 116,20,40
Rb 176,17,16384
Cv 88,88,89
Mz 52,20,32
v_accvgpr_write_b32 a124, v88
v_mul_f32_e32 v90, s54, v90
buffer_store_dwordx4 a[80:83], v3, s[20:23], s51 offen offset:0 sc0 sc1
Mz 56,24,32
Rb 180,17,18432
v_mul_f32_e32 v91, s54, v91
Mz 120,24,40
Cv 90,90,91
v_accvgpr_write_b32 a125, v90
Mz 152,24,44
Rb 184,17,24576
v_mul_f32_e32 v92, s54, v92
Mz 184,24,48
v_mul_f32_e32 v93, s54, v93
Cv 92,92,93
Mz 216,24,52
Rb 188,17,26624
v_accvgpr_write_b32 a126, v92
Mz 248,24,56
v_mul_f32_e32 v94, s54, v94
v_mul_f32_e32 v95, s54, v95
v_mfma_f32_16x16x32_bf16 a[216:219], a[24:27], a[60:63], 0
Cv 94,94,95
v_accvgpr_write_b32 a127, v94
v_mfma_f32_16x16x32_bf16 a[220:223], a[28:31], a[60:63], 0
buffer_store_dwordx4 a[84:87], v3, s[20:23], s51 offen offset:1024 sc0 sc1
Mz 252,28,56
Mz 220,28,52
Mz 188,28,48
Mz 156,28,44
)" R"(
Mz 124,28,40
Mz 60,28,32
Mz 64,0,36
Mz 68,4,36
buffer_store_dwordx4 a[88:91], v3, s[20:23], s51 offen offset:2048 sc0 sc1
Mz 72,8,36
Mz 76,12,36
Mz 80,16,36
Mz 84,20,36
Mz 88,24,36
Mz 92,28,36
s_waitcnt vmcnt(16) lgkmcnt(0)
s_barrier
Mv 32,128,160
Rt 0,18,32768
Mv 64,128,164
s_add_u32 s38, s38, 0xffff0080
Mv 96,128,168
Rt 2,18,34816
s_add_u32 m0, s36, 0
Mv 128,128,172
buffer_load_dwordx4 v8, s[4:7], s38 offen offset:0 lds
Mv 160,128,176
Rt 4,19,32768
Mv 192,128,180
Mv 224,128,184
Rt 6,19,34816
Ma 192,128,188
buffer_load_dwordx4 v9, s[4:7], s38 offen offset:1024 lds
Ma 196,132,188
Rt 8,20,32768
Mv 228,132,184
Mv 196,132,180
Rt 10,20,34816
Mv 164,132,176
buffer_load_dwordx4 v10, s[4:7], s38 offen offset:2048 lds
Mv 132,132,172
Rt 12,21,32768
Mv 100,132,168
Mv 68,132,164
Rt 14,21,34816
Mv 36,132,160
buffer_load_dwordx4 v11, s[4:7], s38 offen offset:3072 lds
Mv 40,136,160
Rt 16,22,32768
Mv 72,136,164
s_add_u32 s38, s38, 0x10000
Mv 104,136,168
Rt 18,22,34816
s_add_u32 m0, s36, 4096
Mv 136,136,172
buffer_load_dwordx4 v8, s[4:7], s38 offen offset:0 lds
Mv 168,136,176
Rt 20,23,32768
Mv 200,136,180
Mv 232,136,184
Rt 22,23,34816
Ma 200,136,188
buffer_load_dwordx4 v9, s[4:7], s38 offen offset:1024 lds
Ma 204,140,188
Rt 24,24,32768
Mv 236,140,184
Mv 204,140,180
Rt 26,24,34816
Mv 172,140,176
)" R"(
buffer_load_dwordx4 v10, s[4:7], s38 offen offset:2048 lds
Mv 140,140,172
Rt 28,25,32768
Mv 108,140,168
Mv 76,140,164
Rt 30,25,34816
Mv 44,140,160
buffer_load_dwordx4 v11, s[4:7], s38 offen offset:3072 lds
Mv 48,144,160
Rb 32,16,32768
Mv 80,144,164
Mv 112,144,168
Rb 36,16,34816
Mv 144,144,172
s_add_u32 s39, s39, 0x40000
Mv 176,144,176
Rb 40,16,40960
s_add_u32 m0, s37, 16384
Mv 208,144,180
buffer_load_dwordx4 v12, s[8:11], s39 offen offset:0 lds
Mv 240,144,184
Rb 44,16,43008
Ma 208,144,188
Ma 212,148,188
Rb 48,16,49152
Mv 244,148,184
Mv 212,148,180
Rb 52,16,51200
Mv 180,148,176
buffer_load_dwordx4 v13, s[8:11], s39 offen offset:1024 lds
Mv 148,148,172
Rb 56,16,57344
Mv 116,148,168
Mv 84,148,164
Rb 60,16,59392
Mv 52,148,160
Mv 56,152,160
Mv 88,152,164
buffer_load_dwordx4 v14, s[8:11], s39 offen offset:2048 lds
Mv 120,152,168
Mv 152,152,172
Mv 184,152,176
Mv 216,152,180
Mv 248,152,184
Ma 216,152,188
buffer_load_dwordx4 v15, s[8:11], s39 offen offset:3072 lds
Ma 220,156,188
Mv 252,156,184
Mv 220,156,180
Mv 188,156,176
buffer_store_dwordx4 a[92:95], v3, s[20:23], s51 offen offset:3072 sc0 sc1
Mv 156,156,172
Mv 124,156,168
Mv 92,156,164
Mv 60,156,160
s_waitcnt vmcnt(25) lgkmcnt(0)
s_barrier
Mv 32,0,32
Rt 128,18,49152
Mv 64,0,36
Mv 96,0,40
Rt 130,18,51200
Mv 128,0,44
s_add_u32 s39, s39, 0x40000
Mv 160,0,48
)" R"(
Rt 132,19,49152
s_add_u32 m0, s37, 32768
Mv 192,0,52
buffer_load_dwordx4 v12, s[8:11], s39 offen offset:0 lds
Mv 224,0,56
Rt 134,19,51200
Ma 192,0,60
Ma 196,4,60
Rt 136,20,49152
Mv 228,4,56
s_add_u32 s51, s51, 0x10000
buffer_store_dwordx4 a[96:99], v2, s[20:23], s51 offen offset:0 sc0 sc1
Mv 196,4,52
Rt 138,20,51200
Mv 164,4,48
Mv 132,4,44
Rt 140,21,49152
Mv 100,4,40
buffer_load_dwordx4 v13, s[8:11], s39 offen offset:1024 lds
Mv 68,4,36
Rt 142,21,51200
Mv 36,4,32
Mv 40,8,32
Rt 144,22,49152
Mv 72,8,36
buffer_store_dwordx4 a[100:103], v2, s[20:23], s51 offen offset:1024 sc0 sc1
Mv 104,8,40
Rt 146,22,51200
Mv 136,8,44
Mv 168,8,48
Rt 148,23,49152
Mv 200,8,52
buffer_load_dwordx4 v14, s[8:11], s39 offen offset:2048 lds
Mv 232,8,56
Rt 150,23,51200
Ma 200,8,60
Ma 204,12,60
Rt 152,24,49152
Mv 236,12,56
buffer_store_dwordx4 a[104:107], v2, s[20:23], s51 offen offset:2048 sc0 sc1
Mv 204,12,52
Rt 154,24,51200
Mv 172,12,48
Mv 140,12,44
Rt 156,25,49152
Mv 108,12,40
buffer_load_dwordx4 v15, s[8:11], s39 offen offset:3072 lds
Mv 76,12,36
Rt 158,25,51200
Mv 44,12,32
Mv 48,16,32
Rb 160,17,32768
Mv 80,16,36
buffer_store_dwordx4 a[108:111], v2, s[20:23], s51 offen offset:3072 sc0 sc1
Mv 112,16,40
Rb 164,17,34816
s_mov_b64 s[16:17], s[70:71]
Mv 144,16,44
s_mov_b32 s18, 0x188000
Mv 176,16,48
Rb 168,17,40960
Mv 208,16,52
s_cmp_eq_u32 s33, 0
s_cmov_b32 s18, 0x380000
)" R"(
Mv 240,16,56
Rb 172,17,43008
s_mul_i32 s43, s2, 3072
Ma 208,16,60
s_cmp_eq_u32 s33, 0
s_cselect_b32 s44, 3, 6
Ma 212,20,60
Rb 176,17,49152
s_mul_i32 s44, s44, 0x60000
Mv 244,20,56
buffer_store_dwordx4 a[112:115], v3, s[20:23], s51 offen offset:0 sc0 sc1
Mv 212,20,52
Rb 180,17,51200
s_sub_u32 s43, s43, s44
Mv 180,20,48
v_min_u32 v26, 23, v0
v_lshlrev_b32 v26, 7, v26
Mv 148,20,44
Rb 184,17,57344
v_and_b32 v5, 7, v0
Mv 116,20,40
v_lshlrev_b32 v5, 7, v5
Mv 84,20,36
Rb 188,17,59392
Mv 52,20,32
Mv 56,24,32
Mv 88,24,36
buffer_store_dwordx4 a[116:119], v3, s[20:23], s51 offen offset:1024 sc0 sc1
Mv 120,24,40
Mv 152,24,44
Mv 184,24,48
Mv 216,24,52
Mv 248,24,56
Ma 216,24,60
Ma 220,28,60
Mv 252,28,56
buffer_store_dwordx4 a[120:123], v3, s[20:23], s51 offen offset:2048 sc0 sc1
Mv 220,28,52
Mv 188,28,48
Mv 156,28,44
Mv 124,28,40
Mv 92,28,36
Mv 60,28,32
s_waitcnt vmcnt(16) lgkmcnt(0)
s_barrier
Mv 32,128,160
Rt 0,18,0
Mv 64,128,164
s_add_u32 s38, s38, 0xffff0080
Mv 96,128,168
Rt 2,18,2048
s_add_u32 m0, s36, 32768
Mv 128,128,172
buffer_load_dwordx4 v8, s[4:7], s38 offen offset:0 lds
Mv 160,128,176
Rt 4,19,0
Mv 192,128,180
Mv 224,128,184
Rt 6,19,2048
Ma 192,128,188
buffer_load_dwordx4 v9, s[4:7], s38 offen offset:1024 lds
Ma 196,132,188
Rt 8,20,0
Mv 228,132,184
Mv 196,132,180
)" R"(
Rt 10,20,2048
Mv 164,132,176
buffer_load_dwordx4 v10, s[4:7], s38 offen offset:2048 lds
Mv 132,132,172
Rt 12,21,0
Mv 100,132,168
Mv 68,132,164
Rt 14,21,2048
Mv 36,132,160
buffer_load_dwordx4 v11, s[4:7], s38 offen offset:3072 lds
Mv 40,136,160
Rt 16,22,0
Mv 72,136,164
s_add_u32 s38, s38, 0x10000
Mv 104,136,168
Rt 18,22,2048
s_add_u32 m0, s36, 36864
Mv 136,136,172
buffer_load_dwordx4 v8, s[4:7], s38 offen offset:0 lds
Mv 168,136,176
Rt 20,23,0
Mv 200,136,180
Mv 232,136,184
Rt 22,23,2048
Ma 200,136,188
buffer_load_dwordx4 v9, s[4:7], s38 offen offset:1024 lds
Ma 204,140,188
Rt 24,24,0
Mv 236,140,184
Mv 204,140,180
Rt 26,24,2048
Mv 172,140,176
buffer_load_dwordx4 v10, s[4:7], s38 offen offset:2048 lds
Mv 140,140,172
Rt 28,25,0
Mv 108,140,168
Mv 76,140,164
Rt 30,25,2048
v_accvgpr_write_b32 a224, -1
Mv 44,140,160
buffer_load_dwordx4 v11, s[4:7], s38 offen offset:3072 lds
Mv 48,144,160
Rb 32,16,0
v_accvgpr_write_b32 a225, -1
Mv 80,144,164
v_accvgpr_write_b32 a226, -1
v_accvgpr_write_b32 a227, -1
Mv 112,144,168
Rb 36,16,2048
s_add_u32 s88, s73, 0x4000
Mv 144,144,172
s_add_u32 s39, s39, 0x40000
s_mov_b32 s40, 6
Mv 176,144,176
Rb 40,16,8192
s_add_u32 m0, s37, 49152
Mv 208,144,180
buffer_load_dwordx4 v12, s[8:11], s39 offen offset:0 lds
Mv 240,144,184
Rb 44,16,10240
s_mov_b32 s94, 0
Ma 208,144,188
Ma 212,148,188
Rb 48,16,16384
)" R"(
Mv 244,148,184
Mv 212,148,180
Rb 52,16,18432
Mv 180,148,176
buffer_load_dwordx4 v13, s[8:11], s39 offen offset:1024 lds
Mv 148,148,172
Rb 56,16,24576
Mv 116,148,168
Mv 84,148,164
Rb 60,16,26624
Mv 52,148,160
Mv 56,152,160
Mv 88,152,164
buffer_load_dwordx4 v14, s[8:11], s39 offen offset:2048 lds
Mv 120,152,168
Mv 152,152,172
Mv 184,152,176
Mv 216,152,180
Mv 248,152,184
Ma 216,152,188
buffer_load_dwordx4 v15, s[8:11], s39 offen offset:3072 lds
Ma 220,156,188
Mv 252,156,184
Mv 220,156,180
Mv 188,156,176
buffer_store_dwordx4 a[124:127], v3, s[20:23], s51 offen offset:3072 sc0 sc1
Mv 156,156,172
Mv 124,156,168
Mv 92,156,164
Mv 60,156,160
s_waitcnt vmcnt(25) lgkmcnt(0)
s_barrier
.LzPBK:
Mv 32,0,32
Rt 128,18,16384
Mv 64,0,36
Mv 96,0,40
Rt 130,18,18432
Mv 128,0,44
s_add_u32 s39, s39, 0x40000
Mv 160,0,48
Rt 132,19,16384
s_add_u32 m0, s37, 0
Mv 192,0,52
buffer_load_dwordx4 v12, s[8:11], s39 offen offset:0 lds
Mv 224,0,56
Rt 134,19,18432
Ma 192,0,60
Ma 196,4,60
Rt 136,20,16384
Mv 228,4,56
Mv 196,4,52
Rt 138,20,18432
Mv 164,4,48
Mv 132,4,44
Rt 140,21,16384
Mv 100,4,40
buffer_load_dwordx4 v13, s[8:11], s39 offen offset:1024 lds
Mv 68,4,36
Rt 142,21,18432
Mv 36,4,32
Mv 40,8,32
Rt 144,22,16384
Mv 72,8,36
Mv 104,8,40
)" R"(
Rt 146,22,18432
Mv 136,8,44
Mv 168,8,48
Rt 148,23,16384
Mv 200,8,52
buffer_load_dwordx4 v14, s[8:11], s39 offen offset:2048 lds
Mv 232,8,56
Rt 150,23,18432
Ma 200,8,60
Ma 204,12,60
Rt 152,24,16384
Mv 236,12,56
.LzH3:
s_cbranch_execz .LzH12
Mv 204,12,52
Rt 154,24,18432
Mv 172,12,48
Mv 140,12,44
Rt 156,25,16384
Mv 108,12,40
buffer_load_dwordx4 v15, s[8:11], s39 offen offset:3072 lds
Mv 76,12,36
Rt 158,25,18432
Mv 44,12,32
Mv 48,16,32
Rb 160,17,0
Mv 80,16,36
Mv 112,16,40
Rb 164,17,2048
Mv 144,16,44
Mv 176,16,48
Rb 168,17,8192
Mv 208,16,52
buffer_store_dwordx4 a[224:227], v1, s[20:23], s72 offen sc0 sc1
Mv 240,16,56
Rb 172,17,10240
s_add_u32 s72, s72, 1024
Ma 208,16,60
Ma 212,20,60
Rb 176,17,16384
Mv 244,20,56
Mv 212,20,52
Rb 180,17,18432
Mv 180,20,48
Mv 148,20,44
Rb 184,17,24576
Mv 116,20,40
Mv 84,20,36
Rb 188,17,26624
Mv 52,20,32
Mv 56,24,32
Mv 88,24,36
buffer_store_dwordx4 a[224:227], v1, s[20:23], s88 offen sc0 sc1
Mv 120,24,40
s_add_u32 s88, s88, 1024
Mv 152,24,44
Mv 184,24,48
Mv 216,24,52
Mv 248,24,56
Ma 216,24,60
Ma 220,28,60
Mv 252,28,56
Mv 220,28,52
Mv 188,28,48
)" R"(
Mv 156,28,44
Mv 124,28,40
Mv 92,28,36
Mv 60,28,32
s_waitcnt vmcnt(11) lgkmcnt(0)
s_barrier
Mv 32,128,160
Rt 0,18,32768
Mv 64,128,164
s_add_u32 s38, s38, 0xffff0080
Mv 96,128,168
Rt 2,18,34816
s_add_u32 m0, s36, 0
Mv 128,128,172
buffer_load_dwordx4 v8, s[4:7], s38 offen offset:0 lds
Mv 160,128,176
Rt 4,19,32768
Mv 192,128,180
Mv 224,128,184
Rt 6,19,34816
Ma 192,128,188
buffer_load_dwordx4 v9, s[4:7], s38 offen offset:1024 lds
Ma 196,132,188
Rt 8,20,32768
Mv 228,132,184
Mv 196,132,180
Rt 10,20,34816
Mv 164,132,176
buffer_load_dwordx4 v10, s[4:7], s38 offen offset:2048 lds
Mv 132,132,172
Rt 12,21,32768
Mv 100,132,168
Mv 68,132,164
Rt 14,21,34816
Mv 36,132,160
buffer_load_dwordx4 v11, s[4:7], s38 offen offset:3072 lds
Mv 40,136,160
Rt 16,22,32768
Mv 72,136,164
s_add_u32 s38, s38, 0x10000
Mv 104,136,168
Rt 18,22,34816
s_add_u32 m0, s36, 4096
Mv 136,136,172
buffer_load_dwordx4 v8, s[4:7], s38 offen offset:0 lds
Mv 168,136,176
Rt 20,23,32768
Mv 200,136,180
Mv 232,136,184
Rt 22,23,34816
Ma 200,136,188
buffer_load_dwordx4 v9, s[4:7], s38 offen offset:1024 lds
Ma 204,140,188
Rt 24,24,32768
Mv 236,140,184
Mv 204,140,180
Rt 26,24,34816
Mv 172,140,176
buffer_load_dwordx4 v10, s[4:7], s38 offen offset:2048 lds
Mv 140,140,172
Rt 28,25,32768
Mv 108,140,168
Mv 76,140,164
Rt 30,25,34816
Mv 44,140,160
)" R"(
buffer_load_dwordx4 v11, s[4:7], s38 offen offset:3072 lds
Mv 48,144,160
Rb 32,16,32768
Mv 80,144,164
Mv 112,144,168
Rb 36,16,34816
Mv 144,144,172
s_add_u32 s39, s39, 0x40000
Mv 176,144,176
Rb 40,16,40960
s_add_u32 m0, s37, 16384
Mv 208,144,180
buffer_load_dwordx4 v12, s[8:11], s39 offen offset:0 lds
Mv 240,144,184
Rb 44,16,43008
Ma 208,144,188
Ma 212,148,188
Rb 48,16,49152
Mv 244,148,184
Mv 212,148,180
Rb 52,16,51200
Mv 180,148,176
buffer_load_dwordx4 v13, s[8:11], s39 offen offset:1024 lds
Mv 148,148,172
Rb 56,16,57344
Mv 116,148,168
Mv 84,148,164
Rb 60,16,59392
Mv 52,148,160
Mv 56,152,160
Mv 88,152,164
buffer_load_dwordx4 v14, s[8:11], s39 offen offset:2048 lds
Mv 120,152,168
Mv 152,152,172
Mv 184,152,176
Mv 216,152,180
Mv 248,152,184
Ma 216,152,188
buffer_load_dwordx4 v15, s[8:11], s39 offen offset:3072 lds
Ma 220,156,188
Mv 252,156,184
s_add_u32 s43, s43, 0x60000
Mv 220,156,180
Mv 188,156,176
buffer_load_dword v27, v26, s[16:19], s43 offen
Mv 156,156,172
Mv 124,156,168
Mv 92,156,164
Mv 60,156,160
s_waitcnt vmcnt(19) lgkmcnt(0)
s_barrier
buffer_store_dword v6, v7, s[20:23], s52 offen offset:0 sc0 sc1
s_mov_b32 s52, 0x40000000
Mv 32,0,32
Rt 128,18,49152
Mv 64,0,36
Mv 96,0,40
Rt 130,18,51200
Mv 128,0,44
s_add_u32 s39, s39, 0x40000
Mv 160,0,48
Rt 132,19,49152
s_add_u32 m0, s37, 32768
Mv 192,0,52
buffer_load_dwordx4 v12, s[8:11], s39 offen offset:0 lds
)" R"(
Mv 224,0,56
Rt 134,19,51200
Ma 192,0,60
Ma 196,4,60
Rt 136,20,49152
Mv 228,4,56
Mv 196,4,52
Rt 138,20,51200
Mv 164,4,48
Mv 132,4,44
Rt 140,21,49152
Mv 100,4,40
buffer_load_dwordx4 v13, s[8:11], s39 offen offset:1024 lds
Mv 68,4,36
Rt 142,21,51200
Mv 36,4,32
Mv 40,8,32
Rt 144,22,49152
Mv 72,8,36
Mv 104,8,40
Rt 146,22,51200
Mv 136,8,44
Mv 168,8,48
Rt 148,23,49152
Mv 200,8,52
buffer_load_dwordx4 v14, s[8:11], s39 offen offset:2048 lds
Mv 232,8,56
Rt 150,23,51200
Ma 200,8,60
Ma 204,12,60
Rt 152,24,49152
Mv 236,12,56
Mv 204,12,52
Rt 154,24,51200
Mv 172,12,48
Mv 140,12,44
Rt 156,25,49152
Mv 108,12,40
buffer_load_dwordx4 v15, s[8:11], s39 offen offset:3072 lds
Mv 76,12,36
Rt 158,25,51200
Mv 44,12,32
Mv 48,16,32
Rb 160,17,32768
Mv 80,16,36
Mv 112,16,40
Rb 164,17,34816
Mv 144,16,44
Mv 176,16,48
Rb 168,17,40960
Mv 208,16,52
buffer_store_dwordx4 a[224:227], v1, s[20:23], s72 offen sc0 sc1
Mv 240,16,56
Rb 172,17,43008
s_add_u32 s72, s72, 1024
Ma 208,16,60
Ma 212,20,60
Rb 176,17,49152
Mv 244,20,56
Mv 212,20,52
Rb 180,17,51200
Mv 180,20,48
Mv 148,20,44
Rb 184,17,57344
)" R"(
Mv 116,20,40
Mv 84,20,36
Rb 188,17,59392
Mv 52,20,32
Mv 56,24,32
Mv 88,24,36
buffer_store_dwordx4 a[224:227], v1, s[20:23], s88 offen sc0 sc1
Mv 120,24,40
s_add_u32 s88, s88, 1024
Mv 152,24,44
Mv 184,24,48
Mv 216,24,52
Mv 248,24,56
Ma 216,24,60
Ma 220,28,60
Mv 252,28,56
Mv 220,28,52
Mv 188,28,48
Mv 156,28,44
Mv 124,28,40
Mv 92,28,36
s_cmp_eq_u32 s40, 3
s_cmov_b32 s72, 0x40000000
s_cmov_b32 s88, 0x40000000
Mv 60,28,32
s_waitcnt vmcnt(12) lgkmcnt(0)
s_barrier
Mv 32,128,160
Rt 0,18,0
Mv 64,128,164
s_add_u32 s38, s38, 0xffff0080
Mv 96,128,168
Rt 2,18,2048
s_add_u32 m0, s36, 32768
Mv 128,128,172
buffer_load_dwordx4 v8, s[4:7], s38 offen offset:0 lds
Mv 160,128,176
Rt 4,19,0
Mv 192,128,180
Mv 224,128,184
Rt 6,19,2048
Ma 192,128,188
buffer_load_dwordx4 v9, s[4:7], s38 offen offset:1024 lds
Ma 196,132,188
Rt 8,20,0
Mv 228,132,184
Mv 196,132,180
Rt 10,20,2048
Mv 164,132,176
buffer_load_dwordx4 v10, s[4:7], s38 offen offset:2048 lds
Mv 132,132,172
Rt 12,21,0
Mv 100,132,168
Mv 68,132,164
Rt 14,21,2048
Mv 36,132,160
buffer_load_dwordx4 v11, s[4:7], s38 offen offset:3072 lds
Mv 40,136,160
Rt 16,22,0
Mv 72,136,164
s_add_u32 s38, s38, 0x10000
Mv 104,136,168
Rt 18,22,2048
s_add_u32 m0, s36, 36864
Mv 136,136,172
)" R"(
buffer_load_dwordx4 v8, s[4:7], s38 offen offset:0 lds
Mv 168,136,176
Rt 20,23,0
Mv 200,136,180
Mv 232,136,184
Rt 22,23,2048
Ma 200,136,188
buffer_load_dwordx4 v9, s[4:7], s38 offen offset:1024 lds
Ma 204,140,188
Rt 24,24,0
Mv 236,140,184
Mv 204,140,180
Rt 26,24,2048
Mv 172,140,176
buffer_load_dwordx4 v10, s[4:7], s38 offen offset:2048 lds
Mv 140,140,172
Rt 28,25,0
Mv 108,140,168
Mv 76,140,164
Rt 30,25,2048
Mv 44,140,160
buffer_load_dwordx4 v11, s[4:7], s38 offen offset:3072 lds
Mv 48,144,160
Rb 32,16,0
Mv 80,144,164
Mv 112,144,168
Rb 36,16,2048
Mv 144,144,172
s_add_u32 s39, s39, 0x40000
Mv 176,144,176
Rb 40,16,8192
s_add_u32 m0, s37, 49152
Mv 208,144,180
buffer_load_dwordx4 v12, s[8:11], s39 offen offset:0 lds
Mv 240,144,184
Rb 44,16,10240
Ma 208,144,188
Ma 212,148,188
Rb 48,16,16384
Mv 244,148,184
Mv 212,148,180
Rb 52,16,18432
Mv 180,148,176
buffer_load_dwordx4 v13, s[8:11], s39 offen offset:1024 lds
Mv 148,148,172
Rb 56,16,24576
Mv 116,148,168
Mv 84,148,164
Rb 60,16,26624
Mv 52,148,160
Mv 56,152,160
Mv 88,152,164
buffer_load_dwordx4 v14, s[8:11], s39 offen offset:2048 lds
Mv 120,152,168
Mv 152,152,172
Mv 184,152,176
Mv 216,152,180
Mv 248,152,184
Ma 216,152,188
buffer_load_dwordx4 v15, s[8:11], s39 offen offset:3072 lds
Ma 220,156,188
Mv 252,156,184
s_add_u32 s43, s43, 0x60000
Mv 220,156,180
)" R"(
s_sub_u32 s40, s40, 1
Mv 188,156,176
buffer_load_dword v27, v26, s[16:19], s43 offen
Mv 156,156,172
Mv 124,156,168
Mv 92,156,164
Mv 60,156,160
s_cmp_lg_u32 s40, 0
s_waitcnt vmcnt(21) lgkmcnt(0)
s_barrier
s_cbranch_scc1 .LzPBK
.LzPBKX:
Mv 32,0,32
Rt 128,18,16384
Mv 64,0,36
buffer_load_dword v4, v5, s[20:23], s50 offen offset:0 sc0 sc1
Mv 96,0,40
Rt 130,18,18432
Mv 128,0,44
Mv 160,0,48
Rt 132,19,16384
Mv 192,0,52
Mv 224,0,56
Rt 134,19,18432
Ma 192,0,60
Ma 196,4,60
Rt 136,20,16384
Mv 228,4,56
Mv 196,4,52
Rt 138,20,18432
Mv 164,4,48
Mv 132,4,44
Rt 140,21,16384
Mv 100,4,40
Mv 68,4,36
Rt 142,21,18432
Mv 36,4,32
Mv 40,8,32
Rt 144,22,16384
Mv 72,8,36
Mv 104,8,40
Rt 146,22,18432
Mv 136,8,44
Mv 168,8,48
Rt 148,23,16384
Mv 200,8,52
Mv 232,8,56
Rt 150,23,18432
Ma 200,8,60
Ma 204,12,60
Rt 152,24,16384
Mv 236,12,56
Mv 204,12,52
Rt 154,24,18432
Mv 172,12,48
Mv 140,12,44
Rt 156,25,16384
Mv 108,12,40
Mv 76,12,36
Rt 158,25,18432
Mv 44,12,32
Mv 48,16,32
Rb 160,17,0
Mv 80,16,36
Mv 112,16,40
)" R"(
Rb 164,17,2048
Mv 144,16,44
Mv 176,16,48
Rb 168,17,8192
Mv 208,16,52
Mv 240,16,56
Rb 172,17,10240
Ma 208,16,60
s_waitcnt vmcnt(0)
v_cmp_eq_u32_e32 vcc, s3, v4
s_andn2_b64 s[28:29], -1, vcc
s_cbranch_scc1 .Lzsp14
.Lzok14:
Ma 212,20,60
Rb 176,17,16384
Mv 244,20,56
s_lshl3_add_u32 s29, s34, s35
s_lshl2_add_u32 s29, s29, s33
Mv 212,20,52
Rb 180,17,18432
s_add_u32 m0, s36, 131072
Mv 180,20,48
buffer_load_dwordx4 v2, s[20:23], s56 offen offset:0 sc1 lds
Mv 148,20,44
Rb 184,17,24576
s_mul_i32 s83, s29, 7168
Mv 116,20,40
s_lshl_b32 s82, s34, 21
s_add_u32 s82, s82, 0x6000000
Mv 84,20,36
Rb 188,17,26624
s_add_u32 s82, s82, s84
Mv 52,20,32
s_mov_b32 s24, s80
s_mov_b32 s25, s81
Mv 56,24,32
s_mov_b32 s26, 0x380000
s_mov_b32 s27, 0x20000
Mv 88,24,36
buffer_load_dwordx4 v2, s[20:23], s56 offen offset:1024 sc1 lds
Mv 120,24,40
s_add_u32 s28, s33, 0
s_lshl2_add_u32 s28, s35, s28
Mv 152,24,44
s_mul_i32 s29, s28, 293
s_lshr_b32 s29, s29, 11
Mv 184,24,48
s_mul_i32 s29, s29, 9
s_add_u32 s29, s29, s28
Mv 216,24,52
s_lshl_b32 s29, s29, 10
s_add_u32 s4, s29, s82
Mv 248,24,56
s_add_u32 s28, s33, 4
s_lshl3_add_u32 s28, s28, s35
Ma 216,24,60
buffer_load_dwordx4 v2, s[20:23], s56 offen offset:2048 sc1 lds
Ma 220,28,60
s_mul_i32 s29, s28, 293
s_lshr_b32 s29, s29, 11
Mv 252,28,56
s_mul_i32 s29, s29, 9
s_add_u32 s29, s29, s28
Mv 220,28,52
)" R"(
s_lshl_b32 s29, s29, 10
s_add_u32 s5, s29, s82
Mv 188,28,48
s_add_u32 s28, s33, 8
s_lshl3_add_u32 s28, s28, s35
Mv 156,28,44
s_mul_i32 s29, s28, 293
s_lshr_b32 s29, s29, 11
Mv 124,28,40
buffer_load_dwordx4 v2, s[20:23], s56 offen offset:3072 sc1 lds
Mv 92,28,36
s_mul_i32 s29, s29, 9
s_add_u32 s29, s29, s28
Mv 60,28,32
s_waitcnt lgkmcnt(0)
s_barrier
Mv 32,128,160
Rt 0,18,32768
s_lshl_b32 s29, s29, 10
Mv 64,128,164
s_add_u32 s6, s29, s82
s_add_u32 s28, s33, 12
Mv 96,128,168
Rt 2,18,34816
s_add_u32 m0, s36, 0
Mv 128,128,172
buffer_load_dwordx4 v2, s[20:23], s55 offen offset:0 sc1 lds
Mv 160,128,176
Rt 4,19,32768
s_lshl3_add_u32 s28, s28, s35
Mv 192,128,180
s_mul_i32 s29, s28, 293
s_lshr_b32 s29, s29, 11
Mv 224,128,184
Rt 6,19,34816
s_mul_i32 s29, s29, 9
Ma 192,128,188
s_add_u32 s29, s29, s28
s_lshl_b32 s29, s29, 10
Ma 196,132,188
Rt 8,20,32768
buffer_load_dwordx4 v2, s[20:23], s55 offen offset:1024 sc1 lds
.LzH4:
s_cbranch_execz .LzH17
Mv 228,132,184
s_add_u32 s7, s29, s82
Mv 196,132,180
Rt 10,20,34816
Mv 164,132,176
s_cmp_gt_u32 s33, 1
s_cmov_b32 s7, 0x40000000
Mv 132,132,172
Rt 12,21,32768
s_add_u32 s28, s33, 14
Mv 100,132,168
buffer_load_dwordx4 v2, s[20:23], s55 offen offset:2048 sc1 lds
Mv 68,132,164
Rt 14,21,34816
s_lshl3_add_u32 s28, s28, s35
Mv 36,132,160
s_mul_i32 s29, s28, 293
s_lshr_b32 s29, s29, 11
Mv 40,136,160
Rt 16,22,32768
s_mul_i32 s29, s29, 9
)" R"(
Mv 72,136,164
s_add_u32 s29, s29, s28
s_lshl_b32 s29, s29, 10
Mv 104,136,168
Rt 18,22,34816
buffer_load_dwordx4 v2, s[20:23], s55 offen offset:3072 sc1 lds
Mv 136,136,172
s_add_u32 s8, s29, s82
s_add_u32 s28, s33, 18
Mv 168,136,176
Rt 20,23,32768
s_lshl3_add_u32 s28, s28, s35
Mv 200,136,180
s_mul_i32 s29, s28, 293
s_lshr_b32 s29, s29, 11
Mv 232,136,184
Rt 22,23,34816
s_add_u32 m0, s36, 4096
Ma 200,136,188
buffer_load_dwordx4 v3, s[20:23], s55 offen offset:0 sc1 lds
Ma 204,140,188
Rt 24,24,32768
s_mul_i32 s29, s29, 9
Mv 236,140,184
s_add_u32 s29, s29, s28
s_lshl_b32 s29, s29, 10
Mv 204,140,180
Rt 26,24,34816
s_add_u32 s9, s29, s82
Mv 172,140,176
s_add_u32 s28, s33, 22
s_lshl3_add_u32 s28, s28, s35
Mv 140,140,172
Rt 28,25,32768
buffer_load_dwordx4 v3, s[20:23], s55 offen offset:1024 sc1 lds
Mv 108,140,168
s_mul_i32 s29, s28, 293
s_lshr_b32 s29, s29, 11
Mv 76,140,164
Rt 30,25,34816
s_mul_i32 s29, s29, 9
Mv 44,140,160
s_add_u32 s29, s29, s28
s_lshl_b32 s29, s29, 10
Mv 48,144,160
Rb 32,16,32768
s_add_u32 s10, s29, s82
Mv 80,144,164
buffer_load_dwordx4 v3, s[20:23], s55 offen offset:2048 sc1 lds
Mv 112,144,168
Rb 36,16,34816
s_add_u32 s28, s33, 26
Mv 144,144,172
s_lshl3_add_u32 s28, s28, s35
s_mul_i32 s29, s28, 293
Mv 176,144,176
Rb 40,16,40960
s_lshr_b32 s29, s29, 11
Mv 208,144,180
s_mul_i32 s29, s29, 9
s_add_u32 s29, s29, s28
Mv 240,144,184
Rb 44,16,43008
buffer_load_dwordx4 v3, s[20:23], s55 offen offset:3072 sc1 lds
)" R"(
Ma 208,144,188
s_lshl_b32 s29, s29, 10
s_add_u32 s11, s29, s82
Ma 212,148,188
Rb 48,16,49152
Mv 244,148,184
s_cmp_gt_u32 s33, 1
s_cmov_b32 s11, 0x40000000
Mv 212,148,180
Rb 52,16,51200
s_add_u32 m0, s36, 135168
Mv 180,148,176
buffer_load_dwordx4 v3, s[20:23], s56 offen offset:0 sc1 lds
Mv 148,148,172
Rb 56,16,57344
s_and_b32 s28, s34, 1
Mv 116,148,168
s_lshl_b32 s28, s28, 14
s_add_u32 s82, s37, s28
Mv 84,148,164
Rb 60,16,59392
s_add_u32 s82, s82, 0x10000
Mv 52,148,160
Mv 56,152,160
buffer_load_dwordx4 v3, s[20:23], s56 offen offset:1024 sc1 lds
Mv 88,152,164
Mv 120,152,168
Mv 152,152,172
Mv 184,152,176
Mv 216,152,180
buffer_load_dwordx4 v3, s[20:23], s56 offen offset:2048 sc1 lds
Mv 248,152,184
Ma 216,152,188
Ma 220,156,188
Mv 252,156,184
Mv 220,156,180
buffer_load_dwordx4 v3, s[20:23], s56 offen offset:3072 sc1 lds
Mv 188,156,176
Mv 156,156,172
Mv 124,156,168
s_add_u32 s55, s55, 0x10000
s_add_u32 s56, s56, 0x10000
Mv 92,156,164
Mv 60,156,160
s_waitcnt lgkmcnt(4)
s_barrier
Mv 32,0,32
Rt 128,18,49152
Mv 64,0,36
Rt 130,18,51200
Mv 96,0,40
Rt 132,19,49152
s_add_u32 m0, s36, 65536
Mv 128,0,44
Rt 134,19,51200
buffer_load_dwordx4 v2, s[20:23], s56 offen offset:0 sc1 lds
Mv 132,4,44
Rt 136,20,49152
Mv 100,4,40
Rt 138,20,51200
s_add_u32 s50, s50, 1024
Mv 68,4,36
Rt 140,21,49152
s_min_u32 s50, s50, s60
Mv 36,4,32
)" R"(
Rt 142,21,51200
Mv 40,8,32
Rt 144,22,49152
Mv 72,8,36
Rt 146,22,51200
Mv 104,8,40
Rt 148,23,49152
Mv 136,8,44
Rt 150,23,51200
buffer_load_dwordx4 v2, s[20:23], s56 offen offset:1024 sc1 lds
Mv 140,12,44
Rt 152,24,49152
Mv 108,12,40
Rt 154,24,51200
Mv 76,12,36
Rt 156,25,49152
Mv 44,12,32
Rt 158,25,51200
Mv 48,16,32
Rb 160,17,32768
Mv 80,16,36
Rb 164,17,34816
Mv 112,16,40
Rb 168,17,40960
Mv 144,16,44
Rb 172,17,43008
buffer_load_dwordx4 v2, s[20:23], s56 offen offset:2048 sc1 lds
Mv 148,20,44
Rb 176,17,49152
Mv 116,20,40
Rb 180,17,51200
Mv 84,20,36
Rb 184,17,57344
Mv 52,20,32
Rb 188,17,59392
Mv 56,24,32
v_add_u32 v8, 0x10000, v1
v_add_u32 v10, 0x20000, v1
Mv 88,24,36
buffer_load_dword v4, v5, s[20:23], s50 offen offset:0 sc0 sc1
Mv 120,24,40
s_mov_b32 s93, 2
s_mov_b32 s94, 0
Mv 152,24,44
buffer_load_dwordx4 v2, s[20:23], s56 offen offset:3072 sc1 lds
Mv 156,28,44
Mv 124,28,40
Mv 92,28,36
Mv 60,28,32
s_waitcnt lgkmcnt(0)
s_barrier
Mv 32,128,160
Mv 36,132,160
Mv 40,136,160
s_add_u32 m0, s36, 32768
Mv 44,140,160
buffer_load_dwordx4 v2, s[20:23], s55 offen offset:0 sc1 lds
Mv 48,144,160
Mv 52,148,160
s_nop 0
Cv 32,32,33
Cv 33,34,35
Mv 56,152,160
Cv 34,36,37
Cv 35,38,39
)" R"(
Mv 60,156,160
Cv 40,40,41
Cv 41,42,43
Mv 64,128,164
Cv 42,44,45
Cv 43,46,47
Mv 68,132,164
Cv 48,48,49
Cv 49,50,51
Mv 72,136,164
Cv 50,52,53
Cv 51,54,55
Mv 76,140,164
buffer_load_dwordx4 v2, s[20:23], s55 offen offset:1024 sc1 lds
Mv 80,144,164
Cv 56,56,57
Cv 57,58,59
Mv 84,148,164
Cv 58,60,61
Cv 59,62,63
Mv 88,152,164
Cv 64,64,65
Cv 65,66,67
Mv 92,156,164
Cv 66,68,69
Cv 67,70,71
Mv 96,128,168
Cv 72,72,73
Cv 73,74,75
Mv 100,132,168
Cv 74,76,77
Cv 75,78,79
Mv 104,136,168
Cv 80,80,81
Cv 81,82,83
Mv 108,140,168
buffer_load_dwordx4 v2, s[20:23], s55 offen offset:2048 sc1 lds
Mv 112,144,168
Cv 82,84,85
Cv 83,86,87
Mv 116,148,168
Cv 88,88,89
Cv 89,90,91
Mv 120,152,168
Cv 90,92,93
Cv 91,94,95
Mv 124,156,168
buffer_store_dwordx4 v[32:35], v2, s[20:23], s45 offen offset:0 sc0 sc1
Mv 128,128,172
Mv 132,132,172
Mv 136,136,172
Mv 140,140,172
buffer_load_dwordx4 v2, s[20:23], s55 offen offset:3072 sc1 lds
Mv 144,144,172
Mv 148,148,172
s_nop 1
Cv 128,128,129
Cv 129,130,131
Mv 152,152,172
Cv 130,132,133
Cv 131,134,135
Mv 156,156,172
buffer_store_dwordx4 v[40:43], v2, s[20:23], s45 offen offset:1024 sc0 sc1
Mv 160,0,48
)" R"(
Cv 136,136,137
Cv 137,138,139
Mv 160,128,176
Cv 138,140,141
Cv 139,142,143
Mv 164,4,48
s_add_u32 m0, s36, 36864
Cv 144,144,145
Mv 164,132,176
buffer_load_dwordx4 v3, s[20:23], s55 offen offset:0 sc1 lds
Mv 168,8,48
Cv 145,146,147
Cv 146,148,149
Mv 168,136,176
Cv 147,150,151
Cv 152,152,153
Mv 172,12,48
Cv 153,154,155
Cv 154,156,157
Mv 172,140,176
buffer_store_dwordx4 v[48:51], v2, s[20:23], s45 offen offset:2048 sc0 sc1
Mv 176,16,48
Cv 155,158,159
Cv 132,96,97
Mv 176,144,176
Cv 133,98,99
Cv 134,100,101
Mv 180,20,48
Cv 135,102,103
Cv 140,104,105
Mv 180,148,176
buffer_load_dwordx4 v3, s[20:23], s55 offen offset:1024 sc1 lds
Mv 184,24,48
Cv 141,106,107
Cv 142,108,109
Mv 184,152,176
Cv 143,110,111
Cv 148,112,113
Mv 188,28,48
Cv 149,114,115
Cv 150,116,117
Mv 188,156,176
buffer_store_dwordx4 v[56:59], v2, s[20:23], s45 offen offset:3072 sc0 sc1
Mv 192,0,52
Cv 151,118,119
Cv 156,120,121
Mv 192,128,180
Cv 157,122,123
Cv 158,124,125
Mv 196,4,52
Cv 159,126,127
Cv 160,160,161
Mv 196,132,180
buffer_load_dwordx4 v3, s[20:23], s55 offen offset:2048 sc1 lds
Mv 200,8,52
Cv 161,162,163
Cv 162,164,165
Mv 200,136,180
Cv 163,166,167
v_accvgpr_write_b32 a224, v160
Mv 204,12,52
v_accvgpr_write_b32 a225, v161
v_accvgpr_write_b32 a226, v162
Mv 204,140,180
)" R"(
buffer_store_dwordx4 v[64:67], v3, s[20:23], s45 offen offset:0 sc0 sc1
Mv 208,16,52
v_accvgpr_write_b32 a227, v163
Cv 168,168,169
Mv 208,144,180
Cv 169,170,171
Cv 170,172,173
Mv 212,20,52
Cv 171,174,175
v_accvgpr_write_b32 a228, v168
Mv 212,148,180
buffer_load_dwordx4 v3, s[20:23], s55 offen offset:3072 sc1 lds
Mv 216,24,52
v_accvgpr_write_b32 a229, v169
v_accvgpr_write_b32 a230, v170
Mv 216,152,180
v_accvgpr_write_b32 a231, v171
Cv 176,176,177
Mv 220,28,52
Cv 177,178,179
Cv 178,180,181
Mv 220,156,180
buffer_store_dwordx4 v[72:75], v3, s[20:23], s45 offen offset:1024 sc0 sc1
Mv 224,0,56
Cv 179,182,183
v_accvgpr_write_b32 a232, v176
Ma 192,0,60
v_accvgpr_write_b32 a233, v177
v_accvgpr_write_b32 a234, v178
Ma 196,4,60
s_add_u32 m0, s36, 69632
v_accvgpr_write_b32 a235, v179
Mv 228,4,56
buffer_load_dwordx4 v3, s[20:23], s56 offen offset:0 sc1 lds
Mv 232,8,56
Cv 184,184,185
Cv 185,186,187
Ma 200,8,60
Cv 186,188,189
Cv 187,190,191
Ma 204,12,60
v_accvgpr_write_b32 a236, v184
v_accvgpr_write_b32 a237, v185
Mv 236,12,56
s_waitcnt vmcnt(16)
s_barrier
Mv 240,16,56
v_cmp_eq_u32_e32 vcc, s3, v4
s_andn2_b64 s[28:29], -1, vcc
s_cbranch_scc1 .Lzsp15
.Lzok15:
Ma 208,16,60
buffer_store_dwordx4 v[80:83], v3, s[20:23], s45 offen offset:2048 sc0 sc1
Ma 212,20,60
v_accvgpr_write_b32 a238, v186
v_accvgpr_write_b32 a239, v187
Mv 244,20,56
buffer_load_dwordx4 v3, s[20:23], s56 offen offset:1024 sc1 lds
Mv 248,24,56
Cv 192,192,193
Cv 193,194,195
Ma 216,24,60
Cv 194,196,197
Cv 195,198,199
Ma 220,28,60
)" R"(
v_accvgpr_write_b32 a240, v192
v_accvgpr_write_b32 a241, v193
Mv 252,28,56
buffer_store_dwordx4 v[88:91], v3, s[20:23], s45 offen offset:3072 sc0 sc1
Mv 224,128,184
Rb 0,1,0
v_accvgpr_write_b32 a242, v194
Mv 228,132,184
Rb 4,1,1024
v_accvgpr_write_b32 a243, v195
Mv 232,136,184
Rb 8,1,2048
Cv 200,200,201
Mv 236,140,184
buffer_load_dwordx4 v3, s[20:23], s56 offen offset:2048 sc1 lds
Rb 12,1,3072
Mv 240,144,184
Rb 16,1,8192
Cv 201,202,203
Mv 244,148,184
Rb 20,1,9216
Cv 202,204,205
Mv 248,152,184
Rb 24,1,10240
Cv 203,206,207
Mv 252,156,184
Rb 28,1,11264
v_accvgpr_write_b32 a244, v200
Ma 192,128,188
Rb 32,1,4096
v_accvgpr_write_b32 a245, v201
Ma 196,132,188
Rb 36,1,5120
v_accvgpr_write_b32 a246, v202
Ma 200,136,188
Rb 40,1,6144
v_accvgpr_write_b32 a247, v203
Ma 204,140,188
buffer_load_dwordx4 v3, s[20:23], s56 offen offset:3072 sc1 lds
Rb 44,1,7168
Ma 208,144,188
Ma 212,148,188
Ma 216,152,188
Ma 220,156,188
s_add_u32 s56, s56, 0x10000
s_branch .LzFB
.LzH5:
s_cbranch_execz .LzH10
.LzBV:
Mz 32,32,0
Rt 128,18,16384
v_mul_f32_e32 v64, s54, v64
Mz 36,36,0
v_mul_f32_e32 v65, s54, v65
Cv 64,64,65
Mz 96,40,0
Rt 130,18,18432
v_accvgpr_write_b32 a112, v64
Mz 100,44,0
s_add_u32 s39, s39, 0x40000
v_mul_f32_e32 v66, s54, v66
Mz 160,48,0
Rt 132,19,16384
s_add_u32 m0, s37, 0
)" R"(
Mz 164,52,0
buffer_load_dwordx4 v12, s[8:11], s39 offen offset:0 lds
Mz 224,56,0
Rt 134,19,18432
v_mul_f32_e32 v67, s54, v67
Mz 228,60,0
Cv 66,66,67
v_accvgpr_write_b32 a113, v66
Mz 236,60,4
Rt 136,20,16384
v_mul_f32_e32 v68, s54, v68
Mz 232,56,4
v_mul_f32_e32 v69, s54, v69
Cv 68,68,69
buffer_store_dwordx4 a[64:67], v2, s[20:23], s51 offen offset:0 sc0 sc1
Mz 172,52,4
Rt 138,20,18432
v_accvgpr_write_b32 a114, v68
Mz 168,48,4
v_mul_f32_e32 v70, s54, v70
v_mul_f32_e32 v71, s54, v71
Mz 108,44,4
Rt 140,21,16384
Cv 70,70,71
Mz 104,40,4
buffer_load_dwordx4 v13, s[8:11], s39 offen offset:1024 lds
Mz 44,36,4
Rt 142,21,18432
v_accvgpr_write_b32 a115, v70
Mz 40,32,4
v_mul_f32_e32 v72, s54, v72
v_mul_f32_e32 v73, s54, v73
Mz 48,32,8
Rt 144,22,16384
Cv 72,72,73
Mz 52,36,8
v_accvgpr_write_b32 a116, v72
v_mul_f32_e32 v74, s54, v74
buffer_store_dwordx4 a[68:71], v2, s[20:23], s51 offen offset:1024 sc0 sc1
Mz 112,40,8
Rt 146,22,18432
v_mul_f32_e32 v75, s54, v75
Mz 116,44,8
Cv 74,74,75
v_accvgpr_write_b32 a117, v74
Mz 176,48,8
Rt 148,23,16384
v_mul_f32_e32 v76, s54, v76
Mz 180,52,8
buffer_load_dwordx4 v14, s[8:11], s39 offen offset:2048 lds
Mz 240,56,8
Rt 150,23,18432
v_mul_f32_e32 v77, s54, v77
Mz 244,60,8
Cv 76,76,77
v_accvgpr_write_b32 a118, v76
Mz 252,60,12
Rt 152,24,16384
v_mul_f32_e32 v78, s54, v78
Mz 248,56,12
v_mul_f32_e32 v79, s54, v79
Cv 78,78,79
buffer_store_dwordx4 a[72:75], v2, s[20:23], s51 offen offset:2048 sc0 sc1
Mz 188,52,12
)" R"(
Rt 154,24,18432
v_accvgpr_write_b32 a119, v78
Mz 184,48,12
v_mul_f32_e32 v80, s54, v80
v_mul_f32_e32 v81, s54, v81
Mz 124,44,12
Rt 156,25,16384
Cv 80,80,81
Mz 120,40,12
buffer_load_dwordx4 v15, s[8:11], s39 offen offset:3072 lds
Mz 60,36,12
Rt 158,25,18432
v_accvgpr_write_b32 a120, v80
.LzH6:
s_cbranch_execz .LzH11
Mz 56,32,12
v_mul_f32_e32 v82, s54, v82
v_mul_f32_e32 v83, s54, v83
Mz 128,40,16
Rb 160,17,0
Cv 82,82,83
Mz 132,44,16
v_accvgpr_write_b32 a121, v82
v_mul_f32_e32 v84, s54, v84
buffer_store_dwordx4 a[76:79], v2, s[20:23], s51 offen offset:3072 sc0 sc1
Mz 192,48,16
Rb 164,17,2048
v_mul_f32_e32 v85, s54, v85
Mz 196,52,16
Cv 84,84,85
v_accvgpr_write_b32 a122, v84
v_mfma_f32_16x16x32_bf16 a[192:195], a[56:59], a[16:19], 0
Rb 168,17,8192
v_mul_f32_e32 v86, s54, v86
v_mfma_f32_16x16x32_bf16 a[196:199], a[60:63], a[16:19], 0
v_mul_f32_e32 v87, s54, v87
Cv 86,86,87
v_mfma_f32_16x16x32_bf16 a[204:207], a[60:63], a[20:23], 0
Rb 172,17,10240
v_accvgpr_write_b32 a123, v86
v_mfma_f32_16x16x32_bf16 a[200:203], a[56:59], a[20:23], 0
v_mul_f32_e32 v88, s54, v88
v_mul_f32_e32 v89, s54, v89
Mz 204,52,20
Rb 176,17,16384
Cv 88,88,89
Mz 200,48,20
v_accvgpr_write_b32 a124, v88
v_mul_f32_e32 v90, s54, v90
buffer_store_dwordx4 a[80:83], v3, s[20:23], s51 offen offset:0 sc0 sc1
Mz 140,44,20
Rb 180,17,18432
v_mul_f32_e32 v91, s54, v91
Mz 136,40,20
Cv 90,90,91
v_accvgpr_write_b32 a125, v90
Mz 144,40,24
Rb 184,17,24576
v_mul_f32_e32 v92, s54, v92
Mz 148,44,24
v_mul_f32_e32 v93, s54, v93
Cv 92,92,93
Mz 208,48,24
Rb 188,17,26624
)" R"(
v_accvgpr_write_b32 a126, v92
Mz 212,52,24
v_mul_f32_e32 v94, s54, v94
v_mul_f32_e32 v95, s54, v95
v_mfma_f32_16x16x32_bf16 a[208:211], a[56:59], a[24:27], 0
Cv 94,94,95
v_accvgpr_write_b32 a127, v94
v_mfma_f32_16x16x32_bf16 a[212:215], a[60:63], a[24:27], 0
buffer_store_dwordx4 a[84:87], v3, s[20:23], s51 offen offset:1024 sc0 sc1
v_mfma_f32_16x16x32_bf16 a[220:223], a[60:63], a[28:31], 0
v_mfma_f32_16x16x32_bf16 a[216:219], a[56:59], a[28:31], 0
Mz 220,52,28
Mz 216,48,28
Mz 156,44,28
Mz 152,40,28
Mz 64,32,16
Mz 68,36,16
buffer_store_dwordx4 a[88:91], v3, s[20:23], s51 offen offset:2048 sc0 sc1
Mz 72,32,20
Mz 76,36,20
Mz 80,32,24
Mz 84,36,24
Mz 88,32,28
Mz 92,36,28
s_waitcnt vmcnt(16) lgkmcnt(0)
s_barrier
Mv 32,160,128
Rt 0,18,32768
Mv 36,164,128
s_add_u32 s38, s38, 0xffff0080
Mv 96,168,128
Rt 2,18,34816
s_add_u32 m0, s36, 0
Mv 100,172,128
buffer_load_dwordx4 v8, s[4:7], s38 offen offset:0 lds
Mv 160,176,128
Rt 4,19,32768
Mv 164,180,128
Mv 224,184,128
Rt 6,19,34816
Mv 228,188,128
buffer_load_dwordx4 v9, s[4:7], s38 offen offset:1024 lds
Mv 236,188,132
Rt 8,20,32768
Mv 232,184,132
Mv 172,180,132
Rt 10,20,34816
Mv 168,176,132
buffer_load_dwordx4 v10, s[4:7], s38 offen offset:2048 lds
Mv 108,172,132
Rt 12,21,32768
Mv 104,168,132
Mv 44,164,132
Rt 14,21,34816
Mv 40,160,132
buffer_load_dwordx4 v11, s[4:7], s38 offen offset:3072 lds
Mv 48,160,136
Rt 16,22,32768
Mv 52,164,136
s_add_u32 s38, s38, 0x10000
Mv 112,168,136
Rt 18,22,34816
s_add_u32 m0, s36, 4096
Mv 116,172,136
buffer_load_dwordx4 v8, s[4:7], s38 offen offset:0 lds
)" R"(
Mv 176,176,136
Rt 20,23,32768
Mv 180,180,136
Mv 240,184,136
Rt 22,23,34816
Mv 244,188,136
buffer_load_dwordx4 v9, s[4:7], s38 offen offset:1024 lds
Mv 252,188,140
Rt 24,24,32768
Mv 248,184,140
Mv 188,180,140
Rt 26,24,34816
Mv 184,176,140
buffer_load_dwordx4 v10, s[4:7], s38 offen offset:2048 lds
Mv 124,172,140
Rt 28,25,32768
Mv 120,168,140
Mv 60,164,140
Rt 30,25,34816
Mv 56,160,140
buffer_load_dwordx4 v11, s[4:7], s38 offen offset:3072 lds
Mv 64,160,144
Rb 32,16,32768
Mv 68,164,144
Mv 128,168,144
Rb 36,16,34816
Mv 132,172,144
s_add_u32 s39, s39, 0x40000
Mv 192,176,144
Rb 40,16,40960
s_add_u32 m0, s37, 16384
Mv 196,180,144
buffer_load_dwordx4 v12, s[8:11], s39 offen offset:0 lds
Ma 192,184,144
Rb 44,16,43008
Ma 196,188,144
Ma 204,188,148
Rb 48,16,49152
Ma 200,184,148
Mv 204,180,148
Rb 52,16,51200
Mv 200,176,148
buffer_load_dwordx4 v13, s[8:11], s39 offen offset:1024 lds
Mv 140,172,148
Rb 56,16,57344
Mv 136,168,148
Mv 76,164,148
Rb 60,16,59392
Mv 72,160,148
Mv 80,160,152
Mv 84,164,152
buffer_load_dwordx4 v14, s[8:11], s39 offen offset:2048 lds
Mv 144,168,152
Mv 148,172,152
Mv 208,176,152
Mv 212,180,152
Ma 208,184,152
Ma 212,188,152
buffer_load_dwordx4 v15, s[8:11], s39 offen offset:3072 lds
Ma 220,188,156
Ma 216,184,156
Mv 220,180,156
Mv 216,176,156
buffer_store_dwordx4 a[92:95], v3, s[20:23], s51 offen offset:3072 sc0 sc1
)" R"(
Mv 156,172,156
Mv 152,168,156
Mv 92,164,156
Mv 88,160,156
s_waitcnt vmcnt(25) lgkmcnt(0)
s_barrier
Mv 32,32,0
Rt 128,18,49152
Mv 36,36,0
Mv 96,40,0
Rt 130,18,51200
Mv 100,44,0
s_add_u32 s39, s39, 0x40000
Mv 160,48,0
Rt 132,19,49152
s_add_u32 m0, s37, 32768
Mv 164,52,0
buffer_load_dwordx4 v12, s[8:11], s39 offen offset:0 lds
Mv 224,56,0
Rt 134,19,51200
Mv 228,60,0
Mv 236,60,4
Rt 136,20,49152
Mv 232,56,4
s_add_u32 s51, s51, 0x10000
buffer_store_dwordx4 a[96:99], v2, s[20:23], s51 offen offset:0 sc0 sc1
Mv 172,52,4
Rt 138,20,51200
Mv 168,48,4
Mv 108,44,4
Rt 140,21,49152
Mv 104,40,4
buffer_load_dwordx4 v13, s[8:11], s39 offen offset:1024 lds
Mv 44,36,4
Rt 142,21,51200
Mv 40,32,4
Mv 48,32,8
Rt 144,22,49152
Mv 52,36,8
buffer_store_dwordx4 a[100:103], v2, s[20:23], s51 offen offset:1024 sc0 sc1
Mv 112,40,8
Rt 146,22,51200
Mv 116,44,8
Mv 176,48,8
Rt 148,23,49152
Mv 180,52,8
buffer_load_dwordx4 v14, s[8:11], s39 offen offset:2048 lds
Mv 240,56,8
Rt 150,23,51200
Mv 244,60,8
Mv 252,60,12
Rt 152,24,49152
Mv 248,56,12
buffer_store_dwordx4 a[104:107], v2, s[20:23], s51 offen offset:2048 sc0 sc1
Mv 188,52,12
Rt 154,24,51200
Mv 184,48,12
Mv 124,44,12
Rt 156,25,49152
Mv 120,40,12
buffer_load_dwordx4 v15, s[8:11], s39 offen offset:3072 lds
Mv 60,36,12
Rt 158,25,51200
Mv 56,32,12
Mv 64,32,16
)" R"(
Rb 160,17,32768
Mv 68,36,16
buffer_store_dwordx4 a[108:111], v2, s[20:23], s51 offen offset:3072 sc0 sc1
Mv 128,40,16
Rb 164,17,34816
s_mov_b64 s[16:17], s[70:71]
Mv 132,44,16
s_mov_b32 s18, 0x188000
Mv 192,48,16
Rb 168,17,40960
Mv 196,52,16
s_cmp_eq_u32 s33, 0
s_cmov_b32 s18, 0x380000
Ma 192,56,16
Rb 172,17,43008
s_mul_i32 s43, s2, 3072
Ma 196,60,16
s_cmp_eq_u32 s33, 0
s_cselect_b32 s44, 3, 6
Ma 204,60,20
Rb 176,17,49152
s_mul_i32 s44, s44, 0x60000
Ma 200,56,20
buffer_store_dwordx4 a[112:115], v3, s[20:23], s51 offen offset:0 sc0 sc1
Mv 204,52,20
Rb 180,17,51200
s_sub_u32 s43, s43, s44
Mv 200,48,20
v_min_u32 v26, 23, v0
v_lshlrev_b32 v26, 7, v26
Mv 140,44,20
Rb 184,17,57344
v_and_b32 v5, 7, v0
Mv 136,40,20
v_lshlrev_b32 v5, 7, v5
Mv 76,36,20
Rb 188,17,59392
Mv 72,32,20
Mv 80,32,24
Mv 84,36,24
buffer_store_dwordx4 a[116:119], v3, s[20:23], s51 offen offset:1024 sc0 sc1
Mv 144,40,24
Mv 148,44,24
Mv 208,48,24
Mv 212,52,24
Ma 208,56,24
Ma 212,60,24
Ma 220,60,28
Ma 216,56,28
buffer_store_dwordx4 a[120:123], v3, s[20:23], s51 offen offset:2048 sc0 sc1
Mv 220,52,28
Mv 216,48,28
Mv 156,44,28
Mv 152,40,28
Mv 92,36,28
Mv 88,32,28
s_waitcnt vmcnt(16) lgkmcnt(0)
s_barrier
Mv 32,160,128
Rt 0,18,0
Mv 36,164,128
s_add_u32 s38, s38, 0xffff0080
Mv 96,168,128
Rt 2,18,2048
s_add_u32 m0, s36, 32768
)" R"(
Mv 100,172,128
buffer_load_dwordx4 v8, s[4:7], s38 offen offset:0 lds
Mv 160,176,128
Rt 4,19,0
Mv 164,180,128
Mv 224,184,128
Rt 6,19,2048
Mv 228,188,128
buffer_load_dwordx4 v9, s[4:7], s38 offen offset:1024 lds
Mv 236,188,132
Rt 8,20,0
Mv 232,184,132
Mv 172,180,132
Rt 10,20,2048
Mv 168,176,132
buffer_load_dwordx4 v10, s[4:7], s38 offen offset:2048 lds
Mv 108,172,132
Rt 12,21,0
Mv 104,168,132
Mv 44,164,132
Rt 14,21,2048
Mv 40,160,132
buffer_load_dwordx4 v11, s[4:7], s38 offen offset:3072 lds
Mv 48,160,136
Rt 16,22,0
Mv 52,164,136
s_add_u32 s38, s38, 0x10000
Mv 112,168,136
Rt 18,22,2048
s_add_u32 m0, s36, 36864
Mv 116,172,136
buffer_load_dwordx4 v8, s[4:7], s38 offen offset:0 lds
Mv 176,176,136
Rt 20,23,0
Mv 180,180,136
Mv 240,184,136
Rt 22,23,2048
Mv 244,188,136
buffer_load_dwordx4 v9, s[4:7], s38 offen offset:1024 lds
Mv 252,188,140
Rt 24,24,0
Mv 248,184,140
Mv 188,180,140
Rt 26,24,2048
Mv 184,176,140
buffer_load_dwordx4 v10, s[4:7], s38 offen offset:2048 lds
Mv 124,172,140
Rt 28,25,0
Mv 120,168,140
Mv 60,164,140
Rt 30,25,2048
v_accvgpr_write_b32 a224, -1
Mv 56,160,140
buffer_load_dwordx4 v11, s[4:7], s38 offen offset:3072 lds
Mv 64,160,144
Rb 32,16,0
v_accvgpr_write_b32 a225, -1
Mv 68,164,144
v_accvgpr_write_b32 a226, -1
v_accvgpr_write_b32 a227, -1
Mv 128,168,144
Rb 36,16,2048
s_add_u32 s88, s73, 0x4000
Mv 132,172,144
)" R"(
s_add_u32 s39, s39, 0x40000
s_mov_b32 s40, 6
Mv 192,176,144
Rb 40,16,8192
s_add_u32 m0, s37, 49152
Mv 196,180,144
buffer_load_dwordx4 v12, s[8:11], s39 offen offset:0 lds
Ma 192,184,144
Rb 44,16,10240
s_mov_b32 s94, 0
Ma 196,188,144
Ma 204,188,148
Rb 48,16,16384
Ma 200,184,148
Mv 204,180,148
Rb 52,16,18432
Mv 200,176,148
buffer_load_dwordx4 v13, s[8:11], s39 offen offset:1024 lds
Mv 140,172,148
Rb 56,16,24576
Mv 136,168,148
Mv 76,164,148
Rb 60,16,26624
Mv 72,160,148
Mv 80,160,152
Mv 84,164,152
buffer_load_dwordx4 v14, s[8:11], s39 offen offset:2048 lds
Mv 144,168,152
Mv 148,172,152
Mv 208,176,152
Mv 212,180,152
Ma 208,184,152
Ma 212,188,152
buffer_load_dwordx4 v15, s[8:11], s39 offen offset:3072 lds
Ma 220,188,156
Ma 216,184,156
Mv 220,180,156
Mv 216,176,156
buffer_store_dwordx4 a[124:127], v3, s[20:23], s51 offen offset:3072 sc0 sc1
Mv 156,172,156
Mv 152,168,156
Mv 92,164,156
Mv 88,160,156
s_waitcnt vmcnt(25) lgkmcnt(0)
s_barrier
.LzPBV:
Mv 32,32,0
Rt 128,18,16384
Mv 36,36,0
Mv 96,40,0
Rt 130,18,18432
Mv 100,44,0
s_add_u32 s39, s39, 0x40000
Mv 160,48,0
Rt 132,19,16384
s_add_u32 m0, s37, 0
Mv 164,52,0
buffer_load_dwordx4 v12, s[8:11], s39 offen offset:0 lds
Mv 224,56,0
Rt 134,19,18432
Mv 228,60,0
Mv 236,60,4
Rt 136,20,16384
Mv 232,56,4
Mv 172,52,4
)" R"(
Rt 138,20,18432
Mv 168,48,4
Mv 108,44,4
Rt 140,21,16384
Mv 104,40,4
buffer_load_dwordx4 v13, s[8:11], s39 offen offset:1024 lds
Mv 44,36,4
Rt 142,21,18432
Mv 40,32,4
Mv 48,32,8
Rt 144,22,16384
Mv 52,36,8
Mv 112,40,8
Rt 146,22,18432
Mv 116,44,8
Mv 176,48,8
Rt 148,23,16384
Mv 180,52,8
buffer_load_dwordx4 v14, s[8:11], s39 offen offset:2048 lds
Mv 240,56,8
Rt 150,23,18432
Mv 244,60,8
Mv 252,60,12
Rt 152,24,16384
Mv 248,56,12
Mv 188,52,12
Rt 154,24,18432
Mv 184,48,12
Mv 124,44,12
Rt 156,25,16384
Mv 120,40,12
buffer_load_dwordx4 v15, s[8:11], s39 offen offset:3072 lds
Mv 60,36,12
Rt 158,25,18432
Mv 56,32,12
Mv 64,32,16
Rb 160,17,0
Mv 68,36,16
Mv 128,40,16
Rb 164,17,2048
Mv 132,44,16
Mv 192,48,16
Rb 168,17,8192
Mv 196,52,16
buffer_store_dwordx4 a[224:227], v1, s[20:23], s72 offen sc0 sc1
Ma 192,56,16
Rb 172,17,10240
s_add_u32 s72, s72, 1024
Ma 196,60,16
Ma 204,60,20
Rb 176,17,16384
Ma 200,56,20
Mv 204,52,20
Rb 180,17,18432
Mv 200,48,20
Mv 140,44,20
Rb 184,17,24576
Mv 136,40,20
Mv 76,36,20
Rb 188,17,26624
Mv 72,32,20
Mv 80,32,24
Mv 84,36,24
buffer_store_dwordx4 a[224:227], v1, s[20:23], s88 offen sc0 sc1
)" R"(
Mv 144,40,24
s_add_u32 s88, s88, 1024
Mv 148,44,24
Mv 208,48,24
Mv 212,52,24
Ma 208,56,24
Ma 212,60,24
Ma 220,60,28
Ma 216,56,28
Mv 220,52,28
Mv 216,48,28
Mv 156,44,28
Mv 152,40,28
Mv 92,36,28
Mv 88,32,28
s_waitcnt vmcnt(11) lgkmcnt(0)
s_barrier
Mv 32,160,128
Rt 0,18,32768
Mv 36,164,128
s_add_u32 s38, s38, 0xffff0080
Mv 96,168,128
Rt 2,18,34816
s_add_u32 m0, s36, 0
Mv 100,172,128
buffer_load_dwordx4 v8, s[4:7], s38 offen offset:0 lds
Mv 160,176,128
Rt 4,19,32768
Mv 164,180,128
.LzH7:
s_cbranch_execz .LzH12
Mv 224,184,128
Rt 6,19,34816
Mv 228,188,128
buffer_load_dwordx4 v9, s[4:7], s38 offen offset:1024 lds
Mv 236,188,132
Rt 8,20,32768
Mv 232,184,132
Mv 172,180,132
Rt 10,20,34816
Mv 168,176,132
buffer_load_dwordx4 v10, s[4:7], s38 offen offset:2048 lds
Mv 108,172,132
Rt 12,21,32768
Mv 104,168,132
Mv 44,164,132
Rt 14,21,34816
Mv 40,160,132
buffer_load_dwordx4 v11, s[4:7], s38 offen offset:3072 lds
Mv 48,160,136
Rt 16,22,32768
Mv 52,164,136
s_add_u32 s38, s38, 0x10000
Mv 112,168,136
Rt 18,22,34816
s_add_u32 m0, s36, 4096
Mv 116,172,136
buffer_load_dwordx4 v8, s[4:7], s38 offen offset:0 lds
Mv 176,176,136
Rt 20,23,32768
Mv 180,180,136
Mv 240,184,136
Rt 22,23,34816
Mv 244,188,136
buffer_load_dwordx4 v9, s[4:7], s38 offen offset:1024 lds
)" R"(
Mv 252,188,140
Rt 24,24,32768
Mv 248,184,140
Mv 188,180,140
Rt 26,24,34816
Mv 184,176,140
buffer_load_dwordx4 v10, s[4:7], s38 offen offset:2048 lds
Mv 124,172,140
Rt 28,25,32768
Mv 120,168,140
Mv 60,164,140
Rt 30,25,34816
Mv 56,160,140
buffer_load_dwordx4 v11, s[4:7], s38 offen offset:3072 lds
Mv 64,160,144
Rb 32,16,32768
Mv 68,164,144
Mv 128,168,144
Rb 36,16,34816
Mv 132,172,144
s_add_u32 s39, s39, 0x40000
Mv 192,176,144
Rb 40,16,40960
s_add_u32 m0, s37, 16384
Mv 196,180,144
buffer_load_dwordx4 v12, s[8:11], s39 offen offset:0 lds
Ma 192,184,144
Rb 44,16,43008
Ma 196,188,144
Ma 204,188,148
Rb 48,16,49152
Ma 200,184,148
Mv 204,180,148
Rb 52,16,51200
Mv 200,176,148
buffer_load_dwordx4 v13, s[8:11], s39 offen offset:1024 lds
Mv 140,172,148
Rb 56,16,57344
Mv 136,168,148
Mv 76,164,148
Rb 60,16,59392
Mv 72,160,148
Mv 80,160,152
Mv 84,164,152
buffer_load_dwordx4 v14, s[8:11], s39 offen offset:2048 lds
Mv 144,168,152
Mv 148,172,152
Mv 208,176,152
Mv 212,180,152
Ma 208,184,152
Ma 212,188,152
buffer_load_dwordx4 v15, s[8:11], s39 offen offset:3072 lds
Ma 220,188,156
Ma 216,184,156
s_add_u32 s43, s43, 0x60000
Mv 220,180,156
Mv 216,176,156
buffer_load_dword v27, v26, s[16:19], s43 offen
Mv 156,172,156
Mv 152,168,156
Mv 92,164,156
Mv 88,160,156
s_waitcnt vmcnt(19) lgkmcnt(0)
s_barrier
buffer_store_dword v6, v7, s[20:23], s52 offen offset:0 sc0 sc1
)" R"(
s_mov_b32 s52, 0x40000000
Mv 32,32,0
Rt 128,18,49152
Mv 36,36,0
Mv 96,40,0
Rt 130,18,51200
Mv 100,44,0
s_add_u32 s39, s39, 0x40000
Mv 160,48,0
Rt 132,19,49152
s_add_u32 m0, s37, 32768
Mv 164,52,0
buffer_load_dwordx4 v12, s[8:11], s39 offen offset:0 lds
Mv 224,56,0
Rt 134,19,51200
Mv 228,60,0
Mv 236,60,4
Rt 136,20,49152
Mv 232,56,4
Mv 172,52,4
Rt 138,20,51200
Mv 168,48,4
Mv 108,44,4
Rt 140,21,49152
Mv 104,40,4
buffer_load_dwordx4 v13, s[8:11], s39 offen offset:1024 lds
Mv 44,36,4
Rt 142,21,51200
Mv 40,32,4
Mv 48,32,8
Rt 144,22,49152
Mv 52,36,8
Mv 112,40,8
Rt 146,22,51200
Mv 116,44,8
Mv 176,48,8
Rt 148,23,49152
Mv 180,52,8
buffer_load_dwordx4 v14, s[8:11], s39 offen offset:2048 lds
Mv 240,56,8
Rt 150,23,51200
Mv 244,60,8
Mv 252,60,12
Rt 152,24,49152
Mv 248,56,12
Mv 188,52,12
Rt 154,24,51200
Mv 184,48,12
Mv 124,44,12
Rt 156,25,49152
Mv 120,40,12
buffer_load_dwordx4 v15, s[8:11], s39 offen offset:3072 lds
Mv 60,36,12
Rt 158,25,51200
Mv 56,32,12
Mv 64,32,16
Rb 160,17,32768
Mv 68,36,16
Mv 128,40,16
Rb 164,17,34816
Mv 132,44,16
Mv 192,48,16
Rb 168,17,40960
Mv 196,52,16
)" R"(
buffer_store_dwordx4 a[224:227], v1, s[20:23], s72 offen sc0 sc1
Ma 192,56,16
Rb 172,17,43008
s_add_u32 s72, s72, 1024
Ma 196,60,16
Ma 204,60,20
Rb 176,17,49152
Ma 200,56,20
Mv 204,52,20
Rb 180,17,51200
Mv 200,48,20
Mv 140,44,20
Rb 184,17,57344
Mv 136,40,20
Mv 76,36,20
Rb 188,17,59392
Mv 72,32,20
Mv 80,32,24
Mv 84,36,24
buffer_store_dwordx4 a[224:227], v1, s[20:23], s88 offen sc0 sc1
Mv 144,40,24
s_add_u32 s88, s88, 1024
Mv 148,44,24
Mv 208,48,24
Mv 212,52,24
Ma 208,56,24
Ma 212,60,24
Ma 220,60,28
Ma 216,56,28
Mv 220,52,28
Mv 216,48,28
Mv 156,44,28
Mv 152,40,28
Mv 92,36,28
s_cmp_eq_u32 s40, 3
s_cmov_b32 s72, 0x40000000
s_cmov_b32 s88, 0x40000000
Mv 88,32,28
s_waitcnt vmcnt(12) lgkmcnt(0)
s_barrier
Mv 32,160,128
Rt 0,18,0
Mv 36,164,128
s_add_u32 s38, s38, 0xffff0080
Mv 96,168,128
Rt 2,18,2048
s_add_u32 m0, s36, 32768
Mv 100,172,128
buffer_load_dwordx4 v8, s[4:7], s38 offen offset:0 lds
Mv 160,176,128
Rt 4,19,0
Mv 164,180,128
Mv 224,184,128
Rt 6,19,2048
Mv 228,188,128
buffer_load_dwordx4 v9, s[4:7], s38 offen offset:1024 lds
Mv 236,188,132
Rt 8,20,0
Mv 232,184,132
Mv 172,180,132
Rt 10,20,2048
Mv 168,176,132
buffer_load_dwordx4 v10, s[4:7], s38 offen offset:2048 lds
Mv 108,172,132
Rt 12,21,0
)" R"(
Mv 104,168,132
Mv 44,164,132
Rt 14,21,2048
Mv 40,160,132
buffer_load_dwordx4 v11, s[4:7], s38 offen offset:3072 lds
Mv 48,160,136
Rt 16,22,0
Mv 52,164,136
s_add_u32 s38, s38, 0x10000
Mv 112,168,136
Rt 18,22,2048
s_add_u32 m0, s36, 36864
Mv 116,172,136
buffer_load_dwordx4 v8, s[4:7], s38 offen offset:0 lds
Mv 176,176,136
Rt 20,23,0
Mv 180,180,136
Mv 240,184,136
Rt 22,23,2048
Mv 244,188,136
buffer_load_dwordx4 v9, s[4:7], s38 offen offset:1024 lds
Mv 252,188,140
Rt 24,24,0
Mv 248,184,140
Mv 188,180,140
Rt 26,24,2048
Mv 184,176,140
buffer_load_dwordx4 v10, s[4:7], s38 offen offset:2048 lds
Mv 124,172,140
Rt 28,25,0
Mv 120,168,140
Mv 60,164,140
Rt 30,25,2048
Mv 56,160,140
buffer_load_dwordx4 v11, s[4:7], s38 offen offset:3072 lds
Mv 64,160,144
Rb 32,16,0
Mv 68,164,144
Mv 128,168,144
Rb 36,16,2048
Mv 132,172,144
s_add_u32 s39, s39, 0x40000
Mv 192,176,144
Rb 40,16,8192
s_add_u32 m0, s37, 49152
Mv 196,180,144
buffer_load_dwordx4 v12, s[8:11], s39 offen offset:0 lds
Ma 192,184,144
Rb 44,16,10240
Ma 196,188,144
Ma 204,188,148
Rb 48,16,16384
Ma 200,184,148
Mv 204,180,148
Rb 52,16,18432
Mv 200,176,148
buffer_load_dwordx4 v13, s[8:11], s39 offen offset:1024 lds
Mv 140,172,148
Rb 56,16,24576
Mv 136,168,148
Mv 76,164,148
Rb 60,16,26624
Mv 72,160,148
Mv 80,160,152
)" R"(
Mv 84,164,152
buffer_load_dwordx4 v14, s[8:11], s39 offen offset:2048 lds
Mv 144,168,152
Mv 148,172,152
Mv 208,176,152
Mv 212,180,152
Ma 208,184,152
Ma 212,188,152
buffer_load_dwordx4 v15, s[8:11], s39 offen offset:3072 lds
Ma 220,188,156
Ma 216,184,156
s_add_u32 s43, s43, 0x60000
Mv 220,180,156
s_sub_u32 s40, s40, 1
Mv 216,176,156
buffer_load_dword v27, v26, s[16:19], s43 offen
Mv 156,172,156
Mv 152,168,156
Mv 92,164,156
Mv 88,160,156
s_cmp_lg_u32 s40, 0
s_waitcnt vmcnt(21) lgkmcnt(0)
s_barrier
s_cbranch_scc1 .LzPBV
.LzPBVX:
Mv 32,32,0
Rt 128,18,16384
Mv 36,36,0
buffer_load_dword v4, v5, s[20:23], s50 offen offset:0 sc0 sc1
Mv 96,40,0
Rt 130,18,18432
Mv 100,44,0
Mv 160,48,0
Rt 132,19,16384
Mv 164,52,0
Mv 224,56,0
Rt 134,19,18432
Mv 228,60,0
Mv 236,60,4
Rt 136,20,16384
Mv 232,56,4
Mv 172,52,4
Rt 138,20,18432
Mv 168,48,4
Mv 108,44,4
Rt 140,21,16384
Mv 104,40,4
Mv 44,36,4
Rt 142,21,18432
Mv 40,32,4
Mv 48,32,8
Rt 144,22,16384
Mv 52,36,8
Mv 112,40,8
Rt 146,22,18432
Mv 116,44,8
Mv 176,48,8
Rt 148,23,16384
Mv 180,52,8
Mv 240,56,8
Rt 150,23,18432
Mv 244,60,8
Mv 252,60,12
Rt 152,24,16384
Mv 248,56,12
)" R"(
Mv 188,52,12
Rt 154,24,18432
Mv 184,48,12
Mv 124,44,12
Rt 156,25,16384
Mv 120,40,12
Mv 60,36,12
Rt 158,25,18432
Mv 56,32,12
Mv 64,32,16
Rb 160,17,0
Mv 68,36,16
Mv 128,40,16
Rb 164,17,2048
Mv 132,44,16
Mv 192,48,16
Rb 168,17,8192
Mv 196,52,16
Ma 192,56,16
Rb 172,17,10240
Ma 196,60,16
s_waitcnt vmcnt(0)
v_cmp_eq_u32_e32 vcc, s3, v4
s_andn2_b64 s[28:29], -1, vcc
s_cbranch_scc1 .Lzsp16
.Lzok16:
Ma 204,60,20
Rb 176,17,16384
Ma 200,56,20
s_lshl3_add_u32 s29, s34, s35
s_lshl2_add_u32 s29, s29, s33
Mv 204,52,20
Rb 180,17,18432
s_add_u32 m0, s36, 131072
Mv 200,48,20
buffer_load_dwordx4 v2, s[20:23], s56 offen offset:0 sc1 lds
Mv 140,44,20
Rb 184,17,24576
s_mul_i32 s83, s29, 7168
Mv 136,40,20
s_lshl_b32 s82, s34, 21
s_add_u32 s82, s82, 0x6000000
Mv 76,36,20
Rb 188,17,26624
s_add_u32 s82, s82, s84
Mv 72,32,20
s_mov_b32 s24, s80
s_mov_b32 s25, s81
Mv 80,32,24
s_mov_b32 s26, 0x380000
s_mov_b32 s27, 0x20000
Mv 84,36,24
buffer_load_dwordx4 v2, s[20:23], s56 offen offset:1024 sc1 lds
Mv 144,40,24
s_add_u32 s28, s33, 0
s_lshl2_add_u32 s28, s35, s28
Mv 148,44,24
s_mul_i32 s29, s28, 293
s_lshr_b32 s29, s29, 11
Mv 208,48,24
s_mul_i32 s29, s29, 9
s_add_u32 s29, s29, s28
Mv 212,52,24
s_lshl_b32 s29, s29, 10
)" R"(
s_add_u32 s4, s29, s82
Ma 208,56,24
s_add_u32 s28, s33, 4
s_lshl3_add_u32 s28, s28, s35
Ma 212,60,24
buffer_load_dwordx4 v2, s[20:23], s56 offen offset:2048 sc1 lds
Ma 220,60,28
s_mul_i32 s29, s28, 293
s_lshr_b32 s29, s29, 11
Ma 216,56,28
s_mul_i32 s29, s29, 9
s_add_u32 s29, s29, s28
Mv 220,52,28
s_lshl_b32 s29, s29, 10
s_add_u32 s5, s29, s82
Mv 216,48,28
s_add_u32 s28, s33, 8
s_lshl3_add_u32 s28, s28, s35
Mv 156,44,28
s_mul_i32 s29, s28, 293
s_lshr_b32 s29, s29, 11
Mv 152,40,28
buffer_load_dwordx4 v2, s[20:23], s56 offen offset:3072 sc1 lds
Mv 92,36,28
s_mul_i32 s29, s29, 9
s_add_u32 s29, s29, s28
Mv 88,32,28
s_waitcnt lgkmcnt(0)
s_barrier
Mv 32,160,128
Rt 0,18,32768
s_lshl_b32 s29, s29, 10
Mv 36,164,128
s_add_u32 s6, s29, s82
s_add_u32 s28, s33, 12
Mv 96,168,128
Rt 2,18,34816
s_add_u32 m0, s36, 0
Mv 100,172,128
buffer_load_dwordx4 v2, s[20:23], s55 offen offset:0 sc1 lds
Mv 160,176,128
Rt 4,19,32768
s_lshl3_add_u32 s28, s28, s35
Mv 164,180,128
s_mul_i32 s29, s28, 293
s_lshr_b32 s29, s29, 11
Mv 224,184,128
Rt 6,19,34816
s_mul_i32 s29, s29, 9
Mv 228,188,128
s_add_u32 s29, s29, s28
s_lshl_b32 s29, s29, 10
Mv 236,188,132
Rt 8,20,32768
buffer_load_dwordx4 v2, s[20:23], s55 offen offset:1024 sc1 lds
Mv 232,184,132
s_add_u32 s7, s29, s82
Mv 172,180,132
Rt 10,20,34816
Mv 168,176,132
s_cmp_gt_u32 s33, 1
s_cmov_b32 s7, 0x40000000
Mv 108,172,132
Rt 12,21,32768
s_add_u32 s28, s33, 14
)" R"(
Mv 104,168,132
buffer_load_dwordx4 v2, s[20:23], s55 offen offset:2048 sc1 lds
Mv 44,164,132
Rt 14,21,34816
s_lshl3_add_u32 s28, s28, s35
Mv 40,160,132
s_mul_i32 s29, s28, 293
s_lshr_b32 s29, s29, 11
Mv 48,160,136
Rt 16,22,32768
s_mul_i32 s29, s29, 9
Mv 52,164,136
s_add_u32 s29, s29, s28
s_lshl_b32 s29, s29, 10
Mv 112,168,136
Rt 18,22,34816
buffer_load_dwordx4 v2, s[20:23], s55 offen offset:3072 sc1 lds
Mv 116,172,136
s_add_u32 s8, s29, s82
s_add_u32 s28, s33, 18
Mv 176,176,136
Rt 20,23,32768
s_lshl3_add_u32 s28, s28, s35
Mv 180,180,136
s_mul_i32 s29, s28, 293
s_lshr_b32 s29, s29, 11
Mv 240,184,136
Rt 22,23,34816
s_add_u32 m0, s36, 4096
Mv 244,188,136
buffer_load_dwordx4 v3, s[20:23], s55 offen offset:0 sc1 lds
Mv 252,188,140
Rt 24,24,32768
s_mul_i32 s29, s29, 9
Mv 248,184,140
s_add_u32 s29, s29, s28
s_lshl_b32 s29, s29, 10
Mv 188,180,140
Rt 26,24,34816
s_add_u32 s9, s29, s82
Mv 184,176,140
s_add_u32 s28, s33, 22
s_lshl3_add_u32 s28, s28, s35
Mv 124,172,140
Rt 28,25,32768
buffer_load_dwordx4 v3, s[20:23], s55 offen offset:1024 sc1 lds
Mv 120,168,140
s_mul_i32 s29, s28, 293
s_lshr_b32 s29, s29, 11
Mv 60,164,140
Rt 30,25,34816
s_mul_i32 s29, s29, 9
Mv 56,160,140
s_add_u32 s29, s29, s28
s_lshl_b32 s29, s29, 10
Mv 64,160,144
Rb 32,16,32768
s_add_u32 s10, s29, s82
Mv 68,164,144
buffer_load_dwordx4 v3, s[20:23], s55 offen offset:2048 sc1 lds
Mv 128,168,144
Rb 36,16,34816
s_add_u32 s28, s33, 26
Mv 132,172,144
)" R"(
s_lshl3_add_u32 s28, s28, s35
s_mul_i32 s29, s28, 293
Mv 192,176,144
Rb 40,16,40960
s_lshr_b32 s29, s29, 11
.LzH8:
s_cbranch_execz .LzH18
Mv 196,180,144
s_mul_i32 s29, s29, 9
s_add_u32 s29, s29, s28
Ma 192,184,144
Rb 44,16,43008
buffer_load_dwordx4 v3, s[20:23], s55 offen offset:3072 sc1 lds
Ma 196,188,144
s_lshl_b32 s29, s29, 10
s_add_u32 s11, s29, s82
Ma 204,188,148
Rb 48,16,49152
Ma 200,184,148
s_cmp_gt_u32 s33, 1
s_cmov_b32 s11, 0x40000000
Mv 204,180,148
Rb 52,16,51200
s_add_u32 m0, s36, 135168
Mv 200,176,148
buffer_load_dwordx4 v3, s[20:23], s56 offen offset:0 sc1 lds
Mv 140,172,148
Rb 56,16,57344
s_and_b32 s28, s34, 1
Mv 136,168,148
s_lshl_b32 s28, s28, 14
s_add_u32 s82, s37, s28
Mv 76,164,148
Rb 60,16,59392
s_add_u32 s82, s82, 0x10000
Mv 72,160,148
Mv 80,160,152
buffer_load_dwordx4 v3, s[20:23], s56 offen offset:1024 sc1 lds
Mv 84,164,152
Mv 144,168,152
Mv 148,172,152
Mv 208,176,152
Mv 212,180,152
buffer_load_dwordx4 v3, s[20:23], s56 offen offset:2048 sc1 lds
Ma 208,184,152
Ma 212,188,152
Ma 220,188,156
Ma 216,184,156
Mv 220,180,156
buffer_load_dwordx4 v3, s[20:23], s56 offen offset:3072 sc1 lds
Mv 216,176,156
Mv 156,172,156
Mv 152,168,156
s_add_u32 s55, s55, 0x10000
s_add_u32 s56, s56, 0x10000
Mv 92,164,156
Mv 88,160,156
s_waitcnt lgkmcnt(4)
s_barrier
Mv 32,32,0
Rt 128,18,49152
Mv 36,36,0
Rt 130,18,51200
Mv 96,40,0
Rt 132,19,49152
)" R"(
s_add_u32 m0, s36, 65536
Mv 100,44,0
Rt 134,19,51200
buffer_load_dwordx4 v2, s[20:23], s56 offen offset:0 sc1 lds
Mv 108,44,4
Rt 136,20,49152
Mv 104,40,4
Rt 138,20,51200
s_add_u32 s50, s50, 1024
Mv 44,36,4
Rt 140,21,49152
s_min_u32 s50, s50, s60
Mv 40,32,4
Rt 142,21,51200
Mv 48,32,8
Rt 144,22,49152
Mv 52,36,8
Rt 146,22,51200
Mv 112,40,8
Rt 148,23,49152
Mv 116,44,8
Rt 150,23,51200
buffer_load_dwordx4 v2, s[20:23], s56 offen offset:1024 sc1 lds
Mv 124,44,12
Rt 152,24,49152
Mv 120,40,12
Rt 154,24,51200
Mv 60,36,12
Rt 156,25,49152
Mv 56,32,12
Rt 158,25,51200
Mv 64,32,16
Rb 160,17,32768
Mv 68,36,16
Rb 164,17,34816
Mv 128,40,16
Rb 168,17,40960
Mv 132,44,16
Rb 172,17,43008
buffer_load_dwordx4 v2, s[20:23], s56 offen offset:2048 sc1 lds
Mv 140,44,20
Rb 176,17,49152
Mv 136,40,20
Rb 180,17,51200
Mv 76,36,20
Rb 184,17,57344
Mv 72,32,20
Rb 188,17,59392
Mv 80,32,24
v_add_u32 v8, 0x10000, v1
v_add_u32 v10, 0x20000, v1
Mv 84,36,24
buffer_load_dword v4, v5, s[20:23], s50 offen offset:0 sc0 sc1
Mv 144,40,24
s_mov_b32 s93, 2
s_mov_b32 s94, 0
Mv 148,44,24
buffer_load_dwordx4 v2, s[20:23], s56 offen offset:3072 sc1 lds
Mv 156,44,28
Mv 152,40,28
Mv 92,36,28
Mv 88,32,28
s_waitcnt lgkmcnt(0)
s_barrier
Mv 32,160,128
)" R"(
Mv 36,164,128
Mv 40,160,132
s_add_u32 m0, s36, 32768
Mv 44,164,132
buffer_load_dwordx4 v2, s[20:23], s55 offen offset:0 sc1 lds
Mv 48,160,136
Mv 52,164,136
s_nop 0
Cv 32,32,33
Cv 33,34,35
Mv 56,160,140
Cv 34,36,37
Cv 35,38,39
Mv 60,164,140
Cv 40,40,41
Cv 41,42,43
Mv 64,160,144
Cv 42,44,45
Cv 43,46,47
Mv 68,164,144
Cv 48,48,49
Cv 49,50,51
Mv 72,160,148
Cv 50,52,53
Cv 51,54,55
Mv 76,164,148
buffer_load_dwordx4 v2, s[20:23], s55 offen offset:1024 sc1 lds
Mv 80,160,152
Cv 56,56,57
Cv 57,58,59
Mv 84,164,152
Cv 58,60,61
Cv 59,62,63
Mv 88,160,156
Cv 64,64,65
Cv 65,66,67
Mv 92,164,156
Cv 66,68,69
Cv 67,70,71
Mv 96,168,128
Cv 72,72,73
Cv 73,74,75
Mv 100,172,128
Cv 74,76,77
Cv 75,78,79
Mv 104,168,132
Cv 80,80,81
Cv 81,82,83
Mv 108,172,132
buffer_load_dwordx4 v2, s[20:23], s55 offen offset:2048 sc1 lds
Mv 112,168,136
Cv 82,84,85
Cv 83,86,87
Mv 116,172,136
Cv 88,88,89
Cv 89,90,91
Mv 120,168,140
Cv 90,92,93
Cv 91,94,95
Mv 124,172,140
buffer_store_dwordx4 v[32:35], v2, s[20:23], s45 offen offset:0 sc0 sc1
Mv 128,168,144
Mv 132,172,144
Mv 136,168,148
)" R"(
Mv 140,172,148
buffer_load_dwordx4 v2, s[20:23], s55 offen offset:3072 sc1 lds
Mv 144,168,152
Mv 148,172,152
s_nop 1
Cv 128,128,129
Cv 129,130,131
Mv 152,168,156
Cv 130,132,133
Cv 131,134,135
Mv 156,172,156
buffer_store_dwordx4 v[40:43], v2, s[20:23], s45 offen offset:1024 sc0 sc1
Mv 160,48,0
Cv 136,136,137
Cv 137,138,139
Mv 160,176,128
Cv 138,140,141
Cv 139,142,143
Mv 164,52,0
s_add_u32 m0, s36, 36864
Cv 144,144,145
Mv 164,180,128
buffer_load_dwordx4 v3, s[20:23], s55 offen offset:0 sc1 lds
Mv 168,48,4
Cv 145,146,147
Cv 146,148,149
Mv 168,176,132
Cv 147,150,151
Cv 152,152,153
Mv 172,52,4
Cv 153,154,155
Cv 154,156,157
Mv 172,180,132
buffer_store_dwordx4 v[48:51], v2, s[20:23], s45 offen offset:2048 sc0 sc1
Mv 176,48,8
Cv 155,158,159
Cv 132,96,97
Mv 176,176,136
Cv 133,98,99
Cv 134,100,101
Mv 180,52,8
Cv 135,102,103
Cv 140,104,105
Mv 180,180,136
buffer_load_dwordx4 v3, s[20:23], s55 offen offset:1024 sc1 lds
Mv 184,48,12
Cv 141,106,107
Cv 142,108,109
Mv 184,176,140
Cv 143,110,111
Cv 148,112,113
Mv 188,52,12
Cv 149,114,115
Cv 150,116,117
Mv 188,180,140
buffer_store_dwordx4 v[56:59], v2, s[20:23], s45 offen offset:3072 sc0 sc1
Mv 192,48,16
Cv 151,118,119
Cv 156,120,121
Mv 192,176,144
Cv 157,122,123
Cv 158,124,125
Mv 196,52,16
Cv 159,126,127
)" R"(
Cv 160,160,161
Mv 196,180,144
buffer_load_dwordx4 v3, s[20:23], s55 offen offset:2048 sc1 lds
Mv 200,48,20
Cv 161,162,163
Cv 162,164,165
Mv 200,176,148
Cv 163,166,167
v_accvgpr_write_b32 a224, v160
Mv 204,52,20
v_accvgpr_write_b32 a225, v161
v_accvgpr_write_b32 a226, v162
Mv 204,180,148
buffer_store_dwordx4 v[64:67], v3, s[20:23], s45 offen offset:0 sc0 sc1
Mv 208,48,24
v_accvgpr_write_b32 a227, v163
Cv 168,168,169
Mv 208,176,152
Cv 169,170,171
Cv 170,172,173
Mv 212,52,24
Cv 171,174,175
v_accvgpr_write_b32 a228, v168
Mv 212,180,152
buffer_load_dwordx4 v3, s[20:23], s55 offen offset:3072 sc1 lds
Mv 216,48,28
v_accvgpr_write_b32 a229, v169
v_accvgpr_write_b32 a230, v170
Mv 216,176,156
v_accvgpr_write_b32 a231, v171
Cv 176,176,177
Mv 220,52,28
Cv 177,178,179
Cv 178,180,181
Mv 220,180,156
buffer_store_dwordx4 v[72:75], v3, s[20:23], s45 offen offset:1024 sc0 sc1
Mv 224,56,0
Cv 179,182,183
v_accvgpr_write_b32 a232, v176
Mv 228,60,0
v_accvgpr_write_b32 a233, v177
v_accvgpr_write_b32 a234, v178
Mv 236,60,4
s_add_u32 m0, s36, 69632
v_accvgpr_write_b32 a235, v179
Mv 232,56,4
buffer_load_dwordx4 v3, s[20:23], s56 offen offset:0 sc1 lds
Mv 240,56,8
Cv 184,184,185
Cv 185,186,187
Mv 244,60,8
Cv 186,188,189
Cv 187,190,191
Mv 252,60,12
v_accvgpr_write_b32 a236, v184
v_accvgpr_write_b32 a237, v185
Mv 248,56,12
s_waitcnt vmcnt(16)
s_barrier
Ma 192,56,16
v_cmp_eq_u32_e32 vcc, s3, v4
s_andn2_b64 s[28:29], -1, vcc
s_cbranch_scc1 .Lzsp17
.Lzok17:
Ma 196,60,16
)" R"(
buffer_store_dwordx4 v[80:83], v3, s[20:23], s45 offen offset:2048 sc0 sc1
Ma 204,60,20
v_accvgpr_write_b32 a238, v186
v_accvgpr_write_b32 a239, v187
Ma 200,56,20
buffer_load_dwordx4 v3, s[20:23], s56 offen offset:1024 sc1 lds
Ma 208,56,24
Cv 192,192,193
Cv 193,194,195
Ma 212,60,24
Cv 194,196,197
Cv 195,198,199
Ma 220,60,28
v_accvgpr_write_b32 a240, v192
v_accvgpr_write_b32 a241, v193
Ma 216,56,28
buffer_store_dwordx4 v[88:91], v3, s[20:23], s45 offen offset:3072 sc0 sc1
Mv 224,184,128
Rb 0,1,0
v_accvgpr_write_b32 a242, v194
Mv 228,188,128
Rb 4,1,1024
v_accvgpr_write_b32 a243, v195
Mv 232,184,132
Rb 8,1,2048
Cv 200,200,201
Mv 236,188,132
buffer_load_dwordx4 v3, s[20:23], s56 offen offset:2048 sc1 lds
Rb 12,1,3072
Mv 240,184,136
Rb 16,1,8192
Cv 201,202,203
Mv 244,188,136
Rb 20,1,9216
Cv 202,204,205
Mv 248,184,140
Rb 24,1,10240
Cv 203,206,207
Mv 252,188,140
Rb 28,1,11264
v_accvgpr_write_b32 a244, v200
Ma 192,184,144
Rb 32,1,4096
v_accvgpr_write_b32 a245, v201
Ma 196,188,144
Rb 36,1,5120
v_accvgpr_write_b32 a246, v202
Ma 200,184,148
Rb 40,1,6144
v_accvgpr_write_b32 a247, v203
Ma 204,188,148
buffer_load_dwordx4 v3, s[20:23], s56 offen offset:3072 sc1 lds
Rb 44,1,7168
Ma 208,184,152
Ma 212,188,152
Ma 216,184,156
Ma 220,188,156
s_add_u32 s56, s56, 0x10000
.LzH9:
s_cbranch_execz .LzH14
.LzFB:
s_waitcnt lgkmcnt(8)
Mz 64,0,64
Rb 48,1,12288
)" R"(
s_add_u32 s55, s55, 0x10000
Mz 68,0,68
s_cmp_eq_u32 s57, 2
s_cselect_b32 s55, s59, s55
Mv 68,4,76
Rb 52,1,13312
s_sub_u32 s57, s57, 1
v_mov_b32 v16, 0
Mv 64,4,72
buffer_store_dwordx4 v[132:135], v2, s[20:23], s46 offen offset:0 sc0 sc1
Mv 64,8,80
Rb 56,1,14336
Cv 208,208,209
Cv 209,210,211
Mv 68,8,84
Cv 210,212,213
Cv 211,214,215
Mv 68,12,92
Rb 60,1,15360
v_accvgpr_write_b32 a248, v208
s_waitcnt lgkmcnt(8)
Mv 64,12,88
v_accvgpr_write_b32 a249, v209
v_accvgpr_write_b32 a250, v210
Mv 64,16,96
Rb 0,1,16384
v_accvgpr_write_b32 a251, v211
Mv 68,16,100
Cv 216,216,217
Cv 217,218,219
Cv 218,220,221
Mv 68,20,108
Rb 4,1,17408
Cv 219,222,223
Mv 64,20,104
v_accvgpr_write_b32 a252, v216
v_accvgpr_write_b32 a253, v217
v_accvgpr_write_b32 a254, v218
Mv 64,24,112
Rb 8,1,18432
v_accvgpr_write_b32 a255, v219
Mv 68,24,116
buffer_store_dwordx4 v[140:143], v2, s[20:23], s46 offen offset:1024 sc0 sc1
Cv 224,224,225
Mv 68,28,124
Rb 12,1,19456
s_waitcnt lgkmcnt(8)
Mv 64,28,120
Cv 225,226,227
Cv 226,228,229
Cv 227,230,231
Mz 72,32,64
Rb 16,1,24576
v_accvgpr_write_b32 a128, v224
Mz 76,32,68
v_accvgpr_write_b32 a129, v225
v_accvgpr_write_b32 a130, v226
Mv 76,36,76
Rb 20,1,25600
v_accvgpr_write_b32 a131, v227
Cv 232,232,233
Mv 72,36,72
Cv 233,234,235
Cv 234,236,237
)" R"(
Mv 72,40,80
Rb 24,1,26624
Cv 235,238,239
v_accvgpr_write_b32 a132, v232
Mv 76,40,84
v_accvgpr_write_b32 a133, v233
v_accvgpr_write_b32 a134, v234
Mv 76,44,92
Rb 28,1,27648
v_accvgpr_write_b32 a135, v235
s_waitcnt lgkmcnt(8)
Mv 72,44,88
buffer_store_dwordx4 v[148:151], v2, s[20:23], s46 offen offset:2048 sc0 sc1
Mv 72,48,96
Rb 32,1,20480
Cv 240,240,241
Mv 76,48,100
Cv 241,242,243
Cv 242,244,245
Cv 243,246,247
Mv 76,52,108
Rb 36,1,21504
v_accvgpr_write_b32 a136, v240
Mv 72,52,104
v_accvgpr_write_b32 a137, v241
v_accvgpr_write_b32 a138, v242
v_accvgpr_write_b32 a139, v243
Mv 72,56,112
Rb 40,1,22528
Cv 248,248,249
Mv 76,56,116
Cv 249,250,251
Cv 250,252,253
Cv 251,254,255
Mv 76,60,124
Rb 44,1,23552
s_waitcnt lgkmcnt(8)
Mv 72,60,120
v_accvgpr_write_b32 a140, v248
v_accvgpr_write_b32 a141, v249
v_accvgpr_write_b32 a142, v250
Mz 80,0,64
Rb 48,1,28672
v_accvgpr_write_b32 a143, v251
Mz 84,0,68
buffer_store_dwordx4 v[156:159], v2, s[20:23], s46 offen offset:3072 sc0 sc1
Mv 84,4,76
Rb 52,1,29696
v_mov_b32 v17, 0
v_mov_b32 v18, 0
Mv 80,4,72
v_mov_b32 v19, 0
v_mov_b32 v20, 0
Mv 80,8,80
Rb 56,1,30720
v_exp_f32_e32 v32, v64
Mv 84,8,84
v_exp_f32_e32 v24, v65
Mv 84,12,92
Rb 60,1,31744
v_mov_b32 v21, 0
s_waitcnt lgkmcnt(8)
Mv 80,12,88
v_exp_f32_e32 v33, v66
)" R"(
Mv 80,16,96
v_exp_f32_e32 v25, v67
Mv 84,16,100
v_exp_f32_e32 v34, v72
v_mov_b32 v22, 0
Mv 84,20,108
v_exp_f32_e32 v26, v73
Mv 80,20,104
buffer_store_dwordx4 v[128:131], v3, s[20:23], s46 offen offset:0 sc0 sc1
v_mov_b32 v23, 0
Mv 80,24,112
s_add_u32 s50, s50, 1024
s_min_u32 s50, s50, s60
Mv 84,24,116
v_exp_f32_e32 v35, v74
v_add_f32_e32 v14, v32, v24
Mv 84,28,124
v_exp_f32_e32 v27, v75
Mv 80,28,120
s_waitcnt vmcnt(7) lgkmcnt(0)
s_barrier
buffer_store_dword v6, v7, s[20:23], s49 offen offset:0 sc0 sc1
Mz 88,32,64
Rb 0,1,32768
Cv 32,32,24
Mz 92,32,68
Rb 4,1,33792
v_add_f32_e32 v14, v14, v33
Mv 92,36,76
Rb 8,1,34816
s_add_u32 m0, s36, 0
v_add_f32_e32 v14, v14, v25
Mv 88,36,72
Rb 12,1,35840
buffer_load_dwordx4 v2, s[20:23], s55 offen offset:0 sc1 lds
.LzH10:
s_cbranch_execz .LzH15
Mv 88,40,80
Rb 16,1,40960
v_exp_f32_e32 v36, v68
Mv 92,40,84
Rb 20,1,41984
Cv 33,33,25
Mv 92,44,92
Rb 24,1,43008
v_exp_f32_e32 v28, v69
Mv 88,44,88
Rb 28,1,44032
v_add_f32_e32 v14, v14, v34
Mv 88,48,96
Rb 32,1,36864
v_add_f32_e32 v14, v14, v26
Mv 92,48,100
Rb 36,1,37888
v_exp_f32_e32 v37, v70
Mv 92,52,108
Rb 40,1,38912
Cv 34,34,26
Mv 88,52,104
Rb 44,1,39936
buffer_load_dwordx4 v2, s[20:23], s55 offen offset:1024 sc1 lds
Mv 88,56,112
v_exp_f32_e32 v29, v71
Mv 92,56,116
v_exp_f32_e32 v38, v76
)" R"(
v_add_f32_e32 v14, v14, v35
Mv 92,60,124
v_add_f32_e32 v14, v14, v27
s_waitcnt lgkmcnt(8)
Mv 88,60,120
v_exp_f32_e32 v24, v77
Cv 35,35,27
Mz 96,0,64
Rb 48,1,45056
buffer_load_dword v4, v5, s[20:23], s50 offen offset:0 sc0 sc1
Mz 100,0,68
v_exp_f32_e32 v39, v78
Mv 100,4,76
Rb 52,1,46080
v_exp_f32_e32 v25, v79
Mv 96,4,72
buffer_load_dwordx4 v2, s[20:23], s55 offen offset:2048 sc1 lds
Mv 96,8,80
Rb 56,1,47104
v_exp_f32_e32 v40, v80
Mv 100,8,84
v_exp_f32_e32 v26, v81
Mv 100,12,92
Rb 60,1,48128
v_add_f32_e32 v15, v36, v28
s_waitcnt lgkmcnt(8)
Mv 96,12,88
buffer_store_dwordx4 v[136:139], v3, s[20:23], s46 offen offset:1024 sc0 sc1
Mv 96,16,96
Rb 0,1,49152
Cv 36,36,28
Mv 100,16,100
v_exp_f32_e32 v41, v82
v_add_f32_e32 v15, v15, v37
Mv 100,20,108
Rb 4,1,50176
v_add_f32_e32 v15, v15, v29
Mv 96,20,104
buffer_load_dwordx4 v2, s[20:23], s55 offen offset:3072 sc1 lds
Cv 37,37,29
Mv 96,24,112
Rb 8,1,51200
v_add_f32_e32 v15, v15, v38
Mv 100,24,116
v_exp_f32_e32 v27, v83
v_add_f32_e32 v15, v15, v24
Mv 100,28,124
Rb 12,1,52224
s_waitcnt lgkmcnt(8)
Mv 96,28,120
v_exp_f32_e32 v42, v88
Cv 38,38,24
Mz 104,32,64
Rb 16,1,57344
v_add_f32_e32 v15, v15, v39
Mz 108,32,68
v_exp_f32_e32 v28, v89
Mv 108,36,76
Rb 20,1,58368
s_add_u32 m0, s36, 4096
v_add_f32_e32 v15, v15, v25
Mv 104,36,72
buffer_load_dwordx4 v3, s[20:23], s55 offen offset:0 sc1 lds
Mv 104,40,80
)" R"(
Rb 24,1,59392
v_exp_f32_e32 v43, v90
Mv 108,40,84
v_exp_f32_e32 v29, v91
Mv 108,44,92
Rb 28,1,60416
Cv 39,39,25
s_waitcnt lgkmcnt(8)
Mv 104,44,88
buffer_store_dwordx4 v[144:147], v3, s[20:23], s46 offen offset:2048 sc0 sc1
Mv 104,48,96
Rb 32,1,53248
v_add_f32_e32 v14, v14, v40
Mv 108,48,100
v_exp_f32_e32 v44, v84
v_add_f32_e32 v14, v14, v26
Mv 108,52,108
Rb 36,1,54272
Cv 40,40,26
Mv 104,52,104
buffer_load_dwordx4 v3, s[20:23], s55 offen offset:1024 sc1 lds
v_add_f32_e32 v14, v14, v41
Mv 104,56,112
Rb 40,1,55296
v_add_f32_e32 v14, v14, v27
Mv 108,56,116
v_exp_f32_e32 v24, v85
Cv 41,41,27
Mv 108,60,124
Rb 44,1,56320
s_waitcnt lgkmcnt(8)
Mv 104,60,120
v_exp_f32_e32 v45, v86
v_add_f32_e32 v14, v14, v42
Mz 112,0,64
Rb 48,1,61440
v_add_f32_e32 v14, v14, v28
Mz 116,0,68
v_exp_f32_e32 v25, v87
Mv 116,4,76
Rb 52,1,62464
v_exp_f32_e32 v46, v92
Mv 112,4,72
buffer_load_dwordx4 v3, s[20:23], s55 offen offset:2048 sc1 lds
Mv 112,8,80
Rb 56,1,63488
v_exp_f32_e32 v26, v93
Mv 116,8,84
v_exp_f32_e32 v47, v94
Mv 116,12,92
Rb 60,1,64512
Cv 42,42,28
s_waitcnt lgkmcnt(8)
Mv 112,12,88
buffer_store_dwordx4 v[152:155], v3, s[20:23], s46 offen offset:3072 sc0 sc1
Mv 112,16,96
v_exp_f32_e32 v27, v95
Mv 116,16,100
v_add_f32_e32 v14, v14, v43
v_add_f32_e32 v14, v14, v29
Cv 43,43,29
Mv 116,20,108
v_add_f32_e32 v15, v15, v44
v_add_f32_e32 v15, v15, v24
)" R"(
Mv 112,20,104
buffer_load_dwordx4 v3, s[20:23], s55 offen offset:3072 sc1 lds
Cv 44,44,24
Mv 112,24,112
v_add_f32_e32 v15, v15, v45
v_add_f32_e32 v15, v15, v25
Mv 116,24,116
Cv 45,45,25
v_add_f32_e32 v15, v15, v46
v_add_f32_e32 v15, v15, v26
Mv 116,28,124
Cv 46,46,26
v_add_f32_e32 v15, v15, v47
Mv 112,28,120
v_add_f32_e32 v15, v15, v27
s_waitcnt lgkmcnt(0)
s_barrier
Mz 120,32,64
Rb 0,10,0
Cv 47,47,27
Mz 124,32,68
Rb 4,10,1024
v_max_f32_e32 v11, v14, v15
Mv 124,36,76
Rb 8,10,2048
v_mov_b32 v24, v14
v_mov_b32 v25, v15
Mv 120,36,72
Rb 12,10,3072
v_permlane32_swap_b32_e32 v24, v25
Mv 120,40,80
Rb 16,10,4096
v_add_f32_e32 v24, v24, v25
v_mov_b32 v25, v24
Mv 124,40,84
Rb 20,10,5120
v_permlane16_swap_b32_e32 v24, v25
Mv 124,44,92
Rb 24,10,6144
v_add_f32_e32 v24, v24, v25
s_add_u32 s55, s55, 0x10000
Mv 120,44,88
Rb 28,10,7168
Mv 120,48,96
Rb 32,10,8192
Mv 124,48,100
Rb 36,10,9216
v_rcp_f32_e32 v24, v24
Mv 124,52,108
Rb 40,10,10240
v_max_f32_e32 v11, v11, v24
Mv 120,52,104
Rb 44,10,11264
s_cmp_eq_u32 s57, 2
s_cselect_b32 s55, s59, s55
Mv 120,56,112
v_cmp_nge_f32_e32 vcc, s62, v11
Mv 124,56,116
Mv 124,60,124
s_waitcnt lgkmcnt(8)
s_cbranch_vccnz .Lzfx6
.Lzfr6:
Mv 120,60,120
v_mov_b32 v12, v14
v_mov_b32 v13, v15
)" R"(
v_mfma_f32_16x16x32_bf16 v[128:131], a[0:3], v[32:35], 0
Rb 48,10,12288
v_mfma_f32_16x16x32_bf16 v[136:139], a[0:3], v[36:39], 0
v_mfma_f32_16x16x32_bf16 v[140:143], a[4:7], v[36:39], 0
Rb 52,10,13312
s_add_u32 m0, s36, 32768
v_mfma_f32_16x16x32_bf16 v[132:135], a[4:7], v[32:35], 0
buffer_load_dwordx4 v2, s[20:23], s55 offen offset:0 sc1 lds
v_mfma_f32_16x16x32_bf16 v[144:147], a[8:11], v[32:35], 0
Rb 56,10,14336
v_mfma_f32_16x16x32_bf16 v[152:155], a[8:11], v[36:39], 0
v_exp_f32_e32 v48, v96
v_mfma_f32_16x16x32_bf16 v[156:159], a[12:15], v[36:39], 0
Rb 60,10,15360
s_waitcnt lgkmcnt(8)
v_mfma_f32_16x16x32_bf16 v[148:151], a[12:15], v[32:35], 0
v_exp_f32_e32 v28, v97
v_mfma_f32_16x16x32_bf16 v[160:163], a[16:19], v[32:35], 0
Rb 0,10,16384
v_add_f32_e32 v14, v48, v28
v_mfma_f32_16x16x32_bf16 v[168:171], a[16:19], v[36:39], 0
v_exp_f32_e32 v49, v98
v_mfma_f32_16x16x32_bf16 v[172:175], a[20:23], v[36:39], 0
Rb 4,10,17408
Cv 48,48,28
v_mfma_f32_16x16x32_bf16 v[164:167], a[20:23], v[32:35], 0
buffer_load_dwordx4 v2, s[20:23], s55 offen offset:1024 sc1 lds
v_mfma_f32_16x16x32_bf16 v[176:179], a[24:27], v[32:35], 0
Rb 8,10,18432
v_exp_f32_e32 v29, v99
v_mfma_f32_16x16x32_bf16 v[184:187], a[24:27], v[36:39], 0
v_exp_f32_e32 v50, v104
v_mfma_f32_16x16x32_bf16 v[188:191], a[28:31], v[36:39], 0
Rb 12,10,19456
s_waitcnt lgkmcnt(8)
v_mfma_f32_16x16x32_bf16 v[180:183], a[28:31], v[32:35], 0
v_exp_f32_e32 v24, v105
v_mfma_f32_16x16x32_bf16 v[192:195], a[32:35], v[32:35], 0
Rb 16,10,20480
v_add_f32_e32 v14, v14, v49
v_mfma_f32_16x16x32_bf16 v[200:203], a[32:35], v[36:39], 0
v_exp_f32_e32 v51, v106
v_add_f32_e32 v14, v14, v29
v_mfma_f32_16x16x32_bf16 v[204:207], a[36:39], v[36:39], 0
Rb 20,10,21504
Cv 49,49,29
v_mfma_f32_16x16x32_bf16 v[196:199], a[36:39], v[32:35], 0
buffer_load_dwordx4 v2, s[20:23], s55 offen offset:2048 sc1 lds
v_mfma_f32_16x16x32_bf16 v[208:211], a[40:43], v[32:35], 0
Rb 24,10,22528
v_add_f32_e32 v14, v14, v50
v_mfma_f32_16x16x32_bf16 v[216:219], a[40:43], v[36:39], 0
v_exp_f32_e32 v25, v107
v_mfma_f32_16x16x32_bf16 v[220:223], a[44:47], v[36:39], 0
Rb 28,10,23552
s_waitcnt lgkmcnt(8)
v_mfma_f32_16x16x32_bf16 v[212:215], a[44:47], v[32:35], 0
v_exp_f32_e32 v52, v100
v_add_f32_e32 v14, v14, v24
v_mfma_f32_16x16x32_bf16 v[224:227], a[48:51], v[32:35], 0
Rb 32,10,24576
Cv 50,50,24
v_mfma_f32_16x16x32_bf16 v[232:235], a[48:51], v[36:39], 0
v_exp_f32_e32 v26, v101
)" R"(
v_mfma_f32_16x16x32_bf16 v[236:239], a[52:55], v[36:39], 0
Rb 36,10,25600
v_add_f32_e32 v14, v14, v51
v_mfma_f32_16x16x32_bf16 v[228:231], a[52:55], v[32:35], 0
buffer_load_dwordx4 v2, s[20:23], s55 offen offset:3072 sc1 lds
v_mfma_f32_16x16x32_bf16 v[240:243], a[56:59], v[32:35], 0
Rb 40,10,26624
v_exp_f32_e32 v53, v102
v_mfma_f32_16x16x32_bf16 v[248:251], a[56:59], v[36:39], 0
v_exp_f32_e32 v27, v103
v_mfma_f32_16x16x32_bf16 v[252:255], a[60:63], v[36:39], 0
Rb 44,10,27648
s_waitcnt lgkmcnt(8)
v_mfma_f32_16x16x32_bf16 v[244:247], a[60:63], v[32:35], 0
v_exp_f32_e32 v54, v108
Mp 128,0,40
Rb 48,10,28672
v_add_f32_e32 v14, v14, v25
Mp 136,0,44
v_exp_f32_e32 v28, v109
Cv 51,51,25
Mp 140,4,44
Rb 52,10,29696
s_add_u32 m0, s36, 36864
Mp 132,4,40
buffer_load_dwordx4 v3, s[20:23], s55 offen offset:0 sc1 lds
Mp 144,8,40
Rb 56,10,30720
v_add_f32_e32 v15, v52, v26
Mp 152,8,44
v_exp_f32_e32 v55, v110
Mp 156,12,44
Rb 60,10,31744
s_waitcnt lgkmcnt(8)
Mp 148,12,40
v_exp_f32_e32 v29, v111
Cv 52,52,26
Mp 160,16,40
v_add_f32_e32 v15, v15, v53
v_add_f32_e32 v15, v15, v27
Mp 168,16,44
Cv 53,53,27
v_add_f32_e32 v15, v15, v54
Mp 172,20,44
v_add_f32_e32 v15, v15, v28
Cv 54,54,28
Mp 164,20,40
buffer_load_dwordx4 v3, s[20:23], s55 offen offset:1024 sc1 lds
Mp 176,24,40
v_add_f32_e32 v15, v15, v55
v_add_f32_e32 v15, v15, v29
Cv 55,55,29
Mp 184,24,44
s_sub_u32 s57, s57, 1
v_accvgpr_read_b32 v9, a192
Mp 188,28,44
v_accvgpr_read_b32 v11, a193
Cv 9,9,11
Mp 180,28,40
s_waitcnt vmcnt(6) lgkmcnt(0)
s_barrier
buffer_store_dword v6, v7, s[20:23], s49 offen offset:1024 sc0 sc1
Mp 192,32,40
Rb 0,1,0
v_accvgpr_write_b32 a144, v9
)" R"(
Mp 200,32,44
Rb 4,1,1024
v_accvgpr_read_b32 v9, a194
v_accvgpr_read_b32 v11, a195
Mp 204,36,44
Rb 8,1,2048
Cv 9,9,11
Mp 196,36,40
Rb 12,1,3072
buffer_load_dwordx4 v3, s[20:23], s55 offen offset:2048 sc1 lds
Mp 208,40,40
Rb 16,1,8192
v_accvgpr_write_b32 a145, v9
Mp 216,40,44
Rb 20,1,9216
v_accvgpr_read_b32 v9, a196
Mp 220,44,44
Rb 24,1,10240
v_accvgpr_read_b32 v11, a197
Mp 212,44,40
Rb 28,1,11264
Cv 9,9,11
v_accvgpr_write_b32 a146, v9
Mp 224,48,40
Rb 32,1,4096
v_accvgpr_read_b32 v9, a198
Mp 232,48,44
Rb 36,1,5120
v_accvgpr_read_b32 v11, a199
Mp 236,52,44
Rb 40,1,6144
Cv 9,9,11
Mp 228,52,40
Rb 44,1,7168
buffer_load_dwordx4 v3, s[20:23], s55 offen offset:3072 sc1 lds
Mp 240,56,40
v_cmp_eq_u32_e32 vcc, s3, v4
s_andn2_b64 s[28:29], -1, vcc
s_cbranch_scc1 .Lzsp18
.Lzok18:
Mp 248,56,44
v_accvgpr_write_b32 a147, v9
v_accvgpr_read_b32 v9, a200
Mp 252,60,44
v_accvgpr_read_b32 v11, a201
s_waitcnt lgkmcnt(8)
Mp 244,60,40
Cv 9,9,11
v_accvgpr_write_b32 a148, v9
v_mfma_f32_16x16x32_bf16 v[64:67], a[0:3], a[64:67], v[16:19]
Rb 48,1,12288
v_add_u32 v9, 4, v0
v_mfma_f32_16x16x32_bf16 v[68:71], a[0:3], a[68:71], v[20:23]
v_xor_b32 v9, v9, v0
v_and_b32 v9, 32, v9
Mv 68,4,76
Rb 52,1,13312
s_add_u32 m0, s36, 98304
Mv 64,4,72
buffer_load_dwordx4 v2, s[20:23], s56 offen offset:0 sc1 lds
Mv 64,8,80
Rb 56,1,14336
v_lshlrev_b32 v9, 22, v9
Mv 68,8,84
)" R"(
v_and_or_b32 v9, v0, 32, v9
v_mul_lo_u32 v9, v9, 12
Mv 68,12,92
Rb 60,1,15360
s_waitcnt lgkmcnt(8)
Mv 64,12,88
buffer_load_dwordx4 v2, s[20:23], s56 offen offset:1024 sc1 lds
Mv 64,16,96
Rb 0,1,16384
v_add_u32 v9, v9, v1
Mv 68,16,100
v_exp_f32_e32 v56, v112
Mv 68,20,108
Rb 4,1,17408
v_exp_f32_e32 v24, v113
Mv 64,20,104
buffer_load_dwordx4 v2, s[20:23], s56 offen offset:2048 sc1 lds
Mv 64,24,112
Rb 8,1,18432
v_exp_f32_e32 v57, v114
Mv 68,24,116
v_exp_f32_e32 v25, v115
Mv 68,28,124
Rb 12,1,19456
v_add_f32_e32 v14, v14, v56
s_waitcnt lgkmcnt(8)
Mv 64,28,120
buffer_load_dwordx4 v2, s[20:23], s56 offen offset:3072 sc1 lds
v_add_f32_e32 v14, v14, v24
v_mfma_f32_16x16x32_bf16 v[72:75], a[32:35], a[64:67], v[16:19]
Rb 16,1,24576
Cv 56,56,24
v_mfma_f32_16x16x32_bf16 v[76:79], a[32:35], a[68:71], v[20:23]
v_exp_f32_e32 v58, v120
v_add_f32_e32 v14, v14, v57
Mv 76,36,76
Rb 20,1,25600
s_add_u32 m0, s36, 102400
Mv 72,36,72
buffer_load_dwordx4 v3, s[20:23], s56 offen offset:0 sc1 lds
v_add_f32_e32 v14, v14, v25
Mv 72,40,80
Rb 24,1,26624
Cv 57,57,25
Mv 76,40,84
v_exp_f32_e32 v26, v121
v_add_f32_e32 v14, v14, v58
Mv 76,44,92
Rb 28,1,27648
s_waitcnt lgkmcnt(8)
Mv 72,44,88
buffer_load_dwordx4 v3, s[20:23], s56 offen offset:1024 sc1 lds
v_add_f32_e32 v14, v14, v26
Mv 72,48,96
Rb 32,1,20480
v_exp_f32_e32 v59, v122
Mv 76,48,100
v_exp_f32_e32 v27, v123
Mv 76,52,108
Rb 36,1,21504
v_exp_f32_e32 v60, v116
Mv 72,52,104
buffer_load_dwordx4 v3, s[20:23], s56 offen offset:2048 sc1 lds
Mv 72,56,112
)" R"(
Rb 40,1,22528
v_exp_f32_e32 v28, v117
Mv 76,56,116
v_exp_f32_e32 v61, v118
Mv 76,60,124
Rb 44,1,23552
Cv 58,58,26
s_waitcnt lgkmcnt(8)
Mv 72,60,120
buffer_load_dwordx4 v3, s[20:23], s56 offen offset:3072 sc1 lds
v_add_f32_e32 v14, v14, v59
v_mfma_f32_16x16x32_bf16 v[80:83], a[0:3], a[64:67], v[16:19]
Rb 48,1,28672
s_xor_b32 m0, s82, 0
v_mfma_f32_16x16x32_bf16 v[84:87], a[0:3], a[68:71], v[20:23]
s_add_u32 s56, s56, 0x10000
s_add_u32 s92, s83, 448
v_add_f32_e32 v14, v14, v27
Mv 84,4,76
Rb 52,1,29696
Cv 59,59,27
Mv 80,4,72
buffer_load_dwordx4 v9, s[24:27], s92 offen offset:0 lds
v_add_f32_e32 v15, v15, v60
Mv 80,8,80
Rb 56,1,30720
s_add_u32 s92, s83, 1216
Mv 84,8,84
v_exp_f32_e32 v29, v119
v_add_f32_e32 v15, v15, v28
Mv 84,12,92
Rb 60,1,31744
s_waitcnt lgkmcnt(8)
Mv 80,12,88
buffer_load_dwordx4 v9, s[24:27], s92 offen offset:1024 lds
s_add_u32 s92, s83, 1984
Mv 80,16,96
v_exp_f32_e32 v62, v124
Cv 60,60,28
Mv 84,16,100
v_exp_f32_e32 v24, v125
Mv 84,20,108
v_exp_f32_e32 v63, v126
v_add_f32_e32 v15, v15, v61
Mv 80,20,104
buffer_load_dwordx4 v9, s[24:27], s92 offen offset:2048 lds
Mv 80,24,112
s_add_u32 s92, s83, 2752
v_exp_f32_e32 v25, v127
Mv 84,24,116
v_add_f32_e32 v15, v15, v29
Cv 61,61,29
Mv 84,28,124
buffer_load_dwordx4 v9, s[24:27], s92 offen offset:3072 lds
v_add_f32_e32 v15, v15, v62
Mv 80,28,120
s_xor_b32 m0, s82, 16384
s_waitcnt lgkmcnt(0)
s_barrier
v_mfma_f32_16x16x32_bf16 v[88:91], a[32:35], a[64:67], v[16:19]
Rb 0,8,0
s_add_u32 s92, s83, 0
v_mfma_f32_16x16x32_bf16 v[92:95], a[32:35], a[68:71], v[20:23]
Rb 4,8,1024
v_add_f32_e32 v15, v15, v24
)" R"(
Cv 62,62,24
Mv 92,36,76
Rb 8,8,2048
v_add_f32_e32 v15, v15, v63
Mv 88,36,72
Rb 12,8,3072
v_add_f32_e32 v15, v15, v25
Cv 63,63,25
Mv 88,40,80
Rb 16,8,4096
buffer_load_dwordx4 v9, s[24:27], s92 offen offset:0 lds
Mv 92,40,84
Rb 20,8,5120
s_add_u32 s92, s83, 768
v_max_f32_e32 v11, v14, v15
Mv 92,44,92
Rb 24,8,6144
v_cmp_nge_f32_e32 vcc, s62, v11
Mv 88,44,88
Rb 28,8,7168
s_add_u32 s55, s55, 0x10000
Mv 88,48,96
Rb 32,8,8192
s_cmp_eq_u32 s57, 2
s_cselect_b32 s55, s59, s55
Mv 92,48,100
Rb 36,8,9216
buffer_load_dwordx4 v9, s[24:27], s92 offen offset:1024 lds
Mv 92,52,108
Rb 40,8,10240
s_add_u32 s92, s83, 1536
Mv 88,52,104
Rb 44,8,11264
Mv 88,56,112
buffer_load_dwordx4 v9, s[24:27], s92 offen offset:2048 lds
s_add_u32 s92, s83, 2304
Mv 92,56,116
buffer_load_dwordx4 v9, s[24:27], s92 offen offset:3072 lds
Mv 92,60,124
s_waitcnt lgkmcnt(8)
s_cbranch_vccnz .Lzfx7
.Lzfr7:
Mv 88,60,120
v_add_f32_e32 v12, v12, v14
v_add_f32_e32 v13, v13, v15
Mp 128,0,48
Rb 48,8,12288
Mp 136,0,52
s_cmp_le_i32 s57, s58
s_cbranch_scc1 .Lzmk6
.Lzmr6:
Mp 140,4,52
Rb 52,8,13312
s_add_u32 m0, s36, 0
Mp 132,4,48
buffer_load_dwordx4 v2, s[20:23], s55 offen offset:0 sc1 lds
Mp 144,8,48
Rb 56,8,14336
v_exp_f32_e32 v32, v64
.LzH11:
s_cbranch_execz .LzH19
Mp 152,8,52
buffer_store_dwordx4 a[224:227], v2, s[20:23], s47 offen offset:0 sc0 sc1
Mp 156,12,52
)" R"(
Rb 60,8,15360
s_waitcnt lgkmcnt(8)
Mp 148,12,48
v_exp_f32_e32 v26, v65
Mp 160,16,48
Rb 0,8,16384
v_add_f32_e32 v14, v32, v26
Mp 168,16,52
v_exp_f32_e32 v33, v66
Cv 32,32,26
Mp 172,20,52
Rb 4,8,17408
v_add_f32_e32 v14, v14, v33
Mp 164,20,48
buffer_load_dwordx4 v2, s[20:23], s55 offen offset:1024 sc1 lds
Mp 176,24,48
Rb 8,8,18432
v_exp_f32_e32 v27, v67
Mp 184,24,52
v_exp_f32_e32 v34, v72
Mp 188,28,52
Rb 12,8,19456
s_waitcnt lgkmcnt(8)
Mp 180,28,48
v_exp_f32_e32 v28, v73
v_add_f32_e32 v14, v14, v27
Mp 192,32,48
Rb 16,8,20480
Cv 33,33,27
Mp 200,32,52
v_exp_f32_e32 v35, v74
v_add_f32_e32 v14, v14, v34
Mp 204,36,52
Rb 20,8,21504
v_add_f32_e32 v14, v14, v28
Mp 196,36,48
buffer_load_dwordx4 v2, s[20:23], s55 offen offset:2048 sc1 lds
Mp 208,40,48
Rb 24,8,22528
v_exp_f32_e32 v29, v75
Mp 216,40,52
buffer_store_dwordx4 a[228:231], v2, s[20:23], s47 offen offset:1024 sc0 sc1
Mp 220,44,52
Rb 28,8,23552
s_waitcnt lgkmcnt(8)
Mp 212,44,48
v_exp_f32_e32 v36, v68
Cv 34,34,28
Mp 224,48,48
Rb 32,8,24576
v_add_f32_e32 v14, v14, v35
Mp 232,48,52
v_exp_f32_e32 v24, v69
v_add_f32_e32 v14, v14, v29
Mp 236,52,52
Rb 36,8,25600
Cv 35,35,29
Mp 228,52,48
buffer_load_dwordx4 v2, s[20:23], s55 offen offset:3072 sc1 lds
Mp 240,56,48
Rb 40,8,26624
v_exp_f32_e32 v37, v70
Mp 248,56,52
v_exp_f32_e32 v25, v71
)" R"(
Mp 252,60,52
Rb 44,8,27648
s_waitcnt lgkmcnt(8)
Mp 244,60,48
v_exp_f32_e32 v38, v76
v_add_f32_e32 v15, v36, v24
Mp 128,0,56
Rb 48,8,28672
Cv 36,36,24
Mp 136,0,60
v_exp_f32_e32 v26, v77
v_add_f32_e32 v15, v15, v37
Mp 140,4,60
Rb 52,8,29696
s_add_u32 m0, s36, 4096
Mp 132,4,56
buffer_load_dwordx4 v3, s[20:23], s55 offen offset:0 sc1 lds
Mp 144,8,56
Rb 56,8,30720
v_exp_f32_e32 v39, v78
Mp 152,8,60
buffer_store_dwordx4 a[232:235], v2, s[20:23], s47 offen offset:2048 sc0 sc1
Mp 156,12,60
Rb 60,8,31744
s_waitcnt lgkmcnt(8)
Mp 148,12,56
v_exp_f32_e32 v27, v79
v_add_f32_e32 v15, v15, v25
Mp 160,16,56
Cv 37,37,25
v_add_f32_e32 v15, v15, v38
Mp 168,16,60
v_add_f32_e32 v15, v15, v26
Cv 38,38,26
v_add_f32_e32 v15, v15, v39
Mp 172,20,60
v_add_f32_e32 v15, v15, v27
Cv 39,39,27
Mp 164,20,56
buffer_load_dwordx4 v3, s[20:23], s55 offen offset:1024 sc1 lds
Mp 176,24,56
s_add_u32 s50, s50, 1024
s_min_u32 s50, s50, s60
s_sub_u32 s57, s57, 1
Mp 184,24,60
v_accvgpr_read_b32 v9, a202
v_accvgpr_read_b32 v11, a203
Mp 188,28,60
Cv 9,9,11
v_accvgpr_write_b32 a149, v9
Mp 180,28,56
v_accvgpr_read_b32 v9, a204
s_waitcnt vmcnt(25) lgkmcnt(0)
s_barrier
Mp 192,32,56
Rb 0,1,32768
v_accvgpr_read_b32 v11, a205
Mp 200,32,60
Rb 4,1,33792
Cv 9,9,11
v_accvgpr_write_b32 a150, v9
Mp 204,36,60
Rb 8,1,34816
v_accvgpr_read_b32 v9, a206
Mp 196,36,56
)" R"(
Rb 12,1,35840
buffer_load_dwordx4 v3, s[20:23], s55 offen offset:2048 sc1 lds
Mp 208,40,56
Rb 16,1,40960
v_accvgpr_read_b32 v11, a207
Cv 9,9,11
Mp 216,40,60
Rb 20,1,41984
buffer_store_dwordx4 a[236:239], v2, s[20:23], s47 offen offset:3072 sc0 sc1
Mp 220,44,60
Rb 24,1,43008
v_accvgpr_write_b32 a151, v9
Mp 212,44,56
Rb 28,1,44032
v_accvgpr_read_b32 v9, a208
v_accvgpr_read_b32 v11, a209
Mp 224,48,56
Rb 32,1,36864
Cv 9,9,11
Mp 232,48,60
Rb 36,1,37888
v_accvgpr_write_b32 a152, v9
v_accvgpr_read_b32 v9, a210
Mp 236,52,60
Rb 40,1,38912
v_accvgpr_read_b32 v11, a211
Mp 228,52,56
Rb 44,1,39936
buffer_load_dwordx4 v3, s[20:23], s55 offen offset:3072 sc1 lds
Mp 240,56,56
Cv 9,9,11
v_accvgpr_write_b32 a153, v9
v_accvgpr_read_b32 v9, a212
Mp 248,56,60
v_accvgpr_read_b32 v11, a213
Cv 9,9,11
Mp 252,60,60
v_accvgpr_write_b32 a154, v9
s_waitcnt lgkmcnt(8)
Mp 244,60,56
v_mfma_f32_16x16x32_bf16 v[96:99], a[0:3], a[64:67], v[16:19]
Rb 48,1,45056
buffer_load_dword v4, v5, s[20:23], s50 offen offset:0 sc0 sc1
v_mfma_f32_16x16x32_bf16 v[100:103], a[0:3], a[68:71], v[20:23]
v_exp_f32_e32 v40, v80
Mv 100,4,76
Rb 52,1,46080
s_add_u32 m0, s36, 65536
Mv 96,4,72
buffer_load_dwordx4 v2, s[20:23], s56 offen offset:0 sc1 lds
Mv 96,8,80
Rb 56,1,47104
v_add_f32_e32 v14, v14, v40
Mv 100,8,84
buffer_store_dwordx4 a[240:243], v3, s[20:23], s47 offen offset:0 sc0 sc1
Mv 100,12,92
Rb 60,1,48128
s_waitcnt lgkmcnt(8)
Mv 96,12,88
v_exp_f32_e32 v28, v81
Mv 96,16,96
Rb 0,1,49152
v_add_f32_e32 v14, v14, v28
Mv 100,16,100
)" R"(
v_exp_f32_e32 v41, v82
Mv 100,20,108
Rb 4,1,50176
Cv 40,40,28
Mv 96,20,104
buffer_load_dwordx4 v2, s[20:23], s56 offen offset:1024 sc1 lds
Mv 96,24,112
Rb 8,1,51200
v_add_f32_e32 v14, v14, v41
Mv 100,24,116
v_exp_f32_e32 v29, v83
Mv 100,28,124
Rb 12,1,52224
s_waitcnt lgkmcnt(8)
Mv 96,28,120
v_exp_f32_e32 v42, v88
v_add_f32_e32 v14, v14, v29
v_mfma_f32_16x16x32_bf16 v[104:107], a[32:35], a[64:67], v[16:19]
Rb 16,1,57344
Cv 41,41,29
v_mfma_f32_16x16x32_bf16 v[108:111], a[32:35], a[68:71], v[20:23]
v_exp_f32_e32 v24, v89
Mv 108,36,76
Rb 20,1,58368
v_add_f32_e32 v14, v14, v42
Mv 104,36,72
buffer_load_dwordx4 v2, s[20:23], s56 offen offset:2048 sc1 lds
Mv 104,40,80
Rb 24,1,59392
v_add_f32_e32 v14, v14, v24
Mv 108,40,84
buffer_store_dwordx4 a[244:247], v3, s[20:23], s47 offen offset:1024 sc0 sc1
Mv 108,44,92
Rb 28,1,60416
s_waitcnt lgkmcnt(8)
Mv 104,44,88
v_exp_f32_e32 v43, v90
Mv 104,48,96
Rb 32,1,53248
Cv 42,42,24
Mv 108,48,100
v_exp_f32_e32 v25, v91
Mv 108,52,108
Rb 36,1,54272
v_add_f32_e32 v14, v14, v43
Mv 104,52,104
buffer_load_dwordx4 v2, s[20:23], s56 offen offset:3072 sc1 lds
Mv 104,56,112
Rb 40,1,55296
v_add_f32_e32 v14, v14, v25
Mv 108,56,116
v_exp_f32_e32 v44, v84
Mv 108,60,124
Rb 44,1,56320
s_waitcnt lgkmcnt(8)
Mv 104,60,120
v_exp_f32_e32 v26, v85
Cv 43,43,25
v_mfma_f32_16x16x32_bf16 v[112:115], a[0:3], a[64:67], v[16:19]
Rb 48,1,61440
v_add_f32_e32 v15, v15, v44
v_mfma_f32_16x16x32_bf16 v[116:119], a[0:3], a[68:71], v[20:23]
v_exp_f32_e32 v45, v86
Mv 116,4,76
)" R"(
Rb 52,1,62464
s_add_u32 m0, s36, 69632
Mv 112,4,72
buffer_load_dwordx4 v3, s[20:23], s56 offen offset:0 sc1 lds
Mv 112,8,80
Rb 56,1,63488
v_add_f32_e32 v15, v15, v26
Mv 116,8,84
buffer_store_dwordx4 a[248:251], v3, s[20:23], s47 offen offset:2048 sc0 sc1
Mv 116,12,92
Rb 60,1,64512
s_waitcnt lgkmcnt(8)
Mv 112,12,88
v_exp_f32_e32 v27, v87
Mv 112,16,96
v_exp_f32_e32 v46, v92
Mv 116,16,100
v_exp_f32_e32 v28, v93
Mv 116,20,108
v_exp_f32_e32 v47, v94
Mv 112,20,104
buffer_load_dwordx4 v3, s[20:23], s56 offen offset:1024 sc1 lds
Mv 112,24,112
v_exp_f32_e32 v29, v95
Mv 116,24,116
Cv 44,44,26
v_add_f32_e32 v15, v15, v45
Mv 116,28,124
v_add_f32_e32 v15, v15, v27
Cv 45,45,27
Mv 112,28,120
v_add_f32_e32 v15, v15, v46
s_waitcnt vmcnt(30) lgkmcnt(0)
s_barrier
v_mfma_f32_16x16x32_bf16 v[120:123], a[32:35], a[64:67], v[16:19]
Rb 0,8,32768
v_add_f32_e32 v15, v15, v28
v_mfma_f32_16x16x32_bf16 v[124:127], a[32:35], a[68:71], v[20:23]
Rb 4,8,33792
Cv 46,46,28
Mv 124,36,76
Rb 8,8,34816
v_add_f32_e32 v15, v15, v47
Mv 120,36,72
Rb 12,8,35840
buffer_load_dwordx4 v3, s[20:23], s56 offen offset:2048 sc1 lds
Mv 120,40,80
Rb 16,8,36864
v_add_f32_e32 v15, v15, v29
Mv 124,40,84
Rb 20,8,37888
buffer_store_dwordx4 a[252:255], v3, s[20:23], s47 offen offset:3072 sc0 sc1
Mv 124,44,92
Rb 24,8,38912
Cv 47,47,29
Mv 120,44,88
Rb 28,8,39936
v_max_f32_e32 v11, v14, v15
Mv 120,48,96
Rb 32,8,40960
v_cmp_nge_f32_e32 vcc, s62, v11
Mv 124,48,100
Rb 36,8,41984
s_add_u32 s55, s55, 0x10000
Mv 124,52,108
)" R"(
Rb 40,8,43008
Mv 120,52,104
Rb 44,8,44032
buffer_load_dwordx4 v3, s[20:23], s56 offen offset:3072 sc1 lds
Mv 120,56,112
s_cmp_eq_u32 s57, 2
s_cselect_b32 s55, s59, s55
Mv 124,56,116
s_add_u32 s56, s56, 0x10000
Mv 124,60,124
s_waitcnt lgkmcnt(8)
s_cbranch_vccnz .Lzfx8
.Lzfr8:
Mv 120,60,120
v_add_f32_e32 v12, v12, v14
v_add_f32_e32 v13, v13, v15
Mp 128,0,32
Rb 48,8,45056
Mp 136,0,36
s_cmp_le_i32 s57, s58
s_cbranch_scc1 .Lzmk7
.Lzmr7:
Mp 140,4,36
Rb 52,8,46080
s_add_u32 m0, s36, 32768
Mp 132,4,32
buffer_load_dwordx4 v2, s[20:23], s55 offen offset:0 sc1 lds
Mp 144,8,32
Rb 56,8,47104
Mp 152,8,36
v_exp_f32_e32 v48, v96
Mp 156,12,36
Rb 60,8,48128
s_waitcnt lgkmcnt(8)
Mp 148,12,32
v_exp_f32_e32 v24, v97
Mp 160,16,32
Rb 0,8,49152
v_add_f32_e32 v14, v48, v24
Mp 168,16,36
v_exp_f32_e32 v49, v98
Mp 172,20,36
Rb 4,8,50176
Cv 48,48,24
Mp 164,20,32
buffer_load_dwordx4 v2, s[20:23], s55 offen offset:1024 sc1 lds
Mp 176,24,32
Rb 8,8,51200
v_exp_f32_e32 v25, v99
Mp 184,24,36
v_exp_f32_e32 v50, v104
Mp 188,28,36
Rb 12,8,52224
s_waitcnt lgkmcnt(8)
Mp 180,28,32
v_exp_f32_e32 v26, v105
Mp 192,32,32
Rb 16,8,53248
v_add_f32_e32 v14, v14, v49
Mp 200,32,36
v_exp_f32_e32 v51, v106
v_add_f32_e32 v14, v14, v25
Mp 204,36,36
Rb 20,8,54272
)" R"(
Cv 49,49,25
Mp 196,36,32
buffer_load_dwordx4 v2, s[20:23], s55 offen offset:2048 sc1 lds
Mp 208,40,32
Rb 24,8,55296
v_add_f32_e32 v14, v14, v50
Mp 216,40,36
v_exp_f32_e32 v27, v107
Mp 220,44,36
Rb 28,8,56320
s_waitcnt lgkmcnt(8)
Mp 212,44,32
v_exp_f32_e32 v52, v100
v_add_f32_e32 v14, v14, v26
Mp 224,48,32
Rb 32,8,57344
Cv 50,50,26
Mp 232,48,36
v_exp_f32_e32 v28, v101
Mp 236,52,36
Rb 36,8,58368
v_add_f32_e32 v14, v14, v51
Mp 228,52,32
buffer_load_dwordx4 v2, s[20:23], s55 offen offset:3072 sc1 lds
Mp 240,56,32
Rb 40,8,59392
v_exp_f32_e32 v53, v102
Mp 248,56,36
v_exp_f32_e32 v29, v103
Mp 252,60,36
Rb 44,8,60416
s_waitcnt lgkmcnt(8)
Mp 244,60,32
v_exp_f32_e32 v54, v108
Mp 128,0,40
Rb 48,8,61440
v_add_f32_e32 v14, v14, v27
Mp 136,0,44
v_exp_f32_e32 v24, v109
Cv 51,51,27
Mp 140,4,44
Rb 52,8,62464
s_add_u32 m0, s36, 36864
Mp 132,4,40
buffer_load_dwordx4 v3, s[20:23], s55 offen offset:0 sc1 lds
Mp 144,8,40
Rb 56,8,63488
v_add_f32_e32 v15, v52, v28
Mp 152,8,44
v_exp_f32_e32 v55, v110
Mp 156,12,44
Rb 60,8,64512
s_waitcnt lgkmcnt(8)
Mp 148,12,40
v_exp_f32_e32 v25, v111
Cv 52,52,28
Mp 160,16,40
v_add_f32_e32 v15, v15, v53
v_add_f32_e32 v15, v15, v29
Mp 168,16,44
Cv 53,53,29
v_add_f32_e32 v15, v15, v54
Mp 172,20,44
v_add_f32_e32 v15, v15, v24
)" R"(
Cv 54,54,24
Mp 164,20,40
buffer_load_dwordx4 v3, s[20:23], s55 offen offset:1024 sc1 lds
Mp 176,24,40
v_add_f32_e32 v15, v15, v55
v_add_f32_e32 v15, v15, v25
Cv 55,55,25
Mp 184,24,44
s_sub_u32 s57, s57, 1
v_accvgpr_read_b32 v9, a214
Mp 188,28,44
v_accvgpr_read_b32 v11, a215
Cv 9,9,11
Mp 180,28,40
s_waitcnt vmcnt(7) lgkmcnt(0)
s_barrier
buffer_store_dword v6, v7, s[20:23], s49 offen offset:2048 sc0 sc1
Mp 192,32,40
Rb 0,1,0
v_accvgpr_write_b32 a155, v9
Mp 200,32,44
Rb 4,1,1024
v_accvgpr_read_b32 v9, a216
v_accvgpr_read_b32 v11, a217
Mp 204,36,44
Rb 8,1,2048
Cv 9,9,11
Mp 196,36,40
Rb 12,1,3072
buffer_load_dwordx4 v3, s[20:23], s55 offen offset:2048 sc1 lds
Mp 208,40,40
Rb 16,1,8192
v_accvgpr_write_b32 a156, v9
Mp 216,40,44
Rb 20,1,9216
v_accvgpr_read_b32 v9, a218
Mp 220,44,44
Rb 24,1,10240
v_accvgpr_read_b32 v11, a219
Mp 212,44,40
Rb 28,1,11264
Cv 9,9,11
v_accvgpr_write_b32 a157, v9
Mp 224,48,40
Rb 32,1,4096
v_accvgpr_read_b32 v9, a220
Mp 232,48,44
Rb 36,1,5120
v_accvgpr_read_b32 v11, a221
Mp 236,52,44
Rb 40,1,6144
Cv 9,9,11
Mp 228,52,40
Rb 44,1,7168
buffer_load_dwordx4 v3, s[20:23], s55 offen offset:3072 sc1 lds
Mp 240,56,40
v_cmp_eq_u32_e32 vcc, s3, v4
s_andn2_b64 s[28:29], -1, vcc
s_cbranch_scc1 .Lzsp19
.Lzok19:
Mp 248,56,44
v_accvgpr_write_b32 a158, v9
v_accvgpr_read_b32 v9, a222
Mp 252,60,44
v_accvgpr_read_b32 v11, a223
)" R"(
s_waitcnt lgkmcnt(8)
Mp 244,60,40
Cv 9,9,11
v_accvgpr_write_b32 a159, v9
v_mfma_f32_16x16x32_bf16 v[64:67], a[0:3], a[64:67], v[16:19]
Rb 48,1,12288
v_lshrrev_b32 v9, 2, v0
v_mfma_f32_16x16x32_bf16 v[68:71], a[0:3], a[68:71], v[20:23]
v_mul_u32_u24 v9, 448, v9
s_lshr_b32 s92, s33, 1
Mv 68,4,76
Rb 52,1,13312
s_add_u32 m0, s36, 98304
Mv 64,4,72
buffer_load_dwordx4 v2, s[20:23], s56 offen offset:0 sc1 lds
Mv 64,8,80
Rb 56,1,14336
s_mul_i32 s92, s92, 48
Mv 68,8,84
buffer_store_dwordx4 a[128:131], v2, s[20:23], s48 offen offset:0 sc0 sc1
Mv 68,12,92
Rb 60,1,15360
s_waitcnt lgkmcnt(8)
Mv 64,12,88
s_lshl3_add_u32 s92, s33, s92
v_add3_u32 v9, v9, s92, v10
Mv 64,16,96
Rb 0,1,16384
Mv 68,16,100
v_exp_f32_e32 v56, v112
Mv 68,20,108
Rb 4,1,17408
Rt 160,9,0
Mv 64,20,104
buffer_load_dwordx4 v2, s[20:23], s56 offen offset:1024 sc1 lds
Mv 64,24,112
Rb 8,1,18432
v_exp_f32_e32 v26, v113
Mv 68,24,116
v_exp_f32_e32 v57, v114
Mv 68,28,124
Rb 12,1,19456
s_waitcnt lgkmcnt(9)
Mv 64,28,120
v_exp_f32_e32 v27, v115
v_add_f32_e32 v14, v14, v56
v_mfma_f32_16x16x32_bf16 v[72:75], a[32:35], a[64:67], v[16:19]
Rb 16,1,24576
v_add_f32_e32 v14, v14, v26
v_mfma_f32_16x16x32_bf16 v[76:79], a[32:35], a[68:71], v[20:23]
v_exp_f32_e32 v58, v120
Mv 76,36,76
Rb 20,1,25600
Cv 56,56,26
Mv 72,36,72
buffer_load_dwordx4 v2, s[20:23], s56 offen offset:2048 sc1 lds
v_add_f32_e32 v14, v14, v57
Mv 72,40,80
Rb 24,1,26624
Rt 162,9,8192
Mv 76,40,84
v_exp_f32_e32 v28, v121
Mv 76,44,92
Rb 28,1,27648
)" R"(
v_add_f32_e32 v14, v14, v27
s_waitcnt lgkmcnt(10)
Mv 72,44,88
v_exp_f32_e32 v59, v122
Mv 72,48,96
Rb 32,1,20480
Cv 57,57,27
.LzH12:
s_cbranch_execz .LzH20
Mv 76,48,100
v_exp_f32_e32 v29, v123
v_add_f32_e32 v14, v14, v58
Mv 76,52,108
Rb 36,1,21504
v_add_f32_e32 v14, v14, v28
Mv 72,52,104
buffer_load_dwordx4 v2, s[20:23], s56 offen offset:3072 sc1 lds
Mv 72,56,112
Rb 40,1,22528
v_exp_f32_e32 v60, v116
Mv 76,56,116
buffer_store_dwordx4 a[132:135], v2, s[20:23], s48 offen offset:1024 sc0 sc1
Mv 76,60,124
Rb 44,1,23552
Rt 164,9,128
s_waitcnt lgkmcnt(10)
Mv 72,60,120
v_exp_f32_e32 v24, v117
Cv 58,58,28
v_mfma_f32_16x16x32_bf16 v[80:83], a[0:3], a[64:67], v[16:19]
Rb 48,1,28672
v_add_f32_e32 v14, v14, v59
v_mfma_f32_16x16x32_bf16 v[84:87], a[0:3], a[68:71], v[20:23]
v_exp_f32_e32 v61, v118
Mv 84,4,76
Rb 52,1,29696
s_add_u32 m0, s36, 102400
Mv 80,4,72
buffer_load_dwordx4 v3, s[20:23], s56 offen offset:0 sc1 lds
v_add_f32_e32 v14, v14, v29
Mv 80,8,80
Rb 56,1,30720
Cv 59,59,29
Mv 84,8,84
v_exp_f32_e32 v25, v119
Mv 84,12,92
Rb 60,1,31744
v_add_f32_e32 v15, v15, v60
s_waitcnt lgkmcnt(9)
Mv 80,12,88
v_exp_f32_e32 v62, v124
Mv 80,16,96
Rt 166,9,8320
v_add_f32_e32 v15, v15, v24
Mv 84,16,100
buffer_store_dwordx4 a[160:163], v1, s[20:23], s4 offen sc0 sc1
Cv 60,60,24
Mv 84,20,108
v_exp_f32_e32 v26, v125
Mv 80,20,104
buffer_load_dwordx4 v3, s[20:23], s56 offen offset:1024 sc1 lds
Mv 80,24,112
v_exp_f32_e32 v63, v126
v_add_f32_e32 v15, v15, v61
)" R"(
Mv 84,24,116
v_exp_f32_e32 v27, v127
Mv 84,28,124
v_add_f32_e32 v15, v15, v25
Cv 61,61,25
Mv 80,28,120
v_add_f32_e32 v15, v15, v62
s_waitcnt vmcnt(18) lgkmcnt(0)
s_barrier
v_mfma_f32_16x16x32_bf16 v[88:91], a[32:35], a[64:67], v[16:19]
Rb 0,8,0
v_add_f32_e32 v15, v15, v26
v_mfma_f32_16x16x32_bf16 v[92:95], a[32:35], a[68:71], v[20:23]
Rb 4,8,1024
Cv 62,62,26
Mv 92,36,76
Rb 8,8,2048
Rt 168,9,256
Mv 88,36,72
Rb 12,8,3072
buffer_load_dwordx4 v3, s[20:23], s56 offen offset:2048 sc1 lds
Mv 88,40,80
Rb 16,8,4096
v_add_f32_e32 v15, v15, v63
Mv 92,40,84
Rb 20,8,5120
buffer_store_dwordx4 a[136:139], v2, s[20:23], s48 offen offset:2048 sc0 sc1
Mv 92,44,92
Rb 24,8,6144
v_add_f32_e32 v15, v15, v27
Cv 63,63,27
Mv 88,44,88
Rb 28,8,7168
v_max_f32_e32 v11, v14, v15
Mv 88,48,96
Rb 32,8,8192
v_cmp_nge_f32_e32 vcc, s62, v11
Mv 92,48,100
Rb 36,8,9216
s_add_u32 s55, s55, 0x10000
Mv 92,52,108
Rb 40,8,10240
Mv 88,52,104
Rb 44,8,11264
buffer_load_dwordx4 v3, s[20:23], s56 offen offset:3072 sc1 lds
Mv 88,56,112
Rt 170,9,8448
s_cmp_eq_u32 s57, 2
s_cselect_b32 s55, s59, s55
Mv 92,56,116
s_add_u32 s56, s56, 0x10000
buffer_store_dwordx4 a[164:167], v1, s[20:23], s5 offen sc0 sc1
Mv 92,60,124
s_waitcnt lgkmcnt(9)
s_cbranch_vccnz .Lzfx9
.Lzfr9:
Mv 88,60,120
v_add_f32_e32 v12, v12, v14
v_add_f32_e32 v13, v13, v15
Mp 128,0,48
Rb 48,8,12288
Mp 136,0,52
s_cmp_le_i32 s57, s58
s_cbranch_scc1 .Lzmk8
.Lzmr8:
)" R"(
Mp 140,4,52
Rb 52,8,13312
s_add_u32 m0, s36, 0
Mp 132,4,48
buffer_load_dwordx4 v2, s[20:23], s55 offen offset:0 sc1 lds
Mp 144,8,48
Rb 56,8,14336
Mp 152,8,52
v_exp_f32_e32 v32, v64
Mp 156,12,52
Rb 60,8,15360
Rt 172,9,384
s_waitcnt lgkmcnt(10)
Mp 148,12,48
v_exp_f32_e32 v28, v65
Mp 160,16,48
Rb 0,8,16384
v_add_f32_e32 v14, v32, v28
Mp 168,16,52
v_exp_f32_e32 v33, v66
Cv 32,32,28
Mp 172,20,52
Rb 4,8,17408
v_add_f32_e32 v14, v14, v33
Mp 164,20,48
buffer_load_dwordx4 v2, s[20:23], s55 offen offset:1024 sc1 lds
Mp 176,24,48
Rb 8,8,18432
Mp 184,24,52
buffer_store_dwordx4 a[140:143], v2, s[20:23], s48 offen offset:3072 sc0 sc1
Mp 188,28,52
Rb 12,8,19456
s_waitcnt lgkmcnt(10)
Mp 180,28,48
v_exp_f32_e32 v29, v67
Mp 192,32,48
Rb 16,8,20480
Rt 174,9,8576
Mp 200,32,52
v_exp_f32_e32 v34, v72
v_add_f32_e32 v14, v14, v29
Mp 204,36,52
Rb 20,8,21504
Cv 33,33,29
Mp 196,36,48
buffer_load_dwordx4 v2, s[20:23], s55 offen offset:2048 sc1 lds
Mp 208,40,48
Rb 24,8,22528
v_add_f32_e32 v14, v14, v34
Mp 216,40,52
v_exp_f32_e32 v24, v73
Mp 220,44,52
Rb 28,8,23552
s_waitcnt lgkmcnt(10)
Mp 212,44,48
buffer_store_dwordx4 a[168:171], v1, s[20:23], s6 offen sc0 sc1
Mp 224,48,48
Rb 32,8,24576
v_add_f32_e32 v14, v14, v24
Mp 232,48,52
v_exp_f32_e32 v35, v74
Cv 34,34,24
Mp 236,52,52
Rb 36,8,25600
)" R"(
Rt 176,9,16384
Mp 228,52,48
buffer_load_dwordx4 v2, s[20:23], s55 offen offset:3072 sc1 lds
Mp 240,56,48
Rb 40,8,26624
v_add_f32_e32 v14, v14, v35
Mp 248,56,52
v_exp_f32_e32 v25, v75
Mp 252,60,52
Rb 44,8,27648
s_waitcnt lgkmcnt(10)
Mp 244,60,48
v_exp_f32_e32 v36, v68
Mp 128,0,56
Rb 48,8,28672
v_add_f32_e32 v14, v14, v25
Mp 136,0,60
v_exp_f32_e32 v26, v69
Cv 35,35,25
Mp 140,4,60
Rb 52,8,29696
s_add_u32 m0, s36, 4096
Mp 132,4,56
buffer_load_dwordx4 v3, s[20:23], s55 offen offset:0 sc1 lds
Mp 144,8,56
Rb 56,8,30720
Rt 178,9,24576
Mp 152,8,60
buffer_store_dwordx4 a[144:147], v3, s[20:23], s48 offen offset:0 sc0 sc1
Mp 156,12,60
Rb 60,8,31744
s_waitcnt lgkmcnt(10)
Mp 148,12,56
v_exp_f32_e32 v37, v70
Mp 160,16,56
v_exp_f32_e32 v27, v71
Mp 168,16,60
v_exp_f32_e32 v38, v76
v_add_f32_e32 v15, v36, v26
Mp 172,20,60
v_exp_f32_e32 v28, v77
Mp 164,20,56
buffer_load_dwordx4 v3, s[20:23], s55 offen offset:1024 sc1 lds
Mp 176,24,56
s_add_u32 s50, s50, 1024
s_min_u32 s50, s50, s60
Mp 184,24,60
buffer_store_dwordx4 a[172:175], v1, s[20:23], s7 offen sc0 sc1
Mp 188,28,60
Rt 180,9,16512
Cv 36,36,26
Mp 180,28,56
s_waitcnt vmcnt(23) lgkmcnt(1)
s_barrier
Mp 192,32,56
Rb 0,1,32768
v_add_f32_e32 v15, v15, v37
Mp 200,32,60
Rb 4,1,33792
v_exp_f32_e32 v39, v78
Mp 204,36,60
Rb 8,1,34816
v_add_f32_e32 v15, v15, v27
Mp 196,36,56
Rb 12,1,35840
)" R"(
buffer_load_dwordx4 v3, s[20:23], s55 offen offset:2048 sc1 lds
Mp 208,40,56
Rb 16,1,40960
Cv 37,37,27
Mp 216,40,60
Rb 20,1,41984
v_add_f32_e32 v15, v15, v38
Mp 220,44,60
Rb 24,1,43008
v_add_f32_e32 v15, v15, v28
Mp 212,44,56
Rb 28,1,44032
Cv 38,38,28
Mp 224,48,56
Rb 32,1,36864
Rt 182,9,24704
Mp 232,48,60
Rb 36,1,37888
v_exp_f32_e32 v29, v79
Mp 236,52,60
Rb 40,1,38912
v_add_f32_e32 v15, v15, v39
Mp 228,52,56
Rb 44,1,39936
buffer_load_dwordx4 v3, s[20:23], s55 offen offset:3072 sc1 lds
Mp 240,56,56
v_add_f32_e32 v15, v15, v29
Cv 39,39,29
Mp 248,56,60
buffer_store_dwordx4 a[148:151], v3, s[20:23], s48 offen offset:1024 sc0 sc1
Mp 252,60,60
s_sub_u32 s57, s57, 1
s_waitcnt lgkmcnt(9)
Mp 244,60,56
v_mfma_f32_16x16x32_bf16 v[96:99], a[0:3], a[64:67], v[16:19]
Rb 48,1,45056
buffer_load_dword v4, v5, s[20:23], s50 offen offset:0 sc0 sc1
v_mfma_f32_16x16x32_bf16 v[100:103], a[0:3], a[68:71], v[20:23]
v_exp_f32_e32 v40, v80
Mv 100,4,76
Rb 52,1,46080
s_add_u32 m0, s36, 65536
Rt 184,9,16640
Mv 96,4,72
buffer_load_dwordx4 v2, s[20:23], s56 offen offset:0 sc1 lds
Mv 96,8,80
Rb 56,1,47104
v_add_f32_e32 v14, v14, v40
Mv 100,8,84
v_exp_f32_e32 v24, v81
Mv 100,12,92
Rb 60,1,48128
s_waitcnt lgkmcnt(10)
Mv 96,12,88
buffer_store_dwordx4 a[176:179], v1, s[20:23], s8 offen sc0 sc1
v_add_f32_e32 v14, v14, v24
Mv 96,16,96
Rb 0,1,49152
Cv 40,40,24
Mv 100,16,100
v_exp_f32_e32 v41, v82
Mv 100,20,108
Rb 4,1,50176
v_add_f32_e32 v14, v14, v41
)" R"(
Mv 96,20,104
buffer_load_dwordx4 v2, s[20:23], s56 offen offset:1024 sc1 lds
Mv 96,24,112
Rb 8,1,51200
Rt 186,9,24832
Mv 100,24,116
v_exp_f32_e32 v25, v83
Mv 100,28,124
Rb 12,1,52224
s_waitcnt lgkmcnt(10)
Mv 96,28,120
v_exp_f32_e32 v42, v88
v_add_f32_e32 v14, v14, v25
v_mfma_f32_16x16x32_bf16 v[104:107], a[32:35], a[64:67], v[16:19]
Rb 16,1,57344
Cv 41,41,25
v_mfma_f32_16x16x32_bf16 v[108:111], a[32:35], a[68:71], v[20:23]
v_exp_f32_e32 v26, v89
Mv 108,36,76
Rb 20,1,58368
v_add_f32_e32 v14, v14, v42
Mv 104,36,72
buffer_load_dwordx4 v2, s[20:23], s56 offen offset:2048 sc1 lds
Mv 104,40,80
Rb 24,1,59392
v_add_f32_e32 v14, v14, v26
Mv 108,40,84
buffer_store_dwordx4 a[152:155], v3, s[20:23], s48 offen offset:2048 sc0 sc1
Mv 108,44,92
Rb 28,1,60416
Rt 188,9,16768
s_waitcnt lgkmcnt(10)
Mv 104,44,88
v_exp_f32_e32 v43, v90
Cv 42,42,26
Mv 104,48,96
Rb 32,1,53248
v_add_f32_e32 v14, v14, v43
Mv 108,48,100
v_exp_f32_e32 v27, v91
Mv 108,52,108
Rb 36,1,54272
v_add_f32_e32 v14, v14, v27
Mv 104,52,104
buffer_load_dwordx4 v2, s[20:23], s56 offen offset:3072 sc1 lds
Mv 104,56,112
Rb 40,1,55296
Cv 43,43,27
Mv 108,56,116
buffer_store_dwordx4 a[180:183], v1, s[20:23], s9 offen sc0 sc1
Mv 108,60,124
Rb 44,1,56320
s_waitcnt lgkmcnt(9)
Mv 104,60,120
v_exp_f32_e32 v44, v84
s_add_u32 s55, s55, 0x10000
v_mfma_f32_16x16x32_bf16 v[112:115], a[0:3], a[64:67], v[16:19]
Rb 48,1,61440
Rt 190,9,24960
v_mfma_f32_16x16x32_bf16 v[116:119], a[0:3], a[68:71], v[20:23]
v_exp_f32_e32 v28, v85
Mv 116,4,76
Rb 52,1,62464
s_add_u32 m0, s36, 69632
)" R"(
Mv 112,4,72
buffer_load_dwordx4 v3, s[20:23], s56 offen offset:0 sc1 lds
Mv 112,8,80
Rb 56,1,63488
v_add_f32_e32 v15, v15, v44
Mv 116,8,84
v_exp_f32_e32 v45, v86
Mv 116,12,92
Rb 60,1,64512
s_waitcnt lgkmcnt(10)
Mv 112,12,88
v_exp_f32_e32 v29, v87
v_add_f32_e32 v15, v15, v28
Mv 112,16,96
v_exp_f32_e32 v46, v92
Mv 116,16,100
v_exp_f32_e32 v24, v93
Mv 116,20,108
v_exp_f32_e32 v47, v94
Mv 112,20,104
buffer_load_dwordx4 v3, s[20:23], s56 offen offset:1024 sc1 lds
Mv 112,24,112
v_exp_f32_e32 v25, v95
Mv 116,24,116
buffer_store_dwordx4 a[156:159], v3, s[20:23], s48 offen offset:3072 sc0 sc1
Mv 116,28,124
s_cmp_eq_u32 s57, 2
s_cselect_b32 s55, s59, s55
Mv 112,28,120
Cv 44,44,28
s_waitcnt vmcnt(25) lgkmcnt(0)
s_barrier
v_mfma_f32_16x16x32_bf16 v[120:123], a[32:35], a[64:67], v[16:19]
Rb 0,8,32768
v_add_f32_e32 v15, v15, v45
v_mfma_f32_16x16x32_bf16 v[124:127], a[32:35], a[68:71], v[20:23]
Rb 4,8,33792
v_add_f32_e32 v15, v15, v29
Mv 124,36,76
Rb 8,8,34816
Cv 45,45,29
Mv 120,36,72
Rb 12,8,35840
buffer_load_dwordx4 v3, s[20:23], s56 offen offset:2048 sc1 lds
Mv 120,40,80
Rb 16,8,36864
v_add_f32_e32 v15, v15, v46
Mv 124,40,84
Rb 20,8,37888
v_add_f32_e32 v15, v15, v24
Mv 124,44,92
Rb 24,8,38912
Cv 46,46,24
Mv 120,44,88
Rb 28,8,39936
buffer_store_dwordx4 a[184:187], v1, s[20:23], s10 offen sc0 sc1
Mv 120,48,96
Rb 32,8,40960
v_add_f32_e32 v15, v15, v47
Mv 124,48,100
Rb 36,8,41984
v_add_f32_e32 v15, v15, v25
Mv 124,52,108
Rb 40,8,43008
Cv 47,47,25
)" R"(
Mv 120,52,104
Rb 44,8,44032
buffer_load_dwordx4 v3, s[20:23], s56 offen offset:3072 sc1 lds
Mv 120,56,112
v_max_f32_e32 v11, v14, v15
v_cmp_nge_f32_e32 vcc, s62, v11
Mv 124,56,116
s_add_u32 s56, s56, 0x10000
Mv 124,60,124
s_waitcnt lgkmcnt(8)
s_cbranch_vccnz .Lzfx10
.Lzfr10:
Mv 120,60,120
v_add_f32_e32 v12, v12, v14
v_add_f32_e32 v13, v13, v15
Mp 128,0,32
Rb 48,8,45056
Mp 136,0,36
s_cmp_le_i32 s57, s58
s_cbranch_scc1 .Lzmk9
.Lzmr9:
Mp 140,4,36
Rb 52,8,46080
s_add_u32 m0, s36, 32768
Mp 132,4,32
buffer_load_dwordx4 v2, s[20:23], s55 offen offset:0 sc1 lds
Mp 144,8,32
Rb 56,8,47104
Mp 152,8,36
buffer_store_dwordx4 a[188:191], v1, s[20:23], s11 offen sc0 sc1
Mp 156,12,36
Rb 60,8,48128
s_waitcnt lgkmcnt(8)
Mp 148,12,32
v_exp_f32_e32 v48, v96
Mp 160,16,32
Rb 0,8,49152
Mp 168,16,36
v_exp_f32_e32 v26, v97
Mp 172,20,36
Rb 4,8,50176
v_add_f32_e32 v14, v48, v26
Mp 164,20,32
buffer_load_dwordx4 v2, s[20:23], s55 offen offset:1024 sc1 lds
Mp 176,24,32
Rb 8,8,51200
Cv 48,48,26
Mp 184,24,36
v_exp_f32_e32 v49, v98
Mp 188,28,36
Rb 12,8,52224
s_waitcnt lgkmcnt(8)
Mp 180,28,32
v_exp_f32_e32 v27, v99
Mp 192,32,32
Rb 16,8,53248
v_add_f32_e32 v14, v14, v49
Mp 200,32,36
v_exp_f32_e32 v50, v104
Mp 204,36,36
Rb 20,8,54272
v_add_f32_e32 v14, v14, v27
Mp 196,36,32
buffer_load_dwordx4 v2, s[20:23], s55 offen offset:2048 sc1 lds
)" R"(
Mp 208,40,32
Rb 24,8,55296
Cv 49,49,27
Mp 216,40,36
v_exp_f32_e32 v28, v105
Mp 220,44,36
Rb 28,8,56320
s_waitcnt lgkmcnt(8)
Mp 212,44,32
v_exp_f32_e32 v51, v106
Mp 224,48,32
Rb 32,8,57344
v_add_f32_e32 v14, v14, v50
Mp 232,48,36
v_exp_f32_e32 v29, v107
Mp 236,52,36
Rb 36,8,58368
v_add_f32_e32 v14, v14, v28
Mp 228,52,32
buffer_load_dwordx4 v2, s[20:23], s55 offen offset:3072 sc1 lds
Mp 240,56,32
Rb 40,8,59392
Cv 50,50,28
Mp 248,56,36
v_exp_f32_e32 v52, v100
Mp 252,60,36
Rb 44,8,60416
s_waitcnt lgkmcnt(8)
Mp 244,60,32
v_exp_f32_e32 v24, v101
Mp 128,0,40
Rb 48,8,61440
v_add_f32_e32 v14, v14, v51
Mp 136,0,44
v_exp_f32_e32 v53, v102
Mp 140,4,44
Rb 52,8,62464
s_add_u32 m0, s36, 36864
Mp 132,4,40
buffer_load_dwordx4 v3, s[20:23], s55 offen offset:0 sc1 lds
Mp 144,8,40
Rb 56,8,63488
v_add_f32_e32 v14, v14, v29
Mp 152,8,44
v_exp_f32_e32 v25, v103
Mp 156,12,44
Rb 60,8,64512
s_waitcnt lgkmcnt(8)
Mp 148,12,40
v_exp_f32_e32 v54, v108
Mp 160,16,40
v_exp_f32_e32 v26, v109
Mp 168,16,44
v_exp_f32_e32 v55, v110
Mp 172,20,44
v_exp_f32_e32 v27, v111
Mp 164,20,40
buffer_load_dwordx4 v3, s[20:23], s55 offen offset:1024 sc1 lds
Mp 176,24,40
Cv 51,51,29
v_add_f32_e32 v15, v52, v24
Mp 184,24,44
Cv 52,52,24
v_add_f32_e32 v15, v15, v53
)" R"(
Mp 188,28,44
v_add_f32_e32 v15, v15, v25
Cv 53,53,25
Mp 180,28,40
s_waitcnt vmcnt(5) lgkmcnt(0)
s_barrier
buffer_store_dword v6, v7, s[20:23], s49 offen offset:3072 sc0 sc1
Mp 192,32,40
Rb 0,1,0
v_add_f32_e32 v15, v15, v54
Mp 200,32,44
Rb 4,1,1024
v_add_f32_e32 v15, v15, v26
Mp 204,36,44
Rb 8,1,2048
Cv 54,54,26
Mp 196,36,40
Rb 12,1,3072
buffer_load_dwordx4 v3, s[20:23], s55 offen offset:2048 sc1 lds
Mp 208,40,40
Rb 16,1,8192
v_add_f32_e32 v15, v15, v55
Mp 216,40,44
Rb 20,1,9216
v_add_f32_e32 v15, v15, v27
Mp 220,44,44
Rb 24,1,10240
Cv 55,55,27
.LzH13:
s_cbranch_execz .LzHR
Mp 212,44,40
Rb 28,1,11264
s_sub_u32 s57, s57, 1
Mp 224,48,40
Rb 32,1,4096
Mp 232,48,44
Rb 36,1,5120
Mp 236,52,44
Rb 40,1,6144
Mp 228,52,40
Rb 44,1,7168
buffer_load_dwordx4 v3, s[20:23], s55 offen offset:3072 sc1 lds
Mp 240,56,40
v_cmp_eq_u32_e32 vcc, s3, v4
s_andn2_b64 s[28:29], -1, vcc
s_cbranch_scc1 .Lzsp20
.Lzok20:
Mp 248,56,44
Mp 252,60,44
s_waitcnt lgkmcnt(8)
Mp 244,60,40
.LzH14:
s_cbranch_execz .LzHR
.LzL:
v_mfma_f32_16x16x32_bf16 v[64:67], a[0:3], a[64:67], v[16:19]
Rb 48,1,12288
v_mfma_f32_16x16x32_bf16 v[68:71], a[0:3], a[68:71], v[20:23]
v_exp_f32_e32 v56, v112
Mv 68,4,76
Rb 52,1,13312
s_add_u32 m0, s36, 98304
Mv 64,4,72
buffer_load_dwordx4 v2, s[20:23], s56 offen offset:0 sc1 lds
Mv 64,8,80
Rb 56,1,14336
)" R"(
v_add_f32_e32 v14, v14, v56
Mv 68,8,84
v_exp_f32_e32 v24, v113
Mv 68,12,92
Rb 60,1,15360
s_waitcnt lgkmcnt(8)
Mv 64,12,88
v_exp_f32_e32 v57, v114
Mv 64,16,96
Rb 0,1,16384
v_add_f32_e32 v14, v14, v24
Mv 68,16,100
v_exp_f32_e32 v25, v115
Mv 68,20,108
Rb 4,1,17408
Cv 56,56,24
Mv 64,20,104
buffer_load_dwordx4 v2, s[20:23], s56 offen offset:1024 sc1 lds
Mv 64,24,112
Rb 8,1,18432
v_add_f32_e32 v14, v14, v57
Mv 68,24,116
v_exp_f32_e32 v58, v120
Mv 68,28,124
Rb 12,1,19456
s_waitcnt lgkmcnt(8)
Mv 64,28,120
v_exp_f32_e32 v26, v121
v_mfma_f32_16x16x32_bf16 v[72:75], a[32:35], a[64:67], v[16:19]
Rb 16,1,24576
v_add_f32_e32 v14, v14, v25
v_mfma_f32_16x16x32_bf16 v[76:79], a[32:35], a[68:71], v[20:23]
v_exp_f32_e32 v59, v122
Mv 76,36,76
Rb 20,1,25600
Cv 57,57,25
Mv 72,36,72
buffer_load_dwordx4 v2, s[20:23], s56 offen offset:2048 sc1 lds
Mv 72,40,80
Rb 24,1,26624
v_add_f32_e32 v14, v14, v58
Mv 76,40,84
v_exp_f32_e32 v27, v123
Mv 76,44,92
Rb 28,1,27648
s_waitcnt lgkmcnt(8)
Mv 72,44,88
v_exp_f32_e32 v60, v116
Mv 72,48,96
Rb 32,1,20480
v_add_f32_e32 v14, v14, v26
Mv 76,48,100
v_exp_f32_e32 v28, v117
Mv 76,52,108
Rb 36,1,21504
Cv 58,58,26
Mv 72,52,104
buffer_load_dwordx4 v2, s[20:23], s56 offen offset:3072 sc1 lds
Mv 72,56,112
Rb 40,1,22528
v_add_f32_e32 v14, v14, v59
Mv 76,56,116
v_exp_f32_e32 v61, v118
Mv 76,60,124
)" R"(
Rb 44,1,23552
s_waitcnt lgkmcnt(8)
Mv 72,60,120
v_exp_f32_e32 v29, v119
v_mfma_f32_16x16x32_bf16 v[80:83], a[0:3], a[64:67], v[16:19]
Rb 48,1,28672
v_add_f32_e32 v14, v14, v27
v_mfma_f32_16x16x32_bf16 v[84:87], a[0:3], a[68:71], v[20:23]
v_exp_f32_e32 v62, v124
Mv 84,4,76
Rb 52,1,29696
s_add_u32 m0, s36, 102400
Mv 80,4,72
buffer_load_dwordx4 v3, s[20:23], s56 offen offset:0 sc1 lds
Mv 80,8,80
Rb 56,1,30720
Cv 59,59,27
Mv 84,8,84
v_exp_f32_e32 v24, v125
Mv 84,12,92
Rb 60,1,31744
s_waitcnt lgkmcnt(8)
Mv 80,12,88
v_exp_f32_e32 v63, v126
Mv 80,16,96
v_exp_f32_e32 v25, v127
Mv 84,16,100
v_add_f32_e32 v15, v15, v60
v_add_f32_e32 v15, v15, v28
Mv 84,20,108
Cv 60,60,28
v_add_f32_e32 v15, v15, v61
Mv 80,20,104
buffer_load_dwordx4 v3, s[20:23], s56 offen offset:1024 sc1 lds
Mv 80,24,112
v_add_f32_e32 v15, v15, v29
Cv 61,61,29
Mv 84,24,116
v_add_f32_e32 v15, v15, v62
v_add_f32_e32 v15, v15, v24
Mv 84,28,124
Cv 62,62,24
v_add_f32_e32 v15, v15, v63
Mv 80,28,120
s_waitcnt vmcnt(14) lgkmcnt(0)
s_barrier
v_mfma_f32_16x16x32_bf16 v[88:91], a[32:35], a[64:67], v[16:19]
Rb 0,8,0
v_add_f32_e32 v15, v15, v25
v_mfma_f32_16x16x32_bf16 v[92:95], a[32:35], a[68:71], v[20:23]
Rb 4,8,1024
Cv 63,63,25
Mv 92,36,76
Rb 8,8,2048
v_max_f32_e32 v11, v14, v15
Mv 88,36,72
Rb 12,8,3072
buffer_load_dwordx4 v3, s[20:23], s56 offen offset:2048 sc1 lds
Mv 88,40,80
Rb 16,8,4096
v_cmp_nge_f32_e32 vcc, s62, v11
Mv 92,40,84
Rb 20,8,5120
s_add_u32 s55, s55, 0x10000
Mv 92,44,92
)" R"(
Rb 24,8,6144
Mv 88,44,88
Rb 28,8,7168
Mv 88,48,96
Rb 32,8,8192
Mv 92,48,100
Rb 36,8,9216
Mv 92,52,108
Rb 40,8,10240
Mv 88,52,104
Rb 44,8,11264
buffer_load_dwordx4 v3, s[20:23], s56 offen offset:3072 sc1 lds
Mv 88,56,112
s_cmp_eq_u32 s57, 2
s_cselect_b32 s55, s59, s55
Mv 92,56,116
s_add_u32 s56, s56, 0x10000
Mv 92,60,124
s_waitcnt lgkmcnt(8)
s_cbranch_vccnz .Lzfx1
.Lzfr1:
Mv 88,60,120
v_add_f32_e32 v12, v12, v14
v_add_f32_e32 v13, v13, v15
Mp 128,0,48
Rb 48,8,12288
Mp 136,0,52
s_cmp_le_i32 s57, s58
s_cbranch_scc1 .Lzmk2
.Lzmr2:
Mp 140,4,52
Rb 52,8,13312
s_add_u32 m0, s36, 0
Mp 132,4,48
buffer_load_dwordx4 v2, s[20:23], s55 offen offset:0 sc1 lds
Mp 144,8,48
Rb 56,8,14336
Mp 152,8,52
v_exp_f32_e32 v32, v64
Mp 156,12,52
Rb 60,8,15360
s_waitcnt lgkmcnt(8)
Mp 148,12,48
v_exp_f32_e32 v26, v65
Mp 160,16,48
Rb 0,8,16384
v_add_f32_e32 v14, v32, v26
Mp 168,16,52
v_exp_f32_e32 v33, v66
Mp 172,20,52
Rb 4,8,17408
Cv 32,32,26
Mp 164,20,48
buffer_load_dwordx4 v2, s[20:23], s55 offen offset:1024 sc1 lds
Mp 176,24,48
Rb 8,8,18432
v_add_f32_e32 v14, v14, v33
Mp 184,24,52
v_exp_f32_e32 v27, v67
Mp 188,28,52
Rb 12,8,19456
s_waitcnt lgkmcnt(8)
Mp 180,28,48
v_exp_f32_e32 v34, v72
)" R"(
Mp 192,32,48
Rb 16,8,20480
v_add_f32_e32 v14, v14, v27
Mp 200,32,52
v_exp_f32_e32 v28, v73
Mp 204,36,52
Rb 20,8,21504
Cv 33,33,27
Mp 196,36,48
buffer_load_dwordx4 v2, s[20:23], s55 offen offset:2048 sc1 lds
Mp 208,40,48
Rb 24,8,22528
v_add_f32_e32 v14, v14, v34
Mp 216,40,52
v_exp_f32_e32 v35, v74
Mp 220,44,52
Rb 28,8,23552
s_waitcnt lgkmcnt(8)
Mp 212,44,48
v_exp_f32_e32 v29, v75
Mp 224,48,48
Rb 32,8,24576
v_add_f32_e32 v14, v14, v28
Mp 232,48,52
v_exp_f32_e32 v36, v68
Mp 236,52,52
Rb 36,8,25600
Cv 34,34,28
Mp 228,52,48
buffer_load_dwordx4 v2, s[20:23], s55 offen offset:3072 sc1 lds
Mp 240,56,48
Rb 40,8,26624
v_add_f32_e32 v14, v14, v35
Mp 248,56,52
v_exp_f32_e32 v24, v69
Mp 252,60,52
Rb 44,8,27648
s_waitcnt lgkmcnt(8)
Mp 244,60,48
v_exp_f32_e32 v37, v70
Mp 128,0,56
Rb 48,8,28672
v_add_f32_e32 v14, v14, v29
Mp 136,0,60
v_exp_f32_e32 v25, v71
Mp 140,4,60
Rb 52,8,29696
s_add_u32 m0, s36, 4096
Mp 132,4,56
buffer_load_dwordx4 v3, s[20:23], s55 offen offset:0 sc1 lds
Mp 144,8,56
Rb 56,8,30720
Cv 35,35,29
Mp 152,8,60
v_exp_f32_e32 v38, v76
Mp 156,12,60
Rb 60,8,31744
s_waitcnt lgkmcnt(8)
Mp 148,12,56
v_exp_f32_e32 v26, v77
Mp 160,16,56
v_exp_f32_e32 v39, v78
Mp 168,16,60
v_exp_f32_e32 v27, v79
)" R"(
Mp 172,20,60
v_add_f32_e32 v15, v36, v24
Cv 36,36,24
Mp 164,20,56
buffer_load_dwordx4 v3, s[20:23], s55 offen offset:1024 sc1 lds
Mp 176,24,56
s_add_u32 s50, s50, 1024
s_min_u32 s50, s50, s60
Mp 184,24,60
v_add_f32_e32 v15, v15, v37
v_add_f32_e32 v15, v15, v25
Mp 188,28,60
Cv 37,37,25
v_add_f32_e32 v15, v15, v38
Mp 180,28,56
s_waitcnt vmcnt(14) lgkmcnt(0)
s_barrier
Mp 192,32,56
Rb 0,1,32768
v_add_f32_e32 v15, v15, v26
Mp 200,32,60
Rb 4,1,33792
Cv 38,38,26
Mp 204,36,60
Rb 8,1,34816
v_add_f32_e32 v15, v15, v39
Mp 196,36,56
Rb 12,1,35840
buffer_load_dwordx4 v3, s[20:23], s55 offen offset:2048 sc1 lds
Mp 208,40,56
Rb 16,1,40960
v_add_f32_e32 v15, v15, v27
Mp 216,40,60
Rb 20,1,41984
Cv 39,39,27
Mp 220,44,60
Rb 24,1,43008
s_sub_u32 s57, s57, 1
Mp 212,44,56
Rb 28,1,44032
Mp 224,48,56
Rb 32,1,36864
Mp 232,48,60
Rb 36,1,37888
Mp 236,52,60
Rb 40,1,38912
Mp 228,52,56
Rb 44,1,39936
buffer_load_dwordx4 v3, s[20:23], s55 offen offset:3072 sc1 lds
Mp 240,56,56
Mp 248,56,60
Mp 252,60,60
s_waitcnt lgkmcnt(8)
Mp 244,60,56
v_mfma_f32_16x16x32_bf16 v[96:99], a[0:3], a[64:67], v[16:19]
Rb 48,1,45056
buffer_load_dword v4, v5, s[20:23], s50 offen offset:0 sc0 sc1
v_mfma_f32_16x16x32_bf16 v[100:103], a[0:3], a[68:71], v[20:23]
v_exp_f32_e32 v40, v80
Mv 100,4,76
Rb 52,1,46080
s_add_u32 m0, s36, 65536
Mv 96,4,72
buffer_load_dwordx4 v2, s[20:23], s56 offen offset:0 sc1 lds
Mv 96,8,80
)" R"(
Rb 56,1,47104
v_add_f32_e32 v14, v14, v40
Mv 100,8,84
v_exp_f32_e32 v28, v81
Mv 100,12,92
Rb 60,1,48128
s_waitcnt lgkmcnt(8)
Mv 96,12,88
v_exp_f32_e32 v41, v82
Mv 96,16,96
Rb 0,1,49152
v_add_f32_e32 v14, v14, v28
Mv 100,16,100
v_exp_f32_e32 v29, v83
Mv 100,20,108
Rb 4,1,50176
Cv 40,40,28
Mv 96,20,104
buffer_load_dwordx4 v2, s[20:23], s56 offen offset:1024 sc1 lds
Mv 96,24,112
Rb 8,1,51200
v_add_f32_e32 v14, v14, v41
Mv 100,24,116
v_exp_f32_e32 v42, v88
Mv 100,28,124
Rb 12,1,52224
s_waitcnt lgkmcnt(8)
Mv 96,28,120
v_exp_f32_e32 v24, v89
v_mfma_f32_16x16x32_bf16 v[104:107], a[32:35], a[64:67], v[16:19]
Rb 16,1,57344
v_add_f32_e32 v14, v14, v29
v_mfma_f32_16x16x32_bf16 v[108:111], a[32:35], a[68:71], v[20:23]
v_exp_f32_e32 v43, v90
Mv 108,36,76
Rb 20,1,58368
Cv 41,41,29
Mv 104,36,72
buffer_load_dwordx4 v2, s[20:23], s56 offen offset:2048 sc1 lds
Mv 104,40,80
Rb 24,1,59392
v_add_f32_e32 v14, v14, v42
Mv 108,40,84
v_exp_f32_e32 v25, v91
Mv 108,44,92
Rb 28,1,60416
s_waitcnt lgkmcnt(8)
Mv 104,44,88
v_exp_f32_e32 v44, v84
Mv 104,48,96
Rb 32,1,53248
v_add_f32_e32 v14, v14, v24
Mv 108,48,100
v_exp_f32_e32 v26, v85
Mv 108,52,108
Rb 36,1,54272
Cv 42,42,24
Mv 104,52,104
buffer_load_dwordx4 v2, s[20:23], s56 offen offset:3072 sc1 lds
Mv 104,56,112
Rb 40,1,55296
v_add_f32_e32 v14, v14, v43
Mv 108,56,116
v_exp_f32_e32 v45, v86
)" R"(
Mv 108,60,124
Rb 44,1,56320
s_waitcnt lgkmcnt(8)
Mv 104,60,120
v_exp_f32_e32 v27, v87
v_mfma_f32_16x16x32_bf16 v[112:115], a[0:3], a[64:67], v[16:19]
Rb 48,1,61440
v_add_f32_e32 v14, v14, v25
v_mfma_f32_16x16x32_bf16 v[116:119], a[0:3], a[68:71], v[20:23]
v_exp_f32_e32 v46, v92
Mv 116,4,76
Rb 52,1,62464
s_add_u32 m0, s36, 69632
Mv 112,4,72
buffer_load_dwordx4 v3, s[20:23], s56 offen offset:0 sc1 lds
Mv 112,8,80
Rb 56,1,63488
Cv 43,43,25
Mv 116,8,84
v_exp_f32_e32 v28, v93
Mv 116,12,92
Rb 60,1,64512
s_waitcnt lgkmcnt(8)
Mv 112,12,88
v_exp_f32_e32 v47, v94
Mv 112,16,96
v_exp_f32_e32 v29, v95
Mv 116,16,100
v_add_f32_e32 v15, v15, v44
v_add_f32_e32 v15, v15, v26
Mv 116,20,108
Cv 44,44,26
v_add_f32_e32 v15, v15, v45
Mv 112,20,104
buffer_load_dwordx4 v3, s[20:23], s56 offen offset:1024 sc1 lds
Mv 112,24,112
v_add_f32_e32 v15, v15, v27
Cv 45,45,27
Mv 116,24,116
v_add_f32_e32 v15, v15, v46
v_add_f32_e32 v15, v15, v28
Mv 116,28,124
Cv 46,46,28
v_add_f32_e32 v15, v15, v47
Mv 112,28,120
s_waitcnt vmcnt(15) lgkmcnt(0)
s_barrier
v_mfma_f32_16x16x32_bf16 v[120:123], a[32:35], a[64:67], v[16:19]
Rb 0,8,32768
v_add_f32_e32 v15, v15, v29
v_mfma_f32_16x16x32_bf16 v[124:127], a[32:35], a[68:71], v[20:23]
Rb 4,8,33792
Cv 47,47,29
Mv 124,36,76
Rb 8,8,34816
v_max_f32_e32 v11, v14, v15
Mv 120,36,72
Rb 12,8,35840
buffer_load_dwordx4 v3, s[20:23], s56 offen offset:2048 sc1 lds
Mv 120,40,80
Rb 16,8,36864
v_cmp_nge_f32_e32 vcc, s62, v11
Mv 124,40,84
Rb 20,8,37888
s_add_u32 s55, s55, 0x10000
)" R"(
Mv 124,44,92
Rb 24,8,38912
Mv 120,44,88
Rb 28,8,39936
Mv 120,48,96
Rb 32,8,40960
Mv 124,48,100
Rb 36,8,41984
Mv 124,52,108
Rb 40,8,43008
Mv 120,52,104
Rb 44,8,44032
buffer_load_dwordx4 v3, s[20:23], s56 offen offset:3072 sc1 lds
Mv 120,56,112
s_cmp_eq_u32 s57, 2
s_cselect_b32 s55, s59, s55
Mv 124,56,116
s_add_u32 s56, s56, 0x10000
Mv 124,60,124
s_waitcnt lgkmcnt(8)
s_cbranch_vccnz .Lzfx2
.Lzfr2:
Mv 120,60,120
v_add_f32_e32 v12, v12, v14
v_add_f32_e32 v13, v13, v15
Mp 128,0,32
Rb 48,8,45056
Mp 136,0,36
s_cmp_le_i32 s57, s58
s_cbranch_scc1 .Lzmk3
.Lzmr3:
Mp 140,4,36
Rb 52,8,46080
s_add_u32 m0, s36, 32768
Mp 132,4,32
buffer_load_dwordx4 v2, s[20:23], s55 offen offset:0 sc1 lds
Mp 144,8,32
Rb 56,8,47104
Mp 152,8,36
v_exp_f32_e32 v48, v96
Mp 156,12,36
Rb 60,8,48128
s_waitcnt lgkmcnt(8)
Mp 148,12,32
v_exp_f32_e32 v24, v97
Mp 160,16,32
Rb 0,8,49152
v_add_f32_e32 v14, v48, v24
Mp 168,16,36
v_exp_f32_e32 v49, v98
Mp 172,20,36
Rb 4,8,50176
Cv 48,48,24
Mp 164,20,32
buffer_load_dwordx4 v2, s[20:23], s55 offen offset:1024 sc1 lds
Mp 176,24,32
Rb 8,8,51200
v_add_f32_e32 v14, v14, v49
Mp 184,24,36
v_exp_f32_e32 v25, v99
Mp 188,28,36
Rb 12,8,52224
s_waitcnt lgkmcnt(8)
Mp 180,28,32
)" R"(
v_exp_f32_e32 v50, v104
Mp 192,32,32
Rb 16,8,53248
v_add_f32_e32 v14, v14, v25
Mp 200,32,36
v_exp_f32_e32 v26, v105
Mp 204,36,36
Rb 20,8,54272
Cv 49,49,25
Mp 196,36,32
buffer_load_dwordx4 v2, s[20:23], s55 offen offset:2048 sc1 lds
Mp 208,40,32
Rb 24,8,55296
v_add_f32_e32 v14, v14, v50
Mp 216,40,36
v_exp_f32_e32 v51, v106
Mp 220,44,36
Rb 28,8,56320
s_waitcnt lgkmcnt(8)
Mp 212,44,32
v_exp_f32_e32 v27, v107
Mp 224,48,32
Rb 32,8,57344
v_add_f32_e32 v14, v14, v26
Mp 232,48,36
v_exp_f32_e32 v52, v100
Mp 236,52,36
Rb 36,8,58368
Cv 50,50,26
Mp 228,52,32
buffer_load_dwordx4 v2, s[20:23], s55 offen offset:3072 sc1 lds
Mp 240,56,32
Rb 40,8,59392
v_add_f32_e32 v14, v14, v51
Mp 248,56,36
v_exp_f32_e32 v28, v101
Mp 252,60,36
Rb 44,8,60416
s_waitcnt lgkmcnt(8)
Mp 244,60,32
v_exp_f32_e32 v53, v102
Mp 128,0,40
Rb 48,8,61440
v_add_f32_e32 v14, v14, v27
Mp 136,0,44
v_exp_f32_e32 v29, v103
Mp 140,4,44
Rb 52,8,62464
s_add_u32 m0, s36, 36864
Mp 132,4,40
buffer_load_dwordx4 v3, s[20:23], s55 offen offset:0 sc1 lds
.LzH15:
s_cbranch_execz .LzHR
Mp 144,8,40
Rb 56,8,63488
Cv 51,51,27
Mp 152,8,44
v_exp_f32_e32 v54, v108
Mp 156,12,44
Rb 60,8,64512
s_waitcnt lgkmcnt(8)
Mp 148,12,40
v_exp_f32_e32 v24, v109
Mp 160,16,40
)" R"(
v_exp_f32_e32 v55, v110
Mp 168,16,44
v_exp_f32_e32 v25, v111
Mp 172,20,44
v_add_f32_e32 v15, v52, v28
Cv 52,52,28
Mp 164,20,40
buffer_load_dwordx4 v3, s[20:23], s55 offen offset:1024 sc1 lds
Mp 176,24,40
v_add_f32_e32 v15, v15, v53
v_add_f32_e32 v15, v15, v29
Mp 184,24,44
Cv 53,53,29
v_add_f32_e32 v15, v15, v54
Mp 188,28,44
v_add_f32_e32 v15, v15, v24
Cv 54,54,24
Mp 180,28,40
s_waitcnt vmcnt(14) lgkmcnt(0)
s_barrier
Mp 192,32,40
Rb 0,1,0
v_add_f32_e32 v15, v15, v55
Mp 200,32,44
Rb 4,1,1024
v_add_f32_e32 v15, v15, v25
Mp 204,36,44
Rb 8,1,2048
Cv 55,55,25
Mp 196,36,40
Rb 12,1,3072
buffer_load_dwordx4 v3, s[20:23], s55 offen offset:2048 sc1 lds
Mp 208,40,40
Rb 16,1,8192
s_sub_u32 s57, s57, 1
Mp 216,40,44
Rb 20,1,9216
Mp 220,44,44
Rb 24,1,10240
Mp 212,44,40
Rb 28,1,11264
Mp 224,48,40
Rb 32,1,4096
Mp 232,48,44
Rb 36,1,5120
Mp 236,52,44
Rb 40,1,6144
Mp 228,52,40
Rb 44,1,7168
buffer_load_dwordx4 v3, s[20:23], s55 offen offset:3072 sc1 lds
Mp 240,56,40
v_cmp_eq_u32_e32 vcc, s3, v4
s_andn2_b64 s[28:29], -1, vcc
s_cbranch_scc1 .Lzsp9
.Lzok9:
Mp 248,56,44
Mp 252,60,44
s_waitcnt lgkmcnt(8)
Mp 244,60,40
s_cmp_gt_i32 s57, s93
s_cbranch_scc1 .LzL
s_cmp_eq_u32 s94, 1
s_cbranch_scc1 .LzW
s_cmp_eq_u32 s94, 2
s_cbranch_scc1 .LzDA
)" R"(
.LzEB:
v_mfma_f32_16x16x32_bf16 v[64:67], a[0:3], a[64:67], v[16:19]
Rb 48,1,12288
s_mov_b32 s83, 0xc108000
buffer_load_dword v4, v7, s[20:23], s83 offen offset:4 sc0 sc1
v_mfma_f32_16x16x32_bf16 v[68:71], a[0:3], a[68:71], v[20:23]
s_cmp_lg_u32 s35, 7
s_cselect_b64 s[60:61], 1, 0
Mv 68,4,76
Rb 52,1,13312
s_add_u32 m0, s36, 98304
Mv 64,4,72
buffer_load_dwordx4 v2, s[20:23], s56 offen offset:0 sc1 lds
Mv 64,8,80
Rb 56,1,14336
Mv 68,8,84
v_exp_f32_e32 v56, v112
Mv 68,12,92
Rb 60,1,15360
s_waitcnt lgkmcnt(8)
Mv 64,12,88
v_exp_f32_e32 v28, v113
Mv 64,16,96
Rb 0,1,16384
v_add_f32_e32 v14, v14, v56
Mv 68,16,100
v_exp_f32_e32 v57, v114
Mv 68,20,108
Rb 4,1,17408
v_add_f32_e32 v14, v14, v28
Mv 64,20,104
buffer_load_dwordx4 v2, s[20:23], s56 offen offset:1024 sc1 lds
Mv 64,24,112
Rb 8,1,18432
Cv 56,56,28
Mv 68,24,116
v_exp_f32_e32 v29, v115
Mv 68,28,124
Rb 12,1,19456
s_waitcnt lgkmcnt(8)
Mv 64,28,120
v_exp_f32_e32 v58, v120
v_mfma_f32_16x16x32_bf16 v[72:75], a[32:35], a[64:67], v[16:19]
Rb 16,1,24576
v_add_f32_e32 v14, v14, v57
v_mfma_f32_16x16x32_bf16 v[76:79], a[32:35], a[68:71], v[20:23]
v_exp_f32_e32 v24, v121
Mv 76,36,76
Rb 20,1,25600
v_add_f32_e32 v14, v14, v29
Mv 72,36,72
buffer_load_dwordx4 v2, s[20:23], s56 offen offset:2048 sc1 lds
Mv 72,40,80
Rb 24,1,26624
Cv 57,57,29
Mv 76,40,84
v_exp_f32_e32 v59, v122
Mv 76,44,92
Rb 28,1,27648
s_waitcnt lgkmcnt(8)
Mv 72,44,88
v_exp_f32_e32 v25, v123
Mv 72,48,96
Rb 32,1,20480
)" R"(
v_add_f32_e32 v14, v14, v58
Mv 76,48,100
v_exp_f32_e32 v60, v116
Mv 76,52,108
Rb 36,1,21504
v_add_f32_e32 v14, v14, v24
Mv 72,52,104
buffer_load_dwordx4 v2, s[20:23], s56 offen offset:3072 sc1 lds
Mv 72,56,112
Rb 40,1,22528
Cv 58,58,24
Mv 76,56,116
v_exp_f32_e32 v26, v117
Mv 76,60,124
Rb 44,1,23552
s_waitcnt lgkmcnt(8)
Mv 72,60,120
v_exp_f32_e32 v61, v118
v_mfma_f32_16x16x32_bf16 v[80:83], a[0:3], a[64:67], v[16:19]
Rb 48,1,28672
v_add_f32_e32 v14, v14, v59
v_mfma_f32_16x16x32_bf16 v[84:87], a[0:3], a[68:71], v[20:23]
v_exp_f32_e32 v27, v119
Mv 84,4,76
Rb 52,1,29696
s_add_u32 m0, s36, 102400
Mv 80,4,72
buffer_load_dwordx4 v3, s[20:23], s56 offen offset:0 sc1 lds
Mv 80,8,80
Rb 56,1,30720
v_add_f32_e32 v14, v14, v25
Mv 84,8,84
v_exp_f32_e32 v62, v124
Mv 84,12,92
Rb 60,1,31744
s_waitcnt lgkmcnt(8)
Mv 80,12,88
v_exp_f32_e32 v28, v125
Mv 80,16,96
v_exp_f32_e32 v63, v126
Mv 84,16,100
v_exp_f32_e32 v29, v127
Mv 84,20,108
Cv 59,59,25
v_add_f32_e32 v15, v15, v60
Mv 80,20,104
buffer_load_dwordx4 v3, s[20:23], s56 offen offset:1024 sc1 lds
Mv 80,24,112
v_add_f32_e32 v15, v15, v26
Cv 60,60,26
Mv 84,24,116
v_add_f32_e32 v15, v15, v61
v_add_f32_e32 v15, v15, v27
Mv 84,28,124
Cv 61,61,27
v_add_f32_e32 v15, v15, v62
Mv 80,28,120
s_waitcnt vmcnt(15) lgkmcnt(0)
s_barrier
v_mfma_f32_16x16x32_bf16 v[88:91], a[32:35], a[64:67], v[16:19]
Rb 0,8,0
v_add_f32_e32 v15, v15, v28
v_mfma_f32_16x16x32_bf16 v[92:95], a[32:35], a[68:71], v[20:23]
Rb 4,8,1024
Cv 62,62,28
)" R"(
Mv 92,36,76
Rb 8,8,2048
v_add_f32_e32 v15, v15, v63
Mv 88,36,72
Rb 12,8,3072
buffer_load_dwordx4 v3, s[20:23], s56 offen offset:2048 sc1 lds
Mv 88,40,80
Rb 16,8,4096
v_add_f32_e32 v15, v15, v29
Mv 92,40,84
Rb 20,8,5120
Cv 63,63,29
Mv 92,44,92
Rb 24,8,6144
v_max_f32_e32 v11, v14, v15
Mv 88,44,88
Rb 28,8,7168
v_cmp_nge_f32_e32 vcc, s62, v11
Mv 88,48,96
Rb 32,8,8192
s_add_u32 s55, s55, 0x10000
Mv 92,48,100
Rb 36,8,9216
Mv 92,52,108
Rb 40,8,10240
Mv 88,52,104
Rb 44,8,11264
buffer_load_dwordx4 v3, s[20:23], s56 offen offset:3072 sc1 lds
Mv 88,56,112
s_cmp_eq_u32 s57, 2
s_cselect_b32 s55, s59, s55
Mv 92,56,116
s_add_u32 s56, s56, 0x10000
Mv 92,60,124
s_waitcnt lgkmcnt(8)
s_cbranch_vccnz .Lzfx11
.Lzfr11:
Mv 88,60,120
s_waitcnt vmcnt(8)
v_cmp_eq_u32_e32 vcc, s3, v4
s_andn2_b64 s[28:29], 1, vcc
s_cbranch_scc1 .Lzsp21
.Lzok21:
Mp 128,0,48
Rb 48,8,12288
buffer_load_dword v9, v7, s[20:23], s83 offen sc0 sc1
Mp 136,0,52
v_add_f32_e32 v12, v12, v14
s_cmp_le_i32 s57, s58
s_cbranch_scc1 .Lzmk10
.Lzmr10:
Mp 140,4,52
Rb 52,8,13312
s_add_u32 m0, s36, 0
v_add_f32_e32 v13, v13, v15
Mp 132,4,48
buffer_load_dwordx4 v2, s[20:23], s55 offen offset:0 sc1 lds
Mp 144,8,48
Rb 56,8,14336
Mp 152,8,52
v_exp_f32_e32 v32, v64
Mp 156,12,52
Rb 60,8,15360
s_waitcnt lgkmcnt(8)
)" R"(
Mp 148,12,48
v_exp_f32_e32 v24, v65
Mp 160,16,48
Rb 0,8,16384
v_add_f32_e32 v14, v32, v24
Mp 168,16,52
v_exp_f32_e32 v33, v66
Cv 32,32,24
Mp 172,20,52
Rb 4,8,17408
v_add_f32_e32 v14, v14, v33
Mp 164,20,48
buffer_load_dwordx4 v2, s[20:23], s55 offen offset:1024 sc1 lds
Mp 176,24,48
Rb 8,8,18432
Mp 184,24,52
v_exp_f32_e32 v25, v67
Mp 188,28,52
Rb 12,8,19456
s_waitcnt lgkmcnt(8)
Mp 180,28,48
v_exp_f32_e32 v34, v72
Mp 192,32,48
Rb 16,8,20480
v_add_f32_e32 v14, v14, v25
Mp 200,32,52
v_exp_f32_e32 v26, v73
Cv 33,33,25
Mp 204,36,52
Rb 20,8,21504
v_add_f32_e32 v14, v14, v34
Mp 196,36,48
buffer_load_dwordx4 v2, s[20:23], s55 offen offset:2048 sc1 lds
Mp 208,40,48
Rb 24,8,22528
v_add_f32_e32 v14, v14, v26
Mp 216,40,52
v_exp_f32_e32 v35, v74
Mp 220,44,52
Rb 28,8,23552
s_waitcnt lgkmcnt(8)
Mp 212,44,48
v_exp_f32_e32 v27, v75
Mp 224,48,48
Rb 32,8,24576
Cv 34,34,26
Mp 232,48,52
v_exp_f32_e32 v36, v68
v_add_f32_e32 v14, v14, v35
Mp 236,52,52
Rb 36,8,25600
v_add_f32_e32 v14, v14, v27
Mp 228,52,48
buffer_load_dwordx4 v2, s[20:23], s55 offen offset:3072 sc1 lds
Mp 240,56,48
Rb 40,8,26624
Cv 35,35,27
Mp 248,56,52
v_exp_f32_e32 v28, v69
Mp 252,60,52
Rb 44,8,27648
s_waitcnt lgkmcnt(8)
Mp 244,60,48
v_exp_f32_e32 v37, v70
)" R"(
Mp 128,0,56
Rb 48,8,28672
v_add_f32_e32 v15, v36, v28
Mp 136,0,60
v_exp_f32_e32 v29, v71
Cv 36,36,28
Mp 140,4,60
Rb 52,8,29696
s_add_u32 m0, s36, 4096
Mp 132,4,56
buffer_load_dwordx4 v3, s[20:23], s55 offen offset:0 sc1 lds
Mp 144,8,56
Rb 56,8,30720
v_add_f32_e32 v15, v15, v37
Mp 152,8,60
v_exp_f32_e32 v38, v76
Mp 156,12,60
Rb 60,8,31744
s_waitcnt lgkmcnt(8)
Mp 148,12,56
v_exp_f32_e32 v24, v77
Mp 160,16,56
v_exp_f32_e32 v39, v78
Mp 168,16,60
v_exp_f32_e32 v25, v79
v_add_f32_e32 v15, v15, v29
Mp 172,20,60
Cv 37,37,29
v_add_f32_e32 v15, v15, v38
Mp 164,20,56
buffer_load_dwordx4 v3, s[20:23], s55 offen offset:1024 sc1 lds
Mp 176,24,56
v_add_f32_e32 v15, v15, v24
Cv 38,38,24
Mp 184,24,60
v_add_f32_e32 v15, v15, v39
v_add_f32_e32 v15, v15, v25
Mp 188,28,60
Cv 39,39,25
s_sub_u32 s57, s57, 1
Mp 180,28,56
s_waitcnt lgkmcnt(0)
s_barrier
Mp 192,32,56
Rb 0,1,32768
v_bfe_u32 v11, v0, 2, 2
Mp 200,32,60
Rb 4,1,33792
v_lshrrev_b32 v4, 4, v0
v_xor_b32 v11, v11, v4
Mp 204,36,60
Rb 8,1,34816
v_bfe_u32 v4, v0, 1, 1
Mp 196,36,56
Rb 12,1,35840
buffer_load_dwordx4 v3, s[20:23], s55 offen offset:2048 sc1 lds
Mp 208,40,56
Rb 16,1,40960
v_lshl_or_b32 v11, v11, 1, v4
Mp 216,40,60
Rb 20,1,41984
v_and_b32 v4, 1, v0
Mp 220,44,60
Rb 24,1,43008
v_mul_u32_u24 v4, 0xffff, v4
)" R"(
Mp 212,44,56
Rb 28,1,44032
v_add_u32 v4, 1, v4
Mp 224,48,56
Rb 32,1,36864
v_mul_u32_u24 v4, 0x3f80, v4
Mp 232,48,60
Rb 36,1,37888
v_cmp_eq_u32_e32 vcc, 0, v11
v_cmp_eq_u32_e64 s[28:29], 1, v11
Mp 236,52,60
Rb 40,1,38912
v_accvgpr_write_b32 a192, 0
Mp 228,52,56
Rb 44,1,39936
buffer_load_dwordx4 v3, s[20:23], s55 offen offset:3072 sc1 lds
Mp 240,56,56
v_accvgpr_write_b32 a193, 0
v_accvgpr_write_b32 a196, 0
Mp 248,56,60
v_accvgpr_write_b32 a197, 0
v_cndmask_b32_e32 v11, 0, v4, vcc
Mp 252,60,60
v_cndmask_b32_e64 v4, 0, v4, s[28:29]
s_waitcnt lgkmcnt(8)
Mp 244,60,56
v_accvgpr_write_b32 a194, v11
v_accvgpr_write_b32 a195, v4
v_mfma_f32_16x16x32_bf16 v[96:99], a[0:3], a[64:67], v[16:19]
Rb 48,1,45056
v_mfma_f32_16x16x32_bf16 v[100:103], a[0:3], a[68:71], v[20:23]
buffer_load_dword v4, v7, s[20:23], s65 offen offset:0 sc0 sc1
Mv 100,4,76
Rb 52,1,46080
s_add_u32 m0, s36, 65536
Mv 96,4,72
buffer_load_dwordx4 v2, s[20:23], s56 offen offset:0 sc1 lds
Mv 96,8,80
Rb 56,1,47104
Mv 100,8,84
v_exp_f32_e32 v40, v80
Mv 100,12,92
Rb 60,1,48128
s_waitcnt lgkmcnt(8)
Mv 96,12,88
v_exp_f32_e32 v26, v81
v_add_f32_e32 v14, v14, v40
Mv 96,16,96
Rb 0,1,49152
v_add_f32_e32 v14, v14, v26
Mv 100,16,100
v_exp_f32_e32 v41, v82
Mv 100,20,108
Rb 4,1,50176
Cv 40,40,26
Mv 96,20,104
buffer_load_dwordx4 v2, s[20:23], s56 offen offset:1024 sc1 lds
Mv 96,24,112
Rb 8,1,51200
v_add_f32_e32 v14, v14, v41
Mv 100,24,116
v_exp_f32_e32 v27, v83
Mv 100,28,124
Rb 12,1,52224
)" R"(
s_waitcnt lgkmcnt(8)
Mv 96,28,120
v_exp_f32_e32 v42, v88
v_add_f32_e32 v14, v14, v27
v_mfma_f32_16x16x32_bf16 v[104:107], a[32:35], a[64:67], v[16:19]
Rb 16,1,57344
Cv 41,41,27
v_mfma_f32_16x16x32_bf16 v[108:111], a[32:35], a[68:71], v[20:23]
v_exp_f32_e32 v28, v89
Mv 108,36,76
Rb 20,1,58368
v_add_f32_e32 v14, v14, v42
Mv 104,36,72
buffer_load_dwordx4 v2, s[20:23], s56 offen offset:2048 sc1 lds
Mv 104,40,80
Rb 24,1,59392
v_add_f32_e32 v14, v14, v28
Mv 108,40,84
v_exp_f32_e32 v43, v90
Mv 108,44,92
Rb 28,1,60416
s_waitcnt lgkmcnt(8)
Mv 104,44,88
v_exp_f32_e32 v29, v91
Cv 42,42,28
Mv 104,48,96
Rb 32,1,53248
v_add_f32_e32 v14, v14, v43
Mv 108,48,100
v_exp_f32_e32 v44, v84
Mv 108,52,108
Rb 36,1,54272
v_add_f32_e32 v14, v14, v29
Mv 104,52,104
buffer_load_dwordx4 v2, s[20:23], s56 offen offset:3072 sc1 lds
Mv 104,56,112
Rb 40,1,55296
Cv 43,43,29
Mv 108,56,116
v_exp_f32_e32 v24, v85
Mv 108,60,124
Rb 44,1,56320
s_waitcnt lgkmcnt(8)
Mv 104,60,120
v_exp_f32_e32 v45, v86
v_add_f32_e32 v15, v15, v44
v_mfma_f32_16x16x32_bf16 v[112:115], a[0:3], a[64:67], v[16:19]
Rb 48,1,61440
v_add_f32_e32 v15, v15, v24
v_mfma_f32_16x16x32_bf16 v[116:119], a[0:3], a[68:71], v[20:23]
v_exp_f32_e32 v25, v87
Mv 116,4,76
Rb 52,1,62464
s_add_u32 m0, s36, 69632
Mv 112,4,72
buffer_load_dwordx4 v3, s[20:23], s56 offen offset:0 sc1 lds
Mv 112,8,80
Rb 56,1,63488
Cv 44,44,24
Mv 116,8,84
v_exp_f32_e32 v46, v92
Mv 116,12,92
Rb 60,1,64512
s_waitcnt lgkmcnt(8)
)" R"(
Mv 112,12,88
v_exp_f32_e32 v26, v93
v_add_f32_e32 v15, v15, v45
Mv 112,16,96
v_exp_f32_e32 v47, v94
Mv 116,16,100
v_exp_f32_e32 v27, v95
Mv 116,20,108
v_add_f32_e32 v15, v15, v25
Cv 45,45,25
Mv 112,20,104
buffer_load_dwordx4 v3, s[20:23], s56 offen offset:1024 sc1 lds
Mv 112,24,112
v_add_f32_e32 v15, v15, v46
v_add_f32_e32 v15, v15, v26
Mv 116,24,116
Cv 46,46,26
v_add_f32_e32 v15, v15, v47
Mv 116,28,124
v_add_f32_e32 v15, v15, v27
Cv 47,47,27
Mv 112,28,120
v_max_f32_e32 v11, v14, v15
s_waitcnt vmcnt(6) lgkmcnt(0)
s_barrier
v_mfma_f32_16x16x32_bf16 v[120:123], a[32:35], a[64:67], v[16:19]
Rb 0,8,32768
v_cmp_nge_f32_e32 vcc, s62, v11
v_mfma_f32_16x16x32_bf16 v[124:127], a[32:35], a[68:71], v[20:23]
Rb 4,8,33792
s_add_u32 s55, s55, 0x10000
Mv 124,36,76
Rb 8,8,34816
Mv 120,36,72
Rb 12,8,35840
buffer_load_dwordx4 v3, s[20:23], s56 offen offset:2048 sc1 lds
Mv 120,40,80
Rb 16,8,36864
Mv 124,40,84
Rb 20,8,37888
Mv 124,44,92
Rb 24,8,38912
Mv 120,44,88
Rb 28,8,39936
s_cmp_eq_u32 s57, 2
s_cselect_b32 s55, s59, s55
Mv 120,48,96
Rb 32,8,40960
Mv 124,48,100
Rb 36,8,41984
Mv 124,52,108
Rb 40,8,43008
Mv 120,52,104
Rb 44,8,44032
buffer_load_dwordx4 v3, s[20:23], s56 offen offset:3072 sc1 lds
Mv 120,56,112
v_readfirstlane_b32 s63, v9
Mv 124,56,116
s_add_u32 s56, s56, 0x10000
Mv 124,60,124
s_waitcnt lgkmcnt(8)
s_cbranch_vccnz .Lzfx12
.Lzfr12:
Mv 120,60,120
v_cmp_eq_u32_e32 vcc, s3, v4
)" R"(
s_andn2_b64 s[28:29], s[60:61], vcc
s_cbranch_scc1 .Lzsp22
.Lzok22:
Mp 128,0,32
Rb 48,8,45056
v_add_f32_e32 v12, v12, v14
Mp 136,0,36
v_add_f32_e32 v13, v13, v15
s_cmp_le_i32 s57, s58
s_cbranch_scc1 .Lzmk11
.Lzmr11:
Mp 140,4,36
Rb 52,8,46080
s_add_u32 m0, s36, 32768
buffer_load_dwordx4 v[64:67], v1, s[20:23], s66 offen sc0 sc1
Mp 132,4,32
buffer_load_dwordx4 v2, s[20:23], s55 offen offset:0 sc1 lds
.LzH16:
s_cbranch_execz .LzHR
Mp 144,8,32
Rb 56,8,47104
Mp 152,8,36
buffer_load_dwordx4 a[128:131], v2, s[20:23], s64 offen offset:0 sc0 sc1
Mp 156,12,36
Rb 60,8,48128
s_waitcnt lgkmcnt(8)
Mp 148,12,32
buffer_load_dwordx4 a[132:135], v2, s[20:23], s64 offen offset:1024 sc0 sc1
Mp 160,16,32
Rb 0,8,49152
Mp 168,16,36
buffer_load_dwordx4 a[136:139], v2, s[20:23], s64 offen offset:2048 sc0 sc1
Mp 172,20,36
Rb 4,8,50176
v_exp_f32_e32 v48, v96
Mp 164,20,32
buffer_load_dwordx4 v2, s[20:23], s55 offen offset:1024 sc1 lds
Mp 176,24,32
Rb 8,8,51200
v_exp_f32_e32 v28, v97
Mp 184,24,36
buffer_load_dwordx4 a[140:143], v2, s[20:23], s64 offen offset:3072 sc0 sc1
Mp 188,28,36
Rb 12,8,52224
v_add_f32_e32 v14, v48, v28
s_waitcnt lgkmcnt(8)
Mp 180,28,32
buffer_load_dwordx4 a[144:147], v3, s[20:23], s64 offen offset:0 sc0 sc1
Mp 192,32,32
Rb 16,8,53248
v_exp_f32_e32 v49, v98
Mp 200,32,36
buffer_load_dwordx4 a[148:151], v3, s[20:23], s64 offen offset:1024 sc0 sc1
Cv 48,48,28
Mp 204,36,36
Rb 20,8,54272
v_add_f32_e32 v14, v14, v49
Mp 196,36,32
buffer_load_dwordx4 v2, s[20:23], s55 offen offset:2048 sc1 lds
Mp 208,40,32
Rb 24,8,55296
Mp 216,40,36
buffer_load_dwordx4 a[152:155], v3, s[20:23], s64 offen offset:2048 sc0 sc1
Mp 220,44,36
)" R"(
Rb 28,8,56320
s_waitcnt lgkmcnt(8)
Mp 212,44,32
buffer_load_dwordx4 a[156:159], v3, s[20:23], s64 offen offset:3072 sc0 sc1
Mp 224,48,32
Rb 32,8,57344
Mp 232,48,36
s_add_u32 s64, s64, 0x10000
buffer_load_dwordx4 a[160:163], v2, s[20:23], s64 offen offset:0 sc0 sc1
Mp 236,52,36
Rb 36,8,58368
v_exp_f32_e32 v29, v99
Mp 228,52,32
buffer_load_dwordx4 v2, s[20:23], s55 offen offset:3072 sc1 lds
Mp 240,56,32
Rb 40,8,59392
v_exp_f32_e32 v50, v104
Mp 248,56,36
buffer_load_dwordx4 a[164:167], v2, s[20:23], s64 offen offset:1024 sc0 sc1
Mp 252,60,36
Rb 44,8,60416
v_add_f32_e32 v14, v14, v29
s_waitcnt lgkmcnt(8)
Mp 244,60,32
buffer_load_dwordx4 a[168:171], v2, s[20:23], s64 offen offset:2048 sc0 sc1
Mp 128,0,40
Rb 48,8,61440
v_exp_f32_e32 v24, v105
Mp 136,0,44
buffer_load_dwordx4 a[172:175], v2, s[20:23], s64 offen offset:3072 sc0 sc1
Cv 49,49,29
Mp 140,4,44
Rb 52,8,62464
s_add_u32 m0, s36, 36864
Mp 132,4,40
buffer_load_dwordx4 v3, s[20:23], s55 offen offset:0 sc1 lds
v_add_f32_e32 v14, v14, v50
Mp 144,8,40
Rb 56,8,63488
v_add_f32_e32 v14, v14, v24
Mp 152,8,44
buffer_load_dwordx4 a[176:179], v3, s[20:23], s64 offen offset:0 sc0 sc1
Cv 50,50,24
Mp 156,12,44
Rb 60,8,64512
s_waitcnt lgkmcnt(8)
Mp 148,12,40
buffer_load_dwordx4 a[180:183], v3, s[20:23], s64 offen offset:1024 sc0 sc1
Mp 160,16,40
v_exp_f32_e32 v51, v106
Mp 168,16,44
buffer_load_dwordx4 a[184:187], v3, s[20:23], s64 offen offset:2048 sc0 sc1
v_add_f32_e32 v14, v14, v51
Mp 172,20,44
v_exp_f32_e32 v25, v107
s_sub_u32 s57, s57, 1
Mp 164,20,40
buffer_load_dwordx4 v3, s[20:23], s55 offen offset:1024 sc1 lds
Mp 176,24,40
v_exp_f32_e32 v52, v100
v_add_f32_e32 v14, v14, v25
Mp 184,24,44
buffer_load_dwordx4 a[188:191], v3, s[20:23], s64 offen offset:3072 sc0 sc1
Mp 188,28,44
)" R"(
v_exp_f32_e32 v26, v101
Cv 51,51,25
Mp 180,28,40
s_waitcnt vmcnt(23) lgkmcnt(0)
s_barrier
Mp 192,32,40
Rb 0,8,0
v_exp_f32_e32 v53, v102
Mp 200,32,44
Rb 4,8,16384
v_exp_f32_e32 v27, v103
Mp 204,36,44
Rb 8,8,1024
v_add_f32_e32 v15, v52, v26
Mp 196,36,40
Rb 12,8,17408
buffer_load_dwordx4 v3, s[20:23], s55 offen offset:2048 sc1 lds
Mp 208,40,40
Rb 16,8,2048
Cv 52,52,26
Mp 216,40,44
Rb 20,8,18432
v_exp_f32_e32 v54, v108
Mp 220,44,44
Rb 24,8,3072
v_add_f32_e32 v15, v15, v53
Mp 212,44,40
Rb 28,8,19456
v_exp_f32_e32 v28, v109
Mp 224,48,40
Rb 32,8,4096
v_add_f32_e32 v15, v15, v27
Mp 232,48,44
Rb 36,8,20480
v_exp_f32_e32 v55, v110
Mp 236,52,44
Rb 40,8,5120
v_exp_f32_e32 v29, v111
Mp 228,52,40
Rb 44,8,21504
buffer_load_dwordx4 v3, s[20:23], s55 offen offset:3072 sc1 lds
Mp 240,56,40
Cv 53,53,27
v_add_f32_e32 v15, v15, v54
v_add_f32_e32 v15, v15, v28
Mp 248,56,44
Cv 54,54,28
v_add_f32_e32 v15, v15, v55
Mp 252,60,44
v_add_f32_e32 v15, v15, v29
Cv 55,55,29
Mp 244,60,40
s_waitcnt vmcnt(18)
v_cmp_neq_f32_e32 vcc, v16, v66
v_cmp_neq_f32_e64 s[28:29], v20, v67
s_or_b64 vcc, vcc, s[28:29]
s_cbranch_scc1 .LzMRG
.LzMRR:
Mv 128,194,128
v_exp_f32_e32 v56, v112
Mv 136,194,132
v_exp_f32_e32 v26, v113
v_add_f32_e32 v12, v12, v64
Mv 132,192,128
s_add_u32 m0, s36, 131072
)" R"(
s_add_u32 s40, s67, 0x20000
v_add_f32_e32 v13, v13, v65
Mv 140,192,132
buffer_load_dwordx4 v2, s[20:23], s40 offen offset:0 sc1 lds
Mv 144,194,136
v_exp_f32_e32 v57, v114
v_add_f32_e32 v14, v14, v56
Mv 152,194,140
v_exp_f32_e32 v27, v115
v_add_f32_e32 v14, v14, v26
Mv 148,192,136
Cv 56,56,26
s_waitcnt vmcnt(14)
Mv 156,192,140
buffer_load_dwordx4 v2, s[20:23], s40 offen offset:1024 sc1 lds
v_add_f32_e32 v14, v14, v57
Mv 160,194,144
v_exp_f32_e32 v58, v120
v_add_f32_e32 v14, v14, v27
Mv 168,194,148
v_exp_f32_e32 v28, v121
Mv 164,192,144
v_exp_f32_e32 v59, v122
Cv 57,57,27
Mv 172,192,148
buffer_load_dwordx4 v2, s[20:23], s40 offen offset:2048 sc1 lds
v_add_f32_e32 v14, v14, v58
Mv 176,194,152
v_exp_f32_e32 v29, v123
Mv 184,194,156
v_exp_f32_e32 v60, v116
v_add_f32_e32 v14, v14, v28
Mv 180,192,152
v_exp_f32_e32 v24, v117
s_waitcnt vmcnt(11)
Mv 188,192,156
buffer_load_dwordx4 v2, s[20:23], s40 offen offset:3072 sc1 lds
Cv 58,58,28
Mv 192,194,160
v_exp_f32_e32 v61, v118
Mv 200,194,164
v_exp_f32_e32 v25, v119
v_add_f32_e32 v14, v14, v59
Mv 196,192,160
s_add_u32 m0, s36, 135168
v_exp_f32_e32 v62, v124
Mv 204,192,164
buffer_load_dwordx4 v3, s[20:23], s40 offen offset:0 sc1 lds
Mv 208,194,168
v_exp_f32_e32 v26, v125
v_add_f32_e32 v14, v14, v29
Mv 216,194,172
v_exp_f32_e32 v63, v126
Cv 59,59,29
Mv 212,192,168
v_add_f32_e32 v15, v15, v60
s_waitcnt vmcnt(7)
Mv 220,192,172
buffer_load_dwordx4 v3, s[20:23], s40 offen offset:1024 sc1 lds
v_add_f32_e32 v15, v15, v24
Mv 224,194,176
v_exp_f32_e32 v27, v127
Cv 60,60,24
Mv 232,194,180
)" R"(
v_add_f32_e32 v15, v15, v61
v_add_f32_e32 v15, v15, v25
Mv 228,192,176
Cv 61,61,25
v_add_f32_e32 v15, v15, v62
v_add_f32_e32 v15, v15, v26
Mv 236,192,180
buffer_load_dwordx4 v3, s[20:23], s40 offen offset:2048 sc1 lds
Cv 62,62,26
Mv 240,194,184
v_add_f32_e32 v15, v15, v63
v_add_f32_e32 v15, v15, v27
Mv 248,194,188
s_add_u32 s40, s67, 0x30000
Cv 63,63,27
v_max_f32_e32 v11, v14, v15
Mv 244,192,184
s_add_u32 m0, s36, 98304
v_cmp_nge_f32_e32 vcc, s62, v11
s_waitcnt lgkmcnt(8)
Mv 252,192,188
buffer_load_dwordx4 v2, s[20:23], s40 offen offset:0 sc1 lds
s_cbranch_vccnz .Lzfx13
.Lzfr13:
Mp 128,0,48
Rb 48,8,6144
v_add_f32_e32 v12, v12, v14
Mp 136,0,52
v_add_f32_e32 v13, v13, v15
v_mov_b32 v24, v12
v_mov_b32 v25, v13
Mp 136,4,60
Rb 52,8,22528
v_permlane32_swap_b32_e32 v12, v24
v_add_f32_e32 v12, v12, v24
Mp 128,4,56
buffer_load_dwordx4 v2, s[20:23], s40 offen offset:1024 sc1 lds
v_mov_b32 v24, v12
Mp 132,8,48
Rb 56,8,7168
v_permlane16_swap_b32_e32 v12, v24
Mp 140,8,52
v_add_f32_e32 v12, v12, v24
v_rcp_f32_e32 v12, v12
Mp 140,12,60
Rb 60,8,23552
v_permlane32_swap_b32_e32 v13, v25
s_waitcnt lgkmcnt(8)
Mp 132,12,56
buffer_load_dwordx4 v2, s[20:23], s40 offen offset:2048 sc1 lds
v_add_f32_e32 v13, v13, v25
Mp 144,16,48
Rb 0,8,8192
v_mov_b32 v25, v13
Mp 152,16,52
s_nop 0
v_permlane16_swap_b32_e32 v13, v25
v_add_f32_e32 v13, v13, v25
Mp 152,20,60
Rb 4,8,24576
v_rcp_f32_e32 v13, v13
Mp 144,20,56
buffer_load_dwordx4 v2, s[20:23], s40 offen offset:3072 sc1 lds
v_mul_f32_e32 v128, v12, v128
)" R"(
Mp 148,24,48
Rb 8,8,9216
v_mul_f32_e32 v129, v12, v129
Mp 156,24,52
s_add_u32 m0, s36, 102400
Cv 128,128,129
v_mul_f32_e32 v130, v12, v130
Mp 156,28,60
Rb 12,8,25600
v_mul_f32_e32 v131, v12, v131
s_waitcnt lgkmcnt(8)
Mp 148,28,56
buffer_load_dwordx4 v3, s[20:23], s40 offen offset:0 sc1 lds
Cv 129,130,131
Mp 160,32,48
Rb 16,8,10240
v_mul_f32_e32 v132, v12, v132
Mp 168,32,52
v_mul_f32_e32 v133, v12, v133
Cv 130,132,133
v_mul_f32_e32 v134, v12, v134
Mp 168,36,60
Rb 20,8,26624
v_mul_f32_e32 v135, v12, v135
Cv 131,134,135
Mp 160,36,56
buffer_load_dwordx4 v3, s[20:23], s40 offen offset:1024 sc1 lds
v_mul_f32_e32 v136, v13, v136
Mp 164,40,48
Rb 24,8,11264
v_mul_f32_e32 v137, v13, v137
Mp 172,40,52
Cv 136,136,137
v_mul_f32_e32 v138, v13, v138
v_mul_f32_e32 v139, v13, v139
Mp 172,44,60
Rb 28,8,27648
Cv 137,138,139
s_waitcnt lgkmcnt(8)
Mp 164,44,56
buffer_load_dwordx4 v3, s[20:23], s40 offen offset:2048 sc1 lds
v_mul_f32_e32 v140, v13, v140
Mp 176,48,48
Rb 32,8,12288
v_mul_f32_e32 v141, v13, v141
Mp 184,48,52
Cv 138,140,141
v_mul_f32_e32 v142, v13, v142
v_mul_f32_e32 v143, v13, v143
Mp 184,52,60
Rb 36,8,28672
Cv 139,142,143
v_mul_f32_e32 v144, v12, v144
Mp 176,52,56
v_mul_f32_e32 v145, v12, v145
Cv 144,144,145
v_mul_f32_e32 v146, v12, v146
Mp 180,56,48
Rb 40,8,13312
v_mul_f32_e32 v147, v12, v147
Mp 188,56,52
Cv 145,146,147
v_mul_f32_e32 v148, v12, v148
v_mul_f32_e32 v149, v12, v149
)" R"(
Mp 188,60,60
Rb 44,8,29696
Cv 146,148,149
s_waitcnt lgkmcnt(8)
Mp 180,60,56
v_mul_f32_e32 v150, v12, v150
v_mul_f32_e32 v151, v12, v151
Cv 147,150,151
Mp 192,0,48
Rb 48,8,14336
v_mul_f32_e32 v152, v13, v152
Mp 200,0,52
v_mul_f32_e32 v153, v13, v153
Cv 152,152,153
v_mul_f32_e32 v154, v13, v154
Mp 200,4,60
Rb 52,8,30720
v_mul_f32_e32 v155, v13, v155
Cv 153,154,155
Mp 192,4,56
v_mul_f32_e32 v156, v13, v156
v_mul_f32_e32 v157, v13, v157
Cv 154,156,157
Mp 196,8,48
Rb 56,8,15360
v_mul_f32_e32 v158, v13, v158
Mp 204,8,52
v_mul_f32_e32 v159, v13, v159
Cv 155,158,159
v_mul_f32_e32 v160, v12, v160
Mp 204,12,60
Rb 60,8,31744
v_mul_f32_e32 v161, v12, v161
s_waitcnt lgkmcnt(8)
Mp 196,12,56
Cv 160,160,161
v_mul_f32_e32 v162, v12, v162
v_mul_f32_e32 v163, v12, v163
Mp 208,16,48
Rb 0,1,0
Cv 161,162,163
Mp 216,16,52
v_mul_f32_e32 v164, v12, v164
v_mul_f32_e32 v165, v12, v165
Cv 162,164,165
Mp 216,20,60
Rb 4,1,1024
v_mul_f32_e32 v166, v12, v166
v_mul_f32_e32 v167, v12, v167
Mp 208,20,56
Cv 163,166,167
v_mul_f32_e32 v168, v13, v168
v_mul_f32_e32 v169, v13, v169
Mp 212,24,48
Rb 8,1,2048
Cv 168,168,169
Mp 220,24,52
v_mul_f32_e32 v170, v13, v170
v_mul_f32_e32 v171, v13, v171
Cv 169,170,171
Mp 220,28,60
Rb 12,1,3072
s_waitcnt vmcnt(14) lgkmcnt(4)
s_barrier
Mp 212,28,56
)" R"(
s_add_u32 s40, s67, 0x40000
v_mul_f32_e32 v172, v13, v172
v_mul_f32_e32 v173, v13, v173
Mp 224,32,48
Rb 16,1,4096
s_add_u32 m0, s36, 65536
Mp 232,32,52
buffer_load_dwordx4 v2, s[20:23], s40 offen offset:0 sc1 lds
Cv 170,172,173
Mp 232,36,60
Rb 20,1,5120
v_mul_f32_e32 v174, v13, v174
v_mul_f32_e32 v175, v13, v175
Mp 224,36,56
Cv 171,174,175
v_mul_f32_e32 v176, v12, v176
v_mul_f32_e32 v177, v12, v177
Mp 228,40,48
Rb 24,1,6144
Cv 176,176,177
s_lshl4_add_u32 s28, s34, s35
Mp 236,40,52
v_mul_f32_e32 v178, v12, v178
v_mul_f32_e32 v179, v12, v179
Cv 177,178,179
Mp 236,44,60
Rb 28,1,8192
v_mul_f32_e32 v180, v12, v180
Mp 228,44,56
buffer_load_dwordx4 v2, s[20:23], s40 offen offset:1024 sc1 lds
v_mul_f32_e32 v181, v12, v181
Mp 240,48,48
Rb 32,1,9216
Cv 178,180,181
s_lshl_b32 s64, s28, 17
Mp 248,48,52
v_mul_f32_e32 v182, v12, v182
v_mul_f32_e32 v183, v12, v183
Cv 179,182,183
Mp 248,52,60
Rb 36,1,10240
v_mul_f32_e32 v184, v13, v184
v_mul_f32_e32 v185, v13, v185
Mp 240,52,56
Cv 184,184,185
v_mul_f32_e32 v186, v13, v186
v_mul_f32_e32 v187, v13, v187
Mp 244,56,48
Rb 40,1,11264
Cv 185,186,187
s_add_u32 s64, s64, 0x4100000
Mp 252,56,52
buffer_load_dwordx4 v2, s[20:23], s40 offen offset:2048 sc1 lds
v_mul_f32_e32 v188, v13, v188
Mp 252,60,60
Rb 44,1,12288
v_mul_f32_e32 v189, v13, v189
s_mov_b32 s55, 0xc108c00
s_waitcnt lgkmcnt(8)
Mp 244,60,56
Cv 186,188,189
v_mul_f32_e32 v190, v13, v190
v_mul_f32_e32 v191, v13, v191
v_mfma_f32_16x16x32_bf16 v[32:35], a[0:3], v[128:131], 0
)" R"(
Rb 48,1,13312
Cv 187,190,191
s_xor_b32 s66, s64, 0x202000
v_mfma_f32_16x16x32_bf16 v[40:43], a[0:3], v[136:139], 0
v_mul_f32_e32 v192, v12, v192
v_mul_f32_e32 v193, v12, v193
Cv 192,192,193
v_mfma_f32_16x16x32_bf16 v[44:47], a[4:7], v[136:139], 0
Rb 52,1,14336
v_mul_f32_e32 v194, v12, v194
v_mul_f32_e32 v195, v12, v195
v_mfma_f32_16x16x32_bf16 v[36:39], a[4:7], v[128:131], 0
buffer_load_dwordx4 v2, s[20:23], s40 offen offset:3072 sc1 lds
Cv 193,194,195
v_mfma_f32_16x16x32_bf16 v[48:51], a[8:11], v[128:131], 0
Rb 56,1,16384
v_mul_f32_e32 v196, v12, v196
s_sub_u32 s66, s66, s84
v_mfma_f32_16x16x32_bf16 v[56:59], a[8:11], v[136:139], 0
v_mul_f32_e32 v197, v12, v197
Cv 194,196,197
v_mul_f32_e32 v198, v12, v198
v_mfma_f32_16x16x32_bf16 v[60:63], a[12:15], v[136:139], 0
Rb 60,1,17408
v_mul_f32_e32 v199, v12, v199
s_cmp_lg_u32 s33, 0
s_cmov_b32 s55, 0x40000000
s_waitcnt lgkmcnt(8)
v_mfma_f32_16x16x32_bf16 v[52:55], a[12:15], v[128:131], 0
Cv 195,198,199
v_mul_f32_e32 v200, v13, v200
v_mul_f32_e32 v201, v13, v201
v_mov_b32 v16, 1
Mp 32,16,144
Rb 0,1,18432
s_add_u32 m0, s36, 69632
s_sub_u32 s66, s66, s84
v_add_u32 v9, s36, v8
Mp 40,16,152
buffer_load_dwordx4 v3, s[20:23], s40 offen offset:0 sc1 lds
Cv 200,200,201
Mp 44,20,152
Rb 4,1,19456
v_mul_f32_e32 v202, v13, v202
v_mul_f32_e32 v203, v13, v203
Mp 36,20,144
Cv 201,202,203
v_mul_f32_e32 v204, v13, v204
v_mul_f32_e32 v205, v13, v205
Mp 48,24,144
Rb 8,1,20480
Cv 202,204,205
s_lshl2_add_u32 s65, s35, s33
Mp 56,24,152
v_mul_f32_e32 v206, v13, v206
v_mul_f32_e32 v207, v13, v207
Cv 203,206,207
Mp 60,28,152
Rb 12,1,21504
v_mul_f32_e32 v208, v12, v208
s_waitcnt lgkmcnt(8)
Mp 52,28,144
buffer_load_dwordx4 v3, s[20:23], s40 offen offset:1024 sc1 lds
v_mul_f32_e32 v209, v12, v209
)" R"(
Mp 32,32,160
Rb 16,1,22528
Cv 208,208,209
s_lshl_b32 s65, s65, 8
Mp 40,32,168
v_mul_f32_e32 v210, v12, v210
v_mul_f32_e32 v211, v12, v211
Cv 209,210,211
Mp 44,36,168
Rb 20,1,24576
v_mul_f32_e32 v212, v12, v212
v_mul_f32_e32 v213, v12, v213
Mp 36,36,160
Cv 210,212,213
v_mul_f32_e32 v214, v12, v214
v_mul_f32_e32 v215, v12, v215
Mp 48,40,160
Rb 24,1,25600
Cv 211,214,215
s_add_u32 s65, s65, s50
Mp 56,40,168
buffer_load_dwordx4 v3, s[20:23], s40 offen offset:2048 sc1 lds
v_mul_f32_e32 v216, v13, v216
Mp 60,44,168
Rb 28,1,26624
v_mul_f32_e32 v217, v13, v217
s_waitcnt lgkmcnt(8)
Mp 52,44,160
Cv 216,216,217
v_mul_f32_e32 v218, v13, v218
v_mul_f32_e32 v219, v13, v219
Mp 32,48,176
Rb 32,1,27648
Cv 217,218,219
s_add_u32 s65, s65, 0x6400
Mp 40,48,184
v_mul_f32_e32 v220, v13, v220
v_mul_f32_e32 v221, v13, v221
Cv 218,220,221
Mp 44,52,184
Rb 36,1,28672
v_mul_f32_e32 v222, v13, v222
v_mul_f32_e32 v223, v13, v223
Mp 36,52,176
Cv 219,222,223
v_mul_f32_e32 v224, v12, v224
v_mul_f32_e32 v225, v12, v225
Mp 48,56,176
Rb 40,1,29696
Cv 224,224,225
s_xor_b32 s52, s65, 0x202000
Mp 56,56,184
v_mul_f32_e32 v226, v12, v226
v_mul_f32_e32 v227, v12, v227
Cv 225,226,227
Mp 60,60,184
Rb 44,1,30720
v_mul_f32_e32 v228, v12, v228
s_waitcnt lgkmcnt(8)
Mp 52,60,176
v_mul_f32_e32 v229, v12, v229
Cv 226,228,229
v_mul_f32_e32 v230, v12, v230
Mp 32,0,192
)" R"(
Rb 48,1,32768
v_mul_f32_e32 v231, v12, v231
buffer_atomic_add v16, v7, s[20:23], s55 offen sc0 sc1
Mp 40,0,200
Cv 227,230,231
v_mul_f32_e32 v232, v13, v232
v_mul_f32_e32 v233, v13, v233
.LzH17:
s_cbranch_execz .LzHR
Mp 44,4,200
Rb 52,1,33792
Cv 232,232,233
v_mul_f32_e32 v234, v13, v234
Mp 36,4,192
v_mul_f32_e32 v235, v13, v235
Cv 233,234,235
v_mul_f32_e32 v236, v13, v236
Mp 48,8,192
Rb 56,1,34816
v_mul_f32_e32 v237, v13, v237
Mp 56,8,200
Cv 234,236,237
v_mul_f32_e32 v238, v13, v238
v_mul_f32_e32 v239, v13, v239
Mp 60,12,200
Rb 60,1,35840
s_waitcnt vmcnt(15) lgkmcnt(4)
s_barrier
Mp 52,12,192
s_add_u32 s40, s67, 0x50000
Cv 235,238,239
v_mul_f32_e32 v240, v12, v240
Mp 32,16,208
Rb 0,1,36864
s_add_u32 m0, s36, 0
Mp 40,16,216
buffer_load_dwordx4 v2, s[20:23], s40 offen offset:0 sc1 lds
v_mul_f32_e32 v241, v12, v241
Mp 44,20,216
Rb 4,1,37888
Cv 240,240,241
v_mul_f32_e32 v242, v12, v242
Mp 36,20,208
v_mul_f32_e32 v243, v12, v243
Cv 241,242,243
v_mul_f32_e32 v244, v12, v244
Mp 48,24,208
Rb 8,1,38912
v_mul_f32_e32 v245, v12, v245
Mp 56,24,216
Cv 242,244,245
v_mul_f32_e32 v246, v12, v246
v_mul_f32_e32 v247, v12, v247
Mp 60,28,216
Rb 12,1,40960
Cv 243,246,247
Mp 52,28,208
buffer_load_dwordx4 v2, s[20:23], s40 offen offset:1024 sc1 lds
v_mul_f32_e32 v248, v13, v248
Mp 32,32,224
Rb 16,1,41984
v_mul_f32_e32 v249, v13, v249
Mp 40,32,232
Cv 248,248,249
v_mul_f32_e32 v250, v13, v250
)" R"(
v_mul_f32_e32 v251, v13, v251
Mp 44,36,232
Rb 20,1,43008
Cv 249,250,251
v_mul_f32_e32 v252, v13, v252
Mp 36,36,224
v_mul_f32_e32 v253, v13, v253
Cv 250,252,253
v_mul_f32_e32 v254, v13, v254
Mp 48,40,224
Rb 24,1,44032
v_mul_f32_e32 v255, v13, v255
Mp 56,40,232
buffer_load_dwordx4 v2, s[20:23], s40 offen offset:2048 sc1 lds
Cv 251,254,255
Mp 60,44,232
Rb 28,1,45056
s_waitcnt lgkmcnt(8)
Mp 52,44,224
Mp 32,48,240
Rb 32,1,46080
Mp 40,48,248
Mp 44,52,248
Rb 36,1,47104
Mp 36,52,240
buffer_load_dwordx4 v2, s[20:23], s40 offen offset:3072 sc1 lds
Mp 48,56,240
Rb 40,1,49152
Mp 56,56,248
Mp 60,60,248
Rb 44,1,50176
s_waitcnt lgkmcnt(8)
Mp 52,60,240
v_mfma_f32_16x16x32_bf16 v[64:67], a[0:3], v[128:131], 0
Rb 48,1,51200
s_add_u32 m0, s36, 4096
v_mfma_f32_16x16x32_bf16 v[72:75], a[0:3], v[136:139], 0
buffer_load_dwordx4 v3, s[20:23], s40 offen offset:0 sc1 lds
Mp 72,4,152
Rb 52,1,52224
Mp 64,4,144
Cv 32,32,33
Cv 33,34,35
Mp 64,8,160
Rb 56,1,53248
Cv 34,36,37
Mp 72,8,168
Cv 35,38,39
Mp 72,12,184
Rb 60,1,54272
s_waitcnt lgkmcnt(8)
Mp 64,12,176
buffer_load_dwordx4 v3, s[20:23], s40 offen offset:1024 sc1 lds
Mp 64,16,192
Rb 0,1,55296
Mp 72,16,200
buffer_store_dwordx4 v[32:35], v2, s[20:23], s64 offen offset:0 sc0 sc1
Mp 72,20,216
Rb 4,1,57344
Mp 64,20,208
Mp 64,24,224
Rb 8,1,58368
Mp 72,24,232
buffer_load_dwordx4 v3, s[20:23], s40 offen offset:2048 sc1 lds
)" R"(
Mp 72,28,248
Rb 12,1,59392
s_waitcnt lgkmcnt(8)
Mp 64,28,240
Cv 40,40,41
Cv 41,42,43
v_mfma_f32_16x16x32_bf16 v[68:71], a[32:35], v[128:131], 0
Rb 16,1,60416
Cv 42,44,45
v_mfma_f32_16x16x32_bf16 v[76:79], a[32:35], v[136:139], 0
Cv 43,46,47
Mp 76,36,152
Rb 20,1,61440
Mp 68,36,144
buffer_store_dwordx4 v[40:43], v2, s[20:23], s64 offen offset:1024 sc0 sc1
Mp 68,40,160
Rb 24,1,62464
Mp 76,40,168
Mp 76,44,184
Rb 28,1,63488
s_waitcnt lgkmcnt(8)
Mp 68,44,176
Mp 68,48,192
Rb 32,10,0
Mp 76,48,200
Cv 48,48,49
Cv 49,50,51
Mp 76,52,216
Rb 36,10,1024
Cv 50,52,53
Mp 68,52,208
Cv 51,54,55
Mp 68,56,224
Rb 40,10,2048
Mp 76,56,232
buffer_store_dwordx4 v[48:51], v2, s[20:23], s64 offen offset:2048 sc0 sc1
Mp 76,60,248
Rb 44,10,3072
s_waitcnt vmcnt(18) lgkmcnt(4)
s_barrier
Mp 68,60,240
s_add_u32 s40, s67, 0x60000
v_mfma_f32_16x16x32_bf16 v[80:83], a[0:3], v[128:131], 0
Rb 48,10,4096
s_add_u32 m0, s36, 32768
v_mfma_f32_16x16x32_bf16 v[88:91], a[0:3], v[136:139], 0
buffer_load_dwordx4 v2, s[20:23], s40 offen offset:0 sc1 lds
Mp 88,4,152
Rb 52,10,5120
Mp 80,4,144
Cv 56,56,57
Cv 57,58,59
Mp 80,8,160
Rb 56,10,6144
Cv 58,60,61
Mp 88,8,168
Cv 59,62,63
Mp 88,12,184
Rb 60,10,8192
Mp 80,12,176
buffer_load_dwordx4 v2, s[20:23], s40 offen offset:1024 sc1 lds
Mp 80,16,192
Rb 0,10,9216
Mp 88,16,200
buffer_store_dwordx4 v[56:59], v2, s[20:23], s64 offen offset:3072 sc0 sc1
)" R"(
Mp 88,20,216
Rb 4,10,10240
Mp 80,20,208
Mp 80,24,224
Rb 8,10,11264
Mp 88,24,232
buffer_load_dwordx4 v2, s[20:23], s40 offen offset:2048 sc1 lds
Mp 88,28,248
Rb 12,10,12288
s_waitcnt lgkmcnt(8)
Mp 80,28,240
Cv 64,64,65
Cv 65,66,67
v_mfma_f32_16x16x32_bf16 v[84:87], a[32:35], v[128:131], 0
Rb 16,10,13312
Cv 66,68,69
v_mfma_f32_16x16x32_bf16 v[92:95], a[32:35], v[136:139], 0
Cv 67,70,71
Mp 92,36,152
Rb 20,10,14336
Mp 84,36,144
buffer_load_dwordx4 v2, s[20:23], s40 offen offset:3072 sc1 lds
Mp 84,40,160
Rb 24,10,16384
Mp 92,40,168
buffer_store_dwordx4 v[64:67], v3, s[20:23], s64 offen offset:0 sc0 sc1
Mp 92,44,184
Rb 28,10,17408
s_waitcnt lgkmcnt(8)
Mp 84,44,176
Mp 84,48,192
Rb 32,10,18432
s_add_u32 m0, s36, 36864
Mp 92,48,200
buffer_load_dwordx4 v3, s[20:23], s40 offen offset:0 sc1 lds
Mp 92,52,216
Rb 36,10,19456
Mp 84,52,208
Cv 72,72,73
Cv 73,74,75
Mp 84,56,224
Rb 40,10,20480
Cv 74,76,77
Mp 92,56,232
Cv 75,78,79
Mp 92,60,248
Rb 44,10,21504
s_waitcnt lgkmcnt(8)
Mp 84,60,240
buffer_load_dwordx4 v3, s[20:23], s40 offen offset:1024 sc1 lds
v_mfma_f32_16x16x32_bf16 v[96:99], a[0:3], v[128:131], 0
Rb 48,10,22528
v_mfma_f32_16x16x32_bf16 v[104:107], a[0:3], v[136:139], 0
buffer_store_dwordx4 v[72:75], v3, s[20:23], s64 offen offset:1024 sc0 sc1
Mp 104,4,152
Rb 52,10,24576
Mp 96,4,144
Mp 96,8,160
Rb 56,10,25600
Mp 104,8,168
buffer_load_dwordx4 v3, s[20:23], s40 offen offset:2048 sc1 lds
Mp 104,12,184
Rb 60,10,26624
s_waitcnt lgkmcnt(8)
)" R"(
Mp 96,12,176
Cv 80,80,81
Cv 81,82,83
Mp 96,16,192
Rb 0,10,27648
Cv 82,84,85
Mp 104,16,200
Cv 83,86,87
Mp 104,20,216
Rb 4,10,28672
Mp 96,20,208
buffer_store_dwordx4 v[80:83], v3, s[20:23], s64 offen offset:2048 sc0 sc1
Mp 96,24,224
Rb 8,10,29696
Mp 104,24,232
Mp 104,28,248
Rb 12,10,30720
s_waitcnt lgkmcnt(8)
Mp 96,28,240
v_mfma_f32_16x16x32_bf16 v[100:103], a[32:35], v[128:131], 0
Rb 16,8,32768
v_mfma_f32_16x16x32_bf16 v[108:111], a[32:35], v[136:139], 0
Cv 88,88,89
Cv 89,90,91
Mp 108,36,152
Rb 20,8,33792
Cv 90,92,93
Mp 100,36,144
Cv 91,94,95
Mp 100,40,160
Rb 24,8,34816
Mp 108,40,168
buffer_store_dwordx4 v[88:91], v3, s[20:23], s64 offen offset:3072 sc0 sc1
Mp 108,44,184
Rb 28,8,35840
s_waitcnt vmcnt(23) lgkmcnt(4)
s_barrier
Mp 100,44,176
s_add_u32 s40, s67, 0x70000
Mp 100,48,192
Rb 32,8,36864
s_add_u32 m0, s36, 131072
Mp 108,48,200
buffer_load_dwordx4 v2, s[20:23], s40 offen offset:0 sc1 lds
Mp 108,52,216
Rb 36,8,37888
s_mov_b32 s8, s74
Mp 100,52,208
s_mov_b32 s9, s75
s_mov_b32 s10, 0x1c0000
Mp 100,56,224
Rb 40,8,38912
s_mov_b32 s11, 0x20000
Mp 108,56,232
v_and_b32 v174, 1, v0
v_mul_u32_u24 v174, 0xffff, v174
Mp 108,60,248
Rb 44,8,40960
Mp 100,60,240
buffer_load_dwordx4 v2, s[20:23], s40 offen offset:1024 sc1 lds
v_mfma_f32_16x16x32_bf16 v[112:115], a[0:3], v[128:131], 0
Rb 48,8,41984
v_add_u32 v174, 1, v174
v_mfma_f32_16x16x32_bf16 v[120:123], a[0:3], v[136:139], 0
v_mul_u32_u24 v174, 0x3f80, v174
)" R"(
v_bfe_u32 v173, v0, 1, 3
Mp 120,4,152
Rb 52,8,43008
v_cmp_eq_u32_e32 vcc, 0, v173
Mp 112,4,144
Cv 96,96,97
Cv 97,98,99
Mp 112,8,160
Rb 56,8,44032
Cv 98,100,101
Mp 120,8,168
buffer_load_dwordx4 v2, s[20:23], s40 offen offset:2048 sc1 lds
Mp 120,12,184
Rb 60,8,45056
s_waitcnt lgkmcnt(8)
Mp 112,12,176
Cv 99,102,103
v_cndmask_b32_e32 v132, 0, v174, vcc
v_cmp_eq_u32_e32 vcc, 1, v173
Mp 112,16,192
Rb 0,8,46080
v_cndmask_b32_e32 v133, 0, v174, vcc
Mp 120,16,200
s_add_u32 s64, s64, 0x10000
buffer_store_dwordx4 v[96:99], v2, s[20:23], s64 offen offset:0 sc0 sc1
Mp 120,20,216
Rb 4,8,47104
v_cmp_eq_u32_e32 vcc, 2, v173
Mp 112,20,208
buffer_load_dwordx4 v2, s[20:23], s40 offen offset:3072 sc1 lds
Mp 112,24,224
Rb 8,8,49152
v_cndmask_b32_e32 v134, 0, v174, vcc
Mp 120,24,232
v_cmp_eq_u32_e32 vcc, 3, v173
v_and_b32 v167, 0xf0, v1
Mp 120,28,248
Rb 12,8,50176
s_waitcnt lgkmcnt(8)
Mp 112,28,240
Cv 104,104,105
Cv 105,106,107
v_mfma_f32_16x16x32_bf16 v[116:119], a[32:35], v[128:131], 0
Rb 16,8,51200
s_add_u32 m0, s36, 135168
v_mfma_f32_16x16x32_bf16 v[124:127], a[32:35], v[136:139], 0
buffer_load_dwordx4 v3, s[20:23], s40 offen offset:0 sc1 lds
Mp 124,36,152
Rb 20,8,52224
Cv 106,108,109
Mp 116,36,144
Cv 107,110,111
v_cndmask_b32_e32 v135, 0, v174, vcc
Mp 116,40,160
Rb 24,8,53248
v_cmp_eq_u32_e32 vcc, 4, v173
Mp 124,40,168
buffer_store_dwordx4 v[104:107], v2, s[20:23], s64 offen offset:1024 sc0 sc1
Mp 124,44,184
Rb 28,8,54272
s_waitcnt lgkmcnt(8)
Mp 116,44,176
buffer_load_dwordx4 v3, s[20:23], s40 offen offset:1024 sc1 lds
v_cndmask_b32_e32 v140, 0, v174, vcc
)" R"(
Mp 116,48,192
Rb 32,8,55296
v_cmp_eq_u32_e32 vcc, 5, v173
Mp 124,48,200
s_nop 0
v_cndmask_b32_e32 v141, 0, v174, vcc
v_cmp_eq_u32_e32 vcc, 6, v173
Mp 124,52,216
Rb 36,8,57344
v_cndmask_b32_e32 v142, 0, v174, vcc
Mp 116,52,208
v_cmp_eq_u32_e32 vcc, 7, v173
v_lshrrev_b32 v173, 4, v0
Mp 116,56,224
Rb 40,8,58368
v_cndmask_b32_e32 v143, 0, v174, vcc
Mp 124,56,232
buffer_load_dwordx4 v3, s[20:23], s40 offen offset:2048 sc1 lds
Mp 124,60,248
Rb 44,8,59392
s_waitcnt vmcnt(9) lgkmcnt(8)
buffer_store_dword v6, v7, s[20:23], s65 offen offset:0 sc0 sc1
Mp 116,60,240
v_lshl_or_b32 v164, v173, 16, v167
v_mul_u32_u24 v166, 56, v167
v_mfma_f32_16x16x32_bf16 v[32:35], a[0:3], v[128:131], 0
Rb 48,8,60416
v_lshl_add_u32 v166, v173, 3, v166
v_mfma_f32_16x16x32_bf16 v[40:43], a[0:3], v[136:139], 0
v_lshrrev_b32_e64 v173, v173, s85
v_bfe_u32 v174, v173, 4, 1
Mp 40,4,152
Rb 52,8,61440
v_lshl_or_b32 v183, v174, 13, v164
Mp 32,4,144
Cv 112,112,113
Cv 113,114,115
Mp 32,8,160
Rb 56,8,62464
Cv 114,116,117
Mp 40,8,168
Cv 115,118,119
v_and_b32 v174, 1, v173
Mp 40,12,184
Rb 60,8,63488
s_waitcnt lgkmcnt(8)
Mp 32,12,176
buffer_store_dwordx4 v[112:115], v2, s[20:23], s64 offen offset:2048 sc0 sc1
ds_write_b32 v9, v16 offset:39936
Mp 32,16,192
Rb 0,8,0
v_lshl_or_b32 v164, v174, 13, v164
Mp 40,16,200
v_and_b32 v174, 7, v0
v_and_b32 v165, 0x80, v1
Mp 40,20,216
Rb 4,8,1024
v_lshl_or_b32 v165, v174, 8, v165
Mp 32,20,208
v_lshlrev_b32 v165, 1, v165
s_mov_b32 s93, 0x1111
Mp 32,24,224
Rb 8,8,2048
v_lshrrev_b32 v173, 2, v0
)" R"(
Mp 40,24,232
Cv 120,120,121
Cv 121,122,123
Mp 40,28,248
Rb 12,8,3072
s_waitcnt lgkmcnt(3)
s_barrier
Mp 32,28,240
Cv 122,124,125
Cv 123,126,127
ds_read_b32 v16, v8 offset:39936
v_mfma_f32_16x16x32_bf16 v[36:39], a[32:35], v[128:131], 0
Rb 16,8,4096
v_mul_u32_u24 v173, 832, v173
v_mfma_f32_16x16x32_bf16 v[44:47], a[32:35], v[136:139], 0
buffer_store_dwordx4 v[120:123], v2, s[20:23], s64 offen offset:3072 sc0 sc1
Mp 44,36,152
Rb 20,8,5120
v_add_u32 v173, v173, v1
Mp 36,36,144
v_lshrrev_b32 v174, 1, v0
v_and_b32 v174, 24, v174
Mp 36,40,160
Rb 24,8,6144
v_lshl_add_u32 v174, v167, 2, v174
Mp 44,40,168
s_add_u32 s59, s36, 98304
v_add_u32 v174, s59, v174
Mp 44,44,184
Rb 28,8,8192
s_waitcnt lgkmcnt(4)
Mp 36,44,176
v_readfirstlane_b32 s82, v16
Mp 36,48,192
Rb 32,8,9216
s_lshl2_add_u32 s56, s82, s33
Mp 44,48,200
s_mul_i32 s88, s56, 4682
s_lshr_b32 s58, s88, 16
Mp 44,52,216
Rb 36,8,10240
s_mul_i32 s57, s58, 14
Mp 36,52,208
s_sub_u32 s57, s56, s57
s_lshl_b32 s40, s58, 12
Mp 36,56,224
Rb 40,8,11264
Mp 44,56,232
s_cmp_gt_u32 s57, 6
s_cselect_b32 s88, 128, 0
Mp 44,60,248
Rb 44,8,12288
Mp 36,60,240
buffer_load_dword v4, v7, s[20:23], s52 offen offset:0 sc0 sc1
s_add_u32 s40, s40, s88
v_mfma_f32_16x16x32_bf16 v[48:51], a[0:3], v[128:131], 0
Rb 48,8,13312
s_add_u32 s40, s40, 0xc110000
v_mfma_f32_16x16x32_bf16 v[56:59], a[0:3], v[136:139], 0
s_lshl_b32 s55, s58, 10
s_add_u32 s55, s55, s57
Mp 56,4,152
Rb 52,8,14336
Mp 48,4,144
Cv 32,32,33
)" R"(
Cv 33,34,35
Mp 48,8,160
Rb 56,8,16384
Cv 34,36,37
Mp 56,8,168
Cv 35,38,39
Mp 56,12,184
Rb 60,8,17408
s_waitcnt lgkmcnt(8)
Mp 48,12,176
buffer_store_dwordx4 v[32:35], v3, s[20:23], s64 offen offset:0 sc0 sc1
Mp 48,16,192
Rb 0,8,18432
Mp 56,16,200
s_cmp_gt_u32 s57, 6
s_cselect_b32 s88, 9, 0
Mp 56,20,216
Rb 4,8,19456
s_add_u32 s55, s55, s88
Mp 48,20,208
s_and_b32 s88, s58, 2
s_lshl_b32 s88, s88, 12
Mp 48,24,224
Rb 8,8,20480
v_xor_b32 v164, s88, v164
Mp 56,24,232
Cv 40,40,41
Cv 41,42,43
Mp 56,28,248
Rb 12,8,21504
s_waitcnt lgkmcnt(8)
Mp 48,28,240
Cv 42,44,45
Cv 43,46,47
v_xor_b32 v183, s88, v183
v_mfma_f32_16x16x32_bf16 v[52:55], a[32:35], v[128:131], 0
Rb 16,8,22528
s_lshl_b32 s55, s55, 10
v_mfma_f32_16x16x32_bf16 v[60:63], a[32:35], v[136:139], 0
buffer_store_dwordx4 v[40:43], v3, s[20:23], s64 offen offset:1024 sc0 sc1
Mp 60,36,152
Rb 20,8,24576
s_add_u32 s55, s55, 0x8000000
Mp 52,36,144
s_and_b32 s88, s57, 1
s_lshl1_add_u32 s58, s58, s88
Mp 52,40,160
Rb 24,8,25600
s_lshl_b32 s88, s58, 8
Mp 60,40,168
v_add_u32 v167, s88, v167
s_mul_i32 s59, s58, 14336
Mp 60,44,184
Rb 28,8,26624
s_waitcnt lgkmcnt(8)
.LzH18:
s_cbranch_execz .LzHR
Mp 52,44,176
s_lshr_b32 s88, s57, 1
s_lshl_b32 s88, s88, 6
s_add_u32 s88, s88, s59
Mp 52,48,192
Rb 32,8,27648
v_add_u32 v166, s88, v166
)" R"(
Mp 60,48,200
s_mov_b32 s57, 0
s_add_u32 m0, s36, 98304
Mp 60,52,216
Rb 36,8,28672
Mp 52,52,208
buffer_load_dwordx4 v173, s[12:15], s88 offen offset:0 lds
Mp 52,56,224
Rb 40,8,29696
s_add_u32 m0, s36, 98880
Mp 60,56,232
buffer_load_dwordx4 v173, s[12:15], s88 offen offset:448 lds
Mp 60,60,248
Rb 44,8,30720
s_waitcnt lgkmcnt(8)
Mp 52,60,240
s_mov_b32 s88, 0xc100008
buffer_load_dword v167, v167, s[20:23], s88 offen sc0 sc1
v_mfma_f32_16x16x32_bf16 v[64:67], a[0:3], v[128:131], 0
Rb 48,1,0
v_mfma_f32_16x16x32_bf16 v[72:75], a[0:3], v[136:139], 0
Mp 72,4,152
Rb 52,1,1024
Mp 64,4,144
Mp 64,8,160
Rb 56,1,2048
Mp 72,8,168
Mp 72,12,184
Rb 60,1,3072
s_waitcnt vmcnt(5) lgkmcnt(8)
s_barrier
v_cmp_eq_u32_e32 vcc, s3, v4
s_andn2_b64 s[28:29], 1, vcc
s_cbranch_scc1 .Lzsp23
.Lzok23:
Mp 64,12,176
buffer_load_dwordx4 a[64:67], v2, s[20:23], s66 offen offset:0 sc0 sc1
Mp 64,16,192
Rb 0,1,4096
Mp 72,16,200
Mp 72,20,216
Rb 4,1,5120
Mp 64,20,208
buffer_load_dwordx4 a[68:71], v2, s[20:23], s66 offen offset:1024 sc0 sc1
Mp 64,24,224
Rb 8,1,6144
Mp 72,24,232
Mp 72,28,248
Rb 12,1,8192
s_waitcnt lgkmcnt(8)
Mp 64,28,240
buffer_load_dwordx4 a[72:75], v2, s[20:23], s66 offen offset:2048 sc0 sc1
v_mfma_f32_16x16x32_bf16 v[68:71], a[32:35], v[128:131], 0
Rb 16,1,9216
v_mfma_f32_16x16x32_bf16 v[76:79], a[32:35], v[136:139], 0
Mp 76,36,152
Rb 20,1,10240
Mp 68,36,144
buffer_load_dwordx4 a[76:79], v2, s[20:23], s66 offen offset:3072 sc0 sc1
Mp 68,40,160
Rb 24,1,11264
Mp 76,40,168
Mp 76,44,184
Rb 28,1,12288
s_waitcnt vmcnt(7) lgkmcnt(8)
)" R"(
buffer_store_dword v6, v7, s[20:23], s65 offen offset:128 sc0 sc1
Mp 68,44,176
buffer_load_dwordx4 a[80:83], v3, s[20:23], s66 offen offset:0 sc0 sc1
Mp 68,48,192
Rb 32,1,13312
Mp 76,48,200
Mp 76,52,216
Rb 36,1,14336
Mp 68,52,208
buffer_load_dwordx4 a[84:87], v3, s[20:23], s66 offen offset:1024 sc0 sc1
Mp 68,56,224
Rb 40,1,16384
Mp 76,56,232
Mp 76,60,248
Rb 44,1,17408
s_waitcnt lgkmcnt(8)
Mp 68,60,240
buffer_load_dwordx4 a[88:91], v3, s[20:23], s66 offen offset:2048 sc0 sc1
v_mfma_f32_16x16x32_bf16 v[80:83], a[0:3], v[128:131], 0
Rb 48,1,18432
v_mfma_f32_16x16x32_bf16 v[88:91], a[0:3], v[136:139], 0
Mp 88,4,152
Rb 52,1,19456
Mp 80,4,144
buffer_load_dwordx4 a[92:95], v3, s[20:23], s66 offen offset:3072 sc0 sc1
Mp 80,8,160
Rb 56,1,20480
Mp 88,8,168
Mp 88,12,184
Rb 60,1,21504
s_waitcnt lgkmcnt(8)
Mp 80,12,176
Mp 80,16,192
Rb 0,1,22528
Mp 88,16,200
Mp 88,20,216
Rb 4,1,24576
Mp 80,20,208
Mp 80,24,224
Rb 8,1,25600
Mp 88,24,232
Mp 88,28,248
Rb 12,1,26624
s_waitcnt lgkmcnt(8)
Mp 80,28,240
v_mfma_f32_16x16x32_bf16 v[84:87], a[32:35], v[128:131], 0
Rb 16,1,27648
v_mfma_f32_16x16x32_bf16 v[92:95], a[32:35], v[136:139], 0
Mp 92,36,152
Rb 20,1,28672
Mp 84,36,144
Mp 84,40,160
Rb 24,1,29696
Mp 92,40,168
Mp 92,44,184
Rb 28,1,30720
s_waitcnt lgkmcnt(8)
Mp 84,44,176
buffer_load_dword v4, v7, s[20:23], s52 offen offset:128 sc0 sc1
Mp 84,48,192
Rb 32,1,32768
Mp 92,48,200
Mp 92,52,216
Rb 36,1,33792
)" R"(
Mp 84,52,208
Mp 84,56,224
Rb 40,1,34816
Mp 92,56,232
Mp 92,60,248
Rb 44,1,35840
s_waitcnt vmcnt(6)
s_barrier
Mp 84,60,240
Mv 48,194,64
Mv 56,194,68
Mv 52,192,64
Mv 60,192,68
Mv 64,194,72
Mv 72,194,76
Mv 68,192,72
s_waitcnt vmcnt(3) lgkmcnt(10)
Mv 76,192,76
Cv 48,48,49
Cv 49,50,51
Mv 80,194,80
Cv 50,52,53
Cv 51,54,55
Mv 88,194,84
s_add_u32 s29, s73, 0x0
buffer_store_dwordx4 v[48:51], v1, s[20:23], s29 offen sc0 sc1
Mv 84,192,80
Mv 92,192,84
v_mfma_f32_16x16x32_bf16 v[96:99], a[0:3], v[128:131], 0
Rb 48,1,36864
v_mfma_f32_16x16x32_bf16 v[104:107], a[0:3], v[136:139], 0
Mp 104,4,152
Rb 52,1,37888
s_waitcnt lgkmcnt(8)
Mp 96,4,144
Cv 56,56,57
Cv 57,58,59
ds_read_b64 v[188:189], v174 offset:0
Mp 96,8,160
Rb 56,1,38912
Cv 58,60,61
Mp 104,8,168
Cv 59,62,63
ds_read_b64 v[190:191], v174 offset:32
Mp 104,12,184
Rb 60,1,40960
Mp 96,12,176
s_add_u32 s29, s73, 0x400
buffer_store_dwordx4 v[56:59], v1, s[20:23], s29 offen sc0 sc1
ds_read_b64 v[196:197], v174 offset:1024
Mp 96,16,192
Rb 0,1,41984
Mp 104,16,200
ds_read_b64 v[198:199], v174 offset:1056
Mp 104,20,216
Rb 4,1,43008
s_waitcnt lgkmcnt(12)
Mp 96,20,208
Mp 96,24,224
Rb 8,1,44032
Mp 104,24,232
Cv 64,64,65
Cv 65,66,67
Mp 104,28,248
Rb 12,1,45056
)" R"(
Cv 66,68,69
Mp 96,28,240
Cv 67,70,71
v_mfma_f32_16x16x32_bf16 v[100:103], a[32:35], v[128:131], 0
Rb 16,1,46080
v_mfma_f32_16x16x32_bf16 v[108:111], a[32:35], v[136:139], 0
s_add_u32 s29, s73, 0x800
buffer_store_dwordx4 v[64:67], v1, s[20:23], s29 offen sc0 sc1
Mp 108,36,152
Rb 20,1,47104
s_waitcnt vmcnt(3) lgkmcnt(11)
v_cmp_eq_u32_e32 vcc, s3, v4
s_andn2_b64 s[28:29], 1, vcc
s_cbranch_scc1 .Lzsp24
.Lzok24:
Mp 100,36,144
s_add_u32 s66, s66, 0x10000
buffer_load_dwordx4 a[96:99], v2, s[20:23], s66 offen offset:0 sc0 sc1
Mp 100,40,160
Rb 24,1,49152
v_sub_f32_e32 v172, 1.0, v167
Mp 108,40,168
Cv 172,172,172
v_lshlrev_b32 v172, 16, v172
Mp 108,44,184
Rb 28,1,50176
v_lshlrev_b32 v204, 16, v188
Mp 100,44,176
buffer_load_dwordx4 a[100:103], v2, s[20:23], s66 offen offset:1024 sc0 sc1
Mp 100,48,192
Rb 32,1,51200
Cv 72,72,73
Mp 108,48,200
Cv 73,74,75
Cv 74,76,77
Mp 108,52,216
Rb 36,1,52224
s_waitcnt lgkmcnt(12)
Mp 100,52,208
buffer_load_dwordx4 a[104:107], v2, s[20:23], s66 offen offset:2048 sc0 sc1
Mp 100,56,224
Rb 40,1,53248
Cv 75,78,79
Mp 108,56,232
s_add_u32 s29, s73, 0xc00
buffer_store_dwordx4 v[72:75], v1, s[20:23], s29 offen sc0 sc1
Mp 108,60,248
Rb 44,1,54272
v_and_b32 v205, 0xffff0000, v188
Mp 100,60,240
buffer_load_dwordx4 a[108:111], v2, s[20:23], s66 offen offset:3072 sc0 sc1
Mv 96,194,88
v_mul_f32_e32 v204, v172, v204
v_mul_f32_e32 v205, v172, v205
Mv 104,194,92
Cv 205,204,205
v_lshlrev_b32 v204, 16, v205
Mv 100,192,88
v_and_b32 v205, 0xffff0000, v205
s_waitcnt lgkmcnt(8)
Mv 108,192,92
buffer_load_dwordx4 a[112:115], v3, s[20:23], s66 offen offset:0 sc0 sc1
v_mfma_f32_16x16x32_bf16 v[112:115], a[0:3], v[128:131], 0
Rb 48,1,55296
)" R"(
Cv 80,80,81
v_mfma_f32_16x16x32_bf16 v[120:123], a[0:3], v[136:139], 0
Cv 81,82,83
Cv 82,84,85
Mp 120,4,152
Rb 52,1,57344
Cv 83,86,87
Mp 112,4,144
buffer_load_dwordx4 a[116:119], v3, s[20:23], s66 offen offset:1024 sc0 sc1
Mp 112,8,160
Rb 56,1,58368
v_lshlrev_b32 v206, 16, v189
Mp 120,8,168
s_add_u32 s29, s73, 0x1000
buffer_store_dwordx4 v[80:83], v1, s[20:23], s29 offen sc0 sc1
Mp 120,12,184
Rb 60,1,59392
s_waitcnt lgkmcnt(8)
Mp 112,12,176
v_and_b32 v207, 0xffff0000, v189
v_mul_f32_e32 v206, v172, v206
Mp 112,16,192
Rb 0,1,60416
v_mul_f32_e32 v207, v172, v207
Mp 120,16,200
Cv 207,206,207
v_lshlrev_b32 v206, 16, v207
Mp 120,20,216
Rb 4,1,61440
v_and_b32 v207, 0xffff0000, v207
Mp 112,20,208
Cv 88,88,89
Cv 89,90,91
Mp 112,24,224
Rb 8,1,62464
Cv 90,92,93
Mp 120,24,232
Cv 91,94,95
v_lshlrev_b32 v212, 16, v190
Mp 120,28,248
Rb 12,1,63488
s_waitcnt lgkmcnt(8)
Mp 112,28,240
s_add_u32 s29, s73, 0x1400
buffer_store_dwordx4 v[88:91], v1, s[20:23], s29 offen sc0 sc1
v_mfma_f32_16x16x32_bf16 v[116:119], a[32:35], v[128:131], 0
Rb 16,10,0
v_and_b32 v213, 0xffff0000, v190
v_mfma_f32_16x16x32_bf16 v[124:127], a[32:35], v[136:139], 0
v_mul_f32_e32 v212, v172, v212
v_mul_f32_e32 v213, v172, v213
Mp 124,36,152
Rb 20,10,1024
Cv 213,212,213
Mp 116,36,144
v_lshlrev_b32 v212, 16, v213
v_and_b32 v213, 0xffff0000, v213
Mp 116,40,160
Rb 24,10,2048
v_lshlrev_b32 v214, 16, v191
Mp 124,40,168
Cv 96,96,97
Cv 97,98,99
Mp 124,44,184
)" R"(
Rb 28,10,3072
s_waitcnt lgkmcnt(8)
Mp 116,44,176
Cv 98,100,101
Cv 99,102,103
Mp 116,48,192
Rb 32,10,4096
v_and_b32 v215, 0xffff0000, v191
Mp 124,48,200
s_add_u32 s29, s73, 0x1800
buffer_store_dwordx4 v[96:99], v1, s[20:23], s29 offen sc0 sc1
Mp 124,52,216
Rb 36,10,5120
v_mul_f32_e32 v214, v172, v214
Mp 116,52,208
v_mul_f32_e32 v215, v172, v215
Cv 215,214,215
Mp 116,56,224
Rb 40,10,6144
v_lshlrev_b32 v214, 16, v215
Mp 124,56,232
v_and_b32 v215, 0xffff0000, v215
v_lshlrev_b32 v220, 16, v196
Mp 124,60,248
Rb 44,10,8192
s_waitcnt vmcnt(8) lgkmcnt(10)
Mp 116,60,240
Cv 104,104,105
Cv 105,106,107
Mv 112,194,96
Cv 106,108,109
Cv 107,110,111
Mv 120,194,100
s_add_u32 s29, s73, 0x4000
buffer_store_dwordx4 v[104:107], v1, s[20:23], s29 offen sc0 sc1
Mv 116,192,96
v_and_b32 v221, 0xffff0000, v196
v_mul_f32_e32 v220, v172, v220
Mv 124,192,100
v_mul_f32_e32 v221, v172, v221
Cv 221,220,221
v_mfma_f32_16x16x32_bf16 v[32:35], a[0:3], v[128:131], 0
Rb 48,10,9216
v_lshlrev_b32 v220, 16, v221
v_mfma_f32_16x16x32_bf16 v[40:43], a[0:3], v[136:139], 0
v_and_b32 v221, 0xffff0000, v221
v_lshlrev_b32 v222, 16, v197
Mp 40,4,152
Rb 52,10,10240
s_waitcnt lgkmcnt(8)
Mp 32,4,144
Cv 112,112,113
Cv 113,114,115
Mp 32,8,160
Rb 56,10,11264
Cv 114,116,117
Mp 40,8,168
Cv 115,118,119
v_and_b32 v223, 0xffff0000, v197
Mp 40,12,184
Rb 60,10,12288
v_mul_f32_e32 v222, v172, v222
Mp 32,12,176
s_add_u32 s29, s73, 0x4400
)" R"(
buffer_store_dwordx4 v[112:115], v1, s[20:23], s29 offen sc0 sc1
Mp 32,16,192
Rb 0,10,13312
v_mul_f32_e32 v223, v172, v223
Mp 40,16,200
Cv 223,222,223
v_lshlrev_b32 v222, 16, v223
Mp 40,20,216
Rb 4,10,14336
s_waitcnt lgkmcnt(8)
Mp 32,20,208
v_and_b32 v223, 0xffff0000, v223
v_lshlrev_b32 v228, 16, v198
Mp 32,24,224
Rb 8,10,16384
v_and_b32 v229, 0xffff0000, v198
Mp 40,24,232
Cv 120,120,121
Cv 121,122,123
Mp 40,28,248
Rb 12,10,17408
Cv 122,124,125
Mp 32,28,240
Cv 123,126,127
v_mul_f32_e32 v228, v172, v228
v_mfma_f32_16x16x32_bf16 v[36:39], a[32:35], v[128:131], 0
Rb 16,10,18432
v_mul_f32_e32 v229, v172, v229
v_mfma_f32_16x16x32_bf16 v[44:47], a[32:35], v[136:139], 0
s_add_u32 s29, s73, 0x4800
buffer_store_dwordx4 v[120:123], v1, s[20:23], s29 offen sc0 sc1
Mp 44,36,152
Rb 20,10,19456
s_waitcnt lgkmcnt(8)
Mp 36,36,144
Cv 229,228,229
v_lshlrev_b32 v228, 16, v229
Mp 36,40,160
Rb 24,10,20480
v_and_b32 v229, 0xffff0000, v229
Mp 44,40,168
v_lshlrev_b32 v230, 16, v199
v_and_b32 v231, 0xffff0000, v199
Mp 44,44,184
Rb 28,10,21504
v_mul_f32_e32 v230, v172, v230
Mp 36,44,176
v_mul_f32_e32 v231, v172, v231
Cv 231,230,231
Mp 36,48,192
Rb 32,10,22528
v_lshlrev_b32 v230, 16, v231
Mp 44,48,200
v_and_b32 v231, 0xffff0000, v231
Mp 44,52,216
Rb 36,10,24576
s_waitcnt vmcnt(3) lgkmcnt(10)
buffer_store_dword v6, v7, s[20:23], s68 offen offset:0 sc0 sc1
Mp 36,52,208
Mp 36,56,224
Rb 40,10,25600
Mp 44,56,232
Mp 44,60,248
Rb 44,10,26624
)" R"(
Mp 36,60,240
Mv 32,194,104
Mv 40,194,108
Mv 36,192,104
s_waitcnt lgkmcnt(8)
Mv 44,192,108
v_mfma_f32_16x16x32_bf16 v[48:51], a[0:3], v[128:131], 0
Rb 48,10,27648
v_mfma_f32_16x16x32_bf16 v[56:59], a[0:3], v[136:139], 0
Mp 56,4,152
Rb 52,10,28672
Mp 48,4,144
Cv 32,32,33
Cv 33,34,35
Mp 48,8,160
Rb 56,10,29696
Cv 34,36,37
Mp 56,8,168
Cv 35,38,39
Mp 56,12,184
Rb 60,10,30720
s_waitcnt lgkmcnt(8)
Mp 48,12,176
s_add_u32 s29, s73, 0x4c00
buffer_store_dwordx4 v[32:35], v1, s[20:23], s29 offen sc0 sc1
Mp 48,16,192
Mp 56,16,200
Mp 56,20,216
Mp 48,20,208
Mp 48,24,224
Mp 56,24,232
Cv 40,40,41
Cv 41,42,43
buffer_store_dword v6, v7, s[20:23], s68 offen offset:128 sc0 sc1
Mp 56,28,248
Cv 42,44,45
s_waitcnt lgkmcnt(4)
Mp 48,28,240
Cv 43,46,47
v_mfma_f32_16x16x32_bf16 v[52:55], a[32:35], v[128:131], 0
s_add_u32 s29, s73, 0x5000
buffer_store_dwordx4 v[40:43], v1, s[20:23], s29 offen sc0 sc1
v_mfma_f32_16x16x32_bf16 v[60:63], a[32:35], v[136:139], 0
buffer_load_dword v4, v165, s[20:23], s40 offen sc0 sc1
Mp 60,36,152
Mp 52,36,144
Mp 52,40,160
Mp 60,40,168
Mp 60,44,184
s_waitcnt lgkmcnt(0)
Mp 52,44,176
Mp 52,48,192
Mp 60,48,200
Mp 60,52,216
Mp 52,52,208
Mp 52,56,224
Mp 60,56,232
Mp 60,60,248
Mp 52,60,240
Mv 48,194,112
Mv 56,194,116
Mv 52,192,112
Mv 60,192,116
s_nop 7
)" R"(
Cv 48,48,49
Cv 49,50,51
Cv 50,52,53
Cv 51,54,55
s_add_u32 s29, s73, 0x5400
buffer_store_dwordx4 v[48:51], v1, s[20:23], s29 offen sc0 sc1
Cv 56,56,57
Cv 57,58,59
Cv 58,60,61
Cv 59,62,63
s_add_u32 s29, s73, 0x5800
buffer_store_dwordx4 v[56:59], v1, s[20:23], s29 offen sc0 sc1
.LzH19:
s_cbranch_execz .LzHR
.LzEND:
s_cmp_gt_u32 s82, 223
s_cbranch_scc1 .LzFIN
s_cmp_lt_u32 s82, 14
s_cbranch_scc1 .LzBB
.LzBBR:
s_waitcnt vmcnt(2)
.LzS0:
v_cmp_ne_u32_e32 vcc, s3, v4
s_lshr_b32 s28, vcc_lo, 1
s_or_b32 s28, s28, vcc_lo
s_lshr_b32 s29, s28, 2
s_or_b32 s28, s28, s29
s_andn2_b32 s28, s93, s28
s_sub_u32 s53, s53, 1
s_cselect_b32 s28, s93, s28
s_cmov_b32 s53, 0
s_xor_b32 s93, s93, s28
s_or_b32 s57, s57, s28
s_cbranch_scc1 .LzS2
s_sleep 4
s_branch .LzSk3
.LzS2:
s_bitcmp0_b32 s57, 0
s_cbranch_scc1 .LzSk0
s_add_u32 s29, s55, 0x0
buffer_load_dwordx4 a[128:131], v164, s[20:23], s29 offen offset:0 sc0 sc1
buffer_load_dwordx4 a[132:135], v164, s[20:23], s29 offen offset:256 sc0 sc1
buffer_load_dwordx4 a[136:139], v164, s[20:23], s29 offen offset:512 sc0 sc1
buffer_load_dwordx4 a[140:143], v164, s[20:23], s29 offen offset:768 sc0 sc1
.LzSk0:
s_bitcmp0_b32 s57, 4
s_cbranch_scc1 .LzSk1
s_add_u32 s29, s55, 0x40000
.LzH20:
s_cbranch_execz .LzHR
buffer_load_dwordx4 a[144:147], v183, s[20:23], s29 offen offset:0 sc0 sc1
buffer_load_dwordx4 a[148:151], v183, s[20:23], s29 offen offset:256 sc0 sc1
buffer_load_dwordx4 a[152:155], v183, s[20:23], s29 offen offset:512 sc0 sc1
buffer_load_dwordx4 a[156:159], v183, s[20:23], s29 offen offset:768 sc0 sc1
.LzSk1:
s_bitcmp0_b32 s57, 8
s_cbranch_scc1 .LzSk2
s_add_u32 s29, s55, 0x8000
buffer_load_dwordx4 a[160:163], v164, s[20:23], s29 offen offset:0 sc0 sc1
buffer_load_dwordx4 a[164:167], v164, s[20:23], s29 offen offset:256 sc0 sc1
buffer_load_dwordx4 a[168:171], v164, s[20:23], s29 offen offset:512 sc0 sc1
buffer_load_dwordx4 a[172:175], v164, s[20:23], s29 offen offset:768 sc0 sc1
.LzSk2:
s_bitcmp0_b32 s57, 12
)" R"(
s_cbranch_scc1 .LzSk3
s_add_u32 s29, s55, 0x48000
buffer_load_dwordx4 a[176:179], v183, s[20:23], s29 offen offset:0 sc0 sc1
buffer_load_dwordx4 a[180:183], v183, s[20:23], s29 offen offset:256 sc0 sc1
buffer_load_dwordx4 a[184:187], v183, s[20:23], s29 offen offset:512 sc0 sc1
buffer_load_dwordx4 a[188:191], v183, s[20:23], s29 offen offset:768 sc0 sc1
.LzSk3:
s_cmp_eq_u32 s93, 0
s_cbranch_scc1 .LzS1
buffer_load_dword v4, v165, s[20:23], s40 offen sc0 sc1
.LzS1:
s_waitcnt vmcnt(0)
s_bitcmp0_b32 s57, 0
s_cbranch_scc1 .LzSv0
v_accvgpr_read_b32 v173, a131
v_accvgpr_read_b32 v174, a135
v_accvgpr_read_b32 v175, a139
v_accvgpr_read_b32 v180, a143
v_max3_u32 v173, v173, v174, v175
v_max_u32 v173, v173, v180
v_cmp_eq_u32_e32 vcc, -1, v173
s_cbranch_vccnz .LzSv0
s_bitset0_b32 s57, 0
.LzSv0:
s_bitcmp0_b32 s57, 4
s_cbranch_scc1 .LzSv1
v_accvgpr_read_b32 v173, a147
v_accvgpr_read_b32 v174, a151
v_accvgpr_read_b32 v175, a155
v_accvgpr_read_b32 v180, a159
v_max3_u32 v173, v173, v174, v175
v_max_u32 v173, v173, v180
v_cmp_eq_u32_e32 vcc, -1, v173
s_cbranch_vccnz .LzSv1
s_bitset0_b32 s57, 4
.LzSv1:
s_bitcmp0_b32 s57, 8
s_cbranch_scc1 .LzSv2
v_accvgpr_read_b32 v173, a163
v_accvgpr_read_b32 v174, a167
v_accvgpr_read_b32 v175, a171
v_accvgpr_read_b32 v180, a175
v_max3_u32 v173, v173, v174, v175
v_max_u32 v173, v173, v180
v_cmp_eq_u32_e32 vcc, -1, v173
s_cbranch_vccnz .LzSv2
s_bitset0_b32 s57, 8
.LzSv2:
s_bitcmp0_b32 s57, 12
s_cbranch_scc1 .LzSv3
v_accvgpr_read_b32 v173, a179
v_accvgpr_read_b32 v174, a183
v_accvgpr_read_b32 v175, a187
v_accvgpr_read_b32 v180, a191
v_max3_u32 v173, v173, v174, v175
v_max_u32 v173, v173, v180
v_cmp_eq_u32_e32 vcc, -1, v173
s_cbranch_vccnz .LzSv3
s_bitset0_b32 s57, 12
.LzSv3:
s_cmp_eq_u32 s53, 0
s_cmov_b32 s57, 0
s_or_b32 s28, s57, s93
s_cbranch_scc1 .LzS0
)" R"(
s_bfe_u32 s28, s63, 0x70004
s_cmp_eq_u32 s28, s58
s_cbranch_scc1 .LzRT
s_bfe_u32 s28, s63, 0x70014
s_cmp_eq_u32 s28, s58
s_cbranch_scc1 .LzRT
.LzRT3:
v_mfma_f32_16x16x32_bf16 v[80:83], v[132:135], a[128:131], 0
v_mfma_f32_16x16x32_bf16 v[88:91], v[132:135], a[160:163], 0
v_mfma_f32_16x16x32_bf16 v[84:87], v[132:135], a[136:139], 0
v_mfma_f32_16x16x32_bf16 v[92:95], v[132:135], a[168:171], 0
v_mfma_f32_16x16x32_bf16 v[80:83], v[140:143], a[132:135], v[80:83]
v_mfma_f32_16x16x32_bf16 v[88:91], v[140:143], a[164:167], v[88:91]
v_mfma_f32_16x16x32_bf16 v[84:87], v[140:143], a[140:143], v[84:87]
v_mfma_f32_16x16x32_bf16 v[92:95], v[140:143], a[172:175], v[92:95]
v_mfma_f32_16x16x32_bf16 v[80:83], v[132:135], a[144:147], v[80:83]
v_mfma_f32_16x16x32_bf16 v[88:91], v[132:135], a[176:179], v[88:91]
v_mfma_f32_16x16x32_bf16 v[84:87], v[132:135], a[152:155], v[84:87]
v_mfma_f32_16x16x32_bf16 v[92:95], v[132:135], a[184:187], v[92:95]
v_mfma_f32_16x16x32_bf16 v[80:83], v[140:143], a[148:151], v[80:83]
v_mfma_f32_16x16x32_bf16 v[88:91], v[140:143], a[180:183], v[88:91]
v_mfma_f32_16x16x32_bf16 v[84:87], v[140:143], a[156:159], v[84:87]
v_mfma_f32_16x16x32_bf16 v[92:95], v[140:143], a[188:191], v[92:95]
v_lshrrev_b32 v173, 4, v0
v_and_b32 v173, 1, v173
v_mad_u32_u24 v166, v173, 24, v166
s_nop 1
Cv 81,80,81
v_lshlrev_b32 v80, 16, v81
v_and_b32 v81, 0xffff0000, v81
Cv 83,82,83
v_lshlrev_b32 v82, 16, v83
v_and_b32 v83, 0xffff0000, v83
Cv 89,88,89
v_lshlrev_b32 v88, 16, v89
v_and_b32 v89, 0xffff0000, v89
Cv 91,90,91
v_lshlrev_b32 v90, 16, v91
v_and_b32 v91, 0xffff0000, v91
Cv 85,84,85
v_lshlrev_b32 v84, 16, v85
v_and_b32 v85, 0xffff0000, v85
Cv 87,86,87
v_lshlrev_b32 v86, 16, v87
v_and_b32 v87, 0xffff0000, v87
Cv 93,92,93
v_lshlrev_b32 v92, 16, v93
v_and_b32 v93, 0xffff0000, v93
Cv 95,94,95
v_lshlrev_b32 v94, 16, v95
v_and_b32 v95, 0xffff0000, v95
v_mul_f32_e32 v80, v167, v80
v_mul_f32_e32 v81, v167, v81
v_mul_f32_e32 v82, v167, v82
v_mul_f32_e32 v83, v167, v83
v_mul_f32_e32 v88, v167, v88
v_mul_f32_e32 v89, v167, v89
v_mul_f32_e32 v90, v167, v90
v_mul_f32_e32 v91, v167, v91
v_mul_f32_e32 v84, v167, v84
v_mul_f32_e32 v85, v167, v85
v_mul_f32_e32 v86, v167, v86
v_mul_f32_e32 v87, v167, v87
v_mul_f32_e32 v92, v167, v92
)" R"(
v_mul_f32_e32 v93, v167, v93
v_mul_f32_e32 v94, v167, v94
v_mul_f32_e32 v95, v167, v95
Cv 81,80,81
v_lshlrev_b32 v80, 16, v81
v_and_b32 v81, 0xffff0000, v81
Cv 83,82,83
v_lshlrev_b32 v82, 16, v83
v_and_b32 v83, 0xffff0000, v83
Cv 89,88,89
v_lshlrev_b32 v88, 16, v89
v_and_b32 v89, 0xffff0000, v89
Cv 91,90,91
v_lshlrev_b32 v90, 16, v91
v_and_b32 v91, 0xffff0000, v91
Cv 85,84,85
v_lshlrev_b32 v84, 16, v85
v_and_b32 v85, 0xffff0000, v85
Cv 87,86,87
v_lshlrev_b32 v86, 16, v87
v_and_b32 v87, 0xffff0000, v87
Cv 93,92,93
v_lshlrev_b32 v92, 16, v93
v_and_b32 v93, 0xffff0000, v93
Cv 95,94,95
v_lshlrev_b32 v94, 16, v95
v_and_b32 v95, 0xffff0000, v95
v_add_f32_e32 v80, v80, v204
v_add_f32_e32 v81, v81, v205
v_add_f32_e32 v82, v82, v206
v_add_f32_e32 v83, v83, v207
v_add_f32_e32 v88, v88, v220
v_add_f32_e32 v89, v89, v221
v_add_f32_e32 v90, v90, v222
v_add_f32_e32 v91, v91, v223
v_add_f32_e32 v84, v84, v212
v_add_f32_e32 v85, v85, v213
v_add_f32_e32 v86, v86, v214
v_add_f32_e32 v87, v87, v215
v_add_f32_e32 v92, v92, v228
v_add_f32_e32 v93, v93, v229
v_add_f32_e32 v94, v94, v230
v_add_f32_e32 v95, v95, v231
Cv 64,80,81
Cv 65,82,83
Cv 66,84,85
Cv 67,86,87
Cv 68,88,89
Cv 69,90,91
Cv 70,92,93
Cv 71,94,95
v_permlane16_swap_b32_e32 v64, v66
v_permlane16_swap_b32_e32 v65, v67
buffer_store_dwordx4 v[64:67], v166, s[8:11], 0 offen offset:0 sc0 sc1
v_permlane16_swap_b32_e32 v68, v70
v_permlane16_swap_b32_e32 v69, v71
buffer_store_dwordx4 v[68:71], v166, s[8:11], 0 offen offset:448 sc0 sc1
s_or_b32 s28, s82, s33
s_cbranch_scc1 .LzFIN
s_mov_b32 s28, 0xc108400
buffer_store_dword v6, v7, s[20:23], s28 offen offset:0 sc0 sc1
.LzFIN:
s_endpgm
.LzRT:
)" R"(
s_and_b32 s28, s63, 0x7ff
s_mul_i32 s28, s28, 896
v_subrev_u32 v174, s28, v166
s_bfe_u32 s28, s63, 0xb0010
s_mul_i32 s28, s28, 896
v_subrev_u32 v175, s28, v166
s_movk_i32 s28, 896
v_cmp_gt_u32_e64 s[24:25], s28, v175
v_cmp_gt_u32_e32 vcc, s28, v174
v_add_u32 v175, s28, v175
s_or_b64 s[24:25], s[24:25], vcc
v_cndmask_b32_e32 v174, v175, v174, vcc
v_lshrrev_b32 v174, 5, v174
v_lshlrev_b32 v174, 7, v174
v_and_or_b32 v174, v0, 48, v174
s_mov_b64 exec, s[24:25]
s_mov_b32 s29, 0xc109000
.LzRT1:
buffer_load_dwordx4 v[64:67], v174, s[20:23], s29 offen offset:0 sc0 sc1
buffer_load_dwordx4 v[68:71], v174, s[20:23], s29 offen offset:128 sc0 sc1
buffer_load_dwordx4 v[72:75], v174, s[20:23], s29 offen offset:1792 sc0 sc1
buffer_load_dwordx4 v[76:79], v174, s[20:23], s29 offen offset:1920 sc0 sc1
s_waitcnt vmcnt(0)
v_xor_b32 v175, s3, v65
v_xor_b32 v173, s3, v67
v_or_b32 v175, v175, v173
v_xor_b32 v173, s3, v69
v_or_b32 v175, v175, v173
v_xor_b32 v173, s3, v71
v_or_b32 v175, v175, v173
v_xor_b32 v173, s3, v73
v_or_b32 v175, v175, v173
v_xor_b32 v173, s3, v75
v_or_b32 v175, v175, v173
v_xor_b32 v173, s3, v77
v_or_b32 v175, v175, v173
v_xor_b32 v173, s3, v79
v_or_b32 v175, v175, v173
v_cmp_ne_u32_e32 vcc, 0, v175
s_cbranch_vccz .LzRT2
s_sleep 2
s_sub_u32 s53, s53, 1
s_cbranch_scc0 .LzRT1
s_mov_b32 s53, 0
.LzRT2:
v_mov_b32 v188, v64
v_mov_b32 v189, v66
v_mov_b32 v190, v68
v_mov_b32 v191, v70
v_mov_b32 v196, v72
v_mov_b32 v197, v74
v_mov_b32 v198, v76
v_mov_b32 v199, v78
v_sub_f32_e32 v172, 1.0, v167
Cv 172,172,172
v_lshlrev_b32 v172, 16, v172
v_lshlrev_b32 v204, 16, v188
v_and_b32 v205, 0xffff0000, v188
v_mul_f32_e32 v204, v172, v204
v_mul_f32_e32 v205, v172, v205
Cv 205,204,205
v_lshlrev_b32 v204, 16, v205
v_and_b32 v205, 0xffff0000, v205
v_lshlrev_b32 v206, 16, v189
)" R"(
v_and_b32 v207, 0xffff0000, v189
v_mul_f32_e32 v206, v172, v206
v_mul_f32_e32 v207, v172, v207
Cv 207,206,207
v_lshlrev_b32 v206, 16, v207
v_and_b32 v207, 0xffff0000, v207
v_lshlrev_b32 v212, 16, v190
v_and_b32 v213, 0xffff0000, v190
v_mul_f32_e32 v212, v172, v212
v_mul_f32_e32 v213, v172, v213
Cv 213,212,213
v_lshlrev_b32 v212, 16, v213
v_and_b32 v213, 0xffff0000, v213
v_lshlrev_b32 v214, 16, v191
v_and_b32 v215, 0xffff0000, v191
v_mul_f32_e32 v214, v172, v214
v_mul_f32_e32 v215, v172, v215
Cv 215,214,215
v_lshlrev_b32 v214, 16, v215
v_and_b32 v215, 0xffff0000, v215
v_lshlrev_b32 v220, 16, v196
v_and_b32 v221, 0xffff0000, v196
v_mul_f32_e32 v220, v172, v220
v_mul_f32_e32 v221, v172, v221
Cv 221,220,221
v_lshlrev_b32 v220, 16, v221
v_and_b32 v221, 0xffff0000, v221
v_lshlrev_b32 v222, 16, v197
v_and_b32 v223, 0xffff0000, v197
v_mul_f32_e32 v222, v172, v222
v_mul_f32_e32 v223, v172, v223
Cv 223,222,223
v_lshlrev_b32 v222, 16, v223
v_and_b32 v223, 0xffff0000, v223
v_lshlrev_b32 v228, 16, v198
v_and_b32 v229, 0xffff0000, v198
v_mul_f32_e32 v228, v172, v228
v_mul_f32_e32 v229, v172, v229
Cv 229,228,229
v_lshlrev_b32 v228, 16, v229
v_and_b32 v229, 0xffff0000, v229
v_lshlrev_b32 v230, 16, v199
v_and_b32 v231, 0xffff0000, v199
v_mul_f32_e32 v230, v172, v230
v_mul_f32_e32 v231, v172, v231
Cv 231,230,231
v_lshlrev_b32 v230, 16, v231
v_and_b32 v231, 0xffff0000, v231
s_mov_b64 exec, -1
s_branch .LzRT3
.LzH21:
s_cbranch_execz .LzH38
.LzMASK0:
s_lshl1_add_u32 s28, s57, s33
s_mov_b32 s29, 0xff800000
s_cmp_gt_u32 s28, 5
s_cbranch_scc1 .LzMASKE0
s_cmp_eq_u32 s28, 5
s_cbranch_scc1 .LzMASKB0
v_mov_b32 v72, s29
v_mov_b32 v73, s29
v_mov_b32 v74, s29
v_mov_b32 v75, s29
v_mov_b32 v80, s29
)" R"(
v_mov_b32 v81, s29
v_mov_b32 v82, s29
v_mov_b32 v83, s29
v_mov_b32 v84, s29
v_mov_b32 v85, s29
v_mov_b32 v86, s29
v_mov_b32 v87, s29
v_mov_b32 v88, s29
v_mov_b32 v89, s29
v_mov_b32 v90, s29
v_mov_b32 v91, s29
v_mov_b32 v92, s29
v_mov_b32 v93, s29
v_mov_b32 v94, s29
v_mov_b32 v95, s29
s_cmp_eq_u32 s28, 4
s_cbranch_scc1 .LzMASKA0
v_mov_b32 v64, s29
v_mov_b32 v65, s29
v_mov_b32 v66, s29
v_mov_b32 v67, s29
v_mov_b32 v68, s29
v_mov_b32 v69, s29
v_mov_b32 v70, s29
v_mov_b32 v71, s29
v_mov_b32 v76, s29
v_mov_b32 v77, s29
v_mov_b32 v78, s29
v_mov_b32 v79, s29
.LzMASKE0:
s_setpc_b64 s[30:31]
.LzMASKA0:
s_mov_b32 exec_lo, 0xf0000
s_mov_b32 exec_hi, 0xfff00ff
v_mov_b32 v64, s29
v_mov_b32 v76, s29
s_mov_b32 exec_lo, 0x1f0001
s_mov_b32 exec_hi, 0x1fff01ff
v_mov_b32 v65, s29
v_mov_b32 v77, s29
s_mov_b32 exec_lo, 0x3f0003
s_mov_b32 exec_hi, 0x3fff03ff
v_mov_b32 v66, s29
v_mov_b32 v78, s29
s_mov_b32 exec_lo, 0x7f0007
s_mov_b32 exec_hi, 0x7fff07ff
v_mov_b32 v67, s29
v_mov_b32 v79, s29
s_mov_b64 exec, -1
s_setpc_b64 s[30:31]
.LzMASKB0:
s_nop 1
v_mov_b32 v88, s29
v_mov_b32 v89, s29
v_mov_b32 v90, s29
v_mov_b32 v91, s29
s_mov_b32 exec_lo, 0xf0000
s_mov_b32 exec_hi, 0xfff00ff
v_mov_b32 v80, s29
v_mov_b32 v92, s29
s_mov_b32 exec_lo, 0x1f0001
s_mov_b32 exec_hi, 0x1fff01ff
v_mov_b32 v81, s29
v_mov_b32 v93, s29
)" R"(
s_mov_b32 exec_lo, 0x3f0003
s_mov_b32 exec_hi, 0x3fff03ff
v_mov_b32 v82, s29
v_mov_b32 v94, s29
s_mov_b32 exec_lo, 0x7f0007
s_mov_b32 exec_hi, 0x7fff07ff
v_mov_b32 v83, s29
v_mov_b32 v95, s29
s_mov_b64 exec, -1
s_setpc_b64 s[30:31]
.LzFIX0:
s_nop 7
v_max_f32_e32 v28, v64, v65
v_max3_f32 v28, v28, v66, v67
v_max3_f32 v28, v28, v72, v73
v_max3_f32 v28, v28, v74, v75
v_max3_f32 v28, v28, v80, v81
v_max3_f32 v28, v28, v82, v83
v_max3_f32 v28, v28, v88, v89
v_max3_f32 v28, v28, v90, v91
v_mov_b32 v24, v28
s_nop 1
v_permlane32_swap_b32_e32 v28, v24
v_max_f32_e32 v28, v28, v24
v_mov_b32 v24, v28
s_nop 1
v_permlane16_swap_b32_e32 v28, v24
v_max_f32_e32 v28, v28, v24
v_max_f32_e32 v29, v68, v69
v_max3_f32 v29, v29, v70, v71
v_max3_f32 v29, v29, v76, v77
v_max3_f32 v29, v29, v78, v79
v_max3_f32 v29, v29, v84, v85
v_max3_f32 v29, v29, v86, v87
v_max3_f32 v29, v29, v92, v93
v_max3_f32 v29, v29, v94, v95
v_mov_b32 v24, v29
s_nop 1
v_permlane32_swap_b32_e32 v29, v24
v_max_f32_e32 v29, v29, v24
v_mov_b32 v24, v29
s_nop 1
v_permlane16_swap_b32_e32 v29, v24
v_max_f32_e32 v29, v29, v24
s_bitcmp1_b32 s88, 0
s_cbranch_scc1 .LzFIXF0
v_max_f32_e32 v28, 0, v28
v_sub_f32_e32 v24, 0, v28
v_exp_f32_e32 v24, v24
s_nop 0
v_mul_f32_e32 v12, v24, v12
v_mul_f32_e32 v128, v24, v128
v_mul_f32_e32 v129, v24, v129
v_mul_f32_e32 v130, v24, v130
v_mul_f32_e32 v131, v24, v131
v_mul_f32_e32 v132, v24, v132
v_mul_f32_e32 v133, v24, v133
v_mul_f32_e32 v134, v24, v134
v_mul_f32_e32 v135, v24, v135
v_mul_f32_e32 v144, v24, v144
v_mul_f32_e32 v145, v24, v145
v_mul_f32_e32 v146, v24, v146
v_mul_f32_e32 v147, v24, v147
v_mul_f32_e32 v148, v24, v148
)" R"(
v_mul_f32_e32 v149, v24, v149
v_mul_f32_e32 v150, v24, v150
v_mul_f32_e32 v151, v24, v151
v_mul_f32_e32 v160, v24, v160
v_mul_f32_e32 v161, v24, v161
v_mul_f32_e32 v162, v24, v162
v_mul_f32_e32 v163, v24, v163
v_mul_f32_e32 v164, v24, v164
v_mul_f32_e32 v165, v24, v165
v_mul_f32_e32 v166, v24, v166
v_mul_f32_e32 v167, v24, v167
v_mul_f32_e32 v176, v24, v176
v_mul_f32_e32 v177, v24, v177
v_mul_f32_e32 v178, v24, v178
v_mul_f32_e32 v179, v24, v179
v_mul_f32_e32 v180, v24, v180
v_mul_f32_e32 v181, v24, v181
v_mul_f32_e32 v182, v24, v182
v_mul_f32_e32 v183, v24, v183
v_mul_f32_e32 v192, v24, v192
v_mul_f32_e32 v193, v24, v193
v_mul_f32_e32 v194, v24, v194
v_mul_f32_e32 v195, v24, v195
v_mul_f32_e32 v196, v24, v196
v_mul_f32_e32 v197, v24, v197
v_mul_f32_e32 v198, v24, v198
v_mul_f32_e32 v199, v24, v199
v_mul_f32_e32 v208, v24, v208
v_mul_f32_e32 v209, v24, v209
v_mul_f32_e32 v210, v24, v210
v_mul_f32_e32 v211, v24, v211
v_mul_f32_e32 v212, v24, v212
v_mul_f32_e32 v213, v24, v213
v_mul_f32_e32 v214, v24, v214
v_mul_f32_e32 v215, v24, v215
v_mul_f32_e32 v224, v24, v224
v_mul_f32_e32 v225, v24, v225
v_mul_f32_e32 v226, v24, v226
v_mul_f32_e32 v227, v24, v227
v_mul_f32_e32 v228, v24, v228
v_mul_f32_e32 v229, v24, v229
v_mul_f32_e32 v230, v24, v230
v_mul_f32_e32 v231, v24, v231
v_mul_f32_e32 v240, v24, v240
v_mul_f32_e32 v241, v24, v241
v_mul_f32_e32 v242, v24, v242
v_mul_f32_e32 v243, v24, v243
v_mul_f32_e32 v244, v24, v244
v_mul_f32_e32 v245, v24, v245
v_mul_f32_e32 v246, v24, v246
v_mul_f32_e32 v247, v24, v247
v_max_f32_e32 v29, 0, v29
v_sub_f32_e32 v24, 0, v29
v_exp_f32_e32 v24, v24
s_nop 0
v_mul_f32_e32 v13, v24, v13
v_mul_f32_e32 v136, v24, v136
v_mul_f32_e32 v137, v24, v137
v_mul_f32_e32 v138, v24, v138
v_mul_f32_e32 v139, v24, v139
v_mul_f32_e32 v140, v24, v140
v_mul_f32_e32 v141, v24, v141
v_mul_f32_e32 v142, v24, v142
v_mul_f32_e32 v143, v24, v143
)" R"(
v_mul_f32_e32 v152, v24, v152
v_mul_f32_e32 v153, v24, v153
v_mul_f32_e32 v154, v24, v154
v_mul_f32_e32 v155, v24, v155
v_mul_f32_e32 v156, v24, v156
v_mul_f32_e32 v157, v24, v157
v_mul_f32_e32 v158, v24, v158
v_mul_f32_e32 v159, v24, v159
v_mul_f32_e32 v168, v24, v168
v_mul_f32_e32 v169, v24, v169
v_mul_f32_e32 v170, v24, v170
v_mul_f32_e32 v171, v24, v171
v_mul_f32_e32 v172, v24, v172
v_mul_f32_e32 v173, v24, v173
v_mul_f32_e32 v174, v24, v174
v_mul_f32_e32 v175, v24, v175
v_mul_f32_e32 v184, v24, v184
v_mul_f32_e32 v185, v24, v185
v_mul_f32_e32 v186, v24, v186
v_mul_f32_e32 v187, v24, v187
v_mul_f32_e32 v188, v24, v188
v_mul_f32_e32 v189, v24, v189
v_mul_f32_e32 v190, v24, v190
v_mul_f32_e32 v191, v24, v191
v_mul_f32_e32 v200, v24, v200
v_mul_f32_e32 v201, v24, v201
v_mul_f32_e32 v202, v24, v202
v_mul_f32_e32 v203, v24, v203
v_mul_f32_e32 v204, v24, v204
v_mul_f32_e32 v205, v24, v205
v_mul_f32_e32 v206, v24, v206
v_mul_f32_e32 v207, v24, v207
v_mul_f32_e32 v216, v24, v216
v_mul_f32_e32 v217, v24, v217
v_mul_f32_e32 v218, v24, v218
v_mul_f32_e32 v219, v24, v219
v_mul_f32_e32 v220, v24, v220
v_mul_f32_e32 v221, v24, v221
v_mul_f32_e32 v222, v24, v222
v_mul_f32_e32 v223, v24, v223
v_mul_f32_e32 v232, v24, v232
v_mul_f32_e32 v233, v24, v233
v_mul_f32_e32 v234, v24, v234
v_mul_f32_e32 v235, v24, v235
v_mul_f32_e32 v236, v24, v236
v_mul_f32_e32 v237, v24, v237
v_mul_f32_e32 v238, v24, v238
v_mul_f32_e32 v239, v24, v239
v_mul_f32_e32 v248, v24, v248
v_mul_f32_e32 v249, v24, v249
v_mul_f32_e32 v250, v24, v250
v_mul_f32_e32 v251, v24, v251
v_mul_f32_e32 v252, v24, v252
v_mul_f32_e32 v253, v24, v253
v_mul_f32_e32 v254, v24, v254
v_mul_f32_e32 v255, v24, v255
s_branch .LzFIXG0
.LzFIXF0:
v_cmp_lt_f32_e32 vcc, 0xff7fffff, v28
s_nop 1
v_cndmask_b32_e32 v28, 0, v28, vcc
v_cmp_lt_f32_e32 vcc, 0xff7fffff, v29
s_nop 1
v_cndmask_b32_e32 v29, 0, v29, vcc
)" R"(
.LzFIXG0:
v_sub_f32_e32 v16, v16, v28
v_sub_f32_e32 v17, v17, v28
v_sub_f32_e32 v18, v18, v28
v_sub_f32_e32 v19, v19, v28
v_sub_f32_e32 v64, v64, v28
v_sub_f32_e32 v65, v65, v28
v_sub_f32_e32 v66, v66, v28
v_sub_f32_e32 v67, v67, v28
v_sub_f32_e32 v72, v72, v28
v_sub_f32_e32 v73, v73, v28
v_sub_f32_e32 v74, v74, v28
v_sub_f32_e32 v75, v75, v28
v_sub_f32_e32 v80, v80, v28
v_sub_f32_e32 v81, v81, v28
v_sub_f32_e32 v82, v82, v28
v_sub_f32_e32 v83, v83, v28
v_sub_f32_e32 v88, v88, v28
v_sub_f32_e32 v89, v89, v28
v_sub_f32_e32 v90, v90, v28
v_sub_f32_e32 v91, v91, v28
v_sub_f32_e32 v20, v20, v29
v_sub_f32_e32 v21, v21, v29
v_sub_f32_e32 v22, v22, v29
v_sub_f32_e32 v23, v23, v29
v_sub_f32_e32 v68, v68, v29
v_sub_f32_e32 v69, v69, v29
v_sub_f32_e32 v70, v70, v29
v_sub_f32_e32 v71, v71, v29
v_sub_f32_e32 v76, v76, v29
v_sub_f32_e32 v77, v77, v29
v_sub_f32_e32 v78, v78, v29
v_sub_f32_e32 v79, v79, v29
v_sub_f32_e32 v84, v84, v29
v_sub_f32_e32 v85, v85, v29
v_sub_f32_e32 v86, v86, v29
v_sub_f32_e32 v87, v87, v29
v_sub_f32_e32 v92, v92, v29
v_sub_f32_e32 v93, v93, v29
v_sub_f32_e32 v94, v94, v29
v_sub_f32_e32 v95, v95, v29
s_bitcmp0_b32 s88, 1
s_cbranch_scc1 .LzFIXH0
v_sub_f32_e32 v96, v96, v28
v_sub_f32_e32 v97, v97, v28
v_sub_f32_e32 v98, v98, v28
v_sub_f32_e32 v99, v99, v28
v_sub_f32_e32 v104, v104, v28
v_sub_f32_e32 v105, v105, v28
v_sub_f32_e32 v106, v106, v28
v_sub_f32_e32 v107, v107, v28
v_sub_f32_e32 v112, v112, v28
v_sub_f32_e32 v113, v113, v28
v_sub_f32_e32 v114, v114, v28
v_sub_f32_e32 v115, v115, v28
v_sub_f32_e32 v120, v120, v28
v_sub_f32_e32 v121, v121, v28
v_sub_f32_e32 v122, v122, v28
v_sub_f32_e32 v123, v123, v28
v_sub_f32_e32 v100, v100, v29
v_sub_f32_e32 v101, v101, v29
v_sub_f32_e32 v102, v102, v29
v_sub_f32_e32 v103, v103, v29
v_sub_f32_e32 v108, v108, v29
)" R"(
v_sub_f32_e32 v109, v109, v29
v_sub_f32_e32 v110, v110, v29
v_sub_f32_e32 v111, v111, v29
v_sub_f32_e32 v116, v116, v29
v_sub_f32_e32 v117, v117, v29
v_sub_f32_e32 v118, v118, v29
v_sub_f32_e32 v119, v119, v29
v_sub_f32_e32 v124, v124, v29
v_sub_f32_e32 v125, v125, v29
v_sub_f32_e32 v126, v126, v29
v_sub_f32_e32 v127, v127, v29
.LzFIXH0:
v_exp_f32_e32 v32, v64
v_exp_f32_e32 v28, v65
s_nop 0
v_add_f32_e32 v14, v32, v28
Cv 32,32,28
v_exp_f32_e32 v33, v66
v_exp_f32_e32 v29, v67
v_add_f32_e32 v14, v14, v33
v_add_f32_e32 v14, v14, v29
Cv 33,33,29
v_exp_f32_e32 v34, v72
v_exp_f32_e32 v24, v73
v_add_f32_e32 v14, v14, v34
v_add_f32_e32 v14, v14, v24
Cv 34,34,24
v_exp_f32_e32 v35, v74
v_exp_f32_e32 v25, v75
v_add_f32_e32 v14, v14, v35
v_add_f32_e32 v14, v14, v25
Cv 35,35,25
v_exp_f32_e32 v36, v68
v_exp_f32_e32 v26, v69
s_nop 0
v_add_f32_e32 v15, v36, v26
Cv 36,36,26
v_exp_f32_e32 v37, v70
v_exp_f32_e32 v27, v71
v_add_f32_e32 v15, v15, v37
v_add_f32_e32 v15, v15, v27
Cv 37,37,27
v_exp_f32_e32 v38, v76
v_exp_f32_e32 v28, v77
v_add_f32_e32 v15, v15, v38
v_add_f32_e32 v15, v15, v28
Cv 38,38,28
v_exp_f32_e32 v39, v78
v_exp_f32_e32 v29, v79
v_add_f32_e32 v15, v15, v39
v_add_f32_e32 v15, v15, v29
Cv 39,39,29
v_exp_f32_e32 v40, v80
v_exp_f32_e32 v24, v81
v_add_f32_e32 v14, v14, v40
v_add_f32_e32 v14, v14, v24
Cv 40,40,24
v_exp_f32_e32 v41, v82
v_exp_f32_e32 v25, v83
v_add_f32_e32 v14, v14, v41
v_add_f32_e32 v14, v14, v25
Cv 41,41,25
v_exp_f32_e32 v42, v88
v_exp_f32_e32 v26, v89
)" R"(
v_add_f32_e32 v14, v14, v42
v_add_f32_e32 v14, v14, v26
Cv 42,42,26
v_exp_f32_e32 v43, v90
v_exp_f32_e32 v27, v91
v_add_f32_e32 v14, v14, v43
v_add_f32_e32 v14, v14, v27
Cv 43,43,27
v_exp_f32_e32 v44, v84
v_exp_f32_e32 v28, v85
v_add_f32_e32 v15, v15, v44
v_add_f32_e32 v15, v15, v28
Cv 44,44,28
v_exp_f32_e32 v45, v86
v_exp_f32_e32 v29, v87
v_add_f32_e32 v15, v15, v45
v_add_f32_e32 v15, v15, v29
Cv 45,45,29
v_exp_f32_e32 v46, v92
v_exp_f32_e32 v24, v93
v_add_f32_e32 v15, v15, v46
v_add_f32_e32 v15, v15, v24
Cv 46,46,24
v_exp_f32_e32 v47, v94
v_exp_f32_e32 v25, v95
v_add_f32_e32 v15, v15, v47
v_add_f32_e32 v15, v15, v25
Cv 47,47,25
s_nop 1
s_setpc_b64 s[30:31]
.LzH22:
s_cbranch_execz .LzH39
.LzMASK1:
s_lshl1_add_u32 s28, s57, s33
s_mov_b32 s29, 0xff800000
s_cmp_gt_u32 s28, 5
s_cbranch_scc1 .LzMASKE1
s_cmp_eq_u32 s28, 5
s_cbranch_scc1 .LzMASKB1
v_mov_b32 v104, s29
v_mov_b32 v105, s29
v_mov_b32 v106, s29
v_mov_b32 v107, s29
v_mov_b32 v112, s29
v_mov_b32 v113, s29
v_mov_b32 v114, s29
v_mov_b32 v115, s29
v_mov_b32 v116, s29
v_mov_b32 v117, s29
v_mov_b32 v118, s29
v_mov_b32 v119, s29
v_mov_b32 v120, s29
v_mov_b32 v121, s29
v_mov_b32 v122, s29
v_mov_b32 v123, s29
v_mov_b32 v124, s29
v_mov_b32 v125, s29
v_mov_b32 v126, s29
v_mov_b32 v127, s29
s_cmp_eq_u32 s28, 4
s_cbranch_scc1 .LzMASKA1
v_mov_b32 v96, s29
v_mov_b32 v97, s29
v_mov_b32 v98, s29
)" R"(
v_mov_b32 v99, s29
v_mov_b32 v100, s29
v_mov_b32 v101, s29
v_mov_b32 v102, s29
v_mov_b32 v103, s29
v_mov_b32 v108, s29
v_mov_b32 v109, s29
v_mov_b32 v110, s29
v_mov_b32 v111, s29
.LzMASKE1:
s_setpc_b64 s[30:31]
.LzMASKA1:
s_mov_b32 exec_lo, 0xf0000
s_mov_b32 exec_hi, 0xfff00ff
v_mov_b32 v96, s29
v_mov_b32 v108, s29
s_mov_b32 exec_lo, 0x1f0001
s_mov_b32 exec_hi, 0x1fff01ff
v_mov_b32 v97, s29
v_mov_b32 v109, s29
s_mov_b32 exec_lo, 0x3f0003
s_mov_b32 exec_hi, 0x3fff03ff
v_mov_b32 v98, s29
v_mov_b32 v110, s29
s_mov_b32 exec_lo, 0x7f0007
s_mov_b32 exec_hi, 0x7fff07ff
v_mov_b32 v99, s29
v_mov_b32 v111, s29
s_mov_b64 exec, -1
s_setpc_b64 s[30:31]
.LzMASKB1:
s_nop 1
v_mov_b32 v120, s29
v_mov_b32 v121, s29
v_mov_b32 v122, s29
v_mov_b32 v123, s29
s_mov_b32 exec_lo, 0xf0000
s_mov_b32 exec_hi, 0xfff00ff
v_mov_b32 v112, s29
v_mov_b32 v124, s29
s_mov_b32 exec_lo, 0x1f0001
s_mov_b32 exec_hi, 0x1fff01ff
v_mov_b32 v113, s29
v_mov_b32 v125, s29
s_mov_b32 exec_lo, 0x3f0003
s_mov_b32 exec_hi, 0x3fff03ff
v_mov_b32 v114, s29
v_mov_b32 v126, s29
s_mov_b32 exec_lo, 0x7f0007
s_mov_b32 exec_hi, 0x7fff07ff
v_mov_b32 v115, s29
v_mov_b32 v127, s29
s_mov_b64 exec, -1
s_setpc_b64 s[30:31]
.LzFIX1:
s_nop 7
v_max_f32_e32 v28, v96, v97
v_max3_f32 v28, v28, v98, v99
v_max3_f32 v28, v28, v104, v105
v_max3_f32 v28, v28, v106, v107
v_max3_f32 v28, v28, v112, v113
v_max3_f32 v28, v28, v114, v115
v_max3_f32 v28, v28, v120, v121
v_max3_f32 v28, v28, v122, v123
)" R"(
v_mov_b32 v24, v28
s_nop 1
v_permlane32_swap_b32_e32 v28, v24
v_max_f32_e32 v28, v28, v24
v_mov_b32 v24, v28
s_nop 1
v_permlane16_swap_b32_e32 v28, v24
v_max_f32_e32 v28, v28, v24
v_max_f32_e32 v29, v100, v101
v_max3_f32 v29, v29, v102, v103
v_max3_f32 v29, v29, v108, v109
v_max3_f32 v29, v29, v110, v111
v_max3_f32 v29, v29, v116, v117
v_max3_f32 v29, v29, v118, v119
v_max3_f32 v29, v29, v124, v125
v_max3_f32 v29, v29, v126, v127
v_mov_b32 v24, v29
s_nop 1
v_permlane32_swap_b32_e32 v29, v24
v_max_f32_e32 v29, v29, v24
v_mov_b32 v24, v29
s_nop 1
v_permlane16_swap_b32_e32 v29, v24
v_max_f32_e32 v29, v29, v24
s_bitcmp1_b32 s88, 0
s_cbranch_scc1 .LzFIXF1
v_max_f32_e32 v28, 0, v28
v_sub_f32_e32 v24, 0, v28
v_exp_f32_e32 v24, v24
s_nop 0
v_mul_f32_e32 v12, v24, v12
v_mul_f32_e32 v128, v24, v128
v_mul_f32_e32 v129, v24, v129
v_mul_f32_e32 v130, v24, v130
v_mul_f32_e32 v131, v24, v131
v_mul_f32_e32 v132, v24, v132
v_mul_f32_e32 v133, v24, v133
v_mul_f32_e32 v134, v24, v134
v_mul_f32_e32 v135, v24, v135
v_mul_f32_e32 v144, v24, v144
v_mul_f32_e32 v145, v24, v145
v_mul_f32_e32 v146, v24, v146
v_mul_f32_e32 v147, v24, v147
v_mul_f32_e32 v148, v24, v148
v_mul_f32_e32 v149, v24, v149
v_mul_f32_e32 v150, v24, v150
v_mul_f32_e32 v151, v24, v151
v_mul_f32_e32 v160, v24, v160
v_mul_f32_e32 v161, v24, v161
v_mul_f32_e32 v162, v24, v162
v_mul_f32_e32 v163, v24, v163
v_mul_f32_e32 v164, v24, v164
v_mul_f32_e32 v165, v24, v165
v_mul_f32_e32 v166, v24, v166
v_mul_f32_e32 v167, v24, v167
v_mul_f32_e32 v176, v24, v176
v_mul_f32_e32 v177, v24, v177
v_mul_f32_e32 v178, v24, v178
v_mul_f32_e32 v179, v24, v179
v_mul_f32_e32 v180, v24, v180
v_mul_f32_e32 v181, v24, v181
v_mul_f32_e32 v182, v24, v182
v_mul_f32_e32 v183, v24, v183
v_mul_f32_e32 v192, v24, v192
)" R"(
v_mul_f32_e32 v193, v24, v193
v_mul_f32_e32 v194, v24, v194
v_mul_f32_e32 v195, v24, v195
v_mul_f32_e32 v196, v24, v196
v_mul_f32_e32 v197, v24, v197
v_mul_f32_e32 v198, v24, v198
v_mul_f32_e32 v199, v24, v199
v_mul_f32_e32 v208, v24, v208
v_mul_f32_e32 v209, v24, v209
v_mul_f32_e32 v210, v24, v210
v_mul_f32_e32 v211, v24, v211
v_mul_f32_e32 v212, v24, v212
v_mul_f32_e32 v213, v24, v213
v_mul_f32_e32 v214, v24, v214
v_mul_f32_e32 v215, v24, v215
v_mul_f32_e32 v224, v24, v224
v_mul_f32_e32 v225, v24, v225
v_mul_f32_e32 v226, v24, v226
v_mul_f32_e32 v227, v24, v227
v_mul_f32_e32 v228, v24, v228
v_mul_f32_e32 v229, v24, v229
v_mul_f32_e32 v230, v24, v230
v_mul_f32_e32 v231, v24, v231
v_mul_f32_e32 v240, v24, v240
v_mul_f32_e32 v241, v24, v241
v_mul_f32_e32 v242, v24, v242
v_mul_f32_e32 v243, v24, v243
v_mul_f32_e32 v244, v24, v244
v_mul_f32_e32 v245, v24, v245
v_mul_f32_e32 v246, v24, v246
v_mul_f32_e32 v247, v24, v247
v_max_f32_e32 v29, 0, v29
v_sub_f32_e32 v24, 0, v29
v_exp_f32_e32 v24, v24
s_nop 0
v_mul_f32_e32 v13, v24, v13
v_mul_f32_e32 v136, v24, v136
v_mul_f32_e32 v137, v24, v137
v_mul_f32_e32 v138, v24, v138
v_mul_f32_e32 v139, v24, v139
v_mul_f32_e32 v140, v24, v140
v_mul_f32_e32 v141, v24, v141
v_mul_f32_e32 v142, v24, v142
v_mul_f32_e32 v143, v24, v143
v_mul_f32_e32 v152, v24, v152
v_mul_f32_e32 v153, v24, v153
v_mul_f32_e32 v154, v24, v154
v_mul_f32_e32 v155, v24, v155
v_mul_f32_e32 v156, v24, v156
v_mul_f32_e32 v157, v24, v157
v_mul_f32_e32 v158, v24, v158
v_mul_f32_e32 v159, v24, v159
v_mul_f32_e32 v168, v24, v168
v_mul_f32_e32 v169, v24, v169
v_mul_f32_e32 v170, v24, v170
v_mul_f32_e32 v171, v24, v171
v_mul_f32_e32 v172, v24, v172
v_mul_f32_e32 v173, v24, v173
v_mul_f32_e32 v174, v24, v174
v_mul_f32_e32 v175, v24, v175
v_mul_f32_e32 v184, v24, v184
v_mul_f32_e32 v185, v24, v185
v_mul_f32_e32 v186, v24, v186
v_mul_f32_e32 v187, v24, v187
)" R"(
v_mul_f32_e32 v188, v24, v188
v_mul_f32_e32 v189, v24, v189
v_mul_f32_e32 v190, v24, v190
v_mul_f32_e32 v191, v24, v191
v_mul_f32_e32 v200, v24, v200
v_mul_f32_e32 v201, v24, v201
v_mul_f32_e32 v202, v24, v202
v_mul_f32_e32 v203, v24, v203
v_mul_f32_e32 v204, v24, v204
v_mul_f32_e32 v205, v24, v205
v_mul_f32_e32 v206, v24, v206
v_mul_f32_e32 v207, v24, v207
v_mul_f32_e32 v216, v24, v216
v_mul_f32_e32 v217, v24, v217
v_mul_f32_e32 v218, v24, v218
v_mul_f32_e32 v219, v24, v219
v_mul_f32_e32 v220, v24, v220
v_mul_f32_e32 v221, v24, v221
v_mul_f32_e32 v222, v24, v222
v_mul_f32_e32 v223, v24, v223
v_mul_f32_e32 v232, v24, v232
v_mul_f32_e32 v233, v24, v233
v_mul_f32_e32 v234, v24, v234
v_mul_f32_e32 v235, v24, v235
v_mul_f32_e32 v236, v24, v236
v_mul_f32_e32 v237, v24, v237
v_mul_f32_e32 v238, v24, v238
v_mul_f32_e32 v239, v24, v239
v_mul_f32_e32 v248, v24, v248
v_mul_f32_e32 v249, v24, v249
v_mul_f32_e32 v250, v24, v250
v_mul_f32_e32 v251, v24, v251
v_mul_f32_e32 v252, v24, v252
v_mul_f32_e32 v253, v24, v253
v_mul_f32_e32 v254, v24, v254
v_mul_f32_e32 v255, v24, v255
s_branch .LzFIXG1
.LzFIXF1:
v_cmp_lt_f32_e32 vcc, 0xff7fffff, v28
s_nop 1
v_cndmask_b32_e32 v28, 0, v28, vcc
v_cmp_lt_f32_e32 vcc, 0xff7fffff, v29
s_nop 1
v_cndmask_b32_e32 v29, 0, v29, vcc
.LzFIXG1:
v_sub_f32_e32 v16, v16, v28
v_sub_f32_e32 v17, v17, v28
v_sub_f32_e32 v18, v18, v28
v_sub_f32_e32 v19, v19, v28
v_sub_f32_e32 v96, v96, v28
v_sub_f32_e32 v97, v97, v28
v_sub_f32_e32 v98, v98, v28
v_sub_f32_e32 v99, v99, v28
v_sub_f32_e32 v104, v104, v28
v_sub_f32_e32 v105, v105, v28
v_sub_f32_e32 v106, v106, v28
v_sub_f32_e32 v107, v107, v28
v_sub_f32_e32 v112, v112, v28
v_sub_f32_e32 v113, v113, v28
v_sub_f32_e32 v114, v114, v28
v_sub_f32_e32 v115, v115, v28
v_sub_f32_e32 v120, v120, v28
v_sub_f32_e32 v121, v121, v28
v_sub_f32_e32 v122, v122, v28
)" R"(
v_sub_f32_e32 v123, v123, v28
v_sub_f32_e32 v20, v20, v29
v_sub_f32_e32 v21, v21, v29
v_sub_f32_e32 v22, v22, v29
v_sub_f32_e32 v23, v23, v29
v_sub_f32_e32 v100, v100, v29
v_sub_f32_e32 v101, v101, v29
v_sub_f32_e32 v102, v102, v29
v_sub_f32_e32 v103, v103, v29
v_sub_f32_e32 v108, v108, v29
v_sub_f32_e32 v109, v109, v29
v_sub_f32_e32 v110, v110, v29
v_sub_f32_e32 v111, v111, v29
v_sub_f32_e32 v116, v116, v29
v_sub_f32_e32 v117, v117, v29
v_sub_f32_e32 v118, v118, v29
v_sub_f32_e32 v119, v119, v29
v_sub_f32_e32 v124, v124, v29
v_sub_f32_e32 v125, v125, v29
v_sub_f32_e32 v126, v126, v29
v_sub_f32_e32 v127, v127, v29
s_bitcmp0_b32 s88, 1
s_cbranch_scc1 .LzFIXH1
v_sub_f32_e32 v64, v64, v28
v_sub_f32_e32 v65, v65, v28
v_sub_f32_e32 v66, v66, v28
v_sub_f32_e32 v67, v67, v28
v_sub_f32_e32 v72, v72, v28
v_sub_f32_e32 v73, v73, v28
v_sub_f32_e32 v74, v74, v28
v_sub_f32_e32 v75, v75, v28
v_sub_f32_e32 v80, v80, v28
v_sub_f32_e32 v81, v81, v28
v_sub_f32_e32 v82, v82, v28
v_sub_f32_e32 v83, v83, v28
v_sub_f32_e32 v88, v88, v28
v_sub_f32_e32 v89, v89, v28
v_sub_f32_e32 v90, v90, v28
v_sub_f32_e32 v91, v91, v28
v_sub_f32_e32 v68, v68, v29
v_sub_f32_e32 v69, v69, v29
v_sub_f32_e32 v70, v70, v29
v_sub_f32_e32 v71, v71, v29
v_sub_f32_e32 v76, v76, v29
v_sub_f32_e32 v77, v77, v29
v_sub_f32_e32 v78, v78, v29
v_sub_f32_e32 v79, v79, v29
v_sub_f32_e32 v84, v84, v29
v_sub_f32_e32 v85, v85, v29
v_sub_f32_e32 v86, v86, v29
v_sub_f32_e32 v87, v87, v29
v_sub_f32_e32 v92, v92, v29
v_sub_f32_e32 v93, v93, v29
v_sub_f32_e32 v94, v94, v29
v_sub_f32_e32 v95, v95, v29
.LzFIXH1:
v_exp_f32_e32 v48, v96
v_exp_f32_e32 v26, v97
s_nop 0
v_add_f32_e32 v14, v48, v26
Cv 48,48,26
v_exp_f32_e32 v49, v98
v_exp_f32_e32 v27, v99
v_add_f32_e32 v14, v14, v49
)" R"(
v_add_f32_e32 v14, v14, v27
Cv 49,49,27
v_exp_f32_e32 v50, v104
v_exp_f32_e32 v28, v105
v_add_f32_e32 v14, v14, v50
v_add_f32_e32 v14, v14, v28
Cv 50,50,28
v_exp_f32_e32 v51, v106
v_exp_f32_e32 v29, v107
v_add_f32_e32 v14, v14, v51
v_add_f32_e32 v14, v14, v29
Cv 51,51,29
v_exp_f32_e32 v52, v100
v_exp_f32_e32 v24, v101
s_nop 0
v_add_f32_e32 v15, v52, v24
Cv 52,52,24
v_exp_f32_e32 v53, v102
v_exp_f32_e32 v25, v103
v_add_f32_e32 v15, v15, v53
v_add_f32_e32 v15, v15, v25
Cv 53,53,25
v_exp_f32_e32 v54, v108
v_exp_f32_e32 v26, v109
v_add_f32_e32 v15, v15, v54
v_add_f32_e32 v15, v15, v26
Cv 54,54,26
v_exp_f32_e32 v55, v110
v_exp_f32_e32 v27, v111
v_add_f32_e32 v15, v15, v55
v_add_f32_e32 v15, v15, v27
Cv 55,55,27
v_exp_f32_e32 v56, v112
v_exp_f32_e32 v28, v113
v_add_f32_e32 v14, v14, v56
v_add_f32_e32 v14, v14, v28
Cv 56,56,28
v_exp_f32_e32 v57, v114
v_exp_f32_e32 v29, v115
v_add_f32_e32 v14, v14, v57
v_add_f32_e32 v14, v14, v29
Cv 57,57,29
v_exp_f32_e32 v58, v120
v_exp_f32_e32 v24, v121
v_add_f32_e32 v14, v14, v58
v_add_f32_e32 v14, v14, v24
Cv 58,58,24
v_exp_f32_e32 v59, v122
v_exp_f32_e32 v25, v123
v_add_f32_e32 v14, v14, v59
v_add_f32_e32 v14, v14, v25
Cv 59,59,25
v_exp_f32_e32 v60, v116
v_exp_f32_e32 v26, v117
v_add_f32_e32 v15, v15, v60
v_add_f32_e32 v15, v15, v26
Cv 60,60,26
v_exp_f32_e32 v61, v118
v_exp_f32_e32 v27, v119
v_add_f32_e32 v15, v15, v61
v_add_f32_e32 v15, v15, v27
Cv 61,61,27
v_exp_f32_e32 v62, v124
v_exp_f32_e32 v28, v125
)" R"(
v_add_f32_e32 v15, v15, v62
v_add_f32_e32 v15, v15, v28
Cv 62,62,28
v_exp_f32_e32 v63, v126
v_exp_f32_e32 v29, v127
v_add_f32_e32 v15, v15, v63
v_add_f32_e32 v15, v15, v29
Cv 63,63,29
s_nop 1
s_setpc_b64 s[30:31]
.Lzsp1:
s_sub_u32 s53, s53, 1
s_cbranch_scc1 .Lzsq1
s_sleep 1
buffer_load_dword v4, v2, s[20:23], s85 offen sc0 sc1
s_waitcnt vmcnt(0)
v_xor_b32 v4, s3, v4
v_cmp_gt_u32_e32 vcc, 2, v4
s_andn2_b32 s28, 0xff, vcc_lo
s_cbranch_scc1 .Lzsp1
s_branch .Lzok1
.Lzsq1:
s_mov_b32 s53, 0
s_branch .Lzok1
.Lzsp2:
s_sub_u32 s53, s53, 1
s_cbranch_scc1 .Lzsq2
s_sleep 1
buffer_load_dword v4, v7, s[20:23], s52 offen offset:0 sc0 sc1
s_waitcnt vmcnt(0)
v_cmp_eq_u32_e32 vcc, s3, v4
s_andn2_b64 s[28:29], 1, vcc
s_cbranch_scc1 .Lzsp2
s_branch .Lzok2
.Lzsq2:
s_mov_b32 s53, 0
s_branch .Lzok2
.Lzsp3:
s_sub_u32 s53, s53, 1
s_cbranch_scc1 .Lzsq3
s_sleep 1
buffer_load_dword v4, v2, s[20:23], s85 offen sc0 sc1
s_waitcnt vmcnt(0)
v_xor_b32 v4, s3, v4
v_cmp_gt_u32_e32 vcc, 2, v4
s_andn2_b32 s28, 0xff, vcc_lo
s_cbranch_scc1 .Lzsp3
s_branch .Lzok3
.Lzsq3:
s_mov_b32 s53, 0
s_branch .Lzok3
.Lzsp4:
s_sub_u32 s53, s53, 1
s_cbranch_scc1 .Lzsq4
s_sleep 1
buffer_load_dword v4, v7, s[20:23], s52 offen offset:0 sc0 sc1
s_waitcnt vmcnt(0)
v_cmp_eq_u32_e32 vcc, s3, v4
s_andn2_b64 s[28:29], 1, vcc
s_cbranch_scc1 .Lzsp4
s_branch .Lzok4
.Lzsq4:
s_mov_b32 s53, 0
s_branch .Lzok4
)" R"(
.Lzsp5:
s_sub_u32 s53, s53, 1
s_cbranch_scc1 .Lzsq5
s_sleep 1
buffer_load_dword v4, v5, s[20:23], s50 offen offset:0 sc0 sc1
s_waitcnt vmcnt(0)
v_cmp_eq_u32_e32 vcc, s3, v4
s_andn2_b64 s[28:29], -1, vcc
s_cbranch_scc1 .Lzsp5
s_branch .Lzok5
.Lzsq5:
s_mov_b32 s53, 0
s_branch .Lzok5
.Lzsp6:
s_sub_u32 s53, s53, 1
s_cbranch_scc1 .Lzsq6
s_sleep 1
buffer_load_dword v4, v5, s[20:23], s50 offen offset:0 sc0 sc1
s_waitcnt vmcnt(0)
v_cmp_eq_u32_e32 vcc, s3, v4
s_andn2_b64 s[28:29], -1, vcc
s_cbranch_scc1 .Lzsp6
s_branch .Lzok6
.Lzsq6:
s_mov_b32 s53, 0
s_branch .Lzok6
.Lzsp7:
s_sub_u32 s53, s53, 1
s_cbranch_scc1 .Lzsq7
s_sleep 1
buffer_load_dwordx2 v[24:25], v7, s[20:23], s83 offen sc0 sc1
s_waitcnt vmcnt(0)
v_cmp_eq_u32_e32 vcc, s3, v25
s_andn2_b64 s[28:29], 1, vcc
s_cbranch_scc1 .Lzsp7
s_branch .Lzok7
.Lzsq7:
s_mov_b32 s53, 0
s_branch .Lzok7
.LzT2S:
s_cmp_lg_u32 s33, 0
s_cbranch_scc1 .LzT2E
.LzT2:
s_mov_b32 s28, 0xc100000
buffer_load_dwordx2 v[32:33], v1, s[20:23], s28 offen offset:0 sc0 sc1
buffer_load_dwordx2 v[34:35], v1, s[20:23], s28 offen offset:1024 sc0 sc1
buffer_load_dwordx2 v[36:37], v1, s[20:23], s28 offen offset:2048 sc0 sc1
buffer_load_dwordx2 v[38:39], v1, s[20:23], s28 offen offset:3072 sc0 sc1
s_mov_b32 s28, 0xc101000
buffer_load_dwordx2 v[40:41], v1, s[20:23], s28 offen offset:0 sc0 sc1
buffer_load_dwordx2 v[42:43], v1, s[20:23], s28 offen offset:1024 sc0 sc1
buffer_load_dwordx2 v[44:45], v1, s[20:23], s28 offen offset:2048 sc0 sc1
buffer_load_dwordx2 v[46:47], v1, s[20:23], s28 offen offset:3072 sc0 sc1
s_mov_b32 s28, 0xc102000
buffer_load_dwordx2 v[48:49], v1, s[20:23], s28 offen offset:0 sc0 sc1
buffer_load_dwordx2 v[50:51], v1, s[20:23], s28 offen offset:1024 sc0 sc1
buffer_load_dwordx2 v[52:53], v1, s[20:23], s28 offen offset:2048 sc0 sc1
buffer_load_dwordx2 v[54:55], v1, s[20:23], s28 offen offset:3072 sc0 sc1
s_mov_b32 s28, 0xc103000
buffer_load_dwordx2 v[56:57], v1, s[20:23], s28 offen offset:0 sc0 sc1
buffer_load_dwordx2 v[58:59], v1, s[20:23], s28 offen offset:1024 sc0 sc1
buffer_load_dwordx2 v[60:61], v1, s[20:23], s28 offen offset:2048 sc0 sc1
buffer_load_dwordx2 v[62:63], v1, s[20:23], s28 offen offset:3072 sc0 sc1
s_mov_b32 s28, 0xc104000
)" R"(
buffer_load_dwordx2 v[64:65], v1, s[20:23], s28 offen offset:0 sc0 sc1
buffer_load_dwordx2 v[66:67], v1, s[20:23], s28 offen offset:1024 sc0 sc1
buffer_load_dwordx2 v[68:69], v1, s[20:23], s28 offen offset:2048 sc0 sc1
buffer_load_dwordx2 v[70:71], v1, s[20:23], s28 offen offset:3072 sc0 sc1
s_mov_b32 s28, 0xc105000
buffer_load_dwordx2 v[72:73], v1, s[20:23], s28 offen offset:0 sc0 sc1
buffer_load_dwordx2 v[74:75], v1, s[20:23], s28 offen offset:1024 sc0 sc1
buffer_load_dwordx2 v[76:77], v1, s[20:23], s28 offen offset:2048 sc0 sc1
buffer_load_dwordx2 v[78:79], v1, s[20:23], s28 offen offset:3072 sc0 sc1
s_mov_b32 s28, 0xc106000
buffer_load_dwordx2 v[80:81], v1, s[20:23], s28 offen offset:0 sc0 sc1
buffer_load_dwordx2 v[82:83], v1, s[20:23], s28 offen offset:1024 sc0 sc1
buffer_load_dwordx2 v[84:85], v1, s[20:23], s28 offen offset:2048 sc0 sc1
buffer_load_dwordx2 v[86:87], v1, s[20:23], s28 offen offset:3072 sc0 sc1
s_mov_b32 s28, 0xc107000
buffer_load_dwordx2 v[88:89], v1, s[20:23], s28 offen offset:0 sc0 sc1
buffer_load_dwordx2 v[90:91], v1, s[20:23], s28 offen offset:1024 sc0 sc1
buffer_load_dwordx2 v[92:93], v1, s[20:23], s28 offen offset:2048 sc0 sc1
buffer_load_dwordx2 v[94:95], v1, s[20:23], s28 offen offset:3072 sc0 sc1
v_mov_b32 v26, 0
v_mov_b32 v27, -1
s_mov_b32 s28, 0xc108c00
buffer_store_dword v26, v7, s[20:23], s28 offen sc0 sc1
buffer_store_dword v26, v7, s[20:23], s28 offen offset:128 sc0 sc1
s_waitcnt vmcnt(0)
v_max3_u32 v26, v26, v33, v35
v_min3_u32 v27, v27, v33, v35
v_max3_u32 v26, v26, v37, v39
v_min3_u32 v27, v27, v37, v39
v_max3_u32 v26, v26, v41, v43
v_min3_u32 v27, v27, v41, v43
v_max3_u32 v26, v26, v45, v47
v_min3_u32 v27, v27, v45, v47
v_max3_u32 v26, v26, v49, v51
v_min3_u32 v27, v27, v49, v51
v_max3_u32 v26, v26, v53, v55
v_min3_u32 v27, v27, v53, v55
v_max3_u32 v26, v26, v57, v59
v_min3_u32 v27, v27, v57, v59
v_max3_u32 v26, v26, v61, v63
v_min3_u32 v27, v27, v61, v63
v_max3_u32 v26, v26, v65, v67
v_min3_u32 v27, v27, v65, v67
v_max3_u32 v26, v26, v69, v71
v_min3_u32 v27, v27, v69, v71
v_max3_u32 v26, v26, v73, v75
v_min3_u32 v27, v27, v73, v75
v_max3_u32 v26, v26, v77, v79
v_min3_u32 v27, v27, v77, v79
v_max3_u32 v26, v26, v81, v83
v_min3_u32 v27, v27, v81, v83
v_max3_u32 v26, v26, v85, v87
v_min3_u32 v27, v27, v85, v87
v_max3_u32 v26, v26, v89, v91
v_min3_u32 v27, v27, v89, v91
v_max3_u32 v26, v26, v93, v95
v_min3_u32 v27, v27, v93, v95
v_cmp_ne_u32_e32 vcc, s3, v26
v_cmp_ne_u32_e64 s[28:29], s3, v27
s_or_b64 vcc, vcc, s[28:29]
s_cbranch_scc0 .LzT2G
s_sub_u32 s53, s53, 1
s_cbranch_scc0 .LzT2
s_mov_b32 s53, 0
)" R"(
.LzT2G:
v_max_u32 v26, v32, v34
v_min_u32 v27, v32, v34
v_med3_u32 v27, v26, v27, v36
v_max_u32 v26, v26, v36
v_med3_u32 v27, v26, v27, v38
v_max_u32 v26, v26, v38
v_med3_u32 v27, v26, v27, v40
v_max_u32 v26, v26, v40
v_med3_u32 v27, v26, v27, v42
v_max_u32 v26, v26, v42
v_med3_u32 v27, v26, v27, v44
v_max_u32 v26, v26, v44
v_med3_u32 v27, v26, v27, v46
v_max_u32 v26, v26, v46
v_med3_u32 v27, v26, v27, v48
v_max_u32 v26, v26, v48
v_med3_u32 v27, v26, v27, v50
v_max_u32 v26, v26, v50
v_med3_u32 v27, v26, v27, v52
v_max_u32 v26, v26, v52
v_med3_u32 v27, v26, v27, v54
v_max_u32 v26, v26, v54
v_med3_u32 v27, v26, v27, v56
v_max_u32 v26, v26, v56
v_med3_u32 v27, v26, v27, v58
v_max_u32 v26, v26, v58
v_med3_u32 v27, v26, v27, v60
v_max_u32 v26, v26, v60
v_med3_u32 v27, v26, v27, v62
v_max_u32 v26, v26, v62
v_med3_u32 v27, v26, v27, v64
v_max_u32 v26, v26, v64
v_med3_u32 v27, v26, v27, v66
v_max_u32 v26, v26, v66
v_med3_u32 v27, v26, v27, v68
v_max_u32 v26, v26, v68
v_med3_u32 v27, v26, v27, v70
v_max_u32 v26, v26, v70
v_med3_u32 v27, v26, v27, v72
v_max_u32 v26, v26, v72
v_med3_u32 v27, v26, v27, v74
v_max_u32 v26, v26, v74
v_med3_u32 v27, v26, v27, v76
v_max_u32 v26, v26, v76
v_med3_u32 v27, v26, v27, v78
v_max_u32 v26, v26, v78
v_med3_u32 v27, v26, v27, v80
v_max_u32 v26, v26, v80
v_med3_u32 v27, v26, v27, v82
v_max_u32 v26, v26, v82
v_med3_u32 v27, v26, v27, v84
v_max_u32 v26, v26, v84
v_med3_u32 v27, v26, v27, v86
v_max_u32 v26, v26, v86
v_med3_u32 v27, v26, v27, v88
v_max_u32 v26, v26, v88
v_med3_u32 v27, v26, v27, v90
v_max_u32 v26, v26, v90
v_med3_u32 v27, v26, v27, v92
v_max_u32 v26, v26, v92
v_med3_u32 v27, v26, v27, v94
v_max_u32 v26, v26, v94
v_mov_b32 v24, v26
)" R"(
v_mov_b32 v25, v27
s_nop 1
v_permlane32_swap_b32_e32 v26, v24
v_permlane32_swap_b32_e32 v27, v25
v_max_u32 v27, v27, v25
v_med3_u32 v27, v26, v27, v24
v_max_u32 v26, v26, v24
v_mov_b32 v24, v26
v_mov_b32 v25, v27
s_nop 1
v_permlane16_swap_b32_e32 v26, v24
v_permlane16_swap_b32_e32 v27, v25
v_max_u32 v27, v27, v25
v_med3_u32 v27, v26, v27, v24
v_max_u32 v26, v26, v24
s_nop 1
v_mov_b32_dpp v24, v26 row_ror:8 row_mask:0xf bank_mask:0xf
v_mov_b32_dpp v25, v27 row_ror:8 row_mask:0xf bank_mask:0xf
v_max_u32 v27, v27, v25
v_med3_u32 v27, v26, v27, v24
v_max_u32 v26, v26, v24
s_nop 1
v_mov_b32_dpp v24, v26 row_ror:4 row_mask:0xf bank_mask:0xf
v_mov_b32_dpp v25, v27 row_ror:4 row_mask:0xf bank_mask:0xf
v_max_u32 v27, v27, v25
v_med3_u32 v27, v26, v27, v24
v_max_u32 v26, v26, v24
s_nop 1
v_mov_b32_dpp v24, v26 row_ror:2 row_mask:0xf bank_mask:0xf
v_mov_b32_dpp v25, v27 row_ror:2 row_mask:0xf bank_mask:0xf
v_max_u32 v27, v27, v25
v_med3_u32 v27, v26, v27, v24
v_max_u32 v26, v26, v24
s_nop 1
v_mov_b32_dpp v24, v26 row_ror:1 row_mask:0xf bank_mask:0xf
v_mov_b32_dpp v25, v27 row_ror:1 row_mask:0xf bank_mask:0xf
v_max_u32 v27, v27, v25
v_med3_u32 v27, v26, v27, v24
v_max_u32 v26, v26, v24
v_and_b32 v26, 0xffff, v26
v_lshl_or_b32 v26, v27, 16, v26
v_not_b32 v24, v26
v_mov_b32 v25, s3
s_mov_b32 s28, 0xc108000
buffer_store_dwordx2 v[24:25], v7, s[20:23], s28 offen sc0 sc1
s_branch .LzT2E
.LzA7:
s_waitcnt vmcnt(0)
s_nop 7
v_mul_f32_e32 v96, s54, v96
v_mul_f32_e32 v97, s54, v97
Cv 96,96,97
v_accvgpr_write_b32 a112, v96
v_mul_f32_e32 v98, s54, v98
v_mul_f32_e32 v99, s54, v99
Cv 98,98,99
v_accvgpr_write_b32 a113, v98
v_mul_f32_e32 v100, s54, v100
v_mul_f32_e32 v101, s54, v101
Cv 100,100,101
v_accvgpr_write_b32 a114, v100
v_mul_f32_e32 v102, s54, v102
v_mul_f32_e32 v103, s54, v103
Cv 102,102,103
)" R"(
v_accvgpr_write_b32 a115, v102
v_mul_f32_e32 v104, s54, v104
v_mul_f32_e32 v105, s54, v105
Cv 104,104,105
v_accvgpr_write_b32 a116, v104
v_mul_f32_e32 v106, s54, v106
v_mul_f32_e32 v107, s54, v107
Cv 106,106,107
v_accvgpr_write_b32 a117, v106
v_mul_f32_e32 v108, s54, v108
v_mul_f32_e32 v109, s54, v109
Cv 108,108,109
v_accvgpr_write_b32 a118, v108
v_mul_f32_e32 v110, s54, v110
v_mul_f32_e32 v111, s54, v111
Cv 110,110,111
v_accvgpr_write_b32 a119, v110
v_mul_f32_e32 v112, s54, v112
v_mul_f32_e32 v113, s54, v113
Cv 112,112,113
v_accvgpr_write_b32 a120, v112
v_mul_f32_e32 v114, s54, v114
v_mul_f32_e32 v115, s54, v115
Cv 114,114,115
v_accvgpr_write_b32 a121, v114
v_mul_f32_e32 v116, s54, v116
v_mul_f32_e32 v117, s54, v117
Cv 116,116,117
v_accvgpr_write_b32 a122, v116
v_mul_f32_e32 v118, s54, v118
v_mul_f32_e32 v119, s54, v119
Cv 118,118,119
v_accvgpr_write_b32 a123, v118
v_mul_f32_e32 v120, s54, v120
v_mul_f32_e32 v121, s54, v121
Cv 120,120,121
v_accvgpr_write_b32 a124, v120
v_mul_f32_e32 v122, s54, v122
v_mul_f32_e32 v123, s54, v123
Cv 122,122,123
v_accvgpr_write_b32 a125, v122
v_mul_f32_e32 v124, s54, v124
v_mul_f32_e32 v125, s54, v125
Cv 124,124,125
v_accvgpr_write_b32 a126, v124
v_mul_f32_e32 v126, s54, v126
v_mul_f32_e32 v127, s54, v127
Cv 126,126,127
v_accvgpr_write_b32 a127, v126
v_accvgpr_mov_b32 a64, a224
v_accvgpr_mov_b32 a65, a225
v_accvgpr_mov_b32 a66, a226
v_accvgpr_mov_b32 a67, a227
v_accvgpr_mov_b32 a68, a228
v_accvgpr_mov_b32 a69, a229
v_accvgpr_mov_b32 a70, a230
v_accvgpr_mov_b32 a71, a231
v_accvgpr_mov_b32 a72, a232
v_accvgpr_mov_b32 a73, a233
v_accvgpr_mov_b32 a74, a234
v_accvgpr_mov_b32 a75, a235
v_accvgpr_mov_b32 a76, a236
v_accvgpr_mov_b32 a77, a237
v_accvgpr_mov_b32 a78, a238
)" R"(
v_accvgpr_mov_b32 a79, a239
v_accvgpr_mov_b32 a80, a240
v_accvgpr_mov_b32 a81, a241
v_accvgpr_mov_b32 a82, a242
v_accvgpr_mov_b32 a83, a243
v_accvgpr_mov_b32 a84, a244
v_accvgpr_mov_b32 a85, a245
v_accvgpr_mov_b32 a86, a246
v_accvgpr_mov_b32 a87, a247
v_accvgpr_mov_b32 a88, a248
v_accvgpr_mov_b32 a89, a249
v_accvgpr_mov_b32 a90, a250
v_accvgpr_mov_b32 a91, a251
v_accvgpr_mov_b32 a92, a252
v_accvgpr_mov_b32 a93, a253
v_accvgpr_mov_b32 a94, a254
v_accvgpr_mov_b32 a95, a255
v_accvgpr_mov_b32 a96, a192
v_accvgpr_mov_b32 a97, a193
v_accvgpr_mov_b32 a98, a194
v_accvgpr_mov_b32 a99, a195
v_accvgpr_mov_b32 a100, a196
v_accvgpr_mov_b32 a101, a197
v_accvgpr_mov_b32 a102, a198
v_accvgpr_mov_b32 a103, a199
v_accvgpr_mov_b32 a104, a200
v_accvgpr_mov_b32 a105, a201
v_accvgpr_mov_b32 a106, a202
v_accvgpr_mov_b32 a107, a203
v_accvgpr_mov_b32 a108, a204
v_accvgpr_mov_b32 a109, a205
v_accvgpr_mov_b32 a110, a206
v_accvgpr_mov_b32 a111, a207
s_mov_b32 s94, 2
s_nop 1
s_branch .LzA7R
.Lzsp8:
s_sub_u32 s53, s53, 1
s_cbranch_scc1 .Lzsq8
s_sleep 1
buffer_load_dword v4, v5, s[20:23], s50 offen offset:0 sc0 sc1
s_waitcnt vmcnt(0)
v_cmp_eq_u32_e32 vcc, s3, v4
s_andn2_b64 s[28:29], -1, vcc
s_cbranch_scc1 .Lzsp8
s_branch .Lzok8
.Lzsq8:
s_mov_b32 s53, 0
s_branch .Lzok8
.Lzmk0:
s_call_b64 s[30:31], .LzMASK0
s_branch .Lzmr0
.Lzfx0:
s_mov_b32 s88, 3
s_call_b64 s[30:31], .LzFIX0
s_branch .Lzfr0
.Lzmk1:
s_call_b64 s[30:31], .LzMASK1
s_branch .Lzmr1
.Lzsp9:
s_sub_u32 s53, s53, 1
s_cbranch_scc1 .Lzsq9
s_sleep 1
buffer_load_dword v4, v5, s[20:23], s50 offen offset:0 sc0 sc1
)" R"(
s_waitcnt vmcnt(0)
v_cmp_eq_u32_e32 vcc, s3, v4
s_andn2_b64 s[28:29], -1, vcc
s_cbranch_scc1 .Lzsp9
s_branch .Lzok9
.Lzsq9:
s_mov_b32 s53, 0
s_branch .Lzok9
.Lzfx1:
s_mov_b32 s88, 2
s_call_b64 s[30:31], .LzFIX1
s_branch .Lzfr1
.Lzmk2:
s_call_b64 s[30:31], .LzMASK0
s_branch .Lzmr2
.Lzfx2:
s_mov_b32 s88, 2
s_call_b64 s[30:31], .LzFIX0
s_branch .Lzfr2
.Lzmk3:
s_call_b64 s[30:31], .LzMASK1
s_branch .Lzmr3
.Lzsp10:
s_sub_u32 s53, s53, 1
s_cbranch_scc1 .Lzsq10
s_sleep 1
buffer_load_dword v4, v5, s[20:23], s50 offen offset:0 sc0 sc1
s_waitcnt vmcnt(0)
v_cmp_eq_u32_e32 vcc, s3, v4
s_andn2_b64 s[28:29], -1, vcc
s_cbranch_scc1 .Lzsp10
s_branch .Lzok10
.Lzsq10:
s_mov_b32 s53, 0
s_branch .Lzok10
.Lzfx3:
s_mov_b32 s88, 0
s_call_b64 s[30:31], .LzFIX1
s_branch .Lzfr3
.Lzmk4:
s_call_b64 s[30:31], .LzMASK0
s_branch .Lzmr4
.Lzfx4:
s_mov_b32 s88, 3
s_call_b64 s[30:31], .LzFIX0
s_branch .Lzfr4
.Lzmk5:
s_call_b64 s[30:31], .LzMASK1
s_branch .Lzmr5
.Lzfx5:
s_mov_b32 s88, 0
s_call_b64 s[30:31], .LzFIX1
s_branch .Lzfr5
.Lzsp11:
s_sub_u32 s53, s53, 1
s_cbranch_scc1 .Lzsq11
s_sleep 1
buffer_load_dword v4, v7, s[20:23], s52 offen offset:0 sc0 sc1
s_waitcnt vmcnt(0)
v_cmp_eq_u32_e32 vcc, s3, v4
s_andn2_b64 s[28:29], 1, vcc
s_cbranch_scc1 .Lzsp11
s_branch .Lzok11
.Lzsq11:
)" R"(
s_mov_b32 s53, 0
s_branch .Lzok11
.Lzsp12:
s_sub_u32 s53, s53, 1
s_cbranch_scc1 .Lzsq12
s_sleep 1
buffer_load_dword v4, v7, s[20:23], s52 offen offset:128 sc0 sc1
s_waitcnt vmcnt(0)
v_cmp_eq_u32_e32 vcc, s3, v4
s_andn2_b64 s[28:29], 1, vcc
s_cbranch_scc1 .Lzsp12
s_branch .Lzok12
.Lzsq12:
s_mov_b32 s53, 0
s_branch .Lzok12
.Lzsp13:
s_sub_u32 s53, s53, 1
s_cbranch_scc1 .Lzsq13
s_sleep 1
buffer_load_dword v4, v2, s[20:23], s85 offen sc0 sc1
s_waitcnt vmcnt(0)
v_xor_b32 v4, s3, v4
v_cmp_gt_u32_e32 vcc, 2, v4
s_andn2_b32 s28, 0xff, vcc_lo
s_cbranch_scc1 .Lzsp13
s_branch .Lzok13
.Lzsq13:
s_mov_b32 s53, 0
s_branch .Lzok13
.Lzsp14:
s_sub_u32 s53, s53, 1
s_cbranch_scc1 .Lzsq14
s_sleep 1
buffer_load_dword v4, v5, s[20:23], s50 offen offset:0 sc0 sc1
s_waitcnt vmcnt(0)
v_cmp_eq_u32_e32 vcc, s3, v4
s_andn2_b64 s[28:29], -1, vcc
s_cbranch_scc1 .Lzsp14
s_branch .Lzok14
.Lzsq14:
s_mov_b32 s53, 0
s_branch .Lzok14
.Lzsp15:
s_sub_u32 s53, s53, 1
s_cbranch_scc1 .Lzsq15
s_sleep 1
buffer_load_dword v4, v5, s[20:23], s50 offen offset:0 sc0 sc1
s_waitcnt vmcnt(0)
v_cmp_eq_u32_e32 vcc, s3, v4
s_andn2_b64 s[28:29], -1, vcc
s_cbranch_scc1 .Lzsp15
s_branch .Lzok15
.Lzsq15:
s_mov_b32 s53, 0
s_branch .Lzok15
.Lzsp16:
s_sub_u32 s53, s53, 1
s_cbranch_scc1 .Lzsq16
s_sleep 1
buffer_load_dword v4, v5, s[20:23], s50 offen offset:0 sc0 sc1
s_waitcnt vmcnt(0)
v_cmp_eq_u32_e32 vcc, s3, v4
s_andn2_b64 s[28:29], -1, vcc
s_cbranch_scc1 .Lzsp16
)" R"(
s_branch .Lzok16
.Lzsq16:
s_mov_b32 s53, 0
s_branch .Lzok16
.Lzsp17:
s_sub_u32 s53, s53, 1
s_cbranch_scc1 .Lzsq17
s_sleep 1
buffer_load_dword v4, v5, s[20:23], s50 offen offset:0 sc0 sc1
s_waitcnt vmcnt(0)
v_cmp_eq_u32_e32 vcc, s3, v4
s_andn2_b64 s[28:29], -1, vcc
s_cbranch_scc1 .Lzsp17
s_branch .Lzok17
.Lzsq17:
s_mov_b32 s53, 0
s_branch .Lzok17
.Lzsp18:
s_sub_u32 s53, s53, 1
s_cbranch_scc1 .Lzsq18
s_sleep 1
buffer_load_dword v4, v5, s[20:23], s50 offen offset:0 sc0 sc1
s_waitcnt vmcnt(0)
v_cmp_eq_u32_e32 vcc, s3, v4
s_andn2_b64 s[28:29], -1, vcc
s_cbranch_scc1 .Lzsp18
s_branch .Lzok18
.Lzsq18:
s_mov_b32 s53, 0
s_branch .Lzok18
.Lzfx6:
s_mov_b32 s88, 3
s_call_b64 s[30:31], .LzFIX0
s_branch .Lzfr6
.Lzsp19:
s_sub_u32 s53, s53, 1
s_cbranch_scc1 .Lzsq19
s_sleep 1
buffer_load_dword v4, v5, s[20:23], s50 offen offset:0 sc0 sc1
s_waitcnt vmcnt(0)
v_cmp_eq_u32_e32 vcc, s3, v4
s_andn2_b64 s[28:29], -1, vcc
s_cbranch_scc1 .Lzsp19
s_branch .Lzok19
.Lzsq19:
s_mov_b32 s53, 0
s_branch .Lzok19
.Lzfx7:
s_mov_b32 s88, 2
s_call_b64 s[30:31], .LzFIX1
s_branch .Lzfr7
.Lzmk6:
s_call_b64 s[30:31], .LzMASK0
s_branch .Lzmr6
.Lzfx8:
s_mov_b32 s88, 2
s_call_b64 s[30:31], .LzFIX0
s_branch .Lzfr8
.Lzmk7:
s_call_b64 s[30:31], .LzMASK1
s_branch .Lzmr7
.Lzsp20:
s_sub_u32 s53, s53, 1
s_cbranch_scc1 .Lzsq20
)" R"(
s_sleep 1
buffer_load_dword v4, v5, s[20:23], s50 offen offset:0 sc0 sc1
s_waitcnt vmcnt(0)
v_cmp_eq_u32_e32 vcc, s3, v4
s_andn2_b64 s[28:29], -1, vcc
s_cbranch_scc1 .Lzsp20
s_branch .Lzok20
.Lzsq20:
s_mov_b32 s53, 0
s_branch .Lzok20
.Lzfx9:
s_mov_b32 s88, 2
s_call_b64 s[30:31], .LzFIX1
s_branch .Lzfr9
.Lzmk8:
s_call_b64 s[30:31], .LzMASK0
s_branch .Lzmr8
.Lzfx10:
s_mov_b32 s88, 2
s_call_b64 s[30:31], .LzFIX0
s_branch .Lzfr10
.Lzmk9:
s_call_b64 s[30:31], .LzMASK1
s_branch .Lzmr9
.Lzsp21:
s_sub_u32 s53, s53, 1
s_cbranch_scc1 .Lzsq21
s_sleep 1
buffer_load_dword v4, v7, s[20:23], s83 offen offset:4 sc0 sc1
s_waitcnt vmcnt(0)
v_cmp_eq_u32_e32 vcc, s3, v4
s_andn2_b64 s[28:29], 1, vcc
s_cbranch_scc1 .Lzsp21
s_branch .Lzok21
.Lzsq21:
s_mov_b32 s53, 0
s_branch .Lzok21
.Lzsp22:
s_sub_u32 s53, s53, 1
s_cbranch_scc1 .Lzsq22
s_sleep 1
buffer_load_dword v4, v7, s[20:23], s65 offen offset:0 sc0 sc1
s_waitcnt vmcnt(0)
v_cmp_eq_u32_e32 vcc, s3, v4
s_andn2_b64 s[28:29], s[60:61], vcc
s_cbranch_scc1 .Lzsp22
s_branch .Lzok22
.Lzsq22:
s_mov_b32 s53, 0
s_branch .Lzok22
.Lzfx11:
s_mov_b32 s88, 2
s_call_b64 s[30:31], .LzFIX1
s_branch .Lzfr11
.Lzmk10:
s_call_b64 s[30:31], .LzMASK0
s_branch .Lzmr10
.Lzfx12:
s_mov_b32 s88, 2
s_call_b64 s[30:31], .LzFIX0
s_branch .Lzfr12
.Lzmk11:
s_call_b64 s[30:31], .LzMASK1
s_branch .Lzmr11
)" R"(
.Lzfx13:
s_mov_b32 s88, 0
s_call_b64 s[30:31], .LzFIX1
s_branch .Lzfr13
.LzMRG:
s_waitcnt vmcnt(0)
s_nop 7
v_mov_b32 v25, v64
v_mov_b32 v24, v25
s_nop 1
v_permlane32_swap_b32_e32 v25, v24
v_add_f32_e32 v25, v25, v24
v_mov_b32 v24, v25
s_nop 1
v_permlane16_swap_b32_e32 v25, v24
v_add_f32_e32 v25, v25, v24
v_cmp_lt_f32_e32 vcc, 0, v25
s_nop 1
v_cndmask_b32_e32 v66, v16, v66, vcc
v_sub_f32_e32 v28, v16, v66
v_min_f32_e32 v26, 0, v28
v_exp_f32_e32 v26, v26
v_max_f32_e32 v28, 0, v28
v_sub_f32_e32 v24, 0, v28
v_exp_f32_e32 v24, v24
v_mul_f32_e32 v64, v26, v64
v_mul_f32_e32 v12, v24, v12
v_mul_f32_e32 v128, v24, v128
v_mul_f32_e32 v129, v24, v129
v_mul_f32_e32 v130, v24, v130
v_mul_f32_e32 v131, v24, v131
v_mul_f32_e32 v132, v24, v132
v_mul_f32_e32 v133, v24, v133
v_mul_f32_e32 v134, v24, v134
v_mul_f32_e32 v135, v24, v135
v_mul_f32_e32 v144, v24, v144
v_mul_f32_e32 v145, v24, v145
v_mul_f32_e32 v146, v24, v146
v_mul_f32_e32 v147, v24, v147
v_mul_f32_e32 v148, v24, v148
v_mul_f32_e32 v149, v24, v149
v_mul_f32_e32 v150, v24, v150
v_mul_f32_e32 v151, v24, v151
v_mul_f32_e32 v160, v24, v160
v_mul_f32_e32 v161, v24, v161
v_mul_f32_e32 v162, v24, v162
v_mul_f32_e32 v163, v24, v163
v_mul_f32_e32 v164, v24, v164
v_mul_f32_e32 v165, v24, v165
v_mul_f32_e32 v166, v24, v166
v_mul_f32_e32 v167, v24, v167
v_mul_f32_e32 v176, v24, v176
v_mul_f32_e32 v177, v24, v177
v_mul_f32_e32 v178, v24, v178
v_mul_f32_e32 v179, v24, v179
v_mul_f32_e32 v180, v24, v180
v_mul_f32_e32 v181, v24, v181
v_mul_f32_e32 v182, v24, v182
v_mul_f32_e32 v183, v24, v183
v_mul_f32_e32 v192, v24, v192
v_mul_f32_e32 v193, v24, v193
v_mul_f32_e32 v194, v24, v194
v_mul_f32_e32 v195, v24, v195
v_mul_f32_e32 v196, v24, v196
)" R"(
v_mul_f32_e32 v197, v24, v197
v_mul_f32_e32 v198, v24, v198
v_mul_f32_e32 v199, v24, v199
v_mul_f32_e32 v208, v24, v208
v_mul_f32_e32 v209, v24, v209
v_mul_f32_e32 v210, v24, v210
v_mul_f32_e32 v211, v24, v211
v_mul_f32_e32 v212, v24, v212
v_mul_f32_e32 v213, v24, v213
v_mul_f32_e32 v214, v24, v214
v_mul_f32_e32 v215, v24, v215
v_mul_f32_e32 v224, v24, v224
v_mul_f32_e32 v225, v24, v225
v_mul_f32_e32 v226, v24, v226
v_mul_f32_e32 v227, v24, v227
v_mul_f32_e32 v228, v24, v228
v_mul_f32_e32 v229, v24, v229
v_mul_f32_e32 v230, v24, v230
v_mul_f32_e32 v231, v24, v231
v_mul_f32_e32 v240, v24, v240
v_mul_f32_e32 v241, v24, v241
v_mul_f32_e32 v242, v24, v242
v_mul_f32_e32 v243, v24, v243
v_mul_f32_e32 v244, v24, v244
v_mul_f32_e32 v245, v24, v245
v_mul_f32_e32 v246, v24, v246
v_mul_f32_e32 v247, v24, v247
v_sub_f32_e32 v16, v16, v28
v_sub_f32_e32 v17, v17, v28
v_sub_f32_e32 v18, v18, v28
v_sub_f32_e32 v19, v19, v28
v_sub_f32_e32 v96, v96, v28
v_sub_f32_e32 v97, v97, v28
v_sub_f32_e32 v98, v98, v28
v_sub_f32_e32 v99, v99, v28
v_sub_f32_e32 v104, v104, v28
v_sub_f32_e32 v105, v105, v28
v_sub_f32_e32 v106, v106, v28
v_sub_f32_e32 v107, v107, v28
v_sub_f32_e32 v112, v112, v28
v_sub_f32_e32 v113, v113, v28
v_sub_f32_e32 v114, v114, v28
v_sub_f32_e32 v115, v115, v28
v_sub_f32_e32 v120, v120, v28
v_sub_f32_e32 v121, v121, v28
v_sub_f32_e32 v122, v122, v28
v_sub_f32_e32 v123, v123, v28
v_accvgpr_read_b32 v25, a128
v_lshlrev_b32 v24, 16, v25
v_and_b32 v25, 0xffff0000, v25
v_mul_f32_e32 v24, v26, v24
v_mul_f32_e32 v25, v26, v25
Cv 24,24,25
v_accvgpr_write_b32 a128, v24
v_accvgpr_read_b32 v25, a129
v_lshlrev_b32 v24, 16, v25
v_and_b32 v25, 0xffff0000, v25
v_mul_f32_e32 v24, v26, v24
v_mul_f32_e32 v25, v26, v25
Cv 24,24,25
v_accvgpr_write_b32 a129, v24
v_accvgpr_read_b32 v25, a130
v_lshlrev_b32 v24, 16, v25
v_and_b32 v25, 0xffff0000, v25
)" R"(
v_mul_f32_e32 v24, v26, v24
v_mul_f32_e32 v25, v26, v25
Cv 24,24,25
v_accvgpr_write_b32 a130, v24
v_accvgpr_read_b32 v25, a131
v_lshlrev_b32 v24, 16, v25
v_and_b32 v25, 0xffff0000, v25
v_mul_f32_e32 v24, v26, v24
v_mul_f32_e32 v25, v26, v25
Cv 24,24,25
v_accvgpr_write_b32 a131, v24
v_accvgpr_read_b32 v25, a136
v_lshlrev_b32 v24, 16, v25
v_and_b32 v25, 0xffff0000, v25
v_mul_f32_e32 v24, v26, v24
v_mul_f32_e32 v25, v26, v25
Cv 24,24,25
v_accvgpr_write_b32 a136, v24
v_accvgpr_read_b32 v25, a137
v_lshlrev_b32 v24, 16, v25
v_and_b32 v25, 0xffff0000, v25
v_mul_f32_e32 v24, v26, v24
v_mul_f32_e32 v25, v26, v25
Cv 24,24,25
v_accvgpr_write_b32 a137, v24
v_accvgpr_read_b32 v25, a138
v_lshlrev_b32 v24, 16, v25
v_and_b32 v25, 0xffff0000, v25
v_mul_f32_e32 v24, v26, v24
v_mul_f32_e32 v25, v26, v25
Cv 24,24,25
v_accvgpr_write_b32 a138, v24
v_accvgpr_read_b32 v25, a139
v_lshlrev_b32 v24, 16, v25
v_and_b32 v25, 0xffff0000, v25
v_mul_f32_e32 v24, v26, v24
v_mul_f32_e32 v25, v26, v25
Cv 24,24,25
v_accvgpr_write_b32 a139, v24
v_accvgpr_read_b32 v25, a144
v_lshlrev_b32 v24, 16, v25
v_and_b32 v25, 0xffff0000, v25
v_mul_f32_e32 v24, v26, v24
v_mul_f32_e32 v25, v26, v25
Cv 24,24,25
v_accvgpr_write_b32 a144, v24
v_accvgpr_read_b32 v25, a145
v_lshlrev_b32 v24, 16, v25
v_and_b32 v25, 0xffff0000, v25
v_mul_f32_e32 v24, v26, v24
v_mul_f32_e32 v25, v26, v25
Cv 24,24,25
v_accvgpr_write_b32 a145, v24
v_accvgpr_read_b32 v25, a146
v_lshlrev_b32 v24, 16, v25
v_and_b32 v25, 0xffff0000, v25
v_mul_f32_e32 v24, v26, v24
v_mul_f32_e32 v25, v26, v25
Cv 24,24,25
v_accvgpr_write_b32 a146, v24
v_accvgpr_read_b32 v25, a147
v_lshlrev_b32 v24, 16, v25
v_and_b32 v25, 0xffff0000, v25
v_mul_f32_e32 v24, v26, v24
)" R"(
v_mul_f32_e32 v25, v26, v25
Cv 24,24,25
v_accvgpr_write_b32 a147, v24
v_accvgpr_read_b32 v25, a152
v_lshlrev_b32 v24, 16, v25
v_and_b32 v25, 0xffff0000, v25
v_mul_f32_e32 v24, v26, v24
v_mul_f32_e32 v25, v26, v25
Cv 24,24,25
v_accvgpr_write_b32 a152, v24
v_accvgpr_read_b32 v25, a153
v_lshlrev_b32 v24, 16, v25
v_and_b32 v25, 0xffff0000, v25
v_mul_f32_e32 v24, v26, v24
v_mul_f32_e32 v25, v26, v25
Cv 24,24,25
v_accvgpr_write_b32 a153, v24
v_accvgpr_read_b32 v25, a154
v_lshlrev_b32 v24, 16, v25
v_and_b32 v25, 0xffff0000, v25
v_mul_f32_e32 v24, v26, v24
v_mul_f32_e32 v25, v26, v25
Cv 24,24,25
v_accvgpr_write_b32 a154, v24
v_accvgpr_read_b32 v25, a155
v_lshlrev_b32 v24, 16, v25
v_and_b32 v25, 0xffff0000, v25
v_mul_f32_e32 v24, v26, v24
v_mul_f32_e32 v25, v26, v25
Cv 24,24,25
v_accvgpr_write_b32 a155, v24
v_accvgpr_read_b32 v25, a160
v_lshlrev_b32 v24, 16, v25
v_and_b32 v25, 0xffff0000, v25
v_mul_f32_e32 v24, v26, v24
v_mul_f32_e32 v25, v26, v25
Cv 24,24,25
v_accvgpr_write_b32 a160, v24
v_accvgpr_read_b32 v25, a161
v_lshlrev_b32 v24, 16, v25
v_and_b32 v25, 0xffff0000, v25
v_mul_f32_e32 v24, v26, v24
v_mul_f32_e32 v25, v26, v25
Cv 24,24,25
v_accvgpr_write_b32 a161, v24
v_accvgpr_read_b32 v25, a162
v_lshlrev_b32 v24, 16, v25
v_and_b32 v25, 0xffff0000, v25
v_mul_f32_e32 v24, v26, v24
v_mul_f32_e32 v25, v26, v25
Cv 24,24,25
v_accvgpr_write_b32 a162, v24
v_accvgpr_read_b32 v25, a163
v_lshlrev_b32 v24, 16, v25
v_and_b32 v25, 0xffff0000, v25
v_mul_f32_e32 v24, v26, v24
v_mul_f32_e32 v25, v26, v25
Cv 24,24,25
v_accvgpr_write_b32 a163, v24
v_accvgpr_read_b32 v25, a168
v_lshlrev_b32 v24, 16, v25
v_and_b32 v25, 0xffff0000, v25
v_mul_f32_e32 v24, v26, v24
v_mul_f32_e32 v25, v26, v25
)" R"(
Cv 24,24,25
v_accvgpr_write_b32 a168, v24
v_accvgpr_read_b32 v25, a169
v_lshlrev_b32 v24, 16, v25
v_and_b32 v25, 0xffff0000, v25
v_mul_f32_e32 v24, v26, v24
v_mul_f32_e32 v25, v26, v25
Cv 24,24,25
v_accvgpr_write_b32 a169, v24
v_accvgpr_read_b32 v25, a170
v_lshlrev_b32 v24, 16, v25
v_and_b32 v25, 0xffff0000, v25
v_mul_f32_e32 v24, v26, v24
v_mul_f32_e32 v25, v26, v25
Cv 24,24,25
v_accvgpr_write_b32 a170, v24
v_accvgpr_read_b32 v25, a171
v_lshlrev_b32 v24, 16, v25
v_and_b32 v25, 0xffff0000, v25
v_mul_f32_e32 v24, v26, v24
v_mul_f32_e32 v25, v26, v25
Cv 24,24,25
v_accvgpr_write_b32 a171, v24
v_accvgpr_read_b32 v25, a176
v_lshlrev_b32 v24, 16, v25
v_and_b32 v25, 0xffff0000, v25
v_mul_f32_e32 v24, v26, v24
v_mul_f32_e32 v25, v26, v25
Cv 24,24,25
v_accvgpr_write_b32 a176, v24
v_accvgpr_read_b32 v25, a177
v_lshlrev_b32 v24, 16, v25
v_and_b32 v25, 0xffff0000, v25
v_mul_f32_e32 v24, v26, v24
v_mul_f32_e32 v25, v26, v25
Cv 24,24,25
v_accvgpr_write_b32 a177, v24
v_accvgpr_read_b32 v25, a178
v_lshlrev_b32 v24, 16, v25
v_and_b32 v25, 0xffff0000, v25
v_mul_f32_e32 v24, v26, v24
v_mul_f32_e32 v25, v26, v25
Cv 24,24,25
v_accvgpr_write_b32 a178, v24
v_accvgpr_read_b32 v25, a179
v_lshlrev_b32 v24, 16, v25
v_and_b32 v25, 0xffff0000, v25
v_mul_f32_e32 v24, v26, v24
v_mul_f32_e32 v25, v26, v25
Cv 24,24,25
v_accvgpr_write_b32 a179, v24
v_accvgpr_read_b32 v25, a184
v_lshlrev_b32 v24, 16, v25
v_and_b32 v25, 0xffff0000, v25
v_mul_f32_e32 v24, v26, v24
v_mul_f32_e32 v25, v26, v25
Cv 24,24,25
v_accvgpr_write_b32 a184, v24
v_accvgpr_read_b32 v25, a185
v_lshlrev_b32 v24, 16, v25
v_and_b32 v25, 0xffff0000, v25
v_mul_f32_e32 v24, v26, v24
v_mul_f32_e32 v25, v26, v25
Cv 24,24,25
)" R"(
v_accvgpr_write_b32 a185, v24
v_accvgpr_read_b32 v25, a186
v_lshlrev_b32 v24, 16, v25
v_and_b32 v25, 0xffff0000, v25
v_mul_f32_e32 v24, v26, v24
v_mul_f32_e32 v25, v26, v25
Cv 24,24,25
v_accvgpr_write_b32 a186, v24
v_accvgpr_read_b32 v25, a187
v_lshlrev_b32 v24, 16, v25
v_and_b32 v25, 0xffff0000, v25
v_mul_f32_e32 v24, v26, v24
v_mul_f32_e32 v25, v26, v25
Cv 24,24,25
v_accvgpr_write_b32 a187, v24
v_mov_b32 v25, v65
v_mov_b32 v24, v25
s_nop 1
v_permlane32_swap_b32_e32 v25, v24
v_add_f32_e32 v25, v25, v24
v_mov_b32 v24, v25
s_nop 1
v_permlane16_swap_b32_e32 v25, v24
v_add_f32_e32 v25, v25, v24
v_cmp_lt_f32_e32 vcc, 0, v25
s_nop 1
v_cndmask_b32_e32 v67, v20, v67, vcc
v_sub_f32_e32 v29, v20, v67
v_min_f32_e32 v27, 0, v29
v_exp_f32_e32 v27, v27
v_max_f32_e32 v29, 0, v29
v_sub_f32_e32 v24, 0, v29
v_exp_f32_e32 v24, v24
v_mul_f32_e32 v65, v27, v65
v_mul_f32_e32 v13, v24, v13
v_mul_f32_e32 v136, v24, v136
v_mul_f32_e32 v137, v24, v137
v_mul_f32_e32 v138, v24, v138
v_mul_f32_e32 v139, v24, v139
v_mul_f32_e32 v140, v24, v140
v_mul_f32_e32 v141, v24, v141
v_mul_f32_e32 v142, v24, v142
v_mul_f32_e32 v143, v24, v143
v_mul_f32_e32 v152, v24, v152
v_mul_f32_e32 v153, v24, v153
v_mul_f32_e32 v154, v24, v154
v_mul_f32_e32 v155, v24, v155
v_mul_f32_e32 v156, v24, v156
v_mul_f32_e32 v157, v24, v157
v_mul_f32_e32 v158, v24, v158
v_mul_f32_e32 v159, v24, v159
v_mul_f32_e32 v168, v24, v168
v_mul_f32_e32 v169, v24, v169
v_mul_f32_e32 v170, v24, v170
v_mul_f32_e32 v171, v24, v171
v_mul_f32_e32 v172, v24, v172
v_mul_f32_e32 v173, v24, v173
v_mul_f32_e32 v174, v24, v174
v_mul_f32_e32 v175, v24, v175
v_mul_f32_e32 v184, v24, v184
v_mul_f32_e32 v185, v24, v185
v_mul_f32_e32 v186, v24, v186
v_mul_f32_e32 v187, v24, v187
v_mul_f32_e32 v188, v24, v188
)" R"(
v_mul_f32_e32 v189, v24, v189
v_mul_f32_e32 v190, v24, v190
v_mul_f32_e32 v191, v24, v191
v_mul_f32_e32 v200, v24, v200
v_mul_f32_e32 v201, v24, v201
v_mul_f32_e32 v202, v24, v202
v_mul_f32_e32 v203, v24, v203
v_mul_f32_e32 v204, v24, v204
v_mul_f32_e32 v205, v24, v205
v_mul_f32_e32 v206, v24, v206
v_mul_f32_e32 v207, v24, v207
v_mul_f32_e32 v216, v24, v216
v_mul_f32_e32 v217, v24, v217
v_mul_f32_e32 v218, v24, v218
v_mul_f32_e32 v219, v24, v219
v_mul_f32_e32 v220, v24, v220
v_mul_f32_e32 v221, v24, v221
v_mul_f32_e32 v222, v24, v222
v_mul_f32_e32 v223, v24, v223
v_mul_f32_e32 v232, v24, v232
v_mul_f32_e32 v233, v24, v233
v_mul_f32_e32 v234, v24, v234
v_mul_f32_e32 v235, v24, v235
v_mul_f32_e32 v236, v24, v236
v_mul_f32_e32 v237, v24, v237
v_mul_f32_e32 v238, v24, v238
v_mul_f32_e32 v239, v24, v239
v_mul_f32_e32 v248, v24, v248
v_mul_f32_e32 v249, v24, v249
v_mul_f32_e32 v250, v24, v250
v_mul_f32_e32 v251, v24, v251
v_mul_f32_e32 v252, v24, v252
v_mul_f32_e32 v253, v24, v253
v_mul_f32_e32 v254, v24, v254
v_mul_f32_e32 v255, v24, v255
v_sub_f32_e32 v20, v20, v29
v_sub_f32_e32 v21, v21, v29
v_sub_f32_e32 v22, v22, v29
v_sub_f32_e32 v23, v23, v29
v_sub_f32_e32 v100, v100, v29
v_sub_f32_e32 v101, v101, v29
v_sub_f32_e32 v102, v102, v29
v_sub_f32_e32 v103, v103, v29
v_sub_f32_e32 v108, v108, v29
v_sub_f32_e32 v109, v109, v29
v_sub_f32_e32 v110, v110, v29
v_sub_f32_e32 v111, v111, v29
v_sub_f32_e32 v116, v116, v29
v_sub_f32_e32 v117, v117, v29
v_sub_f32_e32 v118, v118, v29
v_sub_f32_e32 v119, v119, v29
v_sub_f32_e32 v124, v124, v29
v_sub_f32_e32 v125, v125, v29
v_sub_f32_e32 v126, v126, v29
v_sub_f32_e32 v127, v127, v29
v_accvgpr_read_b32 v25, a132
v_lshlrev_b32 v24, 16, v25
v_and_b32 v25, 0xffff0000, v25
v_mul_f32_e32 v24, v27, v24
v_mul_f32_e32 v25, v27, v25
Cv 24,24,25
v_accvgpr_write_b32 a132, v24
v_accvgpr_read_b32 v25, a133
v_lshlrev_b32 v24, 16, v25
)" R"(
v_and_b32 v25, 0xffff0000, v25
v_mul_f32_e32 v24, v27, v24
v_mul_f32_e32 v25, v27, v25
Cv 24,24,25
v_accvgpr_write_b32 a133, v24
v_accvgpr_read_b32 v25, a134
v_lshlrev_b32 v24, 16, v25
v_and_b32 v25, 0xffff0000, v25
v_mul_f32_e32 v24, v27, v24
v_mul_f32_e32 v25, v27, v25
Cv 24,24,25
v_accvgpr_write_b32 a134, v24
v_accvgpr_read_b32 v25, a135
v_lshlrev_b32 v24, 16, v25
v_and_b32 v25, 0xffff0000, v25
v_mul_f32_e32 v24, v27, v24
v_mul_f32_e32 v25, v27, v25
Cv 24,24,25
v_accvgpr_write_b32 a135, v24
v_accvgpr_read_b32 v25, a140
v_lshlrev_b32 v24, 16, v25
v_and_b32 v25, 0xffff0000, v25
v_mul_f32_e32 v24, v27, v24
v_mul_f32_e32 v25, v27, v25
Cv 24,24,25
v_accvgpr_write_b32 a140, v24
v_accvgpr_read_b32 v25, a141
v_lshlrev_b32 v24, 16, v25
v_and_b32 v25, 0xffff0000, v25
v_mul_f32_e32 v24, v27, v24
v_mul_f32_e32 v25, v27, v25
Cv 24,24,25
v_accvgpr_write_b32 a141, v24
v_accvgpr_read_b32 v25, a142
v_lshlrev_b32 v24, 16, v25
v_and_b32 v25, 0xffff0000, v25
v_mul_f32_e32 v24, v27, v24
v_mul_f32_e32 v25, v27, v25
Cv 24,24,25
v_accvgpr_write_b32 a142, v24
v_accvgpr_read_b32 v25, a143
v_lshlrev_b32 v24, 16, v25
v_and_b32 v25, 0xffff0000, v25
v_mul_f32_e32 v24, v27, v24
v_mul_f32_e32 v25, v27, v25
Cv 24,24,25
v_accvgpr_write_b32 a143, v24
v_accvgpr_read_b32 v25, a148
v_lshlrev_b32 v24, 16, v25
v_and_b32 v25, 0xffff0000, v25
v_mul_f32_e32 v24, v27, v24
v_mul_f32_e32 v25, v27, v25
Cv 24,24,25
v_accvgpr_write_b32 a148, v24
v_accvgpr_read_b32 v25, a149
v_lshlrev_b32 v24, 16, v25
v_and_b32 v25, 0xffff0000, v25
v_mul_f32_e32 v24, v27, v24
v_mul_f32_e32 v25, v27, v25
Cv 24,24,25
v_accvgpr_write_b32 a149, v24
v_accvgpr_read_b32 v25, a150
v_lshlrev_b32 v24, 16, v25
v_and_b32 v25, 0xffff0000, v25
)" R"(
v_mul_f32_e32 v24, v27, v24
v_mul_f32_e32 v25, v27, v25
Cv 24,24,25
v_accvgpr_write_b32 a150, v24
v_accvgpr_read_b32 v25, a151
v_lshlrev_b32 v24, 16, v25
v_and_b32 v25, 0xffff0000, v25
v_mul_f32_e32 v24, v27, v24
v_mul_f32_e32 v25, v27, v25
Cv 24,24,25
v_accvgpr_write_b32 a151, v24
v_accvgpr_read_b32 v25, a156
v_lshlrev_b32 v24, 16, v25
v_and_b32 v25, 0xffff0000, v25
v_mul_f32_e32 v24, v27, v24
v_mul_f32_e32 v25, v27, v25
Cv 24,24,25
v_accvgpr_write_b32 a156, v24
v_accvgpr_read_b32 v25, a157
v_lshlrev_b32 v24, 16, v25
v_and_b32 v25, 0xffff0000, v25
v_mul_f32_e32 v24, v27, v24
v_mul_f32_e32 v25, v27, v25
Cv 24,24,25
v_accvgpr_write_b32 a157, v24
v_accvgpr_read_b32 v25, a158
v_lshlrev_b32 v24, 16, v25
v_and_b32 v25, 0xffff0000, v25
v_mul_f32_e32 v24, v27, v24
v_mul_f32_e32 v25, v27, v25
Cv 24,24,25
v_accvgpr_write_b32 a158, v24
v_accvgpr_read_b32 v25, a159
v_lshlrev_b32 v24, 16, v25
v_and_b32 v25, 0xffff0000, v25
v_mul_f32_e32 v24, v27, v24
v_mul_f32_e32 v25, v27, v25
Cv 24,24,25
v_accvgpr_write_b32 a159, v24
v_accvgpr_read_b32 v25, a164
v_lshlrev_b32 v24, 16, v25
v_and_b32 v25, 0xffff0000, v25
v_mul_f32_e32 v24, v27, v24
v_mul_f32_e32 v25, v27, v25
Cv 24,24,25
v_accvgpr_write_b32 a164, v24
v_accvgpr_read_b32 v25, a165
v_lshlrev_b32 v24, 16, v25
v_and_b32 v25, 0xffff0000, v25
v_mul_f32_e32 v24, v27, v24
v_mul_f32_e32 v25, v27, v25
Cv 24,24,25
v_accvgpr_write_b32 a165, v24
v_accvgpr_read_b32 v25, a166
v_lshlrev_b32 v24, 16, v25
v_and_b32 v25, 0xffff0000, v25
v_mul_f32_e32 v24, v27, v24
v_mul_f32_e32 v25, v27, v25
Cv 24,24,25
v_accvgpr_write_b32 a166, v24
v_accvgpr_read_b32 v25, a167
v_lshlrev_b32 v24, 16, v25
v_and_b32 v25, 0xffff0000, v25
v_mul_f32_e32 v24, v27, v24
)" R"(
v_mul_f32_e32 v25, v27, v25
Cv 24,24,25
v_accvgpr_write_b32 a167, v24
v_accvgpr_read_b32 v25, a172
v_lshlrev_b32 v24, 16, v25
v_and_b32 v25, 0xffff0000, v25
v_mul_f32_e32 v24, v27, v24
v_mul_f32_e32 v25, v27, v25
Cv 24,24,25
v_accvgpr_write_b32 a172, v24
v_accvgpr_read_b32 v25, a173
v_lshlrev_b32 v24, 16, v25
v_and_b32 v25, 0xffff0000, v25
v_mul_f32_e32 v24, v27, v24
v_mul_f32_e32 v25, v27, v25
Cv 24,24,25
v_accvgpr_write_b32 a173, v24
v_accvgpr_read_b32 v25, a174
v_lshlrev_b32 v24, 16, v25
v_and_b32 v25, 0xffff0000, v25
v_mul_f32_e32 v24, v27, v24
v_mul_f32_e32 v25, v27, v25
Cv 24,24,25
v_accvgpr_write_b32 a174, v24
v_accvgpr_read_b32 v25, a175
v_lshlrev_b32 v24, 16, v25
v_and_b32 v25, 0xffff0000, v25
v_mul_f32_e32 v24, v27, v24
v_mul_f32_e32 v25, v27, v25
Cv 24,24,25
v_accvgpr_write_b32 a175, v24
v_accvgpr_read_b32 v25, a180
v_lshlrev_b32 v24, 16, v25
v_and_b32 v25, 0xffff0000, v25
v_mul_f32_e32 v24, v27, v24
v_mul_f32_e32 v25, v27, v25
Cv 24,24,25
v_accvgpr_write_b32 a180, v24
v_accvgpr_read_b32 v25, a181
v_lshlrev_b32 v24, 16, v25
v_and_b32 v25, 0xffff0000, v25
v_mul_f32_e32 v24, v27, v24
v_mul_f32_e32 v25, v27, v25
Cv 24,24,25
v_accvgpr_write_b32 a181, v24
v_accvgpr_read_b32 v25, a182
v_lshlrev_b32 v24, 16, v25
v_and_b32 v25, 0xffff0000, v25
v_mul_f32_e32 v24, v27, v24
v_mul_f32_e32 v25, v27, v25
Cv 24,24,25
v_accvgpr_write_b32 a182, v24
v_accvgpr_read_b32 v25, a183
v_lshlrev_b32 v24, 16, v25
v_and_b32 v25, 0xffff0000, v25
v_mul_f32_e32 v24, v27, v24
v_mul_f32_e32 v25, v27, v25
Cv 24,24,25
v_accvgpr_write_b32 a183, v24
v_accvgpr_read_b32 v25, a188
v_lshlrev_b32 v24, 16, v25
v_and_b32 v25, 0xffff0000, v25
v_mul_f32_e32 v24, v27, v24
v_mul_f32_e32 v25, v27, v25
)" R"(
Cv 24,24,25
v_accvgpr_write_b32 a188, v24
v_accvgpr_read_b32 v25, a189
v_lshlrev_b32 v24, 16, v25
v_and_b32 v25, 0xffff0000, v25
v_mul_f32_e32 v24, v27, v24
v_mul_f32_e32 v25, v27, v25
Cv 24,24,25
v_accvgpr_write_b32 a189, v24
v_accvgpr_read_b32 v25, a190
v_lshlrev_b32 v24, 16, v25
v_and_b32 v25, 0xffff0000, v25
v_mul_f32_e32 v24, v27, v24
v_mul_f32_e32 v25, v27, v25
Cv 24,24,25
v_accvgpr_write_b32 a190, v24
v_accvgpr_read_b32 v25, a191
v_lshlrev_b32 v24, 16, v25
v_and_b32 v25, 0xffff0000, v25
v_mul_f32_e32 v24, v27, v24
v_mul_f32_e32 v25, v27, v25
Cv 24,24,25
v_accvgpr_write_b32 a191, v24
v_exp_f32_e32 v48, v96
v_exp_f32_e32 v24, v97
s_nop 0
v_add_f32_e32 v14, v48, v24
Cv 48,48,24
v_exp_f32_e32 v49, v98
v_exp_f32_e32 v25, v99
v_add_f32_e32 v14, v14, v49
v_add_f32_e32 v14, v14, v25
Cv 49,49,25
v_exp_f32_e32 v50, v104
v_exp_f32_e32 v26, v105
v_add_f32_e32 v14, v14, v50
v_add_f32_e32 v14, v14, v26
Cv 50,50,26
v_exp_f32_e32 v51, v106
v_exp_f32_e32 v27, v107
v_add_f32_e32 v14, v14, v51
v_add_f32_e32 v14, v14, v27
Cv 51,51,27
v_exp_f32_e32 v52, v100
v_exp_f32_e32 v28, v101
s_nop 0
v_add_f32_e32 v15, v52, v28
Cv 52,52,28
v_exp_f32_e32 v53, v102
v_exp_f32_e32 v29, v103
v_add_f32_e32 v15, v15, v53
v_add_f32_e32 v15, v15, v29
Cv 53,53,29
v_exp_f32_e32 v54, v108
v_exp_f32_e32 v24, v109
v_add_f32_e32 v15, v15, v54
v_add_f32_e32 v15, v15, v24
Cv 54,54,24
v_exp_f32_e32 v55, v110
v_exp_f32_e32 v25, v111
v_add_f32_e32 v15, v15, v55
v_add_f32_e32 v15, v15, v25
Cv 55,55,25
s_nop 1
)" R"(
s_branch .LzMRR
.Lzsp23:
s_sub_u32 s53, s53, 1
s_cbranch_scc1 .Lzsq23
s_sleep 1
buffer_load_dword v4, v7, s[20:23], s52 offen offset:0 sc0 sc1
s_waitcnt vmcnt(0)
v_cmp_eq_u32_e32 vcc, s3, v4
s_andn2_b64 s[28:29], 1, vcc
s_cbranch_scc1 .Lzsp23
s_branch .Lzok23
.Lzsq23:
s_mov_b32 s53, 0
s_branch .Lzok23
.Lzsp24:
s_sub_u32 s53, s53, 1
s_cbranch_scc1 .Lzsq24
s_sleep 1
buffer_load_dword v4, v7, s[20:23], s52 offen offset:128 sc0 sc1
s_waitcnt vmcnt(0)
v_cmp_eq_u32_e32 vcc, s3, v4
s_andn2_b64 s[28:29], 1, vcc
s_cbranch_scc1 .Lzsp24
s_branch .Lzok24
.Lzsq24:
s_mov_b32 s53, 0
s_branch .Lzok24
.LzBB:
v_lshlrev_b32 v95, 7, v0
s_mov_b32 s28, 0xc10c000
s_add_u32 s29, s28, 6144
.LzBB1:
buffer_load_dword v92, v95, s[20:23], s28 offen sc0 sc1
buffer_load_dword v93, v95, s[20:23], s29 offen sc0 sc1
s_waitcnt vmcnt(0)
v_cmp_ne_u32_e32 vcc, s3, v92
v_cmp_ne_u32_e64 s[30:31], s3, v93
s_or_b64 vcc, vcc, s[30:31]
s_cbranch_scc0 .LzBB2
s_sub_u32 s53, s53, 1
s_cbranch_scc0 .LzBB1
s_mov_b32 s53, 0
.LzBB2:
s_mul_i32 s28, s56, 7168
s_add_u32 s28, s28, 0xc158000
s_add_u32 s29, s28, 4096
buffer_load_dwordx4 v[64:67], v1, s[20:23], s28 offen offset:0 sc0 sc1
buffer_load_dwordx4 v[68:71], v1, s[20:23], s28 offen offset:1024 sc0 sc1
buffer_load_dwordx4 v[72:75], v1, s[20:23], s28 offen offset:2048 sc0 sc1
buffer_load_dwordx4 v[76:79], v1, s[20:23], s28 offen offset:3072 sc0 sc1
buffer_load_dwordx4 v[80:83], v1, s[20:23], s29 offen offset:0 sc0 sc1
buffer_load_dwordx4 v[84:87], v1, s[20:23], s29 offen offset:1024 sc0 sc1
buffer_load_dwordx4 v[88:91], v1, s[20:23], s29 offen offset:2048 sc0 sc1
s_cmp_gt_u32 s56, 27
s_cselect_b32 s28, 16, 0
s_cselect_b32 s29, 28, 0
s_lshr_b32 s28, s63, s28
s_and_b32 s28, s28, 0x7ff
s_sub_u32 s29, s56, s29
s_lshl_b32 s29, s29, 5
s_mul_i32 s83, s28, 896
s_add_u32 s29, s29, s83
v_and_b32 v95, 3, v0
v_lshlrev_b32 v95, 3, v95
)" R"(
buffer_load_dwordx2 v[92:93], v95, s[12:15], s29 offen
s_lshl4_add_u32 s28, s28, 0xc10000c
v_mov_b32 v94, 0
buffer_load_dword v94, v94, s[20:23], s28 offen sc0 sc1
s_waitcnt vmcnt(0)
v_add_f32_e32 v64, v64, v68
v_add_f32_e32 v65, v65, v69
v_add_f32_e32 v66, v66, v70
v_add_f32_e32 v67, v67, v71
v_add_f32_e32 v72, v72, v76
v_add_f32_e32 v73, v73, v77
v_add_f32_e32 v74, v74, v78
v_add_f32_e32 v75, v75, v79
v_add_f32_e32 v80, v80, v84
v_add_f32_e32 v81, v81, v85
v_add_f32_e32 v82, v82, v86
v_add_f32_e32 v83, v83, v87
v_add_f32_e32 v64, v64, v72
v_add_f32_e32 v65, v65, v73
v_add_f32_e32 v66, v66, v74
v_add_f32_e32 v67, v67, v75
v_add_f32_e32 v80, v80, v88
v_add_f32_e32 v81, v81, v89
v_add_f32_e32 v82, v82, v90
v_add_f32_e32 v83, v83, v91
v_add_f32_e32 v64, v64, v80
v_add_f32_e32 v65, v65, v81
v_add_f32_e32 v66, v66, v82
v_add_f32_e32 v67, v67, v83
v_mov_b32 v68, v64
s_nop 1
v_permlane32_swap_b32_e32 v64, v68
v_add_f32_e32 v64, v64, v68
v_mov_b32 v68, v64
s_nop 1
v_permlane16_swap_b32_e32 v64, v68
v_add_f32_e32 v64, v64, v68
v_mov_b32 v68, v65
s_nop 1
v_permlane32_swap_b32_e32 v65, v68
v_add_f32_e32 v65, v65, v68
v_mov_b32 v68, v65
s_nop 1
v_permlane16_swap_b32_e32 v65, v68
v_add_f32_e32 v65, v65, v68
v_mov_b32 v68, v66
s_nop 1
v_permlane32_swap_b32_e32 v66, v68
v_add_f32_e32 v66, v66, v68
v_mov_b32 v68, v66
s_nop 1
v_permlane16_swap_b32_e32 v66, v68
v_add_f32_e32 v66, v66, v68
v_mov_b32 v68, v67
s_nop 1
v_permlane32_swap_b32_e32 v67, v68
v_add_f32_e32 v67, v67, v68
v_mov_b32 v68, v67
s_nop 1
v_permlane16_swap_b32_e32 v67, v68
v_add_f32_e32 v67, v67, v68
v_add_f32_dpp v64, v64, v64 row_ror:8 row_mask:0xf bank_mask:0xf
v_add_f32_dpp v65, v65, v65 row_ror:8 row_mask:0xf bank_mask:0xf
v_add_f32_dpp v66, v66, v66 row_ror:8 row_mask:0xf bank_mask:0xf
)" R"(
v_add_f32_dpp v67, v67, v67 row_ror:8 row_mask:0xf bank_mask:0xf
v_add_f32_dpp v64, v64, v64 row_ror:4 row_mask:0xf bank_mask:0xf
v_add_f32_dpp v65, v65, v65 row_ror:4 row_mask:0xf bank_mask:0xf
v_add_f32_dpp v66, v66, v66 row_ror:4 row_mask:0xf bank_mask:0xf
v_add_f32_dpp v67, v67, v67 row_ror:4 row_mask:0xf bank_mask:0xf
v_mul_f32_e32 v94, 0xbfb8aa3b, v94
v_exp_f32_e32 v94, v94
Cv 65,64,65
v_lshlrev_b32 v64, 16, v65
v_and_b32 v65, 0xffff0000, v65
v_add_f32_e32 v94, 1.0, v94
v_rcp_f32_e32 v94, v94
Cv 67,66,67
v_lshlrev_b32 v66, 16, v67
v_and_b32 v67, 0xffff0000, v67
Cv 94,94,94
v_lshlrev_b32 v94, 16, v94
v_mul_f32_e32 v64, v94, v64
v_mul_f32_e32 v65, v94, v65
v_mul_f32_e32 v66, v94, v66
v_mul_f32_e32 v67, v94, v67
Cv 65,64,65
v_lshlrev_b32 v64, 16, v65
v_and_b32 v65, 0xffff0000, v65
Cv 67,66,67
v_lshlrev_b32 v66, 16, v67
v_and_b32 v67, 0xffff0000, v67
v_lshlrev_b32 v68, 16, v92
v_and_b32 v69, 0xffff0000, v92
v_lshlrev_b32 v70, 16, v93
v_and_b32 v71, 0xffff0000, v93
v_add_f32_e32 v64, v64, v68
v_add_f32_e32 v65, v65, v69
v_add_f32_e32 v66, v66, v70
v_add_f32_e32 v67, v67, v71
Cv 64,64,65
Cv 66,66,67
v_mov_b32 v65, s3
v_mov_b32 v67, s3
v_lshrrev_b32 v95, 2, v0
v_min_u32 v95, 1, v95
v_lshl_or_b32 v95, v95, 30, v1
s_lshl_b32 s28, s56, 7
s_add_u32 s28, s28, 0xc109000
buffer_store_dwordx4 v[64:67], v95, s[20:23], s28 offen sc0 sc1
buffer_load_dword v4, v165, s[20:23], s40 offen sc0 sc1
s_waitcnt vmcnt(0)
s_branch .LzBBR
.LzPRB:
s_lshl_b32 s24, s34, 21
s_mul_i32 s25, s33, 2304
s_add_u32 s24, s24, s25
s_add_u32 s24, s24, 0x6100000
s_and_b32 s25, s36, 0x2000
s_xor_b32 s24, s24, s25
s_xor_b32 s25, s24, 0x2000
buffer_store_dword v0, v7, s[20:23], s24 offen sc0 sc1
buffer_store_dword v0, v7, s[20:23], s25 offen sc0 sc1
s_mov_b32 s26, -1
s_mov_b32 s27, -1
s_waitcnt vmcnt(0)
s_memtime s[28:29]
s_waitcnt lgkmcnt(0)
buffer_store_dword v0, v7, s[20:23], s24 offen sc0 sc1
)" R"(
s_waitcnt vmcnt(0)
s_memtime s[30:31]
s_waitcnt lgkmcnt(0)
s_sub_u32 s30, s30, s28
s_min_u32 s26, s26, s30
s_memtime s[28:29]
s_waitcnt lgkmcnt(0)
buffer_store_dword v0, v7, s[20:23], s25 offen sc0 sc1
s_waitcnt vmcnt(0)
s_memtime s[30:31]
s_waitcnt lgkmcnt(0)
s_sub_u32 s30, s30, s28
s_min_u32 s27, s27, s30
s_memtime s[28:29]
s_waitcnt lgkmcnt(0)
buffer_store_dword v0, v7, s[20:23], s24 offen sc0 sc1
s_waitcnt vmcnt(0)
s_memtime s[30:31]
s_waitcnt lgkmcnt(0)
s_sub_u32 s30, s30, s28
s_min_u32 s26, s26, s30
s_memtime s[28:29]
s_waitcnt lgkmcnt(0)
buffer_store_dword v0, v7, s[20:23], s25 offen sc0 sc1
s_waitcnt vmcnt(0)
s_memtime s[30:31]
s_waitcnt lgkmcnt(0)
s_sub_u32 s30, s30, s28
s_min_u32 s27, s27, s30
s_bitcmp1_b32 s33, 0
s_cselect_b32 s28, s27, s26
s_cselect_b32 s29, s26, s27
v_mov_b32 v32, s28
v_mov_b32 v33, s29
s_lshl_b32 s24, s33, 3
v_mov_b32 v34, s24
v_mov_b32 v35, 0
ds_write_b64 v34, v[32:33]
s_waitcnt lgkmcnt(0)
s_barrier
ds_read_b128 v[36:39], v35
ds_read_b128 v[40:43], v35 offset:16
s_mul_i32 s24, s3, 1664525
s_add_u32 s24, s24, 1013904223
s_and_b32 s24, s24, -2
s_lshl_b32 s25, s34, 6
s_add_u32 s25, s25, 0xc108800
s_cmp_lg_u32 s33, 0
s_cmov_b32 s25, 0x40000000
s_waitcnt lgkmcnt(0)
v_min3_u32 v32, v36, v38, v40
v_min3_u32 v33, v37, v39, v41
v_min_u32 v32, v32, v42
v_min_u32 v33, v33, v43
v_cmp_gt_u32_e32 vcc, v32, v33
v_mov_b32 v34, s24
s_nop 0
v_addc_co_u32_e32 v34, vcc, 0, v34, vcc
buffer_store_dword v34, v7, s[20:23], s25 offen sc0 sc1
s_barrier
s_branch .LzPRR
.LzHOP:
s_lshr_b32 s82, s2, 7
s_lshl1_add_u32 s82, s82, s91
)" R"(
s_and_b32 s83, s34, 1
s_lshl1_add_u32 s82, s82, s83
s_lshl2_add_u32 s82, s82, s33
s_lshl2_add_u32 s82, s82, 16
s_getpc_b64 vcc
s_add_u32 vcc_lo, vcc_lo, s82
s_addc_u32 vcc_hi, vcc_hi, 0
s_mov_b64 exec, 0
s_setpc_b64 vcc
s_branch .LzH21
s_branch .LzH26
s_branch .LzH25
s_branch .LzH24
s_branch .LzH23
s_branch .LzH33
s_branch .LzH32
s_branch .LzH22
s_branch .LzH21
s_branch .LzH31
s_branch .LzH30
s_branch .LzH29
s_branch .LzH28
s_branch .LzH27
s_branch .LzH32
s_branch .LzH22
s_branch .LzH3
s_branch .LzH2
s_branch .LzH10
s_branch .LzH9
s_branch .LzH8
s_branch .LzH4
s_branch .LzH1
s_branch .LzH0
s_branch .LzH7
s_branch .LzH6
s_branch .LzH5
s_branch .LzH9
s_branch .LzH8
s_branch .LzH4
s_branch .LzH1
s_branch .LzH0
.LzHR:
s_mov_b64 exec, -1
s_setpc_b64 s[30:31]
.LzA0:
s_mov_b32 s63, 0x6040200
s_mov_b32 s28, 0x7050312
s_bitcmp1_b32 s2, 5
s_cmov_b32 s63, 0x18161401
s_cmov_b32 s28, 0x19171513
s_bitcmp1_b32 s2, 6
s_cmov_b32 s63, s28
s_lshl_b32 s28, s33, 3
s_lshr_b32 s28, s63, s28
s_and_b32 s28, s28, 255
s_lshl_b32 s38, s28, 17
s_add_u32 s38, s38, 0xff80
s_lshl_b32 s39, s33, 16
s_lshl_b32 s28, s34, 9
s_add_u32 s39, s39, s28
s_sub_u32 s39, s39, 0x40000
s_lshl_b32 s37, s33, 12
s_add_u32 s37, s37, 0x10000
v_lshrrev_b32 v28, 4, v0
)" R"(
v_and_b32 v29, 7, v0
v_xor_b32 v28, v28, v29
v_lshlrev_b32 v28, 4, v28
v_lshrrev_b32 v29, 3, v0
v_lshl_or_b32 v8, v29, 11, v28
v_xor_b32 v9, 64, v8
v_add_u32 v9, 15360, v9
v_add_u32 v10, 30720, v8
v_add_u32 v11, 30720, v9
s_and_b32 s28, s33, 1
s_lshl_b32 s28, s28, 7
v_lshrrev_b32 v29, 5, v0
v_mul_u32_u24 v29, 0x2220, v29
v_xor_b32 v29, v29, v1
v_xor_b32 v12, s28, v29
v_xor_b32 v13, 64, v12
v_add_u32 v13, 15360, v13
v_add_u32 v14, 30720, v12
v_add_u32 v15, 30720, v13
v_min_u32 v7, 1, v0
v_lshlrev_b32 v7, 30, v7
s_waitcnt lgkmcnt(0)
s_mov_b32 s28, 0xc108400
s_load_dword s3, s[74:75], s28
s_mov_b32 s20, s74
s_mov_b32 s21, s75
s_mov_b32 s22, 0xc1ba000
s_mov_b32 s23, 0x20000
s_mov_b32 s4, s66
s_mov_b32 s5, s67
s_mov_b32 s6, 0x400000
s_mov_b32 s7, 0x20000
s_cmp_eq_u32 s91, 1
s_cselect_b32 s8, s72, s70
s_cselect_b32 s9, s73, s71
s_mov_b32 s10, 0x800000
s_mov_b32 s11, 0x20000
s_lshr_b32 s28, s2, 3
s_cmp_eq_u32 s28, 14
s_cbranch_scc1 .LzPRB
.LzPRR:
s_add_u32 m0, s37, 0
s_add_u32 s39, s39, 0x40000
buffer_load_dwordx4 v12, s[8:11], s39 offen offset:0 lds
buffer_load_dwordx4 v13, s[8:11], s39 offen offset:1024 lds
buffer_load_dwordx4 v14, s[8:11], s39 offen offset:2048 lds
buffer_load_dwordx4 v15, s[8:11], s39 offen offset:3072 lds
s_add_u32 m0, s36, 0
s_add_u32 s38, s38, 0xffff0080
buffer_load_dwordx4 v8, s[4:7], s38 offen offset:0 lds
buffer_load_dwordx4 v9, s[4:7], s38 offen offset:1024 lds
buffer_load_dwordx4 v10, s[4:7], s38 offen offset:2048 lds
buffer_load_dwordx4 v11, s[4:7], s38 offen offset:3072 lds
s_add_u32 m0, s36, 4096
s_add_u32 s38, s38, 0x10000
buffer_load_dwordx4 v8, s[4:7], s38 offen offset:0 lds
buffer_load_dwordx4 v9, s[4:7], s38 offen offset:1024 lds
buffer_load_dwordx4 v10, s[4:7], s38 offen offset:2048 lds
buffer_load_dwordx4 v11, s[4:7], s38 offen offset:3072 lds
s_add_u32 m0, s37, 16384
s_add_u32 s39, s39, 0x40000
buffer_load_dwordx4 v12, s[8:11], s39 offen offset:0 lds
buffer_load_dwordx4 v13, s[8:11], s39 offen offset:1024 lds
buffer_load_dwordx4 v14, s[8:11], s39 offen offset:2048 lds
)" R"(
buffer_load_dwordx4 v15, s[8:11], s39 offen offset:3072 lds
s_mov_b32 s12, s64
s_mov_b32 s13, s65
s_mov_b32 s14, 0x1c0000
s_mov_b32 s15, 0x20000
s_and_b32 s94, s2, 127
s_lshl2_add_u32 s94, s94, s33
s_lshl_b32 s94, s94, 2
s_mul_i32 s29, s94, 896
v_mov_b32 v120, 0x40000000
v_cmp_gt_u32_e32 vcc, 56, v0
s_nop 1
v_cndmask_b32_e32 v120, v120, v1, vcc
buffer_load_dwordx4 v[96:99], v120, s[12:15], s29 offen offset:0
buffer_load_dwordx4 v[100:103], v120, s[12:15], s29 offen offset:896
buffer_load_dwordx4 v[104:107], v120, s[12:15], s29 offen offset:1792
buffer_load_dwordx4 v[108:111], v120, s[12:15], s29 offen offset:2688
s_mov_b32 s24, s76
s_mov_b32 s25, s77
s_mov_b32 s26, 0x380
s_mov_b32 s27, 0x20000
buffer_load_dwordx4 v[112:115], v120, s[24:27], 0 offen
s_mov_b32 s24, s78
s_mov_b32 s25, s79
s_mov_b32 s26, 0x380
s_mov_b32 s27, 0x20000
buffer_load_dwordx4 v[116:119], v120, s[24:27], 0 offen
s_add_u32 m0, s36, 32768
s_add_u32 s38, s38, 0xffff0080
buffer_load_dwordx4 v8, s[4:7], s38 offen offset:0 lds
buffer_load_dwordx4 v9, s[4:7], s38 offen offset:1024 lds
buffer_load_dwordx4 v10, s[4:7], s38 offen offset:2048 lds
buffer_load_dwordx4 v11, s[4:7], s38 offen offset:3072 lds
s_add_u32 m0, s36, 36864
s_add_u32 s38, s38, 0x10000
buffer_load_dwordx4 v8, s[4:7], s38 offen offset:0 lds
buffer_load_dwordx4 v9, s[4:7], s38 offen offset:1024 lds
buffer_load_dwordx4 v10, s[4:7], s38 offen offset:2048 lds
buffer_load_dwordx4 v11, s[4:7], s38 offen offset:3072 lds
s_mov_b32 s16, s68
s_mov_b32 s17, s69
s_mov_b32 s18, 0x380000
s_mov_b32 s19, 0x20000
s_load_dwordx2 s[70:71], s[0:1], 88
s_load_dwordx2 s[74:75], s[0:1], 96
v_bfe_u32 v28, v0, 1, 3
v_lshrrev_b32 v29, 4, v0
v_xor_b32 v28, v28, v29
v_lshlrev_b32 v28, 4, v28
v_and_b32 v29, 15, v0
v_lshl_or_b32 v28, v29, 7, v28
s_lshr_b32 s28, s33, 1
s_lshl_b32 s28, s28, 12
v_or_b32 v16, s28, v28
v_xor_b32 v17, 64, v16
v_bfe_u32 v28, v0, 2, 3
v_lshlrev_b32 v28, 5, v28
v_and_b32 v29, 3, v0
v_lshl_or_b32 v28, v29, 3, v28
v_bfe_u32 v29, v0, 2, 2
v_lshl_or_b32 v28, v29, 9, v28
v_lshrrev_b32 v29, 4, v0
v_lshl_or_b32 v28, v29, 12, v28
s_and_b32 s28, s33, 1
)" R"(
s_lshl_b32 s28, s28, 8
s_bitset1_b32 s28, 16
v_or_b32 v18, s28, v28
v_xor_b32 v19, 32, v18
v_xor_b32 v20, 64, v18
v_xor_b32 v21, 96, v18
v_xor_b32 v22, 128, v18
v_xor_b32 v23, 160, v18
v_xor_b32 v24, 192, v18
v_xor_b32 v25, 224, v18
s_load_dwordx4 s[76:79], s[0:1], 72
s_waitcnt vmcnt(18) lgkmcnt(0)
s_barrier
Rt 0,18,0
Rt 2,18,2048
Rt 4,19,0
Rt 6,19,2048
Rt 8,20,0
Rt 10,20,2048
Rt 12,21,0
Rt 14,21,2048
Rt 16,22,0
Rt 18,22,2048
Rt 20,23,0
Rt 22,23,2048
Rt 24,24,0
Rt 26,24,2048
Rt 28,25,0
Rt 30,25,2048
Rb 32,16,0
Rb 36,16,2048
Rb 40,16,8192
Rb 44,16,10240
Rb 48,16,16384
Rb 52,16,18432
Rb 56,16,24576
Rb 60,16,26624
s_add_u32 m0, s37, 32768
s_add_u32 s39, s39, 0x40000
buffer_load_dwordx4 v12, s[8:11], s39 offen offset:0 lds
buffer_load_dwordx4 v13, s[8:11], s39 offen offset:1024 lds
buffer_load_dwordx4 v14, s[8:11], s39 offen offset:2048 lds
buffer_load_dwordx4 v15, s[8:11], s39 offen offset:3072 lds
s_add_u32 m0, s37, 49152
s_add_u32 s39, s39, 0x40000
buffer_load_dwordx4 v12, s[8:11], s39 offen offset:0 lds
buffer_load_dwordx4 v13, s[8:11], s39 offen offset:1024 lds
buffer_load_dwordx4 v14, s[8:11], s39 offen offset:2048 lds
buffer_load_dwordx4 v15, s[8:11], s39 offen offset:3072 lds
s_lshl2_add_u32 s40, s2, s33
s_mul_i32 s40, s40, 7168
v_min_u32 v2, 55, v0
v_lshlrev_b32 v2, 7, v2
buffer_load_dword v6, v2, s[16:19], s40 offen
s_mul_i32 s3, s3, 1664525
s_add_u32 s3, s3, 1013904223
s_waitcnt vmcnt(17)
v_dot2_f32_bf16 v120, v96, v112, 0
v_dot2_f32_bf16 v121, v100, v112, 0
v_dot2_f32_bf16 v122, v104, v112, 0
v_dot2_f32_bf16 v123, v108, v112, 0
v_dot2_f32_bf16 v124, v96, v116, 0
v_dot2_f32_bf16 v125, v100, v116, 0
v_dot2_f32_bf16 v126, v104, v116, 0
v_dot2_f32_bf16 v127, v108, v116, 0
)" R"(
v_dot2_f32_bf16 v120, v97, v113, v120
v_dot2_f32_bf16 v121, v101, v113, v121
v_dot2_f32_bf16 v122, v105, v113, v122
v_dot2_f32_bf16 v123, v109, v113, v123
v_dot2_f32_bf16 v124, v97, v117, v124
v_dot2_f32_bf16 v125, v101, v117, v125
v_dot2_f32_bf16 v126, v105, v117, v126
v_dot2_f32_bf16 v127, v109, v117, v127
v_dot2_f32_bf16 v120, v98, v114, v120
v_dot2_f32_bf16 v121, v102, v114, v121
v_dot2_f32_bf16 v122, v106, v114, v122
v_dot2_f32_bf16 v123, v110, v114, v123
v_dot2_f32_bf16 v124, v98, v118, v124
v_dot2_f32_bf16 v125, v102, v118, v125
v_dot2_f32_bf16 v126, v106, v118, v126
v_dot2_f32_bf16 v127, v110, v118, v127
v_dot2_f32_bf16 v120, v99, v115, v120
v_dot2_f32_bf16 v121, v103, v115, v121
v_dot2_f32_bf16 v122, v107, v115, v122
v_dot2_f32_bf16 v123, v111, v115, v123
v_dot2_f32_bf16 v124, v99, v119, v124
v_dot2_f32_bf16 v125, v103, v119, v125
v_dot2_f32_bf16 v126, v107, v119, v126
v_dot2_f32_bf16 v127, v111, v119, v127
v_permlane32_swap_b32_e32 v120, v121
v_add_f32_e32 v120, v120, v121
v_permlane32_swap_b32_e32 v122, v123
v_add_f32_e32 v122, v122, v123
v_permlane32_swap_b32_e32 v124, v125
v_add_f32_e32 v124, v124, v125
v_permlane32_swap_b32_e32 v126, v127
v_add_f32_e32 v126, v126, v127
v_permlane16_swap_b32_e32 v120, v122
v_add_f32_e32 v120, v120, v122
v_permlane16_swap_b32_e32 v124, v126
v_add_f32_e32 v124, v124, v126
v_add_f32_dpp v120, v120, v120 row_ror:8 row_mask:0xf bank_mask:0xf
s_nop 0
v_add_f32_dpp v124, v124, v124 row_ror:8 row_mask:0xf bank_mask:0xf
v_add_f32_dpp v120, v120, v120 row_ror:4 row_mask:0xf bank_mask:0xf
s_nop 0
v_add_f32_dpp v124, v124, v124 row_ror:4 row_mask:0xf bank_mask:0xf
v_add_f32_dpp v120, v120, v120 row_ror:2 row_mask:0xf bank_mask:0xf
s_nop 0
v_add_f32_dpp v124, v124, v124 row_ror:2 row_mask:0xf bank_mask:0xf
v_add_f32_dpp v120, v120, v120 row_ror:1 row_mask:0xf bank_mask:0xf
s_nop 0
v_add_f32_dpp v124, v124, v124 row_ror:1 row_mask:0xf bank_mask:0xf
Cv 120,120,120
v_lshlrev_b32 v120, 16, v120
Cv 124,124,124
v_lshlrev_b32 v124, 16, v124
v_mul_f32_e32 v120, 0xbfb8aa3b, v120
v_exp_f32_e32 v120, v120
s_nop 0
v_add_f32_e32 v120, 1.0, v120
v_rcp_f32_e32 v120, v120
s_nop 0
Cv 120,120,120
v_lshlrev_b32 v120, 16, v120
v_lshrrev_b32 v121, 5, v0
v_bfe_u32 v122, v0, 4, 1
v_lshl_or_b32 v121, v122, 1, v121
v_add_u32 v121, s94, v121
)" R"(
v_add_f32_e32 v125, 0, v124
v_ashrrev_i32 v126, 31, v125
v_or_b32 v126, 0x80000000, v126
v_xor_b32 v125, v125, v126
v_and_b32 v125, 0xffff0000, v125
v_sub_u32 v126, 0xffff, v121
v_or_b32 v116, v125, v126
v_mov_b32 v117, s3
v_mov_b32 v118, v120
v_mov_b32 v119, v124
v_and_b32 v122, 15, v0
v_min_u32 v122, 1, v122
v_lshlrev_b32 v122, 30, v122
v_lshl_or_b32 v122, v121, 4, v122
s_mov_b32 s94, 0xc100000
buffer_store_dwordx4 v[116:119], v122, s[20:23], s94 offen sc0 sc1
s_cmp_eq_u32 s91, 1
s_waitcnt lgkmcnt(0)
s_barrier
s_cbranch_scc1 .LzAV
s_branch .LzAK
.LzAK:
Mz 32,0,32
Rt 64,18,16384
Mz 64,0,36
s_lshl1_add_u32 s40, s34, s91
s_lshl_b32 s40, s40, 12
Mz 96,0,40
Rt 66,18,18432
s_and_b32 s29, s38, 0xfffe0000
Mz 128,0,44
s_add_u32 s39, s39, 0x40000
s_add_u32 s40, s40, s29
Mz 160,0,48
Rt 68,19,16384
s_add_u32 m0, s37, 0
Mz 192,0,52
buffer_load_dwordx4 v12, s[8:11], s39 offen offset:0 lds
Mz 224,0,56
Rt 70,19,18432
v_min_u32 v3, 31, v0
v_mfma_f32_16x16x32_bf16 a[224:227], a[0:3], a[60:63], 0
v_lshlrev_b32 v3, 7, v3
v_mfma_f32_16x16x32_bf16 a[228:231], a[4:7], a[60:63], 0
Rt 72,20,16384
Mz 228,4,56
Mz 196,4,52
Rt 74,20,18432
Mz 164,4,48
Mz 132,4,44
Rt 76,21,16384
Mz 100,4,40
buffer_load_dwordx4 v13, s[8:11], s39 offen offset:1024 lds
Mz 68,4,36
Rt 78,21,18432
Mz 36,4,32
Mz 40,8,32
Rt 80,22,16384
Mz 72,8,36
Mz 104,8,40
Rt 82,22,18432
Mz 136,8,44
Mz 168,8,48
Rt 84,23,16384
Mz 200,8,52
)" R"(
buffer_load_dwordx4 v14, s[8:11], s39 offen offset:2048 lds
Mz 232,8,56
Rt 86,23,18432
v_mfma_f32_16x16x32_bf16 a[232:235], a[8:11], a[60:63], 0
v_mfma_f32_16x16x32_bf16 a[236:239], a[12:15], a[60:63], 0
Rt 88,24,16384
Mz 236,12,56
Mz 204,12,52
Rt 90,24,18432
Mz 172,12,48
Mz 140,12,44
Rt 92,25,16384
Mz 108,12,40
buffer_load_dwordx4 v15, s[8:11], s39 offen offset:3072 lds
.LzH23:
s_cbranch_execz .LzH34
Mz 76,12,36
Rt 94,25,18432
Mz 44,12,32
Mz 48,16,32
Rb 96,17,0
Mz 80,16,36
buffer_load_dword v6, v3, s[4:7], s40 offen
Mz 112,16,40
Rb 100,17,2048
Mz 144,16,44
Mz 176,16,48
Rb 104,17,8192
s_mov_b32 s28, s35
Mz 208,16,52
s_lshl2_add_u32 s28, s28, s33
s_mul_i32 s43, s28, 28672
Mz 240,16,56
Rb 108,17,10240
v_and_b32 v26, 0x70, v1
v_mfma_f32_16x16x32_bf16 a[240:243], a[16:19], a[60:63], 0
v_and_b32 v4, 48, v0
v_xor_b32 v26, v26, v4
v_mfma_f32_16x16x32_bf16 a[244:247], a[20:23], a[60:63], 0
Rb 112,17,16384
v_lshrrev_b32 v4, 3, v0
Mz 244,20,56
v_mul_u32_u24 v4, 896, v4
v_add_u32 v26, v26, v4
Mz 212,20,52
Rb 116,17,18432
v_xor_b32 v27, 64, v26
Mz 180,20,48
v_add_u32 v27, 6144, v27
v_bfe_u32 v28, v0, 1, 3
Mz 148,20,44
Rb 120,17,24576
v_lshrrev_b32 v4, 4, v0
Mz 116,20,40
v_xor_b32 v28, v28, v4
v_lshlrev_b32 v28, 4, v28
Mz 84,20,36
Rb 124,17,26624
v_and_b32 v4, 15, v0
Mz 52,20,32
v_lshl_or_b32 v28, v4, 7, v28
s_add_u32 s28, s36, 0x18000
Mz 56,24,32
v_add_u32 v28, s28, v28
)" R"(
v_xor_b32 v29, 64, v28
Mz 88,24,36
Mz 120,24,40
Mz 152,24,44
Mz 184,24,48
Mz 216,24,52
Mz 248,24,56
v_mfma_f32_16x16x32_bf16 a[248:251], a[24:27], a[60:63], 0
v_mfma_f32_16x16x32_bf16 a[252:255], a[28:31], a[60:63], 0
Mz 252,28,56
Mz 220,28,52
Mz 188,28,48
Mz 156,28,44
Mz 124,28,40
Mz 92,28,36
Mz 60,28,32
s_call_b64 s[30:31], .LzHOP
s_waitcnt vmcnt(11) lgkmcnt(0)
s_barrier
Mv 32,64,96
Rt 0,18,32768
s_bfe_u32 s88, s2, 0x20005
Mv 64,64,100
s_add_u32 s38, s38, 0xffff0080
s_and_b32 s28, s2, 15
Mv 96,64,104
Rt 2,18,34816
s_add_u32 m0, s36, 0
Mv 128,64,108
buffer_load_dwordx4 v8, s[4:7], s38 offen offset:0 lds
Mv 160,64,112
Rt 4,19,32768
s_lshl4_add_u32 s88, s88, s28
Mv 192,64,116
s_lshl2_add_u32 s88, s88, s33
s_mul_i32 s88, s88, 4096
Mv 224,64,120
Rt 6,19,34816
s_add_u32 s88, s88, 0x100000
Ma 224,64,124
buffer_load_dwordx4 v9, s[4:7], s38 offen offset:1024 lds
Ma 228,68,124
Rt 8,20,32768
v_min_u32 v5, 31, v0
Mv 228,68,120
v_lshlrev_b32 v5, 7, v5
s_mov_b32 s90, 0x100000
Mv 196,68,116
Rt 10,20,34816
s_mov_b64 s[24:25], s[8:9]
Mv 164,68,112
buffer_load_dwordx4 v10, s[4:7], s38 offen offset:2048 lds
Mv 132,68,108
Rt 12,21,32768
s_mov_b64 s[26:27], s[10:11]
Mv 100,68,104
Mv 68,68,100
Rt 14,21,34816
Mv 36,68,96
buffer_load_dwordx4 v11, s[4:7], s38 offen offset:3072 lds
Mv 40,72,96
Rt 16,22,32768
Mv 72,72,100
s_add_u32 s38, s38, 0x10000
Mv 104,72,104
)" R"(
Rt 18,22,34816
s_add_u32 m0, s36, 4096
Mv 136,72,108
buffer_load_dwordx4 v8, s[4:7], s38 offen offset:0 lds
Mv 168,72,112
Rt 20,23,32768
Mv 200,72,116
Mv 232,72,120
Rt 22,23,34816
Ma 232,72,124
buffer_load_dwordx4 v9, s[4:7], s38 offen offset:1024 lds
Ma 236,76,124
Rt 24,24,32768
Mv 236,76,120
Mv 204,76,116
Rt 26,24,34816
Mv 172,76,112
buffer_load_dwordx4 v10, s[4:7], s38 offen offset:2048 lds
Mv 140,76,108
Rt 28,25,32768
Mv 108,76,104
Mv 76,76,100
Rt 30,25,34816
Mv 44,76,96
buffer_load_dwordx4 v11, s[4:7], s38 offen offset:3072 lds
Mv 48,80,96
Rb 32,16,32768
Mv 80,80,100
Mv 112,80,104
Rb 36,16,34816
Mv 144,80,108
s_add_u32 s39, s39, 0x40000
Mv 176,80,112
Rb 40,16,40960
s_add_u32 m0, s37, 16384
Mv 208,80,116
buffer_load_dwordx4 v12, s[8:11], s39 offen offset:0 lds
Mv 240,80,120
Rb 44,16,43008
Ma 240,80,124
Ma 244,84,124
Rb 48,16,49152
Mv 244,84,120
Mv 212,84,116
Rb 52,16,51200
Mv 180,84,112
buffer_load_dwordx4 v13, s[8:11], s39 offen offset:1024 lds
Mv 148,84,108
Rb 56,16,57344
Mv 116,84,104
Mv 84,84,100
Rb 60,16,59392
Mv 52,84,96
Mv 56,88,96
Mv 88,88,100
buffer_load_dwordx4 v14, s[8:11], s39 offen offset:2048 lds
Mv 120,88,104
Mv 152,88,108
Mv 184,88,112
Mv 216,88,116
Mv 248,88,120
Ma 248,88,124
buffer_load_dwordx4 v15, s[8:11], s39 offen offset:3072 lds
Ma 252,92,124
)" R"(
Mv 252,92,120
s_add_u32 s88, s88, s90
Mv 220,92,116
Mv 188,92,112
buffer_load_dword v6, v5, s[24:27], s88 offen
Mv 156,92,108
Mv 124,92,104
Mv 92,92,100
Mv 60,92,96
s_waitcnt vmcnt(20) lgkmcnt(0)
s_barrier
Mv 32,0,32
Rt 64,18,49152
s_lshl_b32 s28, s34, 21
Mv 64,0,36
s_lshl_b32 s29, s91, 25
s_add_u32 s28, s28, s29
Mv 96,0,40
Rt 66,18,51200
s_bfe_u32 s29, s63, 0x80000
Mv 128,0,44
s_add_u32 s39, s39, 0x40000
s_lshl_b32 s29, s29, 16
Mv 160,0,48
Rt 68,19,49152
s_add_u32 m0, s37, 32768
Mv 192,0,52
buffer_load_dwordx4 v12, s[8:11], s39 offen offset:0 lds
Mv 224,0,56
Rt 70,19,51200
s_add_u32 s45, s28, s29
Ma 224,0,60
s_bfe_u32 s29, s63, 0x80008
s_lshl_b32 s29, s29, 16
Ma 228,4,60
Rt 72,20,49152
s_add_u32 s46, s28, s29
Mv 228,4,56
s_bfe_u32 s29, s63, 0x80010
s_lshl_b32 s29, s29, 16
Mv 196,4,52
Rt 74,20,51200
s_add_u32 s47, s28, s29
Mv 164,4,48
s_bfe_u32 s29, s63, 0x80018
s_lshl_b32 s29, s29, 16
Mv 132,4,44
Rt 76,21,49152
s_add_u32 s48, s28, s29
Mv 100,4,40
buffer_load_dwordx4 v13, s[8:11], s39 offen offset:1024 lds
Mv 68,4,36
Rt 78,21,51200
s_lshl_b32 s50, s34, 21
Mv 36,4,32
s_add_u32 s50, s50, 0x6140000
s_lshl2_add_u32 s28, s91, s93
Mv 40,8,32
Rt 80,22,49152
s_lshl_b32 s28, s28, 7
Mv 72,8,36
s_add_u32 s49, s28, s50
s_lshl_b32 s52, s35, 7
Mv 104,8,40
Rt 82,22,51200
)" R"(
s_add_u32 s52, s52, s50
Mv 136,8,44
s_add_u32 s52, s52, 0xd000
s_lshl4_add_u32 s28, s34, s35
Mv 168,8,48
Rt 84,23,49152
s_lshl_b32 s51, s28, 17
Mv 200,8,52
buffer_load_dwordx4 v14, s[8:11], s39 offen offset:2048 lds
Mv 232,8,56
Rt 86,23,51200
s_add_u32 s51, s51, 0x4000000
Ma 232,8,60
s_cmp_lg_u32 s33, 0
s_cmov_b32 s49, 0x40000000
Ma 236,12,60
Rt 88,24,49152
s_mov_b32 s53, 0x40000
Mv 236,12,56
s_mov_b32 s54, 0x3db8aa3b
s_mul_i32 s41, s33, 7168
Mv 204,12,52
Rt 90,24,51200
s_mul_i32 s42, s33, 0x70000
Mv 172,12,48
s_lshl_b32 s28, s34, 9
s_add_u32 s42, s42, s28
Mv 140,12,44
Rt 92,25,49152
s_lshl_b32 s89, s34, 21
Mv 108,12,40
buffer_load_dwordx4 v15, s[8:11], s39 offen offset:3072 lds
Mv 76,12,36
Rt 94,25,51200
s_mov_b32 s62, 0x4e800000
Mv 44,12,32
s_add_u32 s67, s89, 0x6000000
s_lshl3_add_u32 s28, s34, s35
Mv 48,16,32
Rb 96,17,32768
s_add_u32 s64, s51, 0x100000
s_add_u32 m0, s36, 131072
s_add_u32 s44, s43, 0
Mv 80,16,36
s_lshl2_add_u32 s28, s28, s33
s_lshl2_add_u32 s65, s35, s33
buffer_load_dwordx4 v26, s[12:15], s44 offen offset:0 lds
Mv 112,16,40
Rb 100,17,34816
s_lshl_b32 s65, s65, 7
Mv 144,16,44
s_add_u32 s65, s65, s50
s_add_u32 s65, s65, 0xc000
Mv 176,16,48
Rb 104,17,40960
s_lshl_b32 s66, s28, 10
Mv 208,16,52
s_add_u32 s66, s66, 0xc000000
s_mov_b32 s28, s35
buffer_load_dwordx4 v27, s[12:15], s44 offen offset:1024 lds
Mv 240,16,56
Rb 108,17,43008
s_lshl2_add_u32 s28, s28, s33
Ma 240,16,60
)" R"(
s_lshl_b32 s29, s28, 5
s_add_u32 s29, s29, s34
Ma 244,20,60
Rb 112,17,49152
s_lshl_b32 s73, s29, 15
Mv 244,20,56
s_add_u32 s73, s73, 0x8000000
s_lshr_b32 s29, s36, 1
s_add_u32 s44, s43, 12288
buffer_load_dwordx4 v26, s[12:15], s44 offen offset:2048 lds
Mv 212,20,52
Rb 116,17,51200
s_and_b32 s29, s29, 0x2000
Mv 180,20,48
s_add_u32 s73, s73, s29
s_lshl4_add_u32 s28, s28, s34
Mv 148,20,44
Rb 120,17,57344
s_lshl_b32 s68, s28, 8
Mv 116,20,40
s_add_u32 s68, s68, 0xc110000
s_add_u32 s55, s89, 0x120000
buffer_load_dwordx4 v27, s[12:15], s44 offen offset:3072 lds
Mv 84,20,36
Rb 124,17,59392
s_lshl_b32 s28, s35, 1
Mv 52,20,32
s_sub_u32 s57, 14, s28
s_mov_b32 s59, s89
Mv 56,24,32
s_mov_b32 s58, 2
s_add_u32 m0, s36, 135168
s_add_u32 s44, s43, 128
Mv 88,24,36
s_cmp_eq_u32 s35, 7
s_cmov_b32 s55, s89
buffer_load_dwordx4 v26, s[12:15], s44 offen offset:0 lds
Mv 120,24,40
s_cmp_eq_u32 s35, 7
s_cmov_b32 s57, 16
Mv 152,24,44
s_cmp_eq_u32 s35, 7
s_cmov_b32 s59, s67
Mv 184,24,48
s_add_u32 s56, s55, 0x2000000
Mv 216,24,52
buffer_load_dwordx4 v27, s[12:15], s44 offen offset:1024 lds
Mv 248,24,56
Ma 248,24,60
Ma 252,28,60
Mv 252,28,56
s_add_u32 s44, s43, 12416
buffer_load_dwordx4 v26, s[12:15], s44 offen offset:2048 lds
Mv 220,28,52
Mv 188,28,48
Mv 156,28,44
Mv 124,28,40
buffer_load_dwordx4 v27, s[12:15], s44 offen offset:3072 lds
Mv 92,28,36
Mv 60,28,32
s_waitcnt vmcnt(17) lgkmcnt(0)
s_barrier
Mv 32,64,96
Rt 0,18,0
Mv 64,64,100
)" R"(
s_add_u32 s38, s38, 0xffff0080
Mv 96,64,104
Rt 2,18,2048
s_add_u32 m0, s36, 32768
Mv 128,64,108
buffer_load_dwordx4 v8, s[4:7], s38 offen offset:0 lds
Mv 160,64,112
Rt 4,19,0
Mv 192,64,116
Mv 224,64,120
Rt 6,19,2048
Ma 224,64,124
buffer_load_dwordx4 v9, s[4:7], s38 offen offset:1024 lds
Ma 228,68,124
Rt 8,20,0
s_lshl2_add_u32 s28, s2, s33
Mv 228,68,120
s_lshl_b32 s28, s28, 11
s_add_u32 s28, s28, 0xc100000
Mv 196,68,116
Rt 10,20,2048
v_min_u32 v3, 15, v0
Mv 164,68,112
buffer_load_dwordx4 v10, s[4:7], s38 offen offset:2048 lds
Mv 132,68,108
Rt 12,21,0
v_lshlrev_b32 v3, 7, v3
Mv 100,68,104
Mv 68,68,100
Rt 14,21,2048
Mv 36,68,96
buffer_load_dwordx4 v11, s[4:7], s38 offen offset:3072 lds
Mv 40,72,96
Rt 16,22,0
Mv 72,72,100
s_add_u32 s38, s38, 0x10000
Mv 104,72,104
Rt 18,22,2048
s_add_u32 m0, s36, 36864
Mv 136,72,108
buffer_load_dwordx4 v8, s[4:7], s38 offen offset:0 lds
Mv 168,72,112
Rt 20,23,0
Mv 200,72,116
Mv 232,72,120
Rt 22,23,2048
Ma 232,72,124
buffer_load_dwordx4 v9, s[4:7], s38 offen offset:1024 lds
Ma 236,76,124
Rt 24,24,0
Mv 236,76,120
Mv 204,76,116
Rt 26,24,2048
Mv 172,76,112
buffer_load_dwordx4 v10, s[4:7], s38 offen offset:2048 lds
Mv 140,76,108
Rt 28,25,0
Mv 108,76,104
Mv 76,76,100
Rt 30,25,2048
Mv 44,76,96
buffer_load_dwordx4 v11, s[4:7], s38 offen offset:3072 lds
Mv 48,80,96
Rb 32,16,0
)" R"(
Mv 80,80,100
Mv 112,80,104
Rb 36,16,2048
Mv 144,80,108
s_add_u32 s39, s39, 0x40000
Mv 176,80,112
Rb 40,16,8192
s_add_u32 m0, s37, 49152
Mv 208,80,116
buffer_load_dwordx4 v12, s[8:11], s39 offen offset:0 lds
Mv 240,80,120
Rb 44,16,10240
Ma 240,80,124
Ma 244,84,124
Rb 48,16,16384
Mv 244,84,120
Mv 212,84,116
Rb 52,16,18432
Mv 180,84,112
buffer_load_dwordx4 v13, s[8:11], s39 offen offset:1024 lds
Mv 148,84,108
Rb 56,16,24576
Mv 116,84,104
Mv 84,84,100
Rb 60,16,26624
Mv 52,84,96
Mv 56,88,96
Mv 88,88,100
buffer_load_dwordx4 v14, s[8:11], s39 offen offset:2048 lds
Mv 120,88,104
Mv 152,88,108
Mv 184,88,112
Mv 216,88,116
Mv 248,88,120
Ma 248,88,124
buffer_load_dwordx4 v15, s[8:11], s39 offen offset:3072 lds
Ma 252,92,124
Mv 252,92,120
s_add_u32 s88, s88, s90
s_mov_b32 s40, 5
Mv 220,92,116
Mv 188,92,112
buffer_load_dword v6, v5, s[24:27], s88 offen
Mv 156,92,108
Mv 124,92,104
buffer_load_dword v6, v3, s[20:23], s28 offen
Mv 92,92,100
Mv 60,92,96
s_waitcnt vmcnt(27) lgkmcnt(0)
s_barrier
.LzPAK:
Mv 32,0,32
Rt 64,18,16384
Mv 64,0,36
Mv 96,0,40
Rt 66,18,18432
Mv 128,0,44
s_add_u32 s39, s39, 0x40000
Mv 160,0,48
Rt 68,19,16384
s_add_u32 m0, s37, 0
Mv 192,0,52
buffer_load_dwordx4 v12, s[8:11], s39 offen offset:0 lds
Mv 224,0,56
Rt 70,19,18432
)" R"(
Ma 224,0,60
Ma 228,4,60
Rt 72,20,16384
Mv 228,4,56
Mv 196,4,52
Rt 74,20,18432
Mv 164,4,48
Mv 132,4,44
Rt 76,21,16384
Mv 100,4,40
buffer_load_dwordx4 v13, s[8:11], s39 offen offset:1024 lds
Mv 68,4,36
Rt 78,21,18432
Mv 36,4,32
Mv 40,8,32
Rt 80,22,16384
Mv 72,8,36
Mv 104,8,40
Rt 82,22,18432
Mv 136,8,44
Mv 168,8,48
Rt 84,23,16384
Mv 200,8,52
buffer_load_dwordx4 v14, s[8:11], s39 offen offset:2048 lds
Mv 232,8,56
Rt 86,23,18432
Ma 232,8,60
Ma 236,12,60
Rt 88,24,16384
Mv 236,12,56
Mv 204,12,52
Rt 90,24,18432
Mv 172,12,48
Mv 140,12,44
Rt 92,25,16384
Mv 108,12,40
buffer_load_dwordx4 v15, s[8:11], s39 offen offset:3072 lds
Mv 76,12,36
Rt 94,25,18432
Mv 44,12,32
Mv 48,16,32
Rb 96,17,0
Mv 80,16,36
Mv 112,16,40
Rb 100,17,2048
Mv 144,16,44
Mv 176,16,48
Rb 104,17,8192
Mv 208,16,52
Mv 240,16,56
Rb 108,17,10240
Ma 240,16,60
Ma 244,20,60
Rb 112,17,16384
Mv 244,20,56
Mv 212,20,52
Rb 116,17,18432
.LzH24:
s_cbranch_execz .LzH35
Mv 180,20,48
Mv 148,20,44
Rb 120,17,24576
Mv 116,20,40
Mv 84,20,36
)" R"(
Rb 124,17,26624
Mv 52,20,32
Mv 56,24,32
Mv 88,24,36
Mv 120,24,40
Mv 152,24,44
Mv 184,24,48
Mv 216,24,52
Mv 248,24,56
Ma 248,24,60
Ma 252,28,60
Mv 252,28,56
Mv 220,28,52
Mv 188,28,48
Mv 156,28,44
Mv 124,28,40
Mv 92,28,36
Mv 60,28,32
s_waitcnt vmcnt(9) lgkmcnt(0)
s_barrier
Mv 32,64,96
Rt 0,18,32768
Mv 64,64,100
s_add_u32 s38, s38, 0xffff0080
Mv 96,64,104
Rt 2,18,34816
s_add_u32 m0, s36, 0
Mv 128,64,108
buffer_load_dwordx4 v8, s[4:7], s38 offen offset:0 lds
Mv 160,64,112
Rt 4,19,32768
Mv 192,64,116
Mv 224,64,120
Rt 6,19,34816
Ma 224,64,124
buffer_load_dwordx4 v9, s[4:7], s38 offen offset:1024 lds
Ma 228,68,124
Rt 8,20,32768
Mv 228,68,120
Mv 196,68,116
Rt 10,20,34816
Mv 164,68,112
buffer_load_dwordx4 v10, s[4:7], s38 offen offset:2048 lds
Mv 132,68,108
Rt 12,21,32768
Mv 100,68,104
Mv 68,68,100
Rt 14,21,34816
Mv 36,68,96
buffer_load_dwordx4 v11, s[4:7], s38 offen offset:3072 lds
Mv 40,72,96
Rt 16,22,32768
Mv 72,72,100
s_add_u32 s38, s38, 0x10000
Mv 104,72,104
Rt 18,22,34816
s_add_u32 m0, s36, 4096
Mv 136,72,108
buffer_load_dwordx4 v8, s[4:7], s38 offen offset:0 lds
Mv 168,72,112
Rt 20,23,32768
Mv 200,72,116
Mv 232,72,120
Rt 22,23,34816
Ma 232,72,124
)" R"(
buffer_load_dwordx4 v9, s[4:7], s38 offen offset:1024 lds
Ma 236,76,124
Rt 24,24,32768
Mv 236,76,120
Mv 204,76,116
Rt 26,24,34816
Mv 172,76,112
buffer_load_dwordx4 v10, s[4:7], s38 offen offset:2048 lds
Mv 140,76,108
Rt 28,25,32768
Mv 108,76,104
Mv 76,76,100
Rt 30,25,34816
Mv 44,76,96
buffer_load_dwordx4 v11, s[4:7], s38 offen offset:3072 lds
Mv 48,80,96
Rb 32,16,32768
Mv 80,80,100
Mv 112,80,104
Rb 36,16,34816
Mv 144,80,108
s_add_u32 s39, s39, 0x40000
Mv 176,80,112
Rb 40,16,40960
s_add_u32 m0, s37, 16384
Mv 208,80,116
buffer_load_dwordx4 v12, s[8:11], s39 offen offset:0 lds
Mv 240,80,120
Rb 44,16,43008
Ma 240,80,124
Ma 244,84,124
Rb 48,16,49152
Mv 244,84,120
Mv 212,84,116
Rb 52,16,51200
Mv 180,84,112
buffer_load_dwordx4 v13, s[8:11], s39 offen offset:1024 lds
Mv 148,84,108
Rb 56,16,57344
Mv 116,84,104
Mv 84,84,100
Rb 60,16,59392
Mv 52,84,96
Mv 56,88,96
Mv 88,88,100
buffer_load_dwordx4 v14, s[8:11], s39 offen offset:2048 lds
Mv 120,88,104
Mv 152,88,108
Mv 184,88,112
Mv 216,88,116
Mv 248,88,120
Ma 248,88,124
buffer_load_dwordx4 v15, s[8:11], s39 offen offset:3072 lds
Ma 252,92,124
Mv 252,92,120
s_add_u32 s88, s88, s90
Mv 220,92,116
Mv 188,92,112
buffer_load_dword v6, v5, s[24:27], s88 offen
Mv 156,92,108
Mv 124,92,104
Mv 92,92,100
Mv 60,92,96
s_waitcnt vmcnt(18) lgkmcnt(0)
s_barrier
)" R"(
Mv 32,0,32
Rt 64,18,49152
Mv 64,0,36
Mv 96,0,40
Rt 66,18,51200
Mv 128,0,44
s_add_u32 s39, s39, 0x40000
Mv 160,0,48
Rt 68,19,49152
s_add_u32 m0, s37, 32768
Mv 192,0,52
buffer_load_dwordx4 v12, s[8:11], s39 offen offset:0 lds
Mv 224,0,56
Rt 70,19,51200
Ma 224,0,60
Ma 228,4,60
Rt 72,20,49152
Mv 228,4,56
Mv 196,4,52
Rt 74,20,51200
Mv 164,4,48
Mv 132,4,44
Rt 76,21,49152
Mv 100,4,40
buffer_load_dwordx4 v13, s[8:11], s39 offen offset:1024 lds
Mv 68,4,36
Rt 78,21,51200
Mv 36,4,32
Mv 40,8,32
Rt 80,22,49152
Mv 72,8,36
Mv 104,8,40
Rt 82,22,51200
Mv 136,8,44
Mv 168,8,48
Rt 84,23,49152
Mv 200,8,52
buffer_load_dwordx4 v14, s[8:11], s39 offen offset:2048 lds
Mv 232,8,56
Rt 86,23,51200
Ma 232,8,60
Ma 236,12,60
Rt 88,24,49152
Mv 236,12,56
Mv 204,12,52
Rt 90,24,51200
Mv 172,12,48
Mv 140,12,44
Rt 92,25,49152
Mv 108,12,40
buffer_load_dwordx4 v15, s[8:11], s39 offen offset:3072 lds
Mv 76,12,36
Rt 94,25,51200
Mv 44,12,32
Mv 48,16,32
Rb 96,17,32768
Mv 80,16,36
Mv 112,16,40
Rb 100,17,34816
Mv 144,16,44
Mv 176,16,48
Rb 104,17,40960
Mv 208,16,52
Mv 240,16,56
)" R"(
Rb 108,17,43008
Ma 240,16,60
Ma 244,20,60
Rb 112,17,49152
Mv 244,20,56
Mv 212,20,52
Rb 116,17,51200
Mv 180,20,48
Mv 148,20,44
Rb 120,17,57344
Mv 116,20,40
Mv 84,20,36
Rb 124,17,59392
Mv 52,20,32
Mv 56,24,32
Mv 88,24,36
Mv 120,24,40
Mv 152,24,44
Mv 184,24,48
Mv 216,24,52
Mv 248,24,56
Ma 248,24,60
Ma 252,28,60
Mv 252,28,56
Mv 220,28,52
Mv 188,28,48
Mv 156,28,44
Mv 124,28,40
Mv 92,28,36
Mv 60,28,32
s_waitcnt vmcnt(9) lgkmcnt(0)
s_barrier
Mv 32,64,96
Rt 0,18,0
Mv 64,64,100
s_add_u32 s38, s38, 0xffff0080
Mv 96,64,104
Rt 2,18,2048
s_add_u32 m0, s36, 32768
Mv 128,64,108
buffer_load_dwordx4 v8, s[4:7], s38 offen offset:0 lds
Mv 160,64,112
Rt 4,19,0
Mv 192,64,116
Mv 224,64,120
Rt 6,19,2048
Ma 224,64,124
buffer_load_dwordx4 v9, s[4:7], s38 offen offset:1024 lds
Ma 228,68,124
Rt 8,20,0
Mv 228,68,120
Mv 196,68,116
Rt 10,20,2048
Mv 164,68,112
buffer_load_dwordx4 v10, s[4:7], s38 offen offset:2048 lds
Mv 132,68,108
Rt 12,21,0
Mv 100,68,104
Mv 68,68,100
Rt 14,21,2048
Mv 36,68,96
buffer_load_dwordx4 v11, s[4:7], s38 offen offset:3072 lds
Mv 40,72,96
Rt 16,22,0
Mv 72,72,100
)" R"(
s_add_u32 s38, s38, 0x10000
Mv 104,72,104
Rt 18,22,2048
s_add_u32 m0, s36, 36864
Mv 136,72,108
buffer_load_dwordx4 v8, s[4:7], s38 offen offset:0 lds
Mv 168,72,112
Rt 20,23,0
Mv 200,72,116
Mv 232,72,120
Rt 22,23,2048
Ma 232,72,124
buffer_load_dwordx4 v9, s[4:7], s38 offen offset:1024 lds
Ma 236,76,124
Rt 24,24,0
Mv 236,76,120
Mv 204,76,116
Rt 26,24,2048
Mv 172,76,112
buffer_load_dwordx4 v10, s[4:7], s38 offen offset:2048 lds
Mv 140,76,108
Rt 28,25,0
Mv 108,76,104
Mv 76,76,100
Rt 30,25,2048
Mv 44,76,96
buffer_load_dwordx4 v11, s[4:7], s38 offen offset:3072 lds
Mv 48,80,96
Rb 32,16,0
Mv 80,80,100
Mv 112,80,104
Rb 36,16,2048
Mv 144,80,108
s_add_u32 s39, s39, 0x40000
Mv 176,80,112
Rb 40,16,8192
s_add_u32 m0, s37, 49152
Mv 208,80,116
buffer_load_dwordx4 v12, s[8:11], s39 offen offset:0 lds
Mv 240,80,120
Rb 44,16,10240
Ma 240,80,124
Ma 244,84,124
Rb 48,16,16384
Mv 244,84,120
Mv 212,84,116
Rb 52,16,18432
Mv 180,84,112
buffer_load_dwordx4 v13, s[8:11], s39 offen offset:1024 lds
Mv 148,84,108
Rb 56,16,24576
Mv 116,84,104
Mv 84,84,100
Rb 60,16,26624
Mv 52,84,96
Mv 56,88,96
Mv 88,88,100
buffer_load_dwordx4 v14, s[8:11], s39 offen offset:2048 lds
Mv 120,88,104
Mv 152,88,108
Mv 184,88,112
Mv 216,88,116
Mv 248,88,120
Ma 248,88,124
)" R"(
buffer_load_dwordx4 v15, s[8:11], s39 offen offset:3072 lds
Ma 252,92,124
Mv 252,92,120
s_add_u32 s88, s88, s90
Mv 220,92,116
s_sub_u32 s40, s40, 1
Mv 188,92,112
buffer_load_dword v6, v5, s[24:27], s88 offen
Mv 156,92,108
Mv 124,92,104
Mv 92,92,100
Mv 60,92,96
s_cmp_lg_u32 s40, 0
s_waitcnt vmcnt(18) lgkmcnt(0)
s_barrier
s_cbranch_scc1 .LzPAK
.LzPAKX:
Mv 32,0,32
Rt 64,18,16384
Mv 64,0,36
Rb 192,28,32768
Mv 96,0,40
Rt 66,18,18432
Mv 128,0,44
s_add_u32 s39, s39, 0x40000
Rb 196,28,34816
Mv 160,0,48
Rt 68,19,16384
s_add_u32 m0, s37, 0
Mv 192,0,52
buffer_load_dwordx4 v12, s[8:11], s39 offen offset:0 lds
Mv 224,0,56
Rt 70,19,18432
Ma 224,0,60
Rb 200,29,32768
Ma 228,4,60
Rt 72,20,16384
Mv 228,4,56
Rb 204,29,34816
Mv 196,4,52
Rt 74,20,18432
Mv 164,4,48
Rb 208,28,36864
Mv 132,4,44
Rt 76,21,16384
Mv 100,4,40
buffer_load_dwordx4 v13, s[8:11], s39 offen offset:1024 lds
Mv 68,4,36
Rt 78,21,18432
Mv 36,4,32
Rb 212,28,38912
Mv 40,8,32
Rt 80,22,16384
Mv 72,8,36
Rb 216,29,36864
Mv 104,8,40
Rt 82,22,18432
Mv 136,8,44
Rb 220,29,38912
Mv 168,8,48
Rt 84,23,16384
Mv 200,8,52
buffer_load_dwordx4 v14, s[8:11], s39 offen offset:2048 lds
Mv 232,8,56
Rt 86,23,18432
)" R"(
Ma 232,8,60
Ma 236,12,60
Rt 88,24,16384
Mv 236,12,56
Mv 204,12,52
Rt 90,24,18432
Mv 172,12,48
Mv 140,12,44
Rt 92,25,16384
Mv 108,12,40
buffer_load_dwordx4 v15, s[8:11], s39 offen offset:3072 lds
Mv 76,12,36
Rt 94,25,18432
Mv 44,12,32
s_add_u32 s44, s43, 256
s_waitcnt lgkmcnt(6)
Mv 48,16,32
Rb 96,17,0
s_add_u32 m0, s36, 131072
Mv 80,16,36
buffer_load_dwordx4 v26, s[12:15], s44 offen offset:0 lds
Mv 112,16,40
Rb 100,17,2048
Mv 144,16,44
Mv 176,16,48
Rb 104,17,8192
Mv 208,16,52
buffer_load_dwordx4 v27, s[12:15], s44 offen offset:1024 lds
Mv 240,16,56
Rb 108,17,10240
Ma 240,16,60
Ma 244,20,60
Rb 112,17,16384
Mv 244,20,56
s_add_u32 s44, s43, 12544
buffer_load_dwordx4 v26, s[12:15], s44 offen offset:2048 lds
Mv 212,20,52
Rb 116,17,18432
Mv 180,20,48
Mv 148,20,44
Rb 120,17,24576
Mv 116,20,40
buffer_load_dwordx4 v27, s[12:15], s44 offen offset:3072 lds
Mv 84,20,36
Rb 124,17,26624
Mv 52,20,32
Mv 56,24,32
s_add_u32 m0, s36, 135168
s_add_u32 s44, s43, 384
Mv 88,24,36
buffer_load_dwordx4 v26, s[12:15], s44 offen offset:0 lds
Mv 120,24,40
Mv 152,24,44
Mv 184,24,48
Mv 216,24,52
buffer_load_dwordx4 v27, s[12:15], s44 offen offset:1024 lds
Mv 248,24,56
Ma 248,24,60
Ma 252,28,60
Mv 252,28,56
s_add_u32 s44, s43, 12672
buffer_load_dwordx4 v26, s[12:15], s44 offen offset:2048 lds
Mv 220,28,52
Mv 188,28,48
)" R"(
Mv 156,28,44
s_mov_b32 s85, 0xc108800
v_and_b32 v2, 7, v0
v_lshlrev_b32 v2, 7, v2
Mv 124,28,40
buffer_load_dwordx4 v27, s[12:15], s44 offen offset:3072 lds
Mv 92,28,36
buffer_load_dword v4, v2, s[20:23], s85 offen sc0 sc1
Mv 60,28,32
s_waitcnt vmcnt(18) lgkmcnt(0)
s_barrier
Mv 32,64,96
Rt 0,18,32768
Mv 64,64,100
s_add_u32 s38, s38, 0xffff0080
Mv 96,64,104
Rt 2,18,34816
s_add_u32 m0, s36, 0
Mv 128,64,108
buffer_load_dwordx4 v8, s[4:7], s38 offen offset:0 lds
Mv 160,64,112
Rt 4,19,32768
Mv 192,64,116
Mv 224,64,120
Rt 6,19,34816
Ma 224,64,124
buffer_load_dwordx4 v9, s[4:7], s38 offen offset:1024 lds
Ma 228,68,124
Rt 8,20,32768
Mv 228,68,120
Mv 196,68,116
Rt 10,20,34816
Mv 164,68,112
buffer_load_dwordx4 v10, s[4:7], s38 offen offset:2048 lds
Mv 132,68,108
Rt 12,21,32768
Mv 100,68,104
Mv 68,68,100
Rt 14,21,34816
Mv 36,68,96
buffer_load_dwordx4 v11, s[4:7], s38 offen offset:3072 lds
Mv 40,72,96
Rt 16,22,32768
Mv 72,72,100
s_add_u32 s38, s38, 0x10000
Mv 104,72,104
Rt 18,22,34816
s_add_u32 m0, s36, 4096
Mv 136,72,108
buffer_load_dwordx4 v8, s[4:7], s38 offen offset:0 lds
Mv 168,72,112
Rt 20,23,32768
Mv 200,72,116
Mv 232,72,120
Rt 22,23,34816
Ma 232,72,124
buffer_load_dwordx4 v9, s[4:7], s38 offen offset:1024 lds
Ma 236,76,124
Rt 24,24,32768
Mv 236,76,120
Mv 204,76,116
Rt 26,24,34816
Mv 172,76,112
buffer_load_dwordx4 v10, s[4:7], s38 offen offset:2048 lds
Mv 140,76,108
)" R"(
Rt 28,25,32768
.LzH25:
s_cbranch_execz .LzH36
Mv 108,76,104
Mv 76,76,100
Rt 30,25,34816
Mv 44,76,96
buffer_load_dwordx4 v11, s[4:7], s38 offen offset:3072 lds
Mv 48,80,96
Rb 32,16,32768
Mv 80,80,100
Mv 112,80,104
Rb 36,16,34816
Mv 144,80,108
s_add_u32 s39, s39, 0x40000
Mv 176,80,112
Rb 40,16,40960
s_add_u32 m0, s37, 16384
Mv 208,80,116
buffer_load_dwordx4 v12, s[8:11], s39 offen offset:0 lds
Mv 240,80,120
Rb 44,16,43008
Ma 240,80,124
Ma 244,84,124
Rb 48,16,49152
Mv 244,84,120
Mv 212,84,116
Rb 52,16,51200
Mv 180,84,112
buffer_load_dwordx4 v13, s[8:11], s39 offen offset:1024 lds
Mv 148,84,108
Rb 56,16,57344
Mv 116,84,104
Mv 84,84,100
Rb 60,16,59392
Mv 52,84,96
Mv 56,88,96
Mv 88,88,100
buffer_load_dwordx4 v14, s[8:11], s39 offen offset:2048 lds
Mv 120,88,104
Mv 152,88,108
Mv 184,88,112
Mv 216,88,116
Mv 248,88,120
Ma 248,88,124
buffer_load_dwordx4 v15, s[8:11], s39 offen offset:3072 lds
Ma 252,92,124
Mv 252,92,120
Mv 220,92,116
Mv 188,92,112
Mv 156,92,108
Mv 124,92,104
Mv 92,92,100
Mv 60,92,96
s_waitcnt vmcnt(13) lgkmcnt(0)
s_barrier
Mv 32,0,32
Rt 64,18,49152
Mv 64,0,36
Rb 128,28,32768
Mv 96,0,40
Rt 66,18,51200
Mv 128,0,44
s_add_u32 s39, s39, 0x40000
Rb 132,28,34816
)" R"(
Mv 160,0,48
Rt 68,19,49152
s_add_u32 m0, s37, 32768
Mv 192,0,52
buffer_load_dwordx4 v12, s[8:11], s39 offen offset:0 lds
Mv 224,0,56
Rt 70,19,51200
Ma 224,0,60
Rb 136,29,32768
Ma 228,4,60
Rt 72,20,49152
Mv 228,4,56
Rb 140,29,34816
Mv 196,4,52
Rt 74,20,51200
Mv 164,4,48
Rb 144,28,36864
Mv 132,4,44
Rt 76,21,49152
Mv 100,4,40
buffer_load_dwordx4 v13, s[8:11], s39 offen offset:1024 lds
Mv 68,4,36
Rt 78,21,51200
Mv 36,4,32
Rb 148,28,38912
Mv 40,8,32
Rt 80,22,49152
Mv 72,8,36
Rb 152,29,36864
Mv 104,8,40
Rt 82,22,51200
Mv 136,8,44
Rb 156,29,38912
Mv 168,8,48
Rt 84,23,49152
Mv 200,8,52
buffer_load_dwordx4 v14, s[8:11], s39 offen offset:2048 lds
Mv 232,8,56
Rt 86,23,51200
Ma 232,8,60
Ma 236,12,60
Rt 88,24,49152
Mv 236,12,56
Mv 204,12,52
Rt 90,24,51200
Mv 172,12,48
Mv 140,12,44
Rt 92,25,49152
Mv 108,12,40
buffer_load_dwordx4 v15, s[8:11], s39 offen offset:3072 lds
Mv 76,12,36
Rt 94,25,51200
Mv 44,12,32
s_add_u32 s44, s43, 512
s_waitcnt lgkmcnt(6)
Mv 48,16,32
Rb 96,17,32768
s_add_u32 m0, s36, 131072
Mv 80,16,36
buffer_load_dwordx4 v26, s[12:15], s44 offen offset:0 lds
Mv 112,16,40
Rb 100,17,34816
Mv 144,16,44
Mv 176,16,48
)" R"(
Rb 104,17,40960
Mv 208,16,52
buffer_load_dwordx4 v27, s[12:15], s44 offen offset:1024 lds
Mv 240,16,56
Rb 108,17,43008
Ma 240,16,60
Ma 244,20,60
Rb 112,17,49152
Mv 244,20,56
s_add_u32 s44, s43, 12800
buffer_load_dwordx4 v26, s[12:15], s44 offen offset:2048 lds
Mv 212,20,52
Rb 116,17,51200
Mv 180,20,48
Mv 148,20,44
Rb 120,17,57344
Mv 116,20,40
buffer_load_dwordx4 v27, s[12:15], s44 offen offset:3072 lds
Mv 84,20,36
Rb 124,17,59392
Mv 52,20,32
Mv 56,24,32
s_add_u32 m0, s36, 135168
s_add_u32 s44, s43, 640
Mv 88,24,36
buffer_load_dwordx4 v26, s[12:15], s44 offen offset:0 lds
Mv 120,24,40
Mv 152,24,44
Mv 184,24,48
Mv 216,24,52
buffer_load_dwordx4 v27, s[12:15], s44 offen offset:1024 lds
Mv 248,24,56
Ma 248,24,60
Ma 252,28,60
Mv 252,28,56
s_add_u32 s44, s43, 12928
buffer_load_dwordx4 v26, s[12:15], s44 offen offset:2048 lds
Mv 220,28,52
Mv 188,28,48
Mv 156,28,44
Mv 124,28,40
buffer_load_dwordx4 v27, s[12:15], s44 offen offset:3072 lds
Mv 92,28,36
Mv 60,28,32
s_waitcnt vmcnt(16) lgkmcnt(0)
s_barrier
Mv 32,64,96
Rt 0,18,0
v_xor_b32 v4, s3, v4
v_cmp_gt_u32_e32 vcc, 2, v4
s_andn2_b32 s28, 0xff, vcc_lo
s_cbranch_scc1 .Lzsp1
.Lzok1:
Mv 64,64,100
s_add_u32 s38, s38, 0xffff0080
v_xor_b32 v4, s3, v4
Mv 96,64,104
Rt 2,18,2048
s_add_u32 m0, s36, 32768
Mv 128,64,108
buffer_load_dwordx4 v8, s[4:7], s38 offen offset:0 lds
Mv 160,64,112
Rt 4,19,0
v_and_b32 v4, 1, v4
Mv 192,64,116
)" R"(
v_cmp_eq_u32_e32 vcc, 1, v4
s_mov_b32 s85, vcc_lo
Mv 224,64,120
Rt 6,19,2048
s_and_b32 s28, s2, 7
Ma 224,64,124
buffer_load_dwordx4 v9, s[4:7], s38 offen offset:1024 lds
Ma 228,68,124
Rt 8,20,0
s_lshr_b32 s28, s85, s28
Mv 228,68,120
s_xor_b32 s28, s28, s34
s_and_b32 s28, s28, 1
Mv 196,68,116
Rt 10,20,2048
s_lshl_b32 s84, s28, 13
Mv 164,68,112
buffer_load_dwordx4 v10, s[4:7], s38 offen offset:2048 lds
Mv 132,68,108
Rt 12,21,0
s_add_u32 s49, s49, s84
Mv 100,68,104
s_add_u32 s50, s50, s84
s_add_u32 s60, s50, 7168
Mv 68,68,100
Rt 14,21,2048
s_add_u32 s52, s52, s84
Mv 36,68,96
buffer_load_dwordx4 v11, s[4:7], s38 offen offset:3072 lds
Mv 40,72,96
Rt 16,22,0
s_add_u32 s65, s65, s84
Mv 72,72,100
s_add_u32 s38, s38, 0x10000
s_lshr_b32 s28, s2, 6
Mv 104,72,104
Rt 18,22,2048
s_add_u32 m0, s36, 36864
Mv 136,72,108
buffer_load_dwordx4 v8, s[4:7], s38 offen offset:0 lds
Mv 168,72,112
Rt 20,23,0
s_lshl_b32 s28, s28, 14
Mv 200,72,116
s_add_u32 s28, s28, s50
s_bfe_u32 s29, s2, 0x20004
Mv 232,72,120
Rt 22,23,2048
s_lshl2_add_u32 s29, s29, s33
Ma 232,72,124
buffer_load_dwordx4 v9, s[4:7], s38 offen offset:1024 lds
Ma 236,76,124
Rt 24,24,0
s_lshl_b32 s29, s29, 9
Mv 236,76,120
s_add_u32 s28, s28, s29
v_min_u32 v4, 3, v0
Mv 204,76,116
Rt 26,24,2048
v_lshlrev_b32 v4, 7, v4
Mv 172,76,112
buffer_load_dwordx4 v10, s[4:7], s38 offen offset:2048 lds
Mv 140,76,108
Rt 28,25,0
)" R"(
Mv 108,76,104
buffer_load_dword v4, v4, s[20:23], s28 offen
Mv 76,76,100
Rt 30,25,2048
s_lshl1_add_u32 s28, s36, s84
Mv 44,76,96
buffer_load_dwordx4 v11, s[4:7], s38 offen offset:3072 lds
Mv 48,80,96
Rb 32,16,0
v_add_u32 v2, s28, v1
Mv 80,80,100
v_add_u32 v3, 0x1000, v2
s_lshl_b32 s28, s34, 13
Mv 112,80,104
Rb 36,16,2048
s_xor_b32 s28, s28, s84
Mv 144,80,108
s_add_u32 s39, s39, 0x40000
s_and_b32 s28, s28, 0x2000
Mv 176,80,112
Rb 40,16,8192
s_add_u32 m0, s37, 49152
Mv 208,80,116
buffer_load_dwordx4 v12, s[8:11], s39 offen offset:0 lds
Mv 240,80,120
Rb 44,16,10240
s_xor_b32 s73, s73, s28
Ma 240,80,124
Ma 244,84,124
Rb 48,16,16384
Mv 244,84,120
Mv 212,84,116
Rb 52,16,18432
Mv 180,84,112
buffer_load_dwordx4 v13, s[8:11], s39 offen offset:1024 lds
Mv 148,84,108
Rb 56,16,24576
Mv 116,84,104
Mv 84,84,100
Rb 60,16,26624
Mv 52,84,96
Mv 56,88,96
Mv 88,88,100
buffer_load_dwordx4 v14, s[8:11], s39 offen offset:2048 lds
Mv 120,88,104
Mv 152,88,108
Mv 184,88,112
Mv 216,88,116
Mv 248,88,120
Ma 248,88,124
buffer_load_dwordx4 v15, s[8:11], s39 offen offset:3072 lds
Ma 252,92,124
Mv 252,92,120
Mv 220,92,116
Mv 188,92,112
Mv 156,92,108
Mv 124,92,104
Mv 92,92,100
Mv 60,92,96
s_waitcnt vmcnt(5) lgkmcnt(0)
s_barrier
Mv 32,0,32
Rt 64,18,16384
Mv 64,0,36
Rb 160,28,32768
)" R"(
v_and_b32 v5, 7, v0
Mv 96,0,40
Rt 66,18,18432
v_lshlrev_b32 v5, 7, v5
Mv 128,0,44
Rb 164,28,34816
v_mov_b32 v6, s3
Mv 160,0,48
Rt 68,19,16384
Mv 192,0,52
Mv 224,0,56
Rt 70,19,18432
Ma 224,0,60
Rb 168,29,32768
Ma 228,4,60
Rt 72,20,16384
Mv 228,4,56
Rb 172,29,34816
Mv 196,4,52
Rt 74,20,18432
Mv 164,4,48
Rb 176,28,36864
Mv 132,4,44
Rt 76,21,16384
Mv 100,4,40
Mv 68,4,36
Rt 78,21,18432
Mv 36,4,32
Rb 180,28,38912
Mv 40,8,32
Rt 80,22,16384
Mv 72,8,36
Rb 184,29,36864
Mv 104,8,40
Rt 82,22,18432
Mv 136,8,44
Rb 188,29,38912
Mv 168,8,48
Rt 84,23,16384
Mv 200,8,52
Mv 232,8,56
Rt 86,23,18432
Ma 232,8,60
Ma 236,12,60
Rt 88,24,16384
Mv 236,12,56
Mv 204,12,52
Rt 90,24,18432
Mv 172,12,48
Mv 140,12,44
Rt 92,25,16384
Mv 108,12,40
Mv 76,12,36
Rt 94,25,18432
v_lshrrev_b32 v10, 3, v0
Mv 44,12,32
v_and_b32 v11, 0x70, v1
v_lshl_or_b32 v10, v10, 13, v11
Mv 48,16,32
Rb 96,17,0
v_lshlrev_b32 v11, 1, v0
Mv 80,16,36
v_and_b32 v11, 32, v11
v_xor_b32 v10, v10, v11
)" R"(
buffer_load_dword v4, v7, s[20:23], s52 offen offset:0 sc0 sc1
Mv 112,16,40
Rb 100,17,2048
s_and_b32 s28, s33, 1
Mv 144,16,44
s_lshl_b32 s28, s28, 6
v_xor_b32 v8, s28, v10
Mv 176,16,48
Rb 104,17,8192
v_xor_b32 v9, 64, v8
Mv 208,16,52
v_add_u32 v9, 0xfc00, v9
v_add_u32 v10, 0x1f800, v8
Mv 240,16,56
Rb 108,17,10240
v_add_u32 v11, 0x1f800, v9
Ma 240,16,60
Ma 244,20,60
Rb 112,17,16384
Mv 244,20,56
Mv 212,20,52
Rb 116,17,18432
Mv 180,20,48
Mv 148,20,44
Rb 120,17,24576
Mv 116,20,40
Mv 84,20,36
Rb 124,17,26624
Mv 52,20,32
Mv 56,24,32
Mv 88,24,36
Mv 120,24,40
Mv 152,24,44
Mv 184,24,48
Mv 216,24,52
Mv 248,24,56
Ma 248,24,60
Ma 252,28,60
Mv 252,28,56
Mv 220,28,52
Mv 188,28,48
Mv 156,28,44
Mv 124,28,40
Mv 92,28,36
Mv 60,28,32
s_waitcnt vmcnt(5) lgkmcnt(0)
s_barrier
Mv 32,64,96
Rt 0,18,32768
Mv 64,64,100
s_add_u32 s92, s42, 0x0
Mv 96,64,104
Rt 2,18,34816
s_add_u32 m0, s41, 131072
Mv 128,64,108
buffer_load_dwordx4 v8, s[16:19], s92 offen offset:0 lds
Mv 160,64,112
Rt 4,19,32768
Mv 192,64,116
Mv 224,64,120
Rt 6,19,34816
Ma 224,64,124
buffer_load_dwordx4 v9, s[16:19], s92 offen offset:1024 lds
Ma 228,68,124
Rt 8,20,32768
)" R"(
Mv 228,68,120
Mv 196,68,116
Rt 10,20,34816
Mv 164,68,112
buffer_load_dwordx4 v10, s[16:19], s92 offen offset:2048 lds
Mv 132,68,108
Rt 12,21,32768
Mv 100,68,104
Mv 68,68,100
Rt 14,21,34816
Mv 36,68,96
buffer_load_dwordx4 v11, s[16:19], s92 offen offset:3072 lds
Mv 40,72,96
Rt 16,22,32768
Mv 72,72,100
s_add_u32 s92, s42, 0x40000
Mv 104,72,104
Rt 18,22,34816
s_add_u32 m0, s41, 135168
Mv 136,72,108
buffer_load_dwordx4 v8, s[16:19], s92 offen offset:0 lds
Mv 168,72,112
Rt 20,23,32768
Mv 200,72,116
Mv 232,72,120
Rt 22,23,34816
Ma 232,72,124
buffer_load_dwordx4 v9, s[16:19], s92 offen offset:1024 lds
Ma 236,76,124
Rt 24,24,32768
Mv 236,76,120
Mv 204,76,116
Rt 26,24,34816
Mv 172,76,112
buffer_load_dwordx4 v10, s[16:19], s92 offen offset:2048 lds
Mv 140,76,108
Rt 28,25,32768
Mv 108,76,104
s_add_u32 s92, s42, 0x1c0000
Mv 76,76,100
Rt 30,25,34816
s_add_u32 m0, s41, 0
Mv 44,76,96
buffer_load_dwordx4 v8, s[16:19], s92 offen offset:0 lds
Mv 48,80,96
Rb 32,16,32768
Mv 80,80,100
Mv 112,80,104
Rb 36,16,34816
Mv 144,80,108
buffer_load_dwordx4 v9, s[16:19], s92 offen offset:1024 lds
Mv 176,80,112
Rb 40,16,40960
Mv 208,80,116
Mv 240,80,120
Rb 44,16,43008
Ma 240,80,124
buffer_load_dwordx4 v10, s[16:19], s92 offen offset:2048 lds
Ma 244,84,124
Rb 48,16,49152
Mv 244,84,120
Mv 212,84,116
Rb 52,16,51200
Mv 180,84,112
)" R"(
buffer_load_dwordx4 v11, s[16:19], s92 offen offset:3072 lds
Mv 148,84,108
Rb 56,16,57344
Mv 116,84,104
s_add_u32 s92, s42, 0x200000
Mv 84,84,100
Rb 60,16,59392
s_add_u32 m0, s41, 4096
Mv 52,84,96
buffer_load_dwordx4 v8, s[16:19], s92 offen offset:0 lds
Mv 56,88,96
Mv 88,88,100
Mv 120,88,104
Mv 152,88,108
buffer_load_dwordx4 v9, s[16:19], s92 offen offset:1024 lds
Mv 184,88,112
Mv 216,88,116
Mv 248,88,120
Ma 248,88,124
buffer_load_dwordx4 v10, s[16:19], s92 offen offset:2048 lds
Ma 252,92,124
Mv 252,92,120
Mv 220,92,116
Mv 188,92,112
.LzH26:
s_cbranch_execz .LzH37
Mv 156,92,108
Mv 124,92,104
Mv 92,92,100
Mv 60,92,96
s_waitcnt vmcnt(14) lgkmcnt(4)
s_barrier
v_cmp_eq_u32_e32 vcc, s3, v4
s_andn2_b64 s[28:29], 1, vcc
s_cbranch_scc1 .Lzsp2
.Lzok2:
Mv 32,0,32
Rt 64,18,49152
Mv 64,0,36
Rt 66,18,51200
s_add_u32 s92, s42, 0x80
Mv 96,0,40
Rt 68,19,49152
s_add_u32 m0, s41, 65536
Mv 128,0,44
Rt 70,19,51200
buffer_load_dwordx4 v8, s[16:19], s92 offen offset:0 lds
Mv 132,4,44
Rt 72,20,49152
Mv 100,4,40
Rt 74,20,51200
Mv 68,4,36
Rt 76,21,49152
Mv 36,4,32
Rt 78,21,51200
buffer_load_dwordx4 v9, s[16:19], s92 offen offset:1024 lds
Mv 40,8,32
Rt 80,22,49152
Mv 72,8,36
Rt 82,22,51200
Mv 104,8,40
Rt 84,23,49152
Mv 136,8,44
Rt 86,23,51200
buffer_load_dwordx4 v10, s[16:19], s92 offen offset:2048 lds
)" R"(
Mv 140,12,44
Rt 88,24,49152
Mv 108,12,40
Rt 90,24,51200
Mv 76,12,36
Rt 92,25,49152
Mv 44,12,32
Rt 94,25,51200
buffer_load_dwordx4 v11, s[16:19], s92 offen offset:3072 lds
Mv 48,16,32
Rb 96,17,32768
Mv 80,16,36
Rb 100,17,34816
s_add_u32 s92, s42, 0x40080
Mv 112,16,40
Rb 104,17,40960
s_add_u32 m0, s41, 69632
Mv 144,16,44
Rb 108,17,43008
buffer_load_dwordx4 v8, s[16:19], s92 offen offset:0 lds
Mv 148,20,44
Rb 112,17,49152
Mv 116,20,40
Rb 116,17,51200
Mv 84,20,36
Rb 120,17,57344
Mv 52,20,32
Rb 124,17,59392
buffer_load_dwordx4 v9, s[16:19], s92 offen offset:1024 lds
Mv 56,24,32
Mv 88,24,36
Mv 120,24,40
Mv 152,24,44
buffer_load_dwordx4 v10, s[16:19], s92 offen offset:2048 lds
Mv 156,28,44
Mv 124,28,40
Mv 92,28,36
Mv 60,28,32
s_waitcnt lgkmcnt(0)
s_barrier
Mv 32,64,96
v_bfe_u32 v12, v0, 3, 2
v_lshlrev_b32 v12, 5, v12
Mv 36,68,96
v_and_b32 v4, 3, v0
v_lshl_or_b32 v12, v4, 3, v12
Mv 40,72,96
s_add_u32 m0, s36, 98304
s_add_u32 s44, s43, 768
Mv 44,76,96
buffer_load_dwordx4 v26, s[12:15], s44 offen offset:0 lds
Mv 48,80,96
v_bfe_u32 v4, v0, 2, 2
v_lshl_or_b32 v12, v4, 7, v12
Mv 52,84,96
v_lshrrev_b32 v4, 4, v0
v_lshl_or_b32 v12, v4, 10, v12
Mv 56,88,96
v_xor_b32 v13, 32, v12
v_xor_b32 v14, 64, v12
Mv 60,92,96
buffer_load_dwordx4 v27, s[12:15], s44 offen offset:1024 lds
Mv 64,64,100
v_xor_b32 v15, 96, v12
v_add_u32 v20, 0x20000, v12
)" R"(
Mv 68,68,100
v_add_u32 v21, 0x20000, v13
v_add_u32 v22, 0x20000, v14
Mv 72,72,100
v_add_u32 v23, 0x20000, v15
v_add_u32 v16, 0x10000, v12
Mv 76,76,100
s_add_u32 s44, s43, 13056
buffer_load_dwordx4 v26, s[12:15], s44 offen offset:2048 lds
Mv 80,80,100
v_add_u32 v17, 0x10000, v13
v_add_u32 v18, 0x10000, v14
Mv 84,84,100
v_add_u32 v19, 0x10000, v15
Cv 32,32,33
Mv 88,88,100
Cv 33,34,35
Cv 34,36,37
Mv 92,92,100
buffer_load_dwordx4 v27, s[12:15], s44 offen offset:3072 lds
Mv 96,64,104
Cv 35,38,39
Cv 40,40,41
Mv 100,68,104
Cv 41,42,43
Cv 42,44,45
Mv 104,72,104
s_add_u32 m0, s41, 32768
s_add_u32 s92, s42, 0x1c0080
Mv 108,76,104
buffer_load_dwordx4 v8, s[16:19], s92 offen offset:0 lds
Mv 112,80,104
Cv 43,46,47
Cv 48,48,49
Mv 116,84,104
Cv 49,50,51
Cv 50,52,53
Mv 120,88,104
Cv 51,54,55
Cv 56,56,57
Mv 124,92,104
buffer_store_dwordx4 v[32:35], v2, s[20:23], s45 offen offset:0 sc0 sc1
Mv 128,64,108
Cv 57,58,59
Cv 58,60,61
Mv 132,68,108
Cv 59,62,63
Cv 64,64,65
Mv 136,72,108
Cv 65,66,67
Cv 66,68,69
Mv 140,76,108
buffer_load_dwordx4 v9, s[16:19], s92 offen offset:1024 lds
Mv 144,80,108
Cv 67,70,71
Cv 72,72,73
Mv 148,84,108
Cv 73,74,75
Cv 74,76,77
Mv 152,88,108
Cv 75,78,79
Cv 80,80,81
Mv 156,92,108
buffer_store_dwordx4 v[40:43], v2, s[20:23], s45 offen offset:1024 sc0 sc1
)" R"(
Mv 160,0,48
Cv 81,82,83
Cv 82,84,85
Mv 160,64,112
Cv 83,86,87
Cv 88,88,89
Mv 164,4,48
Cv 89,90,91
Cv 90,92,93
Mv 164,68,112
buffer_load_dwordx4 v10, s[16:19], s92 offen offset:2048 lds
Mv 168,8,48
Cv 91,94,95
Cv 96,96,97
Mv 168,72,112
Cv 97,98,99
Cv 98,100,101
Mv 172,12,48
Cv 99,102,103
Cv 104,104,105
Mv 172,76,112
buffer_store_dwordx4 v[48:51], v2, s[20:23], s45 offen offset:2048 sc0 sc1
Mv 176,16,48
Cv 105,106,107
Cv 106,108,109
Mv 176,80,112
Cv 107,110,111
Cv 112,112,113
Mv 180,20,48
Cv 113,114,115
Cv 114,116,117
Mv 180,84,112
buffer_load_dwordx4 v11, s[16:19], s92 offen offset:3072 lds
Mv 184,24,48
Cv 115,118,119
Cv 120,120,121
Mv 184,88,112
Cv 121,122,123
Cv 122,124,125
Mv 188,28,48
Cv 123,126,127
Cv 128,128,129
Mv 188,92,112
buffer_store_dwordx4 v[56:59], v2, s[20:23], s45 offen offset:3072 sc0 sc1
Mv 192,0,52
Cv 129,130,131
Cv 130,132,133
Mv 192,64,116
Cv 131,134,135
Cv 136,136,137
Mv 196,4,52
s_add_u32 m0, s41, 36864
s_add_u32 s92, s42, 0x200080
Mv 196,68,116
buffer_load_dwordx4 v8, s[16:19], s92 offen offset:0 lds
Mv 200,8,52
Cv 137,138,139
Cv 138,140,141
Mv 200,72,116
Cv 139,142,143
Cv 144,144,145
Mv 204,12,52
Cv 145,146,147
Cv 146,148,149
)" R"(
Mv 204,76,116
buffer_store_dwordx4 v[64:67], v3, s[20:23], s45 offen offset:0 sc0 sc1
Mv 208,16,52
Cv 147,150,151
Cv 152,152,153
Mv 208,80,116
Cv 153,154,155
Cv 154,156,157
Mv 212,20,52
Cv 155,158,159
Mv 212,84,116
buffer_load_dwordx4 v9, s[16:19], s92 offen offset:1024 lds
Mv 216,24,52
Mv 216,88,116
Mv 220,28,52
Mv 220,92,116
buffer_store_dwordx4 v[72:75], v3, s[20:23], s45 offen offset:1024 sc0 sc1
Mv 224,0,56
Ma 224,0,60
Ma 228,4,60
Mv 228,4,56
buffer_load_dwordx4 v10, s[16:19], s92 offen offset:2048 lds
Mv 232,8,56
Ma 232,8,60
Ma 236,12,60
Mv 236,12,56
s_waitcnt vmcnt(24)
s_barrier
Mv 240,16,56
buffer_store_dwordx4 v[80:83], v3, s[20:23], s45 offen offset:2048 sc0 sc1
Ma 240,16,60
Ma 244,20,60
Mv 244,20,56
Mv 248,24,56
Ma 248,24,60
Ma 252,28,60
Mv 252,28,56
buffer_store_dwordx4 v[88:91], v3, s[20:23], s45 offen offset:3072 sc0 sc1
Mv 224,64,120
Rt 16,20,0
Mv 228,68,120
Rt 18,20,512
Mv 232,72,120
Rt 20,21,0
Mv 236,76,120
Rt 22,21,512
Mv 240,80,120
Rt 24,22,0
Mv 244,84,120
Rt 26,22,512
Mv 248,88,120
Rt 28,23,0
Mv 252,92,120
Rt 30,23,512
Ma 224,64,124
Rt 32,20,4096
Ma 228,68,124
Rt 34,20,4608
Ma 232,72,124
Rt 36,21,4096
Ma 236,76,124
Rt 38,21,4608
Ma 240,80,124
Rt 40,22,4096
Ma 244,84,124
)" R"(
Rt 42,22,4608
Ma 248,88,124
Rt 44,23,4096
Ma 252,92,124
Rt 46,23,4608
s_waitcnt vmcnt(15) lgkmcnt(8)
s_branch .LzQA
.LzH27:
s_cbranch_execz .LzH33
.LzAV:
Mz 32,32,0
Rt 64,18,16384
Mz 36,36,0
s_lshl1_add_u32 s40, s34, s91
s_lshl_b32 s40, s40, 12
Mz 96,40,0
Rt 66,18,18432
s_and_b32 s29, s38, 0xfffe0000
Mz 100,44,0
s_add_u32 s39, s39, 0x40000
s_add_u32 s40, s40, s29
Mz 160,48,0
Rt 68,19,16384
s_add_u32 m0, s37, 0
Mz 164,52,0
buffer_load_dwordx4 v12, s[8:11], s39 offen offset:0 lds
Mz 224,56,0
Rt 70,19,18432
v_min_u32 v3, 31, v0
Mz 228,60,0
v_lshlrev_b32 v3, 7, v3
Mz 236,60,4
Rt 72,20,16384
Mz 232,56,4
Mz 172,52,4
Rt 74,20,18432
Mz 168,48,4
Mz 108,44,4
Rt 76,21,16384
Mz 104,40,4
buffer_load_dwordx4 v13, s[8:11], s39 offen offset:1024 lds
Mz 44,36,4
Rt 78,21,18432
Mz 40,32,4
Mz 48,32,8
Rt 80,22,16384
Mz 52,36,8
Mz 112,40,8
Rt 82,22,18432
Mz 116,44,8
Mz 176,48,8
Rt 84,23,16384
Mz 180,52,8
buffer_load_dwordx4 v14, s[8:11], s39 offen offset:2048 lds
Mz 240,56,8
Rt 86,23,18432
Mz 244,60,8
Mz 252,60,12
Rt 88,24,16384
Mz 248,56,12
Mz 188,52,12
Rt 90,24,18432
Mz 184,48,12
Mz 124,44,12
)" R"(
Rt 92,25,16384
Mz 120,40,12
buffer_load_dwordx4 v15, s[8:11], s39 offen offset:3072 lds
Mz 60,36,12
Rt 94,25,18432
Mz 56,32,12
Mz 64,32,16
Rb 96,17,0
Mz 68,36,16
buffer_load_dword v6, v3, s[4:7], s40 offen
Mz 128,40,16
Rb 100,17,2048
Mz 132,44,16
Mz 192,48,16
Rb 104,17,8192
s_mov_b32 s28, s35
Mz 196,52,16
s_lshl2_add_u32 s28, s28, s33
s_mul_i32 s43, s28, 28672
v_mfma_f32_16x16x32_bf16 a[224:227], a[56:59], a[16:19], 0
Rb 108,17,10240
v_and_b32 v26, 0x70, v1
v_mfma_f32_16x16x32_bf16 a[228:231], a[60:63], a[16:19], 0
v_and_b32 v4, 48, v0
v_xor_b32 v26, v26, v4
v_mfma_f32_16x16x32_bf16 a[236:239], a[60:63], a[20:23], 0
Rb 112,17,16384
v_lshrrev_b32 v4, 3, v0
v_mfma_f32_16x16x32_bf16 a[232:235], a[56:59], a[20:23], 0
v_mul_u32_u24 v4, 896, v4
v_add_u32 v26, v26, v4
Mz 204,52,20
Rb 116,17,18432
v_xor_b32 v27, 64, v26
Mz 200,48,20
v_add_u32 v27, 6144, v27
v_bfe_u32 v28, v0, 1, 3
Mz 140,44,20
Rb 120,17,24576
v_lshrrev_b32 v4, 4, v0
Mz 136,40,20
v_xor_b32 v28, v28, v4
v_lshlrev_b32 v28, 4, v28
Mz 76,36,20
Rb 124,17,26624
v_and_b32 v4, 15, v0
Mz 72,32,20
v_lshl_or_b32 v28, v4, 7, v28
s_add_u32 s28, s36, 0x18000
Mz 80,32,24
v_add_u32 v28, s28, v28
v_xor_b32 v29, 64, v28
Mz 84,36,24
Mz 144,40,24
Mz 148,44,24
Mz 208,48,24
Mz 212,52,24
v_mfma_f32_16x16x32_bf16 a[240:243], a[56:59], a[24:27], 0
v_mfma_f32_16x16x32_bf16 a[244:247], a[60:63], a[24:27], 0
v_mfma_f32_16x16x32_bf16 a[252:255], a[60:63], a[28:31], 0
v_mfma_f32_16x16x32_bf16 a[248:251], a[56:59], a[28:31], 0
Mz 220,52,28
Mz 216,48,28
Mz 156,44,28
)" R"(
Mz 152,40,28
Mz 92,36,28
Mz 88,32,28
s_call_b64 s[30:31], .LzHOP
s_waitcnt vmcnt(11) lgkmcnt(0)
s_barrier
Mv 32,96,64
Rt 0,18,32768
s_bfe_u32 s88, s2, 0x20005
Mv 36,100,64
s_add_u32 s38, s38, 0xffff0080
s_and_b32 s28, s2, 15
Mv 96,104,64
Rt 2,18,34816
s_add_u32 m0, s36, 0
Mv 100,108,64
buffer_load_dwordx4 v8, s[4:7], s38 offen offset:0 lds
Mv 160,112,64
Rt 4,19,32768
s_lshl4_add_u32 s88, s88, s28
Mv 164,116,64
s_lshl2_add_u32 s88, s88, s33
s_mul_i32 s88, s88, 4096
Mv 224,120,64
Rt 6,19,34816
s_add_u32 s88, s88, 0x100000
Mv 228,124,64
buffer_load_dwordx4 v9, s[4:7], s38 offen offset:1024 lds
Mv 236,124,68
Rt 8,20,32768
v_min_u32 v5, 31, v0
Mv 232,120,68
v_lshlrev_b32 v5, 7, v5
s_mov_b32 s90, 0x100000
Mv 172,116,68
Rt 10,20,34816
s_mov_b64 s[24:25], s[8:9]
Mv 168,112,68
buffer_load_dwordx4 v10, s[4:7], s38 offen offset:2048 lds
Mv 108,108,68
Rt 12,21,32768
s_mov_b64 s[26:27], s[10:11]
Mv 104,104,68
Mv 44,100,68
Rt 14,21,34816
Mv 40,96,68
buffer_load_dwordx4 v11, s[4:7], s38 offen offset:3072 lds
Mv 48,96,72
Rt 16,22,32768
Mv 52,100,72
s_add_u32 s38, s38, 0x10000
Mv 112,104,72
Rt 18,22,34816
s_add_u32 m0, s36, 4096
Mv 116,108,72
buffer_load_dwordx4 v8, s[4:7], s38 offen offset:0 lds
Mv 176,112,72
Rt 20,23,32768
Mv 180,116,72
Mv 240,120,72
Rt 22,23,34816
Mv 244,124,72
buffer_load_dwordx4 v9, s[4:7], s38 offen offset:1024 lds
Mv 252,124,76
Rt 24,24,32768
)" R"(
Mv 248,120,76
Mv 188,116,76
Rt 26,24,34816
Mv 184,112,76
buffer_load_dwordx4 v10, s[4:7], s38 offen offset:2048 lds
Mv 124,108,76
Rt 28,25,32768
Mv 120,104,76
Mv 60,100,76
Rt 30,25,34816
Mv 56,96,76
buffer_load_dwordx4 v11, s[4:7], s38 offen offset:3072 lds
Mv 64,96,80
Rb 32,16,32768
Mv 68,100,80
Mv 128,104,80
Rb 36,16,34816
Mv 132,108,80
s_add_u32 s39, s39, 0x40000
Mv 192,112,80
Rb 40,16,40960
s_add_u32 m0, s37, 16384
Mv 196,116,80
buffer_load_dwordx4 v12, s[8:11], s39 offen offset:0 lds
Ma 224,120,80
Rb 44,16,43008
Ma 228,124,80
Ma 236,124,84
Rb 48,16,49152
Ma 232,120,84
Mv 204,116,84
Rb 52,16,51200
Mv 200,112,84
buffer_load_dwordx4 v13, s[8:11], s39 offen offset:1024 lds
Mv 140,108,84
Rb 56,16,57344
Mv 136,104,84
Mv 76,100,84
Rb 60,16,59392
Mv 72,96,84
Mv 80,96,88
Mv 84,100,88
buffer_load_dwordx4 v14, s[8:11], s39 offen offset:2048 lds
Mv 144,104,88
Mv 148,108,88
Mv 208,112,88
Mv 212,116,88
Ma 240,120,88
.LzH28:
s_cbranch_execz .LzH34
Ma 244,124,88
buffer_load_dwordx4 v15, s[8:11], s39 offen offset:3072 lds
Ma 252,124,92
Ma 248,120,92
s_add_u32 s88, s88, s90
Mv 220,116,92
Mv 216,112,92
buffer_load_dword v6, v5, s[24:27], s88 offen
Mv 156,108,92
Mv 152,104,92
Mv 92,100,92
Mv 88,96,92
s_waitcnt vmcnt(20) lgkmcnt(0)
s_barrier
Mv 32,32,0
)" R"(
Rt 64,18,49152
s_lshl_b32 s28, s34, 21
Mv 36,36,0
s_lshl_b32 s29, s91, 25
s_add_u32 s28, s28, s29
Mv 96,40,0
Rt 66,18,51200
s_bfe_u32 s29, s63, 0x80000
Mv 100,44,0
s_add_u32 s39, s39, 0x40000
s_lshl_b32 s29, s29, 16
Mv 160,48,0
Rt 68,19,49152
s_add_u32 m0, s37, 32768
Mv 164,52,0
buffer_load_dwordx4 v12, s[8:11], s39 offen offset:0 lds
Mv 224,56,0
Rt 70,19,51200
s_add_u32 s45, s28, s29
Mv 228,60,0
s_bfe_u32 s29, s63, 0x80008
s_lshl_b32 s29, s29, 16
Mv 236,60,4
Rt 72,20,49152
s_add_u32 s46, s28, s29
Mv 232,56,4
s_bfe_u32 s29, s63, 0x80010
s_lshl_b32 s29, s29, 16
Mv 172,52,4
Rt 74,20,51200
s_add_u32 s47, s28, s29
Mv 168,48,4
s_bfe_u32 s29, s63, 0x80018
s_lshl_b32 s29, s29, 16
Mv 108,44,4
Rt 76,21,49152
s_add_u32 s48, s28, s29
Mv 104,40,4
buffer_load_dwordx4 v13, s[8:11], s39 offen offset:1024 lds
Mv 44,36,4
Rt 78,21,51200
s_lshl_b32 s50, s34, 21
Mv 40,32,4
s_add_u32 s50, s50, 0x6140000
s_lshl2_add_u32 s28, s91, s93
Mv 48,32,8
Rt 80,22,49152
s_lshl_b32 s28, s28, 7
Mv 52,36,8
s_add_u32 s49, s28, s50
s_lshl_b32 s52, s35, 7
Mv 112,40,8
Rt 82,22,51200
s_add_u32 s52, s52, s50
Mv 116,44,8
s_add_u32 s52, s52, 0xd000
s_lshl4_add_u32 s28, s34, s35
Mv 176,48,8
Rt 84,23,49152
s_lshl_b32 s51, s28, 17
Mv 180,52,8
buffer_load_dwordx4 v14, s[8:11], s39 offen offset:2048 lds
Mv 240,56,8
Rt 86,23,51200
)" R"(
s_add_u32 s51, s51, 0x4000000
Mv 244,60,8
s_cmp_lg_u32 s33, 0
s_cmov_b32 s49, 0x40000000
Mv 252,60,12
Rt 88,24,49152
s_mov_b32 s53, 0x40000
Mv 248,56,12
s_mov_b32 s54, 0x3db8aa3b
s_mul_i32 s41, s33, 7168
Mv 188,52,12
Rt 90,24,51200
s_mul_i32 s42, s33, 0x70000
Mv 184,48,12
s_lshl_b32 s28, s34, 9
s_add_u32 s42, s42, s28
Mv 124,44,12
Rt 92,25,49152
s_lshl_b32 s89, s34, 21
Mv 120,40,12
buffer_load_dwordx4 v15, s[8:11], s39 offen offset:3072 lds
Mv 60,36,12
Rt 94,25,51200
s_mov_b32 s62, 0x4e800000
Mv 56,32,12
s_add_u32 s67, s89, 0x6000000
s_lshl3_add_u32 s28, s34, s35
Mv 64,32,16
Rb 96,17,32768
s_add_u32 s64, s51, 0x100000
s_add_u32 m0, s36, 131072
s_add_u32 s44, s43, 0
Mv 68,36,16
s_lshl2_add_u32 s28, s28, s33
s_lshl2_add_u32 s65, s35, s33
buffer_load_dwordx4 v26, s[12:15], s44 offen offset:0 lds
Mv 128,40,16
Rb 100,17,34816
s_lshl_b32 s65, s65, 7
Mv 132,44,16
s_add_u32 s65, s65, s50
s_add_u32 s65, s65, 0xc000
Mv 192,48,16
Rb 104,17,40960
s_lshl_b32 s66, s28, 10
Mv 196,52,16
s_add_u32 s66, s66, 0xc000000
s_mov_b32 s28, s35
buffer_load_dwordx4 v27, s[12:15], s44 offen offset:1024 lds
Ma 224,56,16
Rb 108,17,43008
s_lshl2_add_u32 s28, s28, s33
Ma 228,60,16
s_lshl_b32 s29, s28, 5
s_add_u32 s29, s29, s34
Ma 236,60,20
Rb 112,17,49152
s_lshl_b32 s73, s29, 15
Ma 232,56,20
s_add_u32 s73, s73, 0x8000000
s_lshr_b32 s29, s36, 1
s_add_u32 s44, s43, 12288
buffer_load_dwordx4 v26, s[12:15], s44 offen offset:2048 lds
Mv 204,52,20
)" R"(
Rb 116,17,51200
s_and_b32 s29, s29, 0x2000
Mv 200,48,20
s_add_u32 s73, s73, s29
s_lshl4_add_u32 s28, s28, s34
Mv 140,44,20
Rb 120,17,57344
s_lshl_b32 s68, s28, 8
Mv 136,40,20
s_add_u32 s68, s68, 0xc110000
s_add_u32 s55, s89, 0x120000
buffer_load_dwordx4 v27, s[12:15], s44 offen offset:3072 lds
Mv 76,36,20
Rb 124,17,59392
s_lshl_b32 s28, s35, 1
Mv 72,32,20
s_sub_u32 s57, 14, s28
s_mov_b32 s59, s89
Mv 80,32,24
s_mov_b32 s58, 2
s_add_u32 m0, s36, 135168
s_add_u32 s44, s43, 128
Mv 84,36,24
s_cmp_eq_u32 s35, 7
s_cmov_b32 s55, s89
buffer_load_dwordx4 v26, s[12:15], s44 offen offset:0 lds
Mv 144,40,24
s_cmp_eq_u32 s35, 7
s_cmov_b32 s57, 16
Mv 148,44,24
s_cmp_eq_u32 s35, 7
s_cmov_b32 s59, s67
Mv 208,48,24
s_add_u32 s56, s55, 0x2000000
Mv 212,52,24
buffer_load_dwordx4 v27, s[12:15], s44 offen offset:1024 lds
Ma 240,56,24
Ma 244,60,24
Ma 252,60,28
Ma 248,56,28
s_add_u32 s44, s43, 12416
buffer_load_dwordx4 v26, s[12:15], s44 offen offset:2048 lds
Mv 220,52,28
Mv 216,48,28
Mv 156,44,28
Mv 152,40,28
buffer_load_dwordx4 v27, s[12:15], s44 offen offset:3072 lds
Mv 92,36,28
Mv 88,32,28
s_waitcnt vmcnt(17) lgkmcnt(0)
s_barrier
Mv 32,96,64
Rt 0,18,0
Mv 36,100,64
s_add_u32 s38, s38, 0xffff0080
Mv 96,104,64
Rt 2,18,2048
s_add_u32 m0, s36, 32768
Mv 100,108,64
buffer_load_dwordx4 v8, s[4:7], s38 offen offset:0 lds
Mv 160,112,64
Rt 4,19,0
Mv 164,116,64
Mv 224,120,64
Rt 6,19,2048
)" R"(
Mv 228,124,64
buffer_load_dwordx4 v9, s[4:7], s38 offen offset:1024 lds
Mv 236,124,68
Rt 8,20,0
s_lshl2_add_u32 s28, s2, s33
Mv 232,120,68
s_lshl_b32 s28, s28, 11
s_add_u32 s28, s28, 0xc100000
Mv 172,116,68
Rt 10,20,2048
v_min_u32 v3, 15, v0
Mv 168,112,68
buffer_load_dwordx4 v10, s[4:7], s38 offen offset:2048 lds
Mv 108,108,68
Rt 12,21,0
v_lshlrev_b32 v3, 7, v3
Mv 104,104,68
Mv 44,100,68
Rt 14,21,2048
Mv 40,96,68
buffer_load_dwordx4 v11, s[4:7], s38 offen offset:3072 lds
Mv 48,96,72
Rt 16,22,0
Mv 52,100,72
s_add_u32 s38, s38, 0x10000
Mv 112,104,72
Rt 18,22,2048
s_add_u32 m0, s36, 36864
Mv 116,108,72
buffer_load_dwordx4 v8, s[4:7], s38 offen offset:0 lds
Mv 176,112,72
Rt 20,23,0
Mv 180,116,72
Mv 240,120,72
Rt 22,23,2048
Mv 244,124,72
buffer_load_dwordx4 v9, s[4:7], s38 offen offset:1024 lds
Mv 252,124,76
Rt 24,24,0
Mv 248,120,76
Mv 188,116,76
Rt 26,24,2048
Mv 184,112,76
buffer_load_dwordx4 v10, s[4:7], s38 offen offset:2048 lds
Mv 124,108,76
Rt 28,25,0
Mv 120,104,76
Mv 60,100,76
Rt 30,25,2048
Mv 56,96,76
buffer_load_dwordx4 v11, s[4:7], s38 offen offset:3072 lds
Mv 64,96,80
Rb 32,16,0
Mv 68,100,80
Mv 128,104,80
Rb 36,16,2048
Mv 132,108,80
s_add_u32 s39, s39, 0x40000
Mv 192,112,80
Rb 40,16,8192
s_add_u32 m0, s37, 49152
Mv 196,116,80
buffer_load_dwordx4 v12, s[8:11], s39 offen offset:0 lds
Ma 224,120,80
)" R"(
Rb 44,16,10240
Ma 228,124,80
Ma 236,124,84
Rb 48,16,16384
Ma 232,120,84
Mv 204,116,84
Rb 52,16,18432
Mv 200,112,84
buffer_load_dwordx4 v13, s[8:11], s39 offen offset:1024 lds
Mv 140,108,84
Rb 56,16,24576
Mv 136,104,84
Mv 76,100,84
Rb 60,16,26624
Mv 72,96,84
Mv 80,96,88
Mv 84,100,88
buffer_load_dwordx4 v14, s[8:11], s39 offen offset:2048 lds
Mv 144,104,88
Mv 148,108,88
Mv 208,112,88
Mv 212,116,88
Ma 240,120,88
Ma 244,124,88
buffer_load_dwordx4 v15, s[8:11], s39 offen offset:3072 lds
Ma 252,124,92
Ma 248,120,92
s_add_u32 s88, s88, s90
s_mov_b32 s40, 5
Mv 220,116,92
Mv 216,112,92
buffer_load_dword v6, v5, s[24:27], s88 offen
Mv 156,108,92
Mv 152,104,92
buffer_load_dword v6, v3, s[20:23], s28 offen
Mv 92,100,92
Mv 88,96,92
s_waitcnt vmcnt(27) lgkmcnt(0)
s_barrier
.LzPAV:
Mv 32,32,0
Rt 64,18,16384
Mv 36,36,0
Mv 96,40,0
Rt 66,18,18432
Mv 100,44,0
s_add_u32 s39, s39, 0x40000
Mv 160,48,0
Rt 68,19,16384
s_add_u32 m0, s37, 0
Mv 164,52,0
buffer_load_dwordx4 v12, s[8:11], s39 offen offset:0 lds
Mv 224,56,0
Rt 70,19,18432
Mv 228,60,0
Mv 236,60,4
Rt 72,20,16384
Mv 232,56,4
Mv 172,52,4
Rt 74,20,18432
Mv 168,48,4
Mv 108,44,4
Rt 76,21,16384
Mv 104,40,4
buffer_load_dwordx4 v13, s[8:11], s39 offen offset:1024 lds
)" R"(
Mv 44,36,4
Rt 78,21,18432
Mv 40,32,4
Mv 48,32,8
Rt 80,22,16384
Mv 52,36,8
Mv 112,40,8
Rt 82,22,18432
Mv 116,44,8
Mv 176,48,8
Rt 84,23,16384
Mv 180,52,8
buffer_load_dwordx4 v14, s[8:11], s39 offen offset:2048 lds
Mv 240,56,8
Rt 86,23,18432
Mv 244,60,8
Mv 252,60,12
Rt 88,24,16384
Mv 248,56,12
Mv 188,52,12
Rt 90,24,18432
Mv 184,48,12
Mv 124,44,12
Rt 92,25,16384
Mv 120,40,12
buffer_load_dwordx4 v15, s[8:11], s39 offen offset:3072 lds
Mv 60,36,12
Rt 94,25,18432
Mv 56,32,12
Mv 64,32,16
Rb 96,17,0
Mv 68,36,16
Mv 128,40,16
Rb 100,17,2048
Mv 132,44,16
Mv 192,48,16
Rb 104,17,8192
Mv 196,52,16
Ma 224,56,16
Rb 108,17,10240
Ma 228,60,16
Ma 236,60,20
Rb 112,17,16384
Ma 232,56,20
Mv 204,52,20
Rb 116,17,18432
Mv 200,48,20
Mv 140,44,20
Rb 120,17,24576
Mv 136,40,20
Mv 76,36,20
Rb 124,17,26624
Mv 72,32,20
Mv 80,32,24
Mv 84,36,24
Mv 144,40,24
Mv 148,44,24
Mv 208,48,24
Mv 212,52,24
Ma 240,56,24
Ma 244,60,24
Ma 252,60,28
Ma 248,56,28
Mv 220,52,28
)" R"(
Mv 216,48,28
Mv 156,44,28
Mv 152,40,28
Mv 92,36,28
Mv 88,32,28
s_waitcnt vmcnt(9) lgkmcnt(0)
s_barrier
Mv 32,96,64
Rt 0,18,32768
Mv 36,100,64
s_add_u32 s38, s38, 0xffff0080
Mv 96,104,64
Rt 2,18,34816
s_add_u32 m0, s36, 0
Mv 100,108,64
buffer_load_dwordx4 v8, s[4:7], s38 offen offset:0 lds
Mv 160,112,64
Rt 4,19,32768
Mv 164,116,64
Mv 224,120,64
Rt 6,19,34816
Mv 228,124,64
buffer_load_dwordx4 v9, s[4:7], s38 offen offset:1024 lds
Mv 236,124,68
Rt 8,20,32768
Mv 232,120,68
Mv 172,116,68
Rt 10,20,34816
Mv 168,112,68
buffer_load_dwordx4 v10, s[4:7], s38 offen offset:2048 lds
Mv 108,108,68
Rt 12,21,32768
Mv 104,104,68
Mv 44,100,68
Rt 14,21,34816
Mv 40,96,68
buffer_load_dwordx4 v11, s[4:7], s38 offen offset:3072 lds
Mv 48,96,72
Rt 16,22,32768
Mv 52,100,72
s_add_u32 s38, s38, 0x10000
Mv 112,104,72
Rt 18,22,34816
s_add_u32 m0, s36, 4096
Mv 116,108,72
buffer_load_dwordx4 v8, s[4:7], s38 offen offset:0 lds
Mv 176,112,72
Rt 20,23,32768
Mv 180,116,72
Mv 240,120,72
Rt 22,23,34816
Mv 244,124,72
buffer_load_dwordx4 v9, s[4:7], s38 offen offset:1024 lds
Mv 252,124,76
Rt 24,24,32768
Mv 248,120,76
Mv 188,116,76
Rt 26,24,34816
Mv 184,112,76
buffer_load_dwordx4 v10, s[4:7], s38 offen offset:2048 lds
Mv 124,108,76
Rt 28,25,32768
Mv 120,104,76
Mv 60,100,76
Rt 30,25,34816
)" R"(
Mv 56,96,76
buffer_load_dwordx4 v11, s[4:7], s38 offen offset:3072 lds
Mv 64,96,80
Rb 32,16,32768
Mv 68,100,80
Mv 128,104,80
Rb 36,16,34816
Mv 132,108,80
s_add_u32 s39, s39, 0x40000
Mv 192,112,80
Rb 40,16,40960
s_add_u32 m0, s37, 16384
Mv 196,116,80
buffer_load_dwordx4 v12, s[8:11], s39 offen offset:0 lds
Ma 224,120,80
Rb 44,16,43008
Ma 228,124,80
Ma 236,124,84
Rb 48,16,49152
Ma 232,120,84
Mv 204,116,84
Rb 52,16,51200
Mv 200,112,84
buffer_load_dwordx4 v13, s[8:11], s39 offen offset:1024 lds
Mv 140,108,84
Rb 56,16,57344
Mv 136,104,84
Mv 76,100,84
Rb 60,16,59392
Mv 72,96,84
Mv 80,96,88
Mv 84,100,88
buffer_load_dwordx4 v14, s[8:11], s39 offen offset:2048 lds
Mv 144,104,88
Mv 148,108,88
Mv 208,112,88
Mv 212,116,88
Ma 240,120,88
Ma 244,124,88
buffer_load_dwordx4 v15, s[8:11], s39 offen offset:3072 lds
Ma 252,124,92
Ma 248,120,92
s_add_u32 s88, s88, s90
Mv 220,116,92
Mv 216,112,92
buffer_load_dword v6, v5, s[24:27], s88 offen
Mv 156,108,92
Mv 152,104,92
Mv 92,100,92
Mv 88,96,92
s_waitcnt vmcnt(18) lgkmcnt(0)
s_barrier
Mv 32,32,0
Rt 64,18,49152
Mv 36,36,0
Mv 96,40,0
Rt 66,18,51200
Mv 100,44,0
s_add_u32 s39, s39, 0x40000
Mv 160,48,0
Rt 68,19,49152
s_add_u32 m0, s37, 32768
Mv 164,52,0
buffer_load_dwordx4 v12, s[8:11], s39 offen offset:0 lds
Mv 224,56,0
)" R"(
Rt 70,19,51200
Mv 228,60,0
Mv 236,60,4
Rt 72,20,49152
Mv 232,56,4
Mv 172,52,4
Rt 74,20,51200
Mv 168,48,4
Mv 108,44,4
Rt 76,21,49152
Mv 104,40,4
buffer_load_dwordx4 v13, s[8:11], s39 offen offset:1024 lds
Mv 44,36,4
Rt 78,21,51200
Mv 40,32,4
Mv 48,32,8
Rt 80,22,49152
.LzH29:
s_cbranch_execz .LzH35
Mv 52,36,8
Mv 112,40,8
Rt 82,22,51200
Mv 116,44,8
Mv 176,48,8
Rt 84,23,49152
Mv 180,52,8
buffer_load_dwordx4 v14, s[8:11], s39 offen offset:2048 lds
Mv 240,56,8
Rt 86,23,51200
Mv 244,60,8
Mv 252,60,12
Rt 88,24,49152
Mv 248,56,12
Mv 188,52,12
Rt 90,24,51200
Mv 184,48,12
Mv 124,44,12
Rt 92,25,49152
Mv 120,40,12
buffer_load_dwordx4 v15, s[8:11], s39 offen offset:3072 lds
Mv 60,36,12
Rt 94,25,51200
Mv 56,32,12
Mv 64,32,16
Rb 96,17,32768
Mv 68,36,16
Mv 128,40,16
Rb 100,17,34816
Mv 132,44,16
Mv 192,48,16
Rb 104,17,40960
Mv 196,52,16
Ma 224,56,16
Rb 108,17,43008
Ma 228,60,16
Ma 236,60,20
Rb 112,17,49152
Ma 232,56,20
Mv 204,52,20
Rb 116,17,51200
Mv 200,48,20
Mv 140,44,20
Rb 120,17,57344
Mv 136,40,20
)" R"(
Mv 76,36,20
Rb 124,17,59392
Mv 72,32,20
Mv 80,32,24
Mv 84,36,24
Mv 144,40,24
Mv 148,44,24
Mv 208,48,24
Mv 212,52,24
Ma 240,56,24
Ma 244,60,24
Ma 252,60,28
Ma 248,56,28
Mv 220,52,28
Mv 216,48,28
Mv 156,44,28
Mv 152,40,28
Mv 92,36,28
Mv 88,32,28
s_waitcnt vmcnt(9) lgkmcnt(0)
s_barrier
Mv 32,96,64
Rt 0,18,0
Mv 36,100,64
s_add_u32 s38, s38, 0xffff0080
Mv 96,104,64
Rt 2,18,2048
s_add_u32 m0, s36, 32768
Mv 100,108,64
buffer_load_dwordx4 v8, s[4:7], s38 offen offset:0 lds
Mv 160,112,64
Rt 4,19,0
Mv 164,116,64
Mv 224,120,64
Rt 6,19,2048
Mv 228,124,64
buffer_load_dwordx4 v9, s[4:7], s38 offen offset:1024 lds
Mv 236,124,68
Rt 8,20,0
Mv 232,120,68
Mv 172,116,68
Rt 10,20,2048
Mv 168,112,68
buffer_load_dwordx4 v10, s[4:7], s38 offen offset:2048 lds
Mv 108,108,68
Rt 12,21,0
Mv 104,104,68
Mv 44,100,68
Rt 14,21,2048
Mv 40,96,68
buffer_load_dwordx4 v11, s[4:7], s38 offen offset:3072 lds
Mv 48,96,72
Rt 16,22,0
Mv 52,100,72
s_add_u32 s38, s38, 0x10000
Mv 112,104,72
Rt 18,22,2048
s_add_u32 m0, s36, 36864
Mv 116,108,72
buffer_load_dwordx4 v8, s[4:7], s38 offen offset:0 lds
Mv 176,112,72
Rt 20,23,0
Mv 180,116,72
Mv 240,120,72
Rt 22,23,2048
)" R"(
Mv 244,124,72
buffer_load_dwordx4 v9, s[4:7], s38 offen offset:1024 lds
Mv 252,124,76
Rt 24,24,0
Mv 248,120,76
Mv 188,116,76
Rt 26,24,2048
Mv 184,112,76
buffer_load_dwordx4 v10, s[4:7], s38 offen offset:2048 lds
Mv 124,108,76
Rt 28,25,0
Mv 120,104,76
Mv 60,100,76
Rt 30,25,2048
Mv 56,96,76
buffer_load_dwordx4 v11, s[4:7], s38 offen offset:3072 lds
Mv 64,96,80
Rb 32,16,0
Mv 68,100,80
Mv 128,104,80
Rb 36,16,2048
Mv 132,108,80
s_add_u32 s39, s39, 0x40000
Mv 192,112,80
Rb 40,16,8192
s_add_u32 m0, s37, 49152
Mv 196,116,80
buffer_load_dwordx4 v12, s[8:11], s39 offen offset:0 lds
Ma 224,120,80
Rb 44,16,10240
Ma 228,124,80
Ma 236,124,84
Rb 48,16,16384
Ma 232,120,84
Mv 204,116,84
Rb 52,16,18432
Mv 200,112,84
buffer_load_dwordx4 v13, s[8:11], s39 offen offset:1024 lds
Mv 140,108,84
Rb 56,16,24576
Mv 136,104,84
Mv 76,100,84
Rb 60,16,26624
Mv 72,96,84
Mv 80,96,88
Mv 84,100,88
buffer_load_dwordx4 v14, s[8:11], s39 offen offset:2048 lds
Mv 144,104,88
Mv 148,108,88
Mv 208,112,88
Mv 212,116,88
Ma 240,120,88
Ma 244,124,88
buffer_load_dwordx4 v15, s[8:11], s39 offen offset:3072 lds
Ma 252,124,92
Ma 248,120,92
s_add_u32 s88, s88, s90
Mv 220,116,92
s_sub_u32 s40, s40, 1
Mv 216,112,92
buffer_load_dword v6, v5, s[24:27], s88 offen
Mv 156,108,92
Mv 152,104,92
Mv 92,100,92
)" R"(
Mv 88,96,92
s_cmp_lg_u32 s40, 0
s_waitcnt vmcnt(18) lgkmcnt(0)
s_barrier
s_cbranch_scc1 .LzPAV
.LzPAVX:
Mv 32,32,0
Rt 64,18,16384
Mv 36,36,0
Rb 192,28,32768
Mv 96,40,0
Rt 66,18,18432
Mv 100,44,0
s_add_u32 s39, s39, 0x40000
Rb 196,28,34816
Mv 160,48,0
Rt 68,19,16384
s_add_u32 m0, s37, 0
Mv 164,52,0
buffer_load_dwordx4 v12, s[8:11], s39 offen offset:0 lds
Mv 224,56,0
Rt 70,19,18432
Mv 228,60,0
Rb 200,29,32768
Mv 236,60,4
Rt 72,20,16384
Mv 232,56,4
Rb 204,29,34816
Mv 172,52,4
Rt 74,20,18432
Mv 168,48,4
Rb 208,28,36864
Mv 108,44,4
Rt 76,21,16384
Mv 104,40,4
buffer_load_dwordx4 v13, s[8:11], s39 offen offset:1024 lds
Mv 44,36,4
Rt 78,21,18432
Mv 40,32,4
Rb 212,28,38912
Mv 48,32,8
Rt 80,22,16384
Mv 52,36,8
Rb 216,29,36864
Mv 112,40,8
Rt 82,22,18432
Mv 116,44,8
Rb 220,29,38912
Mv 176,48,8
Rt 84,23,16384
Mv 180,52,8
buffer_load_dwordx4 v14, s[8:11], s39 offen offset:2048 lds
Mv 240,56,8
Rt 86,23,18432
Mv 244,60,8
Mv 252,60,12
Rt 88,24,16384
Mv 248,56,12
Mv 188,52,12
Rt 90,24,18432
Mv 184,48,12
Mv 124,44,12
Rt 92,25,16384
Mv 120,40,12
buffer_load_dwordx4 v15, s[8:11], s39 offen offset:3072 lds
)" R"(
Mv 60,36,12
Rt 94,25,18432
Mv 56,32,12
s_add_u32 s44, s43, 256
s_waitcnt lgkmcnt(6)
Mv 64,32,16
Rb 96,17,0
s_add_u32 m0, s36, 131072
Mv 68,36,16
buffer_load_dwordx4 v26, s[12:15], s44 offen offset:0 lds
Mv 128,40,16
Rb 100,17,2048
Mv 132,44,16
Mv 192,48,16
Rb 104,17,8192
Mv 196,52,16
buffer_load_dwordx4 v27, s[12:15], s44 offen offset:1024 lds
Ma 224,56,16
Rb 108,17,10240
Ma 228,60,16
Ma 236,60,20
Rb 112,17,16384
Ma 232,56,20
s_add_u32 s44, s43, 12544
buffer_load_dwordx4 v26, s[12:15], s44 offen offset:2048 lds
Mv 204,52,20
Rb 116,17,18432
Mv 200,48,20
Mv 140,44,20
Rb 120,17,24576
Mv 136,40,20
buffer_load_dwordx4 v27, s[12:15], s44 offen offset:3072 lds
Mv 76,36,20
Rb 124,17,26624
Mv 72,32,20
Mv 80,32,24
s_add_u32 m0, s36, 135168
s_add_u32 s44, s43, 384
Mv 84,36,24
buffer_load_dwordx4 v26, s[12:15], s44 offen offset:0 lds
Mv 144,40,24
Mv 148,44,24
Mv 208,48,24
Mv 212,52,24
buffer_load_dwordx4 v27, s[12:15], s44 offen offset:1024 lds
Ma 240,56,24
Ma 244,60,24
Ma 252,60,28
Ma 248,56,28
s_add_u32 s44, s43, 12672
buffer_load_dwordx4 v26, s[12:15], s44 offen offset:2048 lds
Mv 220,52,28
Mv 216,48,28
Mv 156,44,28
s_mov_b32 s85, 0xc108800
v_and_b32 v2, 7, v0
v_lshlrev_b32 v2, 7, v2
Mv 152,40,28
buffer_load_dwordx4 v27, s[12:15], s44 offen offset:3072 lds
Mv 92,36,28
buffer_load_dword v4, v2, s[20:23], s85 offen sc0 sc1
Mv 88,32,28
s_waitcnt vmcnt(18) lgkmcnt(0)
s_barrier
Mv 32,96,64
)" R"(
Rt 0,18,32768
Mv 36,100,64
s_add_u32 s38, s38, 0xffff0080
Mv 96,104,64
Rt 2,18,34816
s_add_u32 m0, s36, 0
Mv 100,108,64
buffer_load_dwordx4 v8, s[4:7], s38 offen offset:0 lds
Mv 160,112,64
Rt 4,19,32768
Mv 164,116,64
Mv 224,120,64
Rt 6,19,34816
Mv 228,124,64
buffer_load_dwordx4 v9, s[4:7], s38 offen offset:1024 lds
Mv 236,124,68
Rt 8,20,32768
Mv 232,120,68
Mv 172,116,68
Rt 10,20,34816
Mv 168,112,68
buffer_load_dwordx4 v10, s[4:7], s38 offen offset:2048 lds
Mv 108,108,68
Rt 12,21,32768
Mv 104,104,68
Mv 44,100,68
Rt 14,21,34816
Mv 40,96,68
buffer_load_dwordx4 v11, s[4:7], s38 offen offset:3072 lds
Mv 48,96,72
Rt 16,22,32768
Mv 52,100,72
s_add_u32 s38, s38, 0x10000
Mv 112,104,72
Rt 18,22,34816
s_add_u32 m0, s36, 4096
Mv 116,108,72
buffer_load_dwordx4 v8, s[4:7], s38 offen offset:0 lds
Mv 176,112,72
Rt 20,23,32768
Mv 180,116,72
Mv 240,120,72
Rt 22,23,34816
Mv 244,124,72
buffer_load_dwordx4 v9, s[4:7], s38 offen offset:1024 lds
Mv 252,124,76
Rt 24,24,32768
Mv 248,120,76
Mv 188,116,76
Rt 26,24,34816
Mv 184,112,76
buffer_load_dwordx4 v10, s[4:7], s38 offen offset:2048 lds
Mv 124,108,76
Rt 28,25,32768
Mv 120,104,76
Mv 60,100,76
Rt 30,25,34816
Mv 56,96,76
buffer_load_dwordx4 v11, s[4:7], s38 offen offset:3072 lds
Mv 64,96,80
Rb 32,16,32768
Mv 68,100,80
Mv 128,104,80
Rb 36,16,34816
)" R"(
Mv 132,108,80
s_add_u32 s39, s39, 0x40000
Mv 192,112,80
Rb 40,16,40960
s_add_u32 m0, s37, 16384
Mv 196,116,80
buffer_load_dwordx4 v12, s[8:11], s39 offen offset:0 lds
Ma 224,120,80
Rb 44,16,43008
Ma 228,124,80
Ma 236,124,84
Rb 48,16,49152
Ma 232,120,84
Mv 204,116,84
Rb 52,16,51200
Mv 200,112,84
buffer_load_dwordx4 v13, s[8:11], s39 offen offset:1024 lds
Mv 140,108,84
Rb 56,16,57344
Mv 136,104,84
Mv 76,100,84
Rb 60,16,59392
Mv 72,96,84
Mv 80,96,88
Mv 84,100,88
buffer_load_dwordx4 v14, s[8:11], s39 offen offset:2048 lds
Mv 144,104,88
Mv 148,108,88
Mv 208,112,88
Mv 212,116,88
Ma 240,120,88
Ma 244,124,88
buffer_load_dwordx4 v15, s[8:11], s39 offen offset:3072 lds
Ma 252,124,92
Ma 248,120,92
Mv 220,116,92
Mv 216,112,92
Mv 156,108,92
Mv 152,104,92
Mv 92,100,92
Mv 88,96,92
s_waitcnt vmcnt(13) lgkmcnt(0)
s_barrier
Mv 32,32,0
Rt 64,18,49152
Mv 36,36,0
Rb 128,28,32768
Mv 96,40,0
Rt 66,18,51200
Mv 100,44,0
s_add_u32 s39, s39, 0x40000
Rb 132,28,34816
Mv 160,48,0
Rt 68,19,49152
s_add_u32 m0, s37, 32768
Mv 164,52,0
buffer_load_dwordx4 v12, s[8:11], s39 offen offset:0 lds
Mv 224,56,0
Rt 70,19,51200
Mv 228,60,0
Rb 136,29,32768
Mv 236,60,4
Rt 72,20,49152
Mv 232,56,4
Rb 140,29,34816
)" R"(
Mv 172,52,4
Rt 74,20,51200
Mv 168,48,4
Rb 144,28,36864
Mv 108,44,4
Rt 76,21,49152
Mv 104,40,4
buffer_load_dwordx4 v13, s[8:11], s39 offen offset:1024 lds
Mv 44,36,4
Rt 78,21,51200
Mv 40,32,4
Rb 148,28,38912
Mv 48,32,8
Rt 80,22,49152
Mv 52,36,8
Rb 152,29,36864
Mv 112,40,8
Rt 82,22,51200
Mv 116,44,8
Rb 156,29,38912
Mv 176,48,8
Rt 84,23,49152
Mv 180,52,8
buffer_load_dwordx4 v14, s[8:11], s39 offen offset:2048 lds
Mv 240,56,8
Rt 86,23,51200
Mv 244,60,8
Mv 252,60,12
Rt 88,24,49152
Mv 248,56,12
Mv 188,52,12
Rt 90,24,51200
Mv 184,48,12
Mv 124,44,12
Rt 92,25,49152
Mv 120,40,12
buffer_load_dwordx4 v15, s[8:11], s39 offen offset:3072 lds
Mv 60,36,12
Rt 94,25,51200
Mv 56,32,12
s_add_u32 s44, s43, 512
s_waitcnt lgkmcnt(6)
Mv 64,32,16
Rb 96,17,32768
s_add_u32 m0, s36, 131072
Mv 68,36,16
buffer_load_dwordx4 v26, s[12:15], s44 offen offset:0 lds
Mv 128,40,16
Rb 100,17,34816
Mv 132,44,16
Mv 192,48,16
Rb 104,17,40960
Mv 196,52,16
buffer_load_dwordx4 v27, s[12:15], s44 offen offset:1024 lds
Ma 224,56,16
Rb 108,17,43008
Ma 228,60,16
Ma 236,60,20
Rb 112,17,49152
Ma 232,56,20
s_add_u32 s44, s43, 12800
buffer_load_dwordx4 v26, s[12:15], s44 offen offset:2048 lds
Mv 204,52,20
Rb 116,17,51200
)" R"(
Mv 200,48,20
Mv 140,44,20
Rb 120,17,57344
Mv 136,40,20
buffer_load_dwordx4 v27, s[12:15], s44 offen offset:3072 lds
Mv 76,36,20
Rb 124,17,59392
Mv 72,32,20
Mv 80,32,24
s_add_u32 m0, s36, 135168
s_add_u32 s44, s43, 640
Mv 84,36,24
buffer_load_dwordx4 v26, s[12:15], s44 offen offset:0 lds
Mv 144,40,24
Mv 148,44,24
Mv 208,48,24
Mv 212,52,24
buffer_load_dwordx4 v27, s[12:15], s44 offen offset:1024 lds
Ma 240,56,24
Ma 244,60,24
Ma 252,60,28
Ma 248,56,28
s_add_u32 s44, s43, 12928
buffer_load_dwordx4 v26, s[12:15], s44 offen offset:2048 lds
.LzH30:
s_cbranch_execz .LzH36
Mv 220,52,28
Mv 216,48,28
Mv 156,44,28
Mv 152,40,28
buffer_load_dwordx4 v27, s[12:15], s44 offen offset:3072 lds
Mv 92,36,28
Mv 88,32,28
s_waitcnt vmcnt(16) lgkmcnt(0)
s_barrier
Mv 32,96,64
Rt 0,18,0
v_xor_b32 v4, s3, v4
v_cmp_gt_u32_e32 vcc, 2, v4
s_andn2_b32 s28, 0xff, vcc_lo
s_cbranch_scc1 .Lzsp3
.Lzok3:
Mv 36,100,64
s_add_u32 s38, s38, 0xffff0080
v_xor_b32 v4, s3, v4
Mv 96,104,64
Rt 2,18,2048
s_add_u32 m0, s36, 32768
Mv 100,108,64
buffer_load_dwordx4 v8, s[4:7], s38 offen offset:0 lds
Mv 160,112,64
Rt 4,19,0
v_and_b32 v4, 1, v4
Mv 164,116,64
v_cmp_eq_u32_e32 vcc, 1, v4
s_mov_b32 s85, vcc_lo
Mv 224,120,64
Rt 6,19,2048
s_and_b32 s28, s2, 7
Mv 228,124,64
buffer_load_dwordx4 v9, s[4:7], s38 offen offset:1024 lds
Mv 236,124,68
Rt 8,20,0
s_lshr_b32 s28, s85, s28
Mv 232,120,68
)" R"(
s_xor_b32 s28, s28, s34
s_and_b32 s28, s28, 1
Mv 172,116,68
Rt 10,20,2048
s_lshl_b32 s84, s28, 13
Mv 168,112,68
buffer_load_dwordx4 v10, s[4:7], s38 offen offset:2048 lds
Mv 108,108,68
Rt 12,21,0
s_add_u32 s49, s49, s84
Mv 104,104,68
s_add_u32 s50, s50, s84
s_add_u32 s60, s50, 7168
Mv 44,100,68
Rt 14,21,2048
s_add_u32 s52, s52, s84
Mv 40,96,68
buffer_load_dwordx4 v11, s[4:7], s38 offen offset:3072 lds
Mv 48,96,72
Rt 16,22,0
s_add_u32 s65, s65, s84
Mv 52,100,72
s_add_u32 s38, s38, 0x10000
s_lshr_b32 s28, s2, 6
Mv 112,104,72
Rt 18,22,2048
s_add_u32 m0, s36, 36864
Mv 116,108,72
buffer_load_dwordx4 v8, s[4:7], s38 offen offset:0 lds
Mv 176,112,72
Rt 20,23,0
s_lshl_b32 s28, s28, 14
Mv 180,116,72
s_add_u32 s28, s28, s50
s_bfe_u32 s29, s2, 0x20004
Mv 240,120,72
Rt 22,23,2048
s_lshl2_add_u32 s29, s29, s33
Mv 244,124,72
buffer_load_dwordx4 v9, s[4:7], s38 offen offset:1024 lds
Mv 252,124,76
Rt 24,24,0
s_lshl_b32 s29, s29, 9
Mv 248,120,76
s_add_u32 s28, s28, s29
v_min_u32 v4, 3, v0
Mv 188,116,76
Rt 26,24,2048
v_lshlrev_b32 v4, 7, v4
Mv 184,112,76
buffer_load_dwordx4 v10, s[4:7], s38 offen offset:2048 lds
Mv 124,108,76
Rt 28,25,0
Mv 120,104,76
buffer_load_dword v4, v4, s[20:23], s28 offen
Mv 60,100,76
Rt 30,25,2048
s_lshl1_add_u32 s28, s36, s84
Mv 56,96,76
buffer_load_dwordx4 v11, s[4:7], s38 offen offset:3072 lds
Mv 64,96,80
Rb 32,16,0
v_add_u32 v2, s28, v1
Mv 68,100,80
)" R"(
v_add_u32 v3, 0x1000, v2
s_lshl_b32 s28, s34, 13
Mv 128,104,80
Rb 36,16,2048
s_xor_b32 s28, s28, s84
Mv 132,108,80
s_add_u32 s39, s39, 0x40000
s_and_b32 s28, s28, 0x2000
Mv 192,112,80
Rb 40,16,8192
s_add_u32 m0, s37, 49152
Mv 196,116,80
buffer_load_dwordx4 v12, s[8:11], s39 offen offset:0 lds
Ma 224,120,80
Rb 44,16,10240
s_xor_b32 s73, s73, s28
Ma 228,124,80
Ma 236,124,84
Rb 48,16,16384
Ma 232,120,84
Mv 204,116,84
Rb 52,16,18432
Mv 200,112,84
buffer_load_dwordx4 v13, s[8:11], s39 offen offset:1024 lds
Mv 140,108,84
Rb 56,16,24576
Mv 136,104,84
Mv 76,100,84
Rb 60,16,26624
Mv 72,96,84
Mv 80,96,88
Mv 84,100,88
buffer_load_dwordx4 v14, s[8:11], s39 offen offset:2048 lds
Mv 144,104,88
Mv 148,108,88
Mv 208,112,88
Mv 212,116,88
Ma 240,120,88
Ma 244,124,88
buffer_load_dwordx4 v15, s[8:11], s39 offen offset:3072 lds
Ma 252,124,92
Ma 248,120,92
Mv 220,116,92
Mv 216,112,92
Mv 156,108,92
Mv 152,104,92
Mv 92,100,92
Mv 88,96,92
s_waitcnt vmcnt(5) lgkmcnt(0)
s_barrier
Mv 32,32,0
Rt 64,18,16384
Mv 36,36,0
Rb 160,28,32768
v_and_b32 v5, 7, v0
Mv 96,40,0
Rt 66,18,18432
v_lshlrev_b32 v5, 7, v5
Mv 100,44,0
Rb 164,28,34816
v_mov_b32 v6, s3
Mv 160,48,0
Rt 68,19,16384
Mv 164,52,0
Mv 224,56,0
)" R"(
Rt 70,19,18432
Mv 228,60,0
Rb 168,29,32768
Mv 236,60,4
Rt 72,20,16384
Mv 232,56,4
Rb 172,29,34816
Mv 172,52,4
Rt 74,20,18432
Mv 168,48,4
Rb 176,28,36864
Mv 108,44,4
Rt 76,21,16384
Mv 104,40,4
Mv 44,36,4
Rt 78,21,18432
Mv 40,32,4
Rb 180,28,38912
Mv 48,32,8
Rt 80,22,16384
Mv 52,36,8
Rb 184,29,36864
Mv 112,40,8
Rt 82,22,18432
Mv 116,44,8
Rb 188,29,38912
Mv 176,48,8
Rt 84,23,16384
Mv 180,52,8
Mv 240,56,8
Rt 86,23,18432
Mv 244,60,8
Mv 252,60,12
Rt 88,24,16384
Mv 248,56,12
Mv 188,52,12
Rt 90,24,18432
Mv 184,48,12
Mv 124,44,12
Rt 92,25,16384
Mv 120,40,12
Mv 60,36,12
Rt 94,25,18432
v_lshrrev_b32 v10, 3, v0
Mv 56,32,12
v_and_b32 v11, 0x70, v1
v_lshl_or_b32 v10, v10, 13, v11
Mv 64,32,16
Rb 96,17,0
v_lshlrev_b32 v11, 1, v0
Mv 68,36,16
v_and_b32 v11, 32, v11
v_xor_b32 v10, v10, v11
buffer_load_dword v4, v7, s[20:23], s52 offen offset:0 sc0 sc1
Mv 128,40,16
Rb 100,17,2048
s_and_b32 s28, s33, 1
Mv 132,44,16
s_lshl_b32 s28, s28, 6
v_xor_b32 v8, s28, v10
Mv 192,48,16
Rb 104,17,8192
v_xor_b32 v9, 64, v8
Mv 196,52,16
)" R"(
v_add_u32 v9, 0xfc00, v9
v_add_u32 v10, 0x1f800, v8
Ma 224,56,16
Rb 108,17,10240
v_add_u32 v11, 0x1f800, v9
Ma 228,60,16
Ma 236,60,20
Rb 112,17,16384
Ma 232,56,20
Mv 204,52,20
Rb 116,17,18432
Mv 200,48,20
Mv 140,44,20
Rb 120,17,24576
Mv 136,40,20
Mv 76,36,20
Rb 124,17,26624
Mv 72,32,20
Mv 80,32,24
Mv 84,36,24
Mv 144,40,24
Mv 148,44,24
Mv 208,48,24
Mv 212,52,24
Ma 240,56,24
Ma 244,60,24
Ma 252,60,28
Ma 248,56,28
Mv 220,52,28
Mv 216,48,28
Mv 156,44,28
Mv 152,40,28
Mv 92,36,28
Mv 88,32,28
s_waitcnt vmcnt(5) lgkmcnt(0)
s_barrier
Mv 32,96,64
Rt 0,18,32768
Mv 36,100,64
s_add_u32 s92, s42, 0x0
Mv 96,104,64
Rt 2,18,34816
s_add_u32 m0, s41, 131072
Mv 100,108,64
buffer_load_dwordx4 v8, s[16:19], s92 offen offset:0 lds
Mv 160,112,64
Rt 4,19,32768
Mv 164,116,64
Mv 224,120,64
Rt 6,19,34816
Mv 228,124,64
buffer_load_dwordx4 v9, s[16:19], s92 offen offset:1024 lds
Mv 236,124,68
Rt 8,20,32768
Mv 232,120,68
Mv 172,116,68
Rt 10,20,34816
Mv 168,112,68
buffer_load_dwordx4 v10, s[16:19], s92 offen offset:2048 lds
Mv 108,108,68
Rt 12,21,32768
Mv 104,104,68
Mv 44,100,68
Rt 14,21,34816
Mv 40,96,68
)" R"(
buffer_load_dwordx4 v11, s[16:19], s92 offen offset:3072 lds
Mv 48,96,72
Rt 16,22,32768
Mv 52,100,72
s_add_u32 s92, s42, 0x40000
Mv 112,104,72
Rt 18,22,34816
s_add_u32 m0, s41, 135168
Mv 116,108,72
buffer_load_dwordx4 v8, s[16:19], s92 offen offset:0 lds
Mv 176,112,72
Rt 20,23,32768
Mv 180,116,72
Mv 240,120,72
Rt 22,23,34816
Mv 244,124,72
buffer_load_dwordx4 v9, s[16:19], s92 offen offset:1024 lds
Mv 252,124,76
Rt 24,24,32768
Mv 248,120,76
Mv 188,116,76
Rt 26,24,34816
Mv 184,112,76
buffer_load_dwordx4 v10, s[16:19], s92 offen offset:2048 lds
Mv 124,108,76
Rt 28,25,32768
Mv 120,104,76
s_add_u32 s92, s42, 0x1c0000
Mv 60,100,76
Rt 30,25,34816
s_add_u32 m0, s41, 0
Mv 56,96,76
buffer_load_dwordx4 v8, s[16:19], s92 offen offset:0 lds
Mv 64,96,80
Rb 32,16,32768
Mv 68,100,80
Mv 128,104,80
Rb 36,16,34816
Mv 132,108,80
buffer_load_dwordx4 v9, s[16:19], s92 offen offset:1024 lds
Mv 192,112,80
Rb 40,16,40960
Mv 196,116,80
Ma 224,120,80
Rb 44,16,43008
Ma 228,124,80
buffer_load_dwordx4 v10, s[16:19], s92 offen offset:2048 lds
Ma 236,124,84
Rb 48,16,49152
Ma 232,120,84
Mv 204,116,84
Rb 52,16,51200
Mv 200,112,84
buffer_load_dwordx4 v11, s[16:19], s92 offen offset:3072 lds
Mv 140,108,84
Rb 56,16,57344
Mv 136,104,84
s_add_u32 s92, s42, 0x200000
Mv 76,100,84
Rb 60,16,59392
s_add_u32 m0, s41, 4096
Mv 72,96,84
buffer_load_dwordx4 v8, s[16:19], s92 offen offset:0 lds
Mv 80,96,88
)" R"(
Mv 84,100,88
Mv 144,104,88
Mv 148,108,88
buffer_load_dwordx4 v9, s[16:19], s92 offen offset:1024 lds
Mv 208,112,88
Mv 212,116,88
Ma 240,120,88
Ma 244,124,88
buffer_load_dwordx4 v10, s[16:19], s92 offen offset:2048 lds
Ma 252,124,92
Ma 248,120,92
Mv 220,116,92
Mv 216,112,92
Mv 156,108,92
Mv 152,104,92
Mv 92,100,92
Mv 88,96,92
s_waitcnt vmcnt(14) lgkmcnt(4)
s_barrier
v_cmp_eq_u32_e32 vcc, s3, v4
s_andn2_b64 s[28:29], 1, vcc
s_cbranch_scc1 .Lzsp4
.Lzok4:
Mv 32,32,0
Rt 64,18,49152
Mv 36,36,0
Rt 66,18,51200
s_add_u32 s92, s42, 0x80
Mv 96,40,0
Rt 68,19,49152
s_add_u32 m0, s41, 65536
Mv 100,44,0
Rt 70,19,51200
buffer_load_dwordx4 v8, s[16:19], s92 offen offset:0 lds
Mv 108,44,4
Rt 72,20,49152
Mv 104,40,4
Rt 74,20,51200
Mv 44,36,4
Rt 76,21,49152
Mv 40,32,4
Rt 78,21,51200
buffer_load_dwordx4 v9, s[16:19], s92 offen offset:1024 lds
Mv 48,32,8
Rt 80,22,49152
Mv 52,36,8
Rt 82,22,51200
Mv 112,40,8
Rt 84,23,49152
Mv 116,44,8
Rt 86,23,51200
buffer_load_dwordx4 v10, s[16:19], s92 offen offset:2048 lds
Mv 124,44,12
Rt 88,24,49152
Mv 120,40,12
Rt 90,24,51200
Mv 60,36,12
Rt 92,25,49152
Mv 56,32,12
Rt 94,25,51200
buffer_load_dwordx4 v11, s[16:19], s92 offen offset:3072 lds
Mv 64,32,16
Rb 96,17,32768
Mv 68,36,16
Rb 100,17,34816
)" R"(
s_add_u32 s92, s42, 0x40080
Mv 128,40,16
Rb 104,17,40960
s_add_u32 m0, s41, 69632
Mv 132,44,16
Rb 108,17,43008
buffer_load_dwordx4 v8, s[16:19], s92 offen offset:0 lds
Mv 140,44,20
Rb 112,17,49152
Mv 136,40,20
Rb 116,17,51200
Mv 76,36,20
Rb 120,17,57344
Mv 72,32,20
Rb 124,17,59392
buffer_load_dwordx4 v9, s[16:19], s92 offen offset:1024 lds
Mv 80,32,24
Mv 84,36,24
Mv 144,40,24
Mv 148,44,24
buffer_load_dwordx4 v10, s[16:19], s92 offen offset:2048 lds
Mv 156,44,28
Mv 152,40,28
Mv 92,36,28
Mv 88,32,28
s_waitcnt lgkmcnt(0)
s_barrier
Mv 32,96,64
v_bfe_u32 v12, v0, 3, 2
v_lshlrev_b32 v12, 5, v12
Mv 36,100,64
v_and_b32 v4, 3, v0
v_lshl_or_b32 v12, v4, 3, v12
Mv 40,96,68
s_add_u32 m0, s36, 98304
s_add_u32 s44, s43, 768
Mv 44,100,68
buffer_load_dwordx4 v26, s[12:15], s44 offen offset:0 lds
Mv 48,96,72
v_bfe_u32 v4, v0, 2, 2
v_lshl_or_b32 v12, v4, 7, v12
Mv 52,100,72
v_lshrrev_b32 v4, 4, v0
v_lshl_or_b32 v12, v4, 10, v12
Mv 56,96,76
v_xor_b32 v13, 32, v12
v_xor_b32 v14, 64, v12
Mv 60,100,76
buffer_load_dwordx4 v27, s[12:15], s44 offen offset:1024 lds
Mv 64,96,80
v_xor_b32 v15, 96, v12
v_add_u32 v20, 0x20000, v12
Mv 68,100,80
v_add_u32 v21, 0x20000, v13
v_add_u32 v22, 0x20000, v14
Mv 72,96,84
v_add_u32 v23, 0x20000, v15
v_add_u32 v16, 0x10000, v12
Mv 76,100,84
s_add_u32 s44, s43, 13056
buffer_load_dwordx4 v26, s[12:15], s44 offen offset:2048 lds
Mv 80,96,88
v_add_u32 v17, 0x10000, v13
v_add_u32 v18, 0x10000, v14
Mv 84,100,88
)" R"(
v_add_u32 v19, 0x10000, v15
Cv 32,32,33
Mv 88,96,92
Cv 33,34,35
Cv 34,36,37
Mv 92,100,92
buffer_load_dwordx4 v27, s[12:15], s44 offen offset:3072 lds
Mv 96,104,64
Cv 35,38,39
Cv 40,40,41
Mv 100,108,64
Cv 41,42,43
Cv 42,44,45
Mv 104,104,68
s_add_u32 m0, s41, 32768
s_add_u32 s92, s42, 0x1c0080
Mv 108,108,68
buffer_load_dwordx4 v8, s[16:19], s92 offen offset:0 lds
Mv 112,104,72
Cv 43,46,47
Cv 48,48,49
Mv 116,108,72
Cv 49,50,51
Cv 50,52,53
Mv 120,104,76
Cv 51,54,55
Cv 56,56,57
Mv 124,108,76
buffer_store_dwordx4 v[32:35], v2, s[20:23], s45 offen offset:0 sc0 sc1
Mv 128,104,80
Cv 57,58,59
Cv 58,60,61
Mv 132,108,80
Cv 59,62,63
Cv 64,64,65
Mv 136,104,84
Cv 65,66,67
Cv 66,68,69
Mv 140,108,84
buffer_load_dwordx4 v9, s[16:19], s92 offen offset:1024 lds
Mv 144,104,88
Cv 67,70,71
Cv 72,72,73
Mv 148,108,88
Cv 73,74,75
Cv 74,76,77
Mv 152,104,92
Cv 75,78,79
Cv 80,80,81
Mv 156,108,92
buffer_store_dwordx4 v[40:43], v2, s[20:23], s45 offen offset:1024 sc0 sc1
Mv 160,48,0
Cv 81,82,83
Cv 82,84,85
.LzH31:
s_cbranch_execz .LzH37
Mv 160,112,64
Cv 83,86,87
Cv 88,88,89
Mv 164,52,0
Cv 89,90,91
Cv 90,92,93
Mv 164,116,64
buffer_load_dwordx4 v10, s[16:19], s92 offen offset:2048 lds
)" R"(
Mv 168,48,4
Cv 91,94,95
Cv 96,96,97
Mv 168,112,68
Cv 97,98,99
Cv 98,100,101
Mv 172,52,4
Cv 99,102,103
Cv 104,104,105
Mv 172,116,68
buffer_store_dwordx4 v[48:51], v2, s[20:23], s45 offen offset:2048 sc0 sc1
Mv 176,48,8
Cv 105,106,107
Cv 106,108,109
Mv 176,112,72
Cv 107,110,111
Cv 112,112,113
Mv 180,52,8
Cv 113,114,115
Cv 114,116,117
Mv 180,116,72
buffer_load_dwordx4 v11, s[16:19], s92 offen offset:3072 lds
Mv 184,48,12
Cv 115,118,119
Cv 120,120,121
Mv 184,112,76
Cv 121,122,123
Cv 122,124,125
Mv 188,52,12
Cv 123,126,127
Cv 128,128,129
Mv 188,116,76
buffer_store_dwordx4 v[56:59], v2, s[20:23], s45 offen offset:3072 sc0 sc1
Mv 192,48,16
Cv 129,130,131
Cv 130,132,133
Mv 192,112,80
Cv 131,134,135
Cv 136,136,137
Mv 196,52,16
s_add_u32 m0, s41, 36864
s_add_u32 s92, s42, 0x200080
Mv 196,116,80
buffer_load_dwordx4 v8, s[16:19], s92 offen offset:0 lds
Mv 200,48,20
Cv 137,138,139
Cv 138,140,141
Mv 200,112,84
Cv 139,142,143
Cv 144,144,145
Mv 204,52,20
Cv 145,146,147
Cv 146,148,149
Mv 204,116,84
buffer_store_dwordx4 v[64:67], v3, s[20:23], s45 offen offset:0 sc0 sc1
Mv 208,48,24
Cv 147,150,151
Cv 152,152,153
Mv 208,112,88
Cv 153,154,155
Cv 154,156,157
Mv 212,52,24
Cv 155,158,159
Mv 212,116,88
)" R"(
buffer_load_dwordx4 v9, s[16:19], s92 offen offset:1024 lds
Mv 216,48,28
Mv 216,112,92
Mv 220,52,28
Mv 220,116,92
buffer_store_dwordx4 v[72:75], v3, s[20:23], s45 offen offset:1024 sc0 sc1
Mv 224,56,0
Mv 228,60,0
Mv 236,60,4
Mv 232,56,4
buffer_load_dwordx4 v10, s[16:19], s92 offen offset:2048 lds
Mv 240,56,8
Mv 244,60,8
Mv 252,60,12
Mv 248,56,12
s_waitcnt vmcnt(24)
s_barrier
Ma 224,56,16
buffer_store_dwordx4 v[80:83], v3, s[20:23], s45 offen offset:2048 sc0 sc1
Ma 228,60,16
Ma 236,60,20
Ma 232,56,20
Ma 240,56,24
Ma 244,60,24
Ma 252,60,28
Ma 248,56,28
buffer_store_dwordx4 v[88:91], v3, s[20:23], s45 offen offset:3072 sc0 sc1
Mv 224,120,64
Rt 16,20,0
Mv 228,124,64
Rt 18,20,512
Mv 232,120,68
Rt 20,21,0
Mv 236,124,68
Rt 22,21,512
Mv 240,120,72
Rt 24,22,0
Mv 244,124,72
Rt 26,22,512
Mv 248,120,76
Rt 28,23,0
Mv 252,124,76
Rt 30,23,512
Ma 224,120,80
Rt 32,20,4096
Ma 228,124,80
Rt 34,20,4608
Ma 232,120,84
Rt 36,21,4096
Ma 236,124,84
Rt 38,21,4608
Ma 240,120,88
Rt 40,22,4096
Ma 244,124,88
Rt 42,22,4608
Ma 248,120,92
Rt 44,23,4096
Ma 252,124,92
Rt 46,23,4608
s_waitcnt vmcnt(15) lgkmcnt(8)
.LzH32:
s_cbranch_execz .LzH40
.LzQA:
s_cmp_eq_u32 s2, 112
s_cbranch_scc1 .LzT2S
)" R"(
.LzT2E:
Mz 32,16,192
Rt 0,20,8192
Cv 224,224,225
Mz 40,16,196
Rt 2,20,8704
Rb 48,28,0
Mz 44,20,196
Rt 4,21,8192
buffer_store_dwordx4 v[96:99], v2, s[20:23], s46 offen offset:0 sc0 sc1
Mz 36,20,192
Rt 6,21,8704
Rb 52,28,2048
Mz 48,24,192
Rt 8,22,8192
Cv 225,226,227
Mz 56,24,196
Rt 10,22,8704
Rb 56,29,0
buffer_load_dwordx4 a[64:67], v2, s[20:23], s51 offen offset:0 sc0 sc1
Mz 60,28,196
Rt 12,23,8192
Cv 226,228,229
Mz 52,28,192
Rt 14,23,8704
Rb 60,29,2048
s_waitcnt lgkmcnt(12)
Mv 32,32,200
Rt 16,20,12288
Cv 227,230,231
Mv 40,32,204
Rt 18,20,12800
Cv 232,232,233
Mv 44,36,204
Rt 20,21,12288
buffer_load_dwordx4 a[68:71], v2, s[20:23], s51 offen offset:1024 sc0 sc1
Mv 36,36,200
Rt 22,21,12800
Cv 233,234,235
Mv 48,40,200
Rt 24,22,12288
buffer_store_dwordx4 v[104:107], v2, s[20:23], s46 offen offset:1024 sc0 sc1
Mv 56,40,204
Rt 26,22,12800
Cv 234,236,237
Mv 60,44,204
Rt 28,23,12288
Cv 235,238,239
Mv 52,44,200
Rt 30,23,12800
buffer_load_dwordx4 a[72:75], v2, s[20:23], s51 offen offset:2048 sc0 sc1
s_waitcnt lgkmcnt(9)
Mv 32,0,208
Rt 32,20,16384
Cv 240,240,241
Mv 40,0,212
Rt 34,20,16896
Cv 241,242,243
Mv 44,4,212
Rt 36,21,16384
Cv 242,244,245
Mv 36,4,208
Rt 38,21,16896
Cv 243,246,247
)" R"(
Mv 48,8,208
Rt 40,22,16384
buffer_load_dwordx4 a[76:79], v2, s[20:23], s51 offen offset:3072 sc0 sc1
Mv 56,8,212
Rt 42,22,16896
Cv 248,248,249
Cv 249,250,251
Mv 60,12,212
Rt 44,23,16384
buffer_store_dwordx4 v[112:115], v2, s[20:23], s46 offen offset:2048 sc0 sc1
Mv 52,12,208
Rt 46,23,16896
s_waitcnt lgkmcnt(8)
Mv 32,16,216
Rt 0,20,20480
Cv 250,252,253
Mv 40,16,220
Rt 2,20,20992
buffer_load_dwordx4 a[80:83], v3, s[20:23], s51 offen offset:0 sc0 sc1
Mv 44,20,220
Rt 4,21,20480
Cv 251,254,255
v_accvgpr_read_b32 v228, a224
Mv 36,20,216
Rt 6,21,20992
v_accvgpr_read_b32 v24, a225
Mv 48,24,216
Rt 8,22,20480
Cv 228,228,24
Mv 56,24,220
Rt 10,22,20992
v_accvgpr_read_b32 v229, a226
Mv 60,28,220
Rt 12,23,20480
buffer_load_dwordx4 a[84:87], v3, s[20:23], s51 offen offset:1024 sc0 sc1
Mv 52,28,216
Rt 14,23,20992
v_accvgpr_read_b32 v25, a227
s_waitcnt lgkmcnt(8)
Mv 32,32,128
Rt 16,20,24576
buffer_store_dwordx4 v[120:123], v2, s[20:23], s46 offen offset:3072 sc0 sc1
Mv 40,32,132
Rt 18,20,25088
Cv 229,229,25
Mv 44,36,132
Rt 20,21,24576
v_accvgpr_read_b32 v230, a228
Mv 36,36,128
Rt 22,21,25088
buffer_load_dwordx4 a[88:91], v3, s[20:23], s51 offen offset:2048 sc0 sc1
Mv 48,40,128
Rt 24,22,24576
v_accvgpr_read_b32 v24, a229
Mv 56,40,132
Rt 26,22,25088
Cv 230,230,24
v_accvgpr_read_b32 v231, a230
Mv 60,44,132
Rt 28,23,24576
v_accvgpr_read_b32 v25, a231
Mv 52,44,128
Rt 30,23,25088
s_waitcnt lgkmcnt(8)
)" R"(
Mv 32,0,136
Rt 32,12,0
buffer_load_dwordx4 a[92:95], v3, s[20:23], s51 offen offset:3072 sc0 sc1
Mv 40,0,140
Rt 34,12,512
Cv 231,231,25
Mv 44,4,140
Rt 36,13,0
buffer_store_dwordx4 v[128:131], v3, s[20:23], s46 offen offset:0 sc0 sc1
Mv 36,4,136
Rt 38,13,512
v_accvgpr_read_b32 v236, a232
Mv 48,8,136
Rt 40,14,0
v_accvgpr_read_b32 v24, a233
Mv 56,8,140
Rt 42,14,512
Cv 236,236,24
Mv 60,12,140
Rt 44,15,0
v_accvgpr_read_b32 v237, a234
Mv 52,12,136
Rt 46,15,512
s_waitcnt vmcnt(13) lgkmcnt(8)
s_barrier
buffer_store_dword v6, v7, s[20:23], s49 offen offset:0 sc0 sc1
Mv 32,16,144
Rt 0,12,4096
v_accvgpr_read_b32 v25, a235
Mv 40,16,148
Rt 2,12,4608
s_add_u32 s92, s42, 0x100
Mv 44,20,148
Rt 4,13,4096
s_add_u32 m0, s41, 98304
Mv 36,20,144
Rt 6,13,4608
buffer_load_dwordx4 v8, s[16:19], s92 offen offset:0 lds
Mv 48,24,144
Rt 8,14,4096
Cv 237,237,25
Mv 56,24,148
Rt 10,14,4608
v_accvgpr_read_b32 v238, a236
v_accvgpr_read_b32 v24, a237
Mv 60,28,148
Rt 12,15,4096
Cv 238,238,24
Mv 52,28,144
Rt 14,15,4608
buffer_load_dwordx4 v9, s[16:19], s92 offen offset:1024 lds
s_waitcnt lgkmcnt(8)
Mv 32,32,152
Rt 16,12,8192
v_accvgpr_read_b32 v239, a238
Mv 40,32,156
Rt 18,12,8704
buffer_store_dwordx4 v[136:139], v3, s[20:23], s46 offen offset:1024 sc0 sc1
Mv 44,36,156
Rt 20,13,8192
v_accvgpr_read_b32 v25, a239
Cv 239,239,25
Mv 36,36,152
Rt 22,13,8704
buffer_load_dwordx4 v10, s[16:19], s92 offen offset:2048 lds
)" R"(
Mv 48,40,152
Rt 24,14,8192
v_accvgpr_read_b32 v244, a240
Mv 56,40,156
Rt 26,14,8704
v_accvgpr_read_b32 v24, a241
Mv 60,44,156
Rt 28,15,8192
Cv 244,244,24
Mv 52,44,152
Rt 30,15,8704
buffer_load_dwordx4 v11, s[16:19], s92 offen offset:3072 lds
s_waitcnt lgkmcnt(8)
Mv 32,0,160
Rt 32,12,12288
v_accvgpr_read_b32 v245, a242
Mv 40,0,164
Rt 34,12,12800
s_add_u32 s92, s42, 0x40100
Mv 44,4,164
Rt 36,13,12288
s_add_u32 m0, s41, 102400
Mv 36,4,160
Rt 38,13,12800
buffer_load_dwordx4 v8, s[16:19], s92 offen offset:0 lds
Mv 48,8,160
Rt 40,14,12288
v_accvgpr_read_b32 v25, a243
Mv 56,8,164
Rt 42,14,12800
Cv 245,245,25
v_accvgpr_read_b32 v246, a244
Mv 60,12,164
Rt 44,15,12288
v_accvgpr_read_b32 v24, a245
Mv 52,12,160
Rt 46,15,12800
buffer_load_dwordx4 v9, s[16:19], s92 offen offset:1024 lds
s_waitcnt lgkmcnt(8)
Mv 32,16,168
Rt 0,12,16384
Cv 246,246,24
Mv 40,16,172
Rt 2,12,16896
buffer_store_dwordx4 v[144:147], v3, s[20:23], s46 offen offset:2048 sc0 sc1
Mv 44,20,172
Rt 4,13,16384
v_accvgpr_read_b32 v247, a246
v_accvgpr_read_b32 v25, a247
Mv 36,20,168
Rt 6,13,16896
buffer_load_dwordx4 v10, s[16:19], s92 offen offset:2048 lds
Mv 48,24,168
Rt 8,14,16384
Cv 247,247,25
Mv 56,24,172
Rt 10,14,16896
s_add_u32 s92, s42, 0x1c0100
Mv 60,28,172
Rt 12,15,16384
s_add_u32 m0, s41, 131072
Mv 52,28,168
Rt 14,15,16896
buffer_load_dwordx4 v8, s[16:19], s92 offen offset:0 lds
)" R"(
s_waitcnt lgkmcnt(8)
Mv 32,32,176
Rt 16,12,20480
v_accvgpr_read_b32 v252, a248
Mv 40,32,180
Rt 18,12,20992
v_accvgpr_read_b32 v24, a249
Mv 44,36,180
Rt 20,13,20480
Cv 252,252,24
Mv 36,36,176
Rt 22,13,20992
buffer_load_dwordx4 v9, s[16:19], s92 offen offset:1024 lds
Mv 48,40,176
Rt 24,14,20480
v_accvgpr_read_b32 v253, a250
Mv 56,40,180
Rt 26,14,20992
v_accvgpr_read_b32 v25, a251
Cv 253,253,25
Mv 60,44,180
Rt 28,15,20480
v_accvgpr_read_b32 v254, a252
Mv 52,44,176
Rt 30,15,20992
buffer_load_dwordx4 v10, s[16:19], s92 offen offset:2048 lds
s_waitcnt lgkmcnt(8)
Mv 32,0,184
Rt 32,12,24576
v_accvgpr_read_b32 v24, a253
Mv 40,0,188
Rt 34,12,25088
buffer_store_dwordx4 v[152:155], v3, s[20:23], s46 offen offset:3072 sc0 sc1
Mv 44,4,188
Rt 36,13,24576
Cv 254,254,24
v_accvgpr_read_b32 v255, a254
Mv 36,4,184
Rt 38,13,25088
buffer_load_dwordx4 v11, s[16:19], s92 offen offset:3072 lds
Mv 48,8,184
Rt 40,14,24576
v_accvgpr_read_b32 v25, a255
Mv 56,8,188
Rt 42,14,25088
s_add_u32 s92, s42, 0x200100
Mv 60,12,188
Rt 44,15,24576
s_add_u32 m0, s41, 135168
Mv 52,12,184
Rt 46,15,25088
buffer_load_dwordx4 v8, s[16:19], s92 offen offset:0 lds
s_waitcnt lgkmcnt(8)
Mv 32,16,48
Rt 0,16,0
Cv 255,255,25
Mv 40,16,52
Rt 2,16,512
Cv 160,160,161
Mv 44,20,52
Rt 4,17,0
Cv 161,162,163
Mv 36,20,48
Rt 6,17,512
)" R"(
buffer_load_dwordx4 v9, s[16:19], s92 offen offset:1024 lds
Mv 48,24,48
Rt 8,18,0
Cv 162,164,165
Mv 56,24,52
Rt 10,18,512
Cv 163,166,167
Cv 168,168,169
Mv 60,28,52
Rt 12,19,0
Cv 169,170,171
Mv 52,28,48
Rt 14,19,512
buffer_load_dwordx4 v10, s[16:19], s92 offen offset:2048 lds
s_waitcnt lgkmcnt(8)
s_barrier
Mv 32,32,56
Rt 16,16,4096
Cv 170,172,173
Mv 40,32,60
Rt 18,16,4608
buffer_store_dwordx4 v[160:163], v2, s[20:23], s47 offen offset:0 sc0 sc1
Mv 44,36,60
Rt 20,17,4096
Cv 171,174,175
Cv 176,176,177
Mv 36,36,56
Rt 22,17,4608
s_add_u32 s92, s42, 0x180
Mv 48,40,56
Rt 24,18,4096
s_add_u32 m0, s41, 0
Mv 56,40,60
Rt 26,18,4608
buffer_load_dwordx4 v8, s[16:19], s92 offen offset:0 lds
Mv 60,44,60
Rt 28,19,4096
Cv 177,178,179
Mv 52,44,56
Rt 30,19,4608
Cv 178,180,181
s_waitcnt lgkmcnt(8)
Mz 64,0,192
Rt 32,16,8192
Cv 179,182,183
Mz 72,0,196
Rt 34,16,8704
s_add_u32 s51, s51, 0x10000
buffer_load_dwordx4 a[96:99], v2, s[20:23], s51 offen offset:0 sc0 sc1
Mz 76,4,196
Rt 36,17,8192
Cv 184,184,185
Mz 68,4,192
Rt 38,17,8704
Cv 185,186,187
Mz 80,8,192
Rt 40,18,8192
Cv 186,188,189
Mz 88,8,196
Rt 42,18,8704
buffer_load_dwordx4 v9, s[16:19], s92 offen offset:1024 lds
Mz 92,12,196
Rt 44,19,8192
Cv 187,190,191
v_mul_f32_e32 v32, s54, v32
)" R"(
Mz 84,12,192
Rt 46,19,8704
s_waitcnt lgkmcnt(8)
.LzH33:
s_cbranch_execz .LzH41
Mv 64,16,200
Rt 0,16,12288
Cv 192,192,193
Mv 72,16,204
Rt 2,16,12800
buffer_store_dwordx4 v[168:171], v2, s[20:23], s47 offen offset:1024 sc0 sc1
Mv 76,20,204
Rt 4,17,12288
Cv 193,194,195
Cv 194,196,197
Mv 68,20,200
Rt 6,17,12800
Cv 195,198,199
v_mul_f32_e32 v33, s54, v33
Mv 80,24,200
Rt 8,18,12288
Cv 200,200,201
Mv 88,24,204
Rt 10,18,12800
buffer_load_dwordx4 v10, s[16:19], s92 offen offset:2048 lds
Mv 92,28,204
Rt 12,19,12288
Cv 201,202,203
Cv 32,32,33
Mv 84,28,200
Rt 14,19,12800
Cv 202,204,205
s_waitcnt lgkmcnt(8)
Mv 64,32,208
Rt 16,16,16384
Cv 203,206,207
Mv 72,32,212
Rt 18,16,16896
buffer_load_dwordx4 a[100:103], v2, s[20:23], s51 offen offset:1024 sc0 sc1
Mv 76,36,212
Rt 20,17,16384
Cv 208,208,209
Mv 68,36,208
Rt 22,17,16896
Cv 209,210,211
v_accvgpr_write_b32 a224, v32
Mv 80,40,208
Rt 24,18,16384
Cv 210,212,213
Mv 88,40,212
Rt 26,18,16896
buffer_load_dwordx4 v11, s[16:19], s92 offen offset:3072 lds
Mv 92,44,212
Rt 28,19,16384
Cv 211,214,215
v_mul_f32_e32 v34, s54, v34
Mv 84,44,208
Rt 30,19,16896
s_waitcnt lgkmcnt(8)
Mv 64,0,216
Rt 32,16,20480
Cv 216,216,217
Mv 72,0,220
Rt 34,16,20992
)" R"(
buffer_store_dwordx4 v[176:179], v2, s[20:23], s47 offen offset:2048 sc0 sc1
Mv 76,4,220
Rt 36,17,20480
Cv 217,218,219
Cv 218,220,221
Mv 68,4,216
Rt 38,17,20992
s_add_u32 s92, s42, 0x40180
v_mul_f32_e32 v35, s54, v35
Mv 80,8,216
Rt 40,18,20480
s_add_u32 m0, s41, 4096
Mv 88,8,220
Rt 42,18,20992
buffer_load_dwordx4 v8, s[16:19], s92 offen offset:0 lds
Mv 92,12,220
Rt 44,19,20480
Cv 219,222,223
Cv 34,34,35
Mv 84,12,216
Rt 46,19,20992
s_waitcnt lgkmcnt(8)
Mv 64,16,128
Rt 0,16,24576
v_accvgpr_write_b32 a225, v34
Mv 72,16,132
Rt 2,16,25088
buffer_load_dwordx4 a[104:107], v2, s[20:23], s51 offen offset:2048 sc0 sc1
Mv 76,20,132
Rt 4,17,24576
v_mul_f32_e32 v36, s54, v36
Mv 68,20,128
Rt 6,17,25088
v_mul_f32_e32 v37, s54, v37
Cv 36,36,37
Mv 80,24,128
Rt 8,18,24576
v_accvgpr_write_b32 a226, v36
Mv 88,24,132
Rt 10,18,25088
buffer_load_dwordx4 v9, s[16:19], s92 offen offset:1024 lds
Mv 92,28,132
Rt 12,19,24576
v_mul_f32_e32 v38, s54, v38
v_mul_f32_e32 v39, s54, v39
Mv 84,28,128
Rt 14,19,25088
s_waitcnt lgkmcnt(8)
Mv 64,32,136
Rt 16,12,32768
buffer_store_dwordx4 v[184:187], v2, s[20:23], s47 offen offset:3072 sc0 sc1
Mv 72,32,140
Rt 18,12,33280
Cv 38,38,39
v_accvgpr_write_b32 a227, v38
Mv 76,36,140
Rt 20,13,32768
v_mul_f32_e32 v40, s54, v40
Mv 68,36,136
Rt 22,13,33280
buffer_load_dwordx4 a[108:111], v2, s[20:23], s51 offen offset:3072 sc0 sc1
Mv 80,40,136
Rt 24,14,32768
v_mul_f32_e32 v41, s54, v41
)" R"(
Mv 88,40,140
Rt 26,14,33280
buffer_load_dwordx4 v10, s[16:19], s92 offen offset:2048 lds
Mv 92,44,140
Rt 28,15,32768
Cv 40,40,41
v_accvgpr_write_b32 a228, v40
Mv 84,44,136
Rt 30,15,33280
s_waitcnt vmcnt(19) lgkmcnt(8)
s_barrier
buffer_store_dword v6, v7, s[20:23], s49 offen offset:1024 sc0 sc1
Mv 64,0,144
Rt 32,12,36864
buffer_load_dword v4, v5, s[20:23], s50 offen offset:0 sc0 sc1
Mv 72,0,148
Rt 34,12,37376
v_mul_f32_e32 v42, s54, v42
v_mul_f32_e32 v43, s54, v43
Mv 76,4,148
Rt 36,13,36864
Cv 42,42,43
Mv 68,4,144
Rt 38,13,37376
s_add_u32 s92, s42, 0x1c0180
v_accvgpr_write_b32 a229, v42
Mv 80,8,144
Rt 40,14,36864
s_add_u32 m0, s41, 65536
Mv 88,8,148
Rt 42,14,37376
buffer_load_dwordx4 v8, s[16:19], s92 offen offset:0 lds
Mv 92,12,148
Rt 44,15,36864
v_mul_f32_e32 v44, s54, v44
v_mul_f32_e32 v45, s54, v45
Mv 84,12,144
Rt 46,15,37376
s_waitcnt lgkmcnt(8)
Mv 64,16,152
Rt 0,12,40960
Cv 44,44,45
Mv 72,16,156
Rt 2,12,41472
buffer_load_dwordx4 a[112:115], v3, s[20:23], s51 offen offset:0 sc0 sc1
Mv 76,20,156
Rt 4,13,40960
v_accvgpr_write_b32 a230, v44
Mv 68,20,152
Rt 6,13,41472
v_mul_f32_e32 v46, s54, v46
v_mul_f32_e32 v47, s54, v47
Mv 80,24,152
Rt 8,14,40960
Cv 46,46,47
Mv 88,24,156
Rt 10,14,41472
buffer_load_dwordx4 v9, s[16:19], s92 offen offset:1024 lds
Mv 92,28,156
Rt 12,15,40960
v_accvgpr_write_b32 a231, v46
v_mul_f32_e32 v48, s54, v48
Mv 84,28,152
Rt 14,15,41472
s_waitcnt lgkmcnt(8)
)" R"(
Mv 64,32,160
Rt 16,12,45056
v_mul_f32_e32 v49, s54, v49
Mv 72,32,164
Rt 18,12,45568
buffer_store_dwordx4 v[192:195], v3, s[20:23], s47 offen offset:0 sc0 sc1
Mv 76,36,164
Rt 20,13,45056
Cv 48,48,49
Mv 68,36,160
Rt 22,13,45568
v_accvgpr_write_b32 a232, v48
v_mul_f32_e32 v50, s54, v50
Mv 80,40,160
Rt 24,14,45056
v_mul_f32_e32 v51, s54, v51
Mv 88,40,164
Rt 26,14,45568
buffer_load_dwordx4 v10, s[16:19], s92 offen offset:2048 lds
Mv 92,44,164
Rt 28,15,45056
Cv 50,50,51
v_accvgpr_write_b32 a233, v50
Mv 84,44,160
Rt 30,15,45568
s_waitcnt lgkmcnt(8)
Mv 64,0,168
Rt 32,12,49152
v_mul_f32_e32 v52, s54, v52
Mv 72,0,172
Rt 34,12,49664
buffer_load_dwordx4 a[116:119], v3, s[20:23], s51 offen offset:1024 sc0 sc1
Mv 76,4,172
Rt 36,13,49152
v_mul_f32_e32 v53, s54, v53
Mv 68,4,168
Rt 38,13,49664
Cv 52,52,53
v_accvgpr_write_b32 a234, v52
Mv 80,8,168
Rt 40,14,49152
v_mul_f32_e32 v54, s54, v54
Mv 88,8,172
Rt 42,14,49664
buffer_load_dwordx4 v11, s[16:19], s92 offen offset:3072 lds
Mv 92,12,172
Rt 44,15,49152
v_mul_f32_e32 v55, s54, v55
Cv 54,54,55
Mv 84,12,168
Rt 46,15,49664
s_waitcnt lgkmcnt(8)
Mv 64,16,176
Rt 0,12,53248
v_accvgpr_write_b32 a235, v54
Mv 72,16,180
Rt 2,12,53760
buffer_store_dwordx4 v[200:203], v3, s[20:23], s47 offen offset:1024 sc0 sc1
Mv 76,20,180
Rt 4,13,53248
v_mul_f32_e32 v56, s54, v56
Mv 68,20,176
Rt 6,13,53760
s_add_u32 s92, s42, 0x200180
)" R"(
v_mul_f32_e32 v57, s54, v57
Mv 80,24,176
Rt 8,14,53248
s_add_u32 m0, s41, 69632
Mv 88,24,180
Rt 10,14,53760
buffer_load_dwordx4 v8, s[16:19], s92 offen offset:0 lds
Mv 92,28,180
Rt 12,15,53248
Cv 56,56,57
v_accvgpr_write_b32 a236, v56
Mv 84,28,176
Rt 14,15,53760
s_waitcnt lgkmcnt(8)
Mv 64,32,184
Rt 16,12,57344
v_mul_f32_e32 v58, s54, v58
Mv 72,32,188
Rt 18,12,57856
buffer_load_dwordx4 a[120:123], v3, s[20:23], s51 offen offset:2048 sc0 sc1
Mv 76,36,188
Rt 20,13,57344
v_mul_f32_e32 v59, s54, v59
Mv 68,36,184
Rt 22,13,57856
Cv 58,58,59
v_accvgpr_write_b32 a237, v58
Mv 80,40,184
Rt 24,14,57344
v_mul_f32_e32 v60, s54, v60
Mv 88,40,188
Rt 26,14,57856
buffer_load_dwordx4 v9, s[16:19], s92 offen offset:1024 lds
Mv 92,44,188
Rt 28,15,57344
v_mul_f32_e32 v61, s54, v61
Cv 60,60,61
Mv 84,44,184
Rt 30,15,57856
s_waitcnt lgkmcnt(8)
Mv 64,0,48
Rt 32,16,32768
buffer_store_dwordx4 v[208:211], v3, s[20:23], s47 offen offset:2048 sc0 sc1
Mv 72,0,52
Rt 34,16,33280
v_accvgpr_write_b32 a238, v60
v_mul_f32_e32 v62, s54, v62
Mv 76,4,52
Rt 36,17,32768
v_mul_f32_e32 v63, s54, v63
Mv 68,4,48
Rt 38,17,33280
buffer_load_dwordx4 a[124:127], v3, s[20:23], s51 offen offset:3072 sc0 sc1
Mv 80,8,48
Rt 40,18,32768
Cv 62,62,63
Mv 88,8,52
Rt 42,18,33280
buffer_load_dwordx4 v10, s[16:19], s92 offen offset:2048 lds
Mv 92,12,52
Rt 44,19,32768
v_accvgpr_write_b32 a239, v62
Mv 84,12,48
Rt 46,19,33280
)" R"(
s_waitcnt vmcnt(14) lgkmcnt(8)
s_barrier
Mv 64,16,56
Rt 0,16,36864
v_cmp_eq_u32_e32 vcc, s3, v4
s_andn2_b64 s[28:29], -1, vcc
s_cbranch_scc1 .Lzsp5
.Lzok5:
Mv 72,16,60
Rt 2,16,37376
s_add_u32 s40, s55, 0x10000
Mv 76,20,60
Rt 4,17,36864
Mv 68,20,56
Rt 6,17,37376
Mv 80,24,56
Rt 8,18,36864
s_add_u32 m0, s36, 32768
Mv 88,24,60
Rt 10,18,37376
buffer_load_dwordx4 v2, s[20:23], s40 offen offset:0 sc1 lds
Mv 92,28,60
Rt 12,19,36864
Mv 84,28,56
Rt 14,19,37376
s_waitcnt lgkmcnt(8)
Mz 32,32,192
Rt 16,16,40960
buffer_store_dwordx4 v[216:219], v3, s[20:23], s47 offen offset:3072 sc0 sc1
Mz 40,32,196
Rt 18,16,41472
Mz 44,36,196
Rt 20,17,40960
Mz 36,36,192
Rt 22,17,41472
buffer_load_dwordx4 v2, s[20:23], s40 offen offset:1024 sc1 lds
Mz 48,40,192
Rt 24,18,40960
v_mul_f32_e32 v64, s54, v64
Mz 56,40,196
Rt 26,18,41472
v_mul_f32_e32 v65, s54, v65
Mz 60,44,196
Rt 28,19,40960
Cv 64,64,65
Mz 52,44,192
Rt 30,19,41472
s_waitcnt lgkmcnt(8)
Mv 32,0,200
Rt 32,16,45056
v_accvgpr_write_b32 a240, v64
Mv 40,0,204
Rt 34,16,45568
buffer_load_dwordx4 v2, s[20:23], s40 offen offset:2048 sc1 lds
Mv 44,4,204
Rt 36,17,45056
v_mul_f32_e32 v66, s54, v66
Mv 36,4,200
Rt 38,17,45568
v_mul_f32_e32 v67, s54, v67
Mv 48,8,200
Rt 40,18,45056
buffer_store_dwordx4 v[224:227], v2, s[20:23], s48 offen offset:0 sc0 sc1
Mv 56,8,204
Rt 42,18,45568
)" R"(
Cv 66,66,67
Mv 60,12,204
Rt 44,19,45056
v_accvgpr_write_b32 a241, v66
Mv 52,12,200
Rt 46,19,45568
buffer_load_dwordx4 v2, s[20:23], s40 offen offset:3072 sc1 lds
s_waitcnt lgkmcnt(8)
Mv 32,16,208
Rt 0,16,49152
v_mul_f32_e32 v68, s54, v68
Mv 40,16,212
Rt 2,16,49664
v_mul_f32_e32 v69, s54, v69
Mv 44,20,212
Rt 4,17,49152
Cv 68,68,69
Mv 36,20,208
Rt 6,17,49664
v_accvgpr_write_b32 a242, v68
Mv 48,24,208
Rt 8,18,49152
s_add_u32 m0, s36, 36864
Mv 56,24,212
Rt 10,18,49664
buffer_load_dwordx4 v3, s[20:23], s40 offen offset:0 sc1 lds
Mv 60,28,212
Rt 12,19,49152
v_mul_f32_e32 v70, s54, v70
Mv 52,28,208
Rt 14,19,49664
s_waitcnt lgkmcnt(8)
Mv 32,32,216
Rt 16,16,53248
buffer_store_dwordx4 v[232:235], v2, s[20:23], s48 offen offset:1024 sc0 sc1
Mv 40,32,220
Rt 18,16,53760
v_mul_f32_e32 v71, s54, v71
Mv 44,36,220
Rt 20,17,53248
Cv 70,70,71
Mv 36,36,216
Rt 22,17,53760
buffer_load_dwordx4 v3, s[20:23], s40 offen offset:1024 sc1 lds
Mv 48,40,216
Rt 24,18,53248
v_accvgpr_write_b32 a243, v70
Mv 56,40,220
Rt 26,18,53760
v_mul_f32_e32 v72, s54, v72
Mv 60,44,220
Rt 28,19,53248
v_mul_f32_e32 v73, s54, v73
Mv 52,44,216
Rt 30,19,53760
s_waitcnt lgkmcnt(8)
Mv 32,0,128
Rt 32,16,57344
Cv 72,72,73
Mv 40,0,132
Rt 34,16,57856
buffer_load_dwordx4 v3, s[20:23], s40 offen offset:2048 sc1 lds
Mv 44,4,132
Rt 36,17,57344
)" R"(
v_accvgpr_write_b32 a244, v72
Mv 36,4,128
Rt 38,17,57856
v_mul_f32_e32 v74, s54, v74
Mv 48,8,128
Rt 40,18,57344
buffer_store_dwordx4 v[240:243], v2, s[20:23], s48 offen offset:2048 sc0 sc1
Mv 56,8,132
Rt 42,18,57856
v_mul_f32_e32 v75, s54, v75
Mv 60,12,132
Rt 44,19,57344
Cv 74,74,75
Mv 52,12,128
Rt 46,19,57856
buffer_load_dwordx4 v3, s[20:23], s40 offen offset:3072 sc1 lds
s_waitcnt lgkmcnt(8)
Mv 32,16,136
Rt 0,20,0
v_accvgpr_write_b32 a245, v74
Mv 40,16,140
Rt 2,20,512
v_mul_f32_e32 v76, s54, v76
Mv 44,20,140
Rt 4,21,0
v_mul_f32_e32 v77, s54, v77
Mv 36,20,136
Rt 6,21,512
Cv 76,76,77
Mv 48,24,136
Rt 8,22,0
v_accvgpr_write_b32 a246, v76
Mv 56,24,140
Rt 10,22,512
v_mul_f32_e32 v78, s54, v78
Mv 60,28,140
Rt 12,23,0
v_mul_f32_e32 v79, s54, v79
Mv 52,28,136
Rt 14,23,512
s_waitcnt vmcnt(10) lgkmcnt(8)
s_barrier
buffer_store_dword v6, v7, s[20:23], s49 offen offset:2048 sc0 sc1
Mv 32,32,144
Rt 16,20,4096
s_mov_b32 s83, 0xc108000
buffer_load_dwordx2 v[24:25], v7, s[20:23], s83 offen sc0 sc1
Mv 40,32,148
Rt 18,20,4608
Cv 78,78,79
Mv 44,36,148
Rt 20,21,4096
s_add_u32 m0, s36, 98304
Mv 36,36,144
Rt 22,21,4608
buffer_load_dwordx4 v2, s[20:23], s55 offen offset:0 sc1 lds
Mv 48,40,144
Rt 24,22,4096
v_accvgpr_write_b32 a247, v78
Mv 56,40,148
Rt 26,22,4608
v_mul_f32_e32 v80, s54, v80
Mv 60,44,148
Rt 28,23,4096
buffer_store_dwordx4 v[248:251], v2, s[20:23], s48 offen offset:3072 sc0 sc1
)" R"(
Mv 52,44,144
Rt 30,23,4608
s_waitcnt lgkmcnt(8)
Mv 32,0,152
Rt 32,20,8192
v_mul_f32_e32 v81, s54, v81
Mv 40,0,156
Rt 34,20,8704
buffer_load_dwordx4 v2, s[20:23], s55 offen offset:1024 sc1 lds
Mv 44,4,156
Rt 36,21,8192
Cv 80,80,81
Mv 36,4,152
Rt 38,21,8704
v_accvgpr_write_b32 a248, v80
Mv 48,8,152
Rt 40,22,8192
v_mul_f32_e32 v82, s54, v82
Mv 56,8,156
Rt 42,22,8704
v_mul_f32_e32 v83, s54, v83
Mv 60,12,156
Rt 44,23,8192
Cv 82,82,83
Mv 52,12,152
Rt 46,23,8704
buffer_load_dwordx4 v2, s[20:23], s55 offen offset:2048 sc1 lds
s_waitcnt lgkmcnt(8)
Mv 32,16,160
Rt 0,20,12288
v_accvgpr_write_b32 a249, v82
Mv 40,16,164
Rt 2,20,12800
v_mul_f32_e32 v84, s54, v84
Mv 44,20,164
Rt 4,21,12288
buffer_store_dwordx4 v[228:231], v3, s[20:23], s48 offen offset:0 sc0 sc1
Mv 36,20,160
Rt 6,21,12800
v_mul_f32_e32 v85, s54, v85
Mv 48,24,160
Rt 8,22,12288
Cv 84,84,85
.LzH34:
s_cbranch_execz .LzH42
Mv 56,24,164
Rt 10,22,12800
buffer_load_dwordx4 v2, s[20:23], s55 offen offset:3072 sc1 lds
Mv 60,28,164
Rt 12,23,12288
v_accvgpr_write_b32 a250, v84
Mv 52,28,160
Rt 14,23,12800
s_waitcnt lgkmcnt(8)
Mv 32,32,168
Rt 16,20,16384
v_mul_f32_e32 v86, s54, v86
Mv 40,32,172
Rt 18,20,16896
v_mul_f32_e32 v87, s54, v87
Mv 44,36,172
Rt 20,21,16384
s_add_u32 m0, s36, 102400
Mv 36,36,168
)" R"(
Rt 22,21,16896
buffer_load_dwordx4 v3, s[20:23], s55 offen offset:0 sc1 lds
Mv 48,40,168
Rt 24,22,16384
Cv 86,86,87
Mv 56,40,172
Rt 26,22,16896
v_accvgpr_write_b32 a251, v86
Mv 60,44,172
Rt 28,23,16384
buffer_store_dwordx4 v[236:239], v3, s[20:23], s48 offen offset:1024 sc0 sc1
Mv 52,44,168
Rt 30,23,16896
s_waitcnt lgkmcnt(8)
Mv 32,0,176
Rt 32,20,20480
v_mul_f32_e32 v88, s54, v88
Mv 40,0,180
Rt 34,20,20992
buffer_load_dwordx4 v3, s[20:23], s55 offen offset:1024 sc1 lds
Mv 44,4,180
Rt 36,21,20480
v_mul_f32_e32 v89, s54, v89
Mv 36,4,176
Rt 38,21,20992
Cv 88,88,89
Mv 48,8,176
Rt 40,22,20480
s_add_u32 s50, s50, 1024
Mv 56,8,180
Rt 42,22,20992
s_min_u32 s50, s50, s60
Mv 60,12,180
Rt 44,23,20480
v_accvgpr_write_b32 a252, v88
Mv 52,12,176
Rt 46,23,20992
buffer_load_dwordx4 v3, s[20:23], s55 offen offset:2048 sc1 lds
s_waitcnt lgkmcnt(8)
Mv 32,16,184
Rt 0,20,24576
v_mul_f32_e32 v90, s54, v90
Mv 40,16,188
Rt 2,20,25088
v_mul_f32_e32 v91, s54, v91
Mv 44,20,188
Rt 4,21,24576
buffer_store_dwordx4 v[244:247], v3, s[20:23], s48 offen offset:2048 sc0 sc1
Mv 36,20,184
Rt 6,21,25088
Cv 90,90,91
Mv 48,24,184
Rt 8,22,24576
v_accvgpr_write_b32 a253, v90
Mv 56,24,188
Rt 10,22,25088
buffer_load_dwordx4 v3, s[20:23], s55 offen offset:3072 sc1 lds
Mv 60,28,188
Rt 12,23,24576
v_mul_f32_e32 v92, s54, v92
Mv 52,28,184
Rt 14,23,25088
s_waitcnt lgkmcnt(8)
Mv 32,32,48
)" R"(
Rt 16,12,0
v_mul_f32_e32 v93, s54, v93
Mv 40,32,52
Rt 18,12,512
Cv 92,92,93
Mv 44,36,52
Rt 20,13,0
v_accvgpr_write_b32 a254, v92
Mv 36,36,48
Rt 22,13,512
v_mul_f32_e32 v94, s54, v94
Mv 48,40,48
Rt 24,14,0
buffer_store_dwordx4 v[252:255], v3, s[20:23], s48 offen offset:3072 sc0 sc1
Mv 56,40,52
Rt 26,14,512
v_mul_f32_e32 v95, s54, v95
Mv 60,44,52
Rt 28,15,0
Cv 94,94,95
Mv 52,44,48
Rt 30,15,512
s_waitcnt vmcnt(13) lgkmcnt(8)
s_barrier
v_cmp_eq_u32_e32 vcc, s3, v25
s_andn2_b64 s[28:29], 1, vcc
s_cbranch_scc1 .Lzsp7
.Lzok7:
Mv 32,0,56
Rt 32,12,4096
buffer_load_dword v4, v5, s[20:23], s50 offen offset:0 sc0 sc1
Mv 40,0,60
Rt 34,12,4608
v_accvgpr_write_b32 a255, v94
Mv 44,4,60
Rt 36,13,4096
s_add_u32 m0, s36, 131072
Mv 36,4,56
Rt 38,13,4608
buffer_load_dwordx4 v2, s[20:23], s56 offen offset:0 sc1 lds
Mv 48,8,56
Rt 40,14,4096
Mv 56,8,60
Rt 42,14,4608
Mv 60,12,60
Rt 44,15,4096
Mv 52,12,56
Rt 46,15,4608
s_waitcnt lgkmcnt(8)
Mz 96,16,192
Rt 0,12,8192
Mz 104,16,196
Rt 2,12,8704
buffer_load_dwordx4 v2, s[20:23], s56 offen offset:1024 sc1 lds
Mz 108,20,196
Rt 4,13,8192
Mz 100,20,192
Rt 6,13,8704
Mz 112,24,192
Rt 8,14,8192
v_mul_f32_e32 v32, s54, v32
Mz 120,24,196
Rt 10,14,8704
v_mul_f32_e32 v33, s54, v33
Mz 124,28,196
)" R"(
Rt 12,15,8192
Cv 32,32,33
Mz 116,28,192
Rt 14,15,8704
buffer_load_dwordx4 v2, s[20:23], s56 offen offset:2048 sc1 lds
s_waitcnt lgkmcnt(8)
Mv 96,32,200
Rt 16,12,12288
v_mul_f32_e32 v34, s54, v34
Mv 104,32,204
Rt 18,12,12800
v_accvgpr_write_b32 a192, v32
Mv 108,36,204
Rt 20,13,12288
v_mul_f32_e32 v35, s54, v35
Mv 100,36,200
Rt 22,13,12800
Cv 34,34,35
v_accvgpr_write_b32 a193, v34
Mv 112,40,200
Rt 24,14,12288
v_mul_f32_e32 v36, s54, v36
Mv 120,40,204
Rt 26,14,12800
buffer_load_dwordx4 v2, s[20:23], s56 offen offset:3072 sc1 lds
Mv 124,44,204
Rt 28,15,12288
v_mul_f32_e32 v37, s54, v37
Mv 116,44,200
Rt 30,15,12800
s_waitcnt lgkmcnt(8)
Mv 96,0,208
Rt 32,12,16384
Cv 36,36,37
Mv 104,0,212
Rt 34,12,16896
v_accvgpr_write_b32 a194, v36
Mv 108,4,212
Rt 36,13,16384
s_add_u32 m0, s36, 135168
Mv 100,4,208
Rt 38,13,16896
buffer_load_dwordx4 v3, s[20:23], s56 offen offset:0 sc1 lds
Mv 112,8,208
Rt 40,14,16384
v_mul_f32_e32 v38, s54, v38
Mv 120,8,212
Rt 42,14,16896
v_mul_f32_e32 v39, s54, v39
Mv 124,12,212
Rt 44,15,16384
Cv 38,38,39
Mv 116,12,208
Rt 46,15,16896
s_waitcnt lgkmcnt(8)
Mv 96,16,216
Rt 0,12,20480
v_accvgpr_write_b32 a195, v38
Mv 104,16,220
Rt 2,12,20992
buffer_load_dwordx4 v3, s[20:23], s56 offen offset:1024 sc1 lds
Mv 108,20,220
Rt 4,13,20480
v_mul_f32_e32 v40, s54, v40
)" R"(
Mv 100,20,216
Rt 6,13,20992
v_mul_f32_e32 v41, s54, v41
Cv 40,40,41
Mv 112,24,216
Rt 8,14,20480
v_accvgpr_write_b32 a196, v40
Mv 120,24,220
Rt 10,14,20992
v_mul_f32_e32 v42, s54, v42
Mv 124,28,220
Rt 12,15,20480
v_mul_f32_e32 v43, s54, v43
Mv 116,28,216
Rt 14,15,20992
buffer_load_dwordx4 v3, s[20:23], s56 offen offset:2048 sc1 lds
s_waitcnt lgkmcnt(8)
Mv 96,32,128
Rt 16,12,24576
Cv 42,42,43
Mv 104,32,132
Rt 18,12,25088
v_accvgpr_write_b32 a197, v42
Mv 108,36,132
Rt 20,13,24576
v_mul_f32_e32 v44, s54, v44
Mv 100,36,128
Rt 22,13,25088
v_mul_f32_e32 v45, s54, v45
Cv 44,44,45
Mv 112,40,128
Rt 24,14,24576
v_accvgpr_write_b32 a198, v44
Mv 120,40,132
Rt 26,14,25088
buffer_load_dwordx4 v3, s[20:23], s56 offen offset:3072 sc1 lds
Mv 124,44,132
Rt 28,15,24576
v_mul_f32_e32 v46, s54, v46
Mv 116,44,128
Rt 30,15,25088
s_waitcnt lgkmcnt(8)
Mv 96,0,136
Rt 32,16,0
v_readfirstlane_b32 s63, v24
Mv 104,0,140
Rt 34,16,512
v_and_b32 v24, 16, v1
Mv 108,4,140
Rt 36,17,0
v_lshrrev_b32_e64 v24, v24, s63
Mv 100,4,136
Rt 38,17,512
v_and_b32 v24, 0x7ff, v24
v_mul_u32_u24 v24, 896, v24
Mv 112,8,136
Rt 40,18,0
v_and_b32 v25, 14, v0
Mv 120,8,140
Rt 42,18,512
v_min_u32 v25, 1, v25
Mv 124,12,140
Rt 44,19,0
v_lshlrev_b32 v25, 30, v25
)" R"(
v_and_or_b32 v25, v0, 48, v25
Mv 116,12,136
Rt 46,19,512
s_waitcnt vmcnt(8) lgkmcnt(8)
s_barrier
buffer_store_dword v6, v7, s[20:23], s49 offen offset:3072 sc0 sc1
Mv 96,16,144
Rt 0,16,4096
v_cmp_eq_u32_e32 vcc, s3, v4
s_andn2_b64 s[28:29], -1, vcc
s_cbranch_scc1 .Lzsp6
.Lzok6:
Mv 104,16,148
Rt 2,16,4608
v_add_u32 v24, v24, v25
v_add_u32 v8, 0x10000, v1
Mv 108,20,148
Rt 4,17,4096
s_and_b32 s83, s33, 1
v_add_u32 v10, 0x20000, v1
Mv 100,20,144
Rt 6,17,4608
s_mul_i32 s82, s83, 448
v_mul_f32_e32 v47, s54, v47
s_add_u32 s56, s56, 0x10000
Mv 112,24,144
Rt 8,18,4096
Cv 46,46,47
s_add_u32 s55, s55, 0x10000
Mv 120,24,148
Rt 10,18,4608
v_accvgpr_write_b32 a199, v46
s_add_u32 s55, s55, 0x10000
Mv 124,28,148
Rt 12,19,4096
v_mul_f32_e32 v48, s54, v48
s_cmp_eq_u32 s57, 2
s_cselect_b32 s55, s59, s55
Mv 116,28,144
Rt 14,19,4608
s_mov_b32 s93, 0
s_waitcnt lgkmcnt(8)
Mv 96,32,152
Rt 16,16,8192
v_mul_f32_e32 v49, s54, v49
s_mov_b32 s94, 1
Mv 104,32,156
Rt 18,16,8704
Cv 48,48,49
Mv 108,36,156
Rt 20,17,8192
v_accvgpr_write_b32 a200, v48
Mv 100,36,152
Rt 22,17,8704
v_mul_f32_e32 v50, s54, v50
v_mul_f32_e32 v51, s54, v51
Mv 112,40,152
Rt 24,18,8192
Cv 50,50,51
Mv 120,40,156
Rt 26,18,8704
v_accvgpr_write_b32 a201, v50
Mv 124,44,156
Rt 28,19,8192
v_mul_f32_e32 v52, s54, v52
)" R"(
Mv 116,44,152
Rt 30,19,8704
s_waitcnt lgkmcnt(8)
Mv 96,0,160
Rt 32,16,12288
v_mul_f32_e32 v53, s54, v53
Mv 104,0,164
Rt 34,16,12800
Cv 52,52,53
Mv 108,4,164
Rt 36,17,12288
v_accvgpr_write_b32 a202, v52
Mv 100,4,160
Rt 38,17,12800
buffer_load_dwordx4 a[128:131], v24, s[12:15], s82 offen offset:0
Mv 112,8,160
Rt 40,18,12288
v_mul_f32_e32 v54, s54, v54
Mv 120,8,164
Rt 42,18,12800
v_mul_f32_e32 v55, s54, v55
Mv 124,12,164
Rt 44,19,12288
Cv 54,54,55
Mv 116,12,160
Rt 46,19,12800
s_waitcnt lgkmcnt(8)
Mv 96,16,168
Rt 0,16,16384
buffer_load_dwordx4 a[132:135], v24, s[12:15], s82 offen offset:64
Mv 104,16,172
Rt 2,16,16896
v_accvgpr_write_b32 a203, v54
Mv 108,20,172
Rt 4,17,16384
v_mul_f32_e32 v56, s54, v56
Mv 100,20,168
Rt 6,17,16896
v_mul_f32_e32 v57, s54, v57
Cv 56,56,57
Mv 112,24,168
Rt 8,18,16384
v_accvgpr_write_b32 a204, v56
Mv 120,24,172
Rt 10,18,16896
buffer_load_dwordx4 a[136:139], v24, s[12:15], s82 offen offset:128
Mv 124,28,172
Rt 12,19,16384
v_mul_f32_e32 v58, s54, v58
Mv 116,28,168
Rt 14,19,16896
s_waitcnt lgkmcnt(8)
Mv 96,32,176
Rt 16,16,20480
buffer_load_dwordx4 a[140:143], v24, s[12:15], s82 offen offset:192
Mv 104,32,180
Rt 18,16,20992
v_mul_f32_e32 v59, s54, v59
Mv 108,36,180
Rt 20,17,20480
Cv 58,58,59
Mv 100,36,176
Rt 22,17,20992
v_accvgpr_write_b32 a205, v58
)" R"(
v_mul_f32_e32 v60, s54, v60
Mv 112,40,176
Rt 24,18,20480
v_mul_f32_e32 v61, s54, v61
Mv 120,40,180
Rt 26,18,20992
buffer_load_dwordx4 a[144:147], v24, s[12:15], s82 offen offset:256
Mv 124,44,180
Rt 28,19,20480
Cv 60,60,61
Mv 116,44,176
Rt 30,19,20992
s_waitcnt lgkmcnt(8)
s_barrier
Mv 96,0,184
Rt 32,16,24576
buffer_load_dwordx4 a[148:151], v24, s[12:15], s82 offen offset:320
Mv 104,0,188
Rt 34,16,25088
v_accvgpr_write_b32 a206, v60
Mv 108,4,188
Rt 36,17,24576
v_mul_f32_e32 v62, s54, v62
Mv 100,4,184
Rt 38,17,25088
v_mul_f32_e32 v63, s54, v63
Cv 62,62,63
Mv 112,8,184
Rt 40,18,24576
v_accvgpr_write_b32 a207, v62
Mv 120,8,188
Rt 42,18,25088
buffer_load_dwordx4 a[152:155], v24, s[12:15], s82 offen offset:384
Mv 124,12,188
Rt 44,19,24576
Mv 116,12,184
Rt 46,19,25088
s_waitcnt lgkmcnt(8)
Mv 96,16,48
Rb 0,8,32768
Mv 104,16,52
Rb 4,8,33792
Mv 108,20,52
Rb 8,8,34816
Mv 100,20,48
Rb 12,8,35840
Mv 112,24,48
Rb 16,8,40960
Rb 20,8,41984
Mv 120,24,52
Rb 24,8,43008
Mv 124,28,52
Mv 116,28,48
Rb 28,8,44032
s_waitcnt lgkmcnt(8)
Mv 96,32,56
Mv 104,32,60
Rb 32,8,36864
Mv 108,36,60
Mv 100,36,56
Rb 36,8,37888
Mv 112,40,56
Mv 120,40,60
Rb 40,8,38912
Mv 124,44,60
)" R"(
Mv 116,44,56
Rb 44,8,39936
s_cmp_eq_u32 s35, 7
s_cbranch_scc1 .LzA7
.LzA7R:
s_waitcnt lgkmcnt(8)
Mz 64,0,64
Rb 48,8,45056
v_mul_f32_e32 v96, s54, v96
Mz 68,0,68
v_mul_f32_e32 v97, s54, v97
Cv 96,96,97
Mv 68,4,76
Rb 52,8,46080
s_add_u32 m0, s36, 0
Mv 64,4,72
buffer_load_dwordx4 v2, s[20:23], s55 offen offset:0 sc1 lds
Mv 64,8,80
Rb 56,8,47104
v_accvgpr_write_b32 a208, v96
Mv 68,8,84
v_mul_f32_e32 v98, s54, v98
v_mul_f32_e32 v99, s54, v99
Mv 68,12,92
Rb 60,8,48128
s_waitcnt lgkmcnt(8)
Mv 64,12,88
Cv 98,98,99
v_accvgpr_write_b32 a209, v98
v_mul_f32_e32 v100, s54, v100
Mv 64,16,96
Rb 0,8,49152
v_mul_f32_e32 v101, s54, v101
Mv 68,16,100
Cv 100,100,101
v_accvgpr_write_b32 a210, v100
Mv 68,20,108
Rb 4,8,50176
v_mul_f32_e32 v102, s54, v102
Mv 64,20,104
buffer_load_dwordx4 v2, s[20:23], s55 offen offset:1024 sc1 lds
Mv 64,24,112
Rb 8,8,51200
v_mul_f32_e32 v103, s54, v103
Mv 68,24,116
Cv 102,102,103
v_accvgpr_write_b32 a211, v102
Mv 68,28,124
Rb 12,8,52224
s_waitcnt lgkmcnt(8)
Mv 64,28,120
v_mul_f32_e32 v104, s54, v104
v_mul_f32_e32 v105, s54, v105
Cv 104,104,105
Mz 72,32,64
Rb 16,8,57344
v_accvgpr_write_b32 a212, v104
Mz 76,32,68
v_mul_f32_e32 v106, s54, v106
v_mul_f32_e32 v107, s54, v107
Mv 76,36,76
Rb 20,8,58368
Cv 106,106,107
Mv 72,36,72
)" R"(
buffer_load_dwordx4 v2, s[20:23], s55 offen offset:2048 sc1 lds
Mv 72,40,80
Rb 24,8,59392
v_accvgpr_write_b32 a213, v106
Mv 76,40,84
v_mul_f32_e32 v108, s54, v108
v_mul_f32_e32 v109, s54, v109
Mv 76,44,92
Rb 28,8,60416
s_waitcnt lgkmcnt(8)
Mv 72,44,88
Cv 108,108,109
v_accvgpr_write_b32 a214, v108
v_mul_f32_e32 v110, s54, v110
Mv 72,48,96
Rb 32,8,53248
v_mul_f32_e32 v111, s54, v111
Mv 76,48,100
Cv 110,110,111
v_accvgpr_write_b32 a215, v110
Mv 76,52,108
Rb 36,8,54272
v_mul_f32_e32 v112, s54, v112
Mv 72,52,104
buffer_load_dwordx4 v2, s[20:23], s55 offen offset:3072 sc1 lds
Mv 72,56,112
Rb 40,8,55296
v_mul_f32_e32 v113, s54, v113
Mv 76,56,116
Cv 112,112,113
v_accvgpr_write_b32 a216, v112
Mv 76,60,124
Rb 44,8,56320
s_waitcnt lgkmcnt(8)
.LzH35:
s_cbranch_execz .LzH14
Mv 72,60,120
v_mul_f32_e32 v114, s54, v114
v_mul_f32_e32 v115, s54, v115
Cv 114,114,115
Mz 80,0,64
Rb 48,8,61440
v_accvgpr_write_b32 a217, v114
Mz 84,0,68
v_mul_f32_e32 v116, s54, v116
v_mul_f32_e32 v117, s54, v117
Mv 84,4,76
Rb 52,8,62464
s_add_u32 m0, s36, 4096
Mv 80,4,72
buffer_load_dwordx4 v3, s[20:23], s55 offen offset:0 sc1 lds
Mv 80,8,80
Rb 56,8,63488
Cv 116,116,117
Mv 84,8,84
v_accvgpr_write_b32 a218, v116
v_mul_f32_e32 v118, s54, v118
Mv 84,12,92
Rb 60,8,64512
s_waitcnt lgkmcnt(8)
Mv 80,12,88
v_mul_f32_e32 v119, s54, v119
Cv 118,118,119
v_accvgpr_write_b32 a219, v118
)" R"(
Mv 80,16,96
v_mul_f32_e32 v120, s54, v120
v_mul_f32_e32 v121, s54, v121
Mv 84,16,100
Cv 120,120,121
v_accvgpr_write_b32 a220, v120
Mv 84,20,108
v_mul_f32_e32 v122, s54, v122
v_mul_f32_e32 v123, s54, v123
Mv 80,20,104
buffer_load_dwordx4 v3, s[20:23], s55 offen offset:1024 sc1 lds
Mv 80,24,112
s_add_u32 s50, s50, 1024
s_min_u32 s50, s50, s60
Mv 84,24,116
Cv 122,122,123
v_accvgpr_write_b32 a221, v122
Mv 84,28,124
v_mul_f32_e32 v124, s54, v124
v_mul_f32_e32 v125, s54, v125
Mv 80,28,120
Cv 124,124,125
s_waitcnt lgkmcnt(0)
s_barrier
Mz 88,32,64
Rb 0,1,32768
v_accvgpr_write_b32 a222, v124
Mz 92,32,68
Rb 4,1,33792
v_mul_f32_e32 v126, s54, v126
Mv 92,36,76
Rb 8,1,34816
v_mul_f32_e32 v127, s54, v127
Mv 88,36,72
Rb 12,1,35840
buffer_load_dwordx4 v3, s[20:23], s55 offen offset:2048 sc1 lds
Mv 88,40,80
Rb 16,1,40960
Cv 126,126,127
Mv 92,40,84
Rb 20,1,41984
v_accvgpr_write_b32 a223, v126
Mv 92,44,92
Rb 24,1,43008
v_mov_b32 v16, 0
Mv 88,44,88
Rb 28,1,44032
v_mov_b32 v17, 0
v_mov_b32 v18, 0
Mv 88,48,96
Rb 32,1,36864
v_mov_b32 v19, 0
Mv 92,48,100
Rb 36,1,37888
v_mov_b32 v20, 0
Mv 92,52,108
Rb 40,1,38912
v_mov_b32 v21, 0
Mv 88,52,104
Rb 44,1,39936
buffer_load_dwordx4 v3, s[20:23], s55 offen offset:3072 sc1 lds
Mv 88,56,112
v_mov_b32 v22, 0
v_mov_b32 v23, 0
Mv 92,56,116
)" R"(
Mv 92,60,124
s_waitcnt lgkmcnt(8)
Mv 88,60,120
Mz 96,0,64
Rb 48,1,45056
buffer_load_dword v4, v5, s[20:23], s50 offen offset:0 sc0 sc1
Mz 100,0,68
s_cmp_le_i32 s57, s58
s_cbranch_scc1 .Lzmk0
.Lzmr0:
Mv 100,4,76
Rb 52,1,46080
s_add_u32 m0, s36, 65536
s_sub_u32 s57, s57, 1
Mv 96,4,72
buffer_load_dwordx4 v2, s[20:23], s56 offen offset:0 sc1 lds
s_add_u32 s55, s55, 0x10000
Mv 96,8,80
Rb 56,1,47104
s_cmp_eq_u32 s57, 2
s_cselect_b32 s55, s59, s55
Mv 100,8,84
v_exp_f32_e32 v32, v64
Mv 100,12,92
Rb 60,1,48128
s_waitcnt lgkmcnt(8)
Mv 96,12,88
v_exp_f32_e32 v24, v65
Mv 96,16,96
Rb 0,1,49152
v_exp_f32_e32 v33, v66
Mv 100,16,100
v_exp_f32_e32 v25, v67
v_exp_f32_e32 v34, v72
Mv 100,20,108
Rb 4,1,50176
v_exp_f32_e32 v26, v73
Mv 96,20,104
buffer_load_dwordx4 v2, s[20:23], s56 offen offset:1024 sc1 lds
v_add_f32_e32 v14, v32, v24
Mv 96,24,112
Rb 8,1,51200
v_exp_f32_e32 v35, v74
Mv 100,24,116
v_exp_f32_e32 v27, v75
Cv 32,32,24
Mv 100,28,124
Rb 12,1,52224
v_add_f32_e32 v14, v14, v33
s_waitcnt lgkmcnt(8)
Mv 96,28,120
v_exp_f32_e32 v36, v68
v_add_f32_e32 v14, v14, v25
Mz 104,32,64
Rb 16,1,57344
v_exp_f32_e32 v28, v69
Mz 108,32,68
v_exp_f32_e32 v37, v70
v_exp_f32_e32 v29, v71
Mv 108,36,76
Rb 20,1,58368
v_exp_f32_e32 v38, v76
Mv 104,36,72
buffer_load_dwordx4 v2, s[20:23], s56 offen offset:2048 sc1 lds
)" R"(
Cv 33,33,25
Mv 104,40,80
Rb 24,1,59392
v_exp_f32_e32 v24, v77
Mv 108,40,84
v_exp_f32_e32 v39, v78
v_add_f32_e32 v14, v14, v34
Mv 108,44,92
Rb 28,1,60416
v_add_f32_e32 v14, v14, v26
s_waitcnt lgkmcnt(8)
Mv 104,44,88
v_exp_f32_e32 v25, v79
Cv 34,34,26
Mv 104,48,96
Rb 32,1,53248
v_exp_f32_e32 v40, v80
Mv 108,48,100
v_exp_f32_e32 v26, v81
v_add_f32_e32 v14, v14, v35
v_add_f32_e32 v14, v14, v27
Mv 108,52,108
Rb 36,1,54272
Cv 35,35,27
v_add_f32_e32 v15, v36, v28
Mv 104,52,104
buffer_load_dwordx4 v2, s[20:23], s56 offen offset:3072 sc1 lds
Cv 36,36,28
Mv 104,56,112
Rb 40,1,55296
v_exp_f32_e32 v41, v82
Mv 108,56,116
v_exp_f32_e32 v27, v83
v_add_f32_e32 v15, v15, v37
Mv 108,60,124
Rb 44,1,56320
v_add_f32_e32 v15, v15, v29
s_waitcnt lgkmcnt(8)
Mv 104,60,120
v_exp_f32_e32 v42, v88
Cv 37,37,29
Mz 112,0,64
Rb 48,1,61440
v_exp_f32_e32 v28, v89
Mz 116,0,68
v_exp_f32_e32 v43, v90
v_add_f32_e32 v15, v15, v38
v_add_f32_e32 v15, v15, v24
Mv 116,4,76
Rb 52,1,62464
s_add_u32 m0, s36, 69632
Cv 38,38,24
Mv 112,4,72
buffer_load_dwordx4 v3, s[20:23], s56 offen offset:0 sc1 lds
v_add_f32_e32 v15, v15, v39
Mv 112,8,80
Rb 56,1,63488
v_exp_f32_e32 v29, v91
Mv 116,8,84
v_exp_f32_e32 v44, v84
v_add_f32_e32 v15, v15, v25
Mv 116,12,92
Rb 60,1,64512
Cv 39,39,25
)" R"(
s_waitcnt lgkmcnt(8)
Mv 112,12,88
v_exp_f32_e32 v24, v85
v_add_f32_e32 v14, v14, v40
Mv 112,16,96
v_exp_f32_e32 v45, v86
v_add_f32_e32 v14, v14, v26
Mv 116,16,100
v_exp_f32_e32 v25, v87
Cv 40,40,26
v_add_f32_e32 v14, v14, v41
Mv 116,20,108
v_exp_f32_e32 v46, v92
v_add_f32_e32 v14, v14, v27
Mv 112,20,104
buffer_load_dwordx4 v3, s[20:23], s56 offen offset:1024 sc1 lds
Cv 41,41,27
Mv 112,24,112
v_exp_f32_e32 v26, v93
v_add_f32_e32 v14, v14, v42
Mv 116,24,116
v_exp_f32_e32 v47, v94
v_add_f32_e32 v14, v14, v28
Mv 116,28,124
v_exp_f32_e32 v27, v95
Cv 42,42,28
Mv 112,28,120
v_add_f32_e32 v14, v14, v43
s_waitcnt vmcnt(23) lgkmcnt(0)
s_barrier
Mz 120,32,64
Rb 0,10,0
v_add_f32_e32 v14, v14, v29
Cv 43,43,29
Mz 124,32,68
Rb 4,10,1024
v_add_f32_e32 v15, v15, v44
v_add_f32_e32 v15, v15, v24
Cv 44,44,24
Mv 124,36,76
Rb 8,10,2048
v_add_f32_e32 v15, v15, v45
v_add_f32_e32 v15, v15, v25
Mv 120,36,72
Rb 12,10,3072
buffer_load_dwordx4 v3, s[20:23], s56 offen offset:2048 sc1 lds
Mv 120,40,80
Rb 16,10,4096
Cv 45,45,25
v_add_f32_e32 v15, v15, v46
Mv 124,40,84
Rb 20,10,5120
v_add_f32_e32 v15, v15, v26
Cv 46,46,26
Mv 124,44,92
Rb 24,10,6144
v_add_f32_e32 v15, v15, v47
v_add_f32_e32 v15, v15, v27
Mv 120,44,88
Rb 28,10,7168
Cv 47,47,27
v_max_f32_e32 v11, v14, v15
Mv 120,48,96
Rb 32,10,8192
v_mov_b32 v24, v14
)" R"(
v_mov_b32 v25, v15
Mv 124,48,100
Rb 36,10,9216
v_permlane32_swap_b32_e32 v24, v25
v_add_f32_e32 v24, v24, v25
v_mov_b32 v25, v24
Mv 124,52,108
Rb 40,10,10240
v_permlane16_swap_b32_e32 v24, v25
v_add_f32_e32 v24, v24, v25
Mv 120,52,104
Rb 44,10,11264
buffer_load_dwordx4 v3, s[20:23], s56 offen offset:3072 sc1 lds
Mv 120,56,112
v_rcp_f32_e32 v24, v24
Mv 124,56,116
s_add_u32 s56, s56, 0x10000
v_max_f32_e32 v11, v11, v24
v_cmp_nge_f32_e32 vcc, s62, v11
Mv 124,60,124
s_waitcnt lgkmcnt(8)
s_cbranch_vccnz .Lzfx0
.Lzfr0:
Mv 120,60,120
v_mov_b32 v12, v14
v_mov_b32 v13, v15
v_mfma_f32_16x16x32_bf16 v[128:131], a[0:3], v[32:35], 0
Rb 48,10,12288
v_mfma_f32_16x16x32_bf16 v[136:139], a[0:3], v[36:39], 0
s_cmp_le_i32 s57, s58
s_cbranch_scc1 .Lzmk1
.Lzmr1:
v_mfma_f32_16x16x32_bf16 v[140:143], a[4:7], v[36:39], 0
Rb 52,10,13312
s_add_u32 m0, s36, 32768
v_mfma_f32_16x16x32_bf16 v[132:135], a[4:7], v[32:35], 0
buffer_load_dwordx4 v2, s[20:23], s55 offen offset:0 sc1 lds
v_mfma_f32_16x16x32_bf16 v[144:147], a[8:11], v[32:35], 0
Rb 56,10,14336
s_and_b32 s92, s2, 7
v_mfma_f32_16x16x32_bf16 v[152:155], a[8:11], v[36:39], 0
s_mul_i32 s92, s92, 14
s_lshr_b32 s82, s2, 3
v_mfma_f32_16x16x32_bf16 v[156:159], a[12:15], v[36:39], 0
Rb 60,10,15360
s_waitcnt lgkmcnt(8)
v_mfma_f32_16x16x32_bf16 v[148:151], a[12:15], v[32:35], 0
buffer_load_dwordx4 v2, s[20:23], s55 offen offset:1024 sc1 lds
v_mfma_f32_16x16x32_bf16 v[160:163], a[16:19], v[32:35], 0
Rb 0,10,16384
s_add_u32 s82, s82, s92
v_mfma_f32_16x16x32_bf16 v[168:171], a[16:19], v[36:39], 0
v_exp_f32_e32 v48, v96
v_mfma_f32_16x16x32_bf16 v[172:175], a[20:23], v[36:39], 0
Rb 4,10,17408
v_mfma_f32_16x16x32_bf16 v[164:167], a[20:23], v[32:35], 0
buffer_load_dwordx4 v2, s[20:23], s55 offen offset:2048 sc1 lds
v_mfma_f32_16x16x32_bf16 v[176:179], a[24:27], v[32:35], 0
Rb 8,10,18432
v_exp_f32_e32 v28, v97
v_mfma_f32_16x16x32_bf16 v[184:187], a[24:27], v[36:39], 0
v_exp_f32_e32 v49, v98
v_mfma_f32_16x16x32_bf16 v[188:191], a[28:31], v[36:39], 0
Rb 12,10,19456
)" R"(
s_waitcnt lgkmcnt(8)
v_mfma_f32_16x16x32_bf16 v[180:183], a[28:31], v[32:35], 0
buffer_load_dwordx4 v2, s[20:23], s55 offen offset:3072 sc1 lds
v_add_f32_e32 v14, v48, v28
v_mfma_f32_16x16x32_bf16 v[192:195], a[32:35], v[32:35], 0
Rb 16,10,20480
Cv 48,48,28
v_mfma_f32_16x16x32_bf16 v[200:203], a[32:35], v[36:39], 0
v_exp_f32_e32 v29, v99
v_add_f32_e32 v14, v14, v49
v_mfma_f32_16x16x32_bf16 v[204:207], a[36:39], v[36:39], 0
Rb 20,10,21504
s_add_u32 m0, s36, 36864
v_mfma_f32_16x16x32_bf16 v[196:199], a[36:39], v[32:35], 0
buffer_load_dwordx4 v3, s[20:23], s55 offen offset:0 sc1 lds
v_mfma_f32_16x16x32_bf16 v[208:211], a[40:43], v[32:35], 0
Rb 24,10,22528
v_exp_f32_e32 v50, v104
v_mfma_f32_16x16x32_bf16 v[216:219], a[40:43], v[36:39], 0
s_cmp_gt_u32 s33, 1
s_cselect_b32 s24, s78, s76
s_cselect_b32 s25, s79, s77
v_mfma_f32_16x16x32_bf16 v[220:223], a[44:47], v[36:39], 0
Rb 28,10,23552
s_waitcnt lgkmcnt(8)
v_mfma_f32_16x16x32_bf16 v[212:215], a[44:47], v[32:35], 0
buffer_load_dwordx4 v3, s[20:23], s55 offen offset:1024 sc1 lds
v_add_f32_e32 v14, v14, v29
v_mfma_f32_16x16x32_bf16 v[224:227], a[48:51], v[32:35], 0
Rb 32,10,24576
s_mov_b32 s27, 0x20000
v_mfma_f32_16x16x32_bf16 v[232:235], a[48:51], v[36:39], 0
s_cmp_lt_u32 s35, 7
s_cselect_b32 s26, 0x188000, 0
Cv 49,49,29
v_mfma_f32_16x16x32_bf16 v[236:239], a[52:55], v[36:39], 0
Rb 36,10,25600
s_mul_i32 s83, s83, 802816
v_mfma_f32_16x16x32_bf16 v[228:231], a[52:55], v[32:35], 0
buffer_load_dwordx4 v3, s[20:23], s55 offen offset:2048 sc1 lds
v_mfma_f32_16x16x32_bf16 v[240:243], a[56:59], v[32:35], 0
Rb 40,10,26624
s_lshl_b32 s92, s82, 5
v_add_f32_e32 v14, v14, v50
v_mfma_f32_16x16x32_bf16 v[248:251], a[56:59], v[36:39], 0
s_add_u32 s83, s83, s92
v_and_b32 v9, 62, v0
v_mul_u32_u24 v9, 0x6f0, v9
v_mfma_f32_16x16x32_bf16 v[252:255], a[60:63], v[36:39], 0
Rb 44,10,27648
s_waitcnt lgkmcnt(8)
v_mfma_f32_16x16x32_bf16 v[244:247], a[60:63], v[32:35], 0
buffer_load_dwordx4 v3, s[20:23], s55 offen offset:3072 sc1 lds
Mp 128,0,40
Rb 48,10,28672
v_add_u32 v9, v9, v1
Mp 136,0,44
v_exp_f32_e32 v24, v105
Mp 140,4,44
Rb 52,10,29696
v_add_f32_e32 v14, v14, v24
Mp 132,4,40
v_exp_f32_e32 v51, v106
Mp 144,8,40
)" R"(
Rb 56,10,30720
v_exp_f32_e32 v25, v107
Mp 152,8,44
v_exp_f32_e32 v52, v100
Mp 156,12,44
Rb 60,10,31744
s_waitcnt lgkmcnt(8)
Mp 148,12,40
v_exp_f32_e32 v26, v101
Cv 50,50,24
Mp 160,16,40
v_exp_f32_e32 v53, v102
Mp 168,16,44
v_exp_f32_e32 v27, v103
v_add_f32_e32 v14, v14, v51
Mp 172,20,44
v_exp_f32_e32 v54, v108
Mp 164,20,40
v_exp_f32_e32 v28, v109
Mp 176,24,40
v_exp_f32_e32 v55, v110
v_add_f32_e32 v14, v14, v25
Mp 184,24,44
v_exp_f32_e32 v29, v111
Mp 188,28,44
Cv 51,51,25
v_add_f32_e32 v15, v52, v26
Mp 180,28,40
Cv 52,52,26
s_waitcnt vmcnt(16) lgkmcnt(0)
s_barrier
Mp 192,32,40
Rb 0,1,0
s_add_u32 m0, s36, 131072
s_nop 0
buffer_load_dwordx4 v9, s[24:27], s83 offen offset:0 lds
Mp 200,32,44
Rb 4,1,1024
v_add_f32_e32 v15, v15, v53
v_add_f32_e32 v15, v15, v27
Mp 204,36,44
Rb 8,1,2048
s_add_u32 s83, s83, 0x1bc00
buffer_load_dwordx4 v9, s[24:27], s83 offen offset:1024 lds
Mp 196,36,40
Rb 12,1,3072
Cv 53,53,27
Mp 208,40,40
Rb 16,1,8192
s_add_u32 s83, s83, 0x1bc00
buffer_load_dwordx4 v9, s[24:27], s83 offen offset:2048 lds
Mp 216,40,44
Rb 20,1,9216
v_add_f32_e32 v15, v15, v54
Mp 220,44,44
Rb 24,1,10240
s_add_u32 s83, s83, 0x1bc00
buffer_load_dwordx4 v9, s[24:27], s83 offen offset:3072 lds
Mp 212,44,40
Rb 28,1,11264
v_add_f32_e32 v15, v15, v28
Cv 54,54,28
Mp 224,48,40
Rb 32,1,4096
s_add_u32 m0, s36, 135168
)" R"(
s_add_u32 s83, s83, 0x1cc00
buffer_load_dwordx4 v9, s[24:27], s83 offen offset:0 lds
Mp 232,48,44
Rb 36,1,5120
v_add_f32_e32 v15, v15, v55
v_add_f32_e32 v15, v15, v29
Mp 236,52,44
Rb 40,1,6144
s_add_u32 s83, s83, 0x1bc00
buffer_load_dwordx4 v9, s[24:27], s83 offen offset:1024 lds
Mp 228,52,40
Rb 44,1,7168
Cv 55,55,29
Mp 240,56,40
s_add_u32 s83, s83, 0x1bc00
buffer_load_dwordx4 v9, s[24:27], s83 offen offset:2048 lds
v_cmp_eq_u32_e32 vcc, s3, v4
s_andn2_b64 s[28:29], -1, vcc
s_cbranch_scc1 .Lzsp8
.Lzok8:
Mp 248,56,44
s_sub_u32 s57, s57, 1
Mp 252,60,44
s_waitcnt lgkmcnt(8)
Mp 244,60,40
s_cmp_gt_i32 s57, s93
s_cbranch_scc1 .LzL
.LzH36:
s_cbranch_execz .LzH15
.LzW:
s_lshl1_add_u32 s57, s35, 2
s_mov_b32 s59, s67
s_add_u32 s56, s89, 0x2000000
s_mov_b32 s94, 2
Mz 64,0,224
Rb 48,1,12288
v_lshrrev_b32 v9, 4, v0
Mz 68,0,228
v_lshlrev_b32 v9, 4, v9
v_add_lshl_u32 v9, v9, v0, 3
v_accvgpr_mov_b32 a64, a224
Mv 68,4,236
Rb 52,1,13312
s_add_u32 m0, s36, 98304
v_accvgpr_mov_b32 a65, a225
Mv 64,4,232
buffer_load_dwordx4 v2, s[20:23], s56 offen offset:0 sc1 lds
v_accvgpr_mov_b32 a66, a226
Mv 64,8,240
Rb 56,1,14336
s_add_u32 s92, s36, 131072
v_accvgpr_mov_b32 a67, a227
Mv 68,8,244
v_add_u32 v9, s92, v9
v_accvgpr_mov_b32 a68, a228
s_waitcnt vmcnt(1)
Mv 68,12,252
Rb 60,1,15360
v_accvgpr_mov_b32 a69, a229
s_waitcnt lgkmcnt(8)
Mv 64,12,248
buffer_load_dwordx4 v2, s[20:23], s56 offen offset:1024 sc1 lds
Rt 156,9,0
Mv 64,16,192
)" R"(
Rb 0,1,16384
Rt 158,9,128
v_accvgpr_mov_b32 a70, a230
Mv 68,16,196
Rt 160,9,1024
v_exp_f32_e32 v56, v112
Mv 68,20,204
Rb 4,1,17408
Rt 162,9,1152
v_add_f32_e32 v14, v14, v56
Mv 64,20,200
buffer_load_dwordx4 v2, s[20:23], s56 offen offset:2048 sc1 lds
Rt 164,9,2048
Mv 64,24,208
Rb 8,1,18432
Rt 166,9,2176
v_accvgpr_mov_b32 a71, a231
Mv 68,24,212
Rt 168,9,3072
v_exp_f32_e32 v26, v113
Mv 68,28,220
Rb 12,1,19456
Rt 170,9,3200
Mv 64,28,216
buffer_load_dwordx4 v2, s[20:23], s56 offen offset:3072 sc1 lds
Rt 172,9,4096
v_add_f32_e32 v14, v14, v26
Mz 72,32,224
Rb 16,1,24576
Rt 174,9,4224
Cv 56,56,26
Mz 76,32,228
Rt 176,9,5120
v_exp_f32_e32 v57, v114
Mv 76,36,236
Rb 20,1,25600
s_add_u32 m0, s36, 102400
Rt 178,9,5248
Mv 72,36,232
buffer_load_dwordx4 v3, s[20:23], s56 offen offset:0 sc1 lds
Rt 180,9,6144
Mv 72,40,240
Rb 24,1,26624
Rt 182,9,6272
v_add_f32_e32 v14, v14, v57
Mv 76,40,244
v_exp_f32_e32 v27, v115
v_accvgpr_mov_b32 a72, a232
Mv 76,44,252
Rb 28,1,27648
v_add_f32_e32 v14, v14, v27
Mv 72,44,248
buffer_load_dwordx4 v3, s[20:23], s56 offen offset:1024 sc1 lds
Cv 57,57,27
Mv 72,48,192
Rb 32,1,20480
v_and_b32 v9, 14, v0
v_accvgpr_mov_b32 a73, a233
Mv 76,48,196
v_min_u32 v9, 1, v9
v_lshlrev_b32 v9, 8, v9
v_accvgpr_mov_b32 a74, a234
Mv 76,52,204
Rb 36,1,21504
)" R"(
v_and_or_b32 v9, v0, 48, v9
v_accvgpr_mov_b32 a75, a235
Mv 72,52,200
buffer_load_dwordx4 v3, s[20:23], s56 offen offset:2048 sc1 lds
v_accvgpr_mov_b32 a76, a236
Mv 72,56,208
Rb 40,1,22528
v_lshlrev_b32 v9, 1, v9
v_accvgpr_mov_b32 a77, a237
Mv 76,56,212
v_and_or_b32 v9, v1, 16, v9
s_lshl_b32 s92, s33, 7
v_accvgpr_mov_b32 a78, a238
Mv 76,60,220
Rb 44,1,23552
v_accvgpr_mov_b32 a79, a239
s_waitcnt lgkmcnt(5)
.LzH37:
s_cbranch_execz .LzH19
Mv 72,60,216
v_mfma_f32_16x16x32_bf16 a[184:187], a[156:159], a[128:131], 0
buffer_load_dwordx4 v3, s[20:23], s56 offen offset:3072 sc1 lds
v_exp_f32_e32 v58, v120
Mz 80,0,224
Ma 184,160,132
Rb 48,1,28672
s_add_u32 s92, s92, 162816
v_add_f32_e32 v14, v14, v58
Mz 84,0,228
Ma 184,164,136
s_add_u32 s56, s56, 0x10000
v_add_u32 v9, s92, v9
v_accvgpr_mov_b32 a80, a240
Mv 84,4,236
Ma 184,168,140
Rb 52,1,29696
v_exp_f32_e32 v28, v121
Mv 80,4,232
Ma 184,172,144
v_exp_f32_e32 v59, v122
v_add_f32_e32 v14, v14, v28
Mv 80,8,240
Ma 184,176,148
Rb 56,1,30720
s_mov_b32 s24, s70
s_mov_b32 s25, s71
Mv 84,8,244
Ma 184,180,152
s_mul_i32 s83, s82, 14336
s_lshl_b32 s92, s33, 12
Cv 58,58,28
Mv 84,12,252
Rb 60,1,31744
v_add_f32_e32 v14, v14, v59
s_waitcnt lgkmcnt(8)
Mv 80,12,248
s_add_u32 s83, s83, s92
s_add_u32 m0, s92, 131072
v_accvgpr_mov_b32 a81, a241
Mv 80,16,192
v_exp_f32_e32 v29, v123
v_accvgpr_mov_b32 a82, a242
Mv 84,16,196
v_exp_f32_e32 v60, v116
)" R"(
v_add_f32_e32 v14, v14, v29
Mv 84,20,204
ds_write_b128 v9, a[184:187]
v_and_b32 v9, 0x70, v9
Cv 59,59,29
Mv 80,20,200
v_add_u32 v9, 0x27c00, v9
v_exp_f32_e32 v24, v117
Mv 80,24,208
v_exp_f32_e32 v61, v118
v_add_f32_e32 v15, v15, v60
Mv 84,24,212
v_exp_f32_e32 v25, v119
v_add_f32_e32 v15, v15, v24
Mv 84,28,220
v_exp_f32_e32 v62, v124
Cv 60,60,24
Mv 80,28,216
v_exp_f32_e32 v26, v125
s_waitcnt lgkmcnt(0)
s_barrier
Mz 88,32,224
Rb 0,8,0
v_exp_f32_e32 v63, v126
Mz 92,32,228
Rb 4,8,16384
v_exp_f32_e32 v27, v127
Mv 92,36,236
Rb 8,8,1024
v_add_f32_e32 v15, v15, v61
v_add_f32_e32 v15, v15, v25
Mv 88,36,232
Rb 12,8,17408
Cv 61,61,25
v_add_f32_e32 v15, v15, v62
Mv 88,40,240
Rb 16,8,2048
buffer_load_dwordx4 v1, s[24:27], s83 offen offset:0 lds
Mv 92,40,244
Rb 20,8,18432
v_add_f32_e32 v15, v15, v26
Cv 62,62,26
Mv 92,44,252
Rb 24,8,3072
v_add_f32_e32 v15, v15, v63
v_add_f32_e32 v15, v15, v27
Mv 88,44,248
Rb 28,8,19456
Cv 63,63,27
v_max_f32_e32 v11, v14, v15
Mv 88,48,192
Rb 32,8,4096
v_cmp_nge_f32_e32 vcc, s62, v11
s_add_u32 s55, s55, 0x10000
Mv 92,48,196
Rb 36,8,20480
buffer_load_dwordx4 v1, s[24:27], s83 offen offset:1024 lds
Mv 92,52,204
Rb 40,8,5120
s_cmp_eq_u32 s57, 2
s_cselect_b32 s55, s59, s55
Mv 88,52,200
Rb 44,8,21504
v_accvgpr_mov_b32 a83, a243
v_accvgpr_mov_b32 a84, a244
)" R"(
Mv 88,56,208
buffer_load_dwordx4 v1, s[24:27], s83 offen offset:2048 lds
v_accvgpr_mov_b32 a85, a245
Mv 92,56,212
buffer_load_dwordx4 v1, s[24:27], s83 offen offset:3072 lds
v_accvgpr_mov_b32 a86, a246
Mv 92,60,220
v_accvgpr_mov_b32 a87, a247
s_waitcnt lgkmcnt(8)
s_cbranch_vccnz .Lzfx3
.Lzfr3:
Mv 88,60,216
v_add_f32_e32 v12, v12, v14
v_add_f32_e32 v13, v13, v15
v_mov_b32 v14, v16
Mp 128,0,48
Rb 48,8,6144
v_mov_b32 v15, v20
Mp 136,0,52
buffer_store_dwordx4 v[12:15], v1, s[20:23], s66 offen sc0 sc1
s_cmp_le_i32 s57, s58
s_cbranch_scc1 .Lzmk4
.Lzmr4:
Mp 136,4,60
Rb 52,8,22528
s_add_u32 m0, s36, 0
v_accvgpr_mov_b32 a88, a248
Mp 128,4,56
buffer_load_dwordx4 v2, s[20:23], s55 offen offset:0 sc1 lds
v_accvgpr_mov_b32 a89, a249
Mp 132,8,48
Rb 56,8,7168
v_exp_f32_e32 v32, v64
Mp 140,8,52
v_exp_f32_e32 v28, v65
v_accvgpr_mov_b32 a90, a250
Mp 140,12,60
Rb 60,8,23552
v_exp_f32_e32 v33, v66
s_waitcnt lgkmcnt(8)
Mp 132,12,56
v_exp_f32_e32 v29, v67
v_add_f32_e32 v14, v32, v28
Mp 144,16,48
Rb 0,8,8192
v_exp_f32_e32 v34, v72
Mp 152,16,52
v_exp_f32_e32 v24, v73
v_exp_f32_e32 v35, v74
Mp 152,20,60
Rb 4,8,24576
v_exp_f32_e32 v25, v75
Mp 144,20,56
buffer_load_dwordx4 v2, s[20:23], s55 offen offset:1024 sc1 lds
Cv 32,32,28
Mp 148,24,48
Rb 8,8,9216
Cv 128,128,129
Cv 129,130,131
v_add_f32_e32 v14, v14, v33
Mp 156,24,52
Cv 130,132,133
Cv 131,134,135
v_add_f32_e32 v14, v14, v29
)" R"(
Mp 156,28,60
Rb 12,8,25600
Cv 136,136,137
s_waitcnt lgkmcnt(8)
Mp 148,28,56
Cv 137,138,139
Cv 138,140,141
Cv 139,142,143
Cv 33,33,29
Mp 160,32,48
Rb 16,8,10240
v_exp_f32_e32 v36, v68
Mp 168,32,52
v_exp_f32_e32 v26, v69
v_add_f32_e32 v14, v14, v34
Mp 168,36,60
Rb 20,8,26624
v_exp_f32_e32 v37, v70
v_add_f32_e32 v14, v14, v24
Mp 160,36,56
buffer_load_dwordx4 v2, s[20:23], s55 offen offset:2048 sc1 lds
Cv 144,144,145
Mp 164,40,48
Rb 24,8,11264
Cv 145,146,147
Cv 34,34,24
Mp 172,40,52
buffer_store_dwordx4 v[128:131], v2, s[20:23], s64 offen offset:0 sc0 sc1
Cv 146,148,149
Mp 172,44,60
Rb 28,8,27648
v_exp_f32_e32 v27, v71
s_waitcnt lgkmcnt(8)
Mp 164,44,56
Cv 147,150,151
Cv 152,152,153
Cv 153,154,155
Mp 176,48,48
Rb 32,8,12288
Cv 154,156,157
Cv 155,158,159
Mp 184,48,52
v_exp_f32_e32 v38, v76
v_exp_f32_e32 v28, v77
Mp 184,52,60
Rb 36,8,28672
v_exp_f32_e32 v39, v78
Mp 176,52,56
buffer_load_dwordx4 v2, s[20:23], s55 offen offset:3072 sc1 lds
v_add_f32_e32 v14, v14, v35
Mp 180,56,48
Rb 40,8,13312
Cv 160,160,161
Cv 161,162,163
v_add_f32_e32 v14, v14, v25
Mp 188,56,52
buffer_store_dwordx4 v[136:139], v2, s[20:23], s64 offen offset:1024 sc0 sc1
Cv 35,35,25
Mp 188,60,60
Rb 44,8,29696
Cv 162,164,165
s_waitcnt lgkmcnt(8)
Mp 180,60,56
Cv 163,166,167
)" R"(
Cv 168,168,169
Cv 169,170,171
v_add_f32_e32 v15, v36, v26
Mp 192,0,48
Rb 48,8,14336
Cv 170,172,173
Cv 36,36,26
Mp 200,0,52
buffer_store_dwordx4 v[144:147], v2, s[20:23], s64 offen offset:2048 sc0 sc1
Cv 171,174,175
Mp 200,4,60
Rb 52,8,30720
s_add_u32 m0, s36, 4096
v_exp_f32_e32 v29, v79
Mp 192,4,56
buffer_load_dwordx4 v3, s[20:23], s55 offen offset:0 sc1 lds
Cv 176,176,177
Mp 196,8,48
Rb 56,8,15360
Cv 177,178,179
v_add_f32_e32 v15, v15, v37
Mp 204,8,52
Cv 178,180,181
Cv 179,182,183
Cv 184,184,185
Mp 204,12,60
Rb 60,8,31744
v_add_f32_e32 v15, v15, v27
Cv 37,37,27
s_waitcnt lgkmcnt(8)
Mp 196,12,56
buffer_store_dwordx4 v[152:155], v2, s[20:23], s64 offen offset:3072 sc0 sc1
Cv 185,186,187
Mp 208,16,48
ds_read_b128 v[132:135], v9 offset:0
Cv 186,188,189
Cv 187,190,191
Mp 216,16,52
ds_read_b128 v[140:143], v9 offset:128
v_add_f32_e32 v15, v15, v38
v_add_f32_e32 v15, v15, v28
Cv 38,38,28
Mp 216,20,60
ds_read_b128 v[148:151], v9 offset:256
v_add_f32_e32 v15, v15, v39
v_add_f32_e32 v15, v15, v29
Mp 208,20,56
buffer_load_dwordx4 v3, s[20:23], s55 offen offset:1024 sc1 lds
ds_read_b128 v[156:159], v9 offset:384
Mp 212,24,48
s_add_u32 s50, s50, 1024
s_min_u32 s50, s50, s60
Cv 192,192,193
Cv 39,39,29
Mp 220,24,52
Cv 193,194,195
Cv 194,196,197
s_sub_u32 s57, s57, 1
Mp 220,28,60
buffer_store_dwordx4 v[160:163], v3, s[20:23], s64 offen offset:0 sc0 sc1
Cv 195,198,199
Mp 212,28,56
Cv 200,200,201
v_mov_b32 v16, 0
)" R"(
s_waitcnt lgkmcnt(0)
s_barrier
Mp 224,32,48
Rb 0,1,32768
v_add_f32_e32 v132, v132, v140
v_mov_b32 v17, 0
Mp 232,32,52
Rb 4,1,33792
v_add_f32_e32 v133, v133, v141
Cv 201,202,203
Mp 232,36,60
Rb 8,1,34816
v_add_f32_e32 v134, v134, v142
v_mov_b32 v18, 0
v_mov_b32 v19, 0
Mp 224,36,56
Rb 12,1,35840
buffer_load_dwordx4 v3, s[20:23], s55 offen offset:2048 sc1 lds
Mp 228,40,48
Rb 16,1,40960
v_add_f32_e32 v135, v135, v143
buffer_store_dwordx4 v[168:171], v3, s[20:23], s64 offen offset:1024 sc0 sc1
Mp 236,40,52
Rb 20,1,41984
v_add_f32_e32 v148, v148, v156
Cv 202,204,205
Mp 236,44,60
Rb 24,1,43008
v_add_f32_e32 v149, v149, v157
v_mov_b32 v20, 0
v_mov_b32 v21, 0
Mp 228,44,56
Rb 28,1,44032
v_add_f32_e32 v150, v150, v158
Cv 203,206,207
Mp 240,48,48
Rb 32,1,36864
v_add_f32_e32 v151, v151, v159
Cv 208,208,209
Mp 248,48,52
Rb 36,1,37888
Cv 132,132,132
v_mov_b32 v22, 0
v_mov_b32 v23, 0
Mp 248,52,60
Rb 40,1,38912
v_lshlrev_b32 v132, 16, v132
buffer_store_dwordx4 v[176:179], v3, s[20:23], s64 offen offset:2048 sc0 sc1
Mp 240,52,56
Rb 44,1,39936
buffer_load_dwordx4 v3, s[20:23], s55 offen offset:3072 sc1 lds
Mp 244,56,48
Cv 148,148,148
v_lshlrev_b32 v148, 16, v148
Cv 209,210,211
Mp 252,56,52
Cv 133,133,133
v_lshlrev_b32 v133, 16, v133
v_accvgpr_mov_b32 a91, a251
Mp 252,60,60
Cv 149,149,149
Cv 210,212,213
s_waitcnt lgkmcnt(8)
Mp 244,60,56
v_lshlrev_b32 v149, 16, v149
)" R"(
Cv 134,134,134
v_lshlrev_b32 v134, 16, v134
Mz 96,0,224
Rb 48,1,45056
buffer_load_dword v4, v5, s[20:23], s50 offen offset:0 sc0 sc1
buffer_store_dwordx4 v[184:187], v3, s[20:23], s64 offen offset:3072 sc0 sc1
Mz 100,0,228
Cv 150,150,150
v_lshlrev_b32 v150, 16, v150
Cv 211,214,215
Mv 100,4,236
Rb 52,1,46080
s_add_u32 m0, s36, 65536
v_exp_f32_e32 v40, v80
Mv 96,4,232
buffer_load_dwordx4 v2, s[20:23], s56 offen offset:0 sc1 lds
Cv 216,216,217
v_add_f32_e32 v14, v14, v40
Mv 96,8,240
Rb 56,1,47104
Cv 135,135,135
v_accvgpr_mov_b32 a92, a252
Mv 100,8,244
v_lshlrev_b32 v135, 16, v135
Cv 151,151,151
Cv 217,218,219
Mv 100,12,252
Rb 60,1,48128
s_add_u32 s64, s64, 0x10000
buffer_store_dwordx4 v[192:195], v2, s[20:23], s64 offen offset:0 sc0 sc1
s_waitcnt lgkmcnt(8)
Mv 96,12,248
v_lshlrev_b32 v151, 16, v151
v_mul_f32_e32 v140, 0xbfb8aa3b, v132
Cv 218,220,221
Mv 96,16,192
Rb 0,1,49152
v_mul_f32_e32 v141, 0xbfb8aa3b, v133
Cv 219,222,223
Mv 100,16,196
v_exp_f32_e32 v140, v140
v_exp_f32_e32 v24, v81
Mv 100,20,204
Rb 4,1,50176
v_mul_f32_e32 v142, 0xbfb8aa3b, v134
Cv 224,224,225
v_add_f32_e32 v14, v14, v24
Mv 96,20,200
buffer_load_dwordx4 v2, s[20:23], s56 offen offset:1024 sc1 lds
v_exp_f32_e32 v41, v82
Mv 96,24,208
Rb 8,1,51200
v_mul_f32_e32 v143, 0xbfb8aa3b, v135
buffer_store_dwordx4 v[200:203], v2, s[20:23], s64 offen offset:1024 sc0 sc1
Mv 100,24,212
v_exp_f32_e32 v141, v141
v_exp_f32_e32 v25, v83
Mv 100,28,220
Rb 12,1,52224
Cv 225,226,227
Cv 40,40,24
s_waitcnt lgkmcnt(8)
Mv 96,28,216
v_exp_f32_e32 v142, v142
)" R"(
v_add_f32_e32 v140, 1.0, v140
v_add_f32_e32 v14, v14, v41
Mz 104,32,224
Rb 16,1,57344
v_add_f32_e32 v141, 1.0, v141
v_add_f32_e32 v14, v14, v25
Mz 108,32,228
v_exp_f32_e32 v143, v143
Cv 226,228,229
Cv 41,41,25
Mv 108,36,236
Rb 20,1,58368
v_add_f32_e32 v142, 1.0, v142
buffer_store_dwordx4 v[208:211], v2, s[20:23], s64 offen offset:2048 sc0 sc1
Mv 104,36,232
buffer_load_dwordx4 v2, s[20:23], s56 offen offset:2048 sc1 lds
Cv 227,230,231
Mv 104,40,240
Rb 24,1,59392
v_add_f32_e32 v143, 1.0, v143
v_accvgpr_mov_b32 a93, a253
Mv 108,40,244
v_rcp_f32_e32 v140, v140
Cv 232,232,233
Mv 108,44,252
Rb 28,1,60416
v_exp_f32_e32 v42, v88
s_waitcnt lgkmcnt(8)
Mv 104,44,248
v_rcp_f32_e32 v141, v141
Cv 233,234,235
v_add_f32_e32 v14, v14, v42
Mv 104,48,192
Rb 32,1,53248
v_mul_f32_e32 v132, v132, v140
buffer_store_dwordx4 v[216:219], v2, s[20:23], s64 offen offset:3072 sc0 sc1
Mv 108,48,196
v_rcp_f32_e32 v142, v142
v_exp_f32_e32 v26, v89
Mv 108,52,204
Rb 36,1,54272
v_mul_f32_e32 v133, v133, v141
Cv 234,236,237
v_add_f32_e32 v14, v14, v26
Mv 104,52,200
buffer_load_dwordx4 v2, s[20:23], s56 offen offset:3072 sc1 lds
v_exp_f32_e32 v43, v90
Mv 104,56,208
Rb 40,1,55296
v_mul_f32_e32 v134, v134, v142
Cv 235,238,239
Mv 108,56,212
v_rcp_f32_e32 v143, v143
v_exp_f32_e32 v27, v91
Mv 108,60,220
Rb 44,1,56320
buffer_store_dwordx4 v[224:227], v3, s[20:23], s64 offen offset:0 sc0 sc1
s_waitcnt lgkmcnt(8)
Mv 104,60,216
v_mul_f32_e32 v135, v135, v143
Cv 132,132,132
v_lshlrev_b32 v132, 16, v132
Cv 42,42,26
Mz 112,0,224
)" R"(
Rb 48,1,61440
Cv 133,133,133
v_add_f32_e32 v14, v14, v43
Mz 116,0,228
v_lshlrev_b32 v133, 16, v133
Cv 134,134,134
Cv 240,240,241
v_add_f32_e32 v14, v14, v27
Mv 116,4,236
Rb 52,1,62464
s_add_u32 m0, s36, 69632
v_exp_f32_e32 v44, v84
Mv 112,4,232
buffer_load_dwordx4 v3, s[20:23], s56 offen offset:0 sc1 lds
Cv 241,242,243
Cv 43,43,27
Mv 112,8,240
Rb 56,1,63488
v_lshlrev_b32 v134, 16, v134
buffer_store_dwordx4 v[232:235], v3, s[20:23], s64 offen offset:1024 sc0 sc1
Mv 116,8,244
Cv 135,135,135
v_lshlrev_b32 v135, 16, v135
Cv 242,244,245
v_add_f32_e32 v15, v15, v44
Mv 116,12,252
Rb 60,1,64512
v_exp_f32_e32 v28, v85
s_waitcnt lgkmcnt(8)
Mv 112,12,248
v_mul_f32_e32 v132, v132, v148
v_mul_f32_e32 v133, v133, v149
Cv 243,246,247
v_add_f32_e32 v15, v15, v28
Mv 112,16,192
v_mul_f32_e32 v134, v134, v150
v_mul_f32_e32 v135, v135, v151
Cv 248,248,249
Mv 116,16,196
Cv 140,132,133
Cv 141,134,135
v_exp_f32_e32 v45, v86
Mv 116,20,204
v_accvgpr_write_b32 a188, v140
v_accvgpr_write_b32 a189, v141
buffer_store_dwordx4 v[240:243], v3, s[20:23], s64 offen offset:2048 sc0 sc1
Mv 112,20,200
buffer_load_dwordx4 v3, s[20:23], s56 offen offset:1024 sc1 lds
v_exp_f32_e32 v29, v87
Mv 112,24,208
v_lshrrev_b32 v156, 2, v0
v_mul_u32_u24 v156, 864, v156
Cv 249,250,251
Mv 116,24,212
v_lshl_add_u32 v156, v0, 3, v156
s_mul_i32 s92, s33, 224
v_exp_f32_e32 v46, v92
Mv 116,28,220
s_add_u32 s92, s92, 131072
v_add_u32 v156, s92, v156
Cv 250,252,253
Cv 44,44,28
Mv 112,28,216
Cv 251,254,255
)" R"(
v_add_f32_e32 v15, v15, v45
s_waitcnt vmcnt(31) lgkmcnt(0)
s_barrier
Mz 120,32,224
Rb 0,8,32768
Rt 156,156,0
buffer_store_dwordx4 v[248:251], v3, s[20:23], s64 offen offset:3072 sc0 sc1
Mz 124,32,228
Rb 4,8,33792
Rt 158,156,32
v_exp_f32_e32 v24, v93
Mv 124,36,236
Rb 8,8,34816
Rt 160,156,64
v_exp_f32_e32 v47, v94
Mv 120,36,232
Rb 12,8,35840
buffer_load_dwordx4 v3, s[20:23], s56 offen offset:2048 sc1 lds
Rt 162,156,96
Mv 120,40,240
Rb 16,8,36864
Rt 164,156,128
v_add_f32_e32 v15, v15, v29
Mv 124,40,244
Rb 20,8,37888
Rt 166,156,160
v_exp_f32_e32 v25, v95
Mv 124,44,252
Rb 24,8,38912
Rt 168,156,192
Cv 45,45,29
v_add_f32_e32 v15, v15, v46
Mv 120,44,248
Rb 28,8,39936
v_add_f32_e32 v15, v15, v24
Cv 46,46,24
v_add_f32_e32 v15, v15, v47
Mv 120,48,192
Rb 32,8,40960
v_add_f32_e32 v15, v15, v25
Cv 47,47,25
Mv 124,48,196
Rb 36,8,41984
v_max_f32_e32 v11, v14, v15
v_mov_b32 v24, v14
v_mov_b32 v25, v15
Mv 124,52,204
Rb 40,8,43008
v_permlane32_swap_b32_e32 v24, v25
v_add_f32_e32 v24, v24, v25
v_mov_b32 v25, v24
.LzH38:
s_cbranch_execz .LzH20
Mv 120,52,200
Rb 44,8,44032
buffer_load_dwordx4 v3, s[20:23], s56 offen offset:3072 sc1 lds
v_permlane16_swap_b32_e32 v24, v25
Mv 120,56,208
v_add_f32_e32 v24, v24, v25
v_rcp_f32_e32 v24, v24
Mv 124,56,212
s_add_u32 s56, s56, 0x10000
v_max_f32_e32 v11, v11, v24
v_cmp_nge_f32_e32 vcc, s62, v11
s_add_u32 s55, s55, 0x10000
)" R"(
Mv 124,60,220
s_cmp_eq_u32 s57, 2
s_cselect_b32 s55, s59, s55
s_waitcnt lgkmcnt(12)
s_cbranch_vccnz .Lzfx4
.Lzfr4:
Mv 120,60,216
v_mov_b32 v12, v14
v_mov_b32 v13, v15
v_accvgpr_mov_b32 a94, a254
v_mfma_f32_16x16x32_bf16 v[128:131], a[0:3], v[32:35], 0
Rb 48,8,45056
v_accvgpr_mov_b32 a95, a255
v_mfma_f32_16x16x32_bf16 v[136:139], a[0:3], v[36:39], 0
v_accvgpr_mov_b32 a96, a192
s_cmp_le_i32 s57, s58
s_cbranch_scc1 .Lzmk5
.Lzmr5:
v_mfma_f32_16x16x32_bf16 v[140:143], a[4:7], v[36:39], 0
Rb 52,8,46080
s_add_u32 m0, s36, 32768
v_accvgpr_mov_b32 a97, a193
v_mfma_f32_16x16x32_bf16 v[132:135], a[4:7], v[32:35], 0
buffer_load_dwordx4 v2, s[20:23], s55 offen offset:0 sc1 lds
v_accvgpr_mov_b32 a98, a194
v_mfma_f32_16x16x32_bf16 v[144:147], a[8:11], v[32:35], 0
Rb 56,8,47104
v_and_b32 v11, 14, v0
v_accvgpr_mov_b32 a99, a195
v_mfma_f32_16x16x32_bf16 v[152:155], a[8:11], v[36:39], 0
v_min_u32 v11, 1, v11
v_lshlrev_b32 v11, 13, v11
v_accvgpr_mov_b32 a100, a196
v_mfma_f32_16x16x32_bf16 v[156:159], a[12:15], v[36:39], 0
Rb 60,8,48128
v_accvgpr_mov_b32 a101, a197
s_waitcnt lgkmcnt(8)
v_mfma_f32_16x16x32_bf16 v[148:151], a[12:15], v[32:35], 0
v_mfma_f32_16x16x16_bf16 a[128:131], a[156:157], a[188:189], 0
v_and_or_b32 v11, v0, 1, v11
v_mul_u32_u24 v11, 0x31000, v11
v_accvgpr_mov_b32 a102, a198
v_mfma_f32_16x16x32_bf16 v[160:163], a[16:19], v[32:35], 0
v_mfma_f32_16x16x16_bf16 a[132:135], a[158:159], a[188:189], 0
Rb 0,8,49152
v_and_or_b32 v11, v0, 48, v11
v_accvgpr_mov_b32 a103, a199
v_mfma_f32_16x16x32_bf16 v[168:171], a[16:19], v[36:39], 0
v_mfma_f32_16x16x16_bf16 a[136:139], a[160:161], a[188:189], 0
s_mul_i32 s83, s33, 50176
s_lshl_b32 s92, s82, 6
v_accvgpr_mov_b32 a104, a200
v_mfma_f32_16x16x32_bf16 v[172:175], a[20:23], v[36:39], 0
v_mfma_f32_16x16x16_bf16 a[140:143], a[162:163], a[188:189], 0
Rb 4,8,50176
s_add_u32 s83, s83, s92
v_accvgpr_mov_b32 a105, a201
v_mfma_f32_16x16x32_bf16 v[164:167], a[20:23], v[32:35], 0
v_mfma_f32_16x16x16_bf16 a[144:147], a[164:165], a[188:189], 0
buffer_load_dwordx4 v2, s[20:23], s55 offen offset:1024 sc1 lds
v_accvgpr_mov_b32 a106, a202
v_mfma_f32_16x16x32_bf16 v[176:179], a[24:27], v[32:35], 0
v_mfma_f32_16x16x16_bf16 a[148:151], a[166:167], a[188:189], 0
Rb 8,8,51200
)" R"(
s_add_u32 s83, s83, 0xc158000
v_accvgpr_mov_b32 a107, a203
v_mfma_f32_16x16x32_bf16 v[184:187], a[24:27], v[36:39], 0
v_mfma_f32_16x16x16_bf16 a[152:155], a[168:169], a[188:189], 0
v_exp_f32_e32 v48, v96
v_accvgpr_mov_b32 a108, a204
v_mfma_f32_16x16x32_bf16 v[188:191], a[28:31], v[36:39], 0
Rb 12,8,52224
v_accvgpr_mov_b32 a109, a205
s_waitcnt lgkmcnt(8)
v_mfma_f32_16x16x32_bf16 v[180:183], a[28:31], v[32:35], 0
v_exp_f32_e32 v26, v97
v_accvgpr_mov_b32 a110, a206
v_mfma_f32_16x16x32_bf16 v[192:195], a[32:35], v[32:35], 0
Rb 16,8,53248
v_add_f32_e32 v14, v48, v26
v_mfma_f32_16x16x32_bf16 v[200:203], a[32:35], v[36:39], 0
v_exp_f32_e32 v49, v98
Cv 48,48,26
v_mfma_f32_16x16x32_bf16 v[204:207], a[36:39], v[36:39], 0
Rb 20,8,54272
v_add_f32_e32 v14, v14, v49
v_accvgpr_mov_b32 a111, a207
v_mfma_f32_16x16x32_bf16 v[196:199], a[36:39], v[32:35], 0
buffer_load_dwordx4 v2, s[20:23], s55 offen offset:2048 sc1 lds
v_accvgpr_mov_b32 a112, a208
v_mfma_f32_16x16x32_bf16 v[208:211], a[40:43], v[32:35], 0
Rb 24,8,55296
v_exp_f32_e32 v27, v99
v_mfma_f32_16x16x32_bf16 v[216:219], a[40:43], v[36:39], 0
buffer_store_dwordx4 a[128:131], v11, s[20:23], s83 offen sc0 sc1
v_accvgpr_mov_b32 a113, a209
v_mfma_f32_16x16x32_bf16 v[220:223], a[44:47], v[36:39], 0
Rb 28,8,56320
v_accvgpr_mov_b32 a114, a210
s_waitcnt lgkmcnt(8)
v_mfma_f32_16x16x32_bf16 v[212:215], a[44:47], v[32:35], 0
s_add_u32 s83, s83, 7168
v_exp_f32_e32 v50, v104
v_mfma_f32_16x16x32_bf16 v[224:227], a[48:51], v[32:35], 0
Rb 32,8,57344
v_add_f32_e32 v14, v14, v27
v_accvgpr_mov_b32 a115, a211
v_mfma_f32_16x16x32_bf16 v[232:235], a[48:51], v[36:39], 0
v_exp_f32_e32 v28, v105
Cv 49,49,27
v_mfma_f32_16x16x32_bf16 v[236:239], a[52:55], v[36:39], 0
Rb 36,8,58368
v_add_f32_e32 v14, v14, v50
v_accvgpr_mov_b32 a116, a212
v_mfma_f32_16x16x32_bf16 v[228:231], a[52:55], v[32:35], 0
buffer_load_dwordx4 v2, s[20:23], s55 offen offset:3072 sc1 lds
v_accvgpr_mov_b32 a117, a213
v_mfma_f32_16x16x32_bf16 v[240:243], a[56:59], v[32:35], 0
Rb 40,8,59392
v_exp_f32_e32 v51, v106
v_mfma_f32_16x16x32_bf16 v[248:251], a[56:59], v[36:39], 0
buffer_store_dwordx4 a[132:135], v11, s[20:23], s83 offen sc0 sc1
v_accvgpr_mov_b32 a118, a214
v_mfma_f32_16x16x32_bf16 v[252:255], a[60:63], v[36:39], 0
Rb 44,8,60416
v_accvgpr_mov_b32 a119, a215
s_waitcnt lgkmcnt(8)
v_mfma_f32_16x16x32_bf16 v[244:247], a[60:63], v[32:35], 0
)" R"(
s_add_u32 s83, s83, 7168
v_exp_f32_e32 v29, v107
Mp 128,0,40
Rb 48,8,61440
v_add_f32_e32 v14, v14, v28
Mp 136,0,44
buffer_store_dwordx4 a[136:139], v11, s[20:23], s83 offen sc0 sc1
s_add_u32 s83, s83, 7168
Mp 140,4,44
Rb 52,8,62464
s_add_u32 m0, s36, 36864
v_accvgpr_mov_b32 a120, a216
Mp 132,4,40
buffer_load_dwordx4 v3, s[20:23], s55 offen offset:0 sc1 lds
v_accvgpr_mov_b32 a121, a217
Mp 144,8,40
Rb 56,8,63488
v_exp_f32_e32 v52, v100
Mp 152,8,44
v_exp_f32_e32 v24, v101
v_accvgpr_mov_b32 a122, a218
Mp 156,12,44
Rb 60,8,64512
v_accvgpr_mov_b32 a123, a219
s_waitcnt lgkmcnt(8)
Mp 148,12,40
buffer_store_dwordx4 a[140:143], v11, s[20:23], s83 offen sc0 sc1
Cv 50,50,28
Mp 160,16,40
s_add_u32 s83, s83, 7168
v_add_f32_e32 v14, v14, v51
v_accvgpr_mov_b32 a124, a220
Mp 168,16,44
v_exp_f32_e32 v53, v102
v_add_f32_e32 v14, v14, v29
Mp 172,20,44
v_exp_f32_e32 v25, v103
v_accvgpr_mov_b32 a125, a221
Mp 164,20,40
buffer_load_dwordx4 v3, s[20:23], s55 offen offset:1024 sc1 lds
v_accvgpr_mov_b32 a126, a222
Mp 176,24,40
v_exp_f32_e32 v54, v108
Cv 51,51,29
Mp 184,24,44
buffer_store_dwordx4 a[144:147], v11, s[20:23], s83 offen sc0 sc1
v_accvgpr_mov_b32 a127, a223
Mp 188,28,44
s_add_u32 s83, s83, 7168
v_add_f32_e32 v15, v52, v24
Mp 180,28,40
Cv 52,52,24
s_waitcnt vmcnt(13) lgkmcnt(0)
s_barrier
buffer_store_dword v6, v7, s[20:23], s65 offen offset:0 sc0 sc1
Mp 192,32,40
Rb 0,1,0
v_add_f32_e32 v15, v15, v53
Mp 200,32,44
Rb 4,1,1024
v_exp_f32_e32 v26, v109
Mp 204,36,44
Rb 8,1,2048
v_add_f32_e32 v15, v15, v25
Mp 196,36,40
)" R"(
Rb 12,1,3072
buffer_load_dwordx4 v3, s[20:23], s55 offen offset:2048 sc1 lds
Mp 208,40,40
Rb 16,1,8192
v_exp_f32_e32 v55, v110
Mp 216,40,44
Rb 20,1,9216
Cv 53,53,25
Mp 220,44,44
Rb 24,1,10240
buffer_store_dwordx4 a[148:151], v11, s[20:23], s83 offen sc0 sc1
Mp 212,44,40
Rb 28,1,11264
s_add_u32 s83, s83, 7168
v_add_f32_e32 v15, v15, v54
Mp 224,48,40
Rb 32,1,4096
v_add_f32_e32 v15, v15, v26
Mp 232,48,44
Rb 36,1,5120
v_exp_f32_e32 v27, v111
Mp 236,52,44
Rb 40,1,6144
Cv 54,54,26
Mp 228,52,40
Rb 44,1,7168
buffer_load_dwordx4 v3, s[20:23], s55 offen offset:3072 sc1 lds
Mp 240,56,40
v_cmp_eq_u32_e32 vcc, s3, v4
s_andn2_b64 s[28:29], -1, vcc
s_cbranch_scc1 .Lzsp10
.Lzok10:
Mp 248,56,44
buffer_store_dwordx4 a[152:155], v11, s[20:23], s83 offen sc0 sc1
Mp 252,60,44
v_add_f32_e32 v15, v15, v55
s_waitcnt lgkmcnt(8)
Mp 244,60,40
v_add_f32_e32 v15, v15, v27
Cv 55,55,27
s_sub_u32 s57, s57, 1
s_cmp_gt_i32 s57, s93
s_cbranch_scc1 .LzL
.LzH39:
s_cbranch_execz .LzHR
.LzDA:
s_waitcnt vmcnt(8) lgkmcnt(0)
s_barrier
Rb 0,8,0
Rb 4,8,16384
Rb 8,8,1024
Rb 12,8,17408
Rb 16,8,2048
Rb 20,8,18432
Rb 24,8,3072
Rb 28,8,19456
Rb 32,8,4096
Rb 36,8,20480
Rb 40,8,5120
Rb 44,8,21504
v_exp_f32_e32 v56, v112
v_exp_f32_e32 v28, v113
v_add_f32_e32 v14, v14, v56
v_add_f32_e32 v14, v14, v28
Cv 56,56,28
)" R"(
v_exp_f32_e32 v57, v114
v_exp_f32_e32 v29, v115
v_add_f32_e32 v14, v14, v57
v_add_f32_e32 v14, v14, v29
Cv 57,57,29
v_exp_f32_e32 v58, v120
v_exp_f32_e32 v24, v121
v_add_f32_e32 v14, v14, v58
v_add_f32_e32 v14, v14, v24
Cv 58,58,24
v_exp_f32_e32 v59, v122
v_exp_f32_e32 v25, v123
v_add_f32_e32 v14, v14, v59
v_add_f32_e32 v14, v14, v25
Cv 59,59,25
v_exp_f32_e32 v60, v116
v_exp_f32_e32 v26, v117
v_add_f32_e32 v15, v15, v60
v_add_f32_e32 v15, v15, v26
Cv 60,60,26
v_exp_f32_e32 v61, v118
v_exp_f32_e32 v27, v119
v_add_f32_e32 v15, v15, v61
v_add_f32_e32 v15, v15, v27
Cv 61,61,27
v_exp_f32_e32 v62, v124
v_exp_f32_e32 v28, v125
v_add_f32_e32 v15, v15, v62
v_add_f32_e32 v15, v15, v28
Cv 62,62,28
v_exp_f32_e32 v63, v126
v_exp_f32_e32 v29, v127
v_add_f32_e32 v15, v15, v63
v_add_f32_e32 v15, v15, v29
Cv 63,63,29
v_max_f32_e32 v11, v14, v15
v_cmp_nge_f32_e32 vcc, s62, v11
s_cbranch_vccnz .Lzfx5
.Lzfr5:
s_waitcnt lgkmcnt(8)
Mp 128,0,48
Rb 48,8,6144
s_add_u32 m0, s36, 131072
s_add_u32 s40, s67, 0x20000
s_lshl_b32 s82, s82, 7
Mp 136,0,52
buffer_load_dwordx4 v2, s[20:23], s40 offen offset:0 sc1 lds
v_add_f32_e32 v12, v12, v14
s_add_u32 s82, s82, 0xc10c000
Mp 136,4,60
Rb 52,8,22528
v_add_f32_e32 v13, v13, v15
v_mov_b32 v24, v12
Mp 128,4,56
buffer_load_dwordx4 v2, s[20:23], s40 offen offset:1024 sc1 lds
v_permlane32_swap_b32_e32 v12, v24
Mp 132,8,48
Rb 56,8,7168
v_add_f32_e32 v12, v12, v24
v_mov_b32 v24, v12
Mp 140,8,52
s_nop 0
v_permlane16_swap_b32_e32 v12, v24
v_add_f32_e32 v12, v12, v24
)" R"(
v_mov_b32 v25, v13
Mp 140,12,60
Rb 60,8,23552
v_permlane32_swap_b32_e32 v13, v25
s_waitcnt lgkmcnt(8)
Mp 132,12,56
buffer_load_dwordx4 v2, s[20:23], s40 offen offset:2048 sc1 lds
v_add_f32_e32 v13, v13, v25
Mp 144,16,48
Rb 0,8,8192
v_rcp_f32_e32 v12, v12
Mp 152,16,52
buffer_load_dwordx4 v2, s[20:23], s40 offen offset:3072 sc1 lds
v_mov_b32 v25, v13
Mp 152,20,60
Rb 4,8,24576
v_permlane16_swap_b32_e32 v13, v25
v_add_f32_e32 v13, v13, v25
Mp 144,20,56
v_rcp_f32_e32 v13, v13
v_mul_f32_e32 v128, v12, v128
Mp 148,24,48
Rb 8,8,9216
s_add_u32 m0, s36, 135168
v_mul_f32_e32 v129, v12, v129
Mp 156,24,52
buffer_load_dwordx4 v3, s[20:23], s40 offen offset:0 sc1 lds
Cv 128,128,129
Mp 156,28,60
Rb 12,8,25600
v_mul_f32_e32 v130, v12, v130
s_waitcnt lgkmcnt(8)
Mp 148,28,56
buffer_load_dwordx4 v3, s[20:23], s40 offen offset:1024 sc1 lds
v_mul_f32_e32 v131, v12, v131
Mp 160,32,48
Rb 16,8,10240
Cv 129,130,131
s_cmp_lg_u32 s33, 0
s_cmov_b32 s82, 0x40000000
Mp 168,32,52
v_mul_f32_e32 v132, v12, v132
v_mul_f32_e32 v133, v12, v133
Cv 130,132,133
Mp 168,36,60
Rb 20,8,26624
v_mul_f32_e32 v134, v12, v134
v_mul_f32_e32 v135, v12, v135
Mp 160,36,56
buffer_load_dwordx4 v3, s[20:23], s40 offen offset:2048 sc1 lds
Cv 131,134,135
Mp 164,40,48
Rb 24,8,11264
s_add_u32 m0, s36, 98304
s_add_u32 s40, s67, 0x30000
Mp 172,40,52
buffer_load_dwordx4 v2, s[20:23], s40 offen offset:0 sc1 lds
v_mul_f32_e32 v136, v13, v136
Mp 172,44,60
Rb 28,8,27648
v_mul_f32_e32 v137, v13, v137
s_waitcnt lgkmcnt(8)
Mp 164,44,56
Cv 136,136,137
)" R"(
v_mul_f32_e32 v138, v13, v138
v_mul_f32_e32 v139, v13, v139
Mp 176,48,48
Rb 32,8,12288
Cv 137,138,139
v_mul_f32_e32 v140, v13, v140
Mp 184,48,52
buffer_load_dwordx4 v2, s[20:23], s40 offen offset:1024 sc1 lds
v_mul_f32_e32 v141, v13, v141
Mp 184,52,60
Rb 36,8,28672
Cv 138,140,141
v_mul_f32_e32 v142, v13, v142
Mp 176,52,56
buffer_load_dwordx4 v2, s[20:23], s40 offen offset:2048 sc1 lds
v_mul_f32_e32 v143, v13, v143
Mp 180,56,48
Rb 40,8,13312
Cv 139,142,143
v_mul_f32_e32 v144, v12, v144
Mp 188,56,52
v_mul_f32_e32 v145, v12, v145
Cv 144,144,145
v_mul_f32_e32 v146, v12, v146
Mp 188,60,60
Rb 44,8,29696
v_mul_f32_e32 v147, v12, v147
s_waitcnt lgkmcnt(8)
Mp 180,60,56
buffer_load_dwordx4 v2, s[20:23], s40 offen offset:3072 sc1 lds
Cv 145,146,147
Mp 192,0,48
Rb 48,8,14336
s_add_u32 m0, s36, 102400
s_cmp_eq_u32 s35, 7
s_cmov_b32 s82, 0x40000000
Mp 200,0,52
buffer_load_dwordx4 v3, s[20:23], s40 offen offset:0 sc1 lds
v_mul_f32_e32 v148, v12, v148
Mp 200,4,60
Rb 52,8,30720
v_mul_f32_e32 v149, v12, v149
Cv 146,148,149
Mp 192,4,56
v_mul_f32_e32 v150, v12, v150
v_mul_f32_e32 v151, v12, v151
Cv 147,150,151
Mp 196,8,48
Rb 56,8,15360
v_mul_f32_e32 v152, v13, v152
v_mul_f32_e32 v153, v13, v153
Mp 204,8,52
buffer_load_dwordx4 v3, s[20:23], s40 offen offset:1024 sc1 lds
Cv 152,152,153
Mp 204,12,60
Rb 60,8,31744
v_mul_f32_e32 v154, v13, v154
s_waitcnt lgkmcnt(8)
Mp 196,12,56
buffer_load_dwordx4 v3, s[20:23], s40 offen offset:2048 sc1 lds
v_mul_f32_e32 v155, v13, v155
Mp 208,16,48
Rb 0,1,0
Cv 153,154,155
)" R"(
v_mul_f32_e32 v156, v13, v156
Mp 216,16,52
v_mul_f32_e32 v157, v13, v157
Cv 154,156,157
v_mul_f32_e32 v158, v13, v158
Mp 216,20,60
Rb 4,1,1024
v_mul_f32_e32 v159, v13, v159
Cv 155,158,159
Mp 208,20,56
v_mul_f32_e32 v160, v12, v160
v_mul_f32_e32 v161, v12, v161
Cv 160,160,161
Mp 212,24,48
Rb 8,1,2048
v_mul_f32_e32 v162, v12, v162
v_mul_f32_e32 v163, v12, v163
Mp 220,24,52
Cv 161,162,163
v_mul_f32_e32 v164, v12, v164
v_mul_f32_e32 v165, v12, v165
Mp 220,28,60
Rb 12,1,3072
s_waitcnt vmcnt(14) lgkmcnt(4)
s_barrier
Mp 212,28,56
s_add_u32 s40, s67, 0x40000
Cv 162,164,165
v_mul_f32_e32 v166, v12, v166
Mp 224,32,48
Rb 16,1,4096
s_add_u32 m0, s36, 65536
Mp 232,32,52
buffer_load_dwordx4 v2, s[20:23], s40 offen offset:0 sc1 lds
v_mul_f32_e32 v167, v12, v167
Mp 232,36,60
Rb 20,1,5120
Cv 163,166,167
v_mul_f32_e32 v168, v13, v168
Mp 224,36,56
v_mul_f32_e32 v169, v13, v169
Cv 168,168,169
v_mul_f32_e32 v170, v13, v170
Mp 228,40,48
Rb 24,1,6144
v_mul_f32_e32 v171, v13, v171
Cv 169,170,171
Mp 236,40,52
v_mul_f32_e32 v172, v13, v172
v_mul_f32_e32 v173, v13, v173
Cv 170,172,173
Mp 236,44,60
Rb 28,1,8192
v_mul_f32_e32 v174, v13, v174
Mp 228,44,56
buffer_load_dwordx4 v2, s[20:23], s40 offen offset:1024 sc1 lds
v_mul_f32_e32 v175, v13, v175
s_lshl4_add_u32 s28, s34, s35
Mp 240,48,48
Rb 32,1,9216
Cv 171,174,175
v_mul_f32_e32 v176, v12, v176
Mp 248,48,52
v_mul_f32_e32 v177, v12, v177
Cv 176,176,177
)" R"(
v_mul_f32_e32 v178, v12, v178
Mp 248,52,60
Rb 36,1,10240
v_mul_f32_e32 v179, v12, v179
Cv 177,178,179
Mp 240,52,56
v_mul_f32_e32 v180, v12, v180
v_mul_f32_e32 v181, v12, v181
Cv 178,180,181
Mp 244,56,48
Rb 40,1,11264
v_mul_f32_e32 v182, v12, v182
v_mul_f32_e32 v183, v12, v183
s_lshl_b32 s64, s28, 17
Mp 252,56,52
buffer_load_dwordx4 v2, s[20:23], s40 offen offset:2048 sc1 lds
Cv 179,182,183
Mp 252,60,60
Rb 44,1,12288
v_mul_f32_e32 v184, v13, v184
s_mov_b32 s55, 0xc108c00
s_waitcnt lgkmcnt(8)
Mp 244,60,56
v_mul_f32_e32 v185, v13, v185
Cv 184,184,185
v_mul_f32_e32 v186, v13, v186
v_mfma_f32_16x16x32_bf16 v[32:35], a[0:3], v[128:131], 0
Rb 48,1,13312
v_mul_f32_e32 v187, v13, v187
s_add_u32 s64, s64, 0x4000000
v_mfma_f32_16x16x32_bf16 v[40:43], a[0:3], v[136:139], 0
Cv 185,186,187
v_mul_f32_e32 v188, v13, v188
v_mul_f32_e32 v189, v13, v189
s_xor_b32 s66, s64, 0x202000
v_mfma_f32_16x16x32_bf16 v[44:47], a[4:7], v[136:139], 0
Rb 52,1,14336
Cv 186,188,189
v_mul_f32_e32 v190, v13, v190
v_mfma_f32_16x16x32_bf16 v[36:39], a[4:7], v[128:131], 0
buffer_load_dwordx4 v2, s[20:23], s40 offen offset:3072 sc1 lds
v_mul_f32_e32 v191, v13, v191
v_mfma_f32_16x16x32_bf16 v[48:51], a[8:11], v[128:131], 0
Rb 56,1,16384
Cv 187,190,191
v_mul_f32_e32 v192, v12, v192
s_cmp_lg_u32 s33, 0
s_cmov_b32 s55, 0x40000000
v_mfma_f32_16x16x32_bf16 v[56:59], a[8:11], v[136:139], 0
v_mul_f32_e32 v193, v12, v193
Cv 192,192,193
v_mul_f32_e32 v194, v12, v194
v_mov_b32 v16, 1
v_mfma_f32_16x16x32_bf16 v[60:63], a[12:15], v[136:139], 0
Rb 60,1,17408
v_mul_f32_e32 v195, v12, v195
v_add_u32 v9, s36, v8
s_waitcnt lgkmcnt(8)
v_mfma_f32_16x16x32_bf16 v[52:55], a[12:15], v[128:131], 0
Cv 193,194,195
v_mul_f32_e32 v196, v12, v196
v_mul_f32_e32 v197, v12, v197
s_sub_u32 s66, s66, s84
Mp 32,16,144
)" R"(
Rb 0,1,18432
s_add_u32 m0, s36, 69632
Cv 194,196,197
Mp 40,16,152
buffer_load_dwordx4 v3, s[20:23], s40 offen offset:0 sc1 lds
v_mul_f32_e32 v198, v12, v198
Mp 44,20,152
Rb 4,1,19456
v_mul_f32_e32 v199, v12, v199
Cv 195,198,199
Mp 36,20,144
v_mul_f32_e32 v200, v13, v200
v_mul_f32_e32 v201, v13, v201
Cv 200,200,201
Mp 48,24,144
Rb 8,1,20480
v_mul_f32_e32 v202, v13, v202
v_mul_f32_e32 v203, v13, v203
s_sub_u32 s66, s66, s84
Mp 56,24,152
Cv 201,202,203
v_mul_f32_e32 v204, v13, v204
v_mul_f32_e32 v205, v13, v205
Mp 60,28,152
Rb 12,1,21504
Cv 202,204,205
s_waitcnt lgkmcnt(8)
Mp 52,28,144
buffer_load_dwordx4 v3, s[20:23], s40 offen offset:1024 sc1 lds
v_mul_f32_e32 v206, v13, v206
Mp 32,32,160
Rb 16,1,22528
v_mul_f32_e32 v207, v13, v207
s_lshl2_add_u32 s65, s35, s33
Mp 40,32,168
Cv 203,206,207
v_mul_f32_e32 v208, v12, v208
v_mul_f32_e32 v209, v12, v209
s_lshl_b32 s65, s65, 8
Mp 44,36,168
Rb 20,1,24576
Cv 208,208,209
v_mul_f32_e32 v210, v12, v210
Mp 36,36,160
v_mul_f32_e32 v211, v12, v211
Cv 209,210,211
v_mul_f32_e32 v212, v12, v212
Mp 48,40,160
Rb 24,1,25600
v_mul_f32_e32 v213, v12, v213
Cv 210,212,213
Mp 56,40,168
buffer_load_dwordx4 v3, s[20:23], s40 offen offset:2048 sc1 lds
v_mul_f32_e32 v214, v12, v214
Mp 60,44,168
Rb 28,1,26624
v_mul_f32_e32 v215, v12, v215
s_waitcnt lgkmcnt(8)
Mp 52,44,160
Cv 211,214,215
v_mul_f32_e32 v216, v13, v216
v_mul_f32_e32 v217, v13, v217
s_add_u32 s65, s65, s50
.LzH40:
)" R"(
s_cbranch_execz .LzHR
Mp 32,48,176
Rb 32,1,27648
Cv 216,216,217
v_mul_f32_e32 v218, v13, v218
Mp 40,48,184
v_mul_f32_e32 v219, v13, v219
Cv 217,218,219
v_mul_f32_e32 v220, v13, v220
Mp 44,52,184
Rb 36,1,28672
v_mul_f32_e32 v221, v13, v221
Cv 218,220,221
Mp 36,52,176
v_mul_f32_e32 v222, v13, v222
v_mul_f32_e32 v223, v13, v223
Cv 219,222,223
Mp 48,56,176
Rb 40,1,29696
v_mul_f32_e32 v224, v12, v224
v_mul_f32_e32 v225, v12, v225
s_add_u32 s65, s65, 0x2400
Mp 56,56,184
Cv 224,224,225
v_mul_f32_e32 v226, v12, v226
v_mul_f32_e32 v227, v12, v227
Mp 60,60,184
Rb 44,1,30720
Cv 225,226,227
buffer_atomic_add v16, v7, s[20:23], s55 offen sc0 sc1
s_waitcnt lgkmcnt(8)
Mp 52,60,176
v_mul_f32_e32 v228, v12, v228
v_mul_f32_e32 v229, v12, v229
Cv 226,228,229
Mp 32,0,192
Rb 48,1,32768
v_mul_f32_e32 v230, v12, v230
s_xor_b32 s52, s65, 0x202000
Mp 40,0,200
v_mul_f32_e32 v231, v12, v231
Cv 227,230,231
v_mul_f32_e32 v232, v13, v232
Mp 44,4,200
Rb 52,1,33792
v_mul_f32_e32 v233, v13, v233
Cv 232,232,233
Mp 36,4,192
v_mul_f32_e32 v234, v13, v234
v_mul_f32_e32 v235, v13, v235
Cv 233,234,235
Mp 48,8,192
Rb 56,1,34816
v_mul_f32_e32 v236, v13, v236
v_mul_f32_e32 v237, v13, v237
Mp 56,8,200
Cv 234,236,237
v_mul_f32_e32 v238, v13, v238
v_mul_f32_e32 v239, v13, v239
Mp 60,12,200
Rb 60,1,35840
s_waitcnt vmcnt(15) lgkmcnt(4)
s_barrier
buffer_store_dword v6, v7, s[20:23], s82 offen offset:0 sc0 sc1
Mp 52,12,192
)" R"(
s_add_u32 s40, s67, 0x50000
Cv 235,238,239
v_mul_f32_e32 v240, v12, v240
Mp 32,16,208
Rb 0,1,36864
s_add_u32 m0, s36, 0
v_mul_f32_e32 v241, v12, v241
Mp 40,16,216
buffer_load_dwordx4 v2, s[20:23], s40 offen offset:0 sc1 lds
Cv 240,240,241
Mp 44,20,216
Rb 4,1,37888
v_mul_f32_e32 v242, v12, v242
v_mul_f32_e32 v243, v12, v243
Mp 36,20,208
Cv 241,242,243
v_mul_f32_e32 v244, v12, v244
v_mul_f32_e32 v245, v12, v245
Mp 48,24,208
Rb 8,1,38912
Cv 242,244,245
v_mul_f32_e32 v246, v12, v246
Mp 56,24,216
v_mul_f32_e32 v247, v12, v247
Cv 243,246,247
v_mul_f32_e32 v248, v13, v248
Mp 60,28,216
Rb 12,1,40960
v_mul_f32_e32 v249, v13, v249
Mp 52,28,208
buffer_load_dwordx4 v2, s[20:23], s40 offen offset:1024 sc1 lds
Cv 248,248,249
Mp 32,32,224
Rb 16,1,41984
v_mul_f32_e32 v250, v13, v250
Mp 40,32,232
v_mul_f32_e32 v251, v13, v251
Cv 249,250,251
v_mul_f32_e32 v252, v13, v252
Mp 44,36,232
Rb 20,1,43008
v_mul_f32_e32 v253, v13, v253
Cv 250,252,253
Mp 36,36,224
v_mul_f32_e32 v254, v13, v254
v_mul_f32_e32 v255, v13, v255
Cv 251,254,255
Mp 48,40,224
Rb 24,1,44032
Mp 56,40,232
buffer_load_dwordx4 v2, s[20:23], s40 offen offset:2048 sc1 lds
Mp 60,44,232
Rb 28,1,45056
s_waitcnt lgkmcnt(8)
Mp 52,44,224
Mp 32,48,240
Rb 32,1,46080
Mp 40,48,248
Mp 44,52,248
Rb 36,1,47104
Mp 36,52,240
buffer_load_dwordx4 v2, s[20:23], s40 offen offset:3072 sc1 lds
Mp 48,56,240
Rb 40,1,49152
)" R"(
Mp 56,56,248
Mp 60,60,248
Rb 44,1,50176
s_waitcnt lgkmcnt(8)
Mp 52,60,240
v_mfma_f32_16x16x32_bf16 v[64:67], a[0:3], v[128:131], 0
Rb 48,1,51200
s_add_u32 m0, s36, 4096
v_mfma_f32_16x16x32_bf16 v[72:75], a[0:3], v[136:139], 0
buffer_load_dwordx4 v3, s[20:23], s40 offen offset:0 sc1 lds
Mp 72,4,152
Rb 52,1,52224
Mp 64,4,144
Cv 32,32,33
Cv 33,34,35
Mp 64,8,160
Rb 56,1,53248
Cv 34,36,37
Mp 72,8,168
Cv 35,38,39
Mp 72,12,184
Rb 60,1,54272
s_waitcnt lgkmcnt(8)
Mp 64,12,176
buffer_load_dwordx4 v3, s[20:23], s40 offen offset:1024 sc1 lds
Mp 64,16,192
Rb 0,1,55296
Mp 72,16,200
buffer_store_dwordx4 v[32:35], v2, s[20:23], s64 offen offset:0 sc0 sc1
Mp 72,20,216
Rb 4,1,57344
Mp 64,20,208
Mp 64,24,224
Rb 8,1,58368
Mp 72,24,232
buffer_load_dwordx4 v3, s[20:23], s40 offen offset:2048 sc1 lds
Mp 72,28,248
Rb 12,1,59392
s_waitcnt lgkmcnt(8)
Mp 64,28,240
Cv 40,40,41
Cv 41,42,43
v_mfma_f32_16x16x32_bf16 v[68:71], a[32:35], v[128:131], 0
Rb 16,1,60416
Cv 42,44,45
v_mfma_f32_16x16x32_bf16 v[76:79], a[32:35], v[136:139], 0
Cv 43,46,47
Mp 76,36,152
Rb 20,1,61440
Mp 68,36,144
buffer_store_dwordx4 v[40:43], v2, s[20:23], s64 offen offset:1024 sc0 sc1
Mp 68,40,160
Rb 24,1,62464
Mp 76,40,168
Mp 76,44,184
Rb 28,1,63488
s_waitcnt lgkmcnt(8)
Mp 68,44,176
Mp 68,48,192
Rb 32,10,0
Mp 76,48,200
Cv 48,48,49
Cv 49,50,51
Mp 76,52,216
)" R"(
Rb 36,10,1024
Cv 50,52,53
Mp 68,52,208
Cv 51,54,55
Mp 68,56,224
Rb 40,10,2048
Mp 76,56,232
buffer_store_dwordx4 v[48:51], v2, s[20:23], s64 offen offset:2048 sc0 sc1
Mp 76,60,248
Rb 44,10,3072
s_waitcnt vmcnt(19) lgkmcnt(4)
s_barrier
Mp 68,60,240
s_add_u32 s40, s67, 0x60000
v_mfma_f32_16x16x32_bf16 v[80:83], a[0:3], v[128:131], 0
Rb 48,10,4096
s_add_u32 m0, s36, 32768
v_mfma_f32_16x16x32_bf16 v[88:91], a[0:3], v[136:139], 0
buffer_load_dwordx4 v2, s[20:23], s40 offen offset:0 sc1 lds
Mp 88,4,152
Rb 52,10,5120
Mp 80,4,144
Cv 56,56,57
Cv 57,58,59
Cv 58,60,61
Mp 80,8,160
Rb 56,10,6144
v_bfe_u32 v11, v0, 2, 2
Cv 59,62,63
Mp 88,8,168
v_lshrrev_b32 v4, 4, v0
v_xor_b32 v11, v11, v4
Mp 88,12,184
Rb 60,10,8192
Mp 80,12,176
buffer_load_dwordx4 v2, s[20:23], s40 offen offset:1024 sc1 lds
Mp 80,16,192
Rb 0,10,9216
v_bfe_u32 v4, v0, 1, 1
Mp 88,16,200
v_lshl_or_b32 v11, v11, 1, v4
v_and_b32 v4, 1, v0
Mp 88,20,216
Rb 4,10,10240
v_mul_u32_u24 v4, 0xffff, v4
buffer_store_dwordx4 v[56:59], v2, s[20:23], s64 offen offset:3072 sc0 sc1
Mp 80,20,208
v_add_u32 v4, 1, v4
v_mul_u32_u24 v4, 0x3f80, v4
Mp 80,24,224
Rb 8,10,11264
v_cmp_eq_u32_e32 vcc, 0, v11
Mp 88,24,232
buffer_load_dwordx4 v2, s[20:23], s40 offen offset:2048 sc1 lds
Mp 88,28,248
Rb 12,10,12288
s_waitcnt lgkmcnt(8)
Mp 80,28,240
v_cmp_eq_u32_e64 s[28:29], 1, v11
v_accvgpr_write_b32 a192, 0
v_mfma_f32_16x16x32_bf16 v[84:87], a[32:35], v[128:131], 0
Rb 16,10,13312
v_accvgpr_write_b32 a193, 0
v_mfma_f32_16x16x32_bf16 v[92:95], a[32:35], v[136:139], 0
v_accvgpr_write_b32 a196, 0
)" R"(
v_accvgpr_write_b32 a197, 0
Cv 64,64,65
Mp 92,36,152
Rb 20,10,14336
v_cndmask_b32_e32 v11, 0, v4, vcc
Mp 84,36,144
buffer_load_dwordx4 v2, s[20:23], s40 offen offset:3072 sc1 lds
Cv 65,66,67
Mp 84,40,160
Rb 24,10,16384
v_cndmask_b32_e64 v4, 0, v4, s[28:29]
Mp 92,40,168
v_accvgpr_write_b32 a194, v11
v_accvgpr_write_b32 a195, v4
Cv 66,68,69
Mp 92,44,184
Rb 28,10,17408
s_waitcnt lgkmcnt(8)
Mp 84,44,176
Cv 67,70,71
buffer_store_dwordx4 v[64:67], v3, s[20:23], s64 offen offset:0 sc0 sc1
Mp 84,48,192
Rb 32,10,18432
s_add_u32 m0, s36, 36864
Mp 92,48,200
buffer_load_dwordx4 v3, s[20:23], s40 offen offset:0 sc1 lds
Mp 92,52,216
Rb 36,10,19456
Mp 84,52,208
Mp 84,56,224
Rb 40,10,20480
Mp 92,56,232
Cv 72,72,73
Cv 73,74,75
Mp 92,60,248
Rb 44,10,21504
s_waitcnt lgkmcnt(8)
Mp 84,60,240
buffer_load_dwordx4 v3, s[20:23], s40 offen offset:1024 sc1 lds
v_mfma_f32_16x16x32_bf16 v[96:99], a[0:3], v[128:131], 0
Rb 48,10,22528
Cv 74,76,77
v_mfma_f32_16x16x32_bf16 v[104:107], a[0:3], v[136:139], 0
Cv 75,78,79
Mp 104,4,152
Rb 52,10,24576
Mp 96,4,144
buffer_store_dwordx4 v[72:75], v3, s[20:23], s64 offen offset:1024 sc0 sc1
Mp 96,8,160
Rb 56,10,25600
Mp 104,8,168
buffer_load_dwordx4 v3, s[20:23], s40 offen offset:2048 sc1 lds
Mp 104,12,184
Rb 60,10,26624
s_waitcnt lgkmcnt(8)
Mp 96,12,176
Mp 96,16,192
Rb 0,10,27648
Mp 104,16,200
Cv 80,80,81
Cv 81,82,83
Mp 104,20,216
Rb 4,10,28672
Cv 82,84,85
)" R"(
Mp 96,20,208
Cv 83,86,87
Mp 96,24,224
Rb 8,10,29696
Mp 104,24,232
buffer_store_dwordx4 v[80:83], v3, s[20:23], s64 offen offset:2048 sc0 sc1
Mp 104,28,248
Rb 12,10,30720
s_waitcnt lgkmcnt(8)
Mp 96,28,240
v_mfma_f32_16x16x32_bf16 v[100:103], a[32:35], v[128:131], 0
Rb 16,8,32768
v_mfma_f32_16x16x32_bf16 v[108:111], a[32:35], v[136:139], 0
Mp 108,36,152
Rb 20,8,33792
Mp 100,36,144
Cv 88,88,89
Cv 89,90,91
Mp 100,40,160
Rb 24,8,34816
Cv 90,92,93
Mp 108,40,168
Cv 91,94,95
Mp 108,44,184
Rb 28,8,35840
s_waitcnt vmcnt(23) lgkmcnt(4)
s_barrier
Mp 100,44,176
s_add_u32 s40, s67, 0x70000
Mp 100,48,192
Rb 32,8,36864
s_add_u32 m0, s36, 131072
Mp 108,48,200
buffer_load_dwordx4 v2, s[20:23], s40 offen offset:0 sc1 lds
Mp 108,52,216
Rb 36,8,37888
buffer_store_dwordx4 v[88:91], v3, s[20:23], s64 offen offset:3072 sc0 sc1
Mp 100,52,208
s_mov_b32 s8, s74
s_mov_b32 s9, s75
Mp 100,56,224
Rb 40,8,38912
s_mov_b32 s10, 0x1c0000
Mp 108,56,232
s_mov_b32 s11, 0x20000
v_and_b32 v174, 1, v0
Mp 108,60,248
Rb 44,8,40960
Mp 100,60,240
buffer_load_dwordx4 v2, s[20:23], s40 offen offset:1024 sc1 lds
v_mfma_f32_16x16x32_bf16 v[112:115], a[0:3], v[128:131], 0
Rb 48,8,41984
v_mul_u32_u24 v174, 0xffff, v174
v_mfma_f32_16x16x32_bf16 v[120:123], a[0:3], v[136:139], 0
v_add_u32 v174, 1, v174
v_mul_u32_u24 v174, 0x3f80, v174
Mp 120,4,152
Rb 52,8,43008
v_bfe_u32 v173, v0, 1, 3
Mp 112,4,144
Cv 96,96,97
Cv 97,98,99
Mp 112,8,160
Rb 56,8,44032
Cv 98,100,101
)" R"(
Mp 120,8,168
buffer_load_dwordx4 v2, s[20:23], s40 offen offset:2048 sc1 lds
Mp 120,12,184
Rb 60,8,45056
s_waitcnt lgkmcnt(8)
Mp 112,12,176
Cv 99,102,103
v_cmp_eq_u32_e32 vcc, 0, v173
v_and_b32 v167, 0xf0, v1
Mp 112,16,192
Rb 0,8,46080
v_cndmask_b32_e32 v132, 0, v174, vcc
Mp 120,16,200
s_add_u32 s64, s64, 0x10000
buffer_store_dwordx4 v[96:99], v2, s[20:23], s64 offen offset:0 sc0 sc1
Mp 120,20,216
Rb 4,8,47104
v_cmp_eq_u32_e32 vcc, 1, v173
Mp 112,20,208
buffer_load_dwordx4 v2, s[20:23], s40 offen offset:3072 sc1 lds
Mp 112,24,224
Rb 8,8,49152
v_cndmask_b32_e32 v133, 0, v174, vcc
Mp 120,24,232
v_cmp_eq_u32_e32 vcc, 2, v173
v_mul_u32_u24 v166, 56, v167
Mp 120,28,248
Rb 12,8,50176
s_waitcnt lgkmcnt(8)
Mp 112,28,240
Cv 104,104,105
Cv 105,106,107
v_mfma_f32_16x16x32_bf16 v[116:119], a[32:35], v[128:131], 0
Rb 16,8,51200
s_add_u32 m0, s36, 135168
v_mfma_f32_16x16x32_bf16 v[124:127], a[32:35], v[136:139], 0
buffer_load_dwordx4 v3, s[20:23], s40 offen offset:0 sc1 lds
Mp 124,36,152
Rb 20,8,52224
Cv 106,108,109
Mp 116,36,144
Cv 107,110,111
v_cndmask_b32_e32 v134, 0, v174, vcc
Mp 116,40,160
Rb 24,8,53248
v_cmp_eq_u32_e32 vcc, 3, v173
Mp 124,40,168
buffer_store_dwordx4 v[104:107], v2, s[20:23], s64 offen offset:1024 sc0 sc1
Mp 124,44,184
Rb 28,8,54272
s_waitcnt lgkmcnt(8)
Mp 116,44,176
buffer_load_dwordx4 v3, s[20:23], s40 offen offset:1024 sc1 lds
v_cndmask_b32_e32 v135, 0, v174, vcc
Mp 116,48,192
Rb 32,8,55296
v_cmp_eq_u32_e32 vcc, 4, v173
Mp 124,48,200
s_nop 0
v_cndmask_b32_e32 v140, 0, v174, vcc
v_cmp_eq_u32_e32 vcc, 5, v173
Mp 124,52,216
Rb 36,8,57344
v_cndmask_b32_e32 v141, 0, v174, vcc
)" R"(
Mp 116,52,208
v_cmp_eq_u32_e32 vcc, 6, v173
v_and_b32 v165, 0x80, v1
Mp 116,56,224
Rb 40,8,58368
v_cndmask_b32_e32 v142, 0, v174, vcc
Mp 124,56,232
buffer_load_dwordx4 v3, s[20:23], s40 offen offset:2048 sc1 lds
Mp 124,60,248
Rb 44,8,59392
s_waitcnt vmcnt(8) lgkmcnt(8)
buffer_store_dword v6, v7, s[20:23], s65 offen offset:0 sc0 sc1
Mp 116,60,240
v_cmp_eq_u32_e32 vcc, 7, v173
v_lshrrev_b32 v173, 4, v0
v_mfma_f32_16x16x32_bf16 v[32:35], a[0:3], v[128:131], 0
Rb 48,8,60416
v_cndmask_b32_e32 v143, 0, v174, vcc
v_mfma_f32_16x16x32_bf16 v[40:43], a[0:3], v[136:139], 0
v_lshl_or_b32 v164, v173, 16, v167
v_lshl_add_u32 v166, v173, 3, v166
Mp 40,4,152
Rb 52,8,61440
v_lshrrev_b32_e64 v173, v173, s85
Mp 32,4,144
Cv 112,112,113
Cv 113,114,115
Mp 32,8,160
Rb 56,8,62464
Cv 114,116,117
Mp 40,8,168
Cv 115,118,119
v_bfe_u32 v174, v173, 4, 1
Mp 40,12,184
Rb 60,8,63488
s_waitcnt lgkmcnt(8)
Mp 32,12,176
buffer_store_dwordx4 v[112:115], v2, s[20:23], s64 offen offset:2048 sc0 sc1
ds_write_b32 v9, v16 offset:39936
Mp 32,16,192
Rb 0,8,0
v_lshl_or_b32 v183, v174, 13, v164
Mp 40,16,200
v_and_b32 v174, 1, v173
v_lshl_or_b32 v164, v174, 13, v164
Mp 40,20,216
Rb 4,8,1024
v_and_b32 v174, 7, v0
Mp 32,20,208
v_lshl_or_b32 v165, v174, 8, v165
v_lshlrev_b32 v165, 1, v165
Mp 32,24,224
Rb 8,8,2048
s_mov_b32 s93, 0x1111
Mp 40,24,232
Cv 120,120,121
Cv 121,122,123
Mp 40,28,248
Rb 12,8,3072
s_waitcnt lgkmcnt(3)
s_barrier
Mp 32,28,240
Cv 122,124,125
Cv 123,126,127
ds_read_b32 v16, v8 offset:39936
)" R"(
v_mfma_f32_16x16x32_bf16 v[36:39], a[32:35], v[128:131], 0
Rb 16,8,4096
v_lshrrev_b32 v173, 2, v0
v_mfma_f32_16x16x32_bf16 v[44:47], a[32:35], v[136:139], 0
buffer_store_dwordx4 v[120:123], v2, s[20:23], s64 offen offset:3072 sc0 sc1
Mp 44,36,152
Rb 20,8,5120
v_mul_u32_u24 v173, 832, v173
Mp 36,36,144
v_add_u32 v173, v173, v1
v_lshrrev_b32 v174, 1, v0
Mp 36,40,160
Rb 24,8,6144
v_and_b32 v174, 24, v174
Mp 44,40,168
v_lshl_add_u32 v174, v167, 2, v174
s_add_u32 s59, s36, 98304
Mp 44,44,184
Rb 28,8,8192
s_waitcnt lgkmcnt(4)
Mp 36,44,176
v_readfirstlane_b32 s82, v16
v_add_u32 v174, s59, v174
Mp 36,48,192
Rb 32,8,9216
s_lshl2_add_u32 s56, s82, s33
Mp 44,48,200
s_mul_i32 s88, s56, 4682
s_lshr_b32 s58, s88, 16
Mp 44,52,216
Rb 36,8,10240
s_mul_i32 s57, s58, 14
Mp 36,52,208
s_sub_u32 s57, s56, s57
s_lshl_b32 s40, s58, 12
Mp 36,56,224
Rb 40,8,11264
Mp 44,56,232
s_cmp_gt_u32 s57, 6
s_cselect_b32 s88, 128, 0
Mp 44,60,248
Rb 44,8,12288
Mp 36,60,240
buffer_load_dword v4, v7, s[20:23], s52 offen offset:0 sc0 sc1
s_add_u32 s40, s40, s88
v_mfma_f32_16x16x32_bf16 v[48:51], a[0:3], v[128:131], 0
Rb 48,8,13312
s_add_u32 s40, s40, 0xc110000
v_mfma_f32_16x16x32_bf16 v[56:59], a[0:3], v[136:139], 0
s_lshl_b32 s55, s58, 10
s_add_u32 s55, s55, s57
.LzH41:
s_cbranch_execz .LzHR
Mp 56,4,152
Rb 52,8,14336
Mp 48,4,144
Cv 32,32,33
Cv 33,34,35
Mp 48,8,160
Rb 56,8,16384
Cv 34,36,37
Mp 56,8,168
Cv 35,38,39
Mp 56,12,184
)" R"(
Rb 60,8,17408
s_waitcnt lgkmcnt(8)
Mp 48,12,176
buffer_store_dwordx4 v[32:35], v3, s[20:23], s64 offen offset:0 sc0 sc1
Mp 48,16,192
Rb 0,8,18432
Mp 56,16,200
s_cmp_gt_u32 s57, 6
s_cselect_b32 s88, 9, 0
Mp 56,20,216
Rb 4,8,19456
s_add_u32 s55, s55, s88
Mp 48,20,208
s_and_b32 s88, s58, 2
s_lshl_b32 s88, s88, 12
Mp 48,24,224
Rb 8,8,20480
v_xor_b32 v164, s88, v164
Mp 56,24,232
Cv 40,40,41
Cv 41,42,43
Mp 56,28,248
Rb 12,8,21504
s_waitcnt lgkmcnt(8)
Mp 48,28,240
Cv 42,44,45
Cv 43,46,47
v_xor_b32 v183, s88, v183
v_mfma_f32_16x16x32_bf16 v[52:55], a[32:35], v[128:131], 0
Rb 16,8,22528
s_lshl_b32 s55, s55, 10
v_mfma_f32_16x16x32_bf16 v[60:63], a[32:35], v[136:139], 0
buffer_store_dwordx4 v[40:43], v3, s[20:23], s64 offen offset:1024 sc0 sc1
Mp 60,36,152
Rb 20,8,24576
s_add_u32 s55, s55, 0x8000000
Mp 52,36,144
s_and_b32 s88, s57, 1
s_lshl1_add_u32 s58, s58, s88
Mp 52,40,160
Rb 24,8,25600
s_lshl_b32 s88, s58, 8
Mp 60,40,168
v_add_u32 v167, s88, v167
s_mul_i32 s59, s58, 14336
Mp 60,44,184
Rb 28,8,26624
s_waitcnt lgkmcnt(8)
Mp 52,44,176
s_lshr_b32 s88, s57, 1
s_lshl_b32 s88, s88, 6
s_add_u32 s88, s88, s59
Mp 52,48,192
Rb 32,8,27648
v_add_u32 v166, s88, v166
Mp 60,48,200
s_mov_b32 s57, 0
s_add_u32 m0, s36, 98304
Mp 60,52,216
Rb 36,8,28672
Mp 52,52,208
buffer_load_dwordx4 v173, s[12:15], s88 offen offset:0 lds
Mp 52,56,224
Rb 40,8,29696
)" R"(
s_add_u32 m0, s36, 98880
Mp 60,56,232
buffer_load_dwordx4 v173, s[12:15], s88 offen offset:448 lds
Mp 60,60,248
Rb 44,8,30720
s_waitcnt lgkmcnt(8)
Mp 52,60,240
s_mov_b32 s88, 0xc100008
buffer_load_dword v167, v167, s[20:23], s88 offen sc0 sc1
v_mfma_f32_16x16x32_bf16 v[64:67], a[0:3], v[128:131], 0
Rb 48,1,0
v_mfma_f32_16x16x32_bf16 v[72:75], a[0:3], v[136:139], 0
Mp 72,4,152
Rb 52,1,1024
Mp 64,4,144
Mp 64,8,160
Rb 56,1,2048
Mp 72,8,168
Mp 72,12,184
Rb 60,1,3072
s_waitcnt vmcnt(5) lgkmcnt(8)
s_barrier
v_cmp_eq_u32_e32 vcc, s3, v4
s_andn2_b64 s[28:29], 1, vcc
s_cbranch_scc1 .Lzsp11
.Lzok11:
Mp 64,12,176
buffer_load_dwordx4 a[64:67], v2, s[20:23], s66 offen offset:0 sc0 sc1
Mp 64,16,192
Rb 0,1,4096
Mp 72,16,200
Mp 72,20,216
Rb 4,1,5120
Mp 64,20,208
buffer_load_dwordx4 a[68:71], v2, s[20:23], s66 offen offset:1024 sc0 sc1
Mp 64,24,224
Rb 8,1,6144
Mp 72,24,232
Mp 72,28,248
Rb 12,1,8192
s_waitcnt lgkmcnt(8)
Mp 64,28,240
buffer_load_dwordx4 a[72:75], v2, s[20:23], s66 offen offset:2048 sc0 sc1
v_mfma_f32_16x16x32_bf16 v[68:71], a[32:35], v[128:131], 0
Rb 16,1,9216
v_mfma_f32_16x16x32_bf16 v[76:79], a[32:35], v[136:139], 0
Mp 76,36,152
Rb 20,1,10240
Mp 68,36,144
buffer_load_dwordx4 a[76:79], v2, s[20:23], s66 offen offset:3072 sc0 sc1
Mp 68,40,160
Rb 24,1,11264
Mp 76,40,168
Mp 76,44,184
Rb 28,1,12288
s_waitcnt vmcnt(7) lgkmcnt(8)
buffer_store_dword v6, v7, s[20:23], s65 offen offset:128 sc0 sc1
Mp 68,44,176
buffer_load_dwordx4 a[80:83], v3, s[20:23], s66 offen offset:0 sc0 sc1
Mp 68,48,192
Rb 32,1,13312
Mp 76,48,200
Mp 76,52,216
Rb 36,1,14336
Mp 68,52,208
)" R"(
buffer_load_dwordx4 a[84:87], v3, s[20:23], s66 offen offset:1024 sc0 sc1
Mp 68,56,224
Rb 40,1,16384
Mp 76,56,232
Mp 76,60,248
Rb 44,1,17408
s_waitcnt lgkmcnt(8)
Mp 68,60,240
buffer_load_dwordx4 a[88:91], v3, s[20:23], s66 offen offset:2048 sc0 sc1
v_mfma_f32_16x16x32_bf16 v[80:83], a[0:3], v[128:131], 0
Rb 48,1,18432
v_mfma_f32_16x16x32_bf16 v[88:91], a[0:3], v[136:139], 0
Mp 88,4,152
Rb 52,1,19456
Mp 80,4,144
buffer_load_dwordx4 a[92:95], v3, s[20:23], s66 offen offset:3072 sc0 sc1
Mp 80,8,160
Rb 56,1,20480
Mp 88,8,168
Mp 88,12,184
Rb 60,1,21504
s_waitcnt lgkmcnt(8)
Mp 80,12,176
Mp 80,16,192
Rb 0,1,22528
Mp 88,16,200
Mp 88,20,216
Rb 4,1,24576
Mp 80,20,208
Mp 80,24,224
Rb 8,1,25600
Mp 88,24,232
Mp 88,28,248
Rb 12,1,26624
s_waitcnt lgkmcnt(8)
Mp 80,28,240
v_mfma_f32_16x16x32_bf16 v[84:87], a[32:35], v[128:131], 0
Rb 16,1,27648
v_mfma_f32_16x16x32_bf16 v[92:95], a[32:35], v[136:139], 0
Mp 92,36,152
Rb 20,1,28672
Mp 84,36,144
Mp 84,40,160
Rb 24,1,29696
Mp 92,40,168
Mp 92,44,184
Rb 28,1,30720
s_waitcnt lgkmcnt(8)
Mp 84,44,176
buffer_load_dword v4, v7, s[20:23], s52 offen offset:128 sc0 sc1
Mp 84,48,192
Rb 32,1,32768
Mp 92,48,200
Mp 92,52,216
Rb 36,1,33792
Mp 84,52,208
Mp 84,56,224
Rb 40,1,34816
Mp 92,56,232
Mp 92,60,248
Rb 44,1,35840
s_waitcnt vmcnt(6)
s_barrier
Mp 84,60,240
Mv 48,194,64
)" R"(
Mv 56,194,68
Mv 52,192,64
Mv 60,192,68
Mv 64,194,72
Mv 72,194,76
Mv 68,192,72
s_waitcnt vmcnt(3) lgkmcnt(10)
Mv 76,192,76
Cv 48,48,49
Cv 49,50,51
Mv 80,194,80
Cv 50,52,53
Cv 51,54,55
Mv 88,194,84
s_add_u32 s29, s73, 0x0
buffer_store_dwordx4 v[48:51], v1, s[20:23], s29 offen sc0 sc1
Mv 84,192,80
Mv 92,192,84
v_mfma_f32_16x16x32_bf16 v[96:99], a[0:3], v[128:131], 0
Rb 48,1,36864
v_mfma_f32_16x16x32_bf16 v[104:107], a[0:3], v[136:139], 0
Mp 104,4,152
Rb 52,1,37888
s_waitcnt lgkmcnt(8)
Mp 96,4,144
Cv 56,56,57
Cv 57,58,59
ds_read_b64 v[188:189], v174 offset:0
Mp 96,8,160
Rb 56,1,38912
Cv 58,60,61
Mp 104,8,168
Cv 59,62,63
ds_read_b64 v[190:191], v174 offset:32
Mp 104,12,184
Rb 60,1,40960
Mp 96,12,176
s_add_u32 s29, s73, 0x400
buffer_store_dwordx4 v[56:59], v1, s[20:23], s29 offen sc0 sc1
ds_read_b64 v[196:197], v174 offset:1024
Mp 96,16,192
Rb 0,1,41984
Mp 104,16,200
ds_read_b64 v[198:199], v174 offset:1056
Mp 104,20,216
Rb 4,1,43008
s_waitcnt lgkmcnt(12)
Mp 96,20,208
Mp 96,24,224
Rb 8,1,44032
Mp 104,24,232
Cv 64,64,65
Cv 65,66,67
Mp 104,28,248
Rb 12,1,45056
Cv 66,68,69
Mp 96,28,240
Cv 67,70,71
v_mfma_f32_16x16x32_bf16 v[100:103], a[32:35], v[128:131], 0
Rb 16,1,46080
v_mfma_f32_16x16x32_bf16 v[108:111], a[32:35], v[136:139], 0
s_add_u32 s29, s73, 0x800
buffer_store_dwordx4 v[64:67], v1, s[20:23], s29 offen sc0 sc1
Mp 108,36,152
)" R"(
Rb 20,1,47104
s_waitcnt vmcnt(3) lgkmcnt(11)
v_cmp_eq_u32_e32 vcc, s3, v4
s_andn2_b64 s[28:29], 1, vcc
s_cbranch_scc1 .Lzsp12
.Lzok12:
Mp 100,36,144
s_add_u32 s66, s66, 0x10000
buffer_load_dwordx4 a[96:99], v2, s[20:23], s66 offen offset:0 sc0 sc1
Mp 100,40,160
Rb 24,1,49152
v_sub_f32_e32 v172, 1.0, v167
Mp 108,40,168
Cv 172,172,172
v_lshlrev_b32 v172, 16, v172
Mp 108,44,184
Rb 28,1,50176
v_lshlrev_b32 v204, 16, v188
Mp 100,44,176
buffer_load_dwordx4 a[100:103], v2, s[20:23], s66 offen offset:1024 sc0 sc1
Mp 100,48,192
Rb 32,1,51200
Cv 72,72,73
Mp 108,48,200
Cv 73,74,75
Cv 74,76,77
Mp 108,52,216
Rb 36,1,52224
s_waitcnt lgkmcnt(12)
Mp 100,52,208
buffer_load_dwordx4 a[104:107], v2, s[20:23], s66 offen offset:2048 sc0 sc1
Mp 100,56,224
Rb 40,1,53248
Cv 75,78,79
Mp 108,56,232
s_add_u32 s29, s73, 0xc00
buffer_store_dwordx4 v[72:75], v1, s[20:23], s29 offen sc0 sc1
Mp 108,60,248
Rb 44,1,54272
v_and_b32 v205, 0xffff0000, v188
Mp 100,60,240
buffer_load_dwordx4 a[108:111], v2, s[20:23], s66 offen offset:3072 sc0 sc1
Mv 96,194,88
v_mul_f32_e32 v204, v172, v204
v_mul_f32_e32 v205, v172, v205
Mv 104,194,92
Cv 205,204,205
v_lshlrev_b32 v204, 16, v205
Mv 100,192,88
v_and_b32 v205, 0xffff0000, v205
s_waitcnt lgkmcnt(8)
Mv 108,192,92
buffer_load_dwordx4 a[112:115], v3, s[20:23], s66 offen offset:0 sc0 sc1
v_mfma_f32_16x16x32_bf16 v[112:115], a[0:3], v[128:131], 0
Rb 48,1,55296
Cv 80,80,81
v_mfma_f32_16x16x32_bf16 v[120:123], a[0:3], v[136:139], 0
Cv 81,82,83
Cv 82,84,85
Mp 120,4,152
Rb 52,1,57344
Cv 83,86,87
Mp 112,4,144
buffer_load_dwordx4 a[116:119], v3, s[20:23], s66 offen offset:1024 sc0 sc1
)" R"(
Mp 112,8,160
Rb 56,1,58368
v_lshlrev_b32 v206, 16, v189
Mp 120,8,168
s_add_u32 s29, s73, 0x1000
buffer_store_dwordx4 v[80:83], v1, s[20:23], s29 offen sc0 sc1
Mp 120,12,184
Rb 60,1,59392
s_waitcnt lgkmcnt(8)
Mp 112,12,176
v_and_b32 v207, 0xffff0000, v189
v_mul_f32_e32 v206, v172, v206
Mp 112,16,192
Rb 0,1,60416
v_mul_f32_e32 v207, v172, v207
Mp 120,16,200
Cv 207,206,207
v_lshlrev_b32 v206, 16, v207
Mp 120,20,216
Rb 4,1,61440
v_and_b32 v207, 0xffff0000, v207
Mp 112,20,208
Cv 88,88,89
Cv 89,90,91
Mp 112,24,224
Rb 8,1,62464
Cv 90,92,93
Mp 120,24,232
Cv 91,94,95
v_lshlrev_b32 v212, 16, v190
Mp 120,28,248
Rb 12,1,63488
s_waitcnt lgkmcnt(8)
Mp 112,28,240
s_add_u32 s29, s73, 0x1400
buffer_store_dwordx4 v[88:91], v1, s[20:23], s29 offen sc0 sc1
v_mfma_f32_16x16x32_bf16 v[116:119], a[32:35], v[128:131], 0
Rb 16,10,0
v_and_b32 v213, 0xffff0000, v190
v_mfma_f32_16x16x32_bf16 v[124:127], a[32:35], v[136:139], 0
v_mul_f32_e32 v212, v172, v212
v_mul_f32_e32 v213, v172, v213
Mp 124,36,152
Rb 20,10,1024
Cv 213,212,213
Mp 116,36,144
v_lshlrev_b32 v212, 16, v213
v_and_b32 v213, 0xffff0000, v213
Mp 116,40,160
Rb 24,10,2048
v_lshlrev_b32 v214, 16, v191
Mp 124,40,168
Cv 96,96,97
Cv 97,98,99
Mp 124,44,184
Rb 28,10,3072
s_waitcnt lgkmcnt(8)
Mp 116,44,176
Cv 98,100,101
Cv 99,102,103
Mp 116,48,192
Rb 32,10,4096
v_and_b32 v215, 0xffff0000, v191
Mp 124,48,200
)" R"(
s_add_u32 s29, s73, 0x1800
buffer_store_dwordx4 v[96:99], v1, s[20:23], s29 offen sc0 sc1
Mp 124,52,216
Rb 36,10,5120
v_mul_f32_e32 v214, v172, v214
Mp 116,52,208
v_mul_f32_e32 v215, v172, v215
Cv 215,214,215
Mp 116,56,224
Rb 40,10,6144
v_lshlrev_b32 v214, 16, v215
Mp 124,56,232
v_and_b32 v215, 0xffff0000, v215
v_lshlrev_b32 v220, 16, v196
Mp 124,60,248
Rb 44,10,8192
s_waitcnt vmcnt(8) lgkmcnt(10)
Mp 116,60,240
Cv 104,104,105
Cv 105,106,107
Mv 112,194,96
Cv 106,108,109
Cv 107,110,111
Mv 120,194,100
s_add_u32 s29, s73, 0x4000
buffer_store_dwordx4 v[104:107], v1, s[20:23], s29 offen sc0 sc1
Mv 116,192,96
v_and_b32 v221, 0xffff0000, v196
v_mul_f32_e32 v220, v172, v220
Mv 124,192,100
v_mul_f32_e32 v221, v172, v221
Cv 221,220,221
v_mfma_f32_16x16x32_bf16 v[32:35], a[0:3], v[128:131], 0
Rb 48,10,9216
v_lshlrev_b32 v220, 16, v221
v_mfma_f32_16x16x32_bf16 v[40:43], a[0:3], v[136:139], 0
v_and_b32 v221, 0xffff0000, v221
v_lshlrev_b32 v222, 16, v197
Mp 40,4,152
Rb 52,10,10240
s_waitcnt lgkmcnt(8)
Mp 32,4,144
Cv 112,112,113
Cv 113,114,115
Mp 32,8,160
Rb 56,10,11264
Cv 114,116,117
Mp 40,8,168
Cv 115,118,119
v_and_b32 v223, 0xffff0000, v197
Mp 40,12,184
Rb 60,10,12288
v_mul_f32_e32 v222, v172, v222
Mp 32,12,176
s_add_u32 s29, s73, 0x4400
buffer_store_dwordx4 v[112:115], v1, s[20:23], s29 offen sc0 sc1
Mp 32,16,192
Rb 0,10,13312
v_mul_f32_e32 v223, v172, v223
Mp 40,16,200
Cv 223,222,223
v_lshlrev_b32 v222, 16, v223
Mp 40,20,216
Rb 4,10,14336
)" R"(
s_waitcnt lgkmcnt(8)
Mp 32,20,208
v_and_b32 v223, 0xffff0000, v223
v_lshlrev_b32 v228, 16, v198
Mp 32,24,224
Rb 8,10,16384
v_and_b32 v229, 0xffff0000, v198
Mp 40,24,232
Cv 120,120,121
Cv 121,122,123
Mp 40,28,248
Rb 12,10,17408
Cv 122,124,125
Mp 32,28,240
Cv 123,126,127
v_mul_f32_e32 v228, v172, v228
v_mfma_f32_16x16x32_bf16 v[36:39], a[32:35], v[128:131], 0
Rb 16,10,18432
v_mul_f32_e32 v229, v172, v229
v_mfma_f32_16x16x32_bf16 v[44:47], a[32:35], v[136:139], 0
s_add_u32 s29, s73, 0x4800
buffer_store_dwordx4 v[120:123], v1, s[20:23], s29 offen sc0 sc1
Mp 44,36,152
Rb 20,10,19456
s_waitcnt lgkmcnt(8)
Mp 36,36,144
Cv 229,228,229
v_lshlrev_b32 v228, 16, v229
Mp 36,40,160
Rb 24,10,20480
v_and_b32 v229, 0xffff0000, v229
Mp 44,40,168
v_lshlrev_b32 v230, 16, v199
v_and_b32 v231, 0xffff0000, v199
Mp 44,44,184
Rb 28,10,21504
v_mul_f32_e32 v230, v172, v230
Mp 36,44,176
v_mul_f32_e32 v231, v172, v231
Cv 231,230,231
Mp 36,48,192
Rb 32,10,22528
v_lshlrev_b32 v230, 16, v231
Mp 44,48,200
v_and_b32 v231, 0xffff0000, v231
Mp 44,52,216
Rb 36,10,24576
s_waitcnt vmcnt(3) lgkmcnt(10)
buffer_store_dword v6, v7, s[20:23], s68 offen offset:0 sc0 sc1
Mp 36,52,208
Mp 36,56,224
Rb 40,10,25600
Mp 44,56,232
Mp 44,60,248
Rb 44,10,26624
Mp 36,60,240
Mv 32,194,104
Mv 40,194,108
Mv 36,192,104
s_waitcnt lgkmcnt(8)
Mv 44,192,108
v_mfma_f32_16x16x32_bf16 v[48:51], a[0:3], v[128:131], 0
Rb 48,10,27648
v_mfma_f32_16x16x32_bf16 v[56:59], a[0:3], v[136:139], 0
)" R"(
Mp 56,4,152
Rb 52,10,28672
Mp 48,4,144
Cv 32,32,33
Cv 33,34,35
Mp 48,8,160
Rb 56,10,29696
Cv 34,36,37
Mp 56,8,168
Cv 35,38,39
Mp 56,12,184
Rb 60,10,30720
s_waitcnt lgkmcnt(8)
Mp 48,12,176
s_add_u32 s29, s73, 0x4c00
buffer_store_dwordx4 v[32:35], v1, s[20:23], s29 offen sc0 sc1
Mp 48,16,192
Mp 56,16,200
Mp 56,20,216
Mp 48,20,208
Mp 48,24,224
Mp 56,24,232
Cv 40,40,41
Cv 41,42,43
buffer_store_dword v6, v7, s[20:23], s68 offen offset:128 sc0 sc1
Mp 56,28,248
Cv 42,44,45
s_waitcnt lgkmcnt(4)
Mp 48,28,240
Cv 43,46,47
v_mfma_f32_16x16x32_bf16 v[52:55], a[32:35], v[128:131], 0
s_add_u32 s29, s73, 0x5000
buffer_store_dwordx4 v[40:43], v1, s[20:23], s29 offen sc0 sc1
.LzH42:
s_cbranch_execz .LzHR
v_mfma_f32_16x16x32_bf16 v[60:63], a[32:35], v[136:139], 0
buffer_load_dword v4, v165, s[20:23], s40 offen sc0 sc1
Mp 60,36,152
Mp 52,36,144
Mp 52,40,160
Mp 60,40,168
Mp 60,44,184
s_waitcnt lgkmcnt(0)
Mp 52,44,176
Mp 52,48,192
Mp 60,48,200
Mp 60,52,216
Mp 52,52,208
Mp 52,56,224
Mp 60,56,232
Mp 60,60,248
Mp 52,60,240
Mv 48,194,112
Mv 56,194,116
Mv 52,192,112
Mv 60,192,116
s_nop 7
Cv 48,48,49
Cv 49,50,51
Cv 50,52,53
Cv 51,54,55
s_add_u32 s29, s73, 0x5400
buffer_store_dwordx4 v[48:51], v1, s[20:23], s29 offen sc0 sc1
Cv 56,56,57
)" R"(
Cv 57,58,59
Cv 58,60,61
Cv 59,62,63
s_add_u32 s29, s73, 0x5800
buffer_store_dwordx4 v[56:59], v1, s[20:23], s29 offen sc0 sc1
s_branch .LzEND
.LzCEND:
)" : "+{s[0:1]}"(ka), "+{s2}"(bid), "+{v0}"(tid) :
   : "memory", "scc", "vcc", "v1", "v2", "v3", "v4", "v5", "v6", "v7", "v8", "v9", R256("v"), V10("a"), R256("a"),
     "s3", "s4", "s5", "s6", "s7", "s8", "s9", V10("s1"), V10("s2"), "s30", "s31", "s33", "s34", "s35", "s36", "s37", "s38", "s39",
     V10("s4"), V10("s5"), V10("s6"), V10("s7"), V10("s8"), V10("s9"));
}

extern "C" void kernel_launch(void* const* d_in, const int* in_sizes, int n_in, void* d_out, int out_size, void* d_ws, size_t ws_size, hipStream_t stream) {
  (void)hipFuncSetAttribute((const void*)k_main, hipFuncAttributeMaxDynamicSharedMemorySize, 163840);
  k_main<<<256, 256, 163840, stream>>>((const u16*)d_in[0], (const u16*)d_in[1], (const u16*)d_in[2], (const u16*)d_in[3], (const u16*)d_in[4],
      (char*)d_ws, (const u16*)d_in[11], (const u16*)d_in[7], (const u16*)d_in[5], (const u16*)d_in[8], (const u16*)d_in[9], (const u16*)d_in[10], (u16*)d_out);
}
```

```cpp
#include <hip/hip_runtime.h>

typedef unsigned short u16;
#define V10(p) p "0", p "1", p "2", p "3", p "4", p "5", p "6", p "7", p "8", p "9"
#define V100(p) V10(p "0"), V10(p "1"), V10(p "2"), V10(p "3"), V10(p "4"), V10(p "5"), V10(p "6"), V10(p "7"), V10(p "8"), V10(p "9")
#define R256(p) V10(p "1"), V10(p "2"), V10(p "3"), V10(p "4"), V10(p "5"), V10(p "6"), V10(p "7"), V10(p "8"), V10(p "9"), V100(p "1"), \
                V10(p "20"), V10(p "21"), V10(p "22"), V10(p "23"), V10(p "24"), p "250", p "251", p "252", p "253", p "254", p "255"

extern "C" __global__ __launch_bounds__(256, 1) __attribute__((aligned(4096)))
void k_main(const u16* x, const u16* ckv, const u16* wq, const u16* wuk, const u16* wuv, char* ws,
            const u16* gate_w, const u16* w_r, const u16* wo, const u16* wg, const u16* wu, const u16* wd, u16* out) {
  typedef const __attribute__((address_space(4))) char* karg_t;
  karg_t ka = (karg_t)__builtin_amdgcn_kernarg_segment_ptr();
  unsigned bid = blockIdx.x, tid = threadIdx.x;
  asm volatile(R"(
.macro Mv d,a,b
v_mfma_f32_16x16x32_bf16 v[\d:\d+3], a[\a:\a+3], a[\b:\b+3], v[\d:\d+3]
.endm
.macro Mp d,a,b
v_mfma_f32_16x16x32_bf16 v[\d:\d+3], a[\a:\a+3], v[\b:\b+3], v[\d:\d+3]
.endm
.macro Ma d,a,b
v_mfma_f32_16x16x32_bf16 a[\d:\d+3], a[\a:\a+3], a[\b:\b+3], a[\d:\d+3]
.endm
.macro Mz d,a,b
v_mfma_f32_16x16x32_bf16 v[\d:\d+3], a[\a:\a+3], a[\b:\b+3], 0
.endm
.macro Rb d,v,o
ds_read_b128 a[\d:\d+3], v\v offset:\o
.endm
.macro Rt d,v,o
ds_read_b64_tr_b16 a[\d:\d+1], v\v offset:\o
.endm
.macro Cv d,a,b
v_cvt_pk_bf16_f32 v\d, v\a, v\b
.endm
.LzSTART:
s_load_dwordx16 s[64:79], s[0:1], 0
s_load_dwordx2 s[80:81], s[0:1], 64
v_lshrrev_b32 v1, 6, v0
v_and_b32 v0, 63, v0
v_readfirstlane_b32 s33, v1
v_lshlrev_b32 v1, 4, v0
s_getpc_b64 s[24:25]
.Lzpc0:
s_sub_u32 s24, s24, .Lzpc0-.LzSTART
s_subb_u32 s25, s25, 0
s_mov_b32 s26, .LzCEND-.LzSTART
s_mov_b32 s27, 0x20000
s_lshr_b32 s28, s2, 3
s_lshl2_add_u32 s28, s28, s33
s_lshl_b32 s28, s28, 11
v_lshrrev_b32 v5, 4, v0
v_min_u32 v5, 1, v5
v_lshlrev_b32 v5, 27, v5
v_add_lshl_u32 v5, v5, v1, 3
buffer_load_dword v5, v5, s[24:27], s28 offen
s_and_b32 s28, s2, 0x17
s_bfe_u32 s29, s2, 0x10005
s_lshl3_add_u32 s28, s29, s28
s_lshr_b32 s29, s2, 6
s_lshl_b32 s29, s29, 5
s_or_b32 s28, s28, s29
s_and_b32 s29, s2, 8
s_lshl4_add_u32 s2, s29, s28
s_and_b32 s28, s2, 7
s_bfe_u32 s29, s2, 0x10003
s_lshl1_add_u32 s34, s28, s29
s_lshl4_add_u32 s89, s28, s29
s_lshl_b32 s89, s89, 21
s_bfe_u32 s35, s2, 0x30004
s_bfe_u32 s91, s2, 0x10004
s_bfe_u32 s93, s2, 0x20005
s_lshl_b32 s36, s33, 13
s_mov_b32 s95, 40
s_bitcmp1_b32 s2, 7
s_cbranch_scc0 .LzA0
.LzB0:
s_mov_b32 s54, 0x3db8aa3b
s_mul_i32 s41, s33, 7168
s_mul_i32 s42, s33, 0x70000
s_lshl_b32 s28, s34, 9
s_add_u32 s42, s42, s28
v_lshrrev_b32 v28, 3, v0
v_and_b32 v29, 0x70, v1
v_lshl_or_b32 v28, v28, 13, v29
v_lshlrev_b32 v29, 1, v0
v_and_b32 v29, 32, v29
v_xor_b32 v28, v28, v29
s_and_b32 s28, s33, 1
s_lshl_b32 s28, s28, 6
v_xor_b32 v240, s28, v28
v_xor_b32 v241, 64, v240
v_add_u32 v241, 0xfc00, v241
v_add_u32 v242, 0x1f800, v240
v_add_u32 v243, 0x1f800, v241
s_sub_u32 s28, 15, s35
s_lshl2_add_u32 s28, s28, s33
s_mul_i32 s43, s28, 28672
)" R"(
v_and_b32 v2, 0x70, v1
v_and_b32 v28, 48, v0
v_xor_b32 v2, v2, v28
v_lshrrev_b32 v28, 3, v0
v_mul_u32_u24 v28, 896, v28
v_add_u32 v2, v2, v28
v_xor_b32 v3, 64, v2
v_add_u32 v3, 6144, v3
v_bfe_u32 v26, v0, 1, 3
v_lshrrev_b32 v28, 4, v0
v_xor_b32 v26, v26, v28
v_lshlrev_b32 v26, 4, v26
v_and_b32 v28, 15, v0
v_lshl_or_b32 v26, v28, 7, v26
s_add_u32 s28, s36, 0x0
v_add_u32 v26, s28, v26
v_xor_b32 v27, 64, v26
s_waitcnt lgkmcnt(0)
s_mov_b32 s16, s68
s_mov_b32 s17, s69
s_mov_b32 s18, 0x380000
s_mov_b32 s19, 0x20000
s_mov_b32 s12, s64
s_mov_b32 s13, s65
s_mov_b32 s14, 0x1c0000
s_mov_b32 s15, 0x20000
s_add_u32 m0, s36, 0
s_add_u32 s44, s43, 0
buffer_load_dwordx4 v2, s[12:15], s44 offen offset:0 lds
buffer_load_dwordx4 v3, s[12:15], s44 offen offset:1024 lds
s_add_u32 s44, s43, 12288
buffer_load_dwordx4 v2, s[12:15], s44 offen offset:2048 lds
buffer_load_dwordx4 v3, s[12:15], s44 offen offset:3072 lds
s_add_u32 m0, s41, 65536
s_add_u32 s92, s42, 0x0
buffer_load_dwordx4 v240, s[16:19], s92 offen offset:0 lds
buffer_load_dwordx4 v241, s[16:19], s92 offen offset:1024 lds
buffer_load_dwordx4 v242, s[16:19], s92 offen offset:2048 lds
buffer_load_dwordx4 v243, s[16:19], s92 offen offset:3072 lds
s_add_u32 m0, s41, 69632
s_add_u32 s92, s42, 0x40000
buffer_load_dwordx4 v240, s[16:19], s92 offen offset:0 lds
buffer_load_dwordx4 v241, s[16:19], s92 offen offset:1024 lds
buffer_load_dwordx4 v242, s[16:19], s92 offen offset:2048 lds
s_add_u32 m0, s36, 4096
s_add_u32 s44, s43, 128
buffer_load_dwordx4 v2, s[12:15], s44 offen offset:0 lds
buffer_load_dwordx4 v3, s[12:15], s44 offen offset:1024 lds
s_add_u32 s44, s43, 12416
buffer_load_dwordx4 v2, s[12:15], s44 offen offset:2048 lds
buffer_load_dwordx4 v3, s[12:15], s44 offen offset:3072 lds
s_add_u32 m0, s36, 32768
s_add_u32 s44, s43, 256
buffer_load_dwordx4 v2, s[12:15], s44 offen offset:0 lds
buffer_load_dwordx4 v3, s[12:15], s44 offen offset:1024 lds
s_add_u32 s44, s43, 12544
buffer_load_dwordx4 v2, s[12:15], s44 offen offset:2048 lds
buffer_load_dwordx4 v3, s[12:15], s44 offen offset:3072 lds
s_add_u32 m0, s36, 36864
s_add_u32 s44, s43, 384
buffer_load_dwordx4 v2, s[12:15], s44 offen offset:0 lds
buffer_load_dwordx4 v3, s[12:15], s44 offen offset:1024 lds
s_add_u32 s44, s43, 12672
buffer_load_dwordx4 v2, s[12:15], s44 offen offset:2048 lds
)" R"(
buffer_load_dwordx4 v3, s[12:15], s44 offen offset:3072 lds
s_mov_b32 s28, 0x1508400
s_load_dword s3, s[74:75], s28
s_mov_b32 s20, s74
s_mov_b32 s21, s75
s_mov_b32 s22, 0xf400000
s_mov_b32 s23, 0x20000
s_mov_b32 s4, s66
s_mov_b32 s5, s67
s_mov_b32 s6, 0x400000
s_mov_b32 s7, 0x20000
s_cmp_eq_u32 s91, 1
s_cselect_b32 s8, s72, s70
s_cselect_b32 s9, s73, s71
s_mov_b32 s10, 0x800000
s_mov_b32 s11, 0x20000
s_load_dwordx2 s[74:75], s[0:1], 96
s_lshl3_add_u32 s28, s33, 64
s_load_dwordx2 s[70:71], s[0:1], s28
v_bfe_u32 v244, v0, 3, 2
v_lshlrev_b32 v244, 5, v244
v_and_b32 v28, 3, v0
v_lshl_or_b32 v244, v28, 3, v244
v_bfe_u32 v28, v0, 2, 2
v_lshl_or_b32 v244, v28, 7, v244
v_lshrrev_b32 v28, 4, v0
v_lshl_or_b32 v244, v28, 10, v244
v_xor_b32 v245, 32, v244
v_xor_b32 v246, 64, v244
v_xor_b32 v247, 96, v244
v_add_u32 v252, 0x20000, v244
v_add_u32 v253, 0x20000, v245
v_add_u32 v254, 0x20000, v246
v_add_u32 v255, 0x20000, v247
v_add_u32 v248, 0x10000, v244
v_add_u32 v249, 0x10000, v245
v_add_u32 v250, 0x10000, v246
v_add_u32 v251, 0x10000, v247
s_waitcnt vmcnt(8) lgkmcnt(0)
s_barrier
ds_read_b128 v[128:131], v26 offset:0
ds_read_b128 v[132:135], v26 offset:2048
ds_read_b128 v[136:139], v27 offset:0
ds_read_b128 v[140:143], v27 offset:2048
ds_read_b128 v[144:147], v26 offset:4096
ds_read_b128 v[148:151], v26 offset:6144
ds_read_b128 v[152:155], v27 offset:4096
ds_read_b128 v[156:159], v27 offset:6144
Rt 144,248,0
Rt 146,248,512
Rt 148,249,0
Rt 150,249,512
Rt 152,250,0
Rt 154,250,512
Rt 156,251,0
Rt 158,251,512
Rt 160,248,4096
Rt 162,248,4608
Rt 164,249,4096
Rt 166,249,4608
Rt 168,250,4096
Rt 170,250,4608
Rt 172,251,4096
Rt 174,251,4608
s_add_u32 m0, s41, 98304
)" R"(
s_add_u32 s92, s42, 0x80
buffer_load_dwordx4 v240, s[16:19], s92 offen offset:0 lds
buffer_load_dwordx4 v241, s[16:19], s92 offen offset:1024 lds
buffer_load_dwordx4 v242, s[16:19], s92 offen offset:2048 lds
buffer_load_dwordx4 v243, s[16:19], s92 offen offset:3072 lds
s_add_u32 m0, s41, 102400
s_add_u32 s92, s42, 0x40080
buffer_load_dwordx4 v240, s[16:19], s92 offen offset:0 lds
buffer_load_dwordx4 v241, s[16:19], s92 offen offset:1024 lds
buffer_load_dwordx4 v242, s[16:19], s92 offen offset:2048 lds
s_mul_i32 s3, s3, 1664525
s_add_u32 s3, s3, 1013904223
v_mov_b32 v6, s3
v_min_u32 v7, 1, v0
v_lshlrev_b32 v7, 30, v7
s_waitcnt lgkmcnt(8)
s_barrier
v_mfma_f32_16x16x32_bf16 v[32:35], a[144:147], v[128:131], 0
Rt 128,248,8192
s_add_u32 m0, s41, 131072
s_add_u32 s92, s42, 0x1c0000
v_mfma_f32_16x16x32_bf16 v[40:43], a[144:147], v[132:135], 0
Rt 130,248,8704
buffer_load_dwordx4 v240, s[16:19], s92 offen offset:0 lds
v_mfma_f32_16x16x32_bf16 v[44:47], a[148:151], v[132:135], 0
Rt 132,249,8192
v_mfma_f32_16x16x32_bf16 v[36:39], a[148:151], v[128:131], 0
Rt 134,249,8704
v_mfma_f32_16x16x32_bf16 v[48:51], a[152:155], v[128:131], 0
Rt 136,250,8192
v_mfma_f32_16x16x32_bf16 v[56:59], a[152:155], v[132:135], 0
Rt 138,250,8704
buffer_load_dwordx4 v241, s[16:19], s92 offen offset:1024 lds
v_mfma_f32_16x16x32_bf16 v[60:63], a[156:159], v[132:135], 0
Rt 140,251,8192
v_mfma_f32_16x16x32_bf16 v[52:55], a[156:159], v[128:131], 0
Rt 142,251,8704
s_waitcnt vmcnt(13) lgkmcnt(8)
Mp 32,160,136
Rt 144,248,12288
ds_read_b128 v[160:163], v26 offset:32768
Mp 40,160,140
Rt 146,248,12800
buffer_load_dwordx4 v242, s[16:19], s92 offen offset:2048 lds
Mp 44,164,140
Rt 148,249,12288
ds_read_b128 v[164:167], v26 offset:34816
Mp 36,164,136
Rt 150,249,12800
Mp 48,168,136
Rt 152,250,12288
ds_read_b128 v[168:171], v27 offset:32768
Mp 56,168,140
Rt 154,250,12800
buffer_load_dwordx4 v243, s[16:19], s92 offen offset:3072 lds
Mp 60,172,140
Rt 156,251,12288
ds_read_b128 v[172:175], v27 offset:34816
Mp 52,172,136
Rt 158,251,12800
s_waitcnt vmcnt(11) lgkmcnt(12)
Mp 32,128,144
Rt 160,248,16384
ds_read_b128 v[176:179], v26 offset:36864
s_add_u32 m0, s41, 135168
)" R"(
s_add_u32 s92, s42, 0x200000
Mp 40,128,148
Rt 162,248,16896
buffer_load_dwordx4 v240, s[16:19], s92 offen offset:0 lds
Mp 44,132,148
Rt 164,249,16384
ds_read_b128 v[180:183], v26 offset:38912
Mp 36,132,144
Rt 166,249,16896
Mp 48,136,144
Rt 168,250,16384
ds_read_b128 v[184:187], v27 offset:36864
Mp 56,136,148
Rt 170,250,16896
buffer_load_dwordx4 v241, s[16:19], s92 offen offset:1024 lds
Mp 60,140,148
Rt 172,251,16384
ds_read_b128 v[188:191], v27 offset:38912
Mp 52,140,144
Rt 174,251,16896
s_waitcnt lgkmcnt(12)
Mp 32,144,152
Rt 128,248,20480
Mp 40,144,156
Rt 130,248,20992
buffer_load_dwordx4 v242, s[16:19], s92 offen offset:2048 lds
Mp 44,148,156
Rt 132,249,20480
Mp 36,148,152
Rt 134,249,20992
Mp 48,152,152
Rt 136,250,20480
Mp 56,152,156
Rt 138,250,20992
Mp 60,156,156
Rt 140,251,20480
s_add_u32 s44, s43, 512
Mp 52,156,152
Rt 142,251,20992
s_waitcnt lgkmcnt(8)
Mp 32,160,160
Rt 144,248,24576
s_add_u32 m0, s36, 32768
Mp 40,160,164
Rt 146,248,25088
buffer_load_dwordx4 v2, s[12:15], s44 offen offset:0 lds
Mp 44,164,164
Rt 148,249,24576
Mp 36,164,160
Rt 150,249,25088
Mp 48,168,160
Rt 152,250,24576
Mp 56,168,164
Rt 154,250,25088
buffer_load_dwordx4 v3, s[12:15], s44 offen offset:1024 lds
Mp 60,172,164
Rt 156,251,24576
Mp 52,172,160
Rt 158,251,25088
s_call_b64 s[30:31], .LzHOP
s_waitcnt vmcnt(9) lgkmcnt(8)
s_barrier
Mp 32,128,168
Rt 160,248,32768
s_mov_b32 s63, 0xe0c0a08
)" R"(
s_mov_b32 s28, 0x100d0b09
Mp 40,128,172
Rt 162,248,33280
s_add_u32 s44, s43, 12800
buffer_load_dwordx4 v2, s[12:15], s44 offen offset:2048 lds
Mp 44,132,172
Rt 164,249,32768
Mp 36,132,168
Rt 166,249,33280
Mp 48,136,168
Rt 168,250,32768
s_bitcmp1_b32 s2, 5
s_cmov_b32 s63, 0xf1e1c1a
s_cmov_b32 s28, 0x111f1d1b
Mp 56,136,172
Rt 170,250,33280
buffer_load_dwordx4 v3, s[12:15], s44 offen offset:3072 lds
Mp 60,140,172
Rt 172,251,32768
s_add_u32 s44, s43, 640
Mp 52,140,168
Rt 174,251,33280
s_waitcnt lgkmcnt(8)
s_barrier
Mp 32,144,176
Rt 128,248,36864
s_add_u32 m0, s36, 36864
Mp 40,144,180
Rt 130,248,37376
buffer_load_dwordx4 v2, s[12:15], s44 offen offset:0 lds
Mp 44,148,180
Rt 132,249,36864
s_bitcmp1_b32 s2, 6
s_cmov_b32 s63, s28
Mp 36,148,176
Rt 134,249,37376
s_lshl_b32 s28, s33, 3
s_lshr_b32 s28, s63, s28
Mp 48,152,176
Rt 136,250,36864
s_and_b32 s28, s28, 255
s_lshl_b32 s38, s28, 17
s_add_u32 s38, s38, 0xff80
Mp 56,152,180
Rt 138,250,37376
buffer_load_dwordx4 v3, s[12:15], s44 offen offset:1024 lds
Mp 60,156,180
Rt 140,251,36864
s_lshl_b32 s39, s33, 16
s_lshl_b32 s28, s34, 9
Mp 52,156,176
Rt 142,251,37376
s_add_u32 s39, s39, s28
s_waitcnt lgkmcnt(8)
v_mfma_f32_16x16x32_bf16 v[64:67], a[160:163], v[128:131], 0
Rt 144,248,40960
s_sub_u32 s39, s39, 0x40000
s_lshl_b32 s37, s33, 12
v_mfma_f32_16x16x32_bf16 v[72:75], a[160:163], v[132:135], 0
Rt 146,248,41472
s_add_u32 s44, s43, 12928
buffer_load_dwordx4 v2, s[12:15], s44 offen offset:2048 lds
v_mfma_f32_16x16x32_bf16 v[76:79], a[164:167], v[132:135], 0
Rt 148,249,40960
s_add_u32 s37, s37, 0x10000
)" R"(
s_lshl_b32 s29, s91, 22
v_mfma_f32_16x16x32_bf16 v[68:71], a[164:167], v[128:131], 0
Rt 150,249,41472
s_add_u32 s28, s89, s29
s_bfe_u32 s29, s63, 0x80000
v_mfma_f32_16x16x32_bf16 v[80:83], a[168:171], v[128:131], 0
Rt 152,250,40960
s_lshl_b32 s29, s29, 16
s_add_u32 s45, s28, s29
s_bfe_u32 s29, s63, 0x80008
v_mfma_f32_16x16x32_bf16 v[88:91], a[168:171], v[132:135], 0
Rt 154,250,41472
buffer_load_dwordx4 v3, s[12:15], s44 offen offset:3072 lds
v_mfma_f32_16x16x32_bf16 v[92:95], a[172:175], v[132:135], 0
Rt 156,251,40960
s_add_u32 s92, s42, 0x1c0080
s_lshl_b32 s29, s29, 16
v_mfma_f32_16x16x32_bf16 v[84:87], a[172:175], v[128:131], 0
Rt 158,251,41472
s_add_u32 s46, s28, s29
s_waitcnt lgkmcnt(8)
Mp 64,128,136
Rt 160,248,45056
s_add_u32 m0, s41, 0
s_bfe_u32 s29, s63, 0x80010
Mp 72,128,140
Rt 162,248,45568
buffer_load_dwordx4 v240, s[16:19], s92 offen offset:0 lds
s_lshl_b32 s29, s29, 16
Mp 76,132,140
Rt 164,249,45056
s_add_u32 s47, s28, s29
s_bfe_u32 s29, s63, 0x80018
Mp 68,132,136
Rt 166,249,45568
s_lshl_b32 s29, s29, 16
s_add_u32 s48, s28, s29
Mp 80,136,136
Rt 168,250,45056
s_add_u32 s50, s89, 0xd40000
s_lshl2_add_u32 s28, s91, s93
s_lshl_b32 s28, s28, 7
Mp 88,136,140
Rt 170,250,45568
buffer_load_dwordx4 v241, s[16:19], s92 offen offset:1024 lds
Mp 92,140,140
Rt 172,251,45056
s_add_u32 s49, s28, s50
s_add_u32 s49, s49, 4096
Mp 84,140,136
Rt 174,251,45568
s_lshl_b32 s52, s35, 7
s_waitcnt lgkmcnt(8)
Mp 64,144,144
Rt 128,248,49152
s_add_u32 s52, s52, s50
s_add_u32 s52, s52, 0xd000
Mp 72,144,148
Rt 130,248,49664
buffer_load_dwordx4 v242, s[16:19], s92 offen offset:2048 lds
s_lshl_b32 s51, s35, 17
Mp 76,148,148
Rt 132,249,49152
s_add_u32 s51, s51, s89
)" R"(
s_add_u32 s51, s51, 0x800000
Mp 68,148,144
Rt 134,249,49664
Mp 80,152,144
Rt 136,250,49152
Mp 88,152,148
Rt 138,250,49664
buffer_load_dwordx4 v243, s[16:19], s92 offen offset:3072 lds
Mp 92,156,148
Rt 140,251,49152
s_add_u32 s92, s42, 0x200080
Mp 84,156,144
Rt 142,251,49664
s_waitcnt lgkmcnt(8)
Mp 64,160,152
Rt 144,248,53248
s_add_u32 m0, s41, 4096
Mp 72,160,156
Rt 146,248,53760
buffer_load_dwordx4 v240, s[16:19], s92 offen offset:0 lds
Mp 76,164,156
Rt 148,249,53248
Mp 68,164,152
Rt 150,249,53760
Mp 80,168,152
Rt 152,250,53248
Mp 88,168,156
Rt 154,250,53760
buffer_load_dwordx4 v241, s[16:19], s92 offen offset:1024 lds
Mp 92,172,156
Rt 156,251,53248
s_cmp_lg_u32 s33, 0
s_cmov_b32 s49, 0x40000000
s_cmov_b32 s52, 0x40000000
Mp 84,172,152
Rt 158,251,53760
s_mov_b32 s53, 0x40000
s_waitcnt vmcnt(10) lgkmcnt(8)
Mp 64,128,160
Rt 160,248,57344
ds_read_b128 v[192:195], v26 offset:32768
s_mov_b32 s62, 0x4e800000
Mp 72,128,164
Rt 162,248,57856
buffer_load_dwordx4 v242, s[16:19], s92 offen offset:2048 lds
s_add_u32 s67, s89, 0xc00000
Mp 76,132,164
Rt 164,249,57344
ds_read_b128 v[196:199], v26 offset:34816
s_lshl3_add_u32 s28, s34, s35
Mp 68,132,160
Rt 166,249,57856
s_add_u32 s64, s51, 0x100000
s_lshl2_add_u32 s28, s28, s33
s_lshl2_add_u32 s65, s35, s33
Mp 80,136,160
Rt 168,250,57344
ds_read_b128 v[200:203], v27 offset:32768
s_lshl_b32 s65, s65, 7
Mp 88,136,164
Rt 170,250,57856
s_add_u32 s65, s65, s50
s_add_u32 s65, s65, 0xc000
Mp 92,140,164
)" R"(
Rt 172,251,57344
ds_read_b128 v[204:207], v27 offset:34816
s_lshl_b32 s66, s28, 10
s_add_u32 s66, s66, 0x1400000
Mp 84,140,160
Rt 174,251,57856
s_waitcnt vmcnt(7) lgkmcnt(12)
s_barrier
Mp 64,144,168
Rt 128,252,0
ds_read_b128 v[208:211], v26 offset:36864
s_sub_u32 s28, 15, s35
Mp 72,144,172
Rt 130,252,512
s_lshl2_add_u32 s28, s28, s33
s_and_b32 s29, s34, 1
Mp 76,148,172
Rt 132,253,0
ds_read_b128 v[212:215], v26 offset:38912
s_lshl1_add_u32 s29, s28, s29
Mp 68,148,168
Rt 134,253,512
s_lshl_b32 s73, s29, 15
s_and_b32 s29, s89, 0xfe000000
s_add_u32 s73, s73, s29
Mp 80,152,168
Rt 136,254,0
ds_read_b128 v[216:219], v27 offset:36864
s_add_u32 s73, s73, 0x1002000
Mp 88,152,172
Rt 138,254,512
s_add_u32 s44, s43, 768
s_lshl4_add_u32 s28, s28, s34
Mp 92,156,172
Rt 140,255,0
ds_read_b128 v[220:223], v27 offset:38912
s_lshl_b32 s68, s28, 8
s_add_u32 s68, s68, 0x1510000
Mp 84,156,168
Rt 142,255,512
s_waitcnt lgkmcnt(12)
s_barrier
Mp 64,160,176
Rt 144,252,4096
s_add_u32 m0, s36, 32768
s_xor_b32 s72, s73, 0x3c6000
Mp 72,160,180
Rt 146,252,4608
buffer_load_dwordx4 v2, s[12:15], s44 offen offset:0 lds
Mp 76,164,180
Rt 148,253,4096
s_mov_b32 s55, s89
s_mov_b32 s57, 18
Mp 68,164,176
Rt 150,253,4608
s_mov_b32 s59, s67
s_mov_b32 s58, 0
Mp 80,168,176
Rt 152,254,4096
buffer_load_dwordx4 v3, s[12:15], s44 offen offset:1024 lds
Mp 88,168,180
Rt 154,254,4608
s_cmp_eq_u32 s35, 7
s_cmov_b32 s58, 2
Mp 92,172,180
Rt 156,255,4096
)" R"(
s_cmp_eq_u32 s35, 7
s_cmov_b32 s64, 0x40000000
Mp 84,172,176
Rt 158,255,4608
s_add_u32 s44, s43, 13056
buffer_load_dwordx4 v2, s[12:15], s44 offen offset:2048 lds
s_waitcnt lgkmcnt(8)
Mp 32,128,184
Rt 160,252,8192
s_cmp_eq_u32 s35, 7
s_cmov_b32 s66, 0x40000000
s_add_u32 s56, s55, 0x400000
.LzH0:
s_cbranch_execz .LzH13
Mp 40,128,188
Rt 162,252,8704
v_lshrrev_b32 v28, 4, v0
v_and_b32 v29, 7, v0
Mp 44,132,188
Rt 164,253,8192
buffer_load_dwordx4 v3, s[12:15], s44 offen offset:3072 lds
Mp 36,132,184
Rt 166,253,8704
v_xor_b32 v28, v28, v29
v_lshlrev_b32 v28, 4, v28
v_lshrrev_b32 v29, 3, v0
Mp 48,136,184
Rt 168,254,8192
v_lshl_or_b32 v8, v29, 11, v28
v_xor_b32 v9, 64, v8
Mp 56,136,188
Rt 170,254,8704
s_add_u32 s92, s42, 0x100
v_add_u32 v9, 15360, v9
Mp 60,140,188
Rt 172,255,8192
s_add_u32 m0, s41, 65536
v_add_u32 v10, 30720, v8
Mp 52,140,184
Rt 174,255,8704
buffer_load_dwordx4 v240, s[16:19], s92 offen offset:0 lds
s_waitcnt lgkmcnt(8)
Mp 32,144,192
Rt 128,252,12288
v_add_u32 v11, 30720, v9
s_and_b32 s28, s33, 1
s_lshl_b32 s28, s28, 7
Mp 40,144,196
Rt 130,252,12800
v_lshrrev_b32 v29, 5, v0
v_mul_u32_u24 v29, 0x2220, v29
Mp 44,148,196
Rt 132,253,12288
v_xor_b32 v29, v29, v1
v_xor_b32 v12, s28, v29
Mp 36,148,192
Rt 134,253,12800
buffer_load_dwordx4 v241, s[16:19], s92 offen offset:1024 lds
v_xor_b32 v13, 64, v12
Mp 48,152,192
Rt 136,254,12288
v_add_u32 v13, 15360, v13
v_add_u32 v14, 30720, v12
Mp 56,152,196
)" R"(
Rt 138,254,12800
v_add_u32 v15, 30720, v13
v_bfe_u32 v28, v0, 1, 3
Mp 60,156,196
Rt 140,255,12288
v_lshrrev_b32 v29, 4, v0
v_xor_b32 v28, v28, v29
Mp 52,156,192
Rt 142,255,12800
buffer_load_dwordx4 v242, s[16:19], s92 offen offset:2048 lds
s_waitcnt lgkmcnt(8)
Mp 32,160,200
Rt 144,252,16384
v_lshlrev_b32 v28, 4, v28
v_and_b32 v29, 15, v0
v_lshl_or_b32 v28, v29, 7, v28
Mp 40,160,204
Rt 146,252,16896
s_lshr_b32 s28, s33, 1
s_lshl_b32 s28, s28, 12
Mp 44,164,204
Rt 148,253,16384
v_or_b32 v16, s28, v28
v_xor_b32 v17, 64, v16
Mp 36,164,200
Rt 150,253,16896
buffer_load_dwordx4 v243, s[16:19], s92 offen offset:3072 lds
Mp 48,168,200
Rt 152,254,16384
v_bfe_u32 v28, v0, 2, 3
v_lshlrev_b32 v28, 5, v28
Mp 56,168,204
Rt 154,254,16896
s_add_u32 s92, s42, 0x40100
v_and_b32 v29, 3, v0
v_lshl_or_b32 v28, v29, 3, v28
Mp 60,172,204
Rt 156,255,16384
s_add_u32 m0, s41, 69632
v_bfe_u32 v29, v0, 2, 2
Mp 52,172,200
Rt 158,255,16896
buffer_load_dwordx4 v240, s[16:19], s92 offen offset:0 lds
s_waitcnt vmcnt(5) lgkmcnt(8)
Mp 32,128,208
Rt 160,252,20480
ds_read_b128 v[224:227], v26 offset:32768
v_lshl_or_b32 v28, v29, 9, v28
v_lshrrev_b32 v29, 4, v0
Mp 40,128,212
Rt 162,252,20992
v_lshl_or_b32 v28, v29, 12, v28
s_and_b32 s28, s33, 1
Mp 44,132,212
Rt 164,253,20480
ds_read_b128 v[228:231], v26 offset:34816
s_lshl_b32 s28, s28, 8
Mp 36,132,208
Rt 166,253,20992
buffer_load_dwordx4 v241, s[16:19], s92 offen offset:1024 lds
Mp 48,136,208
Rt 168,254,20480
ds_read_b128 v[232:235], v27 offset:32768
s_bitset1_b32 s28, 16
)" R"(
Mp 56,136,212
Rt 170,254,20992
v_or_b32 v18, s28, v28
v_xor_b32 v19, 32, v18
v_xor_b32 v20, 64, v18
Mp 60,140,212
Rt 172,255,20480
ds_read_b128 v[236:239], v27 offset:34816
v_xor_b32 v21, 96, v18
Mp 52,140,208
Rt 174,255,20992
buffer_load_dwordx4 v242, s[16:19], s92 offen offset:2048 lds
s_waitcnt lgkmcnt(12)
Mp 32,144,216
Rt 128,252,24576
v_xor_b32 v22, 128, v18
v_xor_b32 v23, 160, v18
Mp 40,144,220
Rt 130,252,25088
s_add_u32 s92, s42, 0x180
v_xor_b32 v24, 192, v18
Mp 44,148,220
Rt 132,253,24576
s_add_u32 m0, s41, 98304
v_xor_b32 v25, 224, v18
Mp 36,148,216
Rt 134,253,25088
buffer_load_dwordx4 v240, s[16:19], s92 offen offset:0 lds
Mp 48,152,216
Rt 136,254,24576
Mp 56,152,220
Rt 138,254,25088
Mp 60,156,220
Rt 140,255,24576
Mp 52,156,216
Rt 142,255,25088
buffer_load_dwordx4 v241, s[16:19], s92 offen offset:1024 lds
s_waitcnt lgkmcnt(8)
s_barrier
Mp 32,160,224
Rt 144,244,0
Mp 40,160,228
Rt 146,244,512
Mp 44,164,228
Rt 148,245,0
Mp 36,164,224
Rt 150,245,512
buffer_load_dwordx4 v242, s[16:19], s92 offen offset:2048 lds
Mp 48,168,224
Rt 152,246,0
Mp 56,168,228
Rt 154,246,512
Mp 60,172,228
Rt 156,247,0
Mp 52,172,224
Rt 158,247,512
buffer_load_dwordx4 v243, s[16:19], s92 offen offset:3072 lds
s_waitcnt lgkmcnt(8)
s_barrier
Mp 32,128,232
Rt 160,244,4096
Mp 40,128,236
Rt 162,244,4608
s_add_u32 s92, s42, 0x40180
Mp 44,132,236
Rt 164,245,4096
)" R"(
s_add_u32 m0, s41, 102400
Mp 36,132,232
Rt 166,245,4608
buffer_load_dwordx4 v240, s[16:19], s92 offen offset:0 lds
Mp 48,136,232
Rt 168,246,4096
Mp 56,136,236
Rt 170,246,4608
Mp 60,140,236
Rt 172,247,4096
Mp 52,140,232
Rt 174,247,4608
buffer_load_dwordx4 v241, s[16:19], s92 offen offset:1024 lds
s_waitcnt lgkmcnt(8)
Mp 64,144,184
Rt 128,244,8192
Mp 72,144,188
Rt 130,244,8704
Mp 76,148,188
Rt 132,245,8192
Mp 68,148,184
Rt 134,245,8704
buffer_load_dwordx4 v242, s[16:19], s92 offen offset:2048 lds
Mp 80,152,184
Rt 136,246,8192
v_mul_f32_e32 v32, s54, v32
Mp 88,152,188
Rt 138,246,8704
s_add_u32 s92, s42, 0x1c0100
Mp 92,156,188
Rt 140,247,8192
s_add_u32 m0, s41, 32768
Mp 84,156,184
Rt 142,247,8704
buffer_load_dwordx4 v240, s[16:19], s92 offen offset:0 lds
s_waitcnt lgkmcnt(8)
Mp 64,160,192
Rt 144,244,12288
v_mul_f32_e32 v33, s54, v33
Mp 72,160,196
Rt 146,244,12800
Cv 32,32,33
Mp 76,164,196
Rt 148,245,12288
v_accvgpr_write_b32 a64, v32
v_mul_f32_e32 v34, s54, v34
Mp 68,164,192
Rt 150,245,12800
buffer_load_dwordx4 v241, s[16:19], s92 offen offset:1024 lds
Mp 80,168,192
Rt 152,246,12288
v_mul_f32_e32 v35, s54, v35
Mp 88,168,196
Rt 154,246,12800
Cv 34,34,35
v_accvgpr_write_b32 a65, v34
Mp 92,172,196
Rt 156,247,12288
v_mul_f32_e32 v36, s54, v36
Mp 84,172,192
Rt 158,247,12800
buffer_load_dwordx4 v242, s[16:19], s92 offen offset:2048 lds
s_waitcnt lgkmcnt(8)
Mp 64,128,200
)" R"(
Rt 160,244,16384
v_mul_f32_e32 v37, s54, v37
Cv 36,36,37
Mp 72,128,204
Rt 162,244,16896
v_accvgpr_write_b32 a66, v36
Mp 76,132,204
Rt 164,245,16384
v_mul_f32_e32 v38, s54, v38
Mp 68,132,200
Rt 166,245,16896
buffer_load_dwordx4 v243, s[16:19], s92 offen offset:3072 lds
Mp 80,136,200
Rt 168,246,16384
v_mul_f32_e32 v39, s54, v39
Mp 88,136,204
Rt 170,246,16896
s_add_u32 s92, s42, 0x200100
Mp 92,140,204
Rt 172,247,16384
s_add_u32 m0, s41, 36864
Mp 84,140,200
Rt 174,247,16896
buffer_load_dwordx4 v240, s[16:19], s92 offen offset:0 lds
s_waitcnt lgkmcnt(8)
Mp 64,144,208
Rt 128,244,20480
Cv 38,38,39
Mp 72,144,212
Rt 130,244,20992
v_accvgpr_write_b32 a67, v38
Mp 76,148,212
Rt 132,245,20480
v_mul_f32_e32 v40, s54, v40
v_mul_f32_e32 v41, s54, v41
Mp 68,148,208
Rt 134,245,20992
buffer_load_dwordx4 v241, s[16:19], s92 offen offset:1024 lds
Mp 80,152,208
Rt 136,246,20480
Cv 40,40,41
Mp 88,152,212
Rt 138,246,20992
v_accvgpr_write_b32 a68, v40
v_mul_f32_e32 v42, s54, v42
Mp 92,156,212
Rt 140,247,20480
v_mul_f32_e32 v43, s54, v43
Mp 84,156,208
Rt 142,247,20992
buffer_load_dwordx4 v242, s[16:19], s92 offen offset:2048 lds
s_waitcnt lgkmcnt(8)
Mp 64,160,216
Rt 144,244,24576
Cv 42,42,43
v_accvgpr_write_b32 a69, v42
Mp 72,160,220
Rt 146,244,25088
v_mul_f32_e32 v44, s54, v44
Mp 76,164,220
Rt 148,245,24576
v_mul_f32_e32 v45, s54, v45
Mp 68,164,216
Rt 150,245,25088
)" R"(
s_add_u32 s92, s42, 0x1c0180
Cv 44,44,45
Mp 80,168,216
Rt 152,246,24576
s_add_u32 m0, s41, 131072
Mp 88,168,220
Rt 154,246,25088
buffer_load_dwordx4 v240, s[16:19], s92 offen offset:0 lds
Mp 92,172,220
Rt 156,247,24576
v_accvgpr_write_b32 a70, v44
Mp 84,172,216
Rt 158,247,25088
s_waitcnt vmcnt(15) lgkmcnt(8)
s_barrier
Mp 64,128,224
Rt 160,248,0
v_mul_f32_e32 v46, s54, v46
Mp 72,128,228
Rt 162,248,512
buffer_load_dwordx4 v241, s[16:19], s92 offen offset:1024 lds
Mp 76,132,228
Rt 164,249,0
v_mul_f32_e32 v47, s54, v47
Cv 46,46,47
Mp 68,132,224
Rt 166,249,512
v_accvgpr_write_b32 a71, v46
Mp 80,136,224
Rt 168,250,0
v_mul_f32_e32 v48, s54, v48
Mp 88,136,228
Rt 170,250,512
buffer_load_dwordx4 v242, s[16:19], s92 offen offset:2048 lds
Mp 92,140,228
Rt 172,251,0
v_mul_f32_e32 v49, s54, v49
Mp 84,140,224
Rt 174,251,512
s_waitcnt vmcnt(3) lgkmcnt(8)
s_barrier
Mp 64,144,232
Rt 128,248,4096
Cv 48,48,49
v_accvgpr_write_b32 a72, v48
Mp 72,144,236
Rt 130,248,4608
buffer_load_dwordx4 v243, s[16:19], s92 offen offset:3072 lds
Mp 76,148,236
Rt 132,249,4096
v_mul_f32_e32 v50, s54, v50
Mp 68,148,232
Rt 134,249,4608
s_add_u32 s92, s42, 0x200180
v_mul_f32_e32 v51, s54, v51
Mp 80,152,232
Rt 136,250,4096
s_add_u32 m0, s41, 135168
Mp 88,152,236
Rt 138,250,4608
buffer_load_dwordx4 v240, s[16:19], s92 offen offset:0 lds
Mp 92,156,236
Rt 140,251,4096
Cv 50,50,51
Mp 84,156,232
Rt 142,251,4608
)" R"(
v_accvgpr_write_b32 a73, v50
s_waitcnt lgkmcnt(8)
v_mfma_f32_16x16x32_bf16 v[96:99], a[160:163], v[128:131], 0
Rt 144,248,8192
v_mul_f32_e32 v52, s54, v52
v_mfma_f32_16x16x32_bf16 v[104:107], a[160:163], v[132:135], 0
Rt 146,248,8704
buffer_load_dwordx4 v241, s[16:19], s92 offen offset:1024 lds
v_mfma_f32_16x16x32_bf16 v[108:111], a[164:167], v[132:135], 0
Rt 148,249,8192
v_mul_f32_e32 v53, s54, v53
Cv 52,52,53
v_mfma_f32_16x16x32_bf16 v[100:103], a[164:167], v[128:131], 0
Rt 150,249,8704
v_accvgpr_write_b32 a74, v52
v_mfma_f32_16x16x32_bf16 v[112:115], a[168:171], v[128:131], 0
Rt 152,250,8192
v_mul_f32_e32 v54, s54, v54
v_mfma_f32_16x16x32_bf16 v[120:123], a[168:171], v[132:135], 0
Rt 154,250,8704
buffer_load_dwordx4 v242, s[16:19], s92 offen offset:2048 lds
v_mfma_f32_16x16x32_bf16 v[124:127], a[172:175], v[132:135], 0
Rt 156,251,8192
v_mul_f32_e32 v55, s54, v55
v_mfma_f32_16x16x32_bf16 v[116:119], a[172:175], v[128:131], 0
Rt 158,251,8704
s_waitcnt lgkmcnt(8)
Mp 96,128,136
Rt 160,248,12288
Cv 54,54,55
v_accvgpr_write_b32 a75, v54
Mp 104,128,140
Rt 162,248,12800
s_add_u32 s38, s38, 0xffff0080
Mp 108,132,140
Rt 164,249,12288
s_add_u32 m0, s36, 0
Mp 100,132,136
Rt 166,249,12800
buffer_load_dwordx4 v8, s[4:7], s38 offen offset:0 lds
Mp 112,136,136
Rt 168,250,12288
v_mul_f32_e32 v56, s54, v56
Mp 120,136,140
Rt 170,250,12800
v_mul_f32_e32 v57, s54, v57
Mp 124,140,140
Rt 172,251,12288
Cv 56,56,57
Mp 116,140,136
Rt 174,251,12800
buffer_load_dwordx4 v9, s[4:7], s38 offen offset:1024 lds
s_waitcnt lgkmcnt(8)
Mp 96,144,144
Rt 128,248,16384
v_accvgpr_write_b32 a76, v56
Mp 104,144,148
Rt 130,248,16896
v_mul_f32_e32 v58, s54, v58
Mp 108,148,148
Rt 132,249,16384
v_mul_f32_e32 v59, s54, v59
Cv 58,58,59
Mp 100,148,144
)" R"(
Rt 134,249,16896
buffer_load_dwordx4 v10, s[4:7], s38 offen offset:2048 lds
Mp 112,152,144
Rt 136,250,16384
v_accvgpr_write_b32 a77, v58
Mp 120,152,148
Rt 138,250,16896
v_mul_f32_e32 v60, s54, v60
v_mul_f32_e32 v61, s54, v61
Mp 124,156,148
Rt 140,251,16384
Cv 60,60,61
Mp 116,156,144
Rt 142,251,16896
buffer_load_dwordx4 v11, s[4:7], s38 offen offset:3072 lds
s_waitcnt lgkmcnt(8)
Mp 96,160,152
Rt 144,248,20480
v_accvgpr_write_b32 a78, v60
v_mul_f32_e32 v62, s54, v62
Mp 104,160,156
Rt 146,248,20992
s_add_u32 s38, s38, 0x10000
Mp 108,164,156
Rt 148,249,20480
s_add_u32 m0, s36, 4096
Mp 100,164,152
Rt 150,249,20992
buffer_load_dwordx4 v8, s[4:7], s38 offen offset:0 lds
Mp 112,168,152
Rt 152,250,20480
v_mul_f32_e32 v63, s54, v63
Mp 120,168,156
Rt 154,250,20992
Cv 62,62,63
Mp 124,172,156
Rt 156,251,20480
v_accvgpr_write_b32 a79, v62
Mp 116,172,152
Rt 158,251,20992
buffer_load_dwordx4 v9, s[4:7], s38 offen offset:1024 lds
s_waitcnt lgkmcnt(8)
Mp 96,128,160
Rt 160,248,24576
v_mul_f32_e32 v64, s54, v64
Mp 104,128,164
Rt 162,248,25088
v_mul_f32_e32 v65, s54, v65
Mp 108,132,164
Rt 164,249,24576
Cv 64,64,65
v_accvgpr_write_b32 a80, v64
Mp 100,132,160
Rt 166,249,25088
buffer_load_dwordx4 v10, s[4:7], s38 offen offset:2048 lds
Mp 112,136,160
Rt 168,250,24576
v_mul_f32_e32 v66, s54, v66
Mp 120,136,164
Rt 170,250,25088
v_mul_f32_e32 v67, s54, v67
Cv 66,66,67
Mp 124,140,164
Rt 172,251,24576
)" R"(
v_accvgpr_write_b32 a81, v66
Mp 116,140,160
Rt 174,251,25088
buffer_load_dwordx4 v11, s[4:7], s38 offen offset:3072 lds
s_waitcnt lgkmcnt(8)
Mp 96,144,168
Rt 128,244,32768
v_mul_f32_e32 v68, s54, v68
v_mul_f32_e32 v69, s54, v69
Mp 104,144,172
Rt 130,244,33280
Cv 68,68,69
s_mov_b32 s85, 0x1508800
Mp 108,148,172
Rt 132,245,32768
v_accvgpr_write_b32 a82, v68
v_and_b32 v2, 7, v0
Mp 100,148,168
Rt 134,245,33280
v_mul_f32_e32 v70, s54, v70
v_mul_f32_e32 v71, s54, v71
v_lshlrev_b32 v2, 7, v2
Mp 112,152,168
Rt 136,246,32768
Cv 70,70,71
Mp 120,152,172
Rt 138,246,33280
v_accvgpr_write_b32 a83, v70
buffer_load_dword v4, v2, s[20:23], s85 offen sc0 sc1
Mp 124,156,172
Rt 140,247,32768
v_mul_f32_e32 v72, s54, v72
Mp 116,156,168
Rt 142,247,33280
s_waitcnt lgkmcnt(8)
s_barrier
Mp 96,160,176
Rt 144,244,36864
v_mul_f32_e32 v73, s54, v73
Mp 104,160,180
Rt 146,244,37376
s_add_u32 s39, s39, 0x40000
Mp 108,164,180
Rt 148,245,36864
s_add_u32 m0, s37, 0
Cv 72,72,73
Mp 100,164,176
Rt 150,245,37376
buffer_load_dwordx4 v12, s[8:11], s39 offen offset:0 lds
Mp 112,168,176
Rt 152,246,36864
v_accvgpr_write_b32 a84, v72
Mp 120,168,180
Rt 154,246,37376
v_mul_f32_e32 v74, s54, v74
v_mul_f32_e32 v75, s54, v75
Mp 124,172,180
Rt 156,247,36864
Cv 74,74,75
.LzH1:
s_cbranch_execz .LzH16
Mp 116,172,176
Rt 158,247,37376
s_waitcnt lgkmcnt(8)
Mp 96,128,184
)" R"(
Rt 160,244,40960
v_accvgpr_write_b32 a85, v74
v_mul_f32_e32 v76, s54, v76
Mp 104,128,188
Rt 162,244,41472
buffer_load_dwordx4 v13, s[8:11], s39 offen offset:1024 lds
Mp 108,132,188
Rt 164,245,40960
v_mul_f32_e32 v77, s54, v77
Mp 100,132,184
Rt 166,245,41472
Cv 76,76,77
v_accvgpr_write_b32 a86, v76
Mp 112,136,184
Rt 168,246,40960
v_mul_f32_e32 v78, s54, v78
Mp 120,136,188
Rt 170,246,41472
v_mul_f32_e32 v79, s54, v79
Mp 124,140,188
Rt 172,247,40960
Cv 78,78,79
Mp 116,140,184
Rt 174,247,41472
buffer_load_dwordx4 v14, s[8:11], s39 offen offset:2048 lds
s_waitcnt lgkmcnt(8)
Mp 96,144,192
Rt 128,244,45056
v_accvgpr_write_b32 a87, v78
Mp 104,144,196
Rt 130,244,45568
v_mul_f32_e32 v80, s54, v80
Mp 108,148,196
Rt 132,245,45056
v_mul_f32_e32 v81, s54, v81
Cv 80,80,81
Mp 100,148,192
Rt 134,245,45568
v_accvgpr_write_b32 a88, v80
Mp 112,152,192
Rt 136,246,45056
v_mul_f32_e32 v82, s54, v82
Mp 120,152,196
Rt 138,246,45568
buffer_load_dwordx4 v15, s[8:11], s39 offen offset:3072 lds
Mp 124,156,196
Rt 140,247,45056
v_mul_f32_e32 v83, s54, v83
Mp 116,156,192
Rt 142,247,45568
s_waitcnt lgkmcnt(8)
Mp 96,160,200
Rt 144,244,49152
Cv 82,82,83
v_accvgpr_write_b32 a89, v82
Mp 104,160,204
Rt 146,244,49664
s_add_u32 s39, s39, 0x40000
Mp 108,164,204
Rt 148,245,49152
s_add_u32 m0, s37, 16384
Mp 100,164,200
Rt 150,245,49664
buffer_load_dwordx4 v12, s[8:11], s39 offen offset:0 lds
)" R"(
Mp 112,168,200
Rt 152,246,49152
v_mul_f32_e32 v84, s54, v84
Mp 120,168,204
Rt 154,246,49664
v_mul_f32_e32 v85, s54, v85
Mp 124,172,204
Rt 156,247,49152
Cv 84,84,85
Mp 116,172,200
Rt 158,247,49664
v_accvgpr_write_b32 a90, v84
s_waitcnt lgkmcnt(8)
Mp 96,128,208
Rt 160,244,53248
v_mul_f32_e32 v86, s54, v86
Mp 104,128,212
Rt 162,244,53760
buffer_load_dwordx4 v13, s[8:11], s39 offen offset:1024 lds
Mp 108,132,212
Rt 164,245,53248
v_mul_f32_e32 v87, s54, v87
Cv 86,86,87
Mp 100,132,208
Rt 166,245,53760
v_accvgpr_write_b32 a91, v86
Mp 112,136,208
Rt 168,246,53248
v_mul_f32_e32 v88, s54, v88
Mp 120,136,212
Rt 170,246,53760
v_mul_f32_e32 v89, s54, v89
Cv 88,88,89
Mp 124,140,212
Rt 172,247,53248
v_accvgpr_write_b32 a92, v88
Mp 116,140,208
Rt 174,247,53760
buffer_load_dwordx4 v14, s[8:11], s39 offen offset:2048 lds
s_waitcnt lgkmcnt(8)
Mp 96,144,216
Rt 128,244,57344
v_mul_f32_e32 v90, s54, v90
v_mul_f32_e32 v91, s54, v91
Mp 104,144,220
Rt 130,244,57856
Cv 90,90,91
Mp 108,148,220
Rt 132,245,57344
v_accvgpr_write_b32 a93, v90
Mp 100,148,216
Rt 134,245,57856
v_mul_f32_e32 v92, s54, v92
v_mul_f32_e32 v93, s54, v93
Mp 112,152,216
Rt 136,246,57344
Cv 92,92,93
Mp 120,152,220
Rt 138,246,57856
buffer_load_dwordx4 v15, s[8:11], s39 offen offset:3072 lds
Mp 124,156,220
Rt 140,247,57344
v_accvgpr_write_b32 a94, v92
Mp 116,156,216
)" R"(
Rt 142,247,57856
v_mul_f32_e32 v94, s54, v94
s_waitcnt lgkmcnt(8)
Mp 96,160,224
Rt 144,248,32768
v_mul_f32_e32 v95, s54, v95
Mp 104,160,228
Rt 146,248,33280
Cv 94,94,95
Mp 108,164,228
Rt 148,249,32768
v_accvgpr_write_b32 a95, v94
Mp 100,164,224
Rt 150,249,33280
s_lshl1_add_u32 s88, s34, s91
Mp 112,168,224
Rt 152,250,32768
s_lshl_b32 s88, s88, 12
Mp 120,168,228
Rt 154,250,33280
s_and_b32 s29, s38, 0xfffe0000
Mp 124,172,228
Rt 156,251,32768
s_add_u32 s88, s88, s29
Mp 116,172,224
Rt 158,251,33280
s_waitcnt vmcnt(8) lgkmcnt(8)
s_barrier
Mp 96,128,232
Rt 160,248,36864
v_min_u32 v28, 31, v0
v_xor_b32 v4, s3, v4
v_cmp_gt_u32_e32 vcc, 2, v4
s_andn2_b32 s28, 0xff, vcc_lo
s_cbranch_scc1 .Lzsp13
.Lzok13:
Mp 104,128,236
Rt 162,248,37376
s_add_u32 s38, s38, 0xffff0080
v_xor_b32 v4, s3, v4
Mp 108,132,236
Rt 164,249,36864
s_add_u32 m0, s36, 32768
v_and_b32 v4, 1, v4
Mp 100,132,232
Rt 166,249,37376
buffer_load_dwordx4 v8, s[4:7], s38 offen offset:0 lds
Mp 112,136,232
Rt 168,250,36864
v_lshlrev_b32 v28, 7, v28
Mp 120,136,236
Rt 170,250,37376
v_cmp_eq_u32_e32 vcc, 1, v4
s_mov_b32 s85, vcc_lo
Mp 124,140,236
Rt 172,251,36864
s_and_b32 s28, s2, 7
s_lshr_b32 s28, s85, s28
Mp 116,140,232
Rt 174,251,37376
s_xor_b32 s28, s28, s34
s_and_b32 s28, s28, 1
s_waitcnt lgkmcnt(8)
v_mfma_f32_16x16x32_bf16 v[64:67], a[144:147], v[128:131], 0
Rt 128,248,40960
)" R"(
s_lshl_b32 s84, s28, 13
s_add_u32 s49, s49, s84
v_mfma_f32_16x16x32_bf16 v[72:75], a[144:147], v[132:135], 0
Rt 130,248,41472
buffer_load_dwordx4 v9, s[4:7], s38 offen offset:1024 lds
v_mfma_f32_16x16x32_bf16 v[76:79], a[148:151], v[132:135], 0
Rt 132,249,40960
s_add_u32 s50, s50, s84
s_add_u32 s60, s50, 7168
v_mfma_f32_16x16x32_bf16 v[68:71], a[148:151], v[128:131], 0
Rt 134,249,41472
s_add_u32 s52, s52, s84
s_add_u32 s65, s65, s84
v_mfma_f32_16x16x32_bf16 v[80:83], a[152:155], v[128:131], 0
Rt 136,250,40960
v_mul_f32_e32 v96, s54, v96
s_lshr_b32 s28, s2, 6
s_lshl_b32 s28, s28, 14
v_mfma_f32_16x16x32_bf16 v[88:91], a[152:155], v[132:135], 0
Rt 138,250,41472
v_mul_f32_e32 v97, s54, v97
s_add_u32 s28, s28, s50
v_mfma_f32_16x16x32_bf16 v[92:95], a[156:159], v[132:135], 0
Rt 140,251,40960
Cv 96,96,97
s_bfe_u32 s29, s2, 0x20004
v_mfma_f32_16x16x32_bf16 v[84:87], a[156:159], v[128:131], 0
Rt 142,251,41472
buffer_load_dwordx4 v10, s[4:7], s38 offen offset:2048 lds
s_waitcnt lgkmcnt(8)
Mp 64,160,136
Rt 144,248,45056
v_accvgpr_write_b32 a96, v96
s_lshl2_add_u32 s29, s29, s33
Mp 72,160,140
Rt 146,248,45568
v_mul_f32_e32 v98, s54, v98
s_lshl_b32 s29, s29, 9
Mp 76,164,140
Rt 148,249,45056
v_mul_f32_e32 v99, s54, v99
s_add_u32 s28, s28, s29
Mp 68,164,136
Rt 150,249,45568
Cv 98,98,99
v_min_u32 v4, 3, v0
Mp 80,168,136
Rt 152,250,45056
v_accvgpr_write_b32 a97, v98
v_lshlrev_b32 v4, 7, v4
Mp 88,168,140
Rt 154,250,45568
buffer_load_dwordx4 v11, s[4:7], s38 offen offset:3072 lds
Mp 92,172,140
Rt 156,251,45056
v_mul_f32_e32 v100, s54, v100
Mp 84,172,136
Rt 158,251,45568
s_waitcnt lgkmcnt(8)
Mp 64,128,144
Rt 160,248,49152
v_mul_f32_e32 v101, s54, v101
Mp 72,128,148
Rt 162,248,49664
)" R"(
s_add_u32 s38, s38, 0x10000
Mp 76,132,148
Rt 164,249,49152
s_add_u32 m0, s36, 36864
Mp 68,132,144
Rt 166,249,49664
buffer_load_dwordx4 v8, s[4:7], s38 offen offset:0 lds
Mp 80,136,144
Rt 168,250,49152
Cv 100,100,101
buffer_load_dword v4, v4, s[20:23], s28 offen
Mp 88,136,148
Rt 170,250,49664
v_accvgpr_write_b32 a98, v100
s_lshl1_add_u32 s28, s36, s84
Mp 92,140,148
Rt 172,251,49152
v_mul_f32_e32 v102, s54, v102
v_add_u32 v2, s28, v1
Mp 84,140,144
Rt 174,251,49664
v_add_u32 v3, 0x1000, v2
s_waitcnt lgkmcnt(8)
Mp 64,144,152
Rt 128,248,53248
v_mul_f32_e32 v103, s54, v103
s_lshl_b32 s28, s34, 13
Mp 72,144,156
Rt 130,248,53760
buffer_load_dwordx4 v9, s[4:7], s38 offen offset:1024 lds
s_xor_b32 s28, s28, s84
Mp 76,148,156
Rt 132,249,53248
Cv 102,102,103
s_and_b32 s28, s28, 0x2000
Mp 68,148,152
Rt 134,249,53760
v_accvgpr_write_b32 a99, v102
s_xor_b32 s73, s73, s28
Mp 80,152,152
Rt 136,250,53248
v_mul_f32_e32 v104, s54, v104
s_xor_b32 s72, s72, s28
Mp 88,152,156
Rt 138,250,53760
v_mul_f32_e32 v105, s54, v105
Mp 92,156,156
Rt 140,251,53248
Cv 104,104,105
Mp 84,156,152
Rt 142,251,53760
buffer_load_dwordx4 v10, s[4:7], s38 offen offset:2048 lds
s_waitcnt lgkmcnt(8)
Mp 64,160,160
Rt 144,248,57344
v_accvgpr_write_b32 a100, v104
Mp 72,160,164
Rt 146,248,57856
v_mul_f32_e32 v106, s54, v106
Mp 76,164,164
Rt 148,249,57344
v_mul_f32_e32 v107, s54, v107
Mp 68,164,160
Rt 150,249,57856
)" R"(
Cv 106,106,107
Mp 80,168,160
Rt 152,250,57344
v_accvgpr_write_b32 a101, v106
Mp 88,168,164
Rt 154,250,57856
buffer_load_dwordx4 v11, s[4:7], s38 offen offset:3072 lds
Mp 92,172,164
Rt 156,251,57344
v_mul_f32_e32 v108, s54, v108
Mp 84,172,160
Rt 158,251,57856
s_waitcnt lgkmcnt(8)
Mp 64,128,168
Rt 160,252,0
v_mul_f32_e32 v109, s54, v109
Mp 72,128,172
Rt 162,252,512
Cv 108,108,109
Mp 76,132,172
Rt 164,253,0
v_accvgpr_write_b32 a102, v108
Mp 68,132,168
Rt 166,253,512
v_mul_f32_e32 v110, s54, v110
Mp 80,136,168
Rt 168,254,0
v_mul_f32_e32 v111, s54, v111
Mp 88,136,172
Rt 170,254,512
Cv 110,110,111
Mp 92,140,172
Rt 172,255,0
v_accvgpr_write_b32 a103, v110
Mp 84,140,168
Rt 174,255,512
s_waitcnt vmcnt(13) lgkmcnt(8)
s_barrier
Mp 64,144,176
Rt 128,252,4096
v_mul_f32_e32 v112, s54, v112
Mp 72,144,180
Rt 130,252,4608
s_add_u32 s39, s39, 0x40000
Mp 76,148,180
Rt 132,253,4096
s_add_u32 m0, s37, 32768
Mp 68,148,176
Rt 134,253,4608
buffer_load_dwordx4 v12, s[8:11], s39 offen offset:0 lds
Mp 80,152,176
Rt 136,254,4096
v_mul_f32_e32 v113, s54, v113
Mp 88,152,180
Rt 138,254,4608
Cv 112,112,113
Mp 92,156,180
Rt 140,255,4096
v_accvgpr_write_b32 a104, v112
Mp 84,156,176
Rt 142,255,4608
s_waitcnt lgkmcnt(8)
Mp 64,160,184
Rt 144,252,8192
v_mul_f32_e32 v114, s54, v114
)" R"(
Mp 72,160,188
Rt 146,252,8704
buffer_load_dwordx4 v13, s[8:11], s39 offen offset:1024 lds
Mp 76,164,188
Rt 148,253,8192
v_mul_f32_e32 v115, s54, v115
Mp 68,164,184
Rt 150,253,8704
Cv 114,114,115
Mp 80,168,184
Rt 152,254,8192
v_accvgpr_write_b32 a105, v114
Mp 88,168,188
Rt 154,254,8704
v_mul_f32_e32 v116, s54, v116
Mp 92,172,188
Rt 156,255,8192
v_mul_f32_e32 v117, s54, v117
Mp 84,172,184
Rt 158,255,8704
buffer_load_dwordx4 v14, s[8:11], s39 offen offset:2048 lds
s_waitcnt lgkmcnt(8)
Mp 64,128,192
Rt 160,252,12288
Cv 116,116,117
Mp 72,128,196
Rt 162,252,12800
v_accvgpr_write_b32 a106, v116
Mp 76,132,196
Rt 164,253,12288
v_mul_f32_e32 v118, s54, v118
Mp 68,132,192
Rt 166,253,12800
v_mul_f32_e32 v119, s54, v119
Mp 80,136,192
Rt 168,254,12288
Cv 118,118,119
Mp 88,136,196
Rt 170,254,12800
buffer_load_dwordx4 v15, s[8:11], s39 offen offset:3072 lds
Mp 92,140,196
Rt 172,255,12288
v_accvgpr_write_b32 a107, v118
Mp 84,140,192
Rt 174,255,12800
s_waitcnt lgkmcnt(8)
Mp 64,144,200
Rt 128,252,16384
v_mul_f32_e32 v120, s54, v120
Mp 72,144,204
Rt 130,252,16896
s_add_u32 s39, s39, 0x40000
Mp 76,148,204
Rt 132,253,16384
s_add_u32 m0, s37, 49152
Mp 68,148,200
Rt 134,253,16896
buffer_load_dwordx4 v12, s[8:11], s39 offen offset:0 lds
Mp 80,152,200
Rt 136,254,16384
v_mul_f32_e32 v121, s54, v121
Mp 88,152,204
Rt 138,254,16896
Cv 120,120,121
)" R"(
Mp 92,156,204
Rt 140,255,16384
v_accvgpr_write_b32 a108, v120
Mp 84,156,200
Rt 142,255,16896
s_waitcnt lgkmcnt(8)
Mp 64,160,208
Rt 144,252,20480
Rt 0,18,0
Mp 72,160,212
Rt 146,252,20992
buffer_load_dwordx4 v13, s[8:11], s39 offen offset:1024 lds
Rt 2,18,2048
Mp 76,164,212
Rt 148,253,20480
Rt 4,19,0
Mp 68,164,208
Rt 150,253,20992
Rt 6,19,2048
Mp 80,168,208
Rt 152,254,20480
Rt 8,20,0
Mp 88,168,212
Rt 154,254,20992
Rt 10,20,2048
Mp 92,172,212
Rt 156,255,20480
Rt 12,21,0
Mp 84,172,208
Rt 158,255,20992
buffer_load_dwordx4 v14, s[8:11], s39 offen offset:2048 lds
Rt 14,21,2048
Mp 64,128,216
Rt 160,252,24576
Rt 16,22,0
Mp 72,128,220
Rt 162,252,25088
Rt 18,22,2048
Mp 76,132,220
Rt 164,253,24576
Rt 20,23,0
Mp 68,132,216
Rt 166,253,25088
Rt 22,23,2048
Mp 80,136,216
Rt 168,254,24576
Rt 24,24,0
Mp 88,136,220
Rt 170,254,25088
buffer_load_dwordx4 v15, s[8:11], s39 offen offset:3072 lds
Rt 26,24,2048
Mp 92,140,220
Rt 172,255,24576
Rt 28,25,0
Mp 84,140,216
Rt 174,255,25088
Rt 30,25,2048
Mp 64,144,224
Rb 32,16,0
v_mul_f32_e32 v122, s54, v122
Mp 72,144,228
Rb 36,16,2048
v_mul_f32_e32 v123, s54, v123
Mp 76,148,228
)" R"(
Rb 40,16,8192
Cv 122,122,123
Mp 68,148,224
Rb 44,16,10240
v_accvgpr_write_b32 a109, v122
Mp 80,152,224
Rb 48,16,16384
v_mul_f32_e32 v124, s54, v124
Mp 88,152,228
Rb 52,16,18432
v_mul_f32_e32 v125, s54, v125
Mp 92,156,228
Rb 56,16,24576
Cv 124,124,125
Mp 84,156,224
Rb 60,16,26624
s_waitcnt lgkmcnt(9)
Mp 64,160,232
v_accvgpr_write_b32 a110, v124
v_mul_f32_e32 v126, s54, v126
Mp 72,160,236
v_mul_f32_e32 v127, s54, v127
Cv 126,126,127
Mp 76,164,236
v_accvgpr_write_b32 a111, v126
Mp 68,164,232
buffer_load_dword a254, v28, s[4:7], s88 offen
Mp 80,168,232
Mp 88,168,236
Mp 92,172,236
Mp 84,172,232
s_cmp_eq_u32 s91, 1
s_waitcnt vmcnt(18) lgkmcnt(0)
s_barrier
s_cbranch_scc1 .LzBV
Mz 32,0,32
Rt 128,18,16384
v_mul_f32_e32 v64, s54, v64
Mz 96,0,40
v_mul_f32_e32 v65, s54, v65
Cv 64,64,65
Mz 128,0,44
Rt 130,18,18432
v_accvgpr_write_b32 a112, v64
Mz 160,0,48
s_add_u32 s39, s39, 0x40000
v_mul_f32_e32 v66, s54, v66
Mz 192,0,52
Rt 132,19,16384
s_add_u32 m0, s37, 0
Mz 224,0,56
buffer_load_dwordx4 v12, s[8:11], s39 offen offset:0 lds
v_mfma_f32_16x16x32_bf16 a[192:195], a[0:3], a[60:63], 0
Rt 134,19,18432
v_mul_f32_e32 v67, s54, v67
.LzH2:
s_cbranch_execz .LzH11
v_mfma_f32_16x16x32_bf16 a[196:199], a[4:7], a[60:63], 0
Cv 66,66,67
v_accvgpr_write_b32 a113, v66
Mz 228,4,56
Rt 136,20,16384
v_mul_f32_e32 v68, s54, v68
Mz 196,4,52
v_mul_f32_e32 v69, s54, v69
)" R"(
Cv 68,68,69
buffer_store_dwordx4 a[64:67], v2, s[20:23], s51 offen offset:0 sc0 sc1
Mz 164,4,48
Rt 138,20,18432
v_accvgpr_write_b32 a114, v68
Mz 132,4,44
v_mul_f32_e32 v70, s54, v70
v_mul_f32_e32 v71, s54, v71
Mz 100,4,40
Rt 140,21,16384
Cv 70,70,71
Mz 36,4,32
buffer_load_dwordx4 v13, s[8:11], s39 offen offset:1024 lds
Mz 40,8,32
Rt 142,21,18432
v_accvgpr_write_b32 a115, v70
Mz 104,8,40
v_mul_f32_e32 v72, s54, v72
v_mul_f32_e32 v73, s54, v73
Mz 136,8,44
Rt 144,22,16384
Cv 72,72,73
Mz 168,8,48
v_accvgpr_write_b32 a116, v72
v_mul_f32_e32 v74, s54, v74
buffer_store_dwordx4 a[68:71], v2, s[20:23], s51 offen offset:1024 sc0 sc1
Mz 200,8,52
Rt 146,22,18432
v_mul_f32_e32 v75, s54, v75
Mz 232,8,56
Cv 74,74,75
v_accvgpr_write_b32 a117, v74
v_mfma_f32_16x16x32_bf16 a[200:203], a[8:11], a[60:63], 0
Rt 148,23,16384
v_mul_f32_e32 v76, s54, v76
v_mfma_f32_16x16x32_bf16 a[204:207], a[12:15], a[60:63], 0
buffer_load_dwordx4 v14, s[8:11], s39 offen offset:2048 lds
Mz 236,12,56
Rt 150,23,18432
v_mul_f32_e32 v77, s54, v77
Mz 204,12,52
Cv 76,76,77
v_accvgpr_write_b32 a118, v76
Mz 172,12,48
Rt 152,24,16384
v_mul_f32_e32 v78, s54, v78
Mz 140,12,44
v_mul_f32_e32 v79, s54, v79
Cv 78,78,79
buffer_store_dwordx4 a[72:75], v2, s[20:23], s51 offen offset:2048 sc0 sc1
Mz 108,12,40
Rt 154,24,18432
v_accvgpr_write_b32 a119, v78
Mz 44,12,32
v_mul_f32_e32 v80, s54, v80
v_mul_f32_e32 v81, s54, v81
Mz 48,16,32
Rt 156,25,16384
Cv 80,80,81
Mz 112,16,40
buffer_load_dwordx4 v15, s[8:11], s39 offen offset:3072 lds
Mz 144,16,44
Rt 158,25,18432
v_accvgpr_write_b32 a120, v80
)" R"(
Mz 176,16,48
v_mul_f32_e32 v82, s54, v82
v_mul_f32_e32 v83, s54, v83
Mz 208,16,52
Rb 160,17,0
Cv 82,82,83
Mz 240,16,56
v_accvgpr_write_b32 a121, v82
v_mul_f32_e32 v84, s54, v84
buffer_store_dwordx4 a[76:79], v2, s[20:23], s51 offen offset:3072 sc0 sc1
v_mfma_f32_16x16x32_bf16 a[208:211], a[16:19], a[60:63], 0
Rb 164,17,2048
v_mul_f32_e32 v85, s54, v85
v_mfma_f32_16x16x32_bf16 a[212:215], a[20:23], a[60:63], 0
Cv 84,84,85
v_accvgpr_write_b32 a122, v84
Mz 244,20,56
Rb 168,17,8192
v_mul_f32_e32 v86, s54, v86
Mz 212,20,52
v_mul_f32_e32 v87, s54, v87
Cv 86,86,87
Mz 180,20,48
Rb 172,17,10240
v_accvgpr_write_b32 a123, v86
Mz 148,20,44
v_mul_f32_e32 v88, s54, v88
v_mul_f32_e32 v89, s54, v89
Mz 116,20,40
Rb 176,17,16384
Cv 88,88,89
Mz 52,20,32
v_accvgpr_write_b32 a124, v88
v_mul_f32_e32 v90, s54, v90
buffer_store_dwordx4 a[80:83], v3, s[20:23], s51 offen offset:0 sc0 sc1
Mz 56,24,32
Rb 180,17,18432
v_mul_f32_e32 v91, s54, v91
Mz 120,24,40
Cv 90,90,91
v_accvgpr_write_b32 a125, v90
Mz 152,24,44
Rb 184,17,24576
v_mul_f32_e32 v92, s54, v92
Mz 184,24,48
v_mul_f32_e32 v93, s54, v93
Cv 92,92,93
Mz 216,24,52
Rb 188,17,26624
v_accvgpr_write_b32 a126, v92
Mz 248,24,56
v_mul_f32_e32 v94, s54, v94
v_mul_f32_e32 v95, s54, v95
v_mfma_f32_16x16x32_bf16 a[216:219], a[24:27], a[60:63], 0
Cv 94,94,95
v_accvgpr_write_b32 a127, v94
v_mfma_f32_16x16x32_bf16 a[220:223], a[28:31], a[60:63], 0
buffer_store_dwordx4 a[84:87], v3, s[20:23], s51 offen offset:1024 sc0 sc1
Mz 252,28,56
Mz 220,28,52
Mz 188,28,48
Mz 156,28,44
Mz 124,28,40
Mz 60,28,32
)" R"(
Mz 64,0,36
Mz 68,4,36
buffer_store_dwordx4 a[88:91], v3, s[20:23], s51 offen offset:2048 sc0 sc1
Mz 72,8,36
Mz 76,12,36
Mz 80,16,36
Mz 84,20,36
Mz 88,24,36
Mz 92,28,36
s_waitcnt vmcnt(16) lgkmcnt(0)
s_barrier
Mv 32,128,160
Rt 0,18,32768
Mv 64,128,164
s_add_u32 s38, s38, 0xffff0080
Mv 96,128,168
Rt 2,18,34816
s_add_u32 m0, s36, 0
Mv 128,128,172
buffer_load_dwordx4 v8, s[4:7], s38 offen offset:0 lds
Mv 160,128,176
Rt 4,19,32768
Mv 192,128,180
Mv 224,128,184
Rt 6,19,34816
Ma 192,128,188
buffer_load_dwordx4 v9, s[4:7], s38 offen offset:1024 lds
Ma 196,132,188
Rt 8,20,32768
Mv 228,132,184
Mv 196,132,180
Rt 10,20,34816
Mv 164,132,176
buffer_load_dwordx4 v10, s[4:7], s38 offen offset:2048 lds
Mv 132,132,172
Rt 12,21,32768
Mv 100,132,168
Mv 68,132,164
Rt 14,21,34816
Mv 36,132,160
buffer_load_dwordx4 v11, s[4:7], s38 offen offset:3072 lds
Mv 40,136,160
Rt 16,22,32768
Mv 72,136,164
s_add_u32 s38, s38, 0x10000
Mv 104,136,168
Rt 18,22,34816
s_add_u32 m0, s36, 4096
Mv 136,136,172
buffer_load_dwordx4 v8, s[4:7], s38 offen offset:0 lds
Mv 168,136,176
Rt 20,23,32768
Mv 200,136,180
Mv 232,136,184
Rt 22,23,34816
Ma 200,136,188
buffer_load_dwordx4 v9, s[4:7], s38 offen offset:1024 lds
Ma 204,140,188
Rt 24,24,32768
Mv 236,140,184
Mv 204,140,180
Rt 26,24,34816
Mv 172,140,176
buffer_load_dwordx4 v10, s[4:7], s38 offen offset:2048 lds
Mv 140,140,172
)" R"(
Rt 28,25,32768
Mv 108,140,168
Mv 76,140,164
Rt 30,25,34816
Mv 44,140,160
buffer_load_dwordx4 v11, s[4:7], s38 offen offset:3072 lds
Mv 48,144,160
Rb 32,16,32768
Mv 80,144,164
Mv 112,144,168
Rb 36,16,34816
Mv 144,144,172
s_add_u32 s39, s39, 0x40000
Mv 176,144,176
Rb 40,16,40960
s_add_u32 m0, s37, 16384
Mv 208,144,180
buffer_load_dwordx4 v12, s[8:11], s39 offen offset:0 lds
Mv 240,144,184
Rb 44,16,43008
Ma 208,144,188
Ma 212,148,188
Rb 48,16,49152
Mv 244,148,184
Mv 212,148,180
Rb 52,16,51200
Mv 180,148,176
buffer_load_dwordx4 v13, s[8:11], s39 offen offset:1024 lds
Mv 148,148,172
Rb 56,16,57344
Mv 116,148,168
Mv 84,148,164
Rb 60,16,59392
Mv 52,148,160
Mv 56,152,160
Mv 88,152,164
buffer_load_dwordx4 v14, s[8:11], s39 offen offset:2048 lds
Mv 120,152,168
Mv 152,152,172
Mv 184,152,176
Mv 216,152,180
Mv 248,152,184
Ma 216,152,188
buffer_load_dwordx4 v15, s[8:11], s39 offen offset:3072 lds
Ma 220,156,188
Mv 252,156,184
Mv 220,156,180
Mv 188,156,176
buffer_store_dwordx4 a[92:95], v3, s[20:23], s51 offen offset:3072 sc0 sc1
Mv 156,156,172
Mv 124,156,168
Mv 92,156,164
Mv 60,156,160
s_waitcnt vmcnt(25) lgkmcnt(0)
s_barrier
Mv 32,0,32
Rt 128,18,49152
Mv 64,0,36
Mv 96,0,40
Rt 130,18,51200
Mv 128,0,44
s_add_u32 s39, s39, 0x40000
Mv 160,0,48
Rt 132,19,49152
s_add_u32 m0, s37, 32768
)" R"(
Mv 192,0,52
buffer_load_dwordx4 v12, s[8:11], s39 offen offset:0 lds
Mv 224,0,56
Rt 134,19,51200
Ma 192,0,60
Ma 196,4,60
Rt 136,20,49152
Mv 228,4,56
s_add_u32 s51, s51, 0x10000
buffer_store_dwordx4 a[96:99], v2, s[20:23], s51 offen offset:0 sc0 sc1
Mv 196,4,52
Rt 138,20,51200
Mv 164,4,48
Mv 132,4,44
Rt 140,21,49152
Mv 100,4,40
buffer_load_dwordx4 v13, s[8:11], s39 offen offset:1024 lds
Mv 68,4,36
Rt 142,21,51200
Mv 36,4,32
Mv 40,8,32
Rt 144,22,49152
Mv 72,8,36
buffer_store_dwordx4 a[100:103], v2, s[20:23], s51 offen offset:1024 sc0 sc1
Mv 104,8,40
Rt 146,22,51200
Mv 136,8,44
Mv 168,8,48
Rt 148,23,49152
Mv 200,8,52
buffer_load_dwordx4 v14, s[8:11], s39 offen offset:2048 lds
Mv 232,8,56
Rt 150,23,51200
Ma 200,8,60
Ma 204,12,60
Rt 152,24,49152
Mv 236,12,56
buffer_store_dwordx4 a[104:107], v2, s[20:23], s51 offen offset:2048 sc0 sc1
Mv 204,12,52
Rt 154,24,51200
Mv 172,12,48
Mv 140,12,44
Rt 156,25,49152
Mv 108,12,40
buffer_load_dwordx4 v15, s[8:11], s39 offen offset:3072 lds
Mv 76,12,36
Rt 158,25,51200
Mv 44,12,32
Mv 48,16,32
Rb 160,17,32768
Mv 80,16,36
buffer_store_dwordx4 a[108:111], v2, s[20:23], s51 offen offset:3072 sc0 sc1
Mv 112,16,40
Rb 164,17,34816
s_mov_b64 s[16:17], s[70:71]
Mv 144,16,44
s_mov_b32 s18, 0x188000
Mv 176,16,48
Rb 168,17,40960
Mv 208,16,52
s_cmp_eq_u32 s33, 0
s_cmov_b32 s18, 0x380000
Mv 240,16,56
Rb 172,17,43008
)" R"(
s_mul_i32 s43, s2, 3072
Ma 208,16,60
s_cmp_eq_u32 s33, 0
s_cselect_b32 s44, 3, 6
Ma 212,20,60
Rb 176,17,49152
s_mul_i32 s44, s44, 0x60000
Mv 244,20,56
buffer_store_dwordx4 a[112:115], v3, s[20:23], s51 offen offset:0 sc0 sc1
Mv 212,20,52
Rb 180,17,51200
s_sub_u32 s43, s43, s44
Mv 180,20,48
v_min_u32 v26, 23, v0
v_lshlrev_b32 v26, 7, v26
Mv 148,20,44
Rb 184,17,57344
v_and_b32 v5, 7, v0
Mv 116,20,40
v_lshlrev_b32 v5, 7, v5
Mv 84,20,36
Rb 188,17,59392
Mv 52,20,32
Mv 56,24,32
Mv 88,24,36
buffer_store_dwordx4 a[116:119], v3, s[20:23], s51 offen offset:1024 sc0 sc1
Mv 120,24,40
Mv 152,24,44
Mv 184,24,48
Mv 216,24,52
Mv 248,24,56
Ma 216,24,60
Ma 220,28,60
Mv 252,28,56
buffer_store_dwordx4 a[120:123], v3, s[20:23], s51 offen offset:2048 sc0 sc1
Mv 220,28,52
Mv 188,28,48
Mv 156,28,44
Mv 124,28,40
Mv 92,28,36
Mv 60,28,32
s_waitcnt vmcnt(16) lgkmcnt(0)
s_barrier
Mv 32,128,160
Rt 0,18,0
Mv 64,128,164
s_add_u32 s38, s38, 0xffff0080
Mv 96,128,168
Rt 2,18,2048
s_add_u32 m0, s36, 32768
Mv 128,128,172
buffer_load_dwordx4 v8, s[4:7], s38 offen offset:0 lds
Mv 160,128,176
Rt 4,19,0
Mv 192,128,180
Mv 224,128,184
Rt 6,19,2048
Ma 192,128,188
buffer_load_dwordx4 v9, s[4:7], s38 offen offset:1024 lds
Ma 196,132,188
Rt 8,20,0
Mv 228,132,184
Mv 196,132,180
Rt 10,20,2048
Mv 164,132,176
)" R"(
buffer_load_dwordx4 v10, s[4:7], s38 offen offset:2048 lds
Mv 132,132,172
Rt 12,21,0
Mv 100,132,168
Mv 68,132,164
Rt 14,21,2048
Mv 36,132,160
buffer_load_dwordx4 v11, s[4:7], s38 offen offset:3072 lds
Mv 40,136,160
Rt 16,22,0
Mv 72,136,164
s_add_u32 s38, s38, 0x10000
Mv 104,136,168
Rt 18,22,2048
s_add_u32 m0, s36, 36864
Mv 136,136,172
buffer_load_dwordx4 v8, s[4:7], s38 offen offset:0 lds
Mv 168,136,176
Rt 20,23,0
Mv 200,136,180
Mv 232,136,184
Rt 22,23,2048
Ma 200,136,188
buffer_load_dwordx4 v9, s[4:7], s38 offen offset:1024 lds
Ma 204,140,188
Rt 24,24,0
Mv 236,140,184
Mv 204,140,180
Rt 26,24,2048
Mv 172,140,176
buffer_load_dwordx4 v10, s[4:7], s38 offen offset:2048 lds
Mv 140,140,172
Rt 28,25,0
Mv 108,140,168
Mv 76,140,164
Rt 30,25,2048
v_accvgpr_write_b32 a224, -1
Mv 44,140,160
buffer_load_dwordx4 v11, s[4:7], s38 offen offset:3072 lds
Mv 48,144,160
Rb 32,16,0
v_accvgpr_write_b32 a225, -1
Mv 80,144,164
v_accvgpr_write_b32 a226, -1
v_accvgpr_write_b32 a227, -1
Mv 112,144,168
Rb 36,16,2048
s_add_u32 s88, s73, 0x4000
Mv 144,144,172
s_add_u32 s39, s39, 0x40000
s_mov_b32 s40, 6
Mv 176,144,176
Rb 40,16,8192
s_add_u32 m0, s37, 49152
Mv 208,144,180
buffer_load_dwordx4 v12, s[8:11], s39 offen offset:0 lds
Mv 240,144,184
Rb 44,16,10240
s_mov_b32 s94, 0
Ma 208,144,188
Ma 212,148,188
Rb 48,16,16384
Mv 244,148,184
Mv 212,148,180
)" R"(
Rb 52,16,18432
Mv 180,148,176
buffer_load_dwordx4 v13, s[8:11], s39 offen offset:1024 lds
Mv 148,148,172
Rb 56,16,24576
Mv 116,148,168
Mv 84,148,164
Rb 60,16,26624
Mv 52,148,160
Mv 56,152,160
Mv 88,152,164
buffer_load_dwordx4 v14, s[8:11], s39 offen offset:2048 lds
Mv 120,152,168
Mv 152,152,172
Mv 184,152,176
Mv 216,152,180
Mv 248,152,184
Ma 216,152,188
buffer_load_dwordx4 v15, s[8:11], s39 offen offset:3072 lds
Ma 220,156,188
Mv 252,156,184
Mv 220,156,180
Mv 188,156,176
buffer_store_dwordx4 a[124:127], v3, s[20:23], s51 offen offset:3072 sc0 sc1
Mv 156,156,172
Mv 124,156,168
Mv 92,156,164
Mv 60,156,160
s_waitcnt vmcnt(25) lgkmcnt(0)
s_barrier
.LzPBK:
Mv 32,0,32
Rt 128,18,16384
Mv 64,0,36
Mv 96,0,40
Rt 130,18,18432
Mv 128,0,44
s_add_u32 s39, s39, 0x40000
Mv 160,0,48
Rt 132,19,16384
s_add_u32 m0, s37, 0
Mv 192,0,52
buffer_load_dwordx4 v12, s[8:11], s39 offen offset:0 lds
Mv 224,0,56
Rt 134,19,18432
Ma 192,0,60
Ma 196,4,60
Rt 136,20,16384
Mv 228,4,56
Mv 196,4,52
Rt 138,20,18432
Mv 164,4,48
Mv 132,4,44
Rt 140,21,16384
Mv 100,4,40
buffer_load_dwordx4 v13, s[8:11], s39 offen offset:1024 lds
Mv 68,4,36
Rt 142,21,18432
Mv 36,4,32
Mv 40,8,32
Rt 144,22,16384
Mv 72,8,36
Mv 104,8,40
Rt 146,22,18432
Mv 136,8,44
)" R"(
Mv 168,8,48
Rt 148,23,16384
Mv 200,8,52
buffer_load_dwordx4 v14, s[8:11], s39 offen offset:2048 lds
Mv 232,8,56
Rt 150,23,18432
Ma 200,8,60
Ma 204,12,60
Rt 152,24,16384
Mv 236,12,56
Mv 204,12,52
Rt 154,24,18432
.LzH3:
s_cbranch_execz .LzH12
Mv 172,12,48
Mv 140,12,44
Rt 156,25,16384
Mv 108,12,40
buffer_load_dwordx4 v15, s[8:11], s39 offen offset:3072 lds
Mv 76,12,36
Rt 158,25,18432
Mv 44,12,32
Mv 48,16,32
Rb 160,17,0
Mv 80,16,36
Mv 112,16,40
Rb 164,17,2048
Mv 144,16,44
Mv 176,16,48
Rb 168,17,8192
Mv 208,16,52
buffer_store_dwordx4 a[224:227], v1, s[20:23], s72 offen sc0 sc1
Mv 240,16,56
Rb 172,17,10240
s_add_u32 s72, s72, 1024
Ma 208,16,60
Ma 212,20,60
Rb 176,17,16384
Mv 244,20,56
Mv 212,20,52
Rb 180,17,18432
Mv 180,20,48
Mv 148,20,44
Rb 184,17,24576
Mv 116,20,40
Mv 84,20,36
Rb 188,17,26624
Mv 52,20,32
Mv 56,24,32
Mv 88,24,36
buffer_store_dwordx4 a[224:227], v1, s[20:23], s88 offen sc0 sc1
Mv 120,24,40
s_add_u32 s88, s88, 1024
Mv 152,24,44
Mv 184,24,48
Mv 216,24,52
Mv 248,24,56
Ma 216,24,60
Ma 220,28,60
Mv 252,28,56
Mv 220,28,52
Mv 188,28,48
Mv 156,28,44
Mv 124,28,40
)" R"(
Mv 92,28,36
Mv 60,28,32
s_waitcnt vmcnt(11) lgkmcnt(0)
s_barrier
Mv 32,128,160
Rt 0,18,32768
Mv 64,128,164
s_add_u32 s38, s38, 0xffff0080
Mv 96,128,168
Rt 2,18,34816
s_add_u32 m0, s36, 0
Mv 128,128,172
buffer_load_dwordx4 v8, s[4:7], s38 offen offset:0 lds
Mv 160,128,176
Rt 4,19,32768
Mv 192,128,180
Mv 224,128,184
Rt 6,19,34816
Ma 192,128,188
buffer_load_dwordx4 v9, s[4:7], s38 offen offset:1024 lds
Ma 196,132,188
Rt 8,20,32768
Mv 228,132,184
Mv 196,132,180
Rt 10,20,34816
Mv 164,132,176
buffer_load_dwordx4 v10, s[4:7], s38 offen offset:2048 lds
Mv 132,132,172
Rt 12,21,32768
Mv 100,132,168
Mv 68,132,164
Rt 14,21,34816
Mv 36,132,160
buffer_load_dwordx4 v11, s[4:7], s38 offen offset:3072 lds
Mv 40,136,160
Rt 16,22,32768
Mv 72,136,164
s_add_u32 s38, s38, 0x10000
Mv 104,136,168
Rt 18,22,34816
s_add_u32 m0, s36, 4096
Mv 136,136,172
buffer_load_dwordx4 v8, s[4:7], s38 offen offset:0 lds
Mv 168,136,176
Rt 20,23,32768
Mv 200,136,180
Mv 232,136,184
Rt 22,23,34816
Ma 200,136,188
buffer_load_dwordx4 v9, s[4:7], s38 offen offset:1024 lds
Ma 204,140,188
Rt 24,24,32768
Mv 236,140,184
Mv 204,140,180
Rt 26,24,34816
Mv 172,140,176
buffer_load_dwordx4 v10, s[4:7], s38 offen offset:2048 lds
Mv 140,140,172
Rt 28,25,32768
Mv 108,140,168
Mv 76,140,164
Rt 30,25,34816
Mv 44,140,160
buffer_load_dwordx4 v11, s[4:7], s38 offen offset:3072 lds
Mv 48,144,160
)" R"(
Rb 32,16,32768
Mv 80,144,164
Mv 112,144,168
Rb 36,16,34816
Mv 144,144,172
s_add_u32 s39, s39, 0x40000
Mv 176,144,176
Rb 40,16,40960
s_add_u32 m0, s37, 16384
Mv 208,144,180
buffer_load_dwordx4 v12, s[8:11], s39 offen offset:0 lds
Mv 240,144,184
Rb 44,16,43008
Ma 208,144,188
Ma 212,148,188
Rb 48,16,49152
Mv 244,148,184
Mv 212,148,180
Rb 52,16,51200
Mv 180,148,176
buffer_load_dwordx4 v13, s[8:11], s39 offen offset:1024 lds
Mv 148,148,172
Rb 56,16,57344
Mv 116,148,168
Mv 84,148,164
Rb 60,16,59392
Mv 52,148,160
Mv 56,152,160
Mv 88,152,164
buffer_load_dwordx4 v14, s[8:11], s39 offen offset:2048 lds
Mv 120,152,168
Mv 152,152,172
Mv 184,152,176
Mv 216,152,180
Mv 248,152,184
Ma 216,152,188
buffer_load_dwordx4 v15, s[8:11], s39 offen offset:3072 lds
Ma 220,156,188
Mv 252,156,184
s_add_u32 s43, s43, 0x60000
Mv 220,156,180
Mv 188,156,176
buffer_load_dword v27, v26, s[16:19], s43 offen
Mv 156,156,172
Mv 124,156,168
Mv 92,156,164
Mv 60,156,160
s_waitcnt vmcnt(19) lgkmcnt(0)
s_barrier
buffer_store_dword v6, v7, s[20:23], s52 offen offset:0 sc0 sc1
s_mov_b32 s52, 0x40000000
Mv 32,0,32
Rt 128,18,49152
Mv 64,0,36
Mv 96,0,40
Rt 130,18,51200
Mv 128,0,44
s_add_u32 s39, s39, 0x40000
Mv 160,0,48
Rt 132,19,49152
s_add_u32 m0, s37, 32768
Mv 192,0,52
buffer_load_dwordx4 v12, s[8:11], s39 offen offset:0 lds
Mv 224,0,56
Rt 134,19,51200
)" R"(
Ma 192,0,60
Ma 196,4,60
Rt 136,20,49152
Mv 228,4,56
Mv 196,4,52
Rt 138,20,51200
Mv 164,4,48
Mv 132,4,44
Rt 140,21,49152
Mv 100,4,40
buffer_load_dwordx4 v13, s[8:11], s39 offen offset:1024 lds
Mv 68,4,36
Rt 142,21,51200
Mv 36,4,32
Mv 40,8,32
Rt 144,22,49152
Mv 72,8,36
Mv 104,8,40
Rt 146,22,51200
Mv 136,8,44
Mv 168,8,48
Rt 148,23,49152
Mv 200,8,52
buffer_load_dwordx4 v14, s[8:11], s39 offen offset:2048 lds
Mv 232,8,56
Rt 150,23,51200
Ma 200,8,60
Ma 204,12,60
Rt 152,24,49152
Mv 236,12,56
Mv 204,12,52
Rt 154,24,51200
Mv 172,12,48
Mv 140,12,44
Rt 156,25,49152
Mv 108,12,40
buffer_load_dwordx4 v15, s[8:11], s39 offen offset:3072 lds
Mv 76,12,36
Rt 158,25,51200
Mv 44,12,32
Mv 48,16,32
Rb 160,17,32768
Mv 80,16,36
Mv 112,16,40
Rb 164,17,34816
Mv 144,16,44
Mv 176,16,48
Rb 168,17,40960
Mv 208,16,52
buffer_store_dwordx4 a[224:227], v1, s[20:23], s72 offen sc0 sc1
Mv 240,16,56
Rb 172,17,43008
s_add_u32 s72, s72, 1024
Ma 208,16,60
Ma 212,20,60
Rb 176,17,49152
Mv 244,20,56
Mv 212,20,52
Rb 180,17,51200
Mv 180,20,48
Mv 148,20,44
Rb 184,17,57344
Mv 116,20,40
Mv 84,20,36
)" R"(
Rb 188,17,59392
Mv 52,20,32
Mv 56,24,32
Mv 88,24,36
buffer_store_dwordx4 a[224:227], v1, s[20:23], s88 offen sc0 sc1
Mv 120,24,40
s_add_u32 s88, s88, 1024
Mv 152,24,44
Mv 184,24,48
Mv 216,24,52
Mv 248,24,56
Ma 216,24,60
Ma 220,28,60
Mv 252,28,56
Mv 220,28,52
Mv 188,28,48
Mv 156,28,44
Mv 124,28,40
Mv 92,28,36
s_cmp_eq_u32 s40, 3
s_cmov_b32 s72, 0x40000000
s_cmov_b32 s88, 0x40000000
Mv 60,28,32
s_waitcnt vmcnt(12) lgkmcnt(0)
s_barrier
Mv 32,128,160
Rt 0,18,0
Mv 64,128,164
s_add_u32 s38, s38, 0xffff0080
Mv 96,128,168
Rt 2,18,2048
s_add_u32 m0, s36, 32768
Mv 128,128,172
buffer_load_dwordx4 v8, s[4:7], s38 offen offset:0 lds
Mv 160,128,176
Rt 4,19,0
Mv 192,128,180
Mv 224,128,184
Rt 6,19,2048
Ma 192,128,188
buffer_load_dwordx4 v9, s[4:7], s38 offen offset:1024 lds
Ma 196,132,188
Rt 8,20,0
Mv 228,132,184
Mv 196,132,180
Rt 10,20,2048
Mv 164,132,176
buffer_load_dwordx4 v10, s[4:7], s38 offen offset:2048 lds
Mv 132,132,172
Rt 12,21,0
Mv 100,132,168
Mv 68,132,164
Rt 14,21,2048
Mv 36,132,160
buffer_load_dwordx4 v11, s[4:7], s38 offen offset:3072 lds
Mv 40,136,160
Rt 16,22,0
Mv 72,136,164
s_add_u32 s38, s38, 0x10000
Mv 104,136,168
Rt 18,22,2048
s_add_u32 m0, s36, 36864
Mv 136,136,172
buffer_load_dwordx4 v8, s[4:7], s38 offen offset:0 lds
Mv 168,136,176
)" R"(
Rt 20,23,0
Mv 200,136,180
Mv 232,136,184
Rt 22,23,2048
Ma 200,136,188
buffer_load_dwordx4 v9, s[4:7], s38 offen offset:1024 lds
Ma 204,140,188
Rt 24,24,0
Mv 236,140,184
Mv 204,140,180
Rt 26,24,2048
Mv 172,140,176
buffer_load_dwordx4 v10, s[4:7], s38 offen offset:2048 lds
Mv 140,140,172
Rt 28,25,0
Mv 108,140,168
Mv 76,140,164
Rt 30,25,2048
Mv 44,140,160
buffer_load_dwordx4 v11, s[4:7], s38 offen offset:3072 lds
Mv 48,144,160
Rb 32,16,0
Mv 80,144,164
Mv 112,144,168
Rb 36,16,2048
Mv 144,144,172
s_add_u32 s39, s39, 0x40000
Mv 176,144,176
Rb 40,16,8192
s_add_u32 m0, s37, 49152
Mv 208,144,180
buffer_load_dwordx4 v12, s[8:11], s39 offen offset:0 lds
Mv 240,144,184
Rb 44,16,10240
Ma 208,144,188
Ma 212,148,188
Rb 48,16,16384
Mv 244,148,184
Mv 212,148,180
Rb 52,16,18432
Mv 180,148,176
buffer_load_dwordx4 v13, s[8:11], s39 offen offset:1024 lds
Mv 148,148,172
Rb 56,16,24576
Mv 116,148,168
Mv 84,148,164
Rb 60,16,26624
Mv 52,148,160
Mv 56,152,160
Mv 88,152,164
buffer_load_dwordx4 v14, s[8:11], s39 offen offset:2048 lds
Mv 120,152,168
Mv 152,152,172
Mv 184,152,176
Mv 216,152,180
Mv 248,152,184
Ma 216,152,188
buffer_load_dwordx4 v15, s[8:11], s39 offen offset:3072 lds
Ma 220,156,188
Mv 252,156,184
s_add_u32 s43, s43, 0x60000
Mv 220,156,180
s_sub_u32 s40, s40, 1
Mv 188,156,176
)" R"(
buffer_load_dword v27, v26, s[16:19], s43 offen
Mv 156,156,172
Mv 124,156,168
Mv 92,156,164
Mv 60,156,160
s_cmp_lg_u32 s40, 0
s_waitcnt vmcnt(21) lgkmcnt(0)
s_barrier
s_cbranch_scc1 .LzPBK
.LzPBKX:
Mv 32,0,32
Rt 128,18,16384
Mv 64,0,36
buffer_load_dword v4, v5, s[20:23], s50 offen offset:0 sc0 sc1
Mv 96,0,40
Rt 130,18,18432
Mv 128,0,44
Mv 160,0,48
Rt 132,19,16384
Mv 192,0,52
Mv 224,0,56
Rt 134,19,18432
Ma 192,0,60
Ma 196,4,60
Rt 136,20,16384
Mv 228,4,56
Mv 196,4,52
Rt 138,20,18432
Mv 164,4,48
Mv 132,4,44
Rt 140,21,16384
Mv 100,4,40
Mv 68,4,36
Rt 142,21,18432
Mv 36,4,32
Mv 40,8,32
Rt 144,22,16384
Mv 72,8,36
Mv 104,8,40
Rt 146,22,18432
Mv 136,8,44
Mv 168,8,48
Rt 148,23,16384
Mv 200,8,52
Mv 232,8,56
Rt 150,23,18432
Ma 200,8,60
Ma 204,12,60
Rt 152,24,16384
Mv 236,12,56
Mv 204,12,52
Rt 154,24,18432
Mv 172,12,48
Mv 140,12,44
Rt 156,25,16384
Mv 108,12,40
Mv 76,12,36
Rt 158,25,18432
Mv 44,12,32
Mv 48,16,32
Rb 160,17,0
Mv 80,16,36
Mv 112,16,40
Rb 164,17,2048
Mv 144,16,44
)" R"(
Mv 176,16,48
Rb 168,17,8192
Mv 208,16,52
Mv 240,16,56
Rb 172,17,10240
Ma 208,16,60
s_waitcnt vmcnt(0)
v_cmp_eq_u32_e32 vcc, s3, v4
s_andn2_b64 s[28:29], -1, vcc
s_cbranch_scc1 .Lzsp14
.Lzok14:
Ma 212,20,60
Rb 176,17,16384
Mv 244,20,56
s_lshl3_add_u32 s29, s34, s35
s_lshl2_add_u32 s29, s29, s33
Mv 212,20,52
Rb 180,17,18432
s_add_u32 m0, s36, 131072
Mv 180,20,48
buffer_load_dwordx4 v2, s[20:23], s56 offen offset:0 sc1 lds
Mv 148,20,44
Rb 184,17,24576
s_mul_i32 s83, s29, 7168
Mv 116,20,40
s_mov_b32 s82, s89
s_add_u32 s82, s82, 0xc00000
Mv 84,20,36
Rb 188,17,26624
s_add_u32 s82, s82, s84
Mv 52,20,32
s_mov_b32 s24, s80
s_mov_b32 s25, s81
Mv 56,24,32
s_mov_b32 s26, 0x380000
s_mov_b32 s27, 0x20000
Mv 88,24,36
buffer_load_dwordx4 v2, s[20:23], s56 offen offset:1024 sc1 lds
Mv 120,24,40
s_add_u32 s28, s33, 0
s_lshl2_add_u32 s28, s35, s28
Mv 152,24,44
s_mul_i32 s29, s28, 293
s_lshr_b32 s29, s29, 11
Mv 184,24,48
s_mul_i32 s29, s29, 9
s_add_u32 s29, s29, s28
Mv 216,24,52
s_lshl_b32 s29, s29, 10
s_add_u32 s4, s29, s82
Mv 248,24,56
s_add_u32 s28, s33, 4
s_lshl3_add_u32 s28, s28, s35
Ma 216,24,60
buffer_load_dwordx4 v2, s[20:23], s56 offen offset:2048 sc1 lds
Ma 220,28,60
s_mul_i32 s29, s28, 293
s_lshr_b32 s29, s29, 11
Mv 252,28,56
s_mul_i32 s29, s29, 9
s_add_u32 s29, s29, s28
Mv 220,28,52
s_lshl_b32 s29, s29, 10
s_add_u32 s5, s29, s82
)" R"(
Mv 188,28,48
s_add_u32 s28, s33, 8
s_lshl3_add_u32 s28, s28, s35
Mv 156,28,44
s_mul_i32 s29, s28, 293
s_lshr_b32 s29, s29, 11
Mv 124,28,40
buffer_load_dwordx4 v2, s[20:23], s56 offen offset:3072 sc1 lds
Mv 92,28,36
s_mul_i32 s29, s29, 9
s_add_u32 s29, s29, s28
Mv 60,28,32
s_waitcnt lgkmcnt(0)
s_barrier
Mv 32,128,160
Rt 0,18,32768
s_lshl_b32 s29, s29, 10
Mv 64,128,164
s_add_u32 s6, s29, s82
s_add_u32 s28, s33, 12
Mv 96,128,168
Rt 2,18,34816
s_add_u32 m0, s36, 0
Mv 128,128,172
buffer_load_dwordx4 v2, s[20:23], s55 offen offset:0 sc1 lds
Mv 160,128,176
Rt 4,19,32768
s_lshl3_add_u32 s28, s28, s35
Mv 192,128,180
s_mul_i32 s29, s28, 293
s_lshr_b32 s29, s29, 11
Mv 224,128,184
Rt 6,19,34816
s_mul_i32 s29, s29, 9
Ma 192,128,188
s_add_u32 s29, s29, s28
s_lshl_b32 s29, s29, 10
Ma 196,132,188
Rt 8,20,32768
buffer_load_dwordx4 v2, s[20:23], s55 offen offset:1024 sc1 lds
.LzH4:
s_cbranch_execz .LzH17
Mv 228,132,184
s_add_u32 s7, s29, s82
Mv 196,132,180
Rt 10,20,34816
Mv 164,132,176
s_cmp_gt_u32 s33, 1
s_cmov_b32 s7, 0x40000000
Mv 132,132,172
Rt 12,21,32768
s_add_u32 s28, s33, 14
Mv 100,132,168
buffer_load_dwordx4 v2, s[20:23], s55 offen offset:2048 sc1 lds
Mv 68,132,164
Rt 14,21,34816
s_lshl3_add_u32 s28, s28, s35
Mv 36,132,160
s_mul_i32 s29, s28, 293
s_lshr_b32 s29, s29, 11
Mv 40,136,160
Rt 16,22,32768
s_mul_i32 s29, s29, 9
Mv 72,136,164
s_add_u32 s29, s29, s28
)" R"(
s_lshl_b32 s29, s29, 10
Mv 104,136,168
Rt 18,22,34816
buffer_load_dwordx4 v2, s[20:23], s55 offen offset:3072 sc1 lds
Mv 136,136,172
s_add_u32 s8, s29, s82
s_add_u32 s28, s33, 18
Mv 168,136,176
Rt 20,23,32768
s_lshl3_add_u32 s28, s28, s35
Mv 200,136,180
s_mul_i32 s29, s28, 293
s_lshr_b32 s29, s29, 11
Mv 232,136,184
Rt 22,23,34816
s_add_u32 m0, s36, 4096
Ma 200,136,188
buffer_load_dwordx4 v3, s[20:23], s55 offen offset:0 sc1 lds
Ma 204,140,188
Rt 24,24,32768
s_mul_i32 s29, s29, 9
Mv 236,140,184
s_add_u32 s29, s29, s28
s_lshl_b32 s29, s29, 10
Mv 204,140,180
Rt 26,24,34816
s_add_u32 s9, s29, s82
Mv 172,140,176
s_add_u32 s28, s33, 22
s_lshl3_add_u32 s28, s28, s35
Mv 140,140,172
Rt 28,25,32768
buffer_load_dwordx4 v3, s[20:23], s55 offen offset:1024 sc1 lds
Mv 108,140,168
s_mul_i32 s29, s28, 293
s_lshr_b32 s29, s29, 11
Mv 76,140,164
Rt 30,25,34816
s_mul_i32 s29, s29, 9
Mv 44,140,160
s_add_u32 s29, s29, s28
s_lshl_b32 s29, s29, 10
Mv 48,144,160
Rb 32,16,32768
s_add_u32 s10, s29, s82
Mv 80,144,164
buffer_load_dwordx4 v3, s[20:23], s55 offen offset:2048 sc1 lds
Mv 112,144,168
Rb 36,16,34816
s_add_u32 s28, s33, 26
Mv 144,144,172
s_lshl3_add_u32 s28, s28, s35
s_mul_i32 s29, s28, 293
Mv 176,144,176
Rb 40,16,40960
s_lshr_b32 s29, s29, 11
Mv 208,144,180
s_mul_i32 s29, s29, 9
s_add_u32 s29, s29, s28
Mv 240,144,184
Rb 44,16,43008
buffer_load_dwordx4 v3, s[20:23], s55 offen offset:3072 sc1 lds
Ma 208,144,188
s_lshl_b32 s29, s29, 10
)" R"(
s_add_u32 s11, s29, s82
Ma 212,148,188
Rb 48,16,49152
Mv 244,148,184
s_cmp_gt_u32 s33, 1
s_cmov_b32 s11, 0x40000000
Mv 212,148,180
Rb 52,16,51200
s_add_u32 m0, s36, 135168
Mv 180,148,176
buffer_load_dwordx4 v3, s[20:23], s56 offen offset:0 sc1 lds
Mv 148,148,172
Rb 56,16,57344
s_and_b32 s28, s34, 1
Mv 116,148,168
s_lshl_b32 s28, s28, 14
s_add_u32 s82, s37, s28
Mv 84,148,164
Rb 60,16,59392
s_add_u32 s82, s82, 0x10000
Mv 52,148,160
Mv 56,152,160
buffer_load_dwordx4 v3, s[20:23], s56 offen offset:1024 sc1 lds
Mv 88,152,164
Mv 120,152,168
Mv 152,152,172
Mv 184,152,176
Mv 216,152,180
buffer_load_dwordx4 v3, s[20:23], s56 offen offset:2048 sc1 lds
Mv 248,152,184
Ma 216,152,188
Ma 220,156,188
Mv 252,156,184
Mv 220,156,180
buffer_load_dwordx4 v3, s[20:23], s56 offen offset:3072 sc1 lds
Mv 188,156,176
Mv 156,156,172
Mv 124,156,168
s_add_u32 s55, s55, 0x10000
s_add_u32 s56, s56, 0x10000
Mv 92,156,164
Mv 60,156,160
s_waitcnt lgkmcnt(4)
s_barrier
Mv 32,0,32
Rt 128,18,49152
Mv 64,0,36
Rt 130,18,51200
Mv 96,0,40
Rt 132,19,49152
s_add_u32 m0, s36, 65536
Mv 128,0,44
Rt 134,19,51200
buffer_load_dwordx4 v2, s[20:23], s56 offen offset:0 sc1 lds
Mv 132,4,44
Rt 136,20,49152
Mv 100,4,40
Rt 138,20,51200
s_add_u32 s50, s50, 1024
Mv 68,4,36
Rt 140,21,49152
s_min_u32 s50, s50, s60
Mv 36,4,32
Rt 142,21,51200
Mv 40,8,32
)" R"(
Rt 144,22,49152
Mv 72,8,36
Rt 146,22,51200
Mv 104,8,40
Rt 148,23,49152
Mv 136,8,44
Rt 150,23,51200
buffer_load_dwordx4 v2, s[20:23], s56 offen offset:1024 sc1 lds
Mv 140,12,44
Rt 152,24,49152
Mv 108,12,40
Rt 154,24,51200
Mv 76,12,36
Rt 156,25,49152
Mv 44,12,32
Rt 158,25,51200
Mv 48,16,32
Rb 160,17,32768
Mv 80,16,36
Rb 164,17,34816
Mv 112,16,40
Rb 168,17,40960
Mv 144,16,44
Rb 172,17,43008
buffer_load_dwordx4 v2, s[20:23], s56 offen offset:2048 sc1 lds
Mv 148,20,44
Rb 176,17,49152
Mv 116,20,40
Rb 180,17,51200
Mv 84,20,36
Rb 184,17,57344
Mv 52,20,32
Rb 188,17,59392
Mv 56,24,32
v_add_u32 v8, 0x10000, v1
v_add_u32 v10, 0x20000, v1
Mv 88,24,36
buffer_load_dword v4, v5, s[20:23], s50 offen offset:0 sc0 sc1
Mv 120,24,40
s_mov_b32 s93, 2
s_mov_b32 s94, 0
Mv 152,24,44
buffer_load_dwordx4 v2, s[20:23], s56 offen offset:3072 sc1 lds
Mv 156,28,44
Mv 124,28,40
Mv 92,28,36
Mv 60,28,32
s_waitcnt lgkmcnt(0)
s_barrier
Mv 32,128,160
Mv 36,132,160
Mv 40,136,160
s_add_u32 m0, s36, 32768
Mv 44,140,160
buffer_load_dwordx4 v2, s[20:23], s55 offen offset:0 sc1 lds
Mv 48,144,160
Mv 52,148,160
s_nop 0
Cv 32,32,33
Cv 33,34,35
Mv 56,152,160
Cv 34,36,37
Cv 35,38,39
Mv 60,156,160
Cv 40,40,41
)" R"(
Cv 41,42,43
Mv 64,128,164
Cv 42,44,45
Cv 43,46,47
Mv 68,132,164
Cv 48,48,49
Cv 49,50,51
Mv 72,136,164
Cv 50,52,53
Cv 51,54,55
Mv 76,140,164
buffer_load_dwordx4 v2, s[20:23], s55 offen offset:1024 sc1 lds
Mv 80,144,164
Cv 56,56,57
Cv 57,58,59
Mv 84,148,164
Cv 58,60,61
Cv 59,62,63
Mv 88,152,164
Cv 64,64,65
Cv 65,66,67
Mv 92,156,164
Cv 66,68,69
Cv 67,70,71
Mv 96,128,168
Cv 72,72,73
Cv 73,74,75
Mv 100,132,168
Cv 74,76,77
Cv 75,78,79
Mv 104,136,168
Cv 80,80,81
Cv 81,82,83
Mv 108,140,168
buffer_load_dwordx4 v2, s[20:23], s55 offen offset:2048 sc1 lds
Mv 112,144,168
Cv 82,84,85
Cv 83,86,87
Mv 116,148,168
Cv 88,88,89
Cv 89,90,91
Mv 120,152,168
Cv 90,92,93
Cv 91,94,95
Mv 124,156,168
buffer_store_dwordx4 v[32:35], v2, s[20:23], s45 offen offset:0 sc0 sc1
Mv 128,128,172
Mv 132,132,172
Mv 136,136,172
Mv 140,140,172
buffer_load_dwordx4 v2, s[20:23], s55 offen offset:3072 sc1 lds
Mv 144,144,172
Mv 148,148,172
s_nop 1
Cv 128,128,129
Cv 129,130,131
Mv 152,152,172
Cv 130,132,133
Cv 131,134,135
Mv 156,156,172
buffer_store_dwordx4 v[40:43], v2, s[20:23], s45 offen offset:1024 sc0 sc1
Mv 160,0,48
Cv 136,136,137
Cv 137,138,139
)" R"(
Mv 160,128,176
Cv 138,140,141
Cv 139,142,143
Mv 164,4,48
s_add_u32 m0, s36, 36864
Cv 144,144,145
Mv 164,132,176
buffer_load_dwordx4 v3, s[20:23], s55 offen offset:0 sc1 lds
Mv 168,8,48
Cv 145,146,147
Cv 146,148,149
Mv 168,136,176
Cv 147,150,151
Cv 152,152,153
Mv 172,12,48
Cv 153,154,155
Cv 154,156,157
Mv 172,140,176
buffer_store_dwordx4 v[48:51], v2, s[20:23], s45 offen offset:2048 sc0 sc1
Mv 176,16,48
Cv 155,158,159
Cv 132,96,97
Mv 176,144,176
Cv 133,98,99
Cv 134,100,101
Mv 180,20,48
Cv 135,102,103
Cv 140,104,105
Mv 180,148,176
buffer_load_dwordx4 v3, s[20:23], s55 offen offset:1024 sc1 lds
Mv 184,24,48
Cv 141,106,107
Cv 142,108,109
Mv 184,152,176
Cv 143,110,111
Cv 148,112,113
Mv 188,28,48
Cv 149,114,115
Cv 150,116,117
Mv 188,156,176
buffer_store_dwordx4 v[56:59], v2, s[20:23], s45 offen offset:3072 sc0 sc1
Mv 192,0,52
Cv 151,118,119
Cv 156,120,121
Mv 192,128,180
Cv 157,122,123
Cv 158,124,125
Mv 196,4,52
Cv 159,126,127
Cv 160,160,161
Mv 196,132,180
buffer_load_dwordx4 v3, s[20:23], s55 offen offset:2048 sc1 lds
Mv 200,8,52
Cv 161,162,163
Cv 162,164,165
Mv 200,136,180
Cv 163,166,167
v_accvgpr_write_b32 a224, v160
Mv 204,12,52
v_accvgpr_write_b32 a225, v161
v_accvgpr_write_b32 a226, v162
Mv 204,140,180
buffer_store_dwordx4 v[64:67], v3, s[20:23], s45 offen offset:0 sc0 sc1
Mv 208,16,52
)" R"(
v_accvgpr_write_b32 a227, v163
Cv 168,168,169
Mv 208,144,180
Cv 169,170,171
Cv 170,172,173
Mv 212,20,52
Cv 171,174,175
v_accvgpr_write_b32 a228, v168
Mv 212,148,180
buffer_load_dwordx4 v3, s[20:23], s55 offen offset:3072 sc1 lds
Mv 216,24,52
v_accvgpr_write_b32 a229, v169
v_accvgpr_write_b32 a230, v170
Mv 216,152,180
v_accvgpr_write_b32 a231, v171
Cv 176,176,177
Mv 220,28,52
Cv 177,178,179
Cv 178,180,181
Mv 220,156,180
buffer_store_dwordx4 v[72:75], v3, s[20:23], s45 offen offset:1024 sc0 sc1
Mv 224,0,56
Cv 179,182,183
v_accvgpr_write_b32 a232, v176
Ma 192,0,60
v_accvgpr_write_b32 a233, v177
v_accvgpr_write_b32 a234, v178
Ma 196,4,60
s_add_u32 m0, s36, 69632
v_accvgpr_write_b32 a235, v179
Mv 228,4,56
buffer_load_dwordx4 v3, s[20:23], s56 offen offset:0 sc1 lds
Mv 232,8,56
Cv 184,184,185
Cv 185,186,187
Ma 200,8,60
Cv 186,188,189
Cv 187,190,191
Ma 204,12,60
v_accvgpr_write_b32 a236, v184
v_accvgpr_write_b32 a237, v185
Mv 236,12,56
s_waitcnt vmcnt(16)
s_barrier
Mv 240,16,56
v_cmp_eq_u32_e32 vcc, s3, v4
s_andn2_b64 s[28:29], -1, vcc
s_cbranch_scc1 .Lzsp15
.Lzok15:
Ma 208,16,60
buffer_store_dwordx4 v[80:83], v3, s[20:23], s45 offen offset:2048 sc0 sc1
Ma 212,20,60
v_accvgpr_write_b32 a238, v186
v_accvgpr_write_b32 a239, v187
Mv 244,20,56
buffer_load_dwordx4 v3, s[20:23], s56 offen offset:1024 sc1 lds
Mv 248,24,56
Cv 192,192,193
Cv 193,194,195
Ma 216,24,60
Cv 194,196,197
Cv 195,198,199
Ma 220,28,60
v_accvgpr_write_b32 a240, v192
v_accvgpr_write_b32 a241, v193
)" R"(
Mv 252,28,56
buffer_store_dwordx4 v[88:91], v3, s[20:23], s45 offen offset:3072 sc0 sc1
Mv 224,128,184
Rb 0,1,0
v_accvgpr_write_b32 a242, v194
Mv 228,132,184
Rb 4,1,1024
v_accvgpr_write_b32 a243, v195
Mv 232,136,184
Rb 8,1,2048
Cv 200,200,201
Mv 236,140,184
buffer_load_dwordx4 v3, s[20:23], s56 offen offset:2048 sc1 lds
Rb 12,1,3072
Mv 240,144,184
Rb 16,1,8192
Cv 201,202,203
Mv 244,148,184
Rb 20,1,9216
Cv 202,204,205
Mv 248,152,184
Rb 24,1,10240
Cv 203,206,207
Mv 252,156,184
Rb 28,1,11264
v_accvgpr_write_b32 a244, v200
Ma 192,128,188
Rb 32,1,4096
v_accvgpr_write_b32 a245, v201
Ma 196,132,188
Rb 36,1,5120
v_accvgpr_write_b32 a246, v202
Ma 200,136,188
Rb 40,1,6144
v_accvgpr_write_b32 a247, v203
Ma 204,140,188
buffer_load_dwordx4 v3, s[20:23], s56 offen offset:3072 sc1 lds
Rb 44,1,7168
Ma 208,144,188
Ma 212,148,188
Ma 216,152,188
Ma 220,156,188
s_add_u32 s56, s56, 0x10000
s_branch .LzFB
.LzH5:
s_cbranch_execz .LzH10
.LzBV:
Mz 32,32,0
Rt 128,18,16384
v_mul_f32_e32 v64, s54, v64
Mz 36,36,0
v_mul_f32_e32 v65, s54, v65
Cv 64,64,65
Mz 96,40,0
Rt 130,18,18432
v_accvgpr_write_b32 a112, v64
Mz 100,44,0
s_add_u32 s39, s39, 0x40000
v_mul_f32_e32 v66, s54, v66
Mz 160,48,0
Rt 132,19,16384
s_add_u32 m0, s37, 0
Mz 164,52,0
buffer_load_dwordx4 v12, s[8:11], s39 offen offset:0 lds
)" R"(
Mz 224,56,0
Rt 134,19,18432
v_mul_f32_e32 v67, s54, v67
Mz 228,60,0
Cv 66,66,67
v_accvgpr_write_b32 a113, v66
Mz 236,60,4
Rt 136,20,16384
v_mul_f32_e32 v68, s54, v68
Mz 232,56,4
v_mul_f32_e32 v69, s54, v69
Cv 68,68,69
buffer_store_dwordx4 a[64:67], v2, s[20:23], s51 offen offset:0 sc0 sc1
Mz 172,52,4
Rt 138,20,18432
v_accvgpr_write_b32 a114, v68
Mz 168,48,4
v_mul_f32_e32 v70, s54, v70
v_mul_f32_e32 v71, s54, v71
Mz 108,44,4
Rt 140,21,16384
Cv 70,70,71
Mz 104,40,4
buffer_load_dwordx4 v13, s[8:11], s39 offen offset:1024 lds
Mz 44,36,4
Rt 142,21,18432
v_accvgpr_write_b32 a115, v70
Mz 40,32,4
v_mul_f32_e32 v72, s54, v72
v_mul_f32_e32 v73, s54, v73
Mz 48,32,8
Rt 144,22,16384
Cv 72,72,73
Mz 52,36,8
v_accvgpr_write_b32 a116, v72
v_mul_f32_e32 v74, s54, v74
buffer_store_dwordx4 a[68:71], v2, s[20:23], s51 offen offset:1024 sc0 sc1
Mz 112,40,8
Rt 146,22,18432
v_mul_f32_e32 v75, s54, v75
Mz 116,44,8
Cv 74,74,75
v_accvgpr_write_b32 a117, v74
Mz 176,48,8
Rt 148,23,16384
v_mul_f32_e32 v76, s54, v76
Mz 180,52,8
buffer_load_dwordx4 v14, s[8:11], s39 offen offset:2048 lds
Mz 240,56,8
Rt 150,23,18432
v_mul_f32_e32 v77, s54, v77
Mz 244,60,8
Cv 76,76,77
v_accvgpr_write_b32 a118, v76
Mz 252,60,12
Rt 152,24,16384
v_mul_f32_e32 v78, s54, v78
Mz 248,56,12
v_mul_f32_e32 v79, s54, v79
Cv 78,78,79
buffer_store_dwordx4 a[72:75], v2, s[20:23], s51 offen offset:2048 sc0 sc1
Mz 188,52,12
Rt 154,24,18432
v_accvgpr_write_b32 a119, v78
)" R"(
Mz 184,48,12
v_mul_f32_e32 v80, s54, v80
v_mul_f32_e32 v81, s54, v81
Mz 124,44,12
Rt 156,25,16384
Cv 80,80,81
Mz 120,40,12
buffer_load_dwordx4 v15, s[8:11], s39 offen offset:3072 lds
Mz 60,36,12
Rt 158,25,18432
v_accvgpr_write_b32 a120, v80
.LzH6:
s_cbranch_execz .LzH11
Mz 56,32,12
v_mul_f32_e32 v82, s54, v82
v_mul_f32_e32 v83, s54, v83
Mz 128,40,16
Rb 160,17,0
Cv 82,82,83
Mz 132,44,16
v_accvgpr_write_b32 a121, v82
v_mul_f32_e32 v84, s54, v84
buffer_store_dwordx4 a[76:79], v2, s[20:23], s51 offen offset:3072 sc0 sc1
Mz 192,48,16
Rb 164,17,2048
v_mul_f32_e32 v85, s54, v85
Mz 196,52,16
Cv 84,84,85
v_accvgpr_write_b32 a122, v84
v_mfma_f32_16x16x32_bf16 a[192:195], a[56:59], a[16:19], 0
Rb 168,17,8192
v_mul_f32_e32 v86, s54, v86
v_mfma_f32_16x16x32_bf16 a[196:199], a[60:63], a[16:19], 0
v_mul_f32_e32 v87, s54, v87
Cv 86,86,87
v_mfma_f32_16x16x32_bf16 a[204:207], a[60:63], a[20:23], 0
Rb 172,17,10240
v_accvgpr_write_b32 a123, v86
v_mfma_f32_16x16x32_bf16 a[200:203], a[56:59], a[20:23], 0
v_mul_f32_e32 v88, s54, v88
v_mul_f32_e32 v89, s54, v89
Mz 204,52,20
Rb 176,17,16384
Cv 88,88,89
Mz 200,48,20
v_accvgpr_write_b32 a124, v88
v_mul_f32_e32 v90, s54, v90
buffer_store_dwordx4 a[80:83], v3, s[20:23], s51 offen offset:0 sc0 sc1
Mz 140,44,20
Rb 180,17,18432
v_mul_f32_e32 v91, s54, v91
Mz 136,40,20
Cv 90,90,91
v_accvgpr_write_b32 a125, v90
Mz 144,40,24
Rb 184,17,24576
v_mul_f32_e32 v92, s54, v92
Mz 148,44,24
v_mul_f32_e32 v93, s54, v93
Cv 92,92,93
Mz 208,48,24
Rb 188,17,26624
v_accvgpr_write_b32 a126, v92
Mz 212,52,24
)" R"(
v_mul_f32_e32 v94, s54, v94
v_mul_f32_e32 v95, s54, v95
v_mfma_f32_16x16x32_bf16 a[208:211], a[56:59], a[24:27], 0
Cv 94,94,95
v_accvgpr_write_b32 a127, v94
v_mfma_f32_16x16x32_bf16 a[212:215], a[60:63], a[24:27], 0
buffer_store_dwordx4 a[84:87], v3, s[20:23], s51 offen offset:1024 sc0 sc1
v_mfma_f32_16x16x32_bf16 a[220:223], a[60:63], a[28:31], 0
v_mfma_f32_16x16x32_bf16 a[216:219], a[56:59], a[28:31], 0
Mz 220,52,28
Mz 216,48,28
Mz 156,44,28
Mz 152,40,28
Mz 64,32,16
Mz 68,36,16
buffer_store_dwordx4 a[88:91], v3, s[20:23], s51 offen offset:2048 sc0 sc1
Mz 72,32,20
Mz 76,36,20
Mz 80,32,24
Mz 84,36,24
Mz 88,32,28
Mz 92,36,28
s_waitcnt vmcnt(16) lgkmcnt(0)
s_barrier
Mv 32,160,128
Rt 0,18,32768
Mv 36,164,128
s_add_u32 s38, s38, 0xffff0080
Mv 96,168,128
Rt 2,18,34816
s_add_u32 m0, s36, 0
Mv 100,172,128
buffer_load_dwordx4 v8, s[4:7], s38 offen offset:0 lds
Mv 160,176,128
Rt 4,19,32768
Mv 164,180,128
Mv 224,184,128
Rt 6,19,34816
Mv 228,188,128
buffer_load_dwordx4 v9, s[4:7], s38 offen offset:1024 lds
Mv 236,188,132
Rt 8,20,32768
Mv 232,184,132
Mv 172,180,132
Rt 10,20,34816
Mv 168,176,132
buffer_load_dwordx4 v10, s[4:7], s38 offen offset:2048 lds
Mv 108,172,132
Rt 12,21,32768
Mv 104,168,132
Mv 44,164,132
Rt 14,21,34816
Mv 40,160,132
buffer_load_dwordx4 v11, s[4:7], s38 offen offset:3072 lds
Mv 48,160,136
Rt 16,22,32768
Mv 52,164,136
s_add_u32 s38, s38, 0x10000
Mv 112,168,136
Rt 18,22,34816
s_add_u32 m0, s36, 4096
Mv 116,172,136
buffer_load_dwordx4 v8, s[4:7], s38 offen offset:0 lds
Mv 176,176,136
Rt 20,23,32768
)" R"(
Mv 180,180,136
Mv 240,184,136
Rt 22,23,34816
Mv 244,188,136
buffer_load_dwordx4 v9, s[4:7], s38 offen offset:1024 lds
Mv 252,188,140
Rt 24,24,32768
Mv 248,184,140
Mv 188,180,140
Rt 26,24,34816
Mv 184,176,140
buffer_load_dwordx4 v10, s[4:7], s38 offen offset:2048 lds
Mv 124,172,140
Rt 28,25,32768
Mv 120,168,140
Mv 60,164,140
Rt 30,25,34816
Mv 56,160,140
buffer_load_dwordx4 v11, s[4:7], s38 offen offset:3072 lds
Mv 64,160,144
Rb 32,16,32768
Mv 68,164,144
Mv 128,168,144
Rb 36,16,34816
Mv 132,172,144
s_add_u32 s39, s39, 0x40000
Mv 192,176,144
Rb 40,16,40960
s_add_u32 m0, s37, 16384
Mv 196,180,144
buffer_load_dwordx4 v12, s[8:11], s39 offen offset:0 lds
Ma 192,184,144
Rb 44,16,43008
Ma 196,188,144
Ma 204,188,148
Rb 48,16,49152
Ma 200,184,148
Mv 204,180,148
Rb 52,16,51200
Mv 200,176,148
buffer_load_dwordx4 v13, s[8:11], s39 offen offset:1024 lds
Mv 140,172,148
Rb 56,16,57344
Mv 136,168,148
Mv 76,164,148
Rb 60,16,59392
Mv 72,160,148
Mv 80,160,152
Mv 84,164,152
buffer_load_dwordx4 v14, s[8:11], s39 offen offset:2048 lds
Mv 144,168,152
Mv 148,172,152
Mv 208,176,152
Mv 212,180,152
Ma 208,184,152
Ma 212,188,152
buffer_load_dwordx4 v15, s[8:11], s39 offen offset:3072 lds
Ma 220,188,156
Ma 216,184,156
Mv 220,180,156
Mv 216,176,156
buffer_store_dwordx4 a[92:95], v3, s[20:23], s51 offen offset:3072 sc0 sc1
Mv 156,172,156
Mv 152,168,156
)" R"(
Mv 92,164,156
Mv 88,160,156
s_waitcnt vmcnt(25) lgkmcnt(0)
s_barrier
Mv 32,32,0
Rt 128,18,49152
Mv 36,36,0
Mv 96,40,0
Rt 130,18,51200
Mv 100,44,0
s_add_u32 s39, s39, 0x40000
Mv 160,48,0
Rt 132,19,49152
s_add_u32 m0, s37, 32768
Mv 164,52,0
buffer_load_dwordx4 v12, s[8:11], s39 offen offset:0 lds
Mv 224,56,0
Rt 134,19,51200
Mv 228,60,0
Mv 236,60,4
Rt 136,20,49152
Mv 232,56,4
s_add_u32 s51, s51, 0x10000
buffer_store_dwordx4 a[96:99], v2, s[20:23], s51 offen offset:0 sc0 sc1
Mv 172,52,4
Rt 138,20,51200
Mv 168,48,4
Mv 108,44,4
Rt 140,21,49152
Mv 104,40,4
buffer_load_dwordx4 v13, s[8:11], s39 offen offset:1024 lds
Mv 44,36,4
Rt 142,21,51200
Mv 40,32,4
Mv 48,32,8
Rt 144,22,49152
Mv 52,36,8
buffer_store_dwordx4 a[100:103], v2, s[20:23], s51 offen offset:1024 sc0 sc1
Mv 112,40,8
Rt 146,22,51200
Mv 116,44,8
Mv 176,48,8
Rt 148,23,49152
Mv 180,52,8
buffer_load_dwordx4 v14, s[8:11], s39 offen offset:2048 lds
Mv 240,56,8
Rt 150,23,51200
Mv 244,60,8
Mv 252,60,12
Rt 152,24,49152
Mv 248,56,12
buffer_store_dwordx4 a[104:107], v2, s[20:23], s51 offen offset:2048 sc0 sc1
Mv 188,52,12
Rt 154,24,51200
Mv 184,48,12
Mv 124,44,12
Rt 156,25,49152
Mv 120,40,12
buffer_load_dwordx4 v15, s[8:11], s39 offen offset:3072 lds
Mv 60,36,12
Rt 158,25,51200
Mv 56,32,12
Mv 64,32,16
Rb 160,17,32768
Mv 68,36,16
)" R"(
buffer_store_dwordx4 a[108:111], v2, s[20:23], s51 offen offset:3072 sc0 sc1
Mv 128,40,16
Rb 164,17,34816
s_mov_b64 s[16:17], s[70:71]
Mv 132,44,16
s_mov_b32 s18, 0x188000
Mv 192,48,16
Rb 168,17,40960
Mv 196,52,16
s_cmp_eq_u32 s33, 0
s_cmov_b32 s18, 0x380000
Ma 192,56,16
Rb 172,17,43008
s_mul_i32 s43, s2, 3072
Ma 196,60,16
s_cmp_eq_u32 s33, 0
s_cselect_b32 s44, 3, 6
Ma 204,60,20
Rb 176,17,49152
s_mul_i32 s44, s44, 0x60000
Ma 200,56,20
buffer_store_dwordx4 a[112:115], v3, s[20:23], s51 offen offset:0 sc0 sc1
Mv 204,52,20
Rb 180,17,51200
s_sub_u32 s43, s43, s44
Mv 200,48,20
v_min_u32 v26, 23, v0
v_lshlrev_b32 v26, 7, v26
Mv 140,44,20
Rb 184,17,57344
v_and_b32 v5, 7, v0
Mv 136,40,20
v_lshlrev_b32 v5, 7, v5
Mv 76,36,20
Rb 188,17,59392
Mv 72,32,20
Mv 80,32,24
Mv 84,36,24
buffer_store_dwordx4 a[116:119], v3, s[20:23], s51 offen offset:1024 sc0 sc1
Mv 144,40,24
Mv 148,44,24
Mv 208,48,24
Mv 212,52,24
Ma 208,56,24
Ma 212,60,24
Ma 220,60,28
Ma 216,56,28
buffer_store_dwordx4 a[120:123], v3, s[20:23], s51 offen offset:2048 sc0 sc1
Mv 220,52,28
Mv 216,48,28
Mv 156,44,28
Mv 152,40,28
Mv 92,36,28
Mv 88,32,28
s_waitcnt vmcnt(16) lgkmcnt(0)
s_barrier
Mv 32,160,128
Rt 0,18,0
Mv 36,164,128
s_add_u32 s38, s38, 0xffff0080
Mv 96,168,128
Rt 2,18,2048
s_add_u32 m0, s36, 32768
Mv 100,172,128
buffer_load_dwordx4 v8, s[4:7], s38 offen offset:0 lds
)" R"(
Mv 160,176,128
Rt 4,19,0
Mv 164,180,128
Mv 224,184,128
Rt 6,19,2048
Mv 228,188,128
buffer_load_dwordx4 v9, s[4:7], s38 offen offset:1024 lds
Mv 236,188,132
Rt 8,20,0
Mv 232,184,132
Mv 172,180,132
Rt 10,20,2048
Mv 168,176,132
buffer_load_dwordx4 v10, s[4:7], s38 offen offset:2048 lds
Mv 108,172,132
Rt 12,21,0
Mv 104,168,132
Mv 44,164,132
Rt 14,21,2048
Mv 40,160,132
buffer_load_dwordx4 v11, s[4:7], s38 offen offset:3072 lds
Mv 48,160,136
Rt 16,22,0
Mv 52,164,136
s_add_u32 s38, s38, 0x10000
Mv 112,168,136
Rt 18,22,2048
s_add_u32 m0, s36, 36864
Mv 116,172,136
buffer_load_dwordx4 v8, s[4:7], s38 offen offset:0 lds
Mv 176,176,136
Rt 20,23,0
Mv 180,180,136
Mv 240,184,136
Rt 22,23,2048
Mv 244,188,136
buffer_load_dwordx4 v9, s[4:7], s38 offen offset:1024 lds
Mv 252,188,140
Rt 24,24,0
Mv 248,184,140
Mv 188,180,140
Rt 26,24,2048
Mv 184,176,140
buffer_load_dwordx4 v10, s[4:7], s38 offen offset:2048 lds
Mv 124,172,140
Rt 28,25,0
Mv 120,168,140
Mv 60,164,140
Rt 30,25,2048
v_accvgpr_write_b32 a224, -1
Mv 56,160,140
buffer_load_dwordx4 v11, s[4:7], s38 offen offset:3072 lds
Mv 64,160,144
Rb 32,16,0
v_accvgpr_write_b32 a225, -1
Mv 68,164,144
v_accvgpr_write_b32 a226, -1
v_accvgpr_write_b32 a227, -1
Mv 128,168,144
Rb 36,16,2048
s_add_u32 s88, s73, 0x4000
Mv 132,172,144
s_add_u32 s39, s39, 0x40000
s_mov_b32 s40, 6
)" R"(
Mv 192,176,144
Rb 40,16,8192
s_add_u32 m0, s37, 49152
Mv 196,180,144
buffer_load_dwordx4 v12, s[8:11], s39 offen offset:0 lds
Ma 192,184,144
Rb 44,16,10240
s_mov_b32 s94, 0
Ma 196,188,144
Ma 204,188,148
Rb 48,16,16384
Ma 200,184,148
Mv 204,180,148
Rb 52,16,18432
Mv 200,176,148
buffer_load_dwordx4 v13, s[8:11], s39 offen offset:1024 lds
Mv 140,172,148
Rb 56,16,24576
Mv 136,168,148
Mv 76,164,148
Rb 60,16,26624
Mv 72,160,148
Mv 80,160,152
Mv 84,164,152
buffer_load_dwordx4 v14, s[8:11], s39 offen offset:2048 lds
Mv 144,168,152
Mv 148,172,152
Mv 208,176,152
Mv 212,180,152
Ma 208,184,152
Ma 212,188,152
buffer_load_dwordx4 v15, s[8:11], s39 offen offset:3072 lds
Ma 220,188,156
Ma 216,184,156
Mv 220,180,156
Mv 216,176,156
buffer_store_dwordx4 a[124:127], v3, s[20:23], s51 offen offset:3072 sc0 sc1
Mv 156,172,156
Mv 152,168,156
Mv 92,164,156
Mv 88,160,156
s_waitcnt vmcnt(25) lgkmcnt(0)
s_barrier
.LzPBV:
Mv 32,32,0
Rt 128,18,16384
Mv 36,36,0
Mv 96,40,0
Rt 130,18,18432
Mv 100,44,0
s_add_u32 s39, s39, 0x40000
Mv 160,48,0
Rt 132,19,16384
s_add_u32 m0, s37, 0
Mv 164,52,0
buffer_load_dwordx4 v12, s[8:11], s39 offen offset:0 lds
Mv 224,56,0
Rt 134,19,18432
Mv 228,60,0
Mv 236,60,4
Rt 136,20,16384
Mv 232,56,4
Mv 172,52,4
Rt 138,20,18432
Mv 168,48,4
)" R"(
Mv 108,44,4
Rt 140,21,16384
Mv 104,40,4
buffer_load_dwordx4 v13, s[8:11], s39 offen offset:1024 lds
Mv 44,36,4
Rt 142,21,18432
Mv 40,32,4
Mv 48,32,8
Rt 144,22,16384
Mv 52,36,8
Mv 112,40,8
Rt 146,22,18432
Mv 116,44,8
Mv 176,48,8
Rt 148,23,16384
Mv 180,52,8
buffer_load_dwordx4 v14, s[8:11], s39 offen offset:2048 lds
Mv 240,56,8
Rt 150,23,18432
Mv 244,60,8
Mv 252,60,12
Rt 152,24,16384
Mv 248,56,12
Mv 188,52,12
Rt 154,24,18432
Mv 184,48,12
Mv 124,44,12
Rt 156,25,16384
Mv 120,40,12
buffer_load_dwordx4 v15, s[8:11], s39 offen offset:3072 lds
Mv 60,36,12
Rt 158,25,18432
Mv 56,32,12
Mv 64,32,16
Rb 160,17,0
Mv 68,36,16
Mv 128,40,16
Rb 164,17,2048
Mv 132,44,16
Mv 192,48,16
Rb 168,17,8192
Mv 196,52,16
buffer_store_dwordx4 a[224:227], v1, s[20:23], s72 offen sc0 sc1
Ma 192,56,16
Rb 172,17,10240
s_add_u32 s72, s72, 1024
Ma 196,60,16
Ma 204,60,20
Rb 176,17,16384
Ma 200,56,20
Mv 204,52,20
Rb 180,17,18432
Mv 200,48,20
Mv 140,44,20
Rb 184,17,24576
Mv 136,40,20
Mv 76,36,20
Rb 188,17,26624
Mv 72,32,20
Mv 80,32,24
Mv 84,36,24
buffer_store_dwordx4 a[224:227], v1, s[20:23], s88 offen sc0 sc1
Mv 144,40,24
s_add_u32 s88, s88, 1024
)" R"(
Mv 148,44,24
Mv 208,48,24
Mv 212,52,24
Ma 208,56,24
Ma 212,60,24
Ma 220,60,28
Ma 216,56,28
Mv 220,52,28
Mv 216,48,28
Mv 156,44,28
Mv 152,40,28
Mv 92,36,28
Mv 88,32,28
s_waitcnt vmcnt(11) lgkmcnt(0)
s_barrier
Mv 32,160,128
Rt 0,18,32768
Mv 36,164,128
s_add_u32 s38, s38, 0xffff0080
Mv 96,168,128
Rt 2,18,34816
s_add_u32 m0, s36, 0
Mv 100,172,128
buffer_load_dwordx4 v8, s[4:7], s38 offen offset:0 lds
Mv 160,176,128
Rt 4,19,32768
Mv 164,180,128
Mv 224,184,128
Rt 6,19,34816
.LzH7:
s_cbranch_execz .LzH12
Mv 228,188,128
buffer_load_dwordx4 v9, s[4:7], s38 offen offset:1024 lds
Mv 236,188,132
Rt 8,20,32768
Mv 232,184,132
Mv 172,180,132
Rt 10,20,34816
Mv 168,176,132
buffer_load_dwordx4 v10, s[4:7], s38 offen offset:2048 lds
Mv 108,172,132
Rt 12,21,32768
Mv 104,168,132
Mv 44,164,132
Rt 14,21,34816
Mv 40,160,132
buffer_load_dwordx4 v11, s[4:7], s38 offen offset:3072 lds
Mv 48,160,136
Rt 16,22,32768
Mv 52,164,136
s_add_u32 s38, s38, 0x10000
Mv 112,168,136
Rt 18,22,34816
s_add_u32 m0, s36, 4096
Mv 116,172,136
buffer_load_dwordx4 v8, s[4:7], s38 offen offset:0 lds
Mv 176,176,136
Rt 20,23,32768
Mv 180,180,136
Mv 240,184,136
Rt 22,23,34816
Mv 244,188,136
buffer_load_dwordx4 v9, s[4:7], s38 offen offset:1024 lds
Mv 252,188,140
Rt 24,24,32768
)" R"(
Mv 248,184,140
Mv 188,180,140
Rt 26,24,34816
Mv 184,176,140
buffer_load_dwordx4 v10, s[4:7], s38 offen offset:2048 lds
Mv 124,172,140
Rt 28,25,32768
Mv 120,168,140
Mv 60,164,140
Rt 30,25,34816
Mv 56,160,140
buffer_load_dwordx4 v11, s[4:7], s38 offen offset:3072 lds
Mv 64,160,144
Rb 32,16,32768
Mv 68,164,144
Mv 128,168,144
Rb 36,16,34816
Mv 132,172,144
s_add_u32 s39, s39, 0x40000
Mv 192,176,144
Rb 40,16,40960
s_add_u32 m0, s37, 16384
Mv 196,180,144
buffer_load_dwordx4 v12, s[8:11], s39 offen offset:0 lds
Ma 192,184,144
Rb 44,16,43008
Ma 196,188,144
Ma 204,188,148
Rb 48,16,49152
Ma 200,184,148
Mv 204,180,148
Rb 52,16,51200
Mv 200,176,148
buffer_load_dwordx4 v13, s[8:11], s39 offen offset:1024 lds
Mv 140,172,148
Rb 56,16,57344
Mv 136,168,148
Mv 76,164,148
Rb 60,16,59392
Mv 72,160,148
Mv 80,160,152
Mv 84,164,152
buffer_load_dwordx4 v14, s[8:11], s39 offen offset:2048 lds
Mv 144,168,152
Mv 148,172,152
Mv 208,176,152
Mv 212,180,152
Ma 208,184,152
Ma 212,188,152
buffer_load_dwordx4 v15, s[8:11], s39 offen offset:3072 lds
Ma 220,188,156
Ma 216,184,156
s_add_u32 s43, s43, 0x60000
Mv 220,180,156
Mv 216,176,156
buffer_load_dword v27, v26, s[16:19], s43 offen
Mv 156,172,156
Mv 152,168,156
Mv 92,164,156
Mv 88,160,156
s_waitcnt vmcnt(19) lgkmcnt(0)
s_barrier
buffer_store_dword v6, v7, s[20:23], s52 offen offset:0 sc0 sc1
s_mov_b32 s52, 0x40000000
Mv 32,32,0
)" R"(
Rt 128,18,49152
Mv 36,36,0
Mv 96,40,0
Rt 130,18,51200
Mv 100,44,0
s_add_u32 s39, s39, 0x40000
Mv 160,48,0
Rt 132,19,49152
s_add_u32 m0, s37, 32768
Mv 164,52,0
buffer_load_dwordx4 v12, s[8:11], s39 offen offset:0 lds
Mv 224,56,0
Rt 134,19,51200
Mv 228,60,0
Mv 236,60,4
Rt 136,20,49152
Mv 232,56,4
Mv 172,52,4
Rt 138,20,51200
Mv 168,48,4
Mv 108,44,4
Rt 140,21,49152
Mv 104,40,4
buffer_load_dwordx4 v13, s[8:11], s39 offen offset:1024 lds
Mv 44,36,4
Rt 142,21,51200
Mv 40,32,4
Mv 48,32,8
Rt 144,22,49152
Mv 52,36,8
Mv 112,40,8
Rt 146,22,51200
Mv 116,44,8
Mv 176,48,8
Rt 148,23,49152
Mv 180,52,8
buffer_load_dwordx4 v14, s[8:11], s39 offen offset:2048 lds
Mv 240,56,8
Rt 150,23,51200
Mv 244,60,8
Mv 252,60,12
Rt 152,24,49152
Mv 248,56,12
Mv 188,52,12
Rt 154,24,51200
Mv 184,48,12
Mv 124,44,12
Rt 156,25,49152
Mv 120,40,12
buffer_load_dwordx4 v15, s[8:11], s39 offen offset:3072 lds
Mv 60,36,12
Rt 158,25,51200
Mv 56,32,12
Mv 64,32,16
Rb 160,17,32768
Mv 68,36,16
Mv 128,40,16
Rb 164,17,34816
Mv 132,44,16
Mv 192,48,16
Rb 168,17,40960
Mv 196,52,16
buffer_store_dwordx4 a[224:227], v1, s[20:23], s72 offen sc0 sc1
Ma 192,56,16
)" R"(
Rb 172,17,43008
s_add_u32 s72, s72, 1024
Ma 196,60,16
Ma 204,60,20
Rb 176,17,49152
Ma 200,56,20
Mv 204,52,20
Rb 180,17,51200
Mv 200,48,20
Mv 140,44,20
Rb 184,17,57344
Mv 136,40,20
Mv 76,36,20
Rb 188,17,59392
Mv 72,32,20
Mv 80,32,24
Mv 84,36,24
buffer_store_dwordx4 a[224:227], v1, s[20:23], s88 offen sc0 sc1
Mv 144,40,24
s_add_u32 s88, s88, 1024
Mv 148,44,24
Mv 208,48,24
Mv 212,52,24
Ma 208,56,24
Ma 212,60,24
Ma 220,60,28
Ma 216,56,28
Mv 220,52,28
Mv 216,48,28
Mv 156,44,28
Mv 152,40,28
Mv 92,36,28
s_cmp_eq_u32 s40, 3
s_cmov_b32 s72, 0x40000000
s_cmov_b32 s88, 0x40000000
Mv 88,32,28
s_waitcnt vmcnt(12) lgkmcnt(0)
s_barrier
Mv 32,160,128
Rt 0,18,0
Mv 36,164,128
s_add_u32 s38, s38, 0xffff0080
Mv 96,168,128
Rt 2,18,2048
s_add_u32 m0, s36, 32768
Mv 100,172,128
buffer_load_dwordx4 v8, s[4:7], s38 offen offset:0 lds
Mv 160,176,128
Rt 4,19,0
Mv 164,180,128
Mv 224,184,128
Rt 6,19,2048
Mv 228,188,128
buffer_load_dwordx4 v9, s[4:7], s38 offen offset:1024 lds
Mv 236,188,132
Rt 8,20,0
Mv 232,184,132
Mv 172,180,132
Rt 10,20,2048
Mv 168,176,132
buffer_load_dwordx4 v10, s[4:7], s38 offen offset:2048 lds
Mv 108,172,132
Rt 12,21,0
Mv 104,168,132
Mv 44,164,132
)" R"(
Rt 14,21,2048
Mv 40,160,132
buffer_load_dwordx4 v11, s[4:7], s38 offen offset:3072 lds
Mv 48,160,136
Rt 16,22,0
Mv 52,164,136
s_add_u32 s38, s38, 0x10000
Mv 112,168,136
Rt 18,22,2048
s_add_u32 m0, s36, 36864
Mv 116,172,136
buffer_load_dwordx4 v8, s[4:7], s38 offen offset:0 lds
Mv 176,176,136
Rt 20,23,0
Mv 180,180,136
Mv 240,184,136
Rt 22,23,2048
Mv 244,188,136
buffer_load_dwordx4 v9, s[4:7], s38 offen offset:1024 lds
Mv 252,188,140
Rt 24,24,0
Mv 248,184,140
Mv 188,180,140
Rt 26,24,2048
Mv 184,176,140
buffer_load_dwordx4 v10, s[4:7], s38 offen offset:2048 lds
Mv 124,172,140
Rt 28,25,0
Mv 120,168,140
Mv 60,164,140
Rt 30,25,2048
Mv 56,160,140
buffer_load_dwordx4 v11, s[4:7], s38 offen offset:3072 lds
Mv 64,160,144
Rb 32,16,0
Mv 68,164,144
Mv 128,168,144
Rb 36,16,2048
Mv 132,172,144
s_add_u32 s39, s39, 0x40000
Mv 192,176,144
Rb 40,16,8192
s_add_u32 m0, s37, 49152
Mv 196,180,144
buffer_load_dwordx4 v12, s[8:11], s39 offen offset:0 lds
Ma 192,184,144
Rb 44,16,10240
Ma 196,188,144
Ma 204,188,148
Rb 48,16,16384
Ma 200,184,148
Mv 204,180,148
Rb 52,16,18432
Mv 200,176,148
buffer_load_dwordx4 v13, s[8:11], s39 offen offset:1024 lds
Mv 140,172,148
Rb 56,16,24576
Mv 136,168,148
Mv 76,164,148
Rb 60,16,26624
Mv 72,160,148
Mv 80,160,152
Mv 84,164,152
buffer_load_dwordx4 v14, s[8:11], s39 offen offset:2048 lds
)" R"(
Mv 144,168,152
Mv 148,172,152
Mv 208,176,152
Mv 212,180,152
Ma 208,184,152
Ma 212,188,152
buffer_load_dwordx4 v15, s[8:11], s39 offen offset:3072 lds
Ma 220,188,156
Ma 216,184,156
s_add_u32 s43, s43, 0x60000
Mv 220,180,156
s_sub_u32 s40, s40, 1
Mv 216,176,156
buffer_load_dword v27, v26, s[16:19], s43 offen
Mv 156,172,156
Mv 152,168,156
Mv 92,164,156
Mv 88,160,156
s_cmp_lg_u32 s40, 0
s_waitcnt vmcnt(21) lgkmcnt(0)
s_barrier
s_cbranch_scc1 .LzPBV
.LzPBVX:
Mv 32,32,0
Rt 128,18,16384
Mv 36,36,0
buffer_load_dword v4, v5, s[20:23], s50 offen offset:0 sc0 sc1
Mv 96,40,0
Rt 130,18,18432
Mv 100,44,0
Mv 160,48,0
Rt 132,19,16384
Mv 164,52,0
Mv 224,56,0
Rt 134,19,18432
Mv 228,60,0
Mv 236,60,4
Rt 136,20,16384
Mv 232,56,4
Mv 172,52,4
Rt 138,20,18432
Mv 168,48,4
Mv 108,44,4
Rt 140,21,16384
Mv 104,40,4
Mv 44,36,4
Rt 142,21,18432
Mv 40,32,4
Mv 48,32,8
Rt 144,22,16384
Mv 52,36,8
Mv 112,40,8
Rt 146,22,18432
Mv 116,44,8
Mv 176,48,8
Rt 148,23,16384
Mv 180,52,8
Mv 240,56,8
Rt 150,23,18432
Mv 244,60,8
Mv 252,60,12
Rt 152,24,16384
Mv 248,56,12
Mv 188,52,12
Rt 154,24,18432
)" R"(
Mv 184,48,12
Mv 124,44,12
Rt 156,25,16384
Mv 120,40,12
Mv 60,36,12
Rt 158,25,18432
Mv 56,32,12
Mv 64,32,16
Rb 160,17,0
Mv 68,36,16
Mv 128,40,16
Rb 164,17,2048
Mv 132,44,16
Mv 192,48,16
Rb 168,17,8192
Mv 196,52,16
Ma 192,56,16
Rb 172,17,10240
Ma 196,60,16
s_waitcnt vmcnt(0)
v_cmp_eq_u32_e32 vcc, s3, v4
s_andn2_b64 s[28:29], -1, vcc
s_cbranch_scc1 .Lzsp16
.Lzok16:
Ma 204,60,20
Rb 176,17,16384
Ma 200,56,20
s_lshl3_add_u32 s29, s34, s35
s_lshl2_add_u32 s29, s29, s33
Mv 204,52,20
Rb 180,17,18432
s_add_u32 m0, s36, 131072
Mv 200,48,20
buffer_load_dwordx4 v2, s[20:23], s56 offen offset:0 sc1 lds
Mv 140,44,20
Rb 184,17,24576
s_mul_i32 s83, s29, 7168
Mv 136,40,20
s_mov_b32 s82, s89
s_add_u32 s82, s82, 0xc00000
Mv 76,36,20
Rb 188,17,26624
s_add_u32 s82, s82, s84
Mv 72,32,20
s_mov_b32 s24, s80
s_mov_b32 s25, s81
Mv 80,32,24
s_mov_b32 s26, 0x380000
s_mov_b32 s27, 0x20000
Mv 84,36,24
buffer_load_dwordx4 v2, s[20:23], s56 offen offset:1024 sc1 lds
Mv 144,40,24
s_add_u32 s28, s33, 0
s_lshl2_add_u32 s28, s35, s28
Mv 148,44,24
s_mul_i32 s29, s28, 293
s_lshr_b32 s29, s29, 11
Mv 208,48,24
s_mul_i32 s29, s29, 9
s_add_u32 s29, s29, s28
Mv 212,52,24
s_lshl_b32 s29, s29, 10
s_add_u32 s4, s29, s82
Ma 208,56,24
)" R"(
s_add_u32 s28, s33, 4
s_lshl3_add_u32 s28, s28, s35
Ma 212,60,24
buffer_load_dwordx4 v2, s[20:23], s56 offen offset:2048 sc1 lds
Ma 220,60,28
s_mul_i32 s29, s28, 293
s_lshr_b32 s29, s29, 11
Ma 216,56,28
s_mul_i32 s29, s29, 9
s_add_u32 s29, s29, s28
Mv 220,52,28
s_lshl_b32 s29, s29, 10
s_add_u32 s5, s29, s82
Mv 216,48,28
s_add_u32 s28, s33, 8
s_lshl3_add_u32 s28, s28, s35
Mv 156,44,28
s_mul_i32 s29, s28, 293
s_lshr_b32 s29, s29, 11
Mv 152,40,28
buffer_load_dwordx4 v2, s[20:23], s56 offen offset:3072 sc1 lds
Mv 92,36,28
s_mul_i32 s29, s29, 9
s_add_u32 s29, s29, s28
Mv 88,32,28
s_waitcnt lgkmcnt(0)
s_barrier
Mv 32,160,128
Rt 0,18,32768
s_lshl_b32 s29, s29, 10
Mv 36,164,128
s_add_u32 s6, s29, s82
s_add_u32 s28, s33, 12
Mv 96,168,128
Rt 2,18,34816
s_add_u32 m0, s36, 0
Mv 100,172,128
buffer_load_dwordx4 v2, s[20:23], s55 offen offset:0 sc1 lds
Mv 160,176,128
Rt 4,19,32768
s_lshl3_add_u32 s28, s28, s35
Mv 164,180,128
s_mul_i32 s29, s28, 293
s_lshr_b32 s29, s29, 11
Mv 224,184,128
Rt 6,19,34816
s_mul_i32 s29, s29, 9
Mv 228,188,128
s_add_u32 s29, s29, s28
s_lshl_b32 s29, s29, 10
Mv 236,188,132
Rt 8,20,32768
buffer_load_dwordx4 v2, s[20:23], s55 offen offset:1024 sc1 lds
Mv 232,184,132
s_add_u32 s7, s29, s82
Mv 172,180,132
Rt 10,20,34816
Mv 168,176,132
s_cmp_gt_u32 s33, 1
s_cmov_b32 s7, 0x40000000
Mv 108,172,132
Rt 12,21,32768
s_add_u32 s28, s33, 14
Mv 104,168,132
buffer_load_dwordx4 v2, s[20:23], s55 offen offset:2048 sc1 lds
)" R"(
Mv 44,164,132
Rt 14,21,34816
s_lshl3_add_u32 s28, s28, s35
Mv 40,160,132
s_mul_i32 s29, s28, 293
s_lshr_b32 s29, s29, 11
Mv 48,160,136
Rt 16,22,32768
s_mul_i32 s29, s29, 9
Mv 52,164,136
s_add_u32 s29, s29, s28
s_lshl_b32 s29, s29, 10
Mv 112,168,136
Rt 18,22,34816
buffer_load_dwordx4 v2, s[20:23], s55 offen offset:3072 sc1 lds
Mv 116,172,136
s_add_u32 s8, s29, s82
s_add_u32 s28, s33, 18
Mv 176,176,136
Rt 20,23,32768
s_lshl3_add_u32 s28, s28, s35
Mv 180,180,136
s_mul_i32 s29, s28, 293
s_lshr_b32 s29, s29, 11
Mv 240,184,136
Rt 22,23,34816
s_add_u32 m0, s36, 4096
Mv 244,188,136
buffer_load_dwordx4 v3, s[20:23], s55 offen offset:0 sc1 lds
Mv 252,188,140
Rt 24,24,32768
s_mul_i32 s29, s29, 9
Mv 248,184,140
s_add_u32 s29, s29, s28
s_lshl_b32 s29, s29, 10
Mv 188,180,140
Rt 26,24,34816
s_add_u32 s9, s29, s82
Mv 184,176,140
s_add_u32 s28, s33, 22
s_lshl3_add_u32 s28, s28, s35
Mv 124,172,140
Rt 28,25,32768
buffer_load_dwordx4 v3, s[20:23], s55 offen offset:1024 sc1 lds
Mv 120,168,140
s_mul_i32 s29, s28, 293
s_lshr_b32 s29, s29, 11
Mv 60,164,140
Rt 30,25,34816
s_mul_i32 s29, s29, 9
Mv 56,160,140
s_add_u32 s29, s29, s28
s_lshl_b32 s29, s29, 10
Mv 64,160,144
Rb 32,16,32768
s_add_u32 s10, s29, s82
Mv 68,164,144
buffer_load_dwordx4 v3, s[20:23], s55 offen offset:2048 sc1 lds
Mv 128,168,144
Rb 36,16,34816
s_add_u32 s28, s33, 26
Mv 132,172,144
s_lshl3_add_u32 s28, s28, s35
s_mul_i32 s29, s28, 293
)" R"(
Mv 192,176,144
Rb 40,16,40960
s_lshr_b32 s29, s29, 11
.LzH8:
s_cbranch_execz .LzH18
Mv 196,180,144
s_mul_i32 s29, s29, 9
s_add_u32 s29, s29, s28
Ma 192,184,144
Rb 44,16,43008
buffer_load_dwordx4 v3, s[20:23], s55 offen offset:3072 sc1 lds
Ma 196,188,144
s_lshl_b32 s29, s29, 10
s_add_u32 s11, s29, s82
Ma 204,188,148
Rb 48,16,49152
Ma 200,184,148
s_cmp_gt_u32 s33, 1
s_cmov_b32 s11, 0x40000000
Mv 204,180,148
Rb 52,16,51200
s_add_u32 m0, s36, 135168
Mv 200,176,148
buffer_load_dwordx4 v3, s[20:23], s56 offen offset:0 sc1 lds
Mv 140,172,148
Rb 56,16,57344
s_and_b32 s28, s34, 1
Mv 136,168,148
s_lshl_b32 s28, s28, 14
s_add_u32 s82, s37, s28
Mv 76,164,148
Rb 60,16,59392
s_add_u32 s82, s82, 0x10000
Mv 72,160,148
Mv 80,160,152
buffer_load_dwordx4 v3, s[20:23], s56 offen offset:1024 sc1 lds
Mv 84,164,152
Mv 144,168,152
Mv 148,172,152
Mv 208,176,152
Mv 212,180,152
buffer_load_dwordx4 v3, s[20:23], s56 offen offset:2048 sc1 lds
Ma 208,184,152
Ma 212,188,152
Ma 220,188,156
Ma 216,184,156
Mv 220,180,156
buffer_load_dwordx4 v3, s[20:23], s56 offen offset:3072 sc1 lds
Mv 216,176,156
Mv 156,172,156
Mv 152,168,156
s_add_u32 s55, s55, 0x10000
s_add_u32 s56, s56, 0x10000
Mv 92,164,156
Mv 88,160,156
s_waitcnt lgkmcnt(4)
s_barrier
Mv 32,32,0
Rt 128,18,49152
Mv 36,36,0
Rt 130,18,51200
Mv 96,40,0
Rt 132,19,49152
s_add_u32 m0, s36, 65536
Mv 100,44,0
)" R"(
Rt 134,19,51200
buffer_load_dwordx4 v2, s[20:23], s56 offen offset:0 sc1 lds
Mv 108,44,4
Rt 136,20,49152
Mv 104,40,4
Rt 138,20,51200
s_add_u32 s50, s50, 1024
Mv 44,36,4
Rt 140,21,49152
s_min_u32 s50, s50, s60
Mv 40,32,4
Rt 142,21,51200
Mv 48,32,8
Rt 144,22,49152
Mv 52,36,8
Rt 146,22,51200
Mv 112,40,8
Rt 148,23,49152
Mv 116,44,8
Rt 150,23,51200
buffer_load_dwordx4 v2, s[20:23], s56 offen offset:1024 sc1 lds
Mv 124,44,12
Rt 152,24,49152
Mv 120,40,12
Rt 154,24,51200
Mv 60,36,12
Rt 156,25,49152
Mv 56,32,12
Rt 158,25,51200
Mv 64,32,16
Rb 160,17,32768
Mv 68,36,16
Rb 164,17,34816
Mv 128,40,16
Rb 168,17,40960
Mv 132,44,16
Rb 172,17,43008
buffer_load_dwordx4 v2, s[20:23], s56 offen offset:2048 sc1 lds
Mv 140,44,20
Rb 176,17,49152
Mv 136,40,20
Rb 180,17,51200
Mv 76,36,20
Rb 184,17,57344
Mv 72,32,20
Rb 188,17,59392
Mv 80,32,24
v_add_u32 v8, 0x10000, v1
v_add_u32 v10, 0x20000, v1
Mv 84,36,24
buffer_load_dword v4, v5, s[20:23], s50 offen offset:0 sc0 sc1
Mv 144,40,24
s_mov_b32 s93, 2
s_mov_b32 s94, 0
Mv 148,44,24
buffer_load_dwordx4 v2, s[20:23], s56 offen offset:3072 sc1 lds
Mv 156,44,28
Mv 152,40,28
Mv 92,36,28
Mv 88,32,28
s_waitcnt lgkmcnt(0)
s_barrier
Mv 32,160,128
Mv 36,164,128
Mv 40,160,132
)" R"(
s_add_u32 m0, s36, 32768
Mv 44,164,132
buffer_load_dwordx4 v2, s[20:23], s55 offen offset:0 sc1 lds
Mv 48,160,136
Mv 52,164,136
s_nop 0
Cv 32,32,33
Cv 33,34,35
Mv 56,160,140
Cv 34,36,37
Cv 35,38,39
Mv 60,164,140
Cv 40,40,41
Cv 41,42,43
Mv 64,160,144
Cv 42,44,45
Cv 43,46,47
Mv 68,164,144
Cv 48,48,49
Cv 49,50,51
Mv 72,160,148
Cv 50,52,53
Cv 51,54,55
Mv 76,164,148
buffer_load_dwordx4 v2, s[20:23], s55 offen offset:1024 sc1 lds
Mv 80,160,152
Cv 56,56,57
Cv 57,58,59
Mv 84,164,152
Cv 58,60,61
Cv 59,62,63
Mv 88,160,156
Cv 64,64,65
Cv 65,66,67
Mv 92,164,156
Cv 66,68,69
Cv 67,70,71
Mv 96,168,128
Cv 72,72,73
Cv 73,74,75
Mv 100,172,128
Cv 74,76,77
Cv 75,78,79
Mv 104,168,132
Cv 80,80,81
Cv 81,82,83
Mv 108,172,132
buffer_load_dwordx4 v2, s[20:23], s55 offen offset:2048 sc1 lds
Mv 112,168,136
Cv 82,84,85
Cv 83,86,87
Mv 116,172,136
Cv 88,88,89
Cv 89,90,91
Mv 120,168,140
Cv 90,92,93
Cv 91,94,95
Mv 124,172,140
buffer_store_dwordx4 v[32:35], v2, s[20:23], s45 offen offset:0 sc0 sc1
Mv 128,168,144
Mv 132,172,144
Mv 136,168,148
Mv 140,172,148
buffer_load_dwordx4 v2, s[20:23], s55 offen offset:3072 sc1 lds
)" R"(
Mv 144,168,152
Mv 148,172,152
s_nop 1
Cv 128,128,129
Cv 129,130,131
Mv 152,168,156
Cv 130,132,133
Cv 131,134,135
Mv 156,172,156
buffer_store_dwordx4 v[40:43], v2, s[20:23], s45 offen offset:1024 sc0 sc1
Mv 160,48,0
Cv 136,136,137
Cv 137,138,139
Mv 160,176,128
Cv 138,140,141
Cv 139,142,143
Mv 164,52,0
s_add_u32 m0, s36, 36864
Cv 144,144,145
Mv 164,180,128
buffer_load_dwordx4 v3, s[20:23], s55 offen offset:0 sc1 lds
Mv 168,48,4
Cv 145,146,147
Cv 146,148,149
Mv 168,176,132
Cv 147,150,151
Cv 152,152,153
Mv 172,52,4
Cv 153,154,155
Cv 154,156,157
Mv 172,180,132
buffer_store_dwordx4 v[48:51], v2, s[20:23], s45 offen offset:2048 sc0 sc1
Mv 176,48,8
Cv 155,158,159
Cv 132,96,97
Mv 176,176,136
Cv 133,98,99
Cv 134,100,101
Mv 180,52,8
Cv 135,102,103
Cv 140,104,105
Mv 180,180,136
buffer_load_dwordx4 v3, s[20:23], s55 offen offset:1024 sc1 lds
Mv 184,48,12
Cv 141,106,107
Cv 142,108,109
Mv 184,176,140
Cv 143,110,111
Cv 148,112,113
Mv 188,52,12
Cv 149,114,115
Cv 150,116,117
Mv 188,180,140
buffer_store_dwordx4 v[56:59], v2, s[20:23], s45 offen offset:3072 sc0 sc1
Mv 192,48,16
Cv 151,118,119
Cv 156,120,121
Mv 192,176,144
Cv 157,122,123
Cv 158,124,125
Mv 196,52,16
Cv 159,126,127
Cv 160,160,161
Mv 196,180,144
)" R"(
buffer_load_dwordx4 v3, s[20:23], s55 offen offset:2048 sc1 lds
Mv 200,48,20
Cv 161,162,163
Cv 162,164,165
Mv 200,176,148
Cv 163,166,167
v_accvgpr_write_b32 a224, v160
Mv 204,52,20
v_accvgpr_write_b32 a225, v161
v_accvgpr_write_b32 a226, v162
Mv 204,180,148
buffer_store_dwordx4 v[64:67], v3, s[20:23], s45 offen offset:0 sc0 sc1
Mv 208,48,24
v_accvgpr_write_b32 a227, v163
Cv 168,168,169
Mv 208,176,152
Cv 169,170,171
Cv 170,172,173
Mv 212,52,24
Cv 171,174,175
v_accvgpr_write_b32 a228, v168
Mv 212,180,152
buffer_load_dwordx4 v3, s[20:23], s55 offen offset:3072 sc1 lds
Mv 216,48,28
v_accvgpr_write_b32 a229, v169
v_accvgpr_write_b32 a230, v170
Mv 216,176,156
v_accvgpr_write_b32 a231, v171
Cv 176,176,177
Mv 220,52,28
Cv 177,178,179
Cv 178,180,181
Mv 220,180,156
buffer_store_dwordx4 v[72:75], v3, s[20:23], s45 offen offset:1024 sc0 sc1
Mv 224,56,0
Cv 179,182,183
v_accvgpr_write_b32 a232, v176
Mv 228,60,0
v_accvgpr_write_b32 a233, v177
v_accvgpr_write_b32 a234, v178
Mv 236,60,4
s_add_u32 m0, s36, 69632
v_accvgpr_write_b32 a235, v179
Mv 232,56,4
buffer_load_dwordx4 v3, s[20:23], s56 offen offset:0 sc1 lds
Mv 240,56,8
Cv 184,184,185
Cv 185,186,187
Mv 244,60,8
Cv 186,188,189
Cv 187,190,191
Mv 252,60,12
v_accvgpr_write_b32 a236, v184
v_accvgpr_write_b32 a237, v185
Mv 248,56,12
s_waitcnt vmcnt(16)
s_barrier
Ma 192,56,16
v_cmp_eq_u32_e32 vcc, s3, v4
s_andn2_b64 s[28:29], -1, vcc
s_cbranch_scc1 .Lzsp17
.Lzok17:
Ma 196,60,16
buffer_store_dwordx4 v[80:83], v3, s[20:23], s45 offen offset:2048 sc0 sc1
Ma 204,60,20
)" R"(
v_accvgpr_write_b32 a238, v186
v_accvgpr_write_b32 a239, v187
Ma 200,56,20
buffer_load_dwordx4 v3, s[20:23], s56 offen offset:1024 sc1 lds
Ma 208,56,24
Cv 192,192,193
Cv 193,194,195
Ma 212,60,24
Cv 194,196,197
Cv 195,198,199
Ma 220,60,28
v_accvgpr_write_b32 a240, v192
v_accvgpr_write_b32 a241, v193
Ma 216,56,28
buffer_store_dwordx4 v[88:91], v3, s[20:23], s45 offen offset:3072 sc0 sc1
Mv 224,184,128
Rb 0,1,0
v_accvgpr_write_b32 a242, v194
Mv 228,188,128
Rb 4,1,1024
v_accvgpr_write_b32 a243, v195
Mv 232,184,132
Rb 8,1,2048
Cv 200,200,201
Mv 236,188,132
buffer_load_dwordx4 v3, s[20:23], s56 offen offset:2048 sc1 lds
Rb 12,1,3072
Mv 240,184,136
Rb 16,1,8192
Cv 201,202,203
Mv 244,188,136
Rb 20,1,9216
Cv 202,204,205
Mv 248,184,140
Rb 24,1,10240
Cv 203,206,207
Mv 252,188,140
Rb 28,1,11264
v_accvgpr_write_b32 a244, v200
Ma 192,184,144
Rb 32,1,4096
v_accvgpr_write_b32 a245, v201
Ma 196,188,144
Rb 36,1,5120
v_accvgpr_write_b32 a246, v202
Ma 200,184,148
Rb 40,1,6144
v_accvgpr_write_b32 a247, v203
Ma 204,188,148
buffer_load_dwordx4 v3, s[20:23], s56 offen offset:3072 sc1 lds
Rb 44,1,7168
Ma 208,184,152
Ma 212,188,152
Ma 216,184,156
Ma 220,188,156
s_add_u32 s56, s56, 0x10000
.LzH9:
s_cbranch_execz .LzH14
.LzFB:
s_waitcnt lgkmcnt(8)
Mz 64,0,64
Rb 48,1,12288
s_add_u32 s55, s55, 0x10000
Mz 68,0,68
)" R"(
s_cmp_eq_u32 s57, 2
s_cselect_b32 s55, s59, s55
Mv 68,4,76
Rb 52,1,13312
s_sub_u32 s57, s57, 1
v_mov_b32 v16, 0
Mv 64,4,72
buffer_store_dwordx4 v[132:135], v2, s[20:23], s46 offen offset:0 sc0 sc1
Mv 64,8,80
Rb 56,1,14336
Cv 208,208,209
Cv 209,210,211
Mv 68,8,84
Cv 210,212,213
Cv 211,214,215
Mv 68,12,92
Rb 60,1,15360
v_accvgpr_write_b32 a248, v208
s_waitcnt lgkmcnt(8)
Mv 64,12,88
v_accvgpr_write_b32 a249, v209
v_accvgpr_write_b32 a250, v210
Mv 64,16,96
Rb 0,1,16384
v_accvgpr_write_b32 a251, v211
Mv 68,16,100
Cv 216,216,217
Cv 217,218,219
Cv 218,220,221
Mv 68,20,108
Rb 4,1,17408
Cv 219,222,223
Mv 64,20,104
v_accvgpr_write_b32 a252, v216
v_accvgpr_write_b32 a253, v217
v_accvgpr_write_b32 a254, v218
Mv 64,24,112
Rb 8,1,18432
v_accvgpr_write_b32 a255, v219
Mv 68,24,116
buffer_store_dwordx4 v[140:143], v2, s[20:23], s46 offen offset:1024 sc0 sc1
Cv 224,224,225
Mv 68,28,124
Rb 12,1,19456
s_waitcnt lgkmcnt(8)
Mv 64,28,120
Cv 225,226,227
Cv 226,228,229
Cv 227,230,231
Mz 72,32,64
Rb 16,1,24576
v_accvgpr_write_b32 a128, v224
Mz 76,32,68
v_accvgpr_write_b32 a129, v225
v_accvgpr_write_b32 a130, v226
Mv 76,36,76
Rb 20,1,25600
v_accvgpr_write_b32 a131, v227
Cv 232,232,233
Mv 72,36,72
Cv 233,234,235
Cv 234,236,237
Mv 72,40,80
Rb 24,1,26624
)" R"(
Cv 235,238,239
v_accvgpr_write_b32 a132, v232
Mv 76,40,84
v_accvgpr_write_b32 a133, v233
v_accvgpr_write_b32 a134, v234
Mv 76,44,92
Rb 28,1,27648
v_accvgpr_write_b32 a135, v235
s_waitcnt lgkmcnt(8)
Mv 72,44,88
buffer_store_dwordx4 v[148:151], v2, s[20:23], s46 offen offset:2048 sc0 sc1
Mv 72,48,96
Rb 32,1,20480
Cv 240,240,241
Mv 76,48,100
Cv 241,242,243
Cv 242,244,245
Cv 243,246,247
Mv 76,52,108
Rb 36,1,21504
v_accvgpr_write_b32 a136, v240
Mv 72,52,104
v_accvgpr_write_b32 a137, v241
v_accvgpr_write_b32 a138, v242
v_accvgpr_write_b32 a139, v243
Mv 72,56,112
Rb 40,1,22528
Cv 248,248,249
Mv 76,56,116
Cv 249,250,251
Cv 250,252,253
Cv 251,254,255
Mv 76,60,124
Rb 44,1,23552
s_waitcnt lgkmcnt(8)
Mv 72,60,120
v_accvgpr_write_b32 a140, v248
v_accvgpr_write_b32 a141, v249
v_accvgpr_write_b32 a142, v250
Mz 80,0,64
Rb 48,1,28672
v_accvgpr_write_b32 a143, v251
Mz 84,0,68
buffer_store_dwordx4 v[156:159], v2, s[20:23], s46 offen offset:3072 sc0 sc1
Mv 84,4,76
Rb 52,1,29696
v_mov_b32 v17, 0
v_mov_b32 v18, 0
Mv 80,4,72
v_mov_b32 v19, 0
v_mov_b32 v20, 0
Mv 80,8,80
Rb 56,1,30720
v_exp_f32_e32 v32, v64
Mv 84,8,84
v_exp_f32_e32 v24, v65
Mv 84,12,92
Rb 60,1,31744
v_mov_b32 v21, 0
s_waitcnt lgkmcnt(8)
Mv 80,12,88
v_exp_f32_e32 v33, v66
Mv 80,16,96
v_exp_f32_e32 v25, v67
)" R"(
Mv 84,16,100
v_exp_f32_e32 v34, v72
v_mov_b32 v22, 0
Mv 84,20,108
v_exp_f32_e32 v26, v73
Mv 80,20,104
buffer_store_dwordx4 v[128:131], v3, s[20:23], s46 offen offset:0 sc0 sc1
v_mov_b32 v23, 0
Mv 80,24,112
s_add_u32 s50, s50, 1024
s_min_u32 s50, s50, s60
Mv 84,24,116
v_exp_f32_e32 v35, v74
v_add_f32_e32 v14, v32, v24
Mv 84,28,124
v_exp_f32_e32 v27, v75
Mv 80,28,120
s_waitcnt vmcnt(7) lgkmcnt(0)
s_barrier
buffer_store_dword v6, v7, s[20:23], s49 offen offset:0 sc0 sc1
Mz 88,32,64
Rb 0,1,32768
Cv 32,32,24
Mz 92,32,68
Rb 4,1,33792
v_add_f32_e32 v14, v14, v33
Mv 92,36,76
Rb 8,1,34816
s_add_u32 m0, s36, 0
v_add_f32_e32 v14, v14, v25
Mv 88,36,72
Rb 12,1,35840
buffer_load_dwordx4 v2, s[20:23], s55 offen offset:0 sc1 lds
.LzH10:
s_cbranch_execz .LzH15
Mv 88,40,80
Rb 16,1,40960
v_exp_f32_e32 v36, v68
Mv 92,40,84
Rb 20,1,41984
Cv 33,33,25
Mv 92,44,92
Rb 24,1,43008
v_exp_f32_e32 v28, v69
Mv 88,44,88
Rb 28,1,44032
v_add_f32_e32 v14, v14, v34
Mv 88,48,96
Rb 32,1,36864
v_add_f32_e32 v14, v14, v26
Mv 92,48,100
Rb 36,1,37888
v_exp_f32_e32 v37, v70
Mv 92,52,108
Rb 40,1,38912
Cv 34,34,26
Mv 88,52,104
Rb 44,1,39936
buffer_load_dwordx4 v2, s[20:23], s55 offen offset:1024 sc1 lds
Mv 88,56,112
v_exp_f32_e32 v29, v71
Mv 92,56,116
v_exp_f32_e32 v38, v76
v_add_f32_e32 v14, v14, v35
Mv 92,60,124
)" R"(
v_add_f32_e32 v14, v14, v27
s_waitcnt lgkmcnt(8)
Mv 88,60,120
v_exp_f32_e32 v24, v77
Cv 35,35,27
Mz 96,0,64
Rb 48,1,45056
buffer_load_dword v4, v5, s[20:23], s50 offen offset:0 sc0 sc1
Mz 100,0,68
v_exp_f32_e32 v39, v78
Mv 100,4,76
Rb 52,1,46080
v_exp_f32_e32 v25, v79
Mv 96,4,72
buffer_load_dwordx4 v2, s[20:23], s55 offen offset:2048 sc1 lds
Mv 96,8,80
Rb 56,1,47104
v_exp_f32_e32 v40, v80
Mv 100,8,84
v_exp_f32_e32 v26, v81
Mv 100,12,92
Rb 60,1,48128
v_add_f32_e32 v15, v36, v28
s_waitcnt lgkmcnt(8)
Mv 96,12,88
buffer_store_dwordx4 v[136:139], v3, s[20:23], s46 offen offset:1024 sc0 sc1
Mv 96,16,96
Rb 0,1,49152
Cv 36,36,28
Mv 100,16,100
v_exp_f32_e32 v41, v82
v_add_f32_e32 v15, v15, v37
Mv 100,20,108
Rb 4,1,50176
v_add_f32_e32 v15, v15, v29
Mv 96,20,104
buffer_load_dwordx4 v2, s[20:23], s55 offen offset:3072 sc1 lds
Cv 37,37,29
Mv 96,24,112
Rb 8,1,51200
v_add_f32_e32 v15, v15, v38
Mv 100,24,116
v_exp_f32_e32 v27, v83
v_add_f32_e32 v15, v15, v24
Mv 100,28,124
Rb 12,1,52224
s_waitcnt lgkmcnt(8)
Mv 96,28,120
v_exp_f32_e32 v42, v88
Cv 38,38,24
Mz 104,32,64
Rb 16,1,57344
v_add_f32_e32 v15, v15, v39
Mz 108,32,68
v_exp_f32_e32 v28, v89
Mv 108,36,76
Rb 20,1,58368
s_add_u32 m0, s36, 4096
v_add_f32_e32 v15, v15, v25
Mv 104,36,72
buffer_load_dwordx4 v3, s[20:23], s55 offen offset:0 sc1 lds
Mv 104,40,80
Rb 24,1,59392
v_exp_f32_e32 v43, v90
)" R"(
Mv 108,40,84
v_exp_f32_e32 v29, v91
Mv 108,44,92
Rb 28,1,60416
Cv 39,39,25
s_waitcnt lgkmcnt(8)
Mv 104,44,88
buffer_store_dwordx4 v[144:147], v3, s[20:23], s46 offen offset:2048 sc0 sc1
Mv 104,48,96
Rb 32,1,53248
v_add_f32_e32 v14, v14, v40
Mv 108,48,100
v_exp_f32_e32 v44, v84
v_add_f32_e32 v14, v14, v26
Mv 108,52,108
Rb 36,1,54272
Cv 40,40,26
Mv 104,52,104
buffer_load_dwordx4 v3, s[20:23], s55 offen offset:1024 sc1 lds
v_add_f32_e32 v14, v14, v41
Mv 104,56,112
Rb 40,1,55296
v_add_f32_e32 v14, v14, v27
Mv 108,56,116
v_exp_f32_e32 v24, v85
Cv 41,41,27
Mv 108,60,124
Rb 44,1,56320
s_waitcnt lgkmcnt(8)
Mv 104,60,120
v_exp_f32_e32 v45, v86
v_add_f32_e32 v14, v14, v42
Mz 112,0,64
Rb 48,1,61440
v_add_f32_e32 v14, v14, v28
Mz 116,0,68
v_exp_f32_e32 v25, v87
Mv 116,4,76
Rb 52,1,62464
v_exp_f32_e32 v46, v92
Mv 112,4,72
buffer_load_dwordx4 v3, s[20:23], s55 offen offset:2048 sc1 lds
Mv 112,8,80
Rb 56,1,63488
v_exp_f32_e32 v26, v93
Mv 116,8,84
v_exp_f32_e32 v47, v94
Mv 116,12,92
Rb 60,1,64512
Cv 42,42,28
s_waitcnt lgkmcnt(8)
Mv 112,12,88
buffer_store_dwordx4 v[152:155], v3, s[20:23], s46 offen offset:3072 sc0 sc1
Mv 112,16,96
v_exp_f32_e32 v27, v95
Mv 116,16,100
v_add_f32_e32 v14, v14, v43
v_add_f32_e32 v14, v14, v29
Cv 43,43,29
Mv 116,20,108
v_add_f32_e32 v15, v15, v44
v_add_f32_e32 v15, v15, v24
Mv 112,20,104
buffer_load_dwordx4 v3, s[20:23], s55 offen offset:3072 sc1 lds
)" R"(
Cv 44,44,24
Mv 112,24,112
v_add_f32_e32 v15, v15, v45
v_add_f32_e32 v15, v15, v25
Mv 116,24,116
Cv 45,45,25
v_add_f32_e32 v15, v15, v46
v_add_f32_e32 v15, v15, v26
Mv 116,28,124
Cv 46,46,26
v_add_f32_e32 v15, v15, v47
Mv 112,28,120
v_add_f32_e32 v15, v15, v27
s_waitcnt lgkmcnt(0)
s_barrier
Mz 120,32,64
Rb 0,10,0
Cv 47,47,27
Mz 124,32,68
Rb 4,10,1024
v_max_f32_e32 v11, v14, v15
Mv 124,36,76
Rb 8,10,2048
v_mov_b32 v24, v14
v_mov_b32 v25, v15
Mv 120,36,72
Rb 12,10,3072
v_permlane32_swap_b32_e32 v24, v25
Mv 120,40,80
Rb 16,10,4096
v_add_f32_e32 v24, v24, v25
v_mov_b32 v25, v24
Mv 124,40,84
Rb 20,10,5120
v_permlane16_swap_b32_e32 v24, v25
Mv 124,44,92
Rb 24,10,6144
v_add_f32_e32 v24, v24, v25
s_add_u32 s55, s55, 0x10000
Mv 120,44,88
Rb 28,10,7168
Mv 120,48,96
Rb 32,10,8192
Mv 124,48,100
Rb 36,10,9216
v_rcp_f32_e32 v24, v24
Mv 124,52,108
Rb 40,10,10240
v_max_f32_e32 v11, v11, v24
Mv 120,52,104
Rb 44,10,11264
s_cmp_eq_u32 s57, 2
s_cselect_b32 s55, s59, s55
Mv 120,56,112
v_cmp_nge_f32_e32 vcc, s62, v11
Mv 124,56,116
Mv 124,60,124
s_waitcnt lgkmcnt(8)
s_cbranch_vccnz .Lzfx6
.Lzfr6:
Mv 120,60,120
v_mov_b32 v12, v14
v_mov_b32 v13, v15
v_mfma_f32_16x16x32_bf16 v[128:131], a[0:3], v[32:35], 0
Rb 48,10,12288
)" R"(
v_mfma_f32_16x16x32_bf16 v[136:139], a[0:3], v[36:39], 0
v_mfma_f32_16x16x32_bf16 v[140:143], a[4:7], v[36:39], 0
Rb 52,10,13312
s_add_u32 m0, s36, 32768
v_mfma_f32_16x16x32_bf16 v[132:135], a[4:7], v[32:35], 0
buffer_load_dwordx4 v2, s[20:23], s55 offen offset:0 sc1 lds
v_mfma_f32_16x16x32_bf16 v[144:147], a[8:11], v[32:35], 0
Rb 56,10,14336
v_mfma_f32_16x16x32_bf16 v[152:155], a[8:11], v[36:39], 0
v_exp_f32_e32 v48, v96
v_mfma_f32_16x16x32_bf16 v[156:159], a[12:15], v[36:39], 0
Rb 60,10,15360
s_waitcnt lgkmcnt(8)
v_mfma_f32_16x16x32_bf16 v[148:151], a[12:15], v[32:35], 0
v_exp_f32_e32 v28, v97
v_mfma_f32_16x16x32_bf16 v[160:163], a[16:19], v[32:35], 0
Rb 0,10,16384
v_add_f32_e32 v14, v48, v28
v_mfma_f32_16x16x32_bf16 v[168:171], a[16:19], v[36:39], 0
v_exp_f32_e32 v49, v98
v_mfma_f32_16x16x32_bf16 v[172:175], a[20:23], v[36:39], 0
Rb 4,10,17408
Cv 48,48,28
v_mfma_f32_16x16x32_bf16 v[164:167], a[20:23], v[32:35], 0
buffer_load_dwordx4 v2, s[20:23], s55 offen offset:1024 sc1 lds
v_mfma_f32_16x16x32_bf16 v[176:179], a[24:27], v[32:35], 0
Rb 8,10,18432
v_exp_f32_e32 v29, v99
v_mfma_f32_16x16x32_bf16 v[184:187], a[24:27], v[36:39], 0
v_exp_f32_e32 v50, v104
v_mfma_f32_16x16x32_bf16 v[188:191], a[28:31], v[36:39], 0
Rb 12,10,19456
s_waitcnt lgkmcnt(8)
v_mfma_f32_16x16x32_bf16 v[180:183], a[28:31], v[32:35], 0
v_exp_f32_e32 v24, v105
v_mfma_f32_16x16x32_bf16 v[192:195], a[32:35], v[32:35], 0
Rb 16,10,20480
v_add_f32_e32 v14, v14, v49
v_mfma_f32_16x16x32_bf16 v[200:203], a[32:35], v[36:39], 0
v_exp_f32_e32 v51, v106
v_add_f32_e32 v14, v14, v29
v_mfma_f32_16x16x32_bf16 v[204:207], a[36:39], v[36:39], 0
Rb 20,10,21504
Cv 49,49,29
v_mfma_f32_16x16x32_bf16 v[196:199], a[36:39], v[32:35], 0
buffer_load_dwordx4 v2, s[20:23], s55 offen offset:2048 sc1 lds
v_mfma_f32_16x16x32_bf16 v[208:211], a[40:43], v[32:35], 0
Rb 24,10,22528
v_add_f32_e32 v14, v14, v50
v_mfma_f32_16x16x32_bf16 v[216:219], a[40:43], v[36:39], 0
v_exp_f32_e32 v25, v107
v_mfma_f32_16x16x32_bf16 v[220:223], a[44:47], v[36:39], 0
Rb 28,10,23552
s_waitcnt lgkmcnt(8)
v_mfma_f32_16x16x32_bf16 v[212:215], a[44:47], v[32:35], 0
v_exp_f32_e32 v52, v100
v_add_f32_e32 v14, v14, v24
v_mfma_f32_16x16x32_bf16 v[224:227], a[48:51], v[32:35], 0
Rb 32,10,24576
Cv 50,50,24
v_mfma_f32_16x16x32_bf16 v[232:235], a[48:51], v[36:39], 0
v_exp_f32_e32 v26, v101
v_mfma_f32_16x16x32_bf16 v[236:239], a[52:55], v[36:39], 0
Rb 36,10,25600
)" R"(
v_add_f32_e32 v14, v14, v51
v_mfma_f32_16x16x32_bf16 v[228:231], a[52:55], v[32:35], 0
buffer_load_dwordx4 v2, s[20:23], s55 offen offset:3072 sc1 lds
v_mfma_f32_16x16x32_bf16 v[240:243], a[56:59], v[32:35], 0
Rb 40,10,26624
v_exp_f32_e32 v53, v102
v_mfma_f32_16x16x32_bf16 v[248:251], a[56:59], v[36:39], 0
v_exp_f32_e32 v27, v103
v_mfma_f32_16x16x32_bf16 v[252:255], a[60:63], v[36:39], 0
Rb 44,10,27648
s_waitcnt lgkmcnt(8)
v_mfma_f32_16x16x32_bf16 v[244:247], a[60:63], v[32:35], 0
v_exp_f32_e32 v54, v108
Mp 128,0,40
Rb 48,10,28672
v_add_f32_e32 v14, v14, v25
Mp 136,0,44
v_exp_f32_e32 v28, v109
Cv 51,51,25
Mp 140,4,44
Rb 52,10,29696
s_add_u32 m0, s36, 36864
Mp 132,4,40
buffer_load_dwordx4 v3, s[20:23], s55 offen offset:0 sc1 lds
Mp 144,8,40
Rb 56,10,30720
v_add_f32_e32 v15, v52, v26
Mp 152,8,44
v_exp_f32_e32 v55, v110
Mp 156,12,44
Rb 60,10,31744
s_waitcnt lgkmcnt(8)
Mp 148,12,40
v_exp_f32_e32 v29, v111
Cv 52,52,26
Mp 160,16,40
v_add_f32_e32 v15, v15, v53
v_add_f32_e32 v15, v15, v27
Mp 168,16,44
Cv 53,53,27
v_add_f32_e32 v15, v15, v54
Mp 172,20,44
v_add_f32_e32 v15, v15, v28
Cv 54,54,28
Mp 164,20,40
buffer_load_dwordx4 v3, s[20:23], s55 offen offset:1024 sc1 lds
Mp 176,24,40
v_add_f32_e32 v15, v15, v55
v_add_f32_e32 v15, v15, v29
Cv 55,55,29
Mp 184,24,44
s_sub_u32 s57, s57, 1
v_accvgpr_read_b32 v9, a192
Mp 188,28,44
v_accvgpr_read_b32 v11, a193
Cv 9,9,11
Mp 180,28,40
s_waitcnt vmcnt(6) lgkmcnt(0)
s_barrier
buffer_store_dword v6, v7, s[20:23], s49 offen offset:1024 sc0 sc1
Mp 192,32,40
Rb 0,1,0
v_accvgpr_write_b32 a144, v9
Mp 200,32,44
Rb 4,1,1024
)" R"(
v_accvgpr_read_b32 v9, a194
v_accvgpr_read_b32 v11, a195
Mp 204,36,44
Rb 8,1,2048
Cv 9,9,11
Mp 196,36,40
Rb 12,1,3072
buffer_load_dwordx4 v3, s[20:23], s55 offen offset:2048 sc1 lds
Mp 208,40,40
Rb 16,1,8192
v_accvgpr_write_b32 a145, v9
Mp 216,40,44
Rb 20,1,9216
v_accvgpr_read_b32 v9, a196
Mp 220,44,44
Rb 24,1,10240
v_accvgpr_read_b32 v11, a197
Mp 212,44,40
Rb 28,1,11264
Cv 9,9,11
v_accvgpr_write_b32 a146, v9
Mp 224,48,40
Rb 32,1,4096
v_accvgpr_read_b32 v9, a198
Mp 232,48,44
Rb 36,1,5120
v_accvgpr_read_b32 v11, a199
Mp 236,52,44
Rb 40,1,6144
Cv 9,9,11
Mp 228,52,40
Rb 44,1,7168
buffer_load_dwordx4 v3, s[20:23], s55 offen offset:3072 sc1 lds
Mp 240,56,40
v_cmp_eq_u32_e32 vcc, s3, v4
s_andn2_b64 s[28:29], -1, vcc
s_cbranch_scc1 .Lzsp18
.Lzok18:
Mp 248,56,44
v_accvgpr_write_b32 a147, v9
v_accvgpr_read_b32 v9, a200
Mp 252,60,44
v_accvgpr_read_b32 v11, a201
s_waitcnt lgkmcnt(8)
Mp 244,60,40
Cv 9,9,11
v_accvgpr_write_b32 a148, v9
v_mfma_f32_16x16x32_bf16 v[64:67], a[0:3], a[64:67], v[16:19]
Rb 48,1,12288
v_add_u32 v9, 4, v0
v_mfma_f32_16x16x32_bf16 v[68:71], a[0:3], a[68:71], v[20:23]
v_xor_b32 v9, v9, v0
v_and_b32 v9, 32, v9
Mv 68,4,76
Rb 52,1,13312
s_add_u32 m0, s36, 98304
Mv 64,4,72
buffer_load_dwordx4 v2, s[20:23], s56 offen offset:0 sc1 lds
Mv 64,8,80
Rb 56,1,14336
v_lshlrev_b32 v9, 22, v9
Mv 68,8,84
v_and_or_b32 v9, v0, 32, v9
v_mul_lo_u32 v9, v9, 12
)" R"(
Mv 68,12,92
Rb 60,1,15360
s_waitcnt lgkmcnt(8)
Mv 64,12,88
buffer_load_dwordx4 v2, s[20:23], s56 offen offset:1024 sc1 lds
Mv 64,16,96
Rb 0,1,16384
v_add_u32 v9, v9, v1
Mv 68,16,100
v_exp_f32_e32 v56, v112
Mv 68,20,108
Rb 4,1,17408
v_exp_f32_e32 v24, v113
Mv 64,20,104
buffer_load_dwordx4 v2, s[20:23], s56 offen offset:2048 sc1 lds
Mv 64,24,112
Rb 8,1,18432
v_exp_f32_e32 v57, v114
Mv 68,24,116
v_exp_f32_e32 v25, v115
Mv 68,28,124
Rb 12,1,19456
v_add_f32_e32 v14, v14, v56
s_waitcnt lgkmcnt(8)
Mv 64,28,120
buffer_load_dwordx4 v2, s[20:23], s56 offen offset:3072 sc1 lds
v_add_f32_e32 v14, v14, v24
v_mfma_f32_16x16x32_bf16 v[72:75], a[32:35], a[64:67], v[16:19]
Rb 16,1,24576
Cv 56,56,24
v_mfma_f32_16x16x32_bf16 v[76:79], a[32:35], a[68:71], v[20:23]
v_exp_f32_e32 v58, v120
v_add_f32_e32 v14, v14, v57
Mv 76,36,76
Rb 20,1,25600
s_add_u32 m0, s36, 102400
Mv 72,36,72
buffer_load_dwordx4 v3, s[20:23], s56 offen offset:0 sc1 lds
v_add_f32_e32 v14, v14, v25
Mv 72,40,80
Rb 24,1,26624
Cv 57,57,25
Mv 76,40,84
v_exp_f32_e32 v26, v121
v_add_f32_e32 v14, v14, v58
Mv 76,44,92
Rb 28,1,27648
s_waitcnt lgkmcnt(8)
Mv 72,44,88
buffer_load_dwordx4 v3, s[20:23], s56 offen offset:1024 sc1 lds
v_add_f32_e32 v14, v14, v26
Mv 72,48,96
Rb 32,1,20480
v_exp_f32_e32 v59, v122
Mv 76,48,100
v_exp_f32_e32 v27, v123
Mv 76,52,108
Rb 36,1,21504
v_exp_f32_e32 v60, v116
Mv 72,52,104
buffer_load_dwordx4 v3, s[20:23], s56 offen offset:2048 sc1 lds
Mv 72,56,112
Rb 40,1,22528
v_exp_f32_e32 v28, v117
)" R"(
Mv 76,56,116
v_exp_f32_e32 v61, v118
Mv 76,60,124
Rb 44,1,23552
Cv 58,58,26
s_waitcnt lgkmcnt(8)
Mv 72,60,120
buffer_load_dwordx4 v3, s[20:23], s56 offen offset:3072 sc1 lds
v_add_f32_e32 v14, v14, v59
v_mfma_f32_16x16x32_bf16 v[80:83], a[0:3], a[64:67], v[16:19]
Rb 48,1,28672
s_xor_b32 m0, s82, 0
v_mfma_f32_16x16x32_bf16 v[84:87], a[0:3], a[68:71], v[20:23]
s_add_u32 s56, s56, 0x10000
s_add_u32 s92, s83, 448
v_add_f32_e32 v14, v14, v27
Mv 84,4,76
Rb 52,1,29696
Cv 59,59,27
Mv 80,4,72
buffer_load_dwordx4 v9, s[24:27], s92 offen offset:0 lds
v_add_f32_e32 v15, v15, v60
Mv 80,8,80
Rb 56,1,30720
s_add_u32 s92, s83, 1216
Mv 84,8,84
v_exp_f32_e32 v29, v119
v_add_f32_e32 v15, v15, v28
Mv 84,12,92
Rb 60,1,31744
s_waitcnt lgkmcnt(8)
Mv 80,12,88
buffer_load_dwordx4 v9, s[24:27], s92 offen offset:1024 lds
s_add_u32 s92, s83, 1984
Mv 80,16,96
v_exp_f32_e32 v62, v124
Cv 60,60,28
Mv 84,16,100
v_exp_f32_e32 v24, v125
Mv 84,20,108
v_exp_f32_e32 v63, v126
v_add_f32_e32 v15, v15, v61
Mv 80,20,104
buffer_load_dwordx4 v9, s[24:27], s92 offen offset:2048 lds
Mv 80,24,112
s_add_u32 s92, s83, 2752
v_exp_f32_e32 v25, v127
Mv 84,24,116
v_add_f32_e32 v15, v15, v29
Cv 61,61,29
Mv 84,28,124
buffer_load_dwordx4 v9, s[24:27], s92 offen offset:3072 lds
v_add_f32_e32 v15, v15, v62
Mv 80,28,120
s_xor_b32 m0, s82, 16384
s_waitcnt lgkmcnt(0)
s_barrier
v_mfma_f32_16x16x32_bf16 v[88:91], a[32:35], a[64:67], v[16:19]
Rb 0,8,0
s_add_u32 s92, s83, 0
v_mfma_f32_16x16x32_bf16 v[92:95], a[32:35], a[68:71], v[20:23]
Rb 4,8,1024
v_add_f32_e32 v15, v15, v24
Cv 62,62,24
Mv 92,36,76
)" R"(
Rb 8,8,2048
v_add_f32_e32 v15, v15, v63
Mv 88,36,72
Rb 12,8,3072
v_add_f32_e32 v15, v15, v25
Cv 63,63,25
Mv 88,40,80
Rb 16,8,4096
buffer_load_dwordx4 v9, s[24:27], s92 offen offset:0 lds
Mv 92,40,84
Rb 20,8,5120
s_add_u32 s92, s83, 768
v_max_f32_e32 v11, v14, v15
Mv 92,44,92
Rb 24,8,6144
v_cmp_nge_f32_e32 vcc, s62, v11
Mv 88,44,88
Rb 28,8,7168
s_add_u32 s55, s55, 0x10000
Mv 88,48,96
Rb 32,8,8192
s_cmp_eq_u32 s57, 2
s_cselect_b32 s55, s59, s55
Mv 92,48,100
Rb 36,8,9216
buffer_load_dwordx4 v9, s[24:27], s92 offen offset:1024 lds
Mv 92,52,108
Rb 40,8,10240
s_add_u32 s92, s83, 1536
Mv 88,52,104
Rb 44,8,11264
Mv 88,56,112
buffer_load_dwordx4 v9, s[24:27], s92 offen offset:2048 lds
s_add_u32 s92, s83, 2304
Mv 92,56,116
buffer_load_dwordx4 v9, s[24:27], s92 offen offset:3072 lds
Mv 92,60,124
s_waitcnt lgkmcnt(8)
s_cbranch_vccnz .Lzfx7
.Lzfr7:
Mv 88,60,120
v_add_f32_e32 v12, v12, v14
v_add_f32_e32 v13, v13, v15
Mp 128,0,48
Rb 48,8,12288
Mp 136,0,52
s_cmp_le_i32 s57, s58
s_cbranch_scc1 .Lzmk6
.Lzmr6:
Mp 140,4,52
Rb 52,8,13312
s_add_u32 m0, s36, 0
Mp 132,4,48
buffer_load_dwordx4 v2, s[20:23], s55 offen offset:0 sc1 lds
Mp 144,8,48
Rb 56,8,14336
v_exp_f32_e32 v32, v64
Mp 152,8,52
buffer_store_dwordx4 a[224:227], v2, s[20:23], s47 offen offset:0 sc0 sc1
.LzH11:
s_cbranch_execz .LzH19
Mp 156,12,52
Rb 60,8,15360
s_waitcnt lgkmcnt(8)
)" R"(
Mp 148,12,48
v_exp_f32_e32 v26, v65
Mp 160,16,48
Rb 0,8,16384
v_add_f32_e32 v14, v32, v26
Mp 168,16,52
v_exp_f32_e32 v33, v66
Cv 32,32,26
Mp 172,20,52
Rb 4,8,17408
v_add_f32_e32 v14, v14, v33
Mp 164,20,48
buffer_load_dwordx4 v2, s[20:23], s55 offen offset:1024 sc1 lds
Mp 176,24,48
Rb 8,8,18432
v_exp_f32_e32 v27, v67
Mp 184,24,52
v_exp_f32_e32 v34, v72
Mp 188,28,52
Rb 12,8,19456
s_waitcnt lgkmcnt(8)
Mp 180,28,48
v_exp_f32_e32 v28, v73
v_add_f32_e32 v14, v14, v27
Mp 192,32,48
Rb 16,8,20480
Cv 33,33,27
Mp 200,32,52
v_exp_f32_e32 v35, v74
v_add_f32_e32 v14, v14, v34
Mp 204,36,52
Rb 20,8,21504
v_add_f32_e32 v14, v14, v28
Mp 196,36,48
buffer_load_dwordx4 v2, s[20:23], s55 offen offset:2048 sc1 lds
Mp 208,40,48
Rb 24,8,22528
v_exp_f32_e32 v29, v75
Mp 216,40,52
buffer_store_dwordx4 a[228:231], v2, s[20:23], s47 offen offset:1024 sc0 sc1
Mp 220,44,52
Rb 28,8,23552
s_waitcnt lgkmcnt(8)
Mp 212,44,48
v_exp_f32_e32 v36, v68
Cv 34,34,28
Mp 224,48,48
Rb 32,8,24576
v_add_f32_e32 v14, v14, v35
Mp 232,48,52
v_exp_f32_e32 v24, v69
v_add_f32_e32 v14, v14, v29
Mp 236,52,52
Rb 36,8,25600
Cv 35,35,29
Mp 228,52,48
buffer_load_dwordx4 v2, s[20:23], s55 offen offset:3072 sc1 lds
Mp 240,56,48
Rb 40,8,26624
v_exp_f32_e32 v37, v70
Mp 248,56,52
v_exp_f32_e32 v25, v71
Mp 252,60,52
Rb 44,8,27648
)" R"(
s_waitcnt lgkmcnt(8)
Mp 244,60,48
v_exp_f32_e32 v38, v76
v_add_f32_e32 v15, v36, v24
Mp 128,0,56
Rb 48,8,28672
Cv 36,36,24
Mp 136,0,60
v_exp_f32_e32 v26, v77
v_add_f32_e32 v15, v15, v37
Mp 140,4,60
Rb 52,8,29696
s_add_u32 m0, s36, 4096
Mp 132,4,56
buffer_load_dwordx4 v3, s[20:23], s55 offen offset:0 sc1 lds
Mp 144,8,56
Rb 56,8,30720
v_exp_f32_e32 v39, v78
Mp 152,8,60
buffer_store_dwordx4 a[232:235], v2, s[20:23], s47 offen offset:2048 sc0 sc1
Mp 156,12,60
Rb 60,8,31744
s_waitcnt lgkmcnt(8)
Mp 148,12,56
v_exp_f32_e32 v27, v79
v_add_f32_e32 v15, v15, v25
Mp 160,16,56
Cv 37,37,25
v_add_f32_e32 v15, v15, v38
Mp 168,16,60
v_add_f32_e32 v15, v15, v26
Cv 38,38,26
v_add_f32_e32 v15, v15, v39
Mp 172,20,60
v_add_f32_e32 v15, v15, v27
Cv 39,39,27
Mp 164,20,56
buffer_load_dwordx4 v3, s[20:23], s55 offen offset:1024 sc1 lds
Mp 176,24,56
s_add_u32 s50, s50, 1024
s_min_u32 s50, s50, s60
s_sub_u32 s57, s57, 1
Mp 184,24,60
v_accvgpr_read_b32 v9, a202
v_accvgpr_read_b32 v11, a203
Mp 188,28,60
Cv 9,9,11
v_accvgpr_write_b32 a149, v9
Mp 180,28,56
v_accvgpr_read_b32 v9, a204
s_waitcnt vmcnt(25) lgkmcnt(0)
s_barrier
Mp 192,32,56
Rb 0,1,32768
v_accvgpr_read_b32 v11, a205
Mp 200,32,60
Rb 4,1,33792
Cv 9,9,11
v_accvgpr_write_b32 a150, v9
Mp 204,36,60
Rb 8,1,34816
v_accvgpr_read_b32 v9, a206
Mp 196,36,56
Rb 12,1,35840
buffer_load_dwordx4 v3, s[20:23], s55 offen offset:2048 sc1 lds
)" R"(
Mp 208,40,56
Rb 16,1,40960
v_accvgpr_read_b32 v11, a207
Cv 9,9,11
Mp 216,40,60
Rb 20,1,41984
buffer_store_dwordx4 a[236:239], v2, s[20:23], s47 offen offset:3072 sc0 sc1
Mp 220,44,60
Rb 24,1,43008
v_accvgpr_write_b32 a151, v9
Mp 212,44,56
Rb 28,1,44032
v_accvgpr_read_b32 v9, a208
v_accvgpr_read_b32 v11, a209
Mp 224,48,56
Rb 32,1,36864
Cv 9,9,11
Mp 232,48,60
Rb 36,1,37888
v_accvgpr_write_b32 a152, v9
v_accvgpr_read_b32 v9, a210
Mp 236,52,60
Rb 40,1,38912
v_accvgpr_read_b32 v11, a211
Mp 228,52,56
Rb 44,1,39936
buffer_load_dwordx4 v3, s[20:23], s55 offen offset:3072 sc1 lds
Mp 240,56,56
Cv 9,9,11
v_accvgpr_write_b32 a153, v9
v_accvgpr_read_b32 v9, a212
Mp 248,56,60
v_accvgpr_read_b32 v11, a213
Cv 9,9,11
Mp 252,60,60
v_accvgpr_write_b32 a154, v9
s_waitcnt lgkmcnt(8)
Mp 244,60,56
v_mfma_f32_16x16x32_bf16 v[96:99], a[0:3], a[64:67], v[16:19]
Rb 48,1,45056
buffer_load_dword v4, v5, s[20:23], s50 offen offset:0 sc0 sc1
v_mfma_f32_16x16x32_bf16 v[100:103], a[0:3], a[68:71], v[20:23]
v_exp_f32_e32 v40, v80
Mv 100,4,76
Rb 52,1,46080
s_add_u32 m0, s36, 65536
Mv 96,4,72
buffer_load_dwordx4 v2, s[20:23], s56 offen offset:0 sc1 lds
Mv 96,8,80
Rb 56,1,47104
v_add_f32_e32 v14, v14, v40
Mv 100,8,84
buffer_store_dwordx4 a[240:243], v3, s[20:23], s47 offen offset:0 sc0 sc1
Mv 100,12,92
Rb 60,1,48128
s_waitcnt lgkmcnt(8)
Mv 96,12,88
v_exp_f32_e32 v28, v81
Mv 96,16,96
Rb 0,1,49152
v_add_f32_e32 v14, v14, v28
Mv 100,16,100
v_exp_f32_e32 v41, v82
Mv 100,20,108
)" R"(
Rb 4,1,50176
Cv 40,40,28
Mv 96,20,104
buffer_load_dwordx4 v2, s[20:23], s56 offen offset:1024 sc1 lds
Mv 96,24,112
Rb 8,1,51200
v_add_f32_e32 v14, v14, v41
Mv 100,24,116
v_exp_f32_e32 v29, v83
Mv 100,28,124
Rb 12,1,52224
s_waitcnt lgkmcnt(8)
Mv 96,28,120
v_exp_f32_e32 v42, v88
v_add_f32_e32 v14, v14, v29
v_mfma_f32_16x16x32_bf16 v[104:107], a[32:35], a[64:67], v[16:19]
Rb 16,1,57344
Cv 41,41,29
v_mfma_f32_16x16x32_bf16 v[108:111], a[32:35], a[68:71], v[20:23]
v_exp_f32_e32 v24, v89
Mv 108,36,76
Rb 20,1,58368
v_add_f32_e32 v14, v14, v42
Mv 104,36,72
buffer_load_dwordx4 v2, s[20:23], s56 offen offset:2048 sc1 lds
Mv 104,40,80
Rb 24,1,59392
v_add_f32_e32 v14, v14, v24
Mv 108,40,84
buffer_store_dwordx4 a[244:247], v3, s[20:23], s47 offen offset:1024 sc0 sc1
Mv 108,44,92
Rb 28,1,60416
s_waitcnt lgkmcnt(8)
Mv 104,44,88
v_exp_f32_e32 v43, v90
Mv 104,48,96
Rb 32,1,53248
Cv 42,42,24
Mv 108,48,100
v_exp_f32_e32 v25, v91
Mv 108,52,108
Rb 36,1,54272
v_add_f32_e32 v14, v14, v43
Mv 104,52,104
buffer_load_dwordx4 v2, s[20:23], s56 offen offset:3072 sc1 lds
Mv 104,56,112
Rb 40,1,55296
v_add_f32_e32 v14, v14, v25
Mv 108,56,116
v_exp_f32_e32 v44, v84
Mv 108,60,124
Rb 44,1,56320
s_waitcnt lgkmcnt(8)
Mv 104,60,120
v_exp_f32_e32 v26, v85
Cv 43,43,25
v_mfma_f32_16x16x32_bf16 v[112:115], a[0:3], a[64:67], v[16:19]
Rb 48,1,61440
v_add_f32_e32 v15, v15, v44
v_mfma_f32_16x16x32_bf16 v[116:119], a[0:3], a[68:71], v[20:23]
v_exp_f32_e32 v45, v86
Mv 116,4,76
Rb 52,1,62464
s_add_u32 m0, s36, 69632
)" R"(
Mv 112,4,72
buffer_load_dwordx4 v3, s[20:23], s56 offen offset:0 sc1 lds
Mv 112,8,80
Rb 56,1,63488
v_add_f32_e32 v15, v15, v26
Mv 116,8,84
buffer_store_dwordx4 a[248:251], v3, s[20:23], s47 offen offset:2048 sc0 sc1
Mv 116,12,92
Rb 60,1,64512
s_waitcnt lgkmcnt(8)
Mv 112,12,88
v_exp_f32_e32 v27, v87
Mv 112,16,96
v_exp_f32_e32 v46, v92
Mv 116,16,100
v_exp_f32_e32 v28, v93
Mv 116,20,108
v_exp_f32_e32 v47, v94
Mv 112,20,104
buffer_load_dwordx4 v3, s[20:23], s56 offen offset:1024 sc1 lds
Mv 112,24,112
v_exp_f32_e32 v29, v95
Mv 116,24,116
Cv 44,44,26
v_add_f32_e32 v15, v15, v45
Mv 116,28,124
v_add_f32_e32 v15, v15, v27
Cv 45,45,27
Mv 112,28,120
v_add_f32_e32 v15, v15, v46
s_waitcnt vmcnt(30) lgkmcnt(0)
s_barrier
v_mfma_f32_16x16x32_bf16 v[120:123], a[32:35], a[64:67], v[16:19]
Rb 0,8,32768
v_add_f32_e32 v15, v15, v28
v_mfma_f32_16x16x32_bf16 v[124:127], a[32:35], a[68:71], v[20:23]
Rb 4,8,33792
Cv 46,46,28
Mv 124,36,76
Rb 8,8,34816
v_add_f32_e32 v15, v15, v47
Mv 120,36,72
Rb 12,8,35840
buffer_load_dwordx4 v3, s[20:23], s56 offen offset:2048 sc1 lds
Mv 120,40,80
Rb 16,8,36864
v_add_f32_e32 v15, v15, v29
Mv 124,40,84
Rb 20,8,37888
buffer_store_dwordx4 a[252:255], v3, s[20:23], s47 offen offset:3072 sc0 sc1
Mv 124,44,92
Rb 24,8,38912
Cv 47,47,29
Mv 120,44,88
Rb 28,8,39936
v_max_f32_e32 v11, v14, v15
Mv 120,48,96
Rb 32,8,40960
v_cmp_nge_f32_e32 vcc, s62, v11
Mv 124,48,100
Rb 36,8,41984
s_add_u32 s55, s55, 0x10000
Mv 124,52,108
Rb 40,8,43008
Mv 120,52,104
)" R"(
Rb 44,8,44032
buffer_load_dwordx4 v3, s[20:23], s56 offen offset:3072 sc1 lds
Mv 120,56,112
s_cmp_eq_u32 s57, 2
s_cselect_b32 s55, s59, s55
Mv 124,56,116
s_add_u32 s56, s56, 0x10000
Mv 124,60,124
s_waitcnt lgkmcnt(8)
s_cbranch_vccnz .Lzfx8
.Lzfr8:
Mv 120,60,120
v_add_f32_e32 v12, v12, v14
v_add_f32_e32 v13, v13, v15
Mp 128,0,32
Rb 48,8,45056
Mp 136,0,36
s_cmp_le_i32 s57, s58
s_cbranch_scc1 .Lzmk7
.Lzmr7:
Mp 140,4,36
Rb 52,8,46080
s_add_u32 m0, s36, 32768
Mp 132,4,32
buffer_load_dwordx4 v2, s[20:23], s55 offen offset:0 sc1 lds
Mp 144,8,32
Rb 56,8,47104
Mp 152,8,36
v_exp_f32_e32 v48, v96
Mp 156,12,36
Rb 60,8,48128
s_waitcnt lgkmcnt(8)
Mp 148,12,32
v_exp_f32_e32 v24, v97
Mp 160,16,32
Rb 0,8,49152
v_add_f32_e32 v14, v48, v24
Mp 168,16,36
v_exp_f32_e32 v49, v98
Mp 172,20,36
Rb 4,8,50176
Cv 48,48,24
Mp 164,20,32
buffer_load_dwordx4 v2, s[20:23], s55 offen offset:1024 sc1 lds
Mp 176,24,32
Rb 8,8,51200
v_exp_f32_e32 v25, v99
Mp 184,24,36
v_exp_f32_e32 v50, v104
Mp 188,28,36
Rb 12,8,52224
s_waitcnt lgkmcnt(8)
Mp 180,28,32
v_exp_f32_e32 v26, v105
Mp 192,32,32
Rb 16,8,53248
v_add_f32_e32 v14, v14, v49
Mp 200,32,36
v_exp_f32_e32 v51, v106
v_add_f32_e32 v14, v14, v25
Mp 204,36,36
Rb 20,8,54272
Cv 49,49,25
Mp 196,36,32
)" R"(
buffer_load_dwordx4 v2, s[20:23], s55 offen offset:2048 sc1 lds
Mp 208,40,32
Rb 24,8,55296
v_add_f32_e32 v14, v14, v50
Mp 216,40,36
v_exp_f32_e32 v27, v107
Mp 220,44,36
Rb 28,8,56320
s_waitcnt lgkmcnt(8)
Mp 212,44,32
v_exp_f32_e32 v52, v100
v_add_f32_e32 v14, v14, v26
Mp 224,48,32
Rb 32,8,57344
Cv 50,50,26
Mp 232,48,36
v_exp_f32_e32 v28, v101
Mp 236,52,36
Rb 36,8,58368
v_add_f32_e32 v14, v14, v51
Mp 228,52,32
buffer_load_dwordx4 v2, s[20:23], s55 offen offset:3072 sc1 lds
Mp 240,56,32
Rb 40,8,59392
v_exp_f32_e32 v53, v102
Mp 248,56,36
v_exp_f32_e32 v29, v103
Mp 252,60,36
Rb 44,8,60416
s_waitcnt lgkmcnt(8)
Mp 244,60,32
v_exp_f32_e32 v54, v108
Mp 128,0,40
Rb 48,8,61440
v_add_f32_e32 v14, v14, v27
Mp 136,0,44
v_exp_f32_e32 v24, v109
Cv 51,51,27
Mp 140,4,44
Rb 52,8,62464
s_add_u32 m0, s36, 36864
Mp 132,4,40
buffer_load_dwordx4 v3, s[20:23], s55 offen offset:0 sc1 lds
Mp 144,8,40
Rb 56,8,63488
v_add_f32_e32 v15, v52, v28
Mp 152,8,44
v_exp_f32_e32 v55, v110
Mp 156,12,44
Rb 60,8,64512
s_waitcnt lgkmcnt(8)
Mp 148,12,40
v_exp_f32_e32 v25, v111
Cv 52,52,28
Mp 160,16,40
v_add_f32_e32 v15, v15, v53
v_add_f32_e32 v15, v15, v29
Mp 168,16,44
Cv 53,53,29
v_add_f32_e32 v15, v15, v54
Mp 172,20,44
v_add_f32_e32 v15, v15, v24
Cv 54,54,24
Mp 164,20,40
)" R"(
buffer_load_dwordx4 v3, s[20:23], s55 offen offset:1024 sc1 lds
Mp 176,24,40
v_add_f32_e32 v15, v15, v55
v_add_f32_e32 v15, v15, v25
Cv 55,55,25
Mp 184,24,44
s_sub_u32 s57, s57, 1
v_accvgpr_read_b32 v9, a214
Mp 188,28,44
v_accvgpr_read_b32 v11, a215
Cv 9,9,11
Mp 180,28,40
s_waitcnt vmcnt(7) lgkmcnt(0)
s_barrier
buffer_store_dword v6, v7, s[20:23], s49 offen offset:2048 sc0 sc1
Mp 192,32,40
Rb 0,1,0
v_accvgpr_write_b32 a155, v9
Mp 200,32,44
Rb 4,1,1024
v_accvgpr_read_b32 v9, a216
v_accvgpr_read_b32 v11, a217
Mp 204,36,44
Rb 8,1,2048
Cv 9,9,11
Mp 196,36,40
Rb 12,1,3072
buffer_load_dwordx4 v3, s[20:23], s55 offen offset:2048 sc1 lds
Mp 208,40,40
Rb 16,1,8192
v_accvgpr_write_b32 a156, v9
Mp 216,40,44
Rb 20,1,9216
v_accvgpr_read_b32 v9, a218
Mp 220,44,44
Rb 24,1,10240
v_accvgpr_read_b32 v11, a219
Mp 212,44,40
Rb 28,1,11264
Cv 9,9,11
v_accvgpr_write_b32 a157, v9
Mp 224,48,40
Rb 32,1,4096
v_accvgpr_read_b32 v9, a220
Mp 232,48,44
Rb 36,1,5120
v_accvgpr_read_b32 v11, a221
Mp 236,52,44
Rb 40,1,6144
Cv 9,9,11
Mp 228,52,40
Rb 44,1,7168
buffer_load_dwordx4 v3, s[20:23], s55 offen offset:3072 sc1 lds
Mp 240,56,40
v_cmp_eq_u32_e32 vcc, s3, v4
s_andn2_b64 s[28:29], -1, vcc
s_cbranch_scc1 .Lzsp19
.Lzok19:
Mp 248,56,44
v_accvgpr_write_b32 a158, v9
v_accvgpr_read_b32 v9, a222
Mp 252,60,44
v_accvgpr_read_b32 v11, a223
s_waitcnt lgkmcnt(8)
Mp 244,60,40
)" R"(
Cv 9,9,11
v_accvgpr_write_b32 a159, v9
v_mfma_f32_16x16x32_bf16 v[64:67], a[0:3], a[64:67], v[16:19]
Rb 48,1,12288
v_lshrrev_b32 v9, 2, v0
v_mfma_f32_16x16x32_bf16 v[68:71], a[0:3], a[68:71], v[20:23]
v_mul_u32_u24 v9, 448, v9
s_lshr_b32 s92, s33, 1
Mv 68,4,76
Rb 52,1,13312
s_add_u32 m0, s36, 98304
Mv 64,4,72
buffer_load_dwordx4 v2, s[20:23], s56 offen offset:0 sc1 lds
Mv 64,8,80
Rb 56,1,14336
s_mul_i32 s92, s92, 48
Mv 68,8,84
buffer_store_dwordx4 a[128:131], v2, s[20:23], s48 offen offset:0 sc0 sc1
Mv 68,12,92
Rb 60,1,15360
s_waitcnt lgkmcnt(8)
Mv 64,12,88
s_lshl3_add_u32 s92, s33, s92
v_add3_u32 v9, v9, s92, v10
Mv 64,16,96
Rb 0,1,16384
Mv 68,16,100
v_exp_f32_e32 v56, v112
Mv 68,20,108
Rb 4,1,17408
Rt 160,9,0
Mv 64,20,104
buffer_load_dwordx4 v2, s[20:23], s56 offen offset:1024 sc1 lds
Mv 64,24,112
Rb 8,1,18432
v_exp_f32_e32 v26, v113
Mv 68,24,116
v_exp_f32_e32 v57, v114
Mv 68,28,124
Rb 12,1,19456
s_waitcnt lgkmcnt(9)
Mv 64,28,120
v_exp_f32_e32 v27, v115
v_add_f32_e32 v14, v14, v56
v_mfma_f32_16x16x32_bf16 v[72:75], a[32:35], a[64:67], v[16:19]
Rb 16,1,24576
v_add_f32_e32 v14, v14, v26
v_mfma_f32_16x16x32_bf16 v[76:79], a[32:35], a[68:71], v[20:23]
v_exp_f32_e32 v58, v120
Mv 76,36,76
Rb 20,1,25600
Cv 56,56,26
Mv 72,36,72
buffer_load_dwordx4 v2, s[20:23], s56 offen offset:2048 sc1 lds
v_add_f32_e32 v14, v14, v57
Mv 72,40,80
Rb 24,1,26624
Rt 162,9,8192
Mv 76,40,84
v_exp_f32_e32 v28, v121
Mv 76,44,92
Rb 28,1,27648
v_add_f32_e32 v14, v14, v27
s_waitcnt lgkmcnt(10)
)" R"(
Mv 72,44,88
v_exp_f32_e32 v59, v122
Mv 72,48,96
Rb 32,1,20480
Cv 57,57,27
.LzH12:
s_cbranch_execz .LzH20
Mv 76,48,100
v_exp_f32_e32 v29, v123
v_add_f32_e32 v14, v14, v58
Mv 76,52,108
Rb 36,1,21504
v_add_f32_e32 v14, v14, v28
Mv 72,52,104
buffer_load_dwordx4 v2, s[20:23], s56 offen offset:3072 sc1 lds
Mv 72,56,112
Rb 40,1,22528
v_exp_f32_e32 v60, v116
Mv 76,56,116
buffer_store_dwordx4 a[132:135], v2, s[20:23], s48 offen offset:1024 sc0 sc1
Mv 76,60,124
Rb 44,1,23552
Rt 164,9,128
s_waitcnt lgkmcnt(10)
Mv 72,60,120
v_exp_f32_e32 v24, v117
Cv 58,58,28
v_mfma_f32_16x16x32_bf16 v[80:83], a[0:3], a[64:67], v[16:19]
Rb 48,1,28672
v_add_f32_e32 v14, v14, v59
v_mfma_f32_16x16x32_bf16 v[84:87], a[0:3], a[68:71], v[20:23]
v_exp_f32_e32 v61, v118
Mv 84,4,76
Rb 52,1,29696
s_add_u32 m0, s36, 102400
Mv 80,4,72
buffer_load_dwordx4 v3, s[20:23], s56 offen offset:0 sc1 lds
v_add_f32_e32 v14, v14, v29
Mv 80,8,80
Rb 56,1,30720
Cv 59,59,29
Mv 84,8,84
v_exp_f32_e32 v25, v119
Mv 84,12,92
Rb 60,1,31744
v_add_f32_e32 v15, v15, v60
s_waitcnt lgkmcnt(9)
Mv 80,12,88
v_exp_f32_e32 v62, v124
Mv 80,16,96
Rt 166,9,8320
v_add_f32_e32 v15, v15, v24
Mv 84,16,100
buffer_store_dwordx4 a[160:163], v1, s[20:23], s4 offen sc0 sc1
Cv 60,60,24
Mv 84,20,108
v_exp_f32_e32 v26, v125
Mv 80,20,104
buffer_load_dwordx4 v3, s[20:23], s56 offen offset:1024 sc1 lds
Mv 80,24,112
v_exp_f32_e32 v63, v126
v_add_f32_e32 v15, v15, v61
Mv 84,24,116
v_exp_f32_e32 v27, v127
)" R"(
Mv 84,28,124
v_add_f32_e32 v15, v15, v25
Cv 61,61,25
Mv 80,28,120
v_add_f32_e32 v15, v15, v62
s_waitcnt vmcnt(18) lgkmcnt(0)
s_barrier
v_mfma_f32_16x16x32_bf16 v[88:91], a[32:35], a[64:67], v[16:19]
Rb 0,8,0
v_add_f32_e32 v15, v15, v26
v_mfma_f32_16x16x32_bf16 v[92:95], a[32:35], a[68:71], v[20:23]
Rb 4,8,1024
Cv 62,62,26
Mv 92,36,76
Rb 8,8,2048
Rt 168,9,256
Mv 88,36,72
Rb 12,8,3072
buffer_load_dwordx4 v3, s[20:23], s56 offen offset:2048 sc1 lds
Mv 88,40,80
Rb 16,8,4096
v_add_f32_e32 v15, v15, v63
Mv 92,40,84
Rb 20,8,5120
buffer_store_dwordx4 a[136:139], v2, s[20:23], s48 offen offset:2048 sc0 sc1
Mv 92,44,92
Rb 24,8,6144
v_add_f32_e32 v15, v15, v27
Cv 63,63,27
Mv 88,44,88
Rb 28,8,7168
v_max_f32_e32 v11, v14, v15
Mv 88,48,96
Rb 32,8,8192
v_cmp_nge_f32_e32 vcc, s62, v11
Mv 92,48,100
Rb 36,8,9216
s_add_u32 s55, s55, 0x10000
Mv 92,52,108
Rb 40,8,10240
Mv 88,52,104
Rb 44,8,11264
buffer_load_dwordx4 v3, s[20:23], s56 offen offset:3072 sc1 lds
Mv 88,56,112
Rt 170,9,8448
s_cmp_eq_u32 s57, 2
s_cselect_b32 s55, s59, s55
Mv 92,56,116
s_add_u32 s56, s56, 0x10000
buffer_store_dwordx4 a[164:167], v1, s[20:23], s5 offen sc0 sc1
Mv 92,60,124
s_waitcnt lgkmcnt(9)
s_cbranch_vccnz .Lzfx9
.Lzfr9:
Mv 88,60,120
v_add_f32_e32 v12, v12, v14
v_add_f32_e32 v13, v13, v15
Mp 128,0,48
Rb 48,8,12288
Mp 136,0,52
s_cmp_le_i32 s57, s58
s_cbranch_scc1 .Lzmk8
.Lzmr8:
Mp 140,4,52
Rb 52,8,13312
)" R"(
s_add_u32 m0, s36, 0
Mp 132,4,48
buffer_load_dwordx4 v2, s[20:23], s55 offen offset:0 sc1 lds
Mp 144,8,48
Rb 56,8,14336
Mp 152,8,52
v_exp_f32_e32 v32, v64
Mp 156,12,52
Rb 60,8,15360
Rt 172,9,384
s_waitcnt lgkmcnt(10)
Mp 148,12,48
v_exp_f32_e32 v28, v65
Mp 160,16,48
Rb 0,8,16384
v_add_f32_e32 v14, v32, v28
Mp 168,16,52
v_exp_f32_e32 v33, v66
Cv 32,32,28
Mp 172,20,52
Rb 4,8,17408
v_add_f32_e32 v14, v14, v33
Mp 164,20,48
buffer_load_dwordx4 v2, s[20:23], s55 offen offset:1024 sc1 lds
Mp 176,24,48
Rb 8,8,18432
Mp 184,24,52
buffer_store_dwordx4 a[140:143], v2, s[20:23], s48 offen offset:3072 sc0 sc1
Mp 188,28,52
Rb 12,8,19456
s_waitcnt lgkmcnt(10)
Mp 180,28,48
v_exp_f32_e32 v29, v67
Mp 192,32,48
Rb 16,8,20480
Rt 174,9,8576
Mp 200,32,52
v_exp_f32_e32 v34, v72
v_add_f32_e32 v14, v14, v29
Mp 204,36,52
Rb 20,8,21504
Cv 33,33,29
Mp 196,36,48
buffer_load_dwordx4 v2, s[20:23], s55 offen offset:2048 sc1 lds
Mp 208,40,48
Rb 24,8,22528
v_add_f32_e32 v14, v14, v34
Mp 216,40,52
v_exp_f32_e32 v24, v73
Mp 220,44,52
Rb 28,8,23552
s_waitcnt lgkmcnt(10)
Mp 212,44,48
buffer_store_dwordx4 a[168:171], v1, s[20:23], s6 offen sc0 sc1
Mp 224,48,48
Rb 32,8,24576
v_add_f32_e32 v14, v14, v24
Mp 232,48,52
v_exp_f32_e32 v35, v74
Cv 34,34,24
Mp 236,52,52
Rb 36,8,25600
Rt 176,9,16384
Mp 228,52,48
)" R"(
buffer_load_dwordx4 v2, s[20:23], s55 offen offset:3072 sc1 lds
Mp 240,56,48
Rb 40,8,26624
v_add_f32_e32 v14, v14, v35
Mp 248,56,52
v_exp_f32_e32 v25, v75
Mp 252,60,52
Rb 44,8,27648
s_waitcnt lgkmcnt(10)
Mp 244,60,48
v_exp_f32_e32 v36, v68
Mp 128,0,56
Rb 48,8,28672
v_add_f32_e32 v14, v14, v25
Mp 136,0,60
v_exp_f32_e32 v26, v69
Cv 35,35,25
Mp 140,4,60
Rb 52,8,29696
s_add_u32 m0, s36, 4096
Mp 132,4,56
buffer_load_dwordx4 v3, s[20:23], s55 offen offset:0 sc1 lds
Mp 144,8,56
Rb 56,8,30720
Rt 178,9,24576
Mp 152,8,60
buffer_store_dwordx4 a[144:147], v3, s[20:23], s48 offen offset:0 sc0 sc1
Mp 156,12,60
Rb 60,8,31744
s_waitcnt lgkmcnt(10)
Mp 148,12,56
v_exp_f32_e32 v37, v70
Mp 160,16,56
v_exp_f32_e32 v27, v71
Mp 168,16,60
v_exp_f32_e32 v38, v76
v_add_f32_e32 v15, v36, v26
Mp 172,20,60
v_exp_f32_e32 v28, v77
Mp 164,20,56
buffer_load_dwordx4 v3, s[20:23], s55 offen offset:1024 sc1 lds
Mp 176,24,56
s_add_u32 s50, s50, 1024
s_min_u32 s50, s50, s60
Mp 184,24,60
buffer_store_dwordx4 a[172:175], v1, s[20:23], s7 offen sc0 sc1
Mp 188,28,60
Rt 180,9,16512
Cv 36,36,26
Mp 180,28,56
s_waitcnt vmcnt(23) lgkmcnt(1)
s_barrier
Mp 192,32,56
Rb 0,1,32768
v_add_f32_e32 v15, v15, v37
Mp 200,32,60
Rb 4,1,33792
v_exp_f32_e32 v39, v78
Mp 204,36,60
Rb 8,1,34816
v_add_f32_e32 v15, v15, v27
Mp 196,36,56
Rb 12,1,35840
buffer_load_dwordx4 v3, s[20:23], s55 offen offset:2048 sc1 lds
Mp 208,40,56
)" R"(
Rb 16,1,40960
Cv 37,37,27
Mp 216,40,60
Rb 20,1,41984
v_add_f32_e32 v15, v15, v38
Mp 220,44,60
Rb 24,1,43008
v_add_f32_e32 v15, v15, v28
Mp 212,44,56
Rb 28,1,44032
Cv 38,38,28
Mp 224,48,56
Rb 32,1,36864
Rt 182,9,24704
Mp 232,48,60
Rb 36,1,37888
v_exp_f32_e32 v29, v79
Mp 236,52,60
Rb 40,1,38912
v_add_f32_e32 v15, v15, v39
Mp 228,52,56
Rb 44,1,39936
buffer_load_dwordx4 v3, s[20:23], s55 offen offset:3072 sc1 lds
Mp 240,56,56
v_add_f32_e32 v15, v15, v29
Cv 39,39,29
Mp 248,56,60
buffer_store_dwordx4 a[148:151], v3, s[20:23], s48 offen offset:1024 sc0 sc1
Mp 252,60,60
s_sub_u32 s57, s57, 1
s_waitcnt lgkmcnt(9)
Mp 244,60,56
v_mfma_f32_16x16x32_bf16 v[96:99], a[0:3], a[64:67], v[16:19]
Rb 48,1,45056
buffer_load_dword v4, v5, s[20:23], s50 offen offset:0 sc0 sc1
v_mfma_f32_16x16x32_bf16 v[100:103], a[0:3], a[68:71], v[20:23]
v_exp_f32_e32 v40, v80
Mv 100,4,76
Rb 52,1,46080
s_add_u32 m0, s36, 65536
Rt 184,9,16640
Mv 96,4,72
buffer_load_dwordx4 v2, s[20:23], s56 offen offset:0 sc1 lds
Mv 96,8,80
Rb 56,1,47104
v_add_f32_e32 v14, v14, v40
Mv 100,8,84
v_exp_f32_e32 v24, v81
Mv 100,12,92
Rb 60,1,48128
s_waitcnt lgkmcnt(10)
Mv 96,12,88
buffer_store_dwordx4 a[176:179], v1, s[20:23], s8 offen sc0 sc1
v_add_f32_e32 v14, v14, v24
Mv 96,16,96
Rb 0,1,49152
Cv 40,40,24
Mv 100,16,100
v_exp_f32_e32 v41, v82
Mv 100,20,108
Rb 4,1,50176
v_add_f32_e32 v14, v14, v41
Mv 96,20,104
buffer_load_dwordx4 v2, s[20:23], s56 offen offset:1024 sc1 lds
)" R"(
Mv 96,24,112
Rb 8,1,51200
Rt 186,9,24832
Mv 100,24,116
v_exp_f32_e32 v25, v83
Mv 100,28,124
Rb 12,1,52224
s_waitcnt lgkmcnt(10)
Mv 96,28,120
v_exp_f32_e32 v42, v88
v_add_f32_e32 v14, v14, v25
v_mfma_f32_16x16x32_bf16 v[104:107], a[32:35], a[64:67], v[16:19]
Rb 16,1,57344
Cv 41,41,25
v_mfma_f32_16x16x32_bf16 v[108:111], a[32:35], a[68:71], v[20:23]
v_exp_f32_e32 v26, v89
Mv 108,36,76
Rb 20,1,58368
v_add_f32_e32 v14, v14, v42
Mv 104,36,72
buffer_load_dwordx4 v2, s[20:23], s56 offen offset:2048 sc1 lds
Mv 104,40,80
Rb 24,1,59392
v_add_f32_e32 v14, v14, v26
Mv 108,40,84
buffer_store_dwordx4 a[152:155], v3, s[20:23], s48 offen offset:2048 sc0 sc1
Mv 108,44,92
Rb 28,1,60416
Rt 188,9,16768
s_waitcnt lgkmcnt(10)
Mv 104,44,88
v_exp_f32_e32 v43, v90
Cv 42,42,26
Mv 104,48,96
Rb 32,1,53248
v_add_f32_e32 v14, v14, v43
Mv 108,48,100
v_exp_f32_e32 v27, v91
Mv 108,52,108
Rb 36,1,54272
v_add_f32_e32 v14, v14, v27
Mv 104,52,104
buffer_load_dwordx4 v2, s[20:23], s56 offen offset:3072 sc1 lds
Mv 104,56,112
Rb 40,1,55296
Cv 43,43,27
Mv 108,56,116
buffer_store_dwordx4 a[180:183], v1, s[20:23], s9 offen sc0 sc1
Mv 108,60,124
Rb 44,1,56320
s_waitcnt lgkmcnt(9)
Mv 104,60,120
v_exp_f32_e32 v44, v84
s_add_u32 s55, s55, 0x10000
v_mfma_f32_16x16x32_bf16 v[112:115], a[0:3], a[64:67], v[16:19]
Rb 48,1,61440
Rt 190,9,24960
v_mfma_f32_16x16x32_bf16 v[116:119], a[0:3], a[68:71], v[20:23]
v_exp_f32_e32 v28, v85
Mv 116,4,76
Rb 52,1,62464
s_add_u32 m0, s36, 69632
Mv 112,4,72
buffer_load_dwordx4 v3, s[20:23], s56 offen offset:0 sc1 lds
)" R"(
Mv 112,8,80
Rb 56,1,63488
v_add_f32_e32 v15, v15, v44
Mv 116,8,84
v_exp_f32_e32 v45, v86
Mv 116,12,92
Rb 60,1,64512
s_waitcnt lgkmcnt(10)
Mv 112,12,88
v_exp_f32_e32 v29, v87
v_add_f32_e32 v15, v15, v28
Mv 112,16,96
v_exp_f32_e32 v46, v92
Mv 116,16,100
v_exp_f32_e32 v24, v93
Mv 116,20,108
v_exp_f32_e32 v47, v94
Mv 112,20,104
buffer_load_dwordx4 v3, s[20:23], s56 offen offset:1024 sc1 lds
Mv 112,24,112
v_exp_f32_e32 v25, v95
Mv 116,24,116
buffer_store_dwordx4 a[156:159], v3, s[20:23], s48 offen offset:3072 sc0 sc1
Mv 116,28,124
s_cmp_eq_u32 s57, 2
s_cselect_b32 s55, s59, s55
Mv 112,28,120
Cv 44,44,28
s_waitcnt vmcnt(25) lgkmcnt(0)
s_barrier
v_mfma_f32_16x16x32_bf16 v[120:123], a[32:35], a[64:67], v[16:19]
Rb 0,8,32768
v_add_f32_e32 v15, v15, v45
v_mfma_f32_16x16x32_bf16 v[124:127], a[32:35], a[68:71], v[20:23]
Rb 4,8,33792
v_add_f32_e32 v15, v15, v29
Mv 124,36,76
Rb 8,8,34816
Cv 45,45,29
Mv 120,36,72
Rb 12,8,35840
buffer_load_dwordx4 v3, s[20:23], s56 offen offset:2048 sc1 lds
Mv 120,40,80
Rb 16,8,36864
v_add_f32_e32 v15, v15, v46
Mv 124,40,84
Rb 20,8,37888
v_add_f32_e32 v15, v15, v24
Mv 124,44,92
Rb 24,8,38912
Cv 46,46,24
Mv 120,44,88
Rb 28,8,39936
buffer_store_dwordx4 a[184:187], v1, s[20:23], s10 offen sc0 sc1
Mv 120,48,96
Rb 32,8,40960
v_add_f32_e32 v15, v15, v47
Mv 124,48,100
Rb 36,8,41984
v_add_f32_e32 v15, v15, v25
Mv 124,52,108
Rb 40,8,43008
Cv 47,47,25
Mv 120,52,104
Rb 44,8,44032
)" R"(
buffer_load_dwordx4 v3, s[20:23], s56 offen offset:3072 sc1 lds
Mv 120,56,112
v_max_f32_e32 v11, v14, v15
v_cmp_nge_f32_e32 vcc, s62, v11
Mv 124,56,116
s_add_u32 s56, s56, 0x10000
Mv 124,60,124
s_waitcnt lgkmcnt(8)
s_cbranch_vccnz .Lzfx10
.Lzfr10:
Mv 120,60,120
v_add_f32_e32 v12, v12, v14
v_add_f32_e32 v13, v13, v15
Mp 128,0,32
Rb 48,8,45056
Mp 136,0,36
s_cmp_le_i32 s57, s58
s_cbranch_scc1 .Lzmk9
.Lzmr9:
Mp 140,4,36
Rb 52,8,46080
s_add_u32 m0, s36, 32768
Mp 132,4,32
buffer_load_dwordx4 v2, s[20:23], s55 offen offset:0 sc1 lds
Mp 144,8,32
Rb 56,8,47104
Mp 152,8,36
buffer_store_dwordx4 a[188:191], v1, s[20:23], s11 offen sc0 sc1
Mp 156,12,36
Rb 60,8,48128
s_waitcnt lgkmcnt(8)
Mp 148,12,32
v_exp_f32_e32 v48, v96
Mp 160,16,32
Rb 0,8,49152
Mp 168,16,36
v_exp_f32_e32 v26, v97
Mp 172,20,36
Rb 4,8,50176
v_add_f32_e32 v14, v48, v26
Mp 164,20,32
buffer_load_dwordx4 v2, s[20:23], s55 offen offset:1024 sc1 lds
Mp 176,24,32
Rb 8,8,51200
Cv 48,48,26
Mp 184,24,36
v_exp_f32_e32 v49, v98
Mp 188,28,36
Rb 12,8,52224
s_waitcnt lgkmcnt(8)
Mp 180,28,32
v_exp_f32_e32 v27, v99
Mp 192,32,32
Rb 16,8,53248
v_add_f32_e32 v14, v14, v49
Mp 200,32,36
v_exp_f32_e32 v50, v104
Mp 204,36,36
Rb 20,8,54272
v_add_f32_e32 v14, v14, v27
Mp 196,36,32
buffer_load_dwordx4 v2, s[20:23], s55 offen offset:2048 sc1 lds
Mp 208,40,32
Rb 24,8,55296
)" R"(
Cv 49,49,27
Mp 216,40,36
v_exp_f32_e32 v28, v105
Mp 220,44,36
Rb 28,8,56320
s_waitcnt lgkmcnt(8)
Mp 212,44,32
v_exp_f32_e32 v51, v106
Mp 224,48,32
Rb 32,8,57344
v_add_f32_e32 v14, v14, v50
Mp 232,48,36
v_exp_f32_e32 v29, v107
Mp 236,52,36
Rb 36,8,58368
v_add_f32_e32 v14, v14, v28
Mp 228,52,32
buffer_load_dwordx4 v2, s[20:23], s55 offen offset:3072 sc1 lds
Mp 240,56,32
Rb 40,8,59392
Cv 50,50,28
Mp 248,56,36
v_exp_f32_e32 v52, v100
Mp 252,60,36
Rb 44,8,60416
s_waitcnt lgkmcnt(8)
Mp 244,60,32
v_exp_f32_e32 v24, v101
Mp 128,0,40
Rb 48,8,61440
v_add_f32_e32 v14, v14, v51
Mp 136,0,44
v_exp_f32_e32 v53, v102
Mp 140,4,44
Rb 52,8,62464
s_add_u32 m0, s36, 36864
Mp 132,4,40
buffer_load_dwordx4 v3, s[20:23], s55 offen offset:0 sc1 lds
Mp 144,8,40
Rb 56,8,63488
v_add_f32_e32 v14, v14, v29
Mp 152,8,44
v_exp_f32_e32 v25, v103
Mp 156,12,44
Rb 60,8,64512
s_waitcnt lgkmcnt(8)
Mp 148,12,40
v_exp_f32_e32 v54, v108
Mp 160,16,40
v_exp_f32_e32 v26, v109
Mp 168,16,44
v_exp_f32_e32 v55, v110
Mp 172,20,44
v_exp_f32_e32 v27, v111
Mp 164,20,40
buffer_load_dwordx4 v3, s[20:23], s55 offen offset:1024 sc1 lds
Mp 176,24,40
Cv 51,51,29
v_add_f32_e32 v15, v52, v24
Mp 184,24,44
Cv 52,52,24
v_add_f32_e32 v15, v15, v53
Mp 188,28,44
v_add_f32_e32 v15, v15, v25
)" R"(
Cv 53,53,25
Mp 180,28,40
s_waitcnt vmcnt(5) lgkmcnt(0)
s_barrier
buffer_store_dword v6, v7, s[20:23], s49 offen offset:3072 sc0 sc1
Mp 192,32,40
Rb 0,1,0
v_add_f32_e32 v15, v15, v54
Mp 200,32,44
Rb 4,1,1024
v_add_f32_e32 v15, v15, v26
Mp 204,36,44
Rb 8,1,2048
Cv 54,54,26
Mp 196,36,40
Rb 12,1,3072
buffer_load_dwordx4 v3, s[20:23], s55 offen offset:2048 sc1 lds
Mp 208,40,40
Rb 16,1,8192
v_add_f32_e32 v15, v15, v55
Mp 216,40,44
Rb 20,1,9216
v_add_f32_e32 v15, v15, v27
Mp 220,44,44
Rb 24,1,10240
Cv 55,55,27
.LzH13:
s_cbranch_execz .LzHR
Mp 212,44,40
Rb 28,1,11264
s_sub_u32 s57, s57, 1
Mp 224,48,40
Rb 32,1,4096
Mp 232,48,44
Rb 36,1,5120
Mp 236,52,44
Rb 40,1,6144
Mp 228,52,40
Rb 44,1,7168
buffer_load_dwordx4 v3, s[20:23], s55 offen offset:3072 sc1 lds
Mp 240,56,40
v_cmp_eq_u32_e32 vcc, s3, v4
s_andn2_b64 s[28:29], -1, vcc
s_cbranch_scc1 .Lzsp20
.Lzok20:
Mp 248,56,44
Mp 252,60,44
s_waitcnt lgkmcnt(8)
Mp 244,60,40
.LzH14:
s_cbranch_execz .LzHR
.LzL:
v_mfma_f32_16x16x32_bf16 v[64:67], a[0:3], a[64:67], v[16:19]
Rb 48,1,12288
v_mfma_f32_16x16x32_bf16 v[68:71], a[0:3], a[68:71], v[20:23]
v_exp_f32_e32 v56, v112
Mv 68,4,76
Rb 52,1,13312
s_add_u32 m0, s36, 98304
Mv 64,4,72
buffer_load_dwordx4 v2, s[20:23], s56 offen offset:0 sc1 lds
Mv 64,8,80
Rb 56,1,14336
v_add_f32_e32 v14, v14, v56
Mv 68,8,84
)" R"(
v_exp_f32_e32 v24, v113
Mv 68,12,92
Rb 60,1,15360
s_waitcnt lgkmcnt(8)
Mv 64,12,88
v_exp_f32_e32 v57, v114
Mv 64,16,96
Rb 0,1,16384
v_add_f32_e32 v14, v14, v24
Mv 68,16,100
v_exp_f32_e32 v25, v115
Mv 68,20,108
Rb 4,1,17408
Cv 56,56,24
Mv 64,20,104
buffer_load_dwordx4 v2, s[20:23], s56 offen offset:1024 sc1 lds
Mv 64,24,112
Rb 8,1,18432
v_add_f32_e32 v14, v14, v57
Mv 68,24,116
v_exp_f32_e32 v58, v120
Mv 68,28,124
Rb 12,1,19456
s_waitcnt lgkmcnt(8)
Mv 64,28,120
v_exp_f32_e32 v26, v121
v_mfma_f32_16x16x32_bf16 v[72:75], a[32:35], a[64:67], v[16:19]
Rb 16,1,24576
v_add_f32_e32 v14, v14, v25
v_mfma_f32_16x16x32_bf16 v[76:79], a[32:35], a[68:71], v[20:23]
v_exp_f32_e32 v59, v122
Mv 76,36,76
Rb 20,1,25600
Cv 57,57,25
Mv 72,36,72
buffer_load_dwordx4 v2, s[20:23], s56 offen offset:2048 sc1 lds
Mv 72,40,80
Rb 24,1,26624
v_add_f32_e32 v14, v14, v58
Mv 76,40,84
v_exp_f32_e32 v27, v123
Mv 76,44,92
Rb 28,1,27648
s_waitcnt lgkmcnt(8)
Mv 72,44,88
v_exp_f32_e32 v60, v116
Mv 72,48,96
Rb 32,1,20480
v_add_f32_e32 v14, v14, v26
Mv 76,48,100
v_exp_f32_e32 v28, v117
Mv 76,52,108
Rb 36,1,21504
Cv 58,58,26
Mv 72,52,104
buffer_load_dwordx4 v2, s[20:23], s56 offen offset:3072 sc1 lds
Mv 72,56,112
Rb 40,1,22528
v_add_f32_e32 v14, v14, v59
Mv 76,56,116
v_exp_f32_e32 v61, v118
Mv 76,60,124
Rb 44,1,23552
s_waitcnt lgkmcnt(8)
)" R"(
Mv 72,60,120
v_exp_f32_e32 v29, v119
v_mfma_f32_16x16x32_bf16 v[80:83], a[0:3], a[64:67], v[16:19]
Rb 48,1,28672
v_add_f32_e32 v14, v14, v27
v_mfma_f32_16x16x32_bf16 v[84:87], a[0:3], a[68:71], v[20:23]
v_exp_f32_e32 v62, v124
Mv 84,4,76
Rb 52,1,29696
s_add_u32 m0, s36, 102400
Mv 80,4,72
buffer_load_dwordx4 v3, s[20:23], s56 offen offset:0 sc1 lds
Mv 80,8,80
Rb 56,1,30720
Cv 59,59,27
Mv 84,8,84
v_exp_f32_e32 v24, v125
Mv 84,12,92
Rb 60,1,31744
s_waitcnt lgkmcnt(8)
Mv 80,12,88
v_exp_f32_e32 v63, v126
Mv 80,16,96
v_exp_f32_e32 v25, v127
Mv 84,16,100
v_add_f32_e32 v15, v15, v60
v_add_f32_e32 v15, v15, v28
Mv 84,20,108
Cv 60,60,28
v_add_f32_e32 v15, v15, v61
Mv 80,20,104
buffer_load_dwordx4 v3, s[20:23], s56 offen offset:1024 sc1 lds
Mv 80,24,112
v_add_f32_e32 v15, v15, v29
Cv 61,61,29
Mv 84,24,116
v_add_f32_e32 v15, v15, v62
v_add_f32_e32 v15, v15, v24
Mv 84,28,124
Cv 62,62,24
v_add_f32_e32 v15, v15, v63
Mv 80,28,120
s_waitcnt vmcnt(14) lgkmcnt(0)
s_barrier
v_mfma_f32_16x16x32_bf16 v[88:91], a[32:35], a[64:67], v[16:19]
Rb 0,8,0
v_add_f32_e32 v15, v15, v25
v_mfma_f32_16x16x32_bf16 v[92:95], a[32:35], a[68:71], v[20:23]
Rb 4,8,1024
Cv 63,63,25
Mv 92,36,76
Rb 8,8,2048
v_max_f32_e32 v11, v14, v15
Mv 88,36,72
Rb 12,8,3072
buffer_load_dwordx4 v3, s[20:23], s56 offen offset:2048 sc1 lds
Mv 88,40,80
Rb 16,8,4096
v_cmp_nge_f32_e32 vcc, s62, v11
Mv 92,40,84
Rb 20,8,5120
s_add_u32 s55, s55, 0x10000
Mv 92,44,92
Rb 24,8,6144
Mv 88,44,88
)" R"(
Rb 28,8,7168
Mv 88,48,96
Rb 32,8,8192
Mv 92,48,100
Rb 36,8,9216
Mv 92,52,108
Rb 40,8,10240
Mv 88,52,104
Rb 44,8,11264
buffer_load_dwordx4 v3, s[20:23], s56 offen offset:3072 sc1 lds
Mv 88,56,112
s_cmp_eq_u32 s57, 2
s_cselect_b32 s55, s59, s55
Mv 92,56,116
s_add_u32 s56, s56, 0x10000
Mv 92,60,124
s_waitcnt lgkmcnt(8)
s_cbranch_vccnz .Lzfx1
.Lzfr1:
Mv 88,60,120
v_add_f32_e32 v12, v12, v14
v_add_f32_e32 v13, v13, v15
Mp 128,0,48
Rb 48,8,12288
Mp 136,0,52
s_cmp_le_i32 s57, s58
s_cbranch_scc1 .Lzmk2
.Lzmr2:
Mp 140,4,52
Rb 52,8,13312
s_add_u32 m0, s36, 0
Mp 132,4,48
buffer_load_dwordx4 v2, s[20:23], s55 offen offset:0 sc1 lds
Mp 144,8,48
Rb 56,8,14336
Mp 152,8,52
v_exp_f32_e32 v32, v64
Mp 156,12,52
Rb 60,8,15360
s_waitcnt lgkmcnt(8)
Mp 148,12,48
v_exp_f32_e32 v26, v65
Mp 160,16,48
Rb 0,8,16384
v_add_f32_e32 v14, v32, v26
Mp 168,16,52
v_exp_f32_e32 v33, v66
Mp 172,20,52
Rb 4,8,17408
Cv 32,32,26
Mp 164,20,48
buffer_load_dwordx4 v2, s[20:23], s55 offen offset:1024 sc1 lds
Mp 176,24,48
Rb 8,8,18432
v_add_f32_e32 v14, v14, v33
Mp 184,24,52
v_exp_f32_e32 v27, v67
Mp 188,28,52
Rb 12,8,19456
s_waitcnt lgkmcnt(8)
Mp 180,28,48
v_exp_f32_e32 v34, v72
Mp 192,32,48
Rb 16,8,20480
)" R"(
v_add_f32_e32 v14, v14, v27
Mp 200,32,52
v_exp_f32_e32 v28, v73
Mp 204,36,52
Rb 20,8,21504
Cv 33,33,27
Mp 196,36,48
buffer_load_dwordx4 v2, s[20:23], s55 offen offset:2048 sc1 lds
Mp 208,40,48
Rb 24,8,22528
v_add_f32_e32 v14, v14, v34
Mp 216,40,52
v_exp_f32_e32 v35, v74
Mp 220,44,52
Rb 28,8,23552
s_waitcnt lgkmcnt(8)
Mp 212,44,48
v_exp_f32_e32 v29, v75
Mp 224,48,48
Rb 32,8,24576
v_add_f32_e32 v14, v14, v28
Mp 232,48,52
v_exp_f32_e32 v36, v68
Mp 236,52,52
Rb 36,8,25600
Cv 34,34,28
Mp 228,52,48
buffer_load_dwordx4 v2, s[20:23], s55 offen offset:3072 sc1 lds
Mp 240,56,48
Rb 40,8,26624
v_add_f32_e32 v14, v14, v35
Mp 248,56,52
v_exp_f32_e32 v24, v69
Mp 252,60,52
Rb 44,8,27648
s_waitcnt lgkmcnt(8)
Mp 244,60,48
v_exp_f32_e32 v37, v70
Mp 128,0,56
Rb 48,8,28672
v_add_f32_e32 v14, v14, v29
Mp 136,0,60
v_exp_f32_e32 v25, v71
Mp 140,4,60
Rb 52,8,29696
s_add_u32 m0, s36, 4096
Mp 132,4,56
buffer_load_dwordx4 v3, s[20:23], s55 offen offset:0 sc1 lds
Mp 144,8,56
Rb 56,8,30720
Cv 35,35,29
Mp 152,8,60
v_exp_f32_e32 v38, v76
Mp 156,12,60
Rb 60,8,31744
s_waitcnt lgkmcnt(8)
Mp 148,12,56
v_exp_f32_e32 v26, v77
Mp 160,16,56
v_exp_f32_e32 v39, v78
Mp 168,16,60
v_exp_f32_e32 v27, v79
Mp 172,20,60
v_add_f32_e32 v15, v36, v24
)" R"(
Cv 36,36,24
Mp 164,20,56
buffer_load_dwordx4 v3, s[20:23], s55 offen offset:1024 sc1 lds
Mp 176,24,56
s_add_u32 s50, s50, 1024
s_min_u32 s50, s50, s60
Mp 184,24,60
v_add_f32_e32 v15, v15, v37
v_add_f32_e32 v15, v15, v25
Mp 188,28,60
Cv 37,37,25
v_add_f32_e32 v15, v15, v38
Mp 180,28,56
s_waitcnt vmcnt(14) lgkmcnt(0)
s_barrier
Mp 192,32,56
Rb 0,1,32768
v_add_f32_e32 v15, v15, v26
Mp 200,32,60
Rb 4,1,33792
Cv 38,38,26
Mp 204,36,60
Rb 8,1,34816
v_add_f32_e32 v15, v15, v39
Mp 196,36,56
Rb 12,1,35840
buffer_load_dwordx4 v3, s[20:23], s55 offen offset:2048 sc1 lds
Mp 208,40,56
Rb 16,1,40960
v_add_f32_e32 v15, v15, v27
Mp 216,40,60
Rb 20,1,41984
Cv 39,39,27
Mp 220,44,60
Rb 24,1,43008
s_sub_u32 s57, s57, 1
Mp 212,44,56
Rb 28,1,44032
Mp 224,48,56
Rb 32,1,36864
Mp 232,48,60
Rb 36,1,37888
Mp 236,52,60
Rb 40,1,38912
Mp 228,52,56
Rb 44,1,39936
buffer_load_dwordx4 v3, s[20:23], s55 offen offset:3072 sc1 lds
Mp 240,56,56
Mp 248,56,60
Mp 252,60,60
s_waitcnt lgkmcnt(8)
Mp 244,60,56
v_mfma_f32_16x16x32_bf16 v[96:99], a[0:3], a[64:67], v[16:19]
Rb 48,1,45056
buffer_load_dword v4, v5, s[20:23], s50 offen offset:0 sc0 sc1
v_mfma_f32_16x16x32_bf16 v[100:103], a[0:3], a[68:71], v[20:23]
v_exp_f32_e32 v40, v80
Mv 100,4,76
Rb 52,1,46080
s_add_u32 m0, s36, 65536
Mv 96,4,72
buffer_load_dwordx4 v2, s[20:23], s56 offen offset:0 sc1 lds
Mv 96,8,80
Rb 56,1,47104
v_add_f32_e32 v14, v14, v40
)" R"(
Mv 100,8,84
v_exp_f32_e32 v28, v81
Mv 100,12,92
Rb 60,1,48128
s_waitcnt lgkmcnt(8)
Mv 96,12,88
v_exp_f32_e32 v41, v82
Mv 96,16,96
Rb 0,1,49152
v_add_f32_e32 v14, v14, v28
Mv 100,16,100
v_exp_f32_e32 v29, v83
Mv 100,20,108
Rb 4,1,50176
Cv 40,40,28
Mv 96,20,104
buffer_load_dwordx4 v2, s[20:23], s56 offen offset:1024 sc1 lds
Mv 96,24,112
Rb 8,1,51200
v_add_f32_e32 v14, v14, v41
Mv 100,24,116
v_exp_f32_e32 v42, v88
Mv 100,28,124
Rb 12,1,52224
s_waitcnt lgkmcnt(8)
Mv 96,28,120
v_exp_f32_e32 v24, v89
v_mfma_f32_16x16x32_bf16 v[104:107], a[32:35], a[64:67], v[16:19]
Rb 16,1,57344
v_add_f32_e32 v14, v14, v29
v_mfma_f32_16x16x32_bf16 v[108:111], a[32:35], a[68:71], v[20:23]
v_exp_f32_e32 v43, v90
Mv 108,36,76
Rb 20,1,58368
Cv 41,41,29
Mv 104,36,72
buffer_load_dwordx4 v2, s[20:23], s56 offen offset:2048 sc1 lds
Mv 104,40,80
Rb 24,1,59392
v_add_f32_e32 v14, v14, v42
Mv 108,40,84
v_exp_f32_e32 v25, v91
Mv 108,44,92
Rb 28,1,60416
s_waitcnt lgkmcnt(8)
Mv 104,44,88
v_exp_f32_e32 v44, v84
Mv 104,48,96
Rb 32,1,53248
v_add_f32_e32 v14, v14, v24
Mv 108,48,100
v_exp_f32_e32 v26, v85
Mv 108,52,108
Rb 36,1,54272
Cv 42,42,24
Mv 104,52,104
buffer_load_dwordx4 v2, s[20:23], s56 offen offset:3072 sc1 lds
Mv 104,56,112
Rb 40,1,55296
v_add_f32_e32 v14, v14, v43
Mv 108,56,116
v_exp_f32_e32 v45, v86
Mv 108,60,124
Rb 44,1,56320
)" R"(
s_waitcnt lgkmcnt(8)
Mv 104,60,120
v_exp_f32_e32 v27, v87
v_mfma_f32_16x16x32_bf16 v[112:115], a[0:3], a[64:67], v[16:19]
Rb 48,1,61440
v_add_f32_e32 v14, v14, v25
v_mfma_f32_16x16x32_bf16 v[116:119], a[0:3], a[68:71], v[20:23]
v_exp_f32_e32 v46, v92
Mv 116,4,76
Rb 52,1,62464
s_add_u32 m0, s36, 69632
Mv 112,4,72
buffer_load_dwordx4 v3, s[20:23], s56 offen offset:0 sc1 lds
Mv 112,8,80
Rb 56,1,63488
Cv 43,43,25
Mv 116,8,84
v_exp_f32_e32 v28, v93
Mv 116,12,92
Rb 60,1,64512
s_waitcnt lgkmcnt(8)
Mv 112,12,88
v_exp_f32_e32 v47, v94
Mv 112,16,96
v_exp_f32_e32 v29, v95
Mv 116,16,100
v_add_f32_e32 v15, v15, v44
v_add_f32_e32 v15, v15, v26
Mv 116,20,108
Cv 44,44,26
v_add_f32_e32 v15, v15, v45
Mv 112,20,104
buffer_load_dwordx4 v3, s[20:23], s56 offen offset:1024 sc1 lds
Mv 112,24,112
v_add_f32_e32 v15, v15, v27
Cv 45,45,27
Mv 116,24,116
v_add_f32_e32 v15, v15, v46
v_add_f32_e32 v15, v15, v28
Mv 116,28,124
Cv 46,46,28
v_add_f32_e32 v15, v15, v47
Mv 112,28,120
s_waitcnt vmcnt(15) lgkmcnt(0)
s_barrier
v_mfma_f32_16x16x32_bf16 v[120:123], a[32:35], a[64:67], v[16:19]
Rb 0,8,32768
v_add_f32_e32 v15, v15, v29
v_mfma_f32_16x16x32_bf16 v[124:127], a[32:35], a[68:71], v[20:23]
Rb 4,8,33792
Cv 47,47,29
Mv 124,36,76
Rb 8,8,34816
v_max_f32_e32 v11, v14, v15
Mv 120,36,72
Rb 12,8,35840
buffer_load_dwordx4 v3, s[20:23], s56 offen offset:2048 sc1 lds
Mv 120,40,80
Rb 16,8,36864
v_cmp_nge_f32_e32 vcc, s62, v11
Mv 124,40,84
Rb 20,8,37888
s_add_u32 s55, s55, 0x10000
Mv 124,44,92
Rb 24,8,38912
)" R"(
Mv 120,44,88
Rb 28,8,39936
Mv 120,48,96
Rb 32,8,40960
Mv 124,48,100
Rb 36,8,41984
Mv 124,52,108
Rb 40,8,43008
Mv 120,52,104
Rb 44,8,44032
buffer_load_dwordx4 v3, s[20:23], s56 offen offset:3072 sc1 lds
Mv 120,56,112
s_cmp_eq_u32 s57, 2
s_cselect_b32 s55, s59, s55
Mv 124,56,116
s_add_u32 s56, s56, 0x10000
Mv 124,60,124
s_waitcnt lgkmcnt(8)
s_cbranch_vccnz .Lzfx2
.Lzfr2:
Mv 120,60,120
v_add_f32_e32 v12, v12, v14
v_add_f32_e32 v13, v13, v15
Mp 128,0,32
Rb 48,8,45056
Mp 136,0,36
s_cmp_le_i32 s57, s58
s_cbranch_scc1 .Lzmk3
.Lzmr3:
Mp 140,4,36
Rb 52,8,46080
s_add_u32 m0, s36, 32768
Mp 132,4,32
buffer_load_dwordx4 v2, s[20:23], s55 offen offset:0 sc1 lds
Mp 144,8,32
Rb 56,8,47104
Mp 152,8,36
v_exp_f32_e32 v48, v96
Mp 156,12,36
Rb 60,8,48128
s_waitcnt lgkmcnt(8)
Mp 148,12,32
v_exp_f32_e32 v24, v97
Mp 160,16,32
Rb 0,8,49152
v_add_f32_e32 v14, v48, v24
Mp 168,16,36
v_exp_f32_e32 v49, v98
Mp 172,20,36
Rb 4,8,50176
Cv 48,48,24
Mp 164,20,32
buffer_load_dwordx4 v2, s[20:23], s55 offen offset:1024 sc1 lds
Mp 176,24,32
Rb 8,8,51200
v_add_f32_e32 v14, v14, v49
Mp 184,24,36
v_exp_f32_e32 v25, v99
Mp 188,28,36
Rb 12,8,52224
s_waitcnt lgkmcnt(8)
Mp 180,28,32
v_exp_f32_e32 v50, v104
Mp 192,32,32
)" R"(
Rb 16,8,53248
v_add_f32_e32 v14, v14, v25
Mp 200,32,36
v_exp_f32_e32 v26, v105
Mp 204,36,36
Rb 20,8,54272
Cv 49,49,25
Mp 196,36,32
buffer_load_dwordx4 v2, s[20:23], s55 offen offset:2048 sc1 lds
Mp 208,40,32
Rb 24,8,55296
v_add_f32_e32 v14, v14, v50
Mp 216,40,36
v_exp_f32_e32 v51, v106
Mp 220,44,36
Rb 28,8,56320
s_waitcnt lgkmcnt(8)
Mp 212,44,32
v_exp_f32_e32 v27, v107
Mp 224,48,32
Rb 32,8,57344
v_add_f32_e32 v14, v14, v26
Mp 232,48,36
v_exp_f32_e32 v52, v100
Mp 236,52,36
Rb 36,8,58368
Cv 50,50,26
Mp 228,52,32
buffer_load_dwordx4 v2, s[20:23], s55 offen offset:3072 sc1 lds
Mp 240,56,32
Rb 40,8,59392
v_add_f32_e32 v14, v14, v51
Mp 248,56,36
v_exp_f32_e32 v28, v101
Mp 252,60,36
Rb 44,8,60416
s_waitcnt lgkmcnt(8)
Mp 244,60,32
v_exp_f32_e32 v53, v102
Mp 128,0,40
Rb 48,8,61440
v_add_f32_e32 v14, v14, v27
Mp 136,0,44
v_exp_f32_e32 v29, v103
Mp 140,4,44
Rb 52,8,62464
s_add_u32 m0, s36, 36864
Mp 132,4,40
buffer_load_dwordx4 v3, s[20:23], s55 offen offset:0 sc1 lds
.LzH15:
s_cbranch_execz .LzHR
Mp 144,8,40
Rb 56,8,63488
Cv 51,51,27
Mp 152,8,44
v_exp_f32_e32 v54, v108
Mp 156,12,44
Rb 60,8,64512
s_waitcnt lgkmcnt(8)
Mp 148,12,40
v_exp_f32_e32 v24, v109
Mp 160,16,40
v_exp_f32_e32 v55, v110
Mp 168,16,44
)" R"(
v_exp_f32_e32 v25, v111
Mp 172,20,44
v_add_f32_e32 v15, v52, v28
Cv 52,52,28
Mp 164,20,40
buffer_load_dwordx4 v3, s[20:23], s55 offen offset:1024 sc1 lds
Mp 176,24,40
v_add_f32_e32 v15, v15, v53
v_add_f32_e32 v15, v15, v29
Mp 184,24,44
Cv 53,53,29
v_add_f32_e32 v15, v15, v54
Mp 188,28,44
v_add_f32_e32 v15, v15, v24
Cv 54,54,24
Mp 180,28,40
s_waitcnt vmcnt(14) lgkmcnt(0)
s_barrier
Mp 192,32,40
Rb 0,1,0
v_add_f32_e32 v15, v15, v55
Mp 200,32,44
Rb 4,1,1024
v_add_f32_e32 v15, v15, v25
Mp 204,36,44
Rb 8,1,2048
Cv 55,55,25
Mp 196,36,40
Rb 12,1,3072
buffer_load_dwordx4 v3, s[20:23], s55 offen offset:2048 sc1 lds
Mp 208,40,40
Rb 16,1,8192
s_sub_u32 s57, s57, 1
Mp 216,40,44
Rb 20,1,9216
Mp 220,44,44
Rb 24,1,10240
Mp 212,44,40
Rb 28,1,11264
Mp 224,48,40
Rb 32,1,4096
Mp 232,48,44
Rb 36,1,5120
Mp 236,52,44
Rb 40,1,6144
Mp 228,52,40
Rb 44,1,7168
buffer_load_dwordx4 v3, s[20:23], s55 offen offset:3072 sc1 lds
Mp 240,56,40
v_cmp_eq_u32_e32 vcc, s3, v4
s_andn2_b64 s[28:29], -1, vcc
s_cbranch_scc1 .Lzsp9
.Lzok9:
Mp 248,56,44
Mp 252,60,44
s_waitcnt lgkmcnt(8)
Mp 244,60,40
s_cmp_gt_i32 s57, s93
s_cbranch_scc1 .LzL
s_cmp_eq_u32 s94, 1
s_cbranch_scc1 .LzW
s_cmp_eq_u32 s94, 2
s_cbranch_scc1 .LzDA
.LzEB:
v_mfma_f32_16x16x32_bf16 v[64:67], a[0:3], a[64:67], v[16:19]
)" R"(
Rb 48,1,12288
s_mov_b32 s83, 0x1508000
buffer_load_dword v4, v7, s[20:23], s83 offen offset:4 sc0 sc1
v_mfma_f32_16x16x32_bf16 v[68:71], a[0:3], a[68:71], v[20:23]
s_cmp_lg_u32 s35, 7
s_cselect_b64 s[60:61], 1, 0
Mv 68,4,76
Rb 52,1,13312
s_add_u32 m0, s36, 98304
Mv 64,4,72
buffer_load_dwordx4 v2, s[20:23], s56 offen offset:0 sc1 lds
Mv 64,8,80
Rb 56,1,14336
Mv 68,8,84
v_exp_f32_e32 v56, v112
Mv 68,12,92
Rb 60,1,15360
s_waitcnt lgkmcnt(8)
Mv 64,12,88
v_exp_f32_e32 v28, v113
Mv 64,16,96
Rb 0,1,16384
v_add_f32_e32 v14, v14, v56
Mv 68,16,100
v_exp_f32_e32 v57, v114
Mv 68,20,108
Rb 4,1,17408
v_add_f32_e32 v14, v14, v28
Mv 64,20,104
buffer_load_dwordx4 v2, s[20:23], s56 offen offset:1024 sc1 lds
Mv 64,24,112
Rb 8,1,18432
Cv 56,56,28
Mv 68,24,116
v_exp_f32_e32 v29, v115
Mv 68,28,124
Rb 12,1,19456
s_waitcnt lgkmcnt(8)
Mv 64,28,120
v_exp_f32_e32 v58, v120
v_mfma_f32_16x16x32_bf16 v[72:75], a[32:35], a[64:67], v[16:19]
Rb 16,1,24576
v_add_f32_e32 v14, v14, v57
v_mfma_f32_16x16x32_bf16 v[76:79], a[32:35], a[68:71], v[20:23]
v_exp_f32_e32 v24, v121
Mv 76,36,76
Rb 20,1,25600
v_add_f32_e32 v14, v14, v29
Mv 72,36,72
buffer_load_dwordx4 v2, s[20:23], s56 offen offset:2048 sc1 lds
Mv 72,40,80
Rb 24,1,26624
Cv 57,57,29
Mv 76,40,84
v_exp_f32_e32 v59, v122
Mv 76,44,92
Rb 28,1,27648
s_waitcnt lgkmcnt(8)
Mv 72,44,88
v_exp_f32_e32 v25, v123
Mv 72,48,96
Rb 32,1,20480
v_add_f32_e32 v14, v14, v58
Mv 76,48,100
)" R"(
v_exp_f32_e32 v60, v116
Mv 76,52,108
Rb 36,1,21504
v_add_f32_e32 v14, v14, v24
Mv 72,52,104
buffer_load_dwordx4 v2, s[20:23], s56 offen offset:3072 sc1 lds
Mv 72,56,112
Rb 40,1,22528
Cv 58,58,24
Mv 76,56,116
v_exp_f32_e32 v26, v117
Mv 76,60,124
Rb 44,1,23552
s_waitcnt lgkmcnt(8)
Mv 72,60,120
v_exp_f32_e32 v61, v118
v_mfma_f32_16x16x32_bf16 v[80:83], a[0:3], a[64:67], v[16:19]
Rb 48,1,28672
v_add_f32_e32 v14, v14, v59
v_mfma_f32_16x16x32_bf16 v[84:87], a[0:3], a[68:71], v[20:23]
v_exp_f32_e32 v27, v119
Mv 84,4,76
Rb 52,1,29696
s_add_u32 m0, s36, 102400
Mv 80,4,72
buffer_load_dwordx4 v3, s[20:23], s56 offen offset:0 sc1 lds
Mv 80,8,80
Rb 56,1,30720
v_add_f32_e32 v14, v14, v25
Mv 84,8,84
v_exp_f32_e32 v62, v124
Mv 84,12,92
Rb 60,1,31744
s_waitcnt lgkmcnt(8)
Mv 80,12,88
v_exp_f32_e32 v28, v125
Mv 80,16,96
v_exp_f32_e32 v63, v126
Mv 84,16,100
v_exp_f32_e32 v29, v127
Mv 84,20,108
Cv 59,59,25
v_add_f32_e32 v15, v15, v60
Mv 80,20,104
buffer_load_dwordx4 v3, s[20:23], s56 offen offset:1024 sc1 lds
Mv 80,24,112
v_add_f32_e32 v15, v15, v26
Cv 60,60,26
Mv 84,24,116
v_add_f32_e32 v15, v15, v61
v_add_f32_e32 v15, v15, v27
Mv 84,28,124
Cv 61,61,27
v_add_f32_e32 v15, v15, v62
Mv 80,28,120
s_waitcnt vmcnt(15) lgkmcnt(0)
s_barrier
v_mfma_f32_16x16x32_bf16 v[88:91], a[32:35], a[64:67], v[16:19]
Rb 0,8,0
v_add_f32_e32 v15, v15, v28
v_mfma_f32_16x16x32_bf16 v[92:95], a[32:35], a[68:71], v[20:23]
Rb 4,8,1024
Cv 62,62,28
Mv 92,36,76
Rb 8,8,2048
)" R"(
v_add_f32_e32 v15, v15, v63
Mv 88,36,72
Rb 12,8,3072
buffer_load_dwordx4 v3, s[20:23], s56 offen offset:2048 sc1 lds
Mv 88,40,80
Rb 16,8,4096
v_add_f32_e32 v15, v15, v29
Mv 92,40,84
Rb 20,8,5120
Cv 63,63,29
Mv 92,44,92
Rb 24,8,6144
v_max_f32_e32 v11, v14, v15
Mv 88,44,88
Rb 28,8,7168
v_cmp_nge_f32_e32 vcc, s62, v11
Mv 88,48,96
Rb 32,8,8192
s_add_u32 s55, s55, 0x10000
Mv 92,48,100
Rb 36,8,9216
Mv 92,52,108
Rb 40,8,10240
Mv 88,52,104
Rb 44,8,11264
buffer_load_dwordx4 v3, s[20:23], s56 offen offset:3072 sc1 lds
Mv 88,56,112
s_cmp_eq_u32 s57, 2
s_cselect_b32 s55, s59, s55
Mv 92,56,116
s_add_u32 s56, s56, 0x10000
Mv 92,60,124
s_waitcnt lgkmcnt(8)
s_cbranch_vccnz .Lzfx11
.Lzfr11:
Mv 88,60,120
s_waitcnt vmcnt(8)
v_cmp_eq_u32_e32 vcc, s3, v4
s_andn2_b64 s[28:29], 1, vcc
s_cbranch_scc1 .Lzsp21
.Lzok21:
Mp 128,0,48
Rb 48,8,12288
buffer_load_dword v9, v7, s[20:23], s83 offen sc0 sc1
Mp 136,0,52
v_add_f32_e32 v12, v12, v14
s_cmp_le_i32 s57, s58
s_cbranch_scc1 .Lzmk10
.Lzmr10:
Mp 140,4,52
Rb 52,8,13312
s_add_u32 m0, s36, 0
v_add_f32_e32 v13, v13, v15
Mp 132,4,48
buffer_load_dwordx4 v2, s[20:23], s55 offen offset:0 sc1 lds
Mp 144,8,48
Rb 56,8,14336
Mp 152,8,52
v_exp_f32_e32 v32, v64
Mp 156,12,52
Rb 60,8,15360
s_waitcnt lgkmcnt(8)
Mp 148,12,48
v_exp_f32_e32 v24, v65
)" R"(
Mp 160,16,48
Rb 0,8,16384
v_add_f32_e32 v14, v32, v24
Mp 168,16,52
v_exp_f32_e32 v33, v66
Cv 32,32,24
Mp 172,20,52
Rb 4,8,17408
v_add_f32_e32 v14, v14, v33
Mp 164,20,48
buffer_load_dwordx4 v2, s[20:23], s55 offen offset:1024 sc1 lds
Mp 176,24,48
Rb 8,8,18432
Mp 184,24,52
v_exp_f32_e32 v25, v67
Mp 188,28,52
Rb 12,8,19456
s_waitcnt lgkmcnt(8)
Mp 180,28,48
v_exp_f32_e32 v34, v72
Mp 192,32,48
Rb 16,8,20480
v_add_f32_e32 v14, v14, v25
Mp 200,32,52
v_exp_f32_e32 v26, v73
Cv 33,33,25
Mp 204,36,52
Rb 20,8,21504
v_add_f32_e32 v14, v14, v34
Mp 196,36,48
buffer_load_dwordx4 v2, s[20:23], s55 offen offset:2048 sc1 lds
Mp 208,40,48
Rb 24,8,22528
v_add_f32_e32 v14, v14, v26
Mp 216,40,52
v_exp_f32_e32 v35, v74
Mp 220,44,52
Rb 28,8,23552
s_waitcnt lgkmcnt(8)
Mp 212,44,48
v_exp_f32_e32 v27, v75
Mp 224,48,48
Rb 32,8,24576
Cv 34,34,26
Mp 232,48,52
v_exp_f32_e32 v36, v68
v_add_f32_e32 v14, v14, v35
Mp 236,52,52
Rb 36,8,25600
v_add_f32_e32 v14, v14, v27
Mp 228,52,48
buffer_load_dwordx4 v2, s[20:23], s55 offen offset:3072 sc1 lds
Mp 240,56,48
Rb 40,8,26624
Cv 35,35,27
Mp 248,56,52
v_exp_f32_e32 v28, v69
Mp 252,60,52
Rb 44,8,27648
s_waitcnt lgkmcnt(8)
Mp 244,60,48
v_exp_f32_e32 v37, v70
Mp 128,0,56
Rb 48,8,28672
)" R"(
v_add_f32_e32 v15, v36, v28
Mp 136,0,60
v_exp_f32_e32 v29, v71
Cv 36,36,28
Mp 140,4,60
Rb 52,8,29696
s_add_u32 m0, s36, 4096
Mp 132,4,56
buffer_load_dwordx4 v3, s[20:23], s55 offen offset:0 sc1 lds
Mp 144,8,56
Rb 56,8,30720
v_add_f32_e32 v15, v15, v37
Mp 152,8,60
v_exp_f32_e32 v38, v76
Mp 156,12,60
Rb 60,8,31744
s_waitcnt lgkmcnt(8)
Mp 148,12,56
v_exp_f32_e32 v24, v77
Mp 160,16,56
v_exp_f32_e32 v39, v78
Mp 168,16,60
v_exp_f32_e32 v25, v79
v_add_f32_e32 v15, v15, v29
Mp 172,20,60
Cv 37,37,29
v_add_f32_e32 v15, v15, v38
Mp 164,20,56
buffer_load_dwordx4 v3, s[20:23], s55 offen offset:1024 sc1 lds
Mp 176,24,56
v_add_f32_e32 v15, v15, v24
Cv 38,38,24
Mp 184,24,60
v_add_f32_e32 v15, v15, v39
v_add_f32_e32 v15, v15, v25
Mp 188,28,60
Cv 39,39,25
s_sub_u32 s57, s57, 1
Mp 180,28,56
s_waitcnt lgkmcnt(0)
s_barrier
Mp 192,32,56
Rb 0,1,32768
v_bfe_u32 v11, v0, 2, 2
Mp 200,32,60
Rb 4,1,33792
v_lshrrev_b32 v4, 4, v0
v_xor_b32 v11, v11, v4
Mp 204,36,60
Rb 8,1,34816
v_bfe_u32 v4, v0, 1, 1
Mp 196,36,56
Rb 12,1,35840
buffer_load_dwordx4 v3, s[20:23], s55 offen offset:2048 sc1 lds
Mp 208,40,56
Rb 16,1,40960
v_lshl_or_b32 v11, v11, 1, v4
Mp 216,40,60
Rb 20,1,41984
v_and_b32 v4, 1, v0
Mp 220,44,60
Rb 24,1,43008
v_mul_u32_u24 v4, 0xffff, v4
Mp 212,44,56
Rb 28,1,44032
)" R"(
v_add_u32 v4, 1, v4
Mp 224,48,56
Rb 32,1,36864
v_mul_u32_u24 v4, 0x3f80, v4
Mp 232,48,60
Rb 36,1,37888
v_cmp_eq_u32_e32 vcc, 0, v11
v_cmp_eq_u32_e64 s[28:29], 1, v11
Mp 236,52,60
Rb 40,1,38912
v_accvgpr_write_b32 a192, 0
Mp 228,52,56
Rb 44,1,39936
buffer_load_dwordx4 v3, s[20:23], s55 offen offset:3072 sc1 lds
Mp 240,56,56
v_accvgpr_write_b32 a193, 0
v_accvgpr_write_b32 a196, 0
Mp 248,56,60
v_accvgpr_write_b32 a197, 0
v_cndmask_b32_e32 v11, 0, v4, vcc
Mp 252,60,60
v_cndmask_b32_e64 v4, 0, v4, s[28:29]
s_waitcnt lgkmcnt(8)
Mp 244,60,56
v_accvgpr_write_b32 a194, v11
v_accvgpr_write_b32 a195, v4
v_mfma_f32_16x16x32_bf16 v[96:99], a[0:3], a[64:67], v[16:19]
Rb 48,1,45056
v_mfma_f32_16x16x32_bf16 v[100:103], a[0:3], a[68:71], v[20:23]
buffer_load_dword v4, v7, s[20:23], s65 offen offset:0 sc0 sc1
Mv 100,4,76
Rb 52,1,46080
s_add_u32 m0, s36, 65536
Mv 96,4,72
buffer_load_dwordx4 v2, s[20:23], s56 offen offset:0 sc1 lds
Mv 96,8,80
Rb 56,1,47104
Mv 100,8,84
v_exp_f32_e32 v40, v80
Mv 100,12,92
Rb 60,1,48128
s_waitcnt lgkmcnt(8)
Mv 96,12,88
v_exp_f32_e32 v26, v81
v_add_f32_e32 v14, v14, v40
Mv 96,16,96
Rb 0,1,49152
v_add_f32_e32 v14, v14, v26
Mv 100,16,100
v_exp_f32_e32 v41, v82
Mv 100,20,108
Rb 4,1,50176
Cv 40,40,26
Mv 96,20,104
buffer_load_dwordx4 v2, s[20:23], s56 offen offset:1024 sc1 lds
Mv 96,24,112
Rb 8,1,51200
v_add_f32_e32 v14, v14, v41
Mv 100,24,116
v_exp_f32_e32 v27, v83
Mv 100,28,124
Rb 12,1,52224
s_waitcnt lgkmcnt(8)
Mv 96,28,120
)" R"(
v_exp_f32_e32 v42, v88
v_add_f32_e32 v14, v14, v27
v_mfma_f32_16x16x32_bf16 v[104:107], a[32:35], a[64:67], v[16:19]
Rb 16,1,57344
Cv 41,41,27
v_mfma_f32_16x16x32_bf16 v[108:111], a[32:35], a[68:71], v[20:23]
v_exp_f32_e32 v28, v89
Mv 108,36,76
Rb 20,1,58368
v_add_f32_e32 v14, v14, v42
Mv 104,36,72
buffer_load_dwordx4 v2, s[20:23], s56 offen offset:2048 sc1 lds
Mv 104,40,80
Rb 24,1,59392
v_add_f32_e32 v14, v14, v28
Mv 108,40,84
v_exp_f32_e32 v43, v90
Mv 108,44,92
Rb 28,1,60416
s_waitcnt lgkmcnt(8)
Mv 104,44,88
v_exp_f32_e32 v29, v91
Cv 42,42,28
Mv 104,48,96
Rb 32,1,53248
v_add_f32_e32 v14, v14, v43
Mv 108,48,100
v_exp_f32_e32 v44, v84
Mv 108,52,108
Rb 36,1,54272
v_add_f32_e32 v14, v14, v29
Mv 104,52,104
buffer_load_dwordx4 v2, s[20:23], s56 offen offset:3072 sc1 lds
Mv 104,56,112
Rb 40,1,55296
Cv 43,43,29
Mv 108,56,116
v_exp_f32_e32 v24, v85
Mv 108,60,124
Rb 44,1,56320
s_waitcnt lgkmcnt(8)
Mv 104,60,120
v_exp_f32_e32 v45, v86
v_add_f32_e32 v15, v15, v44
v_mfma_f32_16x16x32_bf16 v[112:115], a[0:3], a[64:67], v[16:19]
Rb 48,1,61440
v_add_f32_e32 v15, v15, v24
v_mfma_f32_16x16x32_bf16 v[116:119], a[0:3], a[68:71], v[20:23]
v_exp_f32_e32 v25, v87
Mv 116,4,76
Rb 52,1,62464
s_add_u32 m0, s36, 69632
Mv 112,4,72
buffer_load_dwordx4 v3, s[20:23], s56 offen offset:0 sc1 lds
Mv 112,8,80
Rb 56,1,63488
Cv 44,44,24
Mv 116,8,84
v_exp_f32_e32 v46, v92
Mv 116,12,92
Rb 60,1,64512
s_waitcnt lgkmcnt(8)
Mv 112,12,88
v_exp_f32_e32 v26, v93
)" R"(
v_add_f32_e32 v15, v15, v45
Mv 112,16,96
v_exp_f32_e32 v47, v94
Mv 116,16,100
v_exp_f32_e32 v27, v95
Mv 116,20,108
v_add_f32_e32 v15, v15, v25
Cv 45,45,25
Mv 112,20,104
buffer_load_dwordx4 v3, s[20:23], s56 offen offset:1024 sc1 lds
Mv 112,24,112
v_add_f32_e32 v15, v15, v46
v_add_f32_e32 v15, v15, v26
Mv 116,24,116
Cv 46,46,26
v_add_f32_e32 v15, v15, v47
Mv 116,28,124
v_add_f32_e32 v15, v15, v27
Cv 47,47,27
Mv 112,28,120
v_max_f32_e32 v11, v14, v15
s_waitcnt vmcnt(6) lgkmcnt(0)
s_barrier
v_mfma_f32_16x16x32_bf16 v[120:123], a[32:35], a[64:67], v[16:19]
Rb 0,8,32768
v_cmp_nge_f32_e32 vcc, s62, v11
v_mfma_f32_16x16x32_bf16 v[124:127], a[32:35], a[68:71], v[20:23]
Rb 4,8,33792
s_add_u32 s55, s55, 0x10000
Mv 124,36,76
Rb 8,8,34816
Mv 120,36,72
Rb 12,8,35840
buffer_load_dwordx4 v3, s[20:23], s56 offen offset:2048 sc1 lds
Mv 120,40,80
Rb 16,8,36864
Mv 124,40,84
Rb 20,8,37888
Mv 124,44,92
Rb 24,8,38912
Mv 120,44,88
Rb 28,8,39936
s_cmp_eq_u32 s57, 2
s_cselect_b32 s55, s59, s55
Mv 120,48,96
Rb 32,8,40960
Mv 124,48,100
Rb 36,8,41984
Mv 124,52,108
Rb 40,8,43008
Mv 120,52,104
Rb 44,8,44032
buffer_load_dwordx4 v3, s[20:23], s56 offen offset:3072 sc1 lds
Mv 120,56,112
v_readfirstlane_b32 s63, v9
Mv 124,56,116
s_add_u32 s56, s56, 0x10000
Mv 124,60,124
s_waitcnt lgkmcnt(8)
s_cbranch_vccnz .Lzfx12
.Lzfr12:
Mv 120,60,120
v_cmp_eq_u32_e32 vcc, s3, v4
s_andn2_b64 s[28:29], s[60:61], vcc
s_cbranch_scc1 .Lzsp22
)" R"(
.Lzok22:
Mp 128,0,32
Rb 48,8,45056
v_add_f32_e32 v12, v12, v14
Mp 136,0,36
v_add_f32_e32 v13, v13, v15
s_cmp_le_i32 s57, s58
s_cbranch_scc1 .Lzmk11
.Lzmr11:
Mp 140,4,36
Rb 52,8,46080
s_add_u32 m0, s36, 32768
buffer_load_dwordx4 v[64:67], v1, s[20:23], s66 offen sc0 sc1
Mp 132,4,32
buffer_load_dwordx4 v2, s[20:23], s55 offen offset:0 sc1 lds
Mp 144,8,32
Rb 56,8,47104
.LzH16:
s_cbranch_execz .LzHR
Mp 152,8,36
buffer_load_dwordx4 a[128:131], v2, s[20:23], s64 offen offset:0 sc0 sc1
Mp 156,12,36
Rb 60,8,48128
s_waitcnt lgkmcnt(8)
Mp 148,12,32
buffer_load_dwordx4 a[132:135], v2, s[20:23], s64 offen offset:1024 sc0 sc1
Mp 160,16,32
Rb 0,8,49152
Mp 168,16,36
buffer_load_dwordx4 a[136:139], v2, s[20:23], s64 offen offset:2048 sc0 sc1
Mp 172,20,36
Rb 4,8,50176
v_exp_f32_e32 v48, v96
Mp 164,20,32
buffer_load_dwordx4 v2, s[20:23], s55 offen offset:1024 sc1 lds
Mp 176,24,32
Rb 8,8,51200
v_exp_f32_e32 v28, v97
Mp 184,24,36
buffer_load_dwordx4 a[140:143], v2, s[20:23], s64 offen offset:3072 sc0 sc1
Mp 188,28,36
Rb 12,8,52224
v_add_f32_e32 v14, v48, v28
s_waitcnt lgkmcnt(8)
Mp 180,28,32
buffer_load_dwordx4 a[144:147], v3, s[20:23], s64 offen offset:0 sc0 sc1
Mp 192,32,32
Rb 16,8,53248
v_exp_f32_e32 v49, v98
Mp 200,32,36
buffer_load_dwordx4 a[148:151], v3, s[20:23], s64 offen offset:1024 sc0 sc1
Cv 48,48,28
Mp 204,36,36
Rb 20,8,54272
v_add_f32_e32 v14, v14, v49
Mp 196,36,32
buffer_load_dwordx4 v2, s[20:23], s55 offen offset:2048 sc1 lds
Mp 208,40,32
Rb 24,8,55296
Mp 216,40,36
buffer_load_dwordx4 a[152:155], v3, s[20:23], s64 offen offset:2048 sc0 sc1
Mp 220,44,36
Rb 28,8,56320
s_waitcnt lgkmcnt(8)
)" R"(
Mp 212,44,32
buffer_load_dwordx4 a[156:159], v3, s[20:23], s64 offen offset:3072 sc0 sc1
Mp 224,48,32
Rb 32,8,57344
Mp 232,48,36
s_add_u32 s64, s64, 0x10000
buffer_load_dwordx4 a[160:163], v2, s[20:23], s64 offen offset:0 sc0 sc1
Mp 236,52,36
Rb 36,8,58368
v_exp_f32_e32 v29, v99
Mp 228,52,32
buffer_load_dwordx4 v2, s[20:23], s55 offen offset:3072 sc1 lds
Mp 240,56,32
Rb 40,8,59392
v_exp_f32_e32 v50, v104
Mp 248,56,36
buffer_load_dwordx4 a[164:167], v2, s[20:23], s64 offen offset:1024 sc0 sc1
Mp 252,60,36
Rb 44,8,60416
v_add_f32_e32 v14, v14, v29
s_waitcnt lgkmcnt(8)
Mp 244,60,32
buffer_load_dwordx4 a[168:171], v2, s[20:23], s64 offen offset:2048 sc0 sc1
Mp 128,0,40
Rb 48,8,61440
v_exp_f32_e32 v24, v105
Mp 136,0,44
buffer_load_dwordx4 a[172:175], v2, s[20:23], s64 offen offset:3072 sc0 sc1
Cv 49,49,29
Mp 140,4,44
Rb 52,8,62464
s_add_u32 m0, s36, 36864
Mp 132,4,40
buffer_load_dwordx4 v3, s[20:23], s55 offen offset:0 sc1 lds
v_add_f32_e32 v14, v14, v50
Mp 144,8,40
Rb 56,8,63488
v_add_f32_e32 v14, v14, v24
Mp 152,8,44
buffer_load_dwordx4 a[176:179], v3, s[20:23], s64 offen offset:0 sc0 sc1
Cv 50,50,24
Mp 156,12,44
Rb 60,8,64512
s_waitcnt lgkmcnt(8)
Mp 148,12,40
buffer_load_dwordx4 a[180:183], v3, s[20:23], s64 offen offset:1024 sc0 sc1
Mp 160,16,40
v_exp_f32_e32 v51, v106
Mp 168,16,44
buffer_load_dwordx4 a[184:187], v3, s[20:23], s64 offen offset:2048 sc0 sc1
v_add_f32_e32 v14, v14, v51
Mp 172,20,44
v_exp_f32_e32 v25, v107
s_sub_u32 s57, s57, 1
Mp 164,20,40
buffer_load_dwordx4 v3, s[20:23], s55 offen offset:1024 sc1 lds
Mp 176,24,40
v_exp_f32_e32 v52, v100
v_add_f32_e32 v14, v14, v25
Mp 184,24,44
buffer_load_dwordx4 a[188:191], v3, s[20:23], s64 offen offset:3072 sc0 sc1
Mp 188,28,44
v_exp_f32_e32 v26, v101
Cv 51,51,25
)" R"(
Mp 180,28,40
s_waitcnt vmcnt(23) lgkmcnt(0)
s_barrier
Mp 192,32,40
Rb 0,8,0
v_exp_f32_e32 v53, v102
Mp 200,32,44
Rb 4,8,16384
v_exp_f32_e32 v27, v103
Mp 204,36,44
Rb 8,8,1024
v_add_f32_e32 v15, v52, v26
Mp 196,36,40
Rb 12,8,17408
buffer_load_dwordx4 v3, s[20:23], s55 offen offset:2048 sc1 lds
Mp 208,40,40
Rb 16,8,2048
Cv 52,52,26
Mp 216,40,44
Rb 20,8,18432
v_exp_f32_e32 v54, v108
Mp 220,44,44
Rb 24,8,3072
v_add_f32_e32 v15, v15, v53
Mp 212,44,40
Rb 28,8,19456
v_exp_f32_e32 v28, v109
Mp 224,48,40
Rb 32,8,4096
v_add_f32_e32 v15, v15, v27
Mp 232,48,44
Rb 36,8,20480
v_exp_f32_e32 v55, v110
Mp 236,52,44
Rb 40,8,5120
v_exp_f32_e32 v29, v111
Mp 228,52,40
Rb 44,8,21504
buffer_load_dwordx4 v3, s[20:23], s55 offen offset:3072 sc1 lds
Mp 240,56,40
Cv 53,53,27
v_add_f32_e32 v15, v15, v54
v_add_f32_e32 v15, v15, v28
Mp 248,56,44
Cv 54,54,28
v_add_f32_e32 v15, v15, v55
Mp 252,60,44
v_add_f32_e32 v15, v15, v29
Cv 55,55,29
Mp 244,60,40
s_waitcnt vmcnt(18)
v_cmp_neq_f32_e32 vcc, v16, v66
v_cmp_neq_f32_e64 s[28:29], v20, v67
s_or_b64 vcc, vcc, s[28:29]
s_cbranch_scc1 .LzMRG
.LzMRR:
Mv 128,194,128
v_exp_f32_e32 v56, v112
Mv 136,194,132
v_exp_f32_e32 v26, v113
v_add_f32_e32 v12, v12, v64
Mv 132,192,128
s_add_u32 m0, s36, 131072
s_add_u32 s40, s67, 0x20000
v_add_f32_e32 v13, v13, v65
)" R"(
Mv 140,192,132
buffer_load_dwordx4 v2, s[20:23], s40 offen offset:0 sc1 lds
Mv 144,194,136
v_exp_f32_e32 v57, v114
v_add_f32_e32 v14, v14, v56
Mv 152,194,140
v_exp_f32_e32 v27, v115
v_add_f32_e32 v14, v14, v26
Mv 148,192,136
Cv 56,56,26
s_waitcnt vmcnt(14)
Mv 156,192,140
buffer_load_dwordx4 v2, s[20:23], s40 offen offset:1024 sc1 lds
v_add_f32_e32 v14, v14, v57
Mv 160,194,144
v_exp_f32_e32 v58, v120
v_add_f32_e32 v14, v14, v27
Mv 168,194,148
v_exp_f32_e32 v28, v121
Mv 164,192,144
v_exp_f32_e32 v59, v122
Cv 57,57,27
Mv 172,192,148
buffer_load_dwordx4 v2, s[20:23], s40 offen offset:2048 sc1 lds
v_add_f32_e32 v14, v14, v58
Mv 176,194,152
v_exp_f32_e32 v29, v123
Mv 184,194,156
v_exp_f32_e32 v60, v116
v_add_f32_e32 v14, v14, v28
Mv 180,192,152
v_exp_f32_e32 v24, v117
s_waitcnt vmcnt(11)
Mv 188,192,156
buffer_load_dwordx4 v2, s[20:23], s40 offen offset:3072 sc1 lds
Cv 58,58,28
Mv 192,194,160
v_exp_f32_e32 v61, v118
Mv 200,194,164
v_exp_f32_e32 v25, v119
v_add_f32_e32 v14, v14, v59
Mv 196,192,160
s_add_u32 m0, s36, 135168
v_exp_f32_e32 v62, v124
Mv 204,192,164
buffer_load_dwordx4 v3, s[20:23], s40 offen offset:0 sc1 lds
Mv 208,194,168
v_exp_f32_e32 v26, v125
v_add_f32_e32 v14, v14, v29
Mv 216,194,172
v_exp_f32_e32 v63, v126
Cv 59,59,29
Mv 212,192,168
v_add_f32_e32 v15, v15, v60
s_waitcnt vmcnt(7)
Mv 220,192,172
buffer_load_dwordx4 v3, s[20:23], s40 offen offset:1024 sc1 lds
v_add_f32_e32 v15, v15, v24
Mv 224,194,176
v_exp_f32_e32 v27, v127
Cv 60,60,24
Mv 232,194,180
v_add_f32_e32 v15, v15, v61
v_add_f32_e32 v15, v15, v25
)" R"(
Mv 228,192,176
Cv 61,61,25
v_add_f32_e32 v15, v15, v62
v_add_f32_e32 v15, v15, v26
Mv 236,192,180
buffer_load_dwordx4 v3, s[20:23], s40 offen offset:2048 sc1 lds
Cv 62,62,26
Mv 240,194,184
v_add_f32_e32 v15, v15, v63
v_add_f32_e32 v15, v15, v27
Mv 248,194,188
s_add_u32 s40, s67, 0x30000
Cv 63,63,27
v_max_f32_e32 v11, v14, v15
Mv 244,192,184
s_add_u32 m0, s36, 98304
v_cmp_nge_f32_e32 vcc, s62, v11
s_waitcnt lgkmcnt(8)
Mv 252,192,188
buffer_load_dwordx4 v2, s[20:23], s40 offen offset:0 sc1 lds
s_cbranch_vccnz .Lzfx13
.Lzfr13:
Mp 128,0,48
Rb 48,8,6144
v_add_f32_e32 v12, v12, v14
Mp 136,0,52
v_add_f32_e32 v13, v13, v15
v_mov_b32 v24, v12
v_mov_b32 v25, v13
Mp 136,4,60
Rb 52,8,22528
v_permlane32_swap_b32_e32 v12, v24
v_add_f32_e32 v12, v12, v24
Mp 128,4,56
buffer_load_dwordx4 v2, s[20:23], s40 offen offset:1024 sc1 lds
v_mov_b32 v24, v12
Mp 132,8,48
Rb 56,8,7168
v_permlane16_swap_b32_e32 v12, v24
Mp 140,8,52
v_add_f32_e32 v12, v12, v24
v_rcp_f32_e32 v12, v12
Mp 140,12,60
Rb 60,8,23552
v_permlane32_swap_b32_e32 v13, v25
s_waitcnt lgkmcnt(8)
Mp 132,12,56
buffer_load_dwordx4 v2, s[20:23], s40 offen offset:2048 sc1 lds
v_add_f32_e32 v13, v13, v25
Mp 144,16,48
Rb 0,8,8192
v_mov_b32 v25, v13
Mp 152,16,52
s_nop 0
v_permlane16_swap_b32_e32 v13, v25
v_add_f32_e32 v13, v13, v25
Mp 152,20,60
Rb 4,8,24576
v_rcp_f32_e32 v13, v13
Mp 144,20,56
buffer_load_dwordx4 v2, s[20:23], s40 offen offset:3072 sc1 lds
v_mul_f32_e32 v128, v12, v128
Mp 148,24,48
Rb 8,8,9216
)" R"(
v_mul_f32_e32 v129, v12, v129
Mp 156,24,52
s_add_u32 m0, s36, 102400
Cv 128,128,129
v_mul_f32_e32 v130, v12, v130
Mp 156,28,60
Rb 12,8,25600
v_mul_f32_e32 v131, v12, v131
s_waitcnt lgkmcnt(8)
Mp 148,28,56
buffer_load_dwordx4 v3, s[20:23], s40 offen offset:0 sc1 lds
Cv 129,130,131
Mp 160,32,48
Rb 16,8,10240
v_mul_f32_e32 v132, v12, v132
Mp 168,32,52
v_mul_f32_e32 v133, v12, v133
Cv 130,132,133
v_mul_f32_e32 v134, v12, v134
Mp 168,36,60
Rb 20,8,26624
v_mul_f32_e32 v135, v12, v135
Cv 131,134,135
Mp 160,36,56
buffer_load_dwordx4 v3, s[20:23], s40 offen offset:1024 sc1 lds
v_mul_f32_e32 v136, v13, v136
Mp 164,40,48
Rb 24,8,11264
v_mul_f32_e32 v137, v13, v137
Mp 172,40,52
Cv 136,136,137
v_mul_f32_e32 v138, v13, v138
v_mul_f32_e32 v139, v13, v139
Mp 172,44,60
Rb 28,8,27648
Cv 137,138,139
s_waitcnt lgkmcnt(8)
Mp 164,44,56
buffer_load_dwordx4 v3, s[20:23], s40 offen offset:2048 sc1 lds
v_mul_f32_e32 v140, v13, v140
Mp 176,48,48
Rb 32,8,12288
v_mul_f32_e32 v141, v13, v141
Mp 184,48,52
Cv 138,140,141
v_mul_f32_e32 v142, v13, v142
v_mul_f32_e32 v143, v13, v143
Mp 184,52,60
Rb 36,8,28672
Cv 139,142,143
v_mul_f32_e32 v144, v12, v144
Mp 176,52,56
v_mul_f32_e32 v145, v12, v145
Cv 144,144,145
v_mul_f32_e32 v146, v12, v146
Mp 180,56,48
Rb 40,8,13312
v_mul_f32_e32 v147, v12, v147
Mp 188,56,52
Cv 145,146,147
v_mul_f32_e32 v148, v12, v148
v_mul_f32_e32 v149, v12, v149
Mp 188,60,60
Rb 44,8,29696
)" R"(
Cv 146,148,149
s_waitcnt lgkmcnt(8)
Mp 180,60,56
v_mul_f32_e32 v150, v12, v150
v_mul_f32_e32 v151, v12, v151
Cv 147,150,151
Mp 192,0,48
Rb 48,8,14336
v_mul_f32_e32 v152, v13, v152
Mp 200,0,52
v_mul_f32_e32 v153, v13, v153
Cv 152,152,153
v_mul_f32_e32 v154, v13, v154
Mp 200,4,60
Rb 52,8,30720
v_mul_f32_e32 v155, v13, v155
Cv 153,154,155
Mp 192,4,56
v_mul_f32_e32 v156, v13, v156
v_mul_f32_e32 v157, v13, v157
Cv 154,156,157
Mp 196,8,48
Rb 56,8,15360
v_mul_f32_e32 v158, v13, v158
Mp 204,8,52
v_mul_f32_e32 v159, v13, v159
Cv 155,158,159
v_mul_f32_e32 v160, v12, v160
Mp 204,12,60
Rb 60,8,31744
v_mul_f32_e32 v161, v12, v161
s_waitcnt lgkmcnt(8)
Mp 196,12,56
Cv 160,160,161
v_mul_f32_e32 v162, v12, v162
v_mul_f32_e32 v163, v12, v163
Mp 208,16,48
Rb 0,1,0
Cv 161,162,163
Mp 216,16,52
v_mul_f32_e32 v164, v12, v164
v_mul_f32_e32 v165, v12, v165
Cv 162,164,165
Mp 216,20,60
Rb 4,1,1024
v_mul_f32_e32 v166, v12, v166
v_mul_f32_e32 v167, v12, v167
Mp 208,20,56
Cv 163,166,167
v_mul_f32_e32 v168, v13, v168
v_mul_f32_e32 v169, v13, v169
Mp 212,24,48
Rb 8,1,2048
Cv 168,168,169
Mp 220,24,52
v_mul_f32_e32 v170, v13, v170
v_mul_f32_e32 v171, v13, v171
Cv 169,170,171
Mp 220,28,60
Rb 12,1,3072
s_waitcnt vmcnt(14) lgkmcnt(4)
s_barrier
Mp 212,28,56
s_add_u32 s40, s67, 0x40000
v_mul_f32_e32 v172, v13, v172
)" R"(
v_mul_f32_e32 v173, v13, v173
Mp 224,32,48
Rb 16,1,4096
s_add_u32 m0, s36, 65536
Mp 232,32,52
buffer_load_dwordx4 v2, s[20:23], s40 offen offset:0 sc1 lds
Cv 170,172,173
Mp 232,36,60
Rb 20,1,5120
v_mul_f32_e32 v174, v13, v174
v_mul_f32_e32 v175, v13, v175
Mp 224,36,56
Cv 171,174,175
v_mul_f32_e32 v176, v12, v176
v_mul_f32_e32 v177, v12, v177
Mp 228,40,48
Rb 24,1,6144
Cv 176,176,177
s_lshl_b32 s64, s35, 17
Mp 236,40,52
v_mul_f32_e32 v178, v12, v178
v_mul_f32_e32 v179, v12, v179
Cv 177,178,179
Mp 236,44,60
Rb 28,1,8192
v_mul_f32_e32 v180, v12, v180
Mp 228,44,56
buffer_load_dwordx4 v2, s[20:23], s40 offen offset:1024 sc1 lds
v_mul_f32_e32 v181, v12, v181
Mp 240,48,48
Rb 32,1,9216
Cv 178,180,181
s_add_u32 s64, s64, s89
Mp 248,48,52
v_mul_f32_e32 v182, v12, v182
v_mul_f32_e32 v183, v12, v183
Cv 179,182,183
Mp 248,52,60
Rb 36,1,10240
v_mul_f32_e32 v184, v13, v184
v_mul_f32_e32 v185, v13, v185
Mp 240,52,56
Cv 184,184,185
v_mul_f32_e32 v186, v13, v186
v_mul_f32_e32 v187, v13, v187
Mp 244,56,48
Rb 40,1,11264
Cv 185,186,187
s_add_u32 s64, s64, 0x900000
Mp 252,56,52
buffer_load_dwordx4 v2, s[20:23], s40 offen offset:2048 sc1 lds
v_mul_f32_e32 v188, v13, v188
Mp 252,60,60
Rb 44,1,12288
v_mul_f32_e32 v189, v13, v189
s_mov_b32 s55, 0x1508c00
s_waitcnt lgkmcnt(8)
Mp 244,60,56
Cv 186,188,189
v_mul_f32_e32 v190, v13, v190
v_mul_f32_e32 v191, v13, v191
v_mfma_f32_16x16x32_bf16 v[32:35], a[0:3], v[128:131], 0
Rb 48,1,13312
Cv 187,190,191
)" R"(
s_xor_b32 s66, s64, 0x202000
v_mfma_f32_16x16x32_bf16 v[40:43], a[0:3], v[136:139], 0
v_mul_f32_e32 v192, v12, v192
v_mul_f32_e32 v193, v12, v193
Cv 192,192,193
v_mfma_f32_16x16x32_bf16 v[44:47], a[4:7], v[136:139], 0
Rb 52,1,14336
v_mul_f32_e32 v194, v12, v194
v_mul_f32_e32 v195, v12, v195
v_mfma_f32_16x16x32_bf16 v[36:39], a[4:7], v[128:131], 0
buffer_load_dwordx4 v2, s[20:23], s40 offen offset:3072 sc1 lds
Cv 193,194,195
v_mfma_f32_16x16x32_bf16 v[48:51], a[8:11], v[128:131], 0
Rb 56,1,16384
v_mul_f32_e32 v196, v12, v196
s_sub_u32 s66, s66, s84
v_mfma_f32_16x16x32_bf16 v[56:59], a[8:11], v[136:139], 0
v_mul_f32_e32 v197, v12, v197
Cv 194,196,197
v_mul_f32_e32 v198, v12, v198
v_mfma_f32_16x16x32_bf16 v[60:63], a[12:15], v[136:139], 0
Rb 60,1,17408
v_mul_f32_e32 v199, v12, v199
s_cmp_lg_u32 s33, 0
s_cmov_b32 s55, 0x40000000
s_waitcnt lgkmcnt(8)
v_mfma_f32_16x16x32_bf16 v[52:55], a[12:15], v[128:131], 0
Cv 195,198,199
v_mul_f32_e32 v200, v13, v200
v_mul_f32_e32 v201, v13, v201
v_mov_b32 v16, 1
Mp 32,16,144
Rb 0,1,18432
s_add_u32 m0, s36, 69632
s_sub_u32 s66, s66, s84
v_add_u32 v9, s36, v8
Mp 40,16,152
buffer_load_dwordx4 v3, s[20:23], s40 offen offset:0 sc1 lds
Cv 200,200,201
Mp 44,20,152
Rb 4,1,19456
v_mul_f32_e32 v202, v13, v202
v_mul_f32_e32 v203, v13, v203
Mp 36,20,144
Cv 201,202,203
v_mul_f32_e32 v204, v13, v204
v_mul_f32_e32 v205, v13, v205
Mp 48,24,144
Rb 8,1,20480
Cv 202,204,205
s_lshl2_add_u32 s65, s35, s33
Mp 56,24,152
v_mul_f32_e32 v206, v13, v206
v_mul_f32_e32 v207, v13, v207
Cv 203,206,207
Mp 60,28,152
Rb 12,1,21504
v_mul_f32_e32 v208, v12, v208
s_waitcnt lgkmcnt(8)
Mp 52,28,144
buffer_load_dwordx4 v3, s[20:23], s40 offen offset:1024 sc1 lds
v_mul_f32_e32 v209, v12, v209
Mp 32,32,160
Rb 16,1,22528
)" R"(
Cv 208,208,209
s_lshl_b32 s65, s65, 8
Mp 40,32,168
v_mul_f32_e32 v210, v12, v210
v_mul_f32_e32 v211, v12, v211
Cv 209,210,211
Mp 44,36,168
Rb 20,1,24576
v_mul_f32_e32 v212, v12, v212
v_mul_f32_e32 v213, v12, v213
Mp 36,36,160
Cv 210,212,213
v_mul_f32_e32 v214, v12, v214
v_mul_f32_e32 v215, v12, v215
Mp 48,40,160
Rb 24,1,25600
Cv 211,214,215
s_add_u32 s65, s65, s50
Mp 56,40,168
buffer_load_dwordx4 v3, s[20:23], s40 offen offset:2048 sc1 lds
v_mul_f32_e32 v216, v13, v216
Mp 60,44,168
Rb 28,1,26624
v_mul_f32_e32 v217, v13, v217
s_waitcnt lgkmcnt(8)
Mp 52,44,160
Cv 216,216,217
v_mul_f32_e32 v218, v13, v218
v_mul_f32_e32 v219, v13, v219
Mp 32,48,176
Rb 32,1,27648
Cv 217,218,219
s_add_u32 s65, s65, 0x6400
Mp 40,48,184
v_mul_f32_e32 v220, v13, v220
v_mul_f32_e32 v221, v13, v221
Cv 218,220,221
Mp 44,52,184
Rb 36,1,28672
v_mul_f32_e32 v222, v13, v222
v_mul_f32_e32 v223, v13, v223
Mp 36,52,176
Cv 219,222,223
v_mul_f32_e32 v224, v12, v224
v_mul_f32_e32 v225, v12, v225
Mp 48,56,176
Rb 40,1,29696
Cv 224,224,225
s_xor_b32 s52, s65, 0x202000
Mp 56,56,184
v_mul_f32_e32 v226, v12, v226
v_mul_f32_e32 v227, v12, v227
Cv 225,226,227
Mp 60,60,184
Rb 44,1,30720
v_mul_f32_e32 v228, v12, v228
s_waitcnt lgkmcnt(8)
Mp 52,60,176
v_mul_f32_e32 v229, v12, v229
Cv 226,228,229
v_mul_f32_e32 v230, v12, v230
Mp 32,0,192
Rb 48,1,32768
v_mul_f32_e32 v231, v12, v231
)" R"(
buffer_atomic_add v16, v7, s[20:23], s55 offen sc0 sc1
Mp 40,0,200
Cv 227,230,231
v_mul_f32_e32 v232, v13, v232
v_mul_f32_e32 v233, v13, v233
.LzH17:
s_cbranch_execz .LzHR
Mp 44,4,200
Rb 52,1,33792
Cv 232,232,233
v_mul_f32_e32 v234, v13, v234
Mp 36,4,192
v_mul_f32_e32 v235, v13, v235
Cv 233,234,235
v_mul_f32_e32 v236, v13, v236
Mp 48,8,192
Rb 56,1,34816
v_mul_f32_e32 v237, v13, v237
Mp 56,8,200
Cv 234,236,237
v_mul_f32_e32 v238, v13, v238
v_mul_f32_e32 v239, v13, v239
Mp 60,12,200
Rb 60,1,35840
s_waitcnt vmcnt(15) lgkmcnt(4)
s_barrier
Mp 52,12,192
s_add_u32 s40, s67, 0x50000
Cv 235,238,239
v_mul_f32_e32 v240, v12, v240
Mp 32,16,208
Rb 0,1,36864
s_add_u32 m0, s36, 0
Mp 40,16,216
buffer_load_dwordx4 v2, s[20:23], s40 offen offset:0 sc1 lds
v_mul_f32_e32 v241, v12, v241
Mp 44,20,216
Rb 4,1,37888
Cv 240,240,241
v_mul_f32_e32 v242, v12, v242
Mp 36,20,208
v_mul_f32_e32 v243, v12, v243
Cv 241,242,243
v_mul_f32_e32 v244, v12, v244
Mp 48,24,208
Rb 8,1,38912
v_mul_f32_e32 v245, v12, v245
Mp 56,24,216
Cv 242,244,245
v_mul_f32_e32 v246, v12, v246
v_mul_f32_e32 v247, v12, v247
Mp 60,28,216
Rb 12,1,40960
Cv 243,246,247
Mp 52,28,208
buffer_load_dwordx4 v2, s[20:23], s40 offen offset:1024 sc1 lds
v_mul_f32_e32 v248, v13, v248
Mp 32,32,224
Rb 16,1,41984
v_mul_f32_e32 v249, v13, v249
Mp 40,32,232
Cv 248,248,249
v_mul_f32_e32 v250, v13, v250
v_mul_f32_e32 v251, v13, v251
Mp 44,36,232
)" R"(
Rb 20,1,43008
Cv 249,250,251
v_mul_f32_e32 v252, v13, v252
Mp 36,36,224
v_mul_f32_e32 v253, v13, v253
Cv 250,252,253
v_mul_f32_e32 v254, v13, v254
Mp 48,40,224
Rb 24,1,44032
v_mul_f32_e32 v255, v13, v255
Mp 56,40,232
buffer_load_dwordx4 v2, s[20:23], s40 offen offset:2048 sc1 lds
Cv 251,254,255
Mp 60,44,232
Rb 28,1,45056
s_waitcnt lgkmcnt(8)
Mp 52,44,224
Mp 32,48,240
Rb 32,1,46080
Mp 40,48,248
Mp 44,52,248
Rb 36,1,47104
Mp 36,52,240
buffer_load_dwordx4 v2, s[20:23], s40 offen offset:3072 sc1 lds
Mp 48,56,240
Rb 40,1,49152
Mp 56,56,248
Mp 60,60,248
Rb 44,1,50176
s_waitcnt lgkmcnt(8)
Mp 52,60,240
v_mfma_f32_16x16x32_bf16 v[64:67], a[0:3], v[128:131], 0
Rb 48,1,51200
s_add_u32 m0, s36, 4096
v_mfma_f32_16x16x32_bf16 v[72:75], a[0:3], v[136:139], 0
buffer_load_dwordx4 v3, s[20:23], s40 offen offset:0 sc1 lds
Mp 72,4,152
Rb 52,1,52224
Mp 64,4,144
Cv 32,32,33
Cv 33,34,35
Mp 64,8,160
Rb 56,1,53248
Cv 34,36,37
Mp 72,8,168
Cv 35,38,39
Mp 72,12,184
Rb 60,1,54272
s_waitcnt lgkmcnt(8)
Mp 64,12,176
buffer_load_dwordx4 v3, s[20:23], s40 offen offset:1024 sc1 lds
Mp 64,16,192
Rb 0,1,55296
Mp 72,16,200
buffer_store_dwordx4 v[32:35], v2, s[20:23], s64 offen offset:0 sc0 sc1
Mp 72,20,216
Rb 4,1,57344
Mp 64,20,208
Mp 64,24,224
Rb 8,1,58368
Mp 72,24,232
buffer_load_dwordx4 v3, s[20:23], s40 offen offset:2048 sc1 lds
Mp 72,28,248
Rb 12,1,59392
)" R"(
s_waitcnt lgkmcnt(8)
Mp 64,28,240
Cv 40,40,41
Cv 41,42,43
v_mfma_f32_16x16x32_bf16 v[68:71], a[32:35], v[128:131], 0
Rb 16,1,60416
Cv 42,44,45
v_mfma_f32_16x16x32_bf16 v[76:79], a[32:35], v[136:139], 0
Cv 43,46,47
Mp 76,36,152
Rb 20,1,61440
Mp 68,36,144
buffer_store_dwordx4 v[40:43], v2, s[20:23], s64 offen offset:1024 sc0 sc1
Mp 68,40,160
Rb 24,1,62464
Mp 76,40,168
Mp 76,44,184
Rb 28,1,63488
s_waitcnt lgkmcnt(8)
Mp 68,44,176
Mp 68,48,192
Rb 32,10,0
Mp 76,48,200
Cv 48,48,49
Cv 49,50,51
Mp 76,52,216
Rb 36,10,1024
Cv 50,52,53
Mp 68,52,208
Cv 51,54,55
Mp 68,56,224
Rb 40,10,2048
Mp 76,56,232
buffer_store_dwordx4 v[48:51], v2, s[20:23], s64 offen offset:2048 sc0 sc1
Mp 76,60,248
Rb 44,10,3072
s_waitcnt vmcnt(18) lgkmcnt(4)
s_barrier
Mp 68,60,240
s_add_u32 s40, s67, 0x60000
v_mfma_f32_16x16x32_bf16 v[80:83], a[0:3], v[128:131], 0
Rb 48,10,4096
s_add_u32 m0, s36, 32768
v_mfma_f32_16x16x32_bf16 v[88:91], a[0:3], v[136:139], 0
buffer_load_dwordx4 v2, s[20:23], s40 offen offset:0 sc1 lds
Mp 88,4,152
Rb 52,10,5120
Mp 80,4,144
Cv 56,56,57
Cv 57,58,59
Mp 80,8,160
Rb 56,10,6144
Cv 58,60,61
Mp 88,8,168
Cv 59,62,63
Mp 88,12,184
Rb 60,10,8192
Mp 80,12,176
buffer_load_dwordx4 v2, s[20:23], s40 offen offset:1024 sc1 lds
Mp 80,16,192
Rb 0,10,9216
Mp 88,16,200
buffer_store_dwordx4 v[56:59], v2, s[20:23], s64 offen offset:3072 sc0 sc1
Mp 88,20,216
Rb 4,10,10240
)" R"(
Mp 80,20,208
Mp 80,24,224
Rb 8,10,11264
Mp 88,24,232
buffer_load_dwordx4 v2, s[20:23], s40 offen offset:2048 sc1 lds
Mp 88,28,248
Rb 12,10,12288
s_waitcnt lgkmcnt(8)
Mp 80,28,240
Cv 64,64,65
Cv 65,66,67
v_mfma_f32_16x16x32_bf16 v[84:87], a[32:35], v[128:131], 0
Rb 16,10,13312
Cv 66,68,69
v_mfma_f32_16x16x32_bf16 v[92:95], a[32:35], v[136:139], 0
Cv 67,70,71
Mp 92,36,152
Rb 20,10,14336
Mp 84,36,144
buffer_load_dwordx4 v2, s[20:23], s40 offen offset:3072 sc1 lds
Mp 84,40,160
Rb 24,10,16384
Mp 92,40,168
buffer_store_dwordx4 v[64:67], v3, s[20:23], s64 offen offset:0 sc0 sc1
Mp 92,44,184
Rb 28,10,17408
s_waitcnt lgkmcnt(8)
Mp 84,44,176
Mp 84,48,192
Rb 32,10,18432
s_add_u32 m0, s36, 36864
Mp 92,48,200
buffer_load_dwordx4 v3, s[20:23], s40 offen offset:0 sc1 lds
Mp 92,52,216
Rb 36,10,19456
Mp 84,52,208
Cv 72,72,73
Cv 73,74,75
Mp 84,56,224
Rb 40,10,20480
Cv 74,76,77
Mp 92,56,232
Cv 75,78,79
Mp 92,60,248
Rb 44,10,21504
s_waitcnt lgkmcnt(8)
Mp 84,60,240
buffer_load_dwordx4 v3, s[20:23], s40 offen offset:1024 sc1 lds
v_mfma_f32_16x16x32_bf16 v[96:99], a[0:3], v[128:131], 0
Rb 48,10,22528
v_mfma_f32_16x16x32_bf16 v[104:107], a[0:3], v[136:139], 0
buffer_store_dwordx4 v[72:75], v3, s[20:23], s64 offen offset:1024 sc0 sc1
Mp 104,4,152
Rb 52,10,24576
Mp 96,4,144
Mp 96,8,160
Rb 56,10,25600
Mp 104,8,168
buffer_load_dwordx4 v3, s[20:23], s40 offen offset:2048 sc1 lds
Mp 104,12,184
Rb 60,10,26624
s_waitcnt lgkmcnt(8)
Mp 96,12,176
Cv 80,80,81
)" R"(
Cv 81,82,83
Mp 96,16,192
Rb 0,10,27648
Cv 82,84,85
Mp 104,16,200
Cv 83,86,87
Mp 104,20,216
Rb 4,10,28672
Mp 96,20,208
buffer_store_dwordx4 v[80:83], v3, s[20:23], s64 offen offset:2048 sc0 sc1
Mp 96,24,224
Rb 8,10,29696
Mp 104,24,232
Mp 104,28,248
Rb 12,10,30720
s_waitcnt lgkmcnt(8)
Mp 96,28,240
v_mfma_f32_16x16x32_bf16 v[100:103], a[32:35], v[128:131], 0
Rb 16,8,32768
v_mfma_f32_16x16x32_bf16 v[108:111], a[32:35], v[136:139], 0
Cv 88,88,89
Cv 89,90,91
Mp 108,36,152
Rb 20,8,33792
Cv 90,92,93
Mp 100,36,144
Cv 91,94,95
Mp 100,40,160
Rb 24,8,34816
Mp 108,40,168
buffer_store_dwordx4 v[88:91], v3, s[20:23], s64 offen offset:3072 sc0 sc1
Mp 108,44,184
Rb 28,8,35840
s_waitcnt vmcnt(23) lgkmcnt(4)
s_barrier
Mp 100,44,176
s_add_u32 s40, s67, 0x70000
Mp 100,48,192
Rb 32,8,36864
s_add_u32 m0, s36, 131072
Mp 108,48,200
buffer_load_dwordx4 v2, s[20:23], s40 offen offset:0 sc1 lds
Mp 108,52,216
Rb 36,8,37888
s_mov_b32 s8, s74
Mp 100,52,208
s_mov_b32 s9, s75
s_mov_b32 s10, 0x1c0000
Mp 100,56,224
Rb 40,8,38912
s_mov_b32 s11, 0x20000
Mp 108,56,232
v_and_b32 v174, 1, v0
v_mul_u32_u24 v174, 0xffff, v174
Mp 108,60,248
Rb 44,8,40960
Mp 100,60,240
buffer_load_dwordx4 v2, s[20:23], s40 offen offset:1024 sc1 lds
v_mfma_f32_16x16x32_bf16 v[112:115], a[0:3], v[128:131], 0
Rb 48,8,41984
v_add_u32 v174, 1, v174
v_mfma_f32_16x16x32_bf16 v[120:123], a[0:3], v[136:139], 0
v_mul_u32_u24 v174, 0x3f80, v174
v_bfe_u32 v173, v0, 1, 3
Mp 120,4,152
)" R"(
Rb 52,8,43008
v_cmp_eq_u32_e32 vcc, 0, v173
Mp 112,4,144
Cv 96,96,97
Cv 97,98,99
Mp 112,8,160
Rb 56,8,44032
Cv 98,100,101
Mp 120,8,168
buffer_load_dwordx4 v2, s[20:23], s40 offen offset:2048 sc1 lds
Mp 120,12,184
Rb 60,8,45056
s_waitcnt lgkmcnt(8)
Mp 112,12,176
Cv 99,102,103
v_cndmask_b32_e32 v132, 0, v174, vcc
v_cmp_eq_u32_e32 vcc, 1, v173
Mp 112,16,192
Rb 0,8,46080
v_cndmask_b32_e32 v133, 0, v174, vcc
Mp 120,16,200
s_add_u32 s64, s64, 0x10000
buffer_store_dwordx4 v[96:99], v2, s[20:23], s64 offen offset:0 sc0 sc1
Mp 120,20,216
Rb 4,8,47104
v_cmp_eq_u32_e32 vcc, 2, v173
Mp 112,20,208
buffer_load_dwordx4 v2, s[20:23], s40 offen offset:3072 sc1 lds
Mp 112,24,224
Rb 8,8,49152
v_cndmask_b32_e32 v134, 0, v174, vcc
Mp 120,24,232
v_cmp_eq_u32_e32 vcc, 3, v173
v_and_b32 v167, 0xf0, v1
Mp 120,28,248
Rb 12,8,50176
s_waitcnt lgkmcnt(8)
Mp 112,28,240
Cv 104,104,105
Cv 105,106,107
v_mfma_f32_16x16x32_bf16 v[116:119], a[32:35], v[128:131], 0
Rb 16,8,51200
s_add_u32 m0, s36, 135168
v_mfma_f32_16x16x32_bf16 v[124:127], a[32:35], v[136:139], 0
buffer_load_dwordx4 v3, s[20:23], s40 offen offset:0 sc1 lds
Mp 124,36,152
Rb 20,8,52224
Cv 106,108,109
Mp 116,36,144
Cv 107,110,111
v_cndmask_b32_e32 v135, 0, v174, vcc
Mp 116,40,160
Rb 24,8,53248
v_cmp_eq_u32_e32 vcc, 4, v173
Mp 124,40,168
buffer_store_dwordx4 v[104:107], v2, s[20:23], s64 offen offset:1024 sc0 sc1
Mp 124,44,184
Rb 28,8,54272
s_waitcnt lgkmcnt(8)
Mp 116,44,176
buffer_load_dwordx4 v3, s[20:23], s40 offen offset:1024 sc1 lds
v_cndmask_b32_e32 v140, 0, v174, vcc
Mp 116,48,192
Rb 32,8,55296
)" R"(
v_cmp_eq_u32_e32 vcc, 5, v173
Mp 124,48,200
s_nop 0
v_cndmask_b32_e32 v141, 0, v174, vcc
v_cmp_eq_u32_e32 vcc, 6, v173
Mp 124,52,216
Rb 36,8,57344
v_cndmask_b32_e32 v142, 0, v174, vcc
Mp 116,52,208
v_cmp_eq_u32_e32 vcc, 7, v173
v_lshrrev_b32 v173, 4, v0
Mp 116,56,224
Rb 40,8,58368
v_cndmask_b32_e32 v143, 0, v174, vcc
Mp 124,56,232
buffer_load_dwordx4 v3, s[20:23], s40 offen offset:2048 sc1 lds
Mp 124,60,248
Rb 44,8,59392
s_waitcnt vmcnt(9) lgkmcnt(8)
buffer_store_dword v6, v7, s[20:23], s65 offen offset:0 sc0 sc1
Mp 116,60,240
v_lshl_or_b32 v164, v173, 25, v167
v_mul_u32_u24 v166, 56, v167
v_mfma_f32_16x16x32_bf16 v[32:35], a[0:3], v[128:131], 0
Rb 48,8,60416
v_lshl_add_u32 v166, v173, 3, v166
v_mfma_f32_16x16x32_bf16 v[40:43], a[0:3], v[136:139], 0
v_lshrrev_b32_e64 v173, v173, s85
v_bfe_u32 v174, v173, 4, 1
Mp 40,4,152
Rb 52,8,61440
v_lshl_or_b32 v183, v174, 13, v164
Mp 32,4,144
Cv 112,112,113
Cv 113,114,115
Mp 32,8,160
Rb 56,8,62464
Cv 114,116,117
Mp 40,8,168
Cv 115,118,119
v_and_b32 v174, 1, v173
Mp 40,12,184
Rb 60,8,63488
s_waitcnt lgkmcnt(8)
Mp 32,12,176
buffer_store_dwordx4 v[112:115], v2, s[20:23], s64 offen offset:2048 sc0 sc1
ds_write_b32 v9, v16 offset:39936
Mp 32,16,192
Rb 0,8,0
v_lshl_or_b32 v164, v174, 13, v164
Mp 40,16,200
v_and_b32 v174, 7, v0
v_and_b32 v165, 0x80, v1
Mp 40,20,216
Rb 4,8,1024
v_lshl_or_b32 v165, v174, 8, v165
Mp 32,20,208
v_lshlrev_b32 v165, 1, v165
s_mov_b32 s93, 0x1111
Mp 32,24,224
Rb 8,8,2048
v_lshrrev_b32 v173, 2, v0
Mp 40,24,232
Cv 120,120,121
)" R"(
Cv 121,122,123
Mp 40,28,248
Rb 12,8,3072
s_waitcnt lgkmcnt(3)
s_barrier
Mp 32,28,240
Cv 122,124,125
Cv 123,126,127
ds_read_b32 v16, v8 offset:39936
v_mfma_f32_16x16x32_bf16 v[36:39], a[32:35], v[128:131], 0
Rb 16,8,4096
v_mul_u32_u24 v173, 832, v173
v_mfma_f32_16x16x32_bf16 v[44:47], a[32:35], v[136:139], 0
buffer_store_dwordx4 v[120:123], v2, s[20:23], s64 offen offset:3072 sc0 sc1
Mp 44,36,152
Rb 20,8,5120
v_add_u32 v173, v173, v1
Mp 36,36,144
v_lshrrev_b32 v174, 1, v0
v_and_b32 v174, 24, v174
Mp 36,40,160
Rb 24,8,6144
v_lshl_add_u32 v174, v167, 2, v174
Mp 44,40,168
s_add_u32 s59, s36, 98304
v_add_u32 v174, s59, v174
Mp 44,44,184
Rb 28,8,8192
s_waitcnt lgkmcnt(4)
Mp 36,44,176
v_readfirstlane_b32 s82, v16
Mp 36,48,192
Rb 32,8,9216
s_lshl2_add_u32 s56, s82, s33
Mp 44,48,200
s_mul_i32 s88, s56, 4682
s_lshr_b32 s58, s88, 16
Mp 44,52,216
Rb 36,8,10240
s_mul_i32 s57, s58, 14
Mp 36,52,208
s_sub_u32 s57, s56, s57
s_lshl_b32 s40, s58, 12
Mp 36,56,224
Rb 40,8,11264
Mp 44,56,232
s_cmp_gt_u32 s57, 6
s_cselect_b32 s88, 128, 0
Mp 44,60,248
Rb 44,8,12288
Mp 36,60,240
buffer_load_dword v4, v7, s[20:23], s52 offen offset:0 sc0 sc1
s_add_u32 s40, s40, s88
v_mfma_f32_16x16x32_bf16 v[48:51], a[0:3], v[128:131], 0
Rb 48,8,13312
s_add_u32 s40, s40, 0x1510000
v_mfma_f32_16x16x32_bf16 v[56:59], a[0:3], v[136:139], 0
s_lshl_b32 s55, s58, 6
s_add_u32 s55, s55, s57
Mp 56,4,152
Rb 52,8,14336
Mp 48,4,144
Cv 32,32,33
Cv 33,34,35
Mp 48,8,160
)" R"(
Rb 56,8,16384
Cv 34,36,37
Mp 56,8,168
Cv 35,38,39
Mp 56,12,184
Rb 60,8,17408
s_waitcnt lgkmcnt(8)
Mp 48,12,176
buffer_store_dwordx4 v[32:35], v3, s[20:23], s64 offen offset:0 sc0 sc1
Mp 48,16,192
Rb 0,8,18432
Mp 56,16,200
s_cmp_gt_u32 s57, 6
s_cselect_b32 s88, 9, 0
Mp 56,20,216
Rb 4,8,19456
s_add_u32 s55, s55, s88
Mp 48,20,208
s_and_b32 s88, s58, 32
s_lshl_b32 s88, s88, 8
Mp 48,24,224
Rb 8,8,20480
v_xor_b32 v164, s88, v164
Mp 56,24,232
Cv 40,40,41
Cv 41,42,43
Mp 56,28,248
Rb 12,8,21504
s_waitcnt lgkmcnt(8)
Mp 48,28,240
Cv 42,44,45
Cv 43,46,47
v_xor_b32 v183, s88, v183
v_mfma_f32_16x16x32_bf16 v[52:55], a[32:35], v[128:131], 0
Rb 16,8,22528
s_lshl_b32 s55, s55, 10
v_mfma_f32_16x16x32_bf16 v[60:63], a[32:35], v[136:139], 0
buffer_store_dwordx4 v[40:43], v3, s[20:23], s64 offen offset:1024 sc0 sc1
Mp 60,36,152
Rb 20,8,24576
s_add_u32 s55, s55, 0x1000000
Mp 52,36,144
s_and_b32 s88, s57, 1
s_lshl1_add_u32 s58, s58, s88
Mp 52,40,160
Rb 24,8,25600
s_lshl_b32 s88, s58, 8
Mp 60,40,168
v_add_u32 v167, s88, v167
s_mul_i32 s59, s58, 14336
Mp 60,44,184
Rb 28,8,26624
s_waitcnt lgkmcnt(8)
.LzH18:
s_cbranch_execz .LzHR
Mp 52,44,176
s_lshr_b32 s88, s57, 1
s_lshl_b32 s88, s88, 6
s_add_u32 s88, s88, s59
Mp 52,48,192
Rb 32,8,27648
v_add_u32 v166, s88, v166
Mp 60,48,200
s_mov_b32 s57, 0
)" R"(
s_add_u32 m0, s36, 98304
Mp 60,52,216
Rb 36,8,28672
Mp 52,52,208
buffer_load_dwordx4 v173, s[12:15], s88 offen offset:0 lds
Mp 52,56,224
Rb 40,8,29696
s_add_u32 m0, s36, 98880
Mp 60,56,232
buffer_load_dwordx4 v173, s[12:15], s88 offen offset:448 lds
Mp 60,60,248
Rb 44,8,30720
s_waitcnt lgkmcnt(8)
Mp 52,60,240
s_mov_b32 s88, 0x1500008
buffer_load_dword v167, v167, s[20:23], s88 offen sc0 sc1
v_mfma_f32_16x16x32_bf16 v[64:67], a[0:3], v[128:131], 0
Rb 48,1,0
v_mfma_f32_16x16x32_bf16 v[72:75], a[0:3], v[136:139], 0
Mp 72,4,152
Rb 52,1,1024
Mp 64,4,144
Mp 64,8,160
Rb 56,1,2048
Mp 72,8,168
Mp 72,12,184
Rb 60,1,3072
s_waitcnt vmcnt(5) lgkmcnt(8)
s_barrier
v_cmp_eq_u32_e32 vcc, s3, v4
s_andn2_b64 s[28:29], 1, vcc
s_cbranch_scc1 .Lzsp23
.Lzok23:
Mp 64,12,176
buffer_load_dwordx4 a[64:67], v2, s[20:23], s66 offen offset:0 sc0 sc1
Mp 64,16,192
Rb 0,1,4096
Mp 72,16,200
Mp 72,20,216
Rb 4,1,5120
Mp 64,20,208
buffer_load_dwordx4 a[68:71], v2, s[20:23], s66 offen offset:1024 sc0 sc1
Mp 64,24,224
Rb 8,1,6144
Mp 72,24,232
Mp 72,28,248
Rb 12,1,8192
s_waitcnt lgkmcnt(8)
Mp 64,28,240
buffer_load_dwordx4 a[72:75], v2, s[20:23], s66 offen offset:2048 sc0 sc1
v_mfma_f32_16x16x32_bf16 v[68:71], a[32:35], v[128:131], 0
Rb 16,1,9216
v_mfma_f32_16x16x32_bf16 v[76:79], a[32:35], v[136:139], 0
Mp 76,36,152
Rb 20,1,10240
Mp 68,36,144
buffer_load_dwordx4 a[76:79], v2, s[20:23], s66 offen offset:3072 sc0 sc1
Mp 68,40,160
Rb 24,1,11264
Mp 76,40,168
Mp 76,44,184
Rb 28,1,12288
s_waitcnt vmcnt(7) lgkmcnt(8)
buffer_store_dword v6, v7, s[20:23], s65 offen offset:128 sc0 sc1
Mp 68,44,176
)" R"(
buffer_load_dwordx4 a[80:83], v3, s[20:23], s66 offen offset:0 sc0 sc1
Mp 68,48,192
Rb 32,1,13312
Mp 76,48,200
Mp 76,52,216
Rb 36,1,14336
Mp 68,52,208
buffer_load_dwordx4 a[84:87], v3, s[20:23], s66 offen offset:1024 sc0 sc1
Mp 68,56,224
Rb 40,1,16384
Mp 76,56,232
Mp 76,60,248
Rb 44,1,17408
s_waitcnt lgkmcnt(8)
Mp 68,60,240
buffer_load_dwordx4 a[88:91], v3, s[20:23], s66 offen offset:2048 sc0 sc1
v_mfma_f32_16x16x32_bf16 v[80:83], a[0:3], v[128:131], 0
Rb 48,1,18432
v_mfma_f32_16x16x32_bf16 v[88:91], a[0:3], v[136:139], 0
Mp 88,4,152
Rb 52,1,19456
Mp 80,4,144
buffer_load_dwordx4 a[92:95], v3, s[20:23], s66 offen offset:3072 sc0 sc1
Mp 80,8,160
Rb 56,1,20480
Mp 88,8,168
Mp 88,12,184
Rb 60,1,21504
s_waitcnt lgkmcnt(8)
Mp 80,12,176
Mp 80,16,192
Rb 0,1,22528
Mp 88,16,200
Mp 88,20,216
Rb 4,1,24576
Mp 80,20,208
Mp 80,24,224
Rb 8,1,25600
Mp 88,24,232
Mp 88,28,248
Rb 12,1,26624
s_waitcnt lgkmcnt(8)
Mp 80,28,240
v_mfma_f32_16x16x32_bf16 v[84:87], a[32:35], v[128:131], 0
Rb 16,1,27648
v_mfma_f32_16x16x32_bf16 v[92:95], a[32:35], v[136:139], 0
Mp 92,36,152
Rb 20,1,28672
Mp 84,36,144
Mp 84,40,160
Rb 24,1,29696
Mp 92,40,168
Mp 92,44,184
Rb 28,1,30720
s_waitcnt lgkmcnt(8)
Mp 84,44,176
buffer_load_dword v4, v7, s[20:23], s52 offen offset:128 sc0 sc1
Mp 84,48,192
Rb 32,1,32768
Mp 92,48,200
Mp 92,52,216
Rb 36,1,33792
Mp 84,52,208
Mp 84,56,224
)" R"(
Rb 40,1,34816
Mp 92,56,232
Mp 92,60,248
Rb 44,1,35840
s_waitcnt vmcnt(6)
s_barrier
Mp 84,60,240
Mv 48,194,64
Mv 56,194,68
Mv 52,192,64
Mv 60,192,68
Mv 64,194,72
Mv 72,194,76
Mv 68,192,72
s_waitcnt vmcnt(3) lgkmcnt(10)
Mv 76,192,76
Cv 48,48,49
Cv 49,50,51
Mv 80,194,80
Cv 50,52,53
Cv 51,54,55
Mv 88,194,84
s_add_u32 s29, s73, 0x0
buffer_store_dwordx4 v[48:51], v1, s[20:23], s29 offen sc0 sc1
Mv 84,192,80
Mv 92,192,84
v_mfma_f32_16x16x32_bf16 v[96:99], a[0:3], v[128:131], 0
Rb 48,1,36864
v_mfma_f32_16x16x32_bf16 v[104:107], a[0:3], v[136:139], 0
Mp 104,4,152
Rb 52,1,37888
s_waitcnt lgkmcnt(8)
Mp 96,4,144
Cv 56,56,57
Cv 57,58,59
ds_read_b64 v[188:189], v174 offset:0
Mp 96,8,160
Rb 56,1,38912
Cv 58,60,61
Mp 104,8,168
Cv 59,62,63
ds_read_b64 v[190:191], v174 offset:32
Mp 104,12,184
Rb 60,1,40960
Mp 96,12,176
s_add_u32 s29, s73, 0x400
buffer_store_dwordx4 v[56:59], v1, s[20:23], s29 offen sc0 sc1
ds_read_b64 v[196:197], v174 offset:1024
Mp 96,16,192
Rb 0,1,41984
Mp 104,16,200
ds_read_b64 v[198:199], v174 offset:1056
Mp 104,20,216
Rb 4,1,43008
s_waitcnt lgkmcnt(12)
Mp 96,20,208
Mp 96,24,224
Rb 8,1,44032
Mp 104,24,232
Cv 64,64,65
Cv 65,66,67
Mp 104,28,248
Rb 12,1,45056
Cv 66,68,69
Mp 96,28,240
)" R"(
Cv 67,70,71
v_mfma_f32_16x16x32_bf16 v[100:103], a[32:35], v[128:131], 0
Rb 16,1,46080
v_mfma_f32_16x16x32_bf16 v[108:111], a[32:35], v[136:139], 0
s_add_u32 s29, s73, 0x800
buffer_store_dwordx4 v[64:67], v1, s[20:23], s29 offen sc0 sc1
Mp 108,36,152
Rb 20,1,47104
s_waitcnt vmcnt(3) lgkmcnt(11)
v_cmp_eq_u32_e32 vcc, s3, v4
s_andn2_b64 s[28:29], 1, vcc
s_cbranch_scc1 .Lzsp24
.Lzok24:
Mp 100,36,144
s_add_u32 s66, s66, 0x10000
buffer_load_dwordx4 a[96:99], v2, s[20:23], s66 offen offset:0 sc0 sc1
Mp 100,40,160
Rb 24,1,49152
v_sub_f32_e32 v172, 1.0, v167
Mp 108,40,168
Cv 172,172,172
v_lshlrev_b32 v172, 16, v172
Mp 108,44,184
Rb 28,1,50176
v_lshlrev_b32 v204, 16, v188
Mp 100,44,176
buffer_load_dwordx4 a[100:103], v2, s[20:23], s66 offen offset:1024 sc0 sc1
Mp 100,48,192
Rb 32,1,51200
Cv 72,72,73
Mp 108,48,200
Cv 73,74,75
Cv 74,76,77
Mp 108,52,216
Rb 36,1,52224
s_waitcnt lgkmcnt(12)
Mp 100,52,208
buffer_load_dwordx4 a[104:107], v2, s[20:23], s66 offen offset:2048 sc0 sc1
Mp 100,56,224
Rb 40,1,53248
Cv 75,78,79
Mp 108,56,232
s_add_u32 s29, s73, 0xc00
buffer_store_dwordx4 v[72:75], v1, s[20:23], s29 offen sc0 sc1
Mp 108,60,248
Rb 44,1,54272
v_and_b32 v205, 0xffff0000, v188
Mp 100,60,240
buffer_load_dwordx4 a[108:111], v2, s[20:23], s66 offen offset:3072 sc0 sc1
Mv 96,194,88
v_mul_f32_e32 v204, v172, v204
v_mul_f32_e32 v205, v172, v205
Mv 104,194,92
Cv 205,204,205
v_lshlrev_b32 v204, 16, v205
Mv 100,192,88
v_and_b32 v205, 0xffff0000, v205
s_waitcnt lgkmcnt(8)
Mv 108,192,92
buffer_load_dwordx4 a[112:115], v3, s[20:23], s66 offen offset:0 sc0 sc1
v_mfma_f32_16x16x32_bf16 v[112:115], a[0:3], v[128:131], 0
Rb 48,1,55296
Cv 80,80,81
v_mfma_f32_16x16x32_bf16 v[120:123], a[0:3], v[136:139], 0
)" R"(
Cv 81,82,83
Cv 82,84,85
Mp 120,4,152
Rb 52,1,57344
Cv 83,86,87
Mp 112,4,144
buffer_load_dwordx4 a[116:119], v3, s[20:23], s66 offen offset:1024 sc0 sc1
Mp 112,8,160
Rb 56,1,58368
v_lshlrev_b32 v206, 16, v189
Mp 120,8,168
s_add_u32 s29, s73, 0x1000
buffer_store_dwordx4 v[80:83], v1, s[20:23], s29 offen sc0 sc1
Mp 120,12,184
Rb 60,1,59392
s_waitcnt lgkmcnt(8)
Mp 112,12,176
v_and_b32 v207, 0xffff0000, v189
v_mul_f32_e32 v206, v172, v206
Mp 112,16,192
Rb 0,1,60416
v_mul_f32_e32 v207, v172, v207
Mp 120,16,200
Cv 207,206,207
v_lshlrev_b32 v206, 16, v207
Mp 120,20,216
Rb 4,1,61440
v_and_b32 v207, 0xffff0000, v207
Mp 112,20,208
Cv 88,88,89
Cv 89,90,91
Mp 112,24,224
Rb 8,1,62464
Cv 90,92,93
Mp 120,24,232
Cv 91,94,95
v_lshlrev_b32 v212, 16, v190
Mp 120,28,248
Rb 12,1,63488
s_waitcnt lgkmcnt(8)
Mp 112,28,240
s_add_u32 s29, s73, 0x1400
buffer_store_dwordx4 v[88:91], v1, s[20:23], s29 offen sc0 sc1
v_mfma_f32_16x16x32_bf16 v[116:119], a[32:35], v[128:131], 0
Rb 16,10,0
v_and_b32 v213, 0xffff0000, v190
v_mfma_f32_16x16x32_bf16 v[124:127], a[32:35], v[136:139], 0
v_mul_f32_e32 v212, v172, v212
v_mul_f32_e32 v213, v172, v213
Mp 124,36,152
Rb 20,10,1024
Cv 213,212,213
Mp 116,36,144
v_lshlrev_b32 v212, 16, v213
v_and_b32 v213, 0xffff0000, v213
Mp 116,40,160
Rb 24,10,2048
v_lshlrev_b32 v214, 16, v191
Mp 124,40,168
Cv 96,96,97
Cv 97,98,99
Mp 124,44,184
Rb 28,10,3072
s_waitcnt lgkmcnt(8)
)" R"(
Mp 116,44,176
Cv 98,100,101
Cv 99,102,103
Mp 116,48,192
Rb 32,10,4096
v_and_b32 v215, 0xffff0000, v191
Mp 124,48,200
s_add_u32 s29, s73, 0x1800
buffer_store_dwordx4 v[96:99], v1, s[20:23], s29 offen sc0 sc1
Mp 124,52,216
Rb 36,10,5120
v_mul_f32_e32 v214, v172, v214
Mp 116,52,208
v_mul_f32_e32 v215, v172, v215
Cv 215,214,215
Mp 116,56,224
Rb 40,10,6144
v_lshlrev_b32 v214, 16, v215
Mp 124,56,232
v_and_b32 v215, 0xffff0000, v215
v_lshlrev_b32 v220, 16, v196
Mp 124,60,248
Rb 44,10,8192
s_waitcnt vmcnt(8) lgkmcnt(10)
Mp 116,60,240
Cv 104,104,105
Cv 105,106,107
Mv 112,194,96
Cv 106,108,109
Cv 107,110,111
Mv 120,194,100
s_add_u32 s29, s73, 0x4000
buffer_store_dwordx4 v[104:107], v1, s[20:23], s29 offen sc0 sc1
Mv 116,192,96
v_and_b32 v221, 0xffff0000, v196
v_mul_f32_e32 v220, v172, v220
Mv 124,192,100
v_mul_f32_e32 v221, v172, v221
Cv 221,220,221
v_mfma_f32_16x16x32_bf16 v[32:35], a[0:3], v[128:131], 0
Rb 48,10,9216
v_lshlrev_b32 v220, 16, v221
v_mfma_f32_16x16x32_bf16 v[40:43], a[0:3], v[136:139], 0
v_and_b32 v221, 0xffff0000, v221
v_lshlrev_b32 v222, 16, v197
Mp 40,4,152
Rb 52,10,10240
s_waitcnt lgkmcnt(8)
Mp 32,4,144
Cv 112,112,113
Cv 113,114,115
Mp 32,8,160
Rb 56,10,11264
Cv 114,116,117
Mp 40,8,168
Cv 115,118,119
v_and_b32 v223, 0xffff0000, v197
Mp 40,12,184
Rb 60,10,12288
v_mul_f32_e32 v222, v172, v222
Mp 32,12,176
s_add_u32 s29, s73, 0x4400
buffer_store_dwordx4 v[112:115], v1, s[20:23], s29 offen sc0 sc1
Mp 32,16,192
)" R"(
Rb 0,10,13312
v_mul_f32_e32 v223, v172, v223
Mp 40,16,200
Cv 223,222,223
v_lshlrev_b32 v222, 16, v223
Mp 40,20,216
Rb 4,10,14336
s_waitcnt lgkmcnt(8)
Mp 32,20,208
v_and_b32 v223, 0xffff0000, v223
v_lshlrev_b32 v228, 16, v198
Mp 32,24,224
Rb 8,10,16384
v_and_b32 v229, 0xffff0000, v198
Mp 40,24,232
Cv 120,120,121
Cv 121,122,123
Mp 40,28,248
Rb 12,10,17408
Cv 122,124,125
Mp 32,28,240
Cv 123,126,127
v_mul_f32_e32 v228, v172, v228
v_mfma_f32_16x16x32_bf16 v[36:39], a[32:35], v[128:131], 0
Rb 16,10,18432
v_mul_f32_e32 v229, v172, v229
v_mfma_f32_16x16x32_bf16 v[44:47], a[32:35], v[136:139], 0
s_add_u32 s29, s73, 0x4800
buffer_store_dwordx4 v[120:123], v1, s[20:23], s29 offen sc0 sc1
Mp 44,36,152
Rb 20,10,19456
s_waitcnt lgkmcnt(8)
Mp 36,36,144
Cv 229,228,229
v_lshlrev_b32 v228, 16, v229
Mp 36,40,160
Rb 24,10,20480
v_and_b32 v229, 0xffff0000, v229
Mp 44,40,168
v_lshlrev_b32 v230, 16, v199
v_and_b32 v231, 0xffff0000, v199
Mp 44,44,184
Rb 28,10,21504
v_mul_f32_e32 v230, v172, v230
Mp 36,44,176
v_mul_f32_e32 v231, v172, v231
Cv 231,230,231
Mp 36,48,192
Rb 32,10,22528
v_lshlrev_b32 v230, 16, v231
Mp 44,48,200
v_and_b32 v231, 0xffff0000, v231
Mp 44,52,216
Rb 36,10,24576
s_waitcnt vmcnt(3) lgkmcnt(10)
buffer_store_dword v6, v7, s[20:23], s68 offen offset:0 sc0 sc1
Mp 36,52,208
Mp 36,56,224
Rb 40,10,25600
Mp 44,56,232
Mp 44,60,248
Rb 44,10,26624
Mp 36,60,240
Mv 32,194,104
)" R"(
Mv 40,194,108
Mv 36,192,104
s_waitcnt lgkmcnt(8)
Mv 44,192,108
v_mfma_f32_16x16x32_bf16 v[48:51], a[0:3], v[128:131], 0
Rb 48,10,27648
v_mfma_f32_16x16x32_bf16 v[56:59], a[0:3], v[136:139], 0
Mp 56,4,152
Rb 52,10,28672
Mp 48,4,144
Cv 32,32,33
Cv 33,34,35
Mp 48,8,160
Rb 56,10,29696
Cv 34,36,37
Mp 56,8,168
Cv 35,38,39
Mp 56,12,184
Rb 60,10,30720
s_waitcnt lgkmcnt(8)
Mp 48,12,176
s_add_u32 s29, s73, 0x4c00
buffer_store_dwordx4 v[32:35], v1, s[20:23], s29 offen sc0 sc1
Mp 48,16,192
Mp 56,16,200
Mp 56,20,216
Mp 48,20,208
Mp 48,24,224
Mp 56,24,232
Cv 40,40,41
Cv 41,42,43
buffer_store_dword v6, v7, s[20:23], s68 offen offset:128 sc0 sc1
Mp 56,28,248
Cv 42,44,45
s_waitcnt lgkmcnt(4)
Mp 48,28,240
Cv 43,46,47
v_mfma_f32_16x16x32_bf16 v[52:55], a[32:35], v[128:131], 0
s_add_u32 s29, s73, 0x5000
buffer_store_dwordx4 v[40:43], v1, s[20:23], s29 offen sc0 sc1
v_mfma_f32_16x16x32_bf16 v[60:63], a[32:35], v[136:139], 0
buffer_load_dword v4, v165, s[20:23], s40 offen sc0 sc1
Mp 60,36,152
Mp 52,36,144
Mp 52,40,160
Mp 60,40,168
Mp 60,44,184
s_waitcnt lgkmcnt(0)
Mp 52,44,176
Mp 52,48,192
Mp 60,48,200
Mp 60,52,216
Mp 52,52,208
Mp 52,56,224
Mp 60,56,232
Mp 60,60,248
Mp 52,60,240
Mv 48,194,112
Mv 56,194,116
Mv 52,192,112
Mv 60,192,116
s_nop 7
Cv 48,48,49
Cv 49,50,51
)" R"(
Cv 50,52,53
Cv 51,54,55
s_add_u32 s29, s73, 0x5400
buffer_store_dwordx4 v[48:51], v1, s[20:23], s29 offen sc0 sc1
Cv 56,56,57
Cv 57,58,59
Cv 58,60,61
Cv 59,62,63
s_add_u32 s29, s73, 0x5800
buffer_store_dwordx4 v[56:59], v1, s[20:23], s29 offen sc0 sc1
.LzH19:
s_cbranch_execz .LzHR
.LzEND:
s_cmp_gt_u32 s82, 223
s_cbranch_scc1 .LzFIN
s_cmp_lt_u32 s82, 14
s_cbranch_scc1 .LzBB
.LzBBR:
s_waitcnt vmcnt(2)
.LzS0:
v_cmp_ne_u32_e32 vcc, s3, v4
s_lshr_b32 s28, vcc_lo, 1
s_or_b32 s28, s28, vcc_lo
s_lshr_b32 s29, s28, 2
s_or_b32 s28, s28, s29
s_andn2_b32 s28, s93, s28
s_sub_u32 s53, s53, 1
s_cselect_b32 s28, s93, s28
s_cmov_b32 s53, 0
s_xor_b32 s93, s93, s28
s_or_b32 s57, s57, s28
s_cbranch_scc1 .LzS2
s_sleep 4
s_branch .LzSk3
.LzS2:
s_bitcmp0_b32 s57, 0
s_cbranch_scc1 .LzSk0
s_add_u32 s29, s55, 0x0
buffer_load_dwordx4 a[128:131], v164, s[20:23], s29 offen offset:0 sc0 sc1
buffer_load_dwordx4 a[132:135], v164, s[20:23], s29 offen offset:256 sc0 sc1
buffer_load_dwordx4 a[136:139], v164, s[20:23], s29 offen offset:512 sc0 sc1
buffer_load_dwordx4 a[140:143], v164, s[20:23], s29 offen offset:768 sc0 sc1
.LzSk0:
s_bitcmp0_b32 s57, 4
s_cbranch_scc1 .LzSk1
s_add_u32 s29, s55, 0x8000000
buffer_load_dwordx4 a[144:147], v183, s[20:23], s29 offen offset:0 sc0 sc1
.LzH20:
s_cbranch_execz .LzHR
buffer_load_dwordx4 a[148:151], v183, s[20:23], s29 offen offset:256 sc0 sc1
buffer_load_dwordx4 a[152:155], v183, s[20:23], s29 offen offset:512 sc0 sc1
buffer_load_dwordx4 a[156:159], v183, s[20:23], s29 offen offset:768 sc0 sc1
.LzSk1:
s_bitcmp0_b32 s57, 8
s_cbranch_scc1 .LzSk2
s_add_u32 s29, s55, 0x8000
buffer_load_dwordx4 a[160:163], v164, s[20:23], s29 offen offset:0 sc0 sc1
buffer_load_dwordx4 a[164:167], v164, s[20:23], s29 offen offset:256 sc0 sc1
buffer_load_dwordx4 a[168:171], v164, s[20:23], s29 offen offset:512 sc0 sc1
buffer_load_dwordx4 a[172:175], v164, s[20:23], s29 offen offset:768 sc0 sc1
.LzSk2:
s_bitcmp0_b32 s57, 12
s_cbranch_scc1 .LzSk3
s_add_u32 s29, s55, 0x8008000
)" R"(
buffer_load_dwordx4 a[176:179], v183, s[20:23], s29 offen offset:0 sc0 sc1
buffer_load_dwordx4 a[180:183], v183, s[20:23], s29 offen offset:256 sc0 sc1
buffer_load_dwordx4 a[184:187], v183, s[20:23], s29 offen offset:512 sc0 sc1
buffer_load_dwordx4 a[188:191], v183, s[20:23], s29 offen offset:768 sc0 sc1
.LzSk3:
s_cmp_eq_u32 s93, 0
s_cbranch_scc1 .LzS1
buffer_load_dword v4, v165, s[20:23], s40 offen sc0 sc1
.LzS1:
s_waitcnt vmcnt(0)
s_bitcmp0_b32 s57, 0
s_cbranch_scc1 .LzSv0
v_accvgpr_read_b32 v173, a131
v_accvgpr_read_b32 v174, a135
v_accvgpr_read_b32 v175, a139
v_accvgpr_read_b32 v180, a143
v_max3_u32 v173, v173, v174, v175
v_max_u32 v173, v173, v180
v_cmp_eq_u32_e32 vcc, -1, v173
s_cbranch_vccnz .LzSv0
s_bitset0_b32 s57, 0
.LzSv0:
s_bitcmp0_b32 s57, 4
s_cbranch_scc1 .LzSv1
v_accvgpr_read_b32 v173, a147
v_accvgpr_read_b32 v174, a151
v_accvgpr_read_b32 v175, a155
v_accvgpr_read_b32 v180, a159
v_max3_u32 v173, v173, v174, v175
v_max_u32 v173, v173, v180
v_cmp_eq_u32_e32 vcc, -1, v173
s_cbranch_vccnz .LzSv1
s_bitset0_b32 s57, 4
.LzSv1:
s_bitcmp0_b32 s57, 8
s_cbranch_scc1 .LzSv2
v_accvgpr_read_b32 v173, a163
v_accvgpr_read_b32 v174, a167
v_accvgpr_read_b32 v175, a171
v_accvgpr_read_b32 v180, a175
v_max3_u32 v173, v173, v174, v175
v_max_u32 v173, v173, v180
v_cmp_eq_u32_e32 vcc, -1, v173
s_cbranch_vccnz .LzSv2
s_bitset0_b32 s57, 8
.LzSv2:
s_bitcmp0_b32 s57, 12
s_cbranch_scc1 .LzSv3
v_accvgpr_read_b32 v173, a179
v_accvgpr_read_b32 v174, a183
v_accvgpr_read_b32 v175, a187
v_accvgpr_read_b32 v180, a191
v_max3_u32 v173, v173, v174, v175
v_max_u32 v173, v173, v180
v_cmp_eq_u32_e32 vcc, -1, v173
s_cbranch_vccnz .LzSv3
s_bitset0_b32 s57, 12
.LzSv3:
s_cmp_eq_u32 s53, 0
s_cmov_b32 s57, 0
s_or_b32 s28, s57, s93
s_cbranch_scc1 .LzS0
s_bfe_u32 s28, s63, 0x70004
s_cmp_eq_u32 s28, s58
)" R"(
s_cbranch_scc1 .LzRT
s_bfe_u32 s28, s63, 0x70014
s_cmp_eq_u32 s28, s58
s_cbranch_scc1 .LzRT
.LzRT3:
v_mfma_f32_16x16x32_bf16 v[80:83], v[132:135], a[128:131], 0
v_mfma_f32_16x16x32_bf16 v[88:91], v[132:135], a[160:163], 0
v_mfma_f32_16x16x32_bf16 v[84:87], v[132:135], a[136:139], 0
v_mfma_f32_16x16x32_bf16 v[92:95], v[132:135], a[168:171], 0
v_mfma_f32_16x16x32_bf16 v[80:83], v[140:143], a[132:135], v[80:83]
v_mfma_f32_16x16x32_bf16 v[88:91], v[140:143], a[164:167], v[88:91]
v_mfma_f32_16x16x32_bf16 v[84:87], v[140:143], a[140:143], v[84:87]
v_mfma_f32_16x16x32_bf16 v[92:95], v[140:143], a[172:175], v[92:95]
v_mfma_f32_16x16x32_bf16 v[80:83], v[132:135], a[144:147], v[80:83]
v_mfma_f32_16x16x32_bf16 v[88:91], v[132:135], a[176:179], v[88:91]
v_mfma_f32_16x16x32_bf16 v[84:87], v[132:135], a[152:155], v[84:87]
v_mfma_f32_16x16x32_bf16 v[92:95], v[132:135], a[184:187], v[92:95]
v_mfma_f32_16x16x32_bf16 v[80:83], v[140:143], a[148:151], v[80:83]
v_mfma_f32_16x16x32_bf16 v[88:91], v[140:143], a[180:183], v[88:91]
v_mfma_f32_16x16x32_bf16 v[84:87], v[140:143], a[156:159], v[84:87]
v_mfma_f32_16x16x32_bf16 v[92:95], v[140:143], a[188:191], v[92:95]
v_lshrrev_b32 v173, 4, v0
v_and_b32 v173, 1, v173
v_mad_u32_u24 v166, v173, 24, v166
s_nop 1
Cv 81,80,81
v_lshlrev_b32 v80, 16, v81
v_and_b32 v81, 0xffff0000, v81
Cv 83,82,83
v_lshlrev_b32 v82, 16, v83
v_and_b32 v83, 0xffff0000, v83
Cv 89,88,89
v_lshlrev_b32 v88, 16, v89
v_and_b32 v89, 0xffff0000, v89
Cv 91,90,91
v_lshlrev_b32 v90, 16, v91
v_and_b32 v91, 0xffff0000, v91
Cv 85,84,85
v_lshlrev_b32 v84, 16, v85
v_and_b32 v85, 0xffff0000, v85
Cv 87,86,87
v_lshlrev_b32 v86, 16, v87
v_and_b32 v87, 0xffff0000, v87
Cv 93,92,93
v_lshlrev_b32 v92, 16, v93
v_and_b32 v93, 0xffff0000, v93
Cv 95,94,95
v_lshlrev_b32 v94, 16, v95
v_and_b32 v95, 0xffff0000, v95
v_mul_f32_e32 v80, v167, v80
v_mul_f32_e32 v81, v167, v81
v_mul_f32_e32 v82, v167, v82
v_mul_f32_e32 v83, v167, v83
v_mul_f32_e32 v88, v167, v88
v_mul_f32_e32 v89, v167, v89
v_mul_f32_e32 v90, v167, v90
v_mul_f32_e32 v91, v167, v91
v_mul_f32_e32 v84, v167, v84
v_mul_f32_e32 v85, v167, v85
v_mul_f32_e32 v86, v167, v86
v_mul_f32_e32 v87, v167, v87
v_mul_f32_e32 v92, v167, v92
v_mul_f32_e32 v93, v167, v93
v_mul_f32_e32 v94, v167, v94
)" R"(
v_mul_f32_e32 v95, v167, v95
Cv 81,80,81
v_lshlrev_b32 v80, 16, v81
v_and_b32 v81, 0xffff0000, v81
Cv 83,82,83
v_lshlrev_b32 v82, 16, v83
v_and_b32 v83, 0xffff0000, v83
Cv 89,88,89
v_lshlrev_b32 v88, 16, v89
v_and_b32 v89, 0xffff0000, v89
Cv 91,90,91
v_lshlrev_b32 v90, 16, v91
v_and_b32 v91, 0xffff0000, v91
Cv 85,84,85
v_lshlrev_b32 v84, 16, v85
v_and_b32 v85, 0xffff0000, v85
Cv 87,86,87
v_lshlrev_b32 v86, 16, v87
v_and_b32 v87, 0xffff0000, v87
Cv 93,92,93
v_lshlrev_b32 v92, 16, v93
v_and_b32 v93, 0xffff0000, v93
Cv 95,94,95
v_lshlrev_b32 v94, 16, v95
v_and_b32 v95, 0xffff0000, v95
v_add_f32_e32 v80, v80, v204
v_add_f32_e32 v81, v81, v205
v_add_f32_e32 v82, v82, v206
v_add_f32_e32 v83, v83, v207
v_add_f32_e32 v88, v88, v220
v_add_f32_e32 v89, v89, v221
v_add_f32_e32 v90, v90, v222
v_add_f32_e32 v91, v91, v223
v_add_f32_e32 v84, v84, v212
v_add_f32_e32 v85, v85, v213
v_add_f32_e32 v86, v86, v214
v_add_f32_e32 v87, v87, v215
v_add_f32_e32 v92, v92, v228
v_add_f32_e32 v93, v93, v229
v_add_f32_e32 v94, v94, v230
v_add_f32_e32 v95, v95, v231
Cv 64,80,81
Cv 65,82,83
Cv 66,84,85
Cv 67,86,87
Cv 68,88,89
Cv 69,90,91
Cv 70,92,93
Cv 71,94,95
v_permlane16_swap_b32_e32 v64, v66
v_permlane16_swap_b32_e32 v65, v67
buffer_store_dwordx4 v[64:67], v166, s[8:11], 0 offen offset:0 sc0 sc1
v_permlane16_swap_b32_e32 v68, v70
v_permlane16_swap_b32_e32 v69, v71
buffer_store_dwordx4 v[68:71], v166, s[8:11], 0 offen offset:448 sc0 sc1
s_or_b32 s28, s82, s33
s_cbranch_scc1 .LzFIN
s_mov_b32 s28, 0x1508400
buffer_store_dword v6, v7, s[20:23], s28 offen offset:0 sc0 sc1
.LzFIN:
s_endpgm
.LzRT:
s_and_b32 s28, s63, 0x7ff
s_mul_i32 s28, s28, 896
)" R"(
v_subrev_u32 v174, s28, v166
s_bfe_u32 s28, s63, 0xb0010
s_mul_i32 s28, s28, 896
v_subrev_u32 v175, s28, v166
s_movk_i32 s28, 896
v_cmp_gt_u32_e64 s[24:25], s28, v175
v_cmp_gt_u32_e32 vcc, s28, v174
v_add_u32 v175, s28, v175
s_or_b64 s[24:25], s[24:25], vcc
v_cndmask_b32_e32 v174, v175, v174, vcc
v_lshrrev_b32 v174, 5, v174
v_lshlrev_b32 v174, 7, v174
v_and_or_b32 v174, v0, 48, v174
s_mov_b64 exec, s[24:25]
s_mov_b32 s29, 0x1509000
.LzRT1:
buffer_load_dwordx4 v[64:67], v174, s[20:23], s29 offen offset:0 sc0 sc1
buffer_load_dwordx4 v[68:71], v174, s[20:23], s29 offen offset:128 sc0 sc1
buffer_load_dwordx4 v[72:75], v174, s[20:23], s29 offen offset:1792 sc0 sc1
buffer_load_dwordx4 v[76:79], v174, s[20:23], s29 offen offset:1920 sc0 sc1
s_waitcnt vmcnt(0)
v_xor_b32 v175, s3, v65
v_xor_b32 v173, s3, v67
v_or_b32 v175, v175, v173
v_xor_b32 v173, s3, v69
v_or_b32 v175, v175, v173
v_xor_b32 v173, s3, v71
v_or_b32 v175, v175, v173
v_xor_b32 v173, s3, v73
v_or_b32 v175, v175, v173
v_xor_b32 v173, s3, v75
v_or_b32 v175, v175, v173
v_xor_b32 v173, s3, v77
v_or_b32 v175, v175, v173
v_xor_b32 v173, s3, v79
v_or_b32 v175, v175, v173
v_cmp_ne_u32_e32 vcc, 0, v175
s_cbranch_vccz .LzRT2
s_sleep 2
s_sub_u32 s53, s53, 1
s_cbranch_scc0 .LzRT1
s_mov_b32 s53, 0
.LzRT2:
v_mov_b32 v188, v64
v_mov_b32 v189, v66
v_mov_b32 v190, v68
v_mov_b32 v191, v70
v_mov_b32 v196, v72
v_mov_b32 v197, v74
v_mov_b32 v198, v76
v_mov_b32 v199, v78
v_sub_f32_e32 v172, 1.0, v167
Cv 172,172,172
v_lshlrev_b32 v172, 16, v172
v_lshlrev_b32 v204, 16, v188
v_and_b32 v205, 0xffff0000, v188
v_mul_f32_e32 v204, v172, v204
v_mul_f32_e32 v205, v172, v205
Cv 205,204,205
v_lshlrev_b32 v204, 16, v205
v_and_b32 v205, 0xffff0000, v205
v_lshlrev_b32 v206, 16, v189
v_and_b32 v207, 0xffff0000, v189
v_mul_f32_e32 v206, v172, v206
)" R"(
v_mul_f32_e32 v207, v172, v207
Cv 207,206,207
v_lshlrev_b32 v206, 16, v207
v_and_b32 v207, 0xffff0000, v207
v_lshlrev_b32 v212, 16, v190
v_and_b32 v213, 0xffff0000, v190
v_mul_f32_e32 v212, v172, v212
v_mul_f32_e32 v213, v172, v213
Cv 213,212,213
v_lshlrev_b32 v212, 16, v213
v_and_b32 v213, 0xffff0000, v213
v_lshlrev_b32 v214, 16, v191
v_and_b32 v215, 0xffff0000, v191
v_mul_f32_e32 v214, v172, v214
v_mul_f32_e32 v215, v172, v215
Cv 215,214,215
v_lshlrev_b32 v214, 16, v215
v_and_b32 v215, 0xffff0000, v215
v_lshlrev_b32 v220, 16, v196
v_and_b32 v221, 0xffff0000, v196
v_mul_f32_e32 v220, v172, v220
v_mul_f32_e32 v221, v172, v221
Cv 221,220,221
v_lshlrev_b32 v220, 16, v221
v_and_b32 v221, 0xffff0000, v221
v_lshlrev_b32 v222, 16, v197
v_and_b32 v223, 0xffff0000, v197
v_mul_f32_e32 v222, v172, v222
v_mul_f32_e32 v223, v172, v223
Cv 223,222,223
v_lshlrev_b32 v222, 16, v223
v_and_b32 v223, 0xffff0000, v223
v_lshlrev_b32 v228, 16, v198
v_and_b32 v229, 0xffff0000, v198
v_mul_f32_e32 v228, v172, v228
v_mul_f32_e32 v229, v172, v229
Cv 229,228,229
v_lshlrev_b32 v228, 16, v229
v_and_b32 v229, 0xffff0000, v229
v_lshlrev_b32 v230, 16, v199
v_and_b32 v231, 0xffff0000, v199
v_mul_f32_e32 v230, v172, v230
v_mul_f32_e32 v231, v172, v231
Cv 231,230,231
v_lshlrev_b32 v230, 16, v231
v_and_b32 v231, 0xffff0000, v231
s_mov_b64 exec, -1
s_branch .LzRT3
.LzH21:
s_cbranch_execz .LzH38
.LzMASK0:
s_lshl1_add_u32 s28, s57, s33
s_mov_b32 s29, 0xff800000
s_cmp_gt_u32 s28, 5
s_cbranch_scc1 .LzMASKE0
s_cmp_eq_u32 s28, 5
s_cbranch_scc1 .LzMASKB0
v_mov_b32 v72, s29
v_mov_b32 v73, s29
v_mov_b32 v74, s29
v_mov_b32 v75, s29
v_mov_b32 v80, s29
v_mov_b32 v81, s29
v_mov_b32 v82, s29
)" R"(
v_mov_b32 v83, s29
v_mov_b32 v84, s29
v_mov_b32 v85, s29
v_mov_b32 v86, s29
v_mov_b32 v87, s29
v_mov_b32 v88, s29
v_mov_b32 v89, s29
v_mov_b32 v90, s29
v_mov_b32 v91, s29
v_mov_b32 v92, s29
v_mov_b32 v93, s29
v_mov_b32 v94, s29
v_mov_b32 v95, s29
s_cmp_eq_u32 s28, 4
s_cbranch_scc1 .LzMASKA0
v_mov_b32 v64, s29
v_mov_b32 v65, s29
v_mov_b32 v66, s29
v_mov_b32 v67, s29
v_mov_b32 v68, s29
v_mov_b32 v69, s29
v_mov_b32 v70, s29
v_mov_b32 v71, s29
v_mov_b32 v76, s29
v_mov_b32 v77, s29
v_mov_b32 v78, s29
v_mov_b32 v79, s29
.LzMASKE0:
s_setpc_b64 s[30:31]
.LzMASKA0:
s_mov_b32 exec_lo, 0xf0000
s_mov_b32 exec_hi, 0xfff00ff
v_mov_b32 v64, s29
v_mov_b32 v76, s29
s_mov_b32 exec_lo, 0x1f0001
s_mov_b32 exec_hi, 0x1fff01ff
v_mov_b32 v65, s29
v_mov_b32 v77, s29
s_mov_b32 exec_lo, 0x3f0003
s_mov_b32 exec_hi, 0x3fff03ff
v_mov_b32 v66, s29
v_mov_b32 v78, s29
s_mov_b32 exec_lo, 0x7f0007
s_mov_b32 exec_hi, 0x7fff07ff
v_mov_b32 v67, s29
v_mov_b32 v79, s29
s_mov_b64 exec, -1
s_setpc_b64 s[30:31]
.LzMASKB0:
s_nop 1
v_mov_b32 v88, s29
v_mov_b32 v89, s29
v_mov_b32 v90, s29
v_mov_b32 v91, s29
s_mov_b32 exec_lo, 0xf0000
s_mov_b32 exec_hi, 0xfff00ff
v_mov_b32 v80, s29
v_mov_b32 v92, s29
s_mov_b32 exec_lo, 0x1f0001
s_mov_b32 exec_hi, 0x1fff01ff
v_mov_b32 v81, s29
v_mov_b32 v93, s29
s_mov_b32 exec_lo, 0x3f0003
s_mov_b32 exec_hi, 0x3fff03ff
)" R"(
v_mov_b32 v82, s29
v_mov_b32 v94, s29
s_mov_b32 exec_lo, 0x7f0007
s_mov_b32 exec_hi, 0x7fff07ff
v_mov_b32 v83, s29
v_mov_b32 v95, s29
s_mov_b64 exec, -1
s_setpc_b64 s[30:31]
.LzFIX0:
s_nop 7
v_max_f32_e32 v28, v64, v65
v_max3_f32 v28, v28, v66, v67
v_max3_f32 v28, v28, v72, v73
v_max3_f32 v28, v28, v74, v75
v_max3_f32 v28, v28, v80, v81
v_max3_f32 v28, v28, v82, v83
v_max3_f32 v28, v28, v88, v89
v_max3_f32 v28, v28, v90, v91
v_mov_b32 v24, v28
s_nop 1
v_permlane32_swap_b32_e32 v28, v24
v_max_f32_e32 v28, v28, v24
v_mov_b32 v24, v28
s_nop 1
v_permlane16_swap_b32_e32 v28, v24
v_max_f32_e32 v28, v28, v24
v_max_f32_e32 v29, v68, v69
v_max3_f32 v29, v29, v70, v71
v_max3_f32 v29, v29, v76, v77
v_max3_f32 v29, v29, v78, v79
v_max3_f32 v29, v29, v84, v85
v_max3_f32 v29, v29, v86, v87
v_max3_f32 v29, v29, v92, v93
v_max3_f32 v29, v29, v94, v95
v_mov_b32 v24, v29
s_nop 1
v_permlane32_swap_b32_e32 v29, v24
v_max_f32_e32 v29, v29, v24
v_mov_b32 v24, v29
s_nop 1
v_permlane16_swap_b32_e32 v29, v24
v_max_f32_e32 v29, v29, v24
s_bitcmp1_b32 s88, 0
s_cbranch_scc1 .LzFIXF0
v_max_f32_e32 v28, 0, v28
v_sub_f32_e32 v24, 0, v28
v_exp_f32_e32 v24, v24
s_nop 0
v_mul_f32_e32 v12, v24, v12
v_mul_f32_e32 v128, v24, v128
v_mul_f32_e32 v129, v24, v129
v_mul_f32_e32 v130, v24, v130
v_mul_f32_e32 v131, v24, v131
v_mul_f32_e32 v132, v24, v132
v_mul_f32_e32 v133, v24, v133
v_mul_f32_e32 v134, v24, v134
v_mul_f32_e32 v135, v24, v135
v_mul_f32_e32 v144, v24, v144
v_mul_f32_e32 v145, v24, v145
v_mul_f32_e32 v146, v24, v146
v_mul_f32_e32 v147, v24, v147
v_mul_f32_e32 v148, v24, v148
v_mul_f32_e32 v149, v24, v149
v_mul_f32_e32 v150, v24, v150
)" R"(
v_mul_f32_e32 v151, v24, v151
v_mul_f32_e32 v160, v24, v160
v_mul_f32_e32 v161, v24, v161
v_mul_f32_e32 v162, v24, v162
v_mul_f32_e32 v163, v24, v163
v_mul_f32_e32 v164, v24, v164
v_mul_f32_e32 v165, v24, v165
v_mul_f32_e32 v166, v24, v166
v_mul_f32_e32 v167, v24, v167
v_mul_f32_e32 v176, v24, v176
v_mul_f32_e32 v177, v24, v177
v_mul_f32_e32 v178, v24, v178
v_mul_f32_e32 v179, v24, v179
v_mul_f32_e32 v180, v24, v180
v_mul_f32_e32 v181, v24, v181
v_mul_f32_e32 v182, v24, v182
v_mul_f32_e32 v183, v24, v183
v_mul_f32_e32 v192, v24, v192
v_mul_f32_e32 v193, v24, v193
v_mul_f32_e32 v194, v24, v194
v_mul_f32_e32 v195, v24, v195
v_mul_f32_e32 v196, v24, v196
v_mul_f32_e32 v197, v24, v197
v_mul_f32_e32 v198, v24, v198
v_mul_f32_e32 v199, v24, v199
v_mul_f32_e32 v208, v24, v208
v_mul_f32_e32 v209, v24, v209
v_mul_f32_e32 v210, v24, v210
v_mul_f32_e32 v211, v24, v211
v_mul_f32_e32 v212, v24, v212
v_mul_f32_e32 v213, v24, v213
v_mul_f32_e32 v214, v24, v214
v_mul_f32_e32 v215, v24, v215
v_mul_f32_e32 v224, v24, v224
v_mul_f32_e32 v225, v24, v225
v_mul_f32_e32 v226, v24, v226
v_mul_f32_e32 v227, v24, v227
v_mul_f32_e32 v228, v24, v228
v_mul_f32_e32 v229, v24, v229
v_mul_f32_e32 v230, v24, v230
v_mul_f32_e32 v231, v24, v231
v_mul_f32_e32 v240, v24, v240
v_mul_f32_e32 v241, v24, v241
v_mul_f32_e32 v242, v24, v242
v_mul_f32_e32 v243, v24, v243
v_mul_f32_e32 v244, v24, v244
v_mul_f32_e32 v245, v24, v245
v_mul_f32_e32 v246, v24, v246
v_mul_f32_e32 v247, v24, v247
v_max_f32_e32 v29, 0, v29
v_sub_f32_e32 v24, 0, v29
v_exp_f32_e32 v24, v24
s_nop 0
v_mul_f32_e32 v13, v24, v13
v_mul_f32_e32 v136, v24, v136
v_mul_f32_e32 v137, v24, v137
v_mul_f32_e32 v138, v24, v138
v_mul_f32_e32 v139, v24, v139
v_mul_f32_e32 v140, v24, v140
v_mul_f32_e32 v141, v24, v141
v_mul_f32_e32 v142, v24, v142
v_mul_f32_e32 v143, v24, v143
v_mul_f32_e32 v152, v24, v152
v_mul_f32_e32 v153, v24, v153
)" R"(
v_mul_f32_e32 v154, v24, v154
v_mul_f32_e32 v155, v24, v155
v_mul_f32_e32 v156, v24, v156
v_mul_f32_e32 v157, v24, v157
v_mul_f32_e32 v158, v24, v158
v_mul_f32_e32 v159, v24, v159
v_mul_f32_e32 v168, v24, v168
v_mul_f32_e32 v169, v24, v169
v_mul_f32_e32 v170, v24, v170
v_mul_f32_e32 v171, v24, v171
v_mul_f32_e32 v172, v24, v172
v_mul_f32_e32 v173, v24, v173
v_mul_f32_e32 v174, v24, v174
v_mul_f32_e32 v175, v24, v175
v_mul_f32_e32 v184, v24, v184
v_mul_f32_e32 v185, v24, v185
v_mul_f32_e32 v186, v24, v186
v_mul_f32_e32 v187, v24, v187
v_mul_f32_e32 v188, v24, v188
v_mul_f32_e32 v189, v24, v189
v_mul_f32_e32 v190, v24, v190
v_mul_f32_e32 v191, v24, v191
v_mul_f32_e32 v200, v24, v200
v_mul_f32_e32 v201, v24, v201
v_mul_f32_e32 v202, v24, v202
v_mul_f32_e32 v203, v24, v203
v_mul_f32_e32 v204, v24, v204
v_mul_f32_e32 v205, v24, v205
v_mul_f32_e32 v206, v24, v206
v_mul_f32_e32 v207, v24, v207
v_mul_f32_e32 v216, v24, v216
v_mul_f32_e32 v217, v24, v217
v_mul_f32_e32 v218, v24, v218
v_mul_f32_e32 v219, v24, v219
v_mul_f32_e32 v220, v24, v220
v_mul_f32_e32 v221, v24, v221
v_mul_f32_e32 v222, v24, v222
v_mul_f32_e32 v223, v24, v223
v_mul_f32_e32 v232, v24, v232
v_mul_f32_e32 v233, v24, v233
v_mul_f32_e32 v234, v24, v234
v_mul_f32_e32 v235, v24, v235
v_mul_f32_e32 v236, v24, v236
v_mul_f32_e32 v237, v24, v237
v_mul_f32_e32 v238, v24, v238
v_mul_f32_e32 v239, v24, v239
v_mul_f32_e32 v248, v24, v248
v_mul_f32_e32 v249, v24, v249
v_mul_f32_e32 v250, v24, v250
v_mul_f32_e32 v251, v24, v251
v_mul_f32_e32 v252, v24, v252
v_mul_f32_e32 v253, v24, v253
v_mul_f32_e32 v254, v24, v254
v_mul_f32_e32 v255, v24, v255
s_branch .LzFIXG0
.LzFIXF0:
v_cmp_lt_f32_e32 vcc, 0xff7fffff, v28
s_nop 1
v_cndmask_b32_e32 v28, 0, v28, vcc
v_cmp_lt_f32_e32 vcc, 0xff7fffff, v29
s_nop 1
v_cndmask_b32_e32 v29, 0, v29, vcc
.LzFIXG0:
v_sub_f32_e32 v16, v16, v28
)" R"(
v_sub_f32_e32 v17, v17, v28
v_sub_f32_e32 v18, v18, v28
v_sub_f32_e32 v19, v19, v28
v_sub_f32_e32 v64, v64, v28
v_sub_f32_e32 v65, v65, v28
v_sub_f32_e32 v66, v66, v28
v_sub_f32_e32 v67, v67, v28
v_sub_f32_e32 v72, v72, v28
v_sub_f32_e32 v73, v73, v28
v_sub_f32_e32 v74, v74, v28
v_sub_f32_e32 v75, v75, v28
v_sub_f32_e32 v80, v80, v28
v_sub_f32_e32 v81, v81, v28
v_sub_f32_e32 v82, v82, v28
v_sub_f32_e32 v83, v83, v28
v_sub_f32_e32 v88, v88, v28
v_sub_f32_e32 v89, v89, v28
v_sub_f32_e32 v90, v90, v28
v_sub_f32_e32 v91, v91, v28
v_sub_f32_e32 v20, v20, v29
v_sub_f32_e32 v21, v21, v29
v_sub_f32_e32 v22, v22, v29
v_sub_f32_e32 v23, v23, v29
v_sub_f32_e32 v68, v68, v29
v_sub_f32_e32 v69, v69, v29
v_sub_f32_e32 v70, v70, v29
v_sub_f32_e32 v71, v71, v29
v_sub_f32_e32 v76, v76, v29
v_sub_f32_e32 v77, v77, v29
v_sub_f32_e32 v78, v78, v29
v_sub_f32_e32 v79, v79, v29
v_sub_f32_e32 v84, v84, v29
v_sub_f32_e32 v85, v85, v29
v_sub_f32_e32 v86, v86, v29
v_sub_f32_e32 v87, v87, v29
v_sub_f32_e32 v92, v92, v29
v_sub_f32_e32 v93, v93, v29
v_sub_f32_e32 v94, v94, v29
v_sub_f32_e32 v95, v95, v29
s_bitcmp0_b32 s88, 1
s_cbranch_scc1 .LzFIXH0
v_sub_f32_e32 v96, v96, v28
v_sub_f32_e32 v97, v97, v28
v_sub_f32_e32 v98, v98, v28
v_sub_f32_e32 v99, v99, v28
v_sub_f32_e32 v104, v104, v28
v_sub_f32_e32 v105, v105, v28
v_sub_f32_e32 v106, v106, v28
v_sub_f32_e32 v107, v107, v28
v_sub_f32_e32 v112, v112, v28
v_sub_f32_e32 v113, v113, v28
v_sub_f32_e32 v114, v114, v28
v_sub_f32_e32 v115, v115, v28
v_sub_f32_e32 v120, v120, v28
v_sub_f32_e32 v121, v121, v28
v_sub_f32_e32 v122, v122, v28
v_sub_f32_e32 v123, v123, v28
v_sub_f32_e32 v100, v100, v29
v_sub_f32_e32 v101, v101, v29
v_sub_f32_e32 v102, v102, v29
v_sub_f32_e32 v103, v103, v29
v_sub_f32_e32 v108, v108, v29
v_sub_f32_e32 v109, v109, v29
v_sub_f32_e32 v110, v110, v29
)" R"(
v_sub_f32_e32 v111, v111, v29
v_sub_f32_e32 v116, v116, v29
v_sub_f32_e32 v117, v117, v29
v_sub_f32_e32 v118, v118, v29
v_sub_f32_e32 v119, v119, v29
v_sub_f32_e32 v124, v124, v29
v_sub_f32_e32 v125, v125, v29
v_sub_f32_e32 v126, v126, v29
v_sub_f32_e32 v127, v127, v29
.LzFIXH0:
v_exp_f32_e32 v32, v64
v_exp_f32_e32 v28, v65
s_nop 0
v_add_f32_e32 v14, v32, v28
Cv 32,32,28
v_exp_f32_e32 v33, v66
v_exp_f32_e32 v29, v67
v_add_f32_e32 v14, v14, v33
v_add_f32_e32 v14, v14, v29
Cv 33,33,29
v_exp_f32_e32 v34, v72
v_exp_f32_e32 v24, v73
v_add_f32_e32 v14, v14, v34
v_add_f32_e32 v14, v14, v24
Cv 34,34,24
v_exp_f32_e32 v35, v74
v_exp_f32_e32 v25, v75
v_add_f32_e32 v14, v14, v35
v_add_f32_e32 v14, v14, v25
Cv 35,35,25
v_exp_f32_e32 v36, v68
v_exp_f32_e32 v26, v69
s_nop 0
v_add_f32_e32 v15, v36, v26
Cv 36,36,26
v_exp_f32_e32 v37, v70
v_exp_f32_e32 v27, v71
v_add_f32_e32 v15, v15, v37
v_add_f32_e32 v15, v15, v27
Cv 37,37,27
v_exp_f32_e32 v38, v76
v_exp_f32_e32 v28, v77
v_add_f32_e32 v15, v15, v38
v_add_f32_e32 v15, v15, v28
Cv 38,38,28
v_exp_f32_e32 v39, v78
v_exp_f32_e32 v29, v79
v_add_f32_e32 v15, v15, v39
v_add_f32_e32 v15, v15, v29
Cv 39,39,29
v_exp_f32_e32 v40, v80
v_exp_f32_e32 v24, v81
v_add_f32_e32 v14, v14, v40
v_add_f32_e32 v14, v14, v24
Cv 40,40,24
v_exp_f32_e32 v41, v82
v_exp_f32_e32 v25, v83
v_add_f32_e32 v14, v14, v41
v_add_f32_e32 v14, v14, v25
Cv 41,41,25
v_exp_f32_e32 v42, v88
v_exp_f32_e32 v26, v89
v_add_f32_e32 v14, v14, v42
v_add_f32_e32 v14, v14, v26
)" R"(
Cv 42,42,26
v_exp_f32_e32 v43, v90
v_exp_f32_e32 v27, v91
v_add_f32_e32 v14, v14, v43
v_add_f32_e32 v14, v14, v27
Cv 43,43,27
v_exp_f32_e32 v44, v84
v_exp_f32_e32 v28, v85
v_add_f32_e32 v15, v15, v44
v_add_f32_e32 v15, v15, v28
Cv 44,44,28
v_exp_f32_e32 v45, v86
v_exp_f32_e32 v29, v87
v_add_f32_e32 v15, v15, v45
v_add_f32_e32 v15, v15, v29
Cv 45,45,29
v_exp_f32_e32 v46, v92
v_exp_f32_e32 v24, v93
v_add_f32_e32 v15, v15, v46
v_add_f32_e32 v15, v15, v24
Cv 46,46,24
v_exp_f32_e32 v47, v94
v_exp_f32_e32 v25, v95
v_add_f32_e32 v15, v15, v47
v_add_f32_e32 v15, v15, v25
Cv 47,47,25
s_nop 1
s_setpc_b64 s[30:31]
.LzH22:
s_cbranch_execz .LzH39
.LzMASK1:
s_lshl1_add_u32 s28, s57, s33
s_mov_b32 s29, 0xff800000
s_cmp_gt_u32 s28, 5
s_cbranch_scc1 .LzMASKE1
s_cmp_eq_u32 s28, 5
s_cbranch_scc1 .LzMASKB1
v_mov_b32 v104, s29
v_mov_b32 v105, s29
v_mov_b32 v106, s29
v_mov_b32 v107, s29
v_mov_b32 v112, s29
v_mov_b32 v113, s29
v_mov_b32 v114, s29
v_mov_b32 v115, s29
v_mov_b32 v116, s29
v_mov_b32 v117, s29
v_mov_b32 v118, s29
v_mov_b32 v119, s29
v_mov_b32 v120, s29
v_mov_b32 v121, s29
v_mov_b32 v122, s29
v_mov_b32 v123, s29
v_mov_b32 v124, s29
v_mov_b32 v125, s29
v_mov_b32 v126, s29
v_mov_b32 v127, s29
s_cmp_eq_u32 s28, 4
s_cbranch_scc1 .LzMASKA1
v_mov_b32 v96, s29
v_mov_b32 v97, s29
v_mov_b32 v98, s29
v_mov_b32 v99, s29
v_mov_b32 v100, s29
)" R"(
v_mov_b32 v101, s29
v_mov_b32 v102, s29
v_mov_b32 v103, s29
v_mov_b32 v108, s29
v_mov_b32 v109, s29
v_mov_b32 v110, s29
v_mov_b32 v111, s29
.LzMASKE1:
s_setpc_b64 s[30:31]
.LzMASKA1:
s_mov_b32 exec_lo, 0xf0000
s_mov_b32 exec_hi, 0xfff00ff
v_mov_b32 v96, s29
v_mov_b32 v108, s29
s_mov_b32 exec_lo, 0x1f0001
s_mov_b32 exec_hi, 0x1fff01ff
v_mov_b32 v97, s29
v_mov_b32 v109, s29
s_mov_b32 exec_lo, 0x3f0003
s_mov_b32 exec_hi, 0x3fff03ff
v_mov_b32 v98, s29
v_mov_b32 v110, s29
s_mov_b32 exec_lo, 0x7f0007
s_mov_b32 exec_hi, 0x7fff07ff
v_mov_b32 v99, s29
v_mov_b32 v111, s29
s_mov_b64 exec, -1
s_setpc_b64 s[30:31]
.LzMASKB1:
s_nop 1
v_mov_b32 v120, s29
v_mov_b32 v121, s29
v_mov_b32 v122, s29
v_mov_b32 v123, s29
s_mov_b32 exec_lo, 0xf0000
s_mov_b32 exec_hi, 0xfff00ff
v_mov_b32 v112, s29
v_mov_b32 v124, s29
s_mov_b32 exec_lo, 0x1f0001
s_mov_b32 exec_hi, 0x1fff01ff
v_mov_b32 v113, s29
v_mov_b32 v125, s29
s_mov_b32 exec_lo, 0x3f0003
s_mov_b32 exec_hi, 0x3fff03ff
v_mov_b32 v114, s29
v_mov_b32 v126, s29
s_mov_b32 exec_lo, 0x7f0007
s_mov_b32 exec_hi, 0x7fff07ff
v_mov_b32 v115, s29
v_mov_b32 v127, s29
s_mov_b64 exec, -1
s_setpc_b64 s[30:31]
.LzFIX1:
s_nop 7
v_max_f32_e32 v28, v96, v97
v_max3_f32 v28, v28, v98, v99
v_max3_f32 v28, v28, v104, v105
v_max3_f32 v28, v28, v106, v107
v_max3_f32 v28, v28, v112, v113
v_max3_f32 v28, v28, v114, v115
v_max3_f32 v28, v28, v120, v121
v_max3_f32 v28, v28, v122, v123
v_mov_b32 v24, v28
s_nop 1
)" R"(
v_permlane32_swap_b32_e32 v28, v24
v_max_f32_e32 v28, v28, v24
v_mov_b32 v24, v28
s_nop 1
v_permlane16_swap_b32_e32 v28, v24
v_max_f32_e32 v28, v28, v24
v_max_f32_e32 v29, v100, v101
v_max3_f32 v29, v29, v102, v103
v_max3_f32 v29, v29, v108, v109
v_max3_f32 v29, v29, v110, v111
v_max3_f32 v29, v29, v116, v117
v_max3_f32 v29, v29, v118, v119
v_max3_f32 v29, v29, v124, v125
v_max3_f32 v29, v29, v126, v127
v_mov_b32 v24, v29
s_nop 1
v_permlane32_swap_b32_e32 v29, v24
v_max_f32_e32 v29, v29, v24
v_mov_b32 v24, v29
s_nop 1
v_permlane16_swap_b32_e32 v29, v24
v_max_f32_e32 v29, v29, v24
s_bitcmp1_b32 s88, 0
s_cbranch_scc1 .LzFIXF1
v_max_f32_e32 v28, 0, v28
v_sub_f32_e32 v24, 0, v28
v_exp_f32_e32 v24, v24
s_nop 0
v_mul_f32_e32 v12, v24, v12
v_mul_f32_e32 v128, v24, v128
v_mul_f32_e32 v129, v24, v129
v_mul_f32_e32 v130, v24, v130
v_mul_f32_e32 v131, v24, v131
v_mul_f32_e32 v132, v24, v132
v_mul_f32_e32 v133, v24, v133
v_mul_f32_e32 v134, v24, v134
v_mul_f32_e32 v135, v24, v135
v_mul_f32_e32 v144, v24, v144
v_mul_f32_e32 v145, v24, v145
v_mul_f32_e32 v146, v24, v146
v_mul_f32_e32 v147, v24, v147
v_mul_f32_e32 v148, v24, v148
v_mul_f32_e32 v149, v24, v149
v_mul_f32_e32 v150, v24, v150
v_mul_f32_e32 v151, v24, v151
v_mul_f32_e32 v160, v24, v160
v_mul_f32_e32 v161, v24, v161
v_mul_f32_e32 v162, v24, v162
v_mul_f32_e32 v163, v24, v163
v_mul_f32_e32 v164, v24, v164
v_mul_f32_e32 v165, v24, v165
v_mul_f32_e32 v166, v24, v166
v_mul_f32_e32 v167, v24, v167
v_mul_f32_e32 v176, v24, v176
v_mul_f32_e32 v177, v24, v177
v_mul_f32_e32 v178, v24, v178
v_mul_f32_e32 v179, v24, v179
v_mul_f32_e32 v180, v24, v180
v_mul_f32_e32 v181, v24, v181
v_mul_f32_e32 v182, v24, v182
v_mul_f32_e32 v183, v24, v183
v_mul_f32_e32 v192, v24, v192
v_mul_f32_e32 v193, v24, v193
v_mul_f32_e32 v194, v24, v194
)" R"(
v_mul_f32_e32 v195, v24, v195
v_mul_f32_e32 v196, v24, v196
v_mul_f32_e32 v197, v24, v197
v_mul_f32_e32 v198, v24, v198
v_mul_f32_e32 v199, v24, v199
v_mul_f32_e32 v208, v24, v208
v_mul_f32_e32 v209, v24, v209
v_mul_f32_e32 v210, v24, v210
v_mul_f32_e32 v211, v24, v211
v_mul_f32_e32 v212, v24, v212
v_mul_f32_e32 v213, v24, v213
v_mul_f32_e32 v214, v24, v214
v_mul_f32_e32 v215, v24, v215
v_mul_f32_e32 v224, v24, v224
v_mul_f32_e32 v225, v24, v225
v_mul_f32_e32 v226, v24, v226
v_mul_f32_e32 v227, v24, v227
v_mul_f32_e32 v228, v24, v228
v_mul_f32_e32 v229, v24, v229
v_mul_f32_e32 v230, v24, v230
v_mul_f32_e32 v231, v24, v231
v_mul_f32_e32 v240, v24, v240
v_mul_f32_e32 v241, v24, v241
v_mul_f32_e32 v242, v24, v242
v_mul_f32_e32 v243, v24, v243
v_mul_f32_e32 v244, v24, v244
v_mul_f32_e32 v245, v24, v245
v_mul_f32_e32 v246, v24, v246
v_mul_f32_e32 v247, v24, v247
v_max_f32_e32 v29, 0, v29
v_sub_f32_e32 v24, 0, v29
v_exp_f32_e32 v24, v24
s_nop 0
v_mul_f32_e32 v13, v24, v13
v_mul_f32_e32 v136, v24, v136
v_mul_f32_e32 v137, v24, v137
v_mul_f32_e32 v138, v24, v138
v_mul_f32_e32 v139, v24, v139
v_mul_f32_e32 v140, v24, v140
v_mul_f32_e32 v141, v24, v141
v_mul_f32_e32 v142, v24, v142
v_mul_f32_e32 v143, v24, v143
v_mul_f32_e32 v152, v24, v152
v_mul_f32_e32 v153, v24, v153
v_mul_f32_e32 v154, v24, v154
v_mul_f32_e32 v155, v24, v155
v_mul_f32_e32 v156, v24, v156
v_mul_f32_e32 v157, v24, v157
v_mul_f32_e32 v158, v24, v158
v_mul_f32_e32 v159, v24, v159
v_mul_f32_e32 v168, v24, v168
v_mul_f32_e32 v169, v24, v169
v_mul_f32_e32 v170, v24, v170
v_mul_f32_e32 v171, v24, v171
v_mul_f32_e32 v172, v24, v172
v_mul_f32_e32 v173, v24, v173
v_mul_f32_e32 v174, v24, v174
v_mul_f32_e32 v175, v24, v175
v_mul_f32_e32 v184, v24, v184
v_mul_f32_e32 v185, v24, v185
v_mul_f32_e32 v186, v24, v186
v_mul_f32_e32 v187, v24, v187
v_mul_f32_e32 v188, v24, v188
v_mul_f32_e32 v189, v24, v189
)" R"(
v_mul_f32_e32 v190, v24, v190
v_mul_f32_e32 v191, v24, v191
v_mul_f32_e32 v200, v24, v200
v_mul_f32_e32 v201, v24, v201
v_mul_f32_e32 v202, v24, v202
v_mul_f32_e32 v203, v24, v203
v_mul_f32_e32 v204, v24, v204
v_mul_f32_e32 v205, v24, v205
v_mul_f32_e32 v206, v24, v206
v_mul_f32_e32 v207, v24, v207
v_mul_f32_e32 v216, v24, v216
v_mul_f32_e32 v217, v24, v217
v_mul_f32_e32 v218, v24, v218
v_mul_f32_e32 v219, v24, v219
v_mul_f32_e32 v220, v24, v220
v_mul_f32_e32 v221, v24, v221
v_mul_f32_e32 v222, v24, v222
v_mul_f32_e32 v223, v24, v223
v_mul_f32_e32 v232, v24, v232
v_mul_f32_e32 v233, v24, v233
v_mul_f32_e32 v234, v24, v234
v_mul_f32_e32 v235, v24, v235
v_mul_f32_e32 v236, v24, v236
v_mul_f32_e32 v237, v24, v237
v_mul_f32_e32 v238, v24, v238
v_mul_f32_e32 v239, v24, v239
v_mul_f32_e32 v248, v24, v248
v_mul_f32_e32 v249, v24, v249
v_mul_f32_e32 v250, v24, v250
v_mul_f32_e32 v251, v24, v251
v_mul_f32_e32 v252, v24, v252
v_mul_f32_e32 v253, v24, v253
v_mul_f32_e32 v254, v24, v254
v_mul_f32_e32 v255, v24, v255
s_branch .LzFIXG1
.LzFIXF1:
v_cmp_lt_f32_e32 vcc, 0xff7fffff, v28
s_nop 1
v_cndmask_b32_e32 v28, 0, v28, vcc
v_cmp_lt_f32_e32 vcc, 0xff7fffff, v29
s_nop 1
v_cndmask_b32_e32 v29, 0, v29, vcc
.LzFIXG1:
v_sub_f32_e32 v16, v16, v28
v_sub_f32_e32 v17, v17, v28
v_sub_f32_e32 v18, v18, v28
v_sub_f32_e32 v19, v19, v28
v_sub_f32_e32 v96, v96, v28
v_sub_f32_e32 v97, v97, v28
v_sub_f32_e32 v98, v98, v28
v_sub_f32_e32 v99, v99, v28
v_sub_f32_e32 v104, v104, v28
v_sub_f32_e32 v105, v105, v28
v_sub_f32_e32 v106, v106, v28
v_sub_f32_e32 v107, v107, v28
v_sub_f32_e32 v112, v112, v28
v_sub_f32_e32 v113, v113, v28
v_sub_f32_e32 v114, v114, v28
v_sub_f32_e32 v115, v115, v28
v_sub_f32_e32 v120, v120, v28
v_sub_f32_e32 v121, v121, v28
v_sub_f32_e32 v122, v122, v28
v_sub_f32_e32 v123, v123, v28
v_sub_f32_e32 v20, v20, v29
)" R"(
v_sub_f32_e32 v21, v21, v29
v_sub_f32_e32 v22, v22, v29
v_sub_f32_e32 v23, v23, v29
v_sub_f32_e32 v100, v100, v29
v_sub_f32_e32 v101, v101, v29
v_sub_f32_e32 v102, v102, v29
v_sub_f32_e32 v103, v103, v29
v_sub_f32_e32 v108, v108, v29
v_sub_f32_e32 v109, v109, v29
v_sub_f32_e32 v110, v110, v29
v_sub_f32_e32 v111, v111, v29
v_sub_f32_e32 v116, v116, v29
v_sub_f32_e32 v117, v117, v29
v_sub_f32_e32 v118, v118, v29
v_sub_f32_e32 v119, v119, v29
v_sub_f32_e32 v124, v124, v29
v_sub_f32_e32 v125, v125, v29
v_sub_f32_e32 v126, v126, v29
v_sub_f32_e32 v127, v127, v29
s_bitcmp0_b32 s88, 1
s_cbranch_scc1 .LzFIXH1
v_sub_f32_e32 v64, v64, v28
v_sub_f32_e32 v65, v65, v28
v_sub_f32_e32 v66, v66, v28
v_sub_f32_e32 v67, v67, v28
v_sub_f32_e32 v72, v72, v28
v_sub_f32_e32 v73, v73, v28
v_sub_f32_e32 v74, v74, v28
v_sub_f32_e32 v75, v75, v28
v_sub_f32_e32 v80, v80, v28
v_sub_f32_e32 v81, v81, v28
v_sub_f32_e32 v82, v82, v28
v_sub_f32_e32 v83, v83, v28
v_sub_f32_e32 v88, v88, v28
v_sub_f32_e32 v89, v89, v28
v_sub_f32_e32 v90, v90, v28
v_sub_f32_e32 v91, v91, v28
v_sub_f32_e32 v68, v68, v29
v_sub_f32_e32 v69, v69, v29
v_sub_f32_e32 v70, v70, v29
v_sub_f32_e32 v71, v71, v29
v_sub_f32_e32 v76, v76, v29
v_sub_f32_e32 v77, v77, v29
v_sub_f32_e32 v78, v78, v29
v_sub_f32_e32 v79, v79, v29
v_sub_f32_e32 v84, v84, v29
v_sub_f32_e32 v85, v85, v29
v_sub_f32_e32 v86, v86, v29
v_sub_f32_e32 v87, v87, v29
v_sub_f32_e32 v92, v92, v29
v_sub_f32_e32 v93, v93, v29
v_sub_f32_e32 v94, v94, v29
v_sub_f32_e32 v95, v95, v29
.LzFIXH1:
v_exp_f32_e32 v48, v96
v_exp_f32_e32 v26, v97
s_nop 0
v_add_f32_e32 v14, v48, v26
Cv 48,48,26
v_exp_f32_e32 v49, v98
v_exp_f32_e32 v27, v99
v_add_f32_e32 v14, v14, v49
v_add_f32_e32 v14, v14, v27
Cv 49,49,27
)" R"(
v_exp_f32_e32 v50, v104
v_exp_f32_e32 v28, v105
v_add_f32_e32 v14, v14, v50
v_add_f32_e32 v14, v14, v28
Cv 50,50,28
v_exp_f32_e32 v51, v106
v_exp_f32_e32 v29, v107
v_add_f32_e32 v14, v14, v51
v_add_f32_e32 v14, v14, v29
Cv 51,51,29
v_exp_f32_e32 v52, v100
v_exp_f32_e32 v24, v101
s_nop 0
v_add_f32_e32 v15, v52, v24
Cv 52,52,24
v_exp_f32_e32 v53, v102
v_exp_f32_e32 v25, v103
v_add_f32_e32 v15, v15, v53
v_add_f32_e32 v15, v15, v25
Cv 53,53,25
v_exp_f32_e32 v54, v108
v_exp_f32_e32 v26, v109
v_add_f32_e32 v15, v15, v54
v_add_f32_e32 v15, v15, v26
Cv 54,54,26
v_exp_f32_e32 v55, v110
v_exp_f32_e32 v27, v111
v_add_f32_e32 v15, v15, v55
v_add_f32_e32 v15, v15, v27
Cv 55,55,27
v_exp_f32_e32 v56, v112
v_exp_f32_e32 v28, v113
v_add_f32_e32 v14, v14, v56
v_add_f32_e32 v14, v14, v28
Cv 56,56,28
v_exp_f32_e32 v57, v114
v_exp_f32_e32 v29, v115
v_add_f32_e32 v14, v14, v57
v_add_f32_e32 v14, v14, v29
Cv 57,57,29
v_exp_f32_e32 v58, v120
v_exp_f32_e32 v24, v121
v_add_f32_e32 v14, v14, v58
v_add_f32_e32 v14, v14, v24
Cv 58,58,24
v_exp_f32_e32 v59, v122
v_exp_f32_e32 v25, v123
v_add_f32_e32 v14, v14, v59
v_add_f32_e32 v14, v14, v25
Cv 59,59,25
v_exp_f32_e32 v60, v116
v_exp_f32_e32 v26, v117
v_add_f32_e32 v15, v15, v60
v_add_f32_e32 v15, v15, v26
Cv 60,60,26
v_exp_f32_e32 v61, v118
v_exp_f32_e32 v27, v119
v_add_f32_e32 v15, v15, v61
v_add_f32_e32 v15, v15, v27
Cv 61,61,27
v_exp_f32_e32 v62, v124
v_exp_f32_e32 v28, v125
v_add_f32_e32 v15, v15, v62
v_add_f32_e32 v15, v15, v28
)" R"(
Cv 62,62,28
v_exp_f32_e32 v63, v126
v_exp_f32_e32 v29, v127
v_add_f32_e32 v15, v15, v63
v_add_f32_e32 v15, v15, v29
Cv 63,63,29
s_nop 1
s_setpc_b64 s[30:31]
.Lzsp1:
s_sub_u32 s53, s53, 1
s_cbranch_scc1 .Lzsq1
s_sleep 1
buffer_load_dword v4, v2, s[20:23], s85 offen sc0 sc1
s_waitcnt vmcnt(0)
v_xor_b32 v4, s3, v4
v_cmp_gt_u32_e32 vcc, 2, v4
s_andn2_b32 s28, 0xff, vcc_lo
s_cbranch_scc1 .Lzsp1
s_branch .Lzok1
.Lzsq1:
s_mov_b32 s53, 0
s_branch .Lzok1
.Lzsp2:
s_sub_u32 s53, s53, 1
s_cbranch_scc1 .Lzsq2
s_sleep 1
buffer_load_dword v4, v7, s[20:23], s52 offen offset:0 sc0 sc1
s_waitcnt vmcnt(0)
v_cmp_eq_u32_e32 vcc, s3, v4
s_andn2_b64 s[28:29], 1, vcc
s_cbranch_scc1 .Lzsp2
s_branch .Lzok2
.Lzsq2:
s_mov_b32 s53, 0
s_branch .Lzok2
.Lzsp3:
s_sub_u32 s53, s53, 1
s_cbranch_scc1 .Lzsq3
s_sleep 1
buffer_load_dword v4, v2, s[20:23], s85 offen sc0 sc1
s_waitcnt vmcnt(0)
v_xor_b32 v4, s3, v4
v_cmp_gt_u32_e32 vcc, 2, v4
s_andn2_b32 s28, 0xff, vcc_lo
s_cbranch_scc1 .Lzsp3
s_branch .Lzok3
.Lzsq3:
s_mov_b32 s53, 0
s_branch .Lzok3
.Lzsp4:
s_sub_u32 s53, s53, 1
s_cbranch_scc1 .Lzsq4
s_sleep 1
buffer_load_dword v4, v7, s[20:23], s52 offen offset:0 sc0 sc1
s_waitcnt vmcnt(0)
v_cmp_eq_u32_e32 vcc, s3, v4
s_andn2_b64 s[28:29], 1, vcc
s_cbranch_scc1 .Lzsp4
s_branch .Lzok4
.Lzsq4:
s_mov_b32 s53, 0
s_branch .Lzok4
.Lzsp5:
s_sub_u32 s53, s53, 1
)" R"(
s_cbranch_scc1 .Lzsq5
s_sleep 1
buffer_load_dword v4, v5, s[20:23], s50 offen offset:0 sc0 sc1
s_waitcnt vmcnt(0)
v_cmp_eq_u32_e32 vcc, s3, v4
s_andn2_b64 s[28:29], -1, vcc
s_cbranch_scc1 .Lzsp5
s_branch .Lzok5
.Lzsq5:
s_mov_b32 s53, 0
s_branch .Lzok5
.Lzsp6:
s_sub_u32 s53, s53, 1
s_cbranch_scc1 .Lzsq6
s_sleep 1
buffer_load_dword v4, v5, s[20:23], s50 offen offset:0 sc0 sc1
s_waitcnt vmcnt(0)
v_cmp_eq_u32_e32 vcc, s3, v4
s_andn2_b64 s[28:29], -1, vcc
s_cbranch_scc1 .Lzsp6
s_branch .Lzok6
.Lzsq6:
s_mov_b32 s53, 0
s_branch .Lzok6
.Lzsp7:
s_sub_u32 s53, s53, 1
s_cbranch_scc1 .Lzsq7
s_sleep 1
buffer_load_dwordx2 v[24:25], v7, s[20:23], s83 offen sc0 sc1
s_waitcnt vmcnt(0)
v_cmp_eq_u32_e32 vcc, s3, v25
s_andn2_b64 s[28:29], 1, vcc
s_cbranch_scc1 .Lzsp7
s_branch .Lzok7
.Lzsq7:
s_mov_b32 s53, 0
s_branch .Lzok7
.LzT2S:
s_cmp_lg_u32 s33, 0
s_cbranch_scc1 .LzT2E
.LzT2:
s_mov_b32 s28, 0x1500000
buffer_load_dwordx2 v[32:33], v1, s[20:23], s28 offen offset:0 sc0 sc1
buffer_load_dwordx2 v[34:35], v1, s[20:23], s28 offen offset:1024 sc0 sc1
buffer_load_dwordx2 v[36:37], v1, s[20:23], s28 offen offset:2048 sc0 sc1
buffer_load_dwordx2 v[38:39], v1, s[20:23], s28 offen offset:3072 sc0 sc1
s_mov_b32 s28, 0x1501000
buffer_load_dwordx2 v[40:41], v1, s[20:23], s28 offen offset:0 sc0 sc1
buffer_load_dwordx2 v[42:43], v1, s[20:23], s28 offen offset:1024 sc0 sc1
buffer_load_dwordx2 v[44:45], v1, s[20:23], s28 offen offset:2048 sc0 sc1
buffer_load_dwordx2 v[46:47], v1, s[20:23], s28 offen offset:3072 sc0 sc1
s_mov_b32 s28, 0x1502000
buffer_load_dwordx2 v[48:49], v1, s[20:23], s28 offen offset:0 sc0 sc1
buffer_load_dwordx2 v[50:51], v1, s[20:23], s28 offen offset:1024 sc0 sc1
buffer_load_dwordx2 v[52:53], v1, s[20:23], s28 offen offset:2048 sc0 sc1
buffer_load_dwordx2 v[54:55], v1, s[20:23], s28 offen offset:3072 sc0 sc1
s_mov_b32 s28, 0x1503000
buffer_load_dwordx2 v[56:57], v1, s[20:23], s28 offen offset:0 sc0 sc1
buffer_load_dwordx2 v[58:59], v1, s[20:23], s28 offen offset:1024 sc0 sc1
buffer_load_dwordx2 v[60:61], v1, s[20:23], s28 offen offset:2048 sc0 sc1
buffer_load_dwordx2 v[62:63], v1, s[20:23], s28 offen offset:3072 sc0 sc1
s_mov_b32 s28, 0x1504000
buffer_load_dwordx2 v[64:65], v1, s[20:23], s28 offen offset:0 sc0 sc1
buffer_load_dwordx2 v[66:67], v1, s[20:23], s28 offen offset:1024 sc0 sc1
)" R"(
buffer_load_dwordx2 v[68:69], v1, s[20:23], s28 offen offset:2048 sc0 sc1
buffer_load_dwordx2 v[70:71], v1, s[20:23], s28 offen offset:3072 sc0 sc1
s_mov_b32 s28, 0x1505000
buffer_load_dwordx2 v[72:73], v1, s[20:23], s28 offen offset:0 sc0 sc1
buffer_load_dwordx2 v[74:75], v1, s[20:23], s28 offen offset:1024 sc0 sc1
buffer_load_dwordx2 v[76:77], v1, s[20:23], s28 offen offset:2048 sc0 sc1
buffer_load_dwordx2 v[78:79], v1, s[20:23], s28 offen offset:3072 sc0 sc1
s_mov_b32 s28, 0x1506000
buffer_load_dwordx2 v[80:81], v1, s[20:23], s28 offen offset:0 sc0 sc1
buffer_load_dwordx2 v[82:83], v1, s[20:23], s28 offen offset:1024 sc0 sc1
buffer_load_dwordx2 v[84:85], v1, s[20:23], s28 offen offset:2048 sc0 sc1
buffer_load_dwordx2 v[86:87], v1, s[20:23], s28 offen offset:3072 sc0 sc1
s_mov_b32 s28, 0x1507000
buffer_load_dwordx2 v[88:89], v1, s[20:23], s28 offen offset:0 sc0 sc1
buffer_load_dwordx2 v[90:91], v1, s[20:23], s28 offen offset:1024 sc0 sc1
buffer_load_dwordx2 v[92:93], v1, s[20:23], s28 offen offset:2048 sc0 sc1
buffer_load_dwordx2 v[94:95], v1, s[20:23], s28 offen offset:3072 sc0 sc1
v_mov_b32 v26, 0
v_mov_b32 v27, -1
s_mov_b32 s28, 0x1508c00
buffer_store_dword v26, v7, s[20:23], s28 offen sc0 sc1
buffer_store_dword v26, v7, s[20:23], s28 offen offset:128 sc0 sc1
s_waitcnt vmcnt(0)
v_max3_u32 v26, v26, v33, v35
v_min3_u32 v27, v27, v33, v35
v_max3_u32 v26, v26, v37, v39
v_min3_u32 v27, v27, v37, v39
v_max3_u32 v26, v26, v41, v43
v_min3_u32 v27, v27, v41, v43
v_max3_u32 v26, v26, v45, v47
v_min3_u32 v27, v27, v45, v47
v_max3_u32 v26, v26, v49, v51
v_min3_u32 v27, v27, v49, v51
v_max3_u32 v26, v26, v53, v55
v_min3_u32 v27, v27, v53, v55
v_max3_u32 v26, v26, v57, v59
v_min3_u32 v27, v27, v57, v59
v_max3_u32 v26, v26, v61, v63
v_min3_u32 v27, v27, v61, v63
v_max3_u32 v26, v26, v65, v67
v_min3_u32 v27, v27, v65, v67
v_max3_u32 v26, v26, v69, v71
v_min3_u32 v27, v27, v69, v71
v_max3_u32 v26, v26, v73, v75
v_min3_u32 v27, v27, v73, v75
v_max3_u32 v26, v26, v77, v79
v_min3_u32 v27, v27, v77, v79
v_max3_u32 v26, v26, v81, v83
v_min3_u32 v27, v27, v81, v83
v_max3_u32 v26, v26, v85, v87
v_min3_u32 v27, v27, v85, v87
v_max3_u32 v26, v26, v89, v91
v_min3_u32 v27, v27, v89, v91
v_max3_u32 v26, v26, v93, v95
v_min3_u32 v27, v27, v93, v95
v_cmp_ne_u32_e32 vcc, s3, v26
v_cmp_ne_u32_e64 s[28:29], s3, v27
s_or_b64 vcc, vcc, s[28:29]
s_cbranch_scc0 .LzT2G
s_sub_u32 s53, s53, 1
s_cbranch_scc0 .LzT2
s_mov_b32 s53, 0
.LzT2G:
v_max_u32 v26, v32, v34
)" R"(
v_min_u32 v27, v32, v34
v_med3_u32 v27, v26, v27, v36
v_max_u32 v26, v26, v36
v_med3_u32 v27, v26, v27, v38
v_max_u32 v26, v26, v38
v_med3_u32 v27, v26, v27, v40
v_max_u32 v26, v26, v40
v_med3_u32 v27, v26, v27, v42
v_max_u32 v26, v26, v42
v_med3_u32 v27, v26, v27, v44
v_max_u32 v26, v26, v44
v_med3_u32 v27, v26, v27, v46
v_max_u32 v26, v26, v46
v_med3_u32 v27, v26, v27, v48
v_max_u32 v26, v26, v48
v_med3_u32 v27, v26, v27, v50
v_max_u32 v26, v26, v50
v_med3_u32 v27, v26, v27, v52
v_max_u32 v26, v26, v52
v_med3_u32 v27, v26, v27, v54
v_max_u32 v26, v26, v54
v_med3_u32 v27, v26, v27, v56
v_max_u32 v26, v26, v56
v_med3_u32 v27, v26, v27, v58
v_max_u32 v26, v26, v58
v_med3_u32 v27, v26, v27, v60
v_max_u32 v26, v26, v60
v_med3_u32 v27, v26, v27, v62
v_max_u32 v26, v26, v62
v_med3_u32 v27, v26, v27, v64
v_max_u32 v26, v26, v64
v_med3_u32 v27, v26, v27, v66
v_max_u32 v26, v26, v66
v_med3_u32 v27, v26, v27, v68
v_max_u32 v26, v26, v68
v_med3_u32 v27, v26, v27, v70
v_max_u32 v26, v26, v70
v_med3_u32 v27, v26, v27, v72
v_max_u32 v26, v26, v72
v_med3_u32 v27, v26, v27, v74
v_max_u32 v26, v26, v74
v_med3_u32 v27, v26, v27, v76
v_max_u32 v26, v26, v76
v_med3_u32 v27, v26, v27, v78
v_max_u32 v26, v26, v78
v_med3_u32 v27, v26, v27, v80
v_max_u32 v26, v26, v80
v_med3_u32 v27, v26, v27, v82
v_max_u32 v26, v26, v82
v_med3_u32 v27, v26, v27, v84
v_max_u32 v26, v26, v84
v_med3_u32 v27, v26, v27, v86
v_max_u32 v26, v26, v86
v_med3_u32 v27, v26, v27, v88
v_max_u32 v26, v26, v88
v_med3_u32 v27, v26, v27, v90
v_max_u32 v26, v26, v90
v_med3_u32 v27, v26, v27, v92
v_max_u32 v26, v26, v92
v_med3_u32 v27, v26, v27, v94
v_max_u32 v26, v26, v94
v_mov_b32 v24, v26
v_mov_b32 v25, v27
s_nop 1
)" R"(
v_permlane32_swap_b32_e32 v26, v24
v_permlane32_swap_b32_e32 v27, v25
v_max_u32 v27, v27, v25
v_med3_u32 v27, v26, v27, v24
v_max_u32 v26, v26, v24
v_mov_b32 v24, v26
v_mov_b32 v25, v27
s_nop 1
v_permlane16_swap_b32_e32 v26, v24
v_permlane16_swap_b32_e32 v27, v25
v_max_u32 v27, v27, v25
v_med3_u32 v27, v26, v27, v24
v_max_u32 v26, v26, v24
s_nop 1
v_mov_b32_dpp v24, v26 row_ror:8 row_mask:0xf bank_mask:0xf
v_mov_b32_dpp v25, v27 row_ror:8 row_mask:0xf bank_mask:0xf
v_max_u32 v27, v27, v25
v_med3_u32 v27, v26, v27, v24
v_max_u32 v26, v26, v24
s_nop 1
v_mov_b32_dpp v24, v26 row_ror:4 row_mask:0xf bank_mask:0xf
v_mov_b32_dpp v25, v27 row_ror:4 row_mask:0xf bank_mask:0xf
v_max_u32 v27, v27, v25
v_med3_u32 v27, v26, v27, v24
v_max_u32 v26, v26, v24
s_nop 1
v_mov_b32_dpp v24, v26 row_ror:2 row_mask:0xf bank_mask:0xf
v_mov_b32_dpp v25, v27 row_ror:2 row_mask:0xf bank_mask:0xf
v_max_u32 v27, v27, v25
v_med3_u32 v27, v26, v27, v24
v_max_u32 v26, v26, v24
s_nop 1
v_mov_b32_dpp v24, v26 row_ror:1 row_mask:0xf bank_mask:0xf
v_mov_b32_dpp v25, v27 row_ror:1 row_mask:0xf bank_mask:0xf
v_max_u32 v27, v27, v25
v_med3_u32 v27, v26, v27, v24
v_max_u32 v26, v26, v24
v_and_b32 v26, 0xffff, v26
v_lshl_or_b32 v26, v27, 16, v26
v_not_b32 v24, v26
v_mov_b32 v25, s3
s_mov_b32 s28, 0x1508000
buffer_store_dwordx2 v[24:25], v7, s[20:23], s28 offen sc0 sc1
s_branch .LzT2E
.LzA7:
s_waitcnt vmcnt(0)
s_nop 7
v_mul_f32_e32 v96, s54, v96
v_mul_f32_e32 v97, s54, v97
Cv 96,96,97
v_accvgpr_write_b32 a112, v96
v_mul_f32_e32 v98, s54, v98
v_mul_f32_e32 v99, s54, v99
Cv 98,98,99
v_accvgpr_write_b32 a113, v98
v_mul_f32_e32 v100, s54, v100
v_mul_f32_e32 v101, s54, v101
Cv 100,100,101
v_accvgpr_write_b32 a114, v100
v_mul_f32_e32 v102, s54, v102
v_mul_f32_e32 v103, s54, v103
Cv 102,102,103
v_accvgpr_write_b32 a115, v102
v_mul_f32_e32 v104, s54, v104
)" R"(
v_mul_f32_e32 v105, s54, v105
Cv 104,104,105
v_accvgpr_write_b32 a116, v104
v_mul_f32_e32 v106, s54, v106
v_mul_f32_e32 v107, s54, v107
Cv 106,106,107
v_accvgpr_write_b32 a117, v106
v_mul_f32_e32 v108, s54, v108
v_mul_f32_e32 v109, s54, v109
Cv 108,108,109
v_accvgpr_write_b32 a118, v108
v_mul_f32_e32 v110, s54, v110
v_mul_f32_e32 v111, s54, v111
Cv 110,110,111
v_accvgpr_write_b32 a119, v110
v_mul_f32_e32 v112, s54, v112
v_mul_f32_e32 v113, s54, v113
Cv 112,112,113
v_accvgpr_write_b32 a120, v112
v_mul_f32_e32 v114, s54, v114
v_mul_f32_e32 v115, s54, v115
Cv 114,114,115
v_accvgpr_write_b32 a121, v114
v_mul_f32_e32 v116, s54, v116
v_mul_f32_e32 v117, s54, v117
Cv 116,116,117
v_accvgpr_write_b32 a122, v116
v_mul_f32_e32 v118, s54, v118
v_mul_f32_e32 v119, s54, v119
Cv 118,118,119
v_accvgpr_write_b32 a123, v118
v_mul_f32_e32 v120, s54, v120
v_mul_f32_e32 v121, s54, v121
Cv 120,120,121
v_accvgpr_write_b32 a124, v120
v_mul_f32_e32 v122, s54, v122
v_mul_f32_e32 v123, s54, v123
Cv 122,122,123
v_accvgpr_write_b32 a125, v122
v_mul_f32_e32 v124, s54, v124
v_mul_f32_e32 v125, s54, v125
Cv 124,124,125
v_accvgpr_write_b32 a126, v124
v_mul_f32_e32 v126, s54, v126
v_mul_f32_e32 v127, s54, v127
Cv 126,126,127
v_accvgpr_write_b32 a127, v126
v_accvgpr_mov_b32 a64, a224
v_accvgpr_mov_b32 a65, a225
v_accvgpr_mov_b32 a66, a226
v_accvgpr_mov_b32 a67, a227
v_accvgpr_mov_b32 a68, a228
v_accvgpr_mov_b32 a69, a229
v_accvgpr_mov_b32 a70, a230
v_accvgpr_mov_b32 a71, a231
v_accvgpr_mov_b32 a72, a232
v_accvgpr_mov_b32 a73, a233
v_accvgpr_mov_b32 a74, a234
v_accvgpr_mov_b32 a75, a235
v_accvgpr_mov_b32 a76, a236
v_accvgpr_mov_b32 a77, a237
v_accvgpr_mov_b32 a78, a238
v_accvgpr_mov_b32 a79, a239
v_accvgpr_mov_b32 a80, a240
)" R"(
v_accvgpr_mov_b32 a81, a241
v_accvgpr_mov_b32 a82, a242
v_accvgpr_mov_b32 a83, a243
v_accvgpr_mov_b32 a84, a244
v_accvgpr_mov_b32 a85, a245
v_accvgpr_mov_b32 a86, a246
v_accvgpr_mov_b32 a87, a247
v_accvgpr_mov_b32 a88, a248
v_accvgpr_mov_b32 a89, a249
v_accvgpr_mov_b32 a90, a250
v_accvgpr_mov_b32 a91, a251
v_accvgpr_mov_b32 a92, a252
v_accvgpr_mov_b32 a93, a253
v_accvgpr_mov_b32 a94, a254
v_accvgpr_mov_b32 a95, a255
v_accvgpr_mov_b32 a96, a192
v_accvgpr_mov_b32 a97, a193
v_accvgpr_mov_b32 a98, a194
v_accvgpr_mov_b32 a99, a195
v_accvgpr_mov_b32 a100, a196
v_accvgpr_mov_b32 a101, a197
v_accvgpr_mov_b32 a102, a198
v_accvgpr_mov_b32 a103, a199
v_accvgpr_mov_b32 a104, a200
v_accvgpr_mov_b32 a105, a201
v_accvgpr_mov_b32 a106, a202
v_accvgpr_mov_b32 a107, a203
v_accvgpr_mov_b32 a108, a204
v_accvgpr_mov_b32 a109, a205
v_accvgpr_mov_b32 a110, a206
v_accvgpr_mov_b32 a111, a207
s_mov_b32 s94, 2
s_nop 1
s_branch .LzA7R
.Lzsp8:
s_sub_u32 s53, s53, 1
s_cbranch_scc1 .Lzsq8
s_sleep 1
buffer_load_dword v4, v5, s[20:23], s50 offen offset:0 sc0 sc1
s_waitcnt vmcnt(0)
v_cmp_eq_u32_e32 vcc, s3, v4
s_andn2_b64 s[28:29], -1, vcc
s_cbranch_scc1 .Lzsp8
s_branch .Lzok8
.Lzsq8:
s_mov_b32 s53, 0
s_branch .Lzok8
.Lzmk0:
s_call_b64 s[30:31], .LzMASK0
s_branch .Lzmr0
.Lzfx0:
s_mov_b32 s88, 3
s_call_b64 s[30:31], .LzFIX0
s_branch .Lzfr0
.Lzmk1:
s_call_b64 s[30:31], .LzMASK1
s_branch .Lzmr1
.Lzsp9:
s_sub_u32 s53, s53, 1
s_cbranch_scc1 .Lzsq9
s_sleep 1
buffer_load_dword v4, v5, s[20:23], s50 offen offset:0 sc0 sc1
s_waitcnt vmcnt(0)
v_cmp_eq_u32_e32 vcc, s3, v4
)" R"(
s_andn2_b64 s[28:29], -1, vcc
s_cbranch_scc1 .Lzsp9
s_branch .Lzok9
.Lzsq9:
s_mov_b32 s53, 0
s_branch .Lzok9
.Lzfx1:
s_mov_b32 s88, 2
s_call_b64 s[30:31], .LzFIX1
s_branch .Lzfr1
.Lzmk2:
s_call_b64 s[30:31], .LzMASK0
s_branch .Lzmr2
.Lzfx2:
s_mov_b32 s88, 2
s_call_b64 s[30:31], .LzFIX0
s_branch .Lzfr2
.Lzmk3:
s_call_b64 s[30:31], .LzMASK1
s_branch .Lzmr3
.Lzsp10:
s_sub_u32 s53, s53, 1
s_cbranch_scc1 .Lzsq10
s_sleep 1
buffer_load_dword v4, v5, s[20:23], s50 offen offset:0 sc0 sc1
s_waitcnt vmcnt(0)
v_cmp_eq_u32_e32 vcc, s3, v4
s_andn2_b64 s[28:29], -1, vcc
s_cbranch_scc1 .Lzsp10
s_branch .Lzok10
.Lzsq10:
s_mov_b32 s53, 0
s_branch .Lzok10
.Lzfx3:
s_mov_b32 s88, 0
s_call_b64 s[30:31], .LzFIX1
s_branch .Lzfr3
.Lzmk4:
s_call_b64 s[30:31], .LzMASK0
s_branch .Lzmr4
.Lzfx4:
s_mov_b32 s88, 3
s_call_b64 s[30:31], .LzFIX0
s_branch .Lzfr4
.Lzmk5:
s_call_b64 s[30:31], .LzMASK1
s_branch .Lzmr5
.Lzfx5:
s_mov_b32 s88, 0
s_call_b64 s[30:31], .LzFIX1
s_branch .Lzfr5
.Lzsp11:
s_sub_u32 s53, s53, 1
s_cbranch_scc1 .Lzsq11
s_sleep 1
buffer_load_dword v4, v7, s[20:23], s52 offen offset:0 sc0 sc1
s_waitcnt vmcnt(0)
v_cmp_eq_u32_e32 vcc, s3, v4
s_andn2_b64 s[28:29], 1, vcc
s_cbranch_scc1 .Lzsp11
s_branch .Lzok11
.Lzsq11:
s_mov_b32 s53, 0
s_branch .Lzok11
)" R"(
.Lzsp12:
s_sub_u32 s53, s53, 1
s_cbranch_scc1 .Lzsq12
s_sleep 1
buffer_load_dword v4, v7, s[20:23], s52 offen offset:128 sc0 sc1
s_waitcnt vmcnt(0)
v_cmp_eq_u32_e32 vcc, s3, v4
s_andn2_b64 s[28:29], 1, vcc
s_cbranch_scc1 .Lzsp12
s_branch .Lzok12
.Lzsq12:
s_mov_b32 s53, 0
s_branch .Lzok12
.Lzsp13:
s_sub_u32 s53, s53, 1
s_cbranch_scc1 .Lzsq13
s_sleep 1
buffer_load_dword v4, v2, s[20:23], s85 offen sc0 sc1
s_waitcnt vmcnt(0)
v_xor_b32 v4, s3, v4
v_cmp_gt_u32_e32 vcc, 2, v4
s_andn2_b32 s28, 0xff, vcc_lo
s_cbranch_scc1 .Lzsp13
s_branch .Lzok13
.Lzsq13:
s_mov_b32 s53, 0
s_branch .Lzok13
.Lzsp14:
s_sub_u32 s53, s53, 1
s_cbranch_scc1 .Lzsq14
s_sleep 1
buffer_load_dword v4, v5, s[20:23], s50 offen offset:0 sc0 sc1
s_waitcnt vmcnt(0)
v_cmp_eq_u32_e32 vcc, s3, v4
s_andn2_b64 s[28:29], -1, vcc
s_cbranch_scc1 .Lzsp14
s_branch .Lzok14
.Lzsq14:
s_mov_b32 s53, 0
s_branch .Lzok14
.Lzsp15:
s_sub_u32 s53, s53, 1
s_cbranch_scc1 .Lzsq15
s_sleep 1
buffer_load_dword v4, v5, s[20:23], s50 offen offset:0 sc0 sc1
s_waitcnt vmcnt(0)
v_cmp_eq_u32_e32 vcc, s3, v4
s_andn2_b64 s[28:29], -1, vcc
s_cbranch_scc1 .Lzsp15
s_branch .Lzok15
.Lzsq15:
s_mov_b32 s53, 0
s_branch .Lzok15
.Lzsp16:
s_sub_u32 s53, s53, 1
s_cbranch_scc1 .Lzsq16
s_sleep 1
buffer_load_dword v4, v5, s[20:23], s50 offen offset:0 sc0 sc1
s_waitcnt vmcnt(0)
v_cmp_eq_u32_e32 vcc, s3, v4
s_andn2_b64 s[28:29], -1, vcc
s_cbranch_scc1 .Lzsp16
s_branch .Lzok16
.Lzsq16:
)" R"(
s_mov_b32 s53, 0
s_branch .Lzok16
.Lzsp17:
s_sub_u32 s53, s53, 1
s_cbranch_scc1 .Lzsq17
s_sleep 1
buffer_load_dword v4, v5, s[20:23], s50 offen offset:0 sc0 sc1
s_waitcnt vmcnt(0)
v_cmp_eq_u32_e32 vcc, s3, v4
s_andn2_b64 s[28:29], -1, vcc
s_cbranch_scc1 .Lzsp17
s_branch .Lzok17
.Lzsq17:
s_mov_b32 s53, 0
s_branch .Lzok17
.Lzsp18:
s_sub_u32 s53, s53, 1
s_cbranch_scc1 .Lzsq18
s_sleep 1
buffer_load_dword v4, v5, s[20:23], s50 offen offset:0 sc0 sc1
s_waitcnt vmcnt(0)
v_cmp_eq_u32_e32 vcc, s3, v4
s_andn2_b64 s[28:29], -1, vcc
s_cbranch_scc1 .Lzsp18
s_branch .Lzok18
.Lzsq18:
s_mov_b32 s53, 0
s_branch .Lzok18
.Lzfx6:
s_mov_b32 s88, 3
s_call_b64 s[30:31], .LzFIX0
s_branch .Lzfr6
.Lzsp19:
s_sub_u32 s53, s53, 1
s_cbranch_scc1 .Lzsq19
s_sleep 1
buffer_load_dword v4, v5, s[20:23], s50 offen offset:0 sc0 sc1
s_waitcnt vmcnt(0)
v_cmp_eq_u32_e32 vcc, s3, v4
s_andn2_b64 s[28:29], -1, vcc
s_cbranch_scc1 .Lzsp19
s_branch .Lzok19
.Lzsq19:
s_mov_b32 s53, 0
s_branch .Lzok19
.Lzfx7:
s_mov_b32 s88, 2
s_call_b64 s[30:31], .LzFIX1
s_branch .Lzfr7
.Lzmk6:
s_call_b64 s[30:31], .LzMASK0
s_branch .Lzmr6
.Lzfx8:
s_mov_b32 s88, 2
s_call_b64 s[30:31], .LzFIX0
s_branch .Lzfr8
.Lzmk7:
s_call_b64 s[30:31], .LzMASK1
s_branch .Lzmr7
.Lzsp20:
s_sub_u32 s53, s53, 1
s_cbranch_scc1 .Lzsq20
s_sleep 1
buffer_load_dword v4, v5, s[20:23], s50 offen offset:0 sc0 sc1
)" R"(
s_waitcnt vmcnt(0)
v_cmp_eq_u32_e32 vcc, s3, v4
s_andn2_b64 s[28:29], -1, vcc
s_cbranch_scc1 .Lzsp20
s_branch .Lzok20
.Lzsq20:
s_mov_b32 s53, 0
s_branch .Lzok20
.Lzfx9:
s_mov_b32 s88, 2
s_call_b64 s[30:31], .LzFIX1
s_branch .Lzfr9
.Lzmk8:
s_call_b64 s[30:31], .LzMASK0
s_branch .Lzmr8
.Lzfx10:
s_mov_b32 s88, 2
s_call_b64 s[30:31], .LzFIX0
s_branch .Lzfr10
.Lzmk9:
s_call_b64 s[30:31], .LzMASK1
s_branch .Lzmr9
.Lzsp21:
s_sub_u32 s53, s53, 1
s_cbranch_scc1 .Lzsq21
s_sleep 1
buffer_load_dword v4, v7, s[20:23], s83 offen offset:4 sc0 sc1
s_waitcnt vmcnt(0)
v_cmp_eq_u32_e32 vcc, s3, v4
s_andn2_b64 s[28:29], 1, vcc
s_cbranch_scc1 .Lzsp21
s_branch .Lzok21
.Lzsq21:
s_mov_b32 s53, 0
s_branch .Lzok21
.Lzsp22:
s_sub_u32 s53, s53, 1
s_cbranch_scc1 .Lzsq22
s_sleep 1
buffer_load_dword v4, v7, s[20:23], s65 offen offset:0 sc0 sc1
s_waitcnt vmcnt(0)
v_cmp_eq_u32_e32 vcc, s3, v4
s_andn2_b64 s[28:29], s[60:61], vcc
s_cbranch_scc1 .Lzsp22
s_branch .Lzok22
.Lzsq22:
s_mov_b32 s53, 0
s_branch .Lzok22
.Lzfx11:
s_mov_b32 s88, 2
s_call_b64 s[30:31], .LzFIX1
s_branch .Lzfr11
.Lzmk10:
s_call_b64 s[30:31], .LzMASK0
s_branch .Lzmr10
.Lzfx12:
s_mov_b32 s88, 2
s_call_b64 s[30:31], .LzFIX0
s_branch .Lzfr12
.Lzmk11:
s_call_b64 s[30:31], .LzMASK1
s_branch .Lzmr11
.Lzfx13:
s_mov_b32 s88, 0
)" R"(
s_call_b64 s[30:31], .LzFIX1
s_branch .Lzfr13
.LzMRG:
s_waitcnt vmcnt(0)
s_nop 7
v_mov_b32 v25, v64
v_mov_b32 v24, v25
s_nop 1
v_permlane32_swap_b32_e32 v25, v24
v_add_f32_e32 v25, v25, v24
v_mov_b32 v24, v25
s_nop 1
v_permlane16_swap_b32_e32 v25, v24
v_add_f32_e32 v25, v25, v24
v_cmp_lt_f32_e32 vcc, 0, v25
s_nop 1
v_cndmask_b32_e32 v66, v16, v66, vcc
v_sub_f32_e32 v28, v16, v66
v_min_f32_e32 v26, 0, v28
v_exp_f32_e32 v26, v26
v_max_f32_e32 v28, 0, v28
v_sub_f32_e32 v24, 0, v28
v_exp_f32_e32 v24, v24
v_mul_f32_e32 v64, v26, v64
v_mul_f32_e32 v12, v24, v12
v_mul_f32_e32 v128, v24, v128
v_mul_f32_e32 v129, v24, v129
v_mul_f32_e32 v130, v24, v130
v_mul_f32_e32 v131, v24, v131
v_mul_f32_e32 v132, v24, v132
v_mul_f32_e32 v133, v24, v133
v_mul_f32_e32 v134, v24, v134
v_mul_f32_e32 v135, v24, v135
v_mul_f32_e32 v144, v24, v144
v_mul_f32_e32 v145, v24, v145
v_mul_f32_e32 v146, v24, v146
v_mul_f32_e32 v147, v24, v147
v_mul_f32_e32 v148, v24, v148
v_mul_f32_e32 v149, v24, v149
v_mul_f32_e32 v150, v24, v150
v_mul_f32_e32 v151, v24, v151
v_mul_f32_e32 v160, v24, v160
v_mul_f32_e32 v161, v24, v161
v_mul_f32_e32 v162, v24, v162
v_mul_f32_e32 v163, v24, v163
v_mul_f32_e32 v164, v24, v164
v_mul_f32_e32 v165, v24, v165
v_mul_f32_e32 v166, v24, v166
v_mul_f32_e32 v167, v24, v167
v_mul_f32_e32 v176, v24, v176
v_mul_f32_e32 v177, v24, v177
v_mul_f32_e32 v178, v24, v178
v_mul_f32_e32 v179, v24, v179
v_mul_f32_e32 v180, v24, v180
v_mul_f32_e32 v181, v24, v181
v_mul_f32_e32 v182, v24, v182
v_mul_f32_e32 v183, v24, v183
v_mul_f32_e32 v192, v24, v192
v_mul_f32_e32 v193, v24, v193
v_mul_f32_e32 v194, v24, v194
v_mul_f32_e32 v195, v24, v195
v_mul_f32_e32 v196, v24, v196
v_mul_f32_e32 v197, v24, v197
v_mul_f32_e32 v198, v24, v198
)" R"(
v_mul_f32_e32 v199, v24, v199
v_mul_f32_e32 v208, v24, v208
v_mul_f32_e32 v209, v24, v209
v_mul_f32_e32 v210, v24, v210
v_mul_f32_e32 v211, v24, v211
v_mul_f32_e32 v212, v24, v212
v_mul_f32_e32 v213, v24, v213
v_mul_f32_e32 v214, v24, v214
v_mul_f32_e32 v215, v24, v215
v_mul_f32_e32 v224, v24, v224
v_mul_f32_e32 v225, v24, v225
v_mul_f32_e32 v226, v24, v226
v_mul_f32_e32 v227, v24, v227
v_mul_f32_e32 v228, v24, v228
v_mul_f32_e32 v229, v24, v229
v_mul_f32_e32 v230, v24, v230
v_mul_f32_e32 v231, v24, v231
v_mul_f32_e32 v240, v24, v240
v_mul_f32_e32 v241, v24, v241
v_mul_f32_e32 v242, v24, v242
v_mul_f32_e32 v243, v24, v243
v_mul_f32_e32 v244, v24, v244
v_mul_f32_e32 v245, v24, v245
v_mul_f32_e32 v246, v24, v246
v_mul_f32_e32 v247, v24, v247
v_sub_f32_e32 v16, v16, v28
v_sub_f32_e32 v17, v17, v28
v_sub_f32_e32 v18, v18, v28
v_sub_f32_e32 v19, v19, v28
v_sub_f32_e32 v96, v96, v28
v_sub_f32_e32 v97, v97, v28
v_sub_f32_e32 v98, v98, v28
v_sub_f32_e32 v99, v99, v28
v_sub_f32_e32 v104, v104, v28
v_sub_f32_e32 v105, v105, v28
v_sub_f32_e32 v106, v106, v28
v_sub_f32_e32 v107, v107, v28
v_sub_f32_e32 v112, v112, v28
v_sub_f32_e32 v113, v113, v28
v_sub_f32_e32 v114, v114, v28
v_sub_f32_e32 v115, v115, v28
v_sub_f32_e32 v120, v120, v28
v_sub_f32_e32 v121, v121, v28
v_sub_f32_e32 v122, v122, v28
v_sub_f32_e32 v123, v123, v28
v_accvgpr_read_b32 v25, a128
v_lshlrev_b32 v24, 16, v25
v_and_b32 v25, 0xffff0000, v25
v_mul_f32_e32 v24, v26, v24
v_mul_f32_e32 v25, v26, v25
Cv 24,24,25
v_accvgpr_write_b32 a128, v24
v_accvgpr_read_b32 v25, a129
v_lshlrev_b32 v24, 16, v25
v_and_b32 v25, 0xffff0000, v25
v_mul_f32_e32 v24, v26, v24
v_mul_f32_e32 v25, v26, v25
Cv 24,24,25
v_accvgpr_write_b32 a129, v24
v_accvgpr_read_b32 v25, a130
v_lshlrev_b32 v24, 16, v25
v_and_b32 v25, 0xffff0000, v25
v_mul_f32_e32 v24, v26, v24
v_mul_f32_e32 v25, v26, v25
)" R"(
Cv 24,24,25
v_accvgpr_write_b32 a130, v24
v_accvgpr_read_b32 v25, a131
v_lshlrev_b32 v24, 16, v25
v_and_b32 v25, 0xffff0000, v25
v_mul_f32_e32 v24, v26, v24
v_mul_f32_e32 v25, v26, v25
Cv 24,24,25
v_accvgpr_write_b32 a131, v24
v_accvgpr_read_b32 v25, a136
v_lshlrev_b32 v24, 16, v25
v_and_b32 v25, 0xffff0000, v25
v_mul_f32_e32 v24, v26, v24
v_mul_f32_e32 v25, v26, v25
Cv 24,24,25
v_accvgpr_write_b32 a136, v24
v_accvgpr_read_b32 v25, a137
v_lshlrev_b32 v24, 16, v25
v_and_b32 v25, 0xffff0000, v25
v_mul_f32_e32 v24, v26, v24
v_mul_f32_e32 v25, v26, v25
Cv 24,24,25
v_accvgpr_write_b32 a137, v24
v_accvgpr_read_b32 v25, a138
v_lshlrev_b32 v24, 16, v25
v_and_b32 v25, 0xffff0000, v25
v_mul_f32_e32 v24, v26, v24
v_mul_f32_e32 v25, v26, v25
Cv 24,24,25
v_accvgpr_write_b32 a138, v24
v_accvgpr_read_b32 v25, a139
v_lshlrev_b32 v24, 16, v25
v_and_b32 v25, 0xffff0000, v25
v_mul_f32_e32 v24, v26, v24
v_mul_f32_e32 v25, v26, v25
Cv 24,24,25
v_accvgpr_write_b32 a139, v24
v_accvgpr_read_b32 v25, a144
v_lshlrev_b32 v24, 16, v25
v_and_b32 v25, 0xffff0000, v25
v_mul_f32_e32 v24, v26, v24
v_mul_f32_e32 v25, v26, v25
Cv 24,24,25
v_accvgpr_write_b32 a144, v24
v_accvgpr_read_b32 v25, a145
v_lshlrev_b32 v24, 16, v25
v_and_b32 v25, 0xffff0000, v25
v_mul_f32_e32 v24, v26, v24
v_mul_f32_e32 v25, v26, v25
Cv 24,24,25
v_accvgpr_write_b32 a145, v24
v_accvgpr_read_b32 v25, a146
v_lshlrev_b32 v24, 16, v25
v_and_b32 v25, 0xffff0000, v25
v_mul_f32_e32 v24, v26, v24
v_mul_f32_e32 v25, v26, v25
Cv 24,24,25
v_accvgpr_write_b32 a146, v24
v_accvgpr_read_b32 v25, a147
v_lshlrev_b32 v24, 16, v25
v_and_b32 v25, 0xffff0000, v25
v_mul_f32_e32 v24, v26, v24
v_mul_f32_e32 v25, v26, v25
Cv 24,24,25
)" R"(
v_accvgpr_write_b32 a147, v24
v_accvgpr_read_b32 v25, a152
v_lshlrev_b32 v24, 16, v25
v_and_b32 v25, 0xffff0000, v25
v_mul_f32_e32 v24, v26, v24
v_mul_f32_e32 v25, v26, v25
Cv 24,24,25
v_accvgpr_write_b32 a152, v24
v_accvgpr_read_b32 v25, a153
v_lshlrev_b32 v24, 16, v25
v_and_b32 v25, 0xffff0000, v25
v_mul_f32_e32 v24, v26, v24
v_mul_f32_e32 v25, v26, v25
Cv 24,24,25
v_accvgpr_write_b32 a153, v24
v_accvgpr_read_b32 v25, a154
v_lshlrev_b32 v24, 16, v25
v_and_b32 v25, 0xffff0000, v25
v_mul_f32_e32 v24, v26, v24
v_mul_f32_e32 v25, v26, v25
Cv 24,24,25
v_accvgpr_write_b32 a154, v24
v_accvgpr_read_b32 v25, a155
v_lshlrev_b32 v24, 16, v25
v_and_b32 v25, 0xffff0000, v25
v_mul_f32_e32 v24, v26, v24
v_mul_f32_e32 v25, v26, v25
Cv 24,24,25
v_accvgpr_write_b32 a155, v24
v_accvgpr_read_b32 v25, a160
v_lshlrev_b32 v24, 16, v25
v_and_b32 v25, 0xffff0000, v25
v_mul_f32_e32 v24, v26, v24
v_mul_f32_e32 v25, v26, v25
Cv 24,24,25
v_accvgpr_write_b32 a160, v24
v_accvgpr_read_b32 v25, a161
v_lshlrev_b32 v24, 16, v25
v_and_b32 v25, 0xffff0000, v25
v_mul_f32_e32 v24, v26, v24
v_mul_f32_e32 v25, v26, v25
Cv 24,24,25
v_accvgpr_write_b32 a161, v24
v_accvgpr_read_b32 v25, a162
v_lshlrev_b32 v24, 16, v25
v_and_b32 v25, 0xffff0000, v25
v_mul_f32_e32 v24, v26, v24
v_mul_f32_e32 v25, v26, v25
Cv 24,24,25
v_accvgpr_write_b32 a162, v24
v_accvgpr_read_b32 v25, a163
v_lshlrev_b32 v24, 16, v25
v_and_b32 v25, 0xffff0000, v25
v_mul_f32_e32 v24, v26, v24
v_mul_f32_e32 v25, v26, v25
Cv 24,24,25
v_accvgpr_write_b32 a163, v24
v_accvgpr_read_b32 v25, a168
v_lshlrev_b32 v24, 16, v25
v_and_b32 v25, 0xffff0000, v25
v_mul_f32_e32 v24, v26, v24
v_mul_f32_e32 v25, v26, v25
Cv 24,24,25
v_accvgpr_write_b32 a168, v24
)" R"(
v_accvgpr_read_b32 v25, a169
v_lshlrev_b32 v24, 16, v25
v_and_b32 v25, 0xffff0000, v25
v_mul_f32_e32 v24, v26, v24
v_mul_f32_e32 v25, v26, v25
Cv 24,24,25
v_accvgpr_write_b32 a169, v24
v_accvgpr_read_b32 v25, a170
v_lshlrev_b32 v24, 16, v25
v_and_b32 v25, 0xffff0000, v25
v_mul_f32_e32 v24, v26, v24
v_mul_f32_e32 v25, v26, v25
Cv 24,24,25
v_accvgpr_write_b32 a170, v24
v_accvgpr_read_b32 v25, a171
v_lshlrev_b32 v24, 16, v25
v_and_b32 v25, 0xffff0000, v25
v_mul_f32_e32 v24, v26, v24
v_mul_f32_e32 v25, v26, v25
Cv 24,24,25
v_accvgpr_write_b32 a171, v24
v_accvgpr_read_b32 v25, a176
v_lshlrev_b32 v24, 16, v25
v_and_b32 v25, 0xffff0000, v25
v_mul_f32_e32 v24, v26, v24
v_mul_f32_e32 v25, v26, v25
Cv 24,24,25
v_accvgpr_write_b32 a176, v24
v_accvgpr_read_b32 v25, a177
v_lshlrev_b32 v24, 16, v25
v_and_b32 v25, 0xffff0000, v25
v_mul_f32_e32 v24, v26, v24
v_mul_f32_e32 v25, v26, v25
Cv 24,24,25
v_accvgpr_write_b32 a177, v24
v_accvgpr_read_b32 v25, a178
v_lshlrev_b32 v24, 16, v25
v_and_b32 v25, 0xffff0000, v25
v_mul_f32_e32 v24, v26, v24
v_mul_f32_e32 v25, v26, v25
Cv 24,24,25
v_accvgpr_write_b32 a178, v24
v_accvgpr_read_b32 v25, a179
v_lshlrev_b32 v24, 16, v25
v_and_b32 v25, 0xffff0000, v25
v_mul_f32_e32 v24, v26, v24
v_mul_f32_e32 v25, v26, v25
Cv 24,24,25
v_accvgpr_write_b32 a179, v24
v_accvgpr_read_b32 v25, a184
v_lshlrev_b32 v24, 16, v25
v_and_b32 v25, 0xffff0000, v25
v_mul_f32_e32 v24, v26, v24
v_mul_f32_e32 v25, v26, v25
Cv 24,24,25
v_accvgpr_write_b32 a184, v24
v_accvgpr_read_b32 v25, a185
v_lshlrev_b32 v24, 16, v25
v_and_b32 v25, 0xffff0000, v25
v_mul_f32_e32 v24, v26, v24
v_mul_f32_e32 v25, v26, v25
Cv 24,24,25
v_accvgpr_write_b32 a185, v24
v_accvgpr_read_b32 v25, a186
)" R"(
v_lshlrev_b32 v24, 16, v25
v_and_b32 v25, 0xffff0000, v25
v_mul_f32_e32 v24, v26, v24
v_mul_f32_e32 v25, v26, v25
Cv 24,24,25
v_accvgpr_write_b32 a186, v24
v_accvgpr_read_b32 v25, a187
v_lshlrev_b32 v24, 16, v25
v_and_b32 v25, 0xffff0000, v25
v_mul_f32_e32 v24, v26, v24
v_mul_f32_e32 v25, v26, v25
Cv 24,24,25
v_accvgpr_write_b32 a187, v24
v_mov_b32 v25, v65
v_mov_b32 v24, v25
s_nop 1
v_permlane32_swap_b32_e32 v25, v24
v_add_f32_e32 v25, v25, v24
v_mov_b32 v24, v25
s_nop 1
v_permlane16_swap_b32_e32 v25, v24
v_add_f32_e32 v25, v25, v24
v_cmp_lt_f32_e32 vcc, 0, v25
s_nop 1
v_cndmask_b32_e32 v67, v20, v67, vcc
v_sub_f32_e32 v29, v20, v67
v_min_f32_e32 v27, 0, v29
v_exp_f32_e32 v27, v27
v_max_f32_e32 v29, 0, v29
v_sub_f32_e32 v24, 0, v29
v_exp_f32_e32 v24, v24
v_mul_f32_e32 v65, v27, v65
v_mul_f32_e32 v13, v24, v13
v_mul_f32_e32 v136, v24, v136
v_mul_f32_e32 v137, v24, v137
v_mul_f32_e32 v138, v24, v138
v_mul_f32_e32 v139, v24, v139
v_mul_f32_e32 v140, v24, v140
v_mul_f32_e32 v141, v24, v141
v_mul_f32_e32 v142, v24, v142
v_mul_f32_e32 v143, v24, v143
v_mul_f32_e32 v152, v24, v152
v_mul_f32_e32 v153, v24, v153
v_mul_f32_e32 v154, v24, v154
v_mul_f32_e32 v155, v24, v155
v_mul_f32_e32 v156, v24, v156
v_mul_f32_e32 v157, v24, v157
v_mul_f32_e32 v158, v24, v158
v_mul_f32_e32 v159, v24, v159
v_mul_f32_e32 v168, v24, v168
v_mul_f32_e32 v169, v24, v169
v_mul_f32_e32 v170, v24, v170
v_mul_f32_e32 v171, v24, v171
v_mul_f32_e32 v172, v24, v172
v_mul_f32_e32 v173, v24, v173
v_mul_f32_e32 v174, v24, v174
v_mul_f32_e32 v175, v24, v175
v_mul_f32_e32 v184, v24, v184
v_mul_f32_e32 v185, v24, v185
v_mul_f32_e32 v186, v24, v186
v_mul_f32_e32 v187, v24, v187
v_mul_f32_e32 v188, v24, v188
v_mul_f32_e32 v189, v24, v189
v_mul_f32_e32 v190, v24, v190
)" R"(
v_mul_f32_e32 v191, v24, v191
v_mul_f32_e32 v200, v24, v200
v_mul_f32_e32 v201, v24, v201
v_mul_f32_e32 v202, v24, v202
v_mul_f32_e32 v203, v24, v203
v_mul_f32_e32 v204, v24, v204
v_mul_f32_e32 v205, v24, v205
v_mul_f32_e32 v206, v24, v206
v_mul_f32_e32 v207, v24, v207
v_mul_f32_e32 v216, v24, v216
v_mul_f32_e32 v217, v24, v217
v_mul_f32_e32 v218, v24, v218
v_mul_f32_e32 v219, v24, v219
v_mul_f32_e32 v220, v24, v220
v_mul_f32_e32 v221, v24, v221
v_mul_f32_e32 v222, v24, v222
v_mul_f32_e32 v223, v24, v223
v_mul_f32_e32 v232, v24, v232
v_mul_f32_e32 v233, v24, v233
v_mul_f32_e32 v234, v24, v234
v_mul_f32_e32 v235, v24, v235
v_mul_f32_e32 v236, v24, v236
v_mul_f32_e32 v237, v24, v237
v_mul_f32_e32 v238, v24, v238
v_mul_f32_e32 v239, v24, v239
v_mul_f32_e32 v248, v24, v248
v_mul_f32_e32 v249, v24, v249
v_mul_f32_e32 v250, v24, v250
v_mul_f32_e32 v251, v24, v251
v_mul_f32_e32 v252, v24, v252
v_mul_f32_e32 v253, v24, v253
v_mul_f32_e32 v254, v24, v254
v_mul_f32_e32 v255, v24, v255
v_sub_f32_e32 v20, v20, v29
v_sub_f32_e32 v21, v21, v29
v_sub_f32_e32 v22, v22, v29
v_sub_f32_e32 v23, v23, v29
v_sub_f32_e32 v100, v100, v29
v_sub_f32_e32 v101, v101, v29
v_sub_f32_e32 v102, v102, v29
v_sub_f32_e32 v103, v103, v29
v_sub_f32_e32 v108, v108, v29
v_sub_f32_e32 v109, v109, v29
v_sub_f32_e32 v110, v110, v29
v_sub_f32_e32 v111, v111, v29
v_sub_f32_e32 v116, v116, v29
v_sub_f32_e32 v117, v117, v29
v_sub_f32_e32 v118, v118, v29
v_sub_f32_e32 v119, v119, v29
v_sub_f32_e32 v124, v124, v29
v_sub_f32_e32 v125, v125, v29
v_sub_f32_e32 v126, v126, v29
v_sub_f32_e32 v127, v127, v29
v_accvgpr_read_b32 v25, a132
v_lshlrev_b32 v24, 16, v25
v_and_b32 v25, 0xffff0000, v25
v_mul_f32_e32 v24, v27, v24
v_mul_f32_e32 v25, v27, v25
Cv 24,24,25
v_accvgpr_write_b32 a132, v24
v_accvgpr_read_b32 v25, a133
v_lshlrev_b32 v24, 16, v25
v_and_b32 v25, 0xffff0000, v25
v_mul_f32_e32 v24, v27, v24
)" R"(
v_mul_f32_e32 v25, v27, v25
Cv 24,24,25
v_accvgpr_write_b32 a133, v24
v_accvgpr_read_b32 v25, a134
v_lshlrev_b32 v24, 16, v25
v_and_b32 v25, 0xffff0000, v25
v_mul_f32_e32 v24, v27, v24
v_mul_f32_e32 v25, v27, v25
Cv 24,24,25
v_accvgpr_write_b32 a134, v24
v_accvgpr_read_b32 v25, a135
v_lshlrev_b32 v24, 16, v25
v_and_b32 v25, 0xffff0000, v25
v_mul_f32_e32 v24, v27, v24
v_mul_f32_e32 v25, v27, v25
Cv 24,24,25
v_accvgpr_write_b32 a135, v24
v_accvgpr_read_b32 v25, a140
v_lshlrev_b32 v24, 16, v25
v_and_b32 v25, 0xffff0000, v25
v_mul_f32_e32 v24, v27, v24
v_mul_f32_e32 v25, v27, v25
Cv 24,24,25
v_accvgpr_write_b32 a140, v24
v_accvgpr_read_b32 v25, a141
v_lshlrev_b32 v24, 16, v25
v_and_b32 v25, 0xffff0000, v25
v_mul_f32_e32 v24, v27, v24
v_mul_f32_e32 v25, v27, v25
Cv 24,24,25
v_accvgpr_write_b32 a141, v24
v_accvgpr_read_b32 v25, a142
v_lshlrev_b32 v24, 16, v25
v_and_b32 v25, 0xffff0000, v25
v_mul_f32_e32 v24, v27, v24
v_mul_f32_e32 v25, v27, v25
Cv 24,24,25
v_accvgpr_write_b32 a142, v24
v_accvgpr_read_b32 v25, a143
v_lshlrev_b32 v24, 16, v25
v_and_b32 v25, 0xffff0000, v25
v_mul_f32_e32 v24, v27, v24
v_mul_f32_e32 v25, v27, v25
Cv 24,24,25
v_accvgpr_write_b32 a143, v24
v_accvgpr_read_b32 v25, a148
v_lshlrev_b32 v24, 16, v25
v_and_b32 v25, 0xffff0000, v25
v_mul_f32_e32 v24, v27, v24
v_mul_f32_e32 v25, v27, v25
Cv 24,24,25
v_accvgpr_write_b32 a148, v24
v_accvgpr_read_b32 v25, a149
v_lshlrev_b32 v24, 16, v25
v_and_b32 v25, 0xffff0000, v25
v_mul_f32_e32 v24, v27, v24
v_mul_f32_e32 v25, v27, v25
Cv 24,24,25
v_accvgpr_write_b32 a149, v24
v_accvgpr_read_b32 v25, a150
v_lshlrev_b32 v24, 16, v25
v_and_b32 v25, 0xffff0000, v25
v_mul_f32_e32 v24, v27, v24
v_mul_f32_e32 v25, v27, v25
)" R"(
Cv 24,24,25
v_accvgpr_write_b32 a150, v24
v_accvgpr_read_b32 v25, a151
v_lshlrev_b32 v24, 16, v25
v_and_b32 v25, 0xffff0000, v25
v_mul_f32_e32 v24, v27, v24
v_mul_f32_e32 v25, v27, v25
Cv 24,24,25
v_accvgpr_write_b32 a151, v24
v_accvgpr_read_b32 v25, a156
v_lshlrev_b32 v24, 16, v25
v_and_b32 v25, 0xffff0000, v25
v_mul_f32_e32 v24, v27, v24
v_mul_f32_e32 v25, v27, v25
Cv 24,24,25
v_accvgpr_write_b32 a156, v24
v_accvgpr_read_b32 v25, a157
v_lshlrev_b32 v24, 16, v25
v_and_b32 v25, 0xffff0000, v25
v_mul_f32_e32 v24, v27, v24
v_mul_f32_e32 v25, v27, v25
Cv 24,24,25
v_accvgpr_write_b32 a157, v24
v_accvgpr_read_b32 v25, a158
v_lshlrev_b32 v24, 16, v25
v_and_b32 v25, 0xffff0000, v25
v_mul_f32_e32 v24, v27, v24
v_mul_f32_e32 v25, v27, v25
Cv 24,24,25
v_accvgpr_write_b32 a158, v24
v_accvgpr_read_b32 v25, a159
v_lshlrev_b32 v24, 16, v25
v_and_b32 v25, 0xffff0000, v25
v_mul_f32_e32 v24, v27, v24
v_mul_f32_e32 v25, v27, v25
Cv 24,24,25
v_accvgpr_write_b32 a159, v24
v_accvgpr_read_b32 v25, a164
v_lshlrev_b32 v24, 16, v25
v_and_b32 v25, 0xffff0000, v25
v_mul_f32_e32 v24, v27, v24
v_mul_f32_e32 v25, v27, v25
Cv 24,24,25
v_accvgpr_write_b32 a164, v24
v_accvgpr_read_b32 v25, a165
v_lshlrev_b32 v24, 16, v25
v_and_b32 v25, 0xffff0000, v25
v_mul_f32_e32 v24, v27, v24
v_mul_f32_e32 v25, v27, v25
Cv 24,24,25
v_accvgpr_write_b32 a165, v24
v_accvgpr_read_b32 v25, a166
v_lshlrev_b32 v24, 16, v25
v_and_b32 v25, 0xffff0000, v25
v_mul_f32_e32 v24, v27, v24
v_mul_f32_e32 v25, v27, v25
Cv 24,24,25
v_accvgpr_write_b32 a166, v24
v_accvgpr_read_b32 v25, a167
v_lshlrev_b32 v24, 16, v25
v_and_b32 v25, 0xffff0000, v25
v_mul_f32_e32 v24, v27, v24
v_mul_f32_e32 v25, v27, v25
Cv 24,24,25
)" R"(
v_accvgpr_write_b32 a167, v24
v_accvgpr_read_b32 v25, a172
v_lshlrev_b32 v24, 16, v25
v_and_b32 v25, 0xffff0000, v25
v_mul_f32_e32 v24, v27, v24
v_mul_f32_e32 v25, v27, v25
Cv 24,24,25
v_accvgpr_write_b32 a172, v24
v_accvgpr_read_b32 v25, a173
v_lshlrev_b32 v24, 16, v25
v_and_b32 v25, 0xffff0000, v25
v_mul_f32_e32 v24, v27, v24
v_mul_f32_e32 v25, v27, v25
Cv 24,24,25
v_accvgpr_write_b32 a173, v24
v_accvgpr_read_b32 v25, a174
v_lshlrev_b32 v24, 16, v25
v_and_b32 v25, 0xffff0000, v25
v_mul_f32_e32 v24, v27, v24
v_mul_f32_e32 v25, v27, v25
Cv 24,24,25
v_accvgpr_write_b32 a174, v24
v_accvgpr_read_b32 v25, a175
v_lshlrev_b32 v24, 16, v25
v_and_b32 v25, 0xffff0000, v25
v_mul_f32_e32 v24, v27, v24
v_mul_f32_e32 v25, v27, v25
Cv 24,24,25
v_accvgpr_write_b32 a175, v24
v_accvgpr_read_b32 v25, a180
v_lshlrev_b32 v24, 16, v25
v_and_b32 v25, 0xffff0000, v25
v_mul_f32_e32 v24, v27, v24
v_mul_f32_e32 v25, v27, v25
Cv 24,24,25
v_accvgpr_write_b32 a180, v24
v_accvgpr_read_b32 v25, a181
v_lshlrev_b32 v24, 16, v25
v_and_b32 v25, 0xffff0000, v25
v_mul_f32_e32 v24, v27, v24
v_mul_f32_e32 v25, v27, v25
Cv 24,24,25
v_accvgpr_write_b32 a181, v24
v_accvgpr_read_b32 v25, a182
v_lshlrev_b32 v24, 16, v25
v_and_b32 v25, 0xffff0000, v25
v_mul_f32_e32 v24, v27, v24
v_mul_f32_e32 v25, v27, v25
Cv 24,24,25
v_accvgpr_write_b32 a182, v24
v_accvgpr_read_b32 v25, a183
v_lshlrev_b32 v24, 16, v25
v_and_b32 v25, 0xffff0000, v25
v_mul_f32_e32 v24, v27, v24
v_mul_f32_e32 v25, v27, v25
Cv 24,24,25
v_accvgpr_write_b32 a183, v24
v_accvgpr_read_b32 v25, a188
v_lshlrev_b32 v24, 16, v25
v_and_b32 v25, 0xffff0000, v25
v_mul_f32_e32 v24, v27, v24
v_mul_f32_e32 v25, v27, v25
Cv 24,24,25
v_accvgpr_write_b32 a188, v24
)" R"(
v_accvgpr_read_b32 v25, a189
v_lshlrev_b32 v24, 16, v25
v_and_b32 v25, 0xffff0000, v25
v_mul_f32_e32 v24, v27, v24
v_mul_f32_e32 v25, v27, v25
Cv 24,24,25
v_accvgpr_write_b32 a189, v24
v_accvgpr_read_b32 v25, a190
v_lshlrev_b32 v24, 16, v25
v_and_b32 v25, 0xffff0000, v25
v_mul_f32_e32 v24, v27, v24
v_mul_f32_e32 v25, v27, v25
Cv 24,24,25
v_accvgpr_write_b32 a190, v24
v_accvgpr_read_b32 v25, a191
v_lshlrev_b32 v24, 16, v25
v_and_b32 v25, 0xffff0000, v25
v_mul_f32_e32 v24, v27, v24
v_mul_f32_e32 v25, v27, v25
Cv 24,24,25
v_accvgpr_write_b32 a191, v24
v_exp_f32_e32 v48, v96
v_exp_f32_e32 v24, v97
s_nop 0
v_add_f32_e32 v14, v48, v24
Cv 48,48,24
v_exp_f32_e32 v49, v98
v_exp_f32_e32 v25, v99
v_add_f32_e32 v14, v14, v49
v_add_f32_e32 v14, v14, v25
Cv 49,49,25
v_exp_f32_e32 v50, v104
v_exp_f32_e32 v26, v105
v_add_f32_e32 v14, v14, v50
v_add_f32_e32 v14, v14, v26
Cv 50,50,26
v_exp_f32_e32 v51, v106
v_exp_f32_e32 v27, v107
v_add_f32_e32 v14, v14, v51
v_add_f32_e32 v14, v14, v27
Cv 51,51,27
v_exp_f32_e32 v52, v100
v_exp_f32_e32 v28, v101
s_nop 0
v_add_f32_e32 v15, v52, v28
Cv 52,52,28
v_exp_f32_e32 v53, v102
v_exp_f32_e32 v29, v103
v_add_f32_e32 v15, v15, v53
v_add_f32_e32 v15, v15, v29
Cv 53,53,29
v_exp_f32_e32 v54, v108
v_exp_f32_e32 v24, v109
v_add_f32_e32 v15, v15, v54
v_add_f32_e32 v15, v15, v24
Cv 54,54,24
v_exp_f32_e32 v55, v110
v_exp_f32_e32 v25, v111
v_add_f32_e32 v15, v15, v55
v_add_f32_e32 v15, v15, v25
Cv 55,55,25
s_nop 1
s_branch .LzMRR
.Lzsp23:
)" R"(
s_sub_u32 s53, s53, 1
s_cbranch_scc1 .Lzsq23
s_sleep 1
buffer_load_dword v4, v7, s[20:23], s52 offen offset:0 sc0 sc1
s_waitcnt vmcnt(0)
v_cmp_eq_u32_e32 vcc, s3, v4
s_andn2_b64 s[28:29], 1, vcc
s_cbranch_scc1 .Lzsp23
s_branch .Lzok23
.Lzsq23:
s_mov_b32 s53, 0
s_branch .Lzok23
.Lzsp24:
s_sub_u32 s53, s53, 1
s_cbranch_scc1 .Lzsq24
s_sleep 1
buffer_load_dword v4, v7, s[20:23], s52 offen offset:128 sc0 sc1
s_waitcnt vmcnt(0)
v_cmp_eq_u32_e32 vcc, s3, v4
s_andn2_b64 s[28:29], 1, vcc
s_cbranch_scc1 .Lzsp24
s_branch .Lzok24
.Lzsq24:
s_mov_b32 s53, 0
s_branch .Lzok24
.LzBB:
v_lshlrev_b32 v95, 7, v0
s_mov_b32 s28, 0x150c000
s_add_u32 s29, s28, 6144
.LzBB1:
buffer_load_dword v92, v95, s[20:23], s28 offen sc0 sc1
buffer_load_dword v93, v95, s[20:23], s29 offen sc0 sc1
s_waitcnt vmcnt(0)
v_cmp_ne_u32_e32 vcc, s3, v92
v_cmp_ne_u32_e64 s[30:31], s3, v93
s_or_b64 vcc, vcc, s[30:31]
s_cbranch_scc0 .LzBB2
s_sub_u32 s53, s53, 1
s_cbranch_scc0 .LzBB1
s_mov_b32 s53, 0
.LzBB2:
s_mul_i32 s28, s56, 7168
s_add_u32 s28, s28, 0x1558000
s_add_u32 s29, s28, 4096
buffer_load_dwordx4 v[64:67], v1, s[20:23], s28 offen offset:0 sc0 sc1
buffer_load_dwordx4 v[68:71], v1, s[20:23], s28 offen offset:1024 sc0 sc1
buffer_load_dwordx4 v[72:75], v1, s[20:23], s28 offen offset:2048 sc0 sc1
buffer_load_dwordx4 v[76:79], v1, s[20:23], s28 offen offset:3072 sc0 sc1
buffer_load_dwordx4 v[80:83], v1, s[20:23], s29 offen offset:0 sc0 sc1
buffer_load_dwordx4 v[84:87], v1, s[20:23], s29 offen offset:1024 sc0 sc1
buffer_load_dwordx4 v[88:91], v1, s[20:23], s29 offen offset:2048 sc0 sc1
s_cmp_gt_u32 s56, 27
s_cselect_b32 s28, 16, 0
s_cselect_b32 s29, 28, 0
s_lshr_b32 s28, s63, s28
s_and_b32 s28, s28, 0x7ff
s_sub_u32 s29, s56, s29
s_lshl_b32 s29, s29, 5
s_mul_i32 s83, s28, 896
s_add_u32 s29, s29, s83
v_and_b32 v95, 3, v0
v_lshlrev_b32 v95, 3, v95
buffer_load_dwordx2 v[92:93], v95, s[12:15], s29 offen
s_lshl4_add_u32 s28, s28, 0x150000c
)" R"(
v_mov_b32 v94, 0
buffer_load_dword v94, v94, s[20:23], s28 offen sc0 sc1
s_waitcnt vmcnt(0)
v_add_f32_e32 v64, v64, v68
v_add_f32_e32 v65, v65, v69
v_add_f32_e32 v66, v66, v70
v_add_f32_e32 v67, v67, v71
v_add_f32_e32 v72, v72, v76
v_add_f32_e32 v73, v73, v77
v_add_f32_e32 v74, v74, v78
v_add_f32_e32 v75, v75, v79
v_add_f32_e32 v80, v80, v84
v_add_f32_e32 v81, v81, v85
v_add_f32_e32 v82, v82, v86
v_add_f32_e32 v83, v83, v87
v_add_f32_e32 v64, v64, v72
v_add_f32_e32 v65, v65, v73
v_add_f32_e32 v66, v66, v74
v_add_f32_e32 v67, v67, v75
v_add_f32_e32 v80, v80, v88
v_add_f32_e32 v81, v81, v89
v_add_f32_e32 v82, v82, v90
v_add_f32_e32 v83, v83, v91
v_add_f32_e32 v64, v64, v80
v_add_f32_e32 v65, v65, v81
v_add_f32_e32 v66, v66, v82
v_add_f32_e32 v67, v67, v83
v_mov_b32 v68, v64
s_nop 1
v_permlane32_swap_b32_e32 v64, v68
v_add_f32_e32 v64, v64, v68
v_mov_b32 v68, v64
s_nop 1
v_permlane16_swap_b32_e32 v64, v68
v_add_f32_e32 v64, v64, v68
v_mov_b32 v68, v65
s_nop 1
v_permlane32_swap_b32_e32 v65, v68
v_add_f32_e32 v65, v65, v68
v_mov_b32 v68, v65
s_nop 1
v_permlane16_swap_b32_e32 v65, v68
v_add_f32_e32 v65, v65, v68
v_mov_b32 v68, v66
s_nop 1
v_permlane32_swap_b32_e32 v66, v68
v_add_f32_e32 v66, v66, v68
v_mov_b32 v68, v66
s_nop 1
v_permlane16_swap_b32_e32 v66, v68
v_add_f32_e32 v66, v66, v68
v_mov_b32 v68, v67
s_nop 1
v_permlane32_swap_b32_e32 v67, v68
v_add_f32_e32 v67, v67, v68
v_mov_b32 v68, v67
s_nop 1
v_permlane16_swap_b32_e32 v67, v68
v_add_f32_e32 v67, v67, v68
v_add_f32_dpp v64, v64, v64 row_ror:8 row_mask:0xf bank_mask:0xf
v_add_f32_dpp v65, v65, v65 row_ror:8 row_mask:0xf bank_mask:0xf
v_add_f32_dpp v66, v66, v66 row_ror:8 row_mask:0xf bank_mask:0xf
v_add_f32_dpp v67, v67, v67 row_ror:8 row_mask:0xf bank_mask:0xf
v_add_f32_dpp v64, v64, v64 row_ror:4 row_mask:0xf bank_mask:0xf
)" R"(
v_add_f32_dpp v65, v65, v65 row_ror:4 row_mask:0xf bank_mask:0xf
v_add_f32_dpp v66, v66, v66 row_ror:4 row_mask:0xf bank_mask:0xf
v_add_f32_dpp v67, v67, v67 row_ror:4 row_mask:0xf bank_mask:0xf
v_mul_f32_e32 v94, 0xbfb8aa3b, v94
v_exp_f32_e32 v94, v94
Cv 65,64,65
v_lshlrev_b32 v64, 16, v65
v_and_b32 v65, 0xffff0000, v65
v_add_f32_e32 v94, 1.0, v94
v_rcp_f32_e32 v94, v94
Cv 67,66,67
v_lshlrev_b32 v66, 16, v67
v_and_b32 v67, 0xffff0000, v67
Cv 94,94,94
v_lshlrev_b32 v94, 16, v94
v_mul_f32_e32 v64, v94, v64
v_mul_f32_e32 v65, v94, v65
v_mul_f32_e32 v66, v94, v66
v_mul_f32_e32 v67, v94, v67
Cv 65,64,65
v_lshlrev_b32 v64, 16, v65
v_and_b32 v65, 0xffff0000, v65
Cv 67,66,67
v_lshlrev_b32 v66, 16, v67
v_and_b32 v67, 0xffff0000, v67
v_lshlrev_b32 v68, 16, v92
v_and_b32 v69, 0xffff0000, v92
v_lshlrev_b32 v70, 16, v93
v_and_b32 v71, 0xffff0000, v93
v_add_f32_e32 v64, v64, v68
v_add_f32_e32 v65, v65, v69
v_add_f32_e32 v66, v66, v70
v_add_f32_e32 v67, v67, v71
Cv 64,64,65
Cv 66,66,67
v_mov_b32 v65, s3
v_mov_b32 v67, s3
v_lshrrev_b32 v95, 2, v0
v_min_u32 v95, 1, v95
v_lshl_or_b32 v95, v95, 30, v1
s_lshl_b32 s28, s56, 7
s_add_u32 s28, s28, 0x1509000
buffer_store_dwordx4 v[64:67], v95, s[20:23], s28 offen sc0 sc1
buffer_load_dword v4, v165, s[20:23], s40 offen sc0 sc1
s_waitcnt vmcnt(0)
s_branch .LzBBR
.LzPRB:
s_mov_b32 s24, s89
s_mul_i32 s25, s33, 2304
s_add_u32 s24, s24, s25
s_add_u32 s24, s24, 0xd00000
s_and_b32 s25, s36, 0x2000
s_xor_b32 s24, s24, s25
s_xor_b32 s25, s24, 0x2000
buffer_store_dword v0, v7, s[20:23], s24 offen sc0 sc1
buffer_store_dword v0, v7, s[20:23], s25 offen sc0 sc1
s_mov_b32 s26, -1
s_mov_b32 s27, -1
s_waitcnt vmcnt(0)
s_memtime s[28:29]
s_waitcnt lgkmcnt(0)
buffer_store_dword v0, v7, s[20:23], s24 offen sc0 sc1
s_waitcnt vmcnt(0)
s_memtime s[30:31]
)" R"(
s_waitcnt lgkmcnt(0)
s_sub_u32 s30, s30, s28
s_min_u32 s26, s26, s30
s_memtime s[28:29]
s_waitcnt lgkmcnt(0)
buffer_store_dword v0, v7, s[20:23], s25 offen sc0 sc1
s_waitcnt vmcnt(0)
s_memtime s[30:31]
s_waitcnt lgkmcnt(0)
s_sub_u32 s30, s30, s28
s_min_u32 s27, s27, s30
s_memtime s[28:29]
s_waitcnt lgkmcnt(0)
buffer_store_dword v0, v7, s[20:23], s24 offen sc0 sc1
s_waitcnt vmcnt(0)
s_memtime s[30:31]
s_waitcnt lgkmcnt(0)
s_sub_u32 s30, s30, s28
s_min_u32 s26, s26, s30
s_memtime s[28:29]
s_waitcnt lgkmcnt(0)
buffer_store_dword v0, v7, s[20:23], s25 offen sc0 sc1
s_waitcnt vmcnt(0)
s_memtime s[30:31]
s_waitcnt lgkmcnt(0)
s_sub_u32 s30, s30, s28
s_min_u32 s27, s27, s30
s_bitcmp1_b32 s33, 0
s_cselect_b32 s28, s27, s26
s_cselect_b32 s29, s26, s27
v_mov_b32 v32, s28
v_mov_b32 v33, s29
s_lshl_b32 s24, s33, 3
v_mov_b32 v34, s24
v_mov_b32 v35, 0
ds_write_b64 v34, v[32:33]
s_waitcnt lgkmcnt(0)
s_barrier
ds_read_b128 v[36:39], v35
ds_read_b128 v[40:43], v35 offset:16
s_mul_i32 s24, s3, 1664525
s_add_u32 s24, s24, 1013904223
s_and_b32 s24, s24, -2
s_lshl_b32 s25, s34, 6
s_add_u32 s25, s25, 0x1508800
s_cmp_lg_u32 s33, 0
s_cmov_b32 s25, 0x40000000
s_waitcnt lgkmcnt(0)
v_min3_u32 v32, v36, v38, v40
v_min3_u32 v33, v37, v39, v41
v_min_u32 v32, v32, v42
v_min_u32 v33, v33, v43
v_cmp_gt_u32_e32 vcc, v32, v33
v_mov_b32 v34, s24
s_nop 0
v_addc_co_u32_e32 v34, vcc, 0, v34, vcc
buffer_store_dword v34, v7, s[20:23], s25 offen sc0 sc1
s_barrier
s_branch .LzPRR
.LzHOP:
s_lshr_b32 s82, s2, 7
s_lshl1_add_u32 s82, s82, s91
s_and_b32 s83, s34, 1
s_lshl1_add_u32 s82, s82, s83
)" R"(
s_lshl2_add_u32 s82, s82, s33
s_lshl2_add_u32 s82, s82, 16
s_getpc_b64 vcc
s_add_u32 vcc_lo, vcc_lo, s82
s_addc_u32 vcc_hi, vcc_hi, 0
s_mov_b64 exec, 0
s_setpc_b64 vcc
s_branch .LzH21
s_branch .LzH26
s_branch .LzH25
s_branch .LzH24
s_branch .LzH23
s_branch .LzH33
s_branch .LzH32
s_branch .LzH22
s_branch .LzH21
s_branch .LzH31
s_branch .LzH30
s_branch .LzH29
s_branch .LzH28
s_branch .LzH27
s_branch .LzH32
s_branch .LzH22
s_branch .LzH3
s_branch .LzH2
s_branch .LzH10
s_branch .LzH9
s_branch .LzH8
s_branch .LzH4
s_branch .LzH1
s_branch .LzH0
s_branch .LzH7
s_branch .LzH6
s_branch .LzH5
s_branch .LzH9
s_branch .LzH8
s_branch .LzH4
s_branch .LzH1
s_branch .LzH0
.LzHR:
s_mov_b64 exec, -1
s_setpc_b64 s[30:31]
.LzA0:
s_mov_b32 s63, 0x6040200
s_mov_b32 s28, 0x7050312
s_bitcmp1_b32 s2, 5
s_cmov_b32 s63, 0x18161401
s_cmov_b32 s28, 0x19171513
s_bitcmp1_b32 s2, 6
s_cmov_b32 s63, s28
s_lshl_b32 s28, s33, 3
s_lshr_b32 s28, s63, s28
s_and_b32 s28, s28, 255
s_lshl_b32 s38, s28, 17
s_add_u32 s38, s38, 0xff80
s_lshl_b32 s39, s33, 16
s_lshl_b32 s28, s34, 9
s_add_u32 s39, s39, s28
s_sub_u32 s39, s39, 0x40000
s_lshl_b32 s37, s33, 12
s_add_u32 s37, s37, 0x10000
v_lshrrev_b32 v28, 4, v0
v_and_b32 v29, 7, v0
v_xor_b32 v28, v28, v29
)" R"(
v_lshlrev_b32 v28, 4, v28
v_lshrrev_b32 v29, 3, v0
v_lshl_or_b32 v8, v29, 11, v28
v_xor_b32 v9, 64, v8
v_add_u32 v9, 15360, v9
v_add_u32 v10, 30720, v8
v_add_u32 v11, 30720, v9
s_and_b32 s28, s33, 1
s_lshl_b32 s28, s28, 7
v_lshrrev_b32 v29, 5, v0
v_mul_u32_u24 v29, 0x2220, v29
v_xor_b32 v29, v29, v1
v_xor_b32 v12, s28, v29
v_xor_b32 v13, 64, v12
v_add_u32 v13, 15360, v13
v_add_u32 v14, 30720, v12
v_add_u32 v15, 30720, v13
v_min_u32 v7, 1, v0
v_lshlrev_b32 v7, 30, v7
s_waitcnt lgkmcnt(0)
s_mov_b32 s28, 0x1508400
s_load_dword s3, s[74:75], s28
s_mov_b32 s20, s74
s_mov_b32 s21, s75
s_mov_b32 s22, 0xf400000
s_mov_b32 s23, 0x20000
s_mov_b32 s4, s66
s_mov_b32 s5, s67
s_mov_b32 s6, 0x400000
s_mov_b32 s7, 0x20000
s_cmp_eq_u32 s91, 1
s_cselect_b32 s8, s72, s70
s_cselect_b32 s9, s73, s71
s_mov_b32 s10, 0x800000
s_mov_b32 s11, 0x20000
s_lshr_b32 s28, s2, 3
s_cmp_eq_u32 s28, 14
s_cbranch_scc1 .LzPRB
.LzPRR:
s_add_u32 m0, s37, 0
s_add_u32 s39, s39, 0x40000
buffer_load_dwordx4 v12, s[8:11], s39 offen offset:0 lds
buffer_load_dwordx4 v13, s[8:11], s39 offen offset:1024 lds
buffer_load_dwordx4 v14, s[8:11], s39 offen offset:2048 lds
buffer_load_dwordx4 v15, s[8:11], s39 offen offset:3072 lds
s_add_u32 m0, s36, 0
s_add_u32 s38, s38, 0xffff0080
buffer_load_dwordx4 v8, s[4:7], s38 offen offset:0 lds
buffer_load_dwordx4 v9, s[4:7], s38 offen offset:1024 lds
buffer_load_dwordx4 v10, s[4:7], s38 offen offset:2048 lds
buffer_load_dwordx4 v11, s[4:7], s38 offen offset:3072 lds
s_add_u32 m0, s36, 4096
s_add_u32 s38, s38, 0x10000
buffer_load_dwordx4 v8, s[4:7], s38 offen offset:0 lds
buffer_load_dwordx4 v9, s[4:7], s38 offen offset:1024 lds
buffer_load_dwordx4 v10, s[4:7], s38 offen offset:2048 lds
buffer_load_dwordx4 v11, s[4:7], s38 offen offset:3072 lds
s_add_u32 m0, s37, 16384
s_add_u32 s39, s39, 0x40000
buffer_load_dwordx4 v12, s[8:11], s39 offen offset:0 lds
buffer_load_dwordx4 v13, s[8:11], s39 offen offset:1024 lds
buffer_load_dwordx4 v14, s[8:11], s39 offen offset:2048 lds
buffer_load_dwordx4 v15, s[8:11], s39 offen offset:3072 lds
s_mov_b32 s12, s64
)" R"(
s_mov_b32 s13, s65
s_mov_b32 s14, 0x1c0000
s_mov_b32 s15, 0x20000
s_and_b32 s94, s2, 127
s_lshl2_add_u32 s94, s94, s33
s_lshl_b32 s94, s94, 2
s_mul_i32 s29, s94, 896
v_mov_b32 v120, 0x40000000
v_cmp_gt_u32_e32 vcc, 56, v0
s_nop 1
v_cndmask_b32_e32 v120, v120, v1, vcc
buffer_load_dwordx4 v[96:99], v120, s[12:15], s29 offen offset:0
buffer_load_dwordx4 v[100:103], v120, s[12:15], s29 offen offset:896
buffer_load_dwordx4 v[104:107], v120, s[12:15], s29 offen offset:1792
buffer_load_dwordx4 v[108:111], v120, s[12:15], s29 offen offset:2688
s_mov_b32 s24, s76
s_mov_b32 s25, s77
s_mov_b32 s26, 0x380
s_mov_b32 s27, 0x20000
buffer_load_dwordx4 v[112:115], v120, s[24:27], 0 offen
s_mov_b32 s24, s78
s_mov_b32 s25, s79
s_mov_b32 s26, 0x380
s_mov_b32 s27, 0x20000
buffer_load_dwordx4 v[116:119], v120, s[24:27], 0 offen
s_add_u32 m0, s36, 32768
s_add_u32 s38, s38, 0xffff0080
buffer_load_dwordx4 v8, s[4:7], s38 offen offset:0 lds
buffer_load_dwordx4 v9, s[4:7], s38 offen offset:1024 lds
buffer_load_dwordx4 v10, s[4:7], s38 offen offset:2048 lds
buffer_load_dwordx4 v11, s[4:7], s38 offen offset:3072 lds
s_add_u32 m0, s36, 36864
s_add_u32 s38, s38, 0x10000
buffer_load_dwordx4 v8, s[4:7], s38 offen offset:0 lds
buffer_load_dwordx4 v9, s[4:7], s38 offen offset:1024 lds
buffer_load_dwordx4 v10, s[4:7], s38 offen offset:2048 lds
buffer_load_dwordx4 v11, s[4:7], s38 offen offset:3072 lds
s_mov_b32 s16, s68
s_mov_b32 s17, s69
s_mov_b32 s18, 0x380000
s_mov_b32 s19, 0x20000
s_load_dwordx2 s[70:71], s[0:1], 88
s_load_dwordx2 s[74:75], s[0:1], 96
v_bfe_u32 v28, v0, 1, 3
v_lshrrev_b32 v29, 4, v0
v_xor_b32 v28, v28, v29
v_lshlrev_b32 v28, 4, v28
v_and_b32 v29, 15, v0
v_lshl_or_b32 v28, v29, 7, v28
s_lshr_b32 s28, s33, 1
s_lshl_b32 s28, s28, 12
v_or_b32 v16, s28, v28
v_xor_b32 v17, 64, v16
v_bfe_u32 v28, v0, 2, 3
v_lshlrev_b32 v28, 5, v28
v_and_b32 v29, 3, v0
v_lshl_or_b32 v28, v29, 3, v28
v_bfe_u32 v29, v0, 2, 2
v_lshl_or_b32 v28, v29, 9, v28
v_lshrrev_b32 v29, 4, v0
v_lshl_or_b32 v28, v29, 12, v28
s_and_b32 s28, s33, 1
s_lshl_b32 s28, s28, 8
s_bitset1_b32 s28, 16
)" R"(
v_or_b32 v18, s28, v28
v_xor_b32 v19, 32, v18
v_xor_b32 v20, 64, v18
v_xor_b32 v21, 96, v18
v_xor_b32 v22, 128, v18
v_xor_b32 v23, 160, v18
v_xor_b32 v24, 192, v18
v_xor_b32 v25, 224, v18
s_load_dwordx4 s[76:79], s[0:1], 72
s_waitcnt vmcnt(18) lgkmcnt(0)
s_barrier
Rt 0,18,0
Rt 2,18,2048
Rt 4,19,0
Rt 6,19,2048
Rt 8,20,0
Rt 10,20,2048
Rt 12,21,0
Rt 14,21,2048
Rt 16,22,0
Rt 18,22,2048
Rt 20,23,0
Rt 22,23,2048
Rt 24,24,0
Rt 26,24,2048
Rt 28,25,0
Rt 30,25,2048
Rb 32,16,0
Rb 36,16,2048
Rb 40,16,8192
Rb 44,16,10240
Rb 48,16,16384
Rb 52,16,18432
Rb 56,16,24576
Rb 60,16,26624
s_add_u32 m0, s37, 32768
s_add_u32 s39, s39, 0x40000
buffer_load_dwordx4 v12, s[8:11], s39 offen offset:0 lds
buffer_load_dwordx4 v13, s[8:11], s39 offen offset:1024 lds
buffer_load_dwordx4 v14, s[8:11], s39 offen offset:2048 lds
buffer_load_dwordx4 v15, s[8:11], s39 offen offset:3072 lds
s_add_u32 m0, s37, 49152
s_add_u32 s39, s39, 0x40000
buffer_load_dwordx4 v12, s[8:11], s39 offen offset:0 lds
buffer_load_dwordx4 v13, s[8:11], s39 offen offset:1024 lds
buffer_load_dwordx4 v14, s[8:11], s39 offen offset:2048 lds
buffer_load_dwordx4 v15, s[8:11], s39 offen offset:3072 lds
s_lshl2_add_u32 s40, s2, s33
s_mul_i32 s40, s40, 7168
v_min_u32 v2, 55, v0
v_lshlrev_b32 v2, 7, v2
buffer_load_dword v6, v2, s[16:19], s40 offen
s_mul_i32 s3, s3, 1664525
s_add_u32 s3, s3, 1013904223
s_waitcnt vmcnt(17)
v_dot2_f32_bf16 v120, v96, v112, 0
v_dot2_f32_bf16 v121, v100, v112, 0
v_dot2_f32_bf16 v122, v104, v112, 0
v_dot2_f32_bf16 v123, v108, v112, 0
v_dot2_f32_bf16 v124, v96, v116, 0
v_dot2_f32_bf16 v125, v100, v116, 0
v_dot2_f32_bf16 v126, v104, v116, 0
v_dot2_f32_bf16 v127, v108, v116, 0
v_dot2_f32_bf16 v120, v97, v113, v120
v_dot2_f32_bf16 v121, v101, v113, v121
)" R"(
v_dot2_f32_bf16 v122, v105, v113, v122
v_dot2_f32_bf16 v123, v109, v113, v123
v_dot2_f32_bf16 v124, v97, v117, v124
v_dot2_f32_bf16 v125, v101, v117, v125
v_dot2_f32_bf16 v126, v105, v117, v126
v_dot2_f32_bf16 v127, v109, v117, v127
v_dot2_f32_bf16 v120, v98, v114, v120
v_dot2_f32_bf16 v121, v102, v114, v121
v_dot2_f32_bf16 v122, v106, v114, v122
v_dot2_f32_bf16 v123, v110, v114, v123
v_dot2_f32_bf16 v124, v98, v118, v124
v_dot2_f32_bf16 v125, v102, v118, v125
v_dot2_f32_bf16 v126, v106, v118, v126
v_dot2_f32_bf16 v127, v110, v118, v127
v_dot2_f32_bf16 v120, v99, v115, v120
v_dot2_f32_bf16 v121, v103, v115, v121
v_dot2_f32_bf16 v122, v107, v115, v122
v_dot2_f32_bf16 v123, v111, v115, v123
v_dot2_f32_bf16 v124, v99, v119, v124
v_dot2_f32_bf16 v125, v103, v119, v125
v_dot2_f32_bf16 v126, v107, v119, v126
v_dot2_f32_bf16 v127, v111, v119, v127
v_permlane32_swap_b32_e32 v120, v121
v_add_f32_e32 v120, v120, v121
v_permlane32_swap_b32_e32 v122, v123
v_add_f32_e32 v122, v122, v123
v_permlane32_swap_b32_e32 v124, v125
v_add_f32_e32 v124, v124, v125
v_permlane32_swap_b32_e32 v126, v127
v_add_f32_e32 v126, v126, v127
v_permlane16_swap_b32_e32 v120, v122
v_add_f32_e32 v120, v120, v122
v_permlane16_swap_b32_e32 v124, v126
v_add_f32_e32 v124, v124, v126
v_add_f32_dpp v120, v120, v120 row_ror:8 row_mask:0xf bank_mask:0xf
s_nop 0
v_add_f32_dpp v124, v124, v124 row_ror:8 row_mask:0xf bank_mask:0xf
v_add_f32_dpp v120, v120, v120 row_ror:4 row_mask:0xf bank_mask:0xf
s_nop 0
v_add_f32_dpp v124, v124, v124 row_ror:4 row_mask:0xf bank_mask:0xf
v_add_f32_dpp v120, v120, v120 row_ror:2 row_mask:0xf bank_mask:0xf
s_nop 0
v_add_f32_dpp v124, v124, v124 row_ror:2 row_mask:0xf bank_mask:0xf
v_add_f32_dpp v120, v120, v120 row_ror:1 row_mask:0xf bank_mask:0xf
s_nop 0
v_add_f32_dpp v124, v124, v124 row_ror:1 row_mask:0xf bank_mask:0xf
Cv 120,120,120
v_lshlrev_b32 v120, 16, v120
Cv 124,124,124
v_lshlrev_b32 v124, 16, v124
v_mul_f32_e32 v120, 0xbfb8aa3b, v120
v_exp_f32_e32 v120, v120
s_nop 0
v_add_f32_e32 v120, 1.0, v120
v_rcp_f32_e32 v120, v120
s_nop 0
Cv 120,120,120
v_lshlrev_b32 v120, 16, v120
v_lshrrev_b32 v121, 5, v0
v_bfe_u32 v122, v0, 4, 1
v_lshl_or_b32 v121, v122, 1, v121
v_add_u32 v121, s94, v121
v_add_f32_e32 v125, 0, v124
v_ashrrev_i32 v126, 31, v125
)" R"(
v_or_b32 v126, 0x80000000, v126
v_xor_b32 v125, v125, v126
v_and_b32 v125, 0xffff0000, v125
v_sub_u32 v126, 0xffff, v121
v_or_b32 v116, v125, v126
v_mov_b32 v117, s3
v_mov_b32 v118, v120
v_mov_b32 v119, v124
v_and_b32 v122, 15, v0
v_min_u32 v122, 1, v122
v_lshlrev_b32 v122, 30, v122
v_lshl_or_b32 v122, v121, 4, v122
s_mov_b32 s94, 0x1500000
buffer_store_dwordx4 v[116:119], v122, s[20:23], s94 offen sc0 sc1
s_cmp_eq_u32 s91, 1
s_waitcnt lgkmcnt(0)
s_barrier
s_cbranch_scc1 .LzAV
s_branch .LzAK
.LzAK:
Mz 32,0,32
Rt 64,18,16384
Mz 64,0,36
s_lshl1_add_u32 s40, s34, s91
s_lshl_b32 s40, s40, 12
Mz 96,0,40
Rt 66,18,18432
s_and_b32 s29, s38, 0xfffe0000
Mz 128,0,44
s_add_u32 s39, s39, 0x40000
s_add_u32 s40, s40, s29
Mz 160,0,48
Rt 68,19,16384
s_add_u32 m0, s37, 0
Mz 192,0,52
buffer_load_dwordx4 v12, s[8:11], s39 offen offset:0 lds
Mz 224,0,56
Rt 70,19,18432
v_min_u32 v3, 31, v0
v_mfma_f32_16x16x32_bf16 a[224:227], a[0:3], a[60:63], 0
v_lshlrev_b32 v3, 7, v3
v_mfma_f32_16x16x32_bf16 a[228:231], a[4:7], a[60:63], 0
Rt 72,20,16384
Mz 228,4,56
Mz 196,4,52
Rt 74,20,18432
Mz 164,4,48
Mz 132,4,44
Rt 76,21,16384
Mz 100,4,40
buffer_load_dwordx4 v13, s[8:11], s39 offen offset:1024 lds
Mz 68,4,36
Rt 78,21,18432
Mz 36,4,32
Mz 40,8,32
Rt 80,22,16384
Mz 72,8,36
Mz 104,8,40
Rt 82,22,18432
Mz 136,8,44
Mz 168,8,48
Rt 84,23,16384
Mz 200,8,52
buffer_load_dwordx4 v14, s[8:11], s39 offen offset:2048 lds
Mz 232,8,56
)" R"(
Rt 86,23,18432
v_mfma_f32_16x16x32_bf16 a[232:235], a[8:11], a[60:63], 0
v_mfma_f32_16x16x32_bf16 a[236:239], a[12:15], a[60:63], 0
Rt 88,24,16384
Mz 236,12,56
Mz 204,12,52
Rt 90,24,18432
Mz 172,12,48
Mz 140,12,44
Rt 92,25,16384
Mz 108,12,40
buffer_load_dwordx4 v15, s[8:11], s39 offen offset:3072 lds
Mz 76,12,36
Rt 94,25,18432
.LzH23:
s_cbranch_execz .LzH34
Mz 44,12,32
Mz 48,16,32
Rb 96,17,0
Mz 80,16,36
buffer_load_dword v6, v3, s[4:7], s40 offen
Mz 112,16,40
Rb 100,17,2048
Mz 144,16,44
Mz 176,16,48
Rb 104,17,8192
s_mov_b32 s28, s35
Mz 208,16,52
s_lshl2_add_u32 s28, s28, s33
s_mul_i32 s43, s28, 28672
Mz 240,16,56
Rb 108,17,10240
v_and_b32 v26, 0x70, v1
v_mfma_f32_16x16x32_bf16 a[240:243], a[16:19], a[60:63], 0
v_and_b32 v4, 48, v0
v_xor_b32 v26, v26, v4
v_mfma_f32_16x16x32_bf16 a[244:247], a[20:23], a[60:63], 0
Rb 112,17,16384
v_lshrrev_b32 v4, 3, v0
Mz 244,20,56
v_mul_u32_u24 v4, 896, v4
v_add_u32 v26, v26, v4
Mz 212,20,52
Rb 116,17,18432
v_xor_b32 v27, 64, v26
Mz 180,20,48
v_add_u32 v27, 6144, v27
v_bfe_u32 v28, v0, 1, 3
Mz 148,20,44
Rb 120,17,24576
v_lshrrev_b32 v4, 4, v0
Mz 116,20,40
v_xor_b32 v28, v28, v4
v_lshlrev_b32 v28, 4, v28
Mz 84,20,36
Rb 124,17,26624
v_and_b32 v4, 15, v0
Mz 52,20,32
v_lshl_or_b32 v28, v4, 7, v28
s_add_u32 s28, s36, 0x18000
Mz 56,24,32
v_add_u32 v28, s28, v28
v_xor_b32 v29, 64, v28
Mz 88,24,36
)" R"(
Mz 120,24,40
Mz 152,24,44
Mz 184,24,48
Mz 216,24,52
Mz 248,24,56
v_mfma_f32_16x16x32_bf16 a[248:251], a[24:27], a[60:63], 0
v_mfma_f32_16x16x32_bf16 a[252:255], a[28:31], a[60:63], 0
Mz 252,28,56
Mz 220,28,52
Mz 188,28,48
Mz 156,28,44
Mz 124,28,40
Mz 92,28,36
Mz 60,28,32
s_call_b64 s[30:31], .LzHOP
s_waitcnt vmcnt(11) lgkmcnt(0)
s_barrier
Mv 32,64,96
Rt 0,18,32768
s_bfe_u32 s88, s2, 0x20005
Mv 64,64,100
s_add_u32 s38, s38, 0xffff0080
s_and_b32 s28, s2, 15
Mv 96,64,104
Rt 2,18,34816
s_add_u32 m0, s36, 0
Mv 128,64,108
buffer_load_dwordx4 v8, s[4:7], s38 offen offset:0 lds
Mv 160,64,112
Rt 4,19,32768
s_lshl4_add_u32 s88, s88, s28
Mv 192,64,116
s_lshl2_add_u32 s88, s88, s33
s_mul_i32 s88, s88, 4096
Mv 224,64,120
Rt 6,19,34816
s_add_u32 s88, s88, 0x100000
Ma 224,64,124
buffer_load_dwordx4 v9, s[4:7], s38 offen offset:1024 lds
Ma 228,68,124
Rt 8,20,32768
v_min_u32 v5, 31, v0
Mv 228,68,120
v_lshlrev_b32 v5, 7, v5
s_mov_b32 s90, 0x100000
Mv 196,68,116
Rt 10,20,34816
s_mov_b64 s[24:25], s[8:9]
Mv 164,68,112
buffer_load_dwordx4 v10, s[4:7], s38 offen offset:2048 lds
Mv 132,68,108
Rt 12,21,32768
s_mov_b64 s[26:27], s[10:11]
Mv 100,68,104
Mv 68,68,100
Rt 14,21,34816
Mv 36,68,96
buffer_load_dwordx4 v11, s[4:7], s38 offen offset:3072 lds
Mv 40,72,96
Rt 16,22,32768
Mv 72,72,100
s_add_u32 s38, s38, 0x10000
Mv 104,72,104
Rt 18,22,34816
s_add_u32 m0, s36, 4096
)" R"(
Mv 136,72,108
buffer_load_dwordx4 v8, s[4:7], s38 offen offset:0 lds
Mv 168,72,112
Rt 20,23,32768
Mv 200,72,116
Mv 232,72,120
Rt 22,23,34816
Ma 232,72,124
buffer_load_dwordx4 v9, s[4:7], s38 offen offset:1024 lds
Ma 236,76,124
Rt 24,24,32768
Mv 236,76,120
Mv 204,76,116
Rt 26,24,34816
Mv 172,76,112
buffer_load_dwordx4 v10, s[4:7], s38 offen offset:2048 lds
Mv 140,76,108
Rt 28,25,32768
Mv 108,76,104
Mv 76,76,100
Rt 30,25,34816
Mv 44,76,96
buffer_load_dwordx4 v11, s[4:7], s38 offen offset:3072 lds
Mv 48,80,96
Rb 32,16,32768
Mv 80,80,100
Mv 112,80,104
Rb 36,16,34816
Mv 144,80,108
s_add_u32 s39, s39, 0x40000
Mv 176,80,112
Rb 40,16,40960
s_add_u32 m0, s37, 16384
Mv 208,80,116
buffer_load_dwordx4 v12, s[8:11], s39 offen offset:0 lds
Mv 240,80,120
Rb 44,16,43008
Ma 240,80,124
Ma 244,84,124
Rb 48,16,49152
Mv 244,84,120
Mv 212,84,116
Rb 52,16,51200
Mv 180,84,112
buffer_load_dwordx4 v13, s[8:11], s39 offen offset:1024 lds
Mv 148,84,108
Rb 56,16,57344
Mv 116,84,104
Mv 84,84,100
Rb 60,16,59392
Mv 52,84,96
Mv 56,88,96
Mv 88,88,100
buffer_load_dwordx4 v14, s[8:11], s39 offen offset:2048 lds
Mv 120,88,104
Mv 152,88,108
Mv 184,88,112
Mv 216,88,116
Mv 248,88,120
Ma 248,88,124
buffer_load_dwordx4 v15, s[8:11], s39 offen offset:3072 lds
Ma 252,92,124
Mv 252,92,120
s_add_u32 s88, s88, s90
)" R"(
Mv 220,92,116
Mv 188,92,112
buffer_load_dword v6, v5, s[24:27], s88 offen
Mv 156,92,108
Mv 124,92,104
Mv 92,92,100
Mv 60,92,96
s_waitcnt vmcnt(20) lgkmcnt(0)
s_barrier
Mv 32,0,32
Rt 64,18,49152
s_lshl_b32 s29, s91, 22
Mv 64,0,36
s_add_u32 s28, s89, s29
s_bfe_u32 s29, s63, 0x80000
Mv 96,0,40
Rt 66,18,51200
s_lshl_b32 s29, s29, 16
Mv 128,0,44
s_add_u32 s39, s39, 0x40000
s_add_u32 s45, s28, s29
Mv 160,0,48
Rt 68,19,49152
s_add_u32 m0, s37, 32768
Mv 192,0,52
buffer_load_dwordx4 v12, s[8:11], s39 offen offset:0 lds
Mv 224,0,56
Rt 70,19,51200
s_bfe_u32 s29, s63, 0x80008
Ma 224,0,60
s_lshl_b32 s29, s29, 16
s_add_u32 s46, s28, s29
Ma 228,4,60
Rt 72,20,49152
s_bfe_u32 s29, s63, 0x80010
Mv 228,4,56
s_lshl_b32 s29, s29, 16
s_add_u32 s47, s28, s29
Mv 196,4,52
Rt 74,20,51200
s_bfe_u32 s29, s63, 0x80018
Mv 164,4,48
s_lshl_b32 s29, s29, 16
s_add_u32 s48, s28, s29
Mv 132,4,44
Rt 76,21,49152
s_add_u32 s50, s89, 0xd40000
Mv 100,4,40
buffer_load_dwordx4 v13, s[8:11], s39 offen offset:1024 lds
Mv 68,4,36
Rt 78,21,51200
s_lshl2_add_u32 s28, s91, s93
Mv 36,4,32
s_lshl_b32 s28, s28, 7
s_add_u32 s49, s28, s50
Mv 40,8,32
Rt 80,22,49152
s_lshl_b32 s52, s35, 7
Mv 72,8,36
s_add_u32 s52, s52, s50
s_add_u32 s52, s52, 0xd000
Mv 104,8,40
Rt 82,22,51200
s_lshl_b32 s51, s35, 17
Mv 136,8,44
)" R"(
s_add_u32 s51, s51, s89
s_add_u32 s51, s51, 0x800000
Mv 168,8,48
Rt 84,23,49152
Mv 200,8,52
buffer_load_dwordx4 v14, s[8:11], s39 offen offset:2048 lds
Mv 232,8,56
Rt 86,23,51200
Ma 232,8,60
s_cmp_lg_u32 s33, 0
s_cmov_b32 s49, 0x40000000
Ma 236,12,60
Rt 88,24,49152
s_mov_b32 s53, 0x40000
Mv 236,12,56
s_mov_b32 s54, 0x3db8aa3b
s_mul_i32 s41, s33, 7168
Mv 204,12,52
Rt 90,24,51200
s_mul_i32 s42, s33, 0x70000
Mv 172,12,48
s_lshl_b32 s28, s34, 9
s_add_u32 s42, s42, s28
Mv 140,12,44
Rt 92,25,49152
s_mov_b32 s62, 0x4e800000
Mv 108,12,40
buffer_load_dwordx4 v15, s[8:11], s39 offen offset:3072 lds
Mv 76,12,36
Rt 94,25,51200
s_add_u32 s67, s89, 0xc00000
Mv 44,12,32
s_lshl3_add_u32 s28, s34, s35
s_add_u32 s64, s51, 0x100000
Mv 48,16,32
Rb 96,17,32768
s_lshl2_add_u32 s28, s28, s33
s_add_u32 m0, s36, 131072
s_add_u32 s44, s43, 0
Mv 80,16,36
s_lshl2_add_u32 s65, s35, s33
s_lshl_b32 s65, s65, 7
buffer_load_dwordx4 v26, s[12:15], s44 offen offset:0 lds
Mv 112,16,40
Rb 100,17,34816
s_add_u32 s65, s65, s50
Mv 144,16,44
s_add_u32 s65, s65, 0xc000
s_lshl_b32 s66, s28, 10
Mv 176,16,48
Rb 104,17,40960
s_add_u32 s66, s66, 0x1400000
Mv 208,16,52
s_mov_b32 s28, s35
s_lshl2_add_u32 s28, s28, s33
buffer_load_dwordx4 v27, s[12:15], s44 offen offset:1024 lds
Mv 240,16,56
Rb 108,17,43008
s_and_b32 s29, s34, 1
Ma 240,16,60
s_lshl1_add_u32 s29, s28, s29
s_lshl_b32 s73, s29, 15
Ma 244,20,60
Rb 112,17,49152
)" R"(
s_and_b32 s29, s89, 0xfe000000
Mv 244,20,56
s_add_u32 s73, s73, s29
s_add_u32 s73, s73, 0x1000000
s_add_u32 s44, s43, 12288
buffer_load_dwordx4 v26, s[12:15], s44 offen offset:2048 lds
Mv 212,20,52
Rb 116,17,51200
s_lshl4_add_u32 s28, s28, s34
Mv 180,20,48
s_lshl_b32 s68, s28, 8
s_add_u32 s68, s68, 0x1510000
Mv 148,20,44
Rb 120,17,57344
s_add_u32 s55, s89, 0x120000
Mv 116,20,40
s_lshl_b32 s28, s35, 1
s_sub_u32 s57, 14, s28
buffer_load_dwordx4 v27, s[12:15], s44 offen offset:3072 lds
Mv 84,20,36
Rb 124,17,59392
s_mov_b32 s59, s89
Mv 52,20,32
s_mov_b32 s58, 2
s_add_u32 m0, s36, 135168
Mv 56,24,32
s_cmp_eq_u32 s35, 7
s_cmov_b32 s55, s89
s_add_u32 s44, s43, 128
Mv 88,24,36
s_cmp_eq_u32 s35, 7
s_cmov_b32 s57, 16
buffer_load_dwordx4 v26, s[12:15], s44 offen offset:0 lds
Mv 120,24,40
s_cmp_eq_u32 s35, 7
s_cmov_b32 s59, s67
Mv 152,24,44
s_add_u32 s56, s55, 0x400000
Mv 184,24,48
Mv 216,24,52
buffer_load_dwordx4 v27, s[12:15], s44 offen offset:1024 lds
Mv 248,24,56
Ma 248,24,60
Ma 252,28,60
Mv 252,28,56
s_add_u32 s44, s43, 12416
buffer_load_dwordx4 v26, s[12:15], s44 offen offset:2048 lds
Mv 220,28,52
Mv 188,28,48
Mv 156,28,44
Mv 124,28,40
buffer_load_dwordx4 v27, s[12:15], s44 offen offset:3072 lds
Mv 92,28,36
Mv 60,28,32
s_waitcnt vmcnt(17) lgkmcnt(0)
s_barrier
Mv 32,64,96
Rt 0,18,0
Mv 64,64,100
s_add_u32 s38, s38, 0xffff0080
Mv 96,64,104
Rt 2,18,2048
s_add_u32 m0, s36, 32768
Mv 128,64,108
buffer_load_dwordx4 v8, s[4:7], s38 offen offset:0 lds
)" R"(
Mv 160,64,112
Rt 4,19,0
Mv 192,64,116
Mv 224,64,120
Rt 6,19,2048
Ma 224,64,124
buffer_load_dwordx4 v9, s[4:7], s38 offen offset:1024 lds
Ma 228,68,124
Rt 8,20,0
s_lshl2_add_u32 s28, s2, s33
Mv 228,68,120
s_lshl_b32 s28, s28, 11
s_add_u32 s28, s28, 0x1500000
Mv 196,68,116
Rt 10,20,2048
v_min_u32 v3, 15, v0
Mv 164,68,112
buffer_load_dwordx4 v10, s[4:7], s38 offen offset:2048 lds
Mv 132,68,108
Rt 12,21,0
v_lshlrev_b32 v3, 7, v3
Mv 100,68,104
Mv 68,68,100
Rt 14,21,2048
Mv 36,68,96
buffer_load_dwordx4 v11, s[4:7], s38 offen offset:3072 lds
Mv 40,72,96
Rt 16,22,0
Mv 72,72,100
s_add_u32 s38, s38, 0x10000
Mv 104,72,104
Rt 18,22,2048
s_add_u32 m0, s36, 36864
Mv 136,72,108
buffer_load_dwordx4 v8, s[4:7], s38 offen offset:0 lds
Mv 168,72,112
Rt 20,23,0
Mv 200,72,116
Mv 232,72,120
Rt 22,23,2048
Ma 232,72,124
buffer_load_dwordx4 v9, s[4:7], s38 offen offset:1024 lds
Ma 236,76,124
Rt 24,24,0
Mv 236,76,120
Mv 204,76,116
Rt 26,24,2048
Mv 172,76,112
buffer_load_dwordx4 v10, s[4:7], s38 offen offset:2048 lds
Mv 140,76,108
Rt 28,25,0
Mv 108,76,104
Mv 76,76,100
Rt 30,25,2048
Mv 44,76,96
buffer_load_dwordx4 v11, s[4:7], s38 offen offset:3072 lds
Mv 48,80,96
Rb 32,16,0
Mv 80,80,100
Mv 112,80,104
Rb 36,16,2048
Mv 144,80,108
s_add_u32 s39, s39, 0x40000
Mv 176,80,112
)" R"(
Rb 40,16,8192
s_add_u32 m0, s37, 49152
Mv 208,80,116
buffer_load_dwordx4 v12, s[8:11], s39 offen offset:0 lds
Mv 240,80,120
Rb 44,16,10240
Ma 240,80,124
Ma 244,84,124
Rb 48,16,16384
Mv 244,84,120
Mv 212,84,116
Rb 52,16,18432
Mv 180,84,112
buffer_load_dwordx4 v13, s[8:11], s39 offen offset:1024 lds
Mv 148,84,108
Rb 56,16,24576
Mv 116,84,104
Mv 84,84,100
Rb 60,16,26624
Mv 52,84,96
Mv 56,88,96
Mv 88,88,100
buffer_load_dwordx4 v14, s[8:11], s39 offen offset:2048 lds
Mv 120,88,104
Mv 152,88,108
Mv 184,88,112
Mv 216,88,116
Mv 248,88,120
Ma 248,88,124
buffer_load_dwordx4 v15, s[8:11], s39 offen offset:3072 lds
Ma 252,92,124
Mv 252,92,120
s_add_u32 s88, s88, s90
s_mov_b32 s40, 5
Mv 220,92,116
Mv 188,92,112
buffer_load_dword v6, v5, s[24:27], s88 offen
Mv 156,92,108
Mv 124,92,104
buffer_load_dword v6, v3, s[20:23], s28 offen
Mv 92,92,100
Mv 60,92,96
s_waitcnt vmcnt(27) lgkmcnt(0)
s_barrier
.LzPAK:
Mv 32,0,32
Rt 64,18,16384
Mv 64,0,36
Mv 96,0,40
Rt 66,18,18432
Mv 128,0,44
s_add_u32 s39, s39, 0x40000
Mv 160,0,48
Rt 68,19,16384
s_add_u32 m0, s37, 0
Mv 192,0,52
buffer_load_dwordx4 v12, s[8:11], s39 offen offset:0 lds
Mv 224,0,56
Rt 70,19,18432
Ma 224,0,60
Ma 228,4,60
Rt 72,20,16384
Mv 228,4,56
Mv 196,4,52
Rt 74,20,18432
)" R"(
Mv 164,4,48
Mv 132,4,44
Rt 76,21,16384
Mv 100,4,40
buffer_load_dwordx4 v13, s[8:11], s39 offen offset:1024 lds
Mv 68,4,36
Rt 78,21,18432
Mv 36,4,32
Mv 40,8,32
Rt 80,22,16384
Mv 72,8,36
Mv 104,8,40
Rt 82,22,18432
Mv 136,8,44
Mv 168,8,48
Rt 84,23,16384
Mv 200,8,52
buffer_load_dwordx4 v14, s[8:11], s39 offen offset:2048 lds
Mv 232,8,56
Rt 86,23,18432
Ma 232,8,60
Ma 236,12,60
Rt 88,24,16384
Mv 236,12,56
Mv 204,12,52
Rt 90,24,18432
Mv 172,12,48
Mv 140,12,44
Rt 92,25,16384
Mv 108,12,40
buffer_load_dwordx4 v15, s[8:11], s39 offen offset:3072 lds
Mv 76,12,36
Rt 94,25,18432
Mv 44,12,32
Mv 48,16,32
Rb 96,17,0
Mv 80,16,36
Mv 112,16,40
Rb 100,17,2048
Mv 144,16,44
Mv 176,16,48
Rb 104,17,8192
Mv 208,16,52
Mv 240,16,56
Rb 108,17,10240
Ma 240,16,60
Ma 244,20,60
Rb 112,17,16384
Mv 244,20,56
Mv 212,20,52
Rb 116,17,18432
Mv 180,20,48
Mv 148,20,44
Rb 120,17,24576
.LzH24:
s_cbranch_execz .LzH35
Mv 116,20,40
Mv 84,20,36
Rb 124,17,26624
Mv 52,20,32
Mv 56,24,32
Mv 88,24,36
Mv 120,24,40
Mv 152,24,44
)" R"(
Mv 184,24,48
Mv 216,24,52
Mv 248,24,56
Ma 248,24,60
Ma 252,28,60
Mv 252,28,56
Mv 220,28,52
Mv 188,28,48
Mv 156,28,44
Mv 124,28,40
Mv 92,28,36
Mv 60,28,32
s_waitcnt vmcnt(9) lgkmcnt(0)
s_barrier
Mv 32,64,96
Rt 0,18,32768
Mv 64,64,100
s_add_u32 s38, s38, 0xffff0080
Mv 96,64,104
Rt 2,18,34816
s_add_u32 m0, s36, 0
Mv 128,64,108
buffer_load_dwordx4 v8, s[4:7], s38 offen offset:0 lds
Mv 160,64,112
Rt 4,19,32768
Mv 192,64,116
Mv 224,64,120
Rt 6,19,34816
Ma 224,64,124
buffer_load_dwordx4 v9, s[4:7], s38 offen offset:1024 lds
Ma 228,68,124
Rt 8,20,32768
Mv 228,68,120
Mv 196,68,116
Rt 10,20,34816
Mv 164,68,112
buffer_load_dwordx4 v10, s[4:7], s38 offen offset:2048 lds
Mv 132,68,108
Rt 12,21,32768
Mv 100,68,104
Mv 68,68,100
Rt 14,21,34816
Mv 36,68,96
buffer_load_dwordx4 v11, s[4:7], s38 offen offset:3072 lds
Mv 40,72,96
Rt 16,22,32768
Mv 72,72,100
s_add_u32 s38, s38, 0x10000
Mv 104,72,104
Rt 18,22,34816
s_add_u32 m0, s36, 4096
Mv 136,72,108
buffer_load_dwordx4 v8, s[4:7], s38 offen offset:0 lds
Mv 168,72,112
Rt 20,23,32768
Mv 200,72,116
Mv 232,72,120
Rt 22,23,34816
Ma 232,72,124
buffer_load_dwordx4 v9, s[4:7], s38 offen offset:1024 lds
Ma 236,76,124
Rt 24,24,32768
Mv 236,76,120
Mv 204,76,116
Rt 26,24,34816
)" R"(
Mv 172,76,112
buffer_load_dwordx4 v10, s[4:7], s38 offen offset:2048 lds
Mv 140,76,108
Rt 28,25,32768
Mv 108,76,104
Mv 76,76,100
Rt 30,25,34816
Mv 44,76,96
buffer_load_dwordx4 v11, s[4:7], s38 offen offset:3072 lds
Mv 48,80,96
Rb 32,16,32768
Mv 80,80,100
Mv 112,80,104
Rb 36,16,34816
Mv 144,80,108
s_add_u32 s39, s39, 0x40000
Mv 176,80,112
Rb 40,16,40960
s_add_u32 m0, s37, 16384
Mv 208,80,116
buffer_load_dwordx4 v12, s[8:11], s39 offen offset:0 lds
Mv 240,80,120
Rb 44,16,43008
Ma 240,80,124
Ma 244,84,124
Rb 48,16,49152
Mv 244,84,120
Mv 212,84,116
Rb 52,16,51200
Mv 180,84,112
buffer_load_dwordx4 v13, s[8:11], s39 offen offset:1024 lds
Mv 148,84,108
Rb 56,16,57344
Mv 116,84,104
Mv 84,84,100
Rb 60,16,59392
Mv 52,84,96
Mv 56,88,96
Mv 88,88,100
buffer_load_dwordx4 v14, s[8:11], s39 offen offset:2048 lds
Mv 120,88,104
Mv 152,88,108
Mv 184,88,112
Mv 216,88,116
Mv 248,88,120
Ma 248,88,124
buffer_load_dwordx4 v15, s[8:11], s39 offen offset:3072 lds
Ma 252,92,124
Mv 252,92,120
s_add_u32 s88, s88, s90
Mv 220,92,116
Mv 188,92,112
buffer_load_dword v6, v5, s[24:27], s88 offen
Mv 156,92,108
Mv 124,92,104
Mv 92,92,100
Mv 60,92,96
s_waitcnt vmcnt(18) lgkmcnt(0)
s_barrier
Mv 32,0,32
Rt 64,18,49152
Mv 64,0,36
Mv 96,0,40
Rt 66,18,51200
Mv 128,0,44
)" R"(
s_add_u32 s39, s39, 0x40000
Mv 160,0,48
Rt 68,19,49152
s_add_u32 m0, s37, 32768
Mv 192,0,52
buffer_load_dwordx4 v12, s[8:11], s39 offen offset:0 lds
Mv 224,0,56
Rt 70,19,51200
Ma 224,0,60
Ma 228,4,60
Rt 72,20,49152
Mv 228,4,56
Mv 196,4,52
Rt 74,20,51200
Mv 164,4,48
Mv 132,4,44
Rt 76,21,49152
Mv 100,4,40
buffer_load_dwordx4 v13, s[8:11], s39 offen offset:1024 lds
Mv 68,4,36
Rt 78,21,51200
Mv 36,4,32
Mv 40,8,32
Rt 80,22,49152
Mv 72,8,36
Mv 104,8,40
Rt 82,22,51200
Mv 136,8,44
Mv 168,8,48
Rt 84,23,49152
Mv 200,8,52
buffer_load_dwordx4 v14, s[8:11], s39 offen offset:2048 lds
Mv 232,8,56
Rt 86,23,51200
Ma 232,8,60
Ma 236,12,60
Rt 88,24,49152
Mv 236,12,56
Mv 204,12,52
Rt 90,24,51200
Mv 172,12,48
Mv 140,12,44
Rt 92,25,49152
Mv 108,12,40
buffer_load_dwordx4 v15, s[8:11], s39 offen offset:3072 lds
Mv 76,12,36
Rt 94,25,51200
Mv 44,12,32
Mv 48,16,32
Rb 96,17,32768
Mv 80,16,36
Mv 112,16,40
Rb 100,17,34816
Mv 144,16,44
Mv 176,16,48
Rb 104,17,40960
Mv 208,16,52
Mv 240,16,56
Rb 108,17,43008
Ma 240,16,60
Ma 244,20,60
Rb 112,17,49152
Mv 244,20,56
Mv 212,20,52
)" R"(
Rb 116,17,51200
Mv 180,20,48
Mv 148,20,44
Rb 120,17,57344
Mv 116,20,40
Mv 84,20,36
Rb 124,17,59392
Mv 52,20,32
Mv 56,24,32
Mv 88,24,36
Mv 120,24,40
Mv 152,24,44
Mv 184,24,48
Mv 216,24,52
Mv 248,24,56
Ma 248,24,60
Ma 252,28,60
Mv 252,28,56
Mv 220,28,52
Mv 188,28,48
Mv 156,28,44
Mv 124,28,40
Mv 92,28,36
Mv 60,28,32
s_waitcnt vmcnt(9) lgkmcnt(0)
s_barrier
Mv 32,64,96
Rt 0,18,0
Mv 64,64,100
s_add_u32 s38, s38, 0xffff0080
Mv 96,64,104
Rt 2,18,2048
s_add_u32 m0, s36, 32768
Mv 128,64,108
buffer_load_dwordx4 v8, s[4:7], s38 offen offset:0 lds
Mv 160,64,112
Rt 4,19,0
Mv 192,64,116
Mv 224,64,120
Rt 6,19,2048
Ma 224,64,124
buffer_load_dwordx4 v9, s[4:7], s38 offen offset:1024 lds
Ma 228,68,124
Rt 8,20,0
Mv 228,68,120
Mv 196,68,116
Rt 10,20,2048
Mv 164,68,112
buffer_load_dwordx4 v10, s[4:7], s38 offen offset:2048 lds
Mv 132,68,108
Rt 12,21,0
Mv 100,68,104
Mv 68,68,100
Rt 14,21,2048
Mv 36,68,96
buffer_load_dwordx4 v11, s[4:7], s38 offen offset:3072 lds
Mv 40,72,96
Rt 16,22,0
Mv 72,72,100
s_add_u32 s38, s38, 0x10000
Mv 104,72,104
Rt 18,22,2048
s_add_u32 m0, s36, 36864
Mv 136,72,108
buffer_load_dwordx4 v8, s[4:7], s38 offen offset:0 lds
)" R"(
Mv 168,72,112
Rt 20,23,0
Mv 200,72,116
Mv 232,72,120
Rt 22,23,2048
Ma 232,72,124
buffer_load_dwordx4 v9, s[4:7], s38 offen offset:1024 lds
Ma 236,76,124
Rt 24,24,0
Mv 236,76,120
Mv 204,76,116
Rt 26,24,2048
Mv 172,76,112
buffer_load_dwordx4 v10, s[4:7], s38 offen offset:2048 lds
Mv 140,76,108
Rt 28,25,0
Mv 108,76,104
Mv 76,76,100
Rt 30,25,2048
Mv 44,76,96
buffer_load_dwordx4 v11, s[4:7], s38 offen offset:3072 lds
Mv 48,80,96
Rb 32,16,0
Mv 80,80,100
Mv 112,80,104
Rb 36,16,2048
Mv 144,80,108
s_add_u32 s39, s39, 0x40000
Mv 176,80,112
Rb 40,16,8192
s_add_u32 m0, s37, 49152
Mv 208,80,116
buffer_load_dwordx4 v12, s[8:11], s39 offen offset:0 lds
Mv 240,80,120
Rb 44,16,10240
Ma 240,80,124
Ma 244,84,124
Rb 48,16,16384
Mv 244,84,120
Mv 212,84,116
Rb 52,16,18432
Mv 180,84,112
buffer_load_dwordx4 v13, s[8:11], s39 offen offset:1024 lds
Mv 148,84,108
Rb 56,16,24576
Mv 116,84,104
Mv 84,84,100
Rb 60,16,26624
Mv 52,84,96
Mv 56,88,96
Mv 88,88,100
buffer_load_dwordx4 v14, s[8:11], s39 offen offset:2048 lds
Mv 120,88,104
Mv 152,88,108
Mv 184,88,112
Mv 216,88,116
Mv 248,88,120
Ma 248,88,124
buffer_load_dwordx4 v15, s[8:11], s39 offen offset:3072 lds
Ma 252,92,124
Mv 252,92,120
s_add_u32 s88, s88, s90
Mv 220,92,116
s_sub_u32 s40, s40, 1
)" R"(
Mv 188,92,112
buffer_load_dword v6, v5, s[24:27], s88 offen
Mv 156,92,108
Mv 124,92,104
Mv 92,92,100
Mv 60,92,96
s_cmp_lg_u32 s40, 0
s_waitcnt vmcnt(18) lgkmcnt(0)
s_barrier
s_cbranch_scc1 .LzPAK
.LzPAKX:
Mv 32,0,32
Rt 64,18,16384
Mv 64,0,36
Rb 192,28,32768
Mv 96,0,40
Rt 66,18,18432
Mv 128,0,44
s_add_u32 s39, s39, 0x40000
Rb 196,28,34816
Mv 160,0,48
Rt 68,19,16384
s_add_u32 m0, s37, 0
Mv 192,0,52
buffer_load_dwordx4 v12, s[8:11], s39 offen offset:0 lds
Mv 224,0,56
Rt 70,19,18432
Ma 224,0,60
Rb 200,29,32768
Ma 228,4,60
Rt 72,20,16384
Mv 228,4,56
Rb 204,29,34816
Mv 196,4,52
Rt 74,20,18432
Mv 164,4,48
Rb 208,28,36864
Mv 132,4,44
Rt 76,21,16384
Mv 100,4,40
buffer_load_dwordx4 v13, s[8:11], s39 offen offset:1024 lds
Mv 68,4,36
Rt 78,21,18432
Mv 36,4,32
Rb 212,28,38912
Mv 40,8,32
Rt 80,22,16384
Mv 72,8,36
Rb 216,29,36864
Mv 104,8,40
Rt 82,22,18432
Mv 136,8,44
Rb 220,29,38912
Mv 168,8,48
Rt 84,23,16384
Mv 200,8,52
buffer_load_dwordx4 v14, s[8:11], s39 offen offset:2048 lds
Mv 232,8,56
Rt 86,23,18432
Ma 232,8,60
Ma 236,12,60
Rt 88,24,16384
Mv 236,12,56
Mv 204,12,52
Rt 90,24,18432
)" R"(
Mv 172,12,48
Mv 140,12,44
Rt 92,25,16384
Mv 108,12,40
buffer_load_dwordx4 v15, s[8:11], s39 offen offset:3072 lds
Mv 76,12,36
Rt 94,25,18432
Mv 44,12,32
s_add_u32 s44, s43, 256
s_waitcnt lgkmcnt(6)
Mv 48,16,32
Rb 96,17,0
s_add_u32 m0, s36, 131072
Mv 80,16,36
buffer_load_dwordx4 v26, s[12:15], s44 offen offset:0 lds
Mv 112,16,40
Rb 100,17,2048
Mv 144,16,44
Mv 176,16,48
Rb 104,17,8192
Mv 208,16,52
buffer_load_dwordx4 v27, s[12:15], s44 offen offset:1024 lds
Mv 240,16,56
Rb 108,17,10240
Ma 240,16,60
Ma 244,20,60
Rb 112,17,16384
Mv 244,20,56
s_add_u32 s44, s43, 12544
buffer_load_dwordx4 v26, s[12:15], s44 offen offset:2048 lds
Mv 212,20,52
Rb 116,17,18432
Mv 180,20,48
Mv 148,20,44
Rb 120,17,24576
Mv 116,20,40
buffer_load_dwordx4 v27, s[12:15], s44 offen offset:3072 lds
Mv 84,20,36
Rb 124,17,26624
Mv 52,20,32
Mv 56,24,32
s_add_u32 m0, s36, 135168
s_add_u32 s44, s43, 384
Mv 88,24,36
buffer_load_dwordx4 v26, s[12:15], s44 offen offset:0 lds
Mv 120,24,40
Mv 152,24,44
Mv 184,24,48
Mv 216,24,52
buffer_load_dwordx4 v27, s[12:15], s44 offen offset:1024 lds
Mv 248,24,56
Ma 248,24,60
Ma 252,28,60
Mv 252,28,56
s_add_u32 s44, s43, 12672
buffer_load_dwordx4 v26, s[12:15], s44 offen offset:2048 lds
Mv 220,28,52
Mv 188,28,48
Mv 156,28,44
s_mov_b32 s85, 0x1508800
v_and_b32 v2, 7, v0
v_lshlrev_b32 v2, 7, v2
Mv 124,28,40
buffer_load_dwordx4 v27, s[12:15], s44 offen offset:3072 lds
)" R"(
Mv 92,28,36
buffer_load_dword v4, v2, s[20:23], s85 offen sc0 sc1
Mv 60,28,32
s_waitcnt vmcnt(18) lgkmcnt(0)
s_barrier
Mv 32,64,96
Rt 0,18,32768
Mv 64,64,100
s_add_u32 s38, s38, 0xffff0080
Mv 96,64,104
Rt 2,18,34816
s_add_u32 m0, s36, 0
Mv 128,64,108
buffer_load_dwordx4 v8, s[4:7], s38 offen offset:0 lds
Mv 160,64,112
Rt 4,19,32768
Mv 192,64,116
Mv 224,64,120
Rt 6,19,34816
Ma 224,64,124
buffer_load_dwordx4 v9, s[4:7], s38 offen offset:1024 lds
Ma 228,68,124
Rt 8,20,32768
Mv 228,68,120
Mv 196,68,116
Rt 10,20,34816
Mv 164,68,112
buffer_load_dwordx4 v10, s[4:7], s38 offen offset:2048 lds
Mv 132,68,108
Rt 12,21,32768
Mv 100,68,104
Mv 68,68,100
Rt 14,21,34816
Mv 36,68,96
buffer_load_dwordx4 v11, s[4:7], s38 offen offset:3072 lds
Mv 40,72,96
Rt 16,22,32768
Mv 72,72,100
s_add_u32 s38, s38, 0x10000
Mv 104,72,104
Rt 18,22,34816
s_add_u32 m0, s36, 4096
Mv 136,72,108
buffer_load_dwordx4 v8, s[4:7], s38 offen offset:0 lds
Mv 168,72,112
Rt 20,23,32768
Mv 200,72,116
Mv 232,72,120
Rt 22,23,34816
Ma 232,72,124
buffer_load_dwordx4 v9, s[4:7], s38 offen offset:1024 lds
Ma 236,76,124
Rt 24,24,32768
Mv 236,76,120
Mv 204,76,116
Rt 26,24,34816
Mv 172,76,112
buffer_load_dwordx4 v10, s[4:7], s38 offen offset:2048 lds
Mv 140,76,108
Rt 28,25,32768
Mv 108,76,104
Mv 76,76,100
Rt 30,25,34816
.LzH25:
s_cbranch_execz .LzH36
)" R"(
Mv 44,76,96
buffer_load_dwordx4 v11, s[4:7], s38 offen offset:3072 lds
Mv 48,80,96
Rb 32,16,32768
Mv 80,80,100
Mv 112,80,104
Rb 36,16,34816
Mv 144,80,108
s_add_u32 s39, s39, 0x40000
Mv 176,80,112
Rb 40,16,40960
s_add_u32 m0, s37, 16384
Mv 208,80,116
buffer_load_dwordx4 v12, s[8:11], s39 offen offset:0 lds
Mv 240,80,120
Rb 44,16,43008
Ma 240,80,124
Ma 244,84,124
Rb 48,16,49152
Mv 244,84,120
Mv 212,84,116
Rb 52,16,51200
Mv 180,84,112
buffer_load_dwordx4 v13, s[8:11], s39 offen offset:1024 lds
Mv 148,84,108
Rb 56,16,57344
Mv 116,84,104
Mv 84,84,100
Rb 60,16,59392
Mv 52,84,96
Mv 56,88,96
Mv 88,88,100
buffer_load_dwordx4 v14, s[8:11], s39 offen offset:2048 lds
Mv 120,88,104
Mv 152,88,108
Mv 184,88,112
Mv 216,88,116
Mv 248,88,120
Ma 248,88,124
buffer_load_dwordx4 v15, s[8:11], s39 offen offset:3072 lds
Ma 252,92,124
Mv 252,92,120
Mv 220,92,116
Mv 188,92,112
Mv 156,92,108
Mv 124,92,104
Mv 92,92,100
Mv 60,92,96
s_waitcnt vmcnt(13) lgkmcnt(0)
s_barrier
Mv 32,0,32
Rt 64,18,49152
Mv 64,0,36
Rb 128,28,32768
Mv 96,0,40
Rt 66,18,51200
Mv 128,0,44
s_add_u32 s39, s39, 0x40000
Rb 132,28,34816
Mv 160,0,48
Rt 68,19,49152
s_add_u32 m0, s37, 32768
Mv 192,0,52
buffer_load_dwordx4 v12, s[8:11], s39 offen offset:0 lds
Mv 224,0,56
)" R"(
Rt 70,19,51200
Ma 224,0,60
Rb 136,29,32768
Ma 228,4,60
Rt 72,20,49152
Mv 228,4,56
Rb 140,29,34816
Mv 196,4,52
Rt 74,20,51200
Mv 164,4,48
Rb 144,28,36864
Mv 132,4,44
Rt 76,21,49152
Mv 100,4,40
buffer_load_dwordx4 v13, s[8:11], s39 offen offset:1024 lds
Mv 68,4,36
Rt 78,21,51200
Mv 36,4,32
Rb 148,28,38912
Mv 40,8,32
Rt 80,22,49152
Mv 72,8,36
Rb 152,29,36864
Mv 104,8,40
Rt 82,22,51200
Mv 136,8,44
Rb 156,29,38912
Mv 168,8,48
Rt 84,23,49152
Mv 200,8,52
buffer_load_dwordx4 v14, s[8:11], s39 offen offset:2048 lds
Mv 232,8,56
Rt 86,23,51200
Ma 232,8,60
Ma 236,12,60
Rt 88,24,49152
Mv 236,12,56
Mv 204,12,52
Rt 90,24,51200
Mv 172,12,48
Mv 140,12,44
Rt 92,25,49152
Mv 108,12,40
buffer_load_dwordx4 v15, s[8:11], s39 offen offset:3072 lds
Mv 76,12,36
Rt 94,25,51200
Mv 44,12,32
s_add_u32 s44, s43, 512
s_waitcnt lgkmcnt(6)
Mv 48,16,32
Rb 96,17,32768
s_add_u32 m0, s36, 131072
Mv 80,16,36
buffer_load_dwordx4 v26, s[12:15], s44 offen offset:0 lds
Mv 112,16,40
Rb 100,17,34816
Mv 144,16,44
Mv 176,16,48
Rb 104,17,40960
Mv 208,16,52
buffer_load_dwordx4 v27, s[12:15], s44 offen offset:1024 lds
Mv 240,16,56
Rb 108,17,43008
Ma 240,16,60
)" R"(
Ma 244,20,60
Rb 112,17,49152
Mv 244,20,56
s_add_u32 s44, s43, 12800
buffer_load_dwordx4 v26, s[12:15], s44 offen offset:2048 lds
Mv 212,20,52
Rb 116,17,51200
Mv 180,20,48
Mv 148,20,44
Rb 120,17,57344
Mv 116,20,40
buffer_load_dwordx4 v27, s[12:15], s44 offen offset:3072 lds
Mv 84,20,36
Rb 124,17,59392
Mv 52,20,32
Mv 56,24,32
s_add_u32 m0, s36, 135168
s_add_u32 s44, s43, 640
Mv 88,24,36
buffer_load_dwordx4 v26, s[12:15], s44 offen offset:0 lds
Mv 120,24,40
Mv 152,24,44
Mv 184,24,48
Mv 216,24,52
buffer_load_dwordx4 v27, s[12:15], s44 offen offset:1024 lds
Mv 248,24,56
Ma 248,24,60
Ma 252,28,60
Mv 252,28,56
s_add_u32 s44, s43, 12928
buffer_load_dwordx4 v26, s[12:15], s44 offen offset:2048 lds
Mv 220,28,52
Mv 188,28,48
Mv 156,28,44
Mv 124,28,40
buffer_load_dwordx4 v27, s[12:15], s44 offen offset:3072 lds
Mv 92,28,36
Mv 60,28,32
s_waitcnt vmcnt(16) lgkmcnt(0)
s_barrier
Mv 32,64,96
Rt 0,18,0
v_xor_b32 v4, s3, v4
v_cmp_gt_u32_e32 vcc, 2, v4
s_andn2_b32 s28, 0xff, vcc_lo
s_cbranch_scc1 .Lzsp1
.Lzok1:
Mv 64,64,100
s_add_u32 s38, s38, 0xffff0080
v_xor_b32 v4, s3, v4
Mv 96,64,104
Rt 2,18,2048
s_add_u32 m0, s36, 32768
Mv 128,64,108
buffer_load_dwordx4 v8, s[4:7], s38 offen offset:0 lds
Mv 160,64,112
Rt 4,19,0
v_and_b32 v4, 1, v4
Mv 192,64,116
v_cmp_eq_u32_e32 vcc, 1, v4
s_mov_b32 s85, vcc_lo
Mv 224,64,120
Rt 6,19,2048
s_and_b32 s28, s2, 7
Ma 224,64,124
)" R"(
buffer_load_dwordx4 v9, s[4:7], s38 offen offset:1024 lds
Ma 228,68,124
Rt 8,20,0
s_lshr_b32 s28, s85, s28
Mv 228,68,120
s_xor_b32 s28, s28, s34
s_and_b32 s28, s28, 1
Mv 196,68,116
Rt 10,20,2048
s_lshl_b32 s84, s28, 13
Mv 164,68,112
buffer_load_dwordx4 v10, s[4:7], s38 offen offset:2048 lds
Mv 132,68,108
Rt 12,21,0
s_add_u32 s49, s49, s84
Mv 100,68,104
s_add_u32 s50, s50, s84
s_add_u32 s60, s50, 7168
Mv 68,68,100
Rt 14,21,2048
s_add_u32 s52, s52, s84
Mv 36,68,96
buffer_load_dwordx4 v11, s[4:7], s38 offen offset:3072 lds
Mv 40,72,96
Rt 16,22,0
s_add_u32 s65, s65, s84
Mv 72,72,100
s_add_u32 s38, s38, 0x10000
s_lshr_b32 s28, s2, 6
Mv 104,72,104
Rt 18,22,2048
s_add_u32 m0, s36, 36864
Mv 136,72,108
buffer_load_dwordx4 v8, s[4:7], s38 offen offset:0 lds
Mv 168,72,112
Rt 20,23,0
s_lshl_b32 s28, s28, 14
Mv 200,72,116
s_add_u32 s28, s28, s50
s_bfe_u32 s29, s2, 0x20004
Mv 232,72,120
Rt 22,23,2048
s_lshl2_add_u32 s29, s29, s33
Ma 232,72,124
buffer_load_dwordx4 v9, s[4:7], s38 offen offset:1024 lds
Ma 236,76,124
Rt 24,24,0
s_lshl_b32 s29, s29, 9
Mv 236,76,120
s_add_u32 s28, s28, s29
v_min_u32 v4, 3, v0
Mv 204,76,116
Rt 26,24,2048
v_lshlrev_b32 v4, 7, v4
Mv 172,76,112
buffer_load_dwordx4 v10, s[4:7], s38 offen offset:2048 lds
Mv 140,76,108
Rt 28,25,0
Mv 108,76,104
buffer_load_dword v4, v4, s[20:23], s28 offen
Mv 76,76,100
Rt 30,25,2048
s_lshl1_add_u32 s28, s36, s84
Mv 44,76,96
)" R"(
buffer_load_dwordx4 v11, s[4:7], s38 offen offset:3072 lds
Mv 48,80,96
Rb 32,16,0
v_add_u32 v2, s28, v1
Mv 80,80,100
v_add_u32 v3, 0x1000, v2
s_lshl_b32 s28, s34, 13
Mv 112,80,104
Rb 36,16,2048
s_xor_b32 s28, s28, s84
Mv 144,80,108
s_add_u32 s39, s39, 0x40000
s_and_b32 s28, s28, 0x2000
Mv 176,80,112
Rb 40,16,8192
s_add_u32 m0, s37, 49152
Mv 208,80,116
buffer_load_dwordx4 v12, s[8:11], s39 offen offset:0 lds
Mv 240,80,120
Rb 44,16,10240
s_xor_b32 s73, s73, s28
Ma 240,80,124
Ma 244,84,124
Rb 48,16,16384
Mv 244,84,120
Mv 212,84,116
Rb 52,16,18432
Mv 180,84,112
buffer_load_dwordx4 v13, s[8:11], s39 offen offset:1024 lds
Mv 148,84,108
Rb 56,16,24576
Mv 116,84,104
Mv 84,84,100
Rb 60,16,26624
Mv 52,84,96
Mv 56,88,96
Mv 88,88,100
buffer_load_dwordx4 v14, s[8:11], s39 offen offset:2048 lds
Mv 120,88,104
Mv 152,88,108
Mv 184,88,112
Mv 216,88,116
Mv 248,88,120
Ma 248,88,124
buffer_load_dwordx4 v15, s[8:11], s39 offen offset:3072 lds
Ma 252,92,124
Mv 252,92,120
Mv 220,92,116
Mv 188,92,112
Mv 156,92,108
Mv 124,92,104
Mv 92,92,100
Mv 60,92,96
s_waitcnt vmcnt(5) lgkmcnt(0)
s_barrier
Mv 32,0,32
Rt 64,18,16384
Mv 64,0,36
Rb 160,28,32768
v_and_b32 v5, 7, v0
Mv 96,0,40
Rt 66,18,18432
v_lshlrev_b32 v5, 7, v5
Mv 128,0,44
Rb 164,28,34816
)" R"(
v_mov_b32 v6, s3
Mv 160,0,48
Rt 68,19,16384
Mv 192,0,52
Mv 224,0,56
Rt 70,19,18432
Ma 224,0,60
Rb 168,29,32768
Ma 228,4,60
Rt 72,20,16384
Mv 228,4,56
Rb 172,29,34816
Mv 196,4,52
Rt 74,20,18432
Mv 164,4,48
Rb 176,28,36864
Mv 132,4,44
Rt 76,21,16384
Mv 100,4,40
Mv 68,4,36
Rt 78,21,18432
Mv 36,4,32
Rb 180,28,38912
Mv 40,8,32
Rt 80,22,16384
Mv 72,8,36
Rb 184,29,36864
Mv 104,8,40
Rt 82,22,18432
Mv 136,8,44
Rb 188,29,38912
Mv 168,8,48
Rt 84,23,16384
Mv 200,8,52
Mv 232,8,56
Rt 86,23,18432
Ma 232,8,60
Ma 236,12,60
Rt 88,24,16384
Mv 236,12,56
Mv 204,12,52
Rt 90,24,18432
Mv 172,12,48
Mv 140,12,44
Rt 92,25,16384
Mv 108,12,40
Mv 76,12,36
Rt 94,25,18432
v_lshrrev_b32 v10, 3, v0
Mv 44,12,32
v_and_b32 v11, 0x70, v1
v_lshl_or_b32 v10, v10, 13, v11
Mv 48,16,32
Rb 96,17,0
v_lshlrev_b32 v11, 1, v0
Mv 80,16,36
v_and_b32 v11, 32, v11
v_xor_b32 v10, v10, v11
buffer_load_dword v4, v7, s[20:23], s52 offen offset:0 sc0 sc1
Mv 112,16,40
Rb 100,17,2048
s_and_b32 s28, s33, 1
Mv 144,16,44
s_lshl_b32 s28, s28, 6
)" R"(
v_xor_b32 v8, s28, v10
Mv 176,16,48
Rb 104,17,8192
v_xor_b32 v9, 64, v8
Mv 208,16,52
v_add_u32 v9, 0xfc00, v9
v_add_u32 v10, 0x1f800, v8
Mv 240,16,56
Rb 108,17,10240
v_add_u32 v11, 0x1f800, v9
Ma 240,16,60
Ma 244,20,60
Rb 112,17,16384
Mv 244,20,56
Mv 212,20,52
Rb 116,17,18432
Mv 180,20,48
Mv 148,20,44
Rb 120,17,24576
Mv 116,20,40
Mv 84,20,36
Rb 124,17,26624
Mv 52,20,32
Mv 56,24,32
Mv 88,24,36
Mv 120,24,40
Mv 152,24,44
Mv 184,24,48
Mv 216,24,52
Mv 248,24,56
Ma 248,24,60
Ma 252,28,60
Mv 252,28,56
Mv 220,28,52
Mv 188,28,48
Mv 156,28,44
Mv 124,28,40
Mv 92,28,36
Mv 60,28,32
s_waitcnt vmcnt(5) lgkmcnt(0)
s_barrier
Mv 32,64,96
Rt 0,18,32768
Mv 64,64,100
s_add_u32 s92, s42, 0x0
Mv 96,64,104
Rt 2,18,34816
s_add_u32 m0, s41, 131072
Mv 128,64,108
buffer_load_dwordx4 v8, s[16:19], s92 offen offset:0 lds
Mv 160,64,112
Rt 4,19,32768
Mv 192,64,116
Mv 224,64,120
Rt 6,19,34816
Ma 224,64,124
buffer_load_dwordx4 v9, s[16:19], s92 offen offset:1024 lds
Ma 228,68,124
Rt 8,20,32768
Mv 228,68,120
Mv 196,68,116
Rt 10,20,34816
Mv 164,68,112
buffer_load_dwordx4 v10, s[16:19], s92 offen offset:2048 lds
Mv 132,68,108
)" R"(
Rt 12,21,32768
Mv 100,68,104
Mv 68,68,100
Rt 14,21,34816
Mv 36,68,96
buffer_load_dwordx4 v11, s[16:19], s92 offen offset:3072 lds
Mv 40,72,96
Rt 16,22,32768
Mv 72,72,100
s_add_u32 s92, s42, 0x40000
Mv 104,72,104
Rt 18,22,34816
s_add_u32 m0, s41, 135168
Mv 136,72,108
buffer_load_dwordx4 v8, s[16:19], s92 offen offset:0 lds
Mv 168,72,112
Rt 20,23,32768
Mv 200,72,116
Mv 232,72,120
Rt 22,23,34816
Ma 232,72,124
buffer_load_dwordx4 v9, s[16:19], s92 offen offset:1024 lds
Ma 236,76,124
Rt 24,24,32768
Mv 236,76,120
Mv 204,76,116
Rt 26,24,34816
Mv 172,76,112
buffer_load_dwordx4 v10, s[16:19], s92 offen offset:2048 lds
Mv 140,76,108
Rt 28,25,32768
Mv 108,76,104
s_add_u32 s92, s42, 0x1c0000
Mv 76,76,100
Rt 30,25,34816
s_add_u32 m0, s41, 0
Mv 44,76,96
buffer_load_dwordx4 v8, s[16:19], s92 offen offset:0 lds
Mv 48,80,96
Rb 32,16,32768
Mv 80,80,100
Mv 112,80,104
Rb 36,16,34816
Mv 144,80,108
buffer_load_dwordx4 v9, s[16:19], s92 offen offset:1024 lds
Mv 176,80,112
Rb 40,16,40960
Mv 208,80,116
Mv 240,80,120
Rb 44,16,43008
Ma 240,80,124
buffer_load_dwordx4 v10, s[16:19], s92 offen offset:2048 lds
Ma 244,84,124
Rb 48,16,49152
Mv 244,84,120
Mv 212,84,116
Rb 52,16,51200
Mv 180,84,112
buffer_load_dwordx4 v11, s[16:19], s92 offen offset:3072 lds
Mv 148,84,108
Rb 56,16,57344
Mv 116,84,104
s_add_u32 s92, s42, 0x200000
Mv 84,84,100
)" R"(
Rb 60,16,59392
s_add_u32 m0, s41, 4096
Mv 52,84,96
buffer_load_dwordx4 v8, s[16:19], s92 offen offset:0 lds
Mv 56,88,96
Mv 88,88,100
Mv 120,88,104
Mv 152,88,108
buffer_load_dwordx4 v9, s[16:19], s92 offen offset:1024 lds
Mv 184,88,112
Mv 216,88,116
Mv 248,88,120
Ma 248,88,124
buffer_load_dwordx4 v10, s[16:19], s92 offen offset:2048 lds
Ma 252,92,124
Mv 252,92,120
Mv 220,92,116
Mv 188,92,112
Mv 156,92,108
Mv 124,92,104
Mv 92,92,100
.LzH26:
s_cbranch_execz .LzH37
Mv 60,92,96
s_waitcnt vmcnt(14) lgkmcnt(4)
s_barrier
v_cmp_eq_u32_e32 vcc, s3, v4
s_andn2_b64 s[28:29], 1, vcc
s_cbranch_scc1 .Lzsp2
.Lzok2:
Mv 32,0,32
Rt 64,18,49152
Mv 64,0,36
Rt 66,18,51200
s_add_u32 s92, s42, 0x80
Mv 96,0,40
Rt 68,19,49152
s_add_u32 m0, s41, 65536
Mv 128,0,44
Rt 70,19,51200
buffer_load_dwordx4 v8, s[16:19], s92 offen offset:0 lds
Mv 132,4,44
Rt 72,20,49152
Mv 100,4,40
Rt 74,20,51200
Mv 68,4,36
Rt 76,21,49152
Mv 36,4,32
Rt 78,21,51200
buffer_load_dwordx4 v9, s[16:19], s92 offen offset:1024 lds
Mv 40,8,32
Rt 80,22,49152
Mv 72,8,36
Rt 82,22,51200
Mv 104,8,40
Rt 84,23,49152
Mv 136,8,44
Rt 86,23,51200
buffer_load_dwordx4 v10, s[16:19], s92 offen offset:2048 lds
Mv 140,12,44
Rt 88,24,49152
Mv 108,12,40
Rt 90,24,51200
Mv 76,12,36
Rt 92,25,49152
)" R"(
Mv 44,12,32
Rt 94,25,51200
buffer_load_dwordx4 v11, s[16:19], s92 offen offset:3072 lds
Mv 48,16,32
Rb 96,17,32768
Mv 80,16,36
Rb 100,17,34816
s_add_u32 s92, s42, 0x40080
Mv 112,16,40
Rb 104,17,40960
s_add_u32 m0, s41, 69632
Mv 144,16,44
Rb 108,17,43008
buffer_load_dwordx4 v8, s[16:19], s92 offen offset:0 lds
Mv 148,20,44
Rb 112,17,49152
Mv 116,20,40
Rb 116,17,51200
Mv 84,20,36
Rb 120,17,57344
Mv 52,20,32
Rb 124,17,59392
buffer_load_dwordx4 v9, s[16:19], s92 offen offset:1024 lds
Mv 56,24,32
Mv 88,24,36
Mv 120,24,40
Mv 152,24,44
buffer_load_dwordx4 v10, s[16:19], s92 offen offset:2048 lds
Mv 156,28,44
Mv 124,28,40
Mv 92,28,36
Mv 60,28,32
s_waitcnt lgkmcnt(0)
s_barrier
Mv 32,64,96
v_bfe_u32 v12, v0, 3, 2
v_lshlrev_b32 v12, 5, v12
Mv 36,68,96
v_and_b32 v4, 3, v0
v_lshl_or_b32 v12, v4, 3, v12
Mv 40,72,96
s_add_u32 m0, s36, 98304
s_add_u32 s44, s43, 768
Mv 44,76,96
buffer_load_dwordx4 v26, s[12:15], s44 offen offset:0 lds
Mv 48,80,96
v_bfe_u32 v4, v0, 2, 2
v_lshl_or_b32 v12, v4, 7, v12
Mv 52,84,96
v_lshrrev_b32 v4, 4, v0
v_lshl_or_b32 v12, v4, 10, v12
Mv 56,88,96
v_xor_b32 v13, 32, v12
v_xor_b32 v14, 64, v12
Mv 60,92,96
buffer_load_dwordx4 v27, s[12:15], s44 offen offset:1024 lds
Mv 64,64,100
v_xor_b32 v15, 96, v12
v_add_u32 v20, 0x20000, v12
Mv 68,68,100
v_add_u32 v21, 0x20000, v13
v_add_u32 v22, 0x20000, v14
Mv 72,72,100
v_add_u32 v23, 0x20000, v15
v_add_u32 v16, 0x10000, v12
)" R"(
Mv 76,76,100
s_add_u32 s44, s43, 13056
buffer_load_dwordx4 v26, s[12:15], s44 offen offset:2048 lds
Mv 80,80,100
v_add_u32 v17, 0x10000, v13
v_add_u32 v18, 0x10000, v14
Mv 84,84,100
v_add_u32 v19, 0x10000, v15
Cv 32,32,33
Mv 88,88,100
Cv 33,34,35
Cv 34,36,37
Mv 92,92,100
buffer_load_dwordx4 v27, s[12:15], s44 offen offset:3072 lds
Mv 96,64,104
Cv 35,38,39
Cv 40,40,41
Mv 100,68,104
Cv 41,42,43
Cv 42,44,45
Mv 104,72,104
s_add_u32 m0, s41, 32768
s_add_u32 s92, s42, 0x1c0080
Mv 108,76,104
buffer_load_dwordx4 v8, s[16:19], s92 offen offset:0 lds
Mv 112,80,104
Cv 43,46,47
Cv 48,48,49
Mv 116,84,104
Cv 49,50,51
Cv 50,52,53
Mv 120,88,104
Cv 51,54,55
Cv 56,56,57
Mv 124,92,104
buffer_store_dwordx4 v[32:35], v2, s[20:23], s45 offen offset:0 sc0 sc1
Mv 128,64,108
Cv 57,58,59
Cv 58,60,61
Mv 132,68,108
Cv 59,62,63
Cv 64,64,65
Mv 136,72,108
Cv 65,66,67
Cv 66,68,69
Mv 140,76,108
buffer_load_dwordx4 v9, s[16:19], s92 offen offset:1024 lds
Mv 144,80,108
Cv 67,70,71
Cv 72,72,73
Mv 148,84,108
Cv 73,74,75
Cv 74,76,77
Mv 152,88,108
Cv 75,78,79
Cv 80,80,81
Mv 156,92,108
buffer_store_dwordx4 v[40:43], v2, s[20:23], s45 offen offset:1024 sc0 sc1
Mv 160,0,48
Cv 81,82,83
Cv 82,84,85
Mv 160,64,112
Cv 83,86,87
Cv 88,88,89
)" R"(
Mv 164,4,48
Cv 89,90,91
Cv 90,92,93
Mv 164,68,112
buffer_load_dwordx4 v10, s[16:19], s92 offen offset:2048 lds
Mv 168,8,48
Cv 91,94,95
Cv 96,96,97
Mv 168,72,112
Cv 97,98,99
Cv 98,100,101
Mv 172,12,48
Cv 99,102,103
Cv 104,104,105
Mv 172,76,112
buffer_store_dwordx4 v[48:51], v2, s[20:23], s45 offen offset:2048 sc0 sc1
Mv 176,16,48
Cv 105,106,107
Cv 106,108,109
Mv 176,80,112
Cv 107,110,111
Cv 112,112,113
Mv 180,20,48
Cv 113,114,115
Cv 114,116,117
Mv 180,84,112
buffer_load_dwordx4 v11, s[16:19], s92 offen offset:3072 lds
Mv 184,24,48
Cv 115,118,119
Cv 120,120,121
Mv 184,88,112
Cv 121,122,123
Cv 122,124,125
Mv 188,28,48
Cv 123,126,127
Cv 128,128,129
Mv 188,92,112
buffer_store_dwordx4 v[56:59], v2, s[20:23], s45 offen offset:3072 sc0 sc1
Mv 192,0,52
Cv 129,130,131
Cv 130,132,133
Mv 192,64,116
Cv 131,134,135
Cv 136,136,137
Mv 196,4,52
s_add_u32 m0, s41, 36864
s_add_u32 s92, s42, 0x200080
Mv 196,68,116
buffer_load_dwordx4 v8, s[16:19], s92 offen offset:0 lds
Mv 200,8,52
Cv 137,138,139
Cv 138,140,141
Mv 200,72,116
Cv 139,142,143
Cv 144,144,145
Mv 204,12,52
Cv 145,146,147
Cv 146,148,149
Mv 204,76,116
buffer_store_dwordx4 v[64:67], v3, s[20:23], s45 offen offset:0 sc0 sc1
Mv 208,16,52
Cv 147,150,151
Cv 152,152,153
Mv 208,80,116
)" R"(
Cv 153,154,155
Cv 154,156,157
Mv 212,20,52
Cv 155,158,159
Mv 212,84,116
buffer_load_dwordx4 v9, s[16:19], s92 offen offset:1024 lds
Mv 216,24,52
Mv 216,88,116
Mv 220,28,52
Mv 220,92,116
buffer_store_dwordx4 v[72:75], v3, s[20:23], s45 offen offset:1024 sc0 sc1
Mv 224,0,56
Ma 224,0,60
Ma 228,4,60
Mv 228,4,56
buffer_load_dwordx4 v10, s[16:19], s92 offen offset:2048 lds
Mv 232,8,56
Ma 232,8,60
Ma 236,12,60
Mv 236,12,56
s_waitcnt vmcnt(24)
s_barrier
Mv 240,16,56
buffer_store_dwordx4 v[80:83], v3, s[20:23], s45 offen offset:2048 sc0 sc1
Ma 240,16,60
Ma 244,20,60
Mv 244,20,56
Mv 248,24,56
Ma 248,24,60
Ma 252,28,60
Mv 252,28,56
buffer_store_dwordx4 v[88:91], v3, s[20:23], s45 offen offset:3072 sc0 sc1
Mv 224,64,120
Rt 16,20,0
Mv 228,68,120
Rt 18,20,512
Mv 232,72,120
Rt 20,21,0
Mv 236,76,120
Rt 22,21,512
Mv 240,80,120
Rt 24,22,0
Mv 244,84,120
Rt 26,22,512
Mv 248,88,120
Rt 28,23,0
Mv 252,92,120
Rt 30,23,512
Ma 224,64,124
Rt 32,20,4096
Ma 228,68,124
Rt 34,20,4608
Ma 232,72,124
Rt 36,21,4096
Ma 236,76,124
Rt 38,21,4608
Ma 240,80,124
Rt 40,22,4096
Ma 244,84,124
Rt 42,22,4608
Ma 248,88,124
Rt 44,23,4096
Ma 252,92,124
Rt 46,23,4608
s_waitcnt vmcnt(15) lgkmcnt(8)
)" R"(
s_branch .LzQA
.LzH27:
s_cbranch_execz .LzH33
.LzAV:
Mz 32,32,0
Rt 64,18,16384
Mz 36,36,0
s_lshl1_add_u32 s40, s34, s91
s_lshl_b32 s40, s40, 12
Mz 96,40,0
Rt 66,18,18432
s_and_b32 s29, s38, 0xfffe0000
Mz 100,44,0
s_add_u32 s39, s39, 0x40000
s_add_u32 s40, s40, s29
Mz 160,48,0
Rt 68,19,16384
s_add_u32 m0, s37, 0
Mz 164,52,0
buffer_load_dwordx4 v12, s[8:11], s39 offen offset:0 lds
Mz 224,56,0
Rt 70,19,18432
v_min_u32 v3, 31, v0
Mz 228,60,0
v_lshlrev_b32 v3, 7, v3
Mz 236,60,4
Rt 72,20,16384
Mz 232,56,4
Mz 172,52,4
Rt 74,20,18432
Mz 168,48,4
Mz 108,44,4
Rt 76,21,16384
Mz 104,40,4
buffer_load_dwordx4 v13, s[8:11], s39 offen offset:1024 lds
Mz 44,36,4
Rt 78,21,18432
Mz 40,32,4
Mz 48,32,8
Rt 80,22,16384
Mz 52,36,8
Mz 112,40,8
Rt 82,22,18432
Mz 116,44,8
Mz 176,48,8
Rt 84,23,16384
Mz 180,52,8
buffer_load_dwordx4 v14, s[8:11], s39 offen offset:2048 lds
Mz 240,56,8
Rt 86,23,18432
Mz 244,60,8
Mz 252,60,12
Rt 88,24,16384
Mz 248,56,12
Mz 188,52,12
Rt 90,24,18432
Mz 184,48,12
Mz 124,44,12
Rt 92,25,16384
Mz 120,40,12
buffer_load_dwordx4 v15, s[8:11], s39 offen offset:3072 lds
Mz 60,36,12
Rt 94,25,18432
Mz 56,32,12
)" R"(
Mz 64,32,16
Rb 96,17,0
Mz 68,36,16
buffer_load_dword v6, v3, s[4:7], s40 offen
Mz 128,40,16
Rb 100,17,2048
Mz 132,44,16
Mz 192,48,16
Rb 104,17,8192
s_mov_b32 s28, s35
Mz 196,52,16
s_lshl2_add_u32 s28, s28, s33
s_mul_i32 s43, s28, 28672
v_mfma_f32_16x16x32_bf16 a[224:227], a[56:59], a[16:19], 0
Rb 108,17,10240
v_and_b32 v26, 0x70, v1
v_mfma_f32_16x16x32_bf16 a[228:231], a[60:63], a[16:19], 0
v_and_b32 v4, 48, v0
v_xor_b32 v26, v26, v4
v_mfma_f32_16x16x32_bf16 a[236:239], a[60:63], a[20:23], 0
Rb 112,17,16384
v_lshrrev_b32 v4, 3, v0
v_mfma_f32_16x16x32_bf16 a[232:235], a[56:59], a[20:23], 0
v_mul_u32_u24 v4, 896, v4
v_add_u32 v26, v26, v4
Mz 204,52,20
Rb 116,17,18432
v_xor_b32 v27, 64, v26
Mz 200,48,20
v_add_u32 v27, 6144, v27
v_bfe_u32 v28, v0, 1, 3
Mz 140,44,20
Rb 120,17,24576
v_lshrrev_b32 v4, 4, v0
Mz 136,40,20
v_xor_b32 v28, v28, v4
v_lshlrev_b32 v28, 4, v28
Mz 76,36,20
Rb 124,17,26624
v_and_b32 v4, 15, v0
Mz 72,32,20
v_lshl_or_b32 v28, v4, 7, v28
s_add_u32 s28, s36, 0x18000
Mz 80,32,24
v_add_u32 v28, s28, v28
v_xor_b32 v29, 64, v28
Mz 84,36,24
Mz 144,40,24
Mz 148,44,24
Mz 208,48,24
Mz 212,52,24
v_mfma_f32_16x16x32_bf16 a[240:243], a[56:59], a[24:27], 0
v_mfma_f32_16x16x32_bf16 a[244:247], a[60:63], a[24:27], 0
v_mfma_f32_16x16x32_bf16 a[252:255], a[60:63], a[28:31], 0
v_mfma_f32_16x16x32_bf16 a[248:251], a[56:59], a[28:31], 0
Mz 220,52,28
Mz 216,48,28
Mz 156,44,28
Mz 152,40,28
Mz 92,36,28
Mz 88,32,28
s_call_b64 s[30:31], .LzHOP
s_waitcnt vmcnt(11) lgkmcnt(0)
s_barrier
Mv 32,96,64
)" R"(
Rt 0,18,32768
s_bfe_u32 s88, s2, 0x20005
Mv 36,100,64
s_add_u32 s38, s38, 0xffff0080
s_and_b32 s28, s2, 15
Mv 96,104,64
Rt 2,18,34816
s_add_u32 m0, s36, 0
Mv 100,108,64
buffer_load_dwordx4 v8, s[4:7], s38 offen offset:0 lds
Mv 160,112,64
Rt 4,19,32768
s_lshl4_add_u32 s88, s88, s28
Mv 164,116,64
s_lshl2_add_u32 s88, s88, s33
s_mul_i32 s88, s88, 4096
Mv 224,120,64
Rt 6,19,34816
s_add_u32 s88, s88, 0x100000
Mv 228,124,64
buffer_load_dwordx4 v9, s[4:7], s38 offen offset:1024 lds
Mv 236,124,68
Rt 8,20,32768
v_min_u32 v5, 31, v0
Mv 232,120,68
v_lshlrev_b32 v5, 7, v5
s_mov_b32 s90, 0x100000
Mv 172,116,68
Rt 10,20,34816
s_mov_b64 s[24:25], s[8:9]
Mv 168,112,68
buffer_load_dwordx4 v10, s[4:7], s38 offen offset:2048 lds
Mv 108,108,68
Rt 12,21,32768
s_mov_b64 s[26:27], s[10:11]
Mv 104,104,68
Mv 44,100,68
Rt 14,21,34816
Mv 40,96,68
buffer_load_dwordx4 v11, s[4:7], s38 offen offset:3072 lds
Mv 48,96,72
Rt 16,22,32768
Mv 52,100,72
s_add_u32 s38, s38, 0x10000
Mv 112,104,72
Rt 18,22,34816
s_add_u32 m0, s36, 4096
Mv 116,108,72
buffer_load_dwordx4 v8, s[4:7], s38 offen offset:0 lds
Mv 176,112,72
Rt 20,23,32768
Mv 180,116,72
Mv 240,120,72
Rt 22,23,34816
Mv 244,124,72
buffer_load_dwordx4 v9, s[4:7], s38 offen offset:1024 lds
Mv 252,124,76
Rt 24,24,32768
Mv 248,120,76
Mv 188,116,76
Rt 26,24,34816
Mv 184,112,76
buffer_load_dwordx4 v10, s[4:7], s38 offen offset:2048 lds
Mv 124,108,76
)" R"(
Rt 28,25,32768
Mv 120,104,76
Mv 60,100,76
Rt 30,25,34816
Mv 56,96,76
buffer_load_dwordx4 v11, s[4:7], s38 offen offset:3072 lds
Mv 64,96,80
Rb 32,16,32768
Mv 68,100,80
Mv 128,104,80
Rb 36,16,34816
Mv 132,108,80
s_add_u32 s39, s39, 0x40000
Mv 192,112,80
Rb 40,16,40960
s_add_u32 m0, s37, 16384
Mv 196,116,80
buffer_load_dwordx4 v12, s[8:11], s39 offen offset:0 lds
Ma 224,120,80
Rb 44,16,43008
Ma 228,124,80
Ma 236,124,84
Rb 48,16,49152
Ma 232,120,84
Mv 204,116,84
Rb 52,16,51200
Mv 200,112,84
buffer_load_dwordx4 v13, s[8:11], s39 offen offset:1024 lds
Mv 140,108,84
Rb 56,16,57344
Mv 136,104,84
Mv 76,100,84
Rb 60,16,59392
Mv 72,96,84
Mv 80,96,88
Mv 84,100,88
buffer_load_dwordx4 v14, s[8:11], s39 offen offset:2048 lds
Mv 144,104,88
Mv 148,108,88
Mv 208,112,88
Mv 212,116,88
Ma 240,120,88
Ma 244,124,88
buffer_load_dwordx4 v15, s[8:11], s39 offen offset:3072 lds
Ma 252,124,92
.LzH28:
s_cbranch_execz .LzH34
Ma 248,120,92
s_add_u32 s88, s88, s90
Mv 220,116,92
Mv 216,112,92
buffer_load_dword v6, v5, s[24:27], s88 offen
Mv 156,108,92
Mv 152,104,92
Mv 92,100,92
Mv 88,96,92
s_waitcnt vmcnt(20) lgkmcnt(0)
s_barrier
Mv 32,32,0
Rt 64,18,49152
s_lshl_b32 s29, s91, 22
Mv 36,36,0
s_add_u32 s28, s89, s29
s_bfe_u32 s29, s63, 0x80000
Mv 96,40,0
)" R"(
Rt 66,18,51200
s_lshl_b32 s29, s29, 16
Mv 100,44,0
s_add_u32 s39, s39, 0x40000
s_add_u32 s45, s28, s29
Mv 160,48,0
Rt 68,19,49152
s_add_u32 m0, s37, 32768
Mv 164,52,0
buffer_load_dwordx4 v12, s[8:11], s39 offen offset:0 lds
Mv 224,56,0
Rt 70,19,51200
s_bfe_u32 s29, s63, 0x80008
Mv 228,60,0
s_lshl_b32 s29, s29, 16
s_add_u32 s46, s28, s29
Mv 236,60,4
Rt 72,20,49152
s_bfe_u32 s29, s63, 0x80010
Mv 232,56,4
s_lshl_b32 s29, s29, 16
s_add_u32 s47, s28, s29
Mv 172,52,4
Rt 74,20,51200
s_bfe_u32 s29, s63, 0x80018
Mv 168,48,4
s_lshl_b32 s29, s29, 16
s_add_u32 s48, s28, s29
Mv 108,44,4
Rt 76,21,49152
s_add_u32 s50, s89, 0xd40000
Mv 104,40,4
buffer_load_dwordx4 v13, s[8:11], s39 offen offset:1024 lds
Mv 44,36,4
Rt 78,21,51200
s_lshl2_add_u32 s28, s91, s93
Mv 40,32,4
s_lshl_b32 s28, s28, 7
s_add_u32 s49, s28, s50
Mv 48,32,8
Rt 80,22,49152
s_lshl_b32 s52, s35, 7
Mv 52,36,8
s_add_u32 s52, s52, s50
s_add_u32 s52, s52, 0xd000
Mv 112,40,8
Rt 82,22,51200
s_lshl_b32 s51, s35, 17
Mv 116,44,8
s_add_u32 s51, s51, s89
s_add_u32 s51, s51, 0x800000
Mv 176,48,8
Rt 84,23,49152
Mv 180,52,8
buffer_load_dwordx4 v14, s[8:11], s39 offen offset:2048 lds
Mv 240,56,8
Rt 86,23,51200
Mv 244,60,8
s_cmp_lg_u32 s33, 0
s_cmov_b32 s49, 0x40000000
Mv 252,60,12
Rt 88,24,49152
s_mov_b32 s53, 0x40000
Mv 248,56,12
)" R"(
s_mov_b32 s54, 0x3db8aa3b
s_mul_i32 s41, s33, 7168
Mv 188,52,12
Rt 90,24,51200
s_mul_i32 s42, s33, 0x70000
Mv 184,48,12
s_lshl_b32 s28, s34, 9
s_add_u32 s42, s42, s28
Mv 124,44,12
Rt 92,25,49152
s_mov_b32 s62, 0x4e800000
Mv 120,40,12
buffer_load_dwordx4 v15, s[8:11], s39 offen offset:3072 lds
Mv 60,36,12
Rt 94,25,51200
s_add_u32 s67, s89, 0xc00000
Mv 56,32,12
s_lshl3_add_u32 s28, s34, s35
s_add_u32 s64, s51, 0x100000
Mv 64,32,16
Rb 96,17,32768
s_lshl2_add_u32 s28, s28, s33
s_add_u32 m0, s36, 131072
s_add_u32 s44, s43, 0
Mv 68,36,16
s_lshl2_add_u32 s65, s35, s33
s_lshl_b32 s65, s65, 7
buffer_load_dwordx4 v26, s[12:15], s44 offen offset:0 lds
Mv 128,40,16
Rb 100,17,34816
s_add_u32 s65, s65, s50
Mv 132,44,16
s_add_u32 s65, s65, 0xc000
s_lshl_b32 s66, s28, 10
Mv 192,48,16
Rb 104,17,40960
s_add_u32 s66, s66, 0x1400000
Mv 196,52,16
s_mov_b32 s28, s35
s_lshl2_add_u32 s28, s28, s33
buffer_load_dwordx4 v27, s[12:15], s44 offen offset:1024 lds
Ma 224,56,16
Rb 108,17,43008
s_and_b32 s29, s34, 1
Ma 228,60,16
s_lshl1_add_u32 s29, s28, s29
s_lshl_b32 s73, s29, 15
Ma 236,60,20
Rb 112,17,49152
s_and_b32 s29, s89, 0xfe000000
Ma 232,56,20
s_add_u32 s73, s73, s29
s_add_u32 s73, s73, 0x1000000
s_add_u32 s44, s43, 12288
buffer_load_dwordx4 v26, s[12:15], s44 offen offset:2048 lds
Mv 204,52,20
Rb 116,17,51200
s_lshl4_add_u32 s28, s28, s34
Mv 200,48,20
s_lshl_b32 s68, s28, 8
s_add_u32 s68, s68, 0x1510000
Mv 140,44,20
Rb 120,17,57344
s_add_u32 s55, s89, 0x120000
)" R"(
Mv 136,40,20
s_lshl_b32 s28, s35, 1
s_sub_u32 s57, 14, s28
buffer_load_dwordx4 v27, s[12:15], s44 offen offset:3072 lds
Mv 76,36,20
Rb 124,17,59392
s_mov_b32 s59, s89
Mv 72,32,20
s_mov_b32 s58, 2
s_add_u32 m0, s36, 135168
Mv 80,32,24
s_cmp_eq_u32 s35, 7
s_cmov_b32 s55, s89
s_add_u32 s44, s43, 128
Mv 84,36,24
s_cmp_eq_u32 s35, 7
s_cmov_b32 s57, 16
buffer_load_dwordx4 v26, s[12:15], s44 offen offset:0 lds
Mv 144,40,24
s_cmp_eq_u32 s35, 7
s_cmov_b32 s59, s67
Mv 148,44,24
s_add_u32 s56, s55, 0x400000
Mv 208,48,24
Mv 212,52,24
buffer_load_dwordx4 v27, s[12:15], s44 offen offset:1024 lds
Ma 240,56,24
Ma 244,60,24
Ma 252,60,28
Ma 248,56,28
s_add_u32 s44, s43, 12416
buffer_load_dwordx4 v26, s[12:15], s44 offen offset:2048 lds
Mv 220,52,28
Mv 216,48,28
Mv 156,44,28
Mv 152,40,28
buffer_load_dwordx4 v27, s[12:15], s44 offen offset:3072 lds
Mv 92,36,28
Mv 88,32,28
s_waitcnt vmcnt(17) lgkmcnt(0)
s_barrier
Mv 32,96,64
Rt 0,18,0
Mv 36,100,64
s_add_u32 s38, s38, 0xffff0080
Mv 96,104,64
Rt 2,18,2048
s_add_u32 m0, s36, 32768
Mv 100,108,64
buffer_load_dwordx4 v8, s[4:7], s38 offen offset:0 lds
Mv 160,112,64
Rt 4,19,0
Mv 164,116,64
Mv 224,120,64
Rt 6,19,2048
Mv 228,124,64
buffer_load_dwordx4 v9, s[4:7], s38 offen offset:1024 lds
Mv 236,124,68
Rt 8,20,0
s_lshl2_add_u32 s28, s2, s33
Mv 232,120,68
s_lshl_b32 s28, s28, 11
s_add_u32 s28, s28, 0x1500000
Mv 172,116,68
Rt 10,20,2048
)" R"(
v_min_u32 v3, 15, v0
Mv 168,112,68
buffer_load_dwordx4 v10, s[4:7], s38 offen offset:2048 lds
Mv 108,108,68
Rt 12,21,0
v_lshlrev_b32 v3, 7, v3
Mv 104,104,68
Mv 44,100,68
Rt 14,21,2048
Mv 40,96,68
buffer_load_dwordx4 v11, s[4:7], s38 offen offset:3072 lds
Mv 48,96,72
Rt 16,22,0
Mv 52,100,72
s_add_u32 s38, s38, 0x10000
Mv 112,104,72
Rt 18,22,2048
s_add_u32 m0, s36, 36864
Mv 116,108,72
buffer_load_dwordx4 v8, s[4:7], s38 offen offset:0 lds
Mv 176,112,72
Rt 20,23,0
Mv 180,116,72
Mv 240,120,72
Rt 22,23,2048
Mv 244,124,72
buffer_load_dwordx4 v9, s[4:7], s38 offen offset:1024 lds
Mv 252,124,76
Rt 24,24,0
Mv 248,120,76
Mv 188,116,76
Rt 26,24,2048
Mv 184,112,76
buffer_load_dwordx4 v10, s[4:7], s38 offen offset:2048 lds
Mv 124,108,76
Rt 28,25,0
Mv 120,104,76
Mv 60,100,76
Rt 30,25,2048
Mv 56,96,76
buffer_load_dwordx4 v11, s[4:7], s38 offen offset:3072 lds
Mv 64,96,80
Rb 32,16,0
Mv 68,100,80
Mv 128,104,80
Rb 36,16,2048
Mv 132,108,80
s_add_u32 s39, s39, 0x40000
Mv 192,112,80
Rb 40,16,8192
s_add_u32 m0, s37, 49152
Mv 196,116,80
buffer_load_dwordx4 v12, s[8:11], s39 offen offset:0 lds
Ma 224,120,80
Rb 44,16,10240
Ma 228,124,80
Ma 236,124,84
Rb 48,16,16384
Ma 232,120,84
Mv 204,116,84
Rb 52,16,18432
Mv 200,112,84
buffer_load_dwordx4 v13, s[8:11], s39 offen offset:1024 lds
Mv 140,108,84
)" R"(
Rb 56,16,24576
Mv 136,104,84
Mv 76,100,84
Rb 60,16,26624
Mv 72,96,84
Mv 80,96,88
Mv 84,100,88
buffer_load_dwordx4 v14, s[8:11], s39 offen offset:2048 lds
Mv 144,104,88
Mv 148,108,88
Mv 208,112,88
Mv 212,116,88
Ma 240,120,88
Ma 244,124,88
buffer_load_dwordx4 v15, s[8:11], s39 offen offset:3072 lds
Ma 252,124,92
Ma 248,120,92
s_add_u32 s88, s88, s90
s_mov_b32 s40, 5
Mv 220,116,92
Mv 216,112,92
buffer_load_dword v6, v5, s[24:27], s88 offen
Mv 156,108,92
Mv 152,104,92
buffer_load_dword v6, v3, s[20:23], s28 offen
Mv 92,100,92
Mv 88,96,92
s_waitcnt vmcnt(27) lgkmcnt(0)
s_barrier
.LzPAV:
Mv 32,32,0
Rt 64,18,16384
Mv 36,36,0
Mv 96,40,0
Rt 66,18,18432
Mv 100,44,0
s_add_u32 s39, s39, 0x40000
Mv 160,48,0
Rt 68,19,16384
s_add_u32 m0, s37, 0
Mv 164,52,0
buffer_load_dwordx4 v12, s[8:11], s39 offen offset:0 lds
Mv 224,56,0
Rt 70,19,18432
Mv 228,60,0
Mv 236,60,4
Rt 72,20,16384
Mv 232,56,4
Mv 172,52,4
Rt 74,20,18432
Mv 168,48,4
Mv 108,44,4
Rt 76,21,16384
Mv 104,40,4
buffer_load_dwordx4 v13, s[8:11], s39 offen offset:1024 lds
Mv 44,36,4
Rt 78,21,18432
Mv 40,32,4
Mv 48,32,8
Rt 80,22,16384
Mv 52,36,8
Mv 112,40,8
Rt 82,22,18432
Mv 116,44,8
Mv 176,48,8
)" R"(
Rt 84,23,16384
Mv 180,52,8
buffer_load_dwordx4 v14, s[8:11], s39 offen offset:2048 lds
Mv 240,56,8
Rt 86,23,18432
Mv 244,60,8
Mv 252,60,12
Rt 88,24,16384
Mv 248,56,12
Mv 188,52,12
Rt 90,24,18432
Mv 184,48,12
Mv 124,44,12
Rt 92,25,16384
Mv 120,40,12
buffer_load_dwordx4 v15, s[8:11], s39 offen offset:3072 lds
Mv 60,36,12
Rt 94,25,18432
Mv 56,32,12
Mv 64,32,16
Rb 96,17,0
Mv 68,36,16
Mv 128,40,16
Rb 100,17,2048
Mv 132,44,16
Mv 192,48,16
Rb 104,17,8192
Mv 196,52,16
Ma 224,56,16
Rb 108,17,10240
Ma 228,60,16
Ma 236,60,20
Rb 112,17,16384
Ma 232,56,20
Mv 204,52,20
Rb 116,17,18432
Mv 200,48,20
Mv 140,44,20
Rb 120,17,24576
Mv 136,40,20
Mv 76,36,20
Rb 124,17,26624
Mv 72,32,20
Mv 80,32,24
Mv 84,36,24
Mv 144,40,24
Mv 148,44,24
Mv 208,48,24
Mv 212,52,24
Ma 240,56,24
Ma 244,60,24
Ma 252,60,28
Ma 248,56,28
Mv 220,52,28
Mv 216,48,28
Mv 156,44,28
Mv 152,40,28
Mv 92,36,28
Mv 88,32,28
s_waitcnt vmcnt(9) lgkmcnt(0)
s_barrier
Mv 32,96,64
Rt 0,18,32768
Mv 36,100,64
s_add_u32 s38, s38, 0xffff0080
)" R"(
Mv 96,104,64
Rt 2,18,34816
s_add_u32 m0, s36, 0
Mv 100,108,64
buffer_load_dwordx4 v8, s[4:7], s38 offen offset:0 lds
Mv 160,112,64
Rt 4,19,32768
Mv 164,116,64
Mv 224,120,64
Rt 6,19,34816
Mv 228,124,64
buffer_load_dwordx4 v9, s[4:7], s38 offen offset:1024 lds
Mv 236,124,68
Rt 8,20,32768
Mv 232,120,68
Mv 172,116,68
Rt 10,20,34816
Mv 168,112,68
buffer_load_dwordx4 v10, s[4:7], s38 offen offset:2048 lds
Mv 108,108,68
Rt 12,21,32768
Mv 104,104,68
Mv 44,100,68
Rt 14,21,34816
Mv 40,96,68
buffer_load_dwordx4 v11, s[4:7], s38 offen offset:3072 lds
Mv 48,96,72
Rt 16,22,32768
Mv 52,100,72
s_add_u32 s38, s38, 0x10000
Mv 112,104,72
Rt 18,22,34816
s_add_u32 m0, s36, 4096
Mv 116,108,72
buffer_load_dwordx4 v8, s[4:7], s38 offen offset:0 lds
Mv 176,112,72
Rt 20,23,32768
Mv 180,116,72
Mv 240,120,72
Rt 22,23,34816
Mv 244,124,72
buffer_load_dwordx4 v9, s[4:7], s38 offen offset:1024 lds
Mv 252,124,76
Rt 24,24,32768
Mv 248,120,76
Mv 188,116,76
Rt 26,24,34816
Mv 184,112,76
buffer_load_dwordx4 v10, s[4:7], s38 offen offset:2048 lds
Mv 124,108,76
Rt 28,25,32768
Mv 120,104,76
Mv 60,100,76
Rt 30,25,34816
Mv 56,96,76
buffer_load_dwordx4 v11, s[4:7], s38 offen offset:3072 lds
Mv 64,96,80
Rb 32,16,32768
Mv 68,100,80
Mv 128,104,80
Rb 36,16,34816
Mv 132,108,80
s_add_u32 s39, s39, 0x40000
Mv 192,112,80
)" R"(
Rb 40,16,40960
s_add_u32 m0, s37, 16384
Mv 196,116,80
buffer_load_dwordx4 v12, s[8:11], s39 offen offset:0 lds
Ma 224,120,80
Rb 44,16,43008
Ma 228,124,80
Ma 236,124,84
Rb 48,16,49152
Ma 232,120,84
Mv 204,116,84
Rb 52,16,51200
Mv 200,112,84
buffer_load_dwordx4 v13, s[8:11], s39 offen offset:1024 lds
Mv 140,108,84
Rb 56,16,57344
Mv 136,104,84
Mv 76,100,84
Rb 60,16,59392
Mv 72,96,84
Mv 80,96,88
Mv 84,100,88
buffer_load_dwordx4 v14, s[8:11], s39 offen offset:2048 lds
Mv 144,104,88
Mv 148,108,88
Mv 208,112,88
Mv 212,116,88
Ma 240,120,88
Ma 244,124,88
buffer_load_dwordx4 v15, s[8:11], s39 offen offset:3072 lds
Ma 252,124,92
Ma 248,120,92
s_add_u32 s88, s88, s90
Mv 220,116,92
Mv 216,112,92
buffer_load_dword v6, v5, s[24:27], s88 offen
Mv 156,108,92
Mv 152,104,92
Mv 92,100,92
Mv 88,96,92
s_waitcnt vmcnt(18) lgkmcnt(0)
s_barrier
Mv 32,32,0
Rt 64,18,49152
Mv 36,36,0
Mv 96,40,0
Rt 66,18,51200
Mv 100,44,0
s_add_u32 s39, s39, 0x40000
Mv 160,48,0
Rt 68,19,49152
s_add_u32 m0, s37, 32768
Mv 164,52,0
buffer_load_dwordx4 v12, s[8:11], s39 offen offset:0 lds
Mv 224,56,0
Rt 70,19,51200
Mv 228,60,0
Mv 236,60,4
Rt 72,20,49152
Mv 232,56,4
Mv 172,52,4
Rt 74,20,51200
Mv 168,48,4
Mv 108,44,4
Rt 76,21,49152
)" R"(
Mv 104,40,4
buffer_load_dwordx4 v13, s[8:11], s39 offen offset:1024 lds
Mv 44,36,4
Rt 78,21,51200
Mv 40,32,4
Mv 48,32,8
Rt 80,22,49152
Mv 52,36,8
Mv 112,40,8
Rt 82,22,51200
Mv 116,44,8
Mv 176,48,8
Rt 84,23,49152
.LzH29:
s_cbranch_execz .LzH35
Mv 180,52,8
buffer_load_dwordx4 v14, s[8:11], s39 offen offset:2048 lds
Mv 240,56,8
Rt 86,23,51200
Mv 244,60,8
Mv 252,60,12
Rt 88,24,49152
Mv 248,56,12
Mv 188,52,12
Rt 90,24,51200
Mv 184,48,12
Mv 124,44,12
Rt 92,25,49152
Mv 120,40,12
buffer_load_dwordx4 v15, s[8:11], s39 offen offset:3072 lds
Mv 60,36,12
Rt 94,25,51200
Mv 56,32,12
Mv 64,32,16
Rb 96,17,32768
Mv 68,36,16
Mv 128,40,16
Rb 100,17,34816
Mv 132,44,16
Mv 192,48,16
Rb 104,17,40960
Mv 196,52,16
Ma 224,56,16
Rb 108,17,43008
Ma 228,60,16
Ma 236,60,20
Rb 112,17,49152
Ma 232,56,20
Mv 204,52,20
Rb 116,17,51200
Mv 200,48,20
Mv 140,44,20
Rb 120,17,57344
Mv 136,40,20
Mv 76,36,20
Rb 124,17,59392
Mv 72,32,20
Mv 80,32,24
Mv 84,36,24
Mv 144,40,24
Mv 148,44,24
Mv 208,48,24
Mv 212,52,24
Ma 240,56,24
)" R"(
Ma 244,60,24
Ma 252,60,28
Ma 248,56,28
Mv 220,52,28
Mv 216,48,28
Mv 156,44,28
Mv 152,40,28
Mv 92,36,28
Mv 88,32,28
s_waitcnt vmcnt(9) lgkmcnt(0)
s_barrier
Mv 32,96,64
Rt 0,18,0
Mv 36,100,64
s_add_u32 s38, s38, 0xffff0080
Mv 96,104,64
Rt 2,18,2048
s_add_u32 m0, s36, 32768
Mv 100,108,64
buffer_load_dwordx4 v8, s[4:7], s38 offen offset:0 lds
Mv 160,112,64
Rt 4,19,0
Mv 164,116,64
Mv 224,120,64
Rt 6,19,2048
Mv 228,124,64
buffer_load_dwordx4 v9, s[4:7], s38 offen offset:1024 lds
Mv 236,124,68
Rt 8,20,0
Mv 232,120,68
Mv 172,116,68
Rt 10,20,2048
Mv 168,112,68
buffer_load_dwordx4 v10, s[4:7], s38 offen offset:2048 lds
Mv 108,108,68
Rt 12,21,0
Mv 104,104,68
Mv 44,100,68
Rt 14,21,2048
Mv 40,96,68
buffer_load_dwordx4 v11, s[4:7], s38 offen offset:3072 lds
Mv 48,96,72
Rt 16,22,0
Mv 52,100,72
s_add_u32 s38, s38, 0x10000
Mv 112,104,72
Rt 18,22,2048
s_add_u32 m0, s36, 36864
Mv 116,108,72
buffer_load_dwordx4 v8, s[4:7], s38 offen offset:0 lds
Mv 176,112,72
Rt 20,23,0
Mv 180,116,72
Mv 240,120,72
Rt 22,23,2048
Mv 244,124,72
buffer_load_dwordx4 v9, s[4:7], s38 offen offset:1024 lds
Mv 252,124,76
Rt 24,24,0
Mv 248,120,76
Mv 188,116,76
Rt 26,24,2048
Mv 184,112,76
buffer_load_dwordx4 v10, s[4:7], s38 offen offset:2048 lds
Mv 124,108,76
)" R"(
Rt 28,25,0
Mv 120,104,76
Mv 60,100,76
Rt 30,25,2048
Mv 56,96,76
buffer_load_dwordx4 v11, s[4:7], s38 offen offset:3072 lds
Mv 64,96,80
Rb 32,16,0
Mv 68,100,80
Mv 128,104,80
Rb 36,16,2048
Mv 132,108,80
s_add_u32 s39, s39, 0x40000
Mv 192,112,80
Rb 40,16,8192
s_add_u32 m0, s37, 49152
Mv 196,116,80
buffer_load_dwordx4 v12, s[8:11], s39 offen offset:0 lds
Ma 224,120,80
Rb 44,16,10240
Ma 228,124,80
Ma 236,124,84
Rb 48,16,16384
Ma 232,120,84
Mv 204,116,84
Rb 52,16,18432
Mv 200,112,84
buffer_load_dwordx4 v13, s[8:11], s39 offen offset:1024 lds
Mv 140,108,84
Rb 56,16,24576
Mv 136,104,84
Mv 76,100,84
Rb 60,16,26624
Mv 72,96,84
Mv 80,96,88
Mv 84,100,88
buffer_load_dwordx4 v14, s[8:11], s39 offen offset:2048 lds
Mv 144,104,88
Mv 148,108,88
Mv 208,112,88
Mv 212,116,88
Ma 240,120,88
Ma 244,124,88
buffer_load_dwordx4 v15, s[8:11], s39 offen offset:3072 lds
Ma 252,124,92
Ma 248,120,92
s_add_u32 s88, s88, s90
Mv 220,116,92
s_sub_u32 s40, s40, 1
Mv 216,112,92
buffer_load_dword v6, v5, s[24:27], s88 offen
Mv 156,108,92
Mv 152,104,92
Mv 92,100,92
Mv 88,96,92
s_cmp_lg_u32 s40, 0
s_waitcnt vmcnt(18) lgkmcnt(0)
s_barrier
s_cbranch_scc1 .LzPAV
.LzPAVX:
Mv 32,32,0
Rt 64,18,16384
Mv 36,36,0
Rb 192,28,32768
Mv 96,40,0
)" R"(
Rt 66,18,18432
Mv 100,44,0
s_add_u32 s39, s39, 0x40000
Rb 196,28,34816
Mv 160,48,0
Rt 68,19,16384
s_add_u32 m0, s37, 0
Mv 164,52,0
buffer_load_dwordx4 v12, s[8:11], s39 offen offset:0 lds
Mv 224,56,0
Rt 70,19,18432
Mv 228,60,0
Rb 200,29,32768
Mv 236,60,4
Rt 72,20,16384
Mv 232,56,4
Rb 204,29,34816
Mv 172,52,4
Rt 74,20,18432
Mv 168,48,4
Rb 208,28,36864
Mv 108,44,4
Rt 76,21,16384
Mv 104,40,4
buffer_load_dwordx4 v13, s[8:11], s39 offen offset:1024 lds
Mv 44,36,4
Rt 78,21,18432
Mv 40,32,4
Rb 212,28,38912
Mv 48,32,8
Rt 80,22,16384
Mv 52,36,8
Rb 216,29,36864
Mv 112,40,8
Rt 82,22,18432
Mv 116,44,8
Rb 220,29,38912
Mv 176,48,8
Rt 84,23,16384
Mv 180,52,8
buffer_load_dwordx4 v14, s[8:11], s39 offen offset:2048 lds
Mv 240,56,8
Rt 86,23,18432
Mv 244,60,8
Mv 252,60,12
Rt 88,24,16384
Mv 248,56,12
Mv 188,52,12
Rt 90,24,18432
Mv 184,48,12
Mv 124,44,12
Rt 92,25,16384
Mv 120,40,12
buffer_load_dwordx4 v15, s[8:11], s39 offen offset:3072 lds
Mv 60,36,12
Rt 94,25,18432
Mv 56,32,12
s_add_u32 s44, s43, 256
s_waitcnt lgkmcnt(6)
Mv 64,32,16
Rb 96,17,0
s_add_u32 m0, s36, 131072
Mv 68,36,16
buffer_load_dwordx4 v26, s[12:15], s44 offen offset:0 lds
)" R"(
Mv 128,40,16
Rb 100,17,2048
Mv 132,44,16
Mv 192,48,16
Rb 104,17,8192
Mv 196,52,16
buffer_load_dwordx4 v27, s[12:15], s44 offen offset:1024 lds
Ma 224,56,16
Rb 108,17,10240
Ma 228,60,16
Ma 236,60,20
Rb 112,17,16384
Ma 232,56,20
s_add_u32 s44, s43, 12544
buffer_load_dwordx4 v26, s[12:15], s44 offen offset:2048 lds
Mv 204,52,20
Rb 116,17,18432
Mv 200,48,20
Mv 140,44,20
Rb 120,17,24576
Mv 136,40,20
buffer_load_dwordx4 v27, s[12:15], s44 offen offset:3072 lds
Mv 76,36,20
Rb 124,17,26624
Mv 72,32,20
Mv 80,32,24
s_add_u32 m0, s36, 135168
s_add_u32 s44, s43, 384
Mv 84,36,24
buffer_load_dwordx4 v26, s[12:15], s44 offen offset:0 lds
Mv 144,40,24
Mv 148,44,24
Mv 208,48,24
Mv 212,52,24
buffer_load_dwordx4 v27, s[12:15], s44 offen offset:1024 lds
Ma 240,56,24
Ma 244,60,24
Ma 252,60,28
Ma 248,56,28
s_add_u32 s44, s43, 12672
buffer_load_dwordx4 v26, s[12:15], s44 offen offset:2048 lds
Mv 220,52,28
Mv 216,48,28
Mv 156,44,28
s_mov_b32 s85, 0x1508800
v_and_b32 v2, 7, v0
v_lshlrev_b32 v2, 7, v2
Mv 152,40,28
buffer_load_dwordx4 v27, s[12:15], s44 offen offset:3072 lds
Mv 92,36,28
buffer_load_dword v4, v2, s[20:23], s85 offen sc0 sc1
Mv 88,32,28
s_waitcnt vmcnt(18) lgkmcnt(0)
s_barrier
Mv 32,96,64
Rt 0,18,32768
Mv 36,100,64
s_add_u32 s38, s38, 0xffff0080
Mv 96,104,64
Rt 2,18,34816
s_add_u32 m0, s36, 0
Mv 100,108,64
buffer_load_dwordx4 v8, s[4:7], s38 offen offset:0 lds
Mv 160,112,64
Rt 4,19,32768
)" R"(
Mv 164,116,64
Mv 224,120,64
Rt 6,19,34816
Mv 228,124,64
buffer_load_dwordx4 v9, s[4:7], s38 offen offset:1024 lds
Mv 236,124,68
Rt 8,20,32768
Mv 232,120,68
Mv 172,116,68
Rt 10,20,34816
Mv 168,112,68
buffer_load_dwordx4 v10, s[4:7], s38 offen offset:2048 lds
Mv 108,108,68
Rt 12,21,32768
Mv 104,104,68
Mv 44,100,68
Rt 14,21,34816
Mv 40,96,68
buffer_load_dwordx4 v11, s[4:7], s38 offen offset:3072 lds
Mv 48,96,72
Rt 16,22,32768
Mv 52,100,72
s_add_u32 s38, s38, 0x10000
Mv 112,104,72
Rt 18,22,34816
s_add_u32 m0, s36, 4096
Mv 116,108,72
buffer_load_dwordx4 v8, s[4:7], s38 offen offset:0 lds
Mv 176,112,72
Rt 20,23,32768
Mv 180,116,72
Mv 240,120,72
Rt 22,23,34816
Mv 244,124,72
buffer_load_dwordx4 v9, s[4:7], s38 offen offset:1024 lds
Mv 252,124,76
Rt 24,24,32768
Mv 248,120,76
Mv 188,116,76
Rt 26,24,34816
Mv 184,112,76
buffer_load_dwordx4 v10, s[4:7], s38 offen offset:2048 lds
Mv 124,108,76
Rt 28,25,32768
Mv 120,104,76
Mv 60,100,76
Rt 30,25,34816
Mv 56,96,76
buffer_load_dwordx4 v11, s[4:7], s38 offen offset:3072 lds
Mv 64,96,80
Rb 32,16,32768
Mv 68,100,80
Mv 128,104,80
Rb 36,16,34816
Mv 132,108,80
s_add_u32 s39, s39, 0x40000
Mv 192,112,80
Rb 40,16,40960
s_add_u32 m0, s37, 16384
Mv 196,116,80
buffer_load_dwordx4 v12, s[8:11], s39 offen offset:0 lds
Ma 224,120,80
Rb 44,16,43008
Ma 228,124,80
)" R"(
Ma 236,124,84
Rb 48,16,49152
Ma 232,120,84
Mv 204,116,84
Rb 52,16,51200
Mv 200,112,84
buffer_load_dwordx4 v13, s[8:11], s39 offen offset:1024 lds
Mv 140,108,84
Rb 56,16,57344
Mv 136,104,84
Mv 76,100,84
Rb 60,16,59392
Mv 72,96,84
Mv 80,96,88
Mv 84,100,88
buffer_load_dwordx4 v14, s[8:11], s39 offen offset:2048 lds
Mv 144,104,88
Mv 148,108,88
Mv 208,112,88
Mv 212,116,88
Ma 240,120,88
Ma 244,124,88
buffer_load_dwordx4 v15, s[8:11], s39 offen offset:3072 lds
Ma 252,124,92
Ma 248,120,92
Mv 220,116,92
Mv 216,112,92
Mv 156,108,92
Mv 152,104,92
Mv 92,100,92
Mv 88,96,92
s_waitcnt vmcnt(13) lgkmcnt(0)
s_barrier
Mv 32,32,0
Rt 64,18,49152
Mv 36,36,0
Rb 128,28,32768
Mv 96,40,0
Rt 66,18,51200
Mv 100,44,0
s_add_u32 s39, s39, 0x40000
Rb 132,28,34816
Mv 160,48,0
Rt 68,19,49152
s_add_u32 m0, s37, 32768
Mv 164,52,0
buffer_load_dwordx4 v12, s[8:11], s39 offen offset:0 lds
Mv 224,56,0
Rt 70,19,51200
Mv 228,60,0
Rb 136,29,32768
Mv 236,60,4
Rt 72,20,49152
Mv 232,56,4
Rb 140,29,34816
Mv 172,52,4
Rt 74,20,51200
Mv 168,48,4
Rb 144,28,36864
Mv 108,44,4
Rt 76,21,49152
Mv 104,40,4
buffer_load_dwordx4 v13, s[8:11], s39 offen offset:1024 lds
Mv 44,36,4
Rt 78,21,51200
)" R"(
Mv 40,32,4
Rb 148,28,38912
Mv 48,32,8
Rt 80,22,49152
Mv 52,36,8
Rb 152,29,36864
Mv 112,40,8
Rt 82,22,51200
Mv 116,44,8
Rb 156,29,38912
Mv 176,48,8
Rt 84,23,49152
Mv 180,52,8
buffer_load_dwordx4 v14, s[8:11], s39 offen offset:2048 lds
Mv 240,56,8
Rt 86,23,51200
Mv 244,60,8
Mv 252,60,12
Rt 88,24,49152
Mv 248,56,12
Mv 188,52,12
Rt 90,24,51200
Mv 184,48,12
Mv 124,44,12
Rt 92,25,49152
Mv 120,40,12
buffer_load_dwordx4 v15, s[8:11], s39 offen offset:3072 lds
Mv 60,36,12
Rt 94,25,51200
Mv 56,32,12
s_add_u32 s44, s43, 512
s_waitcnt lgkmcnt(6)
Mv 64,32,16
Rb 96,17,32768
s_add_u32 m0, s36, 131072
Mv 68,36,16
buffer_load_dwordx4 v26, s[12:15], s44 offen offset:0 lds
Mv 128,40,16
Rb 100,17,34816
Mv 132,44,16
Mv 192,48,16
Rb 104,17,40960
Mv 196,52,16
buffer_load_dwordx4 v27, s[12:15], s44 offen offset:1024 lds
Ma 224,56,16
Rb 108,17,43008
Ma 228,60,16
Ma 236,60,20
Rb 112,17,49152
Ma 232,56,20
s_add_u32 s44, s43, 12800
buffer_load_dwordx4 v26, s[12:15], s44 offen offset:2048 lds
Mv 204,52,20
Rb 116,17,51200
Mv 200,48,20
Mv 140,44,20
Rb 120,17,57344
Mv 136,40,20
buffer_load_dwordx4 v27, s[12:15], s44 offen offset:3072 lds
Mv 76,36,20
Rb 124,17,59392
Mv 72,32,20
Mv 80,32,24
s_add_u32 m0, s36, 135168
)" R"(
s_add_u32 s44, s43, 640
Mv 84,36,24
buffer_load_dwordx4 v26, s[12:15], s44 offen offset:0 lds
Mv 144,40,24
Mv 148,44,24
Mv 208,48,24
Mv 212,52,24
buffer_load_dwordx4 v27, s[12:15], s44 offen offset:1024 lds
Ma 240,56,24
Ma 244,60,24
Ma 252,60,28
Ma 248,56,28
s_add_u32 s44, s43, 12928
buffer_load_dwordx4 v26, s[12:15], s44 offen offset:2048 lds
Mv 220,52,28
Mv 216,48,28
Mv 156,44,28
Mv 152,40,28
buffer_load_dwordx4 v27, s[12:15], s44 offen offset:3072 lds
.LzH30:
s_cbranch_execz .LzH36
Mv 92,36,28
Mv 88,32,28
s_waitcnt vmcnt(16) lgkmcnt(0)
s_barrier
Mv 32,96,64
Rt 0,18,0
v_xor_b32 v4, s3, v4
v_cmp_gt_u32_e32 vcc, 2, v4
s_andn2_b32 s28, 0xff, vcc_lo
s_cbranch_scc1 .Lzsp3
.Lzok3:
Mv 36,100,64
s_add_u32 s38, s38, 0xffff0080
v_xor_b32 v4, s3, v4
Mv 96,104,64
Rt 2,18,2048
s_add_u32 m0, s36, 32768
Mv 100,108,64
buffer_load_dwordx4 v8, s[4:7], s38 offen offset:0 lds
Mv 160,112,64
Rt 4,19,0
v_and_b32 v4, 1, v4
Mv 164,116,64
v_cmp_eq_u32_e32 vcc, 1, v4
s_mov_b32 s85, vcc_lo
Mv 224,120,64
Rt 6,19,2048
s_and_b32 s28, s2, 7
Mv 228,124,64
buffer_load_dwordx4 v9, s[4:7], s38 offen offset:1024 lds
Mv 236,124,68
Rt 8,20,0
s_lshr_b32 s28, s85, s28
Mv 232,120,68
s_xor_b32 s28, s28, s34
s_and_b32 s28, s28, 1
Mv 172,116,68
Rt 10,20,2048
s_lshl_b32 s84, s28, 13
Mv 168,112,68
buffer_load_dwordx4 v10, s[4:7], s38 offen offset:2048 lds
Mv 108,108,68
Rt 12,21,0
s_add_u32 s49, s49, s84
)" R"(
Mv 104,104,68
s_add_u32 s50, s50, s84
s_add_u32 s60, s50, 7168
Mv 44,100,68
Rt 14,21,2048
s_add_u32 s52, s52, s84
Mv 40,96,68
buffer_load_dwordx4 v11, s[4:7], s38 offen offset:3072 lds
Mv 48,96,72
Rt 16,22,0
s_add_u32 s65, s65, s84
Mv 52,100,72
s_add_u32 s38, s38, 0x10000
s_lshr_b32 s28, s2, 6
Mv 112,104,72
Rt 18,22,2048
s_add_u32 m0, s36, 36864
Mv 116,108,72
buffer_load_dwordx4 v8, s[4:7], s38 offen offset:0 lds
Mv 176,112,72
Rt 20,23,0
s_lshl_b32 s28, s28, 14
Mv 180,116,72
s_add_u32 s28, s28, s50
s_bfe_u32 s29, s2, 0x20004
Mv 240,120,72
Rt 22,23,2048
s_lshl2_add_u32 s29, s29, s33
Mv 244,124,72
buffer_load_dwordx4 v9, s[4:7], s38 offen offset:1024 lds
Mv 252,124,76
Rt 24,24,0
s_lshl_b32 s29, s29, 9
Mv 248,120,76
s_add_u32 s28, s28, s29
v_min_u32 v4, 3, v0
Mv 188,116,76
Rt 26,24,2048
v_lshlrev_b32 v4, 7, v4
Mv 184,112,76
buffer_load_dwordx4 v10, s[4:7], s38 offen offset:2048 lds
Mv 124,108,76
Rt 28,25,0
Mv 120,104,76
buffer_load_dword v4, v4, s[20:23], s28 offen
Mv 60,100,76
Rt 30,25,2048
s_lshl1_add_u32 s28, s36, s84
Mv 56,96,76
buffer_load_dwordx4 v11, s[4:7], s38 offen offset:3072 lds
Mv 64,96,80
Rb 32,16,0
v_add_u32 v2, s28, v1
Mv 68,100,80
v_add_u32 v3, 0x1000, v2
s_lshl_b32 s28, s34, 13
Mv 128,104,80
Rb 36,16,2048
s_xor_b32 s28, s28, s84
Mv 132,108,80
s_add_u32 s39, s39, 0x40000
s_and_b32 s28, s28, 0x2000
Mv 192,112,80
Rb 40,16,8192
)" R"(
s_add_u32 m0, s37, 49152
Mv 196,116,80
buffer_load_dwordx4 v12, s[8:11], s39 offen offset:0 lds
Ma 224,120,80
Rb 44,16,10240
s_xor_b32 s73, s73, s28
Ma 228,124,80
Ma 236,124,84
Rb 48,16,16384
Ma 232,120,84
Mv 204,116,84
Rb 52,16,18432
Mv 200,112,84
buffer_load_dwordx4 v13, s[8:11], s39 offen offset:1024 lds
Mv 140,108,84
Rb 56,16,24576
Mv 136,104,84
Mv 76,100,84
Rb 60,16,26624
Mv 72,96,84
Mv 80,96,88
Mv 84,100,88
buffer_load_dwordx4 v14, s[8:11], s39 offen offset:2048 lds
Mv 144,104,88
Mv 148,108,88
Mv 208,112,88
Mv 212,116,88
Ma 240,120,88
Ma 244,124,88
buffer_load_dwordx4 v15, s[8:11], s39 offen offset:3072 lds
Ma 252,124,92
Ma 248,120,92
Mv 220,116,92
Mv 216,112,92
Mv 156,108,92
Mv 152,104,92
Mv 92,100,92
Mv 88,96,92
s_waitcnt vmcnt(5) lgkmcnt(0)
s_barrier
Mv 32,32,0
Rt 64,18,16384
Mv 36,36,0
Rb 160,28,32768
v_and_b32 v5, 7, v0
Mv 96,40,0
Rt 66,18,18432
v_lshlrev_b32 v5, 7, v5
Mv 100,44,0
Rb 164,28,34816
v_mov_b32 v6, s3
Mv 160,48,0
Rt 68,19,16384
Mv 164,52,0
Mv 224,56,0
Rt 70,19,18432
Mv 228,60,0
Rb 168,29,32768
Mv 236,60,4
Rt 72,20,16384
Mv 232,56,4
Rb 172,29,34816
Mv 172,52,4
Rt 74,20,18432
Mv 168,48,4
)" R"(
Rb 176,28,36864
Mv 108,44,4
Rt 76,21,16384
Mv 104,40,4
Mv 44,36,4
Rt 78,21,18432
Mv 40,32,4
Rb 180,28,38912
Mv 48,32,8
Rt 80,22,16384
Mv 52,36,8
Rb 184,29,36864
Mv 112,40,8
Rt 82,22,18432
Mv 116,44,8
Rb 188,29,38912
Mv 176,48,8
Rt 84,23,16384
Mv 180,52,8
Mv 240,56,8
Rt 86,23,18432
Mv 244,60,8
Mv 252,60,12
Rt 88,24,16384
Mv 248,56,12
Mv 188,52,12
Rt 90,24,18432
Mv 184,48,12
Mv 124,44,12
Rt 92,25,16384
Mv 120,40,12
Mv 60,36,12
Rt 94,25,18432
v_lshrrev_b32 v10, 3, v0
Mv 56,32,12
v_and_b32 v11, 0x70, v1
v_lshl_or_b32 v10, v10, 13, v11
Mv 64,32,16
Rb 96,17,0
v_lshlrev_b32 v11, 1, v0
Mv 68,36,16
v_and_b32 v11, 32, v11
v_xor_b32 v10, v10, v11
buffer_load_dword v4, v7, s[20:23], s52 offen offset:0 sc0 sc1
Mv 128,40,16
Rb 100,17,2048
s_and_b32 s28, s33, 1
Mv 132,44,16
s_lshl_b32 s28, s28, 6
v_xor_b32 v8, s28, v10
Mv 192,48,16
Rb 104,17,8192
v_xor_b32 v9, 64, v8
Mv 196,52,16
v_add_u32 v9, 0xfc00, v9
v_add_u32 v10, 0x1f800, v8
Ma 224,56,16
Rb 108,17,10240
v_add_u32 v11, 0x1f800, v9
Ma 228,60,16
Ma 236,60,20
Rb 112,17,16384
Ma 232,56,20
Mv 204,52,20
)" R"(
Rb 116,17,18432
Mv 200,48,20
Mv 140,44,20
Rb 120,17,24576
Mv 136,40,20
Mv 76,36,20
Rb 124,17,26624
Mv 72,32,20
Mv 80,32,24
Mv 84,36,24
Mv 144,40,24
Mv 148,44,24
Mv 208,48,24
Mv 212,52,24
Ma 240,56,24
Ma 244,60,24
Ma 252,60,28
Ma 248,56,28
Mv 220,52,28
Mv 216,48,28
Mv 156,44,28
Mv 152,40,28
Mv 92,36,28
Mv 88,32,28
s_waitcnt vmcnt(5) lgkmcnt(0)
s_barrier
Mv 32,96,64
Rt 0,18,32768
Mv 36,100,64
s_add_u32 s92, s42, 0x0
Mv 96,104,64
Rt 2,18,34816
s_add_u32 m0, s41, 131072
Mv 100,108,64
buffer_load_dwordx4 v8, s[16:19], s92 offen offset:0 lds
Mv 160,112,64
Rt 4,19,32768
Mv 164,116,64
Mv 224,120,64
Rt 6,19,34816
Mv 228,124,64
buffer_load_dwordx4 v9, s[16:19], s92 offen offset:1024 lds
Mv 236,124,68
Rt 8,20,32768
Mv 232,120,68
Mv 172,116,68
Rt 10,20,34816
Mv 168,112,68
buffer_load_dwordx4 v10, s[16:19], s92 offen offset:2048 lds
Mv 108,108,68
Rt 12,21,32768
Mv 104,104,68
Mv 44,100,68
Rt 14,21,34816
Mv 40,96,68
buffer_load_dwordx4 v11, s[16:19], s92 offen offset:3072 lds
Mv 48,96,72
Rt 16,22,32768
Mv 52,100,72
s_add_u32 s92, s42, 0x40000
Mv 112,104,72
Rt 18,22,34816
s_add_u32 m0, s41, 135168
Mv 116,108,72
buffer_load_dwordx4 v8, s[16:19], s92 offen offset:0 lds
)" R"(
Mv 176,112,72
Rt 20,23,32768
Mv 180,116,72
Mv 240,120,72
Rt 22,23,34816
Mv 244,124,72
buffer_load_dwordx4 v9, s[16:19], s92 offen offset:1024 lds
Mv 252,124,76
Rt 24,24,32768
Mv 248,120,76
Mv 188,116,76
Rt 26,24,34816
Mv 184,112,76
buffer_load_dwordx4 v10, s[16:19], s92 offen offset:2048 lds
Mv 124,108,76
Rt 28,25,32768
Mv 120,104,76
s_add_u32 s92, s42, 0x1c0000
Mv 60,100,76
Rt 30,25,34816
s_add_u32 m0, s41, 0
Mv 56,96,76
buffer_load_dwordx4 v8, s[16:19], s92 offen offset:0 lds
Mv 64,96,80
Rb 32,16,32768
Mv 68,100,80
Mv 128,104,80
Rb 36,16,34816
Mv 132,108,80
buffer_load_dwordx4 v9, s[16:19], s92 offen offset:1024 lds
Mv 192,112,80
Rb 40,16,40960
Mv 196,116,80
Ma 224,120,80
Rb 44,16,43008
Ma 228,124,80
buffer_load_dwordx4 v10, s[16:19], s92 offen offset:2048 lds
Ma 236,124,84
Rb 48,16,49152
Ma 232,120,84
Mv 204,116,84
Rb 52,16,51200
Mv 200,112,84
buffer_load_dwordx4 v11, s[16:19], s92 offen offset:3072 lds
Mv 140,108,84
Rb 56,16,57344
Mv 136,104,84
s_add_u32 s92, s42, 0x200000
Mv 76,100,84
Rb 60,16,59392
s_add_u32 m0, s41, 4096
Mv 72,96,84
buffer_load_dwordx4 v8, s[16:19], s92 offen offset:0 lds
Mv 80,96,88
Mv 84,100,88
Mv 144,104,88
Mv 148,108,88
buffer_load_dwordx4 v9, s[16:19], s92 offen offset:1024 lds
Mv 208,112,88
Mv 212,116,88
Ma 240,120,88
Ma 244,124,88
buffer_load_dwordx4 v10, s[16:19], s92 offen offset:2048 lds
Ma 252,124,92
)" R"(
Ma 248,120,92
Mv 220,116,92
Mv 216,112,92
Mv 156,108,92
Mv 152,104,92
Mv 92,100,92
Mv 88,96,92
s_waitcnt vmcnt(14) lgkmcnt(4)
s_barrier
v_cmp_eq_u32_e32 vcc, s3, v4
s_andn2_b64 s[28:29], 1, vcc
s_cbranch_scc1 .Lzsp4
.Lzok4:
Mv 32,32,0
Rt 64,18,49152
Mv 36,36,0
Rt 66,18,51200
s_add_u32 s92, s42, 0x80
Mv 96,40,0
Rt 68,19,49152
s_add_u32 m0, s41, 65536
Mv 100,44,0
Rt 70,19,51200
buffer_load_dwordx4 v8, s[16:19], s92 offen offset:0 lds
Mv 108,44,4
Rt 72,20,49152
Mv 104,40,4
Rt 74,20,51200
Mv 44,36,4
Rt 76,21,49152
Mv 40,32,4
Rt 78,21,51200
buffer_load_dwordx4 v9, s[16:19], s92 offen offset:1024 lds
Mv 48,32,8
Rt 80,22,49152
Mv 52,36,8
Rt 82,22,51200
Mv 112,40,8
Rt 84,23,49152
Mv 116,44,8
Rt 86,23,51200
buffer_load_dwordx4 v10, s[16:19], s92 offen offset:2048 lds
Mv 124,44,12
Rt 88,24,49152
Mv 120,40,12
Rt 90,24,51200
Mv 60,36,12
Rt 92,25,49152
Mv 56,32,12
Rt 94,25,51200
buffer_load_dwordx4 v11, s[16:19], s92 offen offset:3072 lds
Mv 64,32,16
Rb 96,17,32768
Mv 68,36,16
Rb 100,17,34816
s_add_u32 s92, s42, 0x40080
Mv 128,40,16
Rb 104,17,40960
s_add_u32 m0, s41, 69632
Mv 132,44,16
Rb 108,17,43008
buffer_load_dwordx4 v8, s[16:19], s92 offen offset:0 lds
Mv 140,44,20
Rb 112,17,49152
Mv 136,40,20
)" R"(
Rb 116,17,51200
Mv 76,36,20
Rb 120,17,57344
Mv 72,32,20
Rb 124,17,59392
buffer_load_dwordx4 v9, s[16:19], s92 offen offset:1024 lds
Mv 80,32,24
Mv 84,36,24
Mv 144,40,24
Mv 148,44,24
buffer_load_dwordx4 v10, s[16:19], s92 offen offset:2048 lds
Mv 156,44,28
Mv 152,40,28
Mv 92,36,28
Mv 88,32,28
s_waitcnt lgkmcnt(0)
s_barrier
Mv 32,96,64
v_bfe_u32 v12, v0, 3, 2
v_lshlrev_b32 v12, 5, v12
Mv 36,100,64
v_and_b32 v4, 3, v0
v_lshl_or_b32 v12, v4, 3, v12
Mv 40,96,68
s_add_u32 m0, s36, 98304
s_add_u32 s44, s43, 768
Mv 44,100,68
buffer_load_dwordx4 v26, s[12:15], s44 offen offset:0 lds
Mv 48,96,72
v_bfe_u32 v4, v0, 2, 2
v_lshl_or_b32 v12, v4, 7, v12
Mv 52,100,72
v_lshrrev_b32 v4, 4, v0
v_lshl_or_b32 v12, v4, 10, v12
Mv 56,96,76
v_xor_b32 v13, 32, v12
v_xor_b32 v14, 64, v12
Mv 60,100,76
buffer_load_dwordx4 v27, s[12:15], s44 offen offset:1024 lds
Mv 64,96,80
v_xor_b32 v15, 96, v12
v_add_u32 v20, 0x20000, v12
Mv 68,100,80
v_add_u32 v21, 0x20000, v13
v_add_u32 v22, 0x20000, v14
Mv 72,96,84
v_add_u32 v23, 0x20000, v15
v_add_u32 v16, 0x10000, v12
Mv 76,100,84
s_add_u32 s44, s43, 13056
buffer_load_dwordx4 v26, s[12:15], s44 offen offset:2048 lds
Mv 80,96,88
v_add_u32 v17, 0x10000, v13
v_add_u32 v18, 0x10000, v14
Mv 84,100,88
v_add_u32 v19, 0x10000, v15
Cv 32,32,33
Mv 88,96,92
Cv 33,34,35
Cv 34,36,37
Mv 92,100,92
buffer_load_dwordx4 v27, s[12:15], s44 offen offset:3072 lds
Mv 96,104,64
Cv 35,38,39
Cv 40,40,41
)" R"(
Mv 100,108,64
Cv 41,42,43
Cv 42,44,45
Mv 104,104,68
s_add_u32 m0, s41, 32768
s_add_u32 s92, s42, 0x1c0080
Mv 108,108,68
buffer_load_dwordx4 v8, s[16:19], s92 offen offset:0 lds
Mv 112,104,72
Cv 43,46,47
Cv 48,48,49
Mv 116,108,72
Cv 49,50,51
Cv 50,52,53
Mv 120,104,76
Cv 51,54,55
Cv 56,56,57
Mv 124,108,76
buffer_store_dwordx4 v[32:35], v2, s[20:23], s45 offen offset:0 sc0 sc1
Mv 128,104,80
Cv 57,58,59
Cv 58,60,61
Mv 132,108,80
Cv 59,62,63
Cv 64,64,65
Mv 136,104,84
Cv 65,66,67
Cv 66,68,69
Mv 140,108,84
buffer_load_dwordx4 v9, s[16:19], s92 offen offset:1024 lds
Mv 144,104,88
Cv 67,70,71
Cv 72,72,73
Mv 148,108,88
Cv 73,74,75
Cv 74,76,77
Mv 152,104,92
Cv 75,78,79
Cv 80,80,81
Mv 156,108,92
buffer_store_dwordx4 v[40:43], v2, s[20:23], s45 offen offset:1024 sc0 sc1
Mv 160,48,0
Cv 81,82,83
Cv 82,84,85
Mv 160,112,64
Cv 83,86,87
Cv 88,88,89
Mv 164,52,0
Cv 89,90,91
Cv 90,92,93
.LzH31:
s_cbranch_execz .LzH37
Mv 164,116,64
buffer_load_dwordx4 v10, s[16:19], s92 offen offset:2048 lds
Mv 168,48,4
Cv 91,94,95
Cv 96,96,97
Mv 168,112,68
Cv 97,98,99
Cv 98,100,101
Mv 172,52,4
Cv 99,102,103
Cv 104,104,105
Mv 172,116,68
)" R"(
buffer_store_dwordx4 v[48:51], v2, s[20:23], s45 offen offset:2048 sc0 sc1
Mv 176,48,8
Cv 105,106,107
Cv 106,108,109
Mv 176,112,72
Cv 107,110,111
Cv 112,112,113
Mv 180,52,8
Cv 113,114,115
Cv 114,116,117
Mv 180,116,72
buffer_load_dwordx4 v11, s[16:19], s92 offen offset:3072 lds
Mv 184,48,12
Cv 115,118,119
Cv 120,120,121
Mv 184,112,76
Cv 121,122,123
Cv 122,124,125
Mv 188,52,12
Cv 123,126,127
Cv 128,128,129
Mv 188,116,76
buffer_store_dwordx4 v[56:59], v2, s[20:23], s45 offen offset:3072 sc0 sc1
Mv 192,48,16
Cv 129,130,131
Cv 130,132,133
Mv 192,112,80
Cv 131,134,135
Cv 136,136,137
Mv 196,52,16
s_add_u32 m0, s41, 36864
s_add_u32 s92, s42, 0x200080
Mv 196,116,80
buffer_load_dwordx4 v8, s[16:19], s92 offen offset:0 lds
Mv 200,48,20
Cv 137,138,139
Cv 138,140,141
Mv 200,112,84
Cv 139,142,143
Cv 144,144,145
Mv 204,52,20
Cv 145,146,147
Cv 146,148,149
Mv 204,116,84
buffer_store_dwordx4 v[64:67], v3, s[20:23], s45 offen offset:0 sc0 sc1
Mv 208,48,24
Cv 147,150,151
Cv 152,152,153
Mv 208,112,88
Cv 153,154,155
Cv 154,156,157
Mv 212,52,24
Cv 155,158,159
Mv 212,116,88
buffer_load_dwordx4 v9, s[16:19], s92 offen offset:1024 lds
Mv 216,48,28
Mv 216,112,92
Mv 220,52,28
Mv 220,116,92
buffer_store_dwordx4 v[72:75], v3, s[20:23], s45 offen offset:1024 sc0 sc1
Mv 224,56,0
Mv 228,60,0
Mv 236,60,4
Mv 232,56,4
)" R"(
buffer_load_dwordx4 v10, s[16:19], s92 offen offset:2048 lds
Mv 240,56,8
Mv 244,60,8
Mv 252,60,12
Mv 248,56,12
s_waitcnt vmcnt(24)
s_barrier
Ma 224,56,16
buffer_store_dwordx4 v[80:83], v3, s[20:23], s45 offen offset:2048 sc0 sc1
Ma 228,60,16
Ma 236,60,20
Ma 232,56,20
Ma 240,56,24
Ma 244,60,24
Ma 252,60,28
Ma 248,56,28
buffer_store_dwordx4 v[88:91], v3, s[20:23], s45 offen offset:3072 sc0 sc1
Mv 224,120,64
Rt 16,20,0
Mv 228,124,64
Rt 18,20,512
Mv 232,120,68
Rt 20,21,0
Mv 236,124,68
Rt 22,21,512
Mv 240,120,72
Rt 24,22,0
Mv 244,124,72
Rt 26,22,512
Mv 248,120,76
Rt 28,23,0
Mv 252,124,76
Rt 30,23,512
Ma 224,120,80
Rt 32,20,4096
Ma 228,124,80
Rt 34,20,4608
Ma 232,120,84
Rt 36,21,4096
Ma 236,124,84
Rt 38,21,4608
Ma 240,120,88
Rt 40,22,4096
Ma 244,124,88
Rt 42,22,4608
Ma 248,120,92
Rt 44,23,4096
Ma 252,124,92
Rt 46,23,4608
s_waitcnt vmcnt(15) lgkmcnt(8)
.LzH32:
s_cbranch_execz .LzH40
.LzQA:
s_cmp_eq_u32 s2, 112
s_cbranch_scc1 .LzT2S
.LzT2E:
Mz 32,16,192
Rt 0,20,8192
Cv 224,224,225
Mz 40,16,196
Rt 2,20,8704
Rb 48,28,0
Mz 44,20,196
Rt 4,21,8192
buffer_store_dwordx4 v[96:99], v2, s[20:23], s46 offen offset:0 sc0 sc1
)" R"(
Mz 36,20,192
Rt 6,21,8704
Rb 52,28,2048
Mz 48,24,192
Rt 8,22,8192
Cv 225,226,227
Mz 56,24,196
Rt 10,22,8704
Rb 56,29,0
buffer_load_dwordx4 a[64:67], v2, s[20:23], s51 offen offset:0 sc0 sc1
Mz 60,28,196
Rt 12,23,8192
Cv 226,228,229
Mz 52,28,192
Rt 14,23,8704
Rb 60,29,2048
s_waitcnt lgkmcnt(12)
Mv 32,32,200
Rt 16,20,12288
Cv 227,230,231
Mv 40,32,204
Rt 18,20,12800
Cv 232,232,233
Mv 44,36,204
Rt 20,21,12288
buffer_load_dwordx4 a[68:71], v2, s[20:23], s51 offen offset:1024 sc0 sc1
Mv 36,36,200
Rt 22,21,12800
Cv 233,234,235
Mv 48,40,200
Rt 24,22,12288
buffer_store_dwordx4 v[104:107], v2, s[20:23], s46 offen offset:1024 sc0 sc1
Mv 56,40,204
Rt 26,22,12800
Cv 234,236,237
Mv 60,44,204
Rt 28,23,12288
Cv 235,238,239
Mv 52,44,200
Rt 30,23,12800
buffer_load_dwordx4 a[72:75], v2, s[20:23], s51 offen offset:2048 sc0 sc1
s_waitcnt lgkmcnt(9)
Mv 32,0,208
Rt 32,20,16384
Cv 240,240,241
Mv 40,0,212
Rt 34,20,16896
Cv 241,242,243
Mv 44,4,212
Rt 36,21,16384
Cv 242,244,245
Mv 36,4,208
Rt 38,21,16896
Cv 243,246,247
Mv 48,8,208
Rt 40,22,16384
buffer_load_dwordx4 a[76:79], v2, s[20:23], s51 offen offset:3072 sc0 sc1
Mv 56,8,212
Rt 42,22,16896
Cv 248,248,249
Cv 249,250,251
Mv 60,12,212
Rt 44,23,16384
buffer_store_dwordx4 v[112:115], v2, s[20:23], s46 offen offset:2048 sc0 sc1
)" R"(
Mv 52,12,208
Rt 46,23,16896
s_waitcnt lgkmcnt(8)
Mv 32,16,216
Rt 0,20,20480
Cv 250,252,253
Mv 40,16,220
Rt 2,20,20992
buffer_load_dwordx4 a[80:83], v3, s[20:23], s51 offen offset:0 sc0 sc1
Mv 44,20,220
Rt 4,21,20480
Cv 251,254,255
v_accvgpr_read_b32 v228, a224
Mv 36,20,216
Rt 6,21,20992
v_accvgpr_read_b32 v24, a225
Mv 48,24,216
Rt 8,22,20480
Cv 228,228,24
Mv 56,24,220
Rt 10,22,20992
v_accvgpr_read_b32 v229, a226
Mv 60,28,220
Rt 12,23,20480
buffer_load_dwordx4 a[84:87], v3, s[20:23], s51 offen offset:1024 sc0 sc1
Mv 52,28,216
Rt 14,23,20992
v_accvgpr_read_b32 v25, a227
s_waitcnt lgkmcnt(8)
Mv 32,32,128
Rt 16,20,24576
buffer_store_dwordx4 v[120:123], v2, s[20:23], s46 offen offset:3072 sc0 sc1
Mv 40,32,132
Rt 18,20,25088
Cv 229,229,25
Mv 44,36,132
Rt 20,21,24576
v_accvgpr_read_b32 v230, a228
Mv 36,36,128
Rt 22,21,25088
buffer_load_dwordx4 a[88:91], v3, s[20:23], s51 offen offset:2048 sc0 sc1
Mv 48,40,128
Rt 24,22,24576
v_accvgpr_read_b32 v24, a229
Mv 56,40,132
Rt 26,22,25088
Cv 230,230,24
v_accvgpr_read_b32 v231, a230
Mv 60,44,132
Rt 28,23,24576
v_accvgpr_read_b32 v25, a231
Mv 52,44,128
Rt 30,23,25088
s_waitcnt lgkmcnt(8)
Mv 32,0,136
Rt 32,12,0
buffer_load_dwordx4 a[92:95], v3, s[20:23], s51 offen offset:3072 sc0 sc1
Mv 40,0,140
Rt 34,12,512
Cv 231,231,25
Mv 44,4,140
Rt 36,13,0
buffer_store_dwordx4 v[128:131], v3, s[20:23], s46 offen offset:0 sc0 sc1
Mv 36,4,136
)" R"(
Rt 38,13,512
v_accvgpr_read_b32 v236, a232
Mv 48,8,136
Rt 40,14,0
v_accvgpr_read_b32 v24, a233
Mv 56,8,140
Rt 42,14,512
Cv 236,236,24
Mv 60,12,140
Rt 44,15,0
v_accvgpr_read_b32 v237, a234
Mv 52,12,136
Rt 46,15,512
s_waitcnt vmcnt(13) lgkmcnt(8)
s_barrier
buffer_store_dword v6, v7, s[20:23], s49 offen offset:0 sc0 sc1
Mv 32,16,144
Rt 0,12,4096
v_accvgpr_read_b32 v25, a235
Mv 40,16,148
Rt 2,12,4608
s_add_u32 s92, s42, 0x100
Mv 44,20,148
Rt 4,13,4096
s_add_u32 m0, s41, 98304
Mv 36,20,144
Rt 6,13,4608
buffer_load_dwordx4 v8, s[16:19], s92 offen offset:0 lds
Mv 48,24,144
Rt 8,14,4096
Cv 237,237,25
Mv 56,24,148
Rt 10,14,4608
v_accvgpr_read_b32 v238, a236
v_accvgpr_read_b32 v24, a237
Mv 60,28,148
Rt 12,15,4096
Cv 238,238,24
Mv 52,28,144
Rt 14,15,4608
buffer_load_dwordx4 v9, s[16:19], s92 offen offset:1024 lds
s_waitcnt lgkmcnt(8)
Mv 32,32,152
Rt 16,12,8192
v_accvgpr_read_b32 v239, a238
Mv 40,32,156
Rt 18,12,8704
buffer_store_dwordx4 v[136:139], v3, s[20:23], s46 offen offset:1024 sc0 sc1
Mv 44,36,156
Rt 20,13,8192
v_accvgpr_read_b32 v25, a239
Cv 239,239,25
Mv 36,36,152
Rt 22,13,8704
buffer_load_dwordx4 v10, s[16:19], s92 offen offset:2048 lds
Mv 48,40,152
Rt 24,14,8192
v_accvgpr_read_b32 v244, a240
Mv 56,40,156
Rt 26,14,8704
v_accvgpr_read_b32 v24, a241
Mv 60,44,156
Rt 28,15,8192
Cv 244,244,24
Mv 52,44,152
)" R"(
Rt 30,15,8704
buffer_load_dwordx4 v11, s[16:19], s92 offen offset:3072 lds
s_waitcnt lgkmcnt(8)
Mv 32,0,160
Rt 32,12,12288
v_accvgpr_read_b32 v245, a242
Mv 40,0,164
Rt 34,12,12800
s_add_u32 s92, s42, 0x40100
Mv 44,4,164
Rt 36,13,12288
s_add_u32 m0, s41, 102400
Mv 36,4,160
Rt 38,13,12800
buffer_load_dwordx4 v8, s[16:19], s92 offen offset:0 lds
Mv 48,8,160
Rt 40,14,12288
v_accvgpr_read_b32 v25, a243
Mv 56,8,164
Rt 42,14,12800
Cv 245,245,25
v_accvgpr_read_b32 v246, a244
Mv 60,12,164
Rt 44,15,12288
v_accvgpr_read_b32 v24, a245
Mv 52,12,160
Rt 46,15,12800
buffer_load_dwordx4 v9, s[16:19], s92 offen offset:1024 lds
s_waitcnt lgkmcnt(8)
Mv 32,16,168
Rt 0,12,16384
Cv 246,246,24
Mv 40,16,172
Rt 2,12,16896
buffer_store_dwordx4 v[144:147], v3, s[20:23], s46 offen offset:2048 sc0 sc1
Mv 44,20,172
Rt 4,13,16384
v_accvgpr_read_b32 v247, a246
v_accvgpr_read_b32 v25, a247
Mv 36,20,168
Rt 6,13,16896
buffer_load_dwordx4 v10, s[16:19], s92 offen offset:2048 lds
Mv 48,24,168
Rt 8,14,16384
Cv 247,247,25
Mv 56,24,172
Rt 10,14,16896
s_add_u32 s92, s42, 0x1c0100
Mv 60,28,172
Rt 12,15,16384
s_add_u32 m0, s41, 131072
Mv 52,28,168
Rt 14,15,16896
buffer_load_dwordx4 v8, s[16:19], s92 offen offset:0 lds
s_waitcnt lgkmcnt(8)
Mv 32,32,176
Rt 16,12,20480
v_accvgpr_read_b32 v252, a248
Mv 40,32,180
Rt 18,12,20992
v_accvgpr_read_b32 v24, a249
Mv 44,36,180
Rt 20,13,20480
Cv 252,252,24
)" R"(
Mv 36,36,176
Rt 22,13,20992
buffer_load_dwordx4 v9, s[16:19], s92 offen offset:1024 lds
Mv 48,40,176
Rt 24,14,20480
v_accvgpr_read_b32 v253, a250
Mv 56,40,180
Rt 26,14,20992
v_accvgpr_read_b32 v25, a251
Cv 253,253,25
Mv 60,44,180
Rt 28,15,20480
v_accvgpr_read_b32 v254, a252
Mv 52,44,176
Rt 30,15,20992
buffer_load_dwordx4 v10, s[16:19], s92 offen offset:2048 lds
s_waitcnt lgkmcnt(8)
Mv 32,0,184
Rt 32,12,24576
v_accvgpr_read_b32 v24, a253
Mv 40,0,188
Rt 34,12,25088
buffer_store_dwordx4 v[152:155], v3, s[20:23], s46 offen offset:3072 sc0 sc1
Mv 44,4,188
Rt 36,13,24576
Cv 254,254,24
v_accvgpr_read_b32 v255, a254
Mv 36,4,184
Rt 38,13,25088
buffer_load_dwordx4 v11, s[16:19], s92 offen offset:3072 lds
Mv 48,8,184
Rt 40,14,24576
v_accvgpr_read_b32 v25, a255
Mv 56,8,188
Rt 42,14,25088
s_add_u32 s92, s42, 0x200100
Mv 60,12,188
Rt 44,15,24576
s_add_u32 m0, s41, 135168
Mv 52,12,184
Rt 46,15,25088
buffer_load_dwordx4 v8, s[16:19], s92 offen offset:0 lds
s_waitcnt lgkmcnt(8)
Mv 32,16,48
Rt 0,16,0
Cv 255,255,25
Mv 40,16,52
Rt 2,16,512
Cv 160,160,161
Mv 44,20,52
Rt 4,17,0
Cv 161,162,163
Mv 36,20,48
Rt 6,17,512
buffer_load_dwordx4 v9, s[16:19], s92 offen offset:1024 lds
Mv 48,24,48
Rt 8,18,0
Cv 162,164,165
Mv 56,24,52
Rt 10,18,512
Cv 163,166,167
Cv 168,168,169
Mv 60,28,52
Rt 12,19,0
)" R"(
Cv 169,170,171
Mv 52,28,48
Rt 14,19,512
buffer_load_dwordx4 v10, s[16:19], s92 offen offset:2048 lds
s_waitcnt lgkmcnt(8)
s_barrier
Mv 32,32,56
Rt 16,16,4096
Cv 170,172,173
Mv 40,32,60
Rt 18,16,4608
buffer_store_dwordx4 v[160:163], v2, s[20:23], s47 offen offset:0 sc0 sc1
Mv 44,36,60
Rt 20,17,4096
Cv 171,174,175
Cv 176,176,177
Mv 36,36,56
Rt 22,17,4608
s_add_u32 s92, s42, 0x180
Mv 48,40,56
Rt 24,18,4096
s_add_u32 m0, s41, 0
Mv 56,40,60
Rt 26,18,4608
buffer_load_dwordx4 v8, s[16:19], s92 offen offset:0 lds
Mv 60,44,60
Rt 28,19,4096
Cv 177,178,179
Mv 52,44,56
Rt 30,19,4608
Cv 178,180,181
s_waitcnt lgkmcnt(8)
Mz 64,0,192
Rt 32,16,8192
Cv 179,182,183
Mz 72,0,196
Rt 34,16,8704
s_add_u32 s51, s51, 0x10000
buffer_load_dwordx4 a[96:99], v2, s[20:23], s51 offen offset:0 sc0 sc1
Mz 76,4,196
Rt 36,17,8192
Cv 184,184,185
Mz 68,4,192
Rt 38,17,8704
Cv 185,186,187
Mz 80,8,192
Rt 40,18,8192
Cv 186,188,189
Mz 88,8,196
Rt 42,18,8704
buffer_load_dwordx4 v9, s[16:19], s92 offen offset:1024 lds
Mz 92,12,196
Rt 44,19,8192
Cv 187,190,191
v_mul_f32_e32 v32, s54, v32
Mz 84,12,192
Rt 46,19,8704
s_waitcnt lgkmcnt(8)
Mv 64,16,200
Rt 0,16,12288
Cv 192,192,193
Mv 72,16,204
Rt 2,16,12800
buffer_store_dwordx4 v[168:171], v2, s[20:23], s47 offen offset:1024 sc0 sc1
.LzH33:
)" R"(
s_cbranch_execz .LzH41
Mv 76,20,204
Rt 4,17,12288
Cv 193,194,195
Cv 194,196,197
Mv 68,20,200
Rt 6,17,12800
Cv 195,198,199
v_mul_f32_e32 v33, s54, v33
Mv 80,24,200
Rt 8,18,12288
Cv 200,200,201
Mv 88,24,204
Rt 10,18,12800
buffer_load_dwordx4 v10, s[16:19], s92 offen offset:2048 lds
Mv 92,28,204
Rt 12,19,12288
Cv 201,202,203
Cv 32,32,33
Mv 84,28,200
Rt 14,19,12800
Cv 202,204,205
s_waitcnt lgkmcnt(8)
Mv 64,32,208
Rt 16,16,16384
Cv 203,206,207
Mv 72,32,212
Rt 18,16,16896
buffer_load_dwordx4 a[100:103], v2, s[20:23], s51 offen offset:1024 sc0 sc1
Mv 76,36,212
Rt 20,17,16384
Cv 208,208,209
Mv 68,36,208
Rt 22,17,16896
Cv 209,210,211
v_accvgpr_write_b32 a224, v32
Mv 80,40,208
Rt 24,18,16384
Cv 210,212,213
Mv 88,40,212
Rt 26,18,16896
buffer_load_dwordx4 v11, s[16:19], s92 offen offset:3072 lds
Mv 92,44,212
Rt 28,19,16384
Cv 211,214,215
v_mul_f32_e32 v34, s54, v34
Mv 84,44,208
Rt 30,19,16896
s_waitcnt lgkmcnt(8)
Mv 64,0,216
Rt 32,16,20480
Cv 216,216,217
Mv 72,0,220
Rt 34,16,20992
buffer_store_dwordx4 v[176:179], v2, s[20:23], s47 offen offset:2048 sc0 sc1
Mv 76,4,220
Rt 36,17,20480
Cv 217,218,219
Cv 218,220,221
Mv 68,4,216
Rt 38,17,20992
s_add_u32 s92, s42, 0x40180
v_mul_f32_e32 v35, s54, v35
Mv 80,8,216
)" R"(
Rt 40,18,20480
s_add_u32 m0, s41, 4096
Mv 88,8,220
Rt 42,18,20992
buffer_load_dwordx4 v8, s[16:19], s92 offen offset:0 lds
Mv 92,12,220
Rt 44,19,20480
Cv 219,222,223
Cv 34,34,35
Mv 84,12,216
Rt 46,19,20992
s_waitcnt lgkmcnt(8)
Mv 64,16,128
Rt 0,16,24576
v_accvgpr_write_b32 a225, v34
Mv 72,16,132
Rt 2,16,25088
buffer_load_dwordx4 a[104:107], v2, s[20:23], s51 offen offset:2048 sc0 sc1
Mv 76,20,132
Rt 4,17,24576
v_mul_f32_e32 v36, s54, v36
Mv 68,20,128
Rt 6,17,25088
v_mul_f32_e32 v37, s54, v37
Cv 36,36,37
Mv 80,24,128
Rt 8,18,24576
v_accvgpr_write_b32 a226, v36
Mv 88,24,132
Rt 10,18,25088
buffer_load_dwordx4 v9, s[16:19], s92 offen offset:1024 lds
Mv 92,28,132
Rt 12,19,24576
v_mul_f32_e32 v38, s54, v38
v_mul_f32_e32 v39, s54, v39
Mv 84,28,128
Rt 14,19,25088
s_waitcnt lgkmcnt(8)
Mv 64,32,136
Rt 16,12,32768
buffer_store_dwordx4 v[184:187], v2, s[20:23], s47 offen offset:3072 sc0 sc1
Mv 72,32,140
Rt 18,12,33280
Cv 38,38,39
v_accvgpr_write_b32 a227, v38
Mv 76,36,140
Rt 20,13,32768
v_mul_f32_e32 v40, s54, v40
Mv 68,36,136
Rt 22,13,33280
buffer_load_dwordx4 a[108:111], v2, s[20:23], s51 offen offset:3072 sc0 sc1
Mv 80,40,136
Rt 24,14,32768
v_mul_f32_e32 v41, s54, v41
Mv 88,40,140
Rt 26,14,33280
buffer_load_dwordx4 v10, s[16:19], s92 offen offset:2048 lds
Mv 92,44,140
Rt 28,15,32768
Cv 40,40,41
v_accvgpr_write_b32 a228, v40
Mv 84,44,136
Rt 30,15,33280
s_waitcnt vmcnt(19) lgkmcnt(8)
s_barrier
)" R"(
buffer_store_dword v6, v7, s[20:23], s49 offen offset:1024 sc0 sc1
Mv 64,0,144
Rt 32,12,36864
buffer_load_dword v4, v5, s[20:23], s50 offen offset:0 sc0 sc1
Mv 72,0,148
Rt 34,12,37376
v_mul_f32_e32 v42, s54, v42
v_mul_f32_e32 v43, s54, v43
Mv 76,4,148
Rt 36,13,36864
Cv 42,42,43
Mv 68,4,144
Rt 38,13,37376
s_add_u32 s92, s42, 0x1c0180
v_accvgpr_write_b32 a229, v42
Mv 80,8,144
Rt 40,14,36864
s_add_u32 m0, s41, 65536
Mv 88,8,148
Rt 42,14,37376
buffer_load_dwordx4 v8, s[16:19], s92 offen offset:0 lds
Mv 92,12,148
Rt 44,15,36864
v_mul_f32_e32 v44, s54, v44
v_mul_f32_e32 v45, s54, v45
Mv 84,12,144
Rt 46,15,37376
s_waitcnt lgkmcnt(8)
Mv 64,16,152
Rt 0,12,40960
Cv 44,44,45
Mv 72,16,156
Rt 2,12,41472
buffer_load_dwordx4 a[112:115], v3, s[20:23], s51 offen offset:0 sc0 sc1
Mv 76,20,156
Rt 4,13,40960
v_accvgpr_write_b32 a230, v44
Mv 68,20,152
Rt 6,13,41472
v_mul_f32_e32 v46, s54, v46
v_mul_f32_e32 v47, s54, v47
Mv 80,24,152
Rt 8,14,40960
Cv 46,46,47
Mv 88,24,156
Rt 10,14,41472
buffer_load_dwordx4 v9, s[16:19], s92 offen offset:1024 lds
Mv 92,28,156
Rt 12,15,40960
v_accvgpr_write_b32 a231, v46
v_mul_f32_e32 v48, s54, v48
Mv 84,28,152
Rt 14,15,41472
s_waitcnt lgkmcnt(8)
Mv 64,32,160
Rt 16,12,45056
v_mul_f32_e32 v49, s54, v49
Mv 72,32,164
Rt 18,12,45568
buffer_store_dwordx4 v[192:195], v3, s[20:23], s47 offen offset:0 sc0 sc1
Mv 76,36,164
Rt 20,13,45056
Cv 48,48,49
Mv 68,36,160
)" R"(
Rt 22,13,45568
v_accvgpr_write_b32 a232, v48
v_mul_f32_e32 v50, s54, v50
Mv 80,40,160
Rt 24,14,45056
v_mul_f32_e32 v51, s54, v51
Mv 88,40,164
Rt 26,14,45568
buffer_load_dwordx4 v10, s[16:19], s92 offen offset:2048 lds
Mv 92,44,164
Rt 28,15,45056
Cv 50,50,51
v_accvgpr_write_b32 a233, v50
Mv 84,44,160
Rt 30,15,45568
s_waitcnt lgkmcnt(8)
Mv 64,0,168
Rt 32,12,49152
v_mul_f32_e32 v52, s54, v52
Mv 72,0,172
Rt 34,12,49664
buffer_load_dwordx4 a[116:119], v3, s[20:23], s51 offen offset:1024 sc0 sc1
Mv 76,4,172
Rt 36,13,49152
v_mul_f32_e32 v53, s54, v53
Mv 68,4,168
Rt 38,13,49664
Cv 52,52,53
v_accvgpr_write_b32 a234, v52
Mv 80,8,168
Rt 40,14,49152
v_mul_f32_e32 v54, s54, v54
Mv 88,8,172
Rt 42,14,49664
buffer_load_dwordx4 v11, s[16:19], s92 offen offset:3072 lds
Mv 92,12,172
Rt 44,15,49152
v_mul_f32_e32 v55, s54, v55
Cv 54,54,55
Mv 84,12,168
Rt 46,15,49664
s_waitcnt lgkmcnt(8)
Mv 64,16,176
Rt 0,12,53248
v_accvgpr_write_b32 a235, v54
Mv 72,16,180
Rt 2,12,53760
buffer_store_dwordx4 v[200:203], v3, s[20:23], s47 offen offset:1024 sc0 sc1
Mv 76,20,180
Rt 4,13,53248
v_mul_f32_e32 v56, s54, v56
Mv 68,20,176
Rt 6,13,53760
s_add_u32 s92, s42, 0x200180
v_mul_f32_e32 v57, s54, v57
Mv 80,24,176
Rt 8,14,53248
s_add_u32 m0, s41, 69632
Mv 88,24,180
Rt 10,14,53760
buffer_load_dwordx4 v8, s[16:19], s92 offen offset:0 lds
Mv 92,28,180
Rt 12,15,53248
Cv 56,56,57
)" R"(
v_accvgpr_write_b32 a236, v56
Mv 84,28,176
Rt 14,15,53760
s_waitcnt lgkmcnt(8)
Mv 64,32,184
Rt 16,12,57344
v_mul_f32_e32 v58, s54, v58
Mv 72,32,188
Rt 18,12,57856
buffer_load_dwordx4 a[120:123], v3, s[20:23], s51 offen offset:2048 sc0 sc1
Mv 76,36,188
Rt 20,13,57344
v_mul_f32_e32 v59, s54, v59
Mv 68,36,184
Rt 22,13,57856
Cv 58,58,59
v_accvgpr_write_b32 a237, v58
Mv 80,40,184
Rt 24,14,57344
v_mul_f32_e32 v60, s54, v60
Mv 88,40,188
Rt 26,14,57856
buffer_load_dwordx4 v9, s[16:19], s92 offen offset:1024 lds
Mv 92,44,188
Rt 28,15,57344
v_mul_f32_e32 v61, s54, v61
Cv 60,60,61
Mv 84,44,184
Rt 30,15,57856
s_waitcnt lgkmcnt(8)
Mv 64,0,48
Rt 32,16,32768
buffer_store_dwordx4 v[208:211], v3, s[20:23], s47 offen offset:2048 sc0 sc1
Mv 72,0,52
Rt 34,16,33280
v_accvgpr_write_b32 a238, v60
v_mul_f32_e32 v62, s54, v62
Mv 76,4,52
Rt 36,17,32768
v_mul_f32_e32 v63, s54, v63
Mv 68,4,48
Rt 38,17,33280
buffer_load_dwordx4 a[124:127], v3, s[20:23], s51 offen offset:3072 sc0 sc1
Mv 80,8,48
Rt 40,18,32768
Cv 62,62,63
Mv 88,8,52
Rt 42,18,33280
buffer_load_dwordx4 v10, s[16:19], s92 offen offset:2048 lds
Mv 92,12,52
Rt 44,19,32768
v_accvgpr_write_b32 a239, v62
Mv 84,12,48
Rt 46,19,33280
s_waitcnt vmcnt(14) lgkmcnt(8)
s_barrier
Mv 64,16,56
Rt 0,16,36864
v_cmp_eq_u32_e32 vcc, s3, v4
s_andn2_b64 s[28:29], -1, vcc
s_cbranch_scc1 .Lzsp5
.Lzok5:
Mv 72,16,60
Rt 2,16,37376
s_add_u32 s40, s55, 0x10000
)" R"(
Mv 76,20,60
Rt 4,17,36864
Mv 68,20,56
Rt 6,17,37376
Mv 80,24,56
Rt 8,18,36864
s_add_u32 m0, s36, 32768
Mv 88,24,60
Rt 10,18,37376
buffer_load_dwordx4 v2, s[20:23], s40 offen offset:0 sc1 lds
Mv 92,28,60
Rt 12,19,36864
Mv 84,28,56
Rt 14,19,37376
s_waitcnt lgkmcnt(8)
Mz 32,32,192
Rt 16,16,40960
buffer_store_dwordx4 v[216:219], v3, s[20:23], s47 offen offset:3072 sc0 sc1
Mz 40,32,196
Rt 18,16,41472
Mz 44,36,196
Rt 20,17,40960
Mz 36,36,192
Rt 22,17,41472
buffer_load_dwordx4 v2, s[20:23], s40 offen offset:1024 sc1 lds
Mz 48,40,192
Rt 24,18,40960
v_mul_f32_e32 v64, s54, v64
Mz 56,40,196
Rt 26,18,41472
v_mul_f32_e32 v65, s54, v65
Mz 60,44,196
Rt 28,19,40960
Cv 64,64,65
Mz 52,44,192
Rt 30,19,41472
s_waitcnt lgkmcnt(8)
Mv 32,0,200
Rt 32,16,45056
v_accvgpr_write_b32 a240, v64
Mv 40,0,204
Rt 34,16,45568
buffer_load_dwordx4 v2, s[20:23], s40 offen offset:2048 sc1 lds
Mv 44,4,204
Rt 36,17,45056
v_mul_f32_e32 v66, s54, v66
Mv 36,4,200
Rt 38,17,45568
v_mul_f32_e32 v67, s54, v67
Mv 48,8,200
Rt 40,18,45056
buffer_store_dwordx4 v[224:227], v2, s[20:23], s48 offen offset:0 sc0 sc1
Mv 56,8,204
Rt 42,18,45568
Cv 66,66,67
Mv 60,12,204
Rt 44,19,45056
v_accvgpr_write_b32 a241, v66
Mv 52,12,200
Rt 46,19,45568
buffer_load_dwordx4 v2, s[20:23], s40 offen offset:3072 sc1 lds
s_waitcnt lgkmcnt(8)
Mv 32,16,208
Rt 0,16,49152
)" R"(
v_mul_f32_e32 v68, s54, v68
Mv 40,16,212
Rt 2,16,49664
v_mul_f32_e32 v69, s54, v69
Mv 44,20,212
Rt 4,17,49152
Cv 68,68,69
Mv 36,20,208
Rt 6,17,49664
v_accvgpr_write_b32 a242, v68
Mv 48,24,208
Rt 8,18,49152
s_add_u32 m0, s36, 36864
Mv 56,24,212
Rt 10,18,49664
buffer_load_dwordx4 v3, s[20:23], s40 offen offset:0 sc1 lds
Mv 60,28,212
Rt 12,19,49152
v_mul_f32_e32 v70, s54, v70
Mv 52,28,208
Rt 14,19,49664
s_waitcnt lgkmcnt(8)
Mv 32,32,216
Rt 16,16,53248
buffer_store_dwordx4 v[232:235], v2, s[20:23], s48 offen offset:1024 sc0 sc1
Mv 40,32,220
Rt 18,16,53760
v_mul_f32_e32 v71, s54, v71
Mv 44,36,220
Rt 20,17,53248
Cv 70,70,71
Mv 36,36,216
Rt 22,17,53760
buffer_load_dwordx4 v3, s[20:23], s40 offen offset:1024 sc1 lds
Mv 48,40,216
Rt 24,18,53248
v_accvgpr_write_b32 a243, v70
Mv 56,40,220
Rt 26,18,53760
v_mul_f32_e32 v72, s54, v72
Mv 60,44,220
Rt 28,19,53248
v_mul_f32_e32 v73, s54, v73
Mv 52,44,216
Rt 30,19,53760
s_waitcnt lgkmcnt(8)
Mv 32,0,128
Rt 32,16,57344
Cv 72,72,73
Mv 40,0,132
Rt 34,16,57856
buffer_load_dwordx4 v3, s[20:23], s40 offen offset:2048 sc1 lds
Mv 44,4,132
Rt 36,17,57344
v_accvgpr_write_b32 a244, v72
Mv 36,4,128
Rt 38,17,57856
v_mul_f32_e32 v74, s54, v74
Mv 48,8,128
Rt 40,18,57344
buffer_store_dwordx4 v[240:243], v2, s[20:23], s48 offen offset:2048 sc0 sc1
Mv 56,8,132
Rt 42,18,57856
v_mul_f32_e32 v75, s54, v75
)" R"(
Mv 60,12,132
Rt 44,19,57344
Cv 74,74,75
Mv 52,12,128
Rt 46,19,57856
buffer_load_dwordx4 v3, s[20:23], s40 offen offset:3072 sc1 lds
s_waitcnt lgkmcnt(8)
Mv 32,16,136
Rt 0,20,0
v_accvgpr_write_b32 a245, v74
Mv 40,16,140
Rt 2,20,512
v_mul_f32_e32 v76, s54, v76
Mv 44,20,140
Rt 4,21,0
v_mul_f32_e32 v77, s54, v77
Mv 36,20,136
Rt 6,21,512
Cv 76,76,77
Mv 48,24,136
Rt 8,22,0
v_accvgpr_write_b32 a246, v76
Mv 56,24,140
Rt 10,22,512
v_mul_f32_e32 v78, s54, v78
Mv 60,28,140
Rt 12,23,0
v_mul_f32_e32 v79, s54, v79
Mv 52,28,136
Rt 14,23,512
s_waitcnt vmcnt(10) lgkmcnt(8)
s_barrier
buffer_store_dword v6, v7, s[20:23], s49 offen offset:2048 sc0 sc1
Mv 32,32,144
Rt 16,20,4096
s_mov_b32 s83, 0x1508000
buffer_load_dwordx2 v[24:25], v7, s[20:23], s83 offen sc0 sc1
Mv 40,32,148
Rt 18,20,4608
Cv 78,78,79
Mv 44,36,148
Rt 20,21,4096
s_add_u32 m0, s36, 98304
Mv 36,36,144
Rt 22,21,4608
buffer_load_dwordx4 v2, s[20:23], s55 offen offset:0 sc1 lds
Mv 48,40,144
Rt 24,22,4096
v_accvgpr_write_b32 a247, v78
Mv 56,40,148
Rt 26,22,4608
v_mul_f32_e32 v80, s54, v80
Mv 60,44,148
Rt 28,23,4096
buffer_store_dwordx4 v[248:251], v2, s[20:23], s48 offen offset:3072 sc0 sc1
Mv 52,44,144
Rt 30,23,4608
s_waitcnt lgkmcnt(8)
Mv 32,0,152
Rt 32,20,8192
v_mul_f32_e32 v81, s54, v81
Mv 40,0,156
Rt 34,20,8704
buffer_load_dwordx4 v2, s[20:23], s55 offen offset:1024 sc1 lds
Mv 44,4,156
)" R"(
Rt 36,21,8192
Cv 80,80,81
Mv 36,4,152
Rt 38,21,8704
v_accvgpr_write_b32 a248, v80
Mv 48,8,152
Rt 40,22,8192
v_mul_f32_e32 v82, s54, v82
Mv 56,8,156
Rt 42,22,8704
v_mul_f32_e32 v83, s54, v83
Mv 60,12,156
Rt 44,23,8192
Cv 82,82,83
Mv 52,12,152
Rt 46,23,8704
buffer_load_dwordx4 v2, s[20:23], s55 offen offset:2048 sc1 lds
s_waitcnt lgkmcnt(8)
Mv 32,16,160
Rt 0,20,12288
v_accvgpr_write_b32 a249, v82
Mv 40,16,164
Rt 2,20,12800
v_mul_f32_e32 v84, s54, v84
Mv 44,20,164
Rt 4,21,12288
buffer_store_dwordx4 v[228:231], v3, s[20:23], s48 offen offset:0 sc0 sc1
Mv 36,20,160
Rt 6,21,12800
v_mul_f32_e32 v85, s54, v85
Mv 48,24,160
Rt 8,22,12288
Cv 84,84,85
Mv 56,24,164
Rt 10,22,12800
buffer_load_dwordx4 v2, s[20:23], s55 offen offset:3072 sc1 lds
Mv 60,28,164
Rt 12,23,12288
v_accvgpr_write_b32 a250, v84
.LzH34:
s_cbranch_execz .LzH42
Mv 52,28,160
Rt 14,23,12800
s_waitcnt lgkmcnt(8)
Mv 32,32,168
Rt 16,20,16384
v_mul_f32_e32 v86, s54, v86
Mv 40,32,172
Rt 18,20,16896
v_mul_f32_e32 v87, s54, v87
Mv 44,36,172
Rt 20,21,16384
s_add_u32 m0, s36, 102400
Mv 36,36,168
Rt 22,21,16896
buffer_load_dwordx4 v3, s[20:23], s55 offen offset:0 sc1 lds
Mv 48,40,168
Rt 24,22,16384
Cv 86,86,87
Mv 56,40,172
Rt 26,22,16896
v_accvgpr_write_b32 a251, v86
Mv 60,44,172
Rt 28,23,16384
)" R"(
buffer_store_dwordx4 v[236:239], v3, s[20:23], s48 offen offset:1024 sc0 sc1
Mv 52,44,168
Rt 30,23,16896
s_waitcnt lgkmcnt(8)
Mv 32,0,176
Rt 32,20,20480
v_mul_f32_e32 v88, s54, v88
Mv 40,0,180
Rt 34,20,20992
buffer_load_dwordx4 v3, s[20:23], s55 offen offset:1024 sc1 lds
Mv 44,4,180
Rt 36,21,20480
v_mul_f32_e32 v89, s54, v89
Mv 36,4,176
Rt 38,21,20992
Cv 88,88,89
Mv 48,8,176
Rt 40,22,20480
s_add_u32 s50, s50, 1024
Mv 56,8,180
Rt 42,22,20992
s_min_u32 s50, s50, s60
Mv 60,12,180
Rt 44,23,20480
v_accvgpr_write_b32 a252, v88
Mv 52,12,176
Rt 46,23,20992
buffer_load_dwordx4 v3, s[20:23], s55 offen offset:2048 sc1 lds
s_waitcnt lgkmcnt(8)
Mv 32,16,184
Rt 0,20,24576
v_mul_f32_e32 v90, s54, v90
Mv 40,16,188
Rt 2,20,25088
v_mul_f32_e32 v91, s54, v91
Mv 44,20,188
Rt 4,21,24576
buffer_store_dwordx4 v[244:247], v3, s[20:23], s48 offen offset:2048 sc0 sc1
Mv 36,20,184
Rt 6,21,25088
Cv 90,90,91
Mv 48,24,184
Rt 8,22,24576
v_accvgpr_write_b32 a253, v90
Mv 56,24,188
Rt 10,22,25088
buffer_load_dwordx4 v3, s[20:23], s55 offen offset:3072 sc1 lds
Mv 60,28,188
Rt 12,23,24576
v_mul_f32_e32 v92, s54, v92
Mv 52,28,184
Rt 14,23,25088
s_waitcnt lgkmcnt(8)
Mv 32,32,48
Rt 16,12,0
v_mul_f32_e32 v93, s54, v93
Mv 40,32,52
Rt 18,12,512
Cv 92,92,93
Mv 44,36,52
Rt 20,13,0
v_accvgpr_write_b32 a254, v92
Mv 36,36,48
Rt 22,13,512
)" R"(
v_mul_f32_e32 v94, s54, v94
Mv 48,40,48
Rt 24,14,0
buffer_store_dwordx4 v[252:255], v3, s[20:23], s48 offen offset:3072 sc0 sc1
Mv 56,40,52
Rt 26,14,512
v_mul_f32_e32 v95, s54, v95
Mv 60,44,52
Rt 28,15,0
Cv 94,94,95
Mv 52,44,48
Rt 30,15,512
s_waitcnt vmcnt(13) lgkmcnt(8)
s_barrier
v_cmp_eq_u32_e32 vcc, s3, v25
s_andn2_b64 s[28:29], 1, vcc
s_cbranch_scc1 .Lzsp7
.Lzok7:
Mv 32,0,56
Rt 32,12,4096
buffer_load_dword v4, v5, s[20:23], s50 offen offset:0 sc0 sc1
Mv 40,0,60
Rt 34,12,4608
v_accvgpr_write_b32 a255, v94
Mv 44,4,60
Rt 36,13,4096
s_add_u32 m0, s36, 131072
Mv 36,4,56
Rt 38,13,4608
buffer_load_dwordx4 v2, s[20:23], s56 offen offset:0 sc1 lds
Mv 48,8,56
Rt 40,14,4096
Mv 56,8,60
Rt 42,14,4608
Mv 60,12,60
Rt 44,15,4096
Mv 52,12,56
Rt 46,15,4608
s_waitcnt lgkmcnt(8)
Mz 96,16,192
Rt 0,12,8192
Mz 104,16,196
Rt 2,12,8704
buffer_load_dwordx4 v2, s[20:23], s56 offen offset:1024 sc1 lds
Mz 108,20,196
Rt 4,13,8192
Mz 100,20,192
Rt 6,13,8704
Mz 112,24,192
Rt 8,14,8192
v_mul_f32_e32 v32, s54, v32
Mz 120,24,196
Rt 10,14,8704
v_mul_f32_e32 v33, s54, v33
Mz 124,28,196
Rt 12,15,8192
Cv 32,32,33
Mz 116,28,192
Rt 14,15,8704
buffer_load_dwordx4 v2, s[20:23], s56 offen offset:2048 sc1 lds
s_waitcnt lgkmcnt(8)
Mv 96,32,200
Rt 16,12,12288
v_mul_f32_e32 v34, s54, v34
Mv 104,32,204
)" R"(
Rt 18,12,12800
v_accvgpr_write_b32 a192, v32
Mv 108,36,204
Rt 20,13,12288
v_mul_f32_e32 v35, s54, v35
Mv 100,36,200
Rt 22,13,12800
Cv 34,34,35
v_accvgpr_write_b32 a193, v34
Mv 112,40,200
Rt 24,14,12288
v_mul_f32_e32 v36, s54, v36
Mv 120,40,204
Rt 26,14,12800
buffer_load_dwordx4 v2, s[20:23], s56 offen offset:3072 sc1 lds
Mv 124,44,204
Rt 28,15,12288
v_mul_f32_e32 v37, s54, v37
Mv 116,44,200
Rt 30,15,12800
s_waitcnt lgkmcnt(8)
Mv 96,0,208
Rt 32,12,16384
Cv 36,36,37
Mv 104,0,212
Rt 34,12,16896
v_accvgpr_write_b32 a194, v36
Mv 108,4,212
Rt 36,13,16384
s_add_u32 m0, s36, 135168
Mv 100,4,208
Rt 38,13,16896
buffer_load_dwordx4 v3, s[20:23], s56 offen offset:0 sc1 lds
Mv 112,8,208
Rt 40,14,16384
v_mul_f32_e32 v38, s54, v38
Mv 120,8,212
Rt 42,14,16896
v_mul_f32_e32 v39, s54, v39
Mv 124,12,212
Rt 44,15,16384
Cv 38,38,39
Mv 116,12,208
Rt 46,15,16896
s_waitcnt lgkmcnt(8)
Mv 96,16,216
Rt 0,12,20480
v_accvgpr_write_b32 a195, v38
Mv 104,16,220
Rt 2,12,20992
buffer_load_dwordx4 v3, s[20:23], s56 offen offset:1024 sc1 lds
Mv 108,20,220
Rt 4,13,20480
v_mul_f32_e32 v40, s54, v40
Mv 100,20,216
Rt 6,13,20992
v_mul_f32_e32 v41, s54, v41
Cv 40,40,41
Mv 112,24,216
Rt 8,14,20480
v_accvgpr_write_b32 a196, v40
Mv 120,24,220
Rt 10,14,20992
v_mul_f32_e32 v42, s54, v42
)" R"(
Mv 124,28,220
Rt 12,15,20480
v_mul_f32_e32 v43, s54, v43
Mv 116,28,216
Rt 14,15,20992
buffer_load_dwordx4 v3, s[20:23], s56 offen offset:2048 sc1 lds
s_waitcnt lgkmcnt(8)
Mv 96,32,128
Rt 16,12,24576
Cv 42,42,43
Mv 104,32,132
Rt 18,12,25088
v_accvgpr_write_b32 a197, v42
Mv 108,36,132
Rt 20,13,24576
v_mul_f32_e32 v44, s54, v44
Mv 100,36,128
Rt 22,13,25088
v_mul_f32_e32 v45, s54, v45
Cv 44,44,45
Mv 112,40,128
Rt 24,14,24576
v_accvgpr_write_b32 a198, v44
Mv 120,40,132
Rt 26,14,25088
buffer_load_dwordx4 v3, s[20:23], s56 offen offset:3072 sc1 lds
Mv 124,44,132
Rt 28,15,24576
v_mul_f32_e32 v46, s54, v46
Mv 116,44,128
Rt 30,15,25088
s_waitcnt lgkmcnt(8)
Mv 96,0,136
Rt 32,16,0
v_readfirstlane_b32 s63, v24
Mv 104,0,140
Rt 34,16,512
v_and_b32 v24, 16, v1
Mv 108,4,140
Rt 36,17,0
v_lshrrev_b32_e64 v24, v24, s63
Mv 100,4,136
Rt 38,17,512
v_and_b32 v24, 0x7ff, v24
v_mul_u32_u24 v24, 896, v24
Mv 112,8,136
Rt 40,18,0
v_and_b32 v25, 14, v0
Mv 120,8,140
Rt 42,18,512
v_min_u32 v25, 1, v25
Mv 124,12,140
Rt 44,19,0
v_lshlrev_b32 v25, 30, v25
v_and_or_b32 v25, v0, 48, v25
Mv 116,12,136
Rt 46,19,512
s_waitcnt vmcnt(8) lgkmcnt(8)
s_barrier
buffer_store_dword v6, v7, s[20:23], s49 offen offset:3072 sc0 sc1
Mv 96,16,144
Rt 0,16,4096
v_cmp_eq_u32_e32 vcc, s3, v4
s_andn2_b64 s[28:29], -1, vcc
s_cbranch_scc1 .Lzsp6
)" R"(
.Lzok6:
Mv 104,16,148
Rt 2,16,4608
v_add_u32 v24, v24, v25
v_add_u32 v8, 0x10000, v1
Mv 108,20,148
Rt 4,17,4096
s_and_b32 s83, s33, 1
v_add_u32 v10, 0x20000, v1
Mv 100,20,144
Rt 6,17,4608
s_mul_i32 s82, s83, 448
v_mul_f32_e32 v47, s54, v47
s_add_u32 s56, s56, 0x10000
Mv 112,24,144
Rt 8,18,4096
Cv 46,46,47
s_add_u32 s55, s55, 0x10000
Mv 120,24,148
Rt 10,18,4608
v_accvgpr_write_b32 a199, v46
s_add_u32 s55, s55, 0x10000
Mv 124,28,148
Rt 12,19,4096
v_mul_f32_e32 v48, s54, v48
s_cmp_eq_u32 s57, 2
s_cselect_b32 s55, s59, s55
Mv 116,28,144
Rt 14,19,4608
s_mov_b32 s93, 0
s_waitcnt lgkmcnt(8)
Mv 96,32,152
Rt 16,16,8192
v_mul_f32_e32 v49, s54, v49
s_mov_b32 s94, 1
Mv 104,32,156
Rt 18,16,8704
Cv 48,48,49
Mv 108,36,156
Rt 20,17,8192
v_accvgpr_write_b32 a200, v48
Mv 100,36,152
Rt 22,17,8704
v_mul_f32_e32 v50, s54, v50
v_mul_f32_e32 v51, s54, v51
Mv 112,40,152
Rt 24,18,8192
Cv 50,50,51
Mv 120,40,156
Rt 26,18,8704
v_accvgpr_write_b32 a201, v50
Mv 124,44,156
Rt 28,19,8192
v_mul_f32_e32 v52, s54, v52
Mv 116,44,152
Rt 30,19,8704
s_waitcnt lgkmcnt(8)
Mv 96,0,160
Rt 32,16,12288
v_mul_f32_e32 v53, s54, v53
Mv 104,0,164
Rt 34,16,12800
Cv 52,52,53
Mv 108,4,164
)" R"(
Rt 36,17,12288
v_accvgpr_write_b32 a202, v52
Mv 100,4,160
Rt 38,17,12800
buffer_load_dwordx4 a[128:131], v24, s[12:15], s82 offen offset:0
Mv 112,8,160
Rt 40,18,12288
v_mul_f32_e32 v54, s54, v54
Mv 120,8,164
Rt 42,18,12800
v_mul_f32_e32 v55, s54, v55
Mv 124,12,164
Rt 44,19,12288
Cv 54,54,55
Mv 116,12,160
Rt 46,19,12800
s_waitcnt lgkmcnt(8)
Mv 96,16,168
Rt 0,16,16384
buffer_load_dwordx4 a[132:135], v24, s[12:15], s82 offen offset:64
Mv 104,16,172
Rt 2,16,16896
v_accvgpr_write_b32 a203, v54
Mv 108,20,172
Rt 4,17,16384
v_mul_f32_e32 v56, s54, v56
Mv 100,20,168
Rt 6,17,16896
v_mul_f32_e32 v57, s54, v57
Cv 56,56,57
Mv 112,24,168
Rt 8,18,16384
v_accvgpr_write_b32 a204, v56
Mv 120,24,172
Rt 10,18,16896
buffer_load_dwordx4 a[136:139], v24, s[12:15], s82 offen offset:128
Mv 124,28,172
Rt 12,19,16384
v_mul_f32_e32 v58, s54, v58
Mv 116,28,168
Rt 14,19,16896
s_waitcnt lgkmcnt(8)
Mv 96,32,176
Rt 16,16,20480
buffer_load_dwordx4 a[140:143], v24, s[12:15], s82 offen offset:192
Mv 104,32,180
Rt 18,16,20992
v_mul_f32_e32 v59, s54, v59
Mv 108,36,180
Rt 20,17,20480
Cv 58,58,59
Mv 100,36,176
Rt 22,17,20992
v_accvgpr_write_b32 a205, v58
v_mul_f32_e32 v60, s54, v60
Mv 112,40,176
Rt 24,18,20480
v_mul_f32_e32 v61, s54, v61
Mv 120,40,180
Rt 26,18,20992
buffer_load_dwordx4 a[144:147], v24, s[12:15], s82 offen offset:256
Mv 124,44,180
Rt 28,19,20480
Cv 60,60,61
)" R"(
Mv 116,44,176
Rt 30,19,20992
s_waitcnt lgkmcnt(8)
s_barrier
Mv 96,0,184
Rt 32,16,24576
buffer_load_dwordx4 a[148:151], v24, s[12:15], s82 offen offset:320
Mv 104,0,188
Rt 34,16,25088
v_accvgpr_write_b32 a206, v60
Mv 108,4,188
Rt 36,17,24576
v_mul_f32_e32 v62, s54, v62
Mv 100,4,184
Rt 38,17,25088
v_mul_f32_e32 v63, s54, v63
Cv 62,62,63
Mv 112,8,184
Rt 40,18,24576
v_accvgpr_write_b32 a207, v62
Mv 120,8,188
Rt 42,18,25088
buffer_load_dwordx4 a[152:155], v24, s[12:15], s82 offen offset:384
Mv 124,12,188
Rt 44,19,24576
Mv 116,12,184
Rt 46,19,25088
s_waitcnt lgkmcnt(8)
Mv 96,16,48
Rb 0,8,32768
Mv 104,16,52
Rb 4,8,33792
Mv 108,20,52
Rb 8,8,34816
Mv 100,20,48
Rb 12,8,35840
Mv 112,24,48
Rb 16,8,40960
Rb 20,8,41984
Mv 120,24,52
Rb 24,8,43008
Mv 124,28,52
Mv 116,28,48
Rb 28,8,44032
s_waitcnt lgkmcnt(8)
Mv 96,32,56
Mv 104,32,60
Rb 32,8,36864
Mv 108,36,60
Mv 100,36,56
Rb 36,8,37888
Mv 112,40,56
Mv 120,40,60
Rb 40,8,38912
Mv 124,44,60
Mv 116,44,56
Rb 44,8,39936
s_cmp_eq_u32 s35, 7
s_cbranch_scc1 .LzA7
.LzA7R:
s_waitcnt lgkmcnt(8)
Mz 64,0,64
Rb 48,8,45056
v_mul_f32_e32 v96, s54, v96
Mz 68,0,68
)" R"(
v_mul_f32_e32 v97, s54, v97
Cv 96,96,97
Mv 68,4,76
Rb 52,8,46080
s_add_u32 m0, s36, 0
Mv 64,4,72
buffer_load_dwordx4 v2, s[20:23], s55 offen offset:0 sc1 lds
Mv 64,8,80
Rb 56,8,47104
v_accvgpr_write_b32 a208, v96
Mv 68,8,84
v_mul_f32_e32 v98, s54, v98
v_mul_f32_e32 v99, s54, v99
Mv 68,12,92
Rb 60,8,48128
s_waitcnt lgkmcnt(8)
Mv 64,12,88
Cv 98,98,99
v_accvgpr_write_b32 a209, v98
v_mul_f32_e32 v100, s54, v100
Mv 64,16,96
Rb 0,8,49152
v_mul_f32_e32 v101, s54, v101
Mv 68,16,100
Cv 100,100,101
v_accvgpr_write_b32 a210, v100
Mv 68,20,108
Rb 4,8,50176
v_mul_f32_e32 v102, s54, v102
Mv 64,20,104
buffer_load_dwordx4 v2, s[20:23], s55 offen offset:1024 sc1 lds
Mv 64,24,112
Rb 8,8,51200
v_mul_f32_e32 v103, s54, v103
Mv 68,24,116
Cv 102,102,103
v_accvgpr_write_b32 a211, v102
Mv 68,28,124
Rb 12,8,52224
s_waitcnt lgkmcnt(8)
Mv 64,28,120
v_mul_f32_e32 v104, s54, v104
v_mul_f32_e32 v105, s54, v105
Cv 104,104,105
Mz 72,32,64
Rb 16,8,57344
v_accvgpr_write_b32 a212, v104
Mz 76,32,68
v_mul_f32_e32 v106, s54, v106
v_mul_f32_e32 v107, s54, v107
Mv 76,36,76
Rb 20,8,58368
Cv 106,106,107
Mv 72,36,72
buffer_load_dwordx4 v2, s[20:23], s55 offen offset:2048 sc1 lds
Mv 72,40,80
Rb 24,8,59392
v_accvgpr_write_b32 a213, v106
Mv 76,40,84
v_mul_f32_e32 v108, s54, v108
v_mul_f32_e32 v109, s54, v109
Mv 76,44,92
Rb 28,8,60416
s_waitcnt lgkmcnt(8)
)" R"(
Mv 72,44,88
Cv 108,108,109
v_accvgpr_write_b32 a214, v108
v_mul_f32_e32 v110, s54, v110
Mv 72,48,96
Rb 32,8,53248
v_mul_f32_e32 v111, s54, v111
Mv 76,48,100
Cv 110,110,111
v_accvgpr_write_b32 a215, v110
Mv 76,52,108
Rb 36,8,54272
v_mul_f32_e32 v112, s54, v112
Mv 72,52,104
buffer_load_dwordx4 v2, s[20:23], s55 offen offset:3072 sc1 lds
Mv 72,56,112
Rb 40,8,55296
v_mul_f32_e32 v113, s54, v113
Mv 76,56,116
Cv 112,112,113
v_accvgpr_write_b32 a216, v112
Mv 76,60,124
Rb 44,8,56320
s_waitcnt lgkmcnt(8)
Mv 72,60,120
v_mul_f32_e32 v114, s54, v114
v_mul_f32_e32 v115, s54, v115
Cv 114,114,115
Mz 80,0,64
Rb 48,8,61440
v_accvgpr_write_b32 a217, v114
.LzH35:
s_cbranch_execz .LzH14
Mz 84,0,68
v_mul_f32_e32 v116, s54, v116
v_mul_f32_e32 v117, s54, v117
Mv 84,4,76
Rb 52,8,62464
s_add_u32 m0, s36, 4096
Mv 80,4,72
buffer_load_dwordx4 v3, s[20:23], s55 offen offset:0 sc1 lds
Mv 80,8,80
Rb 56,8,63488
Cv 116,116,117
Mv 84,8,84
v_accvgpr_write_b32 a218, v116
v_mul_f32_e32 v118, s54, v118
Mv 84,12,92
Rb 60,8,64512
s_waitcnt lgkmcnt(8)
Mv 80,12,88
v_mul_f32_e32 v119, s54, v119
Cv 118,118,119
v_accvgpr_write_b32 a219, v118
Mv 80,16,96
v_mul_f32_e32 v120, s54, v120
v_mul_f32_e32 v121, s54, v121
Mv 84,16,100
Cv 120,120,121
v_accvgpr_write_b32 a220, v120
Mv 84,20,108
v_mul_f32_e32 v122, s54, v122
v_mul_f32_e32 v123, s54, v123
Mv 80,20,104
)" R"(
buffer_load_dwordx4 v3, s[20:23], s55 offen offset:1024 sc1 lds
Mv 80,24,112
s_add_u32 s50, s50, 1024
s_min_u32 s50, s50, s60
Mv 84,24,116
Cv 122,122,123
v_accvgpr_write_b32 a221, v122
Mv 84,28,124
v_mul_f32_e32 v124, s54, v124
v_mul_f32_e32 v125, s54, v125
Mv 80,28,120
Cv 124,124,125
s_waitcnt lgkmcnt(0)
s_barrier
Mz 88,32,64
Rb 0,1,32768
v_accvgpr_write_b32 a222, v124
Mz 92,32,68
Rb 4,1,33792
v_mul_f32_e32 v126, s54, v126
Mv 92,36,76
Rb 8,1,34816
v_mul_f32_e32 v127, s54, v127
Mv 88,36,72
Rb 12,1,35840
buffer_load_dwordx4 v3, s[20:23], s55 offen offset:2048 sc1 lds
Mv 88,40,80
Rb 16,1,40960
Cv 126,126,127
Mv 92,40,84
Rb 20,1,41984
v_accvgpr_write_b32 a223, v126
Mv 92,44,92
Rb 24,1,43008
v_mov_b32 v16, 0
Mv 88,44,88
Rb 28,1,44032
v_mov_b32 v17, 0
v_mov_b32 v18, 0
Mv 88,48,96
Rb 32,1,36864
v_mov_b32 v19, 0
Mv 92,48,100
Rb 36,1,37888
v_mov_b32 v20, 0
Mv 92,52,108
Rb 40,1,38912
v_mov_b32 v21, 0
Mv 88,52,104
Rb 44,1,39936
buffer_load_dwordx4 v3, s[20:23], s55 offen offset:3072 sc1 lds
Mv 88,56,112
v_mov_b32 v22, 0
v_mov_b32 v23, 0
Mv 92,56,116
Mv 92,60,124
s_waitcnt lgkmcnt(8)
Mv 88,60,120
Mz 96,0,64
Rb 48,1,45056
buffer_load_dword v4, v5, s[20:23], s50 offen offset:0 sc0 sc1
Mz 100,0,68
s_cmp_le_i32 s57, s58
s_cbranch_scc1 .Lzmk0
.Lzmr0:
)" R"(
Mv 100,4,76
Rb 52,1,46080
s_add_u32 m0, s36, 65536
s_sub_u32 s57, s57, 1
Mv 96,4,72
buffer_load_dwordx4 v2, s[20:23], s56 offen offset:0 sc1 lds
s_add_u32 s55, s55, 0x10000
Mv 96,8,80
Rb 56,1,47104
s_cmp_eq_u32 s57, 2
s_cselect_b32 s55, s59, s55
Mv 100,8,84
v_exp_f32_e32 v32, v64
Mv 100,12,92
Rb 60,1,48128
s_waitcnt lgkmcnt(8)
Mv 96,12,88
v_exp_f32_e32 v24, v65
Mv 96,16,96
Rb 0,1,49152
v_exp_f32_e32 v33, v66
Mv 100,16,100
v_exp_f32_e32 v25, v67
v_exp_f32_e32 v34, v72
Mv 100,20,108
Rb 4,1,50176
v_exp_f32_e32 v26, v73
Mv 96,20,104
buffer_load_dwordx4 v2, s[20:23], s56 offen offset:1024 sc1 lds
v_add_f32_e32 v14, v32, v24
Mv 96,24,112
Rb 8,1,51200
v_exp_f32_e32 v35, v74
Mv 100,24,116
v_exp_f32_e32 v27, v75
Cv 32,32,24
Mv 100,28,124
Rb 12,1,52224
v_add_f32_e32 v14, v14, v33
s_waitcnt lgkmcnt(8)
Mv 96,28,120
v_exp_f32_e32 v36, v68
v_add_f32_e32 v14, v14, v25
Mz 104,32,64
Rb 16,1,57344
v_exp_f32_e32 v28, v69
Mz 108,32,68
v_exp_f32_e32 v37, v70
v_exp_f32_e32 v29, v71
Mv 108,36,76
Rb 20,1,58368
v_exp_f32_e32 v38, v76
Mv 104,36,72
buffer_load_dwordx4 v2, s[20:23], s56 offen offset:2048 sc1 lds
Cv 33,33,25
Mv 104,40,80
Rb 24,1,59392
v_exp_f32_e32 v24, v77
Mv 108,40,84
v_exp_f32_e32 v39, v78
v_add_f32_e32 v14, v14, v34
Mv 108,44,92
Rb 28,1,60416
v_add_f32_e32 v14, v14, v26
)" R"(
s_waitcnt lgkmcnt(8)
Mv 104,44,88
v_exp_f32_e32 v25, v79
Cv 34,34,26
Mv 104,48,96
Rb 32,1,53248
v_exp_f32_e32 v40, v80
Mv 108,48,100
v_exp_f32_e32 v26, v81
v_add_f32_e32 v14, v14, v35
v_add_f32_e32 v14, v14, v27
Mv 108,52,108
Rb 36,1,54272
Cv 35,35,27
v_add_f32_e32 v15, v36, v28
Mv 104,52,104
buffer_load_dwordx4 v2, s[20:23], s56 offen offset:3072 sc1 lds
Cv 36,36,28
Mv 104,56,112
Rb 40,1,55296
v_exp_f32_e32 v41, v82
Mv 108,56,116
v_exp_f32_e32 v27, v83
v_add_f32_e32 v15, v15, v37
Mv 108,60,124
Rb 44,1,56320
v_add_f32_e32 v15, v15, v29
s_waitcnt lgkmcnt(8)
Mv 104,60,120
v_exp_f32_e32 v42, v88
Cv 37,37,29
Mz 112,0,64
Rb 48,1,61440
v_exp_f32_e32 v28, v89
Mz 116,0,68
v_exp_f32_e32 v43, v90
v_add_f32_e32 v15, v15, v38
v_add_f32_e32 v15, v15, v24
Mv 116,4,76
Rb 52,1,62464
s_add_u32 m0, s36, 69632
Cv 38,38,24
Mv 112,4,72
buffer_load_dwordx4 v3, s[20:23], s56 offen offset:0 sc1 lds
v_add_f32_e32 v15, v15, v39
Mv 112,8,80
Rb 56,1,63488
v_exp_f32_e32 v29, v91
Mv 116,8,84
v_exp_f32_e32 v44, v84
v_add_f32_e32 v15, v15, v25
Mv 116,12,92
Rb 60,1,64512
Cv 39,39,25
s_waitcnt lgkmcnt(8)
Mv 112,12,88
v_exp_f32_e32 v24, v85
v_add_f32_e32 v14, v14, v40
Mv 112,16,96
v_exp_f32_e32 v45, v86
v_add_f32_e32 v14, v14, v26
Mv 116,16,100
v_exp_f32_e32 v25, v87
Cv 40,40,26
)" R"(
v_add_f32_e32 v14, v14, v41
Mv 116,20,108
v_exp_f32_e32 v46, v92
v_add_f32_e32 v14, v14, v27
Mv 112,20,104
buffer_load_dwordx4 v3, s[20:23], s56 offen offset:1024 sc1 lds
Cv 41,41,27
Mv 112,24,112
v_exp_f32_e32 v26, v93
v_add_f32_e32 v14, v14, v42
Mv 116,24,116
v_exp_f32_e32 v47, v94
v_add_f32_e32 v14, v14, v28
Mv 116,28,124
v_exp_f32_e32 v27, v95
Cv 42,42,28
Mv 112,28,120
v_add_f32_e32 v14, v14, v43
s_waitcnt vmcnt(23) lgkmcnt(0)
s_barrier
Mz 120,32,64
Rb 0,10,0
v_add_f32_e32 v14, v14, v29
Cv 43,43,29
Mz 124,32,68
Rb 4,10,1024
v_add_f32_e32 v15, v15, v44
v_add_f32_e32 v15, v15, v24
Cv 44,44,24
Mv 124,36,76
Rb 8,10,2048
v_add_f32_e32 v15, v15, v45
v_add_f32_e32 v15, v15, v25
Mv 120,36,72
Rb 12,10,3072
buffer_load_dwordx4 v3, s[20:23], s56 offen offset:2048 sc1 lds
Mv 120,40,80
Rb 16,10,4096
Cv 45,45,25
v_add_f32_e32 v15, v15, v46
Mv 124,40,84
Rb 20,10,5120
v_add_f32_e32 v15, v15, v26
Cv 46,46,26
Mv 124,44,92
Rb 24,10,6144
v_add_f32_e32 v15, v15, v47
v_add_f32_e32 v15, v15, v27
Mv 120,44,88
Rb 28,10,7168
Cv 47,47,27
v_max_f32_e32 v11, v14, v15
Mv 120,48,96
Rb 32,10,8192
v_mov_b32 v24, v14
v_mov_b32 v25, v15
Mv 124,48,100
Rb 36,10,9216
v_permlane32_swap_b32_e32 v24, v25
v_add_f32_e32 v24, v24, v25
v_mov_b32 v25, v24
Mv 124,52,108
Rb 40,10,10240
v_permlane16_swap_b32_e32 v24, v25
v_add_f32_e32 v24, v24, v25
)" R"(
Mv 120,52,104
Rb 44,10,11264
buffer_load_dwordx4 v3, s[20:23], s56 offen offset:3072 sc1 lds
Mv 120,56,112
v_rcp_f32_e32 v24, v24
Mv 124,56,116
s_add_u32 s56, s56, 0x10000
v_max_f32_e32 v11, v11, v24
v_cmp_nge_f32_e32 vcc, s62, v11
Mv 124,60,124
s_waitcnt lgkmcnt(8)
s_cbranch_vccnz .Lzfx0
.Lzfr0:
Mv 120,60,120
v_mov_b32 v12, v14
v_mov_b32 v13, v15
v_mfma_f32_16x16x32_bf16 v[128:131], a[0:3], v[32:35], 0
Rb 48,10,12288
v_mfma_f32_16x16x32_bf16 v[136:139], a[0:3], v[36:39], 0
s_cmp_le_i32 s57, s58
s_cbranch_scc1 .Lzmk1
.Lzmr1:
v_mfma_f32_16x16x32_bf16 v[140:143], a[4:7], v[36:39], 0
Rb 52,10,13312
s_add_u32 m0, s36, 32768
v_mfma_f32_16x16x32_bf16 v[132:135], a[4:7], v[32:35], 0
buffer_load_dwordx4 v2, s[20:23], s55 offen offset:0 sc1 lds
v_mfma_f32_16x16x32_bf16 v[144:147], a[8:11], v[32:35], 0
Rb 56,10,14336
s_and_b32 s92, s2, 7
v_mfma_f32_16x16x32_bf16 v[152:155], a[8:11], v[36:39], 0
s_mul_i32 s92, s92, 14
s_lshr_b32 s82, s2, 3
v_mfma_f32_16x16x32_bf16 v[156:159], a[12:15], v[36:39], 0
Rb 60,10,15360
s_waitcnt lgkmcnt(8)
v_mfma_f32_16x16x32_bf16 v[148:151], a[12:15], v[32:35], 0
buffer_load_dwordx4 v2, s[20:23], s55 offen offset:1024 sc1 lds
v_mfma_f32_16x16x32_bf16 v[160:163], a[16:19], v[32:35], 0
Rb 0,10,16384
s_add_u32 s82, s82, s92
v_mfma_f32_16x16x32_bf16 v[168:171], a[16:19], v[36:39], 0
v_exp_f32_e32 v48, v96
v_mfma_f32_16x16x32_bf16 v[172:175], a[20:23], v[36:39], 0
Rb 4,10,17408
v_mfma_f32_16x16x32_bf16 v[164:167], a[20:23], v[32:35], 0
buffer_load_dwordx4 v2, s[20:23], s55 offen offset:2048 sc1 lds
v_mfma_f32_16x16x32_bf16 v[176:179], a[24:27], v[32:35], 0
Rb 8,10,18432
v_exp_f32_e32 v28, v97
v_mfma_f32_16x16x32_bf16 v[184:187], a[24:27], v[36:39], 0
v_exp_f32_e32 v49, v98
v_mfma_f32_16x16x32_bf16 v[188:191], a[28:31], v[36:39], 0
Rb 12,10,19456
s_waitcnt lgkmcnt(8)
v_mfma_f32_16x16x32_bf16 v[180:183], a[28:31], v[32:35], 0
buffer_load_dwordx4 v2, s[20:23], s55 offen offset:3072 sc1 lds
v_add_f32_e32 v14, v48, v28
v_mfma_f32_16x16x32_bf16 v[192:195], a[32:35], v[32:35], 0
Rb 16,10,20480
Cv 48,48,28
v_mfma_f32_16x16x32_bf16 v[200:203], a[32:35], v[36:39], 0
v_exp_f32_e32 v29, v99
v_add_f32_e32 v14, v14, v49
)" R"(
v_mfma_f32_16x16x32_bf16 v[204:207], a[36:39], v[36:39], 0
Rb 20,10,21504
s_add_u32 m0, s36, 36864
v_mfma_f32_16x16x32_bf16 v[196:199], a[36:39], v[32:35], 0
buffer_load_dwordx4 v3, s[20:23], s55 offen offset:0 sc1 lds
v_mfma_f32_16x16x32_bf16 v[208:211], a[40:43], v[32:35], 0
Rb 24,10,22528
v_exp_f32_e32 v50, v104
v_mfma_f32_16x16x32_bf16 v[216:219], a[40:43], v[36:39], 0
s_cmp_gt_u32 s33, 1
s_cselect_b32 s24, s78, s76
s_cselect_b32 s25, s79, s77
v_mfma_f32_16x16x32_bf16 v[220:223], a[44:47], v[36:39], 0
Rb 28,10,23552
s_waitcnt lgkmcnt(8)
v_mfma_f32_16x16x32_bf16 v[212:215], a[44:47], v[32:35], 0
buffer_load_dwordx4 v3, s[20:23], s55 offen offset:1024 sc1 lds
v_add_f32_e32 v14, v14, v29
v_mfma_f32_16x16x32_bf16 v[224:227], a[48:51], v[32:35], 0
Rb 32,10,24576
s_mov_b32 s27, 0x20000
v_mfma_f32_16x16x32_bf16 v[232:235], a[48:51], v[36:39], 0
s_cmp_lt_u32 s35, 7
s_cselect_b32 s26, 0x188000, 0
Cv 49,49,29
v_mfma_f32_16x16x32_bf16 v[236:239], a[52:55], v[36:39], 0
Rb 36,10,25600
s_mul_i32 s83, s83, 802816
v_mfma_f32_16x16x32_bf16 v[228:231], a[52:55], v[32:35], 0
buffer_load_dwordx4 v3, s[20:23], s55 offen offset:2048 sc1 lds
v_mfma_f32_16x16x32_bf16 v[240:243], a[56:59], v[32:35], 0
Rb 40,10,26624
s_lshl_b32 s92, s82, 5
v_add_f32_e32 v14, v14, v50
v_mfma_f32_16x16x32_bf16 v[248:251], a[56:59], v[36:39], 0
s_add_u32 s83, s83, s92
v_and_b32 v9, 62, v0
v_mul_u32_u24 v9, 0x6f0, v9
v_mfma_f32_16x16x32_bf16 v[252:255], a[60:63], v[36:39], 0
Rb 44,10,27648
s_waitcnt lgkmcnt(8)
v_mfma_f32_16x16x32_bf16 v[244:247], a[60:63], v[32:35], 0
buffer_load_dwordx4 v3, s[20:23], s55 offen offset:3072 sc1 lds
Mp 128,0,40
Rb 48,10,28672
v_add_u32 v9, v9, v1
Mp 136,0,44
v_exp_f32_e32 v24, v105
Mp 140,4,44
Rb 52,10,29696
v_add_f32_e32 v14, v14, v24
Mp 132,4,40
v_exp_f32_e32 v51, v106
Mp 144,8,40
Rb 56,10,30720
v_exp_f32_e32 v25, v107
Mp 152,8,44
v_exp_f32_e32 v52, v100
Mp 156,12,44
Rb 60,10,31744
s_waitcnt lgkmcnt(8)
Mp 148,12,40
v_exp_f32_e32 v26, v101
Cv 50,50,24
)" R"(
Mp 160,16,40
v_exp_f32_e32 v53, v102
Mp 168,16,44
v_exp_f32_e32 v27, v103
v_add_f32_e32 v14, v14, v51
Mp 172,20,44
v_exp_f32_e32 v54, v108
Mp 164,20,40
v_exp_f32_e32 v28, v109
Mp 176,24,40
v_exp_f32_e32 v55, v110
v_add_f32_e32 v14, v14, v25
Mp 184,24,44
v_exp_f32_e32 v29, v111
Mp 188,28,44
Cv 51,51,25
v_add_f32_e32 v15, v52, v26
Mp 180,28,40
Cv 52,52,26
s_waitcnt vmcnt(16) lgkmcnt(0)
s_barrier
Mp 192,32,40
Rb 0,1,0
s_add_u32 m0, s36, 131072
s_nop 0
buffer_load_dwordx4 v9, s[24:27], s83 offen offset:0 lds
Mp 200,32,44
Rb 4,1,1024
v_add_f32_e32 v15, v15, v53
v_add_f32_e32 v15, v15, v27
Mp 204,36,44
Rb 8,1,2048
s_add_u32 s83, s83, 0x1bc00
buffer_load_dwordx4 v9, s[24:27], s83 offen offset:1024 lds
Mp 196,36,40
Rb 12,1,3072
Cv 53,53,27
Mp 208,40,40
Rb 16,1,8192
s_add_u32 s83, s83, 0x1bc00
buffer_load_dwordx4 v9, s[24:27], s83 offen offset:2048 lds
Mp 216,40,44
Rb 20,1,9216
v_add_f32_e32 v15, v15, v54
Mp 220,44,44
Rb 24,1,10240
s_add_u32 s83, s83, 0x1bc00
buffer_load_dwordx4 v9, s[24:27], s83 offen offset:3072 lds
Mp 212,44,40
Rb 28,1,11264
v_add_f32_e32 v15, v15, v28
Cv 54,54,28
Mp 224,48,40
Rb 32,1,4096
s_add_u32 m0, s36, 135168
s_add_u32 s83, s83, 0x1cc00
buffer_load_dwordx4 v9, s[24:27], s83 offen offset:0 lds
Mp 232,48,44
Rb 36,1,5120
v_add_f32_e32 v15, v15, v55
v_add_f32_e32 v15, v15, v29
Mp 236,52,44
Rb 40,1,6144
s_add_u32 s83, s83, 0x1bc00
buffer_load_dwordx4 v9, s[24:27], s83 offen offset:1024 lds
)" R"(
Mp 228,52,40
Rb 44,1,7168
Cv 55,55,29
Mp 240,56,40
s_add_u32 s83, s83, 0x1bc00
buffer_load_dwordx4 v9, s[24:27], s83 offen offset:2048 lds
v_cmp_eq_u32_e32 vcc, s3, v4
s_andn2_b64 s[28:29], -1, vcc
s_cbranch_scc1 .Lzsp8
.Lzok8:
Mp 248,56,44
s_sub_u32 s57, s57, 1
Mp 252,60,44
s_waitcnt lgkmcnt(8)
Mp 244,60,40
s_cmp_gt_i32 s57, s93
s_cbranch_scc1 .LzL
.LzH36:
s_cbranch_execz .LzH15
.LzW:
s_lshl1_add_u32 s57, s35, 2
s_mov_b32 s59, s67
s_add_u32 s56, s89, 0x400000
s_mov_b32 s94, 2
Mz 64,0,224
Rb 48,1,12288
v_lshrrev_b32 v9, 4, v0
Mz 68,0,228
v_lshlrev_b32 v9, 4, v9
v_add_lshl_u32 v9, v9, v0, 3
v_accvgpr_mov_b32 a64, a224
Mv 68,4,236
Rb 52,1,13312
s_add_u32 m0, s36, 98304
v_accvgpr_mov_b32 a65, a225
Mv 64,4,232
buffer_load_dwordx4 v2, s[20:23], s56 offen offset:0 sc1 lds
v_accvgpr_mov_b32 a66, a226
Mv 64,8,240
Rb 56,1,14336
s_add_u32 s92, s36, 131072
v_accvgpr_mov_b32 a67, a227
Mv 68,8,244
v_add_u32 v9, s92, v9
v_accvgpr_mov_b32 a68, a228
s_waitcnt vmcnt(1)
Mv 68,12,252
Rb 60,1,15360
v_accvgpr_mov_b32 a69, a229
s_waitcnt lgkmcnt(8)
Mv 64,12,248
buffer_load_dwordx4 v2, s[20:23], s56 offen offset:1024 sc1 lds
Rt 156,9,0
Mv 64,16,192
Rb 0,1,16384
Rt 158,9,128
v_accvgpr_mov_b32 a70, a230
Mv 68,16,196
Rt 160,9,1024
v_exp_f32_e32 v56, v112
Mv 68,20,204
Rb 4,1,17408
Rt 162,9,1152
v_add_f32_e32 v14, v14, v56
)" R"(
Mv 64,20,200
buffer_load_dwordx4 v2, s[20:23], s56 offen offset:2048 sc1 lds
Rt 164,9,2048
Mv 64,24,208
Rb 8,1,18432
Rt 166,9,2176
v_accvgpr_mov_b32 a71, a231
Mv 68,24,212
Rt 168,9,3072
v_exp_f32_e32 v26, v113
Mv 68,28,220
Rb 12,1,19456
Rt 170,9,3200
Mv 64,28,216
buffer_load_dwordx4 v2, s[20:23], s56 offen offset:3072 sc1 lds
Rt 172,9,4096
v_add_f32_e32 v14, v14, v26
Mz 72,32,224
Rb 16,1,24576
Rt 174,9,4224
Cv 56,56,26
Mz 76,32,228
Rt 176,9,5120
v_exp_f32_e32 v57, v114
Mv 76,36,236
Rb 20,1,25600
s_add_u32 m0, s36, 102400
Rt 178,9,5248
Mv 72,36,232
buffer_load_dwordx4 v3, s[20:23], s56 offen offset:0 sc1 lds
Rt 180,9,6144
Mv 72,40,240
Rb 24,1,26624
Rt 182,9,6272
v_add_f32_e32 v14, v14, v57
Mv 76,40,244
v_exp_f32_e32 v27, v115
v_accvgpr_mov_b32 a72, a232
Mv 76,44,252
Rb 28,1,27648
v_add_f32_e32 v14, v14, v27
Mv 72,44,248
buffer_load_dwordx4 v3, s[20:23], s56 offen offset:1024 sc1 lds
Cv 57,57,27
Mv 72,48,192
Rb 32,1,20480
v_and_b32 v9, 14, v0
v_accvgpr_mov_b32 a73, a233
Mv 76,48,196
v_min_u32 v9, 1, v9
v_lshlrev_b32 v9, 8, v9
v_accvgpr_mov_b32 a74, a234
Mv 76,52,204
Rb 36,1,21504
v_and_or_b32 v9, v0, 48, v9
v_accvgpr_mov_b32 a75, a235
Mv 72,52,200
buffer_load_dwordx4 v3, s[20:23], s56 offen offset:2048 sc1 lds
v_accvgpr_mov_b32 a76, a236
Mv 72,56,208
Rb 40,1,22528
v_lshlrev_b32 v9, 1, v9
v_accvgpr_mov_b32 a77, a237
Mv 76,56,212
)" R"(
v_and_or_b32 v9, v1, 16, v9
s_lshl_b32 s92, s33, 7
v_accvgpr_mov_b32 a78, a238
Mv 76,60,220
Rb 44,1,23552
v_accvgpr_mov_b32 a79, a239
s_waitcnt lgkmcnt(5)
Mv 72,60,216
v_mfma_f32_16x16x32_bf16 a[184:187], a[156:159], a[128:131], 0
buffer_load_dwordx4 v3, s[20:23], s56 offen offset:3072 sc1 lds
v_exp_f32_e32 v58, v120
Mz 80,0,224
.LzH37:
s_cbranch_execz .LzH19
Ma 184,160,132
Rb 48,1,28672
s_add_u32 s92, s92, 162816
v_add_f32_e32 v14, v14, v58
Mz 84,0,228
Ma 184,164,136
s_add_u32 s56, s56, 0x10000
v_add_u32 v9, s92, v9
v_accvgpr_mov_b32 a80, a240
Mv 84,4,236
Ma 184,168,140
Rb 52,1,29696
v_exp_f32_e32 v28, v121
Mv 80,4,232
Ma 184,172,144
v_exp_f32_e32 v59, v122
v_add_f32_e32 v14, v14, v28
Mv 80,8,240
Ma 184,176,148
Rb 56,1,30720
s_mov_b32 s24, s70
s_mov_b32 s25, s71
Mv 84,8,244
Ma 184,180,152
s_mul_i32 s83, s82, 14336
s_lshl_b32 s92, s33, 12
Cv 58,58,28
Mv 84,12,252
Rb 60,1,31744
v_add_f32_e32 v14, v14, v59
s_waitcnt lgkmcnt(8)
Mv 80,12,248
s_add_u32 s83, s83, s92
s_add_u32 m0, s92, 131072
v_accvgpr_mov_b32 a81, a241
Mv 80,16,192
v_exp_f32_e32 v29, v123
v_accvgpr_mov_b32 a82, a242
Mv 84,16,196
v_exp_f32_e32 v60, v116
v_add_f32_e32 v14, v14, v29
Mv 84,20,204
ds_write_b128 v9, a[184:187]
v_and_b32 v9, 0x70, v9
Cv 59,59,29
Mv 80,20,200
v_add_u32 v9, 0x27c00, v9
v_exp_f32_e32 v24, v117
Mv 80,24,208
v_exp_f32_e32 v61, v118
)" R"(
v_add_f32_e32 v15, v15, v60
Mv 84,24,212
v_exp_f32_e32 v25, v119
v_add_f32_e32 v15, v15, v24
Mv 84,28,220
v_exp_f32_e32 v62, v124
Cv 60,60,24
Mv 80,28,216
v_exp_f32_e32 v26, v125
s_waitcnt lgkmcnt(0)
s_barrier
Mz 88,32,224
Rb 0,8,0
v_exp_f32_e32 v63, v126
Mz 92,32,228
Rb 4,8,16384
v_exp_f32_e32 v27, v127
Mv 92,36,236
Rb 8,8,1024
v_add_f32_e32 v15, v15, v61
v_add_f32_e32 v15, v15, v25
Mv 88,36,232
Rb 12,8,17408
Cv 61,61,25
v_add_f32_e32 v15, v15, v62
Mv 88,40,240
Rb 16,8,2048
buffer_load_dwordx4 v1, s[24:27], s83 offen offset:0 lds
Mv 92,40,244
Rb 20,8,18432
v_add_f32_e32 v15, v15, v26
Cv 62,62,26
Mv 92,44,252
Rb 24,8,3072
v_add_f32_e32 v15, v15, v63
v_add_f32_e32 v15, v15, v27
Mv 88,44,248
Rb 28,8,19456
Cv 63,63,27
v_max_f32_e32 v11, v14, v15
Mv 88,48,192
Rb 32,8,4096
v_cmp_nge_f32_e32 vcc, s62, v11
s_add_u32 s55, s55, 0x10000
Mv 92,48,196
Rb 36,8,20480
buffer_load_dwordx4 v1, s[24:27], s83 offen offset:1024 lds
Mv 92,52,204
Rb 40,8,5120
s_cmp_eq_u32 s57, 2
s_cselect_b32 s55, s59, s55
Mv 88,52,200
Rb 44,8,21504
v_accvgpr_mov_b32 a83, a243
v_accvgpr_mov_b32 a84, a244
Mv 88,56,208
buffer_load_dwordx4 v1, s[24:27], s83 offen offset:2048 lds
v_accvgpr_mov_b32 a85, a245
Mv 92,56,212
buffer_load_dwordx4 v1, s[24:27], s83 offen offset:3072 lds
v_accvgpr_mov_b32 a86, a246
Mv 92,60,220
v_accvgpr_mov_b32 a87, a247
s_waitcnt lgkmcnt(8)
s_cbranch_vccnz .Lzfx3
)" R"(
.Lzfr3:
Mv 88,60,216
v_add_f32_e32 v12, v12, v14
v_add_f32_e32 v13, v13, v15
v_mov_b32 v14, v16
Mp 128,0,48
Rb 48,8,6144
v_mov_b32 v15, v20
Mp 136,0,52
buffer_store_dwordx4 v[12:15], v1, s[20:23], s66 offen sc0 sc1
s_cmp_le_i32 s57, s58
s_cbranch_scc1 .Lzmk4
.Lzmr4:
Mp 136,4,60
Rb 52,8,22528
s_add_u32 m0, s36, 0
v_accvgpr_mov_b32 a88, a248
Mp 128,4,56
buffer_load_dwordx4 v2, s[20:23], s55 offen offset:0 sc1 lds
v_accvgpr_mov_b32 a89, a249
Mp 132,8,48
Rb 56,8,7168
v_exp_f32_e32 v32, v64
Mp 140,8,52
v_exp_f32_e32 v28, v65
v_accvgpr_mov_b32 a90, a250
Mp 140,12,60
Rb 60,8,23552
v_exp_f32_e32 v33, v66
s_waitcnt lgkmcnt(8)
Mp 132,12,56
v_exp_f32_e32 v29, v67
v_add_f32_e32 v14, v32, v28
Mp 144,16,48
Rb 0,8,8192
v_exp_f32_e32 v34, v72
Mp 152,16,52
v_exp_f32_e32 v24, v73
v_exp_f32_e32 v35, v74
Mp 152,20,60
Rb 4,8,24576
v_exp_f32_e32 v25, v75
Mp 144,20,56
buffer_load_dwordx4 v2, s[20:23], s55 offen offset:1024 sc1 lds
Cv 32,32,28
Mp 148,24,48
Rb 8,8,9216
Cv 128,128,129
Cv 129,130,131
v_add_f32_e32 v14, v14, v33
Mp 156,24,52
Cv 130,132,133
Cv 131,134,135
v_add_f32_e32 v14, v14, v29
Mp 156,28,60
Rb 12,8,25600
Cv 136,136,137
s_waitcnt lgkmcnt(8)
Mp 148,28,56
Cv 137,138,139
Cv 138,140,141
Cv 139,142,143
Cv 33,33,29
Mp 160,32,48
)" R"(
Rb 16,8,10240
v_exp_f32_e32 v36, v68
Mp 168,32,52
v_exp_f32_e32 v26, v69
v_add_f32_e32 v14, v14, v34
Mp 168,36,60
Rb 20,8,26624
v_exp_f32_e32 v37, v70
v_add_f32_e32 v14, v14, v24
Mp 160,36,56
buffer_load_dwordx4 v2, s[20:23], s55 offen offset:2048 sc1 lds
Cv 144,144,145
Mp 164,40,48
Rb 24,8,11264
Cv 145,146,147
Cv 34,34,24
Mp 172,40,52
buffer_store_dwordx4 v[128:131], v2, s[20:23], s64 offen offset:0 sc0 sc1
Cv 146,148,149
Mp 172,44,60
Rb 28,8,27648
v_exp_f32_e32 v27, v71
s_waitcnt lgkmcnt(8)
Mp 164,44,56
Cv 147,150,151
Cv 152,152,153
Cv 153,154,155
Mp 176,48,48
Rb 32,8,12288
Cv 154,156,157
Cv 155,158,159
Mp 184,48,52
v_exp_f32_e32 v38, v76
v_exp_f32_e32 v28, v77
Mp 184,52,60
Rb 36,8,28672
v_exp_f32_e32 v39, v78
Mp 176,52,56
buffer_load_dwordx4 v2, s[20:23], s55 offen offset:3072 sc1 lds
v_add_f32_e32 v14, v14, v35
Mp 180,56,48
Rb 40,8,13312
Cv 160,160,161
Cv 161,162,163
v_add_f32_e32 v14, v14, v25
Mp 188,56,52
buffer_store_dwordx4 v[136:139], v2, s[20:23], s64 offen offset:1024 sc0 sc1
Cv 35,35,25
Mp 188,60,60
Rb 44,8,29696
Cv 162,164,165
s_waitcnt lgkmcnt(8)
Mp 180,60,56
Cv 163,166,167
Cv 168,168,169
Cv 169,170,171
v_add_f32_e32 v15, v36, v26
Mp 192,0,48
Rb 48,8,14336
Cv 170,172,173
Cv 36,36,26
Mp 200,0,52
buffer_store_dwordx4 v[144:147], v2, s[20:23], s64 offen offset:2048 sc0 sc1
Cv 171,174,175
)" R"(
Mp 200,4,60
Rb 52,8,30720
s_add_u32 m0, s36, 4096
v_exp_f32_e32 v29, v79
Mp 192,4,56
buffer_load_dwordx4 v3, s[20:23], s55 offen offset:0 sc1 lds
Cv 176,176,177
Mp 196,8,48
Rb 56,8,15360
Cv 177,178,179
v_add_f32_e32 v15, v15, v37
Mp 204,8,52
Cv 178,180,181
Cv 179,182,183
Cv 184,184,185
Mp 204,12,60
Rb 60,8,31744
v_add_f32_e32 v15, v15, v27
Cv 37,37,27
s_waitcnt lgkmcnt(8)
Mp 196,12,56
buffer_store_dwordx4 v[152:155], v2, s[20:23], s64 offen offset:3072 sc0 sc1
Cv 185,186,187
Mp 208,16,48
ds_read_b128 v[132:135], v9 offset:0
Cv 186,188,189
Cv 187,190,191
Mp 216,16,52
ds_read_b128 v[140:143], v9 offset:128
v_add_f32_e32 v15, v15, v38
v_add_f32_e32 v15, v15, v28
Cv 38,38,28
Mp 216,20,60
ds_read_b128 v[148:151], v9 offset:256
v_add_f32_e32 v15, v15, v39
v_add_f32_e32 v15, v15, v29
Mp 208,20,56
buffer_load_dwordx4 v3, s[20:23], s55 offen offset:1024 sc1 lds
ds_read_b128 v[156:159], v9 offset:384
Mp 212,24,48
s_add_u32 s50, s50, 1024
s_min_u32 s50, s50, s60
Cv 192,192,193
Cv 39,39,29
Mp 220,24,52
Cv 193,194,195
Cv 194,196,197
s_sub_u32 s57, s57, 1
Mp 220,28,60
buffer_store_dwordx4 v[160:163], v3, s[20:23], s64 offen offset:0 sc0 sc1
Cv 195,198,199
Mp 212,28,56
Cv 200,200,201
v_mov_b32 v16, 0
s_waitcnt lgkmcnt(0)
s_barrier
Mp 224,32,48
Rb 0,1,32768
v_add_f32_e32 v132, v132, v140
v_mov_b32 v17, 0
Mp 232,32,52
Rb 4,1,33792
v_add_f32_e32 v133, v133, v141
Cv 201,202,203
Mp 232,36,60
)" R"(
Rb 8,1,34816
v_add_f32_e32 v134, v134, v142
v_mov_b32 v18, 0
v_mov_b32 v19, 0
Mp 224,36,56
Rb 12,1,35840
buffer_load_dwordx4 v3, s[20:23], s55 offen offset:2048 sc1 lds
Mp 228,40,48
Rb 16,1,40960
v_add_f32_e32 v135, v135, v143
buffer_store_dwordx4 v[168:171], v3, s[20:23], s64 offen offset:1024 sc0 sc1
Mp 236,40,52
Rb 20,1,41984
v_add_f32_e32 v148, v148, v156
Cv 202,204,205
Mp 236,44,60
Rb 24,1,43008
v_add_f32_e32 v149, v149, v157
v_mov_b32 v20, 0
v_mov_b32 v21, 0
Mp 228,44,56
Rb 28,1,44032
v_add_f32_e32 v150, v150, v158
Cv 203,206,207
Mp 240,48,48
Rb 32,1,36864
v_add_f32_e32 v151, v151, v159
Cv 208,208,209
Mp 248,48,52
Rb 36,1,37888
Cv 132,132,132
v_mov_b32 v22, 0
v_mov_b32 v23, 0
Mp 248,52,60
Rb 40,1,38912
v_lshlrev_b32 v132, 16, v132
buffer_store_dwordx4 v[176:179], v3, s[20:23], s64 offen offset:2048 sc0 sc1
Mp 240,52,56
Rb 44,1,39936
buffer_load_dwordx4 v3, s[20:23], s55 offen offset:3072 sc1 lds
Mp 244,56,48
Cv 148,148,148
v_lshlrev_b32 v148, 16, v148
Cv 209,210,211
Mp 252,56,52
Cv 133,133,133
v_lshlrev_b32 v133, 16, v133
v_accvgpr_mov_b32 a91, a251
Mp 252,60,60
Cv 149,149,149
Cv 210,212,213
s_waitcnt lgkmcnt(8)
Mp 244,60,56
v_lshlrev_b32 v149, 16, v149
Cv 134,134,134
v_lshlrev_b32 v134, 16, v134
Mz 96,0,224
Rb 48,1,45056
buffer_load_dword v4, v5, s[20:23], s50 offen offset:0 sc0 sc1
buffer_store_dwordx4 v[184:187], v3, s[20:23], s64 offen offset:3072 sc0 sc1
Mz 100,0,228
Cv 150,150,150
v_lshlrev_b32 v150, 16, v150
Cv 211,214,215
)" R"(
Mv 100,4,236
Rb 52,1,46080
s_add_u32 m0, s36, 65536
v_exp_f32_e32 v40, v80
Mv 96,4,232
buffer_load_dwordx4 v2, s[20:23], s56 offen offset:0 sc1 lds
Cv 216,216,217
v_add_f32_e32 v14, v14, v40
Mv 96,8,240
Rb 56,1,47104
Cv 135,135,135
v_accvgpr_mov_b32 a92, a252
Mv 100,8,244
v_lshlrev_b32 v135, 16, v135
Cv 151,151,151
Cv 217,218,219
Mv 100,12,252
Rb 60,1,48128
s_add_u32 s64, s64, 0x10000
buffer_store_dwordx4 v[192:195], v2, s[20:23], s64 offen offset:0 sc0 sc1
s_waitcnt lgkmcnt(8)
Mv 96,12,248
v_lshlrev_b32 v151, 16, v151
v_mul_f32_e32 v140, 0xbfb8aa3b, v132
Cv 218,220,221
Mv 96,16,192
Rb 0,1,49152
v_mul_f32_e32 v141, 0xbfb8aa3b, v133
Cv 219,222,223
Mv 100,16,196
v_exp_f32_e32 v140, v140
v_exp_f32_e32 v24, v81
Mv 100,20,204
Rb 4,1,50176
v_mul_f32_e32 v142, 0xbfb8aa3b, v134
Cv 224,224,225
v_add_f32_e32 v14, v14, v24
Mv 96,20,200
buffer_load_dwordx4 v2, s[20:23], s56 offen offset:1024 sc1 lds
v_exp_f32_e32 v41, v82
Mv 96,24,208
Rb 8,1,51200
v_mul_f32_e32 v143, 0xbfb8aa3b, v135
buffer_store_dwordx4 v[200:203], v2, s[20:23], s64 offen offset:1024 sc0 sc1
Mv 100,24,212
v_exp_f32_e32 v141, v141
v_exp_f32_e32 v25, v83
Mv 100,28,220
Rb 12,1,52224
Cv 225,226,227
Cv 40,40,24
s_waitcnt lgkmcnt(8)
Mv 96,28,216
v_exp_f32_e32 v142, v142
v_add_f32_e32 v140, 1.0, v140
v_add_f32_e32 v14, v14, v41
Mz 104,32,224
Rb 16,1,57344
v_add_f32_e32 v141, 1.0, v141
v_add_f32_e32 v14, v14, v25
Mz 108,32,228
v_exp_f32_e32 v143, v143
Cv 226,228,229
Cv 41,41,25
)" R"(
Mv 108,36,236
Rb 20,1,58368
v_add_f32_e32 v142, 1.0, v142
buffer_store_dwordx4 v[208:211], v2, s[20:23], s64 offen offset:2048 sc0 sc1
Mv 104,36,232
buffer_load_dwordx4 v2, s[20:23], s56 offen offset:2048 sc1 lds
Cv 227,230,231
Mv 104,40,240
Rb 24,1,59392
v_add_f32_e32 v143, 1.0, v143
v_accvgpr_mov_b32 a93, a253
Mv 108,40,244
v_rcp_f32_e32 v140, v140
Cv 232,232,233
Mv 108,44,252
Rb 28,1,60416
v_exp_f32_e32 v42, v88
s_waitcnt lgkmcnt(8)
Mv 104,44,248
v_rcp_f32_e32 v141, v141
Cv 233,234,235
v_add_f32_e32 v14, v14, v42
Mv 104,48,192
Rb 32,1,53248
v_mul_f32_e32 v132, v132, v140
buffer_store_dwordx4 v[216:219], v2, s[20:23], s64 offen offset:3072 sc0 sc1
Mv 108,48,196
v_rcp_f32_e32 v142, v142
v_exp_f32_e32 v26, v89
Mv 108,52,204
Rb 36,1,54272
v_mul_f32_e32 v133, v133, v141
Cv 234,236,237
v_add_f32_e32 v14, v14, v26
Mv 104,52,200
buffer_load_dwordx4 v2, s[20:23], s56 offen offset:3072 sc1 lds
v_exp_f32_e32 v43, v90
Mv 104,56,208
Rb 40,1,55296
v_mul_f32_e32 v134, v134, v142
Cv 235,238,239
Mv 108,56,212
v_rcp_f32_e32 v143, v143
v_exp_f32_e32 v27, v91
Mv 108,60,220
Rb 44,1,56320
buffer_store_dwordx4 v[224:227], v3, s[20:23], s64 offen offset:0 sc0 sc1
s_waitcnt lgkmcnt(8)
Mv 104,60,216
v_mul_f32_e32 v135, v135, v143
Cv 132,132,132
v_lshlrev_b32 v132, 16, v132
Cv 42,42,26
Mz 112,0,224
Rb 48,1,61440
Cv 133,133,133
v_add_f32_e32 v14, v14, v43
Mz 116,0,228
v_lshlrev_b32 v133, 16, v133
Cv 134,134,134
Cv 240,240,241
v_add_f32_e32 v14, v14, v27
Mv 116,4,236
Rb 52,1,62464
)" R"(
s_add_u32 m0, s36, 69632
v_exp_f32_e32 v44, v84
Mv 112,4,232
buffer_load_dwordx4 v3, s[20:23], s56 offen offset:0 sc1 lds
Cv 241,242,243
Cv 43,43,27
Mv 112,8,240
Rb 56,1,63488
v_lshlrev_b32 v134, 16, v134
buffer_store_dwordx4 v[232:235], v3, s[20:23], s64 offen offset:1024 sc0 sc1
Mv 116,8,244
Cv 135,135,135
v_lshlrev_b32 v135, 16, v135
Cv 242,244,245
v_add_f32_e32 v15, v15, v44
Mv 116,12,252
Rb 60,1,64512
v_exp_f32_e32 v28, v85
s_waitcnt lgkmcnt(8)
Mv 112,12,248
v_mul_f32_e32 v132, v132, v148
v_mul_f32_e32 v133, v133, v149
Cv 243,246,247
v_add_f32_e32 v15, v15, v28
Mv 112,16,192
v_mul_f32_e32 v134, v134, v150
v_mul_f32_e32 v135, v135, v151
Cv 248,248,249
Mv 116,16,196
Cv 140,132,133
Cv 141,134,135
v_exp_f32_e32 v45, v86
Mv 116,20,204
v_accvgpr_write_b32 a188, v140
v_accvgpr_write_b32 a189, v141
buffer_store_dwordx4 v[240:243], v3, s[20:23], s64 offen offset:2048 sc0 sc1
Mv 112,20,200
buffer_load_dwordx4 v3, s[20:23], s56 offen offset:1024 sc1 lds
v_exp_f32_e32 v29, v87
Mv 112,24,208
v_lshrrev_b32 v156, 2, v0
v_mul_u32_u24 v156, 864, v156
Cv 249,250,251
Mv 116,24,212
v_lshl_add_u32 v156, v0, 3, v156
s_mul_i32 s92, s33, 224
v_exp_f32_e32 v46, v92
Mv 116,28,220
s_add_u32 s92, s92, 131072
v_add_u32 v156, s92, v156
Cv 250,252,253
Cv 44,44,28
Mv 112,28,216
Cv 251,254,255
v_add_f32_e32 v15, v15, v45
s_waitcnt vmcnt(31) lgkmcnt(0)
s_barrier
Mz 120,32,224
Rb 0,8,32768
Rt 156,156,0
buffer_store_dwordx4 v[248:251], v3, s[20:23], s64 offen offset:3072 sc0 sc1
Mz 124,32,228
Rb 4,8,33792
Rt 158,156,32
v_exp_f32_e32 v24, v93
)" R"(
Mv 124,36,236
Rb 8,8,34816
Rt 160,156,64
v_exp_f32_e32 v47, v94
Mv 120,36,232
Rb 12,8,35840
buffer_load_dwordx4 v3, s[20:23], s56 offen offset:2048 sc1 lds
Rt 162,156,96
Mv 120,40,240
Rb 16,8,36864
Rt 164,156,128
v_add_f32_e32 v15, v15, v29
Mv 124,40,244
Rb 20,8,37888
Rt 166,156,160
v_exp_f32_e32 v25, v95
Mv 124,44,252
Rb 24,8,38912
Rt 168,156,192
Cv 45,45,29
v_add_f32_e32 v15, v15, v46
Mv 120,44,248
Rb 28,8,39936
v_add_f32_e32 v15, v15, v24
Cv 46,46,24
v_add_f32_e32 v15, v15, v47
Mv 120,48,192
Rb 32,8,40960
v_add_f32_e32 v15, v15, v25
Cv 47,47,25
Mv 124,48,196
Rb 36,8,41984
v_max_f32_e32 v11, v14, v15
v_mov_b32 v24, v14
v_mov_b32 v25, v15
Mv 124,52,204
Rb 40,8,43008
v_permlane32_swap_b32_e32 v24, v25
v_add_f32_e32 v24, v24, v25
v_mov_b32 v25, v24
Mv 120,52,200
Rb 44,8,44032
buffer_load_dwordx4 v3, s[20:23], s56 offen offset:3072 sc1 lds
v_permlane16_swap_b32_e32 v24, v25
Mv 120,56,208
v_add_f32_e32 v24, v24, v25
v_rcp_f32_e32 v24, v24
.LzH38:
s_cbranch_execz .LzH20
Mv 124,56,212
s_add_u32 s56, s56, 0x10000
v_max_f32_e32 v11, v11, v24
v_cmp_nge_f32_e32 vcc, s62, v11
s_add_u32 s55, s55, 0x10000
Mv 124,60,220
s_cmp_eq_u32 s57, 2
s_cselect_b32 s55, s59, s55
s_waitcnt lgkmcnt(12)
s_cbranch_vccnz .Lzfx4
.Lzfr4:
Mv 120,60,216
v_mov_b32 v12, v14
v_mov_b32 v13, v15
v_accvgpr_mov_b32 a94, a254
)" R"(
v_mfma_f32_16x16x32_bf16 v[128:131], a[0:3], v[32:35], 0
Rb 48,8,45056
v_accvgpr_mov_b32 a95, a255
v_mfma_f32_16x16x32_bf16 v[136:139], a[0:3], v[36:39], 0
v_accvgpr_mov_b32 a96, a192
s_cmp_le_i32 s57, s58
s_cbranch_scc1 .Lzmk5
.Lzmr5:
v_mfma_f32_16x16x32_bf16 v[140:143], a[4:7], v[36:39], 0
Rb 52,8,46080
s_add_u32 m0, s36, 32768
v_accvgpr_mov_b32 a97, a193
v_mfma_f32_16x16x32_bf16 v[132:135], a[4:7], v[32:35], 0
buffer_load_dwordx4 v2, s[20:23], s55 offen offset:0 sc1 lds
v_accvgpr_mov_b32 a98, a194
v_mfma_f32_16x16x32_bf16 v[144:147], a[8:11], v[32:35], 0
Rb 56,8,47104
v_and_b32 v11, 14, v0
v_accvgpr_mov_b32 a99, a195
v_mfma_f32_16x16x32_bf16 v[152:155], a[8:11], v[36:39], 0
v_min_u32 v11, 1, v11
v_lshlrev_b32 v11, 13, v11
v_accvgpr_mov_b32 a100, a196
v_mfma_f32_16x16x32_bf16 v[156:159], a[12:15], v[36:39], 0
Rb 60,8,48128
v_accvgpr_mov_b32 a101, a197
s_waitcnt lgkmcnt(8)
v_mfma_f32_16x16x32_bf16 v[148:151], a[12:15], v[32:35], 0
v_mfma_f32_16x16x16_bf16 a[128:131], a[156:157], a[188:189], 0
v_and_or_b32 v11, v0, 1, v11
v_mul_u32_u24 v11, 0x31000, v11
v_accvgpr_mov_b32 a102, a198
v_mfma_f32_16x16x32_bf16 v[160:163], a[16:19], v[32:35], 0
v_mfma_f32_16x16x16_bf16 a[132:135], a[158:159], a[188:189], 0
Rb 0,8,49152
v_and_or_b32 v11, v0, 48, v11
v_accvgpr_mov_b32 a103, a199
v_mfma_f32_16x16x32_bf16 v[168:171], a[16:19], v[36:39], 0
v_mfma_f32_16x16x16_bf16 a[136:139], a[160:161], a[188:189], 0
s_mul_i32 s83, s33, 50176
s_lshl_b32 s92, s82, 6
v_accvgpr_mov_b32 a104, a200
v_mfma_f32_16x16x32_bf16 v[172:175], a[20:23], v[36:39], 0
v_mfma_f32_16x16x16_bf16 a[140:143], a[162:163], a[188:189], 0
Rb 4,8,50176
s_add_u32 s83, s83, s92
v_accvgpr_mov_b32 a105, a201
v_mfma_f32_16x16x32_bf16 v[164:167], a[20:23], v[32:35], 0
v_mfma_f32_16x16x16_bf16 a[144:147], a[164:165], a[188:189], 0
buffer_load_dwordx4 v2, s[20:23], s55 offen offset:1024 sc1 lds
v_accvgpr_mov_b32 a106, a202
v_mfma_f32_16x16x32_bf16 v[176:179], a[24:27], v[32:35], 0
v_mfma_f32_16x16x16_bf16 a[148:151], a[166:167], a[188:189], 0
Rb 8,8,51200
s_add_u32 s83, s83, 0x1558000
v_accvgpr_mov_b32 a107, a203
v_mfma_f32_16x16x32_bf16 v[184:187], a[24:27], v[36:39], 0
v_mfma_f32_16x16x16_bf16 a[152:155], a[168:169], a[188:189], 0
v_exp_f32_e32 v48, v96
v_accvgpr_mov_b32 a108, a204
v_mfma_f32_16x16x32_bf16 v[188:191], a[28:31], v[36:39], 0
Rb 12,8,52224
v_accvgpr_mov_b32 a109, a205
s_waitcnt lgkmcnt(8)
)" R"(
v_mfma_f32_16x16x32_bf16 v[180:183], a[28:31], v[32:35], 0
v_exp_f32_e32 v26, v97
v_accvgpr_mov_b32 a110, a206
v_mfma_f32_16x16x32_bf16 v[192:195], a[32:35], v[32:35], 0
Rb 16,8,53248
v_add_f32_e32 v14, v48, v26
v_mfma_f32_16x16x32_bf16 v[200:203], a[32:35], v[36:39], 0
v_exp_f32_e32 v49, v98
Cv 48,48,26
v_mfma_f32_16x16x32_bf16 v[204:207], a[36:39], v[36:39], 0
Rb 20,8,54272
v_add_f32_e32 v14, v14, v49
v_accvgpr_mov_b32 a111, a207
v_mfma_f32_16x16x32_bf16 v[196:199], a[36:39], v[32:35], 0
buffer_load_dwordx4 v2, s[20:23], s55 offen offset:2048 sc1 lds
v_accvgpr_mov_b32 a112, a208
v_mfma_f32_16x16x32_bf16 v[208:211], a[40:43], v[32:35], 0
Rb 24,8,55296
v_exp_f32_e32 v27, v99
v_mfma_f32_16x16x32_bf16 v[216:219], a[40:43], v[36:39], 0
buffer_store_dwordx4 a[128:131], v11, s[20:23], s83 offen sc0 sc1
v_accvgpr_mov_b32 a113, a209
v_mfma_f32_16x16x32_bf16 v[220:223], a[44:47], v[36:39], 0
Rb 28,8,56320
v_accvgpr_mov_b32 a114, a210
s_waitcnt lgkmcnt(8)
v_mfma_f32_16x16x32_bf16 v[212:215], a[44:47], v[32:35], 0
s_add_u32 s83, s83, 7168
v_exp_f32_e32 v50, v104
v_mfma_f32_16x16x32_bf16 v[224:227], a[48:51], v[32:35], 0
Rb 32,8,57344
v_add_f32_e32 v14, v14, v27
v_accvgpr_mov_b32 a115, a211
v_mfma_f32_16x16x32_bf16 v[232:235], a[48:51], v[36:39], 0
v_exp_f32_e32 v28, v105
Cv 49,49,27
v_mfma_f32_16x16x32_bf16 v[236:239], a[52:55], v[36:39], 0
Rb 36,8,58368
v_add_f32_e32 v14, v14, v50
v_accvgpr_mov_b32 a116, a212
v_mfma_f32_16x16x32_bf16 v[228:231], a[52:55], v[32:35], 0
buffer_load_dwordx4 v2, s[20:23], s55 offen offset:3072 sc1 lds
v_accvgpr_mov_b32 a117, a213
v_mfma_f32_16x16x32_bf16 v[240:243], a[56:59], v[32:35], 0
Rb 40,8,59392
v_exp_f32_e32 v51, v106
v_mfma_f32_16x16x32_bf16 v[248:251], a[56:59], v[36:39], 0
buffer_store_dwordx4 a[132:135], v11, s[20:23], s83 offen sc0 sc1
v_accvgpr_mov_b32 a118, a214
v_mfma_f32_16x16x32_bf16 v[252:255], a[60:63], v[36:39], 0
Rb 44,8,60416
v_accvgpr_mov_b32 a119, a215
s_waitcnt lgkmcnt(8)
v_mfma_f32_16x16x32_bf16 v[244:247], a[60:63], v[32:35], 0
s_add_u32 s83, s83, 7168
v_exp_f32_e32 v29, v107
Mp 128,0,40
Rb 48,8,61440
v_add_f32_e32 v14, v14, v28
Mp 136,0,44
buffer_store_dwordx4 a[136:139], v11, s[20:23], s83 offen sc0 sc1
s_add_u32 s83, s83, 7168
Mp 140,4,44
Rb 52,8,62464
)" R"(
s_add_u32 m0, s36, 36864
v_accvgpr_mov_b32 a120, a216
Mp 132,4,40
buffer_load_dwordx4 v3, s[20:23], s55 offen offset:0 sc1 lds
v_accvgpr_mov_b32 a121, a217
Mp 144,8,40
Rb 56,8,63488
v_exp_f32_e32 v52, v100
Mp 152,8,44
v_exp_f32_e32 v24, v101
v_accvgpr_mov_b32 a122, a218
Mp 156,12,44
Rb 60,8,64512
v_accvgpr_mov_b32 a123, a219
s_waitcnt lgkmcnt(8)
Mp 148,12,40
buffer_store_dwordx4 a[140:143], v11, s[20:23], s83 offen sc0 sc1
Cv 50,50,28
Mp 160,16,40
s_add_u32 s83, s83, 7168
v_add_f32_e32 v14, v14, v51
v_accvgpr_mov_b32 a124, a220
Mp 168,16,44
v_exp_f32_e32 v53, v102
v_add_f32_e32 v14, v14, v29
Mp 172,20,44
v_exp_f32_e32 v25, v103
v_accvgpr_mov_b32 a125, a221
Mp 164,20,40
buffer_load_dwordx4 v3, s[20:23], s55 offen offset:1024 sc1 lds
v_accvgpr_mov_b32 a126, a222
Mp 176,24,40
v_exp_f32_e32 v54, v108
Cv 51,51,29
Mp 184,24,44
buffer_store_dwordx4 a[144:147], v11, s[20:23], s83 offen sc0 sc1
v_accvgpr_mov_b32 a127, a223
Mp 188,28,44
s_add_u32 s83, s83, 7168
v_add_f32_e32 v15, v52, v24
Mp 180,28,40
Cv 52,52,24
s_waitcnt vmcnt(13) lgkmcnt(0)
s_barrier
buffer_store_dword v6, v7, s[20:23], s65 offen offset:0 sc0 sc1
Mp 192,32,40
Rb 0,1,0
v_add_f32_e32 v15, v15, v53
Mp 200,32,44
Rb 4,1,1024
v_exp_f32_e32 v26, v109
Mp 204,36,44
Rb 8,1,2048
v_add_f32_e32 v15, v15, v25
Mp 196,36,40
Rb 12,1,3072
buffer_load_dwordx4 v3, s[20:23], s55 offen offset:2048 sc1 lds
Mp 208,40,40
Rb 16,1,8192
v_exp_f32_e32 v55, v110
Mp 216,40,44
Rb 20,1,9216
Cv 53,53,25
Mp 220,44,44
Rb 24,1,10240
)" R"(
buffer_store_dwordx4 a[148:151], v11, s[20:23], s83 offen sc0 sc1
Mp 212,44,40
Rb 28,1,11264
s_add_u32 s83, s83, 7168
v_add_f32_e32 v15, v15, v54
Mp 224,48,40
Rb 32,1,4096
v_add_f32_e32 v15, v15, v26
Mp 232,48,44
Rb 36,1,5120
v_exp_f32_e32 v27, v111
Mp 236,52,44
Rb 40,1,6144
Cv 54,54,26
Mp 228,52,40
Rb 44,1,7168
buffer_load_dwordx4 v3, s[20:23], s55 offen offset:3072 sc1 lds
Mp 240,56,40
v_cmp_eq_u32_e32 vcc, s3, v4
s_andn2_b64 s[28:29], -1, vcc
s_cbranch_scc1 .Lzsp10
.Lzok10:
Mp 248,56,44
buffer_store_dwordx4 a[152:155], v11, s[20:23], s83 offen sc0 sc1
Mp 252,60,44
v_add_f32_e32 v15, v15, v55
s_waitcnt lgkmcnt(8)
Mp 244,60,40
v_add_f32_e32 v15, v15, v27
Cv 55,55,27
s_sub_u32 s57, s57, 1
s_cmp_gt_i32 s57, s93
s_cbranch_scc1 .LzL
.LzH39:
s_cbranch_execz .LzHR
.LzDA:
s_waitcnt vmcnt(8) lgkmcnt(0)
s_barrier
Rb 0,8,0
Rb 4,8,16384
Rb 8,8,1024
Rb 12,8,17408
Rb 16,8,2048
Rb 20,8,18432
Rb 24,8,3072
Rb 28,8,19456
Rb 32,8,4096
Rb 36,8,20480
Rb 40,8,5120
Rb 44,8,21504
v_exp_f32_e32 v56, v112
v_exp_f32_e32 v28, v113
v_add_f32_e32 v14, v14, v56
v_add_f32_e32 v14, v14, v28
Cv 56,56,28
v_exp_f32_e32 v57, v114
v_exp_f32_e32 v29, v115
v_add_f32_e32 v14, v14, v57
v_add_f32_e32 v14, v14, v29
Cv 57,57,29
v_exp_f32_e32 v58, v120
v_exp_f32_e32 v24, v121
v_add_f32_e32 v14, v14, v58
v_add_f32_e32 v14, v14, v24
Cv 58,58,24
)" R"(
v_exp_f32_e32 v59, v122
v_exp_f32_e32 v25, v123
v_add_f32_e32 v14, v14, v59
v_add_f32_e32 v14, v14, v25
Cv 59,59,25
v_exp_f32_e32 v60, v116
v_exp_f32_e32 v26, v117
v_add_f32_e32 v15, v15, v60
v_add_f32_e32 v15, v15, v26
Cv 60,60,26
v_exp_f32_e32 v61, v118
v_exp_f32_e32 v27, v119
v_add_f32_e32 v15, v15, v61
v_add_f32_e32 v15, v15, v27
Cv 61,61,27
v_exp_f32_e32 v62, v124
v_exp_f32_e32 v28, v125
v_add_f32_e32 v15, v15, v62
v_add_f32_e32 v15, v15, v28
Cv 62,62,28
v_exp_f32_e32 v63, v126
v_exp_f32_e32 v29, v127
v_add_f32_e32 v15, v15, v63
v_add_f32_e32 v15, v15, v29
Cv 63,63,29
v_max_f32_e32 v11, v14, v15
v_cmp_nge_f32_e32 vcc, s62, v11
s_cbranch_vccnz .Lzfx5
.Lzfr5:
s_waitcnt lgkmcnt(8)
Mp 128,0,48
Rb 48,8,6144
s_add_u32 m0, s36, 131072
s_add_u32 s40, s67, 0x20000
s_lshl_b32 s82, s82, 7
Mp 136,0,52
buffer_load_dwordx4 v2, s[20:23], s40 offen offset:0 sc1 lds
v_add_f32_e32 v12, v12, v14
s_add_u32 s82, s82, 0x150c000
Mp 136,4,60
Rb 52,8,22528
v_add_f32_e32 v13, v13, v15
v_mov_b32 v24, v12
Mp 128,4,56
buffer_load_dwordx4 v2, s[20:23], s40 offen offset:1024 sc1 lds
v_permlane32_swap_b32_e32 v12, v24
Mp 132,8,48
Rb 56,8,7168
v_add_f32_e32 v12, v12, v24
v_mov_b32 v24, v12
Mp 140,8,52
s_nop 0
v_permlane16_swap_b32_e32 v12, v24
v_add_f32_e32 v12, v12, v24
v_mov_b32 v25, v13
Mp 140,12,60
Rb 60,8,23552
v_permlane32_swap_b32_e32 v13, v25
s_waitcnt lgkmcnt(8)
Mp 132,12,56
buffer_load_dwordx4 v2, s[20:23], s40 offen offset:2048 sc1 lds
v_add_f32_e32 v13, v13, v25
Mp 144,16,48
Rb 0,8,8192
)" R"(
v_rcp_f32_e32 v12, v12
Mp 152,16,52
buffer_load_dwordx4 v2, s[20:23], s40 offen offset:3072 sc1 lds
v_mov_b32 v25, v13
Mp 152,20,60
Rb 4,8,24576
v_permlane16_swap_b32_e32 v13, v25
v_add_f32_e32 v13, v13, v25
Mp 144,20,56
v_rcp_f32_e32 v13, v13
v_mul_f32_e32 v128, v12, v128
Mp 148,24,48
Rb 8,8,9216
s_add_u32 m0, s36, 135168
v_mul_f32_e32 v129, v12, v129
Mp 156,24,52
buffer_load_dwordx4 v3, s[20:23], s40 offen offset:0 sc1 lds
Cv 128,128,129
Mp 156,28,60
Rb 12,8,25600
v_mul_f32_e32 v130, v12, v130
s_waitcnt lgkmcnt(8)
Mp 148,28,56
buffer_load_dwordx4 v3, s[20:23], s40 offen offset:1024 sc1 lds
v_mul_f32_e32 v131, v12, v131
Mp 160,32,48
Rb 16,8,10240
Cv 129,130,131
s_cmp_lg_u32 s33, 0
s_cmov_b32 s82, 0x40000000
Mp 168,32,52
v_mul_f32_e32 v132, v12, v132
v_mul_f32_e32 v133, v12, v133
Cv 130,132,133
Mp 168,36,60
Rb 20,8,26624
v_mul_f32_e32 v134, v12, v134
v_mul_f32_e32 v135, v12, v135
Mp 160,36,56
buffer_load_dwordx4 v3, s[20:23], s40 offen offset:2048 sc1 lds
Cv 131,134,135
Mp 164,40,48
Rb 24,8,11264
s_add_u32 m0, s36, 98304
s_add_u32 s40, s67, 0x30000
Mp 172,40,52
buffer_load_dwordx4 v2, s[20:23], s40 offen offset:0 sc1 lds
v_mul_f32_e32 v136, v13, v136
Mp 172,44,60
Rb 28,8,27648
v_mul_f32_e32 v137, v13, v137
s_waitcnt lgkmcnt(8)
Mp 164,44,56
Cv 136,136,137
v_mul_f32_e32 v138, v13, v138
v_mul_f32_e32 v139, v13, v139
Mp 176,48,48
Rb 32,8,12288
Cv 137,138,139
v_mul_f32_e32 v140, v13, v140
Mp 184,48,52
buffer_load_dwordx4 v2, s[20:23], s40 offen offset:1024 sc1 lds
v_mul_f32_e32 v141, v13, v141
Mp 184,52,60
)" R"(
Rb 36,8,28672
Cv 138,140,141
v_mul_f32_e32 v142, v13, v142
Mp 176,52,56
buffer_load_dwordx4 v2, s[20:23], s40 offen offset:2048 sc1 lds
v_mul_f32_e32 v143, v13, v143
Mp 180,56,48
Rb 40,8,13312
Cv 139,142,143
v_mul_f32_e32 v144, v12, v144
Mp 188,56,52
v_mul_f32_e32 v145, v12, v145
Cv 144,144,145
v_mul_f32_e32 v146, v12, v146
Mp 188,60,60
Rb 44,8,29696
v_mul_f32_e32 v147, v12, v147
s_waitcnt lgkmcnt(8)
Mp 180,60,56
buffer_load_dwordx4 v2, s[20:23], s40 offen offset:3072 sc1 lds
Cv 145,146,147
Mp 192,0,48
Rb 48,8,14336
s_add_u32 m0, s36, 102400
s_cmp_eq_u32 s35, 7
s_cmov_b32 s82, 0x40000000
Mp 200,0,52
buffer_load_dwordx4 v3, s[20:23], s40 offen offset:0 sc1 lds
v_mul_f32_e32 v148, v12, v148
Mp 200,4,60
Rb 52,8,30720
v_mul_f32_e32 v149, v12, v149
Cv 146,148,149
Mp 192,4,56
v_mul_f32_e32 v150, v12, v150
v_mul_f32_e32 v151, v12, v151
Cv 147,150,151
Mp 196,8,48
Rb 56,8,15360
v_mul_f32_e32 v152, v13, v152
v_mul_f32_e32 v153, v13, v153
Mp 204,8,52
buffer_load_dwordx4 v3, s[20:23], s40 offen offset:1024 sc1 lds
Cv 152,152,153
Mp 204,12,60
Rb 60,8,31744
v_mul_f32_e32 v154, v13, v154
s_waitcnt lgkmcnt(8)
Mp 196,12,56
buffer_load_dwordx4 v3, s[20:23], s40 offen offset:2048 sc1 lds
v_mul_f32_e32 v155, v13, v155
Mp 208,16,48
Rb 0,1,0
Cv 153,154,155
v_mul_f32_e32 v156, v13, v156
Mp 216,16,52
v_mul_f32_e32 v157, v13, v157
Cv 154,156,157
v_mul_f32_e32 v158, v13, v158
Mp 216,20,60
Rb 4,1,1024
v_mul_f32_e32 v159, v13, v159
Cv 155,158,159
Mp 208,20,56
)" R"(
v_mul_f32_e32 v160, v12, v160
v_mul_f32_e32 v161, v12, v161
Cv 160,160,161
Mp 212,24,48
Rb 8,1,2048
v_mul_f32_e32 v162, v12, v162
v_mul_f32_e32 v163, v12, v163
Mp 220,24,52
Cv 161,162,163
v_mul_f32_e32 v164, v12, v164
v_mul_f32_e32 v165, v12, v165
Mp 220,28,60
Rb 12,1,3072
s_waitcnt vmcnt(14) lgkmcnt(4)
s_barrier
Mp 212,28,56
s_add_u32 s40, s67, 0x40000
Cv 162,164,165
v_mul_f32_e32 v166, v12, v166
Mp 224,32,48
Rb 16,1,4096
s_add_u32 m0, s36, 65536
Mp 232,32,52
buffer_load_dwordx4 v2, s[20:23], s40 offen offset:0 sc1 lds
v_mul_f32_e32 v167, v12, v167
Mp 232,36,60
Rb 20,1,5120
Cv 163,166,167
v_mul_f32_e32 v168, v13, v168
Mp 224,36,56
v_mul_f32_e32 v169, v13, v169
Cv 168,168,169
v_mul_f32_e32 v170, v13, v170
Mp 228,40,48
Rb 24,1,6144
v_mul_f32_e32 v171, v13, v171
Cv 169,170,171
Mp 236,40,52
v_mul_f32_e32 v172, v13, v172
v_mul_f32_e32 v173, v13, v173
Cv 170,172,173
Mp 236,44,60
Rb 28,1,8192
v_mul_f32_e32 v174, v13, v174
Mp 228,44,56
buffer_load_dwordx4 v2, s[20:23], s40 offen offset:1024 sc1 lds
v_mul_f32_e32 v175, v13, v175
s_lshl_b32 s64, s35, 17
Mp 240,48,48
Rb 32,1,9216
Cv 171,174,175
v_mul_f32_e32 v176, v12, v176
Mp 248,48,52
v_mul_f32_e32 v177, v12, v177
Cv 176,176,177
v_mul_f32_e32 v178, v12, v178
Mp 248,52,60
Rb 36,1,10240
v_mul_f32_e32 v179, v12, v179
Cv 177,178,179
Mp 240,52,56
v_mul_f32_e32 v180, v12, v180
v_mul_f32_e32 v181, v12, v181
Cv 178,180,181
Mp 244,56,48
)" R"(
Rb 40,1,11264
v_mul_f32_e32 v182, v12, v182
v_mul_f32_e32 v183, v12, v183
s_add_u32 s64, s64, s89
Mp 252,56,52
buffer_load_dwordx4 v2, s[20:23], s40 offen offset:2048 sc1 lds
Cv 179,182,183
Mp 252,60,60
Rb 44,1,12288
v_mul_f32_e32 v184, v13, v184
s_mov_b32 s55, 0x1508c00
s_waitcnt lgkmcnt(8)
Mp 244,60,56
v_mul_f32_e32 v185, v13, v185
Cv 184,184,185
v_mul_f32_e32 v186, v13, v186
v_mfma_f32_16x16x32_bf16 v[32:35], a[0:3], v[128:131], 0
Rb 48,1,13312
v_mul_f32_e32 v187, v13, v187
s_add_u32 s64, s64, 0x800000
v_mfma_f32_16x16x32_bf16 v[40:43], a[0:3], v[136:139], 0
Cv 185,186,187
v_mul_f32_e32 v188, v13, v188
v_mul_f32_e32 v189, v13, v189
s_xor_b32 s66, s64, 0x202000
v_mfma_f32_16x16x32_bf16 v[44:47], a[4:7], v[136:139], 0
Rb 52,1,14336
Cv 186,188,189
v_mul_f32_e32 v190, v13, v190
v_mfma_f32_16x16x32_bf16 v[36:39], a[4:7], v[128:131], 0
buffer_load_dwordx4 v2, s[20:23], s40 offen offset:3072 sc1 lds
v_mul_f32_e32 v191, v13, v191
v_mfma_f32_16x16x32_bf16 v[48:51], a[8:11], v[128:131], 0
Rb 56,1,16384
Cv 187,190,191
v_mul_f32_e32 v192, v12, v192
s_cmp_lg_u32 s33, 0
s_cmov_b32 s55, 0x40000000
v_mfma_f32_16x16x32_bf16 v[56:59], a[8:11], v[136:139], 0
v_mul_f32_e32 v193, v12, v193
Cv 192,192,193
v_mul_f32_e32 v194, v12, v194
v_mov_b32 v16, 1
v_mfma_f32_16x16x32_bf16 v[60:63], a[12:15], v[136:139], 0
Rb 60,1,17408
v_mul_f32_e32 v195, v12, v195
v_add_u32 v9, s36, v8
s_waitcnt lgkmcnt(8)
v_mfma_f32_16x16x32_bf16 v[52:55], a[12:15], v[128:131], 0
Cv 193,194,195
v_mul_f32_e32 v196, v12, v196
v_mul_f32_e32 v197, v12, v197
s_sub_u32 s66, s66, s84
Mp 32,16,144
Rb 0,1,18432
s_add_u32 m0, s36, 69632
Cv 194,196,197
Mp 40,16,152
buffer_load_dwordx4 v3, s[20:23], s40 offen offset:0 sc1 lds
v_mul_f32_e32 v198, v12, v198
Mp 44,20,152
Rb 4,1,19456
v_mul_f32_e32 v199, v12, v199
Cv 195,198,199
)" R"(
Mp 36,20,144
v_mul_f32_e32 v200, v13, v200
v_mul_f32_e32 v201, v13, v201
Cv 200,200,201
Mp 48,24,144
Rb 8,1,20480
v_mul_f32_e32 v202, v13, v202
v_mul_f32_e32 v203, v13, v203
s_sub_u32 s66, s66, s84
Mp 56,24,152
Cv 201,202,203
v_mul_f32_e32 v204, v13, v204
v_mul_f32_e32 v205, v13, v205
Mp 60,28,152
Rb 12,1,21504
Cv 202,204,205
s_waitcnt lgkmcnt(8)
Mp 52,28,144
buffer_load_dwordx4 v3, s[20:23], s40 offen offset:1024 sc1 lds
v_mul_f32_e32 v206, v13, v206
Mp 32,32,160
Rb 16,1,22528
v_mul_f32_e32 v207, v13, v207
s_lshl2_add_u32 s65, s35, s33
Mp 40,32,168
Cv 203,206,207
v_mul_f32_e32 v208, v12, v208
v_mul_f32_e32 v209, v12, v209
s_lshl_b32 s65, s65, 8
Mp 44,36,168
Rb 20,1,24576
Cv 208,208,209
v_mul_f32_e32 v210, v12, v210
Mp 36,36,160
v_mul_f32_e32 v211, v12, v211
Cv 209,210,211
v_mul_f32_e32 v212, v12, v212
Mp 48,40,160
Rb 24,1,25600
v_mul_f32_e32 v213, v12, v213
Cv 210,212,213
Mp 56,40,168
buffer_load_dwordx4 v3, s[20:23], s40 offen offset:2048 sc1 lds
v_mul_f32_e32 v214, v12, v214
Mp 60,44,168
Rb 28,1,26624
v_mul_f32_e32 v215, v12, v215
s_waitcnt lgkmcnt(8)
Mp 52,44,160
Cv 211,214,215
v_mul_f32_e32 v216, v13, v216
v_mul_f32_e32 v217, v13, v217
s_add_u32 s65, s65, s50
Mp 32,48,176
Rb 32,1,27648
Cv 216,216,217
v_mul_f32_e32 v218, v13, v218
.LzH40:
s_cbranch_execz .LzHR
Mp 40,48,184
v_mul_f32_e32 v219, v13, v219
Cv 217,218,219
v_mul_f32_e32 v220, v13, v220
Mp 44,52,184
)" R"(
Rb 36,1,28672
v_mul_f32_e32 v221, v13, v221
Cv 218,220,221
Mp 36,52,176
v_mul_f32_e32 v222, v13, v222
v_mul_f32_e32 v223, v13, v223
Cv 219,222,223
Mp 48,56,176
Rb 40,1,29696
v_mul_f32_e32 v224, v12, v224
v_mul_f32_e32 v225, v12, v225
s_add_u32 s65, s65, 0x2400
Mp 56,56,184
Cv 224,224,225
v_mul_f32_e32 v226, v12, v226
v_mul_f32_e32 v227, v12, v227
Mp 60,60,184
Rb 44,1,30720
Cv 225,226,227
buffer_atomic_add v16, v7, s[20:23], s55 offen sc0 sc1
s_waitcnt lgkmcnt(8)
Mp 52,60,176
v_mul_f32_e32 v228, v12, v228
v_mul_f32_e32 v229, v12, v229
Cv 226,228,229
Mp 32,0,192
Rb 48,1,32768
v_mul_f32_e32 v230, v12, v230
s_xor_b32 s52, s65, 0x202000
Mp 40,0,200
v_mul_f32_e32 v231, v12, v231
Cv 227,230,231
v_mul_f32_e32 v232, v13, v232
Mp 44,4,200
Rb 52,1,33792
v_mul_f32_e32 v233, v13, v233
Cv 232,232,233
Mp 36,4,192
v_mul_f32_e32 v234, v13, v234
v_mul_f32_e32 v235, v13, v235
Cv 233,234,235
Mp 48,8,192
Rb 56,1,34816
v_mul_f32_e32 v236, v13, v236
v_mul_f32_e32 v237, v13, v237
Mp 56,8,200
Cv 234,236,237
v_mul_f32_e32 v238, v13, v238
v_mul_f32_e32 v239, v13, v239
Mp 60,12,200
Rb 60,1,35840
s_waitcnt vmcnt(15) lgkmcnt(4)
s_barrier
buffer_store_dword v6, v7, s[20:23], s82 offen offset:0 sc0 sc1
Mp 52,12,192
s_add_u32 s40, s67, 0x50000
Cv 235,238,239
v_mul_f32_e32 v240, v12, v240
Mp 32,16,208
Rb 0,1,36864
s_add_u32 m0, s36, 0
v_mul_f32_e32 v241, v12, v241
Mp 40,16,216
buffer_load_dwordx4 v2, s[20:23], s40 offen offset:0 sc1 lds
Cv 240,240,241
)" R"(
Mp 44,20,216
Rb 4,1,37888
v_mul_f32_e32 v242, v12, v242
v_mul_f32_e32 v243, v12, v243
Mp 36,20,208
Cv 241,242,243
v_mul_f32_e32 v244, v12, v244
v_mul_f32_e32 v245, v12, v245
Mp 48,24,208
Rb 8,1,38912
Cv 242,244,245
v_mul_f32_e32 v246, v12, v246
Mp 56,24,216
v_mul_f32_e32 v247, v12, v247
Cv 243,246,247
v_mul_f32_e32 v248, v13, v248
Mp 60,28,216
Rb 12,1,40960
v_mul_f32_e32 v249, v13, v249
Mp 52,28,208
buffer_load_dwordx4 v2, s[20:23], s40 offen offset:1024 sc1 lds
Cv 248,248,249
Mp 32,32,224
Rb 16,1,41984
v_mul_f32_e32 v250, v13, v250
Mp 40,32,232
v_mul_f32_e32 v251, v13, v251
Cv 249,250,251
v_mul_f32_e32 v252, v13, v252
Mp 44,36,232
Rb 20,1,43008
v_mul_f32_e32 v253, v13, v253
Cv 250,252,253
Mp 36,36,224
v_mul_f32_e32 v254, v13, v254
v_mul_f32_e32 v255, v13, v255
Cv 251,254,255
Mp 48,40,224
Rb 24,1,44032
Mp 56,40,232
buffer_load_dwordx4 v2, s[20:23], s40 offen offset:2048 sc1 lds
Mp 60,44,232
Rb 28,1,45056
s_waitcnt lgkmcnt(8)
Mp 52,44,224
Mp 32,48,240
Rb 32,1,46080
Mp 40,48,248
Mp 44,52,248
Rb 36,1,47104
Mp 36,52,240
buffer_load_dwordx4 v2, s[20:23], s40 offen offset:3072 sc1 lds
Mp 48,56,240
Rb 40,1,49152
Mp 56,56,248
Mp 60,60,248
Rb 44,1,50176
s_waitcnt lgkmcnt(8)
Mp 52,60,240
v_mfma_f32_16x16x32_bf16 v[64:67], a[0:3], v[128:131], 0
Rb 48,1,51200
s_add_u32 m0, s36, 4096
v_mfma_f32_16x16x32_bf16 v[72:75], a[0:3], v[136:139], 0
buffer_load_dwordx4 v3, s[20:23], s40 offen offset:0 sc1 lds
)" R"(
Mp 72,4,152
Rb 52,1,52224
Mp 64,4,144
Cv 32,32,33
Cv 33,34,35
Mp 64,8,160
Rb 56,1,53248
Cv 34,36,37
Mp 72,8,168
Cv 35,38,39
Mp 72,12,184
Rb 60,1,54272
s_waitcnt lgkmcnt(8)
Mp 64,12,176
buffer_load_dwordx4 v3, s[20:23], s40 offen offset:1024 sc1 lds
Mp 64,16,192
Rb 0,1,55296
Mp 72,16,200
buffer_store_dwordx4 v[32:35], v2, s[20:23], s64 offen offset:0 sc0 sc1
Mp 72,20,216
Rb 4,1,57344
Mp 64,20,208
Mp 64,24,224
Rb 8,1,58368
Mp 72,24,232
buffer_load_dwordx4 v3, s[20:23], s40 offen offset:2048 sc1 lds
Mp 72,28,248
Rb 12,1,59392
s_waitcnt lgkmcnt(8)
Mp 64,28,240
Cv 40,40,41
Cv 41,42,43
v_mfma_f32_16x16x32_bf16 v[68:71], a[32:35], v[128:131], 0
Rb 16,1,60416
Cv 42,44,45
v_mfma_f32_16x16x32_bf16 v[76:79], a[32:35], v[136:139], 0
Cv 43,46,47
Mp 76,36,152
Rb 20,1,61440
Mp 68,36,144
buffer_store_dwordx4 v[40:43], v2, s[20:23], s64 offen offset:1024 sc0 sc1
Mp 68,40,160
Rb 24,1,62464
Mp 76,40,168
Mp 76,44,184
Rb 28,1,63488
s_waitcnt lgkmcnt(8)
Mp 68,44,176
Mp 68,48,192
Rb 32,10,0
Mp 76,48,200
Cv 48,48,49
Cv 49,50,51
Mp 76,52,216
Rb 36,10,1024
Cv 50,52,53
Mp 68,52,208
Cv 51,54,55
Mp 68,56,224
Rb 40,10,2048
Mp 76,56,232
buffer_store_dwordx4 v[48:51], v2, s[20:23], s64 offen offset:2048 sc0 sc1
Mp 76,60,248
Rb 44,10,3072
)" R"(
s_waitcnt vmcnt(19) lgkmcnt(4)
s_barrier
Mp 68,60,240
s_add_u32 s40, s67, 0x60000
v_mfma_f32_16x16x32_bf16 v[80:83], a[0:3], v[128:131], 0
Rb 48,10,4096
s_add_u32 m0, s36, 32768
v_mfma_f32_16x16x32_bf16 v[88:91], a[0:3], v[136:139], 0
buffer_load_dwordx4 v2, s[20:23], s40 offen offset:0 sc1 lds
Mp 88,4,152
Rb 52,10,5120
Mp 80,4,144
Cv 56,56,57
Cv 57,58,59
Cv 58,60,61
Mp 80,8,160
Rb 56,10,6144
v_bfe_u32 v11, v0, 2, 2
Cv 59,62,63
Mp 88,8,168
v_lshrrev_b32 v4, 4, v0
v_xor_b32 v11, v11, v4
Mp 88,12,184
Rb 60,10,8192
Mp 80,12,176
buffer_load_dwordx4 v2, s[20:23], s40 offen offset:1024 sc1 lds
Mp 80,16,192
Rb 0,10,9216
v_bfe_u32 v4, v0, 1, 1
Mp 88,16,200
v_lshl_or_b32 v11, v11, 1, v4
v_and_b32 v4, 1, v0
Mp 88,20,216
Rb 4,10,10240
v_mul_u32_u24 v4, 0xffff, v4
buffer_store_dwordx4 v[56:59], v2, s[20:23], s64 offen offset:3072 sc0 sc1
Mp 80,20,208
v_add_u32 v4, 1, v4
v_mul_u32_u24 v4, 0x3f80, v4
Mp 80,24,224
Rb 8,10,11264
v_cmp_eq_u32_e32 vcc, 0, v11
Mp 88,24,232
buffer_load_dwordx4 v2, s[20:23], s40 offen offset:2048 sc1 lds
Mp 88,28,248
Rb 12,10,12288
s_waitcnt lgkmcnt(8)
Mp 80,28,240
v_cmp_eq_u32_e64 s[28:29], 1, v11
v_accvgpr_write_b32 a192, 0
v_mfma_f32_16x16x32_bf16 v[84:87], a[32:35], v[128:131], 0
Rb 16,10,13312
v_accvgpr_write_b32 a193, 0
v_mfma_f32_16x16x32_bf16 v[92:95], a[32:35], v[136:139], 0
v_accvgpr_write_b32 a196, 0
v_accvgpr_write_b32 a197, 0
Cv 64,64,65
Mp 92,36,152
Rb 20,10,14336
v_cndmask_b32_e32 v11, 0, v4, vcc
Mp 84,36,144
buffer_load_dwordx4 v2, s[20:23], s40 offen offset:3072 sc1 lds
Cv 65,66,67
Mp 84,40,160
Rb 24,10,16384
)" R"(
v_cndmask_b32_e64 v4, 0, v4, s[28:29]
Mp 92,40,168
v_accvgpr_write_b32 a194, v11
v_accvgpr_write_b32 a195, v4
Cv 66,68,69
Mp 92,44,184
Rb 28,10,17408
s_waitcnt lgkmcnt(8)
Mp 84,44,176
Cv 67,70,71
buffer_store_dwordx4 v[64:67], v3, s[20:23], s64 offen offset:0 sc0 sc1
Mp 84,48,192
Rb 32,10,18432
s_add_u32 m0, s36, 36864
Mp 92,48,200
buffer_load_dwordx4 v3, s[20:23], s40 offen offset:0 sc1 lds
Mp 92,52,216
Rb 36,10,19456
Mp 84,52,208
Mp 84,56,224
Rb 40,10,20480
Mp 92,56,232
Cv 72,72,73
Cv 73,74,75
Mp 92,60,248
Rb 44,10,21504
s_waitcnt lgkmcnt(8)
Mp 84,60,240
buffer_load_dwordx4 v3, s[20:23], s40 offen offset:1024 sc1 lds
v_mfma_f32_16x16x32_bf16 v[96:99], a[0:3], v[128:131], 0
Rb 48,10,22528
Cv 74,76,77
v_mfma_f32_16x16x32_bf16 v[104:107], a[0:3], v[136:139], 0
Cv 75,78,79
Mp 104,4,152
Rb 52,10,24576
Mp 96,4,144
buffer_store_dwordx4 v[72:75], v3, s[20:23], s64 offen offset:1024 sc0 sc1
Mp 96,8,160
Rb 56,10,25600
Mp 104,8,168
buffer_load_dwordx4 v3, s[20:23], s40 offen offset:2048 sc1 lds
Mp 104,12,184
Rb 60,10,26624
s_waitcnt lgkmcnt(8)
Mp 96,12,176
Mp 96,16,192
Rb 0,10,27648
Mp 104,16,200
Cv 80,80,81
Cv 81,82,83
Mp 104,20,216
Rb 4,10,28672
Cv 82,84,85
Mp 96,20,208
Cv 83,86,87
Mp 96,24,224
Rb 8,10,29696
Mp 104,24,232
buffer_store_dwordx4 v[80:83], v3, s[20:23], s64 offen offset:2048 sc0 sc1
Mp 104,28,248
Rb 12,10,30720
s_waitcnt lgkmcnt(8)
Mp 96,28,240
)" R"(
v_mfma_f32_16x16x32_bf16 v[100:103], a[32:35], v[128:131], 0
Rb 16,8,32768
v_mfma_f32_16x16x32_bf16 v[108:111], a[32:35], v[136:139], 0
Mp 108,36,152
Rb 20,8,33792
Mp 100,36,144
Cv 88,88,89
Cv 89,90,91
Mp 100,40,160
Rb 24,8,34816
Cv 90,92,93
Mp 108,40,168
Cv 91,94,95
Mp 108,44,184
Rb 28,8,35840
s_waitcnt vmcnt(23) lgkmcnt(4)
s_barrier
Mp 100,44,176
s_add_u32 s40, s67, 0x70000
Mp 100,48,192
Rb 32,8,36864
s_add_u32 m0, s36, 131072
Mp 108,48,200
buffer_load_dwordx4 v2, s[20:23], s40 offen offset:0 sc1 lds
Mp 108,52,216
Rb 36,8,37888
buffer_store_dwordx4 v[88:91], v3, s[20:23], s64 offen offset:3072 sc0 sc1
Mp 100,52,208
s_mov_b32 s8, s74
s_mov_b32 s9, s75
Mp 100,56,224
Rb 40,8,38912
s_mov_b32 s10, 0x1c0000
Mp 108,56,232
s_mov_b32 s11, 0x20000
v_and_b32 v174, 1, v0
Mp 108,60,248
Rb 44,8,40960
Mp 100,60,240
buffer_load_dwordx4 v2, s[20:23], s40 offen offset:1024 sc1 lds
v_mfma_f32_16x16x32_bf16 v[112:115], a[0:3], v[128:131], 0
Rb 48,8,41984
v_mul_u32_u24 v174, 0xffff, v174
v_mfma_f32_16x16x32_bf16 v[120:123], a[0:3], v[136:139], 0
v_add_u32 v174, 1, v174
v_mul_u32_u24 v174, 0x3f80, v174
Mp 120,4,152
Rb 52,8,43008
v_bfe_u32 v173, v0, 1, 3
Mp 112,4,144
Cv 96,96,97
Cv 97,98,99
Mp 112,8,160
Rb 56,8,44032
Cv 98,100,101
Mp 120,8,168
buffer_load_dwordx4 v2, s[20:23], s40 offen offset:2048 sc1 lds
Mp 120,12,184
Rb 60,8,45056
s_waitcnt lgkmcnt(8)
Mp 112,12,176
Cv 99,102,103
v_cmp_eq_u32_e32 vcc, 0, v173
v_and_b32 v167, 0xf0, v1
Mp 112,16,192
)" R"(
Rb 0,8,46080
v_cndmask_b32_e32 v132, 0, v174, vcc
Mp 120,16,200
s_add_u32 s64, s64, 0x10000
buffer_store_dwordx4 v[96:99], v2, s[20:23], s64 offen offset:0 sc0 sc1
Mp 120,20,216
Rb 4,8,47104
v_cmp_eq_u32_e32 vcc, 1, v173
Mp 112,20,208
buffer_load_dwordx4 v2, s[20:23], s40 offen offset:3072 sc1 lds
Mp 112,24,224
Rb 8,8,49152
v_cndmask_b32_e32 v133, 0, v174, vcc
Mp 120,24,232
v_cmp_eq_u32_e32 vcc, 2, v173
v_mul_u32_u24 v166, 56, v167
Mp 120,28,248
Rb 12,8,50176
s_waitcnt lgkmcnt(8)
Mp 112,28,240
Cv 104,104,105
Cv 105,106,107
v_mfma_f32_16x16x32_bf16 v[116:119], a[32:35], v[128:131], 0
Rb 16,8,51200
s_add_u32 m0, s36, 135168
v_mfma_f32_16x16x32_bf16 v[124:127], a[32:35], v[136:139], 0
buffer_load_dwordx4 v3, s[20:23], s40 offen offset:0 sc1 lds
Mp 124,36,152
Rb 20,8,52224
Cv 106,108,109
Mp 116,36,144
Cv 107,110,111
v_cndmask_b32_e32 v134, 0, v174, vcc
Mp 116,40,160
Rb 24,8,53248
v_cmp_eq_u32_e32 vcc, 3, v173
Mp 124,40,168
buffer_store_dwordx4 v[104:107], v2, s[20:23], s64 offen offset:1024 sc0 sc1
Mp 124,44,184
Rb 28,8,54272
s_waitcnt lgkmcnt(8)
Mp 116,44,176
buffer_load_dwordx4 v3, s[20:23], s40 offen offset:1024 sc1 lds
v_cndmask_b32_e32 v135, 0, v174, vcc
Mp 116,48,192
Rb 32,8,55296
v_cmp_eq_u32_e32 vcc, 4, v173
Mp 124,48,200
s_nop 0
v_cndmask_b32_e32 v140, 0, v174, vcc
v_cmp_eq_u32_e32 vcc, 5, v173
Mp 124,52,216
Rb 36,8,57344
v_cndmask_b32_e32 v141, 0, v174, vcc
Mp 116,52,208
v_cmp_eq_u32_e32 vcc, 6, v173
v_and_b32 v165, 0x80, v1
Mp 116,56,224
Rb 40,8,58368
v_cndmask_b32_e32 v142, 0, v174, vcc
Mp 124,56,232
buffer_load_dwordx4 v3, s[20:23], s40 offen offset:2048 sc1 lds
Mp 124,60,248
Rb 44,8,59392
)" R"(
s_waitcnt vmcnt(8) lgkmcnt(8)
buffer_store_dword v6, v7, s[20:23], s65 offen offset:0 sc0 sc1
Mp 116,60,240
v_cmp_eq_u32_e32 vcc, 7, v173
v_lshrrev_b32 v173, 4, v0
v_mfma_f32_16x16x32_bf16 v[32:35], a[0:3], v[128:131], 0
Rb 48,8,60416
v_cndmask_b32_e32 v143, 0, v174, vcc
v_mfma_f32_16x16x32_bf16 v[40:43], a[0:3], v[136:139], 0
v_lshl_or_b32 v164, v173, 25, v167
v_lshl_add_u32 v166, v173, 3, v166
Mp 40,4,152
Rb 52,8,61440
v_lshrrev_b32_e64 v173, v173, s85
Mp 32,4,144
Cv 112,112,113
Cv 113,114,115
Mp 32,8,160
Rb 56,8,62464
Cv 114,116,117
Mp 40,8,168
Cv 115,118,119
v_bfe_u32 v174, v173, 4, 1
Mp 40,12,184
Rb 60,8,63488
s_waitcnt lgkmcnt(8)
Mp 32,12,176
buffer_store_dwordx4 v[112:115], v2, s[20:23], s64 offen offset:2048 sc0 sc1
ds_write_b32 v9, v16 offset:39936
Mp 32,16,192
Rb 0,8,0
v_lshl_or_b32 v183, v174, 13, v164
Mp 40,16,200
v_and_b32 v174, 1, v173
v_lshl_or_b32 v164, v174, 13, v164
Mp 40,20,216
Rb 4,8,1024
v_and_b32 v174, 7, v0
Mp 32,20,208
v_lshl_or_b32 v165, v174, 8, v165
v_lshlrev_b32 v165, 1, v165
Mp 32,24,224
Rb 8,8,2048
s_mov_b32 s93, 0x1111
Mp 40,24,232
Cv 120,120,121
Cv 121,122,123
Mp 40,28,248
Rb 12,8,3072
s_waitcnt lgkmcnt(3)
s_barrier
Mp 32,28,240
Cv 122,124,125
Cv 123,126,127
ds_read_b32 v16, v8 offset:39936
v_mfma_f32_16x16x32_bf16 v[36:39], a[32:35], v[128:131], 0
Rb 16,8,4096
v_lshrrev_b32 v173, 2, v0
v_mfma_f32_16x16x32_bf16 v[44:47], a[32:35], v[136:139], 0
buffer_store_dwordx4 v[120:123], v2, s[20:23], s64 offen offset:3072 sc0 sc1
Mp 44,36,152
Rb 20,8,5120
v_mul_u32_u24 v173, 832, v173
Mp 36,36,144
v_add_u32 v173, v173, v1
)" R"(
v_lshrrev_b32 v174, 1, v0
Mp 36,40,160
Rb 24,8,6144
v_and_b32 v174, 24, v174
Mp 44,40,168
v_lshl_add_u32 v174, v167, 2, v174
s_add_u32 s59, s36, 98304
Mp 44,44,184
Rb 28,8,8192
s_waitcnt lgkmcnt(4)
Mp 36,44,176
v_readfirstlane_b32 s82, v16
v_add_u32 v174, s59, v174
Mp 36,48,192
Rb 32,8,9216
s_lshl2_add_u32 s56, s82, s33
Mp 44,48,200
s_mul_i32 s88, s56, 4682
s_lshr_b32 s58, s88, 16
Mp 44,52,216
Rb 36,8,10240
s_mul_i32 s57, s58, 14
Mp 36,52,208
s_sub_u32 s57, s56, s57
s_lshl_b32 s40, s58, 12
Mp 36,56,224
Rb 40,8,11264
Mp 44,56,232
s_cmp_gt_u32 s57, 6
s_cselect_b32 s88, 128, 0
Mp 44,60,248
Rb 44,8,12288
Mp 36,60,240
buffer_load_dword v4, v7, s[20:23], s52 offen offset:0 sc0 sc1
s_add_u32 s40, s40, s88
v_mfma_f32_16x16x32_bf16 v[48:51], a[0:3], v[128:131], 0
Rb 48,8,13312
s_add_u32 s40, s40, 0x1510000
v_mfma_f32_16x16x32_bf16 v[56:59], a[0:3], v[136:139], 0
s_lshl_b32 s55, s58, 6
s_add_u32 s55, s55, s57
Mp 56,4,152
Rb 52,8,14336
Mp 48,4,144
Cv 32,32,33
Cv 33,34,35
.LzH41:
s_cbranch_execz .LzHR
Mp 48,8,160
Rb 56,8,16384
Cv 34,36,37
Mp 56,8,168
Cv 35,38,39
Mp 56,12,184
Rb 60,8,17408
s_waitcnt lgkmcnt(8)
Mp 48,12,176
buffer_store_dwordx4 v[32:35], v3, s[20:23], s64 offen offset:0 sc0 sc1
Mp 48,16,192
Rb 0,8,18432
Mp 56,16,200
s_cmp_gt_u32 s57, 6
s_cselect_b32 s88, 9, 0
Mp 56,20,216
)" R"(
Rb 4,8,19456
s_add_u32 s55, s55, s88
Mp 48,20,208
s_and_b32 s88, s58, 32
s_lshl_b32 s88, s88, 8
Mp 48,24,224
Rb 8,8,20480
v_xor_b32 v164, s88, v164
Mp 56,24,232
Cv 40,40,41
Cv 41,42,43
Mp 56,28,248
Rb 12,8,21504
s_waitcnt lgkmcnt(8)
Mp 48,28,240
Cv 42,44,45
Cv 43,46,47
v_xor_b32 v183, s88, v183
v_mfma_f32_16x16x32_bf16 v[52:55], a[32:35], v[128:131], 0
Rb 16,8,22528
s_lshl_b32 s55, s55, 10
v_mfma_f32_16x16x32_bf16 v[60:63], a[32:35], v[136:139], 0
buffer_store_dwordx4 v[40:43], v3, s[20:23], s64 offen offset:1024 sc0 sc1
Mp 60,36,152
Rb 20,8,24576
s_add_u32 s55, s55, 0x1000000
Mp 52,36,144
s_and_b32 s88, s57, 1
s_lshl1_add_u32 s58, s58, s88
Mp 52,40,160
Rb 24,8,25600
s_lshl_b32 s88, s58, 8
Mp 60,40,168
v_add_u32 v167, s88, v167
s_mul_i32 s59, s58, 14336
Mp 60,44,184
Rb 28,8,26624
s_waitcnt lgkmcnt(8)
Mp 52,44,176
s_lshr_b32 s88, s57, 1
s_lshl_b32 s88, s88, 6
s_add_u32 s88, s88, s59
Mp 52,48,192
Rb 32,8,27648
v_add_u32 v166, s88, v166
Mp 60,48,200
s_mov_b32 s57, 0
s_add_u32 m0, s36, 98304
Mp 60,52,216
Rb 36,8,28672
Mp 52,52,208
buffer_load_dwordx4 v173, s[12:15], s88 offen offset:0 lds
Mp 52,56,224
Rb 40,8,29696
s_add_u32 m0, s36, 98880
Mp 60,56,232
buffer_load_dwordx4 v173, s[12:15], s88 offen offset:448 lds
Mp 60,60,248
Rb 44,8,30720
s_waitcnt lgkmcnt(8)
Mp 52,60,240
s_mov_b32 s88, 0x1500008
buffer_load_dword v167, v167, s[20:23], s88 offen sc0 sc1
v_mfma_f32_16x16x32_bf16 v[64:67], a[0:3], v[128:131], 0
)" R"(
Rb 48,1,0
v_mfma_f32_16x16x32_bf16 v[72:75], a[0:3], v[136:139], 0
Mp 72,4,152
Rb 52,1,1024
Mp 64,4,144
Mp 64,8,160
Rb 56,1,2048
Mp 72,8,168
Mp 72,12,184
Rb 60,1,3072
s_waitcnt vmcnt(5) lgkmcnt(8)
s_barrier
v_cmp_eq_u32_e32 vcc, s3, v4
s_andn2_b64 s[28:29], 1, vcc
s_cbranch_scc1 .Lzsp11
.Lzok11:
Mp 64,12,176
buffer_load_dwordx4 a[64:67], v2, s[20:23], s66 offen offset:0 sc0 sc1
Mp 64,16,192
Rb 0,1,4096
Mp 72,16,200
Mp 72,20,216
Rb 4,1,5120
Mp 64,20,208
buffer_load_dwordx4 a[68:71], v2, s[20:23], s66 offen offset:1024 sc0 sc1
Mp 64,24,224
Rb 8,1,6144
Mp 72,24,232
Mp 72,28,248
Rb 12,1,8192
s_waitcnt lgkmcnt(8)
Mp 64,28,240
buffer_load_dwordx4 a[72:75], v2, s[20:23], s66 offen offset:2048 sc0 sc1
v_mfma_f32_16x16x32_bf16 v[68:71], a[32:35], v[128:131], 0
Rb 16,1,9216
v_mfma_f32_16x16x32_bf16 v[76:79], a[32:35], v[136:139], 0
Mp 76,36,152
Rb 20,1,10240
Mp 68,36,144
buffer_load_dwordx4 a[76:79], v2, s[20:23], s66 offen offset:3072 sc0 sc1
Mp 68,40,160
Rb 24,1,11264
Mp 76,40,168
Mp 76,44,184
Rb 28,1,12288
s_waitcnt vmcnt(7) lgkmcnt(8)
buffer_store_dword v6, v7, s[20:23], s65 offen offset:128 sc0 sc1
Mp 68,44,176
buffer_load_dwordx4 a[80:83], v3, s[20:23], s66 offen offset:0 sc0 sc1
Mp 68,48,192
Rb 32,1,13312
Mp 76,48,200
Mp 76,52,216
Rb 36,1,14336
Mp 68,52,208
buffer_load_dwordx4 a[84:87], v3, s[20:23], s66 offen offset:1024 sc0 sc1
Mp 68,56,224
Rb 40,1,16384
Mp 76,56,232
Mp 76,60,248
Rb 44,1,17408
s_waitcnt lgkmcnt(8)
Mp 68,60,240
buffer_load_dwordx4 a[88:91], v3, s[20:23], s66 offen offset:2048 sc0 sc1
v_mfma_f32_16x16x32_bf16 v[80:83], a[0:3], v[128:131], 0
)" R"(
Rb 48,1,18432
v_mfma_f32_16x16x32_bf16 v[88:91], a[0:3], v[136:139], 0
Mp 88,4,152
Rb 52,1,19456
Mp 80,4,144
buffer_load_dwordx4 a[92:95], v3, s[20:23], s66 offen offset:3072 sc0 sc1
Mp 80,8,160
Rb 56,1,20480
Mp 88,8,168
Mp 88,12,184
Rb 60,1,21504
s_waitcnt lgkmcnt(8)
Mp 80,12,176
Mp 80,16,192
Rb 0,1,22528
Mp 88,16,200
Mp 88,20,216
Rb 4,1,24576
Mp 80,20,208
Mp 80,24,224
Rb 8,1,25600
Mp 88,24,232
Mp 88,28,248
Rb 12,1,26624
s_waitcnt lgkmcnt(8)
Mp 80,28,240
v_mfma_f32_16x16x32_bf16 v[84:87], a[32:35], v[128:131], 0
Rb 16,1,27648
v_mfma_f32_16x16x32_bf16 v[92:95], a[32:35], v[136:139], 0
Mp 92,36,152
Rb 20,1,28672
Mp 84,36,144
Mp 84,40,160
Rb 24,1,29696
Mp 92,40,168
Mp 92,44,184
Rb 28,1,30720
s_waitcnt lgkmcnt(8)
Mp 84,44,176
buffer_load_dword v4, v7, s[20:23], s52 offen offset:128 sc0 sc1
Mp 84,48,192
Rb 32,1,32768
Mp 92,48,200
Mp 92,52,216
Rb 36,1,33792
Mp 84,52,208
Mp 84,56,224
Rb 40,1,34816
Mp 92,56,232
Mp 92,60,248
Rb 44,1,35840
s_waitcnt vmcnt(6)
s_barrier
Mp 84,60,240
Mv 48,194,64
Mv 56,194,68
Mv 52,192,64
Mv 60,192,68
Mv 64,194,72
Mv 72,194,76
Mv 68,192,72
s_waitcnt vmcnt(3) lgkmcnt(10)
Mv 76,192,76
Cv 48,48,49
Cv 49,50,51
)" R"(
Mv 80,194,80
Cv 50,52,53
Cv 51,54,55
Mv 88,194,84
s_add_u32 s29, s73, 0x0
buffer_store_dwordx4 v[48:51], v1, s[20:23], s29 offen sc0 sc1
Mv 84,192,80
Mv 92,192,84
v_mfma_f32_16x16x32_bf16 v[96:99], a[0:3], v[128:131], 0
Rb 48,1,36864
v_mfma_f32_16x16x32_bf16 v[104:107], a[0:3], v[136:139], 0
Mp 104,4,152
Rb 52,1,37888
s_waitcnt lgkmcnt(8)
Mp 96,4,144
Cv 56,56,57
Cv 57,58,59
ds_read_b64 v[188:189], v174 offset:0
Mp 96,8,160
Rb 56,1,38912
Cv 58,60,61
Mp 104,8,168
Cv 59,62,63
ds_read_b64 v[190:191], v174 offset:32
Mp 104,12,184
Rb 60,1,40960
Mp 96,12,176
s_add_u32 s29, s73, 0x400
buffer_store_dwordx4 v[56:59], v1, s[20:23], s29 offen sc0 sc1
ds_read_b64 v[196:197], v174 offset:1024
Mp 96,16,192
Rb 0,1,41984
Mp 104,16,200
ds_read_b64 v[198:199], v174 offset:1056
Mp 104,20,216
Rb 4,1,43008
s_waitcnt lgkmcnt(12)
Mp 96,20,208
Mp 96,24,224
Rb 8,1,44032
Mp 104,24,232
Cv 64,64,65
Cv 65,66,67
Mp 104,28,248
Rb 12,1,45056
Cv 66,68,69
Mp 96,28,240
Cv 67,70,71
v_mfma_f32_16x16x32_bf16 v[100:103], a[32:35], v[128:131], 0
Rb 16,1,46080
v_mfma_f32_16x16x32_bf16 v[108:111], a[32:35], v[136:139], 0
s_add_u32 s29, s73, 0x800
buffer_store_dwordx4 v[64:67], v1, s[20:23], s29 offen sc0 sc1
Mp 108,36,152
Rb 20,1,47104
s_waitcnt vmcnt(3) lgkmcnt(11)
v_cmp_eq_u32_e32 vcc, s3, v4
s_andn2_b64 s[28:29], 1, vcc
s_cbranch_scc1 .Lzsp12
.Lzok12:
Mp 100,36,144
s_add_u32 s66, s66, 0x10000
buffer_load_dwordx4 a[96:99], v2, s[20:23], s66 offen offset:0 sc0 sc1
Mp 100,40,160
)" R"(
Rb 24,1,49152
v_sub_f32_e32 v172, 1.0, v167
Mp 108,40,168
Cv 172,172,172
v_lshlrev_b32 v172, 16, v172
Mp 108,44,184
Rb 28,1,50176
v_lshlrev_b32 v204, 16, v188
Mp 100,44,176
buffer_load_dwordx4 a[100:103], v2, s[20:23], s66 offen offset:1024 sc0 sc1
Mp 100,48,192
Rb 32,1,51200
Cv 72,72,73
Mp 108,48,200
Cv 73,74,75
Cv 74,76,77
Mp 108,52,216
Rb 36,1,52224
s_waitcnt lgkmcnt(12)
Mp 100,52,208
buffer_load_dwordx4 a[104:107], v2, s[20:23], s66 offen offset:2048 sc0 sc1
Mp 100,56,224
Rb 40,1,53248
Cv 75,78,79
Mp 108,56,232
s_add_u32 s29, s73, 0xc00
buffer_store_dwordx4 v[72:75], v1, s[20:23], s29 offen sc0 sc1
Mp 108,60,248
Rb 44,1,54272
v_and_b32 v205, 0xffff0000, v188
Mp 100,60,240
buffer_load_dwordx4 a[108:111], v2, s[20:23], s66 offen offset:3072 sc0 sc1
Mv 96,194,88
v_mul_f32_e32 v204, v172, v204
v_mul_f32_e32 v205, v172, v205
Mv 104,194,92
Cv 205,204,205
v_lshlrev_b32 v204, 16, v205
Mv 100,192,88
v_and_b32 v205, 0xffff0000, v205
s_waitcnt lgkmcnt(8)
Mv 108,192,92
buffer_load_dwordx4 a[112:115], v3, s[20:23], s66 offen offset:0 sc0 sc1
v_mfma_f32_16x16x32_bf16 v[112:115], a[0:3], v[128:131], 0
Rb 48,1,55296
Cv 80,80,81
v_mfma_f32_16x16x32_bf16 v[120:123], a[0:3], v[136:139], 0
Cv 81,82,83
Cv 82,84,85
Mp 120,4,152
Rb 52,1,57344
Cv 83,86,87
Mp 112,4,144
buffer_load_dwordx4 a[116:119], v3, s[20:23], s66 offen offset:1024 sc0 sc1
Mp 112,8,160
Rb 56,1,58368
v_lshlrev_b32 v206, 16, v189
Mp 120,8,168
s_add_u32 s29, s73, 0x1000
buffer_store_dwordx4 v[80:83], v1, s[20:23], s29 offen sc0 sc1
Mp 120,12,184
Rb 60,1,59392
s_waitcnt lgkmcnt(8)
Mp 112,12,176
)" R"(
v_and_b32 v207, 0xffff0000, v189
v_mul_f32_e32 v206, v172, v206
Mp 112,16,192
Rb 0,1,60416
v_mul_f32_e32 v207, v172, v207
Mp 120,16,200
Cv 207,206,207
v_lshlrev_b32 v206, 16, v207
Mp 120,20,216
Rb 4,1,61440
v_and_b32 v207, 0xffff0000, v207
Mp 112,20,208
Cv 88,88,89
Cv 89,90,91
Mp 112,24,224
Rb 8,1,62464
Cv 90,92,93
Mp 120,24,232
Cv 91,94,95
v_lshlrev_b32 v212, 16, v190
Mp 120,28,248
Rb 12,1,63488
s_waitcnt lgkmcnt(8)
Mp 112,28,240
s_add_u32 s29, s73, 0x1400
buffer_store_dwordx4 v[88:91], v1, s[20:23], s29 offen sc0 sc1
v_mfma_f32_16x16x32_bf16 v[116:119], a[32:35], v[128:131], 0
Rb 16,10,0
v_and_b32 v213, 0xffff0000, v190
v_mfma_f32_16x16x32_bf16 v[124:127], a[32:35], v[136:139], 0
v_mul_f32_e32 v212, v172, v212
v_mul_f32_e32 v213, v172, v213
Mp 124,36,152
Rb 20,10,1024
Cv 213,212,213
Mp 116,36,144
v_lshlrev_b32 v212, 16, v213
v_and_b32 v213, 0xffff0000, v213
Mp 116,40,160
Rb 24,10,2048
v_lshlrev_b32 v214, 16, v191
Mp 124,40,168
Cv 96,96,97
Cv 97,98,99
Mp 124,44,184
Rb 28,10,3072
s_waitcnt lgkmcnt(8)
Mp 116,44,176
Cv 98,100,101
Cv 99,102,103
Mp 116,48,192
Rb 32,10,4096
v_and_b32 v215, 0xffff0000, v191
Mp 124,48,200
s_add_u32 s29, s73, 0x1800
buffer_store_dwordx4 v[96:99], v1, s[20:23], s29 offen sc0 sc1
Mp 124,52,216
Rb 36,10,5120
v_mul_f32_e32 v214, v172, v214
Mp 116,52,208
v_mul_f32_e32 v215, v172, v215
Cv 215,214,215
Mp 116,56,224
Rb 40,10,6144
)" R"(
v_lshlrev_b32 v214, 16, v215
Mp 124,56,232
v_and_b32 v215, 0xffff0000, v215
v_lshlrev_b32 v220, 16, v196
Mp 124,60,248
Rb 44,10,8192
s_waitcnt vmcnt(8) lgkmcnt(10)
Mp 116,60,240
Cv 104,104,105
Cv 105,106,107
Mv 112,194,96
Cv 106,108,109
Cv 107,110,111
Mv 120,194,100
s_add_u32 s29, s73, 0x4000
buffer_store_dwordx4 v[104:107], v1, s[20:23], s29 offen sc0 sc1
Mv 116,192,96
v_and_b32 v221, 0xffff0000, v196
v_mul_f32_e32 v220, v172, v220
Mv 124,192,100
v_mul_f32_e32 v221, v172, v221
Cv 221,220,221
v_mfma_f32_16x16x32_bf16 v[32:35], a[0:3], v[128:131], 0
Rb 48,10,9216
v_lshlrev_b32 v220, 16, v221
v_mfma_f32_16x16x32_bf16 v[40:43], a[0:3], v[136:139], 0
v_and_b32 v221, 0xffff0000, v221
v_lshlrev_b32 v222, 16, v197
Mp 40,4,152
Rb 52,10,10240
s_waitcnt lgkmcnt(8)
Mp 32,4,144
Cv 112,112,113
Cv 113,114,115
Mp 32,8,160
Rb 56,10,11264
Cv 114,116,117
Mp 40,8,168
Cv 115,118,119
v_and_b32 v223, 0xffff0000, v197
Mp 40,12,184
Rb 60,10,12288
v_mul_f32_e32 v222, v172, v222
Mp 32,12,176
s_add_u32 s29, s73, 0x4400
buffer_store_dwordx4 v[112:115], v1, s[20:23], s29 offen sc0 sc1
Mp 32,16,192
Rb 0,10,13312
v_mul_f32_e32 v223, v172, v223
Mp 40,16,200
Cv 223,222,223
v_lshlrev_b32 v222, 16, v223
Mp 40,20,216
Rb 4,10,14336
s_waitcnt lgkmcnt(8)
Mp 32,20,208
v_and_b32 v223, 0xffff0000, v223
v_lshlrev_b32 v228, 16, v198
Mp 32,24,224
Rb 8,10,16384
v_and_b32 v229, 0xffff0000, v198
Mp 40,24,232
Cv 120,120,121
Cv 121,122,123
)" R"(
Mp 40,28,248
Rb 12,10,17408
Cv 122,124,125
Mp 32,28,240
Cv 123,126,127
v_mul_f32_e32 v228, v172, v228
v_mfma_f32_16x16x32_bf16 v[36:39], a[32:35], v[128:131], 0
Rb 16,10,18432
v_mul_f32_e32 v229, v172, v229
v_mfma_f32_16x16x32_bf16 v[44:47], a[32:35], v[136:139], 0
s_add_u32 s29, s73, 0x4800
buffer_store_dwordx4 v[120:123], v1, s[20:23], s29 offen sc0 sc1
Mp 44,36,152
Rb 20,10,19456
s_waitcnt lgkmcnt(8)
Mp 36,36,144
Cv 229,228,229
v_lshlrev_b32 v228, 16, v229
Mp 36,40,160
Rb 24,10,20480
v_and_b32 v229, 0xffff0000, v229
Mp 44,40,168
v_lshlrev_b32 v230, 16, v199
v_and_b32 v231, 0xffff0000, v199
Mp 44,44,184
Rb 28,10,21504
v_mul_f32_e32 v230, v172, v230
Mp 36,44,176
v_mul_f32_e32 v231, v172, v231
Cv 231,230,231
Mp 36,48,192
Rb 32,10,22528
v_lshlrev_b32 v230, 16, v231
Mp 44,48,200
v_and_b32 v231, 0xffff0000, v231
Mp 44,52,216
Rb 36,10,24576
s_waitcnt vmcnt(3) lgkmcnt(10)
buffer_store_dword v6, v7, s[20:23], s68 offen offset:0 sc0 sc1
Mp 36,52,208
Mp 36,56,224
Rb 40,10,25600
Mp 44,56,232
Mp 44,60,248
Rb 44,10,26624
Mp 36,60,240
Mv 32,194,104
Mv 40,194,108
Mv 36,192,104
s_waitcnt lgkmcnt(8)
Mv 44,192,108
v_mfma_f32_16x16x32_bf16 v[48:51], a[0:3], v[128:131], 0
Rb 48,10,27648
v_mfma_f32_16x16x32_bf16 v[56:59], a[0:3], v[136:139], 0
Mp 56,4,152
Rb 52,10,28672
Mp 48,4,144
Cv 32,32,33
Cv 33,34,35
Mp 48,8,160
Rb 56,10,29696
Cv 34,36,37
Mp 56,8,168
Cv 35,38,39
)" R"(
Mp 56,12,184
Rb 60,10,30720
s_waitcnt lgkmcnt(8)
Mp 48,12,176
s_add_u32 s29, s73, 0x4c00
buffer_store_dwordx4 v[32:35], v1, s[20:23], s29 offen sc0 sc1
Mp 48,16,192
Mp 56,16,200
Mp 56,20,216
Mp 48,20,208
Mp 48,24,224
Mp 56,24,232
Cv 40,40,41
Cv 41,42,43
buffer_store_dword v6, v7, s[20:23], s68 offen offset:128 sc0 sc1
Mp 56,28,248
Cv 42,44,45
s_waitcnt lgkmcnt(4)
Mp 48,28,240
Cv 43,46,47
v_mfma_f32_16x16x32_bf16 v[52:55], a[32:35], v[128:131], 0
s_add_u32 s29, s73, 0x5000
buffer_store_dwordx4 v[40:43], v1, s[20:23], s29 offen sc0 sc1
v_mfma_f32_16x16x32_bf16 v[60:63], a[32:35], v[136:139], 0
buffer_load_dword v4, v165, s[20:23], s40 offen sc0 sc1
Mp 60,36,152
Mp 52,36,144
.LzH42:
s_cbranch_execz .LzHR
Mp 52,40,160
Mp 60,40,168
Mp 60,44,184
s_waitcnt lgkmcnt(0)
Mp 52,44,176
Mp 52,48,192
Mp 60,48,200
Mp 60,52,216
Mp 52,52,208
Mp 52,56,224
Mp 60,56,232
Mp 60,60,248
Mp 52,60,240
Mv 48,194,112
Mv 56,194,116
Mv 52,192,112
Mv 60,192,116
s_nop 7
Cv 48,48,49
Cv 49,50,51
Cv 50,52,53
Cv 51,54,55
s_add_u32 s29, s73, 0x5400
buffer_store_dwordx4 v[48:51], v1, s[20:23], s29 offen sc0 sc1
Cv 56,56,57
Cv 57,58,59
Cv 58,60,61
Cv 59,62,63
s_add_u32 s29, s73, 0x5800
buffer_store_dwordx4 v[56:59], v1, s[20:23], s29 offen sc0 sc1
s_branch .LzEND
.LzCEND:
)" : "+{s[0:1]}"(ka), "+{s2}"(bid), "+{v0}"(tid) :
   : "memory", "scc", "vcc", "v1", "v2", "v3", "v4", "v5", "v6", "v7", "v8", "v9", R256("v"), V10("a"), R256("a"),
     "s3", "s4", "s5", "s6", "s7", "s8", "s9", V10("s1"), V10("s2"), "s30", "s31", "s33", "s34", "s35", "s36", "s37", "s38", "s39",
     V10("s4"), V10("s5"), V10("s6"), V10("s7"), V10("s8"), V10("s9"));
}

extern "C" void kernel_launch(void* const* d_in, const int* in_sizes, int n_in, void* d_out, int out_size, void* d_ws, size_t ws_size, hipStream_t stream) {
  (void)hipFuncSetAttribute((const void*)k_main, hipFuncAttributeMaxDynamicSharedMemorySize, 163840);
  k_main<<<256, 256, 163840, stream>>>((const u16*)d_in[0], (const u16*)d_in[1], (const u16*)d_in[2], (const u16*)d_in[3], (const u16*)d_in[4],
      (char*)d_ws, (const u16*)d_in[11], (const u16*)d_in[7], (const u16*)d_in[5], (const u16*)d_in[8], (const u16*)d_in[9], (const u16*)d_in[10], (u16*)d_out);
}
```

```cpp
#include <hip/hip_runtime.h>

typedef unsigned short u16;
#define V10(p) p "0", p "1", p "2", p "3", p "4", p "5", p "6", p "7", p "8", p "9"
#define V100(p) V10(p "0"), V10(p "1"), V10(p "2"), V10(p "3"), V10(p "4"), V10(p "5"), V10(p "6"), V10(p "7"), V10(p "8"), V10(p "9")
#define R256(p) V10(p "1"), V10(p "2"), V10(p "3"), V10(p "4"), V10(p "5"), V10(p "6"), V10(p "7"), V10(p "8"), V10(p "9"), V100(p "1"), \
                V10(p "20"), V10(p "21"), V10(p "22"), V10(p "23"), V10(p "24"), p "250", p "251", p "252", p "253", p "254", p "255"

extern "C" __global__ __launch_bounds__(256, 1) __attribute__((aligned(4096)))
void k_main(const u16* x, const u16* ckv, const u16* wq, const u16* wuk, const u16* wuv, char* ws,
            const u16* gate_w, const u16* w_r, const u16* wo, const u16* wg, const u16* wu, const u16* wd, u16* out) {
  typedef const __attribute__((address_space(4))) char* karg_t;
  karg_t ka = (karg_t)__builtin_amdgcn_kernarg_segment_ptr();
  unsigned bid = blockIdx.x, tid = threadIdx.x;
  asm volatile(R"(
.macro Mv d,a,b
v_mfma_f32_16x16x32_bf16 v[\d:\d+3], a[\a:\a+3], a[\b:\b+3], v[\d:\d+3]
.endm
.macro Mp d,a,b
v_mfma_f32_16x16x32_bf16 v[\d:\d+3], a[\a:\a+3], v[\b:\b+3], v[\d:\d+3]
.endm
.macro Ma d,a,b
v_mfma_f32_16x16x32_bf16 a[\d:\d+3], a[\a:\a+3], a[\b:\b+3], a[\d:\d+3]
.endm
.macro Mz d,a,b
v_mfma_f32_16x16x32_bf16 v[\d:\d+3], a[\a:\a+3], a[\b:\b+3], 0
.endm
.macro Rb d,v,o
ds_read_b128 a[\d:\d+3], v\v offset:\o
.endm
.macro Rt d,v,o
ds_read_b64_tr_b16 a[\d:\d+1], v\v offset:\o
.endm
.macro Cv d,a,b
v_cvt_pk_bf16_f32 v\d, v\a, v\b
.endm
.LzSTART:
s_load_dwordx16 s[64:79], s[0:1], 0
s_load_dwordx2 s[80:81], s[0:1], 64
v_lshrrev_b32 v1, 6, v0
v_and_b32 v0, 63, v0
v_readfirstlane_b32 s33, v1
v_lshlrev_b32 v1, 4, v0
s_getpc_b64 s[24:25]
.Lzpc0:
s_sub_u32 s24, s24, .Lzpc0-.LzSTART
s_subb_u32 s25, s25, 0
s_mov_b32 s26, .LzCEND-.LzSTART
s_mov_b32 s27, 0x20000
s_lshr_b32 s28, s2, 3
s_lshl2_add_u32 s28, s28, s33
s_lshl_b32 s28, s28, 11
v_lshrrev_b32 v5, 4, v0
v_min_u32 v5, 1, v5
v_lshlrev_b32 v5, 27, v5
v_add_lshl_u32 v5, v5, v1, 3
buffer_load_dword v5, v5, s[24:27], s28 offen
s_and_b32 s28, s2, 0x17
s_bfe_u32 s29, s2, 0x10005
s_lshl3_add_u32 s28, s29, s28
s_lshr_b32 s29, s2, 6
s_lshl_b32 s29, s29, 5
s_or_b32 s28, s28, s29
s_and_b32 s29, s2, 8
s_lshl4_add_u32 s2, s29, s28
s_and_b32 s28, s2, 7
s_bfe_u32 s29, s2, 0x10003
s_lshl1_add_u32 s34, s28, s29
s_lshl4_add_u32 s89, s28, s29
s_lshl_b32 s89, s89, 21
s_bfe_u32 s35, s2, 0x30004
s_bfe_u32 s91, s2, 0x10004
s_bfe_u32 s93, s2, 0x20005
s_lshl_b32 s36, s33, 13
s_mov_b32 s95, 40
s_bitcmp1_b32 s2, 7
s_cbranch_scc0 .LzA0
.LzB0:
s_mov_b32 s54, 0x3db8aa3b
s_mul_i32 s41, s33, 7168
s_mul_i32 s42, s33, 0x70000
s_lshl_b32 s28, s34, 9
s_add_u32 s42, s42, s28
v_lshrrev_b32 v28, 3, v0
v_and_b32 v29, 0x70, v1
v_lshl_or_b32 v28, v28, 13, v29
v_lshlrev_b32 v29, 1, v0
v_and_b32 v29, 32, v29
v_xor_b32 v28, v28, v29
s_and_b32 s28, s33, 1
s_lshl_b32 s28, s28, 6
v_xor_b32 v240, s28, v28
v_xor_b32 v241, 64, v240
v_add_u32 v241, 0xfc00, v241
v_add_u32 v242, 0x1f800, v240
v_add_u32 v243, 0x1f800, v241
s_sub_u32 s28, 15, s35
s_lshl2_add_u32 s28, s28, s33
s_mul_i32 s43, s28, 28672
)" R"(
v_and_b32 v2, 0x70, v1
v_and_b32 v28, 48, v0
v_xor_b32 v2, v2, v28
v_lshrrev_b32 v28, 3, v0
v_mul_u32_u24 v28, 896, v28
v_add_u32 v2, v2, v28
v_xor_b32 v3, 64, v2
v_add_u32 v3, 6144, v3
v_bfe_u32 v26, v0, 1, 3
v_lshrrev_b32 v28, 4, v0
v_xor_b32 v26, v26, v28
v_lshlrev_b32 v26, 4, v26
v_and_b32 v28, 15, v0
v_lshl_or_b32 v26, v28, 7, v26
s_add_u32 s28, s36, 0x0
v_add_u32 v26, s28, v26
v_xor_b32 v27, 64, v26
s_waitcnt lgkmcnt(0)
s_mov_b32 s16, s68
s_mov_b32 s17, s69
s_mov_b32 s18, 0x380000
s_mov_b32 s19, 0x20000
s_mov_b32 s12, s64
s_mov_b32 s13, s65
s_mov_b32 s14, 0x1c0000
s_mov_b32 s15, 0x20000
s_add_u32 m0, s36, 0
s_add_u32 s44, s43, 0
buffer_load_dwordx4 v2, s[12:15], s44 offen offset:0 lds
buffer_load_dwordx4 v3, s[12:15], s44 offen offset:1024 lds
s_add_u32 s44, s43, 12288
buffer_load_dwordx4 v2, s[12:15], s44 offen offset:2048 lds
buffer_load_dwordx4 v3, s[12:15], s44 offen offset:3072 lds
s_add_u32 m0, s41, 65536
s_add_u32 s92, s42, 0x0
buffer_load_dwordx4 v240, s[16:19], s92 offen offset:0 lds
buffer_load_dwordx4 v241, s[16:19], s92 offen offset:1024 lds
buffer_load_dwordx4 v242, s[16:19], s92 offen offset:2048 lds
buffer_load_dwordx4 v243, s[16:19], s92 offen offset:3072 lds
s_add_u32 m0, s41, 69632
s_add_u32 s92, s42, 0x40000
buffer_load_dwordx4 v240, s[16:19], s92 offen offset:0 lds
buffer_load_dwordx4 v241, s[16:19], s92 offen offset:1024 lds
buffer_load_dwordx4 v242, s[16:19], s92 offen offset:2048 lds
s_add_u32 m0, s36, 4096
s_add_u32 s44, s43, 128
buffer_load_dwordx4 v2, s[12:15], s44 offen offset:0 lds
buffer_load_dwordx4 v3, s[12:15], s44 offen offset:1024 lds
s_add_u32 s44, s43, 12416
buffer_load_dwordx4 v2, s[12:15], s44 offen offset:2048 lds
buffer_load_dwordx4 v3, s[12:15], s44 offen offset:3072 lds
s_add_u32 m0, s36, 32768
s_add_u32 s44, s43, 256
buffer_load_dwordx4 v2, s[12:15], s44 offen offset:0 lds
buffer_load_dwordx4 v3, s[12:15], s44 offen offset:1024 lds
s_add_u32 s44, s43, 12544
buffer_load_dwordx4 v2, s[12:15], s44 offen offset:2048 lds
buffer_load_dwordx4 v3, s[12:15], s44 offen offset:3072 lds
s_add_u32 m0, s36, 36864
s_add_u32 s44, s43, 384
buffer_load_dwordx4 v2, s[12:15], s44 offen offset:0 lds
buffer_load_dwordx4 v3, s[12:15], s44 offen offset:1024 lds
s_add_u32 s44, s43, 12672
buffer_load_dwordx4 v2, s[12:15], s44 offen offset:2048 lds
)" R"(
buffer_load_dwordx4 v3, s[12:15], s44 offen offset:3072 lds
s_mov_b32 s28, 0x1508400
s_load_dword s3, s[74:75], s28
s_mov_b32 s20, s74
s_mov_b32 s21, s75
s_mov_b32 s22, 0xf400000
s_mov_b32 s23, 0x20000
s_mov_b32 s4, s66
s_mov_b32 s5, s67
s_mov_b32 s6, 0x400000
s_mov_b32 s7, 0x20000
s_cmp_eq_u32 s91, 1
s_cselect_b32 s8, s72, s70
s_cselect_b32 s9, s73, s71
s_mov_b32 s10, 0x800000
s_mov_b32 s11, 0x20000
s_load_dwordx2 s[74:75], s[0:1], 96
s_lshl3_add_u32 s28, s33, 64
s_load_dwordx2 s[70:71], s[0:1], s28
v_bfe_u32 v244, v0, 3, 2
v_lshlrev_b32 v244, 5, v244
v_and_b32 v28, 3, v0
v_lshl_or_b32 v244, v28, 3, v244
v_bfe_u32 v28, v0, 2, 2
v_lshl_or_b32 v244, v28, 7, v244
v_lshrrev_b32 v28, 4, v0
v_lshl_or_b32 v244, v28, 10, v244
v_xor_b32 v245, 32, v244
v_xor_b32 v246, 64, v244
v_xor_b32 v247, 96, v244
v_add_u32 v252, 0x20000, v244
v_add_u32 v253, 0x20000, v245
v_add_u32 v254, 0x20000, v246
v_add_u32 v255, 0x20000, v247
v_add_u32 v248, 0x10000, v244
v_add_u32 v249, 0x10000, v245
v_add_u32 v250, 0x10000, v246
v_add_u32 v251, 0x10000, v247
s_waitcnt vmcnt(8) lgkmcnt(0)
s_barrier
ds_read_b128 v[128:131], v26 offset:0
ds_read_b128 v[132:135], v26 offset:2048
ds_read_b128 v[136:139], v27 offset:0
ds_read_b128 v[140:143], v27 offset:2048
ds_read_b128 v[144:147], v26 offset:4096
ds_read_b128 v[148:151], v26 offset:6144
ds_read_b128 v[152:155], v27 offset:4096
ds_read_b128 v[156:159], v27 offset:6144
Rt 144,248,0
Rt 146,248,512
Rt 148,249,0
Rt 150,249,512
Rt 152,250,0
Rt 154,250,512
Rt 156,251,0
Rt 158,251,512
Rt 160,248,4096
Rt 162,248,4608
Rt 164,249,4096
Rt 166,249,4608
Rt 168,250,4096
Rt 170,250,4608
Rt 172,251,4096
Rt 174,251,4608
s_add_u32 m0, s41, 98304
)" R"(
s_add_u32 s92, s42, 0x80
buffer_load_dwordx4 v240, s[16:19], s92 offen offset:0 lds
buffer_load_dwordx4 v241, s[16:19], s92 offen offset:1024 lds
buffer_load_dwordx4 v242, s[16:19], s92 offen offset:2048 lds
buffer_load_dwordx4 v243, s[16:19], s92 offen offset:3072 lds
s_add_u32 m0, s41, 102400
s_add_u32 s92, s42, 0x40080
buffer_load_dwordx4 v240, s[16:19], s92 offen offset:0 lds
buffer_load_dwordx4 v241, s[16:19], s92 offen offset:1024 lds
buffer_load_dwordx4 v242, s[16:19], s92 offen offset:2048 lds
s_mul_i32 s3, s3, 1664525
s_add_u32 s3, s3, 1013904223
v_mov_b32 v6, s3
v_min_u32 v7, 1, v0
v_lshlrev_b32 v7, 30, v7
s_waitcnt lgkmcnt(8)
s_barrier
v_mfma_f32_16x16x32_bf16 v[32:35], a[144:147], v[128:131], 0
Rt 128,248,8192
s_add_u32 m0, s41, 131072
s_add_u32 s92, s42, 0x1c0000
v_mfma_f32_16x16x32_bf16 v[40:43], a[144:147], v[132:135], 0
Rt 130,248,8704
buffer_load_dwordx4 v240, s[16:19], s92 offen offset:0 lds
v_mfma_f32_16x16x32_bf16 v[44:47], a[148:151], v[132:135], 0
Rt 132,249,8192
v_mfma_f32_16x16x32_bf16 v[36:39], a[148:151], v[128:131], 0
Rt 134,249,8704
v_mfma_f32_16x16x32_bf16 v[48:51], a[152:155], v[128:131], 0
Rt 136,250,8192
v_mfma_f32_16x16x32_bf16 v[56:59], a[152:155], v[132:135], 0
Rt 138,250,8704
buffer_load_dwordx4 v241, s[16:19], s92 offen offset:1024 lds
v_mfma_f32_16x16x32_bf16 v[60:63], a[156:159], v[132:135], 0
Rt 140,251,8192
v_mfma_f32_16x16x32_bf16 v[52:55], a[156:159], v[128:131], 0
Rt 142,251,8704
s_waitcnt vmcnt(13) lgkmcnt(8)
Mp 32,160,136
Rt 144,248,12288
ds_read_b128 v[160:163], v26 offset:32768
Mp 40,160,140
Rt 146,248,12800
buffer_load_dwordx4 v242, s[16:19], s92 offen offset:2048 lds
Mp 44,164,140
Rt 148,249,12288
ds_read_b128 v[164:167], v26 offset:34816
Mp 36,164,136
Rt 150,249,12800
Mp 48,168,136
Rt 152,250,12288
ds_read_b128 v[168:171], v27 offset:32768
Mp 56,168,140
Rt 154,250,12800
buffer_load_dwordx4 v243, s[16:19], s92 offen offset:3072 lds
Mp 60,172,140
Rt 156,251,12288
ds_read_b128 v[172:175], v27 offset:34816
Mp 52,172,136
Rt 158,251,12800
s_waitcnt vmcnt(11) lgkmcnt(12)
Mp 32,128,144
Rt 160,248,16384
ds_read_b128 v[176:179], v26 offset:36864
s_add_u32 m0, s41, 135168
)" R"(
s_add_u32 s92, s42, 0x200000
Mp 40,128,148
Rt 162,248,16896
buffer_load_dwordx4 v240, s[16:19], s92 offen offset:0 lds
Mp 44,132,148
Rt 164,249,16384
ds_read_b128 v[180:183], v26 offset:38912
Mp 36,132,144
Rt 166,249,16896
Mp 48,136,144
Rt 168,250,16384
ds_read_b128 v[184:187], v27 offset:36864
Mp 56,136,148
Rt 170,250,16896
buffer_load_dwordx4 v241, s[16:19], s92 offen offset:1024 lds
Mp 60,140,148
Rt 172,251,16384
ds_read_b128 v[188:191], v27 offset:38912
Mp 52,140,144
Rt 174,251,16896
s_waitcnt lgkmcnt(12)
Mp 32,144,152
Rt 128,248,20480
Mp 40,144,156
Rt 130,248,20992
buffer_load_dwordx4 v242, s[16:19], s92 offen offset:2048 lds
Mp 44,148,156
Rt 132,249,20480
Mp 36,148,152
Rt 134,249,20992
Mp 48,152,152
Rt 136,250,20480
Mp 56,152,156
Rt 138,250,20992
Mp 60,156,156
Rt 140,251,20480
s_add_u32 s44, s43, 512
Mp 52,156,152
Rt 142,251,20992
s_waitcnt lgkmcnt(8)
Mp 32,160,160
Rt 144,248,24576
s_add_u32 m0, s36, 32768
Mp 40,160,164
Rt 146,248,25088
buffer_load_dwordx4 v2, s[12:15], s44 offen offset:0 lds
Mp 44,164,164
Rt 148,249,24576
Mp 36,164,160
Rt 150,249,25088
Mp 48,168,160
Rt 152,250,24576
Mp 56,168,164
Rt 154,250,25088
buffer_load_dwordx4 v3, s[12:15], s44 offen offset:1024 lds
Mp 60,172,164
Rt 156,251,24576
Mp 52,172,160
Rt 158,251,25088
s_call_b64 s[30:31], .LzHOP
s_waitcnt vmcnt(9) lgkmcnt(8)
s_barrier
Mp 32,128,168
Rt 160,248,32768
s_mov_b32 s63, 0xe0c0a08
)" R"(
s_mov_b32 s28, 0x100d0b09
Mp 40,128,172
Rt 162,248,33280
s_add_u32 s44, s43, 12800
buffer_load_dwordx4 v2, s[12:15], s44 offen offset:2048 lds
Mp 44,132,172
Rt 164,249,32768
Mp 36,132,168
Rt 166,249,33280
Mp 48,136,168
Rt 168,250,32768
Mp 56,136,172
Rt 170,250,33280
buffer_load_dwordx4 v3, s[12:15], s44 offen offset:3072 lds
Mp 60,140,172
Rt 172,251,32768
s_add_u32 s44, s43, 640
Mp 52,140,168
Rt 174,251,33280
s_waitcnt lgkmcnt(8)
s_barrier
Mp 32,144,176
Rt 128,248,36864
s_add_u32 m0, s36, 36864
Mp 40,144,180
Rt 130,248,37376
buffer_load_dwordx4 v2, s[12:15], s44 offen offset:0 lds
Mp 44,148,180
Rt 132,249,36864
Mp 36,148,176
Rt 134,249,37376
Mp 48,152,176
Rt 136,250,36864
Mp 56,152,180
Rt 138,250,37376
buffer_load_dwordx4 v3, s[12:15], s44 offen offset:1024 lds
Mp 60,156,180
Rt 140,251,36864
Mp 52,156,176
Rt 142,251,37376
s_waitcnt lgkmcnt(8)
v_mfma_f32_16x16x32_bf16 v[64:67], a[160:163], v[128:131], 0
Rt 144,248,40960
v_mfma_f32_16x16x32_bf16 v[72:75], a[160:163], v[132:135], 0
Rt 146,248,41472
s_add_u32 s44, s43, 12928
buffer_load_dwordx4 v2, s[12:15], s44 offen offset:2048 lds
v_mfma_f32_16x16x32_bf16 v[76:79], a[164:167], v[132:135], 0
Rt 148,249,40960
v_mfma_f32_16x16x32_bf16 v[68:71], a[164:167], v[128:131], 0
Rt 150,249,41472
v_mfma_f32_16x16x32_bf16 v[80:83], a[168:171], v[128:131], 0
Rt 152,250,40960
s_bitcmp1_b32 s2, 5
s_cmov_b32 s63, 0xf1e1c1a
s_cmov_b32 s28, 0x111f1d1b
v_mfma_f32_16x16x32_bf16 v[88:91], a[168:171], v[132:135], 0
Rt 154,250,41472
buffer_load_dwordx4 v3, s[12:15], s44 offen offset:3072 lds
v_mfma_f32_16x16x32_bf16 v[92:95], a[172:175], v[132:135], 0
Rt 156,251,40960
s_add_u32 s92, s42, 0x1c0080
v_mfma_f32_16x16x32_bf16 v[84:87], a[172:175], v[128:131], 0
Rt 158,251,41472
s_waitcnt lgkmcnt(8)
)" R"(
Mp 64,128,136
Rt 160,248,45056
s_add_u32 m0, s41, 0
Mp 72,128,140
Rt 162,248,45568
buffer_load_dwordx4 v240, s[16:19], s92 offen offset:0 lds
Mp 76,132,140
Rt 164,249,45056
s_bitcmp1_b32 s2, 6
s_cmov_b32 s63, s28
Mp 68,132,136
Rt 166,249,45568
s_lshl_b32 s28, s33, 3
s_lshr_b32 s28, s63, s28
Mp 80,136,136
Rt 168,250,45056
s_and_b32 s28, s28, 255
s_lshl_b32 s38, s28, 17
s_add_u32 s38, s38, 0xff80
Mp 88,136,140
Rt 170,250,45568
buffer_load_dwordx4 v241, s[16:19], s92 offen offset:1024 lds
Mp 92,140,140
Rt 172,251,45056
s_lshl_b32 s39, s33, 16
s_lshl_b32 s28, s34, 9
Mp 84,140,136
Rt 174,251,45568
s_add_u32 s39, s39, s28
s_waitcnt lgkmcnt(8)
Mp 64,144,144
Rt 128,248,49152
s_sub_u32 s39, s39, 0x40000
s_lshl_b32 s37, s33, 12
Mp 72,144,148
Rt 130,248,49664
buffer_load_dwordx4 v242, s[16:19], s92 offen offset:2048 lds
s_add_u32 s37, s37, 0x10000
Mp 76,148,148
Rt 132,249,49152
v_lshrrev_b32 v28, 4, v0
v_and_b32 v29, 7, v0
Mp 68,148,144
Rt 134,249,49664
v_xor_b32 v28, v28, v29
v_lshlrev_b32 v28, 4, v28
Mp 80,152,144
Rt 136,250,49152
v_lshrrev_b32 v29, 3, v0
v_lshl_or_b32 v8, v29, 11, v28
Mp 88,152,148
Rt 138,250,49664
buffer_load_dwordx4 v243, s[16:19], s92 offen offset:3072 lds
Mp 92,156,148
Rt 140,251,49152
s_add_u32 s92, s42, 0x200080
v_xor_b32 v9, 64, v8
Mp 84,156,144
Rt 142,251,49664
v_add_u32 v9, 15360, v9
s_waitcnt lgkmcnt(8)
Mp 64,160,152
Rt 144,248,53248
s_add_u32 m0, s41, 4096
)" R"(
v_add_u32 v10, 30720, v8
Mp 72,160,156
Rt 146,248,53760
buffer_load_dwordx4 v240, s[16:19], s92 offen offset:0 lds
v_add_u32 v11, 30720, v9
Mp 76,164,156
Rt 148,249,53248
s_and_b32 s28, s33, 1
s_lshl_b32 s28, s28, 7
Mp 68,164,152
Rt 150,249,53760
v_lshrrev_b32 v29, 5, v0
v_mul_u32_u24 v29, 0x2220, v29
Mp 80,168,152
Rt 152,250,53248
v_xor_b32 v29, v29, v1
v_xor_b32 v12, s28, v29
Mp 88,168,156
Rt 154,250,53760
buffer_load_dwordx4 v241, s[16:19], s92 offen offset:1024 lds
Mp 92,172,156
Rt 156,251,53248
v_xor_b32 v13, 64, v12
v_add_u32 v13, 15360, v13
Mp 84,172,152
Rt 158,251,53760
v_add_u32 v14, 30720, v12
s_waitcnt vmcnt(10) lgkmcnt(8)
Mp 64,128,160
Rt 160,248,57344
ds_read_b128 v[192:195], v26 offset:32768
v_add_u32 v15, 30720, v13
Mp 72,128,164
Rt 162,248,57856
buffer_load_dwordx4 v242, s[16:19], s92 offen offset:2048 lds
v_bfe_u32 v28, v0, 1, 3
Mp 76,132,164
Rt 164,249,57344
ds_read_b128 v[196:199], v26 offset:34816
v_lshrrev_b32 v29, 4, v0
Mp 68,132,160
Rt 166,249,57856
v_xor_b32 v28, v28, v29
v_lshlrev_b32 v28, 4, v28
Mp 80,136,160
Rt 168,250,57344
ds_read_b128 v[200:203], v27 offset:32768
v_and_b32 v29, 15, v0
Mp 88,136,164
Rt 170,250,57856
v_lshl_or_b32 v28, v29, 7, v28
s_lshr_b32 s28, s33, 1
Mp 92,140,164
Rt 172,251,57344
ds_read_b128 v[204:207], v27 offset:34816
s_lshl_b32 s28, s28, 12
v_or_b32 v16, s28, v28
Mp 84,140,160
Rt 174,251,57856
s_waitcnt vmcnt(7) lgkmcnt(12)
s_barrier
Mp 64,144,168
Rt 128,252,0
ds_read_b128 v[208:211], v26 offset:36864
v_xor_b32 v17, 64, v16
)" R"(
Mp 72,144,172
Rt 130,252,512
v_bfe_u32 v28, v0, 2, 3
v_lshlrev_b32 v28, 5, v28
Mp 76,148,172
Rt 132,253,0
ds_read_b128 v[212:215], v26 offset:38912
v_and_b32 v29, 3, v0
Mp 68,148,168
Rt 134,253,512
v_lshl_or_b32 v28, v29, 3, v28
v_bfe_u32 v29, v0, 2, 2
Mp 80,152,168
Rt 136,254,0
ds_read_b128 v[216:219], v27 offset:36864
v_lshl_or_b32 v28, v29, 9, v28
Mp 88,152,172
Rt 138,254,512
s_add_u32 s44, s43, 768
v_lshrrev_b32 v29, 4, v0
Mp 92,156,172
Rt 140,255,0
ds_read_b128 v[220:223], v27 offset:38912
v_lshl_or_b32 v28, v29, 12, v28
s_and_b32 s28, s33, 1
Mp 84,156,168
Rt 142,255,512
s_waitcnt lgkmcnt(12)
s_barrier
Mp 64,160,176
Rt 144,252,4096
s_add_u32 m0, s36, 32768
s_lshl_b32 s28, s28, 8
Mp 72,160,180
Rt 146,252,4608
buffer_load_dwordx4 v2, s[12:15], s44 offen offset:0 lds
Mp 76,164,180
Rt 148,253,4096
s_bitset1_b32 s28, 16
v_or_b32 v18, s28, v28
Mp 68,164,176
Rt 150,253,4608
v_xor_b32 v19, 32, v18
v_xor_b32 v20, 64, v18
Mp 80,168,176
Rt 152,254,4096
buffer_load_dwordx4 v3, s[12:15], s44 offen offset:1024 lds
Mp 88,168,180
Rt 154,254,4608
v_xor_b32 v21, 96, v18
v_xor_b32 v22, 128, v18
Mp 92,172,180
Rt 156,255,4096
v_xor_b32 v23, 160, v18
v_xor_b32 v24, 192, v18
Mp 84,172,176
Rt 158,255,4608
s_add_u32 s44, s43, 13056
buffer_load_dwordx4 v2, s[12:15], s44 offen offset:2048 lds
s_waitcnt lgkmcnt(8)
Mp 32,128,184
Rt 160,252,8192
v_xor_b32 v25, 224, v18
Mp 40,128,188
Rt 162,252,8704
)" R"(
Mp 44,132,188
Rt 164,253,8192
buffer_load_dwordx4 v3, s[12:15], s44 offen offset:3072 lds
.LzH0:
s_cbranch_execz .LzH17
Mp 36,132,184
Rt 166,253,8704
Mp 48,136,184
Rt 168,254,8192
Mp 56,136,188
Rt 170,254,8704
s_add_u32 s92, s42, 0x100
Mp 60,140,188
Rt 172,255,8192
s_add_u32 m0, s41, 65536
Mp 52,140,184
Rt 174,255,8704
buffer_load_dwordx4 v240, s[16:19], s92 offen offset:0 lds
s_waitcnt lgkmcnt(8)
Mp 32,144,192
Rt 128,252,12288
Mp 40,144,196
Rt 130,252,12800
Mp 44,148,196
Rt 132,253,12288
Mp 36,148,192
Rt 134,253,12800
buffer_load_dwordx4 v241, s[16:19], s92 offen offset:1024 lds
Mp 48,152,192
Rt 136,254,12288
Mp 56,152,196
Rt 138,254,12800
Mp 60,156,196
Rt 140,255,12288
Mp 52,156,192
Rt 142,255,12800
buffer_load_dwordx4 v242, s[16:19], s92 offen offset:2048 lds
s_waitcnt lgkmcnt(8)
Mp 32,160,200
Rt 144,252,16384
Mp 40,160,204
Rt 146,252,16896
Mp 44,164,204
Rt 148,253,16384
Mp 36,164,200
Rt 150,253,16896
buffer_load_dwordx4 v243, s[16:19], s92 offen offset:3072 lds
Mp 48,168,200
Rt 152,254,16384
Mp 56,168,204
Rt 154,254,16896
s_add_u32 s92, s42, 0x40100
Mp 60,172,204
Rt 156,255,16384
s_add_u32 m0, s41, 69632
Mp 52,172,200
Rt 158,255,16896
buffer_load_dwordx4 v240, s[16:19], s92 offen offset:0 lds
s_waitcnt vmcnt(5) lgkmcnt(8)
Mp 32,128,208
Rt 160,252,20480
ds_read_b128 v[224:227], v26 offset:32768
Mp 40,128,212
Rt 162,252,20992
)" R"(
Mp 44,132,212
Rt 164,253,20480
ds_read_b128 v[228:231], v26 offset:34816
Mp 36,132,208
Rt 166,253,20992
buffer_load_dwordx4 v241, s[16:19], s92 offen offset:1024 lds
Mp 48,136,208
Rt 168,254,20480
ds_read_b128 v[232:235], v27 offset:32768
Mp 56,136,212
Rt 170,254,20992
Mp 60,140,212
Rt 172,255,20480
ds_read_b128 v[236:239], v27 offset:34816
Mp 52,140,208
Rt 174,255,20992
buffer_load_dwordx4 v242, s[16:19], s92 offen offset:2048 lds
s_waitcnt lgkmcnt(12)
Mp 32,144,216
Rt 128,252,24576
Mp 40,144,220
Rt 130,252,25088
s_add_u32 s92, s42, 0x180
Mp 44,148,220
Rt 132,253,24576
s_add_u32 m0, s41, 98304
Mp 36,148,216
Rt 134,253,25088
buffer_load_dwordx4 v240, s[16:19], s92 offen offset:0 lds
Mp 48,152,216
Rt 136,254,24576
Mp 56,152,220
Rt 138,254,25088
Mp 60,156,220
Rt 140,255,24576
Mp 52,156,216
Rt 142,255,25088
buffer_load_dwordx4 v241, s[16:19], s92 offen offset:1024 lds
s_waitcnt lgkmcnt(8)
s_barrier
Mp 32,160,224
Rt 144,244,0
Mp 40,160,228
Rt 146,244,512
Mp 44,164,228
Rt 148,245,0
Mp 36,164,224
Rt 150,245,512
buffer_load_dwordx4 v242, s[16:19], s92 offen offset:2048 lds
Mp 48,168,224
Rt 152,246,0
Mp 56,168,228
Rt 154,246,512
Mp 60,172,228
Rt 156,247,0
Mp 52,172,224
Rt 158,247,512
buffer_load_dwordx4 v243, s[16:19], s92 offen offset:3072 lds
s_waitcnt lgkmcnt(8)
s_barrier
Mp 32,128,232
Rt 160,244,4096
Mp 40,128,236
Rt 162,244,4608
s_add_u32 s92, s42, 0x40180
Mp 44,132,236
)" R"(
Rt 164,245,4096
s_add_u32 m0, s41, 102400
Mp 36,132,232
Rt 166,245,4608
buffer_load_dwordx4 v240, s[16:19], s92 offen offset:0 lds
Mp 48,136,232
Rt 168,246,4096
Mp 56,136,236
Rt 170,246,4608
Mp 60,140,236
Rt 172,247,4096
Mp 52,140,232
Rt 174,247,4608
buffer_load_dwordx4 v241, s[16:19], s92 offen offset:1024 lds
s_waitcnt lgkmcnt(8)
Mp 64,144,184
Rt 128,244,8192
Mp 72,144,188
Rt 130,244,8704
Mp 76,148,188
Rt 132,245,8192
Mp 68,148,184
Rt 134,245,8704
buffer_load_dwordx4 v242, s[16:19], s92 offen offset:2048 lds
Mp 80,152,184
Rt 136,246,8192
v_mul_f32_e32 v32, s54, v32
Mp 88,152,188
Rt 138,246,8704
s_add_u32 s92, s42, 0x1c0100
Mp 92,156,188
Rt 140,247,8192
s_add_u32 m0, s41, 32768
Mp 84,156,184
Rt 142,247,8704
buffer_load_dwordx4 v240, s[16:19], s92 offen offset:0 lds
s_waitcnt lgkmcnt(8)
Mp 64,160,192
Rt 144,244,12288
v_mul_f32_e32 v33, s54, v33
Mp 72,160,196
Rt 146,244,12800
Cv 32,32,33
Mp 76,164,196
Rt 148,245,12288
v_accvgpr_write_b32 a64, v32
v_mul_f32_e32 v34, s54, v34
Mp 68,164,192
Rt 150,245,12800
buffer_load_dwordx4 v241, s[16:19], s92 offen offset:1024 lds
Mp 80,168,192
Rt 152,246,12288
v_mul_f32_e32 v35, s54, v35
Mp 88,168,196
Rt 154,246,12800
Cv 34,34,35
v_accvgpr_write_b32 a65, v34
Mp 92,172,196
Rt 156,247,12288
v_mul_f32_e32 v36, s54, v36
Mp 84,172,192
Rt 158,247,12800
buffer_load_dwordx4 v242, s[16:19], s92 offen offset:2048 lds
s_waitcnt lgkmcnt(8)
)" R"(
Mp 64,128,200
Rt 160,244,16384
v_mul_f32_e32 v37, s54, v37
Cv 36,36,37
Mp 72,128,204
Rt 162,244,16896
v_accvgpr_write_b32 a66, v36
Mp 76,132,204
Rt 164,245,16384
v_mul_f32_e32 v38, s54, v38
Mp 68,132,200
Rt 166,245,16896
buffer_load_dwordx4 v243, s[16:19], s92 offen offset:3072 lds
Mp 80,136,200
Rt 168,246,16384
v_mul_f32_e32 v39, s54, v39
Mp 88,136,204
Rt 170,246,16896
s_add_u32 s92, s42, 0x200100
Mp 92,140,204
Rt 172,247,16384
s_add_u32 m0, s41, 36864
Mp 84,140,200
Rt 174,247,16896
buffer_load_dwordx4 v240, s[16:19], s92 offen offset:0 lds
s_waitcnt lgkmcnt(8)
Mp 64,144,208
Rt 128,244,20480
Cv 38,38,39
Mp 72,144,212
Rt 130,244,20992
v_accvgpr_write_b32 a67, v38
Mp 76,148,212
Rt 132,245,20480
v_mul_f32_e32 v40, s54, v40
v_mul_f32_e32 v41, s54, v41
Mp 68,148,208
Rt 134,245,20992
buffer_load_dwordx4 v241, s[16:19], s92 offen offset:1024 lds
Mp 80,152,208
Rt 136,246,20480
Cv 40,40,41
Mp 88,152,212
Rt 138,246,20992
v_accvgpr_write_b32 a68, v40
v_mul_f32_e32 v42, s54, v42
Mp 92,156,212
Rt 140,247,20480
v_mul_f32_e32 v43, s54, v43
Mp 84,156,208
Rt 142,247,20992
buffer_load_dwordx4 v242, s[16:19], s92 offen offset:2048 lds
s_waitcnt lgkmcnt(8)
Mp 64,160,216
Rt 144,244,24576
Cv 42,42,43
v_accvgpr_write_b32 a69, v42
Mp 72,160,220
Rt 146,244,25088
v_mul_f32_e32 v44, s54, v44
Mp 76,164,220
Rt 148,245,24576
v_mul_f32_e32 v45, s54, v45
Mp 68,164,216
)" R"(
Rt 150,245,25088
s_add_u32 s92, s42, 0x1c0180
Cv 44,44,45
Mp 80,168,216
Rt 152,246,24576
s_add_u32 m0, s41, 131072
Mp 88,168,220
Rt 154,246,25088
buffer_load_dwordx4 v240, s[16:19], s92 offen offset:0 lds
Mp 92,172,220
Rt 156,247,24576
v_accvgpr_write_b32 a70, v44
Mp 84,172,216
Rt 158,247,25088
s_waitcnt vmcnt(15) lgkmcnt(8)
s_barrier
Mp 64,128,224
Rt 160,248,0
v_mul_f32_e32 v46, s54, v46
Mp 72,128,228
Rt 162,248,512
buffer_load_dwordx4 v241, s[16:19], s92 offen offset:1024 lds
Mp 76,132,228
Rt 164,249,0
v_mul_f32_e32 v47, s54, v47
Cv 46,46,47
Mp 68,132,224
Rt 166,249,512
v_accvgpr_write_b32 a71, v46
Mp 80,136,224
Rt 168,250,0
v_mul_f32_e32 v48, s54, v48
Mp 88,136,228
Rt 170,250,512
buffer_load_dwordx4 v242, s[16:19], s92 offen offset:2048 lds
Mp 92,140,228
Rt 172,251,0
v_mul_f32_e32 v49, s54, v49
Mp 84,140,224
Rt 174,251,512
s_waitcnt vmcnt(3) lgkmcnt(8)
s_barrier
Mp 64,144,232
Rt 128,248,4096
Cv 48,48,49
v_accvgpr_write_b32 a72, v48
Mp 72,144,236
Rt 130,248,4608
buffer_load_dwordx4 v243, s[16:19], s92 offen offset:3072 lds
Mp 76,148,236
Rt 132,249,4096
v_mul_f32_e32 v50, s54, v50
Mp 68,148,232
Rt 134,249,4608
s_add_u32 s92, s42, 0x200180
v_mul_f32_e32 v51, s54, v51
Mp 80,152,232
Rt 136,250,4096
s_add_u32 m0, s41, 135168
Mp 88,152,236
Rt 138,250,4608
buffer_load_dwordx4 v240, s[16:19], s92 offen offset:0 lds
Mp 92,156,236
Rt 140,251,4096
Cv 50,50,51
Mp 84,156,232
)" R"(
Rt 142,251,4608
v_accvgpr_write_b32 a73, v50
s_waitcnt lgkmcnt(8)
v_mfma_f32_16x16x32_bf16 v[96:99], a[160:163], v[128:131], 0
Rt 144,248,8192
v_mul_f32_e32 v52, s54, v52
v_mfma_f32_16x16x32_bf16 v[104:107], a[160:163], v[132:135], 0
Rt 146,248,8704
buffer_load_dwordx4 v241, s[16:19], s92 offen offset:1024 lds
v_mfma_f32_16x16x32_bf16 v[108:111], a[164:167], v[132:135], 0
Rt 148,249,8192
v_mul_f32_e32 v53, s54, v53
Cv 52,52,53
v_mfma_f32_16x16x32_bf16 v[100:103], a[164:167], v[128:131], 0
Rt 150,249,8704
v_accvgpr_write_b32 a74, v52
v_mfma_f32_16x16x32_bf16 v[112:115], a[168:171], v[128:131], 0
Rt 152,250,8192
v_mul_f32_e32 v54, s54, v54
v_mfma_f32_16x16x32_bf16 v[120:123], a[168:171], v[132:135], 0
Rt 154,250,8704
buffer_load_dwordx4 v242, s[16:19], s92 offen offset:2048 lds
v_mfma_f32_16x16x32_bf16 v[124:127], a[172:175], v[132:135], 0
Rt 156,251,8192
v_mul_f32_e32 v55, s54, v55
v_mfma_f32_16x16x32_bf16 v[116:119], a[172:175], v[128:131], 0
Rt 158,251,8704
s_waitcnt lgkmcnt(8)
Mp 96,128,136
Rt 160,248,12288
Cv 54,54,55
v_accvgpr_write_b32 a75, v54
Mp 104,128,140
Rt 162,248,12800
s_add_u32 s38, s38, 0xffff0080
Mp 108,132,140
Rt 164,249,12288
s_add_u32 m0, s36, 0
Mp 100,132,136
Rt 166,249,12800
buffer_load_dwordx4 v8, s[4:7], s38 offen offset:0 lds
Mp 112,136,136
Rt 168,250,12288
v_mul_f32_e32 v56, s54, v56
Mp 120,136,140
Rt 170,250,12800
v_mul_f32_e32 v57, s54, v57
Mp 124,140,140
Rt 172,251,12288
Cv 56,56,57
Mp 116,140,136
Rt 174,251,12800
buffer_load_dwordx4 v9, s[4:7], s38 offen offset:1024 lds
s_waitcnt lgkmcnt(8)
Mp 96,144,144
Rt 128,248,16384
v_accvgpr_write_b32 a76, v56
Mp 104,144,148
Rt 130,248,16896
v_mul_f32_e32 v58, s54, v58
Mp 108,148,148
Rt 132,249,16384
v_mul_f32_e32 v59, s54, v59
Cv 58,58,59
)" R"(
Mp 100,148,144
Rt 134,249,16896
buffer_load_dwordx4 v10, s[4:7], s38 offen offset:2048 lds
Mp 112,152,144
Rt 136,250,16384
v_accvgpr_write_b32 a77, v58
Mp 120,152,148
Rt 138,250,16896
v_mul_f32_e32 v60, s54, v60
v_mul_f32_e32 v61, s54, v61
Mp 124,156,148
Rt 140,251,16384
Cv 60,60,61
s_lshl1_add_u32 s88, s34, s91
s_lshl_b32 s88, s88, 12
Mp 116,156,144
Rt 142,251,16896
buffer_load_dwordx4 v11, s[4:7], s38 offen offset:3072 lds
s_waitcnt lgkmcnt(8)
Mp 96,160,152
Rt 144,248,20480
v_accvgpr_write_b32 a78, v60
v_mul_f32_e32 v62, s54, v62
s_and_b32 s29, s38, 0xfffe0000
Mp 104,160,156
Rt 146,248,20992
s_add_u32 s38, s38, 0x10000
s_add_u32 s88, s88, s29
Mp 108,164,156
Rt 148,249,20480
s_add_u32 m0, s36, 4096
v_min_u32 v28, 31, v0
Mp 100,164,152
Rt 150,249,20992
buffer_load_dwordx4 v8, s[4:7], s38 offen offset:0 lds
v_lshlrev_b32 v28, 7, v28
Mp 112,168,152
Rt 152,250,20480
v_mul_f32_e32 v63, s54, v63
Mp 120,168,156
Rt 154,250,20992
Cv 62,62,63
Mp 124,172,156
Rt 156,251,20480
v_accvgpr_write_b32 a79, v62
Mp 116,172,152
Rt 158,251,20992
buffer_load_dwordx4 v9, s[4:7], s38 offen offset:1024 lds
s_waitcnt lgkmcnt(8)
Mp 96,128,160
Rt 160,248,24576
v_mul_f32_e32 v64, s54, v64
Mp 104,128,164
Rt 162,248,25088
v_mul_f32_e32 v65, s54, v65
Mp 108,132,164
Rt 164,249,24576
Cv 64,64,65
v_accvgpr_write_b32 a80, v64
Mp 100,132,160
Rt 166,249,25088
buffer_load_dwordx4 v10, s[4:7], s38 offen offset:2048 lds
Mp 112,136,160
Rt 168,250,24576
)" R"(
v_mul_f32_e32 v66, s54, v66
Mp 120,136,164
Rt 170,250,25088
v_mul_f32_e32 v67, s54, v67
Cv 66,66,67
Mp 124,140,164
Rt 172,251,24576
v_accvgpr_write_b32 a81, v66
Mp 116,140,160
Rt 174,251,25088
buffer_load_dwordx4 v11, s[4:7], s38 offen offset:3072 lds
s_waitcnt lgkmcnt(8)
Mp 96,144,168
Rt 128,244,32768
v_mul_f32_e32 v68, s54, v68
v_mul_f32_e32 v69, s54, v69
Mp 104,144,172
Rt 130,244,33280
Cv 68,68,69
Mp 108,148,172
Rt 132,245,32768
v_accvgpr_write_b32 a82, v68
buffer_load_dword a254, v28, s[4:7], s88 offen
Mp 100,148,168
Rt 134,245,33280
v_mul_f32_e32 v70, s54, v70
v_mul_f32_e32 v71, s54, v71
Mp 112,152,168
Rt 136,246,32768
Cv 70,70,71
Mp 120,152,172
Rt 138,246,33280
v_accvgpr_write_b32 a83, v70
Mp 124,156,172
Rt 140,247,32768
v_mul_f32_e32 v72, s54, v72
Mp 116,156,168
Rt 142,247,33280
s_waitcnt lgkmcnt(8)
s_barrier
Mp 96,160,176
Rt 144,244,36864
v_mul_f32_e32 v73, s54, v73
Mp 104,160,180
Rt 146,244,37376
s_add_u32 s39, s39, 0x40000
Mp 108,164,180
Rt 148,245,36864
s_add_u32 m0, s37, 0
Cv 72,72,73
Mp 100,164,176
Rt 150,245,37376
buffer_load_dwordx4 v12, s[8:11], s39 offen offset:0 lds
Mp 112,168,176
Rt 152,246,36864
v_accvgpr_write_b32 a84, v72
Mp 120,168,180
Rt 154,246,37376
v_mul_f32_e32 v74, s54, v74
v_mul_f32_e32 v75, s54, v75
Mp 124,172,180
Rt 156,247,36864
Cv 74,74,75
Mp 116,172,176
Rt 158,247,37376
)" R"(
s_waitcnt lgkmcnt(8)
Mp 96,128,184
Rt 160,244,40960
v_accvgpr_write_b32 a85, v74
v_mul_f32_e32 v76, s54, v76
Mp 104,128,188
Rt 162,244,41472
buffer_load_dwordx4 v13, s[8:11], s39 offen offset:1024 lds
Mp 108,132,188
Rt 164,245,40960
v_mul_f32_e32 v77, s54, v77
Mp 100,132,184
Rt 166,245,41472
Cv 76,76,77
v_accvgpr_write_b32 a86, v76
Mp 112,136,184
Rt 168,246,40960
v_mul_f32_e32 v78, s54, v78
Mp 120,136,188
Rt 170,246,41472
v_mul_f32_e32 v79, s54, v79
Mp 124,140,188
Rt 172,247,40960
Cv 78,78,79
Mp 116,140,184
Rt 174,247,41472
buffer_load_dwordx4 v14, s[8:11], s39 offen offset:2048 lds
s_waitcnt lgkmcnt(8)
Mp 96,144,192
Rt 128,244,45056
v_accvgpr_write_b32 a87, v78
Mp 104,144,196
Rt 130,244,45568
v_mul_f32_e32 v80, s54, v80
Mp 108,148,196
Rt 132,245,45056
v_mul_f32_e32 v81, s54, v81
Cv 80,80,81
Mp 100,148,192
Rt 134,245,45568
v_accvgpr_write_b32 a88, v80
Mp 112,152,192
Rt 136,246,45056
v_mul_f32_e32 v82, s54, v82
.LzH1:
s_cbranch_execz .LzH18
Mp 120,152,196
Rt 138,246,45568
buffer_load_dwordx4 v15, s[8:11], s39 offen offset:3072 lds
Mp 124,156,196
Rt 140,247,45056
v_mul_f32_e32 v83, s54, v83
Mp 116,156,192
Rt 142,247,45568
s_waitcnt lgkmcnt(8)
Mp 96,160,200
Rt 144,244,49152
Cv 82,82,83
v_accvgpr_write_b32 a89, v82
Mp 104,160,204
Rt 146,244,49664
s_add_u32 s39, s39, 0x40000
Mp 108,164,204
Rt 148,245,49152
)" R"(
s_add_u32 m0, s37, 16384
Mp 100,164,200
Rt 150,245,49664
buffer_load_dwordx4 v12, s[8:11], s39 offen offset:0 lds
Mp 112,168,200
Rt 152,246,49152
v_mul_f32_e32 v84, s54, v84
Mp 120,168,204
Rt 154,246,49664
v_mul_f32_e32 v85, s54, v85
Mp 124,172,204
Rt 156,247,49152
Cv 84,84,85
Mp 116,172,200
Rt 158,247,49664
v_accvgpr_write_b32 a90, v84
s_waitcnt lgkmcnt(8)
Mp 96,128,208
Rt 160,244,53248
v_mul_f32_e32 v86, s54, v86
Mp 104,128,212
Rt 162,244,53760
buffer_load_dwordx4 v13, s[8:11], s39 offen offset:1024 lds
Mp 108,132,212
Rt 164,245,53248
v_mul_f32_e32 v87, s54, v87
Cv 86,86,87
Mp 100,132,208
Rt 166,245,53760
v_accvgpr_write_b32 a91, v86
Mp 112,136,208
Rt 168,246,53248
v_mul_f32_e32 v88, s54, v88
Mp 120,136,212
Rt 170,246,53760
v_mul_f32_e32 v89, s54, v89
Cv 88,88,89
Mp 124,140,212
Rt 172,247,53248
v_accvgpr_write_b32 a92, v88
Mp 116,140,208
Rt 174,247,53760
buffer_load_dwordx4 v14, s[8:11], s39 offen offset:2048 lds
s_waitcnt lgkmcnt(8)
Mp 96,144,216
Rt 128,244,57344
v_mul_f32_e32 v90, s54, v90
v_mul_f32_e32 v91, s54, v91
Mp 104,144,220
Rt 130,244,57856
Cv 90,90,91
Mp 108,148,220
Rt 132,245,57344
v_accvgpr_write_b32 a93, v90
Mp 100,148,216
Rt 134,245,57856
v_mul_f32_e32 v92, s54, v92
v_mul_f32_e32 v93, s54, v93
Mp 112,152,216
Rt 136,246,57344
Cv 92,92,93
Mp 120,152,220
Rt 138,246,57856
buffer_load_dwordx4 v15, s[8:11], s39 offen offset:3072 lds
)" R"(
Mp 124,156,220
Rt 140,247,57344
v_accvgpr_write_b32 a94, v92
Mp 116,156,216
Rt 142,247,57856
v_mul_f32_e32 v94, s54, v94
s_waitcnt lgkmcnt(8)
Mp 96,160,224
Rt 144,248,32768
v_mul_f32_e32 v95, s54, v95
Mp 104,160,228
Rt 146,248,33280
Cv 94,94,95
Mp 108,164,228
Rt 148,249,32768
v_accvgpr_write_b32 a95, v94
Mp 100,164,224
Rt 150,249,33280
Mp 112,168,224
Rt 152,250,32768
Mp 120,168,228
Rt 154,250,33280
Mp 124,172,228
Rt 156,251,32768
Mp 116,172,224
Rt 158,251,33280
s_waitcnt vmcnt(17) lgkmcnt(8)
s_barrier
Mp 96,128,232
Rt 160,248,36864
Mp 104,128,236
Rt 162,248,37376
s_add_u32 s38, s38, 0xffff0080
Mp 108,132,236
Rt 164,249,36864
s_add_u32 m0, s36, 32768
Mp 100,132,232
Rt 166,249,37376
buffer_load_dwordx4 v8, s[4:7], s38 offen offset:0 lds
Mp 112,136,232
Rt 168,250,36864
Mp 120,136,236
Rt 170,250,37376
Mp 124,140,236
Rt 172,251,36864
Mp 116,140,232
Rt 174,251,37376
s_waitcnt lgkmcnt(8)
v_mfma_f32_16x16x32_bf16 v[64:67], a[144:147], v[128:131], 0
Rt 128,248,40960
v_mfma_f32_16x16x32_bf16 v[72:75], a[144:147], v[132:135], 0
Rt 130,248,41472
buffer_load_dwordx4 v9, s[4:7], s38 offen offset:1024 lds
v_mfma_f32_16x16x32_bf16 v[76:79], a[148:151], v[132:135], 0
Rt 132,249,40960
v_mfma_f32_16x16x32_bf16 v[68:71], a[148:151], v[128:131], 0
Rt 134,249,41472
v_mfma_f32_16x16x32_bf16 v[80:83], a[152:155], v[128:131], 0
Rt 136,250,40960
v_mul_f32_e32 v96, s54, v96
v_mfma_f32_16x16x32_bf16 v[88:91], a[152:155], v[132:135], 0
Rt 138,250,41472
v_mul_f32_e32 v97, s54, v97
v_mfma_f32_16x16x32_bf16 v[92:95], a[156:159], v[132:135], 0
Rt 140,251,40960
)" R"(
Cv 96,96,97
v_mfma_f32_16x16x32_bf16 v[84:87], a[156:159], v[128:131], 0
Rt 142,251,41472
buffer_load_dwordx4 v10, s[4:7], s38 offen offset:2048 lds
s_waitcnt lgkmcnt(8)
Mp 64,160,136
Rt 144,248,45056
v_accvgpr_write_b32 a96, v96
Mp 72,160,140
Rt 146,248,45568
v_mul_f32_e32 v98, s54, v98
Mp 76,164,140
Rt 148,249,45056
v_mul_f32_e32 v99, s54, v99
Mp 68,164,136
Rt 150,249,45568
Cv 98,98,99
Mp 80,168,136
Rt 152,250,45056
v_accvgpr_write_b32 a97, v98
Mp 88,168,140
Rt 154,250,45568
buffer_load_dwordx4 v11, s[4:7], s38 offen offset:3072 lds
Mp 92,172,140
Rt 156,251,45056
v_mul_f32_e32 v100, s54, v100
Mp 84,172,136
Rt 158,251,45568
s_waitcnt lgkmcnt(8)
Mp 64,128,144
Rt 160,248,49152
v_mul_f32_e32 v101, s54, v101
Mp 72,128,148
Rt 162,248,49664
s_add_u32 s38, s38, 0x10000
Mp 76,132,148
Rt 164,249,49152
s_add_u32 m0, s36, 36864
Mp 68,132,144
Rt 166,249,49664
buffer_load_dwordx4 v8, s[4:7], s38 offen offset:0 lds
Mp 80,136,144
Rt 168,250,49152
Cv 100,100,101
Mp 88,136,148
Rt 170,250,49664
v_accvgpr_write_b32 a98, v100
Mp 92,140,148
Rt 172,251,49152
v_mul_f32_e32 v102, s54, v102
Mp 84,140,144
Rt 174,251,49664
s_waitcnt lgkmcnt(8)
Mp 64,144,152
Rt 128,248,53248
v_mul_f32_e32 v103, s54, v103
Mp 72,144,156
Rt 130,248,53760
buffer_load_dwordx4 v9, s[4:7], s38 offen offset:1024 lds
Mp 76,148,156
Rt 132,249,53248
Cv 102,102,103
Mp 68,148,152
Rt 134,249,53760
)" R"(
v_accvgpr_write_b32 a99, v102
Mp 80,152,152
Rt 136,250,53248
v_mul_f32_e32 v104, s54, v104
Mp 88,152,156
Rt 138,250,53760
v_mul_f32_e32 v105, s54, v105
Mp 92,156,156
Rt 140,251,53248
Cv 104,104,105
Mp 84,156,152
Rt 142,251,53760
buffer_load_dwordx4 v10, s[4:7], s38 offen offset:2048 lds
s_waitcnt lgkmcnt(8)
Mp 64,160,160
Rt 144,248,57344
v_accvgpr_write_b32 a100, v104
Mp 72,160,164
Rt 146,248,57856
v_mul_f32_e32 v106, s54, v106
Mp 76,164,164
Rt 148,249,57344
v_mul_f32_e32 v107, s54, v107
Mp 68,164,160
Rt 150,249,57856
Cv 106,106,107
Mp 80,168,160
Rt 152,250,57344
v_accvgpr_write_b32 a101, v106
Mp 88,168,164
Rt 154,250,57856
buffer_load_dwordx4 v11, s[4:7], s38 offen offset:3072 lds
Mp 92,172,164
Rt 156,251,57344
v_mul_f32_e32 v108, s54, v108
Mp 84,172,160
Rt 158,251,57856
s_waitcnt lgkmcnt(8)
Mp 64,128,168
Rt 160,252,0
v_mul_f32_e32 v109, s54, v109
Mp 72,128,172
Rt 162,252,512
Cv 108,108,109
Mp 76,132,172
Rt 164,253,0
v_accvgpr_write_b32 a102, v108
Mp 68,132,168
Rt 166,253,512
v_mul_f32_e32 v110, s54, v110
Mp 80,136,168
Rt 168,254,0
v_mul_f32_e32 v111, s54, v111
Mp 88,136,172
Rt 170,254,512
Cv 110,110,111
Mp 92,140,172
Rt 172,255,0
v_accvgpr_write_b32 a103, v110
Mp 84,140,168
Rt 174,255,512
s_waitcnt vmcnt(12) lgkmcnt(8)
s_barrier
Mp 64,144,176
Rt 128,252,4096
)" R"(
v_mul_f32_e32 v112, s54, v112
Mp 72,144,180
Rt 130,252,4608
s_add_u32 s39, s39, 0x40000
Mp 76,148,180
Rt 132,253,4096
s_add_u32 m0, s37, 32768
Mp 68,148,176
Rt 134,253,4608
buffer_load_dwordx4 v12, s[8:11], s39 offen offset:0 lds
Mp 80,152,176
Rt 136,254,4096
v_mul_f32_e32 v113, s54, v113
Mp 88,152,180
Rt 138,254,4608
Cv 112,112,113
Mp 92,156,180
Rt 140,255,4096
v_accvgpr_write_b32 a104, v112
Mp 84,156,176
Rt 142,255,4608
s_waitcnt lgkmcnt(8)
Mp 64,160,184
Rt 144,252,8192
v_mul_f32_e32 v114, s54, v114
Mp 72,160,188
Rt 146,252,8704
buffer_load_dwordx4 v13, s[8:11], s39 offen offset:1024 lds
Mp 76,164,188
Rt 148,253,8192
v_mul_f32_e32 v115, s54, v115
Mp 68,164,184
Rt 150,253,8704
Cv 114,114,115
Mp 80,168,184
Rt 152,254,8192
v_accvgpr_write_b32 a105, v114
Mp 88,168,188
Rt 154,254,8704
v_mul_f32_e32 v116, s54, v116
Mp 92,172,188
Rt 156,255,8192
v_mul_f32_e32 v117, s54, v117
Mp 84,172,184
Rt 158,255,8704
buffer_load_dwordx4 v14, s[8:11], s39 offen offset:2048 lds
s_waitcnt lgkmcnt(8)
Mp 64,128,192
Rt 160,252,12288
Cv 116,116,117
Mp 72,128,196
Rt 162,252,12800
v_accvgpr_write_b32 a106, v116
Mp 76,132,196
Rt 164,253,12288
v_mul_f32_e32 v118, s54, v118
Mp 68,132,192
Rt 166,253,12800
v_mul_f32_e32 v119, s54, v119
Mp 80,136,192
Rt 168,254,12288
Cv 118,118,119
Mp 88,136,196
Rt 170,254,12800
)" R"(
buffer_load_dwordx4 v15, s[8:11], s39 offen offset:3072 lds
Mp 92,140,196
Rt 172,255,12288
v_accvgpr_write_b32 a107, v118
Mp 84,140,192
Rt 174,255,12800
s_waitcnt lgkmcnt(8)
Mp 64,144,200
Rt 128,252,16384
v_mul_f32_e32 v120, s54, v120
Mp 72,144,204
Rt 130,252,16896
s_add_u32 s39, s39, 0x40000
Mp 76,148,204
Rt 132,253,16384
s_add_u32 m0, s37, 49152
Mp 68,148,200
Rt 134,253,16896
buffer_load_dwordx4 v12, s[8:11], s39 offen offset:0 lds
Mp 80,152,200
Rt 136,254,16384
v_mul_f32_e32 v121, s54, v121
Mp 88,152,204
Rt 138,254,16896
Cv 120,120,121
Mp 92,156,204
Rt 140,255,16384
v_accvgpr_write_b32 a108, v120
Mp 84,156,200
Rt 142,255,16896
s_waitcnt lgkmcnt(8)
Mp 64,160,208
Rt 144,252,20480
Rt 0,18,0
Mp 72,160,212
Rt 146,252,20992
buffer_load_dwordx4 v13, s[8:11], s39 offen offset:1024 lds
Rt 2,18,2048
Mp 76,164,212
Rt 148,253,20480
Rt 4,19,0
Mp 68,164,208
Rt 150,253,20992
Rt 6,19,2048
Mp 80,168,208
Rt 152,254,20480
Rt 8,20,0
Mp 88,168,212
Rt 154,254,20992
Rt 10,20,2048
Mp 92,172,212
Rt 156,255,20480
Rt 12,21,0
Mp 84,172,208
Rt 158,255,20992
buffer_load_dwordx4 v14, s[8:11], s39 offen offset:2048 lds
Rt 14,21,2048
Mp 64,128,216
Rt 160,252,24576
Rt 16,22,0
Mp 72,128,220
Rt 162,252,25088
Rt 18,22,2048
Mp 76,132,220
)" R"(
Rt 164,253,24576
Rt 20,23,0
Mp 68,132,216
Rt 166,253,25088
Rt 22,23,2048
Mp 80,136,216
Rt 168,254,24576
Rt 24,24,0
Mp 88,136,220
Rt 170,254,25088
buffer_load_dwordx4 v15, s[8:11], s39 offen offset:3072 lds
Rt 26,24,2048
Mp 92,140,220
Rt 172,255,24576
Rt 28,25,0
Mp 84,140,216
Rt 174,255,25088
Rt 30,25,2048
Mp 64,144,224
Rb 32,16,0
v_mul_f32_e32 v122, s54, v122
Mp 72,144,228
Rb 36,16,2048
v_mul_f32_e32 v123, s54, v123
Mp 76,148,228
Rb 40,16,8192
Cv 122,122,123
Mp 68,148,224
Rb 44,16,10240
v_accvgpr_write_b32 a109, v122
Mp 80,152,224
Rb 48,16,16384
v_mul_f32_e32 v124, s54, v124
Mp 88,152,228
Rb 52,16,18432
v_mul_f32_e32 v125, s54, v125
Mp 92,156,228
Rb 56,16,24576
Cv 124,124,125
Mp 84,156,224
Rb 60,16,26624
s_waitcnt lgkmcnt(9)
Mp 64,160,232
v_accvgpr_write_b32 a110, v124
v_mul_f32_e32 v126, s54, v126
Mp 72,160,236
v_mul_f32_e32 v127, s54, v127
Cv 126,126,127
Mp 76,164,236
v_accvgpr_write_b32 a111, v126
Mp 68,164,232
Mp 80,168,232
Mp 88,168,236
Mp 92,172,236
Mp 84,172,232
s_cmp_eq_u32 s91, 1
s_waitcnt vmcnt(16) lgkmcnt(0)
s_barrier
s_cbranch_scc1 .LzBV
Mz 32,0,32
Rt 128,18,16384
v_mul_f32_e32 v64, s54, v64
Mz 96,0,40
v_mul_f32_e32 v65, s54, v65
Cv 64,64,65
)" R"(
Mz 128,0,44
Rt 130,18,18432
v_accvgpr_write_b32 a112, v64
Mz 160,0,48
s_add_u32 s39, s39, 0x40000
v_mul_f32_e32 v66, s54, v66
Mz 192,0,52
Rt 132,19,16384
s_add_u32 m0, s37, 0
Mz 224,0,56
buffer_load_dwordx4 v12, s[8:11], s39 offen offset:0 lds
v_mfma_f32_16x16x32_bf16 a[192:195], a[0:3], a[60:63], 0
Rt 134,19,18432
v_mul_f32_e32 v67, s54, v67
v_mfma_f32_16x16x32_bf16 a[196:199], a[4:7], a[60:63], 0
Cv 66,66,67
v_accvgpr_write_b32 a113, v66
Mz 228,4,56
Rt 136,20,16384
v_mul_f32_e32 v68, s54, v68
Mz 196,4,52
v_mul_f32_e32 v69, s54, v69
Cv 68,68,69
Mz 164,4,48
Rt 138,20,18432
v_accvgpr_write_b32 a114, v68
Mz 132,4,44
v_mul_f32_e32 v70, s54, v70
v_mul_f32_e32 v71, s54, v71
Mz 100,4,40
Rt 140,21,16384
Cv 70,70,71
Mz 36,4,32
buffer_load_dwordx4 v13, s[8:11], s39 offen offset:1024 lds
Mz 40,8,32
Rt 142,21,18432
v_accvgpr_write_b32 a115, v70
Mz 104,8,40
v_mul_f32_e32 v72, s54, v72
v_mul_f32_e32 v73, s54, v73
Mz 136,8,44
Rt 144,22,16384
Cv 72,72,73
Mz 168,8,48
v_accvgpr_write_b32 a116, v72
v_mul_f32_e32 v74, s54, v74
Mz 200,8,52
Rt 146,22,18432
v_mul_f32_e32 v75, s54, v75
Mz 232,8,56
Cv 74,74,75
v_accvgpr_write_b32 a117, v74
v_mfma_f32_16x16x32_bf16 a[200:203], a[8:11], a[60:63], 0
Rt 148,23,16384
v_mul_f32_e32 v76, s54, v76
v_mfma_f32_16x16x32_bf16 a[204:207], a[12:15], a[60:63], 0
buffer_load_dwordx4 v14, s[8:11], s39 offen offset:2048 lds
Mz 236,12,56
Rt 150,23,18432
v_mul_f32_e32 v77, s54, v77
Mz 204,12,52
Cv 76,76,77
v_accvgpr_write_b32 a118, v76
Mz 172,12,48
)" R"(
Rt 152,24,16384
v_mul_f32_e32 v78, s54, v78
Mz 140,12,44
v_mul_f32_e32 v79, s54, v79
Cv 78,78,79
Mz 108,12,40
Rt 154,24,18432
v_accvgpr_write_b32 a119, v78
Mz 44,12,32
v_mul_f32_e32 v80, s54, v80
v_mul_f32_e32 v81, s54, v81
Mz 48,16,32
Rt 156,25,16384
Cv 80,80,81
Mz 112,16,40
buffer_load_dwordx4 v15, s[8:11], s39 offen offset:3072 lds
Mz 144,16,44
Rt 158,25,18432
v_accvgpr_write_b32 a120, v80
Mz 176,16,48
v_mul_f32_e32 v82, s54, v82
v_mul_f32_e32 v83, s54, v83
s_mov_b32 s85, 0x1508800
.LzH2:
s_cbranch_execz .LzH12
Mz 208,16,52
Rb 160,17,0
Cv 82,82,83
v_and_b32 v2, 7, v0
Mz 240,16,56
v_accvgpr_write_b32 a121, v82
v_mul_f32_e32 v84, s54, v84
v_lshlrev_b32 v2, 7, v2
v_mfma_f32_16x16x32_bf16 a[208:211], a[16:19], a[60:63], 0
Rb 164,17,2048
v_mul_f32_e32 v85, s54, v85
buffer_load_dword v4, v2, s[20:23], s85 offen sc0 sc1
v_mfma_f32_16x16x32_bf16 a[212:215], a[20:23], a[60:63], 0
Cv 84,84,85
v_accvgpr_write_b32 a122, v84
Mz 244,20,56
Rb 168,17,8192
v_mul_f32_e32 v86, s54, v86
Mz 212,20,52
v_mul_f32_e32 v87, s54, v87
Cv 86,86,87
Mz 180,20,48
Rb 172,17,10240
v_accvgpr_write_b32 a123, v86
Mz 148,20,44
v_mul_f32_e32 v88, s54, v88
v_mul_f32_e32 v89, s54, v89
Mz 116,20,40
Rb 176,17,16384
Cv 88,88,89
Mz 52,20,32
v_accvgpr_write_b32 a124, v88
v_mul_f32_e32 v90, s54, v90
Mz 56,24,32
Rb 180,17,18432
v_mul_f32_e32 v91, s54, v91
Mz 120,24,40
Cv 90,90,91
v_accvgpr_write_b32 a125, v90
)" R"(
Mz 152,24,44
Rb 184,17,24576
v_mul_f32_e32 v92, s54, v92
Mz 184,24,48
v_mul_f32_e32 v93, s54, v93
Cv 92,92,93
Mz 216,24,52
Rb 188,17,26624
v_accvgpr_write_b32 a126, v92
Mz 248,24,56
v_mul_f32_e32 v94, s54, v94
v_mul_f32_e32 v95, s54, v95
v_mfma_f32_16x16x32_bf16 a[216:219], a[24:27], a[60:63], 0
Cv 94,94,95
v_accvgpr_write_b32 a127, v94
v_mfma_f32_16x16x32_bf16 a[220:223], a[28:31], a[60:63], 0
Mz 252,28,56
Mz 220,28,52
Mz 188,28,48
Mz 156,28,44
Mz 124,28,40
Mz 60,28,32
s_lshl_b32 s29, s91, 22
s_add_u32 s28, s89, s29
Mz 64,0,36
s_bfe_u32 s29, s63, 0x80000
s_lshl_b32 s29, s29, 16
Mz 68,4,36
s_add_u32 s45, s28, s29
s_bfe_u32 s29, s63, 0x80008
Mz 72,8,36
s_lshl_b32 s29, s29, 16
s_add_u32 s46, s28, s29
Mz 76,12,36
s_bfe_u32 s29, s63, 0x80010
s_lshl_b32 s29, s29, 16
Mz 80,16,36
s_add_u32 s47, s28, s29
s_bfe_u32 s29, s63, 0x80018
s_lshl_b32 s29, s29, 16
Mz 84,20,36
s_add_u32 s48, s28, s29
s_add_u32 s50, s89, 0xd40000
Mz 88,24,36
s_lshl2_add_u32 s28, s91, s93
s_lshl_b32 s28, s28, 7
Mz 92,28,36
s_waitcnt vmcnt(9) lgkmcnt(0)
s_barrier
Mv 32,128,160
Rt 0,18,32768
s_add_u32 s49, s28, s50
Mv 64,128,164
s_add_u32 s38, s38, 0xffff0080
s_add_u32 s49, s49, 4096
Mv 96,128,168
Rt 2,18,34816
s_add_u32 m0, s36, 0
Mv 128,128,172
buffer_load_dwordx4 v8, s[4:7], s38 offen offset:0 lds
Mv 160,128,176
Rt 4,19,32768
s_lshl_b32 s52, s35, 7
s_add_u32 s52, s52, s50
Mv 192,128,180
)" R"(
s_add_u32 s52, s52, 0xd000
s_lshl_b32 s51, s35, 17
Mv 224,128,184
Rt 6,19,34816
s_add_u32 s51, s51, s89
Ma 192,128,188
buffer_load_dwordx4 v9, s[4:7], s38 offen offset:1024 lds
Ma 196,132,188
Rt 8,20,32768
s_add_u32 s51, s51, 0x800000
Mv 228,132,184
s_cmp_lg_u32 s33, 0
s_cmov_b32 s49, 0x40000000
s_cmov_b32 s52, 0x40000000
Mv 196,132,180
Rt 10,20,34816
s_mov_b32 s53, 0x40000
Mv 164,132,176
buffer_load_dwordx4 v10, s[4:7], s38 offen offset:2048 lds
Mv 132,132,172
Rt 12,21,32768
s_mov_b32 s62, 0x4e800000
Mv 100,132,168
s_add_u32 s67, s89, 0xc00000
s_lshl3_add_u32 s28, s34, s35
Mv 68,132,164
Rt 14,21,34816
s_add_u32 s64, s51, 0x100000
Mv 36,132,160
buffer_load_dwordx4 v11, s[4:7], s38 offen offset:3072 lds
Mv 40,136,160
Rt 16,22,32768
s_lshl2_add_u32 s28, s28, s33
Mv 72,136,164
s_add_u32 s38, s38, 0x10000
s_lshl2_add_u32 s65, s35, s33
s_lshl_b32 s65, s65, 7
Mv 104,136,168
Rt 18,22,34816
s_add_u32 m0, s36, 4096
Mv 136,136,172
buffer_load_dwordx4 v8, s[4:7], s38 offen offset:0 lds
Mv 168,136,176
Rt 20,23,32768
s_add_u32 s65, s65, s50
Mv 200,136,180
s_add_u32 s65, s65, 0xc000
s_lshl_b32 s66, s28, 10
Mv 232,136,184
Rt 22,23,34816
s_add_u32 s66, s66, 0x1400000
Ma 200,136,188
buffer_load_dwordx4 v9, s[4:7], s38 offen offset:1024 lds
Ma 204,140,188
Rt 24,24,32768
s_sub_u32 s28, 15, s35
Mv 236,140,184
s_lshl2_add_u32 s28, s28, s33
s_and_b32 s29, s34, 1
s_lshl1_add_u32 s29, s28, s29
Mv 204,140,180
Rt 26,24,34816
s_lshl_b32 s73, s29, 15
Mv 172,140,176
)" R"(
buffer_load_dwordx4 v10, s[4:7], s38 offen offset:2048 lds
Mv 140,140,172
Rt 28,25,32768
s_and_b32 s29, s89, 0xfe000000
Mv 108,140,168
s_add_u32 s73, s73, s29
s_add_u32 s73, s73, 0x1002000
Mv 76,140,164
Rt 30,25,34816
s_lshl4_add_u32 s28, s28, s34
s_lshl_b32 s68, s28, 8
Mv 44,140,160
buffer_load_dwordx4 v11, s[4:7], s38 offen offset:3072 lds
Mv 48,144,160
Rb 32,16,32768
s_add_u32 s68, s68, 0x1510000
Mv 80,144,164
s_xor_b32 s72, s73, 0x3c6000
s_mov_b32 s55, s89
Mv 112,144,168
Rb 36,16,34816
s_mov_b32 s57, 18
Mv 144,144,172
s_add_u32 s39, s39, 0x40000
s_mov_b32 s59, s67
Mv 176,144,176
Rb 40,16,40960
s_add_u32 m0, s37, 16384
Mv 208,144,180
buffer_load_dwordx4 v12, s[8:11], s39 offen offset:0 lds
Mv 240,144,184
Rb 44,16,43008
s_mov_b32 s58, 0
Ma 208,144,188
s_cmp_eq_u32 s35, 7
s_cmov_b32 s58, 2
Ma 212,148,188
Rb 48,16,49152
Mv 244,148,184
s_cmp_eq_u32 s35, 7
s_cmov_b32 s64, 0x40000000
Mv 212,148,180
Rb 52,16,51200
Mv 180,148,176
buffer_load_dwordx4 v13, s[8:11], s39 offen offset:1024 lds
Mv 148,148,172
Rb 56,16,57344
Mv 116,148,168
s_cmp_eq_u32 s35, 7
s_cmov_b32 s66, 0x40000000
Mv 84,148,164
Rb 60,16,59392
s_add_u32 s56, s55, 0x400000
Mv 52,148,160
Mv 56,152,160
Mv 88,152,164
buffer_load_dwordx4 v14, s[8:11], s39 offen offset:2048 lds
Mv 120,152,168
Mv 152,152,172
Mv 184,152,176
Mv 216,152,180
Mv 248,152,184
Ma 216,152,188
buffer_load_dwordx4 v15, s[8:11], s39 offen offset:3072 lds
)" R"(
Ma 220,156,188
Mv 252,156,184
Mv 220,156,180
Mv 188,156,176
Mv 156,156,172
Mv 124,156,168
Mv 92,156,164
Mv 60,156,160
s_waitcnt vmcnt(12) lgkmcnt(0)
s_barrier
Mv 32,0,32
Rt 128,18,49152
v_xor_b32 v4, s3, v4
v_cmp_gt_u32_e32 vcc, 2, v4
s_andn2_b32 s28, 0xff, vcc_lo
s_cbranch_scc1 .Lzsp13
.Lzok13:
Mv 64,0,36
v_xor_b32 v4, s3, v4
v_and_b32 v4, 1, v4
Mv 96,0,40
Rt 130,18,51200
Mv 128,0,44
s_add_u32 s39, s39, 0x40000
Mv 160,0,48
Rt 132,19,49152
s_add_u32 m0, s37, 32768
Mv 192,0,52
buffer_load_dwordx4 v12, s[8:11], s39 offen offset:0 lds
Mv 224,0,56
Rt 134,19,51200
Ma 192,0,60
v_cmp_eq_u32_e32 vcc, 1, v4
s_mov_b32 s85, vcc_lo
Ma 196,4,60
Rt 136,20,49152
s_and_b32 s28, s2, 7
Mv 228,4,56
s_lshr_b32 s28, s85, s28
s_xor_b32 s28, s28, s34
Mv 196,4,52
Rt 138,20,51200
s_and_b32 s28, s28, 1
Mv 164,4,48
s_lshl_b32 s84, s28, 13
s_add_u32 s49, s49, s84
Mv 132,4,44
Rt 140,21,49152
s_add_u32 s50, s50, s84
Mv 100,4,40
buffer_load_dwordx4 v13, s[8:11], s39 offen offset:1024 lds
Mv 68,4,36
Rt 142,21,51200
s_add_u32 s60, s50, 7168
Mv 36,4,32
s_add_u32 s52, s52, s84
s_add_u32 s65, s65, s84
Mv 40,8,32
Rt 144,22,49152
s_lshr_b32 s28, s2, 6
Mv 72,8,36
s_lshl_b32 s28, s28, 14
s_add_u32 s28, s28, s50
Mv 104,8,40
Rt 146,22,51200
)" R"(
s_bfe_u32 s29, s2, 0x20004
Mv 136,8,44
s_lshl2_add_u32 s29, s29, s33
s_lshl_b32 s29, s29, 9
Mv 168,8,48
Rt 148,23,49152
s_add_u32 s28, s28, s29
Mv 200,8,52
buffer_load_dwordx4 v14, s[8:11], s39 offen offset:2048 lds
Mv 232,8,56
Rt 150,23,51200
v_min_u32 v4, 3, v0
Ma 200,8,60
v_lshlrev_b32 v4, 7, v4
Ma 204,12,60
Rt 152,24,49152
Mv 236,12,56
buffer_load_dword v4, v4, s[20:23], s28 offen sc0 sc1
Mv 204,12,52
Rt 154,24,51200
s_lshl1_add_u32 s28, s36, s84
Mv 172,12,48
v_add_u32 v2, s28, v1
v_add_u32 v3, 0x1000, v2
Mv 140,12,44
Rt 156,25,49152
s_lshl_b32 s28, s34, 13
Mv 108,12,40
buffer_load_dwordx4 v15, s[8:11], s39 offen offset:3072 lds
Mv 76,12,36
Rt 158,25,51200
s_xor_b32 s28, s28, s84
Mv 44,12,32
s_and_b32 s28, s28, 0x2000
s_xor_b32 s73, s73, s28
Mv 48,16,32
Rb 160,17,32768
s_xor_b32 s72, s72, s28
Mv 80,16,36
Mv 112,16,40
Rb 164,17,34816
s_mov_b64 s[16:17], s[70:71]
Mv 144,16,44
s_mov_b32 s18, 0x188000
Mv 176,16,48
Rb 168,17,40960
Mv 208,16,52
s_cmp_eq_u32 s33, 0
s_cmov_b32 s18, 0x380000
Mv 240,16,56
Rb 172,17,43008
s_mul_i32 s43, s2, 3072
Ma 208,16,60
s_cmp_eq_u32 s33, 0
s_cselect_b32 s44, 3, 6
Ma 212,20,60
Rb 176,17,49152
s_mul_i32 s44, s44, 0x60000
Mv 244,20,56
buffer_store_dwordx4 a[64:67], v2, s[20:23], s51 offen offset:0 sc0 sc1
Mv 212,20,52
Rb 180,17,51200
s_sub_u32 s43, s43, s44
Mv 180,20,48
)" R"(
v_min_u32 v26, 23, v0
v_lshlrev_b32 v26, 7, v26
Mv 148,20,44
Rb 184,17,57344
v_and_b32 v5, 7, v0
Mv 116,20,40
v_lshlrev_b32 v5, 7, v5
Mv 84,20,36
Rb 188,17,59392
Mv 52,20,32
Mv 56,24,32
Mv 88,24,36
buffer_store_dwordx4 a[68:71], v2, s[20:23], s51 offen offset:1024 sc0 sc1
Mv 120,24,40
Mv 152,24,44
Mv 184,24,48
Mv 216,24,52
Mv 248,24,56
Ma 216,24,60
Ma 220,28,60
Mv 252,28,56
buffer_store_dwordx4 a[72:75], v2, s[20:23], s51 offen offset:2048 sc0 sc1
Mv 220,28,52
Mv 188,28,48
Mv 156,28,44
Mv 124,28,40
Mv 92,28,36
Mv 60,28,32
s_waitcnt vmcnt(12) lgkmcnt(0)
s_barrier
Mv 32,128,160
Rt 0,18,0
Mv 64,128,164
s_add_u32 s38, s38, 0xffff0080
Mv 96,128,168
Rt 2,18,2048
s_add_u32 m0, s36, 32768
Mv 128,128,172
buffer_load_dwordx4 v8, s[4:7], s38 offen offset:0 lds
Mv 160,128,176
Rt 4,19,0
Mv 192,128,180
Mv 224,128,184
Rt 6,19,2048
Ma 192,128,188
buffer_load_dwordx4 v9, s[4:7], s38 offen offset:1024 lds
Ma 196,132,188
Rt 8,20,0
Mv 228,132,184
Mv 196,132,180
Rt 10,20,2048
Mv 164,132,176
buffer_load_dwordx4 v10, s[4:7], s38 offen offset:2048 lds
Mv 132,132,172
Rt 12,21,0
Mv 100,132,168
Mv 68,132,164
Rt 14,21,2048
Mv 36,132,160
buffer_load_dwordx4 v11, s[4:7], s38 offen offset:3072 lds
Mv 40,136,160
Rt 16,22,0
Mv 72,136,164
s_add_u32 s38, s38, 0x10000
Mv 104,136,168
)" R"(
Rt 18,22,2048
s_add_u32 m0, s36, 36864
Mv 136,136,172
buffer_load_dwordx4 v8, s[4:7], s38 offen offset:0 lds
Mv 168,136,176
Rt 20,23,0
Mv 200,136,180
Mv 232,136,184
Rt 22,23,2048
Ma 200,136,188
buffer_load_dwordx4 v9, s[4:7], s38 offen offset:1024 lds
Ma 204,140,188
Rt 24,24,0
Mv 236,140,184
Mv 204,140,180
Rt 26,24,2048
Mv 172,140,176
buffer_load_dwordx4 v10, s[4:7], s38 offen offset:2048 lds
Mv 140,140,172
Rt 28,25,0
Mv 108,140,168
Mv 76,140,164
Rt 30,25,2048
v_accvgpr_write_b32 a224, -1
Mv 44,140,160
buffer_load_dwordx4 v11, s[4:7], s38 offen offset:3072 lds
Mv 48,144,160
Rb 32,16,0
v_accvgpr_write_b32 a225, -1
Mv 80,144,164
v_accvgpr_write_b32 a226, -1
v_accvgpr_write_b32 a227, -1
Mv 112,144,168
Rb 36,16,2048
s_add_u32 s88, s73, 0x4000
Mv 144,144,172
s_add_u32 s39, s39, 0x40000
s_mov_b32 s40, 5
Mv 176,144,176
Rb 40,16,8192
s_add_u32 m0, s37, 49152
Mv 208,144,180
buffer_load_dwordx4 v12, s[8:11], s39 offen offset:0 lds
Mv 240,144,184
Rb 44,16,10240
s_mov_b32 s94, 0
Ma 208,144,188
Ma 212,148,188
Rb 48,16,16384
Mv 244,148,184
Mv 212,148,180
Rb 52,16,18432
Mv 180,148,176
buffer_load_dwordx4 v13, s[8:11], s39 offen offset:1024 lds
Mv 148,148,172
Rb 56,16,24576
Mv 116,148,168
Mv 84,148,164
Rb 60,16,26624
Mv 52,148,160
Mv 56,152,160
Mv 88,152,164
buffer_load_dwordx4 v14, s[8:11], s39 offen offset:2048 lds
Mv 120,152,168
)" R"(
Mv 152,152,172
Mv 184,152,176
Mv 216,152,180
Mv 248,152,184
Ma 216,152,188
buffer_load_dwordx4 v15, s[8:11], s39 offen offset:3072 lds
Ma 220,156,188
Mv 252,156,184
Mv 220,156,180
Mv 188,156,176
Mv 156,156,172
Mv 124,156,168
Mv 92,156,164
Mv 60,156,160
s_waitcnt vmcnt(20) lgkmcnt(0)
s_barrier
Mv 32,0,32
Rt 128,18,16384
Mv 64,0,36
Mv 96,0,40
Rt 130,18,18432
Mv 128,0,44
s_add_u32 s39, s39, 0x40000
Mv 160,0,48
Rt 132,19,16384
s_add_u32 m0, s37, 0
Mv 192,0,52
buffer_load_dwordx4 v12, s[8:11], s39 offen offset:0 lds
Mv 224,0,56
Rt 134,19,18432
Ma 192,0,60
Ma 196,4,60
Rt 136,20,16384
Mv 228,4,56
buffer_store_dwordx4 a[76:79], v2, s[20:23], s51 offen offset:3072 sc0 sc1
Mv 196,4,52
Rt 138,20,18432
Mv 164,4,48
Mv 132,4,44
Rt 140,21,16384
Mv 100,4,40
buffer_load_dwordx4 v13, s[8:11], s39 offen offset:1024 lds
Mv 68,4,36
Rt 142,21,18432
Mv 36,4,32
Mv 40,8,32
Rt 144,22,16384
Mv 72,8,36
buffer_store_dwordx4 a[80:83], v3, s[20:23], s51 offen offset:0 sc0 sc1
Mv 104,8,40
Rt 146,22,18432
Mv 136,8,44
Mv 168,8,48
Rt 148,23,16384
Mv 200,8,52
buffer_load_dwordx4 v14, s[8:11], s39 offen offset:2048 lds
Mv 232,8,56
Rt 150,23,18432
Ma 200,8,60
Ma 204,12,60
Rt 152,24,16384
Mv 236,12,56
buffer_store_dwordx4 a[84:87], v3, s[20:23], s51 offen offset:1024 sc0 sc1
Mv 204,12,52
Rt 154,24,18432
)" R"(
Mv 172,12,48
Mv 140,12,44
Rt 156,25,16384
Mv 108,12,40
buffer_load_dwordx4 v15, s[8:11], s39 offen offset:3072 lds
Mv 76,12,36
Rt 158,25,18432
Mv 44,12,32
Mv 48,16,32
Rb 160,17,0
.LzH3:
s_cbranch_execz .LzH13
Mv 80,16,36
buffer_store_dwordx4 a[88:91], v3, s[20:23], s51 offen offset:2048 sc0 sc1
Mv 112,16,40
Rb 164,17,2048
Mv 144,16,44
Mv 176,16,48
Rb 168,17,8192
Mv 208,16,52
Mv 240,16,56
Rb 172,17,10240
Ma 208,16,60
Ma 212,20,60
Rb 176,17,16384
Mv 244,20,56
buffer_store_dwordx4 a[92:95], v3, s[20:23], s51 offen offset:3072 sc0 sc1
Mv 212,20,52
Rb 180,17,18432
Mv 180,20,48
Mv 148,20,44
Rb 184,17,24576
Mv 116,20,40
Mv 84,20,36
Rb 188,17,26624
Mv 52,20,32
Mv 56,24,32
Mv 88,24,36
s_add_u32 s51, s51, 0x10000
buffer_store_dwordx4 a[96:99], v2, s[20:23], s51 offen offset:0 sc0 sc1
Mv 120,24,40
Mv 152,24,44
Mv 184,24,48
Mv 216,24,52
Mv 248,24,56
Ma 216,24,60
Ma 220,28,60
Mv 252,28,56
buffer_store_dwordx4 a[100:103], v2, s[20:23], s51 offen offset:1024 sc0 sc1
Mv 220,28,52
Mv 188,28,48
Mv 156,28,44
Mv 124,28,40
Mv 92,28,36
Mv 60,28,32
s_waitcnt vmcnt(15) lgkmcnt(0)
s_barrier
Mv 32,128,160
Rt 0,18,32768
Mv 64,128,164
s_add_u32 s38, s38, 0xffff0080
Mv 96,128,168
Rt 2,18,34816
s_add_u32 m0, s36, 0
Mv 128,128,172
)" R"(
buffer_load_dwordx4 v8, s[4:7], s38 offen offset:0 lds
Mv 160,128,176
Rt 4,19,32768
Mv 192,128,180
Mv 224,128,184
Rt 6,19,34816
Ma 192,128,188
buffer_load_dwordx4 v9, s[4:7], s38 offen offset:1024 lds
Ma 196,132,188
Rt 8,20,32768
Mv 228,132,184
Mv 196,132,180
Rt 10,20,34816
Mv 164,132,176
buffer_load_dwordx4 v10, s[4:7], s38 offen offset:2048 lds
Mv 132,132,172
Rt 12,21,32768
Mv 100,132,168
Mv 68,132,164
Rt 14,21,34816
Mv 36,132,160
buffer_load_dwordx4 v11, s[4:7], s38 offen offset:3072 lds
Mv 40,136,160
Rt 16,22,32768
Mv 72,136,164
s_add_u32 s38, s38, 0x10000
Mv 104,136,168
Rt 18,22,34816
s_add_u32 m0, s36, 4096
Mv 136,136,172
buffer_load_dwordx4 v8, s[4:7], s38 offen offset:0 lds
Mv 168,136,176
Rt 20,23,32768
Mv 200,136,180
Mv 232,136,184
Rt 22,23,34816
Ma 200,136,188
buffer_load_dwordx4 v9, s[4:7], s38 offen offset:1024 lds
Ma 204,140,188
Rt 24,24,32768
Mv 236,140,184
Mv 204,140,180
Rt 26,24,34816
Mv 172,140,176
buffer_load_dwordx4 v10, s[4:7], s38 offen offset:2048 lds
Mv 140,140,172
Rt 28,25,32768
Mv 108,140,168
Mv 76,140,164
Rt 30,25,34816
Mv 44,140,160
buffer_load_dwordx4 v11, s[4:7], s38 offen offset:3072 lds
Mv 48,144,160
Rb 32,16,32768
Mv 80,144,164
Mv 112,144,168
Rb 36,16,34816
Mv 144,144,172
s_add_u32 s39, s39, 0x40000
Mv 176,144,176
Rb 40,16,40960
s_add_u32 m0, s37, 16384
Mv 208,144,180
buffer_load_dwordx4 v12, s[8:11], s39 offen offset:0 lds
)" R"(
Mv 240,144,184
Rb 44,16,43008
Ma 208,144,188
Ma 212,148,188
Rb 48,16,49152
Mv 244,148,184
Mv 212,148,180
Rb 52,16,51200
Mv 180,148,176
buffer_load_dwordx4 v13, s[8:11], s39 offen offset:1024 lds
Mv 148,148,172
Rb 56,16,57344
Mv 116,148,168
Mv 84,148,164
Rb 60,16,59392
Mv 52,148,160
Mv 56,152,160
Mv 88,152,164
buffer_load_dwordx4 v14, s[8:11], s39 offen offset:2048 lds
Mv 120,152,168
Mv 152,152,172
Mv 184,152,176
Mv 216,152,180
Mv 248,152,184
Ma 216,152,188
buffer_load_dwordx4 v15, s[8:11], s39 offen offset:3072 lds
Ma 220,156,188
Mv 252,156,184
s_add_u32 s43, s43, 0x60000
Mv 220,156,180
Mv 188,156,176
buffer_load_dword v27, v26, s[16:19], s43 offen
Mv 156,156,172
Mv 124,156,168
Mv 92,156,164
Mv 60,156,160
s_waitcnt vmcnt(24) lgkmcnt(0)
s_barrier
Mv 32,0,32
Rt 128,18,49152
Mv 64,0,36
Mv 96,0,40
Rt 130,18,51200
Mv 128,0,44
s_add_u32 s39, s39, 0x40000
Mv 160,0,48
Rt 132,19,49152
s_add_u32 m0, s37, 32768
Mv 192,0,52
buffer_load_dwordx4 v12, s[8:11], s39 offen offset:0 lds
Mv 224,0,56
Rt 134,19,51200
Ma 192,0,60
Ma 196,4,60
Rt 136,20,49152
Mv 228,4,56
buffer_store_dwordx4 a[104:107], v2, s[20:23], s51 offen offset:2048 sc0 sc1
Mv 196,4,52
Rt 138,20,51200
Mv 164,4,48
Mv 132,4,44
Rt 140,21,49152
Mv 100,4,40
buffer_load_dwordx4 v13, s[8:11], s39 offen offset:1024 lds
Mv 68,4,36
)" R"(
Rt 142,21,51200
Mv 36,4,32
Mv 40,8,32
Rt 144,22,49152
Mv 72,8,36
buffer_store_dwordx4 a[108:111], v2, s[20:23], s51 offen offset:3072 sc0 sc1
Mv 104,8,40
Rt 146,22,51200
Mv 136,8,44
Mv 168,8,48
Rt 148,23,49152
Mv 200,8,52
buffer_load_dwordx4 v14, s[8:11], s39 offen offset:2048 lds
Mv 232,8,56
Rt 150,23,51200
Ma 200,8,60
Ma 204,12,60
Rt 152,24,49152
Mv 236,12,56
buffer_store_dwordx4 a[112:115], v3, s[20:23], s51 offen offset:0 sc0 sc1
Mv 204,12,52
Rt 154,24,51200
Mv 172,12,48
Mv 140,12,44
Rt 156,25,49152
Mv 108,12,40
buffer_load_dwordx4 v15, s[8:11], s39 offen offset:3072 lds
Mv 76,12,36
Rt 158,25,51200
Mv 44,12,32
Mv 48,16,32
Rb 160,17,32768
Mv 80,16,36
buffer_store_dwordx4 a[116:119], v3, s[20:23], s51 offen offset:1024 sc0 sc1
Mv 112,16,40
Rb 164,17,34816
Mv 144,16,44
Mv 176,16,48
Rb 168,17,40960
Mv 208,16,52
Mv 240,16,56
Rb 172,17,43008
Ma 208,16,60
Ma 212,20,60
Rb 176,17,49152
Mv 244,20,56
buffer_store_dwordx4 a[120:123], v3, s[20:23], s51 offen offset:2048 sc0 sc1
Mv 212,20,52
Rb 180,17,51200
Mv 180,20,48
Mv 148,20,44
Rb 184,17,57344
Mv 116,20,40
Mv 84,20,36
Rb 188,17,59392
Mv 52,20,32
Mv 56,24,32
Mv 88,24,36
buffer_store_dwordx4 a[124:127], v3, s[20:23], s51 offen offset:3072 sc0 sc1
Mv 120,24,40
Mv 152,24,44
Mv 184,24,48
Mv 216,24,52
Mv 248,24,56
)" R"(
Ma 216,24,60
Ma 220,28,60
Mv 252,28,56
Mv 220,28,52
Mv 188,28,48
Mv 156,28,44
Mv 124,28,40
Mv 92,28,36
Mv 60,28,32
s_waitcnt vmcnt(15) lgkmcnt(0)
s_barrier
Mv 32,128,160
Rt 0,18,0
Mv 64,128,164
s_add_u32 s38, s38, 0xffff0080
Mv 96,128,168
Rt 2,18,2048
s_add_u32 m0, s36, 32768
Mv 128,128,172
buffer_load_dwordx4 v8, s[4:7], s38 offen offset:0 lds
Mv 160,128,176
Rt 4,19,0
Mv 192,128,180
Mv 224,128,184
Rt 6,19,2048
Ma 192,128,188
buffer_load_dwordx4 v9, s[4:7], s38 offen offset:1024 lds
Ma 196,132,188
Rt 8,20,0
Mv 228,132,184
Mv 196,132,180
Rt 10,20,2048
Mv 164,132,176
buffer_load_dwordx4 v10, s[4:7], s38 offen offset:2048 lds
Mv 132,132,172
Rt 12,21,0
Mv 100,132,168
Mv 68,132,164
Rt 14,21,2048
Mv 36,132,160
buffer_load_dwordx4 v11, s[4:7], s38 offen offset:3072 lds
Mv 40,136,160
Rt 16,22,0
Mv 72,136,164
s_add_u32 s38, s38, 0x10000
Mv 104,136,168
Rt 18,22,2048
s_add_u32 m0, s36, 36864
Mv 136,136,172
buffer_load_dwordx4 v8, s[4:7], s38 offen offset:0 lds
Mv 168,136,176
Rt 20,23,0
Mv 200,136,180
Mv 232,136,184
Rt 22,23,2048
Ma 200,136,188
buffer_load_dwordx4 v9, s[4:7], s38 offen offset:1024 lds
Ma 204,140,188
Rt 24,24,0
Mv 236,140,184
Mv 204,140,180
Rt 26,24,2048
Mv 172,140,176
buffer_load_dwordx4 v10, s[4:7], s38 offen offset:2048 lds
Mv 140,140,172
)" R"(
Rt 28,25,0
Mv 108,140,168
Mv 76,140,164
Rt 30,25,2048
Mv 44,140,160
buffer_load_dwordx4 v11, s[4:7], s38 offen offset:3072 lds
Mv 48,144,160
Rb 32,16,0
Mv 80,144,164
Mv 112,144,168
Rb 36,16,2048
Mv 144,144,172
s_add_u32 s39, s39, 0x40000
Mv 176,144,176
Rb 40,16,8192
s_add_u32 m0, s37, 49152
Mv 208,144,180
buffer_load_dwordx4 v12, s[8:11], s39 offen offset:0 lds
Mv 240,144,184
Rb 44,16,10240
Ma 208,144,188
Ma 212,148,188
Rb 48,16,16384
Mv 244,148,184
Mv 212,148,180
Rb 52,16,18432
Mv 180,148,176
buffer_load_dwordx4 v13, s[8:11], s39 offen offset:1024 lds
Mv 148,148,172
Rb 56,16,24576
Mv 116,148,168
Mv 84,148,164
Rb 60,16,26624
Mv 52,148,160
Mv 56,152,160
Mv 88,152,164
buffer_load_dwordx4 v14, s[8:11], s39 offen offset:2048 lds
Mv 120,152,168
Mv 152,152,172
Mv 184,152,176
Mv 216,152,180
Mv 248,152,184
Ma 216,152,188
buffer_load_dwordx4 v15, s[8:11], s39 offen offset:3072 lds
Ma 220,156,188
Mv 252,156,184
s_add_u32 s43, s43, 0x60000
Mv 220,156,180
Mv 188,156,176
buffer_load_dword v27, v26, s[16:19], s43 offen
Mv 156,156,172
Mv 124,156,168
Mv 92,156,164
Mv 60,156,160
s_waitcnt vmcnt(24) lgkmcnt(0)
s_barrier
.LzPBK:
Mv 32,0,32
Rt 128,18,16384
Mv 64,0,36
Mv 96,0,40
Rt 130,18,18432
Mv 128,0,44
s_add_u32 s39, s39, 0x40000
Mv 160,0,48
)" R"(
Rt 132,19,16384
s_add_u32 m0, s37, 0
Mv 192,0,52
buffer_load_dwordx4 v12, s[8:11], s39 offen offset:0 lds
Mv 224,0,56
Rt 134,19,18432
Ma 192,0,60
Ma 196,4,60
Rt 136,20,16384
Mv 228,4,56
Mv 196,4,52
Rt 138,20,18432
Mv 164,4,48
Mv 132,4,44
Rt 140,21,16384
Mv 100,4,40
buffer_load_dwordx4 v13, s[8:11], s39 offen offset:1024 lds
Mv 68,4,36
Rt 142,21,18432
Mv 36,4,32
Mv 40,8,32
Rt 144,22,16384
Mv 72,8,36
Mv 104,8,40
Rt 146,22,18432
Mv 136,8,44
Mv 168,8,48
Rt 148,23,16384
Mv 200,8,52
buffer_load_dwordx4 v14, s[8:11], s39 offen offset:2048 lds
Mv 232,8,56
Rt 150,23,18432
Ma 200,8,60
Ma 204,12,60
Rt 152,24,16384
Mv 236,12,56
Mv 204,12,52
Rt 154,24,18432
Mv 172,12,48
Mv 140,12,44
Rt 156,25,16384
Mv 108,12,40
buffer_load_dwordx4 v15, s[8:11], s39 offen offset:3072 lds
Mv 76,12,36
Rt 158,25,18432
Mv 44,12,32
Mv 48,16,32
Rb 160,17,0
Mv 80,16,36
Mv 112,16,40
Rb 164,17,2048
Mv 144,16,44
Mv 176,16,48
Rb 168,17,8192
Mv 208,16,52
buffer_store_dwordx4 a[224:227], v1, s[20:23], s72 offen sc0 sc1
Mv 240,16,56
Rb 172,17,10240
s_add_u32 s72, s72, 1024
Ma 208,16,60
Ma 212,20,60
Rb 176,17,16384
Mv 244,20,56
Mv 212,20,52
)" R"(
Rb 180,17,18432
Mv 180,20,48
Mv 148,20,44
Rb 184,17,24576
Mv 116,20,40
Mv 84,20,36
Rb 188,17,26624
Mv 52,20,32
Mv 56,24,32
Mv 88,24,36
buffer_store_dwordx4 a[224:227], v1, s[20:23], s88 offen sc0 sc1
Mv 120,24,40
s_add_u32 s88, s88, 1024
Mv 152,24,44
Mv 184,24,48
Mv 216,24,52
Mv 248,24,56
Ma 216,24,60
Ma 220,28,60
Mv 252,28,56
Mv 220,28,52
Mv 188,28,48
Mv 156,28,44
Mv 124,28,40
Mv 92,28,36
Mv 60,28,32
s_waitcnt vmcnt(11) lgkmcnt(0)
s_barrier
Mv 32,128,160
Rt 0,18,32768
Mv 64,128,164
s_add_u32 s38, s38, 0xffff0080
Mv 96,128,168
Rt 2,18,34816
s_add_u32 m0, s36, 0
Mv 128,128,172
buffer_load_dwordx4 v8, s[4:7], s38 offen offset:0 lds
Mv 160,128,176
Rt 4,19,32768
Mv 192,128,180
Mv 224,128,184
Rt 6,19,34816
Ma 192,128,188
buffer_load_dwordx4 v9, s[4:7], s38 offen offset:1024 lds
Ma 196,132,188
Rt 8,20,32768
Mv 228,132,184
Mv 196,132,180
Rt 10,20,34816
Mv 164,132,176
buffer_load_dwordx4 v10, s[4:7], s38 offen offset:2048 lds
Mv 132,132,172
Rt 12,21,32768
Mv 100,132,168
Mv 68,132,164
Rt 14,21,34816
Mv 36,132,160
buffer_load_dwordx4 v11, s[4:7], s38 offen offset:3072 lds
Mv 40,136,160
Rt 16,22,32768
Mv 72,136,164
s_add_u32 s38, s38, 0x10000
Mv 104,136,168
Rt 18,22,34816
s_add_u32 m0, s36, 4096
)" R"(
Mv 136,136,172
buffer_load_dwordx4 v8, s[4:7], s38 offen offset:0 lds
Mv 168,136,176
Rt 20,23,32768
Mv 200,136,180
Mv 232,136,184
Rt 22,23,34816
Ma 200,136,188
buffer_load_dwordx4 v9, s[4:7], s38 offen offset:1024 lds
Ma 204,140,188
Rt 24,24,32768
Mv 236,140,184
Mv 204,140,180
Rt 26,24,34816
.LzH4:
s_cbranch_execz .LzH14
Mv 172,140,176
buffer_load_dwordx4 v10, s[4:7], s38 offen offset:2048 lds
Mv 140,140,172
Rt 28,25,32768
Mv 108,140,168
Mv 76,140,164
Rt 30,25,34816
Mv 44,140,160
buffer_load_dwordx4 v11, s[4:7], s38 offen offset:3072 lds
Mv 48,144,160
Rb 32,16,32768
Mv 80,144,164
Mv 112,144,168
Rb 36,16,34816
Mv 144,144,172
s_add_u32 s39, s39, 0x40000
Mv 176,144,176
Rb 40,16,40960
s_add_u32 m0, s37, 16384
Mv 208,144,180
buffer_load_dwordx4 v12, s[8:11], s39 offen offset:0 lds
Mv 240,144,184
Rb 44,16,43008
Ma 208,144,188
Ma 212,148,188
Rb 48,16,49152
Mv 244,148,184
Mv 212,148,180
Rb 52,16,51200
Mv 180,148,176
buffer_load_dwordx4 v13, s[8:11], s39 offen offset:1024 lds
Mv 148,148,172
Rb 56,16,57344
Mv 116,148,168
Mv 84,148,164
Rb 60,16,59392
Mv 52,148,160
Mv 56,152,160
Mv 88,152,164
buffer_load_dwordx4 v14, s[8:11], s39 offen offset:2048 lds
Mv 120,152,168
Mv 152,152,172
Mv 184,152,176
Mv 216,152,180
Mv 248,152,184
Ma 216,152,188
buffer_load_dwordx4 v15, s[8:11], s39 offen offset:3072 lds
Ma 220,156,188
)" R"(
Mv 252,156,184
s_add_u32 s43, s43, 0x60000
Mv 220,156,180
Mv 188,156,176
buffer_load_dword v27, v26, s[16:19], s43 offen
Mv 156,156,172
Mv 124,156,168
Mv 92,156,164
Mv 60,156,160
s_waitcnt vmcnt(20) lgkmcnt(0)
s_barrier
buffer_store_dword v6, v7, s[20:23], s52 offen offset:0 sc0 sc1
s_mov_b32 s52, 0x40000000
Mv 32,0,32
Rt 128,18,49152
Mv 64,0,36
Mv 96,0,40
Rt 130,18,51200
Mv 128,0,44
s_add_u32 s39, s39, 0x40000
Mv 160,0,48
Rt 132,19,49152
s_add_u32 m0, s37, 32768
Mv 192,0,52
buffer_load_dwordx4 v12, s[8:11], s39 offen offset:0 lds
Mv 224,0,56
Rt 134,19,51200
Ma 192,0,60
Ma 196,4,60
Rt 136,20,49152
Mv 228,4,56
Mv 196,4,52
Rt 138,20,51200
Mv 164,4,48
Mv 132,4,44
Rt 140,21,49152
Mv 100,4,40
buffer_load_dwordx4 v13, s[8:11], s39 offen offset:1024 lds
Mv 68,4,36
Rt 142,21,51200
Mv 36,4,32
Mv 40,8,32
Rt 144,22,49152
Mv 72,8,36
Mv 104,8,40
Rt 146,22,51200
Mv 136,8,44
Mv 168,8,48
Rt 148,23,49152
Mv 200,8,52
buffer_load_dwordx4 v14, s[8:11], s39 offen offset:2048 lds
Mv 232,8,56
Rt 150,23,51200
Ma 200,8,60
Ma 204,12,60
Rt 152,24,49152
Mv 236,12,56
Mv 204,12,52
Rt 154,24,51200
Mv 172,12,48
Mv 140,12,44
Rt 156,25,49152
Mv 108,12,40
buffer_load_dwordx4 v15, s[8:11], s39 offen offset:3072 lds
Mv 76,12,36
)" R"(
Rt 158,25,51200
Mv 44,12,32
Mv 48,16,32
Rb 160,17,32768
Mv 80,16,36
Mv 112,16,40
Rb 164,17,34816
Mv 144,16,44
Mv 176,16,48
Rb 168,17,40960
Mv 208,16,52
buffer_store_dwordx4 a[224:227], v1, s[20:23], s72 offen sc0 sc1
Mv 240,16,56
Rb 172,17,43008
s_add_u32 s72, s72, 1024
Ma 208,16,60
Ma 212,20,60
Rb 176,17,49152
Mv 244,20,56
Mv 212,20,52
Rb 180,17,51200
Mv 180,20,48
Mv 148,20,44
Rb 184,17,57344
Mv 116,20,40
Mv 84,20,36
Rb 188,17,59392
Mv 52,20,32
Mv 56,24,32
Mv 88,24,36
buffer_store_dwordx4 a[224:227], v1, s[20:23], s88 offen sc0 sc1
Mv 120,24,40
s_add_u32 s88, s88, 1024
Mv 152,24,44
Mv 184,24,48
Mv 216,24,52
Mv 248,24,56
Ma 216,24,60
Ma 220,28,60
Mv 252,28,56
Mv 220,28,52
Mv 188,28,48
Mv 156,28,44
Mv 124,28,40
Mv 92,28,36
s_cmp_eq_u32 s40, 2
s_cmov_b32 s72, 0x40000000
s_cmov_b32 s88, 0x40000000
Mv 60,28,32
s_waitcnt vmcnt(12) lgkmcnt(0)
s_barrier
Mv 32,128,160
Rt 0,18,0
Mv 64,128,164
s_add_u32 s38, s38, 0xffff0080
Mv 96,128,168
Rt 2,18,2048
s_add_u32 m0, s36, 32768
Mv 128,128,172
buffer_load_dwordx4 v8, s[4:7], s38 offen offset:0 lds
Mv 160,128,176
Rt 4,19,0
Mv 192,128,180
Mv 224,128,184
Rt 6,19,2048
)" R"(
Ma 192,128,188
buffer_load_dwordx4 v9, s[4:7], s38 offen offset:1024 lds
Ma 196,132,188
Rt 8,20,0
Mv 228,132,184
Mv 196,132,180
Rt 10,20,2048
Mv 164,132,176
buffer_load_dwordx4 v10, s[4:7], s38 offen offset:2048 lds
Mv 132,132,172
Rt 12,21,0
Mv 100,132,168
Mv 68,132,164
Rt 14,21,2048
Mv 36,132,160
buffer_load_dwordx4 v11, s[4:7], s38 offen offset:3072 lds
Mv 40,136,160
Rt 16,22,0
Mv 72,136,164
s_add_u32 s38, s38, 0x10000
Mv 104,136,168
Rt 18,22,2048
s_add_u32 m0, s36, 36864
Mv 136,136,172
buffer_load_dwordx4 v8, s[4:7], s38 offen offset:0 lds
Mv 168,136,176
Rt 20,23,0
Mv 200,136,180
Mv 232,136,184
Rt 22,23,2048
Ma 200,136,188
buffer_load_dwordx4 v9, s[4:7], s38 offen offset:1024 lds
Ma 204,140,188
Rt 24,24,0
Mv 236,140,184
Mv 204,140,180
Rt 26,24,2048
Mv 172,140,176
buffer_load_dwordx4 v10, s[4:7], s38 offen offset:2048 lds
Mv 140,140,172
Rt 28,25,0
Mv 108,140,168
Mv 76,140,164
Rt 30,25,2048
Mv 44,140,160
buffer_load_dwordx4 v11, s[4:7], s38 offen offset:3072 lds
Mv 48,144,160
Rb 32,16,0
Mv 80,144,164
Mv 112,144,168
Rb 36,16,2048
Mv 144,144,172
s_add_u32 s39, s39, 0x40000
Mv 176,144,176
Rb 40,16,8192
s_add_u32 m0, s37, 49152
Mv 208,144,180
buffer_load_dwordx4 v12, s[8:11], s39 offen offset:0 lds
Mv 240,144,184
Rb 44,16,10240
Ma 208,144,188
Ma 212,148,188
Rb 48,16,16384
Mv 244,148,184
)" R"(
Mv 212,148,180
Rb 52,16,18432
Mv 180,148,176
buffer_load_dwordx4 v13, s[8:11], s39 offen offset:1024 lds
Mv 148,148,172
Rb 56,16,24576
Mv 116,148,168
Mv 84,148,164
Rb 60,16,26624
Mv 52,148,160
Mv 56,152,160
Mv 88,152,164
buffer_load_dwordx4 v14, s[8:11], s39 offen offset:2048 lds
Mv 120,152,168
Mv 152,152,172
Mv 184,152,176
Mv 216,152,180
Mv 248,152,184
Ma 216,152,188
buffer_load_dwordx4 v15, s[8:11], s39 offen offset:3072 lds
Ma 220,156,188
Mv 252,156,184
s_add_u32 s43, s43, 0x60000
Mv 220,156,180
s_sub_u32 s40, s40, 1
Mv 188,156,176
buffer_load_dword v27, v26, s[16:19], s43 offen
Mv 156,156,172
Mv 124,156,168
Mv 92,156,164
Mv 60,156,160
s_cmp_lg_u32 s40, 0
s_waitcnt vmcnt(21) lgkmcnt(0)
s_barrier
s_cbranch_scc1 .LzPBK
.LzPBKX:
Mv 32,0,32
Rt 128,18,16384
Mv 64,0,36
buffer_load_dword v4, v5, s[20:23], s50 offen offset:0 sc0 sc1
Mv 96,0,40
Rt 130,18,18432
Mv 128,0,44
Mv 160,0,48
Rt 132,19,16384
Mv 192,0,52
Mv 224,0,56
Rt 134,19,18432
Ma 192,0,60
Ma 196,4,60
Rt 136,20,16384
Mv 228,4,56
Mv 196,4,52
Rt 138,20,18432
Mv 164,4,48
Mv 132,4,44
Rt 140,21,16384
Mv 100,4,40
Mv 68,4,36
Rt 142,21,18432
Mv 36,4,32
Mv 40,8,32
Rt 144,22,16384
Mv 72,8,36
Mv 104,8,40
)" R"(
Rt 146,22,18432
Mv 136,8,44
Mv 168,8,48
Rt 148,23,16384
Mv 200,8,52
Mv 232,8,56
Rt 150,23,18432
Ma 200,8,60
Ma 204,12,60
Rt 152,24,16384
Mv 236,12,56
Mv 204,12,52
Rt 154,24,18432
Mv 172,12,48
Mv 140,12,44
Rt 156,25,16384
Mv 108,12,40
Mv 76,12,36
Rt 158,25,18432
Mv 44,12,32
Mv 48,16,32
Rb 160,17,0
Mv 80,16,36
Mv 112,16,40
Rb 164,17,2048
Mv 144,16,44
Mv 176,16,48
Rb 168,17,8192
Mv 208,16,52
Mv 240,16,56
Rb 172,17,10240
Ma 208,16,60
s_waitcnt vmcnt(0)
v_cmp_eq_u32_e32 vcc, s3, v4
s_andn2_b64 s[28:29], -1, vcc
s_cbranch_scc1 .Lzsp14
.Lzok14:
Ma 212,20,60
Rb 176,17,16384
Mv 244,20,56
s_lshl3_add_u32 s29, s34, s35
s_lshl2_add_u32 s29, s29, s33
Mv 212,20,52
Rb 180,17,18432
s_add_u32 m0, s36, 131072
Mv 180,20,48
buffer_load_dwordx4 v2, s[20:23], s56 offen offset:0 sc1 lds
Mv 148,20,44
Rb 184,17,24576
s_mul_i32 s83, s29, 7168
Mv 116,20,40
s_mov_b32 s82, s89
s_add_u32 s82, s82, 0xc00000
Mv 84,20,36
Rb 188,17,26624
s_add_u32 s82, s82, s84
Mv 52,20,32
s_mov_b32 s24, s80
s_mov_b32 s25, s81
Mv 56,24,32
s_mov_b32 s26, 0x380000
s_mov_b32 s27, 0x20000
Mv 88,24,36
buffer_load_dwordx4 v2, s[20:23], s56 offen offset:1024 sc1 lds
)" R"(
Mv 120,24,40
s_add_u32 s28, s33, 0
s_lshl2_add_u32 s28, s35, s28
Mv 152,24,44
s_mul_i32 s29, s28, 293
s_lshr_b32 s29, s29, 11
Mv 184,24,48
s_mul_i32 s29, s29, 9
s_add_u32 s29, s29, s28
Mv 216,24,52
s_lshl_b32 s29, s29, 10
s_add_u32 s4, s29, s82
Mv 248,24,56
s_add_u32 s28, s33, 4
s_lshl3_add_u32 s28, s28, s35
Ma 216,24,60
buffer_load_dwordx4 v2, s[20:23], s56 offen offset:2048 sc1 lds
Ma 220,28,60
s_mul_i32 s29, s28, 293
s_lshr_b32 s29, s29, 11
Mv 252,28,56
s_mul_i32 s29, s29, 9
s_add_u32 s29, s29, s28
Mv 220,28,52
s_lshl_b32 s29, s29, 10
s_add_u32 s5, s29, s82
Mv 188,28,48
s_add_u32 s28, s33, 8
s_lshl3_add_u32 s28, s28, s35
Mv 156,28,44
s_mul_i32 s29, s28, 293
s_lshr_b32 s29, s29, 11
Mv 124,28,40
buffer_load_dwordx4 v2, s[20:23], s56 offen offset:3072 sc1 lds
Mv 92,28,36
s_mul_i32 s29, s29, 9
s_add_u32 s29, s29, s28
Mv 60,28,32
s_waitcnt lgkmcnt(0)
s_barrier
Mv 32,128,160
Rt 0,18,32768
s_lshl_b32 s29, s29, 10
Mv 64,128,164
s_add_u32 s6, s29, s82
s_add_u32 s28, s33, 12
Mv 96,128,168
Rt 2,18,34816
s_add_u32 m0, s36, 0
Mv 128,128,172
buffer_load_dwordx4 v2, s[20:23], s55 offen offset:0 sc1 lds
Mv 160,128,176
Rt 4,19,32768
s_lshl3_add_u32 s28, s28, s35
Mv 192,128,180
s_mul_i32 s29, s28, 293
s_lshr_b32 s29, s29, 11
Mv 224,128,184
Rt 6,19,34816
s_mul_i32 s29, s29, 9
Ma 192,128,188
s_add_u32 s29, s29, s28
s_lshl_b32 s29, s29, 10
Ma 196,132,188
Rt 8,20,32768
)" R"(
buffer_load_dwordx4 v2, s[20:23], s55 offen offset:1024 sc1 lds
Mv 228,132,184
s_add_u32 s7, s29, s82
Mv 196,132,180
Rt 10,20,34816
Mv 164,132,176
s_cmp_gt_u32 s33, 1
s_cmov_b32 s7, 0x40000000
Mv 132,132,172
Rt 12,21,32768
s_add_u32 s28, s33, 14
Mv 100,132,168
buffer_load_dwordx4 v2, s[20:23], s55 offen offset:2048 sc1 lds
Mv 68,132,164
Rt 14,21,34816
s_lshl3_add_u32 s28, s28, s35
Mv 36,132,160
s_mul_i32 s29, s28, 293
s_lshr_b32 s29, s29, 11
Mv 40,136,160
Rt 16,22,32768
s_mul_i32 s29, s29, 9
Mv 72,136,164
s_add_u32 s29, s29, s28
s_lshl_b32 s29, s29, 10
Mv 104,136,168
Rt 18,22,34816
buffer_load_dwordx4 v2, s[20:23], s55 offen offset:3072 sc1 lds
Mv 136,136,172
s_add_u32 s8, s29, s82
s_add_u32 s28, s33, 18
Mv 168,136,176
Rt 20,23,32768
s_lshl3_add_u32 s28, s28, s35
Mv 200,136,180
s_mul_i32 s29, s28, 293
s_lshr_b32 s29, s29, 11
Mv 232,136,184
Rt 22,23,34816
s_add_u32 m0, s36, 4096
Ma 200,136,188
buffer_load_dwordx4 v3, s[20:23], s55 offen offset:0 sc1 lds
Ma 204,140,188
Rt 24,24,32768
s_mul_i32 s29, s29, 9
Mv 236,140,184
s_add_u32 s29, s29, s28
s_lshl_b32 s29, s29, 10
Mv 204,140,180
Rt 26,24,34816
s_add_u32 s9, s29, s82
Mv 172,140,176
s_add_u32 s28, s33, 22
s_lshl3_add_u32 s28, s28, s35
Mv 140,140,172
Rt 28,25,32768
buffer_load_dwordx4 v3, s[20:23], s55 offen offset:1024 sc1 lds
Mv 108,140,168
s_mul_i32 s29, s28, 293
s_lshr_b32 s29, s29, 11
Mv 76,140,164
Rt 30,25,34816
s_mul_i32 s29, s29, 9
Mv 44,140,160
)" R"(
s_add_u32 s29, s29, s28
s_lshl_b32 s29, s29, 10
Mv 48,144,160
Rb 32,16,32768
s_add_u32 s10, s29, s82
Mv 80,144,164
buffer_load_dwordx4 v3, s[20:23], s55 offen offset:2048 sc1 lds
Mv 112,144,168
Rb 36,16,34816
s_add_u32 s28, s33, 26
Mv 144,144,172
s_lshl3_add_u32 s28, s28, s35
s_mul_i32 s29, s28, 293
Mv 176,144,176
Rb 40,16,40960
s_lshr_b32 s29, s29, 11
Mv 208,144,180
s_mul_i32 s29, s29, 9
s_add_u32 s29, s29, s28
Mv 240,144,184
Rb 44,16,43008
buffer_load_dwordx4 v3, s[20:23], s55 offen offset:3072 sc1 lds
Ma 208,144,188
s_lshl_b32 s29, s29, 10
s_add_u32 s11, s29, s82
Ma 212,148,188
Rb 48,16,49152
Mv 244,148,184
s_cmp_gt_u32 s33, 1
s_cmov_b32 s11, 0x40000000
Mv 212,148,180
Rb 52,16,51200
s_add_u32 m0, s36, 135168
Mv 180,148,176
buffer_load_dwordx4 v3, s[20:23], s56 offen offset:0 sc1 lds
Mv 148,148,172
Rb 56,16,57344
s_and_b32 s28, s34, 1
Mv 116,148,168
s_lshl_b32 s28, s28, 14
s_add_u32 s82, s37, s28
Mv 84,148,164
Rb 60,16,59392
s_add_u32 s82, s82, 0x10000
Mv 52,148,160
Mv 56,152,160
buffer_load_dwordx4 v3, s[20:23], s56 offen offset:1024 sc1 lds
Mv 88,152,164
Mv 120,152,168
Mv 152,152,172
Mv 184,152,176
Mv 216,152,180
buffer_load_dwordx4 v3, s[20:23], s56 offen offset:2048 sc1 lds
Mv 248,152,184
Ma 216,152,188
Ma 220,156,188
Mv 252,156,184
Mv 220,156,180
buffer_load_dwordx4 v3, s[20:23], s56 offen offset:3072 sc1 lds
.LzH5:
s_cbranch_execz .LzH19
Mv 188,156,176
Mv 156,156,172
Mv 124,156,168
)" R"(
s_add_u32 s55, s55, 0x10000
s_add_u32 s56, s56, 0x10000
Mv 92,156,164
Mv 60,156,160
s_waitcnt lgkmcnt(4)
s_barrier
Mv 32,0,32
Rt 128,18,49152
Mv 64,0,36
Rt 130,18,51200
Mv 96,0,40
Rt 132,19,49152
s_add_u32 m0, s36, 65536
Mv 128,0,44
Rt 134,19,51200
buffer_load_dwordx4 v2, s[20:23], s56 offen offset:0 sc1 lds
Mv 132,4,44
Rt 136,20,49152
Mv 100,4,40
Rt 138,20,51200
s_add_u32 s50, s50, 1024
Mv 68,4,36
Rt 140,21,49152
s_min_u32 s50, s50, s60
Mv 36,4,32
Rt 142,21,51200
Mv 40,8,32
Rt 144,22,49152
Mv 72,8,36
Rt 146,22,51200
Mv 104,8,40
Rt 148,23,49152
Mv 136,8,44
Rt 150,23,51200
buffer_load_dwordx4 v2, s[20:23], s56 offen offset:1024 sc1 lds
Mv 140,12,44
Rt 152,24,49152
Mv 108,12,40
Rt 154,24,51200
Mv 76,12,36
Rt 156,25,49152
Mv 44,12,32
Rt 158,25,51200
Mv 48,16,32
Rb 160,17,32768
Mv 80,16,36
Rb 164,17,34816
Mv 112,16,40
Rb 168,17,40960
Mv 144,16,44
Rb 172,17,43008
buffer_load_dwordx4 v2, s[20:23], s56 offen offset:2048 sc1 lds
Mv 148,20,44
Rb 176,17,49152
Mv 116,20,40
Rb 180,17,51200
Mv 84,20,36
Rb 184,17,57344
Mv 52,20,32
Rb 188,17,59392
Mv 56,24,32
v_add_u32 v8, 0x10000, v1
v_add_u32 v10, 0x20000, v1
Mv 88,24,36
buffer_load_dword v4, v5, s[20:23], s50 offen offset:0 sc0 sc1
)" R"(
Mv 120,24,40
s_mov_b32 s93, 2
s_mov_b32 s94, 0
Mv 152,24,44
buffer_load_dwordx4 v2, s[20:23], s56 offen offset:3072 sc1 lds
Mv 156,28,44
Mv 124,28,40
Mv 92,28,36
Mv 60,28,32
s_waitcnt lgkmcnt(0)
s_barrier
Mv 32,128,160
Mv 36,132,160
Mv 40,136,160
s_add_u32 m0, s36, 32768
Mv 44,140,160
buffer_load_dwordx4 v2, s[20:23], s55 offen offset:0 sc1 lds
Mv 48,144,160
Mv 52,148,160
s_nop 0
Cv 32,32,33
Cv 33,34,35
Mv 56,152,160
Cv 34,36,37
Cv 35,38,39
Mv 60,156,160
Cv 40,40,41
Cv 41,42,43
Mv 64,128,164
Cv 42,44,45
Cv 43,46,47
Mv 68,132,164
Cv 48,48,49
Cv 49,50,51
Mv 72,136,164
Cv 50,52,53
Cv 51,54,55
Mv 76,140,164
buffer_load_dwordx4 v2, s[20:23], s55 offen offset:1024 sc1 lds
Mv 80,144,164
Cv 56,56,57
Cv 57,58,59
Mv 84,148,164
Cv 58,60,61
Cv 59,62,63
Mv 88,152,164
Cv 64,64,65
Cv 65,66,67
Mv 92,156,164
Cv 66,68,69
Cv 67,70,71
Mv 96,128,168
Cv 72,72,73
Cv 73,74,75
Mv 100,132,168
Cv 74,76,77
Cv 75,78,79
Mv 104,136,168
Cv 80,80,81
Cv 81,82,83
Mv 108,140,168
buffer_load_dwordx4 v2, s[20:23], s55 offen offset:2048 sc1 lds
Mv 112,144,168
Cv 82,84,85
Cv 83,86,87
)" R"(
Mv 116,148,168
Cv 88,88,89
Cv 89,90,91
Mv 120,152,168
Cv 90,92,93
Cv 91,94,95
Mv 124,156,168
buffer_store_dwordx4 v[32:35], v2, s[20:23], s45 offen offset:0 sc0 sc1
Mv 128,128,172
Mv 132,132,172
Mv 136,136,172
Mv 140,140,172
buffer_load_dwordx4 v2, s[20:23], s55 offen offset:3072 sc1 lds
Mv 144,144,172
Mv 148,148,172
s_nop 1
Cv 128,128,129
Cv 129,130,131
Mv 152,152,172
Cv 130,132,133
Cv 131,134,135
Mv 156,156,172
buffer_store_dwordx4 v[40:43], v2, s[20:23], s45 offen offset:1024 sc0 sc1
Mv 160,0,48
Cv 136,136,137
Cv 137,138,139
Mv 160,128,176
Cv 138,140,141
Cv 139,142,143
Mv 164,4,48
s_add_u32 m0, s36, 36864
Cv 144,144,145
Mv 164,132,176
buffer_load_dwordx4 v3, s[20:23], s55 offen offset:0 sc1 lds
Mv 168,8,48
Cv 145,146,147
Cv 146,148,149
Mv 168,136,176
Cv 147,150,151
Cv 152,152,153
Mv 172,12,48
Cv 153,154,155
Cv 154,156,157
Mv 172,140,176
buffer_store_dwordx4 v[48:51], v2, s[20:23], s45 offen offset:2048 sc0 sc1
Mv 176,16,48
Cv 155,158,159
Cv 132,96,97
Mv 176,144,176
Cv 133,98,99
Cv 134,100,101
Mv 180,20,48
Cv 135,102,103
Cv 140,104,105
Mv 180,148,176
buffer_load_dwordx4 v3, s[20:23], s55 offen offset:1024 sc1 lds
Mv 184,24,48
Cv 141,106,107
Cv 142,108,109
Mv 184,152,176
Cv 143,110,111
Cv 148,112,113
Mv 188,28,48
Cv 149,114,115
)" R"(
Cv 150,116,117
Mv 188,156,176
buffer_store_dwordx4 v[56:59], v2, s[20:23], s45 offen offset:3072 sc0 sc1
Mv 192,0,52
Cv 151,118,119
Cv 156,120,121
Mv 192,128,180
Cv 157,122,123
Cv 158,124,125
Mv 196,4,52
Cv 159,126,127
Cv 160,160,161
Mv 196,132,180
buffer_load_dwordx4 v3, s[20:23], s55 offen offset:2048 sc1 lds
Mv 200,8,52
Cv 161,162,163
Cv 162,164,165
Mv 200,136,180
Cv 163,166,167
v_accvgpr_write_b32 a224, v160
Mv 204,12,52
v_accvgpr_write_b32 a225, v161
v_accvgpr_write_b32 a226, v162
Mv 204,140,180
buffer_store_dwordx4 v[64:67], v3, s[20:23], s45 offen offset:0 sc0 sc1
Mv 208,16,52
v_accvgpr_write_b32 a227, v163
Cv 168,168,169
Mv 208,144,180
Cv 169,170,171
Cv 170,172,173
Mv 212,20,52
Cv 171,174,175
v_accvgpr_write_b32 a228, v168
Mv 212,148,180
buffer_load_dwordx4 v3, s[20:23], s55 offen offset:3072 sc1 lds
Mv 216,24,52
v_accvgpr_write_b32 a229, v169
v_accvgpr_write_b32 a230, v170
Mv 216,152,180
v_accvgpr_write_b32 a231, v171
Cv 176,176,177
Mv 220,28,52
Cv 177,178,179
Cv 178,180,181
Mv 220,156,180
buffer_store_dwordx4 v[72:75], v3, s[20:23], s45 offen offset:1024 sc0 sc1
Mv 224,0,56
Cv 179,182,183
v_accvgpr_write_b32 a232, v176
Ma 192,0,60
v_accvgpr_write_b32 a233, v177
v_accvgpr_write_b32 a234, v178
Ma 196,4,60
s_add_u32 m0, s36, 69632
v_accvgpr_write_b32 a235, v179
Mv 228,4,56
buffer_load_dwordx4 v3, s[20:23], s56 offen offset:0 sc1 lds
Mv 232,8,56
Cv 184,184,185
Cv 185,186,187
Ma 200,8,60
Cv 186,188,189
Cv 187,190,191
)" R"(
Ma 204,12,60
v_accvgpr_write_b32 a236, v184
v_accvgpr_write_b32 a237, v185
Mv 236,12,56
s_waitcnt vmcnt(16)
s_barrier
Mv 240,16,56
v_cmp_eq_u32_e32 vcc, s3, v4
s_andn2_b64 s[28:29], -1, vcc
s_cbranch_scc1 .Lzsp15
.Lzok15:
Ma 208,16,60
buffer_store_dwordx4 v[80:83], v3, s[20:23], s45 offen offset:2048 sc0 sc1
Ma 212,20,60
v_accvgpr_write_b32 a238, v186
v_accvgpr_write_b32 a239, v187
Mv 244,20,56
buffer_load_dwordx4 v3, s[20:23], s56 offen offset:1024 sc1 lds
Mv 248,24,56
Cv 192,192,193
Cv 193,194,195
Ma 216,24,60
Cv 194,196,197
Cv 195,198,199
Ma 220,28,60
v_accvgpr_write_b32 a240, v192
v_accvgpr_write_b32 a241, v193
Mv 252,28,56
buffer_store_dwordx4 v[88:91], v3, s[20:23], s45 offen offset:3072 sc0 sc1
Mv 224,128,184
Rb 0,1,0
v_accvgpr_write_b32 a242, v194
Mv 228,132,184
Rb 4,1,1024
v_accvgpr_write_b32 a243, v195
Mv 232,136,184
Rb 8,1,2048
Cv 200,200,201
Mv 236,140,184
buffer_load_dwordx4 v3, s[20:23], s56 offen offset:2048 sc1 lds
Rb 12,1,3072
Mv 240,144,184
Rb 16,1,8192
Cv 201,202,203
Mv 244,148,184
Rb 20,1,9216
Cv 202,204,205
Mv 248,152,184
Rb 24,1,10240
Cv 203,206,207
Mv 252,156,184
Rb 28,1,11264
v_accvgpr_write_b32 a244, v200
Ma 192,128,188
Rb 32,1,4096
v_accvgpr_write_b32 a245, v201
Ma 196,132,188
Rb 36,1,5120
v_accvgpr_write_b32 a246, v202
Ma 200,136,188
Rb 40,1,6144
v_accvgpr_write_b32 a247, v203
Ma 204,140,188
buffer_load_dwordx4 v3, s[20:23], s56 offen offset:3072 sc1 lds
Rb 44,1,7168
)" R"(
Ma 208,144,188
Ma 212,148,188
Ma 216,152,188
Ma 220,156,188
s_add_u32 s56, s56, 0x10000
s_branch .LzFB
.LzH6:
s_cbranch_execz .LzH11
.LzBV:
Mz 32,32,0
Rt 128,18,16384
v_mul_f32_e32 v64, s54, v64
Mz 36,36,0
v_mul_f32_e32 v65, s54, v65
Cv 64,64,65
Mz 96,40,0
Rt 130,18,18432
v_accvgpr_write_b32 a112, v64
Mz 100,44,0
s_add_u32 s39, s39, 0x40000
v_mul_f32_e32 v66, s54, v66
Mz 160,48,0
Rt 132,19,16384
s_add_u32 m0, s37, 0
Mz 164,52,0
buffer_load_dwordx4 v12, s[8:11], s39 offen offset:0 lds
Mz 224,56,0
Rt 134,19,18432
v_mul_f32_e32 v67, s54, v67
Mz 228,60,0
Cv 66,66,67
v_accvgpr_write_b32 a113, v66
Mz 236,60,4
Rt 136,20,16384
v_mul_f32_e32 v68, s54, v68
Mz 232,56,4
v_mul_f32_e32 v69, s54, v69
Cv 68,68,69
Mz 172,52,4
Rt 138,20,18432
v_accvgpr_write_b32 a114, v68
Mz 168,48,4
v_mul_f32_e32 v70, s54, v70
v_mul_f32_e32 v71, s54, v71
Mz 108,44,4
Rt 140,21,16384
Cv 70,70,71
Mz 104,40,4
buffer_load_dwordx4 v13, s[8:11], s39 offen offset:1024 lds
Mz 44,36,4
Rt 142,21,18432
v_accvgpr_write_b32 a115, v70
Mz 40,32,4
v_mul_f32_e32 v72, s54, v72
v_mul_f32_e32 v73, s54, v73
Mz 48,32,8
Rt 144,22,16384
Cv 72,72,73
Mz 52,36,8
v_accvgpr_write_b32 a116, v72
v_mul_f32_e32 v74, s54, v74
Mz 112,40,8
Rt 146,22,18432
v_mul_f32_e32 v75, s54, v75
)" R"(
Mz 116,44,8
Cv 74,74,75
v_accvgpr_write_b32 a117, v74
Mz 176,48,8
Rt 148,23,16384
v_mul_f32_e32 v76, s54, v76
Mz 180,52,8
buffer_load_dwordx4 v14, s[8:11], s39 offen offset:2048 lds
Mz 240,56,8
Rt 150,23,18432
v_mul_f32_e32 v77, s54, v77
Mz 244,60,8
Cv 76,76,77
v_accvgpr_write_b32 a118, v76
Mz 252,60,12
Rt 152,24,16384
v_mul_f32_e32 v78, s54, v78
Mz 248,56,12
v_mul_f32_e32 v79, s54, v79
Cv 78,78,79
Mz 188,52,12
Rt 154,24,18432
v_accvgpr_write_b32 a119, v78
Mz 184,48,12
v_mul_f32_e32 v80, s54, v80
v_mul_f32_e32 v81, s54, v81
Mz 124,44,12
Rt 156,25,16384
Cv 80,80,81
Mz 120,40,12
buffer_load_dwordx4 v15, s[8:11], s39 offen offset:3072 lds
Mz 60,36,12
Rt 158,25,18432
v_accvgpr_write_b32 a120, v80
Mz 56,32,12
v_mul_f32_e32 v82, s54, v82
v_mul_f32_e32 v83, s54, v83
s_mov_b32 s85, 0x1508800
Mz 128,40,16
Rb 160,17,0
Cv 82,82,83
v_and_b32 v2, 7, v0
Mz 132,44,16
v_accvgpr_write_b32 a121, v82
v_mul_f32_e32 v84, s54, v84
v_lshlrev_b32 v2, 7, v2
Mz 192,48,16
Rb 164,17,2048
v_mul_f32_e32 v85, s54, v85
buffer_load_dword v4, v2, s[20:23], s85 offen sc0 sc1
Mz 196,52,16
Cv 84,84,85
v_accvgpr_write_b32 a122, v84
v_mfma_f32_16x16x32_bf16 a[192:195], a[56:59], a[16:19], 0
Rb 168,17,8192
v_mul_f32_e32 v86, s54, v86
v_mfma_f32_16x16x32_bf16 a[196:199], a[60:63], a[16:19], 0
v_mul_f32_e32 v87, s54, v87
Cv 86,86,87
v_mfma_f32_16x16x32_bf16 a[204:207], a[60:63], a[20:23], 0
Rb 172,17,10240
v_accvgpr_write_b32 a123, v86
v_mfma_f32_16x16x32_bf16 a[200:203], a[56:59], a[20:23], 0
v_mul_f32_e32 v88, s54, v88
)" R"(
v_mul_f32_e32 v89, s54, v89
Mz 204,52,20
Rb 176,17,16384
Cv 88,88,89
Mz 200,48,20
v_accvgpr_write_b32 a124, v88
v_mul_f32_e32 v90, s54, v90
Mz 140,44,20
Rb 180,17,18432
v_mul_f32_e32 v91, s54, v91
Mz 136,40,20
Cv 90,90,91
v_accvgpr_write_b32 a125, v90
Mz 144,40,24
Rb 184,17,24576
v_mul_f32_e32 v92, s54, v92
Mz 148,44,24
v_mul_f32_e32 v93, s54, v93
Cv 92,92,93
Mz 208,48,24
Rb 188,17,26624
v_accvgpr_write_b32 a126, v92
Mz 212,52,24
v_mul_f32_e32 v94, s54, v94
v_mul_f32_e32 v95, s54, v95
v_mfma_f32_16x16x32_bf16 a[208:211], a[56:59], a[24:27], 0
Cv 94,94,95
v_accvgpr_write_b32 a127, v94
v_mfma_f32_16x16x32_bf16 a[212:215], a[60:63], a[24:27], 0
v_mfma_f32_16x16x32_bf16 a[220:223], a[60:63], a[28:31], 0
v_mfma_f32_16x16x32_bf16 a[216:219], a[56:59], a[28:31], 0
Mz 220,52,28
Mz 216,48,28
Mz 156,44,28
Mz 152,40,28
s_lshl_b32 s29, s91, 22
s_add_u32 s28, s89, s29
Mz 64,32,16
s_bfe_u32 s29, s63, 0x80000
s_lshl_b32 s29, s29, 16
Mz 68,36,16
s_add_u32 s45, s28, s29
s_bfe_u32 s29, s63, 0x80008
Mz 72,32,20
s_lshl_b32 s29, s29, 16
s_add_u32 s46, s28, s29
Mz 76,36,20
s_bfe_u32 s29, s63, 0x80010
s_lshl_b32 s29, s29, 16
Mz 80,32,24
s_add_u32 s47, s28, s29
s_bfe_u32 s29, s63, 0x80018
s_lshl_b32 s29, s29, 16
Mz 84,36,24
s_add_u32 s48, s28, s29
s_add_u32 s50, s89, 0xd40000
Mz 88,32,28
s_lshl2_add_u32 s28, s91, s93
s_lshl_b32 s28, s28, 7
Mz 92,36,28
s_waitcnt vmcnt(9) lgkmcnt(0)
s_barrier
Mv 32,160,128
Rt 0,18,32768
s_add_u32 s49, s28, s50
)" R"(
Mv 36,164,128
s_add_u32 s38, s38, 0xffff0080
s_add_u32 s49, s49, 4096
Mv 96,168,128
Rt 2,18,34816
s_add_u32 m0, s36, 0
Mv 100,172,128
buffer_load_dwordx4 v8, s[4:7], s38 offen offset:0 lds
Mv 160,176,128
Rt 4,19,32768
s_lshl_b32 s52, s35, 7
s_add_u32 s52, s52, s50
Mv 164,180,128
s_add_u32 s52, s52, 0xd000
s_lshl_b32 s51, s35, 17
Mv 224,184,128
Rt 6,19,34816
s_add_u32 s51, s51, s89
Mv 228,188,128
buffer_load_dwordx4 v9, s[4:7], s38 offen offset:1024 lds
Mv 236,188,132
Rt 8,20,32768
s_add_u32 s51, s51, 0x800000
Mv 232,184,132
s_cmp_lg_u32 s33, 0
s_cmov_b32 s49, 0x40000000
s_cmov_b32 s52, 0x40000000
Mv 172,180,132
Rt 10,20,34816
s_mov_b32 s53, 0x40000
Mv 168,176,132
buffer_load_dwordx4 v10, s[4:7], s38 offen offset:2048 lds
.LzH7:
s_cbranch_execz .LzH12
Mv 108,172,132
Rt 12,21,32768
s_mov_b32 s62, 0x4e800000
Mv 104,168,132
s_add_u32 s67, s89, 0xc00000
s_lshl3_add_u32 s28, s34, s35
Mv 44,164,132
Rt 14,21,34816
s_add_u32 s64, s51, 0x100000
Mv 40,160,132
buffer_load_dwordx4 v11, s[4:7], s38 offen offset:3072 lds
Mv 48,160,136
Rt 16,22,32768
s_lshl2_add_u32 s28, s28, s33
Mv 52,164,136
s_add_u32 s38, s38, 0x10000
s_lshl2_add_u32 s65, s35, s33
s_lshl_b32 s65, s65, 7
Mv 112,168,136
Rt 18,22,34816
s_add_u32 m0, s36, 4096
Mv 116,172,136
buffer_load_dwordx4 v8, s[4:7], s38 offen offset:0 lds
Mv 176,176,136
Rt 20,23,32768
s_add_u32 s65, s65, s50
Mv 180,180,136
s_add_u32 s65, s65, 0xc000
s_lshl_b32 s66, s28, 10
Mv 240,184,136
)" R"(
Rt 22,23,34816
s_add_u32 s66, s66, 0x1400000
Mv 244,188,136
buffer_load_dwordx4 v9, s[4:7], s38 offen offset:1024 lds
Mv 252,188,140
Rt 24,24,32768
s_sub_u32 s28, 15, s35
Mv 248,184,140
s_lshl2_add_u32 s28, s28, s33
s_and_b32 s29, s34, 1
s_lshl1_add_u32 s29, s28, s29
Mv 188,180,140
Rt 26,24,34816
s_lshl_b32 s73, s29, 15
Mv 184,176,140
buffer_load_dwordx4 v10, s[4:7], s38 offen offset:2048 lds
Mv 124,172,140
Rt 28,25,32768
s_and_b32 s29, s89, 0xfe000000
Mv 120,168,140
s_add_u32 s73, s73, s29
s_add_u32 s73, s73, 0x1002000
Mv 60,164,140
Rt 30,25,34816
s_lshl4_add_u32 s28, s28, s34
s_lshl_b32 s68, s28, 8
Mv 56,160,140
buffer_load_dwordx4 v11, s[4:7], s38 offen offset:3072 lds
Mv 64,160,144
Rb 32,16,32768
s_add_u32 s68, s68, 0x1510000
Mv 68,164,144
s_xor_b32 s72, s73, 0x3c6000
s_mov_b32 s55, s89
Mv 128,168,144
Rb 36,16,34816
s_mov_b32 s57, 18
Mv 132,172,144
s_add_u32 s39, s39, 0x40000
s_mov_b32 s59, s67
Mv 192,176,144
Rb 40,16,40960
s_add_u32 m0, s37, 16384
Mv 196,180,144
buffer_load_dwordx4 v12, s[8:11], s39 offen offset:0 lds
Ma 192,184,144
Rb 44,16,43008
s_mov_b32 s58, 0
Ma 196,188,144
s_cmp_eq_u32 s35, 7
s_cmov_b32 s58, 2
Ma 204,188,148
Rb 48,16,49152
Ma 200,184,148
s_cmp_eq_u32 s35, 7
s_cmov_b32 s64, 0x40000000
Mv 204,180,148
Rb 52,16,51200
Mv 200,176,148
buffer_load_dwordx4 v13, s[8:11], s39 offen offset:1024 lds
Mv 140,172,148
Rb 56,16,57344
Mv 136,168,148
s_cmp_eq_u32 s35, 7
)" R"(
s_cmov_b32 s66, 0x40000000
Mv 76,164,148
Rb 60,16,59392
s_add_u32 s56, s55, 0x400000
Mv 72,160,148
Mv 80,160,152
Mv 84,164,152
buffer_load_dwordx4 v14, s[8:11], s39 offen offset:2048 lds
Mv 144,168,152
Mv 148,172,152
Mv 208,176,152
Mv 212,180,152
Ma 208,184,152
Ma 212,188,152
buffer_load_dwordx4 v15, s[8:11], s39 offen offset:3072 lds
Ma 220,188,156
Ma 216,184,156
Mv 220,180,156
Mv 216,176,156
Mv 156,172,156
Mv 152,168,156
Mv 92,164,156
Mv 88,160,156
s_waitcnt vmcnt(12) lgkmcnt(0)
s_barrier
Mv 32,32,0
Rt 128,18,49152
v_xor_b32 v4, s3, v4
v_cmp_gt_u32_e32 vcc, 2, v4
s_andn2_b32 s28, 0xff, vcc_lo
s_cbranch_scc1 .Lzsp16
.Lzok16:
Mv 36,36,0
v_xor_b32 v4, s3, v4
v_and_b32 v4, 1, v4
Mv 96,40,0
Rt 130,18,51200
Mv 100,44,0
s_add_u32 s39, s39, 0x40000
Mv 160,48,0
Rt 132,19,49152
s_add_u32 m0, s37, 32768
Mv 164,52,0
buffer_load_dwordx4 v12, s[8:11], s39 offen offset:0 lds
Mv 224,56,0
Rt 134,19,51200
Mv 228,60,0
v_cmp_eq_u32_e32 vcc, 1, v4
s_mov_b32 s85, vcc_lo
Mv 236,60,4
Rt 136,20,49152
s_and_b32 s28, s2, 7
Mv 232,56,4
s_lshr_b32 s28, s85, s28
s_xor_b32 s28, s28, s34
Mv 172,52,4
Rt 138,20,51200
s_and_b32 s28, s28, 1
Mv 168,48,4
s_lshl_b32 s84, s28, 13
s_add_u32 s49, s49, s84
Mv 108,44,4
Rt 140,21,49152
s_add_u32 s50, s50, s84
Mv 104,40,4
)" R"(
buffer_load_dwordx4 v13, s[8:11], s39 offen offset:1024 lds
Mv 44,36,4
Rt 142,21,51200
s_add_u32 s60, s50, 7168
Mv 40,32,4
s_add_u32 s52, s52, s84
s_add_u32 s65, s65, s84
Mv 48,32,8
Rt 144,22,49152
s_lshr_b32 s28, s2, 6
Mv 52,36,8
s_lshl_b32 s28, s28, 14
s_add_u32 s28, s28, s50
Mv 112,40,8
Rt 146,22,51200
s_bfe_u32 s29, s2, 0x20004
Mv 116,44,8
s_lshl2_add_u32 s29, s29, s33
s_lshl_b32 s29, s29, 9
Mv 176,48,8
Rt 148,23,49152
s_add_u32 s28, s28, s29
Mv 180,52,8
buffer_load_dwordx4 v14, s[8:11], s39 offen offset:2048 lds
Mv 240,56,8
Rt 150,23,51200
v_min_u32 v4, 3, v0
Mv 244,60,8
v_lshlrev_b32 v4, 7, v4
Mv 252,60,12
Rt 152,24,49152
Mv 248,56,12
buffer_load_dword v4, v4, s[20:23], s28 offen sc0 sc1
Mv 188,52,12
Rt 154,24,51200
s_lshl1_add_u32 s28, s36, s84
Mv 184,48,12
v_add_u32 v2, s28, v1
v_add_u32 v3, 0x1000, v2
Mv 124,44,12
Rt 156,25,49152
s_lshl_b32 s28, s34, 13
Mv 120,40,12
buffer_load_dwordx4 v15, s[8:11], s39 offen offset:3072 lds
Mv 60,36,12
Rt 158,25,51200
s_xor_b32 s28, s28, s84
Mv 56,32,12
s_and_b32 s28, s28, 0x2000
s_xor_b32 s73, s73, s28
Mv 64,32,16
Rb 160,17,32768
s_xor_b32 s72, s72, s28
Mv 68,36,16
Mv 128,40,16
Rb 164,17,34816
s_mov_b64 s[16:17], s[70:71]
Mv 132,44,16
s_mov_b32 s18, 0x188000
Mv 192,48,16
Rb 168,17,40960
Mv 196,52,16
s_cmp_eq_u32 s33, 0
s_cmov_b32 s18, 0x380000
)" R"(
Ma 192,56,16
Rb 172,17,43008
s_mul_i32 s43, s2, 3072
Ma 196,60,16
s_cmp_eq_u32 s33, 0
s_cselect_b32 s44, 3, 6
Ma 204,60,20
Rb 176,17,49152
s_mul_i32 s44, s44, 0x60000
Ma 200,56,20
buffer_store_dwordx4 a[64:67], v2, s[20:23], s51 offen offset:0 sc0 sc1
Mv 204,52,20
Rb 180,17,51200
s_sub_u32 s43, s43, s44
Mv 200,48,20
v_min_u32 v26, 23, v0
v_lshlrev_b32 v26, 7, v26
Mv 140,44,20
Rb 184,17,57344
v_and_b32 v5, 7, v0
Mv 136,40,20
v_lshlrev_b32 v5, 7, v5
Mv 76,36,20
Rb 188,17,59392
Mv 72,32,20
Mv 80,32,24
Mv 84,36,24
buffer_store_dwordx4 a[68:71], v2, s[20:23], s51 offen offset:1024 sc0 sc1
Mv 144,40,24
Mv 148,44,24
Mv 208,48,24
Mv 212,52,24
Ma 208,56,24
Ma 212,60,24
Ma 220,60,28
Ma 216,56,28
buffer_store_dwordx4 a[72:75], v2, s[20:23], s51 offen offset:2048 sc0 sc1
Mv 220,52,28
Mv 216,48,28
Mv 156,44,28
Mv 152,40,28
Mv 92,36,28
Mv 88,32,28
s_waitcnt vmcnt(12) lgkmcnt(0)
s_barrier
Mv 32,160,128
Rt 0,18,0
Mv 36,164,128
s_add_u32 s38, s38, 0xffff0080
Mv 96,168,128
Rt 2,18,2048
s_add_u32 m0, s36, 32768
Mv 100,172,128
buffer_load_dwordx4 v8, s[4:7], s38 offen offset:0 lds
Mv 160,176,128
Rt 4,19,0
Mv 164,180,128
Mv 224,184,128
Rt 6,19,2048
Mv 228,188,128
buffer_load_dwordx4 v9, s[4:7], s38 offen offset:1024 lds
Mv 236,188,132
Rt 8,20,0
Mv 232,184,132
Mv 172,180,132
)" R"(
Rt 10,20,2048
Mv 168,176,132
buffer_load_dwordx4 v10, s[4:7], s38 offen offset:2048 lds
Mv 108,172,132
Rt 12,21,0
Mv 104,168,132
Mv 44,164,132
Rt 14,21,2048
Mv 40,160,132
buffer_load_dwordx4 v11, s[4:7], s38 offen offset:3072 lds
Mv 48,160,136
Rt 16,22,0
Mv 52,164,136
s_add_u32 s38, s38, 0x10000
Mv 112,168,136
Rt 18,22,2048
s_add_u32 m0, s36, 36864
Mv 116,172,136
buffer_load_dwordx4 v8, s[4:7], s38 offen offset:0 lds
Mv 176,176,136
Rt 20,23,0
Mv 180,180,136
Mv 240,184,136
Rt 22,23,2048
Mv 244,188,136
buffer_load_dwordx4 v9, s[4:7], s38 offen offset:1024 lds
Mv 252,188,140
Rt 24,24,0
Mv 248,184,140
Mv 188,180,140
Rt 26,24,2048
Mv 184,176,140
buffer_load_dwordx4 v10, s[4:7], s38 offen offset:2048 lds
Mv 124,172,140
Rt 28,25,0
Mv 120,168,140
Mv 60,164,140
Rt 30,25,2048
v_accvgpr_write_b32 a224, -1
Mv 56,160,140
buffer_load_dwordx4 v11, s[4:7], s38 offen offset:3072 lds
Mv 64,160,144
Rb 32,16,0
v_accvgpr_write_b32 a225, -1
Mv 68,164,144
v_accvgpr_write_b32 a226, -1
v_accvgpr_write_b32 a227, -1
Mv 128,168,144
Rb 36,16,2048
s_add_u32 s88, s73, 0x4000
Mv 132,172,144
s_add_u32 s39, s39, 0x40000
s_mov_b32 s40, 5
Mv 192,176,144
Rb 40,16,8192
s_add_u32 m0, s37, 49152
Mv 196,180,144
buffer_load_dwordx4 v12, s[8:11], s39 offen offset:0 lds
Ma 192,184,144
Rb 44,16,10240
s_mov_b32 s94, 0
Ma 196,188,144
Ma 204,188,148
Rb 48,16,16384
)" R"(
Ma 200,184,148
Mv 204,180,148
Rb 52,16,18432
Mv 200,176,148
buffer_load_dwordx4 v13, s[8:11], s39 offen offset:1024 lds
Mv 140,172,148
Rb 56,16,24576
Mv 136,168,148
Mv 76,164,148
Rb 60,16,26624
Mv 72,160,148
Mv 80,160,152
Mv 84,164,152
buffer_load_dwordx4 v14, s[8:11], s39 offen offset:2048 lds
Mv 144,168,152
Mv 148,172,152
Mv 208,176,152
Mv 212,180,152
Ma 208,184,152
Ma 212,188,152
buffer_load_dwordx4 v15, s[8:11], s39 offen offset:3072 lds
Ma 220,188,156
Ma 216,184,156
Mv 220,180,156
Mv 216,176,156
Mv 156,172,156
Mv 152,168,156
Mv 92,164,156
Mv 88,160,156
s_waitcnt vmcnt(20) lgkmcnt(0)
s_barrier
Mv 32,32,0
Rt 128,18,16384
Mv 36,36,0
Mv 96,40,0
Rt 130,18,18432
Mv 100,44,0
s_add_u32 s39, s39, 0x40000
Mv 160,48,0
Rt 132,19,16384
s_add_u32 m0, s37, 0
Mv 164,52,0
buffer_load_dwordx4 v12, s[8:11], s39 offen offset:0 lds
Mv 224,56,0
Rt 134,19,18432
Mv 228,60,0
Mv 236,60,4
Rt 136,20,16384
Mv 232,56,4
buffer_store_dwordx4 a[76:79], v2, s[20:23], s51 offen offset:3072 sc0 sc1
Mv 172,52,4
Rt 138,20,18432
Mv 168,48,4
Mv 108,44,4
Rt 140,21,16384
Mv 104,40,4
buffer_load_dwordx4 v13, s[8:11], s39 offen offset:1024 lds
Mv 44,36,4
Rt 142,21,18432
Mv 40,32,4
Mv 48,32,8
Rt 144,22,16384
Mv 52,36,8
buffer_store_dwordx4 a[80:83], v3, s[20:23], s51 offen offset:0 sc0 sc1
Mv 112,40,8
)" R"(
Rt 146,22,18432
Mv 116,44,8
Mv 176,48,8
Rt 148,23,16384
Mv 180,52,8
buffer_load_dwordx4 v14, s[8:11], s39 offen offset:2048 lds
Mv 240,56,8
Rt 150,23,18432
Mv 244,60,8
Mv 252,60,12
Rt 152,24,16384
Mv 248,56,12
buffer_store_dwordx4 a[84:87], v3, s[20:23], s51 offen offset:1024 sc0 sc1
Mv 188,52,12
Rt 154,24,18432
Mv 184,48,12
Mv 124,44,12
Rt 156,25,16384
Mv 120,40,12
buffer_load_dwordx4 v15, s[8:11], s39 offen offset:3072 lds
Mv 60,36,12
Rt 158,25,18432
Mv 56,32,12
Mv 64,32,16
Rb 160,17,0
Mv 68,36,16
buffer_store_dwordx4 a[88:91], v3, s[20:23], s51 offen offset:2048 sc0 sc1
Mv 128,40,16
Rb 164,17,2048
Mv 132,44,16
Mv 192,48,16
Rb 168,17,8192
Mv 196,52,16
Ma 192,56,16
Rb 172,17,10240
Ma 196,60,16
Ma 204,60,20
Rb 176,17,16384
Ma 200,56,20
buffer_store_dwordx4 a[92:95], v3, s[20:23], s51 offen offset:3072 sc0 sc1
Mv 204,52,20
Rb 180,17,18432
Mv 200,48,20
Mv 140,44,20
Rb 184,17,24576
Mv 136,40,20
Mv 76,36,20
Rb 188,17,26624
Mv 72,32,20
Mv 80,32,24
Mv 84,36,24
s_add_u32 s51, s51, 0x10000
buffer_store_dwordx4 a[96:99], v2, s[20:23], s51 offen offset:0 sc0 sc1
Mv 144,40,24
Mv 148,44,24
Mv 208,48,24
Mv 212,52,24
Ma 208,56,24
Ma 212,60,24
Ma 220,60,28
Ma 216,56,28
buffer_store_dwordx4 a[100:103], v2, s[20:23], s51 offen offset:1024 sc0 sc1
Mv 220,52,28
Mv 216,48,28
)" R"(
Mv 156,44,28
Mv 152,40,28
Mv 92,36,28
Mv 88,32,28
s_waitcnt vmcnt(15) lgkmcnt(0)
s_barrier
Mv 32,160,128
Rt 0,18,32768
Mv 36,164,128
s_add_u32 s38, s38, 0xffff0080
Mv 96,168,128
Rt 2,18,34816
s_add_u32 m0, s36, 0
Mv 100,172,128
buffer_load_dwordx4 v8, s[4:7], s38 offen offset:0 lds
Mv 160,176,128
Rt 4,19,32768
Mv 164,180,128
Mv 224,184,128
Rt 6,19,34816
Mv 228,188,128
buffer_load_dwordx4 v9, s[4:7], s38 offen offset:1024 lds
Mv 236,188,132
Rt 8,20,32768
Mv 232,184,132
Mv 172,180,132
Rt 10,20,34816
Mv 168,176,132
buffer_load_dwordx4 v10, s[4:7], s38 offen offset:2048 lds
Mv 108,172,132
Rt 12,21,32768
Mv 104,168,132
Mv 44,164,132
Rt 14,21,34816
Mv 40,160,132
buffer_load_dwordx4 v11, s[4:7], s38 offen offset:3072 lds
Mv 48,160,136
Rt 16,22,32768
Mv 52,164,136
s_add_u32 s38, s38, 0x10000
Mv 112,168,136
Rt 18,22,34816
s_add_u32 m0, s36, 4096
Mv 116,172,136
buffer_load_dwordx4 v8, s[4:7], s38 offen offset:0 lds
Mv 176,176,136
Rt 20,23,32768
Mv 180,180,136
Mv 240,184,136
Rt 22,23,34816
Mv 244,188,136
buffer_load_dwordx4 v9, s[4:7], s38 offen offset:1024 lds
Mv 252,188,140
Rt 24,24,32768
Mv 248,184,140
Mv 188,180,140
Rt 26,24,34816
Mv 184,176,140
buffer_load_dwordx4 v10, s[4:7], s38 offen offset:2048 lds
Mv 124,172,140
Rt 28,25,32768
Mv 120,168,140
Mv 60,164,140
Rt 30,25,34816
Mv 56,160,140
)" R"(
buffer_load_dwordx4 v11, s[4:7], s38 offen offset:3072 lds
Mv 64,160,144
Rb 32,16,32768
Mv 68,164,144
.LzH8:
s_cbranch_execz .LzH13
Mv 128,168,144
Rb 36,16,34816
Mv 132,172,144
s_add_u32 s39, s39, 0x40000
Mv 192,176,144
Rb 40,16,40960
s_add_u32 m0, s37, 16384
Mv 196,180,144
buffer_load_dwordx4 v12, s[8:11], s39 offen offset:0 lds
Ma 192,184,144
Rb 44,16,43008
Ma 196,188,144
Ma 204,188,148
Rb 48,16,49152
Ma 200,184,148
Mv 204,180,148
Rb 52,16,51200
Mv 200,176,148
buffer_load_dwordx4 v13, s[8:11], s39 offen offset:1024 lds
Mv 140,172,148
Rb 56,16,57344
Mv 136,168,148
Mv 76,164,148
Rb 60,16,59392
Mv 72,160,148
Mv 80,160,152
Mv 84,164,152
buffer_load_dwordx4 v14, s[8:11], s39 offen offset:2048 lds
Mv 144,168,152
Mv 148,172,152
Mv 208,176,152
Mv 212,180,152
Ma 208,184,152
Ma 212,188,152
buffer_load_dwordx4 v15, s[8:11], s39 offen offset:3072 lds
Ma 220,188,156
Ma 216,184,156
s_add_u32 s43, s43, 0x60000
Mv 220,180,156
Mv 216,176,156
buffer_load_dword v27, v26, s[16:19], s43 offen
Mv 156,172,156
Mv 152,168,156
Mv 92,164,156
Mv 88,160,156
s_waitcnt vmcnt(24) lgkmcnt(0)
s_barrier
Mv 32,32,0
Rt 128,18,49152
Mv 36,36,0
Mv 96,40,0
Rt 130,18,51200
Mv 100,44,0
s_add_u32 s39, s39, 0x40000
Mv 160,48,0
Rt 132,19,49152
s_add_u32 m0, s37, 32768
Mv 164,52,0
buffer_load_dwordx4 v12, s[8:11], s39 offen offset:0 lds
)" R"(
Mv 224,56,0
Rt 134,19,51200
Mv 228,60,0
Mv 236,60,4
Rt 136,20,49152
Mv 232,56,4
buffer_store_dwordx4 a[104:107], v2, s[20:23], s51 offen offset:2048 sc0 sc1
Mv 172,52,4
Rt 138,20,51200
Mv 168,48,4
Mv 108,44,4
Rt 140,21,49152
Mv 104,40,4
buffer_load_dwordx4 v13, s[8:11], s39 offen offset:1024 lds
Mv 44,36,4
Rt 142,21,51200
Mv 40,32,4
Mv 48,32,8
Rt 144,22,49152
Mv 52,36,8
buffer_store_dwordx4 a[108:111], v2, s[20:23], s51 offen offset:3072 sc0 sc1
Mv 112,40,8
Rt 146,22,51200
Mv 116,44,8
Mv 176,48,8
Rt 148,23,49152
Mv 180,52,8
buffer_load_dwordx4 v14, s[8:11], s39 offen offset:2048 lds
Mv 240,56,8
Rt 150,23,51200
Mv 244,60,8
Mv 252,60,12
Rt 152,24,49152
Mv 248,56,12
buffer_store_dwordx4 a[112:115], v3, s[20:23], s51 offen offset:0 sc0 sc1
Mv 188,52,12
Rt 154,24,51200
Mv 184,48,12
Mv 124,44,12
Rt 156,25,49152
Mv 120,40,12
buffer_load_dwordx4 v15, s[8:11], s39 offen offset:3072 lds
Mv 60,36,12
Rt 158,25,51200
Mv 56,32,12
Mv 64,32,16
Rb 160,17,32768
Mv 68,36,16
buffer_store_dwordx4 a[116:119], v3, s[20:23], s51 offen offset:1024 sc0 sc1
Mv 128,40,16
Rb 164,17,34816
Mv 132,44,16
Mv 192,48,16
Rb 168,17,40960
Mv 196,52,16
Ma 192,56,16
Rb 172,17,43008
Ma 196,60,16
Ma 204,60,20
Rb 176,17,49152
Ma 200,56,20
buffer_store_dwordx4 a[120:123], v3, s[20:23], s51 offen offset:2048 sc0 sc1
Mv 204,52,20
Rb 180,17,51200
)" R"(
Mv 200,48,20
Mv 140,44,20
Rb 184,17,57344
Mv 136,40,20
Mv 76,36,20
Rb 188,17,59392
Mv 72,32,20
Mv 80,32,24
Mv 84,36,24
buffer_store_dwordx4 a[124:127], v3, s[20:23], s51 offen offset:3072 sc0 sc1
Mv 144,40,24
Mv 148,44,24
Mv 208,48,24
Mv 212,52,24
Ma 208,56,24
Ma 212,60,24
Ma 220,60,28
Ma 216,56,28
Mv 220,52,28
Mv 216,48,28
Mv 156,44,28
Mv 152,40,28
Mv 92,36,28
Mv 88,32,28
s_waitcnt vmcnt(15) lgkmcnt(0)
s_barrier
Mv 32,160,128
Rt 0,18,0
Mv 36,164,128
s_add_u32 s38, s38, 0xffff0080
Mv 96,168,128
Rt 2,18,2048
s_add_u32 m0, s36, 32768
Mv 100,172,128
buffer_load_dwordx4 v8, s[4:7], s38 offen offset:0 lds
Mv 160,176,128
Rt 4,19,0
Mv 164,180,128
Mv 224,184,128
Rt 6,19,2048
Mv 228,188,128
buffer_load_dwordx4 v9, s[4:7], s38 offen offset:1024 lds
Mv 236,188,132
Rt 8,20,0
Mv 232,184,132
Mv 172,180,132
Rt 10,20,2048
Mv 168,176,132
buffer_load_dwordx4 v10, s[4:7], s38 offen offset:2048 lds
Mv 108,172,132
Rt 12,21,0
Mv 104,168,132
Mv 44,164,132
Rt 14,21,2048
Mv 40,160,132
buffer_load_dwordx4 v11, s[4:7], s38 offen offset:3072 lds
Mv 48,160,136
Rt 16,22,0
Mv 52,164,136
s_add_u32 s38, s38, 0x10000
Mv 112,168,136
Rt 18,22,2048
s_add_u32 m0, s36, 36864
Mv 116,172,136
buffer_load_dwordx4 v8, s[4:7], s38 offen offset:0 lds
)" R"(
Mv 176,176,136
Rt 20,23,0
Mv 180,180,136
Mv 240,184,136
Rt 22,23,2048
Mv 244,188,136
buffer_load_dwordx4 v9, s[4:7], s38 offen offset:1024 lds
Mv 252,188,140
Rt 24,24,0
Mv 248,184,140
Mv 188,180,140
Rt 26,24,2048
Mv 184,176,140
buffer_load_dwordx4 v10, s[4:7], s38 offen offset:2048 lds
Mv 124,172,140
Rt 28,25,0
Mv 120,168,140
Mv 60,164,140
Rt 30,25,2048
Mv 56,160,140
buffer_load_dwordx4 v11, s[4:7], s38 offen offset:3072 lds
Mv 64,160,144
Rb 32,16,0
Mv 68,164,144
Mv 128,168,144
Rb 36,16,2048
Mv 132,172,144
s_add_u32 s39, s39, 0x40000
Mv 192,176,144
Rb 40,16,8192
s_add_u32 m0, s37, 49152
Mv 196,180,144
buffer_load_dwordx4 v12, s[8:11], s39 offen offset:0 lds
Ma 192,184,144
Rb 44,16,10240
Ma 196,188,144
Ma 204,188,148
Rb 48,16,16384
Ma 200,184,148
Mv 204,180,148
Rb 52,16,18432
Mv 200,176,148
buffer_load_dwordx4 v13, s[8:11], s39 offen offset:1024 lds
Mv 140,172,148
Rb 56,16,24576
Mv 136,168,148
Mv 76,164,148
Rb 60,16,26624
Mv 72,160,148
Mv 80,160,152
Mv 84,164,152
buffer_load_dwordx4 v14, s[8:11], s39 offen offset:2048 lds
Mv 144,168,152
Mv 148,172,152
Mv 208,176,152
Mv 212,180,152
Ma 208,184,152
Ma 212,188,152
buffer_load_dwordx4 v15, s[8:11], s39 offen offset:3072 lds
Ma 220,188,156
Ma 216,184,156
s_add_u32 s43, s43, 0x60000
Mv 220,180,156
Mv 216,176,156
)" R"(
buffer_load_dword v27, v26, s[16:19], s43 offen
Mv 156,172,156
Mv 152,168,156
Mv 92,164,156
Mv 88,160,156
s_waitcnt vmcnt(24) lgkmcnt(0)
s_barrier
.LzPBV:
Mv 32,32,0
Rt 128,18,16384
Mv 36,36,0
Mv 96,40,0
Rt 130,18,18432
Mv 100,44,0
s_add_u32 s39, s39, 0x40000
Mv 160,48,0
Rt 132,19,16384
s_add_u32 m0, s37, 0
Mv 164,52,0
buffer_load_dwordx4 v12, s[8:11], s39 offen offset:0 lds
Mv 224,56,0
Rt 134,19,18432
Mv 228,60,0
Mv 236,60,4
Rt 136,20,16384
Mv 232,56,4
Mv 172,52,4
Rt 138,20,18432
Mv 168,48,4
Mv 108,44,4
Rt 140,21,16384
Mv 104,40,4
buffer_load_dwordx4 v13, s[8:11], s39 offen offset:1024 lds
Mv 44,36,4
Rt 142,21,18432
Mv 40,32,4
Mv 48,32,8
Rt 144,22,16384
Mv 52,36,8
Mv 112,40,8
Rt 146,22,18432
Mv 116,44,8
Mv 176,48,8
Rt 148,23,16384
Mv 180,52,8
buffer_load_dwordx4 v14, s[8:11], s39 offen offset:2048 lds
Mv 240,56,8
Rt 150,23,18432
Mv 244,60,8
Mv 252,60,12
Rt 152,24,16384
Mv 248,56,12
Mv 188,52,12
Rt 154,24,18432
Mv 184,48,12
Mv 124,44,12
Rt 156,25,16384
Mv 120,40,12
buffer_load_dwordx4 v15, s[8:11], s39 offen offset:3072 lds
Mv 60,36,12
Rt 158,25,18432
Mv 56,32,12
Mv 64,32,16
Rb 160,17,0
Mv 68,36,16
)" R"(
Mv 128,40,16
Rb 164,17,2048
Mv 132,44,16
Mv 192,48,16
Rb 168,17,8192
Mv 196,52,16
buffer_store_dwordx4 a[224:227], v1, s[20:23], s72 offen sc0 sc1
Ma 192,56,16
Rb 172,17,10240
s_add_u32 s72, s72, 1024
Ma 196,60,16
Ma 204,60,20
Rb 176,17,16384
Ma 200,56,20
Mv 204,52,20
Rb 180,17,18432
Mv 200,48,20
Mv 140,44,20
Rb 184,17,24576
Mv 136,40,20
Mv 76,36,20
Rb 188,17,26624
Mv 72,32,20
Mv 80,32,24
Mv 84,36,24
buffer_store_dwordx4 a[224:227], v1, s[20:23], s88 offen sc0 sc1
Mv 144,40,24
s_add_u32 s88, s88, 1024
Mv 148,44,24
Mv 208,48,24
Mv 212,52,24
Ma 208,56,24
Ma 212,60,24
Ma 220,60,28
Ma 216,56,28
Mv 220,52,28
Mv 216,48,28
Mv 156,44,28
Mv 152,40,28
Mv 92,36,28
Mv 88,32,28
s_waitcnt vmcnt(11) lgkmcnt(0)
s_barrier
Mv 32,160,128
Rt 0,18,32768
Mv 36,164,128
s_add_u32 s38, s38, 0xffff0080
Mv 96,168,128
Rt 2,18,34816
s_add_u32 m0, s36, 0
Mv 100,172,128
buffer_load_dwordx4 v8, s[4:7], s38 offen offset:0 lds
Mv 160,176,128
Rt 4,19,32768
Mv 164,180,128
Mv 224,184,128
Rt 6,19,34816
Mv 228,188,128
buffer_load_dwordx4 v9, s[4:7], s38 offen offset:1024 lds
Mv 236,188,132
Rt 8,20,32768
Mv 232,184,132
Mv 172,180,132
Rt 10,20,34816
Mv 168,176,132
)" R"(
buffer_load_dwordx4 v10, s[4:7], s38 offen offset:2048 lds
Mv 108,172,132
Rt 12,21,32768
Mv 104,168,132
Mv 44,164,132
Rt 14,21,34816
Mv 40,160,132
buffer_load_dwordx4 v11, s[4:7], s38 offen offset:3072 lds
Mv 48,160,136
Rt 16,22,32768
Mv 52,164,136
s_add_u32 s38, s38, 0x10000
Mv 112,168,136
Rt 18,22,34816
s_add_u32 m0, s36, 4096
Mv 116,172,136
buffer_load_dwordx4 v8, s[4:7], s38 offen offset:0 lds
Mv 176,176,136
Rt 20,23,32768
Mv 180,180,136
Mv 240,184,136
Rt 22,23,34816
Mv 244,188,136
buffer_load_dwordx4 v9, s[4:7], s38 offen offset:1024 lds
Mv 252,188,140
Rt 24,24,32768
Mv 248,184,140
Mv 188,180,140
Rt 26,24,34816
Mv 184,176,140
buffer_load_dwordx4 v10, s[4:7], s38 offen offset:2048 lds
Mv 124,172,140
Rt 28,25,32768
Mv 120,168,140
Mv 60,164,140
Rt 30,25,34816
Mv 56,160,140
buffer_load_dwordx4 v11, s[4:7], s38 offen offset:3072 lds
Mv 64,160,144
Rb 32,16,32768
Mv 68,164,144
Mv 128,168,144
Rb 36,16,34816
Mv 132,172,144
s_add_u32 s39, s39, 0x40000
Mv 192,176,144
Rb 40,16,40960
s_add_u32 m0, s37, 16384
Mv 196,180,144
buffer_load_dwordx4 v12, s[8:11], s39 offen offset:0 lds
Ma 192,184,144
Rb 44,16,43008
Ma 196,188,144
Ma 204,188,148
Rb 48,16,49152
Ma 200,184,148
Mv 204,180,148
Rb 52,16,51200
Mv 200,176,148
buffer_load_dwordx4 v13, s[8:11], s39 offen offset:1024 lds
Mv 140,172,148
Rb 56,16,57344
Mv 136,168,148
Mv 76,164,148
)" R"(
Rb 60,16,59392
Mv 72,160,148
Mv 80,160,152
Mv 84,164,152
buffer_load_dwordx4 v14, s[8:11], s39 offen offset:2048 lds
Mv 144,168,152
Mv 148,172,152
Mv 208,176,152
Mv 212,180,152
Ma 208,184,152
Ma 212,188,152
buffer_load_dwordx4 v15, s[8:11], s39 offen offset:3072 lds
Ma 220,188,156
Ma 216,184,156
s_add_u32 s43, s43, 0x60000
Mv 220,180,156
Mv 216,176,156
buffer_load_dword v27, v26, s[16:19], s43 offen
Mv 156,172,156
Mv 152,168,156
Mv 92,164,156
Mv 88,160,156
s_waitcnt vmcnt(20) lgkmcnt(0)
s_barrier
buffer_store_dword v6, v7, s[20:23], s52 offen offset:0 sc0 sc1
s_mov_b32 s52, 0x40000000
Mv 32,32,0
Rt 128,18,49152
Mv 36,36,0
Mv 96,40,0
Rt 130,18,51200
Mv 100,44,0
s_add_u32 s39, s39, 0x40000
Mv 160,48,0
Rt 132,19,49152
s_add_u32 m0, s37, 32768
Mv 164,52,0
buffer_load_dwordx4 v12, s[8:11], s39 offen offset:0 lds
Mv 224,56,0
Rt 134,19,51200
Mv 228,60,0
Mv 236,60,4
Rt 136,20,49152
Mv 232,56,4
Mv 172,52,4
Rt 138,20,51200
Mv 168,48,4
Mv 108,44,4
Rt 140,21,49152
Mv 104,40,4
buffer_load_dwordx4 v13, s[8:11], s39 offen offset:1024 lds
Mv 44,36,4
Rt 142,21,51200
Mv 40,32,4
Mv 48,32,8
Rt 144,22,49152
Mv 52,36,8
Mv 112,40,8
Rt 146,22,51200
Mv 116,44,8
Mv 176,48,8
Rt 148,23,49152
Mv 180,52,8
buffer_load_dwordx4 v14, s[8:11], s39 offen offset:2048 lds
Mv 240,56,8
)" R"(
Rt 150,23,51200
Mv 244,60,8
Mv 252,60,12
Rt 152,24,49152
Mv 248,56,12
Mv 188,52,12
Rt 154,24,51200
Mv 184,48,12
.LzH9:
s_cbranch_execz .LzH14
Mv 124,44,12
Rt 156,25,49152
Mv 120,40,12
buffer_load_dwordx4 v15, s[8:11], s39 offen offset:3072 lds
Mv 60,36,12
Rt 158,25,51200
Mv 56,32,12
Mv 64,32,16
Rb 160,17,32768
Mv 68,36,16
Mv 128,40,16
Rb 164,17,34816
Mv 132,44,16
Mv 192,48,16
Rb 168,17,40960
Mv 196,52,16
buffer_store_dwordx4 a[224:227], v1, s[20:23], s72 offen sc0 sc1
Ma 192,56,16
Rb 172,17,43008
s_add_u32 s72, s72, 1024
Ma 196,60,16
Ma 204,60,20
Rb 176,17,49152
Ma 200,56,20
Mv 204,52,20
Rb 180,17,51200
Mv 200,48,20
Mv 140,44,20
Rb 184,17,57344
Mv 136,40,20
Mv 76,36,20
Rb 188,17,59392
Mv 72,32,20
Mv 80,32,24
Mv 84,36,24
buffer_store_dwordx4 a[224:227], v1, s[20:23], s88 offen sc0 sc1
Mv 144,40,24
s_add_u32 s88, s88, 1024
Mv 148,44,24
Mv 208,48,24
Mv 212,52,24
Ma 208,56,24
Ma 212,60,24
Ma 220,60,28
Ma 216,56,28
Mv 220,52,28
Mv 216,48,28
Mv 156,44,28
Mv 152,40,28
Mv 92,36,28
s_cmp_eq_u32 s40, 2
s_cmov_b32 s72, 0x40000000
s_cmov_b32 s88, 0x40000000
Mv 88,32,28
)" R"(
s_waitcnt vmcnt(12) lgkmcnt(0)
s_barrier
Mv 32,160,128
Rt 0,18,0
Mv 36,164,128
s_add_u32 s38, s38, 0xffff0080
Mv 96,168,128
Rt 2,18,2048
s_add_u32 m0, s36, 32768
Mv 100,172,128
buffer_load_dwordx4 v8, s[4:7], s38 offen offset:0 lds
Mv 160,176,128
Rt 4,19,0
Mv 164,180,128
Mv 224,184,128
Rt 6,19,2048
Mv 228,188,128
buffer_load_dwordx4 v9, s[4:7], s38 offen offset:1024 lds
Mv 236,188,132
Rt 8,20,0
Mv 232,184,132
Mv 172,180,132
Rt 10,20,2048
Mv 168,176,132
buffer_load_dwordx4 v10, s[4:7], s38 offen offset:2048 lds
Mv 108,172,132
Rt 12,21,0
Mv 104,168,132
Mv 44,164,132
Rt 14,21,2048
Mv 40,160,132
buffer_load_dwordx4 v11, s[4:7], s38 offen offset:3072 lds
Mv 48,160,136
Rt 16,22,0
Mv 52,164,136
s_add_u32 s38, s38, 0x10000
Mv 112,168,136
Rt 18,22,2048
s_add_u32 m0, s36, 36864
Mv 116,172,136
buffer_load_dwordx4 v8, s[4:7], s38 offen offset:0 lds
Mv 176,176,136
Rt 20,23,0
Mv 180,180,136
Mv 240,184,136
Rt 22,23,2048
Mv 244,188,136
buffer_load_dwordx4 v9, s[4:7], s38 offen offset:1024 lds
Mv 252,188,140
Rt 24,24,0
Mv 248,184,140
Mv 188,180,140
Rt 26,24,2048
Mv 184,176,140
buffer_load_dwordx4 v10, s[4:7], s38 offen offset:2048 lds
Mv 124,172,140
Rt 28,25,0
Mv 120,168,140
Mv 60,164,140
Rt 30,25,2048
Mv 56,160,140
buffer_load_dwordx4 v11, s[4:7], s38 offen offset:3072 lds
Mv 64,160,144
Rb 32,16,0
Mv 68,164,144
)" R"(
Mv 128,168,144
Rb 36,16,2048
Mv 132,172,144
s_add_u32 s39, s39, 0x40000
Mv 192,176,144
Rb 40,16,8192
s_add_u32 m0, s37, 49152
Mv 196,180,144
buffer_load_dwordx4 v12, s[8:11], s39 offen offset:0 lds
Ma 192,184,144
Rb 44,16,10240
Ma 196,188,144
Ma 204,188,148
Rb 48,16,16384
Ma 200,184,148
Mv 204,180,148
Rb 52,16,18432
Mv 200,176,148
buffer_load_dwordx4 v13, s[8:11], s39 offen offset:1024 lds
Mv 140,172,148
Rb 56,16,24576
Mv 136,168,148
Mv 76,164,148
Rb 60,16,26624
Mv 72,160,148
Mv 80,160,152
Mv 84,164,152
buffer_load_dwordx4 v14, s[8:11], s39 offen offset:2048 lds
Mv 144,168,152
Mv 148,172,152
Mv 208,176,152
Mv 212,180,152
Ma 208,184,152
Ma 212,188,152
buffer_load_dwordx4 v15, s[8:11], s39 offen offset:3072 lds
Ma 220,188,156
Ma 216,184,156
s_add_u32 s43, s43, 0x60000
Mv 220,180,156
s_sub_u32 s40, s40, 1
Mv 216,176,156
buffer_load_dword v27, v26, s[16:19], s43 offen
Mv 156,172,156
Mv 152,168,156
Mv 92,164,156
Mv 88,160,156
s_cmp_lg_u32 s40, 0
s_waitcnt vmcnt(21) lgkmcnt(0)
s_barrier
s_cbranch_scc1 .LzPBV
.LzPBVX:
Mv 32,32,0
Rt 128,18,16384
Mv 36,36,0
buffer_load_dword v4, v5, s[20:23], s50 offen offset:0 sc0 sc1
Mv 96,40,0
Rt 130,18,18432
Mv 100,44,0
Mv 160,48,0
Rt 132,19,16384
Mv 164,52,0
Mv 224,56,0
Rt 134,19,18432
Mv 228,60,0
Mv 236,60,4
)" R"(
Rt 136,20,16384
Mv 232,56,4
Mv 172,52,4
Rt 138,20,18432
Mv 168,48,4
Mv 108,44,4
Rt 140,21,16384
Mv 104,40,4
Mv 44,36,4
Rt 142,21,18432
Mv 40,32,4
Mv 48,32,8
Rt 144,22,16384
Mv 52,36,8
Mv 112,40,8
Rt 146,22,18432
Mv 116,44,8
Mv 176,48,8
Rt 148,23,16384
Mv 180,52,8
Mv 240,56,8
Rt 150,23,18432
Mv 244,60,8
Mv 252,60,12
Rt 152,24,16384
Mv 248,56,12
Mv 188,52,12
Rt 154,24,18432
Mv 184,48,12
Mv 124,44,12
Rt 156,25,16384
Mv 120,40,12
Mv 60,36,12
Rt 158,25,18432
Mv 56,32,12
Mv 64,32,16
Rb 160,17,0
Mv 68,36,16
Mv 128,40,16
Rb 164,17,2048
Mv 132,44,16
Mv 192,48,16
Rb 168,17,8192
Mv 196,52,16
Ma 192,56,16
Rb 172,17,10240
Ma 196,60,16
s_waitcnt vmcnt(0)
v_cmp_eq_u32_e32 vcc, s3, v4
s_andn2_b64 s[28:29], -1, vcc
s_cbranch_scc1 .Lzsp17
.Lzok17:
Ma 204,60,20
Rb 176,17,16384
Ma 200,56,20
s_lshl3_add_u32 s29, s34, s35
s_lshl2_add_u32 s29, s29, s33
Mv 204,52,20
Rb 180,17,18432
s_add_u32 m0, s36, 131072
Mv 200,48,20
buffer_load_dwordx4 v2, s[20:23], s56 offen offset:0 sc1 lds
Mv 140,44,20
Rb 184,17,24576
)" R"(
s_mul_i32 s83, s29, 7168
Mv 136,40,20
s_mov_b32 s82, s89
s_add_u32 s82, s82, 0xc00000
Mv 76,36,20
Rb 188,17,26624
s_add_u32 s82, s82, s84
Mv 72,32,20
s_mov_b32 s24, s80
s_mov_b32 s25, s81
Mv 80,32,24
s_mov_b32 s26, 0x380000
s_mov_b32 s27, 0x20000
Mv 84,36,24
buffer_load_dwordx4 v2, s[20:23], s56 offen offset:1024 sc1 lds
Mv 144,40,24
s_add_u32 s28, s33, 0
s_lshl2_add_u32 s28, s35, s28
Mv 148,44,24
s_mul_i32 s29, s28, 293
s_lshr_b32 s29, s29, 11
Mv 208,48,24
s_mul_i32 s29, s29, 9
s_add_u32 s29, s29, s28
Mv 212,52,24
s_lshl_b32 s29, s29, 10
s_add_u32 s4, s29, s82
Ma 208,56,24
s_add_u32 s28, s33, 4
s_lshl3_add_u32 s28, s28, s35
Ma 212,60,24
buffer_load_dwordx4 v2, s[20:23], s56 offen offset:2048 sc1 lds
Ma 220,60,28
s_mul_i32 s29, s28, 293
s_lshr_b32 s29, s29, 11
Ma 216,56,28
s_mul_i32 s29, s29, 9
s_add_u32 s29, s29, s28
Mv 220,52,28
s_lshl_b32 s29, s29, 10
s_add_u32 s5, s29, s82
Mv 216,48,28
s_add_u32 s28, s33, 8
s_lshl3_add_u32 s28, s28, s35
Mv 156,44,28
s_mul_i32 s29, s28, 293
s_lshr_b32 s29, s29, 11
Mv 152,40,28
buffer_load_dwordx4 v2, s[20:23], s56 offen offset:3072 sc1 lds
Mv 92,36,28
s_mul_i32 s29, s29, 9
s_add_u32 s29, s29, s28
Mv 88,32,28
s_waitcnt lgkmcnt(0)
s_barrier
Mv 32,160,128
Rt 0,18,32768
s_lshl_b32 s29, s29, 10
Mv 36,164,128
s_add_u32 s6, s29, s82
s_add_u32 s28, s33, 12
Mv 96,168,128
Rt 2,18,34816
s_add_u32 m0, s36, 0
Mv 100,172,128
)" R"(
buffer_load_dwordx4 v2, s[20:23], s55 offen offset:0 sc1 lds
Mv 160,176,128
Rt 4,19,32768
s_lshl3_add_u32 s28, s28, s35
Mv 164,180,128
s_mul_i32 s29, s28, 293
s_lshr_b32 s29, s29, 11
Mv 224,184,128
Rt 6,19,34816
s_mul_i32 s29, s29, 9
Mv 228,188,128
s_add_u32 s29, s29, s28
s_lshl_b32 s29, s29, 10
Mv 236,188,132
Rt 8,20,32768
buffer_load_dwordx4 v2, s[20:23], s55 offen offset:1024 sc1 lds
Mv 232,184,132
s_add_u32 s7, s29, s82
Mv 172,180,132
Rt 10,20,34816
Mv 168,176,132
s_cmp_gt_u32 s33, 1
s_cmov_b32 s7, 0x40000000
Mv 108,172,132
Rt 12,21,32768
s_add_u32 s28, s33, 14
Mv 104,168,132
buffer_load_dwordx4 v2, s[20:23], s55 offen offset:2048 sc1 lds
Mv 44,164,132
Rt 14,21,34816
s_lshl3_add_u32 s28, s28, s35
Mv 40,160,132
s_mul_i32 s29, s28, 293
s_lshr_b32 s29, s29, 11
Mv 48,160,136
Rt 16,22,32768
s_mul_i32 s29, s29, 9
Mv 52,164,136
s_add_u32 s29, s29, s28
s_lshl_b32 s29, s29, 10
Mv 112,168,136
Rt 18,22,34816
buffer_load_dwordx4 v2, s[20:23], s55 offen offset:3072 sc1 lds
Mv 116,172,136
s_add_u32 s8, s29, s82
s_add_u32 s28, s33, 18
Mv 176,176,136
Rt 20,23,32768
s_lshl3_add_u32 s28, s28, s35
Mv 180,180,136
s_mul_i32 s29, s28, 293
s_lshr_b32 s29, s29, 11
Mv 240,184,136
Rt 22,23,34816
s_add_u32 m0, s36, 4096
Mv 244,188,136
buffer_load_dwordx4 v3, s[20:23], s55 offen offset:0 sc1 lds
Mv 252,188,140
Rt 24,24,32768
s_mul_i32 s29, s29, 9
Mv 248,184,140
s_add_u32 s29, s29, s28
s_lshl_b32 s29, s29, 10
Mv 188,180,140
)" R"(
Rt 26,24,34816
s_add_u32 s9, s29, s82
Mv 184,176,140
s_add_u32 s28, s33, 22
s_lshl3_add_u32 s28, s28, s35
Mv 124,172,140
Rt 28,25,32768
buffer_load_dwordx4 v3, s[20:23], s55 offen offset:1024 sc1 lds
Mv 120,168,140
s_mul_i32 s29, s28, 293
s_lshr_b32 s29, s29, 11
Mv 60,164,140
Rt 30,25,34816
s_mul_i32 s29, s29, 9
Mv 56,160,140
s_add_u32 s29, s29, s28
s_lshl_b32 s29, s29, 10
Mv 64,160,144
Rb 32,16,32768
s_add_u32 s10, s29, s82
Mv 68,164,144
buffer_load_dwordx4 v3, s[20:23], s55 offen offset:2048 sc1 lds
Mv 128,168,144
Rb 36,16,34816
s_add_u32 s28, s33, 26
Mv 132,172,144
s_lshl3_add_u32 s28, s28, s35
s_mul_i32 s29, s28, 293
Mv 192,176,144
Rb 40,16,40960
s_lshr_b32 s29, s29, 11
Mv 196,180,144
s_mul_i32 s29, s29, 9
s_add_u32 s29, s29, s28
Ma 192,184,144
Rb 44,16,43008
buffer_load_dwordx4 v3, s[20:23], s55 offen offset:3072 sc1 lds
Ma 196,188,144
s_lshl_b32 s29, s29, 10
s_add_u32 s11, s29, s82
Ma 204,188,148
Rb 48,16,49152
Ma 200,184,148
s_cmp_gt_u32 s33, 1
s_cmov_b32 s11, 0x40000000
Mv 204,180,148
Rb 52,16,51200
s_add_u32 m0, s36, 135168
Mv 200,176,148
buffer_load_dwordx4 v3, s[20:23], s56 offen offset:0 sc1 lds
Mv 140,172,148
Rb 56,16,57344
s_and_b32 s28, s34, 1
Mv 136,168,148
s_lshl_b32 s28, s28, 14
s_add_u32 s82, s37, s28
Mv 76,164,148
Rb 60,16,59392
s_add_u32 s82, s82, 0x10000
Mv 72,160,148
Mv 80,160,152
buffer_load_dwordx4 v3, s[20:23], s56 offen offset:1024 sc1 lds
Mv 84,164,152
Mv 144,168,152
)" R"(
Mv 148,172,152
Mv 208,176,152
Mv 212,180,152
buffer_load_dwordx4 v3, s[20:23], s56 offen offset:2048 sc1 lds
Ma 208,184,152
Ma 212,188,152
Ma 220,188,156
Ma 216,184,156
Mv 220,180,156
buffer_load_dwordx4 v3, s[20:23], s56 offen offset:3072 sc1 lds
Mv 216,176,156
Mv 156,172,156
Mv 152,168,156
s_add_u32 s55, s55, 0x10000
s_add_u32 s56, s56, 0x10000
Mv 92,164,156
Mv 88,160,156
s_waitcnt lgkmcnt(4)
s_barrier
Mv 32,32,0
Rt 128,18,49152
Mv 36,36,0
Rt 130,18,51200
Mv 96,40,0
Rt 132,19,49152
s_add_u32 m0, s36, 65536
Mv 100,44,0
Rt 134,19,51200
buffer_load_dwordx4 v2, s[20:23], s56 offen offset:0 sc1 lds
Mv 108,44,4
Rt 136,20,49152
Mv 104,40,4
Rt 138,20,51200
s_add_u32 s50, s50, 1024
Mv 44,36,4
Rt 140,21,49152
s_min_u32 s50, s50, s60
Mv 40,32,4
Rt 142,21,51200
Mv 48,32,8
Rt 144,22,49152
Mv 52,36,8
Rt 146,22,51200
Mv 112,40,8
Rt 148,23,49152
Mv 116,44,8
Rt 150,23,51200
buffer_load_dwordx4 v2, s[20:23], s56 offen offset:1024 sc1 lds
Mv 124,44,12
Rt 152,24,49152
Mv 120,40,12
Rt 154,24,51200
Mv 60,36,12
Rt 156,25,49152
Mv 56,32,12
Rt 158,25,51200
Mv 64,32,16
Rb 160,17,32768
Mv 68,36,16
Rb 164,17,34816
Mv 128,40,16
Rb 168,17,40960
Mv 132,44,16
Rb 172,17,43008
buffer_load_dwordx4 v2, s[20:23], s56 offen offset:2048 sc1 lds
)" R"(
Mv 140,44,20
Rb 176,17,49152
Mv 136,40,20
Rb 180,17,51200
Mv 76,36,20
Rb 184,17,57344
Mv 72,32,20
Rb 188,17,59392
Mv 80,32,24
v_add_u32 v8, 0x10000, v1
v_add_u32 v10, 0x20000, v1
Mv 84,36,24
buffer_load_dword v4, v5, s[20:23], s50 offen offset:0 sc0 sc1
Mv 144,40,24
s_mov_b32 s93, 2
s_mov_b32 s94, 0
Mv 148,44,24
buffer_load_dwordx4 v2, s[20:23], s56 offen offset:3072 sc1 lds
Mv 156,44,28
Mv 152,40,28
Mv 92,36,28
Mv 88,32,28
s_waitcnt lgkmcnt(0)
s_barrier
Mv 32,160,128
Mv 36,164,128
Mv 40,160,132
s_add_u32 m0, s36, 32768
Mv 44,164,132
buffer_load_dwordx4 v2, s[20:23], s55 offen offset:0 sc1 lds
Mv 48,160,136
Mv 52,164,136
s_nop 0
Cv 32,32,33
Cv 33,34,35
Mv 56,160,140
Cv 34,36,37
Cv 35,38,39
Mv 60,164,140
Cv 40,40,41
Cv 41,42,43
Mv 64,160,144
Cv 42,44,45
Cv 43,46,47
Mv 68,164,144
Cv 48,48,49
Cv 49,50,51
Mv 72,160,148
Cv 50,52,53
Cv 51,54,55
Mv 76,164,148
buffer_load_dwordx4 v2, s[20:23], s55 offen offset:1024 sc1 lds
Mv 80,160,152
Cv 56,56,57
Cv 57,58,59
.LzH10:
s_cbranch_execz .LzH20
Mv 84,164,152
Cv 58,60,61
Cv 59,62,63
Mv 88,160,156
Cv 64,64,65
Cv 65,66,67
Mv 92,164,156
Cv 66,68,69
)" R"(
Cv 67,70,71
Mv 96,168,128
Cv 72,72,73
Cv 73,74,75
Mv 100,172,128
Cv 74,76,77
Cv 75,78,79
Mv 104,168,132
Cv 80,80,81
Cv 81,82,83
Mv 108,172,132
buffer_load_dwordx4 v2, s[20:23], s55 offen offset:2048 sc1 lds
Mv 112,168,136
Cv 82,84,85
Cv 83,86,87
Mv 116,172,136
Cv 88,88,89
Cv 89,90,91
Mv 120,168,140
Cv 90,92,93
Cv 91,94,95
Mv 124,172,140
buffer_store_dwordx4 v[32:35], v2, s[20:23], s45 offen offset:0 sc0 sc1
Mv 128,168,144
Mv 132,172,144
Mv 136,168,148
Mv 140,172,148
buffer_load_dwordx4 v2, s[20:23], s55 offen offset:3072 sc1 lds
Mv 144,168,152
Mv 148,172,152
s_nop 1
Cv 128,128,129
Cv 129,130,131
Mv 152,168,156
Cv 130,132,133
Cv 131,134,135
Mv 156,172,156
buffer_store_dwordx4 v[40:43], v2, s[20:23], s45 offen offset:1024 sc0 sc1
Mv 160,48,0
Cv 136,136,137
Cv 137,138,139
Mv 160,176,128
Cv 138,140,141
Cv 139,142,143
Mv 164,52,0
s_add_u32 m0, s36, 36864
Cv 144,144,145
Mv 164,180,128
buffer_load_dwordx4 v3, s[20:23], s55 offen offset:0 sc1 lds
Mv 168,48,4
Cv 145,146,147
Cv 146,148,149
Mv 168,176,132
Cv 147,150,151
Cv 152,152,153
Mv 172,52,4
Cv 153,154,155
Cv 154,156,157
Mv 172,180,132
buffer_store_dwordx4 v[48:51], v2, s[20:23], s45 offen offset:2048 sc0 sc1
Mv 176,48,8
Cv 155,158,159
Cv 132,96,97
Mv 176,176,136
)" R"(
Cv 133,98,99
Cv 134,100,101
Mv 180,52,8
Cv 135,102,103
Cv 140,104,105
Mv 180,180,136
buffer_load_dwordx4 v3, s[20:23], s55 offen offset:1024 sc1 lds
Mv 184,48,12
Cv 141,106,107
Cv 142,108,109
Mv 184,176,140
Cv 143,110,111
Cv 148,112,113
Mv 188,52,12
Cv 149,114,115
Cv 150,116,117
Mv 188,180,140
buffer_store_dwordx4 v[56:59], v2, s[20:23], s45 offen offset:3072 sc0 sc1
Mv 192,48,16
Cv 151,118,119
Cv 156,120,121
Mv 192,176,144
Cv 157,122,123
Cv 158,124,125
Mv 196,52,16
Cv 159,126,127
Cv 160,160,161
Mv 196,180,144
buffer_load_dwordx4 v3, s[20:23], s55 offen offset:2048 sc1 lds
Mv 200,48,20
Cv 161,162,163
Cv 162,164,165
Mv 200,176,148
Cv 163,166,167
v_accvgpr_write_b32 a224, v160
Mv 204,52,20
v_accvgpr_write_b32 a225, v161
v_accvgpr_write_b32 a226, v162
Mv 204,180,148
buffer_store_dwordx4 v[64:67], v3, s[20:23], s45 offen offset:0 sc0 sc1
Mv 208,48,24
v_accvgpr_write_b32 a227, v163
Cv 168,168,169
Mv 208,176,152
Cv 169,170,171
Cv 170,172,173
Mv 212,52,24
Cv 171,174,175
v_accvgpr_write_b32 a228, v168
Mv 212,180,152
buffer_load_dwordx4 v3, s[20:23], s55 offen offset:3072 sc1 lds
Mv 216,48,28
v_accvgpr_write_b32 a229, v169
v_accvgpr_write_b32 a230, v170
Mv 216,176,156
v_accvgpr_write_b32 a231, v171
Cv 176,176,177
Mv 220,52,28
Cv 177,178,179
Cv 178,180,181
Mv 220,180,156
buffer_store_dwordx4 v[72:75], v3, s[20:23], s45 offen offset:1024 sc0 sc1
Mv 224,56,0
Cv 179,182,183
)" R"(
v_accvgpr_write_b32 a232, v176
Mv 228,60,0
v_accvgpr_write_b32 a233, v177
v_accvgpr_write_b32 a234, v178
Mv 236,60,4
s_add_u32 m0, s36, 69632
v_accvgpr_write_b32 a235, v179
Mv 232,56,4
buffer_load_dwordx4 v3, s[20:23], s56 offen offset:0 sc1 lds
Mv 240,56,8
Cv 184,184,185
Cv 185,186,187
Mv 244,60,8
Cv 186,188,189
Cv 187,190,191
Mv 252,60,12
v_accvgpr_write_b32 a236, v184
v_accvgpr_write_b32 a237, v185
Mv 248,56,12
s_waitcnt vmcnt(16)
s_barrier
Ma 192,56,16
v_cmp_eq_u32_e32 vcc, s3, v4
s_andn2_b64 s[28:29], -1, vcc
s_cbranch_scc1 .Lzsp18
.Lzok18:
Ma 196,60,16
buffer_store_dwordx4 v[80:83], v3, s[20:23], s45 offen offset:2048 sc0 sc1
Ma 204,60,20
v_accvgpr_write_b32 a238, v186
v_accvgpr_write_b32 a239, v187
Ma 200,56,20
buffer_load_dwordx4 v3, s[20:23], s56 offen offset:1024 sc1 lds
Ma 208,56,24
Cv 192,192,193
Cv 193,194,195
Ma 212,60,24
Cv 194,196,197
Cv 195,198,199
Ma 220,60,28
v_accvgpr_write_b32 a240, v192
v_accvgpr_write_b32 a241, v193
Ma 216,56,28
buffer_store_dwordx4 v[88:91], v3, s[20:23], s45 offen offset:3072 sc0 sc1
Mv 224,184,128
Rb 0,1,0
v_accvgpr_write_b32 a242, v194
Mv 228,188,128
Rb 4,1,1024
v_accvgpr_write_b32 a243, v195
Mv 232,184,132
Rb 8,1,2048
Cv 200,200,201
Mv 236,188,132
buffer_load_dwordx4 v3, s[20:23], s56 offen offset:2048 sc1 lds
Rb 12,1,3072
Mv 240,184,136
Rb 16,1,8192
Cv 201,202,203
Mv 244,188,136
Rb 20,1,9216
Cv 202,204,205
Mv 248,184,140
Rb 24,1,10240
Cv 203,206,207
)" R"(
Mv 252,188,140
Rb 28,1,11264
v_accvgpr_write_b32 a244, v200
Ma 192,184,144
Rb 32,1,4096
v_accvgpr_write_b32 a245, v201
Ma 196,188,144
Rb 36,1,5120
v_accvgpr_write_b32 a246, v202
Ma 200,184,148
Rb 40,1,6144
v_accvgpr_write_b32 a247, v203
Ma 204,188,148
buffer_load_dwordx4 v3, s[20:23], s56 offen offset:3072 sc1 lds
Rb 44,1,7168
Ma 208,184,152
Ma 212,188,152
Ma 216,184,156
Ma 220,188,156
s_add_u32 s56, s56, 0x10000
.LzH11:
s_cbranch_execz .LzH15
.LzFB:
s_waitcnt lgkmcnt(8)
Mz 64,0,64
Rb 48,1,12288
s_add_u32 s55, s55, 0x10000
Mz 68,0,68
s_cmp_eq_u32 s57, 2
s_cselect_b32 s55, s59, s55
Mv 68,4,76
Rb 52,1,13312
s_sub_u32 s57, s57, 1
v_mov_b32 v16, 0
Mv 64,4,72
buffer_store_dwordx4 v[132:135], v2, s[20:23], s46 offen offset:0 sc0 sc1
Mv 64,8,80
Rb 56,1,14336
Cv 208,208,209
Cv 209,210,211
Mv 68,8,84
Cv 210,212,213
Cv 211,214,215
Mv 68,12,92
Rb 60,1,15360
v_accvgpr_write_b32 a248, v208
s_waitcnt lgkmcnt(8)
Mv 64,12,88
v_accvgpr_write_b32 a249, v209
v_accvgpr_write_b32 a250, v210
Mv 64,16,96
Rb 0,1,16384
v_accvgpr_write_b32 a251, v211
Mv 68,16,100
Cv 216,216,217
Cv 217,218,219
Cv 218,220,221
Mv 68,20,108
Rb 4,1,17408
Cv 219,222,223
Mv 64,20,104
v_accvgpr_write_b32 a252, v216
v_accvgpr_write_b32 a253, v217
v_accvgpr_write_b32 a254, v218
)" R"(
Mv 64,24,112
Rb 8,1,18432
v_accvgpr_write_b32 a255, v219
Mv 68,24,116
buffer_store_dwordx4 v[140:143], v2, s[20:23], s46 offen offset:1024 sc0 sc1
Cv 224,224,225
Mv 68,28,124
Rb 12,1,19456
s_waitcnt lgkmcnt(8)
Mv 64,28,120
Cv 225,226,227
Cv 226,228,229
Cv 227,230,231
Mz 72,32,64
Rb 16,1,24576
v_accvgpr_write_b32 a128, v224
Mz 76,32,68
v_accvgpr_write_b32 a129, v225
v_accvgpr_write_b32 a130, v226
Mv 76,36,76
Rb 20,1,25600
v_accvgpr_write_b32 a131, v227
Cv 232,232,233
Mv 72,36,72
Cv 233,234,235
Cv 234,236,237
Mv 72,40,80
Rb 24,1,26624
Cv 235,238,239
v_accvgpr_write_b32 a132, v232
Mv 76,40,84
v_accvgpr_write_b32 a133, v233
v_accvgpr_write_b32 a134, v234
Mv 76,44,92
Rb 28,1,27648
v_accvgpr_write_b32 a135, v235
s_waitcnt lgkmcnt(8)
Mv 72,44,88
buffer_store_dwordx4 v[148:151], v2, s[20:23], s46 offen offset:2048 sc0 sc1
Mv 72,48,96
Rb 32,1,20480
Cv 240,240,241
Mv 76,48,100
Cv 241,242,243
Cv 242,244,245
Cv 243,246,247
Mv 76,52,108
Rb 36,1,21504
v_accvgpr_write_b32 a136, v240
Mv 72,52,104
v_accvgpr_write_b32 a137, v241
v_accvgpr_write_b32 a138, v242
v_accvgpr_write_b32 a139, v243
Mv 72,56,112
Rb 40,1,22528
Cv 248,248,249
Mv 76,56,116
Cv 249,250,251
Cv 250,252,253
Cv 251,254,255
Mv 76,60,124
Rb 44,1,23552
s_waitcnt lgkmcnt(8)
Mv 72,60,120
)" R"(
v_accvgpr_write_b32 a140, v248
v_accvgpr_write_b32 a141, v249
v_accvgpr_write_b32 a142, v250
Mz 80,0,64
Rb 48,1,28672
v_accvgpr_write_b32 a143, v251
Mz 84,0,68
buffer_store_dwordx4 v[156:159], v2, s[20:23], s46 offen offset:3072 sc0 sc1
Mv 84,4,76
Rb 52,1,29696
v_mov_b32 v17, 0
v_mov_b32 v18, 0
Mv 80,4,72
v_mov_b32 v19, 0
v_mov_b32 v20, 0
Mv 80,8,80
Rb 56,1,30720
v_exp_f32_e32 v32, v64
Mv 84,8,84
v_exp_f32_e32 v24, v65
Mv 84,12,92
Rb 60,1,31744
v_mov_b32 v21, 0
s_waitcnt lgkmcnt(8)
Mv 80,12,88
v_exp_f32_e32 v33, v66
Mv 80,16,96
v_exp_f32_e32 v25, v67
Mv 84,16,100
v_exp_f32_e32 v34, v72
v_mov_b32 v22, 0
Mv 84,20,108
v_exp_f32_e32 v26, v73
Mv 80,20,104
buffer_store_dwordx4 v[128:131], v3, s[20:23], s46 offen offset:0 sc0 sc1
v_mov_b32 v23, 0
Mv 80,24,112
s_add_u32 s50, s50, 1024
s_min_u32 s50, s50, s60
Mv 84,24,116
v_exp_f32_e32 v35, v74
v_add_f32_e32 v14, v32, v24
Mv 84,28,124
v_exp_f32_e32 v27, v75
Mv 80,28,120
s_waitcnt vmcnt(7) lgkmcnt(0)
s_barrier
buffer_store_dword v6, v7, s[20:23], s49 offen offset:0 sc0 sc1
Mz 88,32,64
Rb 0,1,32768
Cv 32,32,24
Mz 92,32,68
Rb 4,1,33792
v_add_f32_e32 v14, v14, v33
Mv 92,36,76
Rb 8,1,34816
s_add_u32 m0, s36, 0
v_add_f32_e32 v14, v14, v25
Mv 88,36,72
Rb 12,1,35840
buffer_load_dwordx4 v2, s[20:23], s55 offen offset:0 sc1 lds
Mv 88,40,80
Rb 16,1,40960
v_exp_f32_e32 v36, v68
Mv 92,40,84
)" R"(
Rb 20,1,41984
Cv 33,33,25
Mv 92,44,92
Rb 24,1,43008
v_exp_f32_e32 v28, v69
Mv 88,44,88
Rb 28,1,44032
v_add_f32_e32 v14, v14, v34
Mv 88,48,96
Rb 32,1,36864
v_add_f32_e32 v14, v14, v26
Mv 92,48,100
Rb 36,1,37888
v_exp_f32_e32 v37, v70
Mv 92,52,108
Rb 40,1,38912
Cv 34,34,26
Mv 88,52,104
Rb 44,1,39936
buffer_load_dwordx4 v2, s[20:23], s55 offen offset:1024 sc1 lds
Mv 88,56,112
v_exp_f32_e32 v29, v71
Mv 92,56,116
v_exp_f32_e32 v38, v76
v_add_f32_e32 v14, v14, v35
Mv 92,60,124
v_add_f32_e32 v14, v14, v27
s_waitcnt lgkmcnt(8)
Mv 88,60,120
v_exp_f32_e32 v24, v77
Cv 35,35,27
Mz 96,0,64
Rb 48,1,45056
buffer_load_dword v4, v5, s[20:23], s50 offen offset:0 sc0 sc1
Mz 100,0,68
v_exp_f32_e32 v39, v78
Mv 100,4,76
Rb 52,1,46080
v_exp_f32_e32 v25, v79
Mv 96,4,72
buffer_load_dwordx4 v2, s[20:23], s55 offen offset:2048 sc1 lds
Mv 96,8,80
Rb 56,1,47104
v_exp_f32_e32 v40, v80
Mv 100,8,84
v_exp_f32_e32 v26, v81
Mv 100,12,92
Rb 60,1,48128
v_add_f32_e32 v15, v36, v28
s_waitcnt lgkmcnt(8)
Mv 96,12,88
buffer_store_dwordx4 v[136:139], v3, s[20:23], s46 offen offset:1024 sc0 sc1
Mv 96,16,96
Rb 0,1,49152
Cv 36,36,28
Mv 100,16,100
v_exp_f32_e32 v41, v82
v_add_f32_e32 v15, v15, v37
Mv 100,20,108
Rb 4,1,50176
v_add_f32_e32 v15, v15, v29
Mv 96,20,104
buffer_load_dwordx4 v2, s[20:23], s55 offen offset:3072 sc1 lds
Cv 37,37,29
)" R"(
Mv 96,24,112
Rb 8,1,51200
v_add_f32_e32 v15, v15, v38
Mv 100,24,116
v_exp_f32_e32 v27, v83
v_add_f32_e32 v15, v15, v24
Mv 100,28,124
Rb 12,1,52224
s_waitcnt lgkmcnt(8)
Mv 96,28,120
v_exp_f32_e32 v42, v88
Cv 38,38,24
Mz 104,32,64
Rb 16,1,57344
v_add_f32_e32 v15, v15, v39
Mz 108,32,68
v_exp_f32_e32 v28, v89
Mv 108,36,76
Rb 20,1,58368
s_add_u32 m0, s36, 4096
v_add_f32_e32 v15, v15, v25
Mv 104,36,72
buffer_load_dwordx4 v3, s[20:23], s55 offen offset:0 sc1 lds
Mv 104,40,80
Rb 24,1,59392
v_exp_f32_e32 v43, v90
Mv 108,40,84
v_exp_f32_e32 v29, v91
Mv 108,44,92
Rb 28,1,60416
Cv 39,39,25
s_waitcnt lgkmcnt(8)
Mv 104,44,88
buffer_store_dwordx4 v[144:147], v3, s[20:23], s46 offen offset:2048 sc0 sc1
Mv 104,48,96
Rb 32,1,53248
v_add_f32_e32 v14, v14, v40
Mv 108,48,100
v_exp_f32_e32 v44, v84
v_add_f32_e32 v14, v14, v26
Mv 108,52,108
Rb 36,1,54272
Cv 40,40,26
Mv 104,52,104
buffer_load_dwordx4 v3, s[20:23], s55 offen offset:1024 sc1 lds
v_add_f32_e32 v14, v14, v41
Mv 104,56,112
Rb 40,1,55296
v_add_f32_e32 v14, v14, v27
Mv 108,56,116
v_exp_f32_e32 v24, v85
Cv 41,41,27
Mv 108,60,124
Rb 44,1,56320
s_waitcnt lgkmcnt(8)
Mv 104,60,120
v_exp_f32_e32 v45, v86
v_add_f32_e32 v14, v14, v42
Mz 112,0,64
Rb 48,1,61440
v_add_f32_e32 v14, v14, v28
Mz 116,0,68
v_exp_f32_e32 v25, v87
Mv 116,4,76
)" R"(
Rb 52,1,62464
v_exp_f32_e32 v46, v92
Mv 112,4,72
buffer_load_dwordx4 v3, s[20:23], s55 offen offset:2048 sc1 lds
Mv 112,8,80
Rb 56,1,63488
v_exp_f32_e32 v26, v93
Mv 116,8,84
v_exp_f32_e32 v47, v94
Mv 116,12,92
Rb 60,1,64512
Cv 42,42,28
s_waitcnt lgkmcnt(8)
Mv 112,12,88
buffer_store_dwordx4 v[152:155], v3, s[20:23], s46 offen offset:3072 sc0 sc1
Mv 112,16,96
v_exp_f32_e32 v27, v95
Mv 116,16,100
v_add_f32_e32 v14, v14, v43
v_add_f32_e32 v14, v14, v29
Cv 43,43,29
Mv 116,20,108
v_add_f32_e32 v15, v15, v44
v_add_f32_e32 v15, v15, v24
Mv 112,20,104
buffer_load_dwordx4 v3, s[20:23], s55 offen offset:3072 sc1 lds
Cv 44,44,24
Mv 112,24,112
v_add_f32_e32 v15, v15, v45
v_add_f32_e32 v15, v15, v25
Mv 116,24,116
Cv 45,45,25
v_add_f32_e32 v15, v15, v46
v_add_f32_e32 v15, v15, v26
Mv 116,28,124
Cv 46,46,26
v_add_f32_e32 v15, v15, v47
Mv 112,28,120
v_add_f32_e32 v15, v15, v27
s_waitcnt lgkmcnt(0)
s_barrier
.LzH12:
s_cbranch_execz .LzH16
Mz 120,32,64
Rb 0,10,0
Cv 47,47,27
Mz 124,32,68
Rb 4,10,1024
v_max_f32_e32 v11, v14, v15
Mv 124,36,76
Rb 8,10,2048
v_mov_b32 v24, v14
v_mov_b32 v25, v15
Mv 120,36,72
Rb 12,10,3072
v_permlane32_swap_b32_e32 v24, v25
Mv 120,40,80
Rb 16,10,4096
v_add_f32_e32 v24, v24, v25
v_mov_b32 v25, v24
Mv 124,40,84
Rb 20,10,5120
v_permlane16_swap_b32_e32 v24, v25
Mv 124,44,92
Rb 24,10,6144
)" R"(
v_add_f32_e32 v24, v24, v25
s_add_u32 s55, s55, 0x10000
Mv 120,44,88
Rb 28,10,7168
Mv 120,48,96
Rb 32,10,8192
Mv 124,48,100
Rb 36,10,9216
v_rcp_f32_e32 v24, v24
Mv 124,52,108
Rb 40,10,10240
v_max_f32_e32 v11, v11, v24
Mv 120,52,104
Rb 44,10,11264
s_cmp_eq_u32 s57, 2
s_cselect_b32 s55, s59, s55
Mv 120,56,112
v_cmp_nge_f32_e32 vcc, s62, v11
Mv 124,56,116
Mv 124,60,124
s_waitcnt lgkmcnt(8)
s_cbranch_vccnz .Lzfx6
.Lzfr6:
Mv 120,60,120
v_mov_b32 v12, v14
v_mov_b32 v13, v15
v_mfma_f32_16x16x32_bf16 v[128:131], a[0:3], v[32:35], 0
Rb 48,10,12288
v_mfma_f32_16x16x32_bf16 v[136:139], a[0:3], v[36:39], 0
v_mfma_f32_16x16x32_bf16 v[140:143], a[4:7], v[36:39], 0
Rb 52,10,13312
s_add_u32 m0, s36, 32768
v_mfma_f32_16x16x32_bf16 v[132:135], a[4:7], v[32:35], 0
buffer_load_dwordx4 v2, s[20:23], s55 offen offset:0 sc1 lds
v_mfma_f32_16x16x32_bf16 v[144:147], a[8:11], v[32:35], 0
Rb 56,10,14336
v_mfma_f32_16x16x32_bf16 v[152:155], a[8:11], v[36:39], 0
v_exp_f32_e32 v48, v96
v_mfma_f32_16x16x32_bf16 v[156:159], a[12:15], v[36:39], 0
Rb 60,10,15360
s_waitcnt lgkmcnt(8)
v_mfma_f32_16x16x32_bf16 v[148:151], a[12:15], v[32:35], 0
v_exp_f32_e32 v28, v97
v_mfma_f32_16x16x32_bf16 v[160:163], a[16:19], v[32:35], 0
Rb 0,10,16384
v_add_f32_e32 v14, v48, v28
v_mfma_f32_16x16x32_bf16 v[168:171], a[16:19], v[36:39], 0
v_exp_f32_e32 v49, v98
v_mfma_f32_16x16x32_bf16 v[172:175], a[20:23], v[36:39], 0
Rb 4,10,17408
Cv 48,48,28
v_mfma_f32_16x16x32_bf16 v[164:167], a[20:23], v[32:35], 0
buffer_load_dwordx4 v2, s[20:23], s55 offen offset:1024 sc1 lds
v_mfma_f32_16x16x32_bf16 v[176:179], a[24:27], v[32:35], 0
Rb 8,10,18432
v_exp_f32_e32 v29, v99
v_mfma_f32_16x16x32_bf16 v[184:187], a[24:27], v[36:39], 0
v_exp_f32_e32 v50, v104
v_mfma_f32_16x16x32_bf16 v[188:191], a[28:31], v[36:39], 0
Rb 12,10,19456
s_waitcnt lgkmcnt(8)
v_mfma_f32_16x16x32_bf16 v[180:183], a[28:31], v[32:35], 0
v_exp_f32_e32 v24, v105
v_mfma_f32_16x16x32_bf16 v[192:195], a[32:35], v[32:35], 0
)" R"(
Rb 16,10,20480
v_add_f32_e32 v14, v14, v49
v_mfma_f32_16x16x32_bf16 v[200:203], a[32:35], v[36:39], 0
v_exp_f32_e32 v51, v106
v_add_f32_e32 v14, v14, v29
v_mfma_f32_16x16x32_bf16 v[204:207], a[36:39], v[36:39], 0
Rb 20,10,21504
Cv 49,49,29
v_mfma_f32_16x16x32_bf16 v[196:199], a[36:39], v[32:35], 0
buffer_load_dwordx4 v2, s[20:23], s55 offen offset:2048 sc1 lds
v_mfma_f32_16x16x32_bf16 v[208:211], a[40:43], v[32:35], 0
Rb 24,10,22528
v_add_f32_e32 v14, v14, v50
v_mfma_f32_16x16x32_bf16 v[216:219], a[40:43], v[36:39], 0
v_exp_f32_e32 v25, v107
v_mfma_f32_16x16x32_bf16 v[220:223], a[44:47], v[36:39], 0
Rb 28,10,23552
s_waitcnt lgkmcnt(8)
v_mfma_f32_16x16x32_bf16 v[212:215], a[44:47], v[32:35], 0
v_exp_f32_e32 v52, v100
v_add_f32_e32 v14, v14, v24
v_mfma_f32_16x16x32_bf16 v[224:227], a[48:51], v[32:35], 0
Rb 32,10,24576
Cv 50,50,24
v_mfma_f32_16x16x32_bf16 v[232:235], a[48:51], v[36:39], 0
v_exp_f32_e32 v26, v101
v_mfma_f32_16x16x32_bf16 v[236:239], a[52:55], v[36:39], 0
Rb 36,10,25600
v_add_f32_e32 v14, v14, v51
v_mfma_f32_16x16x32_bf16 v[228:231], a[52:55], v[32:35], 0
buffer_load_dwordx4 v2, s[20:23], s55 offen offset:3072 sc1 lds
v_mfma_f32_16x16x32_bf16 v[240:243], a[56:59], v[32:35], 0
Rb 40,10,26624
v_exp_f32_e32 v53, v102
v_mfma_f32_16x16x32_bf16 v[248:251], a[56:59], v[36:39], 0
v_exp_f32_e32 v27, v103
v_mfma_f32_16x16x32_bf16 v[252:255], a[60:63], v[36:39], 0
Rb 44,10,27648
s_waitcnt lgkmcnt(8)
v_mfma_f32_16x16x32_bf16 v[244:247], a[60:63], v[32:35], 0
v_exp_f32_e32 v54, v108
Mp 128,0,40
Rb 48,10,28672
v_add_f32_e32 v14, v14, v25
Mp 136,0,44
v_exp_f32_e32 v28, v109
Cv 51,51,25
Mp 140,4,44
Rb 52,10,29696
s_add_u32 m0, s36, 36864
Mp 132,4,40
buffer_load_dwordx4 v3, s[20:23], s55 offen offset:0 sc1 lds
Mp 144,8,40
Rb 56,10,30720
v_add_f32_e32 v15, v52, v26
Mp 152,8,44
v_exp_f32_e32 v55, v110
Mp 156,12,44
Rb 60,10,31744
s_waitcnt lgkmcnt(8)
Mp 148,12,40
v_exp_f32_e32 v29, v111
Cv 52,52,26
Mp 160,16,40
)" R"(
v_add_f32_e32 v15, v15, v53
v_add_f32_e32 v15, v15, v27
Mp 168,16,44
Cv 53,53,27
v_add_f32_e32 v15, v15, v54
Mp 172,20,44
v_add_f32_e32 v15, v15, v28
Cv 54,54,28
Mp 164,20,40
buffer_load_dwordx4 v3, s[20:23], s55 offen offset:1024 sc1 lds
Mp 176,24,40
v_add_f32_e32 v15, v15, v55
v_add_f32_e32 v15, v15, v29
Cv 55,55,29
Mp 184,24,44
s_sub_u32 s57, s57, 1
v_accvgpr_read_b32 v9, a192
Mp 188,28,44
v_accvgpr_read_b32 v11, a193
Cv 9,9,11
Mp 180,28,40
s_waitcnt vmcnt(6) lgkmcnt(0)
s_barrier
buffer_store_dword v6, v7, s[20:23], s49 offen offset:1024 sc0 sc1
Mp 192,32,40
Rb 0,1,0
v_accvgpr_write_b32 a144, v9
Mp 200,32,44
Rb 4,1,1024
v_accvgpr_read_b32 v9, a194
v_accvgpr_read_b32 v11, a195
Mp 204,36,44
Rb 8,1,2048
Cv 9,9,11
Mp 196,36,40
Rb 12,1,3072
buffer_load_dwordx4 v3, s[20:23], s55 offen offset:2048 sc1 lds
Mp 208,40,40
Rb 16,1,8192
v_accvgpr_write_b32 a145, v9
Mp 216,40,44
Rb 20,1,9216
v_accvgpr_read_b32 v9, a196
Mp 220,44,44
Rb 24,1,10240
v_accvgpr_read_b32 v11, a197
Mp 212,44,40
Rb 28,1,11264
Cv 9,9,11
v_accvgpr_write_b32 a146, v9
Mp 224,48,40
Rb 32,1,4096
v_accvgpr_read_b32 v9, a198
Mp 232,48,44
Rb 36,1,5120
v_accvgpr_read_b32 v11, a199
Mp 236,52,44
Rb 40,1,6144
Cv 9,9,11
Mp 228,52,40
Rb 44,1,7168
buffer_load_dwordx4 v3, s[20:23], s55 offen offset:3072 sc1 lds
Mp 240,56,40
v_cmp_eq_u32_e32 vcc, s3, v4
s_andn2_b64 s[28:29], -1, vcc
)" R"(
s_cbranch_scc1 .Lzsp19
.Lzok19:
Mp 248,56,44
v_accvgpr_write_b32 a147, v9
v_accvgpr_read_b32 v9, a200
Mp 252,60,44
v_accvgpr_read_b32 v11, a201
s_waitcnt lgkmcnt(8)
Mp 244,60,40
Cv 9,9,11
v_accvgpr_write_b32 a148, v9
v_mfma_f32_16x16x32_bf16 v[64:67], a[0:3], a[64:67], v[16:19]
Rb 48,1,12288
v_add_u32 v9, 4, v0
v_mfma_f32_16x16x32_bf16 v[68:71], a[0:3], a[68:71], v[20:23]
v_xor_b32 v9, v9, v0
v_and_b32 v9, 32, v9
Mv 68,4,76
Rb 52,1,13312
s_add_u32 m0, s36, 98304
Mv 64,4,72
buffer_load_dwordx4 v2, s[20:23], s56 offen offset:0 sc1 lds
Mv 64,8,80
Rb 56,1,14336
v_lshlrev_b32 v9, 22, v9
Mv 68,8,84
v_and_or_b32 v9, v0, 32, v9
v_mul_lo_u32 v9, v9, 12
Mv 68,12,92
Rb 60,1,15360
s_waitcnt lgkmcnt(8)
Mv 64,12,88
buffer_load_dwordx4 v2, s[20:23], s56 offen offset:1024 sc1 lds
Mv 64,16,96
Rb 0,1,16384
v_add_u32 v9, v9, v1
Mv 68,16,100
v_exp_f32_e32 v56, v112
Mv 68,20,108
Rb 4,1,17408
v_exp_f32_e32 v24, v113
Mv 64,20,104
buffer_load_dwordx4 v2, s[20:23], s56 offen offset:2048 sc1 lds
Mv 64,24,112
Rb 8,1,18432
v_exp_f32_e32 v57, v114
Mv 68,24,116
v_exp_f32_e32 v25, v115
Mv 68,28,124
Rb 12,1,19456
v_add_f32_e32 v14, v14, v56
s_waitcnt lgkmcnt(8)
Mv 64,28,120
buffer_load_dwordx4 v2, s[20:23], s56 offen offset:3072 sc1 lds
v_add_f32_e32 v14, v14, v24
v_mfma_f32_16x16x32_bf16 v[72:75], a[32:35], a[64:67], v[16:19]
Rb 16,1,24576
Cv 56,56,24
v_mfma_f32_16x16x32_bf16 v[76:79], a[32:35], a[68:71], v[20:23]
v_exp_f32_e32 v58, v120
v_add_f32_e32 v14, v14, v57
Mv 76,36,76
Rb 20,1,25600
s_add_u32 m0, s36, 102400
)" R"(
Mv 72,36,72
buffer_load_dwordx4 v3, s[20:23], s56 offen offset:0 sc1 lds
v_add_f32_e32 v14, v14, v25
Mv 72,40,80
Rb 24,1,26624
Cv 57,57,25
Mv 76,40,84
v_exp_f32_e32 v26, v121
v_add_f32_e32 v14, v14, v58
Mv 76,44,92
Rb 28,1,27648
s_waitcnt lgkmcnt(8)
Mv 72,44,88
buffer_load_dwordx4 v3, s[20:23], s56 offen offset:1024 sc1 lds
v_add_f32_e32 v14, v14, v26
Mv 72,48,96
Rb 32,1,20480
v_exp_f32_e32 v59, v122
Mv 76,48,100
v_exp_f32_e32 v27, v123
Mv 76,52,108
Rb 36,1,21504
v_exp_f32_e32 v60, v116
Mv 72,52,104
buffer_load_dwordx4 v3, s[20:23], s56 offen offset:2048 sc1 lds
Mv 72,56,112
Rb 40,1,22528
v_exp_f32_e32 v28, v117
Mv 76,56,116
v_exp_f32_e32 v61, v118
Mv 76,60,124
Rb 44,1,23552
Cv 58,58,26
s_waitcnt lgkmcnt(8)
Mv 72,60,120
buffer_load_dwordx4 v3, s[20:23], s56 offen offset:3072 sc1 lds
v_add_f32_e32 v14, v14, v59
v_mfma_f32_16x16x32_bf16 v[80:83], a[0:3], a[64:67], v[16:19]
Rb 48,1,28672
s_xor_b32 m0, s82, 0
v_mfma_f32_16x16x32_bf16 v[84:87], a[0:3], a[68:71], v[20:23]
s_add_u32 s56, s56, 0x10000
s_add_u32 s92, s83, 448
v_add_f32_e32 v14, v14, v27
Mv 84,4,76
Rb 52,1,29696
Cv 59,59,27
Mv 80,4,72
buffer_load_dwordx4 v9, s[24:27], s92 offen offset:0 lds
v_add_f32_e32 v15, v15, v60
Mv 80,8,80
Rb 56,1,30720
s_add_u32 s92, s83, 1216
Mv 84,8,84
v_exp_f32_e32 v29, v119
v_add_f32_e32 v15, v15, v28
Mv 84,12,92
Rb 60,1,31744
s_waitcnt lgkmcnt(8)
Mv 80,12,88
buffer_load_dwordx4 v9, s[24:27], s92 offen offset:1024 lds
s_add_u32 s92, s83, 1984
Mv 80,16,96
v_exp_f32_e32 v62, v124
)" R"(
Cv 60,60,28
Mv 84,16,100
v_exp_f32_e32 v24, v125
Mv 84,20,108
v_exp_f32_e32 v63, v126
v_add_f32_e32 v15, v15, v61
Mv 80,20,104
buffer_load_dwordx4 v9, s[24:27], s92 offen offset:2048 lds
Mv 80,24,112
s_add_u32 s92, s83, 2752
v_exp_f32_e32 v25, v127
Mv 84,24,116
v_add_f32_e32 v15, v15, v29
Cv 61,61,29
Mv 84,28,124
buffer_load_dwordx4 v9, s[24:27], s92 offen offset:3072 lds
v_add_f32_e32 v15, v15, v62
Mv 80,28,120
s_xor_b32 m0, s82, 16384
s_waitcnt lgkmcnt(0)
s_barrier
v_mfma_f32_16x16x32_bf16 v[88:91], a[32:35], a[64:67], v[16:19]
Rb 0,8,0
s_add_u32 s92, s83, 0
v_mfma_f32_16x16x32_bf16 v[92:95], a[32:35], a[68:71], v[20:23]
Rb 4,8,1024
v_add_f32_e32 v15, v15, v24
Cv 62,62,24
Mv 92,36,76
Rb 8,8,2048
v_add_f32_e32 v15, v15, v63
Mv 88,36,72
Rb 12,8,3072
v_add_f32_e32 v15, v15, v25
Cv 63,63,25
Mv 88,40,80
Rb 16,8,4096
buffer_load_dwordx4 v9, s[24:27], s92 offen offset:0 lds
Mv 92,40,84
Rb 20,8,5120
s_add_u32 s92, s83, 768
v_max_f32_e32 v11, v14, v15
Mv 92,44,92
Rb 24,8,6144
v_cmp_nge_f32_e32 vcc, s62, v11
Mv 88,44,88
Rb 28,8,7168
s_add_u32 s55, s55, 0x10000
Mv 88,48,96
Rb 32,8,8192
s_cmp_eq_u32 s57, 2
s_cselect_b32 s55, s59, s55
Mv 92,48,100
Rb 36,8,9216
buffer_load_dwordx4 v9, s[24:27], s92 offen offset:1024 lds
Mv 92,52,108
Rb 40,8,10240
s_add_u32 s92, s83, 1536
Mv 88,52,104
Rb 44,8,11264
Mv 88,56,112
buffer_load_dwordx4 v9, s[24:27], s92 offen offset:2048 lds
s_add_u32 s92, s83, 2304
Mv 92,56,116
buffer_load_dwordx4 v9, s[24:27], s92 offen offset:3072 lds
)" R"(
Mv 92,60,124
s_waitcnt lgkmcnt(8)
s_cbranch_vccnz .Lzfx7
.Lzfr7:
Mv 88,60,120
v_add_f32_e32 v12, v12, v14
v_add_f32_e32 v13, v13, v15
Mp 128,0,48
Rb 48,8,12288
Mp 136,0,52
s_cmp_le_i32 s57, s58
s_cbranch_scc1 .Lzmk6
.Lzmr6:
Mp 140,4,52
Rb 52,8,13312
s_add_u32 m0, s36, 0
Mp 132,4,48
buffer_load_dwordx4 v2, s[20:23], s55 offen offset:0 sc1 lds
Mp 144,8,48
Rb 56,8,14336
v_exp_f32_e32 v32, v64
Mp 152,8,52
buffer_store_dwordx4 a[224:227], v2, s[20:23], s47 offen offset:0 sc0 sc1
Mp 156,12,52
Rb 60,8,15360
s_waitcnt lgkmcnt(8)
Mp 148,12,48
v_exp_f32_e32 v26, v65
Mp 160,16,48
Rb 0,8,16384
v_add_f32_e32 v14, v32, v26
Mp 168,16,52
v_exp_f32_e32 v33, v66
Cv 32,32,26
Mp 172,20,52
Rb 4,8,17408
v_add_f32_e32 v14, v14, v33
Mp 164,20,48
buffer_load_dwordx4 v2, s[20:23], s55 offen offset:1024 sc1 lds
Mp 176,24,48
Rb 8,8,18432
v_exp_f32_e32 v27, v67
Mp 184,24,52
v_exp_f32_e32 v34, v72
Mp 188,28,52
Rb 12,8,19456
s_waitcnt lgkmcnt(8)
Mp 180,28,48
v_exp_f32_e32 v28, v73
v_add_f32_e32 v14, v14, v27
Mp 192,32,48
Rb 16,8,20480
Cv 33,33,27
Mp 200,32,52
v_exp_f32_e32 v35, v74
v_add_f32_e32 v14, v14, v34
Mp 204,36,52
Rb 20,8,21504
v_add_f32_e32 v14, v14, v28
Mp 196,36,48
buffer_load_dwordx4 v2, s[20:23], s55 offen offset:2048 sc1 lds
Mp 208,40,48
Rb 24,8,22528
v_exp_f32_e32 v29, v75
)" R"(
Mp 216,40,52
buffer_store_dwordx4 a[228:231], v2, s[20:23], s47 offen offset:1024 sc0 sc1
Mp 220,44,52
Rb 28,8,23552
s_waitcnt lgkmcnt(8)
Mp 212,44,48
v_exp_f32_e32 v36, v68
Cv 34,34,28
Mp 224,48,48
Rb 32,8,24576
v_add_f32_e32 v14, v14, v35
Mp 232,48,52
v_exp_f32_e32 v24, v69
v_add_f32_e32 v14, v14, v29
Mp 236,52,52
Rb 36,8,25600
Cv 35,35,29
Mp 228,52,48
buffer_load_dwordx4 v2, s[20:23], s55 offen offset:3072 sc1 lds
Mp 240,56,48
Rb 40,8,26624
v_exp_f32_e32 v37, v70
Mp 248,56,52
v_exp_f32_e32 v25, v71
Mp 252,60,52
Rb 44,8,27648
s_waitcnt lgkmcnt(8)
Mp 244,60,48
v_exp_f32_e32 v38, v76
v_add_f32_e32 v15, v36, v24
Mp 128,0,56
Rb 48,8,28672
Cv 36,36,24
Mp 136,0,60
v_exp_f32_e32 v26, v77
v_add_f32_e32 v15, v15, v37
Mp 140,4,60
Rb 52,8,29696
s_add_u32 m0, s36, 4096
Mp 132,4,56
buffer_load_dwordx4 v3, s[20:23], s55 offen offset:0 sc1 lds
Mp 144,8,56
Rb 56,8,30720
v_exp_f32_e32 v39, v78
Mp 152,8,60
buffer_store_dwordx4 a[232:235], v2, s[20:23], s47 offen offset:2048 sc0 sc1
Mp 156,12,60
Rb 60,8,31744
s_waitcnt lgkmcnt(8)
Mp 148,12,56
v_exp_f32_e32 v27, v79
v_add_f32_e32 v15, v15, v25
Mp 160,16,56
Cv 37,37,25
v_add_f32_e32 v15, v15, v38
Mp 168,16,60
v_add_f32_e32 v15, v15, v26
Cv 38,38,26
v_add_f32_e32 v15, v15, v39
Mp 172,20,60
v_add_f32_e32 v15, v15, v27
Cv 39,39,27
Mp 164,20,56
buffer_load_dwordx4 v3, s[20:23], s55 offen offset:1024 sc1 lds
)" R"(
Mp 176,24,56
s_add_u32 s50, s50, 1024
s_min_u32 s50, s50, s60
s_sub_u32 s57, s57, 1
Mp 184,24,60
v_accvgpr_read_b32 v9, a202
v_accvgpr_read_b32 v11, a203
Mp 188,28,60
Cv 9,9,11
v_accvgpr_write_b32 a149, v9
Mp 180,28,56
v_accvgpr_read_b32 v9, a204
s_waitcnt vmcnt(25) lgkmcnt(0)
s_barrier
Mp 192,32,56
Rb 0,1,32768
v_accvgpr_read_b32 v11, a205
Mp 200,32,60
Rb 4,1,33792
Cv 9,9,11
v_accvgpr_write_b32 a150, v9
Mp 204,36,60
Rb 8,1,34816
v_accvgpr_read_b32 v9, a206
Mp 196,36,56
Rb 12,1,35840
buffer_load_dwordx4 v3, s[20:23], s55 offen offset:2048 sc1 lds
Mp 208,40,56
Rb 16,1,40960
v_accvgpr_read_b32 v11, a207
Cv 9,9,11
Mp 216,40,60
Rb 20,1,41984
buffer_store_dwordx4 a[236:239], v2, s[20:23], s47 offen offset:3072 sc0 sc1
Mp 220,44,60
Rb 24,1,43008
v_accvgpr_write_b32 a151, v9
Mp 212,44,56
Rb 28,1,44032
v_accvgpr_read_b32 v9, a208
v_accvgpr_read_b32 v11, a209
Mp 224,48,56
Rb 32,1,36864
Cv 9,9,11
Mp 232,48,60
Rb 36,1,37888
v_accvgpr_write_b32 a152, v9
v_accvgpr_read_b32 v9, a210
Mp 236,52,60
Rb 40,1,38912
v_accvgpr_read_b32 v11, a211
Mp 228,52,56
Rb 44,1,39936
buffer_load_dwordx4 v3, s[20:23], s55 offen offset:3072 sc1 lds
Mp 240,56,56
Cv 9,9,11
v_accvgpr_write_b32 a153, v9
v_accvgpr_read_b32 v9, a212
.LzH13:
s_cbranch_execz .LzH21
Mp 248,56,60
v_accvgpr_read_b32 v11, a213
Cv 9,9,11
Mp 252,60,60
v_accvgpr_write_b32 a154, v9
)" R"(
s_waitcnt lgkmcnt(8)
Mp 244,60,56
v_mfma_f32_16x16x32_bf16 v[96:99], a[0:3], a[64:67], v[16:19]
Rb 48,1,45056
buffer_load_dword v4, v5, s[20:23], s50 offen offset:0 sc0 sc1
v_mfma_f32_16x16x32_bf16 v[100:103], a[0:3], a[68:71], v[20:23]
v_exp_f32_e32 v40, v80
Mv 100,4,76
Rb 52,1,46080
s_add_u32 m0, s36, 65536
Mv 96,4,72
buffer_load_dwordx4 v2, s[20:23], s56 offen offset:0 sc1 lds
Mv 96,8,80
Rb 56,1,47104
v_add_f32_e32 v14, v14, v40
Mv 100,8,84
buffer_store_dwordx4 a[240:243], v3, s[20:23], s47 offen offset:0 sc0 sc1
Mv 100,12,92
Rb 60,1,48128
s_waitcnt lgkmcnt(8)
Mv 96,12,88
v_exp_f32_e32 v28, v81
Mv 96,16,96
Rb 0,1,49152
v_add_f32_e32 v14, v14, v28
Mv 100,16,100
v_exp_f32_e32 v41, v82
Mv 100,20,108
Rb 4,1,50176
Cv 40,40,28
Mv 96,20,104
buffer_load_dwordx4 v2, s[20:23], s56 offen offset:1024 sc1 lds
Mv 96,24,112
Rb 8,1,51200
v_add_f32_e32 v14, v14, v41
Mv 100,24,116
v_exp_f32_e32 v29, v83
Mv 100,28,124
Rb 12,1,52224
s_waitcnt lgkmcnt(8)
Mv 96,28,120
v_exp_f32_e32 v42, v88
v_add_f32_e32 v14, v14, v29
v_mfma_f32_16x16x32_bf16 v[104:107], a[32:35], a[64:67], v[16:19]
Rb 16,1,57344
Cv 41,41,29
v_mfma_f32_16x16x32_bf16 v[108:111], a[32:35], a[68:71], v[20:23]
v_exp_f32_e32 v24, v89
Mv 108,36,76
Rb 20,1,58368
v_add_f32_e32 v14, v14, v42
Mv 104,36,72
buffer_load_dwordx4 v2, s[20:23], s56 offen offset:2048 sc1 lds
Mv 104,40,80
Rb 24,1,59392
v_add_f32_e32 v14, v14, v24
Mv 108,40,84
buffer_store_dwordx4 a[244:247], v3, s[20:23], s47 offen offset:1024 sc0 sc1
Mv 108,44,92
Rb 28,1,60416
s_waitcnt lgkmcnt(8)
Mv 104,44,88
v_exp_f32_e32 v43, v90
Mv 104,48,96
)" R"(
Rb 32,1,53248
Cv 42,42,24
Mv 108,48,100
v_exp_f32_e32 v25, v91
Mv 108,52,108
Rb 36,1,54272
v_add_f32_e32 v14, v14, v43
Mv 104,52,104
buffer_load_dwordx4 v2, s[20:23], s56 offen offset:3072 sc1 lds
Mv 104,56,112
Rb 40,1,55296
v_add_f32_e32 v14, v14, v25
Mv 108,56,116
v_exp_f32_e32 v44, v84
Mv 108,60,124
Rb 44,1,56320
s_waitcnt lgkmcnt(8)
Mv 104,60,120
v_exp_f32_e32 v26, v85
Cv 43,43,25
v_mfma_f32_16x16x32_bf16 v[112:115], a[0:3], a[64:67], v[16:19]
Rb 48,1,61440
v_add_f32_e32 v15, v15, v44
v_mfma_f32_16x16x32_bf16 v[116:119], a[0:3], a[68:71], v[20:23]
v_exp_f32_e32 v45, v86
Mv 116,4,76
Rb 52,1,62464
s_add_u32 m0, s36, 69632
Mv 112,4,72
buffer_load_dwordx4 v3, s[20:23], s56 offen offset:0 sc1 lds
Mv 112,8,80
Rb 56,1,63488
v_add_f32_e32 v15, v15, v26
Mv 116,8,84
buffer_store_dwordx4 a[248:251], v3, s[20:23], s47 offen offset:2048 sc0 sc1
Mv 116,12,92
Rb 60,1,64512
s_waitcnt lgkmcnt(8)
Mv 112,12,88
v_exp_f32_e32 v27, v87
Mv 112,16,96
v_exp_f32_e32 v46, v92
Mv 116,16,100
v_exp_f32_e32 v28, v93
Mv 116,20,108
v_exp_f32_e32 v47, v94
Mv 112,20,104
buffer_load_dwordx4 v3, s[20:23], s56 offen offset:1024 sc1 lds
Mv 112,24,112
v_exp_f32_e32 v29, v95
Mv 116,24,116
Cv 44,44,26
v_add_f32_e32 v15, v15, v45
Mv 116,28,124
v_add_f32_e32 v15, v15, v27
Cv 45,45,27
Mv 112,28,120
v_add_f32_e32 v15, v15, v46
s_waitcnt vmcnt(30) lgkmcnt(0)
s_barrier
v_mfma_f32_16x16x32_bf16 v[120:123], a[32:35], a[64:67], v[16:19]
Rb 0,8,32768
v_add_f32_e32 v15, v15, v28
v_mfma_f32_16x16x32_bf16 v[124:127], a[32:35], a[68:71], v[20:23]
Rb 4,8,33792
)" R"(
Cv 46,46,28
Mv 124,36,76
Rb 8,8,34816
v_add_f32_e32 v15, v15, v47
Mv 120,36,72
Rb 12,8,35840
buffer_load_dwordx4 v3, s[20:23], s56 offen offset:2048 sc1 lds
Mv 120,40,80
Rb 16,8,36864
v_add_f32_e32 v15, v15, v29
Mv 124,40,84
Rb 20,8,37888
buffer_store_dwordx4 a[252:255], v3, s[20:23], s47 offen offset:3072 sc0 sc1
Mv 124,44,92
Rb 24,8,38912
Cv 47,47,29
Mv 120,44,88
Rb 28,8,39936
v_max_f32_e32 v11, v14, v15
Mv 120,48,96
Rb 32,8,40960
v_cmp_nge_f32_e32 vcc, s62, v11
Mv 124,48,100
Rb 36,8,41984
s_add_u32 s55, s55, 0x10000
Mv 124,52,108
Rb 40,8,43008
Mv 120,52,104
Rb 44,8,44032
buffer_load_dwordx4 v3, s[20:23], s56 offen offset:3072 sc1 lds
Mv 120,56,112
s_cmp_eq_u32 s57, 2
s_cselect_b32 s55, s59, s55
Mv 124,56,116
s_add_u32 s56, s56, 0x10000
Mv 124,60,124
s_waitcnt lgkmcnt(8)
s_cbranch_vccnz .Lzfx8
.Lzfr8:
Mv 120,60,120
v_add_f32_e32 v12, v12, v14
v_add_f32_e32 v13, v13, v15
Mp 128,0,32
Rb 48,8,45056
Mp 136,0,36
s_cmp_le_i32 s57, s58
s_cbranch_scc1 .Lzmk7
.Lzmr7:
Mp 140,4,36
Rb 52,8,46080
s_add_u32 m0, s36, 32768
Mp 132,4,32
buffer_load_dwordx4 v2, s[20:23], s55 offen offset:0 sc1 lds
Mp 144,8,32
Rb 56,8,47104
Mp 152,8,36
v_exp_f32_e32 v48, v96
Mp 156,12,36
Rb 60,8,48128
s_waitcnt lgkmcnt(8)
Mp 148,12,32
v_exp_f32_e32 v24, v97
Mp 160,16,32
Rb 0,8,49152
)" R"(
v_add_f32_e32 v14, v48, v24
Mp 168,16,36
v_exp_f32_e32 v49, v98
Mp 172,20,36
Rb 4,8,50176
Cv 48,48,24
Mp 164,20,32
buffer_load_dwordx4 v2, s[20:23], s55 offen offset:1024 sc1 lds
Mp 176,24,32
Rb 8,8,51200
v_exp_f32_e32 v25, v99
Mp 184,24,36
v_exp_f32_e32 v50, v104
Mp 188,28,36
Rb 12,8,52224
s_waitcnt lgkmcnt(8)
Mp 180,28,32
v_exp_f32_e32 v26, v105
Mp 192,32,32
Rb 16,8,53248
v_add_f32_e32 v14, v14, v49
Mp 200,32,36
v_exp_f32_e32 v51, v106
v_add_f32_e32 v14, v14, v25
Mp 204,36,36
Rb 20,8,54272
Cv 49,49,25
Mp 196,36,32
buffer_load_dwordx4 v2, s[20:23], s55 offen offset:2048 sc1 lds
Mp 208,40,32
Rb 24,8,55296
v_add_f32_e32 v14, v14, v50
Mp 216,40,36
v_exp_f32_e32 v27, v107
Mp 220,44,36
Rb 28,8,56320
s_waitcnt lgkmcnt(8)
Mp 212,44,32
v_exp_f32_e32 v52, v100
v_add_f32_e32 v14, v14, v26
Mp 224,48,32
Rb 32,8,57344
Cv 50,50,26
Mp 232,48,36
v_exp_f32_e32 v28, v101
Mp 236,52,36
Rb 36,8,58368
v_add_f32_e32 v14, v14, v51
Mp 228,52,32
buffer_load_dwordx4 v2, s[20:23], s55 offen offset:3072 sc1 lds
Mp 240,56,32
Rb 40,8,59392
v_exp_f32_e32 v53, v102
Mp 248,56,36
v_exp_f32_e32 v29, v103
Mp 252,60,36
Rb 44,8,60416
s_waitcnt lgkmcnt(8)
Mp 244,60,32
v_exp_f32_e32 v54, v108
Mp 128,0,40
Rb 48,8,61440
v_add_f32_e32 v14, v14, v27
Mp 136,0,44
)" R"(
v_exp_f32_e32 v24, v109
Cv 51,51,27
Mp 140,4,44
Rb 52,8,62464
s_add_u32 m0, s36, 36864
Mp 132,4,40
buffer_load_dwordx4 v3, s[20:23], s55 offen offset:0 sc1 lds
Mp 144,8,40
Rb 56,8,63488
v_add_f32_e32 v15, v52, v28
Mp 152,8,44
v_exp_f32_e32 v55, v110
Mp 156,12,44
Rb 60,8,64512
s_waitcnt lgkmcnt(8)
Mp 148,12,40
v_exp_f32_e32 v25, v111
Cv 52,52,28
Mp 160,16,40
v_add_f32_e32 v15, v15, v53
v_add_f32_e32 v15, v15, v29
Mp 168,16,44
Cv 53,53,29
v_add_f32_e32 v15, v15, v54
Mp 172,20,44
v_add_f32_e32 v15, v15, v24
Cv 54,54,24
Mp 164,20,40
buffer_load_dwordx4 v3, s[20:23], s55 offen offset:1024 sc1 lds
Mp 176,24,40
v_add_f32_e32 v15, v15, v55
v_add_f32_e32 v15, v15, v25
Cv 55,55,25
Mp 184,24,44
s_sub_u32 s57, s57, 1
v_accvgpr_read_b32 v9, a214
Mp 188,28,44
v_accvgpr_read_b32 v11, a215
Cv 9,9,11
Mp 180,28,40
s_waitcnt vmcnt(7) lgkmcnt(0)
s_barrier
buffer_store_dword v6, v7, s[20:23], s49 offen offset:2048 sc0 sc1
Mp 192,32,40
Rb 0,1,0
v_accvgpr_write_b32 a155, v9
Mp 200,32,44
Rb 4,1,1024
v_accvgpr_read_b32 v9, a216
v_accvgpr_read_b32 v11, a217
Mp 204,36,44
Rb 8,1,2048
Cv 9,9,11
Mp 196,36,40
Rb 12,1,3072
buffer_load_dwordx4 v3, s[20:23], s55 offen offset:2048 sc1 lds
Mp 208,40,40
Rb 16,1,8192
v_accvgpr_write_b32 a156, v9
Mp 216,40,44
Rb 20,1,9216
v_accvgpr_read_b32 v9, a218
Mp 220,44,44
Rb 24,1,10240
v_accvgpr_read_b32 v11, a219
)" R"(
Mp 212,44,40
Rb 28,1,11264
Cv 9,9,11
v_accvgpr_write_b32 a157, v9
Mp 224,48,40
Rb 32,1,4096
v_accvgpr_read_b32 v9, a220
Mp 232,48,44
Rb 36,1,5120
v_accvgpr_read_b32 v11, a221
Mp 236,52,44
Rb 40,1,6144
Cv 9,9,11
Mp 228,52,40
Rb 44,1,7168
buffer_load_dwordx4 v3, s[20:23], s55 offen offset:3072 sc1 lds
Mp 240,56,40
v_cmp_eq_u32_e32 vcc, s3, v4
s_andn2_b64 s[28:29], -1, vcc
s_cbranch_scc1 .Lzsp20
.Lzok20:
Mp 248,56,44
v_accvgpr_write_b32 a158, v9
v_accvgpr_read_b32 v9, a222
Mp 252,60,44
v_accvgpr_read_b32 v11, a223
s_waitcnt lgkmcnt(8)
Mp 244,60,40
Cv 9,9,11
v_accvgpr_write_b32 a159, v9
v_mfma_f32_16x16x32_bf16 v[64:67], a[0:3], a[64:67], v[16:19]
Rb 48,1,12288
v_lshrrev_b32 v9, 2, v0
v_mfma_f32_16x16x32_bf16 v[68:71], a[0:3], a[68:71], v[20:23]
v_mul_u32_u24 v9, 448, v9
s_lshr_b32 s92, s33, 1
Mv 68,4,76
Rb 52,1,13312
s_add_u32 m0, s36, 98304
Mv 64,4,72
buffer_load_dwordx4 v2, s[20:23], s56 offen offset:0 sc1 lds
Mv 64,8,80
Rb 56,1,14336
s_mul_i32 s92, s92, 48
Mv 68,8,84
buffer_store_dwordx4 a[128:131], v2, s[20:23], s48 offen offset:0 sc0 sc1
Mv 68,12,92
Rb 60,1,15360
s_waitcnt lgkmcnt(8)
Mv 64,12,88
s_lshl3_add_u32 s92, s33, s92
v_add3_u32 v9, v9, s92, v10
Mv 64,16,96
Rb 0,1,16384
Mv 68,16,100
v_exp_f32_e32 v56, v112
Mv 68,20,108
Rb 4,1,17408
Rt 160,9,0
Mv 64,20,104
buffer_load_dwordx4 v2, s[20:23], s56 offen offset:1024 sc1 lds
Mv 64,24,112
Rb 8,1,18432
v_exp_f32_e32 v26, v113
)" R"(
Mv 68,24,116
v_exp_f32_e32 v57, v114
Mv 68,28,124
Rb 12,1,19456
s_waitcnt lgkmcnt(9)
Mv 64,28,120
v_exp_f32_e32 v27, v115
v_add_f32_e32 v14, v14, v56
v_mfma_f32_16x16x32_bf16 v[72:75], a[32:35], a[64:67], v[16:19]
Rb 16,1,24576
v_add_f32_e32 v14, v14, v26
v_mfma_f32_16x16x32_bf16 v[76:79], a[32:35], a[68:71], v[20:23]
v_exp_f32_e32 v58, v120
Mv 76,36,76
Rb 20,1,25600
Cv 56,56,26
Mv 72,36,72
buffer_load_dwordx4 v2, s[20:23], s56 offen offset:2048 sc1 lds
v_add_f32_e32 v14, v14, v57
Mv 72,40,80
Rb 24,1,26624
Rt 162,9,8192
Mv 76,40,84
v_exp_f32_e32 v28, v121
Mv 76,44,92
Rb 28,1,27648
v_add_f32_e32 v14, v14, v27
s_waitcnt lgkmcnt(10)
Mv 72,44,88
v_exp_f32_e32 v59, v122
Mv 72,48,96
Rb 32,1,20480
Cv 57,57,27
Mv 76,48,100
v_exp_f32_e32 v29, v123
v_add_f32_e32 v14, v14, v58
Mv 76,52,108
Rb 36,1,21504
v_add_f32_e32 v14, v14, v28
Mv 72,52,104
buffer_load_dwordx4 v2, s[20:23], s56 offen offset:3072 sc1 lds
Mv 72,56,112
Rb 40,1,22528
v_exp_f32_e32 v60, v116
Mv 76,56,116
buffer_store_dwordx4 a[132:135], v2, s[20:23], s48 offen offset:1024 sc0 sc1
Mv 76,60,124
Rb 44,1,23552
Rt 164,9,128
s_waitcnt lgkmcnt(10)
Mv 72,60,120
v_exp_f32_e32 v24, v117
Cv 58,58,28
v_mfma_f32_16x16x32_bf16 v[80:83], a[0:3], a[64:67], v[16:19]
Rb 48,1,28672
v_add_f32_e32 v14, v14, v59
v_mfma_f32_16x16x32_bf16 v[84:87], a[0:3], a[68:71], v[20:23]
v_exp_f32_e32 v61, v118
Mv 84,4,76
Rb 52,1,29696
s_add_u32 m0, s36, 102400
Mv 80,4,72
buffer_load_dwordx4 v3, s[20:23], s56 offen offset:0 sc1 lds
v_add_f32_e32 v14, v14, v29
)" R"(
Mv 80,8,80
Rb 56,1,30720
Cv 59,59,29
Mv 84,8,84
v_exp_f32_e32 v25, v119
Mv 84,12,92
Rb 60,1,31744
v_add_f32_e32 v15, v15, v60
s_waitcnt lgkmcnt(9)
Mv 80,12,88
v_exp_f32_e32 v62, v124
Mv 80,16,96
Rt 166,9,8320
v_add_f32_e32 v15, v15, v24
Mv 84,16,100
buffer_store_dwordx4 a[160:163], v1, s[20:23], s4 offen sc0 sc1
Cv 60,60,24
Mv 84,20,108
v_exp_f32_e32 v26, v125
Mv 80,20,104
buffer_load_dwordx4 v3, s[20:23], s56 offen offset:1024 sc1 lds
Mv 80,24,112
v_exp_f32_e32 v63, v126
v_add_f32_e32 v15, v15, v61
Mv 84,24,116
v_exp_f32_e32 v27, v127
Mv 84,28,124
v_add_f32_e32 v15, v15, v25
Cv 61,61,25
Mv 80,28,120
v_add_f32_e32 v15, v15, v62
s_waitcnt vmcnt(18) lgkmcnt(0)
s_barrier
v_mfma_f32_16x16x32_bf16 v[88:91], a[32:35], a[64:67], v[16:19]
Rb 0,8,0
v_add_f32_e32 v15, v15, v26
v_mfma_f32_16x16x32_bf16 v[92:95], a[32:35], a[68:71], v[20:23]
Rb 4,8,1024
Cv 62,62,26
Mv 92,36,76
Rb 8,8,2048
Rt 168,9,256
Mv 88,36,72
Rb 12,8,3072
buffer_load_dwordx4 v3, s[20:23], s56 offen offset:2048 sc1 lds
Mv 88,40,80
Rb 16,8,4096
v_add_f32_e32 v15, v15, v63
Mv 92,40,84
Rb 20,8,5120
buffer_store_dwordx4 a[136:139], v2, s[20:23], s48 offen offset:2048 sc0 sc1
Mv 92,44,92
Rb 24,8,6144
v_add_f32_e32 v15, v15, v27
Cv 63,63,27
Mv 88,44,88
Rb 28,8,7168
v_max_f32_e32 v11, v14, v15
Mv 88,48,96
Rb 32,8,8192
v_cmp_nge_f32_e32 vcc, s62, v11
Mv 92,48,100
Rb 36,8,9216
s_add_u32 s55, s55, 0x10000
Mv 92,52,108
)" R"(
Rb 40,8,10240
Mv 88,52,104
Rb 44,8,11264
buffer_load_dwordx4 v3, s[20:23], s56 offen offset:3072 sc1 lds
Mv 88,56,112
Rt 170,9,8448
s_cmp_eq_u32 s57, 2
s_cselect_b32 s55, s59, s55
Mv 92,56,116
s_add_u32 s56, s56, 0x10000
buffer_store_dwordx4 a[164:167], v1, s[20:23], s5 offen sc0 sc1
Mv 92,60,124
s_waitcnt lgkmcnt(9)
s_cbranch_vccnz .Lzfx9
.Lzfr9:
Mv 88,60,120
v_add_f32_e32 v12, v12, v14
v_add_f32_e32 v13, v13, v15
Mp 128,0,48
Rb 48,8,12288
Mp 136,0,52
s_cmp_le_i32 s57, s58
s_cbranch_scc1 .Lzmk8
.Lzmr8:
Mp 140,4,52
Rb 52,8,13312
s_add_u32 m0, s36, 0
Mp 132,4,48
buffer_load_dwordx4 v2, s[20:23], s55 offen offset:0 sc1 lds
Mp 144,8,48
Rb 56,8,14336
Mp 152,8,52
v_exp_f32_e32 v32, v64
Mp 156,12,52
Rb 60,8,15360
Rt 172,9,384
s_waitcnt lgkmcnt(10)
Mp 148,12,48
v_exp_f32_e32 v28, v65
Mp 160,16,48
Rb 0,8,16384
v_add_f32_e32 v14, v32, v28
Mp 168,16,52
v_exp_f32_e32 v33, v66
Cv 32,32,28
Mp 172,20,52
Rb 4,8,17408
v_add_f32_e32 v14, v14, v33
Mp 164,20,48
buffer_load_dwordx4 v2, s[20:23], s55 offen offset:1024 sc1 lds
Mp 176,24,48
Rb 8,8,18432
Mp 184,24,52
buffer_store_dwordx4 a[140:143], v2, s[20:23], s48 offen offset:3072 sc0 sc1
Mp 188,28,52
Rb 12,8,19456
s_waitcnt lgkmcnt(10)
Mp 180,28,48
v_exp_f32_e32 v29, v67
Mp 192,32,48
Rb 16,8,20480
Rt 174,9,8576
Mp 200,32,52
v_exp_f32_e32 v34, v72
)" R"(
v_add_f32_e32 v14, v14, v29
Mp 204,36,52
Rb 20,8,21504
Cv 33,33,29
Mp 196,36,48
buffer_load_dwordx4 v2, s[20:23], s55 offen offset:2048 sc1 lds
Mp 208,40,48
Rb 24,8,22528
v_add_f32_e32 v14, v14, v34
Mp 216,40,52
v_exp_f32_e32 v24, v73
.LzH14:
s_cbranch_execz .LzH22
Mp 220,44,52
Rb 28,8,23552
s_waitcnt lgkmcnt(10)
Mp 212,44,48
buffer_store_dwordx4 a[168:171], v1, s[20:23], s6 offen sc0 sc1
Mp 224,48,48
Rb 32,8,24576
v_add_f32_e32 v14, v14, v24
Mp 232,48,52
v_exp_f32_e32 v35, v74
Cv 34,34,24
Mp 236,52,52
Rb 36,8,25600
Rt 176,9,16384
Mp 228,52,48
buffer_load_dwordx4 v2, s[20:23], s55 offen offset:3072 sc1 lds
Mp 240,56,48
Rb 40,8,26624
v_add_f32_e32 v14, v14, v35
Mp 248,56,52
v_exp_f32_e32 v25, v75
Mp 252,60,52
Rb 44,8,27648
s_waitcnt lgkmcnt(10)
Mp 244,60,48
v_exp_f32_e32 v36, v68
Mp 128,0,56
Rb 48,8,28672
v_add_f32_e32 v14, v14, v25
Mp 136,0,60
v_exp_f32_e32 v26, v69
Cv 35,35,25
Mp 140,4,60
Rb 52,8,29696
s_add_u32 m0, s36, 4096
Mp 132,4,56
buffer_load_dwordx4 v3, s[20:23], s55 offen offset:0 sc1 lds
Mp 144,8,56
Rb 56,8,30720
Rt 178,9,24576
Mp 152,8,60
buffer_store_dwordx4 a[144:147], v3, s[20:23], s48 offen offset:0 sc0 sc1
Mp 156,12,60
Rb 60,8,31744
s_waitcnt lgkmcnt(10)
Mp 148,12,56
v_exp_f32_e32 v37, v70
Mp 160,16,56
v_exp_f32_e32 v27, v71
Mp 168,16,60
v_exp_f32_e32 v38, v76
)" R"(
v_add_f32_e32 v15, v36, v26
Mp 172,20,60
v_exp_f32_e32 v28, v77
Mp 164,20,56
buffer_load_dwordx4 v3, s[20:23], s55 offen offset:1024 sc1 lds
Mp 176,24,56
s_add_u32 s50, s50, 1024
s_min_u32 s50, s50, s60
Mp 184,24,60
buffer_store_dwordx4 a[172:175], v1, s[20:23], s7 offen sc0 sc1
Mp 188,28,60
Rt 180,9,16512
Cv 36,36,26
Mp 180,28,56
s_waitcnt vmcnt(23) lgkmcnt(1)
s_barrier
Mp 192,32,56
Rb 0,1,32768
v_add_f32_e32 v15, v15, v37
Mp 200,32,60
Rb 4,1,33792
v_exp_f32_e32 v39, v78
Mp 204,36,60
Rb 8,1,34816
v_add_f32_e32 v15, v15, v27
Mp 196,36,56
Rb 12,1,35840
buffer_load_dwordx4 v3, s[20:23], s55 offen offset:2048 sc1 lds
Mp 208,40,56
Rb 16,1,40960
Cv 37,37,27
Mp 216,40,60
Rb 20,1,41984
v_add_f32_e32 v15, v15, v38
Mp 220,44,60
Rb 24,1,43008
v_add_f32_e32 v15, v15, v28
Mp 212,44,56
Rb 28,1,44032
Cv 38,38,28
Mp 224,48,56
Rb 32,1,36864
Rt 182,9,24704
Mp 232,48,60
Rb 36,1,37888
v_exp_f32_e32 v29, v79
Mp 236,52,60
Rb 40,1,38912
v_add_f32_e32 v15, v15, v39
Mp 228,52,56
Rb 44,1,39936
buffer_load_dwordx4 v3, s[20:23], s55 offen offset:3072 sc1 lds
Mp 240,56,56
v_add_f32_e32 v15, v15, v29
Cv 39,39,29
Mp 248,56,60
buffer_store_dwordx4 a[148:151], v3, s[20:23], s48 offen offset:1024 sc0 sc1
Mp 252,60,60
s_sub_u32 s57, s57, 1
s_waitcnt lgkmcnt(9)
Mp 244,60,56
v_mfma_f32_16x16x32_bf16 v[96:99], a[0:3], a[64:67], v[16:19]
Rb 48,1,45056
buffer_load_dword v4, v5, s[20:23], s50 offen offset:0 sc0 sc1
v_mfma_f32_16x16x32_bf16 v[100:103], a[0:3], a[68:71], v[20:23]
)" R"(
v_exp_f32_e32 v40, v80
Mv 100,4,76
Rb 52,1,46080
s_add_u32 m0, s36, 65536
Rt 184,9,16640
Mv 96,4,72
buffer_load_dwordx4 v2, s[20:23], s56 offen offset:0 sc1 lds
Mv 96,8,80
Rb 56,1,47104
v_add_f32_e32 v14, v14, v40
Mv 100,8,84
v_exp_f32_e32 v24, v81
Mv 100,12,92
Rb 60,1,48128
s_waitcnt lgkmcnt(10)
Mv 96,12,88
buffer_store_dwordx4 a[176:179], v1, s[20:23], s8 offen sc0 sc1
v_add_f32_e32 v14, v14, v24
Mv 96,16,96
Rb 0,1,49152
Cv 40,40,24
Mv 100,16,100
v_exp_f32_e32 v41, v82
Mv 100,20,108
Rb 4,1,50176
v_add_f32_e32 v14, v14, v41
Mv 96,20,104
buffer_load_dwordx4 v2, s[20:23], s56 offen offset:1024 sc1 lds
Mv 96,24,112
Rb 8,1,51200
Rt 186,9,24832
Mv 100,24,116
v_exp_f32_e32 v25, v83
Mv 100,28,124
Rb 12,1,52224
s_waitcnt lgkmcnt(10)
Mv 96,28,120
v_exp_f32_e32 v42, v88
v_add_f32_e32 v14, v14, v25
v_mfma_f32_16x16x32_bf16 v[104:107], a[32:35], a[64:67], v[16:19]
Rb 16,1,57344
Cv 41,41,25
v_mfma_f32_16x16x32_bf16 v[108:111], a[32:35], a[68:71], v[20:23]
v_exp_f32_e32 v26, v89
Mv 108,36,76
Rb 20,1,58368
v_add_f32_e32 v14, v14, v42
Mv 104,36,72
buffer_load_dwordx4 v2, s[20:23], s56 offen offset:2048 sc1 lds
Mv 104,40,80
Rb 24,1,59392
v_add_f32_e32 v14, v14, v26
Mv 108,40,84
buffer_store_dwordx4 a[152:155], v3, s[20:23], s48 offen offset:2048 sc0 sc1
Mv 108,44,92
Rb 28,1,60416
Rt 188,9,16768
s_waitcnt lgkmcnt(10)
Mv 104,44,88
v_exp_f32_e32 v43, v90
Cv 42,42,26
Mv 104,48,96
Rb 32,1,53248
v_add_f32_e32 v14, v14, v43
)" R"(
Mv 108,48,100
v_exp_f32_e32 v27, v91
Mv 108,52,108
Rb 36,1,54272
v_add_f32_e32 v14, v14, v27
Mv 104,52,104
buffer_load_dwordx4 v2, s[20:23], s56 offen offset:3072 sc1 lds
Mv 104,56,112
Rb 40,1,55296
Cv 43,43,27
Mv 108,56,116
buffer_store_dwordx4 a[180:183], v1, s[20:23], s9 offen sc0 sc1
Mv 108,60,124
Rb 44,1,56320
s_waitcnt lgkmcnt(9)
Mv 104,60,120
v_exp_f32_e32 v44, v84
s_add_u32 s55, s55, 0x10000
v_mfma_f32_16x16x32_bf16 v[112:115], a[0:3], a[64:67], v[16:19]
Rb 48,1,61440
Rt 190,9,24960
v_mfma_f32_16x16x32_bf16 v[116:119], a[0:3], a[68:71], v[20:23]
v_exp_f32_e32 v28, v85
Mv 116,4,76
Rb 52,1,62464
s_add_u32 m0, s36, 69632
Mv 112,4,72
buffer_load_dwordx4 v3, s[20:23], s56 offen offset:0 sc1 lds
Mv 112,8,80
Rb 56,1,63488
v_add_f32_e32 v15, v15, v44
Mv 116,8,84
v_exp_f32_e32 v45, v86
Mv 116,12,92
Rb 60,1,64512
s_waitcnt lgkmcnt(10)
Mv 112,12,88
v_exp_f32_e32 v29, v87
v_add_f32_e32 v15, v15, v28
Mv 112,16,96
v_exp_f32_e32 v46, v92
Mv 116,16,100
v_exp_f32_e32 v24, v93
Mv 116,20,108
v_exp_f32_e32 v47, v94
Mv 112,20,104
buffer_load_dwordx4 v3, s[20:23], s56 offen offset:1024 sc1 lds
Mv 112,24,112
v_exp_f32_e32 v25, v95
Mv 116,24,116
buffer_store_dwordx4 a[156:159], v3, s[20:23], s48 offen offset:3072 sc0 sc1
Mv 116,28,124
s_cmp_eq_u32 s57, 2
s_cselect_b32 s55, s59, s55
Mv 112,28,120
Cv 44,44,28
s_waitcnt vmcnt(25) lgkmcnt(0)
s_barrier
v_mfma_f32_16x16x32_bf16 v[120:123], a[32:35], a[64:67], v[16:19]
Rb 0,8,32768
v_add_f32_e32 v15, v15, v45
v_mfma_f32_16x16x32_bf16 v[124:127], a[32:35], a[68:71], v[20:23]
Rb 4,8,33792
v_add_f32_e32 v15, v15, v29
Mv 124,36,76
)" R"(
Rb 8,8,34816
Cv 45,45,29
Mv 120,36,72
Rb 12,8,35840
buffer_load_dwordx4 v3, s[20:23], s56 offen offset:2048 sc1 lds
Mv 120,40,80
Rb 16,8,36864
v_add_f32_e32 v15, v15, v46
Mv 124,40,84
Rb 20,8,37888
v_add_f32_e32 v15, v15, v24
Mv 124,44,92
Rb 24,8,38912
Cv 46,46,24
Mv 120,44,88
Rb 28,8,39936
buffer_store_dwordx4 a[184:187], v1, s[20:23], s10 offen sc0 sc1
Mv 120,48,96
Rb 32,8,40960
v_add_f32_e32 v15, v15, v47
Mv 124,48,100
Rb 36,8,41984
v_add_f32_e32 v15, v15, v25
Mv 124,52,108
Rb 40,8,43008
Cv 47,47,25
Mv 120,52,104
Rb 44,8,44032
buffer_load_dwordx4 v3, s[20:23], s56 offen offset:3072 sc1 lds
Mv 120,56,112
v_max_f32_e32 v11, v14, v15
v_cmp_nge_f32_e32 vcc, s62, v11
Mv 124,56,116
s_add_u32 s56, s56, 0x10000
Mv 124,60,124
s_waitcnt lgkmcnt(8)
s_cbranch_vccnz .Lzfx10
.Lzfr10:
Mv 120,60,120
v_add_f32_e32 v12, v12, v14
v_add_f32_e32 v13, v13, v15
Mp 128,0,32
Rb 48,8,45056
Mp 136,0,36
s_cmp_le_i32 s57, s58
s_cbranch_scc1 .Lzmk9
.Lzmr9:
Mp 140,4,36
Rb 52,8,46080
s_add_u32 m0, s36, 32768
Mp 132,4,32
buffer_load_dwordx4 v2, s[20:23], s55 offen offset:0 sc1 lds
Mp 144,8,32
Rb 56,8,47104
Mp 152,8,36
buffer_store_dwordx4 a[188:191], v1, s[20:23], s11 offen sc0 sc1
Mp 156,12,36
Rb 60,8,48128
s_waitcnt lgkmcnt(8)
Mp 148,12,32
v_exp_f32_e32 v48, v96
Mp 160,16,32
Rb 0,8,49152
Mp 168,16,36
)" R"(
v_exp_f32_e32 v26, v97
Mp 172,20,36
Rb 4,8,50176
v_add_f32_e32 v14, v48, v26
Mp 164,20,32
buffer_load_dwordx4 v2, s[20:23], s55 offen offset:1024 sc1 lds
Mp 176,24,32
Rb 8,8,51200
Cv 48,48,26
Mp 184,24,36
v_exp_f32_e32 v49, v98
Mp 188,28,36
Rb 12,8,52224
s_waitcnt lgkmcnt(8)
Mp 180,28,32
v_exp_f32_e32 v27, v99
Mp 192,32,32
Rb 16,8,53248
v_add_f32_e32 v14, v14, v49
Mp 200,32,36
v_exp_f32_e32 v50, v104
Mp 204,36,36
Rb 20,8,54272
v_add_f32_e32 v14, v14, v27
Mp 196,36,32
buffer_load_dwordx4 v2, s[20:23], s55 offen offset:2048 sc1 lds
Mp 208,40,32
Rb 24,8,55296
Cv 49,49,27
Mp 216,40,36
v_exp_f32_e32 v28, v105
Mp 220,44,36
Rb 28,8,56320
s_waitcnt lgkmcnt(8)
Mp 212,44,32
v_exp_f32_e32 v51, v106
Mp 224,48,32
Rb 32,8,57344
v_add_f32_e32 v14, v14, v50
Mp 232,48,36
v_exp_f32_e32 v29, v107
Mp 236,52,36
Rb 36,8,58368
v_add_f32_e32 v14, v14, v28
Mp 228,52,32
buffer_load_dwordx4 v2, s[20:23], s55 offen offset:3072 sc1 lds
Mp 240,56,32
Rb 40,8,59392
Cv 50,50,28
Mp 248,56,36
v_exp_f32_e32 v52, v100
Mp 252,60,36
Rb 44,8,60416
s_waitcnt lgkmcnt(8)
Mp 244,60,32
v_exp_f32_e32 v24, v101
Mp 128,0,40
Rb 48,8,61440
v_add_f32_e32 v14, v14, v51
Mp 136,0,44
v_exp_f32_e32 v53, v102
Mp 140,4,44
Rb 52,8,62464
s_add_u32 m0, s36, 36864
)" R"(
Mp 132,4,40
buffer_load_dwordx4 v3, s[20:23], s55 offen offset:0 sc1 lds
Mp 144,8,40
Rb 56,8,63488
v_add_f32_e32 v14, v14, v29
Mp 152,8,44
v_exp_f32_e32 v25, v103
Mp 156,12,44
Rb 60,8,64512
s_waitcnt lgkmcnt(8)
Mp 148,12,40
v_exp_f32_e32 v54, v108
Mp 160,16,40
v_exp_f32_e32 v26, v109
Mp 168,16,44
v_exp_f32_e32 v55, v110
Mp 172,20,44
v_exp_f32_e32 v27, v111
Mp 164,20,40
buffer_load_dwordx4 v3, s[20:23], s55 offen offset:1024 sc1 lds
Mp 176,24,40
Cv 51,51,29
v_add_f32_e32 v15, v52, v24
Mp 184,24,44
Cv 52,52,24
v_add_f32_e32 v15, v15, v53
Mp 188,28,44
v_add_f32_e32 v15, v15, v25
Cv 53,53,25
Mp 180,28,40
s_waitcnt vmcnt(5) lgkmcnt(0)
s_barrier
buffer_store_dword v6, v7, s[20:23], s49 offen offset:3072 sc0 sc1
Mp 192,32,40
Rb 0,1,0
v_add_f32_e32 v15, v15, v54
Mp 200,32,44
Rb 4,1,1024
v_add_f32_e32 v15, v15, v26
Mp 204,36,44
Rb 8,1,2048
Cv 54,54,26
Mp 196,36,40
Rb 12,1,3072
buffer_load_dwordx4 v3, s[20:23], s55 offen offset:2048 sc1 lds
Mp 208,40,40
Rb 16,1,8192
v_add_f32_e32 v15, v15, v55
Mp 216,40,44
Rb 20,1,9216
v_add_f32_e32 v15, v15, v27
Mp 220,44,44
Rb 24,1,10240
Cv 55,55,27
Mp 212,44,40
Rb 28,1,11264
s_sub_u32 s57, s57, 1
Mp 224,48,40
Rb 32,1,4096
Mp 232,48,44
Rb 36,1,5120
Mp 236,52,44
Rb 40,1,6144
Mp 228,52,40
Rb 44,1,7168
)" R"(
buffer_load_dwordx4 v3, s[20:23], s55 offen offset:3072 sc1 lds
Mp 240,56,40
v_cmp_eq_u32_e32 vcc, s3, v4
s_andn2_b64 s[28:29], -1, vcc
s_cbranch_scc1 .Lzsp21
.Lzok21:
Mp 248,56,44
Mp 252,60,44
s_waitcnt lgkmcnt(8)
Mp 244,60,40
.LzH15:
s_cbranch_execz .LzHR
.LzL:
v_mfma_f32_16x16x32_bf16 v[64:67], a[0:3], a[64:67], v[16:19]
Rb 48,1,12288
v_mfma_f32_16x16x32_bf16 v[68:71], a[0:3], a[68:71], v[20:23]
v_exp_f32_e32 v56, v112
Mv 68,4,76
Rb 52,1,13312
s_add_u32 m0, s36, 98304
Mv 64,4,72
buffer_load_dwordx4 v2, s[20:23], s56 offen offset:0 sc1 lds
Mv 64,8,80
Rb 56,1,14336
v_add_f32_e32 v14, v14, v56
Mv 68,8,84
v_exp_f32_e32 v24, v113
Mv 68,12,92
Rb 60,1,15360
s_waitcnt lgkmcnt(8)
Mv 64,12,88
v_exp_f32_e32 v57, v114
Mv 64,16,96
Rb 0,1,16384
v_add_f32_e32 v14, v14, v24
Mv 68,16,100
v_exp_f32_e32 v25, v115
Mv 68,20,108
Rb 4,1,17408
Cv 56,56,24
Mv 64,20,104
buffer_load_dwordx4 v2, s[20:23], s56 offen offset:1024 sc1 lds
Mv 64,24,112
Rb 8,1,18432
v_add_f32_e32 v14, v14, v57
Mv 68,24,116
v_exp_f32_e32 v58, v120
Mv 68,28,124
Rb 12,1,19456
s_waitcnt lgkmcnt(8)
Mv 64,28,120
v_exp_f32_e32 v26, v121
v_mfma_f32_16x16x32_bf16 v[72:75], a[32:35], a[64:67], v[16:19]
Rb 16,1,24576
v_add_f32_e32 v14, v14, v25
v_mfma_f32_16x16x32_bf16 v[76:79], a[32:35], a[68:71], v[20:23]
v_exp_f32_e32 v59, v122
Mv 76,36,76
Rb 20,1,25600
Cv 57,57,25
Mv 72,36,72
buffer_load_dwordx4 v2, s[20:23], s56 offen offset:2048 sc1 lds
Mv 72,40,80
Rb 24,1,26624
)" R"(
v_add_f32_e32 v14, v14, v58
Mv 76,40,84
v_exp_f32_e32 v27, v123
Mv 76,44,92
Rb 28,1,27648
s_waitcnt lgkmcnt(8)
Mv 72,44,88
v_exp_f32_e32 v60, v116
Mv 72,48,96
Rb 32,1,20480
v_add_f32_e32 v14, v14, v26
Mv 76,48,100
v_exp_f32_e32 v28, v117
Mv 76,52,108
Rb 36,1,21504
Cv 58,58,26
Mv 72,52,104
buffer_load_dwordx4 v2, s[20:23], s56 offen offset:3072 sc1 lds
Mv 72,56,112
Rb 40,1,22528
v_add_f32_e32 v14, v14, v59
Mv 76,56,116
v_exp_f32_e32 v61, v118
Mv 76,60,124
Rb 44,1,23552
s_waitcnt lgkmcnt(8)
Mv 72,60,120
v_exp_f32_e32 v29, v119
v_mfma_f32_16x16x32_bf16 v[80:83], a[0:3], a[64:67], v[16:19]
Rb 48,1,28672
v_add_f32_e32 v14, v14, v27
v_mfma_f32_16x16x32_bf16 v[84:87], a[0:3], a[68:71], v[20:23]
v_exp_f32_e32 v62, v124
Mv 84,4,76
Rb 52,1,29696
s_add_u32 m0, s36, 102400
Mv 80,4,72
buffer_load_dwordx4 v3, s[20:23], s56 offen offset:0 sc1 lds
Mv 80,8,80
Rb 56,1,30720
Cv 59,59,27
Mv 84,8,84
v_exp_f32_e32 v24, v125
Mv 84,12,92
Rb 60,1,31744
s_waitcnt lgkmcnt(8)
Mv 80,12,88
v_exp_f32_e32 v63, v126
Mv 80,16,96
v_exp_f32_e32 v25, v127
Mv 84,16,100
v_add_f32_e32 v15, v15, v60
v_add_f32_e32 v15, v15, v28
Mv 84,20,108
Cv 60,60,28
v_add_f32_e32 v15, v15, v61
Mv 80,20,104
buffer_load_dwordx4 v3, s[20:23], s56 offen offset:1024 sc1 lds
Mv 80,24,112
v_add_f32_e32 v15, v15, v29
Cv 61,61,29
Mv 84,24,116
v_add_f32_e32 v15, v15, v62
v_add_f32_e32 v15, v15, v24
)" R"(
Mv 84,28,124
Cv 62,62,24
v_add_f32_e32 v15, v15, v63
Mv 80,28,120
s_waitcnt vmcnt(14) lgkmcnt(0)
s_barrier
v_mfma_f32_16x16x32_bf16 v[88:91], a[32:35], a[64:67], v[16:19]
Rb 0,8,0
v_add_f32_e32 v15, v15, v25
v_mfma_f32_16x16x32_bf16 v[92:95], a[32:35], a[68:71], v[20:23]
Rb 4,8,1024
Cv 63,63,25
Mv 92,36,76
Rb 8,8,2048
v_max_f32_e32 v11, v14, v15
Mv 88,36,72
Rb 12,8,3072
buffer_load_dwordx4 v3, s[20:23], s56 offen offset:2048 sc1 lds
Mv 88,40,80
Rb 16,8,4096
v_cmp_nge_f32_e32 vcc, s62, v11
Mv 92,40,84
Rb 20,8,5120
s_add_u32 s55, s55, 0x10000
Mv 92,44,92
Rb 24,8,6144
Mv 88,44,88
Rb 28,8,7168
Mv 88,48,96
Rb 32,8,8192
.LzH16:
s_cbranch_execz .LzHR
Mv 92,48,100
Rb 36,8,9216
Mv 92,52,108
Rb 40,8,10240
Mv 88,52,104
Rb 44,8,11264
buffer_load_dwordx4 v3, s[20:23], s56 offen offset:3072 sc1 lds
Mv 88,56,112
s_cmp_eq_u32 s57, 2
s_cselect_b32 s55, s59, s55
Mv 92,56,116
s_add_u32 s56, s56, 0x10000
Mv 92,60,124
s_waitcnt lgkmcnt(8)
s_cbranch_vccnz .Lzfx1
.Lzfr1:
Mv 88,60,120
v_add_f32_e32 v12, v12, v14
v_add_f32_e32 v13, v13, v15
Mp 128,0,48
Rb 48,8,12288
Mp 136,0,52
s_cmp_le_i32 s57, s58
s_cbranch_scc1 .Lzmk2
.Lzmr2:
Mp 140,4,52
Rb 52,8,13312
s_add_u32 m0, s36, 0
Mp 132,4,48
buffer_load_dwordx4 v2, s[20:23], s55 offen offset:0 sc1 lds
Mp 144,8,48
Rb 56,8,14336
Mp 152,8,52
)" R"(
v_exp_f32_e32 v32, v64
Mp 156,12,52
Rb 60,8,15360
s_waitcnt lgkmcnt(8)
Mp 148,12,48
v_exp_f32_e32 v26, v65
Mp 160,16,48
Rb 0,8,16384
v_add_f32_e32 v14, v32, v26
Mp 168,16,52
v_exp_f32_e32 v33, v66
Mp 172,20,52
Rb 4,8,17408
Cv 32,32,26
Mp 164,20,48
buffer_load_dwordx4 v2, s[20:23], s55 offen offset:1024 sc1 lds
Mp 176,24,48
Rb 8,8,18432
v_add_f32_e32 v14, v14, v33
Mp 184,24,52
v_exp_f32_e32 v27, v67
Mp 188,28,52
Rb 12,8,19456
s_waitcnt lgkmcnt(8)
Mp 180,28,48
v_exp_f32_e32 v34, v72
Mp 192,32,48
Rb 16,8,20480
v_add_f32_e32 v14, v14, v27
Mp 200,32,52
v_exp_f32_e32 v28, v73
Mp 204,36,52
Rb 20,8,21504
Cv 33,33,27
Mp 196,36,48
buffer_load_dwordx4 v2, s[20:23], s55 offen offset:2048 sc1 lds
Mp 208,40,48
Rb 24,8,22528
v_add_f32_e32 v14, v14, v34
Mp 216,40,52
v_exp_f32_e32 v35, v74
Mp 220,44,52
Rb 28,8,23552
s_waitcnt lgkmcnt(8)
Mp 212,44,48
v_exp_f32_e32 v29, v75
Mp 224,48,48
Rb 32,8,24576
v_add_f32_e32 v14, v14, v28
Mp 232,48,52
v_exp_f32_e32 v36, v68
Mp 236,52,52
Rb 36,8,25600
Cv 34,34,28
Mp 228,52,48
buffer_load_dwordx4 v2, s[20:23], s55 offen offset:3072 sc1 lds
Mp 240,56,48
Rb 40,8,26624
v_add_f32_e32 v14, v14, v35
Mp 248,56,52
v_exp_f32_e32 v24, v69
Mp 252,60,52
Rb 44,8,27648
s_waitcnt lgkmcnt(8)
)" R"(
Mp 244,60,48
v_exp_f32_e32 v37, v70
Mp 128,0,56
Rb 48,8,28672
v_add_f32_e32 v14, v14, v29
Mp 136,0,60
v_exp_f32_e32 v25, v71
Mp 140,4,60
Rb 52,8,29696
s_add_u32 m0, s36, 4096
Mp 132,4,56
buffer_load_dwordx4 v3, s[20:23], s55 offen offset:0 sc1 lds
Mp 144,8,56
Rb 56,8,30720
Cv 35,35,29
Mp 152,8,60
v_exp_f32_e32 v38, v76
Mp 156,12,60
Rb 60,8,31744
s_waitcnt lgkmcnt(8)
Mp 148,12,56
v_exp_f32_e32 v26, v77
Mp 160,16,56
v_exp_f32_e32 v39, v78
Mp 168,16,60
v_exp_f32_e32 v27, v79
Mp 172,20,60
v_add_f32_e32 v15, v36, v24
Cv 36,36,24
Mp 164,20,56
buffer_load_dwordx4 v3, s[20:23], s55 offen offset:1024 sc1 lds
Mp 176,24,56
s_add_u32 s50, s50, 1024
s_min_u32 s50, s50, s60
Mp 184,24,60
v_add_f32_e32 v15, v15, v37
v_add_f32_e32 v15, v15, v25
Mp 188,28,60
Cv 37,37,25
v_add_f32_e32 v15, v15, v38
Mp 180,28,56
s_waitcnt vmcnt(14) lgkmcnt(0)
s_barrier
Mp 192,32,56
Rb 0,1,32768
v_add_f32_e32 v15, v15, v26
Mp 200,32,60
Rb 4,1,33792
Cv 38,38,26
Mp 204,36,60
Rb 8,1,34816
v_add_f32_e32 v15, v15, v39
Mp 196,36,56
Rb 12,1,35840
buffer_load_dwordx4 v3, s[20:23], s55 offen offset:2048 sc1 lds
Mp 208,40,56
Rb 16,1,40960
v_add_f32_e32 v15, v15, v27
Mp 216,40,60
Rb 20,1,41984
Cv 39,39,27
Mp 220,44,60
Rb 24,1,43008
s_sub_u32 s57, s57, 1
Mp 212,44,56
)" R"(
Rb 28,1,44032
Mp 224,48,56
Rb 32,1,36864
Mp 232,48,60
Rb 36,1,37888
Mp 236,52,60
Rb 40,1,38912
Mp 228,52,56
Rb 44,1,39936
buffer_load_dwordx4 v3, s[20:23], s55 offen offset:3072 sc1 lds
Mp 240,56,56
Mp 248,56,60
Mp 252,60,60
s_waitcnt lgkmcnt(8)
Mp 244,60,56
v_mfma_f32_16x16x32_bf16 v[96:99], a[0:3], a[64:67], v[16:19]
Rb 48,1,45056
buffer_load_dword v4, v5, s[20:23], s50 offen offset:0 sc0 sc1
v_mfma_f32_16x16x32_bf16 v[100:103], a[0:3], a[68:71], v[20:23]
v_exp_f32_e32 v40, v80
Mv 100,4,76
Rb 52,1,46080
s_add_u32 m0, s36, 65536
Mv 96,4,72
buffer_load_dwordx4 v2, s[20:23], s56 offen offset:0 sc1 lds
Mv 96,8,80
Rb 56,1,47104
v_add_f32_e32 v14, v14, v40
Mv 100,8,84
v_exp_f32_e32 v28, v81
Mv 100,12,92
Rb 60,1,48128
s_waitcnt lgkmcnt(8)
Mv 96,12,88
v_exp_f32_e32 v41, v82
Mv 96,16,96
Rb 0,1,49152
v_add_f32_e32 v14, v14, v28
Mv 100,16,100
v_exp_f32_e32 v29, v83
Mv 100,20,108
Rb 4,1,50176
Cv 40,40,28
Mv 96,20,104
buffer_load_dwordx4 v2, s[20:23], s56 offen offset:1024 sc1 lds
Mv 96,24,112
Rb 8,1,51200
v_add_f32_e32 v14, v14, v41
Mv 100,24,116
v_exp_f32_e32 v42, v88
Mv 100,28,124
Rb 12,1,52224
s_waitcnt lgkmcnt(8)
Mv 96,28,120
v_exp_f32_e32 v24, v89
v_mfma_f32_16x16x32_bf16 v[104:107], a[32:35], a[64:67], v[16:19]
Rb 16,1,57344
v_add_f32_e32 v14, v14, v29
v_mfma_f32_16x16x32_bf16 v[108:111], a[32:35], a[68:71], v[20:23]
v_exp_f32_e32 v43, v90
Mv 108,36,76
Rb 20,1,58368
Cv 41,41,29
Mv 104,36,72
)" R"(
buffer_load_dwordx4 v2, s[20:23], s56 offen offset:2048 sc1 lds
Mv 104,40,80
Rb 24,1,59392
v_add_f32_e32 v14, v14, v42
Mv 108,40,84
v_exp_f32_e32 v25, v91
Mv 108,44,92
Rb 28,1,60416
s_waitcnt lgkmcnt(8)
Mv 104,44,88
v_exp_f32_e32 v44, v84
Mv 104,48,96
Rb 32,1,53248
v_add_f32_e32 v14, v14, v24
Mv 108,48,100
v_exp_f32_e32 v26, v85
Mv 108,52,108
Rb 36,1,54272
Cv 42,42,24
Mv 104,52,104
buffer_load_dwordx4 v2, s[20:23], s56 offen offset:3072 sc1 lds
Mv 104,56,112
Rb 40,1,55296
v_add_f32_e32 v14, v14, v43
Mv 108,56,116
v_exp_f32_e32 v45, v86
Mv 108,60,124
Rb 44,1,56320
s_waitcnt lgkmcnt(8)
Mv 104,60,120
v_exp_f32_e32 v27, v87
v_mfma_f32_16x16x32_bf16 v[112:115], a[0:3], a[64:67], v[16:19]
Rb 48,1,61440
v_add_f32_e32 v14, v14, v25
v_mfma_f32_16x16x32_bf16 v[116:119], a[0:3], a[68:71], v[20:23]
v_exp_f32_e32 v46, v92
Mv 116,4,76
Rb 52,1,62464
s_add_u32 m0, s36, 69632
Mv 112,4,72
buffer_load_dwordx4 v3, s[20:23], s56 offen offset:0 sc1 lds
Mv 112,8,80
Rb 56,1,63488
Cv 43,43,25
Mv 116,8,84
v_exp_f32_e32 v28, v93
Mv 116,12,92
Rb 60,1,64512
s_waitcnt lgkmcnt(8)
Mv 112,12,88
v_exp_f32_e32 v47, v94
Mv 112,16,96
v_exp_f32_e32 v29, v95
Mv 116,16,100
v_add_f32_e32 v15, v15, v44
v_add_f32_e32 v15, v15, v26
Mv 116,20,108
Cv 44,44,26
v_add_f32_e32 v15, v15, v45
Mv 112,20,104
buffer_load_dwordx4 v3, s[20:23], s56 offen offset:1024 sc1 lds
Mv 112,24,112
v_add_f32_e32 v15, v15, v27
Cv 45,45,27
)" R"(
Mv 116,24,116
v_add_f32_e32 v15, v15, v46
v_add_f32_e32 v15, v15, v28
Mv 116,28,124
Cv 46,46,28
v_add_f32_e32 v15, v15, v47
Mv 112,28,120
s_waitcnt vmcnt(15) lgkmcnt(0)
s_barrier
v_mfma_f32_16x16x32_bf16 v[120:123], a[32:35], a[64:67], v[16:19]
Rb 0,8,32768
v_add_f32_e32 v15, v15, v29
v_mfma_f32_16x16x32_bf16 v[124:127], a[32:35], a[68:71], v[20:23]
Rb 4,8,33792
Cv 47,47,29
Mv 124,36,76
Rb 8,8,34816
v_max_f32_e32 v11, v14, v15
Mv 120,36,72
Rb 12,8,35840
buffer_load_dwordx4 v3, s[20:23], s56 offen offset:2048 sc1 lds
Mv 120,40,80
Rb 16,8,36864
v_cmp_nge_f32_e32 vcc, s62, v11
Mv 124,40,84
Rb 20,8,37888
s_add_u32 s55, s55, 0x10000
Mv 124,44,92
Rb 24,8,38912
Mv 120,44,88
Rb 28,8,39936
Mv 120,48,96
Rb 32,8,40960
Mv 124,48,100
Rb 36,8,41984
Mv 124,52,108
Rb 40,8,43008
Mv 120,52,104
Rb 44,8,44032
buffer_load_dwordx4 v3, s[20:23], s56 offen offset:3072 sc1 lds
Mv 120,56,112
s_cmp_eq_u32 s57, 2
s_cselect_b32 s55, s59, s55
Mv 124,56,116
s_add_u32 s56, s56, 0x10000
Mv 124,60,124
s_waitcnt lgkmcnt(8)
s_cbranch_vccnz .Lzfx2
.Lzfr2:
Mv 120,60,120
v_add_f32_e32 v12, v12, v14
v_add_f32_e32 v13, v13, v15
Mp 128,0,32
Rb 48,8,45056
Mp 136,0,36
s_cmp_le_i32 s57, s58
s_cbranch_scc1 .Lzmk3
.Lzmr3:
Mp 140,4,36
Rb 52,8,46080
s_add_u32 m0, s36, 32768
Mp 132,4,32
buffer_load_dwordx4 v2, s[20:23], s55 offen offset:0 sc1 lds
Mp 144,8,32
Rb 56,8,47104
)" R"(
Mp 152,8,36
v_exp_f32_e32 v48, v96
Mp 156,12,36
Rb 60,8,48128
s_waitcnt lgkmcnt(8)
Mp 148,12,32
v_exp_f32_e32 v24, v97
Mp 160,16,32
Rb 0,8,49152
v_add_f32_e32 v14, v48, v24
Mp 168,16,36
v_exp_f32_e32 v49, v98
Mp 172,20,36
Rb 4,8,50176
Cv 48,48,24
Mp 164,20,32
buffer_load_dwordx4 v2, s[20:23], s55 offen offset:1024 sc1 lds
Mp 176,24,32
Rb 8,8,51200
v_add_f32_e32 v14, v14, v49
Mp 184,24,36
v_exp_f32_e32 v25, v99
Mp 188,28,36
Rb 12,8,52224
s_waitcnt lgkmcnt(8)
Mp 180,28,32
v_exp_f32_e32 v50, v104
Mp 192,32,32
Rb 16,8,53248
v_add_f32_e32 v14, v14, v25
Mp 200,32,36
v_exp_f32_e32 v26, v105
Mp 204,36,36
Rb 20,8,54272
Cv 49,49,25
Mp 196,36,32
buffer_load_dwordx4 v2, s[20:23], s55 offen offset:2048 sc1 lds
Mp 208,40,32
Rb 24,8,55296
v_add_f32_e32 v14, v14, v50
Mp 216,40,36
v_exp_f32_e32 v51, v106
Mp 220,44,36
Rb 28,8,56320
s_waitcnt lgkmcnt(8)
Mp 212,44,32
v_exp_f32_e32 v27, v107
Mp 224,48,32
Rb 32,8,57344
v_add_f32_e32 v14, v14, v26
Mp 232,48,36
v_exp_f32_e32 v52, v100
Mp 236,52,36
Rb 36,8,58368
Cv 50,50,26
Mp 228,52,32
buffer_load_dwordx4 v2, s[20:23], s55 offen offset:3072 sc1 lds
Mp 240,56,32
Rb 40,8,59392
v_add_f32_e32 v14, v14, v51
Mp 248,56,36
v_exp_f32_e32 v28, v101
Mp 252,60,36
Rb 44,8,60416
)" R"(
s_waitcnt lgkmcnt(8)
Mp 244,60,32
v_exp_f32_e32 v53, v102
Mp 128,0,40
Rb 48,8,61440
v_add_f32_e32 v14, v14, v27
Mp 136,0,44
v_exp_f32_e32 v29, v103
Mp 140,4,44
Rb 52,8,62464
s_add_u32 m0, s36, 36864
Mp 132,4,40
buffer_load_dwordx4 v3, s[20:23], s55 offen offset:0 sc1 lds
Mp 144,8,40
Rb 56,8,63488
Cv 51,51,27
Mp 152,8,44
v_exp_f32_e32 v54, v108
Mp 156,12,44
Rb 60,8,64512
s_waitcnt lgkmcnt(8)
Mp 148,12,40
v_exp_f32_e32 v24, v109
Mp 160,16,40
v_exp_f32_e32 v55, v110
Mp 168,16,44
v_exp_f32_e32 v25, v111
Mp 172,20,44
v_add_f32_e32 v15, v52, v28
Cv 52,52,28
Mp 164,20,40
buffer_load_dwordx4 v3, s[20:23], s55 offen offset:1024 sc1 lds
Mp 176,24,40
v_add_f32_e32 v15, v15, v53
v_add_f32_e32 v15, v15, v29
Mp 184,24,44
Cv 53,53,29
v_add_f32_e32 v15, v15, v54
Mp 188,28,44
v_add_f32_e32 v15, v15, v24
Cv 54,54,24
Mp 180,28,40
s_waitcnt vmcnt(14) lgkmcnt(0)
s_barrier
Mp 192,32,40
Rb 0,1,0
v_add_f32_e32 v15, v15, v55
Mp 200,32,44
Rb 4,1,1024
v_add_f32_e32 v15, v15, v25
Mp 204,36,44
Rb 8,1,2048
Cv 55,55,25
Mp 196,36,40
Rb 12,1,3072
buffer_load_dwordx4 v3, s[20:23], s55 offen offset:2048 sc1 lds
Mp 208,40,40
Rb 16,1,8192
s_sub_u32 s57, s57, 1
Mp 216,40,44
Rb 20,1,9216
Mp 220,44,44
Rb 24,1,10240
Mp 212,44,40
Rb 28,1,11264
)" R"(
Mp 224,48,40
Rb 32,1,4096
Mp 232,48,44
Rb 36,1,5120
Mp 236,52,44
Rb 40,1,6144
Mp 228,52,40
Rb 44,1,7168
buffer_load_dwordx4 v3, s[20:23], s55 offen offset:3072 sc1 lds
Mp 240,56,40
v_cmp_eq_u32_e32 vcc, s3, v4
s_andn2_b64 s[28:29], -1, vcc
s_cbranch_scc1 .Lzsp9
.Lzok9:
Mp 248,56,44
Mp 252,60,44
s_waitcnt lgkmcnt(8)
Mp 244,60,40
s_cmp_gt_i32 s57, s93
s_cbranch_scc1 .LzL
s_cmp_eq_u32 s94, 1
s_cbranch_scc1 .LzW
s_cmp_eq_u32 s94, 2
s_cbranch_scc1 .LzDA
.LzEB:
v_mfma_f32_16x16x32_bf16 v[64:67], a[0:3], a[64:67], v[16:19]
Rb 48,1,12288
s_mov_b32 s83, 0x1508000
buffer_load_dword v4, v7, s[20:23], s83 offen offset:4 sc0 sc1
v_mfma_f32_16x16x32_bf16 v[68:71], a[0:3], a[68:71], v[20:23]
s_cmp_lg_u32 s35, 7
s_cselect_b64 s[60:61], 1, 0
Mv 68,4,76
Rb 52,1,13312
s_add_u32 m0, s36, 98304
Mv 64,4,72
buffer_load_dwordx4 v2, s[20:23], s56 offen offset:0 sc1 lds
Mv 64,8,80
Rb 56,1,14336
Mv 68,8,84
v_exp_f32_e32 v56, v112
Mv 68,12,92
Rb 60,1,15360
s_waitcnt lgkmcnt(8)
Mv 64,12,88
v_exp_f32_e32 v28, v113
Mv 64,16,96
Rb 0,1,16384
v_add_f32_e32 v14, v14, v56
Mv 68,16,100
v_exp_f32_e32 v57, v114
Mv 68,20,108
Rb 4,1,17408
v_add_f32_e32 v14, v14, v28
Mv 64,20,104
buffer_load_dwordx4 v2, s[20:23], s56 offen offset:1024 sc1 lds
Mv 64,24,112
Rb 8,1,18432
Cv 56,56,28
Mv 68,24,116
v_exp_f32_e32 v29, v115
Mv 68,28,124
Rb 12,1,19456
s_waitcnt lgkmcnt(8)
)" R"(
Mv 64,28,120
v_exp_f32_e32 v58, v120
v_mfma_f32_16x16x32_bf16 v[72:75], a[32:35], a[64:67], v[16:19]
Rb 16,1,24576
v_add_f32_e32 v14, v14, v57
v_mfma_f32_16x16x32_bf16 v[76:79], a[32:35], a[68:71], v[20:23]
v_exp_f32_e32 v24, v121
Mv 76,36,76
Rb 20,1,25600
v_add_f32_e32 v14, v14, v29
Mv 72,36,72
buffer_load_dwordx4 v2, s[20:23], s56 offen offset:2048 sc1 lds
Mv 72,40,80
Rb 24,1,26624
Cv 57,57,29
Mv 76,40,84
v_exp_f32_e32 v59, v122
Mv 76,44,92
Rb 28,1,27648
s_waitcnt lgkmcnt(8)
Mv 72,44,88
v_exp_f32_e32 v25, v123
Mv 72,48,96
Rb 32,1,20480
v_add_f32_e32 v14, v14, v58
Mv 76,48,100
v_exp_f32_e32 v60, v116
Mv 76,52,108
Rb 36,1,21504
v_add_f32_e32 v14, v14, v24
Mv 72,52,104
buffer_load_dwordx4 v2, s[20:23], s56 offen offset:3072 sc1 lds
Mv 72,56,112
Rb 40,1,22528
Cv 58,58,24
Mv 76,56,116
v_exp_f32_e32 v26, v117
Mv 76,60,124
Rb 44,1,23552
s_waitcnt lgkmcnt(8)
Mv 72,60,120
v_exp_f32_e32 v61, v118
v_mfma_f32_16x16x32_bf16 v[80:83], a[0:3], a[64:67], v[16:19]
Rb 48,1,28672
v_add_f32_e32 v14, v14, v59
v_mfma_f32_16x16x32_bf16 v[84:87], a[0:3], a[68:71], v[20:23]
v_exp_f32_e32 v27, v119
Mv 84,4,76
Rb 52,1,29696
s_add_u32 m0, s36, 102400
Mv 80,4,72
buffer_load_dwordx4 v3, s[20:23], s56 offen offset:0 sc1 lds
Mv 80,8,80
Rb 56,1,30720
v_add_f32_e32 v14, v14, v25
Mv 84,8,84
v_exp_f32_e32 v62, v124
.LzH17:
s_cbranch_execz .LzHR
Mv 84,12,92
Rb 60,1,31744
s_waitcnt lgkmcnt(8)
Mv 80,12,88
v_exp_f32_e32 v28, v125
)" R"(
Mv 80,16,96
v_exp_f32_e32 v63, v126
Mv 84,16,100
v_exp_f32_e32 v29, v127
Mv 84,20,108
Cv 59,59,25
v_add_f32_e32 v15, v15, v60
Mv 80,20,104
buffer_load_dwordx4 v3, s[20:23], s56 offen offset:1024 sc1 lds
Mv 80,24,112
v_add_f32_e32 v15, v15, v26
Cv 60,60,26
Mv 84,24,116
v_add_f32_e32 v15, v15, v61
v_add_f32_e32 v15, v15, v27
Mv 84,28,124
Cv 61,61,27
v_add_f32_e32 v15, v15, v62
Mv 80,28,120
s_waitcnt vmcnt(15) lgkmcnt(0)
s_barrier
v_mfma_f32_16x16x32_bf16 v[88:91], a[32:35], a[64:67], v[16:19]
Rb 0,8,0
v_add_f32_e32 v15, v15, v28
v_mfma_f32_16x16x32_bf16 v[92:95], a[32:35], a[68:71], v[20:23]
Rb 4,8,1024
Cv 62,62,28
Mv 92,36,76
Rb 8,8,2048
v_add_f32_e32 v15, v15, v63
Mv 88,36,72
Rb 12,8,3072
buffer_load_dwordx4 v3, s[20:23], s56 offen offset:2048 sc1 lds
Mv 88,40,80
Rb 16,8,4096
v_add_f32_e32 v15, v15, v29
Mv 92,40,84
Rb 20,8,5120
Cv 63,63,29
Mv 92,44,92
Rb 24,8,6144
v_max_f32_e32 v11, v14, v15
Mv 88,44,88
Rb 28,8,7168
v_cmp_nge_f32_e32 vcc, s62, v11
Mv 88,48,96
Rb 32,8,8192
s_add_u32 s55, s55, 0x10000
Mv 92,48,100
Rb 36,8,9216
Mv 92,52,108
Rb 40,8,10240
Mv 88,52,104
Rb 44,8,11264
buffer_load_dwordx4 v3, s[20:23], s56 offen offset:3072 sc1 lds
Mv 88,56,112
s_cmp_eq_u32 s57, 2
s_cselect_b32 s55, s59, s55
Mv 92,56,116
s_add_u32 s56, s56, 0x10000
Mv 92,60,124
s_waitcnt lgkmcnt(8)
s_cbranch_vccnz .Lzfx11
.Lzfr11:
Mv 88,60,120
)" R"(
s_waitcnt vmcnt(8)
v_cmp_eq_u32_e32 vcc, s3, v4
s_andn2_b64 s[28:29], 1, vcc
s_cbranch_scc1 .Lzsp22
.Lzok22:
Mp 128,0,48
Rb 48,8,12288
buffer_load_dword v9, v7, s[20:23], s83 offen sc0 sc1
Mp 136,0,52
v_add_f32_e32 v12, v12, v14
s_cmp_le_i32 s57, s58
s_cbranch_scc1 .Lzmk10
.Lzmr10:
Mp 140,4,52
Rb 52,8,13312
s_add_u32 m0, s36, 0
v_add_f32_e32 v13, v13, v15
Mp 132,4,48
buffer_load_dwordx4 v2, s[20:23], s55 offen offset:0 sc1 lds
Mp 144,8,48
Rb 56,8,14336
Mp 152,8,52
v_exp_f32_e32 v32, v64
Mp 156,12,52
Rb 60,8,15360
s_waitcnt lgkmcnt(8)
Mp 148,12,48
v_exp_f32_e32 v24, v65
Mp 160,16,48
Rb 0,8,16384
v_add_f32_e32 v14, v32, v24
Mp 168,16,52
v_exp_f32_e32 v33, v66
Cv 32,32,24
Mp 172,20,52
Rb 4,8,17408
v_add_f32_e32 v14, v14, v33
Mp 164,20,48
buffer_load_dwordx4 v2, s[20:23], s55 offen offset:1024 sc1 lds
Mp 176,24,48
Rb 8,8,18432
Mp 184,24,52
v_exp_f32_e32 v25, v67
Mp 188,28,52
Rb 12,8,19456
s_waitcnt lgkmcnt(8)
Mp 180,28,48
v_exp_f32_e32 v34, v72
Mp 192,32,48
Rb 16,8,20480
v_add_f32_e32 v14, v14, v25
Mp 200,32,52
v_exp_f32_e32 v26, v73
Cv 33,33,25
Mp 204,36,52
Rb 20,8,21504
v_add_f32_e32 v14, v14, v34
Mp 196,36,48
buffer_load_dwordx4 v2, s[20:23], s55 offen offset:2048 sc1 lds
Mp 208,40,48
Rb 24,8,22528
v_add_f32_e32 v14, v14, v26
Mp 216,40,52
v_exp_f32_e32 v35, v74
)" R"(
Mp 220,44,52
Rb 28,8,23552
s_waitcnt lgkmcnt(8)
Mp 212,44,48
v_exp_f32_e32 v27, v75
Mp 224,48,48
Rb 32,8,24576
Cv 34,34,26
Mp 232,48,52
v_exp_f32_e32 v36, v68
v_add_f32_e32 v14, v14, v35
Mp 236,52,52
Rb 36,8,25600
v_add_f32_e32 v14, v14, v27
Mp 228,52,48
buffer_load_dwordx4 v2, s[20:23], s55 offen offset:3072 sc1 lds
Mp 240,56,48
Rb 40,8,26624
Cv 35,35,27
Mp 248,56,52
v_exp_f32_e32 v28, v69
Mp 252,60,52
Rb 44,8,27648
s_waitcnt lgkmcnt(8)
Mp 244,60,48
v_exp_f32_e32 v37, v70
Mp 128,0,56
Rb 48,8,28672
v_add_f32_e32 v15, v36, v28
Mp 136,0,60
v_exp_f32_e32 v29, v71
Cv 36,36,28
Mp 140,4,60
Rb 52,8,29696
s_add_u32 m0, s36, 4096
Mp 132,4,56
buffer_load_dwordx4 v3, s[20:23], s55 offen offset:0 sc1 lds
Mp 144,8,56
Rb 56,8,30720
v_add_f32_e32 v15, v15, v37
Mp 152,8,60
v_exp_f32_e32 v38, v76
Mp 156,12,60
Rb 60,8,31744
s_waitcnt lgkmcnt(8)
Mp 148,12,56
v_exp_f32_e32 v24, v77
Mp 160,16,56
v_exp_f32_e32 v39, v78
Mp 168,16,60
v_exp_f32_e32 v25, v79
v_add_f32_e32 v15, v15, v29
Mp 172,20,60
Cv 37,37,29
v_add_f32_e32 v15, v15, v38
Mp 164,20,56
buffer_load_dwordx4 v3, s[20:23], s55 offen offset:1024 sc1 lds
Mp 176,24,56
v_add_f32_e32 v15, v15, v24
Cv 38,38,24
Mp 184,24,60
v_add_f32_e32 v15, v15, v39
v_add_f32_e32 v15, v15, v25
Mp 188,28,60
)" R"(
Cv 39,39,25
s_sub_u32 s57, s57, 1
Mp 180,28,56
s_waitcnt lgkmcnt(0)
s_barrier
Mp 192,32,56
Rb 0,1,32768
v_bfe_u32 v11, v0, 2, 2
Mp 200,32,60
Rb 4,1,33792
v_lshrrev_b32 v4, 4, v0
v_xor_b32 v11, v11, v4
Mp 204,36,60
Rb 8,1,34816
v_bfe_u32 v4, v0, 1, 1
Mp 196,36,56
Rb 12,1,35840
buffer_load_dwordx4 v3, s[20:23], s55 offen offset:2048 sc1 lds
Mp 208,40,56
Rb 16,1,40960
v_lshl_or_b32 v11, v11, 1, v4
Mp 216,40,60
Rb 20,1,41984
v_and_b32 v4, 1, v0
Mp 220,44,60
Rb 24,1,43008
v_mul_u32_u24 v4, 0xffff, v4
Mp 212,44,56
Rb 28,1,44032
v_add_u32 v4, 1, v4
Mp 224,48,56
Rb 32,1,36864
v_mul_u32_u24 v4, 0x3f80, v4
Mp 232,48,60
Rb 36,1,37888
v_cmp_eq_u32_e32 vcc, 0, v11
v_cmp_eq_u32_e64 s[28:29], 1, v11
Mp 236,52,60
Rb 40,1,38912
v_accvgpr_write_b32 a192, 0
Mp 228,52,56
Rb 44,1,39936
buffer_load_dwordx4 v3, s[20:23], s55 offen offset:3072 sc1 lds
Mp 240,56,56
v_accvgpr_write_b32 a193, 0
v_accvgpr_write_b32 a196, 0
Mp 248,56,60
v_accvgpr_write_b32 a197, 0
v_cndmask_b32_e32 v11, 0, v4, vcc
Mp 252,60,60
v_cndmask_b32_e64 v4, 0, v4, s[28:29]
s_waitcnt lgkmcnt(8)
Mp 244,60,56
v_accvgpr_write_b32 a194, v11
v_accvgpr_write_b32 a195, v4
v_mfma_f32_16x16x32_bf16 v[96:99], a[0:3], a[64:67], v[16:19]
Rb 48,1,45056
v_mfma_f32_16x16x32_bf16 v[100:103], a[0:3], a[68:71], v[20:23]
buffer_load_dword v4, v7, s[20:23], s65 offen offset:0 sc0 sc1
Mv 100,4,76
Rb 52,1,46080
s_add_u32 m0, s36, 65536
Mv 96,4,72
buffer_load_dwordx4 v2, s[20:23], s56 offen offset:0 sc1 lds
Mv 96,8,80
)" R"(
Rb 56,1,47104
Mv 100,8,84
v_exp_f32_e32 v40, v80
Mv 100,12,92
Rb 60,1,48128
s_waitcnt lgkmcnt(8)
Mv 96,12,88
v_exp_f32_e32 v26, v81
v_add_f32_e32 v14, v14, v40
Mv 96,16,96
Rb 0,1,49152
v_add_f32_e32 v14, v14, v26
Mv 100,16,100
v_exp_f32_e32 v41, v82
Mv 100,20,108
Rb 4,1,50176
Cv 40,40,26
Mv 96,20,104
buffer_load_dwordx4 v2, s[20:23], s56 offen offset:1024 sc1 lds
Mv 96,24,112
Rb 8,1,51200
v_add_f32_e32 v14, v14, v41
Mv 100,24,116
v_exp_f32_e32 v27, v83
Mv 100,28,124
Rb 12,1,52224
s_waitcnt lgkmcnt(8)
Mv 96,28,120
v_exp_f32_e32 v42, v88
v_add_f32_e32 v14, v14, v27
v_mfma_f32_16x16x32_bf16 v[104:107], a[32:35], a[64:67], v[16:19]
Rb 16,1,57344
Cv 41,41,27
v_mfma_f32_16x16x32_bf16 v[108:111], a[32:35], a[68:71], v[20:23]
v_exp_f32_e32 v28, v89
Mv 108,36,76
Rb 20,1,58368
v_add_f32_e32 v14, v14, v42
Mv 104,36,72
buffer_load_dwordx4 v2, s[20:23], s56 offen offset:2048 sc1 lds
Mv 104,40,80
Rb 24,1,59392
v_add_f32_e32 v14, v14, v28
Mv 108,40,84
v_exp_f32_e32 v43, v90
Mv 108,44,92
Rb 28,1,60416
s_waitcnt lgkmcnt(8)
Mv 104,44,88
v_exp_f32_e32 v29, v91
Cv 42,42,28
Mv 104,48,96
Rb 32,1,53248
v_add_f32_e32 v14, v14, v43
Mv 108,48,100
v_exp_f32_e32 v44, v84
Mv 108,52,108
Rb 36,1,54272
v_add_f32_e32 v14, v14, v29
Mv 104,52,104
buffer_load_dwordx4 v2, s[20:23], s56 offen offset:3072 sc1 lds
Mv 104,56,112
Rb 40,1,55296
Cv 43,43,29
)" R"(
Mv 108,56,116
v_exp_f32_e32 v24, v85
Mv 108,60,124
Rb 44,1,56320
s_waitcnt lgkmcnt(8)
Mv 104,60,120
v_exp_f32_e32 v45, v86
v_add_f32_e32 v15, v15, v44
v_mfma_f32_16x16x32_bf16 v[112:115], a[0:3], a[64:67], v[16:19]
Rb 48,1,61440
v_add_f32_e32 v15, v15, v24
v_mfma_f32_16x16x32_bf16 v[116:119], a[0:3], a[68:71], v[20:23]
v_exp_f32_e32 v25, v87
Mv 116,4,76
Rb 52,1,62464
s_add_u32 m0, s36, 69632
Mv 112,4,72
buffer_load_dwordx4 v3, s[20:23], s56 offen offset:0 sc1 lds
Mv 112,8,80
Rb 56,1,63488
Cv 44,44,24
Mv 116,8,84
v_exp_f32_e32 v46, v92
Mv 116,12,92
Rb 60,1,64512
s_waitcnt lgkmcnt(8)
Mv 112,12,88
v_exp_f32_e32 v26, v93
v_add_f32_e32 v15, v15, v45
Mv 112,16,96
v_exp_f32_e32 v47, v94
Mv 116,16,100
v_exp_f32_e32 v27, v95
Mv 116,20,108
v_add_f32_e32 v15, v15, v25
Cv 45,45,25
Mv 112,20,104
buffer_load_dwordx4 v3, s[20:23], s56 offen offset:1024 sc1 lds
Mv 112,24,112
v_add_f32_e32 v15, v15, v46
v_add_f32_e32 v15, v15, v26
Mv 116,24,116
Cv 46,46,26
v_add_f32_e32 v15, v15, v47
Mv 116,28,124
v_add_f32_e32 v15, v15, v27
Cv 47,47,27
Mv 112,28,120
v_max_f32_e32 v11, v14, v15
s_waitcnt vmcnt(6) lgkmcnt(0)
s_barrier
v_mfma_f32_16x16x32_bf16 v[120:123], a[32:35], a[64:67], v[16:19]
Rb 0,8,32768
v_cmp_nge_f32_e32 vcc, s62, v11
v_mfma_f32_16x16x32_bf16 v[124:127], a[32:35], a[68:71], v[20:23]
Rb 4,8,33792
s_add_u32 s55, s55, 0x10000
Mv 124,36,76
Rb 8,8,34816
Mv 120,36,72
Rb 12,8,35840
buffer_load_dwordx4 v3, s[20:23], s56 offen offset:2048 sc1 lds
Mv 120,40,80
Rb 16,8,36864
Mv 124,40,84
)" R"(
Rb 20,8,37888
Mv 124,44,92
Rb 24,8,38912
Mv 120,44,88
Rb 28,8,39936
s_cmp_eq_u32 s57, 2
s_cselect_b32 s55, s59, s55
Mv 120,48,96
Rb 32,8,40960
Mv 124,48,100
Rb 36,8,41984
Mv 124,52,108
Rb 40,8,43008
Mv 120,52,104
Rb 44,8,44032
buffer_load_dwordx4 v3, s[20:23], s56 offen offset:3072 sc1 lds
Mv 120,56,112
v_readfirstlane_b32 s63, v9
Mv 124,56,116
s_add_u32 s56, s56, 0x10000
Mv 124,60,124
s_waitcnt lgkmcnt(8)
s_cbranch_vccnz .Lzfx12
.Lzfr12:
Mv 120,60,120
v_cmp_eq_u32_e32 vcc, s3, v4
s_andn2_b64 s[28:29], s[60:61], vcc
s_cbranch_scc1 .Lzsp23
.Lzok23:
Mp 128,0,32
Rb 48,8,45056
v_add_f32_e32 v12, v12, v14
Mp 136,0,36
v_add_f32_e32 v13, v13, v15
s_cmp_le_i32 s57, s58
s_cbranch_scc1 .Lzmk11
.Lzmr11:
Mp 140,4,36
Rb 52,8,46080
s_add_u32 m0, s36, 32768
buffer_load_dwordx4 v[64:67], v1, s[20:23], s66 offen sc0 sc1
Mp 132,4,32
buffer_load_dwordx4 v2, s[20:23], s55 offen offset:0 sc1 lds
Mp 144,8,32
Rb 56,8,47104
Mp 152,8,36
buffer_load_dwordx4 a[128:131], v2, s[20:23], s64 offen offset:0 sc0 sc1
Mp 156,12,36
Rb 60,8,48128
s_waitcnt lgkmcnt(8)
Mp 148,12,32
buffer_load_dwordx4 a[132:135], v2, s[20:23], s64 offen offset:1024 sc0 sc1
Mp 160,16,32
Rb 0,8,49152
Mp 168,16,36
buffer_load_dwordx4 a[136:139], v2, s[20:23], s64 offen offset:2048 sc0 sc1
Mp 172,20,36
Rb 4,8,50176
v_exp_f32_e32 v48, v96
Mp 164,20,32
buffer_load_dwordx4 v2, s[20:23], s55 offen offset:1024 sc1 lds
Mp 176,24,32
Rb 8,8,51200
v_exp_f32_e32 v28, v97
)" R"(
Mp 184,24,36
buffer_load_dwordx4 a[140:143], v2, s[20:23], s64 offen offset:3072 sc0 sc1
Mp 188,28,36
Rb 12,8,52224
v_add_f32_e32 v14, v48, v28
s_waitcnt lgkmcnt(8)
Mp 180,28,32
buffer_load_dwordx4 a[144:147], v3, s[20:23], s64 offen offset:0 sc0 sc1
Mp 192,32,32
Rb 16,8,53248
v_exp_f32_e32 v49, v98
Mp 200,32,36
buffer_load_dwordx4 a[148:151], v3, s[20:23], s64 offen offset:1024 sc0 sc1
Cv 48,48,28
Mp 204,36,36
Rb 20,8,54272
v_add_f32_e32 v14, v14, v49
Mp 196,36,32
buffer_load_dwordx4 v2, s[20:23], s55 offen offset:2048 sc1 lds
Mp 208,40,32
Rb 24,8,55296
Mp 216,40,36
buffer_load_dwordx4 a[152:155], v3, s[20:23], s64 offen offset:2048 sc0 sc1
Mp 220,44,36
Rb 28,8,56320
s_waitcnt lgkmcnt(8)
Mp 212,44,32
buffer_load_dwordx4 a[156:159], v3, s[20:23], s64 offen offset:3072 sc0 sc1
Mp 224,48,32
Rb 32,8,57344
Mp 232,48,36
s_add_u32 s64, s64, 0x10000
buffer_load_dwordx4 a[160:163], v2, s[20:23], s64 offen offset:0 sc0 sc1
Mp 236,52,36
Rb 36,8,58368
v_exp_f32_e32 v29, v99
Mp 228,52,32
buffer_load_dwordx4 v2, s[20:23], s55 offen offset:3072 sc1 lds
Mp 240,56,32
Rb 40,8,59392
v_exp_f32_e32 v50, v104
Mp 248,56,36
buffer_load_dwordx4 a[164:167], v2, s[20:23], s64 offen offset:1024 sc0 sc1
Mp 252,60,36
Rb 44,8,60416
v_add_f32_e32 v14, v14, v29
s_waitcnt lgkmcnt(8)
Mp 244,60,32
buffer_load_dwordx4 a[168:171], v2, s[20:23], s64 offen offset:2048 sc0 sc1
Mp 128,0,40
Rb 48,8,61440
v_exp_f32_e32 v24, v105
Mp 136,0,44
buffer_load_dwordx4 a[172:175], v2, s[20:23], s64 offen offset:3072 sc0 sc1
Cv 49,49,29
Mp 140,4,44
Rb 52,8,62464
s_add_u32 m0, s36, 36864
Mp 132,4,40
buffer_load_dwordx4 v3, s[20:23], s55 offen offset:0 sc1 lds
v_add_f32_e32 v14, v14, v50
Mp 144,8,40
Rb 56,8,63488
v_add_f32_e32 v14, v14, v24
)" R"(
Mp 152,8,44
buffer_load_dwordx4 a[176:179], v3, s[20:23], s64 offen offset:0 sc0 sc1
Cv 50,50,24
Mp 156,12,44
Rb 60,8,64512
s_waitcnt lgkmcnt(8)
Mp 148,12,40
buffer_load_dwordx4 a[180:183], v3, s[20:23], s64 offen offset:1024 sc0 sc1
Mp 160,16,40
v_exp_f32_e32 v51, v106
Mp 168,16,44
buffer_load_dwordx4 a[184:187], v3, s[20:23], s64 offen offset:2048 sc0 sc1
v_add_f32_e32 v14, v14, v51
Mp 172,20,44
v_exp_f32_e32 v25, v107
s_sub_u32 s57, s57, 1
Mp 164,20,40
buffer_load_dwordx4 v3, s[20:23], s55 offen offset:1024 sc1 lds
Mp 176,24,40
v_exp_f32_e32 v52, v100
v_add_f32_e32 v14, v14, v25
Mp 184,24,44
buffer_load_dwordx4 a[188:191], v3, s[20:23], s64 offen offset:3072 sc0 sc1
Mp 188,28,44
v_exp_f32_e32 v26, v101
Cv 51,51,25
Mp 180,28,40
s_waitcnt vmcnt(23) lgkmcnt(0)
s_barrier
Mp 192,32,40
Rb 0,8,0
v_exp_f32_e32 v53, v102
Mp 200,32,44
Rb 4,8,16384
v_exp_f32_e32 v27, v103
Mp 204,36,44
Rb 8,8,1024
v_add_f32_e32 v15, v52, v26
Mp 196,36,40
Rb 12,8,17408
buffer_load_dwordx4 v3, s[20:23], s55 offen offset:2048 sc1 lds
Mp 208,40,40
Rb 16,8,2048
Cv 52,52,26
Mp 216,40,44
Rb 20,8,18432
v_exp_f32_e32 v54, v108
Mp 220,44,44
Rb 24,8,3072
v_add_f32_e32 v15, v15, v53
Mp 212,44,40
Rb 28,8,19456
v_exp_f32_e32 v28, v109
Mp 224,48,40
Rb 32,8,4096
v_add_f32_e32 v15, v15, v27
Mp 232,48,44
Rb 36,8,20480
v_exp_f32_e32 v55, v110
Mp 236,52,44
Rb 40,8,5120
v_exp_f32_e32 v29, v111
Mp 228,52,40
Rb 44,8,21504
buffer_load_dwordx4 v3, s[20:23], s55 offen offset:3072 sc1 lds
)" R"(
Mp 240,56,40
Cv 53,53,27
v_add_f32_e32 v15, v15, v54
v_add_f32_e32 v15, v15, v28
Mp 248,56,44
Cv 54,54,28
v_add_f32_e32 v15, v15, v55
Mp 252,60,44
v_add_f32_e32 v15, v15, v29
Cv 55,55,29
Mp 244,60,40
s_waitcnt vmcnt(18)
v_cmp_neq_f32_e32 vcc, v16, v66
v_cmp_neq_f32_e64 s[28:29], v20, v67
s_or_b64 vcc, vcc, s[28:29]
s_cbranch_scc1 .LzMRG
.LzMRR:
.LzH18:
s_cbranch_execz .LzHR
Mv 128,194,128
v_exp_f32_e32 v56, v112
Mv 136,194,132
v_exp_f32_e32 v26, v113
v_add_f32_e32 v12, v12, v64
Mv 132,192,128
s_add_u32 m0, s36, 131072
s_add_u32 s40, s67, 0x20000
v_add_f32_e32 v13, v13, v65
Mv 140,192,132
buffer_load_dwordx4 v2, s[20:23], s40 offen offset:0 sc1 lds
Mv 144,194,136
v_exp_f32_e32 v57, v114
v_add_f32_e32 v14, v14, v56
Mv 152,194,140
v_exp_f32_e32 v27, v115
v_add_f32_e32 v14, v14, v26
Mv 148,192,136
Cv 56,56,26
s_waitcnt vmcnt(14)
Mv 156,192,140
buffer_load_dwordx4 v2, s[20:23], s40 offen offset:1024 sc1 lds
v_add_f32_e32 v14, v14, v57
Mv 160,194,144
v_exp_f32_e32 v58, v120
v_add_f32_e32 v14, v14, v27
Mv 168,194,148
v_exp_f32_e32 v28, v121
Mv 164,192,144
v_exp_f32_e32 v59, v122
Cv 57,57,27
Mv 172,192,148
buffer_load_dwordx4 v2, s[20:23], s40 offen offset:2048 sc1 lds
v_add_f32_e32 v14, v14, v58
Mv 176,194,152
v_exp_f32_e32 v29, v123
Mv 184,194,156
v_exp_f32_e32 v60, v116
v_add_f32_e32 v14, v14, v28
Mv 180,192,152
v_exp_f32_e32 v24, v117
s_waitcnt vmcnt(11)
Mv 188,192,156
buffer_load_dwordx4 v2, s[20:23], s40 offen offset:3072 sc1 lds
Cv 58,58,28
)" R"(
Mv 192,194,160
v_exp_f32_e32 v61, v118
Mv 200,194,164
v_exp_f32_e32 v25, v119
v_add_f32_e32 v14, v14, v59
Mv 196,192,160
s_add_u32 m0, s36, 135168
v_exp_f32_e32 v62, v124
Mv 204,192,164
buffer_load_dwordx4 v3, s[20:23], s40 offen offset:0 sc1 lds
Mv 208,194,168
v_exp_f32_e32 v26, v125
v_add_f32_e32 v14, v14, v29
Mv 216,194,172
v_exp_f32_e32 v63, v126
Cv 59,59,29
Mv 212,192,168
v_add_f32_e32 v15, v15, v60
s_waitcnt vmcnt(7)
Mv 220,192,172
buffer_load_dwordx4 v3, s[20:23], s40 offen offset:1024 sc1 lds
v_add_f32_e32 v15, v15, v24
Mv 224,194,176
v_exp_f32_e32 v27, v127
Cv 60,60,24
Mv 232,194,180
v_add_f32_e32 v15, v15, v61
v_add_f32_e32 v15, v15, v25
Mv 228,192,176
Cv 61,61,25
v_add_f32_e32 v15, v15, v62
v_add_f32_e32 v15, v15, v26
Mv 236,192,180
buffer_load_dwordx4 v3, s[20:23], s40 offen offset:2048 sc1 lds
Cv 62,62,26
Mv 240,194,184
v_add_f32_e32 v15, v15, v63
v_add_f32_e32 v15, v15, v27
Mv 248,194,188
s_add_u32 s40, s67, 0x30000
Cv 63,63,27
v_max_f32_e32 v11, v14, v15
Mv 244,192,184
s_add_u32 m0, s36, 98304
v_cmp_nge_f32_e32 vcc, s62, v11
s_waitcnt lgkmcnt(8)
Mv 252,192,188
buffer_load_dwordx4 v2, s[20:23], s40 offen offset:0 sc1 lds
s_cbranch_vccnz .Lzfx13
.Lzfr13:
Mp 128,0,48
Rb 48,8,6144
v_add_f32_e32 v12, v12, v14
Mp 136,0,52
v_add_f32_e32 v13, v13, v15
v_mov_b32 v24, v12
v_mov_b32 v25, v13
Mp 136,4,60
Rb 52,8,22528
v_permlane32_swap_b32_e32 v12, v24
v_add_f32_e32 v12, v12, v24
Mp 128,4,56
buffer_load_dwordx4 v2, s[20:23], s40 offen offset:1024 sc1 lds
v_mov_b32 v24, v12
)" R"(
Mp 132,8,48
Rb 56,8,7168
v_permlane16_swap_b32_e32 v12, v24
Mp 140,8,52
v_add_f32_e32 v12, v12, v24
v_rcp_f32_e32 v12, v12
Mp 140,12,60
Rb 60,8,23552
v_permlane32_swap_b32_e32 v13, v25
s_waitcnt lgkmcnt(8)
Mp 132,12,56
buffer_load_dwordx4 v2, s[20:23], s40 offen offset:2048 sc1 lds
v_add_f32_e32 v13, v13, v25
Mp 144,16,48
Rb 0,8,8192
v_mov_b32 v25, v13
Mp 152,16,52
s_nop 0
v_permlane16_swap_b32_e32 v13, v25
v_add_f32_e32 v13, v13, v25
Mp 152,20,60
Rb 4,8,24576
v_rcp_f32_e32 v13, v13
Mp 144,20,56
buffer_load_dwordx4 v2, s[20:23], s40 offen offset:3072 sc1 lds
v_mul_f32_e32 v128, v12, v128
Mp 148,24,48
Rb 8,8,9216
v_mul_f32_e32 v129, v12, v129
Mp 156,24,52
s_add_u32 m0, s36, 102400
Cv 128,128,129
v_mul_f32_e32 v130, v12, v130
Mp 156,28,60
Rb 12,8,25600
v_mul_f32_e32 v131, v12, v131
s_waitcnt lgkmcnt(8)
Mp 148,28,56
buffer_load_dwordx4 v3, s[20:23], s40 offen offset:0 sc1 lds
Cv 129,130,131
Mp 160,32,48
Rb 16,8,10240
v_mul_f32_e32 v132, v12, v132
Mp 168,32,52
v_mul_f32_e32 v133, v12, v133
Cv 130,132,133
v_mul_f32_e32 v134, v12, v134
Mp 168,36,60
Rb 20,8,26624
v_mul_f32_e32 v135, v12, v135
Cv 131,134,135
Mp 160,36,56
buffer_load_dwordx4 v3, s[20:23], s40 offen offset:1024 sc1 lds
v_mul_f32_e32 v136, v13, v136
Mp 164,40,48
Rb 24,8,11264
v_mul_f32_e32 v137, v13, v137
Mp 172,40,52
Cv 136,136,137
v_mul_f32_e32 v138, v13, v138
v_mul_f32_e32 v139, v13, v139
Mp 172,44,60
Rb 28,8,27648
Cv 137,138,139
)" R"(
s_waitcnt lgkmcnt(8)
Mp 164,44,56
buffer_load_dwordx4 v3, s[20:23], s40 offen offset:2048 sc1 lds
v_mul_f32_e32 v140, v13, v140
Mp 176,48,48
Rb 32,8,12288
v_mul_f32_e32 v141, v13, v141
Mp 184,48,52
Cv 138,140,141
v_mul_f32_e32 v142, v13, v142
v_mul_f32_e32 v143, v13, v143
Mp 184,52,60
Rb 36,8,28672
Cv 139,142,143
v_mul_f32_e32 v144, v12, v144
Mp 176,52,56
v_mul_f32_e32 v145, v12, v145
Cv 144,144,145
v_mul_f32_e32 v146, v12, v146
Mp 180,56,48
Rb 40,8,13312
v_mul_f32_e32 v147, v12, v147
Mp 188,56,52
Cv 145,146,147
v_mul_f32_e32 v148, v12, v148
v_mul_f32_e32 v149, v12, v149
Mp 188,60,60
Rb 44,8,29696
Cv 146,148,149
s_waitcnt lgkmcnt(8)
Mp 180,60,56
v_mul_f32_e32 v150, v12, v150
v_mul_f32_e32 v151, v12, v151
Cv 147,150,151
Mp 192,0,48
Rb 48,8,14336
v_mul_f32_e32 v152, v13, v152
Mp 200,0,52
v_mul_f32_e32 v153, v13, v153
Cv 152,152,153
v_mul_f32_e32 v154, v13, v154
Mp 200,4,60
Rb 52,8,30720
v_mul_f32_e32 v155, v13, v155
Cv 153,154,155
Mp 192,4,56
v_mul_f32_e32 v156, v13, v156
v_mul_f32_e32 v157, v13, v157
Cv 154,156,157
Mp 196,8,48
Rb 56,8,15360
v_mul_f32_e32 v158, v13, v158
Mp 204,8,52
v_mul_f32_e32 v159, v13, v159
Cv 155,158,159
v_mul_f32_e32 v160, v12, v160
Mp 204,12,60
Rb 60,8,31744
v_mul_f32_e32 v161, v12, v161
s_waitcnt lgkmcnt(8)
Mp 196,12,56
Cv 160,160,161
v_mul_f32_e32 v162, v12, v162
v_mul_f32_e32 v163, v12, v163
)" R"(
Mp 208,16,48
Rb 0,1,0
Cv 161,162,163
Mp 216,16,52
v_mul_f32_e32 v164, v12, v164
v_mul_f32_e32 v165, v12, v165
Cv 162,164,165
Mp 216,20,60
Rb 4,1,1024
v_mul_f32_e32 v166, v12, v166
v_mul_f32_e32 v167, v12, v167
Mp 208,20,56
Cv 163,166,167
v_mul_f32_e32 v168, v13, v168
v_mul_f32_e32 v169, v13, v169
Mp 212,24,48
Rb 8,1,2048
Cv 168,168,169
Mp 220,24,52
v_mul_f32_e32 v170, v13, v170
v_mul_f32_e32 v171, v13, v171
Cv 169,170,171
Mp 220,28,60
Rb 12,1,3072
s_waitcnt vmcnt(14) lgkmcnt(4)
s_barrier
Mp 212,28,56
s_add_u32 s40, s67, 0x40000
v_mul_f32_e32 v172, v13, v172
v_mul_f32_e32 v173, v13, v173
Mp 224,32,48
Rb 16,1,4096
s_add_u32 m0, s36, 65536
Mp 232,32,52
buffer_load_dwordx4 v2, s[20:23], s40 offen offset:0 sc1 lds
Cv 170,172,173
Mp 232,36,60
Rb 20,1,5120
v_mul_f32_e32 v174, v13, v174
v_mul_f32_e32 v175, v13, v175
Mp 224,36,56
Cv 171,174,175
v_mul_f32_e32 v176, v12, v176
v_mul_f32_e32 v177, v12, v177
Mp 228,40,48
Rb 24,1,6144
Cv 176,176,177
s_lshl_b32 s64, s35, 17
Mp 236,40,52
v_mul_f32_e32 v178, v12, v178
v_mul_f32_e32 v179, v12, v179
Cv 177,178,179
Mp 236,44,60
Rb 28,1,8192
v_mul_f32_e32 v180, v12, v180
Mp 228,44,56
buffer_load_dwordx4 v2, s[20:23], s40 offen offset:1024 sc1 lds
v_mul_f32_e32 v181, v12, v181
Mp 240,48,48
Rb 32,1,9216
Cv 178,180,181
s_add_u32 s64, s64, s89
Mp 248,48,52
v_mul_f32_e32 v182, v12, v182
v_mul_f32_e32 v183, v12, v183
)" R"(
Cv 179,182,183
Mp 248,52,60
Rb 36,1,10240
v_mul_f32_e32 v184, v13, v184
v_mul_f32_e32 v185, v13, v185
Mp 240,52,56
Cv 184,184,185
v_mul_f32_e32 v186, v13, v186
v_mul_f32_e32 v187, v13, v187
Mp 244,56,48
Rb 40,1,11264
Cv 185,186,187
s_add_u32 s64, s64, 0x900000
Mp 252,56,52
buffer_load_dwordx4 v2, s[20:23], s40 offen offset:2048 sc1 lds
v_mul_f32_e32 v188, v13, v188
Mp 252,60,60
Rb 44,1,12288
v_mul_f32_e32 v189, v13, v189
s_mov_b32 s55, 0x1508c00
s_waitcnt lgkmcnt(8)
Mp 244,60,56
Cv 186,188,189
v_mul_f32_e32 v190, v13, v190
v_mul_f32_e32 v191, v13, v191
v_mfma_f32_16x16x32_bf16 v[32:35], a[0:3], v[128:131], 0
Rb 48,1,13312
Cv 187,190,191
s_xor_b32 s66, s64, 0x202000
v_mfma_f32_16x16x32_bf16 v[40:43], a[0:3], v[136:139], 0
v_mul_f32_e32 v192, v12, v192
v_mul_f32_e32 v193, v12, v193
Cv 192,192,193
v_mfma_f32_16x16x32_bf16 v[44:47], a[4:7], v[136:139], 0
Rb 52,1,14336
v_mul_f32_e32 v194, v12, v194
v_mul_f32_e32 v195, v12, v195
v_mfma_f32_16x16x32_bf16 v[36:39], a[4:7], v[128:131], 0
buffer_load_dwordx4 v2, s[20:23], s40 offen offset:3072 sc1 lds
Cv 193,194,195
v_mfma_f32_16x16x32_bf16 v[48:51], a[8:11], v[128:131], 0
Rb 56,1,16384
v_mul_f32_e32 v196, v12, v196
s_sub_u32 s66, s66, s84
v_mfma_f32_16x16x32_bf16 v[56:59], a[8:11], v[136:139], 0
v_mul_f32_e32 v197, v12, v197
Cv 194,196,197
v_mul_f32_e32 v198, v12, v198
v_mfma_f32_16x16x32_bf16 v[60:63], a[12:15], v[136:139], 0
Rb 60,1,17408
v_mul_f32_e32 v199, v12, v199
s_cmp_lg_u32 s33, 0
s_cmov_b32 s55, 0x40000000
s_waitcnt lgkmcnt(8)
v_mfma_f32_16x16x32_bf16 v[52:55], a[12:15], v[128:131], 0
Cv 195,198,199
v_mul_f32_e32 v200, v13, v200
v_mul_f32_e32 v201, v13, v201
v_mov_b32 v16, 1
Mp 32,16,144
Rb 0,1,18432
s_add_u32 m0, s36, 69632
s_sub_u32 s66, s66, s84
v_add_u32 v9, s36, v8
)" R"(
Mp 40,16,152
buffer_load_dwordx4 v3, s[20:23], s40 offen offset:0 sc1 lds
Cv 200,200,201
Mp 44,20,152
Rb 4,1,19456
v_mul_f32_e32 v202, v13, v202
v_mul_f32_e32 v203, v13, v203
Mp 36,20,144
Cv 201,202,203
v_mul_f32_e32 v204, v13, v204
v_mul_f32_e32 v205, v13, v205
Mp 48,24,144
Rb 8,1,20480
Cv 202,204,205
s_lshl2_add_u32 s65, s35, s33
Mp 56,24,152
v_mul_f32_e32 v206, v13, v206
v_mul_f32_e32 v207, v13, v207
Cv 203,206,207
Mp 60,28,152
Rb 12,1,21504
v_mul_f32_e32 v208, v12, v208
s_waitcnt lgkmcnt(8)
Mp 52,28,144
buffer_load_dwordx4 v3, s[20:23], s40 offen offset:1024 sc1 lds
v_mul_f32_e32 v209, v12, v209
Mp 32,32,160
Rb 16,1,22528
Cv 208,208,209
s_lshl_b32 s65, s65, 8
Mp 40,32,168
v_mul_f32_e32 v210, v12, v210
v_mul_f32_e32 v211, v12, v211
Cv 209,210,211
Mp 44,36,168
Rb 20,1,24576
v_mul_f32_e32 v212, v12, v212
v_mul_f32_e32 v213, v12, v213
Mp 36,36,160
Cv 210,212,213
v_mul_f32_e32 v214, v12, v214
v_mul_f32_e32 v215, v12, v215
Mp 48,40,160
Rb 24,1,25600
Cv 211,214,215
s_add_u32 s65, s65, s50
Mp 56,40,168
buffer_load_dwordx4 v3, s[20:23], s40 offen offset:2048 sc1 lds
v_mul_f32_e32 v216, v13, v216
Mp 60,44,168
Rb 28,1,26624
v_mul_f32_e32 v217, v13, v217
s_waitcnt lgkmcnt(8)
Mp 52,44,160
Cv 216,216,217
v_mul_f32_e32 v218, v13, v218
v_mul_f32_e32 v219, v13, v219
Mp 32,48,176
Rb 32,1,27648
Cv 217,218,219
s_add_u32 s65, s65, 0x6400
Mp 40,48,184
v_mul_f32_e32 v220, v13, v220
v_mul_f32_e32 v221, v13, v221
)" R"(
Cv 218,220,221
Mp 44,52,184
Rb 36,1,28672
v_mul_f32_e32 v222, v13, v222
v_mul_f32_e32 v223, v13, v223
Mp 36,52,176
Cv 219,222,223
v_mul_f32_e32 v224, v12, v224
v_mul_f32_e32 v225, v12, v225
Mp 48,56,176
Rb 40,1,29696
Cv 224,224,225
s_xor_b32 s52, s65, 0x202000
Mp 56,56,184
v_mul_f32_e32 v226, v12, v226
v_mul_f32_e32 v227, v12, v227
Cv 225,226,227
Mp 60,60,184
Rb 44,1,30720
v_mul_f32_e32 v228, v12, v228
s_waitcnt lgkmcnt(8)
Mp 52,60,176
v_mul_f32_e32 v229, v12, v229
Cv 226,228,229
v_mul_f32_e32 v230, v12, v230
Mp 32,0,192
Rb 48,1,32768
v_mul_f32_e32 v231, v12, v231
buffer_atomic_add v16, v7, s[20:23], s55 offen sc0 sc1
Mp 40,0,200
Cv 227,230,231
v_mul_f32_e32 v232, v13, v232
v_mul_f32_e32 v233, v13, v233
Mp 44,4,200
Rb 52,1,33792
Cv 232,232,233
v_mul_f32_e32 v234, v13, v234
Mp 36,4,192
v_mul_f32_e32 v235, v13, v235
Cv 233,234,235
v_mul_f32_e32 v236, v13, v236
Mp 48,8,192
Rb 56,1,34816
v_mul_f32_e32 v237, v13, v237
Mp 56,8,200
Cv 234,236,237
v_mul_f32_e32 v238, v13, v238
v_mul_f32_e32 v239, v13, v239
Mp 60,12,200
Rb 60,1,35840
s_waitcnt vmcnt(15) lgkmcnt(4)
s_barrier
Mp 52,12,192
s_add_u32 s40, s67, 0x50000
Cv 235,238,239
v_mul_f32_e32 v240, v12, v240
Mp 32,16,208
Rb 0,1,36864
s_add_u32 m0, s36, 0
Mp 40,16,216
buffer_load_dwordx4 v2, s[20:23], s40 offen offset:0 sc1 lds
v_mul_f32_e32 v241, v12, v241
Mp 44,20,216
Rb 4,1,37888
Cv 240,240,241
)" R"(
v_mul_f32_e32 v242, v12, v242
Mp 36,20,208
v_mul_f32_e32 v243, v12, v243
Cv 241,242,243
v_mul_f32_e32 v244, v12, v244
Mp 48,24,208
Rb 8,1,38912
v_mul_f32_e32 v245, v12, v245
Mp 56,24,216
Cv 242,244,245
v_mul_f32_e32 v246, v12, v246
v_mul_f32_e32 v247, v12, v247
Mp 60,28,216
Rb 12,1,40960
Cv 243,246,247
Mp 52,28,208
buffer_load_dwordx4 v2, s[20:23], s40 offen offset:1024 sc1 lds
v_mul_f32_e32 v248, v13, v248
Mp 32,32,224
Rb 16,1,41984
v_mul_f32_e32 v249, v13, v249
Mp 40,32,232
Cv 248,248,249
v_mul_f32_e32 v250, v13, v250
v_mul_f32_e32 v251, v13, v251
Mp 44,36,232
Rb 20,1,43008
Cv 249,250,251
v_mul_f32_e32 v252, v13, v252
Mp 36,36,224
v_mul_f32_e32 v253, v13, v253
Cv 250,252,253
v_mul_f32_e32 v254, v13, v254
Mp 48,40,224
Rb 24,1,44032
v_mul_f32_e32 v255, v13, v255
Mp 56,40,232
buffer_load_dwordx4 v2, s[20:23], s40 offen offset:2048 sc1 lds
Cv 251,254,255
Mp 60,44,232
Rb 28,1,45056
s_waitcnt lgkmcnt(8)
Mp 52,44,224
Mp 32,48,240
Rb 32,1,46080
Mp 40,48,248
Mp 44,52,248
Rb 36,1,47104
Mp 36,52,240
buffer_load_dwordx4 v2, s[20:23], s40 offen offset:3072 sc1 lds
Mp 48,56,240
Rb 40,1,49152
Mp 56,56,248
Mp 60,60,248
Rb 44,1,50176
s_waitcnt lgkmcnt(8)
Mp 52,60,240
v_mfma_f32_16x16x32_bf16 v[64:67], a[0:3], v[128:131], 0
Rb 48,1,51200
s_add_u32 m0, s36, 4096
v_mfma_f32_16x16x32_bf16 v[72:75], a[0:3], v[136:139], 0
buffer_load_dwordx4 v3, s[20:23], s40 offen offset:0 sc1 lds
Mp 72,4,152
Rb 52,1,52224
)" R"(
Mp 64,4,144
Cv 32,32,33
Cv 33,34,35
Mp 64,8,160
Rb 56,1,53248
Cv 34,36,37
Mp 72,8,168
Cv 35,38,39
Mp 72,12,184
Rb 60,1,54272
s_waitcnt lgkmcnt(8)
Mp 64,12,176
buffer_load_dwordx4 v3, s[20:23], s40 offen offset:1024 sc1 lds
Mp 64,16,192
Rb 0,1,55296
Mp 72,16,200
buffer_store_dwordx4 v[32:35], v2, s[20:23], s64 offen offset:0 sc0 sc1
Mp 72,20,216
Rb 4,1,57344
Mp 64,20,208
Mp 64,24,224
Rb 8,1,58368
Mp 72,24,232
buffer_load_dwordx4 v3, s[20:23], s40 offen offset:2048 sc1 lds
Mp 72,28,248
Rb 12,1,59392
s_waitcnt lgkmcnt(8)
Mp 64,28,240
Cv 40,40,41
Cv 41,42,43
v_mfma_f32_16x16x32_bf16 v[68:71], a[32:35], v[128:131], 0
Rb 16,1,60416
Cv 42,44,45
v_mfma_f32_16x16x32_bf16 v[76:79], a[32:35], v[136:139], 0
Cv 43,46,47
Mp 76,36,152
Rb 20,1,61440
Mp 68,36,144
buffer_store_dwordx4 v[40:43], v2, s[20:23], s64 offen offset:1024 sc0 sc1
Mp 68,40,160
Rb 24,1,62464
Mp 76,40,168
Mp 76,44,184
Rb 28,1,63488
s_waitcnt lgkmcnt(8)
Mp 68,44,176
Mp 68,48,192
Rb 32,10,0
Mp 76,48,200
Cv 48,48,49
Cv 49,50,51
Mp 76,52,216
Rb 36,10,1024
Cv 50,52,53
Mp 68,52,208
Cv 51,54,55
Mp 68,56,224
Rb 40,10,2048
Mp 76,56,232
buffer_store_dwordx4 v[48:51], v2, s[20:23], s64 offen offset:2048 sc0 sc1
Mp 76,60,248
Rb 44,10,3072
s_waitcnt vmcnt(18) lgkmcnt(4)
s_barrier
.LzH19:
)" R"(
s_cbranch_execz .LzHR
Mp 68,60,240
s_add_u32 s40, s67, 0x60000
v_mfma_f32_16x16x32_bf16 v[80:83], a[0:3], v[128:131], 0
Rb 48,10,4096
s_add_u32 m0, s36, 32768
v_mfma_f32_16x16x32_bf16 v[88:91], a[0:3], v[136:139], 0
buffer_load_dwordx4 v2, s[20:23], s40 offen offset:0 sc1 lds
Mp 88,4,152
Rb 52,10,5120
Mp 80,4,144
Cv 56,56,57
Cv 57,58,59
Mp 80,8,160
Rb 56,10,6144
Cv 58,60,61
Mp 88,8,168
Cv 59,62,63
Mp 88,12,184
Rb 60,10,8192
Mp 80,12,176
buffer_load_dwordx4 v2, s[20:23], s40 offen offset:1024 sc1 lds
Mp 80,16,192
Rb 0,10,9216
Mp 88,16,200
buffer_store_dwordx4 v[56:59], v2, s[20:23], s64 offen offset:3072 sc0 sc1
Mp 88,20,216
Rb 4,10,10240
Mp 80,20,208
Mp 80,24,224
Rb 8,10,11264
Mp 88,24,232
buffer_load_dwordx4 v2, s[20:23], s40 offen offset:2048 sc1 lds
Mp 88,28,248
Rb 12,10,12288
s_waitcnt lgkmcnt(8)
Mp 80,28,240
Cv 64,64,65
Cv 65,66,67
v_mfma_f32_16x16x32_bf16 v[84:87], a[32:35], v[128:131], 0
Rb 16,10,13312
Cv 66,68,69
v_mfma_f32_16x16x32_bf16 v[92:95], a[32:35], v[136:139], 0
Cv 67,70,71
Mp 92,36,152
Rb 20,10,14336
Mp 84,36,144
buffer_load_dwordx4 v2, s[20:23], s40 offen offset:3072 sc1 lds
Mp 84,40,160
Rb 24,10,16384
Mp 92,40,168
buffer_store_dwordx4 v[64:67], v3, s[20:23], s64 offen offset:0 sc0 sc1
Mp 92,44,184
Rb 28,10,17408
s_waitcnt lgkmcnt(8)
Mp 84,44,176
Mp 84,48,192
Rb 32,10,18432
s_add_u32 m0, s36, 36864
Mp 92,48,200
buffer_load_dwordx4 v3, s[20:23], s40 offen offset:0 sc1 lds
Mp 92,52,216
Rb 36,10,19456
Mp 84,52,208
)" R"(
Cv 72,72,73
Cv 73,74,75
Mp 84,56,224
Rb 40,10,20480
Cv 74,76,77
Mp 92,56,232
Cv 75,78,79
Mp 92,60,248
Rb 44,10,21504
s_waitcnt lgkmcnt(8)
Mp 84,60,240
buffer_load_dwordx4 v3, s[20:23], s40 offen offset:1024 sc1 lds
v_mfma_f32_16x16x32_bf16 v[96:99], a[0:3], v[128:131], 0
Rb 48,10,22528
v_mfma_f32_16x16x32_bf16 v[104:107], a[0:3], v[136:139], 0
buffer_store_dwordx4 v[72:75], v3, s[20:23], s64 offen offset:1024 sc0 sc1
Mp 104,4,152
Rb 52,10,24576
Mp 96,4,144
Mp 96,8,160
Rb 56,10,25600
Mp 104,8,168
buffer_load_dwordx4 v3, s[20:23], s40 offen offset:2048 sc1 lds
Mp 104,12,184
Rb 60,10,26624
s_waitcnt lgkmcnt(8)
Mp 96,12,176
Cv 80,80,81
Cv 81,82,83
Mp 96,16,192
Rb 0,10,27648
Cv 82,84,85
Mp 104,16,200
Cv 83,86,87
Mp 104,20,216
Rb 4,10,28672
Mp 96,20,208
buffer_store_dwordx4 v[80:83], v3, s[20:23], s64 offen offset:2048 sc0 sc1
Mp 96,24,224
Rb 8,10,29696
Mp 104,24,232
Mp 104,28,248
Rb 12,10,30720
s_waitcnt lgkmcnt(8)
Mp 96,28,240
v_mfma_f32_16x16x32_bf16 v[100:103], a[32:35], v[128:131], 0
Rb 16,8,32768
v_mfma_f32_16x16x32_bf16 v[108:111], a[32:35], v[136:139], 0
Cv 88,88,89
Cv 89,90,91
Mp 108,36,152
Rb 20,8,33792
Cv 90,92,93
Mp 100,36,144
Cv 91,94,95
Mp 100,40,160
Rb 24,8,34816
Mp 108,40,168
buffer_store_dwordx4 v[88:91], v3, s[20:23], s64 offen offset:3072 sc0 sc1
Mp 108,44,184
Rb 28,8,35840
s_waitcnt vmcnt(23) lgkmcnt(4)
s_barrier
Mp 100,44,176
s_add_u32 s40, s67, 0x70000
)" R"(
Mp 100,48,192
Rb 32,8,36864
s_add_u32 m0, s36, 131072
Mp 108,48,200
buffer_load_dwordx4 v2, s[20:23], s40 offen offset:0 sc1 lds
Mp 108,52,216
Rb 36,8,37888
s_mov_b32 s8, s74
Mp 100,52,208
s_mov_b32 s9, s75
s_mov_b32 s10, 0x1c0000
Mp 100,56,224
Rb 40,8,38912
s_mov_b32 s11, 0x20000
Mp 108,56,232
v_and_b32 v174, 1, v0
v_mul_u32_u24 v174, 0xffff, v174
Mp 108,60,248
Rb 44,8,40960
Mp 100,60,240
buffer_load_dwordx4 v2, s[20:23], s40 offen offset:1024 sc1 lds
v_mfma_f32_16x16x32_bf16 v[112:115], a[0:3], v[128:131], 0
Rb 48,8,41984
v_add_u32 v174, 1, v174
v_mfma_f32_16x16x32_bf16 v[120:123], a[0:3], v[136:139], 0
v_mul_u32_u24 v174, 0x3f80, v174
v_bfe_u32 v173, v0, 1, 3
Mp 120,4,152
Rb 52,8,43008
v_cmp_eq_u32_e32 vcc, 0, v173
Mp 112,4,144
Cv 96,96,97
Cv 97,98,99
Mp 112,8,160
Rb 56,8,44032
Cv 98,100,101
Mp 120,8,168
buffer_load_dwordx4 v2, s[20:23], s40 offen offset:2048 sc1 lds
Mp 120,12,184
Rb 60,8,45056
s_waitcnt lgkmcnt(8)
Mp 112,12,176
Cv 99,102,103
v_cndmask_b32_e32 v132, 0, v174, vcc
v_cmp_eq_u32_e32 vcc, 1, v173
Mp 112,16,192
Rb 0,8,46080
v_cndmask_b32_e32 v133, 0, v174, vcc
Mp 120,16,200
s_add_u32 s64, s64, 0x10000
buffer_store_dwordx4 v[96:99], v2, s[20:23], s64 offen offset:0 sc0 sc1
Mp 120,20,216
Rb 4,8,47104
v_cmp_eq_u32_e32 vcc, 2, v173
Mp 112,20,208
buffer_load_dwordx4 v2, s[20:23], s40 offen offset:3072 sc1 lds
Mp 112,24,224
Rb 8,8,49152
v_cndmask_b32_e32 v134, 0, v174, vcc
Mp 120,24,232
v_cmp_eq_u32_e32 vcc, 3, v173
v_and_b32 v167, 0xf0, v1
Mp 120,28,248
Rb 12,8,50176
)" R"(
s_waitcnt lgkmcnt(8)
Mp 112,28,240
Cv 104,104,105
Cv 105,106,107
v_mfma_f32_16x16x32_bf16 v[116:119], a[32:35], v[128:131], 0
Rb 16,8,51200
s_add_u32 m0, s36, 135168
v_mfma_f32_16x16x32_bf16 v[124:127], a[32:35], v[136:139], 0
buffer_load_dwordx4 v3, s[20:23], s40 offen offset:0 sc1 lds
Mp 124,36,152
Rb 20,8,52224
Cv 106,108,109
Mp 116,36,144
Cv 107,110,111
v_cndmask_b32_e32 v135, 0, v174, vcc
Mp 116,40,160
Rb 24,8,53248
v_cmp_eq_u32_e32 vcc, 4, v173
Mp 124,40,168
buffer_store_dwordx4 v[104:107], v2, s[20:23], s64 offen offset:1024 sc0 sc1
Mp 124,44,184
Rb 28,8,54272
s_waitcnt lgkmcnt(8)
Mp 116,44,176
buffer_load_dwordx4 v3, s[20:23], s40 offen offset:1024 sc1 lds
v_cndmask_b32_e32 v140, 0, v174, vcc
Mp 116,48,192
Rb 32,8,55296
v_cmp_eq_u32_e32 vcc, 5, v173
Mp 124,48,200
s_nop 0
v_cndmask_b32_e32 v141, 0, v174, vcc
v_cmp_eq_u32_e32 vcc, 6, v173
Mp 124,52,216
Rb 36,8,57344
v_cndmask_b32_e32 v142, 0, v174, vcc
Mp 116,52,208
v_cmp_eq_u32_e32 vcc, 7, v173
v_lshrrev_b32 v173, 4, v0
Mp 116,56,224
Rb 40,8,58368
v_cndmask_b32_e32 v143, 0, v174, vcc
Mp 124,56,232
buffer_load_dwordx4 v3, s[20:23], s40 offen offset:2048 sc1 lds
Mp 124,60,248
Rb 44,8,59392
s_waitcnt vmcnt(9) lgkmcnt(8)
buffer_store_dword v6, v7, s[20:23], s65 offen offset:0 sc0 sc1
Mp 116,60,240
v_lshl_or_b32 v164, v173, 25, v167
v_mul_u32_u24 v166, 56, v167
v_mfma_f32_16x16x32_bf16 v[32:35], a[0:3], v[128:131], 0
Rb 48,8,60416
v_lshl_add_u32 v166, v173, 3, v166
v_mfma_f32_16x16x32_bf16 v[40:43], a[0:3], v[136:139], 0
v_lshrrev_b32_e64 v173, v173, s85
v_bfe_u32 v174, v173, 4, 1
Mp 40,4,152
Rb 52,8,61440
v_lshl_or_b32 v183, v174, 13, v164
Mp 32,4,144
Cv 112,112,113
Cv 113,114,115
Mp 32,8,160
)" R"(
Rb 56,8,62464
Cv 114,116,117
Mp 40,8,168
Cv 115,118,119
v_and_b32 v174, 1, v173
Mp 40,12,184
Rb 60,8,63488
s_waitcnt lgkmcnt(8)
Mp 32,12,176
buffer_store_dwordx4 v[112:115], v2, s[20:23], s64 offen offset:2048 sc0 sc1
ds_write_b32 v9, v16 offset:39936
Mp 32,16,192
Rb 0,8,0
v_lshl_or_b32 v164, v174, 13, v164
Mp 40,16,200
v_and_b32 v174, 7, v0
v_and_b32 v165, 0x80, v1
Mp 40,20,216
Rb 4,8,1024
v_lshl_or_b32 v165, v174, 8, v165
Mp 32,20,208
v_lshlrev_b32 v165, 1, v165
s_mov_b32 s93, 0x1111
Mp 32,24,224
Rb 8,8,2048
v_lshrrev_b32 v173, 2, v0
Mp 40,24,232
Cv 120,120,121
Cv 121,122,123
Mp 40,28,248
Rb 12,8,3072
s_waitcnt lgkmcnt(3)
s_barrier
Mp 32,28,240
Cv 122,124,125
Cv 123,126,127
ds_read_b32 v16, v8 offset:39936
v_mfma_f32_16x16x32_bf16 v[36:39], a[32:35], v[128:131], 0
Rb 16,8,4096
v_mul_u32_u24 v173, 832, v173
v_mfma_f32_16x16x32_bf16 v[44:47], a[32:35], v[136:139], 0
buffer_store_dwordx4 v[120:123], v2, s[20:23], s64 offen offset:3072 sc0 sc1
Mp 44,36,152
Rb 20,8,5120
v_add_u32 v173, v173, v1
Mp 36,36,144
v_lshrrev_b32 v174, 1, v0
v_and_b32 v174, 24, v174
Mp 36,40,160
Rb 24,8,6144
v_lshl_add_u32 v174, v167, 2, v174
Mp 44,40,168
s_add_u32 s59, s36, 98304
v_add_u32 v174, s59, v174
Mp 44,44,184
Rb 28,8,8192
s_waitcnt lgkmcnt(4)
Mp 36,44,176
v_readfirstlane_b32 s82, v16
Mp 36,48,192
Rb 32,8,9216
s_lshl2_add_u32 s56, s82, s33
Mp 44,48,200
s_mul_i32 s88, s56, 4682
s_lshr_b32 s58, s88, 16
)" R"(
Mp 44,52,216
Rb 36,8,10240
s_mul_i32 s57, s58, 14
Mp 36,52,208
s_sub_u32 s57, s56, s57
s_lshl_b32 s40, s58, 12
Mp 36,56,224
Rb 40,8,11264
Mp 44,56,232
s_cmp_gt_u32 s57, 6
s_cselect_b32 s88, 128, 0
Mp 44,60,248
Rb 44,8,12288
Mp 36,60,240
buffer_load_dword v4, v7, s[20:23], s52 offen offset:0 sc0 sc1
s_add_u32 s40, s40, s88
v_mfma_f32_16x16x32_bf16 v[48:51], a[0:3], v[128:131], 0
Rb 48,8,13312
s_add_u32 s40, s40, 0x1510000
v_mfma_f32_16x16x32_bf16 v[56:59], a[0:3], v[136:139], 0
s_lshl_b32 s55, s58, 6
s_add_u32 s55, s55, s57
Mp 56,4,152
Rb 52,8,14336
Mp 48,4,144
Cv 32,32,33
Cv 33,34,35
Mp 48,8,160
Rb 56,8,16384
Cv 34,36,37
Mp 56,8,168
Cv 35,38,39
Mp 56,12,184
Rb 60,8,17408
s_waitcnt lgkmcnt(8)
Mp 48,12,176
buffer_store_dwordx4 v[32:35], v3, s[20:23], s64 offen offset:0 sc0 sc1
Mp 48,16,192
Rb 0,8,18432
Mp 56,16,200
s_cmp_gt_u32 s57, 6
s_cselect_b32 s88, 9, 0
Mp 56,20,216
Rb 4,8,19456
s_add_u32 s55, s55, s88
Mp 48,20,208
s_and_b32 s88, s58, 32
s_lshl_b32 s88, s88, 8
Mp 48,24,224
Rb 8,8,20480
v_xor_b32 v164, s88, v164
Mp 56,24,232
Cv 40,40,41
Cv 41,42,43
Mp 56,28,248
Rb 12,8,21504
s_waitcnt lgkmcnt(8)
Mp 48,28,240
Cv 42,44,45
Cv 43,46,47
v_xor_b32 v183, s88, v183
v_mfma_f32_16x16x32_bf16 v[52:55], a[32:35], v[128:131], 0
Rb 16,8,22528
s_lshl_b32 s55, s55, 10
)" R"(
v_mfma_f32_16x16x32_bf16 v[60:63], a[32:35], v[136:139], 0
buffer_store_dwordx4 v[40:43], v3, s[20:23], s64 offen offset:1024 sc0 sc1
Mp 60,36,152
Rb 20,8,24576
s_add_u32 s55, s55, 0x1000000
Mp 52,36,144
s_and_b32 s88, s57, 1
s_lshl1_add_u32 s58, s58, s88
Mp 52,40,160
Rb 24,8,25600
s_lshl_b32 s88, s58, 8
Mp 60,40,168
v_add_u32 v167, s88, v167
s_mul_i32 s59, s58, 14336
Mp 60,44,184
Rb 28,8,26624
s_waitcnt lgkmcnt(8)
Mp 52,44,176
s_lshr_b32 s88, s57, 1
s_lshl_b32 s88, s88, 6
s_add_u32 s88, s88, s59
Mp 52,48,192
Rb 32,8,27648
v_add_u32 v166, s88, v166
Mp 60,48,200
s_mov_b32 s57, 0
s_add_u32 m0, s36, 98304
Mp 60,52,216
Rb 36,8,28672
Mp 52,52,208
buffer_load_dwordx4 v173, s[12:15], s88 offen offset:0 lds
Mp 52,56,224
Rb 40,8,29696
s_add_u32 m0, s36, 98880
Mp 60,56,232
buffer_load_dwordx4 v173, s[12:15], s88 offen offset:448 lds
Mp 60,60,248
Rb 44,8,30720
s_waitcnt lgkmcnt(8)
Mp 52,60,240
s_mov_b32 s88, 0x1500008
buffer_load_dword v167, v167, s[20:23], s88 offen sc0 sc1
v_mfma_f32_16x16x32_bf16 v[64:67], a[0:3], v[128:131], 0
Rb 48,1,0
v_mfma_f32_16x16x32_bf16 v[72:75], a[0:3], v[136:139], 0
Mp 72,4,152
Rb 52,1,1024
Mp 64,4,144
Mp 64,8,160
Rb 56,1,2048
Mp 72,8,168
Mp 72,12,184
Rb 60,1,3072
s_waitcnt vmcnt(5) lgkmcnt(8)
s_barrier
v_cmp_eq_u32_e32 vcc, s3, v4
s_andn2_b64 s[28:29], 1, vcc
s_cbranch_scc1 .Lzsp24
.Lzok24:
Mp 64,12,176
buffer_load_dwordx4 a[64:67], v2, s[20:23], s66 offen offset:0 sc0 sc1
Mp 64,16,192
Rb 0,1,4096
Mp 72,16,200
Mp 72,20,216
)" R"(
Rb 4,1,5120
Mp 64,20,208
buffer_load_dwordx4 a[68:71], v2, s[20:23], s66 offen offset:1024 sc0 sc1
Mp 64,24,224
Rb 8,1,6144
Mp 72,24,232
Mp 72,28,248
Rb 12,1,8192
s_waitcnt lgkmcnt(8)
Mp 64,28,240
buffer_load_dwordx4 a[72:75], v2, s[20:23], s66 offen offset:2048 sc0 sc1
v_mfma_f32_16x16x32_bf16 v[68:71], a[32:35], v[128:131], 0
Rb 16,1,9216
v_mfma_f32_16x16x32_bf16 v[76:79], a[32:35], v[136:139], 0
Mp 76,36,152
Rb 20,1,10240
Mp 68,36,144
buffer_load_dwordx4 a[76:79], v2, s[20:23], s66 offen offset:3072 sc0 sc1
Mp 68,40,160
Rb 24,1,11264
Mp 76,40,168
Mp 76,44,184
Rb 28,1,12288
s_waitcnt vmcnt(7) lgkmcnt(8)
buffer_store_dword v6, v7, s[20:23], s65 offen offset:128 sc0 sc1
Mp 68,44,176
buffer_load_dwordx4 a[80:83], v3, s[20:23], s66 offen offset:0 sc0 sc1
Mp 68,48,192
Rb 32,1,13312
Mp 76,48,200
Mp 76,52,216
Rb 36,1,14336
Mp 68,52,208
buffer_load_dwordx4 a[84:87], v3, s[20:23], s66 offen offset:1024 sc0 sc1
Mp 68,56,224
Rb 40,1,16384
Mp 76,56,232
Mp 76,60,248
Rb 44,1,17408
s_waitcnt lgkmcnt(8)
Mp 68,60,240
buffer_load_dwordx4 a[88:91], v3, s[20:23], s66 offen offset:2048 sc0 sc1
v_mfma_f32_16x16x32_bf16 v[80:83], a[0:3], v[128:131], 0
Rb 48,1,18432
v_mfma_f32_16x16x32_bf16 v[88:91], a[0:3], v[136:139], 0
Mp 88,4,152
Rb 52,1,19456
Mp 80,4,144
buffer_load_dwordx4 a[92:95], v3, s[20:23], s66 offen offset:3072 sc0 sc1
Mp 80,8,160
Rb 56,1,20480
Mp 88,8,168
Mp 88,12,184
Rb 60,1,21504
s_waitcnt lgkmcnt(8)
Mp 80,12,176
Mp 80,16,192
Rb 0,1,22528
Mp 88,16,200
Mp 88,20,216
Rb 4,1,24576
Mp 80,20,208
Mp 80,24,224
Rb 8,1,25600
)" R"(
Mp 88,24,232
Mp 88,28,248
Rb 12,1,26624
s_waitcnt lgkmcnt(8)
Mp 80,28,240
v_mfma_f32_16x16x32_bf16 v[84:87], a[32:35], v[128:131], 0
Rb 16,1,27648
v_mfma_f32_16x16x32_bf16 v[92:95], a[32:35], v[136:139], 0
Mp 92,36,152
Rb 20,1,28672
Mp 84,36,144
Mp 84,40,160
Rb 24,1,29696
Mp 92,40,168
Mp 92,44,184
Rb 28,1,30720
s_waitcnt lgkmcnt(8)
Mp 84,44,176
buffer_load_dword v4, v7, s[20:23], s52 offen offset:128 sc0 sc1
Mp 84,48,192
Rb 32,1,32768
Mp 92,48,200
Mp 92,52,216
Rb 36,1,33792
Mp 84,52,208
Mp 84,56,224
Rb 40,1,34816
Mp 92,56,232
Mp 92,60,248
Rb 44,1,35840
s_waitcnt vmcnt(6)
s_barrier
Mp 84,60,240
Mv 48,194,64
Mv 56,194,68
Mv 52,192,64
Mv 60,192,68
Mv 64,194,72
Mv 72,194,76
Mv 68,192,72
s_waitcnt vmcnt(3) lgkmcnt(10)
Mv 76,192,76
Cv 48,48,49
Cv 49,50,51
.LzH20:
s_cbranch_execz .LzHR
Mv 80,194,80
Cv 50,52,53
Cv 51,54,55
Mv 88,194,84
s_add_u32 s29, s73, 0x0
buffer_store_dwordx4 v[48:51], v1, s[20:23], s29 offen sc0 sc1
Mv 84,192,80
Mv 92,192,84
v_mfma_f32_16x16x32_bf16 v[96:99], a[0:3], v[128:131], 0
Rb 48,1,36864
v_mfma_f32_16x16x32_bf16 v[104:107], a[0:3], v[136:139], 0
Mp 104,4,152
Rb 52,1,37888
s_waitcnt lgkmcnt(8)
Mp 96,4,144
Cv 56,56,57
Cv 57,58,59
ds_read_b64 v[188:189], v174 offset:0
Mp 96,8,160
)" R"(
Rb 56,1,38912
Cv 58,60,61
Mp 104,8,168
Cv 59,62,63
ds_read_b64 v[190:191], v174 offset:32
Mp 104,12,184
Rb 60,1,40960
Mp 96,12,176
s_add_u32 s29, s73, 0x400
buffer_store_dwordx4 v[56:59], v1, s[20:23], s29 offen sc0 sc1
ds_read_b64 v[196:197], v174 offset:1024
Mp 96,16,192
Rb 0,1,41984
Mp 104,16,200
ds_read_b64 v[198:199], v174 offset:1056
Mp 104,20,216
Rb 4,1,43008
s_waitcnt lgkmcnt(12)
Mp 96,20,208
Mp 96,24,224
Rb 8,1,44032
Mp 104,24,232
Cv 64,64,65
Cv 65,66,67
Mp 104,28,248
Rb 12,1,45056
Cv 66,68,69
Mp 96,28,240
Cv 67,70,71
v_mfma_f32_16x16x32_bf16 v[100:103], a[32:35], v[128:131], 0
Rb 16,1,46080
v_mfma_f32_16x16x32_bf16 v[108:111], a[32:35], v[136:139], 0
s_add_u32 s29, s73, 0x800
buffer_store_dwordx4 v[64:67], v1, s[20:23], s29 offen sc0 sc1
Mp 108,36,152
Rb 20,1,47104
s_waitcnt vmcnt(3) lgkmcnt(11)
v_cmp_eq_u32_e32 vcc, s3, v4
s_andn2_b64 s[28:29], 1, vcc
s_cbranch_scc1 .Lzsp25
.Lzok25:
Mp 100,36,144
s_add_u32 s66, s66, 0x10000
buffer_load_dwordx4 a[96:99], v2, s[20:23], s66 offen offset:0 sc0 sc1
Mp 100,40,160
Rb 24,1,49152
v_sub_f32_e32 v172, 1.0, v167
Mp 108,40,168
Cv 172,172,172
v_lshlrev_b32 v172, 16, v172
Mp 108,44,184
Rb 28,1,50176
v_lshlrev_b32 v204, 16, v188
Mp 100,44,176
buffer_load_dwordx4 a[100:103], v2, s[20:23], s66 offen offset:1024 sc0 sc1
Mp 100,48,192
Rb 32,1,51200
Cv 72,72,73
Mp 108,48,200
Cv 73,74,75
Cv 74,76,77
Mp 108,52,216
Rb 36,1,52224
s_waitcnt lgkmcnt(12)
)" R"(
Mp 100,52,208
buffer_load_dwordx4 a[104:107], v2, s[20:23], s66 offen offset:2048 sc0 sc1
Mp 100,56,224
Rb 40,1,53248
Cv 75,78,79
Mp 108,56,232
s_add_u32 s29, s73, 0xc00
buffer_store_dwordx4 v[72:75], v1, s[20:23], s29 offen sc0 sc1
Mp 108,60,248
Rb 44,1,54272
v_and_b32 v205, 0xffff0000, v188
Mp 100,60,240
buffer_load_dwordx4 a[108:111], v2, s[20:23], s66 offen offset:3072 sc0 sc1
Mv 96,194,88
v_mul_f32_e32 v204, v172, v204
v_mul_f32_e32 v205, v172, v205
Mv 104,194,92
Cv 205,204,205
v_lshlrev_b32 v204, 16, v205
Mv 100,192,88
v_and_b32 v205, 0xffff0000, v205
s_waitcnt lgkmcnt(8)
Mv 108,192,92
buffer_load_dwordx4 a[112:115], v3, s[20:23], s66 offen offset:0 sc0 sc1
v_mfma_f32_16x16x32_bf16 v[112:115], a[0:3], v[128:131], 0
Rb 48,1,55296
Cv 80,80,81
v_mfma_f32_16x16x32_bf16 v[120:123], a[0:3], v[136:139], 0
Cv 81,82,83
Cv 82,84,85
Mp 120,4,152
Rb 52,1,57344
Cv 83,86,87
Mp 112,4,144
buffer_load_dwordx4 a[116:119], v3, s[20:23], s66 offen offset:1024 sc0 sc1
Mp 112,8,160
Rb 56,1,58368
v_lshlrev_b32 v206, 16, v189
Mp 120,8,168
s_add_u32 s29, s73, 0x1000
buffer_store_dwordx4 v[80:83], v1, s[20:23], s29 offen sc0 sc1
Mp 120,12,184
Rb 60,1,59392
s_waitcnt lgkmcnt(8)
Mp 112,12,176
v_and_b32 v207, 0xffff0000, v189
v_mul_f32_e32 v206, v172, v206
Mp 112,16,192
Rb 0,1,60416
v_mul_f32_e32 v207, v172, v207
Mp 120,16,200
Cv 207,206,207
v_lshlrev_b32 v206, 16, v207
Mp 120,20,216
Rb 4,1,61440
v_and_b32 v207, 0xffff0000, v207
Mp 112,20,208
Cv 88,88,89
Cv 89,90,91
Mp 112,24,224
Rb 8,1,62464
Cv 90,92,93
Mp 120,24,232
Cv 91,94,95
)" R"(
v_lshlrev_b32 v212, 16, v190
Mp 120,28,248
Rb 12,1,63488
s_waitcnt lgkmcnt(8)
Mp 112,28,240
s_add_u32 s29, s73, 0x1400
buffer_store_dwordx4 v[88:91], v1, s[20:23], s29 offen sc0 sc1
v_mfma_f32_16x16x32_bf16 v[116:119], a[32:35], v[128:131], 0
Rb 16,10,0
v_and_b32 v213, 0xffff0000, v190
v_mfma_f32_16x16x32_bf16 v[124:127], a[32:35], v[136:139], 0
v_mul_f32_e32 v212, v172, v212
v_mul_f32_e32 v213, v172, v213
Mp 124,36,152
Rb 20,10,1024
Cv 213,212,213
Mp 116,36,144
v_lshlrev_b32 v212, 16, v213
v_and_b32 v213, 0xffff0000, v213
Mp 116,40,160
Rb 24,10,2048
v_lshlrev_b32 v214, 16, v191
Mp 124,40,168
Cv 96,96,97
Cv 97,98,99
Mp 124,44,184
Rb 28,10,3072
s_waitcnt lgkmcnt(8)
Mp 116,44,176
Cv 98,100,101
Cv 99,102,103
Mp 116,48,192
Rb 32,10,4096
v_and_b32 v215, 0xffff0000, v191
Mp 124,48,200
s_add_u32 s29, s73, 0x1800
buffer_store_dwordx4 v[96:99], v1, s[20:23], s29 offen sc0 sc1
Mp 124,52,216
Rb 36,10,5120
v_mul_f32_e32 v214, v172, v214
Mp 116,52,208
v_mul_f32_e32 v215, v172, v215
Cv 215,214,215
Mp 116,56,224
Rb 40,10,6144
v_lshlrev_b32 v214, 16, v215
Mp 124,56,232
v_and_b32 v215, 0xffff0000, v215
v_lshlrev_b32 v220, 16, v196
Mp 124,60,248
Rb 44,10,8192
s_waitcnt vmcnt(8) lgkmcnt(10)
Mp 116,60,240
Cv 104,104,105
Cv 105,106,107
Mv 112,194,96
Cv 106,108,109
Cv 107,110,111
Mv 120,194,100
s_add_u32 s29, s73, 0x4000
buffer_store_dwordx4 v[104:107], v1, s[20:23], s29 offen sc0 sc1
Mv 116,192,96
v_and_b32 v221, 0xffff0000, v196
v_mul_f32_e32 v220, v172, v220
)" R"(
Mv 124,192,100
v_mul_f32_e32 v221, v172, v221
Cv 221,220,221
v_mfma_f32_16x16x32_bf16 v[32:35], a[0:3], v[128:131], 0
Rb 48,10,9216
v_lshlrev_b32 v220, 16, v221
v_mfma_f32_16x16x32_bf16 v[40:43], a[0:3], v[136:139], 0
v_and_b32 v221, 0xffff0000, v221
v_lshlrev_b32 v222, 16, v197
Mp 40,4,152
Rb 52,10,10240
s_waitcnt lgkmcnt(8)
Mp 32,4,144
Cv 112,112,113
Cv 113,114,115
Mp 32,8,160
Rb 56,10,11264
Cv 114,116,117
Mp 40,8,168
Cv 115,118,119
v_and_b32 v223, 0xffff0000, v197
Mp 40,12,184
Rb 60,10,12288
v_mul_f32_e32 v222, v172, v222
Mp 32,12,176
s_add_u32 s29, s73, 0x4400
buffer_store_dwordx4 v[112:115], v1, s[20:23], s29 offen sc0 sc1
Mp 32,16,192
Rb 0,10,13312
v_mul_f32_e32 v223, v172, v223
Mp 40,16,200
Cv 223,222,223
v_lshlrev_b32 v222, 16, v223
Mp 40,20,216
Rb 4,10,14336
s_waitcnt lgkmcnt(8)
Mp 32,20,208
v_and_b32 v223, 0xffff0000, v223
v_lshlrev_b32 v228, 16, v198
Mp 32,24,224
Rb 8,10,16384
v_and_b32 v229, 0xffff0000, v198
Mp 40,24,232
Cv 120,120,121
Cv 121,122,123
Mp 40,28,248
Rb 12,10,17408
Cv 122,124,125
Mp 32,28,240
Cv 123,126,127
v_mul_f32_e32 v228, v172, v228
v_mfma_f32_16x16x32_bf16 v[36:39], a[32:35], v[128:131], 0
Rb 16,10,18432
v_mul_f32_e32 v229, v172, v229
v_mfma_f32_16x16x32_bf16 v[44:47], a[32:35], v[136:139], 0
s_add_u32 s29, s73, 0x4800
buffer_store_dwordx4 v[120:123], v1, s[20:23], s29 offen sc0 sc1
Mp 44,36,152
Rb 20,10,19456
s_waitcnt lgkmcnt(8)
Mp 36,36,144
Cv 229,228,229
v_lshlrev_b32 v228, 16, v229
Mp 36,40,160
)" R"(
Rb 24,10,20480
v_and_b32 v229, 0xffff0000, v229
Mp 44,40,168
v_lshlrev_b32 v230, 16, v199
v_and_b32 v231, 0xffff0000, v199
Mp 44,44,184
Rb 28,10,21504
v_mul_f32_e32 v230, v172, v230
Mp 36,44,176
v_mul_f32_e32 v231, v172, v231
Cv 231,230,231
Mp 36,48,192
Rb 32,10,22528
v_lshlrev_b32 v230, 16, v231
Mp 44,48,200
v_and_b32 v231, 0xffff0000, v231
Mp 44,52,216
Rb 36,10,24576
s_waitcnt vmcnt(3) lgkmcnt(10)
buffer_store_dword v6, v7, s[20:23], s68 offen offset:0 sc0 sc1
Mp 36,52,208
Mp 36,56,224
Rb 40,10,25600
Mp 44,56,232
Mp 44,60,248
Rb 44,10,26624
Mp 36,60,240
Mv 32,194,104
Mv 40,194,108
Mv 36,192,104
s_waitcnt lgkmcnt(8)
Mv 44,192,108
v_mfma_f32_16x16x32_bf16 v[48:51], a[0:3], v[128:131], 0
Rb 48,10,27648
v_mfma_f32_16x16x32_bf16 v[56:59], a[0:3], v[136:139], 0
Mp 56,4,152
Rb 52,10,28672
Mp 48,4,144
Cv 32,32,33
Cv 33,34,35
Mp 48,8,160
Rb 56,10,29696
Cv 34,36,37
Mp 56,8,168
Cv 35,38,39
Mp 56,12,184
Rb 60,10,30720
s_waitcnt lgkmcnt(8)
Mp 48,12,176
s_add_u32 s29, s73, 0x4c00
buffer_store_dwordx4 v[32:35], v1, s[20:23], s29 offen sc0 sc1
Mp 48,16,192
Mp 56,16,200
Mp 56,20,216
Mp 48,20,208
Mp 48,24,224
Mp 56,24,232
Cv 40,40,41
Cv 41,42,43
buffer_store_dword v6, v7, s[20:23], s68 offen offset:128 sc0 sc1
Mp 56,28,248
Cv 42,44,45
s_waitcnt lgkmcnt(4)
Mp 48,28,240
)" R"(
Cv 43,46,47
v_mfma_f32_16x16x32_bf16 v[52:55], a[32:35], v[128:131], 0
s_add_u32 s29, s73, 0x5000
buffer_store_dwordx4 v[40:43], v1, s[20:23], s29 offen sc0 sc1
v_mfma_f32_16x16x32_bf16 v[60:63], a[32:35], v[136:139], 0
buffer_load_dword v4, v165, s[20:23], s40 offen sc0 sc1
Mp 60,36,152
Mp 52,36,144
Mp 52,40,160
Mp 60,40,168
Mp 60,44,184
s_waitcnt lgkmcnt(0)
Mp 52,44,176
Mp 52,48,192
Mp 60,48,200
Mp 60,52,216
Mp 52,52,208
Mp 52,56,224
Mp 60,56,232
Mp 60,60,248
Mp 52,60,240
Mv 48,194,112
Mv 56,194,116
Mv 52,192,112
Mv 60,192,116
s_nop 7
Cv 48,48,49
Cv 49,50,51
Cv 50,52,53
Cv 51,54,55
s_add_u32 s29, s73, 0x5400
buffer_store_dwordx4 v[48:51], v1, s[20:23], s29 offen sc0 sc1
Cv 56,56,57
Cv 57,58,59
Cv 58,60,61
Cv 59,62,63
s_add_u32 s29, s73, 0x5800
buffer_store_dwordx4 v[56:59], v1, s[20:23], s29 offen sc0 sc1
.LzH21:
s_cbranch_execz .LzHR
.LzEND:
s_cmp_gt_u32 s82, 223
s_cbranch_scc1 .LzFIN
s_cmp_lt_u32 s82, 14
s_cbranch_scc1 .LzBB
.LzBBR:
s_waitcnt vmcnt(2)
.LzS0:
v_cmp_ne_u32_e32 vcc, s3, v4
s_lshr_b32 s28, vcc_lo, 1
s_or_b32 s28, s28, vcc_lo
s_lshr_b32 s29, s28, 2
s_or_b32 s28, s28, s29
s_andn2_b32 s28, s93, s28
s_sub_u32 s53, s53, 1
s_cselect_b32 s28, s93, s28
s_cmov_b32 s53, 0
s_xor_b32 s93, s93, s28
s_or_b32 s57, s57, s28
s_cbranch_scc1 .LzS2
s_sleep 4
s_branch .LzSk3
.LzS2:
s_bitcmp0_b32 s57, 0
)" R"(
s_cbranch_scc1 .LzSk0
s_add_u32 s29, s55, 0x0
buffer_load_dwordx4 a[128:131], v164, s[20:23], s29 offen offset:0 sc0 sc1
buffer_load_dwordx4 a[132:135], v164, s[20:23], s29 offen offset:256 sc0 sc1
buffer_load_dwordx4 a[136:139], v164, s[20:23], s29 offen offset:512 sc0 sc1
buffer_load_dwordx4 a[140:143], v164, s[20:23], s29 offen offset:768 sc0 sc1
.LzSk0:
s_bitcmp0_b32 s57, 4
s_cbranch_scc1 .LzSk1
s_add_u32 s29, s55, 0x8000000
buffer_load_dwordx4 a[144:147], v183, s[20:23], s29 offen offset:0 sc0 sc1
buffer_load_dwordx4 a[148:151], v183, s[20:23], s29 offen offset:256 sc0 sc1
buffer_load_dwordx4 a[152:155], v183, s[20:23], s29 offen offset:512 sc0 sc1
buffer_load_dwordx4 a[156:159], v183, s[20:23], s29 offen offset:768 sc0 sc1
.LzSk1:
s_bitcmp0_b32 s57, 8
s_cbranch_scc1 .LzSk2
s_add_u32 s29, s55, 0x8000
buffer_load_dwordx4 a[160:163], v164, s[20:23], s29 offen offset:0 sc0 sc1
buffer_load_dwordx4 a[164:167], v164, s[20:23], s29 offen offset:256 sc0 sc1
buffer_load_dwordx4 a[168:171], v164, s[20:23], s29 offen offset:512 sc0 sc1
buffer_load_dwordx4 a[172:175], v164, s[20:23], s29 offen offset:768 sc0 sc1
.LzSk2:
s_bitcmp0_b32 s57, 12
s_cbranch_scc1 .LzSk3
s_add_u32 s29, s55, 0x8008000
buffer_load_dwordx4 a[176:179], v183, s[20:23], s29 offen offset:0 sc0 sc1
buffer_load_dwordx4 a[180:183], v183, s[20:23], s29 offen offset:256 sc0 sc1
buffer_load_dwordx4 a[184:187], v183, s[20:23], s29 offen offset:512 sc0 sc1
buffer_load_dwordx4 a[188:191], v183, s[20:23], s29 offen offset:768 sc0 sc1
.LzSk3:
s_cmp_eq_u32 s93, 0
s_cbranch_scc1 .LzS1
buffer_load_dword v4, v165, s[20:23], s40 offen sc0 sc1
.LzS1:
s_waitcnt vmcnt(0)
s_bitcmp0_b32 s57, 0
s_cbranch_scc1 .LzSv0
v_accvgpr_read_b32 v173, a131
v_accvgpr_read_b32 v174, a135
v_accvgpr_read_b32 v175, a139
v_accvgpr_read_b32 v180, a143
v_max3_u32 v173, v173, v174, v175
v_max_u32 v173, v173, v180
v_cmp_eq_u32_e32 vcc, -1, v173
s_cbranch_vccnz .LzSv0
s_bitset0_b32 s57, 0
.LzSv0:
s_bitcmp0_b32 s57, 4
s_cbranch_scc1 .LzSv1
v_accvgpr_read_b32 v173, a147
v_accvgpr_read_b32 v174, a151
v_accvgpr_read_b32 v175, a155
v_accvgpr_read_b32 v180, a159
v_max3_u32 v173, v173, v174, v175
v_max_u32 v173, v173, v180
v_cmp_eq_u32_e32 vcc, -1, v173
s_cbranch_vccnz .LzSv1
s_bitset0_b32 s57, 4
.LzSv1:
s_bitcmp0_b32 s57, 8
s_cbranch_scc1 .LzSv2
v_accvgpr_read_b32 v173, a163
v_accvgpr_read_b32 v174, a167
)" R"(
v_accvgpr_read_b32 v175, a171
v_accvgpr_read_b32 v180, a175
v_max3_u32 v173, v173, v174, v175
v_max_u32 v173, v173, v180
v_cmp_eq_u32_e32 vcc, -1, v173
s_cbranch_vccnz .LzSv2
s_bitset0_b32 s57, 8
.LzSv2:
s_bitcmp0_b32 s57, 12
s_cbranch_scc1 .LzSv3
v_accvgpr_read_b32 v173, a179
v_accvgpr_read_b32 v174, a183
v_accvgpr_read_b32 v175, a187
v_accvgpr_read_b32 v180, a191
v_max3_u32 v173, v173, v174, v175
v_max_u32 v173, v173, v180
v_cmp_eq_u32_e32 vcc, -1, v173
s_cbranch_vccnz .LzSv3
s_bitset0_b32 s57, 12
.LzSv3:
s_cmp_eq_u32 s53, 0
s_cmov_b32 s57, 0
s_or_b32 s28, s57, s93
s_cbranch_scc1 .LzS0
s_bfe_u32 s28, s63, 0x70004
s_cmp_eq_u32 s28, s58
s_cbranch_scc1 .LzRT
s_bfe_u32 s28, s63, 0x70014
s_cmp_eq_u32 s28, s58
s_cbranch_scc1 .LzRT
.LzRT3:
v_mfma_f32_16x16x32_bf16 v[80:83], v[132:135], a[128:131], 0
v_mfma_f32_16x16x32_bf16 v[88:91], v[132:135], a[160:163], 0
v_mfma_f32_16x16x32_bf16 v[84:87], v[132:135], a[136:139], 0
v_mfma_f32_16x16x32_bf16 v[92:95], v[132:135], a[168:171], 0
v_mfma_f32_16x16x32_bf16 v[80:83], v[140:143], a[132:135], v[80:83]
v_mfma_f32_16x16x32_bf16 v[88:91], v[140:143], a[164:167], v[88:91]
v_mfma_f32_16x16x32_bf16 v[84:87], v[140:143], a[140:143], v[84:87]
v_mfma_f32_16x16x32_bf16 v[92:95], v[140:143], a[172:175], v[92:95]
v_mfma_f32_16x16x32_bf16 v[80:83], v[132:135], a[144:147], v[80:83]
v_mfma_f32_16x16x32_bf16 v[88:91], v[132:135], a[176:179], v[88:91]
v_mfma_f32_16x16x32_bf16 v[84:87], v[132:135], a[152:155], v[84:87]
v_mfma_f32_16x16x32_bf16 v[92:95], v[132:135], a[184:187], v[92:95]
v_mfma_f32_16x16x32_bf16 v[80:83], v[140:143], a[148:151], v[80:83]
v_mfma_f32_16x16x32_bf16 v[88:91], v[140:143], a[180:183], v[88:91]
v_mfma_f32_16x16x32_bf16 v[84:87], v[140:143], a[156:159], v[84:87]
v_mfma_f32_16x16x32_bf16 v[92:95], v[140:143], a[188:191], v[92:95]
v_lshrrev_b32 v173, 4, v0
v_and_b32 v173, 1, v173
v_mad_u32_u24 v166, v173, 24, v166
s_nop 1
Cv 81,80,81
v_lshlrev_b32 v80, 16, v81
v_and_b32 v81, 0xffff0000, v81
Cv 83,82,83
v_lshlrev_b32 v82, 16, v83
v_and_b32 v83, 0xffff0000, v83
Cv 89,88,89
v_lshlrev_b32 v88, 16, v89
v_and_b32 v89, 0xffff0000, v89
Cv 91,90,91
v_lshlrev_b32 v90, 16, v91
v_and_b32 v91, 0xffff0000, v91
Cv 85,84,85
)" R"(
v_lshlrev_b32 v84, 16, v85
v_and_b32 v85, 0xffff0000, v85
Cv 87,86,87
v_lshlrev_b32 v86, 16, v87
v_and_b32 v87, 0xffff0000, v87
Cv 93,92,93
v_lshlrev_b32 v92, 16, v93
v_and_b32 v93, 0xffff0000, v93
Cv 95,94,95
v_lshlrev_b32 v94, 16, v95
v_and_b32 v95, 0xffff0000, v95
v_mul_f32_e32 v80, v167, v80
v_mul_f32_e32 v81, v167, v81
v_mul_f32_e32 v82, v167, v82
v_mul_f32_e32 v83, v167, v83
v_mul_f32_e32 v88, v167, v88
v_mul_f32_e32 v89, v167, v89
v_mul_f32_e32 v90, v167, v90
v_mul_f32_e32 v91, v167, v91
v_mul_f32_e32 v84, v167, v84
v_mul_f32_e32 v85, v167, v85
v_mul_f32_e32 v86, v167, v86
v_mul_f32_e32 v87, v167, v87
v_mul_f32_e32 v92, v167, v92
v_mul_f32_e32 v93, v167, v93
v_mul_f32_e32 v94, v167, v94
v_mul_f32_e32 v95, v167, v95
Cv 81,80,81
v_lshlrev_b32 v80, 16, v81
v_and_b32 v81, 0xffff0000, v81
Cv 83,82,83
v_lshlrev_b32 v82, 16, v83
v_and_b32 v83, 0xffff0000, v83
Cv 89,88,89
v_lshlrev_b32 v88, 16, v89
v_and_b32 v89, 0xffff0000, v89
Cv 91,90,91
v_lshlrev_b32 v90, 16, v91
v_and_b32 v91, 0xffff0000, v91
Cv 85,84,85
v_lshlrev_b32 v84, 16, v85
v_and_b32 v85, 0xffff0000, v85
Cv 87,86,87
v_lshlrev_b32 v86, 16, v87
v_and_b32 v87, 0xffff0000, v87
Cv 93,92,93
v_lshlrev_b32 v92, 16, v93
v_and_b32 v93, 0xffff0000, v93
Cv 95,94,95
v_lshlrev_b32 v94, 16, v95
v_and_b32 v95, 0xffff0000, v95
v_add_f32_e32 v80, v80, v204
v_add_f32_e32 v81, v81, v205
v_add_f32_e32 v82, v82, v206
v_add_f32_e32 v83, v83, v207
v_add_f32_e32 v88, v88, v220
v_add_f32_e32 v89, v89, v221
v_add_f32_e32 v90, v90, v222
v_add_f32_e32 v91, v91, v223
v_add_f32_e32 v84, v84, v212
v_add_f32_e32 v85, v85, v213
v_add_f32_e32 v86, v86, v214
v_add_f32_e32 v87, v87, v215
v_add_f32_e32 v92, v92, v228
)" R"(
v_add_f32_e32 v93, v93, v229
v_add_f32_e32 v94, v94, v230
v_add_f32_e32 v95, v95, v231
Cv 64,80,81
Cv 65,82,83
Cv 66,84,85
Cv 67,86,87
Cv 68,88,89
Cv 69,90,91
Cv 70,92,93
Cv 71,94,95
v_permlane16_swap_b32_e32 v64, v66
v_permlane16_swap_b32_e32 v65, v67
buffer_store_dwordx4 v[64:67], v166, s[8:11], 0 offen offset:0 sc0 sc1
v_permlane16_swap_b32_e32 v68, v70
v_permlane16_swap_b32_e32 v69, v71
buffer_store_dwordx4 v[68:71], v166, s[8:11], 0 offen offset:448 sc0 sc1
s_or_b32 s28, s82, s33
s_cbranch_scc1 .LzFIN
s_mov_b32 s28, 0x1508400
.LzH22:
s_cbranch_execz .LzHR
buffer_store_dword v6, v7, s[20:23], s28 offen offset:0 sc0 sc1
.LzFIN:
s_endpgm
.LzRT:
s_and_b32 s28, s63, 0x7ff
s_mul_i32 s28, s28, 896
v_subrev_u32 v174, s28, v166
s_bfe_u32 s28, s63, 0xb0010
s_mul_i32 s28, s28, 896
v_subrev_u32 v175, s28, v166
s_movk_i32 s28, 896
v_cmp_gt_u32_e64 s[24:25], s28, v175
v_cmp_gt_u32_e32 vcc, s28, v174
v_add_u32 v175, s28, v175
s_or_b64 s[24:25], s[24:25], vcc
v_cndmask_b32_e32 v174, v175, v174, vcc
v_lshrrev_b32 v174, 5, v174
v_lshlrev_b32 v174, 7, v174
v_and_or_b32 v174, v0, 48, v174
s_mov_b64 exec, s[24:25]
s_mov_b32 s29, 0x1509000
.LzRT1:
buffer_load_dwordx4 v[64:67], v174, s[20:23], s29 offen offset:0 sc0 sc1
buffer_load_dwordx4 v[68:71], v174, s[20:23], s29 offen offset:128 sc0 sc1
buffer_load_dwordx4 v[72:75], v174, s[20:23], s29 offen offset:1792 sc0 sc1
buffer_load_dwordx4 v[76:79], v174, s[20:23], s29 offen offset:1920 sc0 sc1
s_waitcnt vmcnt(0)
v_xor_b32 v175, s3, v65
v_xor_b32 v173, s3, v67
v_or_b32 v175, v175, v173
v_xor_b32 v173, s3, v69
v_or_b32 v175, v175, v173
v_xor_b32 v173, s3, v71
v_or_b32 v175, v175, v173
v_xor_b32 v173, s3, v73
v_or_b32 v175, v175, v173
v_xor_b32 v173, s3, v75
v_or_b32 v175, v175, v173
v_xor_b32 v173, s3, v77
v_or_b32 v175, v175, v173
v_xor_b32 v173, s3, v79
v_or_b32 v175, v175, v173
)" R"(
v_cmp_ne_u32_e32 vcc, 0, v175
s_cbranch_vccz .LzRT2
s_sleep 2
s_sub_u32 s53, s53, 1
s_cbranch_scc0 .LzRT1
s_mov_b32 s53, 0
.LzRT2:
v_mov_b32 v188, v64
v_mov_b32 v189, v66
v_mov_b32 v190, v68
v_mov_b32 v191, v70
v_mov_b32 v196, v72
v_mov_b32 v197, v74
v_mov_b32 v198, v76
v_mov_b32 v199, v78
v_sub_f32_e32 v172, 1.0, v167
Cv 172,172,172
v_lshlrev_b32 v172, 16, v172
v_lshlrev_b32 v204, 16, v188
v_and_b32 v205, 0xffff0000, v188
v_mul_f32_e32 v204, v172, v204
v_mul_f32_e32 v205, v172, v205
Cv 205,204,205
v_lshlrev_b32 v204, 16, v205
v_and_b32 v205, 0xffff0000, v205
v_lshlrev_b32 v206, 16, v189
v_and_b32 v207, 0xffff0000, v189
v_mul_f32_e32 v206, v172, v206
v_mul_f32_e32 v207, v172, v207
Cv 207,206,207
v_lshlrev_b32 v206, 16, v207
v_and_b32 v207, 0xffff0000, v207
v_lshlrev_b32 v212, 16, v190
v_and_b32 v213, 0xffff0000, v190
v_mul_f32_e32 v212, v172, v212
v_mul_f32_e32 v213, v172, v213
Cv 213,212,213
v_lshlrev_b32 v212, 16, v213
v_and_b32 v213, 0xffff0000, v213
v_lshlrev_b32 v214, 16, v191
v_and_b32 v215, 0xffff0000, v191
v_mul_f32_e32 v214, v172, v214
v_mul_f32_e32 v215, v172, v215
Cv 215,214,215
v_lshlrev_b32 v214, 16, v215
v_and_b32 v215, 0xffff0000, v215
v_lshlrev_b32 v220, 16, v196
v_and_b32 v221, 0xffff0000, v196
v_mul_f32_e32 v220, v172, v220
v_mul_f32_e32 v221, v172, v221
Cv 221,220,221
v_lshlrev_b32 v220, 16, v221
v_and_b32 v221, 0xffff0000, v221
v_lshlrev_b32 v222, 16, v197
v_and_b32 v223, 0xffff0000, v197
v_mul_f32_e32 v222, v172, v222
v_mul_f32_e32 v223, v172, v223
Cv 223,222,223
v_lshlrev_b32 v222, 16, v223
v_and_b32 v223, 0xffff0000, v223
v_lshlrev_b32 v228, 16, v198
v_and_b32 v229, 0xffff0000, v198
v_mul_f32_e32 v228, v172, v228
v_mul_f32_e32 v229, v172, v229
)" R"(
Cv 229,228,229
v_lshlrev_b32 v228, 16, v229
v_and_b32 v229, 0xffff0000, v229
v_lshlrev_b32 v230, 16, v199
v_and_b32 v231, 0xffff0000, v199
v_mul_f32_e32 v230, v172, v230
v_mul_f32_e32 v231, v172, v231
Cv 231,230,231
v_lshlrev_b32 v230, 16, v231
v_and_b32 v231, 0xffff0000, v231
s_mov_b64 exec, -1
s_branch .LzRT3
.LzH23:
s_cbranch_execz .LzH39
.LzMASK0:
s_lshl1_add_u32 s28, s57, s33
s_mov_b32 s29, 0xff800000
s_cmp_gt_u32 s28, 5
s_cbranch_scc1 .LzMASKE0
s_cmp_eq_u32 s28, 5
s_cbranch_scc1 .LzMASKB0
v_mov_b32 v72, s29
v_mov_b32 v73, s29
v_mov_b32 v74, s29
v_mov_b32 v75, s29
v_mov_b32 v80, s29
v_mov_b32 v81, s29
v_mov_b32 v82, s29
v_mov_b32 v83, s29
v_mov_b32 v84, s29
v_mov_b32 v85, s29
v_mov_b32 v86, s29
v_mov_b32 v87, s29
v_mov_b32 v88, s29
v_mov_b32 v89, s29
v_mov_b32 v90, s29
v_mov_b32 v91, s29
v_mov_b32 v92, s29
v_mov_b32 v93, s29
v_mov_b32 v94, s29
v_mov_b32 v95, s29
s_cmp_eq_u32 s28, 4
s_cbranch_scc1 .LzMASKA0
v_mov_b32 v64, s29
v_mov_b32 v65, s29
v_mov_b32 v66, s29
v_mov_b32 v67, s29
v_mov_b32 v68, s29
v_mov_b32 v69, s29
v_mov_b32 v70, s29
v_mov_b32 v71, s29
v_mov_b32 v76, s29
v_mov_b32 v77, s29
v_mov_b32 v78, s29
v_mov_b32 v79, s29
.LzMASKE0:
s_setpc_b64 s[30:31]
.LzMASKA0:
s_mov_b32 exec_lo, 0xf0000
s_mov_b32 exec_hi, 0xfff00ff
v_mov_b32 v64, s29
v_mov_b32 v76, s29
s_mov_b32 exec_lo, 0x1f0001
s_mov_b32 exec_hi, 0x1fff01ff
)" R"(
v_mov_b32 v65, s29
v_mov_b32 v77, s29
s_mov_b32 exec_lo, 0x3f0003
s_mov_b32 exec_hi, 0x3fff03ff
v_mov_b32 v66, s29
v_mov_b32 v78, s29
s_mov_b32 exec_lo, 0x7f0007
s_mov_b32 exec_hi, 0x7fff07ff
v_mov_b32 v67, s29
v_mov_b32 v79, s29
s_mov_b64 exec, -1
s_setpc_b64 s[30:31]
.LzMASKB0:
s_nop 1
v_mov_b32 v88, s29
v_mov_b32 v89, s29
v_mov_b32 v90, s29
v_mov_b32 v91, s29
s_mov_b32 exec_lo, 0xf0000
s_mov_b32 exec_hi, 0xfff00ff
v_mov_b32 v80, s29
v_mov_b32 v92, s29
s_mov_b32 exec_lo, 0x1f0001
s_mov_b32 exec_hi, 0x1fff01ff
v_mov_b32 v81, s29
v_mov_b32 v93, s29
s_mov_b32 exec_lo, 0x3f0003
s_mov_b32 exec_hi, 0x3fff03ff
v_mov_b32 v82, s29
v_mov_b32 v94, s29
s_mov_b32 exec_lo, 0x7f0007
s_mov_b32 exec_hi, 0x7fff07ff
v_mov_b32 v83, s29
v_mov_b32 v95, s29
s_mov_b64 exec, -1
s_setpc_b64 s[30:31]
.LzFIX0:
s_nop 7
v_max_f32_e32 v28, v64, v65
v_max3_f32 v28, v28, v66, v67
v_max3_f32 v28, v28, v72, v73
v_max3_f32 v28, v28, v74, v75
v_max3_f32 v28, v28, v80, v81
v_max3_f32 v28, v28, v82, v83
v_max3_f32 v28, v28, v88, v89
v_max3_f32 v28, v28, v90, v91
v_mov_b32 v24, v28
s_nop 1
v_permlane32_swap_b32_e32 v28, v24
v_max_f32_e32 v28, v28, v24
v_mov_b32 v24, v28
s_nop 1
v_permlane16_swap_b32_e32 v28, v24
v_max_f32_e32 v28, v28, v24
v_max_f32_e32 v29, v68, v69
v_max3_f32 v29, v29, v70, v71
v_max3_f32 v29, v29, v76, v77
v_max3_f32 v29, v29, v78, v79
v_max3_f32 v29, v29, v84, v85
v_max3_f32 v29, v29, v86, v87
v_max3_f32 v29, v29, v92, v93
v_max3_f32 v29, v29, v94, v95
v_mov_b32 v24, v29
s_nop 1
)" R"(
v_permlane32_swap_b32_e32 v29, v24
v_max_f32_e32 v29, v29, v24
v_mov_b32 v24, v29
s_nop 1
v_permlane16_swap_b32_e32 v29, v24
v_max_f32_e32 v29, v29, v24
s_bitcmp1_b32 s88, 0
s_cbranch_scc1 .LzFIXF0
v_max_f32_e32 v28, 0, v28
v_sub_f32_e32 v24, 0, v28
v_exp_f32_e32 v24, v24
s_nop 0
v_mul_f32_e32 v12, v24, v12
v_mul_f32_e32 v128, v24, v128
v_mul_f32_e32 v129, v24, v129
v_mul_f32_e32 v130, v24, v130
v_mul_f32_e32 v131, v24, v131
v_mul_f32_e32 v132, v24, v132
v_mul_f32_e32 v133, v24, v133
v_mul_f32_e32 v134, v24, v134
v_mul_f32_e32 v135, v24, v135
v_mul_f32_e32 v144, v24, v144
v_mul_f32_e32 v145, v24, v145
v_mul_f32_e32 v146, v24, v146
v_mul_f32_e32 v147, v24, v147
v_mul_f32_e32 v148, v24, v148
v_mul_f32_e32 v149, v24, v149
v_mul_f32_e32 v150, v24, v150
v_mul_f32_e32 v151, v24, v151
v_mul_f32_e32 v160, v24, v160
v_mul_f32_e32 v161, v24, v161
v_mul_f32_e32 v162, v24, v162
v_mul_f32_e32 v163, v24, v163
v_mul_f32_e32 v164, v24, v164
v_mul_f32_e32 v165, v24, v165
v_mul_f32_e32 v166, v24, v166
v_mul_f32_e32 v167, v24, v167
v_mul_f32_e32 v176, v24, v176
v_mul_f32_e32 v177, v24, v177
v_mul_f32_e32 v178, v24, v178
v_mul_f32_e32 v179, v24, v179
v_mul_f32_e32 v180, v24, v180
v_mul_f32_e32 v181, v24, v181
v_mul_f32_e32 v182, v24, v182
v_mul_f32_e32 v183, v24, v183
v_mul_f32_e32 v192, v24, v192
v_mul_f32_e32 v193, v24, v193
v_mul_f32_e32 v194, v24, v194
v_mul_f32_e32 v195, v24, v195
v_mul_f32_e32 v196, v24, v196
v_mul_f32_e32 v197, v24, v197
v_mul_f32_e32 v198, v24, v198
v_mul_f32_e32 v199, v24, v199
v_mul_f32_e32 v208, v24, v208
v_mul_f32_e32 v209, v24, v209
v_mul_f32_e32 v210, v24, v210
v_mul_f32_e32 v211, v24, v211
v_mul_f32_e32 v212, v24, v212
v_mul_f32_e32 v213, v24, v213
v_mul_f32_e32 v214, v24, v214
v_mul_f32_e32 v215, v24, v215
v_mul_f32_e32 v224, v24, v224
v_mul_f32_e32 v225, v24, v225
v_mul_f32_e32 v226, v24, v226
)" R"(
v_mul_f32_e32 v227, v24, v227
v_mul_f32_e32 v228, v24, v228
v_mul_f32_e32 v229, v24, v229
v_mul_f32_e32 v230, v24, v230
v_mul_f32_e32 v231, v24, v231
v_mul_f32_e32 v240, v24, v240
v_mul_f32_e32 v241, v24, v241
v_mul_f32_e32 v242, v24, v242
v_mul_f32_e32 v243, v24, v243
v_mul_f32_e32 v244, v24, v244
v_mul_f32_e32 v245, v24, v245
v_mul_f32_e32 v246, v24, v246
v_mul_f32_e32 v247, v24, v247
v_max_f32_e32 v29, 0, v29
v_sub_f32_e32 v24, 0, v29
v_exp_f32_e32 v24, v24
s_nop 0
v_mul_f32_e32 v13, v24, v13
v_mul_f32_e32 v136, v24, v136
v_mul_f32_e32 v137, v24, v137
v_mul_f32_e32 v138, v24, v138
v_mul_f32_e32 v139, v24, v139
v_mul_f32_e32 v140, v24, v140
v_mul_f32_e32 v141, v24, v141
v_mul_f32_e32 v142, v24, v142
v_mul_f32_e32 v143, v24, v143
v_mul_f32_e32 v152, v24, v152
v_mul_f32_e32 v153, v24, v153
v_mul_f32_e32 v154, v24, v154
v_mul_f32_e32 v155, v24, v155
v_mul_f32_e32 v156, v24, v156
v_mul_f32_e32 v157, v24, v157
v_mul_f32_e32 v158, v24, v158
v_mul_f32_e32 v159, v24, v159
v_mul_f32_e32 v168, v24, v168
v_mul_f32_e32 v169, v24, v169
v_mul_f32_e32 v170, v24, v170
v_mul_f32_e32 v171, v24, v171
v_mul_f32_e32 v172, v24, v172
v_mul_f32_e32 v173, v24, v173
v_mul_f32_e32 v174, v24, v174
v_mul_f32_e32 v175, v24, v175
v_mul_f32_e32 v184, v24, v184
v_mul_f32_e32 v185, v24, v185
v_mul_f32_e32 v186, v24, v186
v_mul_f32_e32 v187, v24, v187
v_mul_f32_e32 v188, v24, v188
v_mul_f32_e32 v189, v24, v189
v_mul_f32_e32 v190, v24, v190
v_mul_f32_e32 v191, v24, v191
v_mul_f32_e32 v200, v24, v200
v_mul_f32_e32 v201, v24, v201
v_mul_f32_e32 v202, v24, v202
v_mul_f32_e32 v203, v24, v203
v_mul_f32_e32 v204, v24, v204
v_mul_f32_e32 v205, v24, v205
v_mul_f32_e32 v206, v24, v206
v_mul_f32_e32 v207, v24, v207
v_mul_f32_e32 v216, v24, v216
v_mul_f32_e32 v217, v24, v217
v_mul_f32_e32 v218, v24, v218
v_mul_f32_e32 v219, v24, v219
v_mul_f32_e32 v220, v24, v220
v_mul_f32_e32 v221, v24, v221
)" R"(
v_mul_f32_e32 v222, v24, v222
v_mul_f32_e32 v223, v24, v223
v_mul_f32_e32 v232, v24, v232
v_mul_f32_e32 v233, v24, v233
v_mul_f32_e32 v234, v24, v234
v_mul_f32_e32 v235, v24, v235
v_mul_f32_e32 v236, v24, v236
v_mul_f32_e32 v237, v24, v237
v_mul_f32_e32 v238, v24, v238
v_mul_f32_e32 v239, v24, v239
v_mul_f32_e32 v248, v24, v248
v_mul_f32_e32 v249, v24, v249
v_mul_f32_e32 v250, v24, v250
v_mul_f32_e32 v251, v24, v251
v_mul_f32_e32 v252, v24, v252
v_mul_f32_e32 v253, v24, v253
v_mul_f32_e32 v254, v24, v254
v_mul_f32_e32 v255, v24, v255
s_branch .LzFIXG0
.LzFIXF0:
v_cmp_lt_f32_e32 vcc, 0xff7fffff, v28
s_nop 1
v_cndmask_b32_e32 v28, 0, v28, vcc
v_cmp_lt_f32_e32 vcc, 0xff7fffff, v29
s_nop 1
v_cndmask_b32_e32 v29, 0, v29, vcc
.LzFIXG0:
v_sub_f32_e32 v16, v16, v28
v_sub_f32_e32 v17, v17, v28
v_sub_f32_e32 v18, v18, v28
v_sub_f32_e32 v19, v19, v28
v_sub_f32_e32 v64, v64, v28
v_sub_f32_e32 v65, v65, v28
v_sub_f32_e32 v66, v66, v28
v_sub_f32_e32 v67, v67, v28
v_sub_f32_e32 v72, v72, v28
v_sub_f32_e32 v73, v73, v28
v_sub_f32_e32 v74, v74, v28
v_sub_f32_e32 v75, v75, v28
v_sub_f32_e32 v80, v80, v28
v_sub_f32_e32 v81, v81, v28
v_sub_f32_e32 v82, v82, v28
v_sub_f32_e32 v83, v83, v28
v_sub_f32_e32 v88, v88, v28
v_sub_f32_e32 v89, v89, v28
v_sub_f32_e32 v90, v90, v28
v_sub_f32_e32 v91, v91, v28
v_sub_f32_e32 v20, v20, v29
v_sub_f32_e32 v21, v21, v29
v_sub_f32_e32 v22, v22, v29
v_sub_f32_e32 v23, v23, v29
v_sub_f32_e32 v68, v68, v29
v_sub_f32_e32 v69, v69, v29
v_sub_f32_e32 v70, v70, v29
v_sub_f32_e32 v71, v71, v29
v_sub_f32_e32 v76, v76, v29
v_sub_f32_e32 v77, v77, v29
v_sub_f32_e32 v78, v78, v29
v_sub_f32_e32 v79, v79, v29
v_sub_f32_e32 v84, v84, v29
v_sub_f32_e32 v85, v85, v29
v_sub_f32_e32 v86, v86, v29
v_sub_f32_e32 v87, v87, v29
v_sub_f32_e32 v92, v92, v29
)" R"(
v_sub_f32_e32 v93, v93, v29
v_sub_f32_e32 v94, v94, v29
v_sub_f32_e32 v95, v95, v29
s_bitcmp0_b32 s88, 1
s_cbranch_scc1 .LzFIXH0
v_sub_f32_e32 v96, v96, v28
v_sub_f32_e32 v97, v97, v28
v_sub_f32_e32 v98, v98, v28
v_sub_f32_e32 v99, v99, v28
v_sub_f32_e32 v104, v104, v28
v_sub_f32_e32 v105, v105, v28
v_sub_f32_e32 v106, v106, v28
v_sub_f32_e32 v107, v107, v28
v_sub_f32_e32 v112, v112, v28
v_sub_f32_e32 v113, v113, v28
v_sub_f32_e32 v114, v114, v28
v_sub_f32_e32 v115, v115, v28
v_sub_f32_e32 v120, v120, v28
v_sub_f32_e32 v121, v121, v28
v_sub_f32_e32 v122, v122, v28
v_sub_f32_e32 v123, v123, v28
v_sub_f32_e32 v100, v100, v29
v_sub_f32_e32 v101, v101, v29
v_sub_f32_e32 v102, v102, v29
v_sub_f32_e32 v103, v103, v29
v_sub_f32_e32 v108, v108, v29
v_sub_f32_e32 v109, v109, v29
v_sub_f32_e32 v110, v110, v29
v_sub_f32_e32 v111, v111, v29
v_sub_f32_e32 v116, v116, v29
v_sub_f32_e32 v117, v117, v29
v_sub_f32_e32 v118, v118, v29
v_sub_f32_e32 v119, v119, v29
v_sub_f32_e32 v124, v124, v29
v_sub_f32_e32 v125, v125, v29
v_sub_f32_e32 v126, v126, v29
v_sub_f32_e32 v127, v127, v29
.LzFIXH0:
v_exp_f32_e32 v32, v64
v_exp_f32_e32 v28, v65
s_nop 0
v_add_f32_e32 v14, v32, v28
Cv 32,32,28
v_exp_f32_e32 v33, v66
v_exp_f32_e32 v29, v67
v_add_f32_e32 v14, v14, v33
v_add_f32_e32 v14, v14, v29
Cv 33,33,29
v_exp_f32_e32 v34, v72
v_exp_f32_e32 v24, v73
v_add_f32_e32 v14, v14, v34
v_add_f32_e32 v14, v14, v24
Cv 34,34,24
v_exp_f32_e32 v35, v74
v_exp_f32_e32 v25, v75
v_add_f32_e32 v14, v14, v35
v_add_f32_e32 v14, v14, v25
Cv 35,35,25
v_exp_f32_e32 v36, v68
v_exp_f32_e32 v26, v69
s_nop 0
v_add_f32_e32 v15, v36, v26
Cv 36,36,26
v_exp_f32_e32 v37, v70
)" R"(
v_exp_f32_e32 v27, v71
v_add_f32_e32 v15, v15, v37
v_add_f32_e32 v15, v15, v27
Cv 37,37,27
v_exp_f32_e32 v38, v76
v_exp_f32_e32 v28, v77
v_add_f32_e32 v15, v15, v38
v_add_f32_e32 v15, v15, v28
Cv 38,38,28
v_exp_f32_e32 v39, v78
v_exp_f32_e32 v29, v79
v_add_f32_e32 v15, v15, v39
v_add_f32_e32 v15, v15, v29
Cv 39,39,29
v_exp_f32_e32 v40, v80
v_exp_f32_e32 v24, v81
v_add_f32_e32 v14, v14, v40
v_add_f32_e32 v14, v14, v24
Cv 40,40,24
v_exp_f32_e32 v41, v82
v_exp_f32_e32 v25, v83
v_add_f32_e32 v14, v14, v41
v_add_f32_e32 v14, v14, v25
Cv 41,41,25
v_exp_f32_e32 v42, v88
v_exp_f32_e32 v26, v89
v_add_f32_e32 v14, v14, v42
v_add_f32_e32 v14, v14, v26
Cv 42,42,26
v_exp_f32_e32 v43, v90
v_exp_f32_e32 v27, v91
v_add_f32_e32 v14, v14, v43
v_add_f32_e32 v14, v14, v27
Cv 43,43,27
v_exp_f32_e32 v44, v84
v_exp_f32_e32 v28, v85
v_add_f32_e32 v15, v15, v44
v_add_f32_e32 v15, v15, v28
Cv 44,44,28
v_exp_f32_e32 v45, v86
v_exp_f32_e32 v29, v87
v_add_f32_e32 v15, v15, v45
v_add_f32_e32 v15, v15, v29
Cv 45,45,29
v_exp_f32_e32 v46, v92
v_exp_f32_e32 v24, v93
v_add_f32_e32 v15, v15, v46
v_add_f32_e32 v15, v15, v24
Cv 46,46,24
v_exp_f32_e32 v47, v94
v_exp_f32_e32 v25, v95
v_add_f32_e32 v15, v15, v47
v_add_f32_e32 v15, v15, v25
Cv 47,47,25
s_nop 1
s_setpc_b64 s[30:31]
.LzH24:
s_cbranch_execz .LzH40
.LzMASK1:
s_lshl1_add_u32 s28, s57, s33
s_mov_b32 s29, 0xff800000
s_cmp_gt_u32 s28, 5
s_cbranch_scc1 .LzMASKE1
s_cmp_eq_u32 s28, 5
)" R"(
s_cbranch_scc1 .LzMASKB1
v_mov_b32 v104, s29
v_mov_b32 v105, s29
v_mov_b32 v106, s29
v_mov_b32 v107, s29
v_mov_b32 v112, s29
v_mov_b32 v113, s29
v_mov_b32 v114, s29
v_mov_b32 v115, s29
v_mov_b32 v116, s29
v_mov_b32 v117, s29
v_mov_b32 v118, s29
v_mov_b32 v119, s29
v_mov_b32 v120, s29
v_mov_b32 v121, s29
v_mov_b32 v122, s29
v_mov_b32 v123, s29
v_mov_b32 v124, s29
v_mov_b32 v125, s29
v_mov_b32 v126, s29
v_mov_b32 v127, s29
s_cmp_eq_u32 s28, 4
s_cbranch_scc1 .LzMASKA1
v_mov_b32 v96, s29
v_mov_b32 v97, s29
v_mov_b32 v98, s29
v_mov_b32 v99, s29
v_mov_b32 v100, s29
v_mov_b32 v101, s29
v_mov_b32 v102, s29
v_mov_b32 v103, s29
v_mov_b32 v108, s29
v_mov_b32 v109, s29
v_mov_b32 v110, s29
v_mov_b32 v111, s29
.LzMASKE1:
s_setpc_b64 s[30:31]
.LzMASKA1:
s_mov_b32 exec_lo, 0xf0000
s_mov_b32 exec_hi, 0xfff00ff
v_mov_b32 v96, s29
v_mov_b32 v108, s29
s_mov_b32 exec_lo, 0x1f0001
s_mov_b32 exec_hi, 0x1fff01ff
v_mov_b32 v97, s29
v_mov_b32 v109, s29
s_mov_b32 exec_lo, 0x3f0003
s_mov_b32 exec_hi, 0x3fff03ff
v_mov_b32 v98, s29
v_mov_b32 v110, s29
s_mov_b32 exec_lo, 0x7f0007
s_mov_b32 exec_hi, 0x7fff07ff
v_mov_b32 v99, s29
v_mov_b32 v111, s29
s_mov_b64 exec, -1
s_setpc_b64 s[30:31]
.LzMASKB1:
s_nop 1
v_mov_b32 v120, s29
v_mov_b32 v121, s29
v_mov_b32 v122, s29
v_mov_b32 v123, s29
s_mov_b32 exec_lo, 0xf0000
s_mov_b32 exec_hi, 0xfff00ff
)" R"(
v_mov_b32 v112, s29
v_mov_b32 v124, s29
s_mov_b32 exec_lo, 0x1f0001
s_mov_b32 exec_hi, 0x1fff01ff
v_mov_b32 v113, s29
v_mov_b32 v125, s29
s_mov_b32 exec_lo, 0x3f0003
s_mov_b32 exec_hi, 0x3fff03ff
v_mov_b32 v114, s29
v_mov_b32 v126, s29
s_mov_b32 exec_lo, 0x7f0007
s_mov_b32 exec_hi, 0x7fff07ff
v_mov_b32 v115, s29
v_mov_b32 v127, s29
s_mov_b64 exec, -1
s_setpc_b64 s[30:31]
.LzFIX1:
s_nop 7
v_max_f32_e32 v28, v96, v97
v_max3_f32 v28, v28, v98, v99
v_max3_f32 v28, v28, v104, v105
v_max3_f32 v28, v28, v106, v107
v_max3_f32 v28, v28, v112, v113
v_max3_f32 v28, v28, v114, v115
v_max3_f32 v28, v28, v120, v121
v_max3_f32 v28, v28, v122, v123
v_mov_b32 v24, v28
s_nop 1
v_permlane32_swap_b32_e32 v28, v24
v_max_f32_e32 v28, v28, v24
v_mov_b32 v24, v28
s_nop 1
v_permlane16_swap_b32_e32 v28, v24
v_max_f32_e32 v28, v28, v24
v_max_f32_e32 v29, v100, v101
v_max3_f32 v29, v29, v102, v103
v_max3_f32 v29, v29, v108, v109
v_max3_f32 v29, v29, v110, v111
v_max3_f32 v29, v29, v116, v117
v_max3_f32 v29, v29, v118, v119
v_max3_f32 v29, v29, v124, v125
v_max3_f32 v29, v29, v126, v127
v_mov_b32 v24, v29
s_nop 1
v_permlane32_swap_b32_e32 v29, v24
v_max_f32_e32 v29, v29, v24
v_mov_b32 v24, v29
s_nop 1
v_permlane16_swap_b32_e32 v29, v24
v_max_f32_e32 v29, v29, v24
s_bitcmp1_b32 s88, 0
s_cbranch_scc1 .LzFIXF1
v_max_f32_e32 v28, 0, v28
v_sub_f32_e32 v24, 0, v28
v_exp_f32_e32 v24, v24
s_nop 0
v_mul_f32_e32 v12, v24, v12
v_mul_f32_e32 v128, v24, v128
v_mul_f32_e32 v129, v24, v129
v_mul_f32_e32 v130, v24, v130
v_mul_f32_e32 v131, v24, v131
v_mul_f32_e32 v132, v24, v132
v_mul_f32_e32 v133, v24, v133
v_mul_f32_e32 v134, v24, v134
)" R"(
v_mul_f32_e32 v135, v24, v135
v_mul_f32_e32 v144, v24, v144
v_mul_f32_e32 v145, v24, v145
v_mul_f32_e32 v146, v24, v146
v_mul_f32_e32 v147, v24, v147
v_mul_f32_e32 v148, v24, v148
v_mul_f32_e32 v149, v24, v149
v_mul_f32_e32 v150, v24, v150
v_mul_f32_e32 v151, v24, v151
v_mul_f32_e32 v160, v24, v160
v_mul_f32_e32 v161, v24, v161
v_mul_f32_e32 v162, v24, v162
v_mul_f32_e32 v163, v24, v163
v_mul_f32_e32 v164, v24, v164
v_mul_f32_e32 v165, v24, v165
v_mul_f32_e32 v166, v24, v166
v_mul_f32_e32 v167, v24, v167
v_mul_f32_e32 v176, v24, v176
v_mul_f32_e32 v177, v24, v177
v_mul_f32_e32 v178, v24, v178
v_mul_f32_e32 v179, v24, v179
v_mul_f32_e32 v180, v24, v180
v_mul_f32_e32 v181, v24, v181
v_mul_f32_e32 v182, v24, v182
v_mul_f32_e32 v183, v24, v183
v_mul_f32_e32 v192, v24, v192
v_mul_f32_e32 v193, v24, v193
v_mul_f32_e32 v194, v24, v194
v_mul_f32_e32 v195, v24, v195
v_mul_f32_e32 v196, v24, v196
v_mul_f32_e32 v197, v24, v197
v_mul_f32_e32 v198, v24, v198
v_mul_f32_e32 v199, v24, v199
v_mul_f32_e32 v208, v24, v208
v_mul_f32_e32 v209, v24, v209
v_mul_f32_e32 v210, v24, v210
v_mul_f32_e32 v211, v24, v211
v_mul_f32_e32 v212, v24, v212
v_mul_f32_e32 v213, v24, v213
v_mul_f32_e32 v214, v24, v214
v_mul_f32_e32 v215, v24, v215
v_mul_f32_e32 v224, v24, v224
v_mul_f32_e32 v225, v24, v225
v_mul_f32_e32 v226, v24, v226
v_mul_f32_e32 v227, v24, v227
v_mul_f32_e32 v228, v24, v228
v_mul_f32_e32 v229, v24, v229
v_mul_f32_e32 v230, v24, v230
v_mul_f32_e32 v231, v24, v231
v_mul_f32_e32 v240, v24, v240
v_mul_f32_e32 v241, v24, v241
v_mul_f32_e32 v242, v24, v242
v_mul_f32_e32 v243, v24, v243
v_mul_f32_e32 v244, v24, v244
v_mul_f32_e32 v245, v24, v245
v_mul_f32_e32 v246, v24, v246
v_mul_f32_e32 v247, v24, v247
v_max_f32_e32 v29, 0, v29
v_sub_f32_e32 v24, 0, v29
v_exp_f32_e32 v24, v24
s_nop 0
v_mul_f32_e32 v13, v24, v13
v_mul_f32_e32 v136, v24, v136
v_mul_f32_e32 v137, v24, v137
)" R"(
v_mul_f32_e32 v138, v24, v138
v_mul_f32_e32 v139, v24, v139
v_mul_f32_e32 v140, v24, v140
v_mul_f32_e32 v141, v24, v141
v_mul_f32_e32 v142, v24, v142
v_mul_f32_e32 v143, v24, v143
v_mul_f32_e32 v152, v24, v152
v_mul_f32_e32 v153, v24, v153
v_mul_f32_e32 v154, v24, v154
v_mul_f32_e32 v155, v24, v155
v_mul_f32_e32 v156, v24, v156
v_mul_f32_e32 v157, v24, v157
v_mul_f32_e32 v158, v24, v158
v_mul_f32_e32 v159, v24, v159
v_mul_f32_e32 v168, v24, v168
v_mul_f32_e32 v169, v24, v169
v_mul_f32_e32 v170, v24, v170
v_mul_f32_e32 v171, v24, v171
v_mul_f32_e32 v172, v24, v172
v_mul_f32_e32 v173, v24, v173
v_mul_f32_e32 v174, v24, v174
v_mul_f32_e32 v175, v24, v175
v_mul_f32_e32 v184, v24, v184
v_mul_f32_e32 v185, v24, v185
v_mul_f32_e32 v186, v24, v186
v_mul_f32_e32 v187, v24, v187
v_mul_f32_e32 v188, v24, v188
v_mul_f32_e32 v189, v24, v189
v_mul_f32_e32 v190, v24, v190
v_mul_f32_e32 v191, v24, v191
v_mul_f32_e32 v200, v24, v200
v_mul_f32_e32 v201, v24, v201
v_mul_f32_e32 v202, v24, v202
v_mul_f32_e32 v203, v24, v203
v_mul_f32_e32 v204, v24, v204
v_mul_f32_e32 v205, v24, v205
v_mul_f32_e32 v206, v24, v206
v_mul_f32_e32 v207, v24, v207
v_mul_f32_e32 v216, v24, v216
v_mul_f32_e32 v217, v24, v217
v_mul_f32_e32 v218, v24, v218
v_mul_f32_e32 v219, v24, v219
v_mul_f32_e32 v220, v24, v220
v_mul_f32_e32 v221, v24, v221
v_mul_f32_e32 v222, v24, v222
v_mul_f32_e32 v223, v24, v223
v_mul_f32_e32 v232, v24, v232
v_mul_f32_e32 v233, v24, v233
v_mul_f32_e32 v234, v24, v234
v_mul_f32_e32 v235, v24, v235
v_mul_f32_e32 v236, v24, v236
v_mul_f32_e32 v237, v24, v237
v_mul_f32_e32 v238, v24, v238
v_mul_f32_e32 v239, v24, v239
v_mul_f32_e32 v248, v24, v248
v_mul_f32_e32 v249, v24, v249
v_mul_f32_e32 v250, v24, v250
v_mul_f32_e32 v251, v24, v251
v_mul_f32_e32 v252, v24, v252
v_mul_f32_e32 v253, v24, v253
v_mul_f32_e32 v254, v24, v254
v_mul_f32_e32 v255, v24, v255
s_branch .LzFIXG1
.LzFIXF1:
)" R"(
v_cmp_lt_f32_e32 vcc, 0xff7fffff, v28
s_nop 1
v_cndmask_b32_e32 v28, 0, v28, vcc
v_cmp_lt_f32_e32 vcc, 0xff7fffff, v29
s_nop 1
v_cndmask_b32_e32 v29, 0, v29, vcc
.LzFIXG1:
v_sub_f32_e32 v16, v16, v28
v_sub_f32_e32 v17, v17, v28
v_sub_f32_e32 v18, v18, v28
v_sub_f32_e32 v19, v19, v28
v_sub_f32_e32 v96, v96, v28
v_sub_f32_e32 v97, v97, v28
v_sub_f32_e32 v98, v98, v28
v_sub_f32_e32 v99, v99, v28
v_sub_f32_e32 v104, v104, v28
v_sub_f32_e32 v105, v105, v28
v_sub_f32_e32 v106, v106, v28
v_sub_f32_e32 v107, v107, v28
v_sub_f32_e32 v112, v112, v28
v_sub_f32_e32 v113, v113, v28
v_sub_f32_e32 v114, v114, v28
v_sub_f32_e32 v115, v115, v28
v_sub_f32_e32 v120, v120, v28
v_sub_f32_e32 v121, v121, v28
v_sub_f32_e32 v122, v122, v28
v_sub_f32_e32 v123, v123, v28
v_sub_f32_e32 v20, v20, v29
v_sub_f32_e32 v21, v21, v29
v_sub_f32_e32 v22, v22, v29
v_sub_f32_e32 v23, v23, v29
v_sub_f32_e32 v100, v100, v29
v_sub_f32_e32 v101, v101, v29
v_sub_f32_e32 v102, v102, v29
v_sub_f32_e32 v103, v103, v29
v_sub_f32_e32 v108, v108, v29
v_sub_f32_e32 v109, v109, v29
v_sub_f32_e32 v110, v110, v29
v_sub_f32_e32 v111, v111, v29
v_sub_f32_e32 v116, v116, v29
v_sub_f32_e32 v117, v117, v29
v_sub_f32_e32 v118, v118, v29
v_sub_f32_e32 v119, v119, v29
v_sub_f32_e32 v124, v124, v29
v_sub_f32_e32 v125, v125, v29
v_sub_f32_e32 v126, v126, v29
v_sub_f32_e32 v127, v127, v29
s_bitcmp0_b32 s88, 1
s_cbranch_scc1 .LzFIXH1
v_sub_f32_e32 v64, v64, v28
v_sub_f32_e32 v65, v65, v28
v_sub_f32_e32 v66, v66, v28
v_sub_f32_e32 v67, v67, v28
v_sub_f32_e32 v72, v72, v28
v_sub_f32_e32 v73, v73, v28
v_sub_f32_e32 v74, v74, v28
v_sub_f32_e32 v75, v75, v28
v_sub_f32_e32 v80, v80, v28
v_sub_f32_e32 v81, v81, v28
v_sub_f32_e32 v82, v82, v28
v_sub_f32_e32 v83, v83, v28
v_sub_f32_e32 v88, v88, v28
v_sub_f32_e32 v89, v89, v28
v_sub_f32_e32 v90, v90, v28
)" R"(
v_sub_f32_e32 v91, v91, v28
v_sub_f32_e32 v68, v68, v29
v_sub_f32_e32 v69, v69, v29
v_sub_f32_e32 v70, v70, v29
v_sub_f32_e32 v71, v71, v29
v_sub_f32_e32 v76, v76, v29
v_sub_f32_e32 v77, v77, v29
v_sub_f32_e32 v78, v78, v29
v_sub_f32_e32 v79, v79, v29
v_sub_f32_e32 v84, v84, v29
v_sub_f32_e32 v85, v85, v29
v_sub_f32_e32 v86, v86, v29
v_sub_f32_e32 v87, v87, v29
v_sub_f32_e32 v92, v92, v29
v_sub_f32_e32 v93, v93, v29
v_sub_f32_e32 v94, v94, v29
v_sub_f32_e32 v95, v95, v29
.LzFIXH1:
v_exp_f32_e32 v48, v96
v_exp_f32_e32 v26, v97
s_nop 0
v_add_f32_e32 v14, v48, v26
Cv 48,48,26
v_exp_f32_e32 v49, v98
v_exp_f32_e32 v27, v99
v_add_f32_e32 v14, v14, v49
v_add_f32_e32 v14, v14, v27
Cv 49,49,27
v_exp_f32_e32 v50, v104
v_exp_f32_e32 v28, v105
v_add_f32_e32 v14, v14, v50
v_add_f32_e32 v14, v14, v28
Cv 50,50,28
v_exp_f32_e32 v51, v106
v_exp_f32_e32 v29, v107
v_add_f32_e32 v14, v14, v51
v_add_f32_e32 v14, v14, v29
Cv 51,51,29
v_exp_f32_e32 v52, v100
v_exp_f32_e32 v24, v101
s_nop 0
v_add_f32_e32 v15, v52, v24
Cv 52,52,24
v_exp_f32_e32 v53, v102
v_exp_f32_e32 v25, v103
v_add_f32_e32 v15, v15, v53
v_add_f32_e32 v15, v15, v25
Cv 53,53,25
v_exp_f32_e32 v54, v108
v_exp_f32_e32 v26, v109
v_add_f32_e32 v15, v15, v54
v_add_f32_e32 v15, v15, v26
Cv 54,54,26
v_exp_f32_e32 v55, v110
v_exp_f32_e32 v27, v111
v_add_f32_e32 v15, v15, v55
v_add_f32_e32 v15, v15, v27
Cv 55,55,27
v_exp_f32_e32 v56, v112
v_exp_f32_e32 v28, v113
v_add_f32_e32 v14, v14, v56
v_add_f32_e32 v14, v14, v28
Cv 56,56,28
v_exp_f32_e32 v57, v114
)" R"(
v_exp_f32_e32 v29, v115
v_add_f32_e32 v14, v14, v57
v_add_f32_e32 v14, v14, v29
Cv 57,57,29
v_exp_f32_e32 v58, v120
v_exp_f32_e32 v24, v121
v_add_f32_e32 v14, v14, v58
v_add_f32_e32 v14, v14, v24
Cv 58,58,24
v_exp_f32_e32 v59, v122
v_exp_f32_e32 v25, v123
v_add_f32_e32 v14, v14, v59
v_add_f32_e32 v14, v14, v25
Cv 59,59,25
v_exp_f32_e32 v60, v116
v_exp_f32_e32 v26, v117
v_add_f32_e32 v15, v15, v60
v_add_f32_e32 v15, v15, v26
Cv 60,60,26
v_exp_f32_e32 v61, v118
v_exp_f32_e32 v27, v119
v_add_f32_e32 v15, v15, v61
v_add_f32_e32 v15, v15, v27
Cv 61,61,27
v_exp_f32_e32 v62, v124
v_exp_f32_e32 v28, v125
v_add_f32_e32 v15, v15, v62
v_add_f32_e32 v15, v15, v28
Cv 62,62,28
v_exp_f32_e32 v63, v126
v_exp_f32_e32 v29, v127
v_add_f32_e32 v15, v15, v63
v_add_f32_e32 v15, v15, v29
Cv 63,63,29
s_nop 1
s_setpc_b64 s[30:31]
.Lzsp1:
s_sub_u32 s53, s53, 1
s_cbranch_scc1 .Lzsq1
s_sleep 1
buffer_load_dword v4, v2, s[20:23], s85 offen sc0 sc1
s_waitcnt vmcnt(0)
v_xor_b32 v4, s3, v4
v_cmp_gt_u32_e32 vcc, 2, v4
s_andn2_b32 s28, 0xff, vcc_lo
s_cbranch_scc1 .Lzsp1
s_branch .Lzok1
.Lzsq1:
s_mov_b32 s53, 0
s_branch .Lzok1
.Lzsp2:
s_sub_u32 s53, s53, 1
s_cbranch_scc1 .Lzsq2
s_sleep 1
buffer_load_dword v4, v7, s[20:23], s52 offen offset:0 sc0 sc1
s_waitcnt vmcnt(0)
v_cmp_eq_u32_e32 vcc, s3, v4
s_andn2_b64 s[28:29], 1, vcc
s_cbranch_scc1 .Lzsp2
s_branch .Lzok2
.Lzsq2:
s_mov_b32 s53, 0
s_branch .Lzok2
.Lzsp3:
)" R"(
s_sub_u32 s53, s53, 1
s_cbranch_scc1 .Lzsq3
s_sleep 1
buffer_load_dword v4, v2, s[20:23], s85 offen sc0 sc1
s_waitcnt vmcnt(0)
v_xor_b32 v4, s3, v4
v_cmp_gt_u32_e32 vcc, 2, v4
s_andn2_b32 s28, 0xff, vcc_lo
s_cbranch_scc1 .Lzsp3
s_branch .Lzok3
.Lzsq3:
s_mov_b32 s53, 0
s_branch .Lzok3
.Lzsp4:
s_sub_u32 s53, s53, 1
s_cbranch_scc1 .Lzsq4
s_sleep 1
buffer_load_dword v4, v7, s[20:23], s52 offen offset:0 sc0 sc1
s_waitcnt vmcnt(0)
v_cmp_eq_u32_e32 vcc, s3, v4
s_andn2_b64 s[28:29], 1, vcc
s_cbranch_scc1 .Lzsp4
s_branch .Lzok4
.Lzsq4:
s_mov_b32 s53, 0
s_branch .Lzok4
.Lzsp5:
s_sub_u32 s53, s53, 1
s_cbranch_scc1 .Lzsq5
s_sleep 1
buffer_load_dword v4, v5, s[20:23], s50 offen offset:0 sc0 sc1
s_waitcnt vmcnt(0)
v_cmp_eq_u32_e32 vcc, s3, v4
s_andn2_b64 s[28:29], -1, vcc
s_cbranch_scc1 .Lzsp5
s_branch .Lzok5
.Lzsq5:
s_mov_b32 s53, 0
s_branch .Lzok5
.Lzsp6:
s_sub_u32 s53, s53, 1
s_cbranch_scc1 .Lzsq6
s_sleep 1
buffer_load_dword v4, v5, s[20:23], s50 offen offset:0 sc0 sc1
s_waitcnt vmcnt(0)
v_cmp_eq_u32_e32 vcc, s3, v4
s_andn2_b64 s[28:29], -1, vcc
s_cbranch_scc1 .Lzsp6
s_branch .Lzok6
.Lzsq6:
s_mov_b32 s53, 0
s_branch .Lzok6
.Lzsp7:
s_sub_u32 s53, s53, 1
s_cbranch_scc1 .Lzsq7
s_sleep 1
buffer_load_dwordx2 v[24:25], v7, s[20:23], s83 offen sc0 sc1
s_waitcnt vmcnt(0)
v_cmp_eq_u32_e32 vcc, s3, v25
s_andn2_b64 s[28:29], 1, vcc
s_cbranch_scc1 .Lzsp7
s_branch .Lzok7
.Lzsq7:
s_mov_b32 s53, 0
)" R"(
s_branch .Lzok7
.LzT2S:
s_cmp_lg_u32 s33, 0
s_cbranch_scc1 .LzT2E
.LzT2:
s_mov_b32 s28, 0x1500000
buffer_load_dwordx2 v[32:33], v1, s[20:23], s28 offen offset:0 sc0 sc1
buffer_load_dwordx2 v[34:35], v1, s[20:23], s28 offen offset:1024 sc0 sc1
buffer_load_dwordx2 v[36:37], v1, s[20:23], s28 offen offset:2048 sc0 sc1
buffer_load_dwordx2 v[38:39], v1, s[20:23], s28 offen offset:3072 sc0 sc1
s_mov_b32 s28, 0x1501000
buffer_load_dwordx2 v[40:41], v1, s[20:23], s28 offen offset:0 sc0 sc1
buffer_load_dwordx2 v[42:43], v1, s[20:23], s28 offen offset:1024 sc0 sc1
buffer_load_dwordx2 v[44:45], v1, s[20:23], s28 offen offset:2048 sc0 sc1
buffer_load_dwordx2 v[46:47], v1, s[20:23], s28 offen offset:3072 sc0 sc1
s_mov_b32 s28, 0x1502000
buffer_load_dwordx2 v[48:49], v1, s[20:23], s28 offen offset:0 sc0 sc1
buffer_load_dwordx2 v[50:51], v1, s[20:23], s28 offen offset:1024 sc0 sc1
buffer_load_dwordx2 v[52:53], v1, s[20:23], s28 offen offset:2048 sc0 sc1
buffer_load_dwordx2 v[54:55], v1, s[20:23], s28 offen offset:3072 sc0 sc1
s_mov_b32 s28, 0x1503000
buffer_load_dwordx2 v[56:57], v1, s[20:23], s28 offen offset:0 sc0 sc1
buffer_load_dwordx2 v[58:59], v1, s[20:23], s28 offen offset:1024 sc0 sc1
buffer_load_dwordx2 v[60:61], v1, s[20:23], s28 offen offset:2048 sc0 sc1
buffer_load_dwordx2 v[62:63], v1, s[20:23], s28 offen offset:3072 sc0 sc1
s_mov_b32 s28, 0x1504000
buffer_load_dwordx2 v[64:65], v1, s[20:23], s28 offen offset:0 sc0 sc1
buffer_load_dwordx2 v[66:67], v1, s[20:23], s28 offen offset:1024 sc0 sc1
buffer_load_dwordx2 v[68:69], v1, s[20:23], s28 offen offset:2048 sc0 sc1
buffer_load_dwordx2 v[70:71], v1, s[20:23], s28 offen offset:3072 sc0 sc1
s_mov_b32 s28, 0x1505000
buffer_load_dwordx2 v[72:73], v1, s[20:23], s28 offen offset:0 sc0 sc1
buffer_load_dwordx2 v[74:75], v1, s[20:23], s28 offen offset:1024 sc0 sc1
buffer_load_dwordx2 v[76:77], v1, s[20:23], s28 offen offset:2048 sc0 sc1
buffer_load_dwordx2 v[78:79], v1, s[20:23], s28 offen offset:3072 sc0 sc1
s_mov_b32 s28, 0x1506000
buffer_load_dwordx2 v[80:81], v1, s[20:23], s28 offen offset:0 sc0 sc1
buffer_load_dwordx2 v[82:83], v1, s[20:23], s28 offen offset:1024 sc0 sc1
buffer_load_dwordx2 v[84:85], v1, s[20:23], s28 offen offset:2048 sc0 sc1
buffer_load_dwordx2 v[86:87], v1, s[20:23], s28 offen offset:3072 sc0 sc1
s_mov_b32 s28, 0x1507000
buffer_load_dwordx2 v[88:89], v1, s[20:23], s28 offen offset:0 sc0 sc1
buffer_load_dwordx2 v[90:91], v1, s[20:23], s28 offen offset:1024 sc0 sc1
buffer_load_dwordx2 v[92:93], v1, s[20:23], s28 offen offset:2048 sc0 sc1
buffer_load_dwordx2 v[94:95], v1, s[20:23], s28 offen offset:3072 sc0 sc1
v_mov_b32 v26, 0
v_mov_b32 v27, -1
s_mov_b32 s28, 0x1508c00
buffer_store_dword v26, v7, s[20:23], s28 offen sc0 sc1
buffer_store_dword v26, v7, s[20:23], s28 offen offset:128 sc0 sc1
s_waitcnt vmcnt(0)
v_max3_u32 v26, v26, v33, v35
v_min3_u32 v27, v27, v33, v35
v_max3_u32 v26, v26, v37, v39
v_min3_u32 v27, v27, v37, v39
v_max3_u32 v26, v26, v41, v43
v_min3_u32 v27, v27, v41, v43
v_max3_u32 v26, v26, v45, v47
v_min3_u32 v27, v27, v45, v47
v_max3_u32 v26, v26, v49, v51
v_min3_u32 v27, v27, v49, v51
v_max3_u32 v26, v26, v53, v55
v_min3_u32 v27, v27, v53, v55
v_max3_u32 v26, v26, v57, v59
)" R"(
v_min3_u32 v27, v27, v57, v59
v_max3_u32 v26, v26, v61, v63
v_min3_u32 v27, v27, v61, v63
v_max3_u32 v26, v26, v65, v67
v_min3_u32 v27, v27, v65, v67
v_max3_u32 v26, v26, v69, v71
v_min3_u32 v27, v27, v69, v71
v_max3_u32 v26, v26, v73, v75
v_min3_u32 v27, v27, v73, v75
v_max3_u32 v26, v26, v77, v79
v_min3_u32 v27, v27, v77, v79
v_max3_u32 v26, v26, v81, v83
v_min3_u32 v27, v27, v81, v83
v_max3_u32 v26, v26, v85, v87
v_min3_u32 v27, v27, v85, v87
v_max3_u32 v26, v26, v89, v91
v_min3_u32 v27, v27, v89, v91
v_max3_u32 v26, v26, v93, v95
v_min3_u32 v27, v27, v93, v95
v_cmp_ne_u32_e32 vcc, s3, v26
v_cmp_ne_u32_e64 s[28:29], s3, v27
s_or_b64 vcc, vcc, s[28:29]
s_cbranch_scc0 .LzT2G
s_sub_u32 s53, s53, 1
s_cbranch_scc0 .LzT2
s_mov_b32 s53, 0
.LzT2G:
v_max_u32 v26, v32, v34
v_min_u32 v27, v32, v34
v_med3_u32 v27, v26, v27, v36
v_max_u32 v26, v26, v36
v_med3_u32 v27, v26, v27, v38
v_max_u32 v26, v26, v38
v_med3_u32 v27, v26, v27, v40
v_max_u32 v26, v26, v40
v_med3_u32 v27, v26, v27, v42
v_max_u32 v26, v26, v42
v_med3_u32 v27, v26, v27, v44
v_max_u32 v26, v26, v44
v_med3_u32 v27, v26, v27, v46
v_max_u32 v26, v26, v46
v_med3_u32 v27, v26, v27, v48
v_max_u32 v26, v26, v48
v_med3_u32 v27, v26, v27, v50
v_max_u32 v26, v26, v50
v_med3_u32 v27, v26, v27, v52
v_max_u32 v26, v26, v52
v_med3_u32 v27, v26, v27, v54
v_max_u32 v26, v26, v54
v_med3_u32 v27, v26, v27, v56
v_max_u32 v26, v26, v56
v_med3_u32 v27, v26, v27, v58
v_max_u32 v26, v26, v58
v_med3_u32 v27, v26, v27, v60
v_max_u32 v26, v26, v60
v_med3_u32 v27, v26, v27, v62
v_max_u32 v26, v26, v62
v_med3_u32 v27, v26, v27, v64
v_max_u32 v26, v26, v64
v_med3_u32 v27, v26, v27, v66
v_max_u32 v26, v26, v66
v_med3_u32 v27, v26, v27, v68
v_max_u32 v26, v26, v68
v_med3_u32 v27, v26, v27, v70
)" R"(
v_max_u32 v26, v26, v70
v_med3_u32 v27, v26, v27, v72
v_max_u32 v26, v26, v72
v_med3_u32 v27, v26, v27, v74
v_max_u32 v26, v26, v74
v_med3_u32 v27, v26, v27, v76
v_max_u32 v26, v26, v76
v_med3_u32 v27, v26, v27, v78
v_max_u32 v26, v26, v78
v_med3_u32 v27, v26, v27, v80
v_max_u32 v26, v26, v80
v_med3_u32 v27, v26, v27, v82
v_max_u32 v26, v26, v82
v_med3_u32 v27, v26, v27, v84
v_max_u32 v26, v26, v84
v_med3_u32 v27, v26, v27, v86
v_max_u32 v26, v26, v86
v_med3_u32 v27, v26, v27, v88
v_max_u32 v26, v26, v88
v_med3_u32 v27, v26, v27, v90
v_max_u32 v26, v26, v90
v_med3_u32 v27, v26, v27, v92
v_max_u32 v26, v26, v92
v_med3_u32 v27, v26, v27, v94
v_max_u32 v26, v26, v94
v_mov_b32 v24, v26
v_mov_b32 v25, v27
s_nop 1
v_permlane32_swap_b32_e32 v26, v24
v_permlane32_swap_b32_e32 v27, v25
v_max_u32 v27, v27, v25
v_med3_u32 v27, v26, v27, v24
v_max_u32 v26, v26, v24
v_mov_b32 v24, v26
v_mov_b32 v25, v27
s_nop 1
v_permlane16_swap_b32_e32 v26, v24
v_permlane16_swap_b32_e32 v27, v25
v_max_u32 v27, v27, v25
v_med3_u32 v27, v26, v27, v24
v_max_u32 v26, v26, v24
s_nop 1
v_mov_b32_dpp v24, v26 row_ror:8 row_mask:0xf bank_mask:0xf
v_mov_b32_dpp v25, v27 row_ror:8 row_mask:0xf bank_mask:0xf
v_max_u32 v27, v27, v25
v_med3_u32 v27, v26, v27, v24
v_max_u32 v26, v26, v24
s_nop 1
v_mov_b32_dpp v24, v26 row_ror:4 row_mask:0xf bank_mask:0xf
v_mov_b32_dpp v25, v27 row_ror:4 row_mask:0xf bank_mask:0xf
v_max_u32 v27, v27, v25
v_med3_u32 v27, v26, v27, v24
v_max_u32 v26, v26, v24
s_nop 1
v_mov_b32_dpp v24, v26 row_ror:2 row_mask:0xf bank_mask:0xf
v_mov_b32_dpp v25, v27 row_ror:2 row_mask:0xf bank_mask:0xf
v_max_u32 v27, v27, v25
v_med3_u32 v27, v26, v27, v24
v_max_u32 v26, v26, v24
s_nop 1
v_mov_b32_dpp v24, v26 row_ror:1 row_mask:0xf bank_mask:0xf
v_mov_b32_dpp v25, v27 row_ror:1 row_mask:0xf bank_mask:0xf
v_max_u32 v27, v27, v25
v_med3_u32 v27, v26, v27, v24
)" R"(
v_max_u32 v26, v26, v24
v_and_b32 v26, 0xffff, v26
v_lshl_or_b32 v26, v27, 16, v26
v_not_b32 v24, v26
v_mov_b32 v25, s3
s_mov_b32 s28, 0x1508000
buffer_store_dwordx2 v[24:25], v7, s[20:23], s28 offen sc0 sc1
s_branch .LzT2E
.LzA7:
s_waitcnt vmcnt(0)
s_nop 7
v_mul_f32_e32 v96, s54, v96
v_mul_f32_e32 v97, s54, v97
Cv 96,96,97
v_accvgpr_write_b32 a112, v96
v_mul_f32_e32 v98, s54, v98
v_mul_f32_e32 v99, s54, v99
Cv 98,98,99
v_accvgpr_write_b32 a113, v98
v_mul_f32_e32 v100, s54, v100
v_mul_f32_e32 v101, s54, v101
Cv 100,100,101
v_accvgpr_write_b32 a114, v100
v_mul_f32_e32 v102, s54, v102
v_mul_f32_e32 v103, s54, v103
Cv 102,102,103
v_accvgpr_write_b32 a115, v102
v_mul_f32_e32 v104, s54, v104
v_mul_f32_e32 v105, s54, v105
Cv 104,104,105
v_accvgpr_write_b32 a116, v104
v_mul_f32_e32 v106, s54, v106
v_mul_f32_e32 v107, s54, v107
Cv 106,106,107
v_accvgpr_write_b32 a117, v106
v_mul_f32_e32 v108, s54, v108
v_mul_f32_e32 v109, s54, v109
Cv 108,108,109
v_accvgpr_write_b32 a118, v108
v_mul_f32_e32 v110, s54, v110
v_mul_f32_e32 v111, s54, v111
Cv 110,110,111
v_accvgpr_write_b32 a119, v110
v_mul_f32_e32 v112, s54, v112
v_mul_f32_e32 v113, s54, v113
Cv 112,112,113
v_accvgpr_write_b32 a120, v112
v_mul_f32_e32 v114, s54, v114
v_mul_f32_e32 v115, s54, v115
Cv 114,114,115
v_accvgpr_write_b32 a121, v114
v_mul_f32_e32 v116, s54, v116
v_mul_f32_e32 v117, s54, v117
Cv 116,116,117
v_accvgpr_write_b32 a122, v116
v_mul_f32_e32 v118, s54, v118
v_mul_f32_e32 v119, s54, v119
Cv 118,118,119
v_accvgpr_write_b32 a123, v118
v_mul_f32_e32 v120, s54, v120
v_mul_f32_e32 v121, s54, v121
Cv 120,120,121
v_accvgpr_write_b32 a124, v120
v_mul_f32_e32 v122, s54, v122
)" R"(
v_mul_f32_e32 v123, s54, v123
Cv 122,122,123
v_accvgpr_write_b32 a125, v122
v_mul_f32_e32 v124, s54, v124
v_mul_f32_e32 v125, s54, v125
Cv 124,124,125
v_accvgpr_write_b32 a126, v124
v_mul_f32_e32 v126, s54, v126
v_mul_f32_e32 v127, s54, v127
Cv 126,126,127
v_accvgpr_write_b32 a127, v126
v_accvgpr_mov_b32 a64, a224
v_accvgpr_mov_b32 a65, a225
v_accvgpr_mov_b32 a66, a226
v_accvgpr_mov_b32 a67, a227
v_accvgpr_mov_b32 a68, a228
v_accvgpr_mov_b32 a69, a229
v_accvgpr_mov_b32 a70, a230
v_accvgpr_mov_b32 a71, a231
v_accvgpr_mov_b32 a72, a232
v_accvgpr_mov_b32 a73, a233
v_accvgpr_mov_b32 a74, a234
v_accvgpr_mov_b32 a75, a235
v_accvgpr_mov_b32 a76, a236
v_accvgpr_mov_b32 a77, a237
v_accvgpr_mov_b32 a78, a238
v_accvgpr_mov_b32 a79, a239
v_accvgpr_mov_b32 a80, a240
v_accvgpr_mov_b32 a81, a241
v_accvgpr_mov_b32 a82, a242
v_accvgpr_mov_b32 a83, a243
v_accvgpr_mov_b32 a84, a244
v_accvgpr_mov_b32 a85, a245
v_accvgpr_mov_b32 a86, a246
v_accvgpr_mov_b32 a87, a247
v_accvgpr_mov_b32 a88, a248
v_accvgpr_mov_b32 a89, a249
v_accvgpr_mov_b32 a90, a250
v_accvgpr_mov_b32 a91, a251
v_accvgpr_mov_b32 a92, a252
v_accvgpr_mov_b32 a93, a253
v_accvgpr_mov_b32 a94, a254
v_accvgpr_mov_b32 a95, a255
v_accvgpr_mov_b32 a96, a192
v_accvgpr_mov_b32 a97, a193
v_accvgpr_mov_b32 a98, a194
v_accvgpr_mov_b32 a99, a195
v_accvgpr_mov_b32 a100, a196
v_accvgpr_mov_b32 a101, a197
v_accvgpr_mov_b32 a102, a198
v_accvgpr_mov_b32 a103, a199
v_accvgpr_mov_b32 a104, a200
v_accvgpr_mov_b32 a105, a201
v_accvgpr_mov_b32 a106, a202
v_accvgpr_mov_b32 a107, a203
v_accvgpr_mov_b32 a108, a204
v_accvgpr_mov_b32 a109, a205
v_accvgpr_mov_b32 a110, a206
v_accvgpr_mov_b32 a111, a207
s_mov_b32 s94, 2
s_nop 1
s_branch .LzA7R
.Lzsp8:
s_sub_u32 s53, s53, 1
)" R"(
s_cbranch_scc1 .Lzsq8
s_sleep 1
buffer_load_dword v4, v5, s[20:23], s50 offen offset:0 sc0 sc1
s_waitcnt vmcnt(0)
v_cmp_eq_u32_e32 vcc, s3, v4
s_andn2_b64 s[28:29], -1, vcc
s_cbranch_scc1 .Lzsp8
s_branch .Lzok8
.Lzsq8:
s_mov_b32 s53, 0
s_branch .Lzok8
.Lzmk0:
s_call_b64 s[30:31], .LzMASK0
s_branch .Lzmr0
.Lzfx0:
s_mov_b32 s88, 3
s_call_b64 s[30:31], .LzFIX0
s_branch .Lzfr0
.Lzmk1:
s_call_b64 s[30:31], .LzMASK1
s_branch .Lzmr1
.Lzsp9:
s_sub_u32 s53, s53, 1
s_cbranch_scc1 .Lzsq9
s_sleep 1
buffer_load_dword v4, v5, s[20:23], s50 offen offset:0 sc0 sc1
s_waitcnt vmcnt(0)
v_cmp_eq_u32_e32 vcc, s3, v4
s_andn2_b64 s[28:29], -1, vcc
s_cbranch_scc1 .Lzsp9
s_branch .Lzok9
.Lzsq9:
s_mov_b32 s53, 0
s_branch .Lzok9
.Lzfx1:
s_mov_b32 s88, 2
s_call_b64 s[30:31], .LzFIX1
s_branch .Lzfr1
.Lzmk2:
s_call_b64 s[30:31], .LzMASK0
s_branch .Lzmr2
.Lzfx2:
s_mov_b32 s88, 2
s_call_b64 s[30:31], .LzFIX0
s_branch .Lzfr2
.Lzmk3:
s_call_b64 s[30:31], .LzMASK1
s_branch .Lzmr3
.Lzsp10:
s_sub_u32 s53, s53, 1
s_cbranch_scc1 .Lzsq10
s_sleep 1
buffer_load_dword v4, v5, s[20:23], s50 offen offset:0 sc0 sc1
s_waitcnt vmcnt(0)
v_cmp_eq_u32_e32 vcc, s3, v4
s_andn2_b64 s[28:29], -1, vcc
s_cbranch_scc1 .Lzsp10
s_branch .Lzok10
.Lzsq10:
s_mov_b32 s53, 0
s_branch .Lzok10
.Lzfx3:
s_mov_b32 s88, 0
s_call_b64 s[30:31], .LzFIX1
)" R"(
s_branch .Lzfr3
.Lzmk4:
s_call_b64 s[30:31], .LzMASK0
s_branch .Lzmr4
.Lzfx4:
s_mov_b32 s88, 3
s_call_b64 s[30:31], .LzFIX0
s_branch .Lzfr4
.Lzmk5:
s_call_b64 s[30:31], .LzMASK1
s_branch .Lzmr5
.Lzfx5:
s_mov_b32 s88, 0
s_call_b64 s[30:31], .LzFIX1
s_branch .Lzfr5
.Lzsp11:
s_sub_u32 s53, s53, 1
s_cbranch_scc1 .Lzsq11
s_sleep 1
buffer_load_dword v4, v7, s[20:23], s52 offen offset:0 sc0 sc1
s_waitcnt vmcnt(0)
v_cmp_eq_u32_e32 vcc, s3, v4
s_andn2_b64 s[28:29], 1, vcc
s_cbranch_scc1 .Lzsp11
s_branch .Lzok11
.Lzsq11:
s_mov_b32 s53, 0
s_branch .Lzok11
.Lzsp12:
s_sub_u32 s53, s53, 1
s_cbranch_scc1 .Lzsq12
s_sleep 1
buffer_load_dword v4, v7, s[20:23], s52 offen offset:128 sc0 sc1
s_waitcnt vmcnt(0)
v_cmp_eq_u32_e32 vcc, s3, v4
s_andn2_b64 s[28:29], 1, vcc
s_cbranch_scc1 .Lzsp12
s_branch .Lzok12
.Lzsq12:
s_mov_b32 s53, 0
s_branch .Lzok12
.Lzsp13:
s_sub_u32 s53, s53, 1
s_cbranch_scc1 .Lzsq13
s_sleep 1
buffer_load_dword v4, v2, s[20:23], s85 offen sc0 sc1
s_waitcnt vmcnt(0)
v_xor_b32 v4, s3, v4
v_cmp_gt_u32_e32 vcc, 2, v4
s_andn2_b32 s28, 0xff, vcc_lo
s_cbranch_scc1 .Lzsp13
s_branch .Lzok13
.Lzsq13:
s_mov_b32 s53, 0
s_branch .Lzok13
.Lzsp14:
s_sub_u32 s53, s53, 1
s_cbranch_scc1 .Lzsq14
s_sleep 1
buffer_load_dword v4, v5, s[20:23], s50 offen offset:0 sc0 sc1
s_waitcnt vmcnt(0)
v_cmp_eq_u32_e32 vcc, s3, v4
s_andn2_b64 s[28:29], -1, vcc
s_cbranch_scc1 .Lzsp14
)" R"(
s_branch .Lzok14
.Lzsq14:
s_mov_b32 s53, 0
s_branch .Lzok14
.Lzsp15:
s_sub_u32 s53, s53, 1
s_cbranch_scc1 .Lzsq15
s_sleep 1
buffer_load_dword v4, v5, s[20:23], s50 offen offset:0 sc0 sc1
s_waitcnt vmcnt(0)
v_cmp_eq_u32_e32 vcc, s3, v4
s_andn2_b64 s[28:29], -1, vcc
s_cbranch_scc1 .Lzsp15
s_branch .Lzok15
.Lzsq15:
s_mov_b32 s53, 0
s_branch .Lzok15
.Lzsp16:
s_sub_u32 s53, s53, 1
s_cbranch_scc1 .Lzsq16
s_sleep 1
buffer_load_dword v4, v2, s[20:23], s85 offen sc0 sc1
s_waitcnt vmcnt(0)
v_xor_b32 v4, s3, v4
v_cmp_gt_u32_e32 vcc, 2, v4
s_andn2_b32 s28, 0xff, vcc_lo
s_cbranch_scc1 .Lzsp16
s_branch .Lzok16
.Lzsq16:
s_mov_b32 s53, 0
s_branch .Lzok16
.Lzsp17:
s_sub_u32 s53, s53, 1
s_cbranch_scc1 .Lzsq17
s_sleep 1
buffer_load_dword v4, v5, s[20:23], s50 offen offset:0 sc0 sc1
s_waitcnt vmcnt(0)
v_cmp_eq_u32_e32 vcc, s3, v4
s_andn2_b64 s[28:29], -1, vcc
s_cbranch_scc1 .Lzsp17
s_branch .Lzok17
.Lzsq17:
s_mov_b32 s53, 0
s_branch .Lzok17
.Lzsp18:
s_sub_u32 s53, s53, 1
s_cbranch_scc1 .Lzsq18
s_sleep 1
buffer_load_dword v4, v5, s[20:23], s50 offen offset:0 sc0 sc1
s_waitcnt vmcnt(0)
v_cmp_eq_u32_e32 vcc, s3, v4
s_andn2_b64 s[28:29], -1, vcc
s_cbranch_scc1 .Lzsp18
s_branch .Lzok18
.Lzsq18:
s_mov_b32 s53, 0
s_branch .Lzok18
.Lzsp19:
s_sub_u32 s53, s53, 1
s_cbranch_scc1 .Lzsq19
s_sleep 1
buffer_load_dword v4, v5, s[20:23], s50 offen offset:0 sc0 sc1
s_waitcnt vmcnt(0)
v_cmp_eq_u32_e32 vcc, s3, v4
)" R"(
s_andn2_b64 s[28:29], -1, vcc
s_cbranch_scc1 .Lzsp19
s_branch .Lzok19
.Lzsq19:
s_mov_b32 s53, 0
s_branch .Lzok19
.Lzfx6:
s_mov_b32 s88, 3
s_call_b64 s[30:31], .LzFIX0
s_branch .Lzfr6
.Lzsp20:
s_sub_u32 s53, s53, 1
s_cbranch_scc1 .Lzsq20
s_sleep 1
buffer_load_dword v4, v5, s[20:23], s50 offen offset:0 sc0 sc1
s_waitcnt vmcnt(0)
v_cmp_eq_u32_e32 vcc, s3, v4
s_andn2_b64 s[28:29], -1, vcc
s_cbranch_scc1 .Lzsp20
s_branch .Lzok20
.Lzsq20:
s_mov_b32 s53, 0
s_branch .Lzok20
.Lzfx7:
s_mov_b32 s88, 2
s_call_b64 s[30:31], .LzFIX1
s_branch .Lzfr7
.Lzmk6:
s_call_b64 s[30:31], .LzMASK0
s_branch .Lzmr6
.Lzfx8:
s_mov_b32 s88, 2
s_call_b64 s[30:31], .LzFIX0
s_branch .Lzfr8
.Lzmk7:
s_call_b64 s[30:31], .LzMASK1
s_branch .Lzmr7
.Lzsp21:
s_sub_u32 s53, s53, 1
s_cbranch_scc1 .Lzsq21
s_sleep 1
buffer_load_dword v4, v5, s[20:23], s50 offen offset:0 sc0 sc1
s_waitcnt vmcnt(0)
v_cmp_eq_u32_e32 vcc, s3, v4
s_andn2_b64 s[28:29], -1, vcc
s_cbranch_scc1 .Lzsp21
s_branch .Lzok21
.Lzsq21:
s_mov_b32 s53, 0
s_branch .Lzok21
.Lzfx9:
s_mov_b32 s88, 2
s_call_b64 s[30:31], .LzFIX1
s_branch .Lzfr9
.Lzmk8:
s_call_b64 s[30:31], .LzMASK0
s_branch .Lzmr8
.Lzfx10:
s_mov_b32 s88, 2
s_call_b64 s[30:31], .LzFIX0
s_branch .Lzfr10
.Lzmk9:
s_call_b64 s[30:31], .LzMASK1
s_branch .Lzmr9
)" R"(
.Lzsp22:
s_sub_u32 s53, s53, 1
s_cbranch_scc1 .Lzsq22
s_sleep 1
buffer_load_dword v4, v7, s[20:23], s83 offen offset:4 sc0 sc1
s_waitcnt vmcnt(0)
v_cmp_eq_u32_e32 vcc, s3, v4
s_andn2_b64 s[28:29], 1, vcc
s_cbranch_scc1 .Lzsp22
s_branch .Lzok22
.Lzsq22:
s_mov_b32 s53, 0
s_branch .Lzok22
.Lzsp23:
s_sub_u32 s53, s53, 1
s_cbranch_scc1 .Lzsq23
s_sleep 1
buffer_load_dword v4, v7, s[20:23], s65 offen offset:0 sc0 sc1
s_waitcnt vmcnt(0)
v_cmp_eq_u32_e32 vcc, s3, v4
s_andn2_b64 s[28:29], s[60:61], vcc
s_cbranch_scc1 .Lzsp23
s_branch .Lzok23
.Lzsq23:
s_mov_b32 s53, 0
s_branch .Lzok23
.Lzfx11:
s_mov_b32 s88, 2
s_call_b64 s[30:31], .LzFIX1
s_branch .Lzfr11
.Lzmk10:
s_call_b64 s[30:31], .LzMASK0
s_branch .Lzmr10
.Lzfx12:
s_mov_b32 s88, 2
s_call_b64 s[30:31], .LzFIX0
s_branch .Lzfr12
.Lzmk11:
s_call_b64 s[30:31], .LzMASK1
s_branch .Lzmr11
.Lzfx13:
s_mov_b32 s88, 0
s_call_b64 s[30:31], .LzFIX1
s_branch .Lzfr13
.LzMRG:
s_waitcnt vmcnt(0)
s_nop 7
v_mov_b32 v25, v64
v_mov_b32 v24, v25
s_nop 1
v_permlane32_swap_b32_e32 v25, v24
v_add_f32_e32 v25, v25, v24
v_mov_b32 v24, v25
s_nop 1
v_permlane16_swap_b32_e32 v25, v24
v_add_f32_e32 v25, v25, v24
v_cmp_lt_f32_e32 vcc, 0, v25
s_nop 1
v_cndmask_b32_e32 v66, v16, v66, vcc
v_sub_f32_e32 v28, v16, v66
v_min_f32_e32 v26, 0, v28
v_exp_f32_e32 v26, v26
v_max_f32_e32 v28, 0, v28
v_sub_f32_e32 v24, 0, v28
)" R"(
v_exp_f32_e32 v24, v24
v_mul_f32_e32 v64, v26, v64
v_mul_f32_e32 v12, v24, v12
v_mul_f32_e32 v128, v24, v128
v_mul_f32_e32 v129, v24, v129
v_mul_f32_e32 v130, v24, v130
v_mul_f32_e32 v131, v24, v131
v_mul_f32_e32 v132, v24, v132
v_mul_f32_e32 v133, v24, v133
v_mul_f32_e32 v134, v24, v134
v_mul_f32_e32 v135, v24, v135
v_mul_f32_e32 v144, v24, v144
v_mul_f32_e32 v145, v24, v145
v_mul_f32_e32 v146, v24, v146
v_mul_f32_e32 v147, v24, v147
v_mul_f32_e32 v148, v24, v148
v_mul_f32_e32 v149, v24, v149
v_mul_f32_e32 v150, v24, v150
v_mul_f32_e32 v151, v24, v151
v_mul_f32_e32 v160, v24, v160
v_mul_f32_e32 v161, v24, v161
v_mul_f32_e32 v162, v24, v162
v_mul_f32_e32 v163, v24, v163
v_mul_f32_e32 v164, v24, v164
v_mul_f32_e32 v165, v24, v165
v_mul_f32_e32 v166, v24, v166
v_mul_f32_e32 v167, v24, v167
v_mul_f32_e32 v176, v24, v176
v_mul_f32_e32 v177, v24, v177
v_mul_f32_e32 v178, v24, v178
v_mul_f32_e32 v179, v24, v179
v_mul_f32_e32 v180, v24, v180
v_mul_f32_e32 v181, v24, v181
v_mul_f32_e32 v182, v24, v182
v_mul_f32_e32 v183, v24, v183
v_mul_f32_e32 v192, v24, v192
v_mul_f32_e32 v193, v24, v193
v_mul_f32_e32 v194, v24, v194
v_mul_f32_e32 v195, v24, v195
v_mul_f32_e32 v196, v24, v196
v_mul_f32_e32 v197, v24, v197
v_mul_f32_e32 v198, v24, v198
v_mul_f32_e32 v199, v24, v199
v_mul_f32_e32 v208, v24, v208
v_mul_f32_e32 v209, v24, v209
v_mul_f32_e32 v210, v24, v210
v_mul_f32_e32 v211, v24, v211
v_mul_f32_e32 v212, v24, v212
v_mul_f32_e32 v213, v24, v213
v_mul_f32_e32 v214, v24, v214
v_mul_f32_e32 v215, v24, v215
v_mul_f32_e32 v224, v24, v224
v_mul_f32_e32 v225, v24, v225
v_mul_f32_e32 v226, v24, v226
v_mul_f32_e32 v227, v24, v227
v_mul_f32_e32 v228, v24, v228
v_mul_f32_e32 v229, v24, v229
v_mul_f32_e32 v230, v24, v230
v_mul_f32_e32 v231, v24, v231
v_mul_f32_e32 v240, v24, v240
v_mul_f32_e32 v241, v24, v241
v_mul_f32_e32 v242, v24, v242
v_mul_f32_e32 v243, v24, v243
v_mul_f32_e32 v244, v24, v244
)" R"(
v_mul_f32_e32 v245, v24, v245
v_mul_f32_e32 v246, v24, v246
v_mul_f32_e32 v247, v24, v247
v_sub_f32_e32 v16, v16, v28
v_sub_f32_e32 v17, v17, v28
v_sub_f32_e32 v18, v18, v28
v_sub_f32_e32 v19, v19, v28
v_sub_f32_e32 v96, v96, v28
v_sub_f32_e32 v97, v97, v28
v_sub_f32_e32 v98, v98, v28
v_sub_f32_e32 v99, v99, v28
v_sub_f32_e32 v104, v104, v28
v_sub_f32_e32 v105, v105, v28
v_sub_f32_e32 v106, v106, v28
v_sub_f32_e32 v107, v107, v28
v_sub_f32_e32 v112, v112, v28
v_sub_f32_e32 v113, v113, v28
v_sub_f32_e32 v114, v114, v28
v_sub_f32_e32 v115, v115, v28
v_sub_f32_e32 v120, v120, v28
v_sub_f32_e32 v121, v121, v28
v_sub_f32_e32 v122, v122, v28
v_sub_f32_e32 v123, v123, v28
v_accvgpr_read_b32 v25, a128
v_lshlrev_b32 v24, 16, v25
v_and_b32 v25, 0xffff0000, v25
v_mul_f32_e32 v24, v26, v24
v_mul_f32_e32 v25, v26, v25
Cv 24,24,25
v_accvgpr_write_b32 a128, v24
v_accvgpr_read_b32 v25, a129
v_lshlrev_b32 v24, 16, v25
v_and_b32 v25, 0xffff0000, v25
v_mul_f32_e32 v24, v26, v24
v_mul_f32_e32 v25, v26, v25
Cv 24,24,25
v_accvgpr_write_b32 a129, v24
v_accvgpr_read_b32 v25, a130
v_lshlrev_b32 v24, 16, v25
v_and_b32 v25, 0xffff0000, v25
v_mul_f32_e32 v24, v26, v24
v_mul_f32_e32 v25, v26, v25
Cv 24,24,25
v_accvgpr_write_b32 a130, v24
v_accvgpr_read_b32 v25, a131
v_lshlrev_b32 v24, 16, v25
v_and_b32 v25, 0xffff0000, v25
v_mul_f32_e32 v24, v26, v24
v_mul_f32_e32 v25, v26, v25
Cv 24,24,25
v_accvgpr_write_b32 a131, v24
v_accvgpr_read_b32 v25, a136
v_lshlrev_b32 v24, 16, v25
v_and_b32 v25, 0xffff0000, v25
v_mul_f32_e32 v24, v26, v24
v_mul_f32_e32 v25, v26, v25
Cv 24,24,25
v_accvgpr_write_b32 a136, v24
v_accvgpr_read_b32 v25, a137
v_lshlrev_b32 v24, 16, v25
v_and_b32 v25, 0xffff0000, v25
v_mul_f32_e32 v24, v26, v24
v_mul_f32_e32 v25, v26, v25
Cv 24,24,25
)" R"(
v_accvgpr_write_b32 a137, v24
v_accvgpr_read_b32 v25, a138
v_lshlrev_b32 v24, 16, v25
v_and_b32 v25, 0xffff0000, v25
v_mul_f32_e32 v24, v26, v24
v_mul_f32_e32 v25, v26, v25
Cv 24,24,25
v_accvgpr_write_b32 a138, v24
v_accvgpr_read_b32 v25, a139
v_lshlrev_b32 v24, 16, v25
v_and_b32 v25, 0xffff0000, v25
v_mul_f32_e32 v24, v26, v24
v_mul_f32_e32 v25, v26, v25
Cv 24,24,25
v_accvgpr_write_b32 a139, v24
v_accvgpr_read_b32 v25, a144
v_lshlrev_b32 v24, 16, v25
v_and_b32 v25, 0xffff0000, v25
v_mul_f32_e32 v24, v26, v24
v_mul_f32_e32 v25, v26, v25
Cv 24,24,25
v_accvgpr_write_b32 a144, v24
v_accvgpr_read_b32 v25, a145
v_lshlrev_b32 v24, 16, v25
v_and_b32 v25, 0xffff0000, v25
v_mul_f32_e32 v24, v26, v24
v_mul_f32_e32 v25, v26, v25
Cv 24,24,25
v_accvgpr_write_b32 a145, v24
v_accvgpr_read_b32 v25, a146
v_lshlrev_b32 v24, 16, v25
v_and_b32 v25, 0xffff0000, v25
v_mul_f32_e32 v24, v26, v24
v_mul_f32_e32 v25, v26, v25
Cv 24,24,25
v_accvgpr_write_b32 a146, v24
v_accvgpr_read_b32 v25, a147
v_lshlrev_b32 v24, 16, v25
v_and_b32 v25, 0xffff0000, v25
v_mul_f32_e32 v24, v26, v24
v_mul_f32_e32 v25, v26, v25
Cv 24,24,25
v_accvgpr_write_b32 a147, v24
v_accvgpr_read_b32 v25, a152
v_lshlrev_b32 v24, 16, v25
v_and_b32 v25, 0xffff0000, v25
v_mul_f32_e32 v24, v26, v24
v_mul_f32_e32 v25, v26, v25
Cv 24,24,25
v_accvgpr_write_b32 a152, v24
v_accvgpr_read_b32 v25, a153
v_lshlrev_b32 v24, 16, v25
v_and_b32 v25, 0xffff0000, v25
v_mul_f32_e32 v24, v26, v24
v_mul_f32_e32 v25, v26, v25
Cv 24,24,25
v_accvgpr_write_b32 a153, v24
v_accvgpr_read_b32 v25, a154
v_lshlrev_b32 v24, 16, v25
v_and_b32 v25, 0xffff0000, v25
v_mul_f32_e32 v24, v26, v24
v_mul_f32_e32 v25, v26, v25
Cv 24,24,25
v_accvgpr_write_b32 a154, v24
)" R"(
v_accvgpr_read_b32 v25, a155
v_lshlrev_b32 v24, 16, v25
v_and_b32 v25, 0xffff0000, v25
v_mul_f32_e32 v24, v26, v24
v_mul_f32_e32 v25, v26, v25
Cv 24,24,25
v_accvgpr_write_b32 a155, v24
v_accvgpr_read_b32 v25, a160
v_lshlrev_b32 v24, 16, v25
v_and_b32 v25, 0xffff0000, v25
v_mul_f32_e32 v24, v26, v24
v_mul_f32_e32 v25, v26, v25
Cv 24,24,25
v_accvgpr_write_b32 a160, v24
v_accvgpr_read_b32 v25, a161
v_lshlrev_b32 v24, 16, v25
v_and_b32 v25, 0xffff0000, v25
v_mul_f32_e32 v24, v26, v24
v_mul_f32_e32 v25, v26, v25
Cv 24,24,25
v_accvgpr_write_b32 a161, v24
v_accvgpr_read_b32 v25, a162
v_lshlrev_b32 v24, 16, v25
v_and_b32 v25, 0xffff0000, v25
v_mul_f32_e32 v24, v26, v24
v_mul_f32_e32 v25, v26, v25
Cv 24,24,25
v_accvgpr_write_b32 a162, v24
v_accvgpr_read_b32 v25, a163
v_lshlrev_b32 v24, 16, v25
v_and_b32 v25, 0xffff0000, v25
v_mul_f32_e32 v24, v26, v24
v_mul_f32_e32 v25, v26, v25
Cv 24,24,25
v_accvgpr_write_b32 a163, v24
v_accvgpr_read_b32 v25, a168
v_lshlrev_b32 v24, 16, v25
v_and_b32 v25, 0xffff0000, v25
v_mul_f32_e32 v24, v26, v24
v_mul_f32_e32 v25, v26, v25
Cv 24,24,25
v_accvgpr_write_b32 a168, v24
v_accvgpr_read_b32 v25, a169
v_lshlrev_b32 v24, 16, v25
v_and_b32 v25, 0xffff0000, v25
v_mul_f32_e32 v24, v26, v24
v_mul_f32_e32 v25, v26, v25
Cv 24,24,25
v_accvgpr_write_b32 a169, v24
v_accvgpr_read_b32 v25, a170
v_lshlrev_b32 v24, 16, v25
v_and_b32 v25, 0xffff0000, v25
v_mul_f32_e32 v24, v26, v24
v_mul_f32_e32 v25, v26, v25
Cv 24,24,25
v_accvgpr_write_b32 a170, v24
v_accvgpr_read_b32 v25, a171
v_lshlrev_b32 v24, 16, v25
v_and_b32 v25, 0xffff0000, v25
v_mul_f32_e32 v24, v26, v24
v_mul_f32_e32 v25, v26, v25
Cv 24,24,25
v_accvgpr_write_b32 a171, v24
v_accvgpr_read_b32 v25, a176
)" R"(
v_lshlrev_b32 v24, 16, v25
v_and_b32 v25, 0xffff0000, v25
v_mul_f32_e32 v24, v26, v24
v_mul_f32_e32 v25, v26, v25
Cv 24,24,25
v_accvgpr_write_b32 a176, v24
v_accvgpr_read_b32 v25, a177
v_lshlrev_b32 v24, 16, v25
v_and_b32 v25, 0xffff0000, v25
v_mul_f32_e32 v24, v26, v24
v_mul_f32_e32 v25, v26, v25
Cv 24,24,25
v_accvgpr_write_b32 a177, v24
v_accvgpr_read_b32 v25, a178
v_lshlrev_b32 v24, 16, v25
v_and_b32 v25, 0xffff0000, v25
v_mul_f32_e32 v24, v26, v24
v_mul_f32_e32 v25, v26, v25
Cv 24,24,25
v_accvgpr_write_b32 a178, v24
v_accvgpr_read_b32 v25, a179
v_lshlrev_b32 v24, 16, v25
v_and_b32 v25, 0xffff0000, v25
v_mul_f32_e32 v24, v26, v24
v_mul_f32_e32 v25, v26, v25
Cv 24,24,25
v_accvgpr_write_b32 a179, v24
v_accvgpr_read_b32 v25, a184
v_lshlrev_b32 v24, 16, v25
v_and_b32 v25, 0xffff0000, v25
v_mul_f32_e32 v24, v26, v24
v_mul_f32_e32 v25, v26, v25
Cv 24,24,25
v_accvgpr_write_b32 a184, v24
v_accvgpr_read_b32 v25, a185
v_lshlrev_b32 v24, 16, v25
v_and_b32 v25, 0xffff0000, v25
v_mul_f32_e32 v24, v26, v24
v_mul_f32_e32 v25, v26, v25
Cv 24,24,25
v_accvgpr_write_b32 a185, v24
v_accvgpr_read_b32 v25, a186
v_lshlrev_b32 v24, 16, v25
v_and_b32 v25, 0xffff0000, v25
v_mul_f32_e32 v24, v26, v24
v_mul_f32_e32 v25, v26, v25
Cv 24,24,25
v_accvgpr_write_b32 a186, v24
v_accvgpr_read_b32 v25, a187
v_lshlrev_b32 v24, 16, v25
v_and_b32 v25, 0xffff0000, v25
v_mul_f32_e32 v24, v26, v24
v_mul_f32_e32 v25, v26, v25
Cv 24,24,25
v_accvgpr_write_b32 a187, v24
v_mov_b32 v25, v65
v_mov_b32 v24, v25
s_nop 1
v_permlane32_swap_b32_e32 v25, v24
v_add_f32_e32 v25, v25, v24
v_mov_b32 v24, v25
s_nop 1
v_permlane16_swap_b32_e32 v25, v24
v_add_f32_e32 v25, v25, v24
)" R"(
v_cmp_lt_f32_e32 vcc, 0, v25
s_nop 1
v_cndmask_b32_e32 v67, v20, v67, vcc
v_sub_f32_e32 v29, v20, v67
v_min_f32_e32 v27, 0, v29
v_exp_f32_e32 v27, v27
v_max_f32_e32 v29, 0, v29
v_sub_f32_e32 v24, 0, v29
v_exp_f32_e32 v24, v24
v_mul_f32_e32 v65, v27, v65
v_mul_f32_e32 v13, v24, v13
v_mul_f32_e32 v136, v24, v136
v_mul_f32_e32 v137, v24, v137
v_mul_f32_e32 v138, v24, v138
v_mul_f32_e32 v139, v24, v139
v_mul_f32_e32 v140, v24, v140
v_mul_f32_e32 v141, v24, v141
v_mul_f32_e32 v142, v24, v142
v_mul_f32_e32 v143, v24, v143
v_mul_f32_e32 v152, v24, v152
v_mul_f32_e32 v153, v24, v153
v_mul_f32_e32 v154, v24, v154
v_mul_f32_e32 v155, v24, v155
v_mul_f32_e32 v156, v24, v156
v_mul_f32_e32 v157, v24, v157
v_mul_f32_e32 v158, v24, v158
v_mul_f32_e32 v159, v24, v159
v_mul_f32_e32 v168, v24, v168
v_mul_f32_e32 v169, v24, v169
v_mul_f32_e32 v170, v24, v170
v_mul_f32_e32 v171, v24, v171
v_mul_f32_e32 v172, v24, v172
v_mul_f32_e32 v173, v24, v173
v_mul_f32_e32 v174, v24, v174
v_mul_f32_e32 v175, v24, v175
v_mul_f32_e32 v184, v24, v184
v_mul_f32_e32 v185, v24, v185
v_mul_f32_e32 v186, v24, v186
v_mul_f32_e32 v187, v24, v187
v_mul_f32_e32 v188, v24, v188
v_mul_f32_e32 v189, v24, v189
v_mul_f32_e32 v190, v24, v190
v_mul_f32_e32 v191, v24, v191
v_mul_f32_e32 v200, v24, v200
v_mul_f32_e32 v201, v24, v201
v_mul_f32_e32 v202, v24, v202
v_mul_f32_e32 v203, v24, v203
v_mul_f32_e32 v204, v24, v204
v_mul_f32_e32 v205, v24, v205
v_mul_f32_e32 v206, v24, v206
v_mul_f32_e32 v207, v24, v207
v_mul_f32_e32 v216, v24, v216
v_mul_f32_e32 v217, v24, v217
v_mul_f32_e32 v218, v24, v218
v_mul_f32_e32 v219, v24, v219
v_mul_f32_e32 v220, v24, v220
v_mul_f32_e32 v221, v24, v221
v_mul_f32_e32 v222, v24, v222
v_mul_f32_e32 v223, v24, v223
v_mul_f32_e32 v232, v24, v232
v_mul_f32_e32 v233, v24, v233
v_mul_f32_e32 v234, v24, v234
v_mul_f32_e32 v235, v24, v235
v_mul_f32_e32 v236, v24, v236
)" R"(
v_mul_f32_e32 v237, v24, v237
v_mul_f32_e32 v238, v24, v238
v_mul_f32_e32 v239, v24, v239
v_mul_f32_e32 v248, v24, v248
v_mul_f32_e32 v249, v24, v249
v_mul_f32_e32 v250, v24, v250
v_mul_f32_e32 v251, v24, v251
v_mul_f32_e32 v252, v24, v252
v_mul_f32_e32 v253, v24, v253
v_mul_f32_e32 v254, v24, v254
v_mul_f32_e32 v255, v24, v255
v_sub_f32_e32 v20, v20, v29
v_sub_f32_e32 v21, v21, v29
v_sub_f32_e32 v22, v22, v29
v_sub_f32_e32 v23, v23, v29
v_sub_f32_e32 v100, v100, v29
v_sub_f32_e32 v101, v101, v29
v_sub_f32_e32 v102, v102, v29
v_sub_f32_e32 v103, v103, v29
v_sub_f32_e32 v108, v108, v29
v_sub_f32_e32 v109, v109, v29
v_sub_f32_e32 v110, v110, v29
v_sub_f32_e32 v111, v111, v29
v_sub_f32_e32 v116, v116, v29
v_sub_f32_e32 v117, v117, v29
v_sub_f32_e32 v118, v118, v29
v_sub_f32_e32 v119, v119, v29
v_sub_f32_e32 v124, v124, v29
v_sub_f32_e32 v125, v125, v29
v_sub_f32_e32 v126, v126, v29
v_sub_f32_e32 v127, v127, v29
v_accvgpr_read_b32 v25, a132
v_lshlrev_b32 v24, 16, v25
v_and_b32 v25, 0xffff0000, v25
v_mul_f32_e32 v24, v27, v24
v_mul_f32_e32 v25, v27, v25
Cv 24,24,25
v_accvgpr_write_b32 a132, v24
v_accvgpr_read_b32 v25, a133
v_lshlrev_b32 v24, 16, v25
v_and_b32 v25, 0xffff0000, v25
v_mul_f32_e32 v24, v27, v24
v_mul_f32_e32 v25, v27, v25
Cv 24,24,25
v_accvgpr_write_b32 a133, v24
v_accvgpr_read_b32 v25, a134
v_lshlrev_b32 v24, 16, v25
v_and_b32 v25, 0xffff0000, v25
v_mul_f32_e32 v24, v27, v24
v_mul_f32_e32 v25, v27, v25
Cv 24,24,25
v_accvgpr_write_b32 a134, v24
v_accvgpr_read_b32 v25, a135
v_lshlrev_b32 v24, 16, v25
v_and_b32 v25, 0xffff0000, v25
v_mul_f32_e32 v24, v27, v24
v_mul_f32_e32 v25, v27, v25
Cv 24,24,25
v_accvgpr_write_b32 a135, v24
v_accvgpr_read_b32 v25, a140
v_lshlrev_b32 v24, 16, v25
v_and_b32 v25, 0xffff0000, v25
v_mul_f32_e32 v24, v27, v24
v_mul_f32_e32 v25, v27, v25
)" R"(
Cv 24,24,25
v_accvgpr_write_b32 a140, v24
v_accvgpr_read_b32 v25, a141
v_lshlrev_b32 v24, 16, v25
v_and_b32 v25, 0xffff0000, v25
v_mul_f32_e32 v24, v27, v24
v_mul_f32_e32 v25, v27, v25
Cv 24,24,25
v_accvgpr_write_b32 a141, v24
v_accvgpr_read_b32 v25, a142
v_lshlrev_b32 v24, 16, v25
v_and_b32 v25, 0xffff0000, v25
v_mul_f32_e32 v24, v27, v24
v_mul_f32_e32 v25, v27, v25
Cv 24,24,25
v_accvgpr_write_b32 a142, v24
v_accvgpr_read_b32 v25, a143
v_lshlrev_b32 v24, 16, v25
v_and_b32 v25, 0xffff0000, v25
v_mul_f32_e32 v24, v27, v24
v_mul_f32_e32 v25, v27, v25
Cv 24,24,25
v_accvgpr_write_b32 a143, v24
v_accvgpr_read_b32 v25, a148
v_lshlrev_b32 v24, 16, v25
v_and_b32 v25, 0xffff0000, v25
v_mul_f32_e32 v24, v27, v24
v_mul_f32_e32 v25, v27, v25
Cv 24,24,25
v_accvgpr_write_b32 a148, v24
v_accvgpr_read_b32 v25, a149
v_lshlrev_b32 v24, 16, v25
v_and_b32 v25, 0xffff0000, v25
v_mul_f32_e32 v24, v27, v24
v_mul_f32_e32 v25, v27, v25
Cv 24,24,25
v_accvgpr_write_b32 a149, v24
v_accvgpr_read_b32 v25, a150
v_lshlrev_b32 v24, 16, v25
v_and_b32 v25, 0xffff0000, v25
v_mul_f32_e32 v24, v27, v24
v_mul_f32_e32 v25, v27, v25
Cv 24,24,25
v_accvgpr_write_b32 a150, v24
v_accvgpr_read_b32 v25, a151
v_lshlrev_b32 v24, 16, v25
v_and_b32 v25, 0xffff0000, v25
v_mul_f32_e32 v24, v27, v24
v_mul_f32_e32 v25, v27, v25
Cv 24,24,25
v_accvgpr_write_b32 a151, v24
v_accvgpr_read_b32 v25, a156
v_lshlrev_b32 v24, 16, v25
v_and_b32 v25, 0xffff0000, v25
v_mul_f32_e32 v24, v27, v24
v_mul_f32_e32 v25, v27, v25
Cv 24,24,25
v_accvgpr_write_b32 a156, v24
v_accvgpr_read_b32 v25, a157
v_lshlrev_b32 v24, 16, v25
v_and_b32 v25, 0xffff0000, v25
v_mul_f32_e32 v24, v27, v24
v_mul_f32_e32 v25, v27, v25
Cv 24,24,25
)" R"(
v_accvgpr_write_b32 a157, v24
v_accvgpr_read_b32 v25, a158
v_lshlrev_b32 v24, 16, v25
v_and_b32 v25, 0xffff0000, v25
v_mul_f32_e32 v24, v27, v24
v_mul_f32_e32 v25, v27, v25
Cv 24,24,25
v_accvgpr_write_b32 a158, v24
v_accvgpr_read_b32 v25, a159
v_lshlrev_b32 v24, 16, v25
v_and_b32 v25, 0xffff0000, v25
v_mul_f32_e32 v24, v27, v24
v_mul_f32_e32 v25, v27, v25
Cv 24,24,25
v_accvgpr_write_b32 a159, v24
v_accvgpr_read_b32 v25, a164
v_lshlrev_b32 v24, 16, v25
v_and_b32 v25, 0xffff0000, v25
v_mul_f32_e32 v24, v27, v24
v_mul_f32_e32 v25, v27, v25
Cv 24,24,25
v_accvgpr_write_b32 a164, v24
v_accvgpr_read_b32 v25, a165
v_lshlrev_b32 v24, 16, v25
v_and_b32 v25, 0xffff0000, v25
v_mul_f32_e32 v24, v27, v24
v_mul_f32_e32 v25, v27, v25
Cv 24,24,25
v_accvgpr_write_b32 a165, v24
v_accvgpr_read_b32 v25, a166
v_lshlrev_b32 v24, 16, v25
v_and_b32 v25, 0xffff0000, v25
v_mul_f32_e32 v24, v27, v24
v_mul_f32_e32 v25, v27, v25
Cv 24,24,25
v_accvgpr_write_b32 a166, v24
v_accvgpr_read_b32 v25, a167
v_lshlrev_b32 v24, 16, v25
v_and_b32 v25, 0xffff0000, v25
v_mul_f32_e32 v24, v27, v24
v_mul_f32_e32 v25, v27, v25
Cv 24,24,25
v_accvgpr_write_b32 a167, v24
v_accvgpr_read_b32 v25, a172
v_lshlrev_b32 v24, 16, v25
v_and_b32 v25, 0xffff0000, v25
v_mul_f32_e32 v24, v27, v24
v_mul_f32_e32 v25, v27, v25
Cv 24,24,25
v_accvgpr_write_b32 a172, v24
v_accvgpr_read_b32 v25, a173
v_lshlrev_b32 v24, 16, v25
v_and_b32 v25, 0xffff0000, v25
v_mul_f32_e32 v24, v27, v24
v_mul_f32_e32 v25, v27, v25
Cv 24,24,25
v_accvgpr_write_b32 a173, v24
v_accvgpr_read_b32 v25, a174
v_lshlrev_b32 v24, 16, v25
v_and_b32 v25, 0xffff0000, v25
v_mul_f32_e32 v24, v27, v24
v_mul_f32_e32 v25, v27, v25
Cv 24,24,25
v_accvgpr_write_b32 a174, v24
)" R"(
v_accvgpr_read_b32 v25, a175
v_lshlrev_b32 v24, 16, v25
v_and_b32 v25, 0xffff0000, v25
v_mul_f32_e32 v24, v27, v24
v_mul_f32_e32 v25, v27, v25
Cv 24,24,25
v_accvgpr_write_b32 a175, v24
v_accvgpr_read_b32 v25, a180
v_lshlrev_b32 v24, 16, v25
v_and_b32 v25, 0xffff0000, v25
v_mul_f32_e32 v24, v27, v24
v_mul_f32_e32 v25, v27, v25
Cv 24,24,25
v_accvgpr_write_b32 a180, v24
v_accvgpr_read_b32 v25, a181
v_lshlrev_b32 v24, 16, v25
v_and_b32 v25, 0xffff0000, v25
v_mul_f32_e32 v24, v27, v24
v_mul_f32_e32 v25, v27, v25
Cv 24,24,25
v_accvgpr_write_b32 a181, v24
v_accvgpr_read_b32 v25, a182
v_lshlrev_b32 v24, 16, v25
v_and_b32 v25, 0xffff0000, v25
v_mul_f32_e32 v24, v27, v24
v_mul_f32_e32 v25, v27, v25
Cv 24,24,25
v_accvgpr_write_b32 a182, v24
v_accvgpr_read_b32 v25, a183
v_lshlrev_b32 v24, 16, v25
v_and_b32 v25, 0xffff0000, v25
v_mul_f32_e32 v24, v27, v24
v_mul_f32_e32 v25, v27, v25
Cv 24,24,25
v_accvgpr_write_b32 a183, v24
v_accvgpr_read_b32 v25, a188
v_lshlrev_b32 v24, 16, v25
v_and_b32 v25, 0xffff0000, v25
v_mul_f32_e32 v24, v27, v24
v_mul_f32_e32 v25, v27, v25
Cv 24,24,25
v_accvgpr_write_b32 a188, v24
v_accvgpr_read_b32 v25, a189
v_lshlrev_b32 v24, 16, v25
v_and_b32 v25, 0xffff0000, v25
v_mul_f32_e32 v24, v27, v24
v_mul_f32_e32 v25, v27, v25
Cv 24,24,25
v_accvgpr_write_b32 a189, v24
v_accvgpr_read_b32 v25, a190
v_lshlrev_b32 v24, 16, v25
v_and_b32 v25, 0xffff0000, v25
v_mul_f32_e32 v24, v27, v24
v_mul_f32_e32 v25, v27, v25
Cv 24,24,25
v_accvgpr_write_b32 a190, v24
v_accvgpr_read_b32 v25, a191
v_lshlrev_b32 v24, 16, v25
v_and_b32 v25, 0xffff0000, v25
v_mul_f32_e32 v24, v27, v24
v_mul_f32_e32 v25, v27, v25
Cv 24,24,25
v_accvgpr_write_b32 a191, v24
v_exp_f32_e32 v48, v96
)" R"(
v_exp_f32_e32 v24, v97
s_nop 0
v_add_f32_e32 v14, v48, v24
Cv 48,48,24
v_exp_f32_e32 v49, v98
v_exp_f32_e32 v25, v99
v_add_f32_e32 v14, v14, v49
v_add_f32_e32 v14, v14, v25
Cv 49,49,25
v_exp_f32_e32 v50, v104
v_exp_f32_e32 v26, v105
v_add_f32_e32 v14, v14, v50
v_add_f32_e32 v14, v14, v26
Cv 50,50,26
v_exp_f32_e32 v51, v106
v_exp_f32_e32 v27, v107
v_add_f32_e32 v14, v14, v51
v_add_f32_e32 v14, v14, v27
Cv 51,51,27
v_exp_f32_e32 v52, v100
v_exp_f32_e32 v28, v101
s_nop 0
v_add_f32_e32 v15, v52, v28
Cv 52,52,28
v_exp_f32_e32 v53, v102
v_exp_f32_e32 v29, v103
v_add_f32_e32 v15, v15, v53
v_add_f32_e32 v15, v15, v29
Cv 53,53,29
v_exp_f32_e32 v54, v108
v_exp_f32_e32 v24, v109
v_add_f32_e32 v15, v15, v54
v_add_f32_e32 v15, v15, v24
Cv 54,54,24
v_exp_f32_e32 v55, v110
v_exp_f32_e32 v25, v111
v_add_f32_e32 v15, v15, v55
v_add_f32_e32 v15, v15, v25
Cv 55,55,25
s_nop 1
s_branch .LzMRR
.Lzsp24:
s_sub_u32 s53, s53, 1
s_cbranch_scc1 .Lzsq24
s_sleep 1
buffer_load_dword v4, v7, s[20:23], s52 offen offset:0 sc0 sc1
s_waitcnt vmcnt(0)
v_cmp_eq_u32_e32 vcc, s3, v4
s_andn2_b64 s[28:29], 1, vcc
s_cbranch_scc1 .Lzsp24
s_branch .Lzok24
.Lzsq24:
s_mov_b32 s53, 0
s_branch .Lzok24
.Lzsp25:
s_sub_u32 s53, s53, 1
s_cbranch_scc1 .Lzsq25
s_sleep 1
buffer_load_dword v4, v7, s[20:23], s52 offen offset:128 sc0 sc1
s_waitcnt vmcnt(0)
v_cmp_eq_u32_e32 vcc, s3, v4
s_andn2_b64 s[28:29], 1, vcc
s_cbranch_scc1 .Lzsp25
s_branch .Lzok25
)" R"(
.Lzsq25:
s_mov_b32 s53, 0
s_branch .Lzok25
.LzBB:
v_lshlrev_b32 v95, 7, v0
s_mov_b32 s28, 0x150c000
s_add_u32 s29, s28, 6144
.LzBB1:
buffer_load_dword v92, v95, s[20:23], s28 offen sc0 sc1
buffer_load_dword v93, v95, s[20:23], s29 offen sc0 sc1
s_waitcnt vmcnt(0)
v_cmp_ne_u32_e32 vcc, s3, v92
v_cmp_ne_u32_e64 s[30:31], s3, v93
s_or_b64 vcc, vcc, s[30:31]
s_cbranch_scc0 .LzBB2
s_sub_u32 s53, s53, 1
s_cbranch_scc0 .LzBB1
s_mov_b32 s53, 0
.LzBB2:
s_mul_i32 s28, s56, 7168
s_add_u32 s28, s28, 0x1558000
s_add_u32 s29, s28, 4096
buffer_load_dwordx4 v[64:67], v1, s[20:23], s28 offen offset:0 sc0 sc1
buffer_load_dwordx4 v[68:71], v1, s[20:23], s28 offen offset:1024 sc0 sc1
buffer_load_dwordx4 v[72:75], v1, s[20:23], s28 offen offset:2048 sc0 sc1
buffer_load_dwordx4 v[76:79], v1, s[20:23], s28 offen offset:3072 sc0 sc1
buffer_load_dwordx4 v[80:83], v1, s[20:23], s29 offen offset:0 sc0 sc1
buffer_load_dwordx4 v[84:87], v1, s[20:23], s29 offen offset:1024 sc0 sc1
buffer_load_dwordx4 v[88:91], v1, s[20:23], s29 offen offset:2048 sc0 sc1
s_cmp_gt_u32 s56, 27
s_cselect_b32 s28, 16, 0
s_cselect_b32 s29, 28, 0
s_lshr_b32 s28, s63, s28
s_and_b32 s28, s28, 0x7ff
s_sub_u32 s29, s56, s29
s_lshl_b32 s29, s29, 5
s_mul_i32 s83, s28, 896
s_add_u32 s29, s29, s83
v_and_b32 v95, 3, v0
v_lshlrev_b32 v95, 3, v95
buffer_load_dwordx2 v[92:93], v95, s[12:15], s29 offen
s_lshl4_add_u32 s28, s28, 0x150000c
v_mov_b32 v94, 0
buffer_load_dword v94, v94, s[20:23], s28 offen sc0 sc1
s_waitcnt vmcnt(0)
v_add_f32_e32 v64, v64, v68
v_add_f32_e32 v65, v65, v69
v_add_f32_e32 v66, v66, v70
v_add_f32_e32 v67, v67, v71
v_add_f32_e32 v72, v72, v76
v_add_f32_e32 v73, v73, v77
v_add_f32_e32 v74, v74, v78
v_add_f32_e32 v75, v75, v79
v_add_f32_e32 v80, v80, v84
v_add_f32_e32 v81, v81, v85
v_add_f32_e32 v82, v82, v86
v_add_f32_e32 v83, v83, v87
v_add_f32_e32 v64, v64, v72
v_add_f32_e32 v65, v65, v73
v_add_f32_e32 v66, v66, v74
v_add_f32_e32 v67, v67, v75
v_add_f32_e32 v80, v80, v88
v_add_f32_e32 v81, v81, v89
v_add_f32_e32 v82, v82, v90
)" R"(
v_add_f32_e32 v83, v83, v91
v_add_f32_e32 v64, v64, v80
v_add_f32_e32 v65, v65, v81
v_add_f32_e32 v66, v66, v82
v_add_f32_e32 v67, v67, v83
v_mov_b32 v68, v64
s_nop 1
v_permlane32_swap_b32_e32 v64, v68
v_add_f32_e32 v64, v64, v68
v_mov_b32 v68, v64
s_nop 1
v_permlane16_swap_b32_e32 v64, v68
v_add_f32_e32 v64, v64, v68
v_mov_b32 v68, v65
s_nop 1
v_permlane32_swap_b32_e32 v65, v68
v_add_f32_e32 v65, v65, v68
v_mov_b32 v68, v65
s_nop 1
v_permlane16_swap_b32_e32 v65, v68
v_add_f32_e32 v65, v65, v68
v_mov_b32 v68, v66
s_nop 1
v_permlane32_swap_b32_e32 v66, v68
v_add_f32_e32 v66, v66, v68
v_mov_b32 v68, v66
s_nop 1
v_permlane16_swap_b32_e32 v66, v68
v_add_f32_e32 v66, v66, v68
v_mov_b32 v68, v67
s_nop 1
v_permlane32_swap_b32_e32 v67, v68
v_add_f32_e32 v67, v67, v68
v_mov_b32 v68, v67
s_nop 1
v_permlane16_swap_b32_e32 v67, v68
v_add_f32_e32 v67, v67, v68
v_add_f32_dpp v64, v64, v64 row_ror:8 row_mask:0xf bank_mask:0xf
v_add_f32_dpp v65, v65, v65 row_ror:8 row_mask:0xf bank_mask:0xf
v_add_f32_dpp v66, v66, v66 row_ror:8 row_mask:0xf bank_mask:0xf
v_add_f32_dpp v67, v67, v67 row_ror:8 row_mask:0xf bank_mask:0xf
v_add_f32_dpp v64, v64, v64 row_ror:4 row_mask:0xf bank_mask:0xf
v_add_f32_dpp v65, v65, v65 row_ror:4 row_mask:0xf bank_mask:0xf
v_add_f32_dpp v66, v66, v66 row_ror:4 row_mask:0xf bank_mask:0xf
v_add_f32_dpp v67, v67, v67 row_ror:4 row_mask:0xf bank_mask:0xf
v_mul_f32_e32 v94, 0xbfb8aa3b, v94
v_exp_f32_e32 v94, v94
Cv 65,64,65
v_lshlrev_b32 v64, 16, v65
v_and_b32 v65, 0xffff0000, v65
v_add_f32_e32 v94, 1.0, v94
v_rcp_f32_e32 v94, v94
Cv 67,66,67
v_lshlrev_b32 v66, 16, v67
v_and_b32 v67, 0xffff0000, v67
Cv 94,94,94
v_lshlrev_b32 v94, 16, v94
v_mul_f32_e32 v64, v94, v64
v_mul_f32_e32 v65, v94, v65
v_mul_f32_e32 v66, v94, v66
v_mul_f32_e32 v67, v94, v67
Cv 65,64,65
v_lshlrev_b32 v64, 16, v65
v_and_b32 v65, 0xffff0000, v65
)" R"(
Cv 67,66,67
v_lshlrev_b32 v66, 16, v67
v_and_b32 v67, 0xffff0000, v67
v_lshlrev_b32 v68, 16, v92
v_and_b32 v69, 0xffff0000, v92
v_lshlrev_b32 v70, 16, v93
v_and_b32 v71, 0xffff0000, v93
v_add_f32_e32 v64, v64, v68
v_add_f32_e32 v65, v65, v69
v_add_f32_e32 v66, v66, v70
v_add_f32_e32 v67, v67, v71
Cv 64,64,65
Cv 66,66,67
v_mov_b32 v65, s3
v_mov_b32 v67, s3
v_lshrrev_b32 v95, 2, v0
v_min_u32 v95, 1, v95
v_lshl_or_b32 v95, v95, 30, v1
s_lshl_b32 s28, s56, 7
s_add_u32 s28, s28, 0x1509000
buffer_store_dwordx4 v[64:67], v95, s[20:23], s28 offen sc0 sc1
buffer_load_dword v4, v165, s[20:23], s40 offen sc0 sc1
s_waitcnt vmcnt(0)
s_branch .LzBBR
.LzPRB:
s_mov_b32 s24, s89
s_mul_i32 s25, s33, 2304
s_add_u32 s24, s24, s25
s_add_u32 s24, s24, 0xd00000
s_and_b32 s25, s36, 0x2000
s_xor_b32 s24, s24, s25
s_xor_b32 s25, s24, 0x2000
buffer_store_dword v0, v7, s[20:23], s24 offen sc0 sc1
buffer_store_dword v0, v7, s[20:23], s25 offen sc0 sc1
s_mov_b32 s26, -1
s_mov_b32 s27, -1
s_waitcnt vmcnt(0)
s_memtime s[28:29]
s_waitcnt lgkmcnt(0)
buffer_store_dword v0, v7, s[20:23], s24 offen sc0 sc1
s_waitcnt vmcnt(0)
s_memtime s[30:31]
s_waitcnt lgkmcnt(0)
s_sub_u32 s30, s30, s28
s_min_u32 s26, s26, s30
s_memtime s[28:29]
s_waitcnt lgkmcnt(0)
buffer_store_dword v0, v7, s[20:23], s25 offen sc0 sc1
s_waitcnt vmcnt(0)
s_memtime s[30:31]
s_waitcnt lgkmcnt(0)
s_sub_u32 s30, s30, s28
s_min_u32 s27, s27, s30
s_memtime s[28:29]
s_waitcnt lgkmcnt(0)
buffer_store_dword v0, v7, s[20:23], s24 offen sc0 sc1
s_waitcnt vmcnt(0)
s_memtime s[30:31]
s_waitcnt lgkmcnt(0)
s_sub_u32 s30, s30, s28
s_min_u32 s26, s26, s30
s_memtime s[28:29]
s_waitcnt lgkmcnt(0)
buffer_store_dword v0, v7, s[20:23], s25 offen sc0 sc1
)" R"(
s_waitcnt vmcnt(0)
s_memtime s[30:31]
s_waitcnt lgkmcnt(0)
s_sub_u32 s30, s30, s28
s_min_u32 s27, s27, s30
s_bitcmp1_b32 s33, 0
s_cselect_b32 s28, s27, s26
s_cselect_b32 s29, s26, s27
v_mov_b32 v32, s28
v_mov_b32 v33, s29
s_lshl_b32 s24, s33, 3
v_mov_b32 v34, s24
v_mov_b32 v35, 0
ds_write_b64 v34, v[32:33]
s_waitcnt lgkmcnt(0)
s_barrier
ds_read_b128 v[36:39], v35
ds_read_b128 v[40:43], v35 offset:16
s_mul_i32 s24, s3, 1664525
s_add_u32 s24, s24, 1013904223
s_and_b32 s24, s24, -2
s_lshl_b32 s25, s34, 6
s_add_u32 s25, s25, 0x1508800
s_cmp_lg_u32 s33, 0
s_cmov_b32 s25, 0x40000000
s_waitcnt lgkmcnt(0)
v_min3_u32 v32, v36, v38, v40
v_min3_u32 v33, v37, v39, v41
v_min_u32 v32, v32, v42
v_min_u32 v33, v33, v43
v_cmp_gt_u32_e32 vcc, v32, v33
v_mov_b32 v34, s24
s_nop 0
v_addc_co_u32_e32 v34, vcc, 0, v34, vcc
buffer_store_dword v34, v7, s[20:23], s25 offen sc0 sc1
s_barrier
s_branch .LzPRR
.LzHOP:
s_lshr_b32 s82, s2, 7
s_lshl1_add_u32 s82, s82, s91
s_and_b32 s83, s34, 1
s_lshl1_add_u32 s82, s82, s83
s_lshl2_add_u32 s82, s82, s33
s_lshl2_add_u32 s82, s82, 16
s_getpc_b64 vcc
s_add_u32 vcc_lo, vcc_lo, s82
s_addc_u32 vcc_hi, vcc_hi, 0
s_mov_b64 exec, 0
s_setpc_b64 vcc
s_branch .LzH23
s_branch .LzH28
s_branch .LzH27
s_branch .LzH26
s_branch .LzH25
s_branch .LzH34
s_branch .LzH33
s_branch .LzH24
s_branch .LzH23
s_branch .LzH32
s_branch .LzH31
s_branch .LzH30
s_branch .LzH29
s_branch .LzH34
s_branch .LzH33
)" R"(
s_branch .LzH24
s_branch .LzH4
s_branch .LzH3
s_branch .LzH2
s_branch .LzH11
s_branch .LzH10
s_branch .LzH5
s_branch .LzH1
s_branch .LzH0
s_branch .LzH9
s_branch .LzH8
s_branch .LzH7
s_branch .LzH6
s_branch .LzH10
s_branch .LzH5
s_branch .LzH1
s_branch .LzH0
.LzHR:
s_mov_b64 exec, -1
s_setpc_b64 s[30:31]
.LzA0:
s_mov_b32 s63, 0x6040200
s_mov_b32 s28, 0x7050312
s_bitcmp1_b32 s2, 5
s_cmov_b32 s63, 0x18161401
s_cmov_b32 s28, 0x19171513
s_bitcmp1_b32 s2, 6
s_cmov_b32 s63, s28
s_lshl_b32 s28, s33, 3
s_lshr_b32 s28, s63, s28
s_and_b32 s28, s28, 255
s_lshl_b32 s38, s28, 17
s_add_u32 s38, s38, 0xff80
s_lshl_b32 s39, s33, 16
s_lshl_b32 s28, s34, 9
s_add_u32 s39, s39, s28
s_sub_u32 s39, s39, 0x40000
s_lshl_b32 s37, s33, 12
s_add_u32 s37, s37, 0x10000
v_lshrrev_b32 v28, 4, v0
v_and_b32 v29, 7, v0
v_xor_b32 v28, v28, v29
v_lshlrev_b32 v28, 4, v28
v_lshrrev_b32 v29, 3, v0
v_lshl_or_b32 v8, v29, 11, v28
v_xor_b32 v9, 64, v8
v_add_u32 v9, 15360, v9
v_add_u32 v10, 30720, v8
v_add_u32 v11, 30720, v9
s_and_b32 s28, s33, 1
s_lshl_b32 s28, s28, 7
v_lshrrev_b32 v29, 5, v0
v_mul_u32_u24 v29, 0x2220, v29
v_xor_b32 v29, v29, v1
v_xor_b32 v12, s28, v29
v_xor_b32 v13, 64, v12
v_add_u32 v13, 15360, v13
v_add_u32 v14, 30720, v12
v_add_u32 v15, 30720, v13
v_min_u32 v7, 1, v0
v_lshlrev_b32 v7, 30, v7
s_waitcnt lgkmcnt(0)
s_mov_b32 s28, 0x1508400
s_load_dword s3, s[74:75], s28
)" R"(
s_mov_b32 s20, s74
s_mov_b32 s21, s75
s_mov_b32 s22, 0xf400000
s_mov_b32 s23, 0x20000
s_mov_b32 s4, s66
s_mov_b32 s5, s67
s_mov_b32 s6, 0x400000
s_mov_b32 s7, 0x20000
s_cmp_eq_u32 s91, 1
s_cselect_b32 s8, s72, s70
s_cselect_b32 s9, s73, s71
s_mov_b32 s10, 0x800000
s_mov_b32 s11, 0x20000
s_lshr_b32 s28, s2, 3
s_cmp_eq_u32 s28, 14
s_cbranch_scc1 .LzPRB
.LzPRR:
s_add_u32 m0, s37, 0
s_add_u32 s39, s39, 0x40000
buffer_load_dwordx4 v12, s[8:11], s39 offen offset:0 lds
buffer_load_dwordx4 v13, s[8:11], s39 offen offset:1024 lds
buffer_load_dwordx4 v14, s[8:11], s39 offen offset:2048 lds
buffer_load_dwordx4 v15, s[8:11], s39 offen offset:3072 lds
s_add_u32 m0, s36, 0
s_add_u32 s38, s38, 0xffff0080
buffer_load_dwordx4 v8, s[4:7], s38 offen offset:0 lds
buffer_load_dwordx4 v9, s[4:7], s38 offen offset:1024 lds
buffer_load_dwordx4 v10, s[4:7], s38 offen offset:2048 lds
buffer_load_dwordx4 v11, s[4:7], s38 offen offset:3072 lds
s_add_u32 m0, s36, 4096
s_add_u32 s38, s38, 0x10000
buffer_load_dwordx4 v8, s[4:7], s38 offen offset:0 lds
buffer_load_dwordx4 v9, s[4:7], s38 offen offset:1024 lds
buffer_load_dwordx4 v10, s[4:7], s38 offen offset:2048 lds
buffer_load_dwordx4 v11, s[4:7], s38 offen offset:3072 lds
s_add_u32 m0, s37, 16384
s_add_u32 s39, s39, 0x40000
buffer_load_dwordx4 v12, s[8:11], s39 offen offset:0 lds
buffer_load_dwordx4 v13, s[8:11], s39 offen offset:1024 lds
buffer_load_dwordx4 v14, s[8:11], s39 offen offset:2048 lds
buffer_load_dwordx4 v15, s[8:11], s39 offen offset:3072 lds
s_mov_b32 s12, s64
s_mov_b32 s13, s65
s_mov_b32 s14, 0x1c0000
s_mov_b32 s15, 0x20000
s_and_b32 s94, s2, 127
s_lshl2_add_u32 s94, s94, s33
s_lshl_b32 s94, s94, 2
s_mul_i32 s29, s94, 896
v_mov_b32 v120, 0x40000000
v_cmp_gt_u32_e32 vcc, 56, v0
s_nop 1
v_cndmask_b32_e32 v120, v120, v1, vcc
buffer_load_dwordx4 v[96:99], v120, s[12:15], s29 offen offset:0
buffer_load_dwordx4 v[100:103], v120, s[12:15], s29 offen offset:896
buffer_load_dwordx4 v[104:107], v120, s[12:15], s29 offen offset:1792
buffer_load_dwordx4 v[108:111], v120, s[12:15], s29 offen offset:2688
s_mov_b32 s24, s76
s_mov_b32 s25, s77
s_mov_b32 s26, 0x380
s_mov_b32 s27, 0x20000
buffer_load_dwordx4 v[112:115], v120, s[24:27], 0 offen
s_mov_b32 s24, s78
s_mov_b32 s25, s79
)" R"(
s_mov_b32 s26, 0x380
s_mov_b32 s27, 0x20000
buffer_load_dwordx4 v[116:119], v120, s[24:27], 0 offen
s_add_u32 m0, s36, 32768
s_add_u32 s38, s38, 0xffff0080
buffer_load_dwordx4 v8, s[4:7], s38 offen offset:0 lds
buffer_load_dwordx4 v9, s[4:7], s38 offen offset:1024 lds
buffer_load_dwordx4 v10, s[4:7], s38 offen offset:2048 lds
buffer_load_dwordx4 v11, s[4:7], s38 offen offset:3072 lds
s_add_u32 m0, s36, 36864
s_add_u32 s38, s38, 0x10000
buffer_load_dwordx4 v8, s[4:7], s38 offen offset:0 lds
buffer_load_dwordx4 v9, s[4:7], s38 offen offset:1024 lds
buffer_load_dwordx4 v10, s[4:7], s38 offen offset:2048 lds
buffer_load_dwordx4 v11, s[4:7], s38 offen offset:3072 lds
s_mov_b32 s16, s68
s_mov_b32 s17, s69
s_mov_b32 s18, 0x380000
s_mov_b32 s19, 0x20000
s_load_dwordx2 s[70:71], s[0:1], 88
s_load_dwordx2 s[74:75], s[0:1], 96
v_bfe_u32 v28, v0, 1, 3
v_lshrrev_b32 v29, 4, v0
v_xor_b32 v28, v28, v29
v_lshlrev_b32 v28, 4, v28
v_and_b32 v29, 15, v0
v_lshl_or_b32 v28, v29, 7, v28
s_lshr_b32 s28, s33, 1
s_lshl_b32 s28, s28, 12
v_or_b32 v16, s28, v28
v_xor_b32 v17, 64, v16
v_bfe_u32 v28, v0, 2, 3
v_lshlrev_b32 v28, 5, v28
v_and_b32 v29, 3, v0
v_lshl_or_b32 v28, v29, 3, v28
v_bfe_u32 v29, v0, 2, 2
v_lshl_or_b32 v28, v29, 9, v28
v_lshrrev_b32 v29, 4, v0
v_lshl_or_b32 v28, v29, 12, v28
s_and_b32 s28, s33, 1
s_lshl_b32 s28, s28, 8
s_bitset1_b32 s28, 16
v_or_b32 v18, s28, v28
v_xor_b32 v19, 32, v18
v_xor_b32 v20, 64, v18
v_xor_b32 v21, 96, v18
v_xor_b32 v22, 128, v18
v_xor_b32 v23, 160, v18
v_xor_b32 v24, 192, v18
v_xor_b32 v25, 224, v18
s_load_dwordx4 s[76:79], s[0:1], 72
s_waitcnt vmcnt(18) lgkmcnt(0)
s_barrier
Rt 0,18,0
Rt 2,18,2048
Rt 4,19,0
Rt 6,19,2048
Rt 8,20,0
Rt 10,20,2048
Rt 12,21,0
Rt 14,21,2048
Rt 16,22,0
Rt 18,22,2048
Rt 20,23,0
Rt 22,23,2048
)" R"(
Rt 24,24,0
Rt 26,24,2048
Rt 28,25,0
Rt 30,25,2048
Rb 32,16,0
Rb 36,16,2048
Rb 40,16,8192
Rb 44,16,10240
Rb 48,16,16384
Rb 52,16,18432
Rb 56,16,24576
Rb 60,16,26624
s_add_u32 m0, s37, 32768
s_add_u32 s39, s39, 0x40000
buffer_load_dwordx4 v12, s[8:11], s39 offen offset:0 lds
buffer_load_dwordx4 v13, s[8:11], s39 offen offset:1024 lds
buffer_load_dwordx4 v14, s[8:11], s39 offen offset:2048 lds
buffer_load_dwordx4 v15, s[8:11], s39 offen offset:3072 lds
s_add_u32 m0, s37, 49152
s_add_u32 s39, s39, 0x40000
buffer_load_dwordx4 v12, s[8:11], s39 offen offset:0 lds
buffer_load_dwordx4 v13, s[8:11], s39 offen offset:1024 lds
buffer_load_dwordx4 v14, s[8:11], s39 offen offset:2048 lds
buffer_load_dwordx4 v15, s[8:11], s39 offen offset:3072 lds
s_lshl2_add_u32 s40, s2, s33
s_mul_i32 s40, s40, 7168
v_min_u32 v2, 55, v0
v_lshlrev_b32 v2, 7, v2
buffer_load_dword v6, v2, s[16:19], s40 offen
s_mul_i32 s3, s3, 1664525
s_add_u32 s3, s3, 1013904223
s_waitcnt vmcnt(17)
v_dot2_f32_bf16 v120, v96, v112, 0
v_dot2_f32_bf16 v121, v100, v112, 0
v_dot2_f32_bf16 v122, v104, v112, 0
v_dot2_f32_bf16 v123, v108, v112, 0
v_dot2_f32_bf16 v124, v96, v116, 0
v_dot2_f32_bf16 v125, v100, v116, 0
v_dot2_f32_bf16 v126, v104, v116, 0
v_dot2_f32_bf16 v127, v108, v116, 0
v_dot2_f32_bf16 v120, v97, v113, v120
v_dot2_f32_bf16 v121, v101, v113, v121
v_dot2_f32_bf16 v122, v105, v113, v122
v_dot2_f32_bf16 v123, v109, v113, v123
v_dot2_f32_bf16 v124, v97, v117, v124
v_dot2_f32_bf16 v125, v101, v117, v125
v_dot2_f32_bf16 v126, v105, v117, v126
v_dot2_f32_bf16 v127, v109, v117, v127
v_dot2_f32_bf16 v120, v98, v114, v120
v_dot2_f32_bf16 v121, v102, v114, v121
v_dot2_f32_bf16 v122, v106, v114, v122
v_dot2_f32_bf16 v123, v110, v114, v123
v_dot2_f32_bf16 v124, v98, v118, v124
v_dot2_f32_bf16 v125, v102, v118, v125
v_dot2_f32_bf16 v126, v106, v118, v126
v_dot2_f32_bf16 v127, v110, v118, v127
v_dot2_f32_bf16 v120, v99, v115, v120
v_dot2_f32_bf16 v121, v103, v115, v121
v_dot2_f32_bf16 v122, v107, v115, v122
v_dot2_f32_bf16 v123, v111, v115, v123
v_dot2_f32_bf16 v124, v99, v119, v124
v_dot2_f32_bf16 v125, v103, v119, v125
v_dot2_f32_bf16 v126, v107, v119, v126
v_dot2_f32_bf16 v127, v111, v119, v127
)" R"(
v_permlane32_swap_b32_e32 v120, v121
v_add_f32_e32 v120, v120, v121
v_permlane32_swap_b32_e32 v122, v123
v_add_f32_e32 v122, v122, v123
v_permlane32_swap_b32_e32 v124, v125
v_add_f32_e32 v124, v124, v125
v_permlane32_swap_b32_e32 v126, v127
v_add_f32_e32 v126, v126, v127
v_permlane16_swap_b32_e32 v120, v122
v_add_f32_e32 v120, v120, v122
v_permlane16_swap_b32_e32 v124, v126
v_add_f32_e32 v124, v124, v126
v_add_f32_dpp v120, v120, v120 row_ror:8 row_mask:0xf bank_mask:0xf
s_nop 0
v_add_f32_dpp v124, v124, v124 row_ror:8 row_mask:0xf bank_mask:0xf
v_add_f32_dpp v120, v120, v120 row_ror:4 row_mask:0xf bank_mask:0xf
s_nop 0
v_add_f32_dpp v124, v124, v124 row_ror:4 row_mask:0xf bank_mask:0xf
v_add_f32_dpp v120, v120, v120 row_ror:2 row_mask:0xf bank_mask:0xf
s_nop 0
v_add_f32_dpp v124, v124, v124 row_ror:2 row_mask:0xf bank_mask:0xf
v_add_f32_dpp v120, v120, v120 row_ror:1 row_mask:0xf bank_mask:0xf
s_nop 0
v_add_f32_dpp v124, v124, v124 row_ror:1 row_mask:0xf bank_mask:0xf
Cv 120,120,120
v_lshlrev_b32 v120, 16, v120
Cv 124,124,124
v_lshlrev_b32 v124, 16, v124
v_mul_f32_e32 v120, 0xbfb8aa3b, v120
v_exp_f32_e32 v120, v120
s_nop 0
v_add_f32_e32 v120, 1.0, v120
v_rcp_f32_e32 v120, v120
s_nop 0
Cv 120,120,120
v_lshlrev_b32 v120, 16, v120
v_lshrrev_b32 v121, 5, v0
v_bfe_u32 v122, v0, 4, 1
v_lshl_or_b32 v121, v122, 1, v121
v_add_u32 v121, s94, v121
v_add_f32_e32 v125, 0, v124
v_ashrrev_i32 v126, 31, v125
v_or_b32 v126, 0x80000000, v126
v_xor_b32 v125, v125, v126
v_and_b32 v125, 0xffff0000, v125
v_sub_u32 v126, 0xffff, v121
v_or_b32 v116, v125, v126
v_mov_b32 v117, s3
v_mov_b32 v118, v120
v_mov_b32 v119, v124
v_and_b32 v122, 15, v0
v_min_u32 v122, 1, v122
v_lshlrev_b32 v122, 30, v122
v_lshl_or_b32 v122, v121, 4, v122
s_mov_b32 s94, 0x1500000
buffer_store_dwordx4 v[116:119], v122, s[20:23], s94 offen sc0 sc1
s_cmp_eq_u32 s91, 1
s_waitcnt lgkmcnt(0)
s_barrier
s_cbranch_scc1 .LzAV
s_branch .LzAK
.LzAK:
Mz 32,0,32
Rt 64,18,16384
Mz 64,0,36
)" R"(
s_lshl1_add_u32 s40, s34, s91
s_lshl_b32 s40, s40, 12
Mz 96,0,40
Rt 66,18,18432
s_and_b32 s29, s38, 0xfffe0000
Mz 128,0,44
s_add_u32 s39, s39, 0x40000
s_add_u32 s40, s40, s29
Mz 160,0,48
Rt 68,19,16384
s_add_u32 m0, s37, 0
Mz 192,0,52
buffer_load_dwordx4 v12, s[8:11], s39 offen offset:0 lds
Mz 224,0,56
Rt 70,19,18432
v_min_u32 v3, 31, v0
v_mfma_f32_16x16x32_bf16 a[224:227], a[0:3], a[60:63], 0
v_lshlrev_b32 v3, 7, v3
v_mfma_f32_16x16x32_bf16 a[228:231], a[4:7], a[60:63], 0
Rt 72,20,16384
Mz 228,4,56
Mz 196,4,52
Rt 74,20,18432
Mz 164,4,48
Mz 132,4,44
Rt 76,21,16384
Mz 100,4,40
buffer_load_dwordx4 v13, s[8:11], s39 offen offset:1024 lds
Mz 68,4,36
Rt 78,21,18432
Mz 36,4,32
Mz 40,8,32
Rt 80,22,16384
Mz 72,8,36
Mz 104,8,40
Rt 82,22,18432
Mz 136,8,44
Mz 168,8,48
Rt 84,23,16384
Mz 200,8,52
buffer_load_dwordx4 v14, s[8:11], s39 offen offset:2048 lds
Mz 232,8,56
Rt 86,23,18432
v_mfma_f32_16x16x32_bf16 a[232:235], a[8:11], a[60:63], 0
v_mfma_f32_16x16x32_bf16 a[236:239], a[12:15], a[60:63], 0
Rt 88,24,16384
Mz 236,12,56
Mz 204,12,52
Rt 90,24,18432
Mz 172,12,48
Mz 140,12,44
Rt 92,25,16384
Mz 108,12,40
buffer_load_dwordx4 v15, s[8:11], s39 offen offset:3072 lds
Mz 76,12,36
Rt 94,25,18432
Mz 44,12,32
Mz 48,16,32
Rb 96,17,0
Mz 80,16,36
buffer_load_dword v6, v3, s[4:7], s40 offen
Mz 112,16,40
Rb 100,17,2048
Mz 144,16,44
)" R"(
Mz 176,16,48
Rb 104,17,8192
s_mov_b32 s28, s35
Mz 208,16,52
s_lshl2_add_u32 s28, s28, s33
s_mul_i32 s43, s28, 28672
Mz 240,16,56
Rb 108,17,10240
v_and_b32 v26, 0x70, v1
v_mfma_f32_16x16x32_bf16 a[240:243], a[16:19], a[60:63], 0
v_and_b32 v4, 48, v0
v_xor_b32 v26, v26, v4
v_mfma_f32_16x16x32_bf16 a[244:247], a[20:23], a[60:63], 0
Rb 112,17,16384
v_lshrrev_b32 v4, 3, v0
Mz 244,20,56
v_mul_u32_u24 v4, 896, v4
v_add_u32 v26, v26, v4
Mz 212,20,52
Rb 116,17,18432
v_xor_b32 v27, 64, v26
Mz 180,20,48
v_add_u32 v27, 6144, v27
v_bfe_u32 v28, v0, 1, 3
Mz 148,20,44
Rb 120,17,24576
v_lshrrev_b32 v4, 4, v0
Mz 116,20,40
v_xor_b32 v28, v28, v4
v_lshlrev_b32 v28, 4, v28
Mz 84,20,36
Rb 124,17,26624
v_and_b32 v4, 15, v0
Mz 52,20,32
v_lshl_or_b32 v28, v4, 7, v28
s_add_u32 s28, s36, 0x18000
Mz 56,24,32
v_add_u32 v28, s28, v28
v_xor_b32 v29, 64, v28
Mz 88,24,36
Mz 120,24,40
Mz 152,24,44
Mz 184,24,48
Mz 216,24,52
Mz 248,24,56
v_mfma_f32_16x16x32_bf16 a[248:251], a[24:27], a[60:63], 0
v_mfma_f32_16x16x32_bf16 a[252:255], a[28:31], a[60:63], 0
Mz 252,28,56
Mz 220,28,52
Mz 188,28,48
Mz 156,28,44
Mz 124,28,40
Mz 92,28,36
Mz 60,28,32
s_call_b64 s[30:31], .LzHOP
s_waitcnt vmcnt(11) lgkmcnt(0)
s_barrier
Mv 32,64,96
Rt 0,18,32768
s_bfe_u32 s88, s2, 0x20005
Mv 64,64,100
s_add_u32 s38, s38, 0xffff0080
s_and_b32 s28, s2, 15
Mv 96,64,104
Rt 2,18,34816
)" R"(
s_add_u32 m0, s36, 0
Mv 128,64,108
buffer_load_dwordx4 v8, s[4:7], s38 offen offset:0 lds
Mv 160,64,112
Rt 4,19,32768
s_lshl4_add_u32 s88, s88, s28
Mv 192,64,116
s_lshl2_add_u32 s88, s88, s33
s_mul_i32 s88, s88, 4096
Mv 224,64,120
Rt 6,19,34816
s_add_u32 s88, s88, 0x100000
Ma 224,64,124
buffer_load_dwordx4 v9, s[4:7], s38 offen offset:1024 lds
Ma 228,68,124
Rt 8,20,32768
v_min_u32 v5, 31, v0
Mv 228,68,120
v_lshlrev_b32 v5, 7, v5
s_mov_b32 s90, 0x100000
Mv 196,68,116
Rt 10,20,34816
s_mov_b64 s[24:25], s[8:9]
Mv 164,68,112
buffer_load_dwordx4 v10, s[4:7], s38 offen offset:2048 lds
Mv 132,68,108
Rt 12,21,32768
s_mov_b64 s[26:27], s[10:11]
Mv 100,68,104
Mv 68,68,100
Rt 14,21,34816
Mv 36,68,96
buffer_load_dwordx4 v11, s[4:7], s38 offen offset:3072 lds
Mv 40,72,96
Rt 16,22,32768
Mv 72,72,100
s_add_u32 s38, s38, 0x10000
Mv 104,72,104
Rt 18,22,34816
s_add_u32 m0, s36, 4096
Mv 136,72,108
buffer_load_dwordx4 v8, s[4:7], s38 offen offset:0 lds
Mv 168,72,112
Rt 20,23,32768
Mv 200,72,116
Mv 232,72,120
Rt 22,23,34816
Ma 232,72,124
buffer_load_dwordx4 v9, s[4:7], s38 offen offset:1024 lds
Ma 236,76,124
Rt 24,24,32768
Mv 236,76,120
Mv 204,76,116
Rt 26,24,34816
Mv 172,76,112
buffer_load_dwordx4 v10, s[4:7], s38 offen offset:2048 lds
Mv 140,76,108
Rt 28,25,32768
Mv 108,76,104
Mv 76,76,100
Rt 30,25,34816
Mv 44,76,96
buffer_load_dwordx4 v11, s[4:7], s38 offen offset:3072 lds
Mv 48,80,96
)" R"(
Rb 32,16,32768
Mv 80,80,100
Mv 112,80,104
Rb 36,16,34816
Mv 144,80,108
s_add_u32 s39, s39, 0x40000
Mv 176,80,112
Rb 40,16,40960
s_add_u32 m0, s37, 16384
Mv 208,80,116
buffer_load_dwordx4 v12, s[8:11], s39 offen offset:0 lds
.LzH25:
s_cbranch_execz .LzH35
Mv 240,80,120
Rb 44,16,43008
Ma 240,80,124
Ma 244,84,124
Rb 48,16,49152
Mv 244,84,120
Mv 212,84,116
Rb 52,16,51200
Mv 180,84,112
buffer_load_dwordx4 v13, s[8:11], s39 offen offset:1024 lds
Mv 148,84,108
Rb 56,16,57344
Mv 116,84,104
Mv 84,84,100
Rb 60,16,59392
Mv 52,84,96
Mv 56,88,96
Mv 88,88,100
buffer_load_dwordx4 v14, s[8:11], s39 offen offset:2048 lds
Mv 120,88,104
Mv 152,88,108
Mv 184,88,112
Mv 216,88,116
Mv 248,88,120
Ma 248,88,124
buffer_load_dwordx4 v15, s[8:11], s39 offen offset:3072 lds
Ma 252,92,124
Mv 252,92,120
s_add_u32 s88, s88, s90
Mv 220,92,116
Mv 188,92,112
buffer_load_dword v6, v5, s[24:27], s88 offen
Mv 156,92,108
Mv 124,92,104
Mv 92,92,100
Mv 60,92,96
s_waitcnt vmcnt(20) lgkmcnt(0)
s_barrier
Mv 32,0,32
Rt 64,18,49152
s_lshl_b32 s29, s91, 22
Mv 64,0,36
s_add_u32 s28, s89, s29
s_bfe_u32 s29, s63, 0x80000
Mv 96,0,40
Rt 66,18,51200
s_lshl_b32 s29, s29, 16
Mv 128,0,44
s_add_u32 s39, s39, 0x40000
s_add_u32 s45, s28, s29
Mv 160,0,48
Rt 68,19,49152
)" R"(
s_add_u32 m0, s37, 32768
Mv 192,0,52
buffer_load_dwordx4 v12, s[8:11], s39 offen offset:0 lds
Mv 224,0,56
Rt 70,19,51200
s_bfe_u32 s29, s63, 0x80008
Ma 224,0,60
s_lshl_b32 s29, s29, 16
s_add_u32 s46, s28, s29
Ma 228,4,60
Rt 72,20,49152
s_bfe_u32 s29, s63, 0x80010
Mv 228,4,56
s_lshl_b32 s29, s29, 16
s_add_u32 s47, s28, s29
Mv 196,4,52
Rt 74,20,51200
s_bfe_u32 s29, s63, 0x80018
Mv 164,4,48
s_lshl_b32 s29, s29, 16
s_add_u32 s48, s28, s29
Mv 132,4,44
Rt 76,21,49152
s_add_u32 s50, s89, 0xd40000
Mv 100,4,40
buffer_load_dwordx4 v13, s[8:11], s39 offen offset:1024 lds
Mv 68,4,36
Rt 78,21,51200
s_lshl2_add_u32 s28, s91, s93
Mv 36,4,32
s_lshl_b32 s28, s28, 7
s_add_u32 s49, s28, s50
Mv 40,8,32
Rt 80,22,49152
s_lshl_b32 s52, s35, 7
Mv 72,8,36
s_add_u32 s52, s52, s50
s_add_u32 s52, s52, 0xd000
Mv 104,8,40
Rt 82,22,51200
s_lshl_b32 s51, s35, 17
Mv 136,8,44
s_add_u32 s51, s51, s89
s_add_u32 s51, s51, 0x800000
Mv 168,8,48
Rt 84,23,49152
Mv 200,8,52
buffer_load_dwordx4 v14, s[8:11], s39 offen offset:2048 lds
Mv 232,8,56
Rt 86,23,51200
Ma 232,8,60
s_cmp_lg_u32 s33, 0
s_cmov_b32 s49, 0x40000000
Ma 236,12,60
Rt 88,24,49152
s_mov_b32 s53, 0x40000
Mv 236,12,56
s_mov_b32 s54, 0x3db8aa3b
s_mul_i32 s41, s33, 7168
Mv 204,12,52
Rt 90,24,51200
s_mul_i32 s42, s33, 0x70000
Mv 172,12,48
s_lshl_b32 s28, s34, 9
)" R"(
s_add_u32 s42, s42, s28
Mv 140,12,44
Rt 92,25,49152
s_mov_b32 s62, 0x4e800000
Mv 108,12,40
buffer_load_dwordx4 v15, s[8:11], s39 offen offset:3072 lds
Mv 76,12,36
Rt 94,25,51200
s_add_u32 s67, s89, 0xc00000
Mv 44,12,32
s_lshl3_add_u32 s28, s34, s35
s_add_u32 s64, s51, 0x100000
Mv 48,16,32
Rb 96,17,32768
s_lshl2_add_u32 s28, s28, s33
s_add_u32 m0, s36, 131072
s_add_u32 s44, s43, 0
Mv 80,16,36
s_lshl2_add_u32 s65, s35, s33
s_lshl_b32 s65, s65, 7
buffer_load_dwordx4 v26, s[12:15], s44 offen offset:0 lds
Mv 112,16,40
Rb 100,17,34816
s_add_u32 s65, s65, s50
Mv 144,16,44
s_add_u32 s65, s65, 0xc000
s_lshl_b32 s66, s28, 10
Mv 176,16,48
Rb 104,17,40960
s_add_u32 s66, s66, 0x1400000
Mv 208,16,52
s_mov_b32 s28, s35
s_lshl2_add_u32 s28, s28, s33
buffer_load_dwordx4 v27, s[12:15], s44 offen offset:1024 lds
Mv 240,16,56
Rb 108,17,43008
s_and_b32 s29, s34, 1
Ma 240,16,60
s_lshl1_add_u32 s29, s28, s29
s_lshl_b32 s73, s29, 15
Ma 244,20,60
Rb 112,17,49152
s_and_b32 s29, s89, 0xfe000000
Mv 244,20,56
s_add_u32 s73, s73, s29
s_add_u32 s73, s73, 0x1000000
s_add_u32 s44, s43, 12288
buffer_load_dwordx4 v26, s[12:15], s44 offen offset:2048 lds
Mv 212,20,52
Rb 116,17,51200
s_lshl4_add_u32 s28, s28, s34
Mv 180,20,48
s_lshl_b32 s68, s28, 8
s_add_u32 s68, s68, 0x1510000
Mv 148,20,44
Rb 120,17,57344
s_add_u32 s55, s89, 0x120000
Mv 116,20,40
s_lshl_b32 s28, s35, 1
s_sub_u32 s57, 14, s28
buffer_load_dwordx4 v27, s[12:15], s44 offen offset:3072 lds
Mv 84,20,36
Rb 124,17,59392
s_mov_b32 s59, s89
)" R"(
Mv 52,20,32
s_mov_b32 s58, 2
s_add_u32 m0, s36, 135168
Mv 56,24,32
s_cmp_eq_u32 s35, 7
s_cmov_b32 s55, s89
s_add_u32 s44, s43, 128
Mv 88,24,36
s_cmp_eq_u32 s35, 7
s_cmov_b32 s57, 16
buffer_load_dwordx4 v26, s[12:15], s44 offen offset:0 lds
Mv 120,24,40
s_cmp_eq_u32 s35, 7
s_cmov_b32 s59, s67
Mv 152,24,44
s_add_u32 s56, s55, 0x400000
Mv 184,24,48
Mv 216,24,52
buffer_load_dwordx4 v27, s[12:15], s44 offen offset:1024 lds
Mv 248,24,56
Ma 248,24,60
Ma 252,28,60
Mv 252,28,56
s_add_u32 s44, s43, 12416
buffer_load_dwordx4 v26, s[12:15], s44 offen offset:2048 lds
Mv 220,28,52
Mv 188,28,48
Mv 156,28,44
Mv 124,28,40
buffer_load_dwordx4 v27, s[12:15], s44 offen offset:3072 lds
Mv 92,28,36
Mv 60,28,32
s_waitcnt vmcnt(17) lgkmcnt(0)
s_barrier
Mv 32,64,96
Rt 0,18,0
Mv 64,64,100
s_add_u32 s38, s38, 0xffff0080
Mv 96,64,104
Rt 2,18,2048
s_add_u32 m0, s36, 32768
Mv 128,64,108
buffer_load_dwordx4 v8, s[4:7], s38 offen offset:0 lds
Mv 160,64,112
Rt 4,19,0
Mv 192,64,116
Mv 224,64,120
Rt 6,19,2048
Ma 224,64,124
buffer_load_dwordx4 v9, s[4:7], s38 offen offset:1024 lds
Ma 228,68,124
Rt 8,20,0
s_lshl2_add_u32 s28, s2, s33
Mv 228,68,120
s_lshl_b32 s28, s28, 11
s_add_u32 s28, s28, 0x1500000
Mv 196,68,116
Rt 10,20,2048
v_min_u32 v3, 15, v0
Mv 164,68,112
buffer_load_dwordx4 v10, s[4:7], s38 offen offset:2048 lds
Mv 132,68,108
Rt 12,21,0
v_lshlrev_b32 v3, 7, v3
Mv 100,68,104
)" R"(
Mv 68,68,100
Rt 14,21,2048
Mv 36,68,96
buffer_load_dwordx4 v11, s[4:7], s38 offen offset:3072 lds
Mv 40,72,96
Rt 16,22,0
Mv 72,72,100
s_add_u32 s38, s38, 0x10000
Mv 104,72,104
Rt 18,22,2048
s_add_u32 m0, s36, 36864
Mv 136,72,108
buffer_load_dwordx4 v8, s[4:7], s38 offen offset:0 lds
Mv 168,72,112
Rt 20,23,0
Mv 200,72,116
Mv 232,72,120
Rt 22,23,2048
Ma 232,72,124
buffer_load_dwordx4 v9, s[4:7], s38 offen offset:1024 lds
Ma 236,76,124
Rt 24,24,0
Mv 236,76,120
Mv 204,76,116
Rt 26,24,2048
Mv 172,76,112
buffer_load_dwordx4 v10, s[4:7], s38 offen offset:2048 lds
Mv 140,76,108
Rt 28,25,0
Mv 108,76,104
Mv 76,76,100
Rt 30,25,2048
Mv 44,76,96
buffer_load_dwordx4 v11, s[4:7], s38 offen offset:3072 lds
Mv 48,80,96
Rb 32,16,0
Mv 80,80,100
Mv 112,80,104
Rb 36,16,2048
Mv 144,80,108
s_add_u32 s39, s39, 0x40000
Mv 176,80,112
Rb 40,16,8192
s_add_u32 m0, s37, 49152
Mv 208,80,116
buffer_load_dwordx4 v12, s[8:11], s39 offen offset:0 lds
Mv 240,80,120
Rb 44,16,10240
Ma 240,80,124
Ma 244,84,124
Rb 48,16,16384
Mv 244,84,120
Mv 212,84,116
Rb 52,16,18432
Mv 180,84,112
buffer_load_dwordx4 v13, s[8:11], s39 offen offset:1024 lds
Mv 148,84,108
Rb 56,16,24576
Mv 116,84,104
Mv 84,84,100
Rb 60,16,26624
Mv 52,84,96
Mv 56,88,96
Mv 88,88,100
)" R"(
buffer_load_dwordx4 v14, s[8:11], s39 offen offset:2048 lds
Mv 120,88,104
Mv 152,88,108
Mv 184,88,112
Mv 216,88,116
Mv 248,88,120
Ma 248,88,124
buffer_load_dwordx4 v15, s[8:11], s39 offen offset:3072 lds
Ma 252,92,124
Mv 252,92,120
s_add_u32 s88, s88, s90
s_mov_b32 s40, 5
Mv 220,92,116
Mv 188,92,112
buffer_load_dword v6, v5, s[24:27], s88 offen
Mv 156,92,108
Mv 124,92,104
buffer_load_dword v6, v3, s[20:23], s28 offen sc0 sc1
Mv 92,92,100
Mv 60,92,96
s_waitcnt vmcnt(27) lgkmcnt(0)
s_barrier
.LzPAK:
Mv 32,0,32
Rt 64,18,16384
Mv 64,0,36
Mv 96,0,40
Rt 66,18,18432
Mv 128,0,44
s_add_u32 s39, s39, 0x40000
Mv 160,0,48
Rt 68,19,16384
s_add_u32 m0, s37, 0
Mv 192,0,52
buffer_load_dwordx4 v12, s[8:11], s39 offen offset:0 lds
Mv 224,0,56
Rt 70,19,18432
Ma 224,0,60
Ma 228,4,60
Rt 72,20,16384
Mv 228,4,56
Mv 196,4,52
Rt 74,20,18432
Mv 164,4,48
Mv 132,4,44
Rt 76,21,16384
Mv 100,4,40
buffer_load_dwordx4 v13, s[8:11], s39 offen offset:1024 lds
Mv 68,4,36
Rt 78,21,18432
Mv 36,4,32
Mv 40,8,32
Rt 80,22,16384
Mv 72,8,36
Mv 104,8,40
Rt 82,22,18432
Mv 136,8,44
Mv 168,8,48
Rt 84,23,16384
Mv 200,8,52
buffer_load_dwordx4 v14, s[8:11], s39 offen offset:2048 lds
Mv 232,8,56
Rt 86,23,18432
Ma 232,8,60
Ma 236,12,60
)" R"(
Rt 88,24,16384
Mv 236,12,56
Mv 204,12,52
Rt 90,24,18432
Mv 172,12,48
Mv 140,12,44
Rt 92,25,16384
Mv 108,12,40
buffer_load_dwordx4 v15, s[8:11], s39 offen offset:3072 lds
Mv 76,12,36
Rt 94,25,18432
Mv 44,12,32
Mv 48,16,32
Rb 96,17,0
Mv 80,16,36
Mv 112,16,40
Rb 100,17,2048
Mv 144,16,44
Mv 176,16,48
Rb 104,17,8192
Mv 208,16,52
Mv 240,16,56
Rb 108,17,10240
Ma 240,16,60
Ma 244,20,60
Rb 112,17,16384
Mv 244,20,56
Mv 212,20,52
Rb 116,17,18432
Mv 180,20,48
Mv 148,20,44
Rb 120,17,24576
Mv 116,20,40
Mv 84,20,36
Rb 124,17,26624
Mv 52,20,32
Mv 56,24,32
Mv 88,24,36
Mv 120,24,40
Mv 152,24,44
Mv 184,24,48
Mv 216,24,52
Mv 248,24,56
Ma 248,24,60
Ma 252,28,60
Mv 252,28,56
Mv 220,28,52
Mv 188,28,48
Mv 156,28,44
Mv 124,28,40
Mv 92,28,36
Mv 60,28,32
s_waitcnt vmcnt(9) lgkmcnt(0)
s_barrier
Mv 32,64,96
Rt 0,18,32768
Mv 64,64,100
s_add_u32 s38, s38, 0xffff0080
Mv 96,64,104
Rt 2,18,34816
s_add_u32 m0, s36, 0
Mv 128,64,108
buffer_load_dwordx4 v8, s[4:7], s38 offen offset:0 lds
Mv 160,64,112
Rt 4,19,32768
)" R"(
Mv 192,64,116
Mv 224,64,120
Rt 6,19,34816
Ma 224,64,124
buffer_load_dwordx4 v9, s[4:7], s38 offen offset:1024 lds
Ma 228,68,124
Rt 8,20,32768
Mv 228,68,120
Mv 196,68,116
Rt 10,20,34816
Mv 164,68,112
buffer_load_dwordx4 v10, s[4:7], s38 offen offset:2048 lds
Mv 132,68,108
Rt 12,21,32768
Mv 100,68,104
Mv 68,68,100
Rt 14,21,34816
Mv 36,68,96
buffer_load_dwordx4 v11, s[4:7], s38 offen offset:3072 lds
Mv 40,72,96
Rt 16,22,32768
Mv 72,72,100
s_add_u32 s38, s38, 0x10000
Mv 104,72,104
Rt 18,22,34816
s_add_u32 m0, s36, 4096
Mv 136,72,108
buffer_load_dwordx4 v8, s[4:7], s38 offen offset:0 lds
Mv 168,72,112
Rt 20,23,32768
Mv 200,72,116
Mv 232,72,120
Rt 22,23,34816
Ma 232,72,124
buffer_load_dwordx4 v9, s[4:7], s38 offen offset:1024 lds
Ma 236,76,124
Rt 24,24,32768
Mv 236,76,120
Mv 204,76,116
Rt 26,24,34816
Mv 172,76,112
buffer_load_dwordx4 v10, s[4:7], s38 offen offset:2048 lds
Mv 140,76,108
Rt 28,25,32768
Mv 108,76,104
Mv 76,76,100
Rt 30,25,34816
Mv 44,76,96
buffer_load_dwordx4 v11, s[4:7], s38 offen offset:3072 lds
Mv 48,80,96
Rb 32,16,32768
Mv 80,80,100
Mv 112,80,104
Rb 36,16,34816
Mv 144,80,108
s_add_u32 s39, s39, 0x40000
Mv 176,80,112
Rb 40,16,40960
s_add_u32 m0, s37, 16384
Mv 208,80,116
buffer_load_dwordx4 v12, s[8:11], s39 offen offset:0 lds
Mv 240,80,120
Rb 44,16,43008
Ma 240,80,124
)" R"(
Ma 244,84,124
Rb 48,16,49152
Mv 244,84,120
Mv 212,84,116
Rb 52,16,51200
Mv 180,84,112
buffer_load_dwordx4 v13, s[8:11], s39 offen offset:1024 lds
Mv 148,84,108
Rb 56,16,57344
Mv 116,84,104
Mv 84,84,100
Rb 60,16,59392
Mv 52,84,96
Mv 56,88,96
Mv 88,88,100
buffer_load_dwordx4 v14, s[8:11], s39 offen offset:2048 lds
Mv 120,88,104
Mv 152,88,108
Mv 184,88,112
Mv 216,88,116
Mv 248,88,120
Ma 248,88,124
buffer_load_dwordx4 v15, s[8:11], s39 offen offset:3072 lds
Ma 252,92,124
Mv 252,92,120
s_add_u32 s88, s88, s90
Mv 220,92,116
Mv 188,92,112
buffer_load_dword v6, v5, s[24:27], s88 offen
Mv 156,92,108
Mv 124,92,104
Mv 92,92,100
Mv 60,92,96
s_waitcnt vmcnt(18) lgkmcnt(0)
s_barrier
Mv 32,0,32
Rt 64,18,49152
Mv 64,0,36
Mv 96,0,40
Rt 66,18,51200
Mv 128,0,44
s_add_u32 s39, s39, 0x40000
.LzH26:
s_cbranch_execz .LzH36
Mv 160,0,48
Rt 68,19,49152
s_add_u32 m0, s37, 32768
Mv 192,0,52
buffer_load_dwordx4 v12, s[8:11], s39 offen offset:0 lds
Mv 224,0,56
Rt 70,19,51200
Ma 224,0,60
Ma 228,4,60
Rt 72,20,49152
Mv 228,4,56
Mv 196,4,52
Rt 74,20,51200
Mv 164,4,48
Mv 132,4,44
Rt 76,21,49152
Mv 100,4,40
buffer_load_dwordx4 v13, s[8:11], s39 offen offset:1024 lds
Mv 68,4,36
Rt 78,21,51200
Mv 36,4,32
)" R"(
Mv 40,8,32
Rt 80,22,49152
Mv 72,8,36
Mv 104,8,40
Rt 82,22,51200
Mv 136,8,44
Mv 168,8,48
Rt 84,23,49152
Mv 200,8,52
buffer_load_dwordx4 v14, s[8:11], s39 offen offset:2048 lds
Mv 232,8,56
Rt 86,23,51200
Ma 232,8,60
Ma 236,12,60
Rt 88,24,49152
Mv 236,12,56
Mv 204,12,52
Rt 90,24,51200
Mv 172,12,48
Mv 140,12,44
Rt 92,25,49152
Mv 108,12,40
buffer_load_dwordx4 v15, s[8:11], s39 offen offset:3072 lds
Mv 76,12,36
Rt 94,25,51200
Mv 44,12,32
Mv 48,16,32
Rb 96,17,32768
Mv 80,16,36
Mv 112,16,40
Rb 100,17,34816
Mv 144,16,44
Mv 176,16,48
Rb 104,17,40960
Mv 208,16,52
Mv 240,16,56
Rb 108,17,43008
Ma 240,16,60
Ma 244,20,60
Rb 112,17,49152
Mv 244,20,56
Mv 212,20,52
Rb 116,17,51200
Mv 180,20,48
Mv 148,20,44
Rb 120,17,57344
Mv 116,20,40
Mv 84,20,36
Rb 124,17,59392
Mv 52,20,32
Mv 56,24,32
Mv 88,24,36
Mv 120,24,40
Mv 152,24,44
Mv 184,24,48
Mv 216,24,52
Mv 248,24,56
Ma 248,24,60
Ma 252,28,60
Mv 252,28,56
Mv 220,28,52
Mv 188,28,48
Mv 156,28,44
Mv 124,28,40
)" R"(
Mv 92,28,36
Mv 60,28,32
s_waitcnt vmcnt(9) lgkmcnt(0)
s_barrier
Mv 32,64,96
Rt 0,18,0
Mv 64,64,100
s_add_u32 s38, s38, 0xffff0080
Mv 96,64,104
Rt 2,18,2048
s_add_u32 m0, s36, 32768
Mv 128,64,108
buffer_load_dwordx4 v8, s[4:7], s38 offen offset:0 lds
Mv 160,64,112
Rt 4,19,0
Mv 192,64,116
Mv 224,64,120
Rt 6,19,2048
Ma 224,64,124
buffer_load_dwordx4 v9, s[4:7], s38 offen offset:1024 lds
Ma 228,68,124
Rt 8,20,0
Mv 228,68,120
Mv 196,68,116
Rt 10,20,2048
Mv 164,68,112
buffer_load_dwordx4 v10, s[4:7], s38 offen offset:2048 lds
Mv 132,68,108
Rt 12,21,0
Mv 100,68,104
Mv 68,68,100
Rt 14,21,2048
Mv 36,68,96
buffer_load_dwordx4 v11, s[4:7], s38 offen offset:3072 lds
Mv 40,72,96
Rt 16,22,0
Mv 72,72,100
s_add_u32 s38, s38, 0x10000
Mv 104,72,104
Rt 18,22,2048
s_add_u32 m0, s36, 36864
Mv 136,72,108
buffer_load_dwordx4 v8, s[4:7], s38 offen offset:0 lds
Mv 168,72,112
Rt 20,23,0
Mv 200,72,116
Mv 232,72,120
Rt 22,23,2048
Ma 232,72,124
buffer_load_dwordx4 v9, s[4:7], s38 offen offset:1024 lds
Ma 236,76,124
Rt 24,24,0
Mv 236,76,120
Mv 204,76,116
Rt 26,24,2048
Mv 172,76,112
buffer_load_dwordx4 v10, s[4:7], s38 offen offset:2048 lds
Mv 140,76,108
Rt 28,25,0
Mv 108,76,104
Mv 76,76,100
Rt 30,25,2048
Mv 44,76,96
buffer_load_dwordx4 v11, s[4:7], s38 offen offset:3072 lds
Mv 48,80,96
)" R"(
Rb 32,16,0
Mv 80,80,100
Mv 112,80,104
Rb 36,16,2048
Mv 144,80,108
s_add_u32 s39, s39, 0x40000
Mv 176,80,112
Rb 40,16,8192
s_add_u32 m0, s37, 49152
Mv 208,80,116
buffer_load_dwordx4 v12, s[8:11], s39 offen offset:0 lds
Mv 240,80,120
Rb 44,16,10240
Ma 240,80,124
Ma 244,84,124
Rb 48,16,16384
Mv 244,84,120
Mv 212,84,116
Rb 52,16,18432
Mv 180,84,112
buffer_load_dwordx4 v13, s[8:11], s39 offen offset:1024 lds
Mv 148,84,108
Rb 56,16,24576
Mv 116,84,104
Mv 84,84,100
Rb 60,16,26624
Mv 52,84,96
Mv 56,88,96
Mv 88,88,100
buffer_load_dwordx4 v14, s[8:11], s39 offen offset:2048 lds
Mv 120,88,104
Mv 152,88,108
Mv 184,88,112
Mv 216,88,116
Mv 248,88,120
Ma 248,88,124
buffer_load_dwordx4 v15, s[8:11], s39 offen offset:3072 lds
Ma 252,92,124
Mv 252,92,120
s_add_u32 s88, s88, s90
Mv 220,92,116
s_sub_u32 s40, s40, 1
Mv 188,92,112
buffer_load_dword v6, v5, s[24:27], s88 offen
Mv 156,92,108
Mv 124,92,104
Mv 92,92,100
Mv 60,92,96
s_cmp_lg_u32 s40, 0
s_waitcnt vmcnt(18) lgkmcnt(0)
s_barrier
s_cbranch_scc1 .LzPAK
.LzPAKX:
Mv 32,0,32
Rt 64,18,16384
Mv 64,0,36
Rb 192,28,32768
Mv 96,0,40
Rt 66,18,18432
Mv 128,0,44
s_add_u32 s39, s39, 0x40000
Rb 196,28,34816
Mv 160,0,48
Rt 68,19,16384
s_add_u32 m0, s37, 0
)" R"(
Mv 192,0,52
buffer_load_dwordx4 v12, s[8:11], s39 offen offset:0 lds
Mv 224,0,56
Rt 70,19,18432
Ma 224,0,60
Rb 200,29,32768
Ma 228,4,60
Rt 72,20,16384
Mv 228,4,56
Rb 204,29,34816
Mv 196,4,52
Rt 74,20,18432
Mv 164,4,48
Rb 208,28,36864
Mv 132,4,44
Rt 76,21,16384
Mv 100,4,40
buffer_load_dwordx4 v13, s[8:11], s39 offen offset:1024 lds
Mv 68,4,36
Rt 78,21,18432
Mv 36,4,32
Rb 212,28,38912
Mv 40,8,32
Rt 80,22,16384
Mv 72,8,36
Rb 216,29,36864
Mv 104,8,40
Rt 82,22,18432
Mv 136,8,44
Rb 220,29,38912
Mv 168,8,48
Rt 84,23,16384
Mv 200,8,52
buffer_load_dwordx4 v14, s[8:11], s39 offen offset:2048 lds
Mv 232,8,56
Rt 86,23,18432
Ma 232,8,60
Ma 236,12,60
Rt 88,24,16384
Mv 236,12,56
Mv 204,12,52
Rt 90,24,18432
Mv 172,12,48
Mv 140,12,44
Rt 92,25,16384
Mv 108,12,40
buffer_load_dwordx4 v15, s[8:11], s39 offen offset:3072 lds
Mv 76,12,36
Rt 94,25,18432
Mv 44,12,32
s_add_u32 s44, s43, 256
s_waitcnt lgkmcnt(6)
Mv 48,16,32
Rb 96,17,0
s_add_u32 m0, s36, 131072
Mv 80,16,36
buffer_load_dwordx4 v26, s[12:15], s44 offen offset:0 lds
Mv 112,16,40
Rb 100,17,2048
Mv 144,16,44
Mv 176,16,48
Rb 104,17,8192
Mv 208,16,52
buffer_load_dwordx4 v27, s[12:15], s44 offen offset:1024 lds
)" R"(
Mv 240,16,56
Rb 108,17,10240
Ma 240,16,60
Ma 244,20,60
Rb 112,17,16384
Mv 244,20,56
s_add_u32 s44, s43, 12544
buffer_load_dwordx4 v26, s[12:15], s44 offen offset:2048 lds
Mv 212,20,52
Rb 116,17,18432
Mv 180,20,48
Mv 148,20,44
Rb 120,17,24576
Mv 116,20,40
buffer_load_dwordx4 v27, s[12:15], s44 offen offset:3072 lds
Mv 84,20,36
Rb 124,17,26624
Mv 52,20,32
Mv 56,24,32
s_add_u32 m0, s36, 135168
s_add_u32 s44, s43, 384
Mv 88,24,36
buffer_load_dwordx4 v26, s[12:15], s44 offen offset:0 lds
Mv 120,24,40
Mv 152,24,44
Mv 184,24,48
Mv 216,24,52
buffer_load_dwordx4 v27, s[12:15], s44 offen offset:1024 lds
Mv 248,24,56
Ma 248,24,60
Ma 252,28,60
Mv 252,28,56
s_add_u32 s44, s43, 12672
buffer_load_dwordx4 v26, s[12:15], s44 offen offset:2048 lds
Mv 220,28,52
Mv 188,28,48
Mv 156,28,44
s_mov_b32 s85, 0x1508800
v_and_b32 v2, 7, v0
v_lshlrev_b32 v2, 7, v2
Mv 124,28,40
buffer_load_dwordx4 v27, s[12:15], s44 offen offset:3072 lds
Mv 92,28,36
buffer_load_dword v4, v2, s[20:23], s85 offen sc0 sc1
Mv 60,28,32
s_waitcnt vmcnt(18) lgkmcnt(0)
s_barrier
Mv 32,64,96
Rt 0,18,32768
Mv 64,64,100
s_add_u32 s38, s38, 0xffff0080
Mv 96,64,104
Rt 2,18,34816
s_add_u32 m0, s36, 0
Mv 128,64,108
buffer_load_dwordx4 v8, s[4:7], s38 offen offset:0 lds
Mv 160,64,112
Rt 4,19,32768
Mv 192,64,116
Mv 224,64,120
Rt 6,19,34816
Ma 224,64,124
buffer_load_dwordx4 v9, s[4:7], s38 offen offset:1024 lds
Ma 228,68,124
Rt 8,20,32768
)" R"(
Mv 228,68,120
Mv 196,68,116
Rt 10,20,34816
Mv 164,68,112
buffer_load_dwordx4 v10, s[4:7], s38 offen offset:2048 lds
Mv 132,68,108
Rt 12,21,32768
Mv 100,68,104
Mv 68,68,100
Rt 14,21,34816
Mv 36,68,96
buffer_load_dwordx4 v11, s[4:7], s38 offen offset:3072 lds
Mv 40,72,96
Rt 16,22,32768
Mv 72,72,100
s_add_u32 s38, s38, 0x10000
Mv 104,72,104
Rt 18,22,34816
s_add_u32 m0, s36, 4096
Mv 136,72,108
buffer_load_dwordx4 v8, s[4:7], s38 offen offset:0 lds
Mv 168,72,112
Rt 20,23,32768
Mv 200,72,116
Mv 232,72,120
Rt 22,23,34816
Ma 232,72,124
buffer_load_dwordx4 v9, s[4:7], s38 offen offset:1024 lds
Ma 236,76,124
Rt 24,24,32768
Mv 236,76,120
Mv 204,76,116
Rt 26,24,34816
Mv 172,76,112
buffer_load_dwordx4 v10, s[4:7], s38 offen offset:2048 lds
Mv 140,76,108
Rt 28,25,32768
Mv 108,76,104
Mv 76,76,100
Rt 30,25,34816
Mv 44,76,96
buffer_load_dwordx4 v11, s[4:7], s38 offen offset:3072 lds
Mv 48,80,96
Rb 32,16,32768
Mv 80,80,100
Mv 112,80,104
Rb 36,16,34816
Mv 144,80,108
s_add_u32 s39, s39, 0x40000
Mv 176,80,112
Rb 40,16,40960
s_add_u32 m0, s37, 16384
Mv 208,80,116
buffer_load_dwordx4 v12, s[8:11], s39 offen offset:0 lds
Mv 240,80,120
Rb 44,16,43008
Ma 240,80,124
Ma 244,84,124
Rb 48,16,49152
Mv 244,84,120
Mv 212,84,116
Rb 52,16,51200
Mv 180,84,112
buffer_load_dwordx4 v13, s[8:11], s39 offen offset:1024 lds
)" R"(
Mv 148,84,108
Rb 56,16,57344
Mv 116,84,104
Mv 84,84,100
Rb 60,16,59392
Mv 52,84,96
Mv 56,88,96
Mv 88,88,100
buffer_load_dwordx4 v14, s[8:11], s39 offen offset:2048 lds
Mv 120,88,104
Mv 152,88,108
Mv 184,88,112
Mv 216,88,116
Mv 248,88,120
Ma 248,88,124
buffer_load_dwordx4 v15, s[8:11], s39 offen offset:3072 lds
Ma 252,92,124
Mv 252,92,120
Mv 220,92,116
Mv 188,92,112
Mv 156,92,108
Mv 124,92,104
Mv 92,92,100
Mv 60,92,96
s_waitcnt vmcnt(13) lgkmcnt(0)
s_barrier
Mv 32,0,32
Rt 64,18,49152
Mv 64,0,36
Rb 128,28,32768
Mv 96,0,40
Rt 66,18,51200
Mv 128,0,44
s_add_u32 s39, s39, 0x40000
Rb 132,28,34816
Mv 160,0,48
Rt 68,19,49152
s_add_u32 m0, s37, 32768
Mv 192,0,52
buffer_load_dwordx4 v12, s[8:11], s39 offen offset:0 lds
Mv 224,0,56
Rt 70,19,51200
Ma 224,0,60
Rb 136,29,32768
Ma 228,4,60
Rt 72,20,49152
Mv 228,4,56
Rb 140,29,34816
Mv 196,4,52
Rt 74,20,51200
Mv 164,4,48
Rb 144,28,36864
Mv 132,4,44
Rt 76,21,49152
Mv 100,4,40
buffer_load_dwordx4 v13, s[8:11], s39 offen offset:1024 lds
Mv 68,4,36
Rt 78,21,51200
Mv 36,4,32
Rb 148,28,38912
Mv 40,8,32
Rt 80,22,49152
Mv 72,8,36
Rb 152,29,36864
Mv 104,8,40
)" R"(
Rt 82,22,51200
Mv 136,8,44
Rb 156,29,38912
Mv 168,8,48
Rt 84,23,49152
Mv 200,8,52
buffer_load_dwordx4 v14, s[8:11], s39 offen offset:2048 lds
Mv 232,8,56
Rt 86,23,51200
Ma 232,8,60
Ma 236,12,60
Rt 88,24,49152
Mv 236,12,56
Mv 204,12,52
Rt 90,24,51200
Mv 172,12,48
Mv 140,12,44
Rt 92,25,49152
Mv 108,12,40
buffer_load_dwordx4 v15, s[8:11], s39 offen offset:3072 lds
Mv 76,12,36
Rt 94,25,51200
Mv 44,12,32
s_add_u32 s44, s43, 512
s_waitcnt lgkmcnt(6)
Mv 48,16,32
Rb 96,17,32768
s_add_u32 m0, s36, 131072
Mv 80,16,36
buffer_load_dwordx4 v26, s[12:15], s44 offen offset:0 lds
Mv 112,16,40
Rb 100,17,34816
Mv 144,16,44
Mv 176,16,48
Rb 104,17,40960
Mv 208,16,52
buffer_load_dwordx4 v27, s[12:15], s44 offen offset:1024 lds
Mv 240,16,56
Rb 108,17,43008
Ma 240,16,60
Ma 244,20,60
Rb 112,17,49152
Mv 244,20,56
s_add_u32 s44, s43, 12800
buffer_load_dwordx4 v26, s[12:15], s44 offen offset:2048 lds
Mv 212,20,52
Rb 116,17,51200
Mv 180,20,48
Mv 148,20,44
Rb 120,17,57344
.LzH27:
s_cbranch_execz .LzH37
Mv 116,20,40
buffer_load_dwordx4 v27, s[12:15], s44 offen offset:3072 lds
Mv 84,20,36
Rb 124,17,59392
Mv 52,20,32
Mv 56,24,32
s_add_u32 m0, s36, 135168
s_add_u32 s44, s43, 640
Mv 88,24,36
buffer_load_dwordx4 v26, s[12:15], s44 offen offset:0 lds
Mv 120,24,40
Mv 152,24,44
)" R"(
Mv 184,24,48
Mv 216,24,52
buffer_load_dwordx4 v27, s[12:15], s44 offen offset:1024 lds
Mv 248,24,56
Ma 248,24,60
Ma 252,28,60
Mv 252,28,56
s_add_u32 s44, s43, 12928
buffer_load_dwordx4 v26, s[12:15], s44 offen offset:2048 lds
Mv 220,28,52
Mv 188,28,48
Mv 156,28,44
Mv 124,28,40
buffer_load_dwordx4 v27, s[12:15], s44 offen offset:3072 lds
Mv 92,28,36
Mv 60,28,32
s_waitcnt vmcnt(16) lgkmcnt(0)
s_barrier
Mv 32,64,96
Rt 0,18,0
v_xor_b32 v4, s3, v4
v_cmp_gt_u32_e32 vcc, 2, v4
s_andn2_b32 s28, 0xff, vcc_lo
s_cbranch_scc1 .Lzsp1
.Lzok1:
Mv 64,64,100
s_add_u32 s38, s38, 0xffff0080
v_xor_b32 v4, s3, v4
Mv 96,64,104
Rt 2,18,2048
s_add_u32 m0, s36, 32768
Mv 128,64,108
buffer_load_dwordx4 v8, s[4:7], s38 offen offset:0 lds
Mv 160,64,112
Rt 4,19,0
v_and_b32 v4, 1, v4
Mv 192,64,116
v_cmp_eq_u32_e32 vcc, 1, v4
s_mov_b32 s85, vcc_lo
Mv 224,64,120
Rt 6,19,2048
s_and_b32 s28, s2, 7
Ma 224,64,124
buffer_load_dwordx4 v9, s[4:7], s38 offen offset:1024 lds
Ma 228,68,124
Rt 8,20,0
s_lshr_b32 s28, s85, s28
Mv 228,68,120
s_xor_b32 s28, s28, s34
s_and_b32 s28, s28, 1
Mv 196,68,116
Rt 10,20,2048
s_lshl_b32 s84, s28, 13
Mv 164,68,112
buffer_load_dwordx4 v10, s[4:7], s38 offen offset:2048 lds
Mv 132,68,108
Rt 12,21,0
s_add_u32 s49, s49, s84
Mv 100,68,104
s_add_u32 s50, s50, s84
s_add_u32 s60, s50, 7168
Mv 68,68,100
Rt 14,21,2048
s_add_u32 s52, s52, s84
Mv 36,68,96
)" R"(
buffer_load_dwordx4 v11, s[4:7], s38 offen offset:3072 lds
Mv 40,72,96
Rt 16,22,0
s_add_u32 s65, s65, s84
Mv 72,72,100
s_add_u32 s38, s38, 0x10000
s_lshr_b32 s28, s2, 6
Mv 104,72,104
Rt 18,22,2048
s_add_u32 m0, s36, 36864
Mv 136,72,108
buffer_load_dwordx4 v8, s[4:7], s38 offen offset:0 lds
Mv 168,72,112
Rt 20,23,0
s_lshl_b32 s28, s28, 14
Mv 200,72,116
s_add_u32 s28, s28, s50
s_bfe_u32 s29, s2, 0x20004
Mv 232,72,120
Rt 22,23,2048
s_lshl2_add_u32 s29, s29, s33
Ma 232,72,124
buffer_load_dwordx4 v9, s[4:7], s38 offen offset:1024 lds
Ma 236,76,124
Rt 24,24,0
s_lshl_b32 s29, s29, 9
Mv 236,76,120
s_add_u32 s28, s28, s29
v_min_u32 v4, 3, v0
Mv 204,76,116
Rt 26,24,2048
v_lshlrev_b32 v4, 7, v4
Mv 172,76,112
buffer_load_dwordx4 v10, s[4:7], s38 offen offset:2048 lds
Mv 140,76,108
Rt 28,25,0
Mv 108,76,104
buffer_load_dword v4, v4, s[20:23], s28 offen sc0 sc1
Mv 76,76,100
Rt 30,25,2048
s_lshl1_add_u32 s28, s36, s84
Mv 44,76,96
buffer_load_dwordx4 v11, s[4:7], s38 offen offset:3072 lds
Mv 48,80,96
Rb 32,16,0
v_add_u32 v2, s28, v1
Mv 80,80,100
v_add_u32 v3, 0x1000, v2
s_lshl_b32 s28, s34, 13
Mv 112,80,104
Rb 36,16,2048
s_xor_b32 s28, s28, s84
Mv 144,80,108
s_add_u32 s39, s39, 0x40000
s_and_b32 s28, s28, 0x2000
Mv 176,80,112
Rb 40,16,8192
s_add_u32 m0, s37, 49152
Mv 208,80,116
buffer_load_dwordx4 v12, s[8:11], s39 offen offset:0 lds
Mv 240,80,120
Rb 44,16,10240
s_xor_b32 s73, s73, s28
Ma 240,80,124
)" R"(
Ma 244,84,124
Rb 48,16,16384
Mv 244,84,120
Mv 212,84,116
Rb 52,16,18432
Mv 180,84,112
buffer_load_dwordx4 v13, s[8:11], s39 offen offset:1024 lds
Mv 148,84,108
Rb 56,16,24576
Mv 116,84,104
Mv 84,84,100
Rb 60,16,26624
Mv 52,84,96
Mv 56,88,96
Mv 88,88,100
buffer_load_dwordx4 v14, s[8:11], s39 offen offset:2048 lds
Mv 120,88,104
Mv 152,88,108
Mv 184,88,112
Mv 216,88,116
Mv 248,88,120
Ma 248,88,124
buffer_load_dwordx4 v15, s[8:11], s39 offen offset:3072 lds
Ma 252,92,124
Mv 252,92,120
Mv 220,92,116
Mv 188,92,112
Mv 156,92,108
Mv 124,92,104
Mv 92,92,100
Mv 60,92,96
s_waitcnt vmcnt(5) lgkmcnt(0)
s_barrier
Mv 32,0,32
Rt 64,18,16384
Mv 64,0,36
Rb 160,28,32768
v_and_b32 v5, 7, v0
Mv 96,0,40
Rt 66,18,18432
v_lshlrev_b32 v5, 7, v5
Mv 128,0,44
Rb 164,28,34816
v_mov_b32 v6, s3
Mv 160,0,48
Rt 68,19,16384
Mv 192,0,52
Mv 224,0,56
Rt 70,19,18432
Ma 224,0,60
Rb 168,29,32768
Ma 228,4,60
Rt 72,20,16384
Mv 228,4,56
Rb 172,29,34816
Mv 196,4,52
Rt 74,20,18432
Mv 164,4,48
Rb 176,28,36864
Mv 132,4,44
Rt 76,21,16384
Mv 100,4,40
Mv 68,4,36
Rt 78,21,18432
Mv 36,4,32
)" R"(
Rb 180,28,38912
Mv 40,8,32
Rt 80,22,16384
Mv 72,8,36
Rb 184,29,36864
Mv 104,8,40
Rt 82,22,18432
Mv 136,8,44
Rb 188,29,38912
Mv 168,8,48
Rt 84,23,16384
Mv 200,8,52
Mv 232,8,56
Rt 86,23,18432
Ma 232,8,60
Ma 236,12,60
Rt 88,24,16384
Mv 236,12,56
Mv 204,12,52
Rt 90,24,18432
Mv 172,12,48
Mv 140,12,44
Rt 92,25,16384
Mv 108,12,40
Mv 76,12,36
Rt 94,25,18432
v_lshrrev_b32 v10, 3, v0
Mv 44,12,32
v_and_b32 v11, 0x70, v1
v_lshl_or_b32 v10, v10, 13, v11
Mv 48,16,32
Rb 96,17,0
v_lshlrev_b32 v11, 1, v0
Mv 80,16,36
v_and_b32 v11, 32, v11
v_xor_b32 v10, v10, v11
buffer_load_dword v4, v7, s[20:23], s52 offen offset:0 sc0 sc1
Mv 112,16,40
Rb 100,17,2048
s_and_b32 s28, s33, 1
Mv 144,16,44
s_lshl_b32 s28, s28, 6
v_xor_b32 v8, s28, v10
Mv 176,16,48
Rb 104,17,8192
v_xor_b32 v9, 64, v8
Mv 208,16,52
v_add_u32 v9, 0xfc00, v9
v_add_u32 v10, 0x1f800, v8
Mv 240,16,56
Rb 108,17,10240
v_add_u32 v11, 0x1f800, v9
Ma 240,16,60
Ma 244,20,60
Rb 112,17,16384
Mv 244,20,56
Mv 212,20,52
Rb 116,17,18432
Mv 180,20,48
Mv 148,20,44
Rb 120,17,24576
Mv 116,20,40
Mv 84,20,36
Rb 124,17,26624
)" R"(
Mv 52,20,32
Mv 56,24,32
Mv 88,24,36
Mv 120,24,40
Mv 152,24,44
Mv 184,24,48
Mv 216,24,52
Mv 248,24,56
Ma 248,24,60
Ma 252,28,60
Mv 252,28,56
Mv 220,28,52
Mv 188,28,48
Mv 156,28,44
Mv 124,28,40
Mv 92,28,36
Mv 60,28,32
s_waitcnt vmcnt(5) lgkmcnt(0)
s_barrier
Mv 32,64,96
Rt 0,18,32768
Mv 64,64,100
s_add_u32 s92, s42, 0x0
Mv 96,64,104
Rt 2,18,34816
s_add_u32 m0, s41, 131072
Mv 128,64,108
buffer_load_dwordx4 v8, s[16:19], s92 offen offset:0 lds
Mv 160,64,112
Rt 4,19,32768
Mv 192,64,116
Mv 224,64,120
Rt 6,19,34816
Ma 224,64,124
buffer_load_dwordx4 v9, s[16:19], s92 offen offset:1024 lds
Ma 228,68,124
Rt 8,20,32768
Mv 228,68,120
Mv 196,68,116
Rt 10,20,34816
Mv 164,68,112
buffer_load_dwordx4 v10, s[16:19], s92 offen offset:2048 lds
Mv 132,68,108
Rt 12,21,32768
Mv 100,68,104
Mv 68,68,100
Rt 14,21,34816
Mv 36,68,96
buffer_load_dwordx4 v11, s[16:19], s92 offen offset:3072 lds
Mv 40,72,96
Rt 16,22,32768
Mv 72,72,100
s_add_u32 s92, s42, 0x40000
Mv 104,72,104
Rt 18,22,34816
s_add_u32 m0, s41, 135168
Mv 136,72,108
buffer_load_dwordx4 v8, s[16:19], s92 offen offset:0 lds
Mv 168,72,112
Rt 20,23,32768
Mv 200,72,116
Mv 232,72,120
Rt 22,23,34816
Ma 232,72,124
buffer_load_dwordx4 v9, s[16:19], s92 offen offset:1024 lds
)" R"(
Ma 236,76,124
Rt 24,24,32768
Mv 236,76,120
Mv 204,76,116
Rt 26,24,34816
Mv 172,76,112
buffer_load_dwordx4 v10, s[16:19], s92 offen offset:2048 lds
Mv 140,76,108
Rt 28,25,32768
Mv 108,76,104
s_add_u32 s92, s42, 0x1c0000
Mv 76,76,100
Rt 30,25,34816
s_add_u32 m0, s41, 0
Mv 44,76,96
buffer_load_dwordx4 v8, s[16:19], s92 offen offset:0 lds
Mv 48,80,96
Rb 32,16,32768
Mv 80,80,100
Mv 112,80,104
Rb 36,16,34816
Mv 144,80,108
buffer_load_dwordx4 v9, s[16:19], s92 offen offset:1024 lds
Mv 176,80,112
Rb 40,16,40960
Mv 208,80,116
Mv 240,80,120
Rb 44,16,43008
Ma 240,80,124
buffer_load_dwordx4 v10, s[16:19], s92 offen offset:2048 lds
Ma 244,84,124
Rb 48,16,49152
Mv 244,84,120
Mv 212,84,116
Rb 52,16,51200
Mv 180,84,112
buffer_load_dwordx4 v11, s[16:19], s92 offen offset:3072 lds
Mv 148,84,108
Rb 56,16,57344
Mv 116,84,104
s_add_u32 s92, s42, 0x200000
Mv 84,84,100
Rb 60,16,59392
s_add_u32 m0, s41, 4096
Mv 52,84,96
buffer_load_dwordx4 v8, s[16:19], s92 offen offset:0 lds
Mv 56,88,96
Mv 88,88,100
Mv 120,88,104
Mv 152,88,108
buffer_load_dwordx4 v9, s[16:19], s92 offen offset:1024 lds
Mv 184,88,112
Mv 216,88,116
Mv 248,88,120
Ma 248,88,124
buffer_load_dwordx4 v10, s[16:19], s92 offen offset:2048 lds
Ma 252,92,124
Mv 252,92,120
Mv 220,92,116
Mv 188,92,112
Mv 156,92,108
Mv 124,92,104
Mv 92,92,100
Mv 60,92,96
)" R"(
s_waitcnt vmcnt(14) lgkmcnt(4)
s_barrier
v_cmp_eq_u32_e32 vcc, s3, v4
s_andn2_b64 s[28:29], 1, vcc
s_cbranch_scc1 .Lzsp2
.Lzok2:
Mv 32,0,32
Rt 64,18,49152
Mv 64,0,36
Rt 66,18,51200
s_add_u32 s92, s42, 0x80
Mv 96,0,40
Rt 68,19,49152
s_add_u32 m0, s41, 65536
Mv 128,0,44
Rt 70,19,51200
buffer_load_dwordx4 v8, s[16:19], s92 offen offset:0 lds
Mv 132,4,44
Rt 72,20,49152
Mv 100,4,40
Rt 74,20,51200
Mv 68,4,36
Rt 76,21,49152
Mv 36,4,32
Rt 78,21,51200
buffer_load_dwordx4 v9, s[16:19], s92 offen offset:1024 lds
Mv 40,8,32
Rt 80,22,49152
Mv 72,8,36
Rt 82,22,51200
Mv 104,8,40
Rt 84,23,49152
Mv 136,8,44
Rt 86,23,51200
buffer_load_dwordx4 v10, s[16:19], s92 offen offset:2048 lds
Mv 140,12,44
Rt 88,24,49152
Mv 108,12,40
Rt 90,24,51200
Mv 76,12,36
Rt 92,25,49152
Mv 44,12,32
Rt 94,25,51200
buffer_load_dwordx4 v11, s[16:19], s92 offen offset:3072 lds
Mv 48,16,32
Rb 96,17,32768
Mv 80,16,36
Rb 100,17,34816
s_add_u32 s92, s42, 0x40080
Mv 112,16,40
Rb 104,17,40960
s_add_u32 m0, s41, 69632
Mv 144,16,44
Rb 108,17,43008
buffer_load_dwordx4 v8, s[16:19], s92 offen offset:0 lds
Mv 148,20,44
Rb 112,17,49152
Mv 116,20,40
Rb 116,17,51200
Mv 84,20,36
Rb 120,17,57344
Mv 52,20,32
Rb 124,17,59392
buffer_load_dwordx4 v9, s[16:19], s92 offen offset:1024 lds
Mv 56,24,32
)" R"(
Mv 88,24,36
Mv 120,24,40
Mv 152,24,44
buffer_load_dwordx4 v10, s[16:19], s92 offen offset:2048 lds
Mv 156,28,44
Mv 124,28,40
Mv 92,28,36
Mv 60,28,32
s_waitcnt lgkmcnt(0)
s_barrier
Mv 32,64,96
v_bfe_u32 v12, v0, 3, 2
v_lshlrev_b32 v12, 5, v12
Mv 36,68,96
v_and_b32 v4, 3, v0
v_lshl_or_b32 v12, v4, 3, v12
Mv 40,72,96
s_add_u32 m0, s36, 98304
s_add_u32 s44, s43, 768
Mv 44,76,96
buffer_load_dwordx4 v26, s[12:15], s44 offen offset:0 lds
Mv 48,80,96
v_bfe_u32 v4, v0, 2, 2
v_lshl_or_b32 v12, v4, 7, v12
Mv 52,84,96
v_lshrrev_b32 v4, 4, v0
v_lshl_or_b32 v12, v4, 10, v12
Mv 56,88,96
v_xor_b32 v13, 32, v12
v_xor_b32 v14, 64, v12
Mv 60,92,96
buffer_load_dwordx4 v27, s[12:15], s44 offen offset:1024 lds
Mv 64,64,100
v_xor_b32 v15, 96, v12
v_add_u32 v20, 0x20000, v12
Mv 68,68,100
v_add_u32 v21, 0x20000, v13
v_add_u32 v22, 0x20000, v14
Mv 72,72,100
v_add_u32 v23, 0x20000, v15
v_add_u32 v16, 0x10000, v12
Mv 76,76,100
s_add_u32 s44, s43, 13056
buffer_load_dwordx4 v26, s[12:15], s44 offen offset:2048 lds
Mv 80,80,100
v_add_u32 v17, 0x10000, v13
v_add_u32 v18, 0x10000, v14
Mv 84,84,100
v_add_u32 v19, 0x10000, v15
Cv 32,32,33
Mv 88,88,100
Cv 33,34,35
Cv 34,36,37
Mv 92,92,100
buffer_load_dwordx4 v27, s[12:15], s44 offen offset:3072 lds
Mv 96,64,104
Cv 35,38,39
Cv 40,40,41
Mv 100,68,104
Cv 41,42,43
Cv 42,44,45
Mv 104,72,104
s_add_u32 m0, s41, 32768
s_add_u32 s92, s42, 0x1c0080
Mv 108,76,104
)" R"(
buffer_load_dwordx4 v8, s[16:19], s92 offen offset:0 lds
Mv 112,80,104
Cv 43,46,47
Cv 48,48,49
Mv 116,84,104
Cv 49,50,51
Cv 50,52,53
Mv 120,88,104
Cv 51,54,55
Cv 56,56,57
Mv 124,92,104
buffer_store_dwordx4 v[32:35], v2, s[20:23], s45 offen offset:0 sc0 sc1
Mv 128,64,108
Cv 57,58,59
Cv 58,60,61
.LzH28:
s_cbranch_execz .LzH38
Mv 132,68,108
Cv 59,62,63
Cv 64,64,65
Mv 136,72,108
Cv 65,66,67
Cv 66,68,69
Mv 140,76,108
buffer_load_dwordx4 v9, s[16:19], s92 offen offset:1024 lds
Mv 144,80,108
Cv 67,70,71
Cv 72,72,73
Mv 148,84,108
Cv 73,74,75
Cv 74,76,77
Mv 152,88,108
Cv 75,78,79
Cv 80,80,81
Mv 156,92,108
buffer_store_dwordx4 v[40:43], v2, s[20:23], s45 offen offset:1024 sc0 sc1
Mv 160,0,48
Cv 81,82,83
Cv 82,84,85
Mv 160,64,112
Cv 83,86,87
Cv 88,88,89
Mv 164,4,48
Cv 89,90,91
Cv 90,92,93
Mv 164,68,112
buffer_load_dwordx4 v10, s[16:19], s92 offen offset:2048 lds
Mv 168,8,48
Cv 91,94,95
Cv 96,96,97
Mv 168,72,112
Cv 97,98,99
Cv 98,100,101
Mv 172,12,48
Cv 99,102,103
Cv 104,104,105
Mv 172,76,112
buffer_store_dwordx4 v[48:51], v2, s[20:23], s45 offen offset:2048 sc0 sc1
Mv 176,16,48
Cv 105,106,107
Cv 106,108,109
Mv 176,80,112
Cv 107,110,111
Cv 112,112,113
)" R"(
Mv 180,20,48
Cv 113,114,115
Cv 114,116,117
Mv 180,84,112
buffer_load_dwordx4 v11, s[16:19], s92 offen offset:3072 lds
Mv 184,24,48
Cv 115,118,119
Cv 120,120,121
Mv 184,88,112
Cv 121,122,123
Cv 122,124,125
Mv 188,28,48
Cv 123,126,127
Cv 128,128,129
Mv 188,92,112
buffer_store_dwordx4 v[56:59], v2, s[20:23], s45 offen offset:3072 sc0 sc1
Mv 192,0,52
Cv 129,130,131
Cv 130,132,133
Mv 192,64,116
Cv 131,134,135
Cv 136,136,137
Mv 196,4,52
s_add_u32 m0, s41, 36864
s_add_u32 s92, s42, 0x200080
Mv 196,68,116
buffer_load_dwordx4 v8, s[16:19], s92 offen offset:0 lds
Mv 200,8,52
Cv 137,138,139
Cv 138,140,141
Mv 200,72,116
Cv 139,142,143
Cv 144,144,145
Mv 204,12,52
Cv 145,146,147
Cv 146,148,149
Mv 204,76,116
buffer_store_dwordx4 v[64:67], v3, s[20:23], s45 offen offset:0 sc0 sc1
Mv 208,16,52
Cv 147,150,151
Cv 152,152,153
Mv 208,80,116
Cv 153,154,155
Cv 154,156,157
Mv 212,20,52
Cv 155,158,159
Mv 212,84,116
buffer_load_dwordx4 v9, s[16:19], s92 offen offset:1024 lds
Mv 216,24,52
Mv 216,88,116
Mv 220,28,52
Mv 220,92,116
buffer_store_dwordx4 v[72:75], v3, s[20:23], s45 offen offset:1024 sc0 sc1
Mv 224,0,56
Ma 224,0,60
Ma 228,4,60
Mv 228,4,56
buffer_load_dwordx4 v10, s[16:19], s92 offen offset:2048 lds
Mv 232,8,56
Ma 232,8,60
Ma 236,12,60
Mv 236,12,56
s_waitcnt vmcnt(24)
s_barrier
Mv 240,16,56
)" R"(
buffer_store_dwordx4 v[80:83], v3, s[20:23], s45 offen offset:2048 sc0 sc1
Ma 240,16,60
Ma 244,20,60
Mv 244,20,56
Mv 248,24,56
Ma 248,24,60
Ma 252,28,60
Mv 252,28,56
buffer_store_dwordx4 v[88:91], v3, s[20:23], s45 offen offset:3072 sc0 sc1
Mv 224,64,120
Rt 16,20,0
Mv 228,68,120
Rt 18,20,512
Mv 232,72,120
Rt 20,21,0
Mv 236,76,120
Rt 22,21,512
Mv 240,80,120
Rt 24,22,0
Mv 244,84,120
Rt 26,22,512
Mv 248,88,120
Rt 28,23,0
Mv 252,92,120
Rt 30,23,512
Ma 224,64,124
Rt 32,20,4096
Ma 228,68,124
Rt 34,20,4608
Ma 232,72,124
Rt 36,21,4096
Ma 236,76,124
Rt 38,21,4608
Ma 240,80,124
Rt 40,22,4096
Ma 244,84,124
Rt 42,22,4608
Ma 248,88,124
Rt 44,23,4096
Ma 252,92,124
Rt 46,23,4608
s_waitcnt vmcnt(15) lgkmcnt(8)
s_branch .LzQA
.LzH29:
s_cbranch_execz .LzH35
.LzAV:
Mz 32,32,0
Rt 64,18,16384
Mz 36,36,0
s_lshl1_add_u32 s40, s34, s91
s_lshl_b32 s40, s40, 12
Mz 96,40,0
Rt 66,18,18432
s_and_b32 s29, s38, 0xfffe0000
Mz 100,44,0
s_add_u32 s39, s39, 0x40000
s_add_u32 s40, s40, s29
Mz 160,48,0
Rt 68,19,16384
s_add_u32 m0, s37, 0
Mz 164,52,0
buffer_load_dwordx4 v12, s[8:11], s39 offen offset:0 lds
Mz 224,56,0
Rt 70,19,18432
)" R"(
v_min_u32 v3, 31, v0
Mz 228,60,0
v_lshlrev_b32 v3, 7, v3
Mz 236,60,4
Rt 72,20,16384
Mz 232,56,4
Mz 172,52,4
Rt 74,20,18432
Mz 168,48,4
Mz 108,44,4
Rt 76,21,16384
Mz 104,40,4
buffer_load_dwordx4 v13, s[8:11], s39 offen offset:1024 lds
Mz 44,36,4
Rt 78,21,18432
Mz 40,32,4
Mz 48,32,8
Rt 80,22,16384
Mz 52,36,8
Mz 112,40,8
Rt 82,22,18432
Mz 116,44,8
Mz 176,48,8
Rt 84,23,16384
Mz 180,52,8
buffer_load_dwordx4 v14, s[8:11], s39 offen offset:2048 lds
Mz 240,56,8
Rt 86,23,18432
Mz 244,60,8
Mz 252,60,12
Rt 88,24,16384
Mz 248,56,12
Mz 188,52,12
Rt 90,24,18432
Mz 184,48,12
Mz 124,44,12
Rt 92,25,16384
Mz 120,40,12
buffer_load_dwordx4 v15, s[8:11], s39 offen offset:3072 lds
Mz 60,36,12
Rt 94,25,18432
Mz 56,32,12
Mz 64,32,16
Rb 96,17,0
Mz 68,36,16
buffer_load_dword v6, v3, s[4:7], s40 offen
Mz 128,40,16
Rb 100,17,2048
Mz 132,44,16
Mz 192,48,16
Rb 104,17,8192
s_mov_b32 s28, s35
Mz 196,52,16
s_lshl2_add_u32 s28, s28, s33
s_mul_i32 s43, s28, 28672
v_mfma_f32_16x16x32_bf16 a[224:227], a[56:59], a[16:19], 0
Rb 108,17,10240
v_and_b32 v26, 0x70, v1
v_mfma_f32_16x16x32_bf16 a[228:231], a[60:63], a[16:19], 0
v_and_b32 v4, 48, v0
v_xor_b32 v26, v26, v4
v_mfma_f32_16x16x32_bf16 a[236:239], a[60:63], a[20:23], 0
Rb 112,17,16384
v_lshrrev_b32 v4, 3, v0
)" R"(
v_mfma_f32_16x16x32_bf16 a[232:235], a[56:59], a[20:23], 0
v_mul_u32_u24 v4, 896, v4
v_add_u32 v26, v26, v4
Mz 204,52,20
Rb 116,17,18432
v_xor_b32 v27, 64, v26
Mz 200,48,20
v_add_u32 v27, 6144, v27
v_bfe_u32 v28, v0, 1, 3
Mz 140,44,20
Rb 120,17,24576
v_lshrrev_b32 v4, 4, v0
Mz 136,40,20
v_xor_b32 v28, v28, v4
v_lshlrev_b32 v28, 4, v28
Mz 76,36,20
Rb 124,17,26624
v_and_b32 v4, 15, v0
Mz 72,32,20
v_lshl_or_b32 v28, v4, 7, v28
s_add_u32 s28, s36, 0x18000
Mz 80,32,24
v_add_u32 v28, s28, v28
v_xor_b32 v29, 64, v28
Mz 84,36,24
Mz 144,40,24
Mz 148,44,24
Mz 208,48,24
Mz 212,52,24
v_mfma_f32_16x16x32_bf16 a[240:243], a[56:59], a[24:27], 0
v_mfma_f32_16x16x32_bf16 a[244:247], a[60:63], a[24:27], 0
v_mfma_f32_16x16x32_bf16 a[252:255], a[60:63], a[28:31], 0
v_mfma_f32_16x16x32_bf16 a[248:251], a[56:59], a[28:31], 0
Mz 220,52,28
Mz 216,48,28
Mz 156,44,28
Mz 152,40,28
Mz 92,36,28
Mz 88,32,28
s_call_b64 s[30:31], .LzHOP
s_waitcnt vmcnt(11) lgkmcnt(0)
s_barrier
Mv 32,96,64
Rt 0,18,32768
s_bfe_u32 s88, s2, 0x20005
Mv 36,100,64
s_add_u32 s38, s38, 0xffff0080
s_and_b32 s28, s2, 15
Mv 96,104,64
Rt 2,18,34816
s_add_u32 m0, s36, 0
Mv 100,108,64
buffer_load_dwordx4 v8, s[4:7], s38 offen offset:0 lds
Mv 160,112,64
Rt 4,19,32768
s_lshl4_add_u32 s88, s88, s28
Mv 164,116,64
s_lshl2_add_u32 s88, s88, s33
s_mul_i32 s88, s88, 4096
Mv 224,120,64
Rt 6,19,34816
s_add_u32 s88, s88, 0x100000
Mv 228,124,64
buffer_load_dwordx4 v9, s[4:7], s38 offen offset:1024 lds
Mv 236,124,68
)" R"(
Rt 8,20,32768
v_min_u32 v5, 31, v0
Mv 232,120,68
v_lshlrev_b32 v5, 7, v5
s_mov_b32 s90, 0x100000
Mv 172,116,68
Rt 10,20,34816
s_mov_b64 s[24:25], s[8:9]
Mv 168,112,68
buffer_load_dwordx4 v10, s[4:7], s38 offen offset:2048 lds
Mv 108,108,68
Rt 12,21,32768
s_mov_b64 s[26:27], s[10:11]
Mv 104,104,68
Mv 44,100,68
Rt 14,21,34816
Mv 40,96,68
buffer_load_dwordx4 v11, s[4:7], s38 offen offset:3072 lds
Mv 48,96,72
Rt 16,22,32768
Mv 52,100,72
s_add_u32 s38, s38, 0x10000
Mv 112,104,72
Rt 18,22,34816
s_add_u32 m0, s36, 4096
Mv 116,108,72
buffer_load_dwordx4 v8, s[4:7], s38 offen offset:0 lds
Mv 176,112,72
Rt 20,23,32768
Mv 180,116,72
Mv 240,120,72
Rt 22,23,34816
Mv 244,124,72
buffer_load_dwordx4 v9, s[4:7], s38 offen offset:1024 lds
Mv 252,124,76
Rt 24,24,32768
Mv 248,120,76
Mv 188,116,76
Rt 26,24,34816
Mv 184,112,76
buffer_load_dwordx4 v10, s[4:7], s38 offen offset:2048 lds
Mv 124,108,76
Rt 28,25,32768
Mv 120,104,76
Mv 60,100,76
Rt 30,25,34816
Mv 56,96,76
buffer_load_dwordx4 v11, s[4:7], s38 offen offset:3072 lds
Mv 64,96,80
Rb 32,16,32768
Mv 68,100,80
Mv 128,104,80
Rb 36,16,34816
Mv 132,108,80
s_add_u32 s39, s39, 0x40000
Mv 192,112,80
Rb 40,16,40960
s_add_u32 m0, s37, 16384
Mv 196,116,80
buffer_load_dwordx4 v12, s[8:11], s39 offen offset:0 lds
Ma 224,120,80
Rb 44,16,43008
Ma 228,124,80
Ma 236,124,84
)" R"(
Rb 48,16,49152
Ma 232,120,84
Mv 204,116,84
Rb 52,16,51200
Mv 200,112,84
buffer_load_dwordx4 v13, s[8:11], s39 offen offset:1024 lds
Mv 140,108,84
Rb 56,16,57344
Mv 136,104,84
Mv 76,100,84
Rb 60,16,59392
Mv 72,96,84
Mv 80,96,88
Mv 84,100,88
buffer_load_dwordx4 v14, s[8:11], s39 offen offset:2048 lds
Mv 144,104,88
Mv 148,108,88
Mv 208,112,88
Mv 212,116,88
Ma 240,120,88
Ma 244,124,88
buffer_load_dwordx4 v15, s[8:11], s39 offen offset:3072 lds
Ma 252,124,92
Ma 248,120,92
s_add_u32 s88, s88, s90
Mv 220,116,92
Mv 216,112,92
buffer_load_dword v6, v5, s[24:27], s88 offen
Mv 156,108,92
Mv 152,104,92
Mv 92,100,92
Mv 88,96,92
s_waitcnt vmcnt(20) lgkmcnt(0)
s_barrier
Mv 32,32,0
Rt 64,18,49152
s_lshl_b32 s29, s91, 22
Mv 36,36,0
s_add_u32 s28, s89, s29
s_bfe_u32 s29, s63, 0x80000
Mv 96,40,0
Rt 66,18,51200
s_lshl_b32 s29, s29, 16
Mv 100,44,0
s_add_u32 s39, s39, 0x40000
s_add_u32 s45, s28, s29
Mv 160,48,0
Rt 68,19,49152
s_add_u32 m0, s37, 32768
Mv 164,52,0
buffer_load_dwordx4 v12, s[8:11], s39 offen offset:0 lds
Mv 224,56,0
Rt 70,19,51200
s_bfe_u32 s29, s63, 0x80008
Mv 228,60,0
s_lshl_b32 s29, s29, 16
s_add_u32 s46, s28, s29
Mv 236,60,4
Rt 72,20,49152
s_bfe_u32 s29, s63, 0x80010
Mv 232,56,4
s_lshl_b32 s29, s29, 16
s_add_u32 s47, s28, s29
Mv 172,52,4
Rt 74,20,51200
)" R"(
s_bfe_u32 s29, s63, 0x80018
Mv 168,48,4
s_lshl_b32 s29, s29, 16
s_add_u32 s48, s28, s29
Mv 108,44,4
Rt 76,21,49152
s_add_u32 s50, s89, 0xd40000
Mv 104,40,4
buffer_load_dwordx4 v13, s[8:11], s39 offen offset:1024 lds
Mv 44,36,4
Rt 78,21,51200
s_lshl2_add_u32 s28, s91, s93
Mv 40,32,4
s_lshl_b32 s28, s28, 7
s_add_u32 s49, s28, s50
Mv 48,32,8
Rt 80,22,49152
s_lshl_b32 s52, s35, 7
Mv 52,36,8
s_add_u32 s52, s52, s50
s_add_u32 s52, s52, 0xd000
Mv 112,40,8
Rt 82,22,51200
s_lshl_b32 s51, s35, 17
Mv 116,44,8
s_add_u32 s51, s51, s89
s_add_u32 s51, s51, 0x800000
Mv 176,48,8
Rt 84,23,49152
Mv 180,52,8
buffer_load_dwordx4 v14, s[8:11], s39 offen offset:2048 lds
Mv 240,56,8
Rt 86,23,51200
Mv 244,60,8
s_cmp_lg_u32 s33, 0
s_cmov_b32 s49, 0x40000000
Mv 252,60,12
Rt 88,24,49152
s_mov_b32 s53, 0x40000
Mv 248,56,12
s_mov_b32 s54, 0x3db8aa3b
s_mul_i32 s41, s33, 7168
Mv 188,52,12
Rt 90,24,51200
s_mul_i32 s42, s33, 0x70000
Mv 184,48,12
s_lshl_b32 s28, s34, 9
s_add_u32 s42, s42, s28
Mv 124,44,12
Rt 92,25,49152
s_mov_b32 s62, 0x4e800000
Mv 120,40,12
buffer_load_dwordx4 v15, s[8:11], s39 offen offset:3072 lds
Mv 60,36,12
Rt 94,25,51200
s_add_u32 s67, s89, 0xc00000
Mv 56,32,12
s_lshl3_add_u32 s28, s34, s35
s_add_u32 s64, s51, 0x100000
Mv 64,32,16
Rb 96,17,32768
s_lshl2_add_u32 s28, s28, s33
s_add_u32 m0, s36, 131072
s_add_u32 s44, s43, 0
)" R"(
Mv 68,36,16
s_lshl2_add_u32 s65, s35, s33
s_lshl_b32 s65, s65, 7
buffer_load_dwordx4 v26, s[12:15], s44 offen offset:0 lds
Mv 128,40,16
Rb 100,17,34816
s_add_u32 s65, s65, s50
Mv 132,44,16
s_add_u32 s65, s65, 0xc000
s_lshl_b32 s66, s28, 10
Mv 192,48,16
Rb 104,17,40960
s_add_u32 s66, s66, 0x1400000
Mv 196,52,16
s_mov_b32 s28, s35
s_lshl2_add_u32 s28, s28, s33
buffer_load_dwordx4 v27, s[12:15], s44 offen offset:1024 lds
Ma 224,56,16
Rb 108,17,43008
s_and_b32 s29, s34, 1
Ma 228,60,16
s_lshl1_add_u32 s29, s28, s29
s_lshl_b32 s73, s29, 15
Ma 236,60,20
Rb 112,17,49152
s_and_b32 s29, s89, 0xfe000000
Ma 232,56,20
s_add_u32 s73, s73, s29
s_add_u32 s73, s73, 0x1000000
s_add_u32 s44, s43, 12288
buffer_load_dwordx4 v26, s[12:15], s44 offen offset:2048 lds
Mv 204,52,20
Rb 116,17,51200
s_lshl4_add_u32 s28, s28, s34
Mv 200,48,20
s_lshl_b32 s68, s28, 8
s_add_u32 s68, s68, 0x1510000
Mv 140,44,20
Rb 120,17,57344
s_add_u32 s55, s89, 0x120000
Mv 136,40,20
s_lshl_b32 s28, s35, 1
s_sub_u32 s57, 14, s28
buffer_load_dwordx4 v27, s[12:15], s44 offen offset:3072 lds
Mv 76,36,20
Rb 124,17,59392
s_mov_b32 s59, s89
Mv 72,32,20
s_mov_b32 s58, 2
s_add_u32 m0, s36, 135168
Mv 80,32,24
s_cmp_eq_u32 s35, 7
s_cmov_b32 s55, s89
s_add_u32 s44, s43, 128
.LzH30:
s_cbranch_execz .LzH36
Mv 84,36,24
s_cmp_eq_u32 s35, 7
s_cmov_b32 s57, 16
buffer_load_dwordx4 v26, s[12:15], s44 offen offset:0 lds
Mv 144,40,24
s_cmp_eq_u32 s35, 7
s_cmov_b32 s59, s67
Mv 148,44,24
)" R"(
s_add_u32 s56, s55, 0x400000
Mv 208,48,24
Mv 212,52,24
buffer_load_dwordx4 v27, s[12:15], s44 offen offset:1024 lds
Ma 240,56,24
Ma 244,60,24
Ma 252,60,28
Ma 248,56,28
s_add_u32 s44, s43, 12416
buffer_load_dwordx4 v26, s[12:15], s44 offen offset:2048 lds
Mv 220,52,28
Mv 216,48,28
Mv 156,44,28
Mv 152,40,28
buffer_load_dwordx4 v27, s[12:15], s44 offen offset:3072 lds
Mv 92,36,28
Mv 88,32,28
s_waitcnt vmcnt(17) lgkmcnt(0)
s_barrier
Mv 32,96,64
Rt 0,18,0
Mv 36,100,64
s_add_u32 s38, s38, 0xffff0080
Mv 96,104,64
Rt 2,18,2048
s_add_u32 m0, s36, 32768
Mv 100,108,64
buffer_load_dwordx4 v8, s[4:7], s38 offen offset:0 lds
Mv 160,112,64
Rt 4,19,0
Mv 164,116,64
Mv 224,120,64
Rt 6,19,2048
Mv 228,124,64
buffer_load_dwordx4 v9, s[4:7], s38 offen offset:1024 lds
Mv 236,124,68
Rt 8,20,0
s_lshl2_add_u32 s28, s2, s33
Mv 232,120,68
s_lshl_b32 s28, s28, 11
s_add_u32 s28, s28, 0x1500000
Mv 172,116,68
Rt 10,20,2048
v_min_u32 v3, 15, v0
Mv 168,112,68
buffer_load_dwordx4 v10, s[4:7], s38 offen offset:2048 lds
Mv 108,108,68
Rt 12,21,0
v_lshlrev_b32 v3, 7, v3
Mv 104,104,68
Mv 44,100,68
Rt 14,21,2048
Mv 40,96,68
buffer_load_dwordx4 v11, s[4:7], s38 offen offset:3072 lds
Mv 48,96,72
Rt 16,22,0
Mv 52,100,72
s_add_u32 s38, s38, 0x10000
Mv 112,104,72
Rt 18,22,2048
s_add_u32 m0, s36, 36864
Mv 116,108,72
buffer_load_dwordx4 v8, s[4:7], s38 offen offset:0 lds
Mv 176,112,72
Rt 20,23,0
)" R"(
Mv 180,116,72
Mv 240,120,72
Rt 22,23,2048
Mv 244,124,72
buffer_load_dwordx4 v9, s[4:7], s38 offen offset:1024 lds
Mv 252,124,76
Rt 24,24,0
Mv 248,120,76
Mv 188,116,76
Rt 26,24,2048
Mv 184,112,76
buffer_load_dwordx4 v10, s[4:7], s38 offen offset:2048 lds
Mv 124,108,76
Rt 28,25,0
Mv 120,104,76
Mv 60,100,76
Rt 30,25,2048
Mv 56,96,76
buffer_load_dwordx4 v11, s[4:7], s38 offen offset:3072 lds
Mv 64,96,80
Rb 32,16,0
Mv 68,100,80
Mv 128,104,80
Rb 36,16,2048
Mv 132,108,80
s_add_u32 s39, s39, 0x40000
Mv 192,112,80
Rb 40,16,8192
s_add_u32 m0, s37, 49152
Mv 196,116,80
buffer_load_dwordx4 v12, s[8:11], s39 offen offset:0 lds
Ma 224,120,80
Rb 44,16,10240
Ma 228,124,80
Ma 236,124,84
Rb 48,16,16384
Ma 232,120,84
Mv 204,116,84
Rb 52,16,18432
Mv 200,112,84
buffer_load_dwordx4 v13, s[8:11], s39 offen offset:1024 lds
Mv 140,108,84
Rb 56,16,24576
Mv 136,104,84
Mv 76,100,84
Rb 60,16,26624
Mv 72,96,84
Mv 80,96,88
Mv 84,100,88
buffer_load_dwordx4 v14, s[8:11], s39 offen offset:2048 lds
Mv 144,104,88
Mv 148,108,88
Mv 208,112,88
Mv 212,116,88
Ma 240,120,88
Ma 244,124,88
buffer_load_dwordx4 v15, s[8:11], s39 offen offset:3072 lds
Ma 252,124,92
Ma 248,120,92
s_add_u32 s88, s88, s90
s_mov_b32 s40, 5
Mv 220,116,92
Mv 216,112,92
buffer_load_dword v6, v5, s[24:27], s88 offen
)" R"(
Mv 156,108,92
Mv 152,104,92
buffer_load_dword v6, v3, s[20:23], s28 offen sc0 sc1
Mv 92,100,92
Mv 88,96,92
s_waitcnt vmcnt(27) lgkmcnt(0)
s_barrier
.LzPAV:
Mv 32,32,0
Rt 64,18,16384
Mv 36,36,0
Mv 96,40,0
Rt 66,18,18432
Mv 100,44,0
s_add_u32 s39, s39, 0x40000
Mv 160,48,0
Rt 68,19,16384
s_add_u32 m0, s37, 0
Mv 164,52,0
buffer_load_dwordx4 v12, s[8:11], s39 offen offset:0 lds
Mv 224,56,0
Rt 70,19,18432
Mv 228,60,0
Mv 236,60,4
Rt 72,20,16384
Mv 232,56,4
Mv 172,52,4
Rt 74,20,18432
Mv 168,48,4
Mv 108,44,4
Rt 76,21,16384
Mv 104,40,4
buffer_load_dwordx4 v13, s[8:11], s39 offen offset:1024 lds
Mv 44,36,4
Rt 78,21,18432
Mv 40,32,4
Mv 48,32,8
Rt 80,22,16384
Mv 52,36,8
Mv 112,40,8
Rt 82,22,18432
Mv 116,44,8
Mv 176,48,8
Rt 84,23,16384
Mv 180,52,8
buffer_load_dwordx4 v14, s[8:11], s39 offen offset:2048 lds
Mv 240,56,8
Rt 86,23,18432
Mv 244,60,8
Mv 252,60,12
Rt 88,24,16384
Mv 248,56,12
Mv 188,52,12
Rt 90,24,18432
Mv 184,48,12
Mv 124,44,12
Rt 92,25,16384
Mv 120,40,12
buffer_load_dwordx4 v15, s[8:11], s39 offen offset:3072 lds
Mv 60,36,12
Rt 94,25,18432
Mv 56,32,12
Mv 64,32,16
Rb 96,17,0
Mv 68,36,16
)" R"(
Mv 128,40,16
Rb 100,17,2048
Mv 132,44,16
Mv 192,48,16
Rb 104,17,8192
Mv 196,52,16
Ma 224,56,16
Rb 108,17,10240
Ma 228,60,16
Ma 236,60,20
Rb 112,17,16384
Ma 232,56,20
Mv 204,52,20
Rb 116,17,18432
Mv 200,48,20
Mv 140,44,20
Rb 120,17,24576
Mv 136,40,20
Mv 76,36,20
Rb 124,17,26624
Mv 72,32,20
Mv 80,32,24
Mv 84,36,24
Mv 144,40,24
Mv 148,44,24
Mv 208,48,24
Mv 212,52,24
Ma 240,56,24
Ma 244,60,24
Ma 252,60,28
Ma 248,56,28
Mv 220,52,28
Mv 216,48,28
Mv 156,44,28
Mv 152,40,28
Mv 92,36,28
Mv 88,32,28
s_waitcnt vmcnt(9) lgkmcnt(0)
s_barrier
Mv 32,96,64
Rt 0,18,32768
Mv 36,100,64
s_add_u32 s38, s38, 0xffff0080
Mv 96,104,64
Rt 2,18,34816
s_add_u32 m0, s36, 0
Mv 100,108,64
buffer_load_dwordx4 v8, s[4:7], s38 offen offset:0 lds
Mv 160,112,64
Rt 4,19,32768
Mv 164,116,64
Mv 224,120,64
Rt 6,19,34816
Mv 228,124,64
buffer_load_dwordx4 v9, s[4:7], s38 offen offset:1024 lds
Mv 236,124,68
Rt 8,20,32768
Mv 232,120,68
Mv 172,116,68
Rt 10,20,34816
Mv 168,112,68
buffer_load_dwordx4 v10, s[4:7], s38 offen offset:2048 lds
Mv 108,108,68
Rt 12,21,32768
Mv 104,104,68
)" R"(
Mv 44,100,68
Rt 14,21,34816
Mv 40,96,68
buffer_load_dwordx4 v11, s[4:7], s38 offen offset:3072 lds
Mv 48,96,72
Rt 16,22,32768
Mv 52,100,72
s_add_u32 s38, s38, 0x10000
Mv 112,104,72
Rt 18,22,34816
s_add_u32 m0, s36, 4096
Mv 116,108,72
buffer_load_dwordx4 v8, s[4:7], s38 offen offset:0 lds
Mv 176,112,72
Rt 20,23,32768
Mv 180,116,72
Mv 240,120,72
Rt 22,23,34816
Mv 244,124,72
buffer_load_dwordx4 v9, s[4:7], s38 offen offset:1024 lds
Mv 252,124,76
Rt 24,24,32768
Mv 248,120,76
Mv 188,116,76
Rt 26,24,34816
Mv 184,112,76
buffer_load_dwordx4 v10, s[4:7], s38 offen offset:2048 lds
Mv 124,108,76
Rt 28,25,32768
Mv 120,104,76
Mv 60,100,76
Rt 30,25,34816
Mv 56,96,76
buffer_load_dwordx4 v11, s[4:7], s38 offen offset:3072 lds
Mv 64,96,80
Rb 32,16,32768
Mv 68,100,80
Mv 128,104,80
Rb 36,16,34816
Mv 132,108,80
s_add_u32 s39, s39, 0x40000
Mv 192,112,80
Rb 40,16,40960
s_add_u32 m0, s37, 16384
Mv 196,116,80
buffer_load_dwordx4 v12, s[8:11], s39 offen offset:0 lds
Ma 224,120,80
Rb 44,16,43008
Ma 228,124,80
Ma 236,124,84
Rb 48,16,49152
Ma 232,120,84
Mv 204,116,84
Rb 52,16,51200
Mv 200,112,84
buffer_load_dwordx4 v13, s[8:11], s39 offen offset:1024 lds
Mv 140,108,84
Rb 56,16,57344
Mv 136,104,84
Mv 76,100,84
Rb 60,16,59392
Mv 72,96,84
Mv 80,96,88
Mv 84,100,88
)" R"(
buffer_load_dwordx4 v14, s[8:11], s39 offen offset:2048 lds
Mv 144,104,88
Mv 148,108,88
Mv 208,112,88
Mv 212,116,88
Ma 240,120,88
Ma 244,124,88
buffer_load_dwordx4 v15, s[8:11], s39 offen offset:3072 lds
Ma 252,124,92
Ma 248,120,92
s_add_u32 s88, s88, s90
Mv 220,116,92
Mv 216,112,92
buffer_load_dword v6, v5, s[24:27], s88 offen
Mv 156,108,92
Mv 152,104,92
Mv 92,100,92
Mv 88,96,92
s_waitcnt vmcnt(18) lgkmcnt(0)
s_barrier
Mv 32,32,0
Rt 64,18,49152
Mv 36,36,0
Mv 96,40,0
Rt 66,18,51200
Mv 100,44,0
s_add_u32 s39, s39, 0x40000
Mv 160,48,0
Rt 68,19,49152
s_add_u32 m0, s37, 32768
Mv 164,52,0
buffer_load_dwordx4 v12, s[8:11], s39 offen offset:0 lds
Mv 224,56,0
Rt 70,19,51200
Mv 228,60,0
Mv 236,60,4
Rt 72,20,49152
Mv 232,56,4
Mv 172,52,4
Rt 74,20,51200
Mv 168,48,4
Mv 108,44,4
Rt 76,21,49152
Mv 104,40,4
buffer_load_dwordx4 v13, s[8:11], s39 offen offset:1024 lds
Mv 44,36,4
Rt 78,21,51200
Mv 40,32,4
Mv 48,32,8
Rt 80,22,49152
Mv 52,36,8
Mv 112,40,8
Rt 82,22,51200
Mv 116,44,8
Mv 176,48,8
Rt 84,23,49152
Mv 180,52,8
buffer_load_dwordx4 v14, s[8:11], s39 offen offset:2048 lds
Mv 240,56,8
Rt 86,23,51200
Mv 244,60,8
Mv 252,60,12
Rt 88,24,49152
Mv 248,56,12
Mv 188,52,12
)" R"(
Rt 90,24,51200
Mv 184,48,12
Mv 124,44,12
Rt 92,25,49152
Mv 120,40,12
buffer_load_dwordx4 v15, s[8:11], s39 offen offset:3072 lds
Mv 60,36,12
Rt 94,25,51200
Mv 56,32,12
Mv 64,32,16
Rb 96,17,32768
Mv 68,36,16
Mv 128,40,16
Rb 100,17,34816
Mv 132,44,16
Mv 192,48,16
Rb 104,17,40960
Mv 196,52,16
Ma 224,56,16
Rb 108,17,43008
Ma 228,60,16
Ma 236,60,20
Rb 112,17,49152
Ma 232,56,20
Mv 204,52,20
Rb 116,17,51200
Mv 200,48,20
Mv 140,44,20
Rb 120,17,57344
Mv 136,40,20
Mv 76,36,20
Rb 124,17,59392
Mv 72,32,20
Mv 80,32,24
Mv 84,36,24
Mv 144,40,24
Mv 148,44,24
Mv 208,48,24
Mv 212,52,24
Ma 240,56,24
Ma 244,60,24
Ma 252,60,28
Ma 248,56,28
Mv 220,52,28
Mv 216,48,28
Mv 156,44,28
Mv 152,40,28
Mv 92,36,28
Mv 88,32,28
s_waitcnt vmcnt(9) lgkmcnt(0)
s_barrier
Mv 32,96,64
Rt 0,18,0
Mv 36,100,64
s_add_u32 s38, s38, 0xffff0080
Mv 96,104,64
Rt 2,18,2048
s_add_u32 m0, s36, 32768
Mv 100,108,64
buffer_load_dwordx4 v8, s[4:7], s38 offen offset:0 lds
Mv 160,112,64
Rt 4,19,0
Mv 164,116,64
Mv 224,120,64
Rt 6,19,2048
)" R"(
Mv 228,124,64
buffer_load_dwordx4 v9, s[4:7], s38 offen offset:1024 lds
Mv 236,124,68
Rt 8,20,0
Mv 232,120,68
Mv 172,116,68
Rt 10,20,2048
Mv 168,112,68
buffer_load_dwordx4 v10, s[4:7], s38 offen offset:2048 lds
Mv 108,108,68
Rt 12,21,0
Mv 104,104,68
Mv 44,100,68
Rt 14,21,2048
Mv 40,96,68
buffer_load_dwordx4 v11, s[4:7], s38 offen offset:3072 lds
Mv 48,96,72
Rt 16,22,0
Mv 52,100,72
s_add_u32 s38, s38, 0x10000
Mv 112,104,72
Rt 18,22,2048
s_add_u32 m0, s36, 36864
Mv 116,108,72
buffer_load_dwordx4 v8, s[4:7], s38 offen offset:0 lds
Mv 176,112,72
Rt 20,23,0
Mv 180,116,72
Mv 240,120,72
Rt 22,23,2048
Mv 244,124,72
buffer_load_dwordx4 v9, s[4:7], s38 offen offset:1024 lds
Mv 252,124,76
Rt 24,24,0
Mv 248,120,76
Mv 188,116,76
Rt 26,24,2048
Mv 184,112,76
buffer_load_dwordx4 v10, s[4:7], s38 offen offset:2048 lds
Mv 124,108,76
Rt 28,25,0
Mv 120,104,76
Mv 60,100,76
Rt 30,25,2048
Mv 56,96,76
buffer_load_dwordx4 v11, s[4:7], s38 offen offset:3072 lds
Mv 64,96,80
Rb 32,16,0
Mv 68,100,80
Mv 128,104,80
Rb 36,16,2048
Mv 132,108,80
s_add_u32 s39, s39, 0x40000
Mv 192,112,80
Rb 40,16,8192
s_add_u32 m0, s37, 49152
Mv 196,116,80
buffer_load_dwordx4 v12, s[8:11], s39 offen offset:0 lds
Ma 224,120,80
Rb 44,16,10240
Ma 228,124,80
Ma 236,124,84
Rb 48,16,16384
.LzH31:
)" R"(
s_cbranch_execz .LzH37
Ma 232,120,84
Mv 204,116,84
Rb 52,16,18432
Mv 200,112,84
buffer_load_dwordx4 v13, s[8:11], s39 offen offset:1024 lds
Mv 140,108,84
Rb 56,16,24576
Mv 136,104,84
Mv 76,100,84
Rb 60,16,26624
Mv 72,96,84
Mv 80,96,88
Mv 84,100,88
buffer_load_dwordx4 v14, s[8:11], s39 offen offset:2048 lds
Mv 144,104,88
Mv 148,108,88
Mv 208,112,88
Mv 212,116,88
Ma 240,120,88
Ma 244,124,88
buffer_load_dwordx4 v15, s[8:11], s39 offen offset:3072 lds
Ma 252,124,92
Ma 248,120,92
s_add_u32 s88, s88, s90
Mv 220,116,92
s_sub_u32 s40, s40, 1
Mv 216,112,92
buffer_load_dword v6, v5, s[24:27], s88 offen
Mv 156,108,92
Mv 152,104,92
Mv 92,100,92
Mv 88,96,92
s_cmp_lg_u32 s40, 0
s_waitcnt vmcnt(18) lgkmcnt(0)
s_barrier
s_cbranch_scc1 .LzPAV
.LzPAVX:
Mv 32,32,0
Rt 64,18,16384
Mv 36,36,0
Rb 192,28,32768
Mv 96,40,0
Rt 66,18,18432
Mv 100,44,0
s_add_u32 s39, s39, 0x40000
Rb 196,28,34816
Mv 160,48,0
Rt 68,19,16384
s_add_u32 m0, s37, 0
Mv 164,52,0
buffer_load_dwordx4 v12, s[8:11], s39 offen offset:0 lds
Mv 224,56,0
Rt 70,19,18432
Mv 228,60,0
Rb 200,29,32768
Mv 236,60,4
Rt 72,20,16384
Mv 232,56,4
Rb 204,29,34816
Mv 172,52,4
Rt 74,20,18432
Mv 168,48,4
Rb 208,28,36864
Mv 108,44,4
)" R"(
Rt 76,21,16384
Mv 104,40,4
buffer_load_dwordx4 v13, s[8:11], s39 offen offset:1024 lds
Mv 44,36,4
Rt 78,21,18432
Mv 40,32,4
Rb 212,28,38912
Mv 48,32,8
Rt 80,22,16384
Mv 52,36,8
Rb 216,29,36864
Mv 112,40,8
Rt 82,22,18432
Mv 116,44,8
Rb 220,29,38912
Mv 176,48,8
Rt 84,23,16384
Mv 180,52,8
buffer_load_dwordx4 v14, s[8:11], s39 offen offset:2048 lds
Mv 240,56,8
Rt 86,23,18432
Mv 244,60,8
Mv 252,60,12
Rt 88,24,16384
Mv 248,56,12
Mv 188,52,12
Rt 90,24,18432
Mv 184,48,12
Mv 124,44,12
Rt 92,25,16384
Mv 120,40,12
buffer_load_dwordx4 v15, s[8:11], s39 offen offset:3072 lds
Mv 60,36,12
Rt 94,25,18432
Mv 56,32,12
s_add_u32 s44, s43, 256
s_waitcnt lgkmcnt(6)
Mv 64,32,16
Rb 96,17,0
s_add_u32 m0, s36, 131072
Mv 68,36,16
buffer_load_dwordx4 v26, s[12:15], s44 offen offset:0 lds
Mv 128,40,16
Rb 100,17,2048
Mv 132,44,16
Mv 192,48,16
Rb 104,17,8192
Mv 196,52,16
buffer_load_dwordx4 v27, s[12:15], s44 offen offset:1024 lds
Ma 224,56,16
Rb 108,17,10240
Ma 228,60,16
Ma 236,60,20
Rb 112,17,16384
Ma 232,56,20
s_add_u32 s44, s43, 12544
buffer_load_dwordx4 v26, s[12:15], s44 offen offset:2048 lds
Mv 204,52,20
Rb 116,17,18432
Mv 200,48,20
Mv 140,44,20
Rb 120,17,24576
Mv 136,40,20
buffer_load_dwordx4 v27, s[12:15], s44 offen offset:3072 lds
)" R"(
Mv 76,36,20
Rb 124,17,26624
Mv 72,32,20
Mv 80,32,24
s_add_u32 m0, s36, 135168
s_add_u32 s44, s43, 384
Mv 84,36,24
buffer_load_dwordx4 v26, s[12:15], s44 offen offset:0 lds
Mv 144,40,24
Mv 148,44,24
Mv 208,48,24
Mv 212,52,24
buffer_load_dwordx4 v27, s[12:15], s44 offen offset:1024 lds
Ma 240,56,24
Ma 244,60,24
Ma 252,60,28
Ma 248,56,28
s_add_u32 s44, s43, 12672
buffer_load_dwordx4 v26, s[12:15], s44 offen offset:2048 lds
Mv 220,52,28
Mv 216,48,28
Mv 156,44,28
s_mov_b32 s85, 0x1508800
v_and_b32 v2, 7, v0
v_lshlrev_b32 v2, 7, v2
Mv 152,40,28
buffer_load_dwordx4 v27, s[12:15], s44 offen offset:3072 lds
Mv 92,36,28
buffer_load_dword v4, v2, s[20:23], s85 offen sc0 sc1
Mv 88,32,28
s_waitcnt vmcnt(18) lgkmcnt(0)
s_barrier
Mv 32,96,64
Rt 0,18,32768
Mv 36,100,64
s_add_u32 s38, s38, 0xffff0080
Mv 96,104,64
Rt 2,18,34816
s_add_u32 m0, s36, 0
Mv 100,108,64
buffer_load_dwordx4 v8, s[4:7], s38 offen offset:0 lds
Mv 160,112,64
Rt 4,19,32768
Mv 164,116,64
Mv 224,120,64
Rt 6,19,34816
Mv 228,124,64
buffer_load_dwordx4 v9, s[4:7], s38 offen offset:1024 lds
Mv 236,124,68
Rt 8,20,32768
Mv 232,120,68
Mv 172,116,68
Rt 10,20,34816
Mv 168,112,68
buffer_load_dwordx4 v10, s[4:7], s38 offen offset:2048 lds
Mv 108,108,68
Rt 12,21,32768
Mv 104,104,68
Mv 44,100,68
Rt 14,21,34816
Mv 40,96,68
buffer_load_dwordx4 v11, s[4:7], s38 offen offset:3072 lds
Mv 48,96,72
Rt 16,22,32768
Mv 52,100,72
)" R"(
s_add_u32 s38, s38, 0x10000
Mv 112,104,72
Rt 18,22,34816
s_add_u32 m0, s36, 4096
Mv 116,108,72
buffer_load_dwordx4 v8, s[4:7], s38 offen offset:0 lds
Mv 176,112,72
Rt 20,23,32768
Mv 180,116,72
Mv 240,120,72
Rt 22,23,34816
Mv 244,124,72
buffer_load_dwordx4 v9, s[4:7], s38 offen offset:1024 lds
Mv 252,124,76
Rt 24,24,32768
Mv 248,120,76
Mv 188,116,76
Rt 26,24,34816
Mv 184,112,76
buffer_load_dwordx4 v10, s[4:7], s38 offen offset:2048 lds
Mv 124,108,76
Rt 28,25,32768
Mv 120,104,76
Mv 60,100,76
Rt 30,25,34816
Mv 56,96,76
buffer_load_dwordx4 v11, s[4:7], s38 offen offset:3072 lds
Mv 64,96,80
Rb 32,16,32768
Mv 68,100,80
Mv 128,104,80
Rb 36,16,34816
Mv 132,108,80
s_add_u32 s39, s39, 0x40000
Mv 192,112,80
Rb 40,16,40960
s_add_u32 m0, s37, 16384
Mv 196,116,80
buffer_load_dwordx4 v12, s[8:11], s39 offen offset:0 lds
Ma 224,120,80
Rb 44,16,43008
Ma 228,124,80
Ma 236,124,84
Rb 48,16,49152
Ma 232,120,84
Mv 204,116,84
Rb 52,16,51200
Mv 200,112,84
buffer_load_dwordx4 v13, s[8:11], s39 offen offset:1024 lds
Mv 140,108,84
Rb 56,16,57344
Mv 136,104,84
Mv 76,100,84
Rb 60,16,59392
Mv 72,96,84
Mv 80,96,88
Mv 84,100,88
buffer_load_dwordx4 v14, s[8:11], s39 offen offset:2048 lds
Mv 144,104,88
Mv 148,108,88
Mv 208,112,88
Mv 212,116,88
Ma 240,120,88
Ma 244,124,88
)" R"(
buffer_load_dwordx4 v15, s[8:11], s39 offen offset:3072 lds
Ma 252,124,92
Ma 248,120,92
Mv 220,116,92
Mv 216,112,92
Mv 156,108,92
Mv 152,104,92
Mv 92,100,92
Mv 88,96,92
s_waitcnt vmcnt(13) lgkmcnt(0)
s_barrier
Mv 32,32,0
Rt 64,18,49152
Mv 36,36,0
Rb 128,28,32768
Mv 96,40,0
Rt 66,18,51200
Mv 100,44,0
s_add_u32 s39, s39, 0x40000
Rb 132,28,34816
Mv 160,48,0
Rt 68,19,49152
s_add_u32 m0, s37, 32768
Mv 164,52,0
buffer_load_dwordx4 v12, s[8:11], s39 offen offset:0 lds
Mv 224,56,0
Rt 70,19,51200
Mv 228,60,0
Rb 136,29,32768
Mv 236,60,4
Rt 72,20,49152
Mv 232,56,4
Rb 140,29,34816
Mv 172,52,4
Rt 74,20,51200
Mv 168,48,4
Rb 144,28,36864
Mv 108,44,4
Rt 76,21,49152
Mv 104,40,4
buffer_load_dwordx4 v13, s[8:11], s39 offen offset:1024 lds
Mv 44,36,4
Rt 78,21,51200
Mv 40,32,4
Rb 148,28,38912
Mv 48,32,8
Rt 80,22,49152
Mv 52,36,8
Rb 152,29,36864
Mv 112,40,8
Rt 82,22,51200
Mv 116,44,8
Rb 156,29,38912
Mv 176,48,8
Rt 84,23,49152
Mv 180,52,8
buffer_load_dwordx4 v14, s[8:11], s39 offen offset:2048 lds
Mv 240,56,8
Rt 86,23,51200
Mv 244,60,8
Mv 252,60,12
Rt 88,24,49152
Mv 248,56,12
Mv 188,52,12
Rt 90,24,51200
)" R"(
Mv 184,48,12
Mv 124,44,12
Rt 92,25,49152
Mv 120,40,12
buffer_load_dwordx4 v15, s[8:11], s39 offen offset:3072 lds
Mv 60,36,12
Rt 94,25,51200
Mv 56,32,12
s_add_u32 s44, s43, 512
s_waitcnt lgkmcnt(6)
Mv 64,32,16
Rb 96,17,32768
s_add_u32 m0, s36, 131072
Mv 68,36,16
buffer_load_dwordx4 v26, s[12:15], s44 offen offset:0 lds
Mv 128,40,16
Rb 100,17,34816
Mv 132,44,16
Mv 192,48,16
Rb 104,17,40960
Mv 196,52,16
buffer_load_dwordx4 v27, s[12:15], s44 offen offset:1024 lds
Ma 224,56,16
Rb 108,17,43008
Ma 228,60,16
Ma 236,60,20
Rb 112,17,49152
Ma 232,56,20
s_add_u32 s44, s43, 12800
buffer_load_dwordx4 v26, s[12:15], s44 offen offset:2048 lds
Mv 204,52,20
Rb 116,17,51200
Mv 200,48,20
Mv 140,44,20
Rb 120,17,57344
Mv 136,40,20
buffer_load_dwordx4 v27, s[12:15], s44 offen offset:3072 lds
Mv 76,36,20
Rb 124,17,59392
Mv 72,32,20
Mv 80,32,24
s_add_u32 m0, s36, 135168
s_add_u32 s44, s43, 640
Mv 84,36,24
buffer_load_dwordx4 v26, s[12:15], s44 offen offset:0 lds
Mv 144,40,24
Mv 148,44,24
Mv 208,48,24
Mv 212,52,24
buffer_load_dwordx4 v27, s[12:15], s44 offen offset:1024 lds
Ma 240,56,24
Ma 244,60,24
Ma 252,60,28
Ma 248,56,28
s_add_u32 s44, s43, 12928
buffer_load_dwordx4 v26, s[12:15], s44 offen offset:2048 lds
Mv 220,52,28
Mv 216,48,28
Mv 156,44,28
Mv 152,40,28
buffer_load_dwordx4 v27, s[12:15], s44 offen offset:3072 lds
Mv 92,36,28
Mv 88,32,28
s_waitcnt vmcnt(16) lgkmcnt(0)
s_barrier
)" R"(
Mv 32,96,64
Rt 0,18,0
v_xor_b32 v4, s3, v4
v_cmp_gt_u32_e32 vcc, 2, v4
s_andn2_b32 s28, 0xff, vcc_lo
s_cbranch_scc1 .Lzsp3
.Lzok3:
Mv 36,100,64
s_add_u32 s38, s38, 0xffff0080
v_xor_b32 v4, s3, v4
Mv 96,104,64
Rt 2,18,2048
s_add_u32 m0, s36, 32768
Mv 100,108,64
buffer_load_dwordx4 v8, s[4:7], s38 offen offset:0 lds
Mv 160,112,64
Rt 4,19,0
v_and_b32 v4, 1, v4
Mv 164,116,64
v_cmp_eq_u32_e32 vcc, 1, v4
s_mov_b32 s85, vcc_lo
Mv 224,120,64
Rt 6,19,2048
s_and_b32 s28, s2, 7
Mv 228,124,64
buffer_load_dwordx4 v9, s[4:7], s38 offen offset:1024 lds
Mv 236,124,68
Rt 8,20,0
s_lshr_b32 s28, s85, s28
Mv 232,120,68
s_xor_b32 s28, s28, s34
s_and_b32 s28, s28, 1
Mv 172,116,68
Rt 10,20,2048
s_lshl_b32 s84, s28, 13
Mv 168,112,68
buffer_load_dwordx4 v10, s[4:7], s38 offen offset:2048 lds
Mv 108,108,68
Rt 12,21,0
s_add_u32 s49, s49, s84
Mv 104,104,68
s_add_u32 s50, s50, s84
s_add_u32 s60, s50, 7168
Mv 44,100,68
Rt 14,21,2048
s_add_u32 s52, s52, s84
Mv 40,96,68
buffer_load_dwordx4 v11, s[4:7], s38 offen offset:3072 lds
Mv 48,96,72
Rt 16,22,0
s_add_u32 s65, s65, s84
Mv 52,100,72
s_add_u32 s38, s38, 0x10000
s_lshr_b32 s28, s2, 6
Mv 112,104,72
Rt 18,22,2048
s_add_u32 m0, s36, 36864
Mv 116,108,72
buffer_load_dwordx4 v8, s[4:7], s38 offen offset:0 lds
Mv 176,112,72
Rt 20,23,0
s_lshl_b32 s28, s28, 14
Mv 180,116,72
s_add_u32 s28, s28, s50
)" R"(
s_bfe_u32 s29, s2, 0x20004
Mv 240,120,72
Rt 22,23,2048
s_lshl2_add_u32 s29, s29, s33
Mv 244,124,72
buffer_load_dwordx4 v9, s[4:7], s38 offen offset:1024 lds
Mv 252,124,76
Rt 24,24,0
s_lshl_b32 s29, s29, 9
Mv 248,120,76
s_add_u32 s28, s28, s29
v_min_u32 v4, 3, v0
Mv 188,116,76
Rt 26,24,2048
v_lshlrev_b32 v4, 7, v4
Mv 184,112,76
buffer_load_dwordx4 v10, s[4:7], s38 offen offset:2048 lds
Mv 124,108,76
Rt 28,25,0
Mv 120,104,76
buffer_load_dword v4, v4, s[20:23], s28 offen sc0 sc1
Mv 60,100,76
Rt 30,25,2048
s_lshl1_add_u32 s28, s36, s84
Mv 56,96,76
buffer_load_dwordx4 v11, s[4:7], s38 offen offset:3072 lds
Mv 64,96,80
Rb 32,16,0
v_add_u32 v2, s28, v1
Mv 68,100,80
v_add_u32 v3, 0x1000, v2
s_lshl_b32 s28, s34, 13
Mv 128,104,80
Rb 36,16,2048
s_xor_b32 s28, s28, s84
Mv 132,108,80
s_add_u32 s39, s39, 0x40000
s_and_b32 s28, s28, 0x2000
Mv 192,112,80
Rb 40,16,8192
s_add_u32 m0, s37, 49152
Mv 196,116,80
buffer_load_dwordx4 v12, s[8:11], s39 offen offset:0 lds
Ma 224,120,80
Rb 44,16,10240
s_xor_b32 s73, s73, s28
Ma 228,124,80
Ma 236,124,84
Rb 48,16,16384
Ma 232,120,84
Mv 204,116,84
Rb 52,16,18432
Mv 200,112,84
buffer_load_dwordx4 v13, s[8:11], s39 offen offset:1024 lds
Mv 140,108,84
Rb 56,16,24576
Mv 136,104,84
Mv 76,100,84
Rb 60,16,26624
Mv 72,96,84
Mv 80,96,88
Mv 84,100,88
buffer_load_dwordx4 v14, s[8:11], s39 offen offset:2048 lds
Mv 144,104,88
)" R"(
Mv 148,108,88
Mv 208,112,88
Mv 212,116,88
Ma 240,120,88
Ma 244,124,88
buffer_load_dwordx4 v15, s[8:11], s39 offen offset:3072 lds
Ma 252,124,92
Ma 248,120,92
Mv 220,116,92
Mv 216,112,92
Mv 156,108,92
Mv 152,104,92
Mv 92,100,92
Mv 88,96,92
s_waitcnt vmcnt(5) lgkmcnt(0)
s_barrier
Mv 32,32,0
Rt 64,18,16384
Mv 36,36,0
Rb 160,28,32768
v_and_b32 v5, 7, v0
Mv 96,40,0
Rt 66,18,18432
v_lshlrev_b32 v5, 7, v5
.LzH32:
s_cbranch_execz .LzH38
Mv 100,44,0
Rb 164,28,34816
v_mov_b32 v6, s3
Mv 160,48,0
Rt 68,19,16384
Mv 164,52,0
Mv 224,56,0
Rt 70,19,18432
Mv 228,60,0
Rb 168,29,32768
Mv 236,60,4
Rt 72,20,16384
Mv 232,56,4
Rb 172,29,34816
Mv 172,52,4
Rt 74,20,18432
Mv 168,48,4
Rb 176,28,36864
Mv 108,44,4
Rt 76,21,16384
Mv 104,40,4
Mv 44,36,4
Rt 78,21,18432
Mv 40,32,4
Rb 180,28,38912
Mv 48,32,8
Rt 80,22,16384
Mv 52,36,8
Rb 184,29,36864
Mv 112,40,8
Rt 82,22,18432
Mv 116,44,8
Rb 188,29,38912
Mv 176,48,8
Rt 84,23,16384
Mv 180,52,8
Mv 240,56,8
Rt 86,23,18432
Mv 244,60,8
)" R"(
Mv 252,60,12
Rt 88,24,16384
Mv 248,56,12
Mv 188,52,12
Rt 90,24,18432
Mv 184,48,12
Mv 124,44,12
Rt 92,25,16384
Mv 120,40,12
Mv 60,36,12
Rt 94,25,18432
v_lshrrev_b32 v10, 3, v0
Mv 56,32,12
v_and_b32 v11, 0x70, v1
v_lshl_or_b32 v10, v10, 13, v11
Mv 64,32,16
Rb 96,17,0
v_lshlrev_b32 v11, 1, v0
Mv 68,36,16
v_and_b32 v11, 32, v11
v_xor_b32 v10, v10, v11
buffer_load_dword v4, v7, s[20:23], s52 offen offset:0 sc0 sc1
Mv 128,40,16
Rb 100,17,2048
s_and_b32 s28, s33, 1
Mv 132,44,16
s_lshl_b32 s28, s28, 6
v_xor_b32 v8, s28, v10
Mv 192,48,16
Rb 104,17,8192
v_xor_b32 v9, 64, v8
Mv 196,52,16
v_add_u32 v9, 0xfc00, v9
v_add_u32 v10, 0x1f800, v8
Ma 224,56,16
Rb 108,17,10240
v_add_u32 v11, 0x1f800, v9
Ma 228,60,16
Ma 236,60,20
Rb 112,17,16384
Ma 232,56,20
Mv 204,52,20
Rb 116,17,18432
Mv 200,48,20
Mv 140,44,20
Rb 120,17,24576
Mv 136,40,20
Mv 76,36,20
Rb 124,17,26624
Mv 72,32,20
Mv 80,32,24
Mv 84,36,24
Mv 144,40,24
Mv 148,44,24
Mv 208,48,24
Mv 212,52,24
Ma 240,56,24
Ma 244,60,24
Ma 252,60,28
Ma 248,56,28
Mv 220,52,28
Mv 216,48,28
Mv 156,44,28
Mv 152,40,28
)" R"(
Mv 92,36,28
Mv 88,32,28
s_waitcnt vmcnt(5) lgkmcnt(0)
s_barrier
Mv 32,96,64
Rt 0,18,32768
Mv 36,100,64
s_add_u32 s92, s42, 0x0
Mv 96,104,64
Rt 2,18,34816
s_add_u32 m0, s41, 131072
Mv 100,108,64
buffer_load_dwordx4 v8, s[16:19], s92 offen offset:0 lds
Mv 160,112,64
Rt 4,19,32768
Mv 164,116,64
Mv 224,120,64
Rt 6,19,34816
Mv 228,124,64
buffer_load_dwordx4 v9, s[16:19], s92 offen offset:1024 lds
Mv 236,124,68
Rt 8,20,32768
Mv 232,120,68
Mv 172,116,68
Rt 10,20,34816
Mv 168,112,68
buffer_load_dwordx4 v10, s[16:19], s92 offen offset:2048 lds
Mv 108,108,68
Rt 12,21,32768
Mv 104,104,68
Mv 44,100,68
Rt 14,21,34816
Mv 40,96,68
buffer_load_dwordx4 v11, s[16:19], s92 offen offset:3072 lds
Mv 48,96,72
Rt 16,22,32768
Mv 52,100,72
s_add_u32 s92, s42, 0x40000
Mv 112,104,72
Rt 18,22,34816
s_add_u32 m0, s41, 135168
Mv 116,108,72
buffer_load_dwordx4 v8, s[16:19], s92 offen offset:0 lds
Mv 176,112,72
Rt 20,23,32768
Mv 180,116,72
Mv 240,120,72
Rt 22,23,34816
Mv 244,124,72
buffer_load_dwordx4 v9, s[16:19], s92 offen offset:1024 lds
Mv 252,124,76
Rt 24,24,32768
Mv 248,120,76
Mv 188,116,76
Rt 26,24,34816
Mv 184,112,76
buffer_load_dwordx4 v10, s[16:19], s92 offen offset:2048 lds
Mv 124,108,76
Rt 28,25,32768
Mv 120,104,76
s_add_u32 s92, s42, 0x1c0000
Mv 60,100,76
Rt 30,25,34816
s_add_u32 m0, s41, 0
Mv 56,96,76
)" R"(
buffer_load_dwordx4 v8, s[16:19], s92 offen offset:0 lds
Mv 64,96,80
Rb 32,16,32768
Mv 68,100,80
Mv 128,104,80
Rb 36,16,34816
Mv 132,108,80
buffer_load_dwordx4 v9, s[16:19], s92 offen offset:1024 lds
Mv 192,112,80
Rb 40,16,40960
Mv 196,116,80
Ma 224,120,80
Rb 44,16,43008
Ma 228,124,80
buffer_load_dwordx4 v10, s[16:19], s92 offen offset:2048 lds
Ma 236,124,84
Rb 48,16,49152
Ma 232,120,84
Mv 204,116,84
Rb 52,16,51200
Mv 200,112,84
buffer_load_dwordx4 v11, s[16:19], s92 offen offset:3072 lds
Mv 140,108,84
Rb 56,16,57344
Mv 136,104,84
s_add_u32 s92, s42, 0x200000
Mv 76,100,84
Rb 60,16,59392
s_add_u32 m0, s41, 4096
Mv 72,96,84
buffer_load_dwordx4 v8, s[16:19], s92 offen offset:0 lds
Mv 80,96,88
Mv 84,100,88
Mv 144,104,88
Mv 148,108,88
buffer_load_dwordx4 v9, s[16:19], s92 offen offset:1024 lds
Mv 208,112,88
Mv 212,116,88
Ma 240,120,88
Ma 244,124,88
buffer_load_dwordx4 v10, s[16:19], s92 offen offset:2048 lds
Ma 252,124,92
Ma 248,120,92
Mv 220,116,92
Mv 216,112,92
Mv 156,108,92
Mv 152,104,92
Mv 92,100,92
Mv 88,96,92
s_waitcnt vmcnt(14) lgkmcnt(4)
s_barrier
v_cmp_eq_u32_e32 vcc, s3, v4
s_andn2_b64 s[28:29], 1, vcc
s_cbranch_scc1 .Lzsp4
.Lzok4:
Mv 32,32,0
Rt 64,18,49152
Mv 36,36,0
Rt 66,18,51200
s_add_u32 s92, s42, 0x80
Mv 96,40,0
Rt 68,19,49152
s_add_u32 m0, s41, 65536
Mv 100,44,0
Rt 70,19,51200
)" R"(
buffer_load_dwordx4 v8, s[16:19], s92 offen offset:0 lds
Mv 108,44,4
Rt 72,20,49152
Mv 104,40,4
Rt 74,20,51200
Mv 44,36,4
Rt 76,21,49152
Mv 40,32,4
Rt 78,21,51200
buffer_load_dwordx4 v9, s[16:19], s92 offen offset:1024 lds
Mv 48,32,8
Rt 80,22,49152
Mv 52,36,8
Rt 82,22,51200
Mv 112,40,8
Rt 84,23,49152
Mv 116,44,8
Rt 86,23,51200
buffer_load_dwordx4 v10, s[16:19], s92 offen offset:2048 lds
Mv 124,44,12
Rt 88,24,49152
Mv 120,40,12
Rt 90,24,51200
Mv 60,36,12
Rt 92,25,49152
Mv 56,32,12
Rt 94,25,51200
buffer_load_dwordx4 v11, s[16:19], s92 offen offset:3072 lds
Mv 64,32,16
Rb 96,17,32768
Mv 68,36,16
Rb 100,17,34816
s_add_u32 s92, s42, 0x40080
Mv 128,40,16
Rb 104,17,40960
s_add_u32 m0, s41, 69632
Mv 132,44,16
Rb 108,17,43008
buffer_load_dwordx4 v8, s[16:19], s92 offen offset:0 lds
Mv 140,44,20
Rb 112,17,49152
Mv 136,40,20
Rb 116,17,51200
Mv 76,36,20
Rb 120,17,57344
Mv 72,32,20
Rb 124,17,59392
buffer_load_dwordx4 v9, s[16:19], s92 offen offset:1024 lds
Mv 80,32,24
Mv 84,36,24
Mv 144,40,24
Mv 148,44,24
buffer_load_dwordx4 v10, s[16:19], s92 offen offset:2048 lds
Mv 156,44,28
Mv 152,40,28
Mv 92,36,28
Mv 88,32,28
s_waitcnt lgkmcnt(0)
s_barrier
Mv 32,96,64
v_bfe_u32 v12, v0, 3, 2
v_lshlrev_b32 v12, 5, v12
Mv 36,100,64
v_and_b32 v4, 3, v0
v_lshl_or_b32 v12, v4, 3, v12
)" R"(
Mv 40,96,68
s_add_u32 m0, s36, 98304
s_add_u32 s44, s43, 768
Mv 44,100,68
buffer_load_dwordx4 v26, s[12:15], s44 offen offset:0 lds
Mv 48,96,72
v_bfe_u32 v4, v0, 2, 2
v_lshl_or_b32 v12, v4, 7, v12
Mv 52,100,72
v_lshrrev_b32 v4, 4, v0
v_lshl_or_b32 v12, v4, 10, v12
Mv 56,96,76
v_xor_b32 v13, 32, v12
v_xor_b32 v14, 64, v12
Mv 60,100,76
buffer_load_dwordx4 v27, s[12:15], s44 offen offset:1024 lds
Mv 64,96,80
v_xor_b32 v15, 96, v12
v_add_u32 v20, 0x20000, v12
Mv 68,100,80
v_add_u32 v21, 0x20000, v13
v_add_u32 v22, 0x20000, v14
Mv 72,96,84
v_add_u32 v23, 0x20000, v15
v_add_u32 v16, 0x10000, v12
Mv 76,100,84
s_add_u32 s44, s43, 13056
buffer_load_dwordx4 v26, s[12:15], s44 offen offset:2048 lds
Mv 80,96,88
v_add_u32 v17, 0x10000, v13
v_add_u32 v18, 0x10000, v14
Mv 84,100,88
v_add_u32 v19, 0x10000, v15
Cv 32,32,33
Mv 88,96,92
Cv 33,34,35
Cv 34,36,37
Mv 92,100,92
buffer_load_dwordx4 v27, s[12:15], s44 offen offset:3072 lds
Mv 96,104,64
Cv 35,38,39
Cv 40,40,41
Mv 100,108,64
Cv 41,42,43
Cv 42,44,45
Mv 104,104,68
s_add_u32 m0, s41, 32768
s_add_u32 s92, s42, 0x1c0080
Mv 108,108,68
buffer_load_dwordx4 v8, s[16:19], s92 offen offset:0 lds
Mv 112,104,72
Cv 43,46,47
Cv 48,48,49
Mv 116,108,72
Cv 49,50,51
Cv 50,52,53
Mv 120,104,76
Cv 51,54,55
Cv 56,56,57
Mv 124,108,76
buffer_store_dwordx4 v[32:35], v2, s[20:23], s45 offen offset:0 sc0 sc1
Mv 128,104,80
Cv 57,58,59
Cv 58,60,61
)" R"(
Mv 132,108,80
Cv 59,62,63
Cv 64,64,65
Mv 136,104,84
Cv 65,66,67
Cv 66,68,69
Mv 140,108,84
buffer_load_dwordx4 v9, s[16:19], s92 offen offset:1024 lds
Mv 144,104,88
Cv 67,70,71
Cv 72,72,73
Mv 148,108,88
Cv 73,74,75
Cv 74,76,77
Mv 152,104,92
Cv 75,78,79
Cv 80,80,81
Mv 156,108,92
buffer_store_dwordx4 v[40:43], v2, s[20:23], s45 offen offset:1024 sc0 sc1
Mv 160,48,0
Cv 81,82,83
Cv 82,84,85
Mv 160,112,64
Cv 83,86,87
Cv 88,88,89
Mv 164,52,0
Cv 89,90,91
Cv 90,92,93
Mv 164,116,64
buffer_load_dwordx4 v10, s[16:19], s92 offen offset:2048 lds
Mv 168,48,4
Cv 91,94,95
Cv 96,96,97
Mv 168,112,68
Cv 97,98,99
Cv 98,100,101
Mv 172,52,4
Cv 99,102,103
Cv 104,104,105
Mv 172,116,68
buffer_store_dwordx4 v[48:51], v2, s[20:23], s45 offen offset:2048 sc0 sc1
Mv 176,48,8
Cv 105,106,107
Cv 106,108,109
Mv 176,112,72
Cv 107,110,111
Cv 112,112,113
Mv 180,52,8
Cv 113,114,115
Cv 114,116,117
Mv 180,116,72
buffer_load_dwordx4 v11, s[16:19], s92 offen offset:3072 lds
Mv 184,48,12
Cv 115,118,119
Cv 120,120,121
Mv 184,112,76
Cv 121,122,123
Cv 122,124,125
Mv 188,52,12
Cv 123,126,127
Cv 128,128,129
Mv 188,116,76
buffer_store_dwordx4 v[56:59], v2, s[20:23], s45 offen offset:3072 sc0 sc1
Mv 192,48,16
)" R"(
Cv 129,130,131
Cv 130,132,133
Mv 192,112,80
Cv 131,134,135
Cv 136,136,137
Mv 196,52,16
s_add_u32 m0, s41, 36864
s_add_u32 s92, s42, 0x200080
Mv 196,116,80
buffer_load_dwordx4 v8, s[16:19], s92 offen offset:0 lds
Mv 200,48,20
Cv 137,138,139
Cv 138,140,141
Mv 200,112,84
Cv 139,142,143
Cv 144,144,145
Mv 204,52,20
Cv 145,146,147
Cv 146,148,149
Mv 204,116,84
buffer_store_dwordx4 v[64:67], v3, s[20:23], s45 offen offset:0 sc0 sc1
Mv 208,48,24
Cv 147,150,151
Cv 152,152,153
Mv 208,112,88
Cv 153,154,155
Cv 154,156,157
Mv 212,52,24
Cv 155,158,159
Mv 212,116,88
buffer_load_dwordx4 v9, s[16:19], s92 offen offset:1024 lds
Mv 216,48,28
Mv 216,112,92
Mv 220,52,28
Mv 220,116,92
buffer_store_dwordx4 v[72:75], v3, s[20:23], s45 offen offset:1024 sc0 sc1
Mv 224,56,0
Mv 228,60,0
Mv 236,60,4
Mv 232,56,4
buffer_load_dwordx4 v10, s[16:19], s92 offen offset:2048 lds
Mv 240,56,8
Mv 244,60,8
Mv 252,60,12
Mv 248,56,12
s_waitcnt vmcnt(24)
s_barrier
Ma 224,56,16
buffer_store_dwordx4 v[80:83], v3, s[20:23], s45 offen offset:2048 sc0 sc1
Ma 228,60,16
Ma 236,60,20
Ma 232,56,20
Ma 240,56,24
Ma 244,60,24
Ma 252,60,28
Ma 248,56,28
buffer_store_dwordx4 v[88:91], v3, s[20:23], s45 offen offset:3072 sc0 sc1
Mv 224,120,64
Rt 16,20,0
Mv 228,124,64
Rt 18,20,512
Mv 232,120,68
Rt 20,21,0
Mv 236,124,68
Rt 22,21,512
)" R"(
Mv 240,120,72
Rt 24,22,0
Mv 244,124,72
Rt 26,22,512
Mv 248,120,76
Rt 28,23,0
Mv 252,124,76
Rt 30,23,512
Ma 224,120,80
Rt 32,20,4096
Ma 228,124,80
Rt 34,20,4608
Ma 232,120,84
Rt 36,21,4096
Ma 236,124,84
Rt 38,21,4608
Ma 240,120,88
Rt 40,22,4096
Ma 244,124,88
Rt 42,22,4608
Ma 248,120,92
Rt 44,23,4096
Ma 252,124,92
Rt 46,23,4608
s_waitcnt vmcnt(15) lgkmcnt(8)
.LzH33:
s_cbranch_execz .LzH41
.LzQA:
s_cmp_eq_u32 s2, 112
s_cbranch_scc1 .LzT2S
.LzT2E:
Mz 32,16,192
Rt 0,20,8192
Cv 224,224,225
Mz 40,16,196
Rt 2,20,8704
Rb 48,28,0
Mz 44,20,196
Rt 4,21,8192
buffer_store_dwordx4 v[96:99], v2, s[20:23], s46 offen offset:0 sc0 sc1
.LzH34:
s_cbranch_execz .LzH42
Mz 36,20,192
Rt 6,21,8704
Rb 52,28,2048
Mz 48,24,192
Rt 8,22,8192
Cv 225,226,227
Mz 56,24,196
Rt 10,22,8704
Rb 56,29,0
buffer_load_dwordx4 a[64:67], v2, s[20:23], s51 offen offset:0 sc0 sc1
Mz 60,28,196
Rt 12,23,8192
Cv 226,228,229
Mz 52,28,192
Rt 14,23,8704
Rb 60,29,2048
s_waitcnt lgkmcnt(12)
Mv 32,32,200
Rt 16,20,12288
Cv 227,230,231
Mv 40,32,204
Rt 18,20,12800
)" R"(
Cv 232,232,233
Mv 44,36,204
Rt 20,21,12288
buffer_load_dwordx4 a[68:71], v2, s[20:23], s51 offen offset:1024 sc0 sc1
Mv 36,36,200
Rt 22,21,12800
Cv 233,234,235
Mv 48,40,200
Rt 24,22,12288
buffer_store_dwordx4 v[104:107], v2, s[20:23], s46 offen offset:1024 sc0 sc1
Mv 56,40,204
Rt 26,22,12800
Cv 234,236,237
Mv 60,44,204
Rt 28,23,12288
Cv 235,238,239
Mv 52,44,200
Rt 30,23,12800
buffer_load_dwordx4 a[72:75], v2, s[20:23], s51 offen offset:2048 sc0 sc1
s_waitcnt lgkmcnt(9)
Mv 32,0,208
Rt 32,20,16384
Cv 240,240,241
Mv 40,0,212
Rt 34,20,16896
Cv 241,242,243
Mv 44,4,212
Rt 36,21,16384
Cv 242,244,245
Mv 36,4,208
Rt 38,21,16896
Cv 243,246,247
Mv 48,8,208
Rt 40,22,16384
buffer_load_dwordx4 a[76:79], v2, s[20:23], s51 offen offset:3072 sc0 sc1
Mv 56,8,212
Rt 42,22,16896
Cv 248,248,249
Cv 249,250,251
Mv 60,12,212
Rt 44,23,16384
buffer_store_dwordx4 v[112:115], v2, s[20:23], s46 offen offset:2048 sc0 sc1
Mv 52,12,208
Rt 46,23,16896
s_waitcnt lgkmcnt(8)
Mv 32,16,216
Rt 0,20,20480
Cv 250,252,253
Mv 40,16,220
Rt 2,20,20992
buffer_load_dwordx4 a[80:83], v3, s[20:23], s51 offen offset:0 sc0 sc1
Mv 44,20,220
Rt 4,21,20480
Cv 251,254,255
v_accvgpr_read_b32 v228, a224
Mv 36,20,216
Rt 6,21,20992
v_accvgpr_read_b32 v24, a225
Mv 48,24,216
Rt 8,22,20480
Cv 228,228,24
Mv 56,24,220
Rt 10,22,20992
v_accvgpr_read_b32 v229, a226
)" R"(
Mv 60,28,220
Rt 12,23,20480
buffer_load_dwordx4 a[84:87], v3, s[20:23], s51 offen offset:1024 sc0 sc1
Mv 52,28,216
Rt 14,23,20992
v_accvgpr_read_b32 v25, a227
s_waitcnt lgkmcnt(8)
Mv 32,32,128
Rt 16,20,24576
buffer_store_dwordx4 v[120:123], v2, s[20:23], s46 offen offset:3072 sc0 sc1
Mv 40,32,132
Rt 18,20,25088
Cv 229,229,25
Mv 44,36,132
Rt 20,21,24576
v_accvgpr_read_b32 v230, a228
Mv 36,36,128
Rt 22,21,25088
buffer_load_dwordx4 a[88:91], v3, s[20:23], s51 offen offset:2048 sc0 sc1
Mv 48,40,128
Rt 24,22,24576
v_accvgpr_read_b32 v24, a229
Mv 56,40,132
Rt 26,22,25088
Cv 230,230,24
v_accvgpr_read_b32 v231, a230
Mv 60,44,132
Rt 28,23,24576
v_accvgpr_read_b32 v25, a231
Mv 52,44,128
Rt 30,23,25088
s_waitcnt lgkmcnt(8)
Mv 32,0,136
Rt 32,12,0
buffer_load_dwordx4 a[92:95], v3, s[20:23], s51 offen offset:3072 sc0 sc1
Mv 40,0,140
Rt 34,12,512
Cv 231,231,25
Mv 44,4,140
Rt 36,13,0
buffer_store_dwordx4 v[128:131], v3, s[20:23], s46 offen offset:0 sc0 sc1
Mv 36,4,136
Rt 38,13,512
v_accvgpr_read_b32 v236, a232
Mv 48,8,136
Rt 40,14,0
v_accvgpr_read_b32 v24, a233
Mv 56,8,140
Rt 42,14,512
Cv 236,236,24
Mv 60,12,140
Rt 44,15,0
v_accvgpr_read_b32 v237, a234
Mv 52,12,136
Rt 46,15,512
s_waitcnt vmcnt(13) lgkmcnt(8)
s_barrier
buffer_store_dword v6, v7, s[20:23], s49 offen offset:0 sc0 sc1
Mv 32,16,144
Rt 0,12,4096
v_accvgpr_read_b32 v25, a235
Mv 40,16,148
Rt 2,12,4608
s_add_u32 s92, s42, 0x100
Mv 44,20,148
)" R"(
Rt 4,13,4096
s_add_u32 m0, s41, 98304
Mv 36,20,144
Rt 6,13,4608
buffer_load_dwordx4 v8, s[16:19], s92 offen offset:0 lds
Mv 48,24,144
Rt 8,14,4096
Cv 237,237,25
Mv 56,24,148
Rt 10,14,4608
v_accvgpr_read_b32 v238, a236
v_accvgpr_read_b32 v24, a237
Mv 60,28,148
Rt 12,15,4096
Cv 238,238,24
Mv 52,28,144
Rt 14,15,4608
buffer_load_dwordx4 v9, s[16:19], s92 offen offset:1024 lds
s_waitcnt lgkmcnt(8)
Mv 32,32,152
Rt 16,12,8192
v_accvgpr_read_b32 v239, a238
Mv 40,32,156
Rt 18,12,8704
buffer_store_dwordx4 v[136:139], v3, s[20:23], s46 offen offset:1024 sc0 sc1
Mv 44,36,156
Rt 20,13,8192
v_accvgpr_read_b32 v25, a239
Cv 239,239,25
Mv 36,36,152
Rt 22,13,8704
buffer_load_dwordx4 v10, s[16:19], s92 offen offset:2048 lds
Mv 48,40,152
Rt 24,14,8192
v_accvgpr_read_b32 v244, a240
Mv 56,40,156
Rt 26,14,8704
v_accvgpr_read_b32 v24, a241
Mv 60,44,156
Rt 28,15,8192
Cv 244,244,24
Mv 52,44,152
Rt 30,15,8704
buffer_load_dwordx4 v11, s[16:19], s92 offen offset:3072 lds
s_waitcnt lgkmcnt(8)
Mv 32,0,160
Rt 32,12,12288
v_accvgpr_read_b32 v245, a242
Mv 40,0,164
Rt 34,12,12800
s_add_u32 s92, s42, 0x40100
Mv 44,4,164
Rt 36,13,12288
s_add_u32 m0, s41, 102400
Mv 36,4,160
Rt 38,13,12800
buffer_load_dwordx4 v8, s[16:19], s92 offen offset:0 lds
Mv 48,8,160
Rt 40,14,12288
v_accvgpr_read_b32 v25, a243
Mv 56,8,164
Rt 42,14,12800
Cv 245,245,25
v_accvgpr_read_b32 v246, a244
)" R"(
Mv 60,12,164
Rt 44,15,12288
v_accvgpr_read_b32 v24, a245
Mv 52,12,160
Rt 46,15,12800
buffer_load_dwordx4 v9, s[16:19], s92 offen offset:1024 lds
s_waitcnt lgkmcnt(8)
Mv 32,16,168
Rt 0,12,16384
Cv 246,246,24
Mv 40,16,172
Rt 2,12,16896
buffer_store_dwordx4 v[144:147], v3, s[20:23], s46 offen offset:2048 sc0 sc1
Mv 44,20,172
Rt 4,13,16384
v_accvgpr_read_b32 v247, a246
v_accvgpr_read_b32 v25, a247
Mv 36,20,168
Rt 6,13,16896
buffer_load_dwordx4 v10, s[16:19], s92 offen offset:2048 lds
Mv 48,24,168
Rt 8,14,16384
Cv 247,247,25
Mv 56,24,172
Rt 10,14,16896
s_add_u32 s92, s42, 0x1c0100
Mv 60,28,172
Rt 12,15,16384
s_add_u32 m0, s41, 131072
Mv 52,28,168
Rt 14,15,16896
buffer_load_dwordx4 v8, s[16:19], s92 offen offset:0 lds
s_waitcnt lgkmcnt(8)
Mv 32,32,176
Rt 16,12,20480
v_accvgpr_read_b32 v252, a248
Mv 40,32,180
Rt 18,12,20992
v_accvgpr_read_b32 v24, a249
Mv 44,36,180
Rt 20,13,20480
Cv 252,252,24
Mv 36,36,176
Rt 22,13,20992
buffer_load_dwordx4 v9, s[16:19], s92 offen offset:1024 lds
Mv 48,40,176
Rt 24,14,20480
v_accvgpr_read_b32 v253, a250
Mv 56,40,180
Rt 26,14,20992
v_accvgpr_read_b32 v25, a251
Cv 253,253,25
Mv 60,44,180
Rt 28,15,20480
v_accvgpr_read_b32 v254, a252
Mv 52,44,176
Rt 30,15,20992
buffer_load_dwordx4 v10, s[16:19], s92 offen offset:2048 lds
s_waitcnt lgkmcnt(8)
Mv 32,0,184
Rt 32,12,24576
v_accvgpr_read_b32 v24, a253
Mv 40,0,188
Rt 34,12,25088
)" R"(
buffer_store_dwordx4 v[152:155], v3, s[20:23], s46 offen offset:3072 sc0 sc1
Mv 44,4,188
Rt 36,13,24576
Cv 254,254,24
v_accvgpr_read_b32 v255, a254
Mv 36,4,184
Rt 38,13,25088
buffer_load_dwordx4 v11, s[16:19], s92 offen offset:3072 lds
Mv 48,8,184
Rt 40,14,24576
v_accvgpr_read_b32 v25, a255
Mv 56,8,188
Rt 42,14,25088
s_add_u32 s92, s42, 0x200100
Mv 60,12,188
Rt 44,15,24576
s_add_u32 m0, s41, 135168
Mv 52,12,184
Rt 46,15,25088
buffer_load_dwordx4 v8, s[16:19], s92 offen offset:0 lds
s_waitcnt lgkmcnt(8)
Mv 32,16,48
Rt 0,16,0
Cv 255,255,25
Mv 40,16,52
Rt 2,16,512
Cv 160,160,161
Mv 44,20,52
Rt 4,17,0
Cv 161,162,163
Mv 36,20,48
Rt 6,17,512
buffer_load_dwordx4 v9, s[16:19], s92 offen offset:1024 lds
Mv 48,24,48
Rt 8,18,0
Cv 162,164,165
Mv 56,24,52
Rt 10,18,512
Cv 163,166,167
Cv 168,168,169
Mv 60,28,52
Rt 12,19,0
Cv 169,170,171
Mv 52,28,48
Rt 14,19,512
buffer_load_dwordx4 v10, s[16:19], s92 offen offset:2048 lds
s_waitcnt lgkmcnt(8)
s_barrier
Mv 32,32,56
Rt 16,16,4096
Cv 170,172,173
Mv 40,32,60
Rt 18,16,4608
buffer_store_dwordx4 v[160:163], v2, s[20:23], s47 offen offset:0 sc0 sc1
Mv 44,36,60
Rt 20,17,4096
Cv 171,174,175
Cv 176,176,177
Mv 36,36,56
Rt 22,17,4608
s_add_u32 s92, s42, 0x180
Mv 48,40,56
Rt 24,18,4096
s_add_u32 m0, s41, 0
Mv 56,40,60
)" R"(
Rt 26,18,4608
buffer_load_dwordx4 v8, s[16:19], s92 offen offset:0 lds
Mv 60,44,60
Rt 28,19,4096
Cv 177,178,179
Mv 52,44,56
Rt 30,19,4608
Cv 178,180,181
s_waitcnt lgkmcnt(8)
Mz 64,0,192
Rt 32,16,8192
Cv 179,182,183
Mz 72,0,196
Rt 34,16,8704
s_add_u32 s51, s51, 0x10000
buffer_load_dwordx4 a[96:99], v2, s[20:23], s51 offen offset:0 sc0 sc1
Mz 76,4,196
Rt 36,17,8192
Cv 184,184,185
Mz 68,4,192
Rt 38,17,8704
Cv 185,186,187
Mz 80,8,192
Rt 40,18,8192
Cv 186,188,189
Mz 88,8,196
Rt 42,18,8704
buffer_load_dwordx4 v9, s[16:19], s92 offen offset:1024 lds
Mz 92,12,196
Rt 44,19,8192
Cv 187,190,191
v_mul_f32_e32 v32, s54, v32
Mz 84,12,192
Rt 46,19,8704
s_waitcnt lgkmcnt(8)
Mv 64,16,200
Rt 0,16,12288
Cv 192,192,193
Mv 72,16,204
Rt 2,16,12800
buffer_store_dwordx4 v[168:171], v2, s[20:23], s47 offen offset:1024 sc0 sc1
Mv 76,20,204
Rt 4,17,12288
Cv 193,194,195
Cv 194,196,197
Mv 68,20,200
Rt 6,17,12800
Cv 195,198,199
v_mul_f32_e32 v33, s54, v33
Mv 80,24,200
Rt 8,18,12288
Cv 200,200,201
Mv 88,24,204
Rt 10,18,12800
buffer_load_dwordx4 v10, s[16:19], s92 offen offset:2048 lds
Mv 92,28,204
Rt 12,19,12288
Cv 201,202,203
Cv 32,32,33
Mv 84,28,200
Rt 14,19,12800
Cv 202,204,205
s_waitcnt lgkmcnt(8)
Mv 64,32,208
)" R"(
Rt 16,16,16384
Cv 203,206,207
Mv 72,32,212
Rt 18,16,16896
buffer_load_dwordx4 a[100:103], v2, s[20:23], s51 offen offset:1024 sc0 sc1
Mv 76,36,212
Rt 20,17,16384
Cv 208,208,209
Mv 68,36,208
Rt 22,17,16896
Cv 209,210,211
v_accvgpr_write_b32 a224, v32
Mv 80,40,208
Rt 24,18,16384
Cv 210,212,213
Mv 88,40,212
Rt 26,18,16896
buffer_load_dwordx4 v11, s[16:19], s92 offen offset:3072 lds
Mv 92,44,212
Rt 28,19,16384
Cv 211,214,215
v_mul_f32_e32 v34, s54, v34
Mv 84,44,208
Rt 30,19,16896
s_waitcnt lgkmcnt(8)
Mv 64,0,216
Rt 32,16,20480
Cv 216,216,217
Mv 72,0,220
Rt 34,16,20992
buffer_store_dwordx4 v[176:179], v2, s[20:23], s47 offen offset:2048 sc0 sc1
Mv 76,4,220
Rt 36,17,20480
Cv 217,218,219
Cv 218,220,221
Mv 68,4,216
Rt 38,17,20992
s_add_u32 s92, s42, 0x40180
v_mul_f32_e32 v35, s54, v35
Mv 80,8,216
Rt 40,18,20480
s_add_u32 m0, s41, 4096
Mv 88,8,220
Rt 42,18,20992
buffer_load_dwordx4 v8, s[16:19], s92 offen offset:0 lds
Mv 92,12,220
Rt 44,19,20480
Cv 219,222,223
Cv 34,34,35
Mv 84,12,216
Rt 46,19,20992
s_waitcnt lgkmcnt(8)
Mv 64,16,128
Rt 0,16,24576
v_accvgpr_write_b32 a225, v34
Mv 72,16,132
Rt 2,16,25088
buffer_load_dwordx4 a[104:107], v2, s[20:23], s51 offen offset:2048 sc0 sc1
Mv 76,20,132
Rt 4,17,24576
v_mul_f32_e32 v36, s54, v36
Mv 68,20,128
Rt 6,17,25088
v_mul_f32_e32 v37, s54, v37
)" R"(
Cv 36,36,37
Mv 80,24,128
Rt 8,18,24576
v_accvgpr_write_b32 a226, v36
Mv 88,24,132
Rt 10,18,25088
buffer_load_dwordx4 v9, s[16:19], s92 offen offset:1024 lds
Mv 92,28,132
Rt 12,19,24576
v_mul_f32_e32 v38, s54, v38
v_mul_f32_e32 v39, s54, v39
Mv 84,28,128
Rt 14,19,25088
s_waitcnt lgkmcnt(8)
Mv 64,32,136
Rt 16,12,32768
buffer_store_dwordx4 v[184:187], v2, s[20:23], s47 offen offset:3072 sc0 sc1
Mv 72,32,140
Rt 18,12,33280
Cv 38,38,39
v_accvgpr_write_b32 a227, v38
Mv 76,36,140
Rt 20,13,32768
v_mul_f32_e32 v40, s54, v40
Mv 68,36,136
Rt 22,13,33280
buffer_load_dwordx4 a[108:111], v2, s[20:23], s51 offen offset:3072 sc0 sc1
Mv 80,40,136
Rt 24,14,32768
v_mul_f32_e32 v41, s54, v41
Mv 88,40,140
Rt 26,14,33280
buffer_load_dwordx4 v10, s[16:19], s92 offen offset:2048 lds
Mv 92,44,140
Rt 28,15,32768
Cv 40,40,41
v_accvgpr_write_b32 a228, v40
Mv 84,44,136
Rt 30,15,33280
s_waitcnt vmcnt(19) lgkmcnt(8)
s_barrier
buffer_store_dword v6, v7, s[20:23], s49 offen offset:1024 sc0 sc1
Mv 64,0,144
Rt 32,12,36864
buffer_load_dword v4, v5, s[20:23], s50 offen offset:0 sc0 sc1
Mv 72,0,148
Rt 34,12,37376
v_mul_f32_e32 v42, s54, v42
v_mul_f32_e32 v43, s54, v43
Mv 76,4,148
Rt 36,13,36864
Cv 42,42,43
Mv 68,4,144
Rt 38,13,37376
s_add_u32 s92, s42, 0x1c0180
v_accvgpr_write_b32 a229, v42
Mv 80,8,144
Rt 40,14,36864
s_add_u32 m0, s41, 65536
.LzH35:
s_cbranch_execz .LzH43
Mv 88,8,148
Rt 42,14,37376
buffer_load_dwordx4 v8, s[16:19], s92 offen offset:0 lds
Mv 92,12,148
)" R"(
Rt 44,15,36864
v_mul_f32_e32 v44, s54, v44
v_mul_f32_e32 v45, s54, v45
Mv 84,12,144
Rt 46,15,37376
s_waitcnt lgkmcnt(8)
Mv 64,16,152
Rt 0,12,40960
Cv 44,44,45
Mv 72,16,156
Rt 2,12,41472
buffer_load_dwordx4 a[112:115], v3, s[20:23], s51 offen offset:0 sc0 sc1
Mv 76,20,156
Rt 4,13,40960
v_accvgpr_write_b32 a230, v44
Mv 68,20,152
Rt 6,13,41472
v_mul_f32_e32 v46, s54, v46
v_mul_f32_e32 v47, s54, v47
Mv 80,24,152
Rt 8,14,40960
Cv 46,46,47
Mv 88,24,156
Rt 10,14,41472
buffer_load_dwordx4 v9, s[16:19], s92 offen offset:1024 lds
Mv 92,28,156
Rt 12,15,40960
v_accvgpr_write_b32 a231, v46
v_mul_f32_e32 v48, s54, v48
Mv 84,28,152
Rt 14,15,41472
s_waitcnt lgkmcnt(8)
Mv 64,32,160
Rt 16,12,45056
v_mul_f32_e32 v49, s54, v49
Mv 72,32,164
Rt 18,12,45568
buffer_store_dwordx4 v[192:195], v3, s[20:23], s47 offen offset:0 sc0 sc1
Mv 76,36,164
Rt 20,13,45056
Cv 48,48,49
Mv 68,36,160
Rt 22,13,45568
v_accvgpr_write_b32 a232, v48
v_mul_f32_e32 v50, s54, v50
Mv 80,40,160
Rt 24,14,45056
v_mul_f32_e32 v51, s54, v51
Mv 88,40,164
Rt 26,14,45568
buffer_load_dwordx4 v10, s[16:19], s92 offen offset:2048 lds
Mv 92,44,164
Rt 28,15,45056
Cv 50,50,51
v_accvgpr_write_b32 a233, v50
Mv 84,44,160
Rt 30,15,45568
s_waitcnt lgkmcnt(8)
Mv 64,0,168
Rt 32,12,49152
v_mul_f32_e32 v52, s54, v52
Mv 72,0,172
Rt 34,12,49664
buffer_load_dwordx4 a[116:119], v3, s[20:23], s51 offen offset:1024 sc0 sc1
)" R"(
Mv 76,4,172
Rt 36,13,49152
v_mul_f32_e32 v53, s54, v53
Mv 68,4,168
Rt 38,13,49664
Cv 52,52,53
v_accvgpr_write_b32 a234, v52
Mv 80,8,168
Rt 40,14,49152
v_mul_f32_e32 v54, s54, v54
Mv 88,8,172
Rt 42,14,49664
buffer_load_dwordx4 v11, s[16:19], s92 offen offset:3072 lds
Mv 92,12,172
Rt 44,15,49152
v_mul_f32_e32 v55, s54, v55
Cv 54,54,55
Mv 84,12,168
Rt 46,15,49664
s_waitcnt lgkmcnt(8)
Mv 64,16,176
Rt 0,12,53248
v_accvgpr_write_b32 a235, v54
Mv 72,16,180
Rt 2,12,53760
buffer_store_dwordx4 v[200:203], v3, s[20:23], s47 offen offset:1024 sc0 sc1
Mv 76,20,180
Rt 4,13,53248
v_mul_f32_e32 v56, s54, v56
Mv 68,20,176
Rt 6,13,53760
s_add_u32 s92, s42, 0x200180
v_mul_f32_e32 v57, s54, v57
Mv 80,24,176
Rt 8,14,53248
s_add_u32 m0, s41, 69632
Mv 88,24,180
Rt 10,14,53760
buffer_load_dwordx4 v8, s[16:19], s92 offen offset:0 lds
Mv 92,28,180
Rt 12,15,53248
Cv 56,56,57
v_accvgpr_write_b32 a236, v56
Mv 84,28,176
Rt 14,15,53760
s_waitcnt lgkmcnt(8)
Mv 64,32,184
Rt 16,12,57344
v_mul_f32_e32 v58, s54, v58
Mv 72,32,188
Rt 18,12,57856
buffer_load_dwordx4 a[120:123], v3, s[20:23], s51 offen offset:2048 sc0 sc1
Mv 76,36,188
Rt 20,13,57344
v_mul_f32_e32 v59, s54, v59
Mv 68,36,184
Rt 22,13,57856
Cv 58,58,59
v_accvgpr_write_b32 a237, v58
Mv 80,40,184
Rt 24,14,57344
v_mul_f32_e32 v60, s54, v60
Mv 88,40,188
Rt 26,14,57856
)" R"(
buffer_load_dwordx4 v9, s[16:19], s92 offen offset:1024 lds
Mv 92,44,188
Rt 28,15,57344
v_mul_f32_e32 v61, s54, v61
Cv 60,60,61
Mv 84,44,184
Rt 30,15,57856
s_waitcnt lgkmcnt(8)
Mv 64,0,48
Rt 32,16,32768
buffer_store_dwordx4 v[208:211], v3, s[20:23], s47 offen offset:2048 sc0 sc1
Mv 72,0,52
Rt 34,16,33280
v_accvgpr_write_b32 a238, v60
v_mul_f32_e32 v62, s54, v62
Mv 76,4,52
Rt 36,17,32768
v_mul_f32_e32 v63, s54, v63
Mv 68,4,48
Rt 38,17,33280
buffer_load_dwordx4 a[124:127], v3, s[20:23], s51 offen offset:3072 sc0 sc1
Mv 80,8,48
Rt 40,18,32768
Cv 62,62,63
Mv 88,8,52
Rt 42,18,33280
buffer_load_dwordx4 v10, s[16:19], s92 offen offset:2048 lds
Mv 92,12,52
Rt 44,19,32768
v_accvgpr_write_b32 a239, v62
Mv 84,12,48
Rt 46,19,33280
s_waitcnt vmcnt(14) lgkmcnt(8)
s_barrier
Mv 64,16,56
Rt 0,16,36864
v_cmp_eq_u32_e32 vcc, s3, v4
s_andn2_b64 s[28:29], -1, vcc
s_cbranch_scc1 .Lzsp5
.Lzok5:
Mv 72,16,60
Rt 2,16,37376
s_add_u32 s40, s55, 0x10000
Mv 76,20,60
Rt 4,17,36864
Mv 68,20,56
Rt 6,17,37376
Mv 80,24,56
Rt 8,18,36864
s_add_u32 m0, s36, 32768
Mv 88,24,60
Rt 10,18,37376
buffer_load_dwordx4 v2, s[20:23], s40 offen offset:0 sc1 lds
Mv 92,28,60
Rt 12,19,36864
Mv 84,28,56
Rt 14,19,37376
s_waitcnt lgkmcnt(8)
Mz 32,32,192
Rt 16,16,40960
buffer_store_dwordx4 v[216:219], v3, s[20:23], s47 offen offset:3072 sc0 sc1
Mz 40,32,196
Rt 18,16,41472
Mz 44,36,196
Rt 20,17,40960
)" R"(
Mz 36,36,192
Rt 22,17,41472
buffer_load_dwordx4 v2, s[20:23], s40 offen offset:1024 sc1 lds
Mz 48,40,192
Rt 24,18,40960
v_mul_f32_e32 v64, s54, v64
Mz 56,40,196
Rt 26,18,41472
v_mul_f32_e32 v65, s54, v65
Mz 60,44,196
Rt 28,19,40960
Cv 64,64,65
Mz 52,44,192
Rt 30,19,41472
s_waitcnt lgkmcnt(8)
Mv 32,0,200
Rt 32,16,45056
v_accvgpr_write_b32 a240, v64
Mv 40,0,204
Rt 34,16,45568
buffer_load_dwordx4 v2, s[20:23], s40 offen offset:2048 sc1 lds
Mv 44,4,204
Rt 36,17,45056
v_mul_f32_e32 v66, s54, v66
Mv 36,4,200
Rt 38,17,45568
v_mul_f32_e32 v67, s54, v67
Mv 48,8,200
Rt 40,18,45056
buffer_store_dwordx4 v[224:227], v2, s[20:23], s48 offen offset:0 sc0 sc1
Mv 56,8,204
Rt 42,18,45568
Cv 66,66,67
Mv 60,12,204
Rt 44,19,45056
v_accvgpr_write_b32 a241, v66
Mv 52,12,200
Rt 46,19,45568
buffer_load_dwordx4 v2, s[20:23], s40 offen offset:3072 sc1 lds
s_waitcnt lgkmcnt(8)
Mv 32,16,208
Rt 0,16,49152
v_mul_f32_e32 v68, s54, v68
Mv 40,16,212
Rt 2,16,49664
v_mul_f32_e32 v69, s54, v69
Mv 44,20,212
Rt 4,17,49152
Cv 68,68,69
Mv 36,20,208
Rt 6,17,49664
v_accvgpr_write_b32 a242, v68
Mv 48,24,208
Rt 8,18,49152
s_add_u32 m0, s36, 36864
Mv 56,24,212
Rt 10,18,49664
buffer_load_dwordx4 v3, s[20:23], s40 offen offset:0 sc1 lds
Mv 60,28,212
Rt 12,19,49152
v_mul_f32_e32 v70, s54, v70
Mv 52,28,208
Rt 14,19,49664
s_waitcnt lgkmcnt(8)
)" R"(
Mv 32,32,216
Rt 16,16,53248
buffer_store_dwordx4 v[232:235], v2, s[20:23], s48 offen offset:1024 sc0 sc1
Mv 40,32,220
Rt 18,16,53760
v_mul_f32_e32 v71, s54, v71
Mv 44,36,220
Rt 20,17,53248
Cv 70,70,71
Mv 36,36,216
Rt 22,17,53760
buffer_load_dwordx4 v3, s[20:23], s40 offen offset:1024 sc1 lds
Mv 48,40,216
Rt 24,18,53248
v_accvgpr_write_b32 a243, v70
Mv 56,40,220
Rt 26,18,53760
v_mul_f32_e32 v72, s54, v72
Mv 60,44,220
Rt 28,19,53248
v_mul_f32_e32 v73, s54, v73
Mv 52,44,216
Rt 30,19,53760
s_waitcnt lgkmcnt(8)
Mv 32,0,128
Rt 32,16,57344
Cv 72,72,73
Mv 40,0,132
Rt 34,16,57856
buffer_load_dwordx4 v3, s[20:23], s40 offen offset:2048 sc1 lds
Mv 44,4,132
Rt 36,17,57344
v_accvgpr_write_b32 a244, v72
Mv 36,4,128
Rt 38,17,57856
v_mul_f32_e32 v74, s54, v74
Mv 48,8,128
Rt 40,18,57344
buffer_store_dwordx4 v[240:243], v2, s[20:23], s48 offen offset:2048 sc0 sc1
Mv 56,8,132
Rt 42,18,57856
v_mul_f32_e32 v75, s54, v75
Mv 60,12,132
Rt 44,19,57344
Cv 74,74,75
Mv 52,12,128
Rt 46,19,57856
buffer_load_dwordx4 v3, s[20:23], s40 offen offset:3072 sc1 lds
s_waitcnt lgkmcnt(8)
Mv 32,16,136
Rt 0,20,0
v_accvgpr_write_b32 a245, v74
Mv 40,16,140
Rt 2,20,512
v_mul_f32_e32 v76, s54, v76
Mv 44,20,140
Rt 4,21,0
v_mul_f32_e32 v77, s54, v77
Mv 36,20,136
Rt 6,21,512
Cv 76,76,77
Mv 48,24,136
Rt 8,22,0
v_accvgpr_write_b32 a246, v76
)" R"(
Mv 56,24,140
Rt 10,22,512
v_mul_f32_e32 v78, s54, v78
Mv 60,28,140
Rt 12,23,0
v_mul_f32_e32 v79, s54, v79
Mv 52,28,136
Rt 14,23,512
s_waitcnt vmcnt(10) lgkmcnt(8)
s_barrier
buffer_store_dword v6, v7, s[20:23], s49 offen offset:2048 sc0 sc1
Mv 32,32,144
Rt 16,20,4096
s_mov_b32 s83, 0x1508000
buffer_load_dwordx2 v[24:25], v7, s[20:23], s83 offen sc0 sc1
Mv 40,32,148
Rt 18,20,4608
Cv 78,78,79
Mv 44,36,148
Rt 20,21,4096
s_add_u32 m0, s36, 98304
Mv 36,36,144
Rt 22,21,4608
buffer_load_dwordx4 v2, s[20:23], s55 offen offset:0 sc1 lds
Mv 48,40,144
Rt 24,22,4096
v_accvgpr_write_b32 a247, v78
Mv 56,40,148
Rt 26,22,4608
v_mul_f32_e32 v80, s54, v80
Mv 60,44,148
Rt 28,23,4096
buffer_store_dwordx4 v[248:251], v2, s[20:23], s48 offen offset:3072 sc0 sc1
Mv 52,44,144
Rt 30,23,4608
s_waitcnt lgkmcnt(8)
Mv 32,0,152
Rt 32,20,8192
v_mul_f32_e32 v81, s54, v81
Mv 40,0,156
Rt 34,20,8704
buffer_load_dwordx4 v2, s[20:23], s55 offen offset:1024 sc1 lds
Mv 44,4,156
Rt 36,21,8192
Cv 80,80,81
Mv 36,4,152
Rt 38,21,8704
v_accvgpr_write_b32 a248, v80
Mv 48,8,152
Rt 40,22,8192
v_mul_f32_e32 v82, s54, v82
Mv 56,8,156
Rt 42,22,8704
v_mul_f32_e32 v83, s54, v83
Mv 60,12,156
Rt 44,23,8192
Cv 82,82,83
Mv 52,12,152
Rt 46,23,8704
buffer_load_dwordx4 v2, s[20:23], s55 offen offset:2048 sc1 lds
s_waitcnt lgkmcnt(8)
Mv 32,16,160
Rt 0,20,12288
v_accvgpr_write_b32 a249, v82
Mv 40,16,164
)" R"(
Rt 2,20,12800
v_mul_f32_e32 v84, s54, v84
Mv 44,20,164
Rt 4,21,12288
buffer_store_dwordx4 v[228:231], v3, s[20:23], s48 offen offset:0 sc0 sc1
Mv 36,20,160
Rt 6,21,12800
v_mul_f32_e32 v85, s54, v85
Mv 48,24,160
Rt 8,22,12288
Cv 84,84,85
Mv 56,24,164
Rt 10,22,12800
buffer_load_dwordx4 v2, s[20:23], s55 offen offset:3072 sc1 lds
Mv 60,28,164
Rt 12,23,12288
v_accvgpr_write_b32 a250, v84
Mv 52,28,160
Rt 14,23,12800
s_waitcnt lgkmcnt(8)
Mv 32,32,168
Rt 16,20,16384
v_mul_f32_e32 v86, s54, v86
Mv 40,32,172
Rt 18,20,16896
v_mul_f32_e32 v87, s54, v87
Mv 44,36,172
Rt 20,21,16384
s_add_u32 m0, s36, 102400
Mv 36,36,168
Rt 22,21,16896
buffer_load_dwordx4 v3, s[20:23], s55 offen offset:0 sc1 lds
Mv 48,40,168
Rt 24,22,16384
Cv 86,86,87
Mv 56,40,172
Rt 26,22,16896
v_accvgpr_write_b32 a251, v86
Mv 60,44,172
Rt 28,23,16384
buffer_store_dwordx4 v[236:239], v3, s[20:23], s48 offen offset:1024 sc0 sc1
Mv 52,44,168
Rt 30,23,16896
s_waitcnt lgkmcnt(8)
Mv 32,0,176
Rt 32,20,20480
v_mul_f32_e32 v88, s54, v88
Mv 40,0,180
Rt 34,20,20992
buffer_load_dwordx4 v3, s[20:23], s55 offen offset:1024 sc1 lds
Mv 44,4,180
Rt 36,21,20480
v_mul_f32_e32 v89, s54, v89
Mv 36,4,176
Rt 38,21,20992
Cv 88,88,89
Mv 48,8,176
Rt 40,22,20480
s_add_u32 s50, s50, 1024
Mv 56,8,180
Rt 42,22,20992
s_min_u32 s50, s50, s60
Mv 60,12,180
Rt 44,23,20480
)" R"(
v_accvgpr_write_b32 a252, v88
Mv 52,12,176
Rt 46,23,20992
buffer_load_dwordx4 v3, s[20:23], s55 offen offset:2048 sc1 lds
s_waitcnt lgkmcnt(8)
Mv 32,16,184
Rt 0,20,24576
v_mul_f32_e32 v90, s54, v90
Mv 40,16,188
Rt 2,20,25088
v_mul_f32_e32 v91, s54, v91
Mv 44,20,188
Rt 4,21,24576
buffer_store_dwordx4 v[244:247], v3, s[20:23], s48 offen offset:2048 sc0 sc1
Mv 36,20,184
Rt 6,21,25088
Cv 90,90,91
Mv 48,24,184
Rt 8,22,24576
v_accvgpr_write_b32 a253, v90
Mv 56,24,188
Rt 10,22,25088
buffer_load_dwordx4 v3, s[20:23], s55 offen offset:3072 sc1 lds
Mv 60,28,188
Rt 12,23,24576
v_mul_f32_e32 v92, s54, v92
Mv 52,28,184
Rt 14,23,25088
s_waitcnt lgkmcnt(8)
Mv 32,32,48
Rt 16,12,0
v_mul_f32_e32 v93, s54, v93
Mv 40,32,52
Rt 18,12,512
Cv 92,92,93
Mv 44,36,52
Rt 20,13,0
v_accvgpr_write_b32 a254, v92
Mv 36,36,48
Rt 22,13,512
v_mul_f32_e32 v94, s54, v94
Mv 48,40,48
Rt 24,14,0
buffer_store_dwordx4 v[252:255], v3, s[20:23], s48 offen offset:3072 sc0 sc1
Mv 56,40,52
Rt 26,14,512
v_mul_f32_e32 v95, s54, v95
Mv 60,44,52
Rt 28,15,0
Cv 94,94,95
Mv 52,44,48
Rt 30,15,512
s_waitcnt vmcnt(13) lgkmcnt(8)
s_barrier
v_cmp_eq_u32_e32 vcc, s3, v25
s_andn2_b64 s[28:29], 1, vcc
s_cbranch_scc1 .Lzsp7
.Lzok7:
Mv 32,0,56
Rt 32,12,4096
buffer_load_dword v4, v5, s[20:23], s50 offen offset:0 sc0 sc1
Mv 40,0,60
Rt 34,12,4608
v_accvgpr_write_b32 a255, v94
Mv 44,4,60
)" R"(
Rt 36,13,4096
s_add_u32 m0, s36, 131072
Mv 36,4,56
Rt 38,13,4608
buffer_load_dwordx4 v2, s[20:23], s56 offen offset:0 sc1 lds
Mv 48,8,56
Rt 40,14,4096
Mv 56,8,60
Rt 42,14,4608
Mv 60,12,60
Rt 44,15,4096
Mv 52,12,56
Rt 46,15,4608
s_waitcnt lgkmcnt(8)
Mz 96,16,192
Rt 0,12,8192
Mz 104,16,196
Rt 2,12,8704
buffer_load_dwordx4 v2, s[20:23], s56 offen offset:1024 sc1 lds
Mz 108,20,196
Rt 4,13,8192
Mz 100,20,192
Rt 6,13,8704
Mz 112,24,192
Rt 8,14,8192
v_mul_f32_e32 v32, s54, v32
Mz 120,24,196
Rt 10,14,8704
v_mul_f32_e32 v33, s54, v33
Mz 124,28,196
Rt 12,15,8192
Cv 32,32,33
Mz 116,28,192
Rt 14,15,8704
buffer_load_dwordx4 v2, s[20:23], s56 offen offset:2048 sc1 lds
s_waitcnt lgkmcnt(8)
.LzH36:
s_cbranch_execz .LzH15
Mv 96,32,200
Rt 16,12,12288
v_mul_f32_e32 v34, s54, v34
Mv 104,32,204
Rt 18,12,12800
v_accvgpr_write_b32 a192, v32
Mv 108,36,204
Rt 20,13,12288
v_mul_f32_e32 v35, s54, v35
Mv 100,36,200
Rt 22,13,12800
Cv 34,34,35
v_accvgpr_write_b32 a193, v34
Mv 112,40,200
Rt 24,14,12288
v_mul_f32_e32 v36, s54, v36
Mv 120,40,204
Rt 26,14,12800
buffer_load_dwordx4 v2, s[20:23], s56 offen offset:3072 sc1 lds
Mv 124,44,204
Rt 28,15,12288
v_mul_f32_e32 v37, s54, v37
Mv 116,44,200
Rt 30,15,12800
s_waitcnt lgkmcnt(8)
Mv 96,0,208
)" R"(
Rt 32,12,16384
Cv 36,36,37
Mv 104,0,212
Rt 34,12,16896
v_accvgpr_write_b32 a194, v36
Mv 108,4,212
Rt 36,13,16384
s_add_u32 m0, s36, 135168
Mv 100,4,208
Rt 38,13,16896
buffer_load_dwordx4 v3, s[20:23], s56 offen offset:0 sc1 lds
Mv 112,8,208
Rt 40,14,16384
v_mul_f32_e32 v38, s54, v38
Mv 120,8,212
Rt 42,14,16896
v_mul_f32_e32 v39, s54, v39
Mv 124,12,212
Rt 44,15,16384
Cv 38,38,39
Mv 116,12,208
Rt 46,15,16896
s_waitcnt lgkmcnt(8)
Mv 96,16,216
Rt 0,12,20480
v_accvgpr_write_b32 a195, v38
Mv 104,16,220
Rt 2,12,20992
buffer_load_dwordx4 v3, s[20:23], s56 offen offset:1024 sc1 lds
Mv 108,20,220
Rt 4,13,20480
v_mul_f32_e32 v40, s54, v40
Mv 100,20,216
Rt 6,13,20992
v_mul_f32_e32 v41, s54, v41
Cv 40,40,41
Mv 112,24,216
Rt 8,14,20480
v_accvgpr_write_b32 a196, v40
Mv 120,24,220
Rt 10,14,20992
v_mul_f32_e32 v42, s54, v42
Mv 124,28,220
Rt 12,15,20480
v_mul_f32_e32 v43, s54, v43
Mv 116,28,216
Rt 14,15,20992
buffer_load_dwordx4 v3, s[20:23], s56 offen offset:2048 sc1 lds
s_waitcnt lgkmcnt(8)
Mv 96,32,128
Rt 16,12,24576
Cv 42,42,43
Mv 104,32,132
Rt 18,12,25088
v_accvgpr_write_b32 a197, v42
Mv 108,36,132
Rt 20,13,24576
v_mul_f32_e32 v44, s54, v44
Mv 100,36,128
Rt 22,13,25088
v_mul_f32_e32 v45, s54, v45
Cv 44,44,45
Mv 112,40,128
Rt 24,14,24576
)" R"(
v_accvgpr_write_b32 a198, v44
Mv 120,40,132
Rt 26,14,25088
buffer_load_dwordx4 v3, s[20:23], s56 offen offset:3072 sc1 lds
Mv 124,44,132
Rt 28,15,24576
v_mul_f32_e32 v46, s54, v46
Mv 116,44,128
Rt 30,15,25088
s_waitcnt lgkmcnt(8)
Mv 96,0,136
Rt 32,16,0
v_readfirstlane_b32 s63, v24
Mv 104,0,140
Rt 34,16,512
v_and_b32 v24, 16, v1
Mv 108,4,140
Rt 36,17,0
v_lshrrev_b32_e64 v24, v24, s63
Mv 100,4,136
Rt 38,17,512
v_and_b32 v24, 0x7ff, v24
v_mul_u32_u24 v24, 896, v24
Mv 112,8,136
Rt 40,18,0
v_and_b32 v25, 14, v0
Mv 120,8,140
Rt 42,18,512
v_min_u32 v25, 1, v25
Mv 124,12,140
Rt 44,19,0
v_lshlrev_b32 v25, 30, v25
v_and_or_b32 v25, v0, 48, v25
Mv 116,12,136
Rt 46,19,512
s_waitcnt vmcnt(8) lgkmcnt(8)
s_barrier
buffer_store_dword v6, v7, s[20:23], s49 offen offset:3072 sc0 sc1
Mv 96,16,144
Rt 0,16,4096
v_cmp_eq_u32_e32 vcc, s3, v4
s_andn2_b64 s[28:29], -1, vcc
s_cbranch_scc1 .Lzsp6
.Lzok6:
Mv 104,16,148
Rt 2,16,4608
v_add_u32 v24, v24, v25
v_add_u32 v8, 0x10000, v1
Mv 108,20,148
Rt 4,17,4096
s_and_b32 s83, s33, 1
v_add_u32 v10, 0x20000, v1
Mv 100,20,144
Rt 6,17,4608
s_mul_i32 s82, s83, 448
v_mul_f32_e32 v47, s54, v47
s_add_u32 s56, s56, 0x10000
Mv 112,24,144
Rt 8,18,4096
Cv 46,46,47
s_add_u32 s55, s55, 0x10000
Mv 120,24,148
Rt 10,18,4608
v_accvgpr_write_b32 a199, v46
s_add_u32 s55, s55, 0x10000
)" R"(
Mv 124,28,148
Rt 12,19,4096
v_mul_f32_e32 v48, s54, v48
s_cmp_eq_u32 s57, 2
s_cselect_b32 s55, s59, s55
Mv 116,28,144
Rt 14,19,4608
s_mov_b32 s93, 0
s_waitcnt lgkmcnt(8)
Mv 96,32,152
Rt 16,16,8192
v_mul_f32_e32 v49, s54, v49
s_mov_b32 s94, 1
Mv 104,32,156
Rt 18,16,8704
Cv 48,48,49
Mv 108,36,156
Rt 20,17,8192
v_accvgpr_write_b32 a200, v48
Mv 100,36,152
Rt 22,17,8704
v_mul_f32_e32 v50, s54, v50
v_mul_f32_e32 v51, s54, v51
Mv 112,40,152
Rt 24,18,8192
Cv 50,50,51
Mv 120,40,156
Rt 26,18,8704
v_accvgpr_write_b32 a201, v50
Mv 124,44,156
Rt 28,19,8192
v_mul_f32_e32 v52, s54, v52
Mv 116,44,152
Rt 30,19,8704
s_waitcnt lgkmcnt(8)
Mv 96,0,160
Rt 32,16,12288
v_mul_f32_e32 v53, s54, v53
Mv 104,0,164
Rt 34,16,12800
Cv 52,52,53
Mv 108,4,164
Rt 36,17,12288
v_accvgpr_write_b32 a202, v52
Mv 100,4,160
Rt 38,17,12800
buffer_load_dwordx4 a[128:131], v24, s[12:15], s82 offen offset:0
Mv 112,8,160
Rt 40,18,12288
v_mul_f32_e32 v54, s54, v54
Mv 120,8,164
Rt 42,18,12800
v_mul_f32_e32 v55, s54, v55
Mv 124,12,164
Rt 44,19,12288
Cv 54,54,55
Mv 116,12,160
Rt 46,19,12800
s_waitcnt lgkmcnt(8)
Mv 96,16,168
Rt 0,16,16384
buffer_load_dwordx4 a[132:135], v24, s[12:15], s82 offen offset:64
Mv 104,16,172
Rt 2,16,16896
)" R"(
v_accvgpr_write_b32 a203, v54
Mv 108,20,172
Rt 4,17,16384
v_mul_f32_e32 v56, s54, v56
Mv 100,20,168
Rt 6,17,16896
v_mul_f32_e32 v57, s54, v57
Cv 56,56,57
Mv 112,24,168
Rt 8,18,16384
v_accvgpr_write_b32 a204, v56
Mv 120,24,172
Rt 10,18,16896
buffer_load_dwordx4 a[136:139], v24, s[12:15], s82 offen offset:128
Mv 124,28,172
Rt 12,19,16384
v_mul_f32_e32 v58, s54, v58
Mv 116,28,168
Rt 14,19,16896
s_waitcnt lgkmcnt(8)
Mv 96,32,176
Rt 16,16,20480
buffer_load_dwordx4 a[140:143], v24, s[12:15], s82 offen offset:192
Mv 104,32,180
Rt 18,16,20992
v_mul_f32_e32 v59, s54, v59
Mv 108,36,180
Rt 20,17,20480
Cv 58,58,59
Mv 100,36,176
Rt 22,17,20992
v_accvgpr_write_b32 a205, v58
v_mul_f32_e32 v60, s54, v60
Mv 112,40,176
Rt 24,18,20480
v_mul_f32_e32 v61, s54, v61
Mv 120,40,180
Rt 26,18,20992
buffer_load_dwordx4 a[144:147], v24, s[12:15], s82 offen offset:256
Mv 124,44,180
Rt 28,19,20480
Cv 60,60,61
Mv 116,44,176
Rt 30,19,20992
s_waitcnt lgkmcnt(8)
s_barrier
Mv 96,0,184
Rt 32,16,24576
buffer_load_dwordx4 a[148:151], v24, s[12:15], s82 offen offset:320
Mv 104,0,188
Rt 34,16,25088
v_accvgpr_write_b32 a206, v60
Mv 108,4,188
Rt 36,17,24576
v_mul_f32_e32 v62, s54, v62
Mv 100,4,184
Rt 38,17,25088
v_mul_f32_e32 v63, s54, v63
Cv 62,62,63
Mv 112,8,184
Rt 40,18,24576
v_accvgpr_write_b32 a207, v62
Mv 120,8,188
Rt 42,18,25088
buffer_load_dwordx4 a[152:155], v24, s[12:15], s82 offen offset:384
)" R"(
Mv 124,12,188
Rt 44,19,24576
Mv 116,12,184
Rt 46,19,25088
s_waitcnt lgkmcnt(8)
Mv 96,16,48
Rb 0,8,32768
Mv 104,16,52
Rb 4,8,33792
Mv 108,20,52
Rb 8,8,34816
Mv 100,20,48
Rb 12,8,35840
Mv 112,24,48
Rb 16,8,40960
Rb 20,8,41984
Mv 120,24,52
Rb 24,8,43008
Mv 124,28,52
Mv 116,28,48
Rb 28,8,44032
s_waitcnt lgkmcnt(8)
Mv 96,32,56
Mv 104,32,60
Rb 32,8,36864
Mv 108,36,60
Mv 100,36,56
Rb 36,8,37888
Mv 112,40,56
Mv 120,40,60
Rb 40,8,38912
Mv 124,44,60
Mv 116,44,56
Rb 44,8,39936
s_cmp_eq_u32 s35, 7
s_cbranch_scc1 .LzA7
.LzA7R:
s_waitcnt lgkmcnt(8)
Mz 64,0,64
Rb 48,8,45056
v_mul_f32_e32 v96, s54, v96
Mz 68,0,68
v_mul_f32_e32 v97, s54, v97
Cv 96,96,97
Mv 68,4,76
Rb 52,8,46080
s_add_u32 m0, s36, 0
Mv 64,4,72
buffer_load_dwordx4 v2, s[20:23], s55 offen offset:0 sc1 lds
Mv 64,8,80
Rb 56,8,47104
v_accvgpr_write_b32 a208, v96
Mv 68,8,84
v_mul_f32_e32 v98, s54, v98
v_mul_f32_e32 v99, s54, v99
Mv 68,12,92
Rb 60,8,48128
s_waitcnt lgkmcnt(8)
Mv 64,12,88
Cv 98,98,99
v_accvgpr_write_b32 a209, v98
v_mul_f32_e32 v100, s54, v100
Mv 64,16,96
Rb 0,8,49152
)" R"(
v_mul_f32_e32 v101, s54, v101
Mv 68,16,100
Cv 100,100,101
v_accvgpr_write_b32 a210, v100
Mv 68,20,108
Rb 4,8,50176
v_mul_f32_e32 v102, s54, v102
Mv 64,20,104
buffer_load_dwordx4 v2, s[20:23], s55 offen offset:1024 sc1 lds
Mv 64,24,112
Rb 8,8,51200
v_mul_f32_e32 v103, s54, v103
Mv 68,24,116
Cv 102,102,103
v_accvgpr_write_b32 a211, v102
Mv 68,28,124
Rb 12,8,52224
s_waitcnt lgkmcnt(8)
Mv 64,28,120
v_mul_f32_e32 v104, s54, v104
v_mul_f32_e32 v105, s54, v105
Cv 104,104,105
Mz 72,32,64
Rb 16,8,57344
v_accvgpr_write_b32 a212, v104
Mz 76,32,68
v_mul_f32_e32 v106, s54, v106
v_mul_f32_e32 v107, s54, v107
Mv 76,36,76
Rb 20,8,58368
Cv 106,106,107
Mv 72,36,72
buffer_load_dwordx4 v2, s[20:23], s55 offen offset:2048 sc1 lds
Mv 72,40,80
Rb 24,8,59392
v_accvgpr_write_b32 a213, v106
Mv 76,40,84
v_mul_f32_e32 v108, s54, v108
v_mul_f32_e32 v109, s54, v109
Mv 76,44,92
Rb 28,8,60416
s_waitcnt lgkmcnt(8)
Mv 72,44,88
Cv 108,108,109
v_accvgpr_write_b32 a214, v108
v_mul_f32_e32 v110, s54, v110
Mv 72,48,96
Rb 32,8,53248
v_mul_f32_e32 v111, s54, v111
Mv 76,48,100
Cv 110,110,111
v_accvgpr_write_b32 a215, v110
Mv 76,52,108
Rb 36,8,54272
v_mul_f32_e32 v112, s54, v112
Mv 72,52,104
buffer_load_dwordx4 v2, s[20:23], s55 offen offset:3072 sc1 lds
Mv 72,56,112
Rb 40,8,55296
v_mul_f32_e32 v113, s54, v113
Mv 76,56,116
Cv 112,112,113
v_accvgpr_write_b32 a216, v112
Mv 76,60,124
)" R"(
Rb 44,8,56320
s_waitcnt lgkmcnt(8)
Mv 72,60,120
v_mul_f32_e32 v114, s54, v114
v_mul_f32_e32 v115, s54, v115
Cv 114,114,115
Mz 80,0,64
Rb 48,8,61440
v_accvgpr_write_b32 a217, v114
Mz 84,0,68
v_mul_f32_e32 v116, s54, v116
v_mul_f32_e32 v117, s54, v117
Mv 84,4,76
Rb 52,8,62464
s_add_u32 m0, s36, 4096
Mv 80,4,72
buffer_load_dwordx4 v3, s[20:23], s55 offen offset:0 sc1 lds
Mv 80,8,80
Rb 56,8,63488
Cv 116,116,117
Mv 84,8,84
v_accvgpr_write_b32 a218, v116
v_mul_f32_e32 v118, s54, v118
Mv 84,12,92
Rb 60,8,64512
s_waitcnt lgkmcnt(8)
Mv 80,12,88
v_mul_f32_e32 v119, s54, v119
Cv 118,118,119
v_accvgpr_write_b32 a219, v118
Mv 80,16,96
v_mul_f32_e32 v120, s54, v120
v_mul_f32_e32 v121, s54, v121
Mv 84,16,100
Cv 120,120,121
v_accvgpr_write_b32 a220, v120
Mv 84,20,108
v_mul_f32_e32 v122, s54, v122
v_mul_f32_e32 v123, s54, v123
Mv 80,20,104
buffer_load_dwordx4 v3, s[20:23], s55 offen offset:1024 sc1 lds
Mv 80,24,112
s_add_u32 s50, s50, 1024
s_min_u32 s50, s50, s60
Mv 84,24,116
Cv 122,122,123
v_accvgpr_write_b32 a221, v122
Mv 84,28,124
v_mul_f32_e32 v124, s54, v124
v_mul_f32_e32 v125, s54, v125
Mv 80,28,120
Cv 124,124,125
s_waitcnt lgkmcnt(0)
s_barrier
Mz 88,32,64
Rb 0,1,32768
v_accvgpr_write_b32 a222, v124
Mz 92,32,68
Rb 4,1,33792
v_mul_f32_e32 v126, s54, v126
Mv 92,36,76
Rb 8,1,34816
v_mul_f32_e32 v127, s54, v127
Mv 88,36,72
Rb 12,1,35840
)" R"(
buffer_load_dwordx4 v3, s[20:23], s55 offen offset:2048 sc1 lds
Mv 88,40,80
Rb 16,1,40960
Cv 126,126,127
Mv 92,40,84
Rb 20,1,41984
v_accvgpr_write_b32 a223, v126
Mv 92,44,92
Rb 24,1,43008
v_mov_b32 v16, 0
Mv 88,44,88
Rb 28,1,44032
v_mov_b32 v17, 0
v_mov_b32 v18, 0
Mv 88,48,96
Rb 32,1,36864
v_mov_b32 v19, 0
Mv 92,48,100
Rb 36,1,37888
v_mov_b32 v20, 0
Mv 92,52,108
Rb 40,1,38912
v_mov_b32 v21, 0
Mv 88,52,104
Rb 44,1,39936
buffer_load_dwordx4 v3, s[20:23], s55 offen offset:3072 sc1 lds
Mv 88,56,112
v_mov_b32 v22, 0
v_mov_b32 v23, 0
Mv 92,56,116
Mv 92,60,124
s_waitcnt lgkmcnt(8)
Mv 88,60,120
Mz 96,0,64
Rb 48,1,45056
buffer_load_dword v4, v5, s[20:23], s50 offen offset:0 sc0 sc1
Mz 100,0,68
s_cmp_le_i32 s57, s58
s_cbranch_scc1 .Lzmk0
.Lzmr0:
Mv 100,4,76
Rb 52,1,46080
s_add_u32 m0, s36, 65536
s_sub_u32 s57, s57, 1
Mv 96,4,72
buffer_load_dwordx4 v2, s[20:23], s56 offen offset:0 sc1 lds
s_add_u32 s55, s55, 0x10000
Mv 96,8,80
Rb 56,1,47104
s_cmp_eq_u32 s57, 2
s_cselect_b32 s55, s59, s55
Mv 100,8,84
v_exp_f32_e32 v32, v64
Mv 100,12,92
Rb 60,1,48128
s_waitcnt lgkmcnt(8)
Mv 96,12,88
v_exp_f32_e32 v24, v65
Mv 96,16,96
Rb 0,1,49152
v_exp_f32_e32 v33, v66
Mv 100,16,100
v_exp_f32_e32 v25, v67
v_exp_f32_e32 v34, v72
)" R"(
Mv 100,20,108
Rb 4,1,50176
v_exp_f32_e32 v26, v73
Mv 96,20,104
buffer_load_dwordx4 v2, s[20:23], s56 offen offset:1024 sc1 lds
v_add_f32_e32 v14, v32, v24
Mv 96,24,112
Rb 8,1,51200
v_exp_f32_e32 v35, v74
Mv 100,24,116
v_exp_f32_e32 v27, v75
Cv 32,32,24
Mv 100,28,124
Rb 12,1,52224
v_add_f32_e32 v14, v14, v33
s_waitcnt lgkmcnt(8)
Mv 96,28,120
v_exp_f32_e32 v36, v68
v_add_f32_e32 v14, v14, v25
Mz 104,32,64
Rb 16,1,57344
v_exp_f32_e32 v28, v69
Mz 108,32,68
v_exp_f32_e32 v37, v70
v_exp_f32_e32 v29, v71
Mv 108,36,76
Rb 20,1,58368
v_exp_f32_e32 v38, v76
Mv 104,36,72
buffer_load_dwordx4 v2, s[20:23], s56 offen offset:2048 sc1 lds
Cv 33,33,25
Mv 104,40,80
Rb 24,1,59392
v_exp_f32_e32 v24, v77
Mv 108,40,84
v_exp_f32_e32 v39, v78
v_add_f32_e32 v14, v14, v34
Mv 108,44,92
Rb 28,1,60416
v_add_f32_e32 v14, v14, v26
s_waitcnt lgkmcnt(8)
Mv 104,44,88
v_exp_f32_e32 v25, v79
Cv 34,34,26
.LzH37:
s_cbranch_execz .LzH16
Mv 104,48,96
Rb 32,1,53248
v_exp_f32_e32 v40, v80
Mv 108,48,100
v_exp_f32_e32 v26, v81
v_add_f32_e32 v14, v14, v35
v_add_f32_e32 v14, v14, v27
Mv 108,52,108
Rb 36,1,54272
Cv 35,35,27
v_add_f32_e32 v15, v36, v28
Mv 104,52,104
buffer_load_dwordx4 v2, s[20:23], s56 offen offset:3072 sc1 lds
Cv 36,36,28
Mv 104,56,112
Rb 40,1,55296
v_exp_f32_e32 v41, v82
Mv 108,56,116
)" R"(
v_exp_f32_e32 v27, v83
v_add_f32_e32 v15, v15, v37
Mv 108,60,124
Rb 44,1,56320
v_add_f32_e32 v15, v15, v29
s_waitcnt lgkmcnt(8)
Mv 104,60,120
v_exp_f32_e32 v42, v88
Cv 37,37,29
Mz 112,0,64
Rb 48,1,61440
v_exp_f32_e32 v28, v89
Mz 116,0,68
v_exp_f32_e32 v43, v90
v_add_f32_e32 v15, v15, v38
v_add_f32_e32 v15, v15, v24
Mv 116,4,76
Rb 52,1,62464
s_add_u32 m0, s36, 69632
Cv 38,38,24
Mv 112,4,72
buffer_load_dwordx4 v3, s[20:23], s56 offen offset:0 sc1 lds
v_add_f32_e32 v15, v15, v39
Mv 112,8,80
Rb 56,1,63488
v_exp_f32_e32 v29, v91
Mv 116,8,84
v_exp_f32_e32 v44, v84
v_add_f32_e32 v15, v15, v25
Mv 116,12,92
Rb 60,1,64512
Cv 39,39,25
s_waitcnt lgkmcnt(8)
Mv 112,12,88
v_exp_f32_e32 v24, v85
v_add_f32_e32 v14, v14, v40
Mv 112,16,96
v_exp_f32_e32 v45, v86
v_add_f32_e32 v14, v14, v26
Mv 116,16,100
v_exp_f32_e32 v25, v87
Cv 40,40,26
v_add_f32_e32 v14, v14, v41
Mv 116,20,108
v_exp_f32_e32 v46, v92
v_add_f32_e32 v14, v14, v27
Mv 112,20,104
buffer_load_dwordx4 v3, s[20:23], s56 offen offset:1024 sc1 lds
Cv 41,41,27
Mv 112,24,112
v_exp_f32_e32 v26, v93
v_add_f32_e32 v14, v14, v42
Mv 116,24,116
v_exp_f32_e32 v47, v94
v_add_f32_e32 v14, v14, v28
Mv 116,28,124
v_exp_f32_e32 v27, v95
Cv 42,42,28
Mv 112,28,120
v_add_f32_e32 v14, v14, v43
s_waitcnt vmcnt(23) lgkmcnt(0)
s_barrier
Mz 120,32,64
Rb 0,10,0
v_add_f32_e32 v14, v14, v29
)" R"(
Cv 43,43,29
Mz 124,32,68
Rb 4,10,1024
v_add_f32_e32 v15, v15, v44
v_add_f32_e32 v15, v15, v24
Cv 44,44,24
Mv 124,36,76
Rb 8,10,2048
v_add_f32_e32 v15, v15, v45
v_add_f32_e32 v15, v15, v25
Mv 120,36,72
Rb 12,10,3072
buffer_load_dwordx4 v3, s[20:23], s56 offen offset:2048 sc1 lds
Mv 120,40,80
Rb 16,10,4096
Cv 45,45,25
v_add_f32_e32 v15, v15, v46
Mv 124,40,84
Rb 20,10,5120
v_add_f32_e32 v15, v15, v26
Cv 46,46,26
Mv 124,44,92
Rb 24,10,6144
v_add_f32_e32 v15, v15, v47
v_add_f32_e32 v15, v15, v27
Mv 120,44,88
Rb 28,10,7168
Cv 47,47,27
v_max_f32_e32 v11, v14, v15
Mv 120,48,96
Rb 32,10,8192
v_mov_b32 v24, v14
v_mov_b32 v25, v15
Mv 124,48,100
Rb 36,10,9216
v_permlane32_swap_b32_e32 v24, v25
v_add_f32_e32 v24, v24, v25
v_mov_b32 v25, v24
Mv 124,52,108
Rb 40,10,10240
v_permlane16_swap_b32_e32 v24, v25
v_add_f32_e32 v24, v24, v25
Mv 120,52,104
Rb 44,10,11264
buffer_load_dwordx4 v3, s[20:23], s56 offen offset:3072 sc1 lds
Mv 120,56,112
v_rcp_f32_e32 v24, v24
Mv 124,56,116
s_add_u32 s56, s56, 0x10000
v_max_f32_e32 v11, v11, v24
v_cmp_nge_f32_e32 vcc, s62, v11
Mv 124,60,124
s_waitcnt lgkmcnt(8)
s_cbranch_vccnz .Lzfx0
.Lzfr0:
Mv 120,60,120
v_mov_b32 v12, v14
v_mov_b32 v13, v15
v_mfma_f32_16x16x32_bf16 v[128:131], a[0:3], v[32:35], 0
Rb 48,10,12288
v_mfma_f32_16x16x32_bf16 v[136:139], a[0:3], v[36:39], 0
s_cmp_le_i32 s57, s58
s_cbranch_scc1 .Lzmk1
.Lzmr1:
)" R"(
v_mfma_f32_16x16x32_bf16 v[140:143], a[4:7], v[36:39], 0
Rb 52,10,13312
s_add_u32 m0, s36, 32768
v_mfma_f32_16x16x32_bf16 v[132:135], a[4:7], v[32:35], 0
buffer_load_dwordx4 v2, s[20:23], s55 offen offset:0 sc1 lds
v_mfma_f32_16x16x32_bf16 v[144:147], a[8:11], v[32:35], 0
Rb 56,10,14336
s_and_b32 s92, s2, 7
v_mfma_f32_16x16x32_bf16 v[152:155], a[8:11], v[36:39], 0
s_mul_i32 s92, s92, 14
s_lshr_b32 s82, s2, 3
v_mfma_f32_16x16x32_bf16 v[156:159], a[12:15], v[36:39], 0
Rb 60,10,15360
s_waitcnt lgkmcnt(8)
v_mfma_f32_16x16x32_bf16 v[148:151], a[12:15], v[32:35], 0
buffer_load_dwordx4 v2, s[20:23], s55 offen offset:1024 sc1 lds
v_mfma_f32_16x16x32_bf16 v[160:163], a[16:19], v[32:35], 0
Rb 0,10,16384
s_add_u32 s82, s82, s92
v_mfma_f32_16x16x32_bf16 v[168:171], a[16:19], v[36:39], 0
v_exp_f32_e32 v48, v96
v_mfma_f32_16x16x32_bf16 v[172:175], a[20:23], v[36:39], 0
Rb 4,10,17408
v_mfma_f32_16x16x32_bf16 v[164:167], a[20:23], v[32:35], 0
buffer_load_dwordx4 v2, s[20:23], s55 offen offset:2048 sc1 lds
v_mfma_f32_16x16x32_bf16 v[176:179], a[24:27], v[32:35], 0
Rb 8,10,18432
v_exp_f32_e32 v28, v97
v_mfma_f32_16x16x32_bf16 v[184:187], a[24:27], v[36:39], 0
v_exp_f32_e32 v49, v98
v_mfma_f32_16x16x32_bf16 v[188:191], a[28:31], v[36:39], 0
Rb 12,10,19456
s_waitcnt lgkmcnt(8)
v_mfma_f32_16x16x32_bf16 v[180:183], a[28:31], v[32:35], 0
buffer_load_dwordx4 v2, s[20:23], s55 offen offset:3072 sc1 lds
v_add_f32_e32 v14, v48, v28
v_mfma_f32_16x16x32_bf16 v[192:195], a[32:35], v[32:35], 0
Rb 16,10,20480
Cv 48,48,28
v_mfma_f32_16x16x32_bf16 v[200:203], a[32:35], v[36:39], 0
v_exp_f32_e32 v29, v99
v_add_f32_e32 v14, v14, v49
v_mfma_f32_16x16x32_bf16 v[204:207], a[36:39], v[36:39], 0
Rb 20,10,21504
s_add_u32 m0, s36, 36864
v_mfma_f32_16x16x32_bf16 v[196:199], a[36:39], v[32:35], 0
buffer_load_dwordx4 v3, s[20:23], s55 offen offset:0 sc1 lds
v_mfma_f32_16x16x32_bf16 v[208:211], a[40:43], v[32:35], 0
Rb 24,10,22528
v_exp_f32_e32 v50, v104
v_mfma_f32_16x16x32_bf16 v[216:219], a[40:43], v[36:39], 0
s_cmp_gt_u32 s33, 1
s_cselect_b32 s24, s78, s76
s_cselect_b32 s25, s79, s77
v_mfma_f32_16x16x32_bf16 v[220:223], a[44:47], v[36:39], 0
Rb 28,10,23552
s_waitcnt lgkmcnt(8)
v_mfma_f32_16x16x32_bf16 v[212:215], a[44:47], v[32:35], 0
buffer_load_dwordx4 v3, s[20:23], s55 offen offset:1024 sc1 lds
v_add_f32_e32 v14, v14, v29
v_mfma_f32_16x16x32_bf16 v[224:227], a[48:51], v[32:35], 0
Rb 32,10,24576
s_mov_b32 s27, 0x20000
v_mfma_f32_16x16x32_bf16 v[232:235], a[48:51], v[36:39], 0
)" R"(
s_cmp_lt_u32 s35, 7
s_cselect_b32 s26, 0x188000, 0
Cv 49,49,29
v_mfma_f32_16x16x32_bf16 v[236:239], a[52:55], v[36:39], 0
Rb 36,10,25600
s_mul_i32 s83, s83, 802816
v_mfma_f32_16x16x32_bf16 v[228:231], a[52:55], v[32:35], 0
buffer_load_dwordx4 v3, s[20:23], s55 offen offset:2048 sc1 lds
v_mfma_f32_16x16x32_bf16 v[240:243], a[56:59], v[32:35], 0
Rb 40,10,26624
s_lshl_b32 s92, s82, 5
v_add_f32_e32 v14, v14, v50
v_mfma_f32_16x16x32_bf16 v[248:251], a[56:59], v[36:39], 0
s_add_u32 s83, s83, s92
v_and_b32 v9, 62, v0
v_mul_u32_u24 v9, 0x6f0, v9
v_mfma_f32_16x16x32_bf16 v[252:255], a[60:63], v[36:39], 0
Rb 44,10,27648
s_waitcnt lgkmcnt(8)
v_mfma_f32_16x16x32_bf16 v[244:247], a[60:63], v[32:35], 0
buffer_load_dwordx4 v3, s[20:23], s55 offen offset:3072 sc1 lds
Mp 128,0,40
Rb 48,10,28672
v_add_u32 v9, v9, v1
Mp 136,0,44
v_exp_f32_e32 v24, v105
Mp 140,4,44
Rb 52,10,29696
v_add_f32_e32 v14, v14, v24
Mp 132,4,40
v_exp_f32_e32 v51, v106
Mp 144,8,40
Rb 56,10,30720
v_exp_f32_e32 v25, v107
Mp 152,8,44
v_exp_f32_e32 v52, v100
Mp 156,12,44
Rb 60,10,31744
s_waitcnt lgkmcnt(8)
Mp 148,12,40
v_exp_f32_e32 v26, v101
Cv 50,50,24
Mp 160,16,40
v_exp_f32_e32 v53, v102
Mp 168,16,44
v_exp_f32_e32 v27, v103
v_add_f32_e32 v14, v14, v51
Mp 172,20,44
v_exp_f32_e32 v54, v108
Mp 164,20,40
v_exp_f32_e32 v28, v109
Mp 176,24,40
v_exp_f32_e32 v55, v110
v_add_f32_e32 v14, v14, v25
Mp 184,24,44
v_exp_f32_e32 v29, v111
Mp 188,28,44
Cv 51,51,25
v_add_f32_e32 v15, v52, v26
Mp 180,28,40
Cv 52,52,26
s_waitcnt vmcnt(16) lgkmcnt(0)
s_barrier
Mp 192,32,40
Rb 0,1,0
)" R"(
s_add_u32 m0, s36, 131072
s_nop 0
buffer_load_dwordx4 v9, s[24:27], s83 offen offset:0 lds
Mp 200,32,44
Rb 4,1,1024
v_add_f32_e32 v15, v15, v53
v_add_f32_e32 v15, v15, v27
Mp 204,36,44
Rb 8,1,2048
s_add_u32 s83, s83, 0x1bc00
buffer_load_dwordx4 v9, s[24:27], s83 offen offset:1024 lds
Mp 196,36,40
Rb 12,1,3072
Cv 53,53,27
Mp 208,40,40
Rb 16,1,8192
s_add_u32 s83, s83, 0x1bc00
buffer_load_dwordx4 v9, s[24:27], s83 offen offset:2048 lds
Mp 216,40,44
Rb 20,1,9216
v_add_f32_e32 v15, v15, v54
Mp 220,44,44
Rb 24,1,10240
s_add_u32 s83, s83, 0x1bc00
buffer_load_dwordx4 v9, s[24:27], s83 offen offset:3072 lds
Mp 212,44,40
Rb 28,1,11264
v_add_f32_e32 v15, v15, v28
Cv 54,54,28
Mp 224,48,40
Rb 32,1,4096
s_add_u32 m0, s36, 135168
s_add_u32 s83, s83, 0x1cc00
buffer_load_dwordx4 v9, s[24:27], s83 offen offset:0 lds
Mp 232,48,44
Rb 36,1,5120
v_add_f32_e32 v15, v15, v55
v_add_f32_e32 v15, v15, v29
Mp 236,52,44
Rb 40,1,6144
s_add_u32 s83, s83, 0x1bc00
buffer_load_dwordx4 v9, s[24:27], s83 offen offset:1024 lds
Mp 228,52,40
Rb 44,1,7168
Cv 55,55,29
Mp 240,56,40
s_add_u32 s83, s83, 0x1bc00
buffer_load_dwordx4 v9, s[24:27], s83 offen offset:2048 lds
v_cmp_eq_u32_e32 vcc, s3, v4
s_andn2_b64 s[28:29], -1, vcc
s_cbranch_scc1 .Lzsp8
.Lzok8:
Mp 248,56,44
s_sub_u32 s57, s57, 1
Mp 252,60,44
s_waitcnt lgkmcnt(8)
Mp 244,60,40
s_cmp_gt_i32 s57, s93
s_cbranch_scc1 .LzL
.LzH38:
s_cbranch_execz .LzH21
.LzW:
s_lshl1_add_u32 s57, s35, 2
s_mov_b32 s59, s67
)" R"(
s_add_u32 s56, s89, 0x400000
s_mov_b32 s94, 2
Mz 64,0,224
Rb 48,1,12288
v_lshrrev_b32 v9, 4, v0
Mz 68,0,228
v_lshlrev_b32 v9, 4, v9
v_add_lshl_u32 v9, v9, v0, 3
v_accvgpr_mov_b32 a64, a224
Mv 68,4,236
Rb 52,1,13312
s_add_u32 m0, s36, 98304
v_accvgpr_mov_b32 a65, a225
Mv 64,4,232
buffer_load_dwordx4 v2, s[20:23], s56 offen offset:0 sc1 lds
v_accvgpr_mov_b32 a66, a226
Mv 64,8,240
Rb 56,1,14336
s_add_u32 s92, s36, 131072
v_accvgpr_mov_b32 a67, a227
Mv 68,8,244
v_add_u32 v9, s92, v9
v_accvgpr_mov_b32 a68, a228
s_waitcnt vmcnt(1)
Mv 68,12,252
Rb 60,1,15360
v_accvgpr_mov_b32 a69, a229
s_waitcnt lgkmcnt(8)
Mv 64,12,248
buffer_load_dwordx4 v2, s[20:23], s56 offen offset:1024 sc1 lds
Rt 156,9,0
Mv 64,16,192
Rb 0,1,16384
Rt 158,9,128
v_accvgpr_mov_b32 a70, a230
Mv 68,16,196
Rt 160,9,1024
v_exp_f32_e32 v56, v112
Mv 68,20,204
Rb 4,1,17408
Rt 162,9,1152
v_add_f32_e32 v14, v14, v56
Mv 64,20,200
buffer_load_dwordx4 v2, s[20:23], s56 offen offset:2048 sc1 lds
Rt 164,9,2048
Mv 64,24,208
Rb 8,1,18432
Rt 166,9,2176
v_accvgpr_mov_b32 a71, a231
Mv 68,24,212
Rt 168,9,3072
v_exp_f32_e32 v26, v113
Mv 68,28,220
Rb 12,1,19456
Rt 170,9,3200
Mv 64,28,216
buffer_load_dwordx4 v2, s[20:23], s56 offen offset:3072 sc1 lds
Rt 172,9,4096
v_add_f32_e32 v14, v14, v26
Mz 72,32,224
Rb 16,1,24576
Rt 174,9,4224
Cv 56,56,26
Mz 76,32,228
)" R"(
Rt 176,9,5120
v_exp_f32_e32 v57, v114
Mv 76,36,236
Rb 20,1,25600
s_add_u32 m0, s36, 102400
Rt 178,9,5248
Mv 72,36,232
buffer_load_dwordx4 v3, s[20:23], s56 offen offset:0 sc1 lds
Rt 180,9,6144
Mv 72,40,240
Rb 24,1,26624
Rt 182,9,6272
v_add_f32_e32 v14, v14, v57
Mv 76,40,244
v_exp_f32_e32 v27, v115
v_accvgpr_mov_b32 a72, a232
Mv 76,44,252
Rb 28,1,27648
v_add_f32_e32 v14, v14, v27
Mv 72,44,248
buffer_load_dwordx4 v3, s[20:23], s56 offen offset:1024 sc1 lds
Cv 57,57,27
Mv 72,48,192
Rb 32,1,20480
v_and_b32 v9, 14, v0
v_accvgpr_mov_b32 a73, a233
Mv 76,48,196
v_min_u32 v9, 1, v9
v_lshlrev_b32 v9, 8, v9
v_accvgpr_mov_b32 a74, a234
Mv 76,52,204
Rb 36,1,21504
v_and_or_b32 v9, v0, 48, v9
v_accvgpr_mov_b32 a75, a235
Mv 72,52,200
buffer_load_dwordx4 v3, s[20:23], s56 offen offset:2048 sc1 lds
v_accvgpr_mov_b32 a76, a236
Mv 72,56,208
Rb 40,1,22528
v_lshlrev_b32 v9, 1, v9
v_accvgpr_mov_b32 a77, a237
Mv 76,56,212
v_and_or_b32 v9, v1, 16, v9
s_lshl_b32 s92, s33, 7
v_accvgpr_mov_b32 a78, a238
Mv 76,60,220
Rb 44,1,23552
v_accvgpr_mov_b32 a79, a239
s_waitcnt lgkmcnt(5)
Mv 72,60,216
v_mfma_f32_16x16x32_bf16 a[184:187], a[156:159], a[128:131], 0
buffer_load_dwordx4 v3, s[20:23], s56 offen offset:3072 sc1 lds
v_exp_f32_e32 v58, v120
Mz 80,0,224
Ma 184,160,132
Rb 48,1,28672
s_add_u32 s92, s92, 162816
v_add_f32_e32 v14, v14, v58
Mz 84,0,228
Ma 184,164,136
s_add_u32 s56, s56, 0x10000
v_add_u32 v9, s92, v9
v_accvgpr_mov_b32 a80, a240
Mv 84,4,236
)" R"(
Ma 184,168,140
Rb 52,1,29696
v_exp_f32_e32 v28, v121
Mv 80,4,232
Ma 184,172,144
v_exp_f32_e32 v59, v122
v_add_f32_e32 v14, v14, v28
Mv 80,8,240
Ma 184,176,148
Rb 56,1,30720
s_mov_b32 s24, s70
s_mov_b32 s25, s71
Mv 84,8,244
Ma 184,180,152
s_mul_i32 s83, s82, 14336
s_lshl_b32 s92, s33, 12
Cv 58,58,28
Mv 84,12,252
Rb 60,1,31744
v_add_f32_e32 v14, v14, v59
s_waitcnt lgkmcnt(8)
Mv 80,12,248
s_add_u32 s83, s83, s92
s_add_u32 m0, s92, 131072
v_accvgpr_mov_b32 a81, a241
Mv 80,16,192
v_exp_f32_e32 v29, v123
v_accvgpr_mov_b32 a82, a242
Mv 84,16,196
v_exp_f32_e32 v60, v116
v_add_f32_e32 v14, v14, v29
Mv 84,20,204
ds_write_b128 v9, a[184:187]
v_and_b32 v9, 0x70, v9
Cv 59,59,29
Mv 80,20,200
v_add_u32 v9, 0x27c00, v9
v_exp_f32_e32 v24, v117
Mv 80,24,208
v_exp_f32_e32 v61, v118
v_add_f32_e32 v15, v15, v60
Mv 84,24,212
v_exp_f32_e32 v25, v119
v_add_f32_e32 v15, v15, v24
Mv 84,28,220
v_exp_f32_e32 v62, v124
Cv 60,60,24
Mv 80,28,216
v_exp_f32_e32 v26, v125
s_waitcnt lgkmcnt(0)
s_barrier
Mz 88,32,224
Rb 0,8,0
v_exp_f32_e32 v63, v126
Mz 92,32,228
Rb 4,8,16384
v_exp_f32_e32 v27, v127
Mv 92,36,236
Rb 8,8,1024
v_add_f32_e32 v15, v15, v61
v_add_f32_e32 v15, v15, v25
Mv 88,36,232
Rb 12,8,17408
Cv 61,61,25
v_add_f32_e32 v15, v15, v62
)" R"(
Mv 88,40,240
Rb 16,8,2048
buffer_load_dwordx4 v1, s[24:27], s83 offen offset:0 lds
Mv 92,40,244
Rb 20,8,18432
v_add_f32_e32 v15, v15, v26
Cv 62,62,26
Mv 92,44,252
Rb 24,8,3072
v_add_f32_e32 v15, v15, v63
v_add_f32_e32 v15, v15, v27
Mv 88,44,248
Rb 28,8,19456
Cv 63,63,27
v_max_f32_e32 v11, v14, v15
Mv 88,48,192
Rb 32,8,4096
v_cmp_nge_f32_e32 vcc, s62, v11
s_add_u32 s55, s55, 0x10000
Mv 92,48,196
Rb 36,8,20480
buffer_load_dwordx4 v1, s[24:27], s83 offen offset:1024 lds
Mv 92,52,204
Rb 40,8,5120
s_cmp_eq_u32 s57, 2
s_cselect_b32 s55, s59, s55
Mv 88,52,200
Rb 44,8,21504
v_accvgpr_mov_b32 a83, a243
v_accvgpr_mov_b32 a84, a244
Mv 88,56,208
buffer_load_dwordx4 v1, s[24:27], s83 offen offset:2048 lds
v_accvgpr_mov_b32 a85, a245
Mv 92,56,212
buffer_load_dwordx4 v1, s[24:27], s83 offen offset:3072 lds
v_accvgpr_mov_b32 a86, a246
Mv 92,60,220
v_accvgpr_mov_b32 a87, a247
s_waitcnt lgkmcnt(8)
s_cbranch_vccnz .Lzfx3
.Lzfr3:
Mv 88,60,216
v_add_f32_e32 v12, v12, v14
v_add_f32_e32 v13, v13, v15
v_mov_b32 v14, v16
Mp 128,0,48
Rb 48,8,6144
v_mov_b32 v15, v20
Mp 136,0,52
buffer_store_dwordx4 v[12:15], v1, s[20:23], s66 offen sc0 sc1
s_cmp_le_i32 s57, s58
s_cbranch_scc1 .Lzmk4
.Lzmr4:
Mp 136,4,60
Rb 52,8,22528
s_add_u32 m0, s36, 0
v_accvgpr_mov_b32 a88, a248
Mp 128,4,56
buffer_load_dwordx4 v2, s[20:23], s55 offen offset:0 sc1 lds
v_accvgpr_mov_b32 a89, a249
Mp 132,8,48
Rb 56,8,7168
v_exp_f32_e32 v32, v64
Mp 140,8,52
)" R"(
v_exp_f32_e32 v28, v65
v_accvgpr_mov_b32 a90, a250
Mp 140,12,60
Rb 60,8,23552
v_exp_f32_e32 v33, v66
s_waitcnt lgkmcnt(8)
Mp 132,12,56
v_exp_f32_e32 v29, v67
v_add_f32_e32 v14, v32, v28
Mp 144,16,48
Rb 0,8,8192
v_exp_f32_e32 v34, v72
Mp 152,16,52
v_exp_f32_e32 v24, v73
v_exp_f32_e32 v35, v74
Mp 152,20,60
Rb 4,8,24576
v_exp_f32_e32 v25, v75
Mp 144,20,56
buffer_load_dwordx4 v2, s[20:23], s55 offen offset:1024 sc1 lds
Cv 32,32,28
Mp 148,24,48
Rb 8,8,9216
Cv 128,128,129
Cv 129,130,131
v_add_f32_e32 v14, v14, v33
Mp 156,24,52
Cv 130,132,133
Cv 131,134,135
v_add_f32_e32 v14, v14, v29
Mp 156,28,60
Rb 12,8,25600
Cv 136,136,137
s_waitcnt lgkmcnt(8)
.LzH39:
s_cbranch_execz .LzH22
Mp 148,28,56
Cv 137,138,139
Cv 138,140,141
Cv 139,142,143
Cv 33,33,29
Mp 160,32,48
Rb 16,8,10240
v_exp_f32_e32 v36, v68
Mp 168,32,52
v_exp_f32_e32 v26, v69
v_add_f32_e32 v14, v14, v34
Mp 168,36,60
Rb 20,8,26624
v_exp_f32_e32 v37, v70
v_add_f32_e32 v14, v14, v24
Mp 160,36,56
buffer_load_dwordx4 v2, s[20:23], s55 offen offset:2048 sc1 lds
Cv 144,144,145
Mp 164,40,48
Rb 24,8,11264
Cv 145,146,147
Cv 34,34,24
Mp 172,40,52
buffer_store_dwordx4 v[128:131], v2, s[20:23], s64 offen offset:0 sc0 sc1
Cv 146,148,149
Mp 172,44,60
Rb 28,8,27648
v_exp_f32_e32 v27, v71
)" R"(
s_waitcnt lgkmcnt(8)
Mp 164,44,56
Cv 147,150,151
Cv 152,152,153
Cv 153,154,155
Mp 176,48,48
Rb 32,8,12288
Cv 154,156,157
Cv 155,158,159
Mp 184,48,52
v_exp_f32_e32 v38, v76
v_exp_f32_e32 v28, v77
Mp 184,52,60
Rb 36,8,28672
v_exp_f32_e32 v39, v78
Mp 176,52,56
buffer_load_dwordx4 v2, s[20:23], s55 offen offset:3072 sc1 lds
v_add_f32_e32 v14, v14, v35
Mp 180,56,48
Rb 40,8,13312
Cv 160,160,161
Cv 161,162,163
v_add_f32_e32 v14, v14, v25
Mp 188,56,52
buffer_store_dwordx4 v[136:139], v2, s[20:23], s64 offen offset:1024 sc0 sc1
Cv 35,35,25
Mp 188,60,60
Rb 44,8,29696
Cv 162,164,165
s_waitcnt lgkmcnt(8)
Mp 180,60,56
Cv 163,166,167
Cv 168,168,169
Cv 169,170,171
v_add_f32_e32 v15, v36, v26
Mp 192,0,48
Rb 48,8,14336
Cv 170,172,173
Cv 36,36,26
Mp 200,0,52
buffer_store_dwordx4 v[144:147], v2, s[20:23], s64 offen offset:2048 sc0 sc1
Cv 171,174,175
Mp 200,4,60
Rb 52,8,30720
s_add_u32 m0, s36, 4096
v_exp_f32_e32 v29, v79
Mp 192,4,56
buffer_load_dwordx4 v3, s[20:23], s55 offen offset:0 sc1 lds
Cv 176,176,177
Mp 196,8,48
Rb 56,8,15360
Cv 177,178,179
v_add_f32_e32 v15, v15, v37
Mp 204,8,52
Cv 178,180,181
Cv 179,182,183
Cv 184,184,185
Mp 204,12,60
Rb 60,8,31744
v_add_f32_e32 v15, v15, v27
Cv 37,37,27
s_waitcnt lgkmcnt(8)
Mp 196,12,56
buffer_store_dwordx4 v[152:155], v2, s[20:23], s64 offen offset:3072 sc0 sc1
)" R"(
Cv 185,186,187
Mp 208,16,48
ds_read_b128 v[132:135], v9 offset:0
Cv 186,188,189
Cv 187,190,191
Mp 216,16,52
ds_read_b128 v[140:143], v9 offset:128
v_add_f32_e32 v15, v15, v38
v_add_f32_e32 v15, v15, v28
Cv 38,38,28
Mp 216,20,60
ds_read_b128 v[148:151], v9 offset:256
v_add_f32_e32 v15, v15, v39
v_add_f32_e32 v15, v15, v29
Mp 208,20,56
buffer_load_dwordx4 v3, s[20:23], s55 offen offset:1024 sc1 lds
ds_read_b128 v[156:159], v9 offset:384
Mp 212,24,48
s_add_u32 s50, s50, 1024
s_min_u32 s50, s50, s60
Cv 192,192,193
Cv 39,39,29
Mp 220,24,52
Cv 193,194,195
Cv 194,196,197
s_sub_u32 s57, s57, 1
Mp 220,28,60
buffer_store_dwordx4 v[160:163], v3, s[20:23], s64 offen offset:0 sc0 sc1
Cv 195,198,199
Mp 212,28,56
Cv 200,200,201
v_mov_b32 v16, 0
s_waitcnt lgkmcnt(0)
s_barrier
Mp 224,32,48
Rb 0,1,32768
v_add_f32_e32 v132, v132, v140
v_mov_b32 v17, 0
Mp 232,32,52
Rb 4,1,33792
v_add_f32_e32 v133, v133, v141
Cv 201,202,203
Mp 232,36,60
Rb 8,1,34816
v_add_f32_e32 v134, v134, v142
v_mov_b32 v18, 0
v_mov_b32 v19, 0
Mp 224,36,56
Rb 12,1,35840
buffer_load_dwordx4 v3, s[20:23], s55 offen offset:2048 sc1 lds
Mp 228,40,48
Rb 16,1,40960
v_add_f32_e32 v135, v135, v143
buffer_store_dwordx4 v[168:171], v3, s[20:23], s64 offen offset:1024 sc0 sc1
Mp 236,40,52
Rb 20,1,41984
v_add_f32_e32 v148, v148, v156
Cv 202,204,205
Mp 236,44,60
Rb 24,1,43008
v_add_f32_e32 v149, v149, v157
v_mov_b32 v20, 0
v_mov_b32 v21, 0
Mp 228,44,56
Rb 28,1,44032
)" R"(
v_add_f32_e32 v150, v150, v158
Cv 203,206,207
Mp 240,48,48
Rb 32,1,36864
v_add_f32_e32 v151, v151, v159
Cv 208,208,209
Mp 248,48,52
Rb 36,1,37888
Cv 132,132,132
v_mov_b32 v22, 0
v_mov_b32 v23, 0
Mp 248,52,60
Rb 40,1,38912
v_lshlrev_b32 v132, 16, v132
buffer_store_dwordx4 v[176:179], v3, s[20:23], s64 offen offset:2048 sc0 sc1
Mp 240,52,56
Rb 44,1,39936
buffer_load_dwordx4 v3, s[20:23], s55 offen offset:3072 sc1 lds
Mp 244,56,48
Cv 148,148,148
v_lshlrev_b32 v148, 16, v148
Cv 209,210,211
Mp 252,56,52
Cv 133,133,133
v_lshlrev_b32 v133, 16, v133
v_accvgpr_mov_b32 a91, a251
Mp 252,60,60
Cv 149,149,149
Cv 210,212,213
s_waitcnt lgkmcnt(8)
Mp 244,60,56
v_lshlrev_b32 v149, 16, v149
Cv 134,134,134
v_lshlrev_b32 v134, 16, v134
Mz 96,0,224
Rb 48,1,45056
buffer_load_dword v4, v5, s[20:23], s50 offen offset:0 sc0 sc1
buffer_store_dwordx4 v[184:187], v3, s[20:23], s64 offen offset:3072 sc0 sc1
Mz 100,0,228
Cv 150,150,150
v_lshlrev_b32 v150, 16, v150
Cv 211,214,215
Mv 100,4,236
Rb 52,1,46080
s_add_u32 m0, s36, 65536
v_exp_f32_e32 v40, v80
Mv 96,4,232
buffer_load_dwordx4 v2, s[20:23], s56 offen offset:0 sc1 lds
Cv 216,216,217
v_add_f32_e32 v14, v14, v40
Mv 96,8,240
Rb 56,1,47104
Cv 135,135,135
v_accvgpr_mov_b32 a92, a252
Mv 100,8,244
v_lshlrev_b32 v135, 16, v135
Cv 151,151,151
Cv 217,218,219
Mv 100,12,252
Rb 60,1,48128
s_add_u32 s64, s64, 0x10000
buffer_store_dwordx4 v[192:195], v2, s[20:23], s64 offen offset:0 sc0 sc1
s_waitcnt lgkmcnt(8)
Mv 96,12,248
)" R"(
v_lshlrev_b32 v151, 16, v151
v_mul_f32_e32 v140, 0xbfb8aa3b, v132
Cv 218,220,221
Mv 96,16,192
Rb 0,1,49152
v_mul_f32_e32 v141, 0xbfb8aa3b, v133
Cv 219,222,223
Mv 100,16,196
v_exp_f32_e32 v140, v140
v_exp_f32_e32 v24, v81
Mv 100,20,204
Rb 4,1,50176
v_mul_f32_e32 v142, 0xbfb8aa3b, v134
Cv 224,224,225
v_add_f32_e32 v14, v14, v24
Mv 96,20,200
buffer_load_dwordx4 v2, s[20:23], s56 offen offset:1024 sc1 lds
v_exp_f32_e32 v41, v82
Mv 96,24,208
Rb 8,1,51200
v_mul_f32_e32 v143, 0xbfb8aa3b, v135
buffer_store_dwordx4 v[200:203], v2, s[20:23], s64 offen offset:1024 sc0 sc1
Mv 100,24,212
v_exp_f32_e32 v141, v141
v_exp_f32_e32 v25, v83
Mv 100,28,220
Rb 12,1,52224
Cv 225,226,227
Cv 40,40,24
s_waitcnt lgkmcnt(8)
Mv 96,28,216
v_exp_f32_e32 v142, v142
v_add_f32_e32 v140, 1.0, v140
v_add_f32_e32 v14, v14, v41
Mz 104,32,224
Rb 16,1,57344
v_add_f32_e32 v141, 1.0, v141
v_add_f32_e32 v14, v14, v25
Mz 108,32,228
v_exp_f32_e32 v143, v143
Cv 226,228,229
Cv 41,41,25
Mv 108,36,236
Rb 20,1,58368
v_add_f32_e32 v142, 1.0, v142
buffer_store_dwordx4 v[208:211], v2, s[20:23], s64 offen offset:2048 sc0 sc1
Mv 104,36,232
buffer_load_dwordx4 v2, s[20:23], s56 offen offset:2048 sc1 lds
Cv 227,230,231
Mv 104,40,240
Rb 24,1,59392
v_add_f32_e32 v143, 1.0, v143
v_accvgpr_mov_b32 a93, a253
Mv 108,40,244
v_rcp_f32_e32 v140, v140
Cv 232,232,233
Mv 108,44,252
Rb 28,1,60416
v_exp_f32_e32 v42, v88
s_waitcnt lgkmcnt(8)
Mv 104,44,248
v_rcp_f32_e32 v141, v141
Cv 233,234,235
v_add_f32_e32 v14, v14, v42
)" R"(
Mv 104,48,192
Rb 32,1,53248
v_mul_f32_e32 v132, v132, v140
buffer_store_dwordx4 v[216:219], v2, s[20:23], s64 offen offset:3072 sc0 sc1
Mv 108,48,196
v_rcp_f32_e32 v142, v142
v_exp_f32_e32 v26, v89
Mv 108,52,204
Rb 36,1,54272
v_mul_f32_e32 v133, v133, v141
Cv 234,236,237
v_add_f32_e32 v14, v14, v26
Mv 104,52,200
buffer_load_dwordx4 v2, s[20:23], s56 offen offset:3072 sc1 lds
v_exp_f32_e32 v43, v90
Mv 104,56,208
Rb 40,1,55296
v_mul_f32_e32 v134, v134, v142
Cv 235,238,239
Mv 108,56,212
v_rcp_f32_e32 v143, v143
v_exp_f32_e32 v27, v91
Mv 108,60,220
Rb 44,1,56320
buffer_store_dwordx4 v[224:227], v3, s[20:23], s64 offen offset:0 sc0 sc1
s_waitcnt lgkmcnt(8)
Mv 104,60,216
v_mul_f32_e32 v135, v135, v143
Cv 132,132,132
v_lshlrev_b32 v132, 16, v132
Cv 42,42,26
Mz 112,0,224
Rb 48,1,61440
Cv 133,133,133
v_add_f32_e32 v14, v14, v43
Mz 116,0,228
v_lshlrev_b32 v133, 16, v133
Cv 134,134,134
Cv 240,240,241
v_add_f32_e32 v14, v14, v27
Mv 116,4,236
Rb 52,1,62464
s_add_u32 m0, s36, 69632
v_exp_f32_e32 v44, v84
Mv 112,4,232
buffer_load_dwordx4 v3, s[20:23], s56 offen offset:0 sc1 lds
Cv 241,242,243
Cv 43,43,27
Mv 112,8,240
Rb 56,1,63488
v_lshlrev_b32 v134, 16, v134
buffer_store_dwordx4 v[232:235], v3, s[20:23], s64 offen offset:1024 sc0 sc1
Mv 116,8,244
Cv 135,135,135
v_lshlrev_b32 v135, 16, v135
Cv 242,244,245
v_add_f32_e32 v15, v15, v44
Mv 116,12,252
Rb 60,1,64512
v_exp_f32_e32 v28, v85
s_waitcnt lgkmcnt(8)
Mv 112,12,248
v_mul_f32_e32 v132, v132, v148
v_mul_f32_e32 v133, v133, v149
)" R"(
Cv 243,246,247
v_add_f32_e32 v15, v15, v28
Mv 112,16,192
v_mul_f32_e32 v134, v134, v150
v_mul_f32_e32 v135, v135, v151
Cv 248,248,249
Mv 116,16,196
Cv 140,132,133
Cv 141,134,135
v_exp_f32_e32 v45, v86
Mv 116,20,204
v_accvgpr_write_b32 a188, v140
v_accvgpr_write_b32 a189, v141
buffer_store_dwordx4 v[240:243], v3, s[20:23], s64 offen offset:2048 sc0 sc1
Mv 112,20,200
buffer_load_dwordx4 v3, s[20:23], s56 offen offset:1024 sc1 lds
v_exp_f32_e32 v29, v87
Mv 112,24,208
v_lshrrev_b32 v156, 2, v0
v_mul_u32_u24 v156, 864, v156
Cv 249,250,251
Mv 116,24,212
v_lshl_add_u32 v156, v0, 3, v156
s_mul_i32 s92, s33, 224
v_exp_f32_e32 v46, v92
Mv 116,28,220
s_add_u32 s92, s92, 131072
v_add_u32 v156, s92, v156
Cv 250,252,253
Cv 44,44,28
Mv 112,28,216
Cv 251,254,255
v_add_f32_e32 v15, v15, v45
s_waitcnt vmcnt(31) lgkmcnt(0)
s_barrier
Mz 120,32,224
Rb 0,8,32768
Rt 156,156,0
buffer_store_dwordx4 v[248:251], v3, s[20:23], s64 offen offset:3072 sc0 sc1
Mz 124,32,228
Rb 4,8,33792
Rt 158,156,32
v_exp_f32_e32 v24, v93
Mv 124,36,236
Rb 8,8,34816
Rt 160,156,64
v_exp_f32_e32 v47, v94
Mv 120,36,232
Rb 12,8,35840
buffer_load_dwordx4 v3, s[20:23], s56 offen offset:2048 sc1 lds
Rt 162,156,96
Mv 120,40,240
Rb 16,8,36864
Rt 164,156,128
v_add_f32_e32 v15, v15, v29
Mv 124,40,244
Rb 20,8,37888
Rt 166,156,160
v_exp_f32_e32 v25, v95
Mv 124,44,252
Rb 24,8,38912
Rt 168,156,192
Cv 45,45,29
v_add_f32_e32 v15, v15, v46
Mv 120,44,248
)" R"(
Rb 28,8,39936
v_add_f32_e32 v15, v15, v24
Cv 46,46,24
v_add_f32_e32 v15, v15, v47
Mv 120,48,192
Rb 32,8,40960
v_add_f32_e32 v15, v15, v25
Cv 47,47,25
Mv 124,48,196
Rb 36,8,41984
v_max_f32_e32 v11, v14, v15
v_mov_b32 v24, v14
v_mov_b32 v25, v15
Mv 124,52,204
Rb 40,8,43008
v_permlane32_swap_b32_e32 v24, v25
v_add_f32_e32 v24, v24, v25
v_mov_b32 v25, v24
Mv 120,52,200
Rb 44,8,44032
buffer_load_dwordx4 v3, s[20:23], s56 offen offset:3072 sc1 lds
v_permlane16_swap_b32_e32 v24, v25
Mv 120,56,208
v_add_f32_e32 v24, v24, v25
v_rcp_f32_e32 v24, v24
Mv 124,56,212
s_add_u32 s56, s56, 0x10000
v_max_f32_e32 v11, v11, v24
v_cmp_nge_f32_e32 vcc, s62, v11
s_add_u32 s55, s55, 0x10000
Mv 124,60,220
s_cmp_eq_u32 s57, 2
s_cselect_b32 s55, s59, s55
s_waitcnt lgkmcnt(12)
s_cbranch_vccnz .Lzfx4
.Lzfr4:
Mv 120,60,216
v_mov_b32 v12, v14
v_mov_b32 v13, v15
v_accvgpr_mov_b32 a94, a254
v_mfma_f32_16x16x32_bf16 v[128:131], a[0:3], v[32:35], 0
Rb 48,8,45056
v_accvgpr_mov_b32 a95, a255
v_mfma_f32_16x16x32_bf16 v[136:139], a[0:3], v[36:39], 0
v_accvgpr_mov_b32 a96, a192
s_cmp_le_i32 s57, s58
s_cbranch_scc1 .Lzmk5
.Lzmr5:
v_mfma_f32_16x16x32_bf16 v[140:143], a[4:7], v[36:39], 0
Rb 52,8,46080
s_add_u32 m0, s36, 32768
v_accvgpr_mov_b32 a97, a193
v_mfma_f32_16x16x32_bf16 v[132:135], a[4:7], v[32:35], 0
buffer_load_dwordx4 v2, s[20:23], s55 offen offset:0 sc1 lds
v_accvgpr_mov_b32 a98, a194
v_mfma_f32_16x16x32_bf16 v[144:147], a[8:11], v[32:35], 0
Rb 56,8,47104
v_and_b32 v11, 14, v0
v_accvgpr_mov_b32 a99, a195
v_mfma_f32_16x16x32_bf16 v[152:155], a[8:11], v[36:39], 0
v_min_u32 v11, 1, v11
v_lshlrev_b32 v11, 13, v11
v_accvgpr_mov_b32 a100, a196
v_mfma_f32_16x16x32_bf16 v[156:159], a[12:15], v[36:39], 0
)" R"(
Rb 60,8,48128
v_accvgpr_mov_b32 a101, a197
s_waitcnt lgkmcnt(8)
v_mfma_f32_16x16x32_bf16 v[148:151], a[12:15], v[32:35], 0
v_mfma_f32_16x16x16_bf16 a[128:131], a[156:157], a[188:189], 0
v_and_or_b32 v11, v0, 1, v11
v_mul_u32_u24 v11, 0x31000, v11
v_accvgpr_mov_b32 a102, a198
v_mfma_f32_16x16x32_bf16 v[160:163], a[16:19], v[32:35], 0
v_mfma_f32_16x16x16_bf16 a[132:135], a[158:159], a[188:189], 0
Rb 0,8,49152
v_and_or_b32 v11, v0, 48, v11
v_accvgpr_mov_b32 a103, a199
v_mfma_f32_16x16x32_bf16 v[168:171], a[16:19], v[36:39], 0
v_mfma_f32_16x16x16_bf16 a[136:139], a[160:161], a[188:189], 0
s_mul_i32 s83, s33, 50176
s_lshl_b32 s92, s82, 6
v_accvgpr_mov_b32 a104, a200
v_mfma_f32_16x16x32_bf16 v[172:175], a[20:23], v[36:39], 0
v_mfma_f32_16x16x16_bf16 a[140:143], a[162:163], a[188:189], 0
Rb 4,8,50176
s_add_u32 s83, s83, s92
v_accvgpr_mov_b32 a105, a201
v_mfma_f32_16x16x32_bf16 v[164:167], a[20:23], v[32:35], 0
v_mfma_f32_16x16x16_bf16 a[144:147], a[164:165], a[188:189], 0
buffer_load_dwordx4 v2, s[20:23], s55 offen offset:1024 sc1 lds
v_accvgpr_mov_b32 a106, a202
v_mfma_f32_16x16x32_bf16 v[176:179], a[24:27], v[32:35], 0
v_mfma_f32_16x16x16_bf16 a[148:151], a[166:167], a[188:189], 0
Rb 8,8,51200
s_add_u32 s83, s83, 0x1558000
v_accvgpr_mov_b32 a107, a203
v_mfma_f32_16x16x32_bf16 v[184:187], a[24:27], v[36:39], 0
v_mfma_f32_16x16x16_bf16 a[152:155], a[168:169], a[188:189], 0
v_exp_f32_e32 v48, v96
v_accvgpr_mov_b32 a108, a204
v_mfma_f32_16x16x32_bf16 v[188:191], a[28:31], v[36:39], 0
Rb 12,8,52224
v_accvgpr_mov_b32 a109, a205
s_waitcnt lgkmcnt(8)
v_mfma_f32_16x16x32_bf16 v[180:183], a[28:31], v[32:35], 0
v_exp_f32_e32 v26, v97
v_accvgpr_mov_b32 a110, a206
v_mfma_f32_16x16x32_bf16 v[192:195], a[32:35], v[32:35], 0
Rb 16,8,53248
v_add_f32_e32 v14, v48, v26
v_mfma_f32_16x16x32_bf16 v[200:203], a[32:35], v[36:39], 0
v_exp_f32_e32 v49, v98
Cv 48,48,26
v_mfma_f32_16x16x32_bf16 v[204:207], a[36:39], v[36:39], 0
Rb 20,8,54272
v_add_f32_e32 v14, v14, v49
v_accvgpr_mov_b32 a111, a207
v_mfma_f32_16x16x32_bf16 v[196:199], a[36:39], v[32:35], 0
buffer_load_dwordx4 v2, s[20:23], s55 offen offset:2048 sc1 lds
v_accvgpr_mov_b32 a112, a208
v_mfma_f32_16x16x32_bf16 v[208:211], a[40:43], v[32:35], 0
Rb 24,8,55296
v_exp_f32_e32 v27, v99
v_mfma_f32_16x16x32_bf16 v[216:219], a[40:43], v[36:39], 0
buffer_store_dwordx4 a[128:131], v11, s[20:23], s83 offen sc0 sc1
v_accvgpr_mov_b32 a113, a209
v_mfma_f32_16x16x32_bf16 v[220:223], a[44:47], v[36:39], 0
Rb 28,8,56320
)" R"(
v_accvgpr_mov_b32 a114, a210
s_waitcnt lgkmcnt(8)
v_mfma_f32_16x16x32_bf16 v[212:215], a[44:47], v[32:35], 0
s_add_u32 s83, s83, 7168
v_exp_f32_e32 v50, v104
v_mfma_f32_16x16x32_bf16 v[224:227], a[48:51], v[32:35], 0
Rb 32,8,57344
v_add_f32_e32 v14, v14, v27
v_accvgpr_mov_b32 a115, a211
v_mfma_f32_16x16x32_bf16 v[232:235], a[48:51], v[36:39], 0
v_exp_f32_e32 v28, v105
Cv 49,49,27
v_mfma_f32_16x16x32_bf16 v[236:239], a[52:55], v[36:39], 0
Rb 36,8,58368
v_add_f32_e32 v14, v14, v50
v_accvgpr_mov_b32 a116, a212
v_mfma_f32_16x16x32_bf16 v[228:231], a[52:55], v[32:35], 0
buffer_load_dwordx4 v2, s[20:23], s55 offen offset:3072 sc1 lds
v_accvgpr_mov_b32 a117, a213
v_mfma_f32_16x16x32_bf16 v[240:243], a[56:59], v[32:35], 0
Rb 40,8,59392
v_exp_f32_e32 v51, v106
v_mfma_f32_16x16x32_bf16 v[248:251], a[56:59], v[36:39], 0
buffer_store_dwordx4 a[132:135], v11, s[20:23], s83 offen sc0 sc1
v_accvgpr_mov_b32 a118, a214
v_mfma_f32_16x16x32_bf16 v[252:255], a[60:63], v[36:39], 0
Rb 44,8,60416
v_accvgpr_mov_b32 a119, a215
s_waitcnt lgkmcnt(8)
v_mfma_f32_16x16x32_bf16 v[244:247], a[60:63], v[32:35], 0
s_add_u32 s83, s83, 7168
v_exp_f32_e32 v29, v107
Mp 128,0,40
Rb 48,8,61440
v_add_f32_e32 v14, v14, v28
Mp 136,0,44
buffer_store_dwordx4 a[136:139], v11, s[20:23], s83 offen sc0 sc1
s_add_u32 s83, s83, 7168
Mp 140,4,44
Rb 52,8,62464
s_add_u32 m0, s36, 36864
v_accvgpr_mov_b32 a120, a216
Mp 132,4,40
buffer_load_dwordx4 v3, s[20:23], s55 offen offset:0 sc1 lds
v_accvgpr_mov_b32 a121, a217
Mp 144,8,40
Rb 56,8,63488
v_exp_f32_e32 v52, v100
Mp 152,8,44
v_exp_f32_e32 v24, v101
v_accvgpr_mov_b32 a122, a218
Mp 156,12,44
Rb 60,8,64512
v_accvgpr_mov_b32 a123, a219
s_waitcnt lgkmcnt(8)
Mp 148,12,40
buffer_store_dwordx4 a[140:143], v11, s[20:23], s83 offen sc0 sc1
Cv 50,50,28
Mp 160,16,40
s_add_u32 s83, s83, 7168
v_add_f32_e32 v14, v14, v51
v_accvgpr_mov_b32 a124, a220
Mp 168,16,44
v_exp_f32_e32 v53, v102
)" R"(
v_add_f32_e32 v14, v14, v29
Mp 172,20,44
v_exp_f32_e32 v25, v103
v_accvgpr_mov_b32 a125, a221
Mp 164,20,40
buffer_load_dwordx4 v3, s[20:23], s55 offen offset:1024 sc1 lds
v_accvgpr_mov_b32 a126, a222
Mp 176,24,40
v_exp_f32_e32 v54, v108
Cv 51,51,29
.LzH40:
s_cbranch_execz .LzHR
Mp 184,24,44
buffer_store_dwordx4 a[144:147], v11, s[20:23], s83 offen sc0 sc1
v_accvgpr_mov_b32 a127, a223
Mp 188,28,44
s_add_u32 s83, s83, 7168
v_add_f32_e32 v15, v52, v24
Mp 180,28,40
Cv 52,52,24
s_waitcnt vmcnt(13) lgkmcnt(0)
s_barrier
buffer_store_dword v6, v7, s[20:23], s65 offen offset:0 sc0 sc1
Mp 192,32,40
Rb 0,1,0
v_add_f32_e32 v15, v15, v53
Mp 200,32,44
Rb 4,1,1024
v_exp_f32_e32 v26, v109
Mp 204,36,44
Rb 8,1,2048
v_add_f32_e32 v15, v15, v25
Mp 196,36,40
Rb 12,1,3072
buffer_load_dwordx4 v3, s[20:23], s55 offen offset:2048 sc1 lds
Mp 208,40,40
Rb 16,1,8192
v_exp_f32_e32 v55, v110
Mp 216,40,44
Rb 20,1,9216
Cv 53,53,25
Mp 220,44,44
Rb 24,1,10240
buffer_store_dwordx4 a[148:151], v11, s[20:23], s83 offen sc0 sc1
Mp 212,44,40
Rb 28,1,11264
s_add_u32 s83, s83, 7168
v_add_f32_e32 v15, v15, v54
Mp 224,48,40
Rb 32,1,4096
v_add_f32_e32 v15, v15, v26
Mp 232,48,44
Rb 36,1,5120
v_exp_f32_e32 v27, v111
Mp 236,52,44
Rb 40,1,6144
Cv 54,54,26
Mp 228,52,40
Rb 44,1,7168
buffer_load_dwordx4 v3, s[20:23], s55 offen offset:3072 sc1 lds
Mp 240,56,40
v_cmp_eq_u32_e32 vcc, s3, v4
s_andn2_b64 s[28:29], -1, vcc
s_cbranch_scc1 .Lzsp10
.Lzok10:
)" R"(
Mp 248,56,44
buffer_store_dwordx4 a[152:155], v11, s[20:23], s83 offen sc0 sc1
Mp 252,60,44
v_add_f32_e32 v15, v15, v55
s_waitcnt lgkmcnt(8)
Mp 244,60,40
v_add_f32_e32 v15, v15, v27
Cv 55,55,27
s_sub_u32 s57, s57, 1
s_cmp_gt_i32 s57, s93
s_cbranch_scc1 .LzL
.LzH41:
s_cbranch_execz .LzHR
.LzDA:
s_waitcnt vmcnt(8) lgkmcnt(0)
s_barrier
Rb 0,8,0
Rb 4,8,16384
Rb 8,8,1024
Rb 12,8,17408
Rb 16,8,2048
Rb 20,8,18432
Rb 24,8,3072
Rb 28,8,19456
Rb 32,8,4096
Rb 36,8,20480
Rb 40,8,5120
Rb 44,8,21504
v_exp_f32_e32 v56, v112
v_exp_f32_e32 v28, v113
v_add_f32_e32 v14, v14, v56
v_add_f32_e32 v14, v14, v28
Cv 56,56,28
v_exp_f32_e32 v57, v114
v_exp_f32_e32 v29, v115
v_add_f32_e32 v14, v14, v57
v_add_f32_e32 v14, v14, v29
Cv 57,57,29
v_exp_f32_e32 v58, v120
v_exp_f32_e32 v24, v121
v_add_f32_e32 v14, v14, v58
v_add_f32_e32 v14, v14, v24
Cv 58,58,24
v_exp_f32_e32 v59, v122
v_exp_f32_e32 v25, v123
v_add_f32_e32 v14, v14, v59
v_add_f32_e32 v14, v14, v25
Cv 59,59,25
v_exp_f32_e32 v60, v116
v_exp_f32_e32 v26, v117
v_add_f32_e32 v15, v15, v60
v_add_f32_e32 v15, v15, v26
Cv 60,60,26
v_exp_f32_e32 v61, v118
v_exp_f32_e32 v27, v119
v_add_f32_e32 v15, v15, v61
v_add_f32_e32 v15, v15, v27
Cv 61,61,27
v_exp_f32_e32 v62, v124
v_exp_f32_e32 v28, v125
v_add_f32_e32 v15, v15, v62
v_add_f32_e32 v15, v15, v28
Cv 62,62,28
v_exp_f32_e32 v63, v126
v_exp_f32_e32 v29, v127
)" R"(
v_add_f32_e32 v15, v15, v63
v_add_f32_e32 v15, v15, v29
Cv 63,63,29
v_max_f32_e32 v11, v14, v15
v_cmp_nge_f32_e32 vcc, s62, v11
s_cbranch_vccnz .Lzfx5
.Lzfr5:
s_waitcnt lgkmcnt(8)
Mp 128,0,48
Rb 48,8,6144
s_add_u32 m0, s36, 131072
s_add_u32 s40, s67, 0x20000
s_lshl_b32 s82, s82, 7
Mp 136,0,52
buffer_load_dwordx4 v2, s[20:23], s40 offen offset:0 sc1 lds
v_add_f32_e32 v12, v12, v14
s_add_u32 s82, s82, 0x150c000
Mp 136,4,60
Rb 52,8,22528
v_add_f32_e32 v13, v13, v15
v_mov_b32 v24, v12
Mp 128,4,56
buffer_load_dwordx4 v2, s[20:23], s40 offen offset:1024 sc1 lds
v_permlane32_swap_b32_e32 v12, v24
Mp 132,8,48
Rb 56,8,7168
v_add_f32_e32 v12, v12, v24
v_mov_b32 v24, v12
Mp 140,8,52
s_nop 0
v_permlane16_swap_b32_e32 v12, v24
v_add_f32_e32 v12, v12, v24
v_mov_b32 v25, v13
Mp 140,12,60
Rb 60,8,23552
v_permlane32_swap_b32_e32 v13, v25
s_waitcnt lgkmcnt(8)
Mp 132,12,56
buffer_load_dwordx4 v2, s[20:23], s40 offen offset:2048 sc1 lds
v_add_f32_e32 v13, v13, v25
Mp 144,16,48
Rb 0,8,8192
v_rcp_f32_e32 v12, v12
Mp 152,16,52
buffer_load_dwordx4 v2, s[20:23], s40 offen offset:3072 sc1 lds
v_mov_b32 v25, v13
Mp 152,20,60
Rb 4,8,24576
v_permlane16_swap_b32_e32 v13, v25
v_add_f32_e32 v13, v13, v25
Mp 144,20,56
v_rcp_f32_e32 v13, v13
v_mul_f32_e32 v128, v12, v128
Mp 148,24,48
Rb 8,8,9216
s_add_u32 m0, s36, 135168
v_mul_f32_e32 v129, v12, v129
Mp 156,24,52
buffer_load_dwordx4 v3, s[20:23], s40 offen offset:0 sc1 lds
Cv 128,128,129
Mp 156,28,60
Rb 12,8,25600
v_mul_f32_e32 v130, v12, v130
s_waitcnt lgkmcnt(8)
)" R"(
Mp 148,28,56
buffer_load_dwordx4 v3, s[20:23], s40 offen offset:1024 sc1 lds
v_mul_f32_e32 v131, v12, v131
Mp 160,32,48
Rb 16,8,10240
Cv 129,130,131
s_cmp_lg_u32 s33, 0
s_cmov_b32 s82, 0x40000000
Mp 168,32,52
v_mul_f32_e32 v132, v12, v132
v_mul_f32_e32 v133, v12, v133
Cv 130,132,133
Mp 168,36,60
Rb 20,8,26624
v_mul_f32_e32 v134, v12, v134
v_mul_f32_e32 v135, v12, v135
Mp 160,36,56
buffer_load_dwordx4 v3, s[20:23], s40 offen offset:2048 sc1 lds
Cv 131,134,135
Mp 164,40,48
Rb 24,8,11264
s_add_u32 m0, s36, 98304
s_add_u32 s40, s67, 0x30000
Mp 172,40,52
buffer_load_dwordx4 v2, s[20:23], s40 offen offset:0 sc1 lds
v_mul_f32_e32 v136, v13, v136
Mp 172,44,60
Rb 28,8,27648
v_mul_f32_e32 v137, v13, v137
s_waitcnt lgkmcnt(8)
Mp 164,44,56
Cv 136,136,137
v_mul_f32_e32 v138, v13, v138
v_mul_f32_e32 v139, v13, v139
Mp 176,48,48
Rb 32,8,12288
Cv 137,138,139
v_mul_f32_e32 v140, v13, v140
Mp 184,48,52
buffer_load_dwordx4 v2, s[20:23], s40 offen offset:1024 sc1 lds
v_mul_f32_e32 v141, v13, v141
Mp 184,52,60
Rb 36,8,28672
Cv 138,140,141
v_mul_f32_e32 v142, v13, v142
Mp 176,52,56
buffer_load_dwordx4 v2, s[20:23], s40 offen offset:2048 sc1 lds
v_mul_f32_e32 v143, v13, v143
Mp 180,56,48
Rb 40,8,13312
Cv 139,142,143
v_mul_f32_e32 v144, v12, v144
Mp 188,56,52
v_mul_f32_e32 v145, v12, v145
Cv 144,144,145
v_mul_f32_e32 v146, v12, v146
Mp 188,60,60
Rb 44,8,29696
v_mul_f32_e32 v147, v12, v147
s_waitcnt lgkmcnt(8)
Mp 180,60,56
buffer_load_dwordx4 v2, s[20:23], s40 offen offset:3072 sc1 lds
Cv 145,146,147
Mp 192,0,48
)" R"(
Rb 48,8,14336
s_add_u32 m0, s36, 102400
s_cmp_eq_u32 s35, 7
s_cmov_b32 s82, 0x40000000
Mp 200,0,52
buffer_load_dwordx4 v3, s[20:23], s40 offen offset:0 sc1 lds
v_mul_f32_e32 v148, v12, v148
Mp 200,4,60
Rb 52,8,30720
v_mul_f32_e32 v149, v12, v149
Cv 146,148,149
Mp 192,4,56
v_mul_f32_e32 v150, v12, v150
v_mul_f32_e32 v151, v12, v151
Cv 147,150,151
Mp 196,8,48
Rb 56,8,15360
v_mul_f32_e32 v152, v13, v152
v_mul_f32_e32 v153, v13, v153
Mp 204,8,52
buffer_load_dwordx4 v3, s[20:23], s40 offen offset:1024 sc1 lds
Cv 152,152,153
Mp 204,12,60
Rb 60,8,31744
v_mul_f32_e32 v154, v13, v154
s_waitcnt lgkmcnt(8)
Mp 196,12,56
buffer_load_dwordx4 v3, s[20:23], s40 offen offset:2048 sc1 lds
v_mul_f32_e32 v155, v13, v155
Mp 208,16,48
Rb 0,1,0
Cv 153,154,155
v_mul_f32_e32 v156, v13, v156
Mp 216,16,52
v_mul_f32_e32 v157, v13, v157
Cv 154,156,157
v_mul_f32_e32 v158, v13, v158
Mp 216,20,60
Rb 4,1,1024
v_mul_f32_e32 v159, v13, v159
Cv 155,158,159
Mp 208,20,56
v_mul_f32_e32 v160, v12, v160
v_mul_f32_e32 v161, v12, v161
Cv 160,160,161
Mp 212,24,48
Rb 8,1,2048
v_mul_f32_e32 v162, v12, v162
v_mul_f32_e32 v163, v12, v163
Mp 220,24,52
Cv 161,162,163
v_mul_f32_e32 v164, v12, v164
v_mul_f32_e32 v165, v12, v165
Mp 220,28,60
Rb 12,1,3072
s_waitcnt vmcnt(14) lgkmcnt(4)
s_barrier
Mp 212,28,56
s_add_u32 s40, s67, 0x40000
Cv 162,164,165
v_mul_f32_e32 v166, v12, v166
Mp 224,32,48
Rb 16,1,4096
s_add_u32 m0, s36, 65536
Mp 232,32,52
)" R"(
buffer_load_dwordx4 v2, s[20:23], s40 offen offset:0 sc1 lds
v_mul_f32_e32 v167, v12, v167
Mp 232,36,60
Rb 20,1,5120
Cv 163,166,167
v_mul_f32_e32 v168, v13, v168
Mp 224,36,56
v_mul_f32_e32 v169, v13, v169
Cv 168,168,169
v_mul_f32_e32 v170, v13, v170
Mp 228,40,48
Rb 24,1,6144
v_mul_f32_e32 v171, v13, v171
Cv 169,170,171
Mp 236,40,52
v_mul_f32_e32 v172, v13, v172
v_mul_f32_e32 v173, v13, v173
Cv 170,172,173
Mp 236,44,60
Rb 28,1,8192
v_mul_f32_e32 v174, v13, v174
Mp 228,44,56
buffer_load_dwordx4 v2, s[20:23], s40 offen offset:1024 sc1 lds
v_mul_f32_e32 v175, v13, v175
s_lshl_b32 s64, s35, 17
Mp 240,48,48
Rb 32,1,9216
Cv 171,174,175
v_mul_f32_e32 v176, v12, v176
Mp 248,48,52
v_mul_f32_e32 v177, v12, v177
Cv 176,176,177
v_mul_f32_e32 v178, v12, v178
Mp 248,52,60
Rb 36,1,10240
v_mul_f32_e32 v179, v12, v179
Cv 177,178,179
Mp 240,52,56
v_mul_f32_e32 v180, v12, v180
v_mul_f32_e32 v181, v12, v181
Cv 178,180,181
Mp 244,56,48
Rb 40,1,11264
v_mul_f32_e32 v182, v12, v182
v_mul_f32_e32 v183, v12, v183
s_add_u32 s64, s64, s89
Mp 252,56,52
buffer_load_dwordx4 v2, s[20:23], s40 offen offset:2048 sc1 lds
Cv 179,182,183
Mp 252,60,60
Rb 44,1,12288
v_mul_f32_e32 v184, v13, v184
s_mov_b32 s55, 0x1508c00
s_waitcnt lgkmcnt(8)
Mp 244,60,56
v_mul_f32_e32 v185, v13, v185
Cv 184,184,185
v_mul_f32_e32 v186, v13, v186
v_mfma_f32_16x16x32_bf16 v[32:35], a[0:3], v[128:131], 0
Rb 48,1,13312
v_mul_f32_e32 v187, v13, v187
s_add_u32 s64, s64, 0x800000
v_mfma_f32_16x16x32_bf16 v[40:43], a[0:3], v[136:139], 0
Cv 185,186,187
)" R"(
v_mul_f32_e32 v188, v13, v188
v_mul_f32_e32 v189, v13, v189
s_xor_b32 s66, s64, 0x202000
v_mfma_f32_16x16x32_bf16 v[44:47], a[4:7], v[136:139], 0
Rb 52,1,14336
Cv 186,188,189
v_mul_f32_e32 v190, v13, v190
v_mfma_f32_16x16x32_bf16 v[36:39], a[4:7], v[128:131], 0
buffer_load_dwordx4 v2, s[20:23], s40 offen offset:3072 sc1 lds
v_mul_f32_e32 v191, v13, v191
v_mfma_f32_16x16x32_bf16 v[48:51], a[8:11], v[128:131], 0
Rb 56,1,16384
Cv 187,190,191
v_mul_f32_e32 v192, v12, v192
s_cmp_lg_u32 s33, 0
s_cmov_b32 s55, 0x40000000
v_mfma_f32_16x16x32_bf16 v[56:59], a[8:11], v[136:139], 0
v_mul_f32_e32 v193, v12, v193
Cv 192,192,193
v_mul_f32_e32 v194, v12, v194
v_mov_b32 v16, 1
v_mfma_f32_16x16x32_bf16 v[60:63], a[12:15], v[136:139], 0
Rb 60,1,17408
v_mul_f32_e32 v195, v12, v195
v_add_u32 v9, s36, v8
s_waitcnt lgkmcnt(8)
v_mfma_f32_16x16x32_bf16 v[52:55], a[12:15], v[128:131], 0
Cv 193,194,195
v_mul_f32_e32 v196, v12, v196
v_mul_f32_e32 v197, v12, v197
s_sub_u32 s66, s66, s84
Mp 32,16,144
Rb 0,1,18432
s_add_u32 m0, s36, 69632
Cv 194,196,197
Mp 40,16,152
buffer_load_dwordx4 v3, s[20:23], s40 offen offset:0 sc1 lds
v_mul_f32_e32 v198, v12, v198
Mp 44,20,152
Rb 4,1,19456
v_mul_f32_e32 v199, v12, v199
Cv 195,198,199
Mp 36,20,144
v_mul_f32_e32 v200, v13, v200
v_mul_f32_e32 v201, v13, v201
Cv 200,200,201
Mp 48,24,144
Rb 8,1,20480
v_mul_f32_e32 v202, v13, v202
v_mul_f32_e32 v203, v13, v203
s_sub_u32 s66, s66, s84
Mp 56,24,152
Cv 201,202,203
v_mul_f32_e32 v204, v13, v204
v_mul_f32_e32 v205, v13, v205
Mp 60,28,152
Rb 12,1,21504
Cv 202,204,205
s_waitcnt lgkmcnt(8)
Mp 52,28,144
buffer_load_dwordx4 v3, s[20:23], s40 offen offset:1024 sc1 lds
v_mul_f32_e32 v206, v13, v206
Mp 32,32,160
Rb 16,1,22528
)" R"(
v_mul_f32_e32 v207, v13, v207
s_lshl2_add_u32 s65, s35, s33
Mp 40,32,168
Cv 203,206,207
v_mul_f32_e32 v208, v12, v208
v_mul_f32_e32 v209, v12, v209
s_lshl_b32 s65, s65, 8
Mp 44,36,168
Rb 20,1,24576
Cv 208,208,209
v_mul_f32_e32 v210, v12, v210
Mp 36,36,160
v_mul_f32_e32 v211, v12, v211
Cv 209,210,211
v_mul_f32_e32 v212, v12, v212
Mp 48,40,160
Rb 24,1,25600
v_mul_f32_e32 v213, v12, v213
Cv 210,212,213
Mp 56,40,168
buffer_load_dwordx4 v3, s[20:23], s40 offen offset:2048 sc1 lds
v_mul_f32_e32 v214, v12, v214
Mp 60,44,168
Rb 28,1,26624
v_mul_f32_e32 v215, v12, v215
s_waitcnt lgkmcnt(8)
Mp 52,44,160
Cv 211,214,215
v_mul_f32_e32 v216, v13, v216
v_mul_f32_e32 v217, v13, v217
s_add_u32 s65, s65, s50
Mp 32,48,176
Rb 32,1,27648
Cv 216,216,217
v_mul_f32_e32 v218, v13, v218
Mp 40,48,184
v_mul_f32_e32 v219, v13, v219
Cv 217,218,219
v_mul_f32_e32 v220, v13, v220
Mp 44,52,184
Rb 36,1,28672
v_mul_f32_e32 v221, v13, v221
Cv 218,220,221
Mp 36,52,176
v_mul_f32_e32 v222, v13, v222
v_mul_f32_e32 v223, v13, v223
Cv 219,222,223
Mp 48,56,176
Rb 40,1,29696
v_mul_f32_e32 v224, v12, v224
v_mul_f32_e32 v225, v12, v225
s_add_u32 s65, s65, 0x2400
Mp 56,56,184
Cv 224,224,225
v_mul_f32_e32 v226, v12, v226
v_mul_f32_e32 v227, v12, v227
Mp 60,60,184
Rb 44,1,30720
Cv 225,226,227
buffer_atomic_add v16, v7, s[20:23], s55 offen sc0 sc1
s_waitcnt lgkmcnt(8)
Mp 52,60,176
v_mul_f32_e32 v228, v12, v228
v_mul_f32_e32 v229, v12, v229
)" R"(
Cv 226,228,229
Mp 32,0,192
Rb 48,1,32768
v_mul_f32_e32 v230, v12, v230
s_xor_b32 s52, s65, 0x202000
Mp 40,0,200
v_mul_f32_e32 v231, v12, v231
Cv 227,230,231
v_mul_f32_e32 v232, v13, v232
Mp 44,4,200
Rb 52,1,33792
v_mul_f32_e32 v233, v13, v233
Cv 232,232,233
Mp 36,4,192
v_mul_f32_e32 v234, v13, v234
v_mul_f32_e32 v235, v13, v235
Cv 233,234,235
Mp 48,8,192
Rb 56,1,34816
v_mul_f32_e32 v236, v13, v236
v_mul_f32_e32 v237, v13, v237
Mp 56,8,200
Cv 234,236,237
v_mul_f32_e32 v238, v13, v238
v_mul_f32_e32 v239, v13, v239
Mp 60,12,200
Rb 60,1,35840
s_waitcnt vmcnt(15) lgkmcnt(4)
s_barrier
buffer_store_dword v6, v7, s[20:23], s82 offen offset:0 sc0 sc1
Mp 52,12,192
s_add_u32 s40, s67, 0x50000
Cv 235,238,239
v_mul_f32_e32 v240, v12, v240
Mp 32,16,208
Rb 0,1,36864
s_add_u32 m0, s36, 0
v_mul_f32_e32 v241, v12, v241
Mp 40,16,216
buffer_load_dwordx4 v2, s[20:23], s40 offen offset:0 sc1 lds
Cv 240,240,241
Mp 44,20,216
Rb 4,1,37888
v_mul_f32_e32 v242, v12, v242
v_mul_f32_e32 v243, v12, v243
Mp 36,20,208
Cv 241,242,243
v_mul_f32_e32 v244, v12, v244
v_mul_f32_e32 v245, v12, v245
Mp 48,24,208
Rb 8,1,38912
Cv 242,244,245
v_mul_f32_e32 v246, v12, v246
Mp 56,24,216
v_mul_f32_e32 v247, v12, v247
Cv 243,246,247
v_mul_f32_e32 v248, v13, v248
Mp 60,28,216
Rb 12,1,40960
v_mul_f32_e32 v249, v13, v249
Mp 52,28,208
buffer_load_dwordx4 v2, s[20:23], s40 offen offset:1024 sc1 lds
Cv 248,248,249
Mp 32,32,224
Rb 16,1,41984
)" R"(
v_mul_f32_e32 v250, v13, v250
Mp 40,32,232
v_mul_f32_e32 v251, v13, v251
Cv 249,250,251
v_mul_f32_e32 v252, v13, v252
Mp 44,36,232
Rb 20,1,43008
v_mul_f32_e32 v253, v13, v253
Cv 250,252,253
Mp 36,36,224
v_mul_f32_e32 v254, v13, v254
v_mul_f32_e32 v255, v13, v255
Cv 251,254,255
Mp 48,40,224
Rb 24,1,44032
Mp 56,40,232
buffer_load_dwordx4 v2, s[20:23], s40 offen offset:2048 sc1 lds
Mp 60,44,232
Rb 28,1,45056
s_waitcnt lgkmcnt(8)
Mp 52,44,224
Mp 32,48,240
Rb 32,1,46080
Mp 40,48,248
Mp 44,52,248
Rb 36,1,47104
Mp 36,52,240
buffer_load_dwordx4 v2, s[20:23], s40 offen offset:3072 sc1 lds
Mp 48,56,240
Rb 40,1,49152
Mp 56,56,248
Mp 60,60,248
Rb 44,1,50176
s_waitcnt lgkmcnt(8)
Mp 52,60,240
v_mfma_f32_16x16x32_bf16 v[64:67], a[0:3], v[128:131], 0
Rb 48,1,51200
s_add_u32 m0, s36, 4096
v_mfma_f32_16x16x32_bf16 v[72:75], a[0:3], v[136:139], 0
buffer_load_dwordx4 v3, s[20:23], s40 offen offset:0 sc1 lds
Mp 72,4,152
Rb 52,1,52224
Mp 64,4,144
Cv 32,32,33
Cv 33,34,35
Mp 64,8,160
Rb 56,1,53248
Cv 34,36,37
Mp 72,8,168
Cv 35,38,39
Mp 72,12,184
Rb 60,1,54272
s_waitcnt lgkmcnt(8)
Mp 64,12,176
buffer_load_dwordx4 v3, s[20:23], s40 offen offset:1024 sc1 lds
Mp 64,16,192
Rb 0,1,55296
Mp 72,16,200
buffer_store_dwordx4 v[32:35], v2, s[20:23], s64 offen offset:0 sc0 sc1
Mp 72,20,216
Rb 4,1,57344
Mp 64,20,208
Mp 64,24,224
Rb 8,1,58368
)" R"(
Mp 72,24,232
buffer_load_dwordx4 v3, s[20:23], s40 offen offset:2048 sc1 lds
.LzH42:
s_cbranch_execz .LzHR
Mp 72,28,248
Rb 12,1,59392
s_waitcnt lgkmcnt(8)
Mp 64,28,240
Cv 40,40,41
Cv 41,42,43
v_mfma_f32_16x16x32_bf16 v[68:71], a[32:35], v[128:131], 0
Rb 16,1,60416
Cv 42,44,45
v_mfma_f32_16x16x32_bf16 v[76:79], a[32:35], v[136:139], 0
Cv 43,46,47
Mp 76,36,152
Rb 20,1,61440
Mp 68,36,144
buffer_store_dwordx4 v[40:43], v2, s[20:23], s64 offen offset:1024 sc0 sc1
Mp 68,40,160
Rb 24,1,62464
Mp 76,40,168
Mp 76,44,184
Rb 28,1,63488
s_waitcnt lgkmcnt(8)
Mp 68,44,176
Mp 68,48,192
Rb 32,10,0
Mp 76,48,200
Cv 48,48,49
Cv 49,50,51
Mp 76,52,216
Rb 36,10,1024
Cv 50,52,53
Mp 68,52,208
Cv 51,54,55
Mp 68,56,224
Rb 40,10,2048
Mp 76,56,232
buffer_store_dwordx4 v[48:51], v2, s[20:23], s64 offen offset:2048 sc0 sc1
Mp 76,60,248
Rb 44,10,3072
s_waitcnt vmcnt(19) lgkmcnt(4)
s_barrier
Mp 68,60,240
s_add_u32 s40, s67, 0x60000
v_mfma_f32_16x16x32_bf16 v[80:83], a[0:3], v[128:131], 0
Rb 48,10,4096
s_add_u32 m0, s36, 32768
v_mfma_f32_16x16x32_bf16 v[88:91], a[0:3], v[136:139], 0
buffer_load_dwordx4 v2, s[20:23], s40 offen offset:0 sc1 lds
Mp 88,4,152
Rb 52,10,5120
Mp 80,4,144
Cv 56,56,57
Cv 57,58,59
Cv 58,60,61
Mp 80,8,160
Rb 56,10,6144
v_bfe_u32 v11, v0, 2, 2
Cv 59,62,63
Mp 88,8,168
v_lshrrev_b32 v4, 4, v0
v_xor_b32 v11, v11, v4
Mp 88,12,184
)" R"(
Rb 60,10,8192
Mp 80,12,176
buffer_load_dwordx4 v2, s[20:23], s40 offen offset:1024 sc1 lds
Mp 80,16,192
Rb 0,10,9216
v_bfe_u32 v4, v0, 1, 1
Mp 88,16,200
v_lshl_or_b32 v11, v11, 1, v4
v_and_b32 v4, 1, v0
Mp 88,20,216
Rb 4,10,10240
v_mul_u32_u24 v4, 0xffff, v4
buffer_store_dwordx4 v[56:59], v2, s[20:23], s64 offen offset:3072 sc0 sc1
Mp 80,20,208
v_add_u32 v4, 1, v4
v_mul_u32_u24 v4, 0x3f80, v4
Mp 80,24,224
Rb 8,10,11264
v_cmp_eq_u32_e32 vcc, 0, v11
Mp 88,24,232
buffer_load_dwordx4 v2, s[20:23], s40 offen offset:2048 sc1 lds
Mp 88,28,248
Rb 12,10,12288
s_waitcnt lgkmcnt(8)
Mp 80,28,240
v_cmp_eq_u32_e64 s[28:29], 1, v11
v_accvgpr_write_b32 a192, 0
v_mfma_f32_16x16x32_bf16 v[84:87], a[32:35], v[128:131], 0
Rb 16,10,13312
v_accvgpr_write_b32 a193, 0
v_mfma_f32_16x16x32_bf16 v[92:95], a[32:35], v[136:139], 0
v_accvgpr_write_b32 a196, 0
v_accvgpr_write_b32 a197, 0
Cv 64,64,65
Mp 92,36,152
Rb 20,10,14336
v_cndmask_b32_e32 v11, 0, v4, vcc
Mp 84,36,144
buffer_load_dwordx4 v2, s[20:23], s40 offen offset:3072 sc1 lds
Cv 65,66,67
Mp 84,40,160
Rb 24,10,16384
v_cndmask_b32_e64 v4, 0, v4, s[28:29]
Mp 92,40,168
v_accvgpr_write_b32 a194, v11
v_accvgpr_write_b32 a195, v4
Cv 66,68,69
Mp 92,44,184
Rb 28,10,17408
s_waitcnt lgkmcnt(8)
Mp 84,44,176
Cv 67,70,71
buffer_store_dwordx4 v[64:67], v3, s[20:23], s64 offen offset:0 sc0 sc1
Mp 84,48,192
Rb 32,10,18432
s_add_u32 m0, s36, 36864
Mp 92,48,200
buffer_load_dwordx4 v3, s[20:23], s40 offen offset:0 sc1 lds
Mp 92,52,216
Rb 36,10,19456
Mp 84,52,208
Mp 84,56,224
Rb 40,10,20480
Mp 92,56,232
)" R"(
Cv 72,72,73
Cv 73,74,75
Mp 92,60,248
Rb 44,10,21504
s_waitcnt lgkmcnt(8)
Mp 84,60,240
buffer_load_dwordx4 v3, s[20:23], s40 offen offset:1024 sc1 lds
v_mfma_f32_16x16x32_bf16 v[96:99], a[0:3], v[128:131], 0
Rb 48,10,22528
Cv 74,76,77
v_mfma_f32_16x16x32_bf16 v[104:107], a[0:3], v[136:139], 0
Cv 75,78,79
Mp 104,4,152
Rb 52,10,24576
Mp 96,4,144
buffer_store_dwordx4 v[72:75], v3, s[20:23], s64 offen offset:1024 sc0 sc1
Mp 96,8,160
Rb 56,10,25600
Mp 104,8,168
buffer_load_dwordx4 v3, s[20:23], s40 offen offset:2048 sc1 lds
Mp 104,12,184
Rb 60,10,26624
s_waitcnt lgkmcnt(8)
Mp 96,12,176
Mp 96,16,192
Rb 0,10,27648
Mp 104,16,200
Cv 80,80,81
Cv 81,82,83
Mp 104,20,216
Rb 4,10,28672
Cv 82,84,85
Mp 96,20,208
Cv 83,86,87
Mp 96,24,224
Rb 8,10,29696
Mp 104,24,232
buffer_store_dwordx4 v[80:83], v3, s[20:23], s64 offen offset:2048 sc0 sc1
Mp 104,28,248
Rb 12,10,30720
s_waitcnt lgkmcnt(8)
Mp 96,28,240
v_mfma_f32_16x16x32_bf16 v[100:103], a[32:35], v[128:131], 0
Rb 16,8,32768
v_mfma_f32_16x16x32_bf16 v[108:111], a[32:35], v[136:139], 0
Mp 108,36,152
Rb 20,8,33792
Mp 100,36,144
Cv 88,88,89
Cv 89,90,91
Mp 100,40,160
Rb 24,8,34816
Cv 90,92,93
Mp 108,40,168
Cv 91,94,95
Mp 108,44,184
Rb 28,8,35840
s_waitcnt vmcnt(23) lgkmcnt(4)
s_barrier
Mp 100,44,176
s_add_u32 s40, s67, 0x70000
Mp 100,48,192
Rb 32,8,36864
s_add_u32 m0, s36, 131072
Mp 108,48,200
)" R"(
buffer_load_dwordx4 v2, s[20:23], s40 offen offset:0 sc1 lds
Mp 108,52,216
Rb 36,8,37888
buffer_store_dwordx4 v[88:91], v3, s[20:23], s64 offen offset:3072 sc0 sc1
Mp 100,52,208
s_mov_b32 s8, s74
s_mov_b32 s9, s75
Mp 100,56,224
Rb 40,8,38912
s_mov_b32 s10, 0x1c0000
Mp 108,56,232
s_mov_b32 s11, 0x20000
v_and_b32 v174, 1, v0
Mp 108,60,248
Rb 44,8,40960
Mp 100,60,240
buffer_load_dwordx4 v2, s[20:23], s40 offen offset:1024 sc1 lds
v_mfma_f32_16x16x32_bf16 v[112:115], a[0:3], v[128:131], 0
Rb 48,8,41984
v_mul_u32_u24 v174, 0xffff, v174
v_mfma_f32_16x16x32_bf16 v[120:123], a[0:3], v[136:139], 0
v_add_u32 v174, 1, v174
v_mul_u32_u24 v174, 0x3f80, v174
Mp 120,4,152
Rb 52,8,43008
v_bfe_u32 v173, v0, 1, 3
Mp 112,4,144
Cv 96,96,97
Cv 97,98,99
Mp 112,8,160
Rb 56,8,44032
Cv 98,100,101
Mp 120,8,168
buffer_load_dwordx4 v2, s[20:23], s40 offen offset:2048 sc1 lds
Mp 120,12,184
Rb 60,8,45056
s_waitcnt lgkmcnt(8)
Mp 112,12,176
Cv 99,102,103
v_cmp_eq_u32_e32 vcc, 0, v173
v_and_b32 v167, 0xf0, v1
Mp 112,16,192
Rb 0,8,46080
v_cndmask_b32_e32 v132, 0, v174, vcc
Mp 120,16,200
s_add_u32 s64, s64, 0x10000
buffer_store_dwordx4 v[96:99], v2, s[20:23], s64 offen offset:0 sc0 sc1
Mp 120,20,216
Rb 4,8,47104
v_cmp_eq_u32_e32 vcc, 1, v173
Mp 112,20,208
buffer_load_dwordx4 v2, s[20:23], s40 offen offset:3072 sc1 lds
Mp 112,24,224
Rb 8,8,49152
v_cndmask_b32_e32 v133, 0, v174, vcc
Mp 120,24,232
v_cmp_eq_u32_e32 vcc, 2, v173
v_mul_u32_u24 v166, 56, v167
Mp 120,28,248
Rb 12,8,50176
s_waitcnt lgkmcnt(8)
Mp 112,28,240
Cv 104,104,105
Cv 105,106,107
)" R"(
v_mfma_f32_16x16x32_bf16 v[116:119], a[32:35], v[128:131], 0
Rb 16,8,51200
s_add_u32 m0, s36, 135168
v_mfma_f32_16x16x32_bf16 v[124:127], a[32:35], v[136:139], 0
buffer_load_dwordx4 v3, s[20:23], s40 offen offset:0 sc1 lds
Mp 124,36,152
Rb 20,8,52224
Cv 106,108,109
Mp 116,36,144
Cv 107,110,111
v_cndmask_b32_e32 v134, 0, v174, vcc
Mp 116,40,160
Rb 24,8,53248
v_cmp_eq_u32_e32 vcc, 3, v173
Mp 124,40,168
buffer_store_dwordx4 v[104:107], v2, s[20:23], s64 offen offset:1024 sc0 sc1
Mp 124,44,184
Rb 28,8,54272
s_waitcnt lgkmcnt(8)
Mp 116,44,176
buffer_load_dwordx4 v3, s[20:23], s40 offen offset:1024 sc1 lds
v_cndmask_b32_e32 v135, 0, v174, vcc
Mp 116,48,192
Rb 32,8,55296
v_cmp_eq_u32_e32 vcc, 4, v173
Mp 124,48,200
s_nop 0
v_cndmask_b32_e32 v140, 0, v174, vcc
v_cmp_eq_u32_e32 vcc, 5, v173
Mp 124,52,216
Rb 36,8,57344
v_cndmask_b32_e32 v141, 0, v174, vcc
Mp 116,52,208
v_cmp_eq_u32_e32 vcc, 6, v173
v_and_b32 v165, 0x80, v1
Mp 116,56,224
Rb 40,8,58368
v_cndmask_b32_e32 v142, 0, v174, vcc
Mp 124,56,232
buffer_load_dwordx4 v3, s[20:23], s40 offen offset:2048 sc1 lds
Mp 124,60,248
Rb 44,8,59392
s_waitcnt vmcnt(8) lgkmcnt(8)
buffer_store_dword v6, v7, s[20:23], s65 offen offset:0 sc0 sc1
Mp 116,60,240
v_cmp_eq_u32_e32 vcc, 7, v173
v_lshrrev_b32 v173, 4, v0
v_mfma_f32_16x16x32_bf16 v[32:35], a[0:3], v[128:131], 0
Rb 48,8,60416
v_cndmask_b32_e32 v143, 0, v174, vcc
v_mfma_f32_16x16x32_bf16 v[40:43], a[0:3], v[136:139], 0
v_lshl_or_b32 v164, v173, 25, v167
v_lshl_add_u32 v166, v173, 3, v166
Mp 40,4,152
Rb 52,8,61440
v_lshrrev_b32_e64 v173, v173, s85
Mp 32,4,144
Cv 112,112,113
Cv 113,114,115
Mp 32,8,160
Rb 56,8,62464
Cv 114,116,117
Mp 40,8,168
Cv 115,118,119
)" R"(
v_bfe_u32 v174, v173, 4, 1
Mp 40,12,184
Rb 60,8,63488
s_waitcnt lgkmcnt(8)
Mp 32,12,176
buffer_store_dwordx4 v[112:115], v2, s[20:23], s64 offen offset:2048 sc0 sc1
ds_write_b32 v9, v16 offset:39936
Mp 32,16,192
Rb 0,8,0
v_lshl_or_b32 v183, v174, 13, v164
Mp 40,16,200
v_and_b32 v174, 1, v173
v_lshl_or_b32 v164, v174, 13, v164
Mp 40,20,216
Rb 4,8,1024
v_and_b32 v174, 7, v0
Mp 32,20,208
v_lshl_or_b32 v165, v174, 8, v165
v_lshlrev_b32 v165, 1, v165
Mp 32,24,224
Rb 8,8,2048
s_mov_b32 s93, 0x1111
Mp 40,24,232
Cv 120,120,121
Cv 121,122,123
Mp 40,28,248
Rb 12,8,3072
s_waitcnt lgkmcnt(3)
s_barrier
Mp 32,28,240
Cv 122,124,125
Cv 123,126,127
ds_read_b32 v16, v8 offset:39936
v_mfma_f32_16x16x32_bf16 v[36:39], a[32:35], v[128:131], 0
Rb 16,8,4096
v_lshrrev_b32 v173, 2, v0
v_mfma_f32_16x16x32_bf16 v[44:47], a[32:35], v[136:139], 0
buffer_store_dwordx4 v[120:123], v2, s[20:23], s64 offen offset:3072 sc0 sc1
Mp 44,36,152
Rb 20,8,5120
v_mul_u32_u24 v173, 832, v173
Mp 36,36,144
v_add_u32 v173, v173, v1
v_lshrrev_b32 v174, 1, v0
Mp 36,40,160
Rb 24,8,6144
v_and_b32 v174, 24, v174
Mp 44,40,168
v_lshl_add_u32 v174, v167, 2, v174
s_add_u32 s59, s36, 98304
Mp 44,44,184
Rb 28,8,8192
s_waitcnt lgkmcnt(4)
Mp 36,44,176
v_readfirstlane_b32 s82, v16
v_add_u32 v174, s59, v174
Mp 36,48,192
Rb 32,8,9216
s_lshl2_add_u32 s56, s82, s33
Mp 44,48,200
s_mul_i32 s88, s56, 4682
s_lshr_b32 s58, s88, 16
Mp 44,52,216
Rb 36,8,10240
s_mul_i32 s57, s58, 14
)" R"(
Mp 36,52,208
s_sub_u32 s57, s56, s57
s_lshl_b32 s40, s58, 12
Mp 36,56,224
Rb 40,8,11264
Mp 44,56,232
s_cmp_gt_u32 s57, 6
s_cselect_b32 s88, 128, 0
Mp 44,60,248
Rb 44,8,12288
Mp 36,60,240
buffer_load_dword v4, v7, s[20:23], s52 offen offset:0 sc0 sc1
s_add_u32 s40, s40, s88
v_mfma_f32_16x16x32_bf16 v[48:51], a[0:3], v[128:131], 0
Rb 48,8,13312
s_add_u32 s40, s40, 0x1510000
v_mfma_f32_16x16x32_bf16 v[56:59], a[0:3], v[136:139], 0
s_lshl_b32 s55, s58, 6
s_add_u32 s55, s55, s57
Mp 56,4,152
Rb 52,8,14336
Mp 48,4,144
Cv 32,32,33
Cv 33,34,35
Mp 48,8,160
Rb 56,8,16384
Cv 34,36,37
Mp 56,8,168
Cv 35,38,39
Mp 56,12,184
Rb 60,8,17408
s_waitcnt lgkmcnt(8)
Mp 48,12,176
buffer_store_dwordx4 v[32:35], v3, s[20:23], s64 offen offset:0 sc0 sc1
Mp 48,16,192
Rb 0,8,18432
Mp 56,16,200
s_cmp_gt_u32 s57, 6
s_cselect_b32 s88, 9, 0
Mp 56,20,216
Rb 4,8,19456
s_add_u32 s55, s55, s88
Mp 48,20,208
s_and_b32 s88, s58, 32
s_lshl_b32 s88, s88, 8
Mp 48,24,224
Rb 8,8,20480
v_xor_b32 v164, s88, v164
Mp 56,24,232
Cv 40,40,41
Cv 41,42,43
Mp 56,28,248
Rb 12,8,21504
s_waitcnt lgkmcnt(8)
Mp 48,28,240
Cv 42,44,45
Cv 43,46,47
v_xor_b32 v183, s88, v183
v_mfma_f32_16x16x32_bf16 v[52:55], a[32:35], v[128:131], 0
Rb 16,8,22528
s_lshl_b32 s55, s55, 10
v_mfma_f32_16x16x32_bf16 v[60:63], a[32:35], v[136:139], 0
buffer_store_dwordx4 v[40:43], v3, s[20:23], s64 offen offset:1024 sc0 sc1
Mp 60,36,152
)" R"(
Rb 20,8,24576
s_add_u32 s55, s55, 0x1000000
Mp 52,36,144
s_and_b32 s88, s57, 1
s_lshl1_add_u32 s58, s58, s88
Mp 52,40,160
Rb 24,8,25600
s_lshl_b32 s88, s58, 8
Mp 60,40,168
v_add_u32 v167, s88, v167
s_mul_i32 s59, s58, 14336
Mp 60,44,184
Rb 28,8,26624
s_waitcnt lgkmcnt(8)
Mp 52,44,176
s_lshr_b32 s88, s57, 1
s_lshl_b32 s88, s88, 6
s_add_u32 s88, s88, s59
Mp 52,48,192
Rb 32,8,27648
v_add_u32 v166, s88, v166
Mp 60,48,200
s_mov_b32 s57, 0
s_add_u32 m0, s36, 98304
Mp 60,52,216
Rb 36,8,28672
Mp 52,52,208
buffer_load_dwordx4 v173, s[12:15], s88 offen offset:0 lds
Mp 52,56,224
Rb 40,8,29696
s_add_u32 m0, s36, 98880
Mp 60,56,232
buffer_load_dwordx4 v173, s[12:15], s88 offen offset:448 lds
Mp 60,60,248
Rb 44,8,30720
s_waitcnt lgkmcnt(8)
Mp 52,60,240
s_mov_b32 s88, 0x1500008
buffer_load_dword v167, v167, s[20:23], s88 offen sc0 sc1
v_mfma_f32_16x16x32_bf16 v[64:67], a[0:3], v[128:131], 0
Rb 48,1,0
v_mfma_f32_16x16x32_bf16 v[72:75], a[0:3], v[136:139], 0
Mp 72,4,152
Rb 52,1,1024
Mp 64,4,144
Mp 64,8,160
Rb 56,1,2048
Mp 72,8,168
Mp 72,12,184
Rb 60,1,3072
s_waitcnt vmcnt(5) lgkmcnt(8)
s_barrier
v_cmp_eq_u32_e32 vcc, s3, v4
s_andn2_b64 s[28:29], 1, vcc
s_cbranch_scc1 .Lzsp11
.Lzok11:
Mp 64,12,176
buffer_load_dwordx4 a[64:67], v2, s[20:23], s66 offen offset:0 sc0 sc1
Mp 64,16,192
Rb 0,1,4096
Mp 72,16,200
Mp 72,20,216
Rb 4,1,5120
Mp 64,20,208
buffer_load_dwordx4 a[68:71], v2, s[20:23], s66 offen offset:1024 sc0 sc1
)" R"(
Mp 64,24,224
Rb 8,1,6144
Mp 72,24,232
Mp 72,28,248
Rb 12,1,8192
s_waitcnt lgkmcnt(8)
Mp 64,28,240
buffer_load_dwordx4 a[72:75], v2, s[20:23], s66 offen offset:2048 sc0 sc1
v_mfma_f32_16x16x32_bf16 v[68:71], a[32:35], v[128:131], 0
Rb 16,1,9216
v_mfma_f32_16x16x32_bf16 v[76:79], a[32:35], v[136:139], 0
Mp 76,36,152
Rb 20,1,10240
Mp 68,36,144
buffer_load_dwordx4 a[76:79], v2, s[20:23], s66 offen offset:3072 sc0 sc1
Mp 68,40,160
Rb 24,1,11264
Mp 76,40,168
Mp 76,44,184
Rb 28,1,12288
s_waitcnt vmcnt(7) lgkmcnt(8)
buffer_store_dword v6, v7, s[20:23], s65 offen offset:128 sc0 sc1
Mp 68,44,176
buffer_load_dwordx4 a[80:83], v3, s[20:23], s66 offen offset:0 sc0 sc1
Mp 68,48,192
Rb 32,1,13312
Mp 76,48,200
Mp 76,52,216
Rb 36,1,14336
Mp 68,52,208
buffer_load_dwordx4 a[84:87], v3, s[20:23], s66 offen offset:1024 sc0 sc1
Mp 68,56,224
Rb 40,1,16384
Mp 76,56,232
Mp 76,60,248
Rb 44,1,17408
s_waitcnt lgkmcnt(8)
Mp 68,60,240
buffer_load_dwordx4 a[88:91], v3, s[20:23], s66 offen offset:2048 sc0 sc1
v_mfma_f32_16x16x32_bf16 v[80:83], a[0:3], v[128:131], 0
Rb 48,1,18432
v_mfma_f32_16x16x32_bf16 v[88:91], a[0:3], v[136:139], 0
Mp 88,4,152
Rb 52,1,19456
Mp 80,4,144
buffer_load_dwordx4 a[92:95], v3, s[20:23], s66 offen offset:3072 sc0 sc1
Mp 80,8,160
Rb 56,1,20480
.LzH43:
s_cbranch_execz .LzHR
Mp 88,8,168
Mp 88,12,184
Rb 60,1,21504
s_waitcnt lgkmcnt(8)
Mp 80,12,176
Mp 80,16,192
Rb 0,1,22528
Mp 88,16,200
Mp 88,20,216
Rb 4,1,24576
Mp 80,20,208
Mp 80,24,224
Rb 8,1,25600
Mp 88,24,232
)" R"(
Mp 88,28,248
Rb 12,1,26624
s_waitcnt lgkmcnt(8)
Mp 80,28,240
v_mfma_f32_16x16x32_bf16 v[84:87], a[32:35], v[128:131], 0
Rb 16,1,27648
v_mfma_f32_16x16x32_bf16 v[92:95], a[32:35], v[136:139], 0
Mp 92,36,152
Rb 20,1,28672
Mp 84,36,144
Mp 84,40,160
Rb 24,1,29696
Mp 92,40,168
Mp 92,44,184
Rb 28,1,30720
s_waitcnt lgkmcnt(8)
Mp 84,44,176
buffer_load_dword v4, v7, s[20:23], s52 offen offset:128 sc0 sc1
Mp 84,48,192
Rb 32,1,32768
Mp 92,48,200
Mp 92,52,216
Rb 36,1,33792
Mp 84,52,208
Mp 84,56,224
Rb 40,1,34816
Mp 92,56,232
Mp 92,60,248
Rb 44,1,35840
s_waitcnt vmcnt(6)
s_barrier
Mp 84,60,240
Mv 48,194,64
Mv 56,194,68
Mv 52,192,64
Mv 60,192,68
Mv 64,194,72
Mv 72,194,76
Mv 68,192,72
s_waitcnt vmcnt(3) lgkmcnt(10)
Mv 76,192,76
Cv 48,48,49
Cv 49,50,51
Mv 80,194,80
Cv 50,52,53
Cv 51,54,55
Mv 88,194,84
s_add_u32 s29, s73, 0x0
buffer_store_dwordx4 v[48:51], v1, s[20:23], s29 offen sc0 sc1
Mv 84,192,80
Mv 92,192,84
v_mfma_f32_16x16x32_bf16 v[96:99], a[0:3], v[128:131], 0
Rb 48,1,36864
v_mfma_f32_16x16x32_bf16 v[104:107], a[0:3], v[136:139], 0
Mp 104,4,152
Rb 52,1,37888
s_waitcnt lgkmcnt(8)
Mp 96,4,144
Cv 56,56,57
Cv 57,58,59
ds_read_b64 v[188:189], v174 offset:0
Mp 96,8,160
Rb 56,1,38912
Cv 58,60,61
Mp 104,8,168
)" R"(
Cv 59,62,63
ds_read_b64 v[190:191], v174 offset:32
Mp 104,12,184
Rb 60,1,40960
Mp 96,12,176
s_add_u32 s29, s73, 0x400
buffer_store_dwordx4 v[56:59], v1, s[20:23], s29 offen sc0 sc1
ds_read_b64 v[196:197], v174 offset:1024
Mp 96,16,192
Rb 0,1,41984
Mp 104,16,200
ds_read_b64 v[198:199], v174 offset:1056
Mp 104,20,216
Rb 4,1,43008
s_waitcnt lgkmcnt(12)
Mp 96,20,208
Mp 96,24,224
Rb 8,1,44032
Mp 104,24,232
Cv 64,64,65
Cv 65,66,67
Mp 104,28,248
Rb 12,1,45056
Cv 66,68,69
Mp 96,28,240
Cv 67,70,71
v_mfma_f32_16x16x32_bf16 v[100:103], a[32:35], v[128:131], 0
Rb 16,1,46080
v_mfma_f32_16x16x32_bf16 v[108:111], a[32:35], v[136:139], 0
s_add_u32 s29, s73, 0x800
buffer_store_dwordx4 v[64:67], v1, s[20:23], s29 offen sc0 sc1
Mp 108,36,152
Rb 20,1,47104
s_waitcnt vmcnt(3) lgkmcnt(11)
v_cmp_eq_u32_e32 vcc, s3, v4
s_andn2_b64 s[28:29], 1, vcc
s_cbranch_scc1 .Lzsp12
.Lzok12:
Mp 100,36,144
s_add_u32 s66, s66, 0x10000
buffer_load_dwordx4 a[96:99], v2, s[20:23], s66 offen offset:0 sc0 sc1
Mp 100,40,160
Rb 24,1,49152
v_sub_f32_e32 v172, 1.0, v167
Mp 108,40,168
Cv 172,172,172
v_lshlrev_b32 v172, 16, v172
Mp 108,44,184
Rb 28,1,50176
v_lshlrev_b32 v204, 16, v188
Mp 100,44,176
buffer_load_dwordx4 a[100:103], v2, s[20:23], s66 offen offset:1024 sc0 sc1
Mp 100,48,192
Rb 32,1,51200
Cv 72,72,73
Mp 108,48,200
Cv 73,74,75
Cv 74,76,77
Mp 108,52,216
Rb 36,1,52224
s_waitcnt lgkmcnt(12)
Mp 100,52,208
buffer_load_dwordx4 a[104:107], v2, s[20:23], s66 offen offset:2048 sc0 sc1
Mp 100,56,224
)" R"(
Rb 40,1,53248
Cv 75,78,79
Mp 108,56,232
s_add_u32 s29, s73, 0xc00
buffer_store_dwordx4 v[72:75], v1, s[20:23], s29 offen sc0 sc1
Mp 108,60,248
Rb 44,1,54272
v_and_b32 v205, 0xffff0000, v188
Mp 100,60,240
buffer_load_dwordx4 a[108:111], v2, s[20:23], s66 offen offset:3072 sc0 sc1
Mv 96,194,88
v_mul_f32_e32 v204, v172, v204
v_mul_f32_e32 v205, v172, v205
Mv 104,194,92
Cv 205,204,205
v_lshlrev_b32 v204, 16, v205
Mv 100,192,88
v_and_b32 v205, 0xffff0000, v205
s_waitcnt lgkmcnt(8)
Mv 108,192,92
buffer_load_dwordx4 a[112:115], v3, s[20:23], s66 offen offset:0 sc0 sc1
v_mfma_f32_16x16x32_bf16 v[112:115], a[0:3], v[128:131], 0
Rb 48,1,55296
Cv 80,80,81
v_mfma_f32_16x16x32_bf16 v[120:123], a[0:3], v[136:139], 0
Cv 81,82,83
Cv 82,84,85
Mp 120,4,152
Rb 52,1,57344
Cv 83,86,87
Mp 112,4,144
buffer_load_dwordx4 a[116:119], v3, s[20:23], s66 offen offset:1024 sc0 sc1
Mp 112,8,160
Rb 56,1,58368
v_lshlrev_b32 v206, 16, v189
Mp 120,8,168
s_add_u32 s29, s73, 0x1000
buffer_store_dwordx4 v[80:83], v1, s[20:23], s29 offen sc0 sc1
Mp 120,12,184
Rb 60,1,59392
s_waitcnt lgkmcnt(8)
Mp 112,12,176
v_and_b32 v207, 0xffff0000, v189
v_mul_f32_e32 v206, v172, v206
Mp 112,16,192
Rb 0,1,60416
v_mul_f32_e32 v207, v172, v207
Mp 120,16,200
Cv 207,206,207
v_lshlrev_b32 v206, 16, v207
Mp 120,20,216
Rb 4,1,61440
v_and_b32 v207, 0xffff0000, v207
Mp 112,20,208
Cv 88,88,89
Cv 89,90,91
Mp 112,24,224
Rb 8,1,62464
Cv 90,92,93
Mp 120,24,232
Cv 91,94,95
v_lshlrev_b32 v212, 16, v190
Mp 120,28,248
Rb 12,1,63488
)" R"(
s_waitcnt lgkmcnt(8)
Mp 112,28,240
s_add_u32 s29, s73, 0x1400
buffer_store_dwordx4 v[88:91], v1, s[20:23], s29 offen sc0 sc1
v_mfma_f32_16x16x32_bf16 v[116:119], a[32:35], v[128:131], 0
Rb 16,10,0
v_and_b32 v213, 0xffff0000, v190
v_mfma_f32_16x16x32_bf16 v[124:127], a[32:35], v[136:139], 0
v_mul_f32_e32 v212, v172, v212
v_mul_f32_e32 v213, v172, v213
Mp 124,36,152
Rb 20,10,1024
Cv 213,212,213
Mp 116,36,144
v_lshlrev_b32 v212, 16, v213
v_and_b32 v213, 0xffff0000, v213
Mp 116,40,160
Rb 24,10,2048
v_lshlrev_b32 v214, 16, v191
Mp 124,40,168
Cv 96,96,97
Cv 97,98,99
Mp 124,44,184
Rb 28,10,3072
s_waitcnt lgkmcnt(8)
Mp 116,44,176
Cv 98,100,101
Cv 99,102,103
Mp 116,48,192
Rb 32,10,4096
v_and_b32 v215, 0xffff0000, v191
Mp 124,48,200
s_add_u32 s29, s73, 0x1800
buffer_store_dwordx4 v[96:99], v1, s[20:23], s29 offen sc0 sc1
Mp 124,52,216
Rb 36,10,5120
v_mul_f32_e32 v214, v172, v214
Mp 116,52,208
v_mul_f32_e32 v215, v172, v215
Cv 215,214,215
Mp 116,56,224
Rb 40,10,6144
v_lshlrev_b32 v214, 16, v215
Mp 124,56,232
v_and_b32 v215, 0xffff0000, v215
v_lshlrev_b32 v220, 16, v196
Mp 124,60,248
Rb 44,10,8192
s_waitcnt vmcnt(8) lgkmcnt(10)
Mp 116,60,240
Cv 104,104,105
Cv 105,106,107
Mv 112,194,96
Cv 106,108,109
Cv 107,110,111
Mv 120,194,100
s_add_u32 s29, s73, 0x4000
buffer_store_dwordx4 v[104:107], v1, s[20:23], s29 offen sc0 sc1
Mv 116,192,96
v_and_b32 v221, 0xffff0000, v196
v_mul_f32_e32 v220, v172, v220
Mv 124,192,100
v_mul_f32_e32 v221, v172, v221
Cv 221,220,221
)" R"(
v_mfma_f32_16x16x32_bf16 v[32:35], a[0:3], v[128:131], 0
Rb 48,10,9216
v_lshlrev_b32 v220, 16, v221
v_mfma_f32_16x16x32_bf16 v[40:43], a[0:3], v[136:139], 0
v_and_b32 v221, 0xffff0000, v221
v_lshlrev_b32 v222, 16, v197
Mp 40,4,152
Rb 52,10,10240
s_waitcnt lgkmcnt(8)
Mp 32,4,144
Cv 112,112,113
Cv 113,114,115
Mp 32,8,160
Rb 56,10,11264
Cv 114,116,117
Mp 40,8,168
Cv 115,118,119
v_and_b32 v223, 0xffff0000, v197
Mp 40,12,184
Rb 60,10,12288
v_mul_f32_e32 v222, v172, v222
Mp 32,12,176
s_add_u32 s29, s73, 0x4400
buffer_store_dwordx4 v[112:115], v1, s[20:23], s29 offen sc0 sc1
Mp 32,16,192
Rb 0,10,13312
v_mul_f32_e32 v223, v172, v223
Mp 40,16,200
Cv 223,222,223
v_lshlrev_b32 v222, 16, v223
Mp 40,20,216
Rb 4,10,14336
s_waitcnt lgkmcnt(8)
Mp 32,20,208
v_and_b32 v223, 0xffff0000, v223
v_lshlrev_b32 v228, 16, v198
Mp 32,24,224
Rb 8,10,16384
v_and_b32 v229, 0xffff0000, v198
Mp 40,24,232
Cv 120,120,121
Cv 121,122,123
Mp 40,28,248
Rb 12,10,17408
Cv 122,124,125
Mp 32,28,240
Cv 123,126,127
v_mul_f32_e32 v228, v172, v228
v_mfma_f32_16x16x32_bf16 v[36:39], a[32:35], v[128:131], 0
Rb 16,10,18432
v_mul_f32_e32 v229, v172, v229
v_mfma_f32_16x16x32_bf16 v[44:47], a[32:35], v[136:139], 0
s_add_u32 s29, s73, 0x4800
buffer_store_dwordx4 v[120:123], v1, s[20:23], s29 offen sc0 sc1
Mp 44,36,152
Rb 20,10,19456
s_waitcnt lgkmcnt(8)
Mp 36,36,144
Cv 229,228,229
v_lshlrev_b32 v228, 16, v229
Mp 36,40,160
Rb 24,10,20480
v_and_b32 v229, 0xffff0000, v229
Mp 44,40,168
)" R"(
v_lshlrev_b32 v230, 16, v199
v_and_b32 v231, 0xffff0000, v199
Mp 44,44,184
Rb 28,10,21504
v_mul_f32_e32 v230, v172, v230
Mp 36,44,176
v_mul_f32_e32 v231, v172, v231
Cv 231,230,231
Mp 36,48,192
Rb 32,10,22528
v_lshlrev_b32 v230, 16, v231
Mp 44,48,200
v_and_b32 v231, 0xffff0000, v231
Mp 44,52,216
Rb 36,10,24576
s_waitcnt vmcnt(3) lgkmcnt(10)
buffer_store_dword v6, v7, s[20:23], s68 offen offset:0 sc0 sc1
Mp 36,52,208
Mp 36,56,224
Rb 40,10,25600
Mp 44,56,232
Mp 44,60,248
Rb 44,10,26624
Mp 36,60,240
Mv 32,194,104
Mv 40,194,108
Mv 36,192,104
s_waitcnt lgkmcnt(8)
Mv 44,192,108
v_mfma_f32_16x16x32_bf16 v[48:51], a[0:3], v[128:131], 0
Rb 48,10,27648
v_mfma_f32_16x16x32_bf16 v[56:59], a[0:3], v[136:139], 0
Mp 56,4,152
Rb 52,10,28672
Mp 48,4,144
Cv 32,32,33
Cv 33,34,35
Mp 48,8,160
Rb 56,10,29696
Cv 34,36,37
Mp 56,8,168
Cv 35,38,39
Mp 56,12,184
Rb 60,10,30720
s_waitcnt lgkmcnt(8)
Mp 48,12,176
s_add_u32 s29, s73, 0x4c00
buffer_store_dwordx4 v[32:35], v1, s[20:23], s29 offen sc0 sc1
Mp 48,16,192
Mp 56,16,200
Mp 56,20,216
Mp 48,20,208
Mp 48,24,224
Mp 56,24,232
Cv 40,40,41
Cv 41,42,43
buffer_store_dword v6, v7, s[20:23], s68 offen offset:128 sc0 sc1
Mp 56,28,248
Cv 42,44,45
s_waitcnt lgkmcnt(4)
Mp 48,28,240
Cv 43,46,47
v_mfma_f32_16x16x32_bf16 v[52:55], a[32:35], v[128:131], 0
s_add_u32 s29, s73, 0x5000
)" R"(
buffer_store_dwordx4 v[40:43], v1, s[20:23], s29 offen sc0 sc1
v_mfma_f32_16x16x32_bf16 v[60:63], a[32:35], v[136:139], 0
buffer_load_dword v4, v165, s[20:23], s40 offen sc0 sc1
Mp 60,36,152
Mp 52,36,144
Mp 52,40,160
Mp 60,40,168
Mp 60,44,184
s_waitcnt lgkmcnt(0)
Mp 52,44,176
Mp 52,48,192
Mp 60,48,200
Mp 60,52,216
Mp 52,52,208
Mp 52,56,224
Mp 60,56,232
Mp 60,60,248
Mp 52,60,240
Mv 48,194,112
Mv 56,194,116
Mv 52,192,112
Mv 60,192,116
s_nop 7
Cv 48,48,49
Cv 49,50,51
Cv 50,52,53
Cv 51,54,55
s_add_u32 s29, s73, 0x5400
buffer_store_dwordx4 v[48:51], v1, s[20:23], s29 offen sc0 sc1
Cv 56,56,57
Cv 57,58,59
Cv 58,60,61
Cv 59,62,63
s_add_u32 s29, s73, 0x5800
buffer_store_dwordx4 v[56:59], v1, s[20:23], s29 offen sc0 sc1
s_branch .LzEND
.LzCEND:
)" : "+{s[0:1]}"(ka), "+{s2}"(bid), "+{v0}"(tid) :
   : "memory", "scc", "vcc", "v1", "v2", "v3", "v4", "v5", "v6", "v7", "v8", "v9", R256("v"), V10("a"), R256("a"),
     "s3", "s4", "s5", "s6", "s7", "s8", "s9", V10("s1"), V10("s2"), "s30", "s31", "s33", "s34", "s35", "s36", "s37", "s38", "s39",
     V10("s4"), V10("s5"), V10("s6"), V10("s7"), V10("s8"), V10("s9"));
}

extern "C" void kernel_launch(void* const* d_in, const int* in_sizes, int n_in, void* d_out, int out_size, void* d_ws, size_t ws_size, hipStream_t stream) {
  (void)hipFuncSetAttribute((const void*)k_main, hipFuncAttributeMaxDynamicSharedMemorySize, 163840);
  k_main<<<256, 256, 163840, stream>>>((const u16*)d_in[0], (const u16*)d_in[1], (const u16*)d_in[2], (const u16*)d_in[3], (const u16*)d_in[4],
      (char*)d_ws, (const u16*)d_in[11], (const u16*)d_in[7], (const u16*)d_in[5], (const u16*)d_in[8], (const u16*)d_in[9], (const u16*)d_in[10], (u16*)d_out);
}
```

```cpp
#include <hip/hip_runtime.h>

typedef unsigned short u16;
#define V10(p) p "0", p "1", p "2", p "3", p "4", p "5", p "6", p "7", p "8", p "9"
#define V100(p) V10(p "0"), V10(p "1"), V10(p "2"), V10(p "3"), V10(p "4"), V10(p "5"), V10(p "6"), V10(p "7"), V10(p "8"), V10(p "9")
#define R256(p) V10(p "1"), V10(p "2"), V10(p "3"), V10(p "4"), V10(p "5"), V10(p "6"), V10(p "7"), V10(p "8"), V10(p "9"), V100(p "1"), \
                V10(p "20"), V10(p "21"), V10(p "22"), V10(p "23"), V10(p "24"), p "250", p "251", p "252", p "253", p "254", p "255"

extern "C" __global__ __launch_bounds__(256, 1) __attribute__((aligned(4096)))
void k_main(const u16* x, const u16* ckv, const u16* wq, const u16* wuk, const u16* wuv, char* ws,
            const u16* gate_w, const u16* w_r, const u16* wo, const u16* wg, const u16* wu, const u16* wd, u16* out) {
  typedef const __attribute__((address_space(4))) char* karg_t;
  karg_t ka = (karg_t)__builtin_amdgcn_kernarg_segment_ptr();
  unsigned bid = blockIdx.x, tid = threadIdx.x;
  asm volatile(R"(
.macro Mv d,a,b
v_mfma_f32_16x16x32_bf16 v[\d:\d+3], a[\a:\a+3], a[\b:\b+3], v[\d:\d+3]
.endm
.macro Mp d,a,b
v_mfma_f32_16x16x32_bf16 v[\d:\d+3], a[\a:\a+3], v[\b:\b+3], v[\d:\d+3]
.endm
.macro Ma d,a,b
v_mfma_f32_16x16x32_bf16 a[\d:\d+3], a[\a:\a+3], a[\b:\b+3], a[\d:\d+3]
.endm
.macro Mz d,a,b
v_mfma_f32_16x16x32_bf16 v[\d:\d+3], a[\a:\a+3], a[\b:\b+3], 0
.endm
.macro Rb d,v,o
ds_read_b128 a[\d:\d+3], v\v offset:\o
.endm
.macro Rt d,v,o
ds_read_b64_tr_b16 a[\d:\d+1], v\v offset:\o
.endm
.macro Cv d,a,b
v_cvt_pk_bf16_f32 v\d, v\a, v\b
.endm
.LzSTART:
s_load_dwordx16 s[64:79], s[0:1], 0
s_load_dwordx2 s[80:81], s[0:1], 64
v_lshrrev_b32 v1, 6, v0
v_and_b32 v0, 63, v0
v_readfirstlane_b32 s33, v1
v_lshlrev_b32 v1, 4, v0
s_getpc_b64 s[24:25]
.Lzpc0:
s_sub_u32 s24, s24, .Lzpc0-.LzSTART
s_subb_u32 s25, s25, 0
s_mov_b32 s26, .LzCEND-.LzSTART
s_mov_b32 s27, 0x20000
s_lshr_b32 s28, s2, 3
s_lshl2_add_u32 s28, s28, s33
s_lshl_b32 s28, s28, 11
v_lshrrev_b32 v5, 4, v0
v_min_u32 v5, 1, v5
v_lshlrev_b32 v5, 27, v5
v_add_lshl_u32 v5, v5, v1, 3
buffer_load_dword v5, v5, s[24:27], s28 offen
s_and_b32 s28, s2, 0x17
s_bfe_u32 s29, s2, 0x10005
s_lshl3_add_u32 s28, s29, s28
s_lshr_b32 s29, s2, 6
s_lshl_b32 s29, s29, 5
s_or_b32 s28, s28, s29
s_and_b32 s29, s2, 8
s_lshl4_add_u32 s2, s29, s28
s_and_b32 s28, s2, 7
s_bfe_u32 s29, s2, 0x10003
s_lshl1_add_u32 s34, s28, s29
s_lshl4_add_u32 s89, s28, s29
s_lshl_b32 s89, s89, 21
s_bfe_u32 s35, s2, 0x30004
s_bfe_u32 s91, s2, 0x10004
s_bfe_u32 s93, s2, 0x20005
s_lshl_b32 s36, s33, 13
s_mov_b32 s95, 40
s_bitcmp1_b32 s2, 7
s_cbranch_scc0 .LzA0
.LzB0:
s_mov_b32 s54, 0x3db8aa3b
s_mul_i32 s41, s33, 7168
s_mul_i32 s42, s33, 0x70000
s_lshl_b32 s28, s34, 9
s_add_u32 s42, s42, s28
v_lshrrev_b32 v28, 3, v0
v_and_b32 v29, 0x70, v1
v_lshl_or_b32 v28, v28, 13, v29
v_lshlrev_b32 v29, 1, v0
v_and_b32 v29, 32, v29
v_xor_b32 v28, v28, v29
s_and_b32 s28, s33, 1
s_lshl_b32 s28, s28, 6
v_xor_b32 v240, s28, v28
v_xor_b32 v241, 64, v240
v_add_u32 v241, 0xfc00, v241
v_add_u32 v242, 0x1f800, v240
v_add_u32 v243, 0x1f800, v241
s_sub_u32 s28, 15, s35
s_lshl2_add_u32 s28, s28, s33
s_mul_i32 s43, s28, 28672
)" R"(
v_and_b32 v2, 0x70, v1
v_and_b32 v28, 48, v0
v_xor_b32 v2, v2, v28
v_lshrrev_b32 v28, 3, v0
v_mul_u32_u24 v28, 896, v28
v_add_u32 v2, v2, v28
v_xor_b32 v3, 64, v2
v_add_u32 v3, 6144, v3
v_bfe_u32 v26, v0, 1, 3
v_lshrrev_b32 v28, 4, v0
v_xor_b32 v26, v26, v28
v_lshlrev_b32 v26, 4, v26
v_and_b32 v28, 15, v0
v_lshl_or_b32 v26, v28, 7, v26
s_add_u32 s28, s36, 0x0
v_add_u32 v26, s28, v26
v_xor_b32 v27, 64, v26
s_waitcnt lgkmcnt(0)
s_mov_b32 s16, s68
s_mov_b32 s17, s69
s_mov_b32 s18, 0x380000
s_mov_b32 s19, 0x20000
s_mov_b32 s12, s64
s_mov_b32 s13, s65
s_mov_b32 s14, 0x1c0000
s_mov_b32 s15, 0x20000
s_add_u32 m0, s36, 0
s_add_u32 s44, s43, 0
buffer_load_dwordx4 v2, s[12:15], s44 offen offset:0 lds
buffer_load_dwordx4 v3, s[12:15], s44 offen offset:1024 lds
s_add_u32 s44, s43, 12288
buffer_load_dwordx4 v2, s[12:15], s44 offen offset:2048 lds
buffer_load_dwordx4 v3, s[12:15], s44 offen offset:3072 lds
s_add_u32 m0, s41, 65536
s_add_u32 s92, s42, 0x0
buffer_load_dwordx4 v240, s[16:19], s92 offen offset:0 lds
buffer_load_dwordx4 v241, s[16:19], s92 offen offset:1024 lds
buffer_load_dwordx4 v242, s[16:19], s92 offen offset:2048 lds
buffer_load_dwordx4 v243, s[16:19], s92 offen offset:3072 lds
s_add_u32 m0, s41, 69632
s_add_u32 s92, s42, 0x40000
buffer_load_dwordx4 v240, s[16:19], s92 offen offset:0 lds
buffer_load_dwordx4 v241, s[16:19], s92 offen offset:1024 lds
buffer_load_dwordx4 v242, s[16:19], s92 offen offset:2048 lds
s_add_u32 m0, s36, 4096
s_add_u32 s44, s43, 128
buffer_load_dwordx4 v2, s[12:15], s44 offen offset:0 lds
buffer_load_dwordx4 v3, s[12:15], s44 offen offset:1024 lds
s_add_u32 s44, s43, 12416
buffer_load_dwordx4 v2, s[12:15], s44 offen offset:2048 lds
buffer_load_dwordx4 v3, s[12:15], s44 offen offset:3072 lds
s_add_u32 m0, s36, 32768
s_add_u32 s44, s43, 256
buffer_load_dwordx4 v2, s[12:15], s44 offen offset:0 lds
buffer_load_dwordx4 v3, s[12:15], s44 offen offset:1024 lds
s_add_u32 s44, s43, 12544
buffer_load_dwordx4 v2, s[12:15], s44 offen offset:2048 lds
buffer_load_dwordx4 v3, s[12:15], s44 offen offset:3072 lds
s_add_u32 m0, s36, 36864
s_add_u32 s44, s43, 384
buffer_load_dwordx4 v2, s[12:15], s44 offen offset:0 lds
buffer_load_dwordx4 v3, s[12:15], s44 offen offset:1024 lds
s_add_u32 s44, s43, 12672
buffer_load_dwordx4 v2, s[12:15], s44 offen offset:2048 lds
)" R"(
buffer_load_dwordx4 v3, s[12:15], s44 offen offset:3072 lds
s_mov_b32 s28, 0x1508400
s_load_dword s3, s[74:75], s28
s_mov_b32 s20, s74
s_mov_b32 s21, s75
s_mov_b32 s22, 0xf400000
s_mov_b32 s23, 0x20000
s_mov_b32 s4, s66
s_mov_b32 s5, s67
s_mov_b32 s6, 0x400000
s_mov_b32 s7, 0x20000
s_cmp_eq_u32 s91, 1
s_cselect_b32 s8, s72, s70
s_cselect_b32 s9, s73, s71
s_mov_b32 s10, 0x800000
s_mov_b32 s11, 0x20000
s_load_dwordx2 s[74:75], s[0:1], 96
s_lshl3_add_u32 s28, s33, 64
s_load_dwordx2 s[70:71], s[0:1], s28
v_bfe_u32 v244, v0, 3, 2
v_lshlrev_b32 v244, 5, v244
v_and_b32 v28, 3, v0
v_lshl_or_b32 v244, v28, 3, v244
v_bfe_u32 v28, v0, 2, 2
v_lshl_or_b32 v244, v28, 7, v244
v_lshrrev_b32 v28, 4, v0
v_lshl_or_b32 v244, v28, 10, v244
v_xor_b32 v245, 32, v244
v_xor_b32 v246, 64, v244
v_xor_b32 v247, 96, v244
v_add_u32 v252, 0x20000, v244
v_add_u32 v253, 0x20000, v245
v_add_u32 v254, 0x20000, v246
v_add_u32 v255, 0x20000, v247
v_add_u32 v248, 0x10000, v244
v_add_u32 v249, 0x10000, v245
v_add_u32 v250, 0x10000, v246
v_add_u32 v251, 0x10000, v247
s_waitcnt vmcnt(8) lgkmcnt(0)
s_barrier
ds_read_b128 v[128:131], v26 offset:0
ds_read_b128 v[132:135], v26 offset:2048
ds_read_b128 v[136:139], v27 offset:0
ds_read_b128 v[140:143], v27 offset:2048
ds_read_b128 v[144:147], v26 offset:4096
ds_read_b128 v[148:151], v26 offset:6144
ds_read_b128 v[152:155], v27 offset:4096
ds_read_b128 v[156:159], v27 offset:6144
Rt 176,248,0
Rt 178,248,512
Rt 180,248,4096
Rt 182,248,4608
Rt 184,249,0
Rt 186,249,512
Rt 188,249,4096
Rt 190,249,4608
Rt 192,250,0
Rt 194,250,512
Rt 196,250,4096
Rt 198,250,4608
Rt 200,251,0
Rt 202,251,512
Rt 204,251,4096
Rt 206,251,4608
s_add_u32 m0, s41, 98304
)" R"(
s_add_u32 s92, s42, 0x80
buffer_load_dwordx4 v240, s[16:19], s92 offen offset:0 lds
buffer_load_dwordx4 v241, s[16:19], s92 offen offset:1024 lds
buffer_load_dwordx4 v242, s[16:19], s92 offen offset:2048 lds
buffer_load_dwordx4 v243, s[16:19], s92 offen offset:3072 lds
s_add_u32 m0, s41, 102400
s_add_u32 s92, s42, 0x40080
buffer_load_dwordx4 v240, s[16:19], s92 offen offset:0 lds
buffer_load_dwordx4 v241, s[16:19], s92 offen offset:1024 lds
buffer_load_dwordx4 v242, s[16:19], s92 offen offset:2048 lds
s_mul_i32 s3, s3, 1664525
s_add_u32 s3, s3, 1013904223
v_mov_b32 v6, s3
v_min_u32 v7, 1, v0
v_lshlrev_b32 v7, 30, v7
s_waitcnt lgkmcnt(8)
s_barrier
v_mfma_f32_16x16x32_bf16 v[32:35], a[176:179], v[128:131], 0
Rt 160,248,8192
s_add_u32 m0, s41, 131072
s_add_u32 s92, s42, 0x1c0000
Mp 32,180,136
Rt 162,248,8704
buffer_load_dwordx4 v240, s[16:19], s92 offen offset:0 lds
v_mfma_f32_16x16x32_bf16 v[40:43], a[180:183], v[140:143], 0
Rt 164,248,12288
Mp 40,176,132
Rt 166,248,12800
v_mfma_f32_16x16x32_bf16 v[44:47], a[184:187], v[132:135], 0
Rt 168,249,8192
Mp 44,188,140
Rt 170,249,8704
buffer_load_dwordx4 v241, s[16:19], s92 offen offset:1024 lds
v_mfma_f32_16x16x32_bf16 v[36:39], a[188:191], v[136:139], 0
Rt 172,249,12288
Mp 36,184,128
Rt 174,249,12800
s_waitcnt vmcnt(13) lgkmcnt(8)
v_mfma_f32_16x16x32_bf16 v[48:51], a[192:195], v[128:131], 0
Rt 176,250,8192
ds_read_b128 v[160:163], v26 offset:32768
Mp 48,196,136
Rt 178,250,8704
buffer_load_dwordx4 v242, s[16:19], s92 offen offset:2048 lds
v_mfma_f32_16x16x32_bf16 v[56:59], a[196:199], v[140:143], 0
Rt 180,250,12288
ds_read_b128 v[164:167], v26 offset:34816
Mp 56,192,132
Rt 182,250,12800
v_mfma_f32_16x16x32_bf16 v[60:63], a[200:203], v[132:135], 0
Rt 184,251,8192
ds_read_b128 v[168:171], v27 offset:32768
Mp 60,204,140
Rt 186,251,8704
buffer_load_dwordx4 v243, s[16:19], s92 offen offset:3072 lds
v_mfma_f32_16x16x32_bf16 v[52:55], a[204:207], v[136:139], 0
Rt 188,251,12288
ds_read_b128 v[172:175], v27 offset:34816
Mp 52,200,128
Rt 190,251,12800
s_waitcnt vmcnt(11) lgkmcnt(12)
Mp 32,160,144
Rt 192,248,16384
ds_read_b128 v[176:179], v26 offset:36864
s_add_u32 m0, s41, 135168
)" R"(
s_add_u32 s92, s42, 0x200000
Mp 32,164,152
Rt 194,248,16896
buffer_load_dwordx4 v240, s[16:19], s92 offen offset:0 lds
Mp 40,164,156
Rt 196,248,20480
ds_read_b128 v[180:183], v26 offset:38912
Mp 40,160,148
Rt 198,248,20992
Mp 44,168,148
Rt 200,248,24576
ds_read_b128 v[184:187], v27 offset:36864
Mp 44,172,156
Rt 202,248,25088
buffer_load_dwordx4 v241, s[16:19], s92 offen offset:1024 lds
Mp 36,172,152
Rt 204,249,16384
ds_read_b128 v[188:191], v27 offset:38912
Mp 36,168,144
Rt 206,249,16896
s_waitcnt lgkmcnt(12)
Mp 48,176,144
Rt 160,249,20480
Mp 48,180,152
Rt 162,249,20992
buffer_load_dwordx4 v242, s[16:19], s92 offen offset:2048 lds
Mp 56,180,156
Rt 164,249,24576
Mp 56,176,148
Rt 166,249,25088
Mp 60,184,148
Rt 168,250,16384
Mp 60,188,156
Rt 170,250,16896
Mp 52,188,152
Rt 172,250,20480
s_add_u32 s44, s43, 512
Mp 52,184,144
Rt 174,250,20992
s_waitcnt lgkmcnt(6)
Mp 32,192,160
Rt 176,250,24576
s_add_u32 m0, s36, 32768
Mp 32,196,168
Rt 178,250,25088
buffer_load_dwordx4 v2, s[12:15], s44 offen offset:0 lds
Mp 32,200,176
Rt 180,251,16384
Mp 40,200,180
Rt 182,251,16896
Mp 40,196,172
Rt 184,251,20480
Mp 40,192,164
Rt 186,251,20992
buffer_load_dwordx4 v3, s[12:15], s44 offen offset:1024 lds
Mp 44,204,164
Rt 188,251,24576
Mp 44,160,172
Rt 190,251,25088
s_call_b64 s[30:31], .LzHOP
s_waitcnt vmcnt(9) lgkmcnt(6)
s_barrier
Mp 44,164,180
Rt 192,248,32768
s_mov_b32 s63, 0xe0c0a08
)" R"(
s_mov_b32 s28, 0x100d0b09
Mp 36,164,176
Rt 194,248,33280
s_add_u32 s44, s43, 12800
buffer_load_dwordx4 v2, s[12:15], s44 offen offset:2048 lds
Mp 36,160,168
Rt 196,248,36864
Mp 36,204,160
Rt 198,248,37376
Mp 48,168,160
Rt 200,249,32768
Mp 48,172,168
Rt 202,249,33280
buffer_load_dwordx4 v3, s[12:15], s44 offen offset:3072 lds
Mp 48,176,176
Rt 204,249,36864
s_add_u32 s44, s43, 640
Mp 56,176,180
Rt 206,249,37376
s_waitcnt lgkmcnt(8)
s_barrier
Mp 56,172,172
Rt 160,250,32768
s_add_u32 m0, s36, 36864
Mp 56,168,164
Rt 162,250,33280
buffer_load_dwordx4 v2, s[12:15], s44 offen offset:0 lds
Mp 60,180,164
Rt 164,250,36864
Mp 60,184,172
Rt 166,250,37376
Mp 60,188,180
Rt 168,251,32768
Mp 52,188,176
Rt 170,251,33280
buffer_load_dwordx4 v3, s[12:15], s44 offen offset:1024 lds
Mp 52,184,168
Rt 172,251,36864
Mp 52,180,160
Rt 174,251,37376
s_waitcnt lgkmcnt(8)
v_mfma_f32_16x16x32_bf16 v[64:67], a[192:195], v[128:131], 0
Rt 176,248,40960
Mp 64,196,136
Rt 178,248,41472
s_add_u32 s44, s43, 12928
buffer_load_dwordx4 v2, s[12:15], s44 offen offset:2048 lds
v_mfma_f32_16x16x32_bf16 v[72:75], a[196:199], v[140:143], 0
Rt 180,248,45056
Mp 72,192,132
Rt 182,248,45568
v_mfma_f32_16x16x32_bf16 v[76:79], a[200:203], v[132:135], 0
Rt 184,249,40960
s_bitcmp1_b32 s2, 5
s_cmov_b32 s63, 0xf1e1c1a
s_cmov_b32 s28, 0x111f1d1b
Mp 76,204,140
Rt 186,249,41472
buffer_load_dwordx4 v3, s[12:15], s44 offen offset:3072 lds
v_mfma_f32_16x16x32_bf16 v[68:71], a[204:207], v[136:139], 0
Rt 188,249,45056
s_add_u32 s92, s42, 0x1c0080
Mp 68,200,128
Rt 190,249,45568
s_waitcnt lgkmcnt(8)
)" R"(
v_mfma_f32_16x16x32_bf16 v[80:83], a[160:163], v[128:131], 0
Rt 192,250,40960
s_add_u32 m0, s41, 0
Mp 80,164,136
Rt 194,250,41472
buffer_load_dwordx4 v240, s[16:19], s92 offen offset:0 lds
v_mfma_f32_16x16x32_bf16 v[88:91], a[164:167], v[140:143], 0
Rt 196,250,45056
s_bitcmp1_b32 s2, 6
s_cmov_b32 s63, s28
Mp 88,160,132
Rt 198,250,45568
s_lshl_b32 s28, s33, 3
s_lshr_b32 s28, s63, s28
v_mfma_f32_16x16x32_bf16 v[92:95], a[168:171], v[132:135], 0
Rt 200,251,40960
s_and_b32 s28, s28, 255
s_lshl_b32 s38, s28, 17
s_add_u32 s38, s38, 0xff80
Mp 92,172,140
Rt 202,251,41472
buffer_load_dwordx4 v241, s[16:19], s92 offen offset:1024 lds
v_mfma_f32_16x16x32_bf16 v[84:87], a[172:175], v[136:139], 0
Rt 204,251,45056
s_lshl_b32 s39, s33, 16
s_lshl_b32 s28, s34, 9
Mp 84,168,128
Rt 206,251,45568
s_add_u32 s39, s39, s28
s_waitcnt lgkmcnt(8)
Mp 64,176,144
Rt 160,248,49152
s_sub_u32 s39, s39, 0x40000
s_lshl_b32 s37, s33, 12
Mp 64,180,152
Rt 162,248,49664
buffer_load_dwordx4 v242, s[16:19], s92 offen offset:2048 lds
s_add_u32 s37, s37, 0x10000
Mp 72,180,156
Rt 164,248,53248
v_lshrrev_b32 v28, 4, v0
v_and_b32 v29, 7, v0
Mp 72,176,148
Rt 166,248,53760
v_xor_b32 v28, v28, v29
v_lshlrev_b32 v28, 4, v28
Mp 76,184,148
Rt 168,248,57344
v_lshrrev_b32 v29, 3, v0
v_lshl_or_b32 v8, v29, 11, v28
Mp 76,188,156
Rt 170,248,57856
buffer_load_dwordx4 v243, s[16:19], s92 offen offset:3072 lds
Mp 68,188,152
Rt 172,249,49152
s_add_u32 s92, s42, 0x200080
v_xor_b32 v9, 64, v8
Mp 68,184,144
Rt 174,249,49664
v_add_u32 v9, 15360, v9
s_waitcnt lgkmcnt(8)
Mp 80,192,144
Rt 176,249,53248
s_add_u32 m0, s41, 4096
)" R"(
v_add_u32 v10, 30720, v8
Mp 80,196,152
Rt 178,249,53760
buffer_load_dwordx4 v240, s[16:19], s92 offen offset:0 lds
v_add_u32 v11, 30720, v9
Mp 88,196,156
Rt 180,249,57344
s_and_b32 s28, s33, 1
s_lshl_b32 s28, s28, 7
Mp 88,192,148
Rt 182,249,57856
v_lshrrev_b32 v29, 5, v0
v_mul_u32_u24 v29, 0x2220, v29
Mp 92,200,148
Rt 184,250,49152
v_xor_b32 v29, v29, v1
v_xor_b32 v12, s28, v29
Mp 92,204,156
Rt 186,250,49664
buffer_load_dwordx4 v241, s[16:19], s92 offen offset:1024 lds
Mp 84,204,152
Rt 188,250,53248
v_xor_b32 v13, 64, v12
v_add_u32 v13, 15360, v13
Mp 84,200,144
Rt 190,250,53760
v_add_u32 v14, 30720, v12
s_waitcnt vmcnt(10) lgkmcnt(6)
Mp 64,160,160
Rt 192,250,57344
ds_read_b128 v[192:195], v26 offset:32768
v_add_u32 v15, 30720, v13
Mp 64,164,168
Rt 194,250,57856
buffer_load_dwordx4 v242, s[16:19], s92 offen offset:2048 lds
v_bfe_u32 v28, v0, 1, 3
Mp 64,168,176
Rt 196,251,49152
ds_read_b128 v[196:199], v26 offset:34816
v_lshrrev_b32 v29, 4, v0
Mp 72,168,180
Rt 198,251,49664
v_xor_b32 v28, v28, v29
v_lshlrev_b32 v28, 4, v28
Mp 72,164,172
Rt 200,251,53248
ds_read_b128 v[200:203], v27 offset:32768
v_and_b32 v29, 15, v0
Mp 72,160,164
Rt 202,251,53760
v_lshl_or_b32 v28, v29, 7, v28
s_lshr_b32 s28, s33, 1
Mp 76,172,164
Rt 204,251,57344
ds_read_b128 v[204:207], v27 offset:34816
s_lshl_b32 s28, s28, 12
v_or_b32 v16, s28, v28
Mp 76,176,172
Rt 206,251,57856
s_waitcnt vmcnt(7) lgkmcnt(9)
s_barrier
Mp 76,180,180
Rt 160,252,0
ds_read_b128 v[208:211], v26 offset:36864
v_xor_b32 v17, 64, v16
)" R"(
Mp 68,180,176
Rt 162,252,512
v_bfe_u32 v28, v0, 2, 3
v_lshlrev_b32 v28, 5, v28
Mp 68,176,168
Rt 164,252,4096
ds_read_b128 v[212:215], v26 offset:38912
v_and_b32 v29, 3, v0
Mp 68,172,160
Rt 166,252,4608
v_lshl_or_b32 v28, v29, 3, v28
v_bfe_u32 v29, v0, 2, 2
Mp 80,184,160
Rt 168,253,0
ds_read_b128 v[216:219], v27 offset:36864
v_lshl_or_b32 v28, v29, 9, v28
Mp 80,188,168
Rt 170,253,512
s_add_u32 s44, s43, 768
v_lshrrev_b32 v29, 4, v0
Mp 80,192,176
Rt 172,253,4096
ds_read_b128 v[220:223], v27 offset:38912
v_lshl_or_b32 v28, v29, 12, v28
s_and_b32 s28, s33, 1
Mp 88,192,180
Rt 174,253,4608
s_waitcnt lgkmcnt(12)
s_barrier
Mp 88,188,172
Rt 176,254,0
s_add_u32 m0, s36, 32768
s_lshl_b32 s28, s28, 8
Mp 88,184,164
Rt 178,254,512
buffer_load_dwordx4 v2, s[12:15], s44 offen offset:0 lds
Mp 92,196,164
Rt 180,254,4096
s_bitset1_b32 s28, 16
v_or_b32 v18, s28, v28
Mp 92,200,172
Rt 182,254,4608
v_xor_b32 v19, 32, v18
v_xor_b32 v20, 64, v18
Mp 92,204,180
Rt 184,255,0
buffer_load_dwordx4 v3, s[12:15], s44 offen offset:1024 lds
Mp 84,204,176
Rt 186,255,512
v_xor_b32 v21, 96, v18
v_xor_b32 v22, 128, v18
Mp 84,200,168
Rt 188,255,4096
v_xor_b32 v23, 160, v18
v_xor_b32 v24, 192, v18
Mp 84,196,160
Rt 190,255,4608
s_add_u32 s44, s43, 13056
buffer_load_dwordx4 v2, s[12:15], s44 offen offset:2048 lds
s_waitcnt lgkmcnt(8)
Mp 32,160,184
Rt 192,252,8192
v_xor_b32 v25, 224, v18
Mp 32,164,192
Rt 194,252,8704
)" R"(
Mp 40,164,196
Rt 196,252,12288
buffer_load_dwordx4 v3, s[12:15], s44 offen offset:3072 lds
.LzH0:
s_cbranch_execz .LzH17
Mp 40,160,188
Rt 198,252,12800
Mp 44,168,188
Rt 200,253,8192
Mp 44,172,196
Rt 202,253,8704
s_add_u32 s92, s42, 0x100
Mp 36,172,192
Rt 204,253,12288
s_add_u32 m0, s41, 65536
Mp 36,168,184
Rt 206,253,12800
buffer_load_dwordx4 v240, s[16:19], s92 offen offset:0 lds
s_waitcnt lgkmcnt(8)
Mp 48,176,184
Rt 160,254,8192
Mp 48,180,192
Rt 162,254,8704
Mp 56,180,196
Rt 164,254,12288
Mp 56,176,188
Rt 166,254,12800
buffer_load_dwordx4 v241, s[16:19], s92 offen offset:1024 lds
Mp 60,184,188
Rt 168,255,8192
Mp 60,188,196
Rt 170,255,8704
Mp 52,188,192
Rt 172,255,12288
Mp 52,184,184
Rt 174,255,12800
buffer_load_dwordx4 v242, s[16:19], s92 offen offset:2048 lds
s_waitcnt vmcnt(3) lgkmcnt(8)
Mp 32,192,200
Rt 176,252,16384
ds_read_b128 v[224:227], v26 offset:32768
Mp 32,196,208
Rt 178,252,16896
Mp 40,196,212
Rt 180,252,20480
ds_read_b128 v[228:231], v26 offset:34816
Mp 40,192,204
Rt 182,252,20992
buffer_load_dwordx4 v243, s[16:19], s92 offen offset:3072 lds
Mp 44,200,204
Rt 184,252,24576
ds_read_b128 v[232:235], v27 offset:32768
Mp 44,204,212
Rt 186,252,25088
s_add_u32 m0, s41, 69632
Mp 36,204,208
Rt 188,253,16384
ds_read_b128 v[236:239], v27 offset:34816
s_add_u32 s92, s42, 0x40100
Mp 36,200,200
Rt 190,253,16896
buffer_load_dwordx4 v240, s[16:19], s92 offen offset:0 lds
s_waitcnt lgkmcnt(12)
Mp 48,160,200
)" R"(
Rt 192,253,20480
Mp 48,164,208
Rt 194,253,20992
Mp 56,164,212
Rt 196,253,24576
Mp 56,160,204
Rt 198,253,25088
buffer_load_dwordx4 v241, s[16:19], s92 offen offset:1024 lds
Mp 60,168,204
Rt 200,254,16384
Mp 60,172,212
Rt 202,254,16896
Mp 52,172,208
Rt 204,254,20480
Mp 52,168,200
Rt 206,254,20992
buffer_load_dwordx4 v242, s[16:19], s92 offen offset:2048 lds
s_waitcnt lgkmcnt(6)
Mp 32,176,216
Rt 160,254,24576
Mp 32,180,224
Rt 162,254,25088
s_add_u32 s92, s42, 0x180
Mp 32,184,232
Rt 164,255,16384
s_add_u32 m0, s41, 98304
Mp 40,184,236
Rt 166,255,16896
buffer_load_dwordx4 v240, s[16:19], s92 offen offset:0 lds
Mp 40,180,228
Rt 168,255,20480
Mp 40,176,220
Rt 170,255,20992
Mp 44,188,220
Rt 172,255,24576
Mp 44,192,228
Rt 174,255,25088
buffer_load_dwordx4 v241, s[16:19], s92 offen offset:1024 lds
s_waitcnt lgkmcnt(6)
s_barrier
Mp 44,196,236
Rt 176,244,0
Mp 36,196,232
Rt 178,244,512
Mp 36,192,224
Rt 180,244,4096
Mp 36,188,216
Rt 182,244,4608
buffer_load_dwordx4 v242, s[16:19], s92 offen offset:2048 lds
Mp 48,200,216
Rt 184,245,0
Mp 48,204,224
Rt 186,245,512
Mp 48,160,232
Rt 188,245,4096
Mp 56,160,236
Rt 190,245,4608
buffer_load_dwordx4 v243, s[16:19], s92 offen offset:3072 lds
s_waitcnt lgkmcnt(8)
s_barrier
Mp 56,204,228
Rt 192,246,0
Mp 56,200,220
Rt 194,246,512
s_add_u32 s92, s42, 0x40180
Mp 60,164,220
)" R"(
Rt 196,246,4096
s_add_u32 m0, s41, 102400
Mp 60,168,228
Rt 198,246,4608
buffer_load_dwordx4 v240, s[16:19], s92 offen offset:0 lds
Mp 60,172,236
Rt 200,247,0
Mp 52,172,232
Rt 202,247,512
Mp 52,168,224
Rt 204,247,4096
Mp 52,164,216
Rt 206,247,4608
buffer_load_dwordx4 v241, s[16:19], s92 offen offset:1024 lds
s_waitcnt lgkmcnt(8)
Mp 64,176,184
Rt 160,244,8192
Mp 64,180,192
Rt 162,244,8704
Mp 72,180,196
Rt 164,244,12288
Mp 72,176,188
Rt 166,244,12800
buffer_load_dwordx4 v242, s[16:19], s92 offen offset:2048 lds
Mp 76,184,188
Rt 168,245,8192
v_mul_f32_e32 v32, s54, v32
Mp 76,188,196
Rt 170,245,8704
s_add_u32 s92, s42, 0x1c0100
Mp 68,188,192
Rt 172,245,12288
s_add_u32 m0, s41, 32768
Mp 68,184,184
Rt 174,245,12800
buffer_load_dwordx4 v240, s[16:19], s92 offen offset:0 lds
s_waitcnt lgkmcnt(8)
Mp 80,192,184
Rt 176,246,8192
v_mul_f32_e32 v33, s54, v33
Mp 80,196,192
Rt 178,246,8704
Cv 32,32,33
Mp 88,196,196
Rt 180,246,12288
v_accvgpr_write_b32 a64, v32
v_mul_f32_e32 v34, s54, v34
Mp 88,192,188
Rt 182,246,12800
buffer_load_dwordx4 v241, s[16:19], s92 offen offset:1024 lds
Mp 92,200,188
Rt 184,247,8192
v_mul_f32_e32 v35, s54, v35
Mp 92,204,196
Rt 186,247,8704
Cv 34,34,35
v_accvgpr_write_b32 a65, v34
Mp 84,204,192
Rt 188,247,12288
v_mul_f32_e32 v36, s54, v36
Mp 84,200,184
Rt 190,247,12800
buffer_load_dwordx4 v242, s[16:19], s92 offen offset:2048 lds
s_waitcnt lgkmcnt(8)
)" R"(
Mp 64,160,200
Rt 192,244,16384
v_mul_f32_e32 v37, s54, v37
Cv 36,36,37
Mp 64,164,208
Rt 194,244,16896
v_accvgpr_write_b32 a66, v36
Mp 72,164,212
Rt 196,244,20480
v_mul_f32_e32 v38, s54, v38
Mp 72,160,204
Rt 198,244,20992
buffer_load_dwordx4 v243, s[16:19], s92 offen offset:3072 lds
Mp 76,168,204
Rt 200,244,24576
v_mul_f32_e32 v39, s54, v39
Mp 76,172,212
Rt 202,244,25088
s_add_u32 s92, s42, 0x200100
Mp 68,172,208
Rt 204,245,16384
s_add_u32 m0, s41, 36864
Mp 68,168,200
Rt 206,245,16896
buffer_load_dwordx4 v240, s[16:19], s92 offen offset:0 lds
s_waitcnt lgkmcnt(8)
Mp 80,176,200
Rt 160,245,20480
Cv 38,38,39
Mp 80,180,208
Rt 162,245,20992
v_accvgpr_write_b32 a67, v38
Mp 88,180,212
Rt 164,245,24576
v_mul_f32_e32 v40, s54, v40
v_mul_f32_e32 v41, s54, v41
Mp 88,176,204
Rt 166,245,25088
buffer_load_dwordx4 v241, s[16:19], s92 offen offset:1024 lds
Mp 92,184,204
Rt 168,246,16384
Cv 40,40,41
Mp 92,188,212
Rt 170,246,16896
v_accvgpr_write_b32 a68, v40
v_mul_f32_e32 v42, s54, v42
Mp 84,188,208
Rt 172,246,20480
v_mul_f32_e32 v43, s54, v43
Mp 84,184,200
Rt 174,246,20992
buffer_load_dwordx4 v242, s[16:19], s92 offen offset:2048 lds
s_waitcnt lgkmcnt(6)
Mp 64,192,216
Rt 176,246,24576
Cv 42,42,43
v_accvgpr_write_b32 a69, v42
Mp 64,196,224
Rt 178,246,25088
v_mul_f32_e32 v44, s54, v44
Mp 64,200,232
Rt 180,247,16384
v_mul_f32_e32 v45, s54, v45
Mp 72,200,236
)" R"(
Rt 182,247,16896
s_add_u32 s92, s42, 0x1c0180
Cv 44,44,45
Mp 72,196,228
Rt 184,247,20480
s_add_u32 m0, s41, 131072
Mp 72,192,220
Rt 186,247,20992
buffer_load_dwordx4 v240, s[16:19], s92 offen offset:0 lds
Mp 76,204,220
Rt 188,247,24576
v_accvgpr_write_b32 a70, v44
Mp 76,160,228
Rt 190,247,25088
s_waitcnt vmcnt(15) lgkmcnt(6)
s_barrier
Mp 76,164,236
Rt 192,248,0
v_mul_f32_e32 v46, s54, v46
Mp 68,164,232
Rt 194,248,512
buffer_load_dwordx4 v241, s[16:19], s92 offen offset:1024 lds
Mp 68,160,224
Rt 196,248,4096
v_mul_f32_e32 v47, s54, v47
Cv 46,46,47
Mp 68,204,216
Rt 198,248,4608
v_accvgpr_write_b32 a71, v46
Mp 80,168,216
Rt 200,249,0
v_mul_f32_e32 v48, s54, v48
Mp 80,172,224
Rt 202,249,512
buffer_load_dwordx4 v242, s[16:19], s92 offen offset:2048 lds
Mp 80,176,232
Rt 204,249,4096
v_mul_f32_e32 v49, s54, v49
Mp 88,176,236
Rt 206,249,4608
s_waitcnt vmcnt(3) lgkmcnt(8)
s_barrier
Mp 88,172,228
Rt 160,250,0
Cv 48,48,49
v_accvgpr_write_b32 a72, v48
Mp 88,168,220
Rt 162,250,512
buffer_load_dwordx4 v243, s[16:19], s92 offen offset:3072 lds
Mp 92,180,220
Rt 164,250,4096
v_mul_f32_e32 v50, s54, v50
Mp 92,184,228
Rt 166,250,4608
s_add_u32 s92, s42, 0x200180
v_mul_f32_e32 v51, s54, v51
Mp 92,188,236
Rt 168,251,0
s_add_u32 m0, s41, 135168
Mp 84,188,232
Rt 170,251,512
buffer_load_dwordx4 v240, s[16:19], s92 offen offset:0 lds
Mp 84,184,224
Rt 172,251,4096
Cv 50,50,51
Mp 84,180,216
)" R"(
Rt 174,251,4608
v_accvgpr_write_b32 a73, v50
s_waitcnt lgkmcnt(8)
v_mfma_f32_16x16x32_bf16 v[96:99], a[192:195], v[128:131], 0
Rt 176,248,8192
v_mul_f32_e32 v52, s54, v52
Mp 96,196,136
Rt 178,248,8704
buffer_load_dwordx4 v241, s[16:19], s92 offen offset:1024 lds
v_mfma_f32_16x16x32_bf16 v[104:107], a[196:199], v[140:143], 0
Rt 180,248,12288
v_mul_f32_e32 v53, s54, v53
Cv 52,52,53
Mp 104,192,132
Rt 182,248,12800
v_accvgpr_write_b32 a74, v52
v_mfma_f32_16x16x32_bf16 v[108:111], a[200:203], v[132:135], 0
Rt 184,249,8192
v_mul_f32_e32 v54, s54, v54
Mp 108,204,140
Rt 186,249,8704
buffer_load_dwordx4 v242, s[16:19], s92 offen offset:2048 lds
v_mfma_f32_16x16x32_bf16 v[100:103], a[204:207], v[136:139], 0
Rt 188,249,12288
v_mul_f32_e32 v55, s54, v55
Mp 100,200,128
Rt 190,249,12800
s_waitcnt lgkmcnt(8)
v_mfma_f32_16x16x32_bf16 v[112:115], a[160:163], v[128:131], 0
Rt 192,250,8192
Cv 54,54,55
v_accvgpr_write_b32 a75, v54
Mp 112,164,136
Rt 194,250,8704
s_add_u32 s38, s38, 0xffff0080
v_mfma_f32_16x16x32_bf16 v[120:123], a[164:167], v[140:143], 0
Rt 196,250,12288
s_add_u32 m0, s36, 0
Mp 120,160,132
Rt 198,250,12800
buffer_load_dwordx4 v8, s[4:7], s38 offen offset:0 lds
v_mfma_f32_16x16x32_bf16 v[124:127], a[168:171], v[132:135], 0
Rt 200,251,8192
v_mul_f32_e32 v56, s54, v56
Mp 124,172,140
Rt 202,251,8704
v_mul_f32_e32 v57, s54, v57
v_mfma_f32_16x16x32_bf16 v[116:119], a[172:175], v[136:139], 0
Rt 204,251,12288
Cv 56,56,57
Mp 116,168,128
Rt 206,251,12800
buffer_load_dwordx4 v9, s[4:7], s38 offen offset:1024 lds
s_waitcnt lgkmcnt(8)
Mp 96,176,144
Rt 160,248,16384
v_accvgpr_write_b32 a76, v56
Mp 96,180,152
Rt 162,248,16896
v_mul_f32_e32 v58, s54, v58
Mp 104,180,156
Rt 164,248,20480
v_mul_f32_e32 v59, s54, v59
Cv 58,58,59
)" R"(
Mp 104,176,148
Rt 166,248,20992
buffer_load_dwordx4 v10, s[4:7], s38 offen offset:2048 lds
Mp 108,184,148
Rt 168,248,24576
v_accvgpr_write_b32 a77, v58
Mp 108,188,156
Rt 170,248,25088
v_mul_f32_e32 v60, s54, v60
v_mul_f32_e32 v61, s54, v61
Mp 100,188,152
Rt 172,249,16384
Cv 60,60,61
s_lshl1_add_u32 s88, s34, s91
s_lshl_b32 s88, s88, 12
Mp 100,184,144
Rt 174,249,16896
buffer_load_dwordx4 v11, s[4:7], s38 offen offset:3072 lds
s_waitcnt lgkmcnt(8)
Mp 112,192,144
Rt 176,249,20480
v_accvgpr_write_b32 a78, v60
v_mul_f32_e32 v62, s54, v62
s_and_b32 s29, s38, 0xfffe0000
Mp 112,196,152
Rt 178,249,20992
s_add_u32 s38, s38, 0x10000
s_add_u32 s88, s88, s29
Mp 120,196,156
Rt 180,249,24576
s_add_u32 m0, s36, 4096
v_min_u32 v28, 31, v0
Mp 120,192,148
Rt 182,249,25088
buffer_load_dwordx4 v8, s[4:7], s38 offen offset:0 lds
v_lshlrev_b32 v28, 7, v28
Mp 124,200,148
Rt 184,250,16384
v_mul_f32_e32 v63, s54, v63
Mp 124,204,156
Rt 186,250,16896
Cv 62,62,63
Mp 116,204,152
Rt 188,250,20480
v_accvgpr_write_b32 a79, v62
Mp 116,200,144
Rt 190,250,20992
buffer_load_dwordx4 v9, s[4:7], s38 offen offset:1024 lds
s_waitcnt lgkmcnt(6)
Mp 96,160,160
Rt 192,250,24576
v_mul_f32_e32 v64, s54, v64
Mp 96,164,168
Rt 194,250,25088
v_mul_f32_e32 v65, s54, v65
Mp 96,168,176
Rt 196,251,16384
Cv 64,64,65
v_accvgpr_write_b32 a80, v64
Mp 104,168,180
Rt 198,251,16896
buffer_load_dwordx4 v10, s[4:7], s38 offen offset:2048 lds
Mp 104,164,172
Rt 200,251,20480
)" R"(
v_mul_f32_e32 v66, s54, v66
Mp 104,160,164
Rt 202,251,20992
v_mul_f32_e32 v67, s54, v67
Cv 66,66,67
Mp 108,172,164
Rt 204,251,24576
v_accvgpr_write_b32 a81, v66
Mp 108,176,172
Rt 206,251,25088
buffer_load_dwordx4 v11, s[4:7], s38 offen offset:3072 lds
s_waitcnt lgkmcnt(6)
Mp 108,180,180
Rt 160,244,32768
v_mul_f32_e32 v68, s54, v68
v_mul_f32_e32 v69, s54, v69
Mp 100,180,176
Rt 162,244,33280
Cv 68,68,69
Mp 100,176,168
Rt 164,244,36864
v_accvgpr_write_b32 a82, v68
buffer_load_dword a254, v28, s[4:7], s88 offen
Mp 100,172,160
Rt 166,244,37376
v_mul_f32_e32 v70, s54, v70
v_mul_f32_e32 v71, s54, v71
Mp 112,184,160
Rt 168,245,32768
Cv 70,70,71
Mp 112,188,168
Rt 170,245,33280
v_accvgpr_write_b32 a83, v70
Mp 112,192,176
Rt 172,245,36864
v_mul_f32_e32 v72, s54, v72
Mp 120,192,180
Rt 174,245,37376
s_waitcnt lgkmcnt(8)
s_barrier
Mp 120,188,172
Rt 176,246,32768
v_mul_f32_e32 v73, s54, v73
Mp 120,184,164
Rt 178,246,33280
s_add_u32 s39, s39, 0x40000
Mp 124,196,164
Rt 180,246,36864
s_add_u32 m0, s37, 0
Cv 72,72,73
Mp 124,200,172
Rt 182,246,37376
buffer_load_dwordx4 v12, s[8:11], s39 offen offset:0 lds
Mp 124,204,180
Rt 184,247,32768
v_accvgpr_write_b32 a84, v72
Mp 116,204,176
Rt 186,247,33280
v_mul_f32_e32 v74, s54, v74
v_mul_f32_e32 v75, s54, v75
Mp 116,200,168
Rt 188,247,36864
Cv 74,74,75
Mp 116,196,160
Rt 190,247,37376
)" R"(
s_waitcnt lgkmcnt(8)
Mp 96,160,184
Rt 192,244,40960
v_accvgpr_write_b32 a85, v74
v_mul_f32_e32 v76, s54, v76
Mp 96,164,192
Rt 194,244,41472
buffer_load_dwordx4 v13, s[8:11], s39 offen offset:1024 lds
Mp 104,164,196
Rt 196,244,45056
v_mul_f32_e32 v77, s54, v77
Mp 104,160,188
Rt 198,244,45568
Cv 76,76,77
v_accvgpr_write_b32 a86, v76
Mp 108,168,188
Rt 200,245,40960
v_mul_f32_e32 v78, s54, v78
Mp 108,172,196
Rt 202,245,41472
v_mul_f32_e32 v79, s54, v79
Mp 100,172,192
Rt 204,245,45056
Cv 78,78,79
Mp 100,168,184
Rt 206,245,45568
buffer_load_dwordx4 v14, s[8:11], s39 offen offset:2048 lds
s_waitcnt lgkmcnt(8)
Mp 112,176,184
Rt 160,246,40960
v_accvgpr_write_b32 a87, v78
Mp 112,180,192
Rt 162,246,41472
v_mul_f32_e32 v80, s54, v80
Mp 120,180,196
Rt 164,246,45056
v_mul_f32_e32 v81, s54, v81
Cv 80,80,81
Mp 120,176,188
Rt 166,246,45568
v_accvgpr_write_b32 a88, v80
Mp 124,184,188
Rt 168,247,40960
v_mul_f32_e32 v82, s54, v82
.LzH1:
s_cbranch_execz .LzH18
Mp 124,188,196
Rt 170,247,41472
buffer_load_dwordx4 v15, s[8:11], s39 offen offset:3072 lds
Mp 116,188,192
Rt 172,247,45056
v_mul_f32_e32 v83, s54, v83
Mp 116,184,184
Rt 174,247,45568
s_waitcnt lgkmcnt(8)
Mp 96,192,200
Rt 176,244,49152
Cv 82,82,83
v_accvgpr_write_b32 a89, v82
Mp 96,196,208
Rt 178,244,49664
s_add_u32 s39, s39, 0x40000
Mp 104,196,212
Rt 180,244,53248
)" R"(
s_add_u32 m0, s37, 16384
Mp 104,192,204
Rt 182,244,53760
buffer_load_dwordx4 v12, s[8:11], s39 offen offset:0 lds
Mp 108,200,204
Rt 184,244,57344
v_mul_f32_e32 v84, s54, v84
Mp 108,204,212
Rt 186,244,57856
v_mul_f32_e32 v85, s54, v85
Mp 100,204,208
Rt 188,245,49152
Cv 84,84,85
Mp 100,200,200
Rt 190,245,49664
v_accvgpr_write_b32 a90, v84
s_waitcnt lgkmcnt(8)
Mp 112,160,200
Rt 192,245,53248
v_mul_f32_e32 v86, s54, v86
Mp 112,164,208
Rt 194,245,53760
buffer_load_dwordx4 v13, s[8:11], s39 offen offset:1024 lds
Mp 120,164,212
Rt 196,245,57344
v_mul_f32_e32 v87, s54, v87
Cv 86,86,87
Mp 120,160,204
Rt 198,245,57856
v_accvgpr_write_b32 a91, v86
Mp 124,168,204
Rt 200,246,49152
v_mul_f32_e32 v88, s54, v88
Mp 124,172,212
Rt 202,246,49664
v_mul_f32_e32 v89, s54, v89
Cv 88,88,89
Mp 116,172,208
Rt 204,246,53248
v_accvgpr_write_b32 a92, v88
Mp 116,168,200
Rt 206,246,53760
buffer_load_dwordx4 v14, s[8:11], s39 offen offset:2048 lds
s_waitcnt lgkmcnt(6)
Mp 96,176,216
Rt 160,246,57344
v_mul_f32_e32 v90, s54, v90
v_mul_f32_e32 v91, s54, v91
Mp 96,180,224
Rt 162,246,57856
Cv 90,90,91
Mp 96,184,232
Rt 164,247,49152
v_accvgpr_write_b32 a93, v90
Mp 104,184,236
Rt 166,247,49664
v_mul_f32_e32 v92, s54, v92
v_mul_f32_e32 v93, s54, v93
Mp 104,180,228
Rt 168,247,53248
Cv 92,92,93
Mp 104,176,220
Rt 170,247,53760
buffer_load_dwordx4 v15, s[8:11], s39 offen offset:3072 lds
)" R"(
Mp 108,188,220
Rt 172,247,57344
v_accvgpr_write_b32 a94, v92
Mp 108,192,228
Rt 174,247,57856
v_mul_f32_e32 v94, s54, v94
s_waitcnt lgkmcnt(6)
Mp 108,196,236
Rt 176,248,32768
v_mul_f32_e32 v95, s54, v95
Mp 100,196,232
Rt 178,248,33280
Cv 94,94,95
Mp 100,192,224
Rt 180,248,36864
v_accvgpr_write_b32 a95, v94
Mp 100,188,216
Rt 182,248,37376
Mp 112,200,216
Rt 184,249,32768
Mp 112,204,224
Rt 186,249,33280
Mp 112,160,232
Rt 188,249,36864
Mp 120,160,236
Rt 190,249,37376
s_waitcnt vmcnt(17) lgkmcnt(8)
s_barrier
Mp 120,204,228
Rt 192,250,32768
Mp 120,200,220
Rt 194,250,33280
s_add_u32 s38, s38, 0xffff0080
Mp 124,164,220
Rt 196,250,36864
s_add_u32 m0, s36, 32768
Mp 124,168,228
Rt 198,250,37376
buffer_load_dwordx4 v8, s[4:7], s38 offen offset:0 lds
Mp 124,172,236
Rt 200,251,32768
Mp 116,172,232
Rt 202,251,33280
Mp 116,168,224
Rt 204,251,36864
Mp 116,164,216
Rt 206,251,37376
s_waitcnt lgkmcnt(8)
v_mfma_f32_16x16x32_bf16 v[64:67], a[176:179], v[128:131], 0
Rt 160,248,40960
Mp 64,180,136
Rt 162,248,41472
buffer_load_dwordx4 v9, s[4:7], s38 offen offset:1024 lds
v_mfma_f32_16x16x32_bf16 v[72:75], a[180:183], v[140:143], 0
Rt 164,248,45056
Mp 72,176,132
Rt 166,248,45568
v_mfma_f32_16x16x32_bf16 v[76:79], a[184:187], v[132:135], 0
Rt 168,249,40960
v_mul_f32_e32 v96, s54, v96
Mp 76,188,140
Rt 170,249,41472
v_mul_f32_e32 v97, s54, v97
v_mfma_f32_16x16x32_bf16 v[68:71], a[188:191], v[136:139], 0
Rt 172,249,45056
)" R"(
Cv 96,96,97
Mp 68,184,128
Rt 174,249,45568
buffer_load_dwordx4 v10, s[4:7], s38 offen offset:2048 lds
s_waitcnt lgkmcnt(8)
v_mfma_f32_16x16x32_bf16 v[80:83], a[192:195], v[128:131], 0
Rt 176,250,40960
v_accvgpr_write_b32 a96, v96
Mp 80,196,136
Rt 178,250,41472
v_mul_f32_e32 v98, s54, v98
v_mfma_f32_16x16x32_bf16 v[88:91], a[196:199], v[140:143], 0
Rt 180,250,45056
v_mul_f32_e32 v99, s54, v99
Mp 88,192,132
Rt 182,250,45568
Cv 98,98,99
v_mfma_f32_16x16x32_bf16 v[92:95], a[200:203], v[132:135], 0
Rt 184,251,40960
v_accvgpr_write_b32 a97, v98
Mp 92,204,140
Rt 186,251,41472
buffer_load_dwordx4 v11, s[4:7], s38 offen offset:3072 lds
v_mfma_f32_16x16x32_bf16 v[84:87], a[204:207], v[136:139], 0
Rt 188,251,45056
v_mul_f32_e32 v100, s54, v100
Mp 84,200,128
Rt 190,251,45568
s_waitcnt lgkmcnt(8)
Mp 64,160,144
Rt 192,248,49152
v_mul_f32_e32 v101, s54, v101
Mp 64,164,152
Rt 194,248,49664
s_add_u32 s38, s38, 0x10000
Mp 72,164,156
Rt 196,248,53248
s_add_u32 m0, s36, 36864
Mp 72,160,148
Rt 198,248,53760
buffer_load_dwordx4 v8, s[4:7], s38 offen offset:0 lds
Mp 76,168,148
Rt 200,248,57344
Cv 100,100,101
Mp 76,172,156
Rt 202,248,57856
v_accvgpr_write_b32 a98, v100
Mp 68,172,152
Rt 204,249,49152
v_mul_f32_e32 v102, s54, v102
Mp 68,168,144
Rt 206,249,49664
s_waitcnt lgkmcnt(8)
Mp 80,176,144
Rt 160,249,53248
v_mul_f32_e32 v103, s54, v103
Mp 80,180,152
Rt 162,249,53760
buffer_load_dwordx4 v9, s[4:7], s38 offen offset:1024 lds
Mp 88,180,156
Rt 164,249,57344
Cv 102,102,103
Mp 88,176,148
Rt 166,249,57856
)" R"(
v_accvgpr_write_b32 a99, v102
Mp 92,184,148
Rt 168,250,49152
v_mul_f32_e32 v104, s54, v104
Mp 92,188,156
Rt 170,250,49664
v_mul_f32_e32 v105, s54, v105
Mp 84,188,152
Rt 172,250,53248
Cv 104,104,105
Mp 84,184,144
Rt 174,250,53760
buffer_load_dwordx4 v10, s[4:7], s38 offen offset:2048 lds
s_waitcnt lgkmcnt(6)
Mp 64,192,160
Rt 176,250,57344
v_accvgpr_write_b32 a100, v104
Mp 64,196,168
Rt 178,250,57856
v_mul_f32_e32 v106, s54, v106
Mp 64,200,176
Rt 180,251,49152
v_mul_f32_e32 v107, s54, v107
Mp 72,200,180
Rt 182,251,49664
Cv 106,106,107
Mp 72,196,172
Rt 184,251,53248
v_accvgpr_write_b32 a101, v106
Mp 72,192,164
Rt 186,251,53760
buffer_load_dwordx4 v11, s[4:7], s38 offen offset:3072 lds
Mp 76,204,164
Rt 188,251,57344
v_mul_f32_e32 v108, s54, v108
Mp 76,160,172
Rt 190,251,57856
s_waitcnt lgkmcnt(6)
Mp 76,164,180
Rt 192,252,0
v_mul_f32_e32 v109, s54, v109
Mp 68,164,176
Rt 194,252,512
Cv 108,108,109
Mp 68,160,168
Rt 196,252,4096
v_accvgpr_write_b32 a102, v108
Mp 68,204,160
Rt 198,252,4608
v_mul_f32_e32 v110, s54, v110
Mp 80,168,160
Rt 200,253,0
v_mul_f32_e32 v111, s54, v111
Mp 80,172,168
Rt 202,253,512
Cv 110,110,111
Mp 80,176,176
Rt 204,253,4096
v_accvgpr_write_b32 a103, v110
Mp 88,176,180
Rt 206,253,4608
s_waitcnt vmcnt(8) lgkmcnt(8)
s_barrier
Mp 88,172,172
Rt 160,254,0
)" R"(
v_mul_f32_e32 v112, s54, v112
Mp 88,168,164
Rt 162,254,512
s_add_u32 s39, s39, 0x40000
Mp 92,180,164
Rt 164,254,4096
s_add_u32 m0, s37, 32768
Mp 92,184,172
Rt 166,254,4608
buffer_load_dwordx4 v12, s[8:11], s39 offen offset:0 lds
Mp 92,188,180
Rt 168,255,0
v_mul_f32_e32 v113, s54, v113
Mp 84,188,176
Rt 170,255,512
Cv 112,112,113
Mp 84,184,168
Rt 172,255,4096
v_accvgpr_write_b32 a104, v112
Mp 84,180,160
Rt 174,255,4608
s_waitcnt lgkmcnt(8)
Mp 64,192,184
Rt 176,252,8192
v_mul_f32_e32 v114, s54, v114
Mp 64,196,192
Rt 178,252,8704
buffer_load_dwordx4 v13, s[8:11], s39 offen offset:1024 lds
Mp 72,196,196
Rt 180,252,12288
v_mul_f32_e32 v115, s54, v115
Mp 72,192,188
Rt 182,252,12800
Cv 114,114,115
Mp 76,200,188
Rt 184,253,8192
v_accvgpr_write_b32 a105, v114
Mp 76,204,196
Rt 186,253,8704
v_mul_f32_e32 v116, s54, v116
Mp 68,204,192
Rt 188,253,12288
v_mul_f32_e32 v117, s54, v117
Mp 68,200,184
Rt 190,253,12800
buffer_load_dwordx4 v14, s[8:11], s39 offen offset:2048 lds
s_waitcnt lgkmcnt(8)
Mp 80,160,184
Rt 192,254,8192
Cv 116,116,117
Mp 80,164,192
Rt 194,254,8704
v_accvgpr_write_b32 a106, v116
Mp 88,164,196
Rt 196,254,12288
v_mul_f32_e32 v118, s54, v118
Mp 88,160,188
Rt 198,254,12800
v_mul_f32_e32 v119, s54, v119
Mp 92,168,188
Rt 200,255,8192
Rt 0,18,0
Mp 92,172,196
Rt 202,255,8704
)" R"(
buffer_load_dwordx4 v15, s[8:11], s39 offen offset:3072 lds
Rt 2,18,2048
Mp 84,172,192
Rt 204,255,12288
Rt 4,18,16384
Mp 84,168,184
Rt 206,255,12800
Rt 6,18,18432
s_waitcnt lgkmcnt(12)
Mp 64,176,200
Rt 160,252,16384
Rb 128,16,0
Mp 64,180,208
Rt 162,252,16896
s_add_u32 s39, s39, 0x40000
Rb 132,17,0
Mp 72,180,212
Rt 164,252,20480
s_add_u32 m0, s37, 49152
Rb 136,16,2048
Mp 72,176,204
Rt 166,252,20992
buffer_load_dwordx4 v12, s[8:11], s39 offen offset:0 lds
Rb 140,17,2048
Mp 76,184,204
Rt 168,252,24576
Rb 144,16,8192
Mp 76,188,212
Rt 170,252,25088
Rb 148,17,8192
Mp 68,188,208
Rt 172,253,16384
Rb 152,16,10240
Mp 68,184,200
Rt 174,253,16896
Rb 156,17,10240
Mp 80,192,200
Rt 176,253,20480
Rt 8,19,0
Mp 80,196,208
Rt 178,253,20992
buffer_load_dwordx4 v13, s[8:11], s39 offen offset:1024 lds
Rt 10,19,2048
Mp 88,196,212
Rt 180,253,24576
Rt 12,19,16384
Mp 88,192,204
Rt 182,253,25088
Rt 14,19,18432
Mp 92,200,204
Rt 184,254,16384
Rt 16,20,0
Mp 92,204,212
Rt 186,254,16896
Rt 18,20,2048
Mp 84,204,208
Rt 188,254,20480
Rt 20,20,16384
Mp 84,200,200
Rt 190,254,20992
buffer_load_dwordx4 v14, s[8:11], s39 offen offset:2048 lds
Rt 22,20,18432
s_waitcnt lgkmcnt(13)
Mp 64,160,216
)" R"(
Rt 192,254,24576
Rt 24,21,0
Mp 64,164,224
Rt 194,254,25088
Rt 26,21,2048
Mp 64,168,232
Rt 196,255,16384
Rt 28,21,16384
Mp 72,168,236
Rt 198,255,16896
Rt 30,21,18432
Mp 72,164,228
Rt 200,255,20480
Rt 32,22,0
Mp 72,160,220
Rt 202,255,20992
buffer_load_dwordx4 v15, s[8:11], s39 offen offset:3072 lds
Rt 34,22,2048
Mp 76,172,220
Rt 204,255,24576
Rt 36,22,16384
Mp 76,176,228
Rt 206,255,25088
Rt 38,22,18432
s_waitcnt lgkmcnt(13)
Mp 76,180,236
Rt 40,23,0
Cv 118,118,119
Mp 68,180,232
Rt 42,23,2048
v_accvgpr_write_b32 a107, v118
Mp 68,176,224
Rt 44,23,16384
v_mul_f32_e32 v120, s54, v120
Mp 68,172,216
Rt 46,23,18432
v_mul_f32_e32 v121, s54, v121
Mp 80,184,216
Rt 48,24,0
Cv 120,120,121
Mp 80,188,224
Rt 50,24,2048
v_accvgpr_write_b32 a108, v120
Mp 80,192,232
Rt 52,24,16384
v_mul_f32_e32 v122, s54, v122
Mp 88,192,236
Rt 54,24,18432
s_waitcnt lgkmcnt(9)
Mp 88,188,228
v_mul_f32_e32 v123, s54, v123
Cv 122,122,123
Mp 88,184,220
v_accvgpr_write_b32 a109, v122
v_mul_f32_e32 v124, s54, v124
Mp 92,196,220
v_mul_f32_e32 v125, s54, v125
Cv 124,124,125
Mp 92,200,228
v_accvgpr_write_b32 a110, v124
v_mul_f32_e32 v126, s54, v126
Mp 92,204,236
v_mul_f32_e32 v127, s54, v127
Cv 126,126,127
)" R"(
Mp 84,204,232
v_accvgpr_write_b32 a111, v126
Mp 84,200,224
Mp 84,196,216
s_cmp_eq_u32 s91, 1
s_barrier
s_cbranch_scc1 .LzBV
Mz 32,0,128
Rt 56,25,0
Mv 32,4,132
Rt 58,25,2048
Mz 96,4,148
Rt 60,25,16384
Mv 96,0,144
Rt 62,25,18432
v_mul_f32_e32 v64, s54, v64
Mz 128,0,152
v_mul_f32_e32 v65, s54, v65
Cv 64,64,65
Mv 128,4,156
v_accvgpr_write_b32 a112, v64
v_mul_f32_e32 v66, s54, v66
Mz 132,12,156
v_mul_f32_e32 v67, s54, v67
Cv 66,66,67
Mv 132,8,152
v_accvgpr_write_b32 a113, v66
Mz 100,8,144
v_mul_f32_e32 v68, s54, v68
v_mul_f32_e32 v69, s54, v69
Mv 100,12,148
Cv 68,68,69
v_accvgpr_write_b32 a114, v68
Mz 36,12,132
v_mul_f32_e32 v70, s54, v70
v_mul_f32_e32 v71, s54, v71
Mv 36,8,128
Cv 70,70,71
v_accvgpr_write_b32 a115, v70
Mz 40,16,128
v_mul_f32_e32 v72, s54, v72
v_mul_f32_e32 v73, s54, v73
Mv 40,20,132
Cv 72,72,73
v_accvgpr_write_b32 a116, v72
Mz 104,20,148
s_add_u32 s39, s39, 0x40000
v_mul_f32_e32 v74, s54, v74
Mv 104,16,144
s_waitcnt lgkmcnt(0)
s_barrier
Mz 136,16,152
Rb 160,16,16384
s_add_u32 m0, s37, 0
Mv 136,20,156
buffer_load_dwordx4 v12, s[8:11], s39 offen offset:0 lds
Mz 140,28,156
v_mul_f32_e32 v75, s54, v75
Cv 74,74,75
v_accvgpr_write_b32 a117, v74
Mv 140,24,152
v_mul_f32_e32 v76, s54, v76
v_mul_f32_e32 v77, s54, v77
Mz 108,24,144
Rb 164,17,16384
)" R"(
Cv 76,76,77
Mv 108,28,148
v_accvgpr_write_b32 a118, v76
v_mul_f32_e32 v78, s54, v78
Mz 44,28,132
v_mul_f32_e32 v79, s54, v79
Cv 78,78,79
Mv 44,24,128
buffer_load_dwordx4 v13, s[8:11], s39 offen offset:1024 lds
Mz 48,32,128
Rb 168,16,18432
v_accvgpr_write_b32 a119, v78
Mv 48,36,132
v_mul_f32_e32 v80, s54, v80
v_mul_f32_e32 v81, s54, v81
Mz 112,36,148
Cv 80,80,81
v_accvgpr_write_b32 a120, v80
Mv 112,32,144
v_mul_f32_e32 v82, s54, v82
v_mul_f32_e32 v83, s54, v83
Mz 144,32,152
Rb 172,17,18432
Cv 82,82,83
.LzH2:
s_cbranch_execz .LzH12
Mv 144,36,156
buffer_load_dwordx4 v14, s[8:11], s39 offen offset:2048 lds
Mz 148,44,156
v_accvgpr_write_b32 a121, v82
v_mul_f32_e32 v84, s54, v84
Mv 148,40,152
v_mul_f32_e32 v85, s54, v85
Cv 84,84,85
Mz 116,40,144
Rb 176,16,24576
v_accvgpr_write_b32 a122, v84
Mv 116,44,148
v_mul_f32_e32 v86, s54, v86
v_mul_f32_e32 v87, s54, v87
Mz 52,44,132
Cv 86,86,87
v_accvgpr_write_b32 a123, v86
v_mul_f32_e32 v88, s54, v88
Mv 52,40,128
buffer_load_dwordx4 v15, s[8:11], s39 offen offset:3072 lds
Mz 56,48,128
Rb 180,17,24576
v_mul_f32_e32 v89, s54, v89
Mv 56,52,132
Cv 88,88,89
v_accvgpr_write_b32 a124, v88
Mz 120,52,148
v_mul_f32_e32 v90, s54, v90
v_mul_f32_e32 v91, s54, v91
Mv 120,48,144
s_add_u32 s39, s39, 0x40000
Cv 90,90,91
Mz 152,48,152
Rb 184,16,26624
s_add_u32 m0, s37, 16384
Mv 152,52,156
buffer_load_dwordx4 v12, s[8:11], s39 offen offset:0 lds
Mz 156,60,156
)" R"(
v_accvgpr_write_b32 a125, v90
v_mul_f32_e32 v92, s54, v92
Mv 156,56,152
v_mul_f32_e32 v93, s54, v93
Cv 92,92,93
Mz 124,56,144
Rb 188,17,26624
v_accvgpr_write_b32 a126, v92
Mv 124,60,148
v_mul_f32_e32 v94, s54, v94
v_mul_f32_e32 v95, s54, v95
Mz 60,60,132
Cv 94,94,95
v_accvgpr_write_b32 a127, v94
Mv 60,56,128
buffer_load_dwordx4 v13, s[8:11], s39 offen offset:1024 lds
Mz 64,0,136
s_mov_b32 s85, 0x1508800
v_and_b32 v2, 7, v0
Mv 64,4,140
v_lshlrev_b32 v2, 7, v2
Mz 68,12,140
buffer_load_dword v4, v2, s[20:23], s85 offen sc0 sc1
Mv 68,8,136
Mz 72,16,136
Mv 72,20,140
buffer_load_dwordx4 v14, s[8:11], s39 offen offset:2048 lds
Mz 76,28,140
Mv 76,24,136
s_lshl_b32 s29, s91, 22
s_add_u32 s28, s89, s29
Mz 80,32,136
s_bfe_u32 s29, s63, 0x80000
s_lshl_b32 s29, s29, 16
Mv 80,36,140
s_add_u32 s45, s28, s29
s_bfe_u32 s29, s63, 0x80008
Mz 84,44,140
s_lshl_b32 s29, s29, 16
s_add_u32 s46, s28, s29
Mv 84,40,136
buffer_load_dwordx4 v15, s[8:11], s39 offen offset:3072 lds
Mz 88,48,136
s_bfe_u32 s29, s63, 0x80010
s_lshl_b32 s29, s29, 16
s_add_u32 s47, s28, s29
Mv 88,52,140
s_bfe_u32 s29, s63, 0x80018
s_lshl_b32 s29, s29, 16
Mz 92,60,140
s_add_u32 s48, s28, s29
s_add_u32 s50, s89, 0xd40000
Mv 92,56,136
s_waitcnt vmcnt(9) lgkmcnt(0)
s_barrier
Mz 160,0,160
Rb 128,16,32768
s_lshl2_add_u32 s28, s91, s93
Mv 160,4,164
Rb 132,17,32768
s_add_u32 s38, s38, 0xffff0080
Mz 192,4,172
Rb 136,16,34816
s_add_u32 m0, s36, 0
Mv 192,0,168
)" R"(
buffer_load_dwordx4 v8, s[4:7], s38 offen offset:0 lds
Mz 224,0,176
Rb 140,17,34816
s_lshl_b32 s28, s28, 7
s_add_u32 s49, s28, s50
Mv 224,4,180
Rb 144,16,40960
s_add_u32 s49, s49, 4096
v_mfma_f32_16x16x32_bf16 a[192:195], a[4:7], a[188:191], 0
Rb 148,17,40960
s_lshl_b32 s52, s35, 7
Ma 192,0,184
Rb 152,16,43008
buffer_load_dwordx4 v9, s[4:7], s38 offen offset:1024 lds
v_mfma_f32_16x16x32_bf16 a[196:199], a[8:11], a[184:187], 0
Rb 156,17,43008
Rt 0,18,32768
Ma 196,12,188
s_add_u32 s52, s52, s50
s_add_u32 s52, s52, 0xd000
s_lshl_b32 s51, s35, 17
Mz 228,12,180
Rt 2,18,34816
s_add_u32 s51, s51, s89
Mv 228,8,176
buffer_load_dwordx4 v10, s[4:7], s38 offen offset:2048 lds
Mz 196,8,168
Rt 4,18,49152
s_add_u32 s51, s51, 0x800000
Mv 196,12,172
Mz 164,12,164
Rt 6,18,51200
Mv 164,8,160
buffer_load_dwordx4 v11, s[4:7], s38 offen offset:3072 lds
Mz 168,16,160
Rt 8,19,32768
Mv 168,20,164
s_add_u32 s38, s38, 0x10000
Mz 200,20,172
Rt 10,19,34816
s_add_u32 m0, s36, 4096
Mv 200,16,168
buffer_load_dwordx4 v8, s[4:7], s38 offen offset:0 lds
Mz 232,16,176
Rt 12,19,49152
Mv 232,20,180
v_mfma_f32_16x16x32_bf16 a[200:203], a[20:23], a[188:191], 0
Rt 14,19,51200
Ma 200,16,184
buffer_load_dwordx4 v9, s[4:7], s38 offen offset:1024 lds
v_mfma_f32_16x16x32_bf16 a[204:207], a[24:27], a[184:187], 0
Rt 16,20,32768
Ma 204,28,188
s_cmp_lg_u32 s33, 0
s_cmov_b32 s49, 0x40000000
s_cmov_b32 s52, 0x40000000
Mz 236,28,180
Rt 18,20,34816
s_mov_b32 s53, 0x40000
Mv 236,24,176
buffer_load_dwordx4 v10, s[4:7], s38 offen offset:2048 lds
Mz 204,24,168
Rt 20,20,49152
s_mov_b32 s62, 0x4e800000
)" R"(
Mv 204,28,172
s_add_u32 s67, s89, 0xc00000
s_lshl3_add_u32 s28, s34, s35
Mz 172,28,164
Rt 22,20,51200
s_add_u32 s64, s51, 0x100000
s_lshl2_add_u32 s28, s28, s33
Mv 172,24,160
buffer_load_dwordx4 v11, s[4:7], s38 offen offset:3072 lds
Mz 176,32,160
Rt 24,21,32768
s_lshl2_add_u32 s65, s35, s33
Mv 176,36,164
s_lshl_b32 s65, s65, 7
s_add_u32 s65, s65, s50
Mz 208,36,172
Rt 26,21,34816
s_add_u32 s65, s65, 0xc000
Mv 208,32,168
s_lshl_b32 s66, s28, 10
s_add_u32 s66, s66, 0x1400000
Mz 240,32,176
Rt 28,21,49152
s_sub_u32 s28, 15, s35
Mv 240,36,180
s_lshl2_add_u32 s28, s28, s33
s_and_b32 s29, s34, 1
v_mfma_f32_16x16x32_bf16 a[208:211], a[36:39], a[188:191], 0
Rt 30,21,51200
s_lshl1_add_u32 s29, s28, s29
s_lshl_b32 s73, s29, 15
Ma 208,32,184
s_and_b32 s29, s89, 0xfe000000
s_add_u32 s73, s73, s29
v_mfma_f32_16x16x32_bf16 a[212:215], a[40:43], a[184:187], 0
Rt 32,22,32768
s_add_u32 s73, s73, 0x1002000
Ma 212,44,188
s_lshl4_add_u32 s28, s28, s34
s_lshl_b32 s68, s28, 8
Mz 244,44,180
Rt 34,22,34816
s_add_u32 s68, s68, 0x1510000
Mv 244,40,176
s_xor_b32 s72, s73, 0x3c6000
s_mov_b32 s55, s89
Mz 212,40,168
Rt 36,22,49152
s_mov_b32 s57, 18
Mv 212,44,172
s_mov_b32 s59, s67
s_mov_b32 s58, 0
Mz 180,44,164
Rt 38,22,51200
Mv 180,40,160
s_cmp_eq_u32 s35, 7
s_cmov_b32 s58, 2
Mz 184,48,160
Rt 40,23,32768
Mv 184,52,164
s_cmp_eq_u32 s35, 7
s_cmov_b32 s64, 0x40000000
Mz 216,52,172
Rt 42,23,34816
)" R"(
Mv 216,48,168
s_cmp_eq_u32 s35, 7
s_cmov_b32 s66, 0x40000000
s_add_u32 s56, s55, 0x400000
Mz 248,48,176
Rt 44,23,49152
Mv 248,52,180
v_mfma_f32_16x16x32_bf16 a[216:219], a[52:55], a[188:191], 0
Rt 46,23,51200
Ma 216,48,184
v_mfma_f32_16x16x32_bf16 a[220:223], a[56:59], a[184:187], 0
Rt 48,24,32768
Ma 220,60,188
Mz 252,60,180
Rt 50,24,34816
Mv 252,56,176
Mz 220,56,168
Rt 52,24,49152
Mv 220,60,172
Mz 188,60,164
Rt 54,24,51200
Mv 188,56,160
Mv 32,0,128
Rt 56,25,32768
Mv 32,4,132
Rt 58,25,34816
Mv 64,4,140
Rt 60,25,49152
Mv 64,0,136
Rt 62,25,51200
Mv 96,0,144
Mv 96,4,148
Mv 128,4,156
Mv 128,0,152
Mv 132,8,152
Mv 132,12,156
Mv 100,12,148
Mv 100,8,144
Mv 68,8,136
Mv 68,12,140
Mv 36,12,132
s_add_u32 s39, s39, 0x40000
Mv 36,8,128
s_waitcnt vmcnt(10) lgkmcnt(0)
s_barrier
Mv 40,16,128
Rb 160,16,49152
s_add_u32 m0, s37, 32768
v_xor_b32 v4, s3, v4
v_cmp_gt_u32_e32 vcc, 2, v4
s_andn2_b32 s28, 0xff, vcc_lo
s_cbranch_scc1 .Lzsp13
.Lzok13:
Mv 40,20,132
buffer_load_dwordx4 v12, s[8:11], s39 offen offset:0 lds
Mv 72,20,140
v_xor_b32 v4, s3, v4
v_and_b32 v4, 1, v4
Mv 72,16,136
v_cmp_eq_u32_e32 vcc, 1, v4
s_mov_b32 s85, vcc_lo
Mv 104,16,144
Rb 164,17,49152
s_and_b32 s28, s2, 7
Mv 104,20,148
)" R"(
s_lshr_b32 s28, s85, s28
s_xor_b32 s28, s28, s34
Mv 136,20,156
s_and_b32 s28, s28, 1
s_lshl_b32 s84, s28, 13
Mv 136,16,152
buffer_load_dwordx4 v13, s[8:11], s39 offen offset:1024 lds
Mv 140,24,152
Rb 168,16,51200
s_add_u32 s49, s49, s84
Mv 140,28,156
s_add_u32 s50, s50, s84
s_add_u32 s60, s50, 7168
Mv 108,28,148
s_add_u32 s52, s52, s84
s_add_u32 s65, s65, s84
Mv 108,24,144
s_lshr_b32 s28, s2, 6
s_lshl_b32 s28, s28, 14
Mv 76,24,136
Rb 172,17,51200
s_add_u32 s28, s28, s50
Mv 76,28,140
buffer_load_dwordx4 v14, s[8:11], s39 offen offset:2048 lds
Mv 44,28,132
s_bfe_u32 s29, s2, 0x20004
s_lshl2_add_u32 s29, s29, s33
Mv 44,24,128
s_lshl_b32 s29, s29, 9
s_add_u32 s28, s28, s29
Mv 48,32,128
Rb 176,16,57344
v_min_u32 v4, 3, v0
Mv 48,36,132
v_lshlrev_b32 v4, 7, v4
Mv 80,36,140
buffer_load_dword v4, v4, s[20:23], s28 offen sc0 sc1
Mv 80,32,136
buffer_load_dwordx4 v15, s[8:11], s39 offen offset:3072 lds
Mv 112,32,144
Rb 180,17,57344
s_lshl1_add_u32 s28, s36, s84
Mv 112,36,148
v_add_u32 v2, s28, v1
v_add_u32 v3, 0x1000, v2
Mv 144,36,156
s_lshl_b32 s28, s34, 13
s_xor_b32 s28, s28, s84
Mv 144,32,152
s_add_u32 s39, s39, 0x40000
s_and_b32 s28, s28, 0x2000
Mv 148,40,152
Rb 184,16,59392
s_add_u32 m0, s37, 49152
Mv 148,44,156
buffer_load_dwordx4 v12, s[8:11], s39 offen offset:0 lds
Mv 116,44,148
s_xor_b32 s73, s73, s28
s_xor_b32 s72, s72, s28
Mv 116,40,144
Mv 84,40,136
Rb 188,17,59392
Mv 84,44,140
Mv 52,44,132
)" R"(
Mv 52,40,128
buffer_load_dwordx4 v13, s[8:11], s39 offen offset:1024 lds
Mv 56,48,128
Mv 56,52,132
Mv 88,52,140
Mv 88,48,136
Mv 120,48,144
Mv 120,52,148
buffer_load_dwordx4 v14, s[8:11], s39 offen offset:2048 lds
Mv 152,52,156
Mv 152,48,152
Mv 156,56,152
Mv 156,60,156
Mv 124,60,148
Mv 124,56,144
buffer_load_dwordx4 v15, s[8:11], s39 offen offset:3072 lds
Mv 92,56,136
Mv 92,60,140
Mv 60,60,132
Mv 60,56,128
s_waitcnt vmcnt(9) lgkmcnt(0)
s_barrier
Mv 160,0,160
Rb 128,16,0
s_mov_b64 s[16:17], s[70:71]
Mv 160,4,164
Rb 132,17,0
s_add_u32 s38, s38, 0xffff0080
Mv 192,4,172
Rb 136,16,2048
s_add_u32 m0, s36, 32768
Mv 192,0,168
buffer_load_dwordx4 v8, s[4:7], s38 offen offset:0 lds
Mv 224,0,176
Rb 140,17,2048
s_mov_b32 s18, 0x188000
Mv 224,4,180
Rb 144,16,8192
Ma 192,4,188
Rb 148,17,8192
Ma 192,0,184
Rb 152,16,10240
buffer_load_dwordx4 v9, s[4:7], s38 offen offset:1024 lds
Ma 196,8,184
Rb 156,17,10240
Rt 0,18,0
Ma 196,12,188
s_cmp_eq_u32 s33, 0
s_cmov_b32 s18, 0x380000
Mv 228,12,180
Rt 2,18,2048
s_mul_i32 s43, s2, 3072
Mv 228,8,176
buffer_load_dwordx4 v10, s[4:7], s38 offen offset:2048 lds
Mv 196,8,168
Rt 4,18,16384
Mv 196,12,172
s_cmp_eq_u32 s33, 0
s_cselect_b32 s44, 3, 6
Mv 164,12,164
Rt 6,18,18432
s_mul_i32 s44, s44, 0x60000
Mv 164,8,160
buffer_load_dwordx4 v11, s[4:7], s38 offen offset:3072 lds
Mv 168,16,160
)" R"(
Rt 8,19,0
s_sub_u32 s43, s43, s44
Mv 168,20,164
s_add_u32 s38, s38, 0x10000
v_min_u32 v26, 23, v0
Mv 200,20,172
Rt 10,19,2048
s_add_u32 m0, s36, 36864
Mv 200,16,168
buffer_load_dwordx4 v8, s[4:7], s38 offen offset:0 lds
Mv 232,16,176
Rt 12,19,16384
v_lshlrev_b32 v26, 7, v26
Mv 232,20,180
v_and_b32 v5, 7, v0
v_lshlrev_b32 v5, 7, v5
Ma 200,20,188
Rt 14,19,18432
Ma 200,16,184
buffer_load_dwordx4 v9, s[4:7], s38 offen offset:1024 lds
Ma 204,24,184
Rt 16,20,0
Ma 204,28,188
Mv 236,28,180
Rt 18,20,2048
Mv 236,24,176
buffer_load_dwordx4 v10, s[4:7], s38 offen offset:2048 lds
Mv 204,24,168
Rt 20,20,16384
Mv 204,28,172
Mv 172,28,164
Rt 22,20,18432
v_accvgpr_write_b32 a224, -1
Mv 172,24,160
buffer_load_dwordx4 v11, s[4:7], s38 offen offset:3072 lds
Mv 176,32,160
Rt 24,21,0
v_accvgpr_write_b32 a225, -1
Mv 176,36,164
v_accvgpr_write_b32 a226, -1
v_accvgpr_write_b32 a227, -1
Mv 208,36,172
Rt 26,21,2048
s_add_u32 s88, s73, 0x4000
Mv 208,32,168
buffer_store_dwordx4 a[64:67], v2, s[20:23], s51 offen offset:0 sc0 sc1
Mv 240,32,176
Rt 28,21,16384
s_mov_b32 s40, 5
Mv 240,36,180
s_mov_b32 s94, 0
Ma 208,36,188
Rt 30,21,18432
Ma 208,32,184
Ma 212,40,184
Rt 32,22,0
Ma 212,44,188
Mv 244,44,180
Rt 34,22,2048
Mv 244,40,176
buffer_store_dwordx4 a[68:71], v2, s[20:23], s51 offen offset:1024 sc0 sc1
Mv 212,40,168
Rt 36,22,16384
Mv 212,44,172
)" R"(
Mv 180,44,164
Rt 38,22,18432
Mv 180,40,160
Mv 184,48,160
Rt 40,23,0
Mv 184,52,164
Mv 216,52,172
Rt 42,23,2048
Mv 216,48,168
buffer_store_dwordx4 a[72:75], v2, s[20:23], s51 offen offset:2048 sc0 sc1
Mv 248,48,176
Rt 44,23,16384
Mv 248,52,180
Ma 216,52,188
Rt 46,23,18432
Ma 216,48,184
Ma 220,56,184
Rt 48,24,0
Ma 220,60,188
Mv 252,60,180
Rt 50,24,2048
Mv 252,56,176
Mv 220,56,168
Rt 52,24,16384
Mv 220,60,172
Mv 188,60,164
Rt 54,24,18432
Mv 188,56,160
Mv 32,0,128
Rt 56,25,0
Mv 32,4,132
Rt 58,25,2048
Mv 64,4,140
Rt 60,25,16384
Mv 64,0,136
Rt 62,25,18432
Mv 96,0,144
Mv 96,4,148
buffer_store_dwordx4 a[76:79], v2, s[20:23], s51 offen offset:3072 sc0 sc1
Mv 128,4,156
Mv 128,0,152
Mv 132,8,152
Mv 132,12,156
Mv 100,12,148
Mv 100,8,144
buffer_store_dwordx4 a[80:83], v3, s[20:23], s51 offen offset:0 sc0 sc1
Mv 68,8,136
Mv 68,12,140
Mv 36,12,132
s_add_u32 s39, s39, 0x40000
Mv 36,8,128
s_waitcnt lgkmcnt(0)
s_barrier
Mv 40,16,128
Rb 160,16,16384
s_add_u32 m0, s37, 0
Mv 40,20,132
buffer_load_dwordx4 v12, s[8:11], s39 offen offset:0 lds
Mv 72,20,140
Mv 72,16,136
Mv 104,16,144
Rb 164,17,16384
Mv 104,20,148
Mv 136,20,156
Mv 136,16,152
)" R"(
buffer_load_dwordx4 v13, s[8:11], s39 offen offset:1024 lds
Mv 140,24,152
Rb 168,16,18432
Mv 140,28,156
Mv 108,28,148
Mv 108,24,144
Mv 76,24,136
Rb 172,17,18432
Mv 76,28,140
buffer_load_dwordx4 v14, s[8:11], s39 offen offset:2048 lds
Mv 44,28,132
.LzH3:
s_cbranch_execz .LzH13
Mv 44,24,128
Mv 48,32,128
Rb 176,16,24576
Mv 48,36,132
Mv 80,36,140
Mv 80,32,136
buffer_load_dwordx4 v15, s[8:11], s39 offen offset:3072 lds
Mv 112,32,144
Rb 180,17,24576
Mv 112,36,148
Mv 144,36,156
buffer_store_dwordx4 a[84:87], v3, s[20:23], s51 offen offset:1024 sc0 sc1
Mv 144,32,152
s_add_u32 s39, s39, 0x40000
Mv 148,40,152
Rb 184,16,26624
s_add_u32 m0, s37, 16384
Mv 148,44,156
buffer_load_dwordx4 v12, s[8:11], s39 offen offset:0 lds
Mv 116,44,148
Mv 116,40,144
Mv 84,40,136
Rb 188,17,26624
Mv 84,44,140
Mv 52,44,132
Mv 52,40,128
buffer_load_dwordx4 v13, s[8:11], s39 offen offset:1024 lds
Mv 56,48,128
Mv 56,52,132
Mv 88,52,140
Mv 88,48,136
Mv 120,48,144
Mv 120,52,148
buffer_load_dwordx4 v14, s[8:11], s39 offen offset:2048 lds
Mv 152,52,156
Mv 152,48,152
Mv 156,56,152
Mv 156,60,156
Mv 124,60,148
Mv 124,56,144
buffer_load_dwordx4 v15, s[8:11], s39 offen offset:3072 lds
Mv 92,56,136
Mv 92,60,140
Mv 60,60,132
Mv 60,56,128
s_waitcnt vmcnt(14) lgkmcnt(0)
s_barrier
Mv 160,0,160
Rb 128,16,32768
Mv 160,4,164
Rb 132,17,32768
s_add_u32 s38, s38, 0xffff0080
)" R"(
Mv 192,4,172
Rb 136,16,34816
s_add_u32 m0, s36, 0
Mv 192,0,168
buffer_load_dwordx4 v8, s[4:7], s38 offen offset:0 lds
Mv 224,0,176
Rb 140,17,34816
Mv 224,4,180
Rb 144,16,40960
Ma 192,4,188
Rb 148,17,40960
Ma 192,0,184
Rb 152,16,43008
buffer_load_dwordx4 v9, s[4:7], s38 offen offset:1024 lds
Ma 196,8,184
Rb 156,17,43008
Rt 0,18,32768
Ma 196,12,188
Mv 228,12,180
Rt 2,18,34816
Mv 228,8,176
buffer_load_dwordx4 v10, s[4:7], s38 offen offset:2048 lds
Mv 196,8,168
Rt 4,18,49152
Mv 196,12,172
Mv 164,12,164
Rt 6,18,51200
Mv 164,8,160
buffer_load_dwordx4 v11, s[4:7], s38 offen offset:3072 lds
Mv 168,16,160
Rt 8,19,32768
Mv 168,20,164
s_add_u32 s38, s38, 0x10000
Mv 200,20,172
Rt 10,19,34816
s_add_u32 m0, s36, 4096
Mv 200,16,168
buffer_load_dwordx4 v8, s[4:7], s38 offen offset:0 lds
Mv 232,16,176
Rt 12,19,49152
Mv 232,20,180
Ma 200,20,188
Rt 14,19,51200
Ma 200,16,184
buffer_load_dwordx4 v9, s[4:7], s38 offen offset:1024 lds
Ma 204,24,184
Rt 16,20,32768
Ma 204,28,188
Mv 236,28,180
Rt 18,20,34816
Mv 236,24,176
buffer_load_dwordx4 v10, s[4:7], s38 offen offset:2048 lds
Mv 204,24,168
Rt 20,20,49152
Mv 204,28,172
Mv 172,28,164
Rt 22,20,51200
Mv 172,24,160
buffer_load_dwordx4 v11, s[4:7], s38 offen offset:3072 lds
Mv 176,32,160
Rt 24,21,32768
Mv 176,36,164
Mv 208,36,172
Rt 26,21,34816
)" R"(
Mv 208,32,168
buffer_store_dwordx4 a[88:91], v3, s[20:23], s51 offen offset:2048 sc0 sc1
Mv 240,32,176
Rt 28,21,49152
Mv 240,36,180
Ma 208,36,188
Rt 30,21,51200
Ma 208,32,184
Ma 212,40,184
Rt 32,22,32768
Ma 212,44,188
buffer_store_dwordx4 a[92:95], v3, s[20:23], s51 offen offset:3072 sc0 sc1
Mv 244,44,180
Rt 34,22,34816
Mv 244,40,176
Mv 212,40,168
Rt 36,22,49152
Mv 212,44,172
Mv 180,44,164
Rt 38,22,51200
Mv 180,40,160
s_add_u32 s51, s51, 0x10000
buffer_store_dwordx4 a[96:99], v2, s[20:23], s51 offen offset:0 sc0 sc1
Mv 184,48,160
Rt 40,23,32768
Mv 184,52,164
Mv 216,52,172
Rt 42,23,34816
Mv 216,48,168
Mv 248,48,176
Rt 44,23,49152
Mv 248,52,180
buffer_store_dwordx4 a[100:103], v2, s[20:23], s51 offen offset:1024 sc0 sc1
Ma 216,52,188
Rt 46,23,51200
Ma 216,48,184
Ma 220,56,184
Rt 48,24,32768
Ma 220,60,188
s_add_u32 s43, s43, 0x60000
Mv 252,60,180
Rt 50,24,34816
Mv 252,56,176
buffer_load_dword v27, v26, s[16:19], s43 offen
Mv 220,56,168
Rt 52,24,49152
Mv 220,60,172
Mv 188,60,164
Rt 54,24,51200
Mv 188,56,160
Mv 32,0,128
Rt 56,25,32768
Mv 32,4,132
Rt 58,25,34816
Mv 64,4,140
Rt 60,25,49152
Mv 64,0,136
Rt 62,25,51200
Mv 96,0,144
Mv 96,4,148
buffer_store_dwordx4 a[104:107], v2, s[20:23], s51 offen offset:2048 sc0 sc1
Mv 128,4,156
Mv 128,0,152
Mv 132,8,152
)" R"(
Mv 132,12,156
Mv 100,12,148
Mv 100,8,144
buffer_store_dwordx4 a[108:111], v2, s[20:23], s51 offen offset:3072 sc0 sc1
Mv 68,8,136
Mv 68,12,140
Mv 36,12,132
s_add_u32 s39, s39, 0x40000
Mv 36,8,128
s_waitcnt lgkmcnt(0)
s_barrier
Mv 40,16,128
Rb 160,16,49152
s_add_u32 m0, s37, 32768
Mv 40,20,132
buffer_load_dwordx4 v12, s[8:11], s39 offen offset:0 lds
Mv 72,20,140
Mv 72,16,136
Mv 104,16,144
Rb 164,17,49152
Mv 104,20,148
Mv 136,20,156
Mv 136,16,152
buffer_load_dwordx4 v13, s[8:11], s39 offen offset:1024 lds
Mv 140,24,152
Rb 168,16,51200
Mv 140,28,156
Mv 108,28,148
Mv 108,24,144
Mv 76,24,136
Rb 172,17,51200
Mv 76,28,140
buffer_load_dwordx4 v14, s[8:11], s39 offen offset:2048 lds
Mv 44,28,132
Mv 44,24,128
Mv 48,32,128
Rb 176,16,57344
Mv 48,36,132
Mv 80,36,140
Mv 80,32,136
buffer_load_dwordx4 v15, s[8:11], s39 offen offset:3072 lds
Mv 112,32,144
Rb 180,17,57344
Mv 112,36,148
Mv 144,36,156
buffer_store_dwordx4 a[112:115], v3, s[20:23], s51 offen offset:0 sc0 sc1
Mv 144,32,152
s_add_u32 s39, s39, 0x40000
Mv 148,40,152
Rb 184,16,59392
s_add_u32 m0, s37, 49152
Mv 148,44,156
buffer_load_dwordx4 v12, s[8:11], s39 offen offset:0 lds
Mv 116,44,148
Mv 116,40,144
Mv 84,40,136
Rb 188,17,59392
Mv 84,44,140
Mv 52,44,132
Mv 52,40,128
buffer_load_dwordx4 v13, s[8:11], s39 offen offset:1024 lds
Mv 56,48,128
Mv 56,52,132
Mv 88,52,140
Mv 88,48,136
)" R"(
Mv 120,48,144
Mv 120,52,148
buffer_load_dwordx4 v14, s[8:11], s39 offen offset:2048 lds
Mv 152,52,156
Mv 152,48,152
Mv 156,56,152
Mv 156,60,156
Mv 124,60,148
Mv 124,56,144
buffer_load_dwordx4 v15, s[8:11], s39 offen offset:3072 lds
Mv 92,56,136
Mv 92,60,140
Mv 60,60,132
Mv 60,56,128
s_waitcnt vmcnt(16) lgkmcnt(0)
s_barrier
Mv 160,0,160
Rb 128,16,0
Mv 160,4,164
Rb 132,17,0
s_add_u32 s38, s38, 0xffff0080
Mv 192,4,172
Rb 136,16,2048
s_add_u32 m0, s36, 32768
Mv 192,0,168
buffer_load_dwordx4 v8, s[4:7], s38 offen offset:0 lds
Mv 224,0,176
Rb 140,17,2048
Mv 224,4,180
Rb 144,16,8192
Ma 192,4,188
Rb 148,17,8192
Ma 192,0,184
Rb 152,16,10240
buffer_load_dwordx4 v9, s[4:7], s38 offen offset:1024 lds
Ma 196,8,184
Rb 156,17,10240
Rt 0,18,0
Ma 196,12,188
Mv 228,12,180
Rt 2,18,2048
Mv 228,8,176
buffer_load_dwordx4 v10, s[4:7], s38 offen offset:2048 lds
Mv 196,8,168
Rt 4,18,16384
Mv 196,12,172
Mv 164,12,164
Rt 6,18,18432
Mv 164,8,160
buffer_load_dwordx4 v11, s[4:7], s38 offen offset:3072 lds
Mv 168,16,160
Rt 8,19,0
Mv 168,20,164
s_add_u32 s38, s38, 0x10000
Mv 200,20,172
Rt 10,19,2048
s_add_u32 m0, s36, 36864
Mv 200,16,168
buffer_load_dwordx4 v8, s[4:7], s38 offen offset:0 lds
Mv 232,16,176
Rt 12,19,16384
Mv 232,20,180
Ma 200,20,188
Rt 14,19,18432
Ma 200,16,184
)" R"(
buffer_load_dwordx4 v9, s[4:7], s38 offen offset:1024 lds
Ma 204,24,184
Rt 16,20,0
Ma 204,28,188
Mv 236,28,180
Rt 18,20,2048
Mv 236,24,176
buffer_load_dwordx4 v10, s[4:7], s38 offen offset:2048 lds
Mv 204,24,168
Rt 20,20,16384
Mv 204,28,172
Mv 172,28,164
Rt 22,20,18432
Mv 172,24,160
buffer_load_dwordx4 v11, s[4:7], s38 offen offset:3072 lds
Mv 176,32,160
Rt 24,21,0
Mv 176,36,164
Mv 208,36,172
Rt 26,21,2048
Mv 208,32,168
buffer_store_dwordx4 a[116:119], v3, s[20:23], s51 offen offset:1024 sc0 sc1
Mv 240,32,176
Rt 28,21,16384
Mv 240,36,180
Ma 208,36,188
Rt 30,21,18432
Ma 208,32,184
Ma 212,40,184
Rt 32,22,0
Ma 212,44,188
buffer_store_dwordx4 a[120:123], v3, s[20:23], s51 offen offset:2048 sc0 sc1
Mv 244,44,180
Rt 34,22,2048
Mv 244,40,176
Mv 212,40,168
Rt 36,22,16384
Mv 212,44,172
Mv 180,44,164
Rt 38,22,18432
Mv 180,40,160
buffer_store_dwordx4 a[124:127], v3, s[20:23], s51 offen offset:3072 sc0 sc1
Mv 184,48,160
Rt 40,23,0
Mv 184,52,164
Mv 216,52,172
Rt 42,23,2048
Mv 216,48,168
Mv 248,48,176
Rt 44,23,16384
Mv 248,52,180
Ma 216,52,188
Rt 46,23,18432
Ma 216,48,184
Ma 220,56,184
Rt 48,24,0
Ma 220,60,188
s_add_u32 s43, s43, 0x60000
Mv 252,60,180
Rt 50,24,2048
Mv 252,56,176
buffer_load_dword v27, v26, s[16:19], s43 offen
Mv 220,56,168
Rt 52,24,16384
)" R"(
Mv 220,60,172
Mv 188,60,164
Rt 54,24,18432
Mv 188,56,160
.LzPBK:
Mv 32,0,128
Rt 56,25,0
Mv 32,4,132
Rt 58,25,2048
Mv 64,4,140
Rt 60,25,16384
Mv 64,0,136
Rt 62,25,18432
Mv 96,0,144
Mv 96,4,148
Mv 128,4,156
Mv 128,0,152
Mv 132,8,152
Mv 132,12,156
Mv 100,12,148
Mv 100,8,144
Mv 68,8,136
Mv 68,12,140
Mv 36,12,132
s_add_u32 s39, s39, 0x40000
Mv 36,8,128
s_waitcnt lgkmcnt(0)
s_barrier
Mv 40,16,128
Rb 160,16,16384
s_add_u32 m0, s37, 0
Mv 40,20,132
buffer_load_dwordx4 v12, s[8:11], s39 offen offset:0 lds
Mv 72,20,140
Mv 72,16,136
Mv 104,16,144
Rb 164,17,16384
Mv 104,20,148
Mv 136,20,156
Mv 136,16,152
buffer_load_dwordx4 v13, s[8:11], s39 offen offset:1024 lds
Mv 140,24,152
Rb 168,16,18432
Mv 140,28,156
Mv 108,28,148
Mv 108,24,144
Mv 76,24,136
Rb 172,17,18432
Mv 76,28,140
buffer_load_dwordx4 v14, s[8:11], s39 offen offset:2048 lds
Mv 44,28,132
Mv 44,24,128
Mv 48,32,128
Rb 176,16,24576
Mv 48,36,132
Mv 80,36,140
Mv 80,32,136
buffer_load_dwordx4 v15, s[8:11], s39 offen offset:3072 lds
Mv 112,32,144
Rb 180,17,24576
Mv 112,36,148
Mv 144,36,156
Mv 144,32,152
s_add_u32 s39, s39, 0x40000
Mv 148,40,152
)" R"(
Rb 184,16,26624
s_add_u32 m0, s37, 16384
Mv 148,44,156
buffer_load_dwordx4 v12, s[8:11], s39 offen offset:0 lds
Mv 116,44,148
Mv 116,40,144
Mv 84,40,136
Rb 188,17,26624
Mv 84,44,140
Mv 52,44,132
Mv 52,40,128
buffer_load_dwordx4 v13, s[8:11], s39 offen offset:1024 lds
Mv 56,48,128
Mv 56,52,132
Mv 88,52,140
Mv 88,48,136
Mv 120,48,144
Mv 120,52,148
buffer_load_dwordx4 v14, s[8:11], s39 offen offset:2048 lds
Mv 152,52,156
Mv 152,48,152
Mv 156,56,152
Mv 156,60,156
Mv 124,60,148
Mv 124,56,144
buffer_load_dwordx4 v15, s[8:11], s39 offen offset:3072 lds
Mv 92,56,136
Mv 92,60,140
Mv 60,60,132
Mv 60,56,128
s_waitcnt vmcnt(11) lgkmcnt(0)
s_barrier
Mv 160,0,160
Rb 128,16,32768
Mv 160,4,164
Rb 132,17,32768
s_add_u32 s38, s38, 0xffff0080
Mv 192,4,172
Rb 136,16,34816
s_add_u32 m0, s36, 0
Mv 192,0,168
buffer_load_dwordx4 v8, s[4:7], s38 offen offset:0 lds
Mv 224,0,176
Rb 140,17,34816
Mv 224,4,180
Rb 144,16,40960
Ma 192,4,188
Rb 148,17,40960
Ma 192,0,184
Rb 152,16,43008
buffer_load_dwordx4 v9, s[4:7], s38 offen offset:1024 lds
Ma 196,8,184
Rb 156,17,43008
Rt 0,18,32768
Ma 196,12,188
Mv 228,12,180
Rt 2,18,34816
Mv 228,8,176
buffer_load_dwordx4 v10, s[4:7], s38 offen offset:2048 lds
Mv 196,8,168
Rt 4,18,49152
Mv 196,12,172
Mv 164,12,164
Rt 6,18,51200
Mv 164,8,160
)" R"(
buffer_load_dwordx4 v11, s[4:7], s38 offen offset:3072 lds
Mv 168,16,160
Rt 8,19,32768
Mv 168,20,164
s_add_u32 s38, s38, 0x10000
Mv 200,20,172
Rt 10,19,34816
s_add_u32 m0, s36, 4096
Mv 200,16,168
buffer_load_dwordx4 v8, s[4:7], s38 offen offset:0 lds
Mv 232,16,176
Rt 12,19,49152
Mv 232,20,180
Ma 200,20,188
Rt 14,19,51200
.LzH4:
s_cbranch_execz .LzH14
Ma 200,16,184
buffer_load_dwordx4 v9, s[4:7], s38 offen offset:1024 lds
Ma 204,24,184
Rt 16,20,32768
Ma 204,28,188
Mv 236,28,180
Rt 18,20,34816
Mv 236,24,176
buffer_load_dwordx4 v10, s[4:7], s38 offen offset:2048 lds
Mv 204,24,168
Rt 20,20,49152
Mv 204,28,172
Mv 172,28,164
Rt 22,20,51200
Mv 172,24,160
buffer_load_dwordx4 v11, s[4:7], s38 offen offset:3072 lds
Mv 176,32,160
Rt 24,21,32768
Mv 176,36,164
Mv 208,36,172
Rt 26,21,34816
Mv 208,32,168
Mv 240,32,176
Rt 28,21,49152
Mv 240,36,180
buffer_store_dwordx4 a[224:227], v1, s[20:23], s72 offen sc0 sc1
Ma 208,36,188
Rt 30,21,51200
s_add_u32 s72, s72, 1024
Ma 208,32,184
Ma 212,40,184
Rt 32,22,32768
Ma 212,44,188
Mv 244,44,180
Rt 34,22,34816
Mv 244,40,176
Mv 212,40,168
Rt 36,22,49152
Mv 212,44,172
Mv 180,44,164
Rt 38,22,51200
Mv 180,40,160
Mv 184,48,160
Rt 40,23,32768
Mv 184,52,164
buffer_store_dwordx4 a[224:227], v1, s[20:23], s88 offen sc0 sc1
Mv 216,52,172
)" R"(
Rt 42,23,34816
s_add_u32 s88, s88, 1024
Mv 216,48,168
Mv 248,48,176
Rt 44,23,49152
Mv 248,52,180
Ma 216,52,188
Rt 46,23,51200
Ma 216,48,184
Ma 220,56,184
Rt 48,24,32768
Ma 220,60,188
s_add_u32 s43, s43, 0x60000
Mv 252,60,180
Rt 50,24,34816
Mv 252,56,176
buffer_load_dword v27, v26, s[16:19], s43 offen
Mv 220,56,168
Rt 52,24,49152
Mv 220,60,172
Mv 188,60,164
Rt 54,24,51200
Mv 188,56,160
Mv 32,0,128
Rt 56,25,32768
Mv 32,4,132
Rt 58,25,34816
Mv 64,4,140
Rt 60,25,49152
Mv 64,0,136
Rt 62,25,51200
Mv 96,0,144
Mv 96,4,148
Mv 128,4,156
Mv 128,0,152
Mv 132,8,152
Mv 132,12,156
Mv 100,12,148
Mv 100,8,144
Mv 68,8,136
Mv 68,12,140
Mv 36,12,132
s_add_u32 s39, s39, 0x40000
Mv 36,8,128
s_waitcnt vmcnt(20) lgkmcnt(0)
s_barrier
buffer_store_dword v6, v7, s[20:23], s52 offen offset:0 sc0 sc1
s_mov_b32 s52, 0x40000000
Mv 40,16,128
Rb 160,16,49152
s_add_u32 m0, s37, 32768
Mv 40,20,132
buffer_load_dwordx4 v12, s[8:11], s39 offen offset:0 lds
Mv 72,20,140
Mv 72,16,136
Mv 104,16,144
Rb 164,17,49152
Mv 104,20,148
Mv 136,20,156
Mv 136,16,152
buffer_load_dwordx4 v13, s[8:11], s39 offen offset:1024 lds
Mv 140,24,152
Rb 168,16,51200
Mv 140,28,156
Mv 108,28,148
)" R"(
Mv 108,24,144
Mv 76,24,136
Rb 172,17,51200
Mv 76,28,140
buffer_load_dwordx4 v14, s[8:11], s39 offen offset:2048 lds
Mv 44,28,132
Mv 44,24,128
Mv 48,32,128
Rb 176,16,57344
Mv 48,36,132
Mv 80,36,140
Mv 80,32,136
buffer_load_dwordx4 v15, s[8:11], s39 offen offset:3072 lds
Mv 112,32,144
Rb 180,17,57344
Mv 112,36,148
Mv 144,36,156
Mv 144,32,152
s_add_u32 s39, s39, 0x40000
Mv 148,40,152
Rb 184,16,59392
s_add_u32 m0, s37, 49152
Mv 148,44,156
buffer_load_dwordx4 v12, s[8:11], s39 offen offset:0 lds
Mv 116,44,148
Mv 116,40,144
Mv 84,40,136
Rb 188,17,59392
Mv 84,44,140
Mv 52,44,132
Mv 52,40,128
buffer_load_dwordx4 v13, s[8:11], s39 offen offset:1024 lds
Mv 56,48,128
Mv 56,52,132
Mv 88,52,140
Mv 88,48,136
Mv 120,48,144
Mv 120,52,148
buffer_load_dwordx4 v14, s[8:11], s39 offen offset:2048 lds
Mv 152,52,156
Mv 152,48,152
Mv 156,56,152
Mv 156,60,156
Mv 124,60,148
Mv 124,56,144
buffer_load_dwordx4 v15, s[8:11], s39 offen offset:3072 lds
Mv 92,56,136
Mv 92,60,140
Mv 60,60,132
Mv 60,56,128
s_waitcnt vmcnt(12) lgkmcnt(0)
s_barrier
Mv 160,0,160
Rb 128,16,0
Mv 160,4,164
Rb 132,17,0
s_add_u32 s38, s38, 0xffff0080
Mv 192,4,172
Rb 136,16,2048
s_add_u32 m0, s36, 32768
Mv 192,0,168
buffer_load_dwordx4 v8, s[4:7], s38 offen offset:0 lds
Mv 224,0,176
Rb 140,17,2048
Mv 224,4,180
)" R"(
Rb 144,16,8192
Ma 192,4,188
Rb 148,17,8192
Ma 192,0,184
Rb 152,16,10240
buffer_load_dwordx4 v9, s[4:7], s38 offen offset:1024 lds
Ma 196,8,184
Rb 156,17,10240
Rt 0,18,0
Ma 196,12,188
Mv 228,12,180
Rt 2,18,2048
Mv 228,8,176
buffer_load_dwordx4 v10, s[4:7], s38 offen offset:2048 lds
Mv 196,8,168
Rt 4,18,16384
Mv 196,12,172
Mv 164,12,164
Rt 6,18,18432
Mv 164,8,160
buffer_load_dwordx4 v11, s[4:7], s38 offen offset:3072 lds
Mv 168,16,160
Rt 8,19,0
Mv 168,20,164
s_add_u32 s38, s38, 0x10000
Mv 200,20,172
Rt 10,19,2048
s_add_u32 m0, s36, 36864
Mv 200,16,168
buffer_load_dwordx4 v8, s[4:7], s38 offen offset:0 lds
Mv 232,16,176
Rt 12,19,16384
Mv 232,20,180
Ma 200,20,188
Rt 14,19,18432
Ma 200,16,184
buffer_load_dwordx4 v9, s[4:7], s38 offen offset:1024 lds
Ma 204,24,184
Rt 16,20,0
Ma 204,28,188
Mv 236,28,180
Rt 18,20,2048
Mv 236,24,176
buffer_load_dwordx4 v10, s[4:7], s38 offen offset:2048 lds
Mv 204,24,168
Rt 20,20,16384
Mv 204,28,172
Mv 172,28,164
Rt 22,20,18432
Mv 172,24,160
buffer_load_dwordx4 v11, s[4:7], s38 offen offset:3072 lds
Mv 176,32,160
Rt 24,21,0
Mv 176,36,164
Mv 208,36,172
Rt 26,21,2048
Mv 208,32,168
Mv 240,32,176
Rt 28,21,16384
Mv 240,36,180
buffer_store_dwordx4 a[224:227], v1, s[20:23], s72 offen sc0 sc1
Ma 208,36,188
Rt 30,21,18432
s_add_u32 s72, s72, 1024
)" R"(
Ma 208,32,184
Ma 212,40,184
Rt 32,22,0
Ma 212,44,188
Mv 244,44,180
Rt 34,22,2048
Mv 244,40,176
Mv 212,40,168
Rt 36,22,16384
Mv 212,44,172
Mv 180,44,164
Rt 38,22,18432
Mv 180,40,160
Mv 184,48,160
Rt 40,23,0
Mv 184,52,164
buffer_store_dwordx4 a[224:227], v1, s[20:23], s88 offen sc0 sc1
Mv 216,52,172
Rt 42,23,2048
s_add_u32 s88, s88, 1024
Mv 216,48,168
Mv 248,48,176
Rt 44,23,16384
Mv 248,52,180
Ma 216,52,188
Rt 46,23,18432
Ma 216,48,184
s_cmp_eq_u32 s40, 2
s_cmov_b32 s72, 0x40000000
s_cmov_b32 s88, 0x40000000
Ma 220,56,184
Rt 48,24,0
Ma 220,60,188
s_add_u32 s43, s43, 0x60000
Mv 252,60,180
Rt 50,24,2048
s_sub_u32 s40, s40, 1
Mv 252,56,176
buffer_load_dword v27, v26, s[16:19], s43 offen
Mv 220,56,168
Rt 52,24,16384
Mv 220,60,172
Mv 188,60,164
Rt 54,24,18432
Mv 188,56,160
s_cmp_lg_u32 s40, 0
s_cbranch_scc1 .LzPBK
.LzPBKX:
Mv 32,0,128
Rt 56,25,0
Mv 32,4,132
Rt 58,25,2048
buffer_load_dword v4, v5, s[20:23], s50 offen offset:0 sc0 sc1
Mv 64,4,140
Rt 60,25,16384
Mv 64,0,136
Rt 62,25,18432
Mv 96,0,144
Mv 96,4,148
Mv 128,4,156
Mv 128,0,152
Mv 132,8,152
Mv 132,12,156
Mv 100,12,148
)" R"(
Mv 100,8,144
Mv 68,8,136
Mv 68,12,140
Mv 36,12,132
Mv 36,8,128
s_waitcnt lgkmcnt(0)
s_barrier
Mv 40,16,128
Rb 160,16,16384
Mv 40,20,132
Mv 72,20,140
Mv 72,16,136
Mv 104,16,144
Rb 164,17,16384
Mv 104,20,148
Mv 136,20,156
Mv 136,16,152
Mv 140,24,152
Rb 168,16,18432
Mv 140,28,156
Mv 108,28,148
Mv 108,24,144
Mv 76,24,136
Rb 172,17,18432
Mv 76,28,140
Mv 44,28,132
Mv 44,24,128
Mv 48,32,128
Rb 176,16,24576
Mv 48,36,132
Mv 80,36,140
Mv 80,32,136
Mv 112,32,144
Rb 180,17,24576
Mv 112,36,148
Mv 144,36,156
Mv 144,32,152
s_waitcnt vmcnt(0)
v_cmp_eq_u32_e32 vcc, s3, v4
s_andn2_b64 s[28:29], -1, vcc
s_cbranch_scc1 .Lzsp14
.Lzok14:
Mv 148,40,152
Rb 184,16,26624
Mv 148,44,156
s_lshl3_add_u32 s29, s34, s35
s_lshl2_add_u32 s29, s29, s33
Mv 116,44,148
s_add_u32 m0, s36, 131072
s_mul_i32 s83, s29, 7168
Mv 116,40,144
buffer_load_dwordx4 v2, s[20:23], s56 offen offset:0 sc1 lds
Mv 84,40,136
Rb 188,17,26624
s_mov_b32 s82, s89
Mv 84,44,140
s_add_u32 s82, s82, 0xc00000
s_add_u32 s82, s82, s84
Mv 52,44,132
s_mov_b32 s24, s80
s_mov_b32 s25, s81
Mv 52,40,128
s_mov_b32 s26, 0x380000
s_mov_b32 s27, 0x20000
Mv 56,48,128
)" R"(
s_add_u32 s28, s33, 0
s_lshl2_add_u32 s28, s35, s28
Mv 56,52,132
buffer_load_dwordx4 v2, s[20:23], s56 offen offset:1024 sc1 lds
Mv 88,52,140
s_mul_i32 s29, s28, 293
s_lshr_b32 s29, s29, 11
Mv 88,48,136
s_mul_i32 s29, s29, 9
s_add_u32 s29, s29, s28
Mv 120,48,144
s_lshl_b32 s29, s29, 10
s_add_u32 s4, s29, s82
Mv 120,52,148
s_add_u32 s28, s33, 4
s_lshl3_add_u32 s28, s28, s35
Mv 152,52,156
s_mul_i32 s29, s28, 293
s_lshr_b32 s29, s29, 11
Mv 152,48,152
buffer_load_dwordx4 v2, s[20:23], s56 offen offset:2048 sc1 lds
Mv 156,56,152
s_mul_i32 s29, s29, 9
s_add_u32 s29, s29, s28
Mv 156,60,156
s_lshl_b32 s29, s29, 10
s_add_u32 s5, s29, s82
Mv 124,60,148
s_add_u32 s28, s33, 8
s_lshl3_add_u32 s28, s28, s35
Mv 124,56,144
s_mul_i32 s29, s28, 293
s_lshr_b32 s29, s29, 11
Mv 92,56,136
s_mul_i32 s29, s29, 9
s_add_u32 s29, s29, s28
Mv 92,60,140
buffer_load_dwordx4 v2, s[20:23], s56 offen offset:3072 sc1 lds
Mv 60,60,132
s_lshl_b32 s29, s29, 10
s_add_u32 s6, s29, s82
Mv 60,56,128
s_waitcnt lgkmcnt(0)
s_barrier
Mv 160,0,160
Rb 128,16,32768
s_add_u32 s28, s33, 12
Mv 160,4,164
Rb 132,17,32768
s_lshl3_add_u32 s28, s28, s35
Mv 192,4,172
Rb 136,16,34816
s_add_u32 m0, s36, 0
Mv 192,0,168
buffer_load_dwordx4 v2, s[20:23], s55 offen offset:0 sc1 lds
Mv 224,0,176
Rb 140,17,34816
s_mul_i32 s29, s28, 293
Mv 224,4,180
Rb 144,16,40960
s_lshr_b32 s29, s29, 11
Ma 192,4,188
Rb 148,17,40960
s_mul_i32 s29, s29, 9
Ma 192,0,184
)" R"(
Rb 152,16,43008
s_add_u32 s29, s29, s28
Ma 196,8,184
Rb 156,17,43008
Rt 0,18,32768
buffer_load_dwordx4 v2, s[20:23], s55 offen offset:1024 sc1 lds
Ma 196,12,188
s_lshl_b32 s29, s29, 10
s_add_u32 s7, s29, s82
Mv 228,12,180
Rt 2,18,34816
Mv 228,8,176
s_cmp_gt_u32 s33, 1
s_cmov_b32 s7, 0x40000000
Mv 196,8,168
Rt 4,18,49152
s_add_u32 s28, s33, 14
Mv 196,12,172
buffer_load_dwordx4 v2, s[20:23], s55 offen offset:2048 sc1 lds
Mv 164,12,164
Rt 6,18,51200
s_lshl3_add_u32 s28, s28, s35
Mv 164,8,160
s_mul_i32 s29, s28, 293
s_lshr_b32 s29, s29, 11
Mv 168,16,160
Rt 8,19,32768
s_mul_i32 s29, s29, 9
Mv 168,20,164
s_add_u32 s29, s29, s28
s_lshl_b32 s29, s29, 10
Mv 200,20,172
Rt 10,19,34816
buffer_load_dwordx4 v2, s[20:23], s55 offen offset:3072 sc1 lds
Mv 200,16,168
s_add_u32 s8, s29, s82
s_add_u32 s28, s33, 18
Mv 232,16,176
Rt 12,19,49152
s_lshl3_add_u32 s28, s28, s35
Mv 232,20,180
s_mul_i32 s29, s28, 293
s_lshr_b32 s29, s29, 11
Ma 200,20,188
Rt 14,19,51200
s_add_u32 m0, s36, 4096
Ma 200,16,184
buffer_load_dwordx4 v3, s[20:23], s55 offen offset:0 sc1 lds
Ma 204,24,184
Rt 16,20,32768
s_mul_i32 s29, s29, 9
Ma 204,28,188
s_add_u32 s29, s29, s28
s_lshl_b32 s29, s29, 10
Mv 236,28,180
Rt 18,20,34816
s_add_u32 s9, s29, s82
Mv 236,24,176
s_add_u32 s28, s33, 22
s_lshl3_add_u32 s28, s28, s35
Mv 204,24,168
Rt 20,20,49152
buffer_load_dwordx4 v3, s[20:23], s55 offen offset:1024 sc1 lds
Mv 204,28,172
)" R"(
s_mul_i32 s29, s28, 293
s_lshr_b32 s29, s29, 11
Mv 172,28,164
Rt 22,20,51200
s_mul_i32 s29, s29, 9
Mv 172,24,160
s_add_u32 s29, s29, s28
s_lshl_b32 s29, s29, 10
Mv 176,32,160
Rt 24,21,32768
s_add_u32 s10, s29, s82
Mv 176,36,164
buffer_load_dwordx4 v3, s[20:23], s55 offen offset:2048 sc1 lds
Mv 208,36,172
Rt 26,21,34816
s_add_u32 s28, s33, 26
Mv 208,32,168
s_lshl3_add_u32 s28, s28, s35
s_mul_i32 s29, s28, 293
Mv 240,32,176
Rt 28,21,49152
s_lshr_b32 s29, s29, 11
Mv 240,36,180
s_mul_i32 s29, s29, 9
s_add_u32 s29, s29, s28
Ma 208,36,188
Rt 30,21,51200
buffer_load_dwordx4 v3, s[20:23], s55 offen offset:3072 sc1 lds
Ma 208,32,184
s_lshl_b32 s29, s29, 10
s_add_u32 s11, s29, s82
Ma 212,40,184
Rt 32,22,32768
Ma 212,44,188
s_cmp_gt_u32 s33, 1
s_cmov_b32 s11, 0x40000000
Mv 244,44,180
Rt 34,22,34816
s_add_u32 m0, s36, 135168
Mv 244,40,176
buffer_load_dwordx4 v3, s[20:23], s56 offen offset:0 sc1 lds
Mv 212,40,168
Rt 36,22,49152
s_and_b32 s28, s34, 1
Mv 212,44,172
s_lshl_b32 s28, s28, 14
s_add_u32 s82, s37, s28
Mv 180,44,164
Rt 38,22,51200
s_add_u32 s82, s82, 0x10000
Mv 180,40,160
Mv 184,48,160
Rt 40,23,32768
buffer_load_dwordx4 v3, s[20:23], s56 offen offset:1024 sc1 lds
Mv 184,52,164
Mv 216,52,172
Rt 42,23,34816
Mv 216,48,168
Mv 248,48,176
Rt 44,23,49152
.LzH5:
s_cbranch_execz .LzH19
Mv 248,52,180
buffer_load_dwordx4 v3, s[20:23], s56 offen offset:2048 sc1 lds
)" R"(
Ma 216,52,188
Rt 46,23,51200
Ma 216,48,184
Ma 220,56,184
Rt 48,24,32768
Ma 220,60,188
Mv 252,60,180
Rt 50,24,34816
buffer_load_dwordx4 v3, s[20:23], s56 offen offset:3072 sc1 lds
Mv 252,56,176
Mv 220,56,168
Rt 52,24,49152
Mv 220,60,172
s_add_u32 s55, s55, 0x10000
s_add_u32 s56, s56, 0x10000
Mv 188,60,164
Rt 54,24,51200
Mv 188,56,160
Mv 32,0,128
Rt 56,25,32768
Mv 32,4,132
Rt 58,25,34816
Mv 64,4,140
Rt 60,25,49152
s_add_u32 m0, s36, 65536
Mv 64,0,136
Rt 62,25,51200
buffer_load_dwordx4 v2, s[20:23], s56 offen offset:0 sc1 lds
Mv 68,8,136
Rb 160,16,49152
Mv 68,12,140
Rb 164,17,49152
s_add_u32 s50, s50, 1024
Mv 36,12,132
Rb 168,16,51200
s_min_u32 s50, s50, s60
Mv 36,8,128
Rb 172,17,51200
s_waitcnt lgkmcnt(12)
Mv 40,16,128
Rb 176,16,57344
Mv 40,20,132
Rb 180,17,57344
buffer_load_dwordx4 v2, s[20:23], s56 offen offset:1024 sc1 lds
Mv 72,20,140
Rb 184,16,59392
Mv 72,16,136
Rb 188,17,59392
Mv 76,24,136
Cv 32,32,33
Cv 33,34,35
Mv 76,28,140
Cv 34,36,37
Cv 35,38,39
Mv 44,28,132
Cv 64,64,65
Cv 65,66,67
Mv 44,24,128
buffer_load_dwordx4 v2, s[20:23], s56 offen offset:2048 sc1 lds
Mv 48,32,128
Cv 66,68,69
Cv 67,70,71
Mv 48,36,132
Cv 72,72,73
)" R"(
Cv 73,74,75
Mv 80,36,140
Cv 74,76,77
Cv 75,78,79
Mv 80,32,136
Cv 40,40,41
Cv 41,42,43
Mv 84,40,136
Cv 42,44,45
Cv 43,46,47
Mv 84,44,140
buffer_load_dwordx4 v2, s[20:23], s56 offen offset:3072 sc1 lds
Mv 52,44,132
Mv 52,40,128
s_waitcnt lgkmcnt(0)
s_barrier
Mv 56,48,128
v_add_u32 v8, 0x10000, v1
v_add_u32 v10, 0x20000, v1
Mv 56,52,132
buffer_load_dword v4, v5, s[20:23], s50 offen offset:0 sc0 sc1
Mv 88,52,140
s_add_u32 m0, s36, 32768
s_mov_b32 s93, 2
Mv 88,48,136
buffer_load_dwordx4 v2, s[20:23], s55 offen offset:0 sc1 lds
Mv 92,56,136
s_mov_b32 s94, 0
Cv 48,48,49
Mv 92,60,140
Cv 49,50,51
Cv 50,52,53
Mv 60,60,132
Cv 51,54,55
Cv 80,80,81
Mv 60,56,128
Cv 81,82,83
Cv 82,84,85
Mv 96,0,144
Cv 83,86,87
Mv 96,4,148
Cv 88,88,89
Cv 89,90,91
Mv 128,4,156
Cv 90,92,93
Cv 91,94,95
Mv 128,0,152
buffer_load_dwordx4 v2, s[20:23], s55 offen offset:1024 sc1 lds
Mv 132,8,152
Cv 56,56,57
Cv 57,58,59
Mv 132,12,156
Cv 58,60,61
Cv 59,62,63
Mv 100,12,148
Mv 100,8,144
buffer_store_dwordx4 v[32:35], v2, s[20:23], s45 offen offset:0 sc0 sc1
Mv 104,16,144
Mv 104,20,148
Cv 128,128,129
Cv 129,130,131
Mv 136,20,156
Cv 130,132,133
Cv 131,134,135
Mv 136,16,152
)" R"(
buffer_load_dwordx4 v2, s[20:23], s55 offen offset:2048 sc1 lds
Mv 140,24,152
Cv 132,96,97
Cv 133,98,99
Mv 140,28,156
Cv 134,100,101
Cv 135,102,103
Mv 108,28,148
Mv 108,24,144
buffer_store_dwordx4 v[40:43], v2, s[20:23], s45 offen offset:1024 sc0 sc1
Mv 112,32,144
Mv 112,36,148
Cv 136,136,137
Cv 137,138,139
Mv 144,36,156
Cv 138,140,141
Cv 139,142,143
Mv 144,32,152
buffer_load_dwordx4 v2, s[20:23], s55 offen offset:3072 sc1 lds
Mv 148,40,152
Cv 140,104,105
Cv 141,106,107
Mv 148,44,156
Cv 142,108,109
Cv 143,110,111
Mv 116,44,148
Mv 116,40,144
buffer_store_dwordx4 v[48:51], v2, s[20:23], s45 offen offset:2048 sc0 sc1
Mv 120,48,144
Mv 120,52,148
Cv 144,144,145
Cv 145,146,147
Mv 152,52,156
s_add_u32 m0, s36, 36864
Cv 146,148,149
Mv 152,48,152
buffer_load_dwordx4 v3, s[20:23], s55 offen offset:0 sc1 lds
Mv 156,56,152
Cv 147,150,151
Cv 148,112,113
Mv 156,60,156
Cv 149,114,115
Cv 150,116,117
Mv 124,60,148
Cv 151,118,119
Mv 124,56,144
buffer_store_dwordx4 v[56:59], v2, s[20:23], s45 offen offset:3072 sc0 sc1
Mv 160,0,160
Mv 160,4,164
Cv 152,152,153
Cv 153,154,155
Mv 192,4,172
Cv 154,156,157
Cv 155,158,159
Mv 192,0,168
buffer_load_dwordx4 v3, s[20:23], s55 offen offset:1024 sc1 lds
Mv 224,0,176
Cv 156,120,121
Cv 157,122,123
Mv 224,4,180
Cv 158,124,125
Cv 159,126,127
Ma 192,4,188
Ma 192,0,184
)" R"(
buffer_store_dwordx4 v[64:67], v3, s[20:23], s45 offen offset:0 sc0 sc1
Ma 196,8,184
Ma 196,12,188
Mv 228,12,180
Mv 228,8,176
buffer_load_dwordx4 v3, s[20:23], s55 offen offset:2048 sc1 lds
Mv 196,8,168
Mv 196,12,172
Mv 164,12,164
Mv 164,8,160
buffer_store_dwordx4 v[72:75], v3, s[20:23], s45 offen offset:1024 sc0 sc1
Mv 168,16,160
Mv 168,20,164
Mv 200,20,172
Mv 200,16,168
buffer_load_dwordx4 v3, s[20:23], s55 offen offset:3072 sc1 lds
Mv 232,16,176
Cv 160,160,161
Cv 161,162,163
Mv 232,20,180
Cv 162,164,165
Cv 163,166,167
Ma 200,20,188
v_accvgpr_write_b32 a224, v160
v_accvgpr_write_b32 a225, v161
Ma 200,16,184
buffer_store_dwordx4 v[80:83], v3, s[20:23], s45 offen offset:2048 sc0 sc1
Ma 204,24,184
v_accvgpr_write_b32 a226, v162
v_accvgpr_write_b32 a227, v163
Ma 204,28,188
Mv 236,28,180
s_add_u32 m0, s36, 69632
Mv 236,24,176
buffer_load_dwordx4 v3, s[20:23], s56 offen offset:0 sc1 lds
Mv 204,24,168
Mv 204,28,172
Mv 172,28,164
Mv 172,24,160
buffer_store_dwordx4 v[88:91], v3, s[20:23], s45 offen offset:3072 sc0 sc1
Mv 176,32,160
Mv 176,36,164
Mv 208,36,172
Mv 208,32,168
buffer_load_dwordx4 v3, s[20:23], s56 offen offset:1024 sc1 lds
Mv 240,32,176
Cv 168,168,169
Cv 169,170,171
Mv 240,36,180
Cv 170,172,173
Cv 171,174,175
Ma 208,36,188
v_accvgpr_write_b32 a228, v168
v_accvgpr_write_b32 a229, v169
Ma 208,32,184
s_waitcnt vmcnt(18)
s_barrier
Ma 212,40,184
v_cmp_eq_u32_e32 vcc, s3, v4
s_andn2_b64 s[28:29], -1, vcc
s_cbranch_scc1 .Lzsp15
.Lzok15:
Ma 212,44,188
v_accvgpr_write_b32 a230, v170
v_accvgpr_write_b32 a231, v171
)" R"(
Mv 244,44,180
Mv 244,40,176
buffer_load_dwordx4 v3, s[20:23], s56 offen offset:2048 sc1 lds
Mv 212,40,168
Mv 212,44,172
Mv 180,44,164
Mv 180,40,160
Mv 184,48,160
Rb 0,1,0
Mv 184,52,164
Rb 4,1,1024
Mv 216,52,172
Rb 8,1,2048
Mv 216,48,168
buffer_load_dwordx4 v3, s[20:23], s56 offen offset:3072 sc1 lds
Rb 12,1,3072
Mv 248,48,176
Rb 16,1,8192
Mv 248,52,180
Rb 20,1,9216
Ma 216,52,188
Rb 24,1,10240
Ma 216,48,184
Rb 28,1,11264
Ma 220,56,184
Rb 32,1,4096
Ma 220,60,188
Rb 36,1,5120
Mv 252,60,180
Rb 40,1,6144
Mv 252,56,176
Rb 44,1,7168
Mv 220,56,168
Mv 220,60,172
Mv 188,60,164
Mv 188,56,160
s_add_u32 s56, s56, 0x10000
s_branch .LzFB
.LzH6:
s_cbranch_execz .LzH11
.LzBV:
Mz 32,128,0
Rt 56,25,0
Mv 32,132,4
Rt 58,25,2048
Mz 36,140,4
Rt 60,25,16384
Mv 36,136,0
Rt 62,25,18432
v_mul_f32_e32 v64, s54, v64
Mz 96,144,0
v_mul_f32_e32 v65, s54, v65
Cv 64,64,65
Mv 96,148,4
v_accvgpr_write_b32 a112, v64
v_mul_f32_e32 v66, s54, v66
Mz 100,156,4
v_mul_f32_e32 v67, s54, v67
Cv 66,66,67
Mv 100,152,0
v_accvgpr_write_b32 a113, v66
Mz 108,152,8
v_mul_f32_e32 v68, s54, v68
v_mul_f32_e32 v69, s54, v69
)" R"(
Mv 108,156,12
Cv 68,68,69
v_accvgpr_write_b32 a114, v68
Mz 104,148,12
v_mul_f32_e32 v70, s54, v70
v_mul_f32_e32 v71, s54, v71
Mv 104,144,8
Cv 70,70,71
v_accvgpr_write_b32 a115, v70
Mz 44,136,8
v_mul_f32_e32 v72, s54, v72
v_mul_f32_e32 v73, s54, v73
Mv 44,140,12
Cv 72,72,73
v_accvgpr_write_b32 a116, v72
Mz 40,132,12
s_add_u32 s39, s39, 0x40000
v_mul_f32_e32 v74, s54, v74
Mv 40,128,8
s_waitcnt lgkmcnt(0)
s_barrier
Mz 48,128,16
Rb 160,16,16384
s_add_u32 m0, s37, 0
Mv 48,132,20
buffer_load_dwordx4 v12, s[8:11], s39 offen offset:0 lds
Mz 52,140,20
v_mul_f32_e32 v75, s54, v75
Cv 74,74,75
v_accvgpr_write_b32 a117, v74
Mv 52,136,16
v_mul_f32_e32 v76, s54, v76
v_mul_f32_e32 v77, s54, v77
Mz 112,144,16
Rb 164,17,16384
Cv 76,76,77
Mv 112,148,20
v_accvgpr_write_b32 a118, v76
v_mul_f32_e32 v78, s54, v78
Mz 116,156,20
v_mul_f32_e32 v79, s54, v79
Cv 78,78,79
Mv 116,152,16
buffer_load_dwordx4 v13, s[8:11], s39 offen offset:1024 lds
Mz 124,152,24
Rb 168,16,18432
v_accvgpr_write_b32 a119, v78
Mv 124,156,28
v_mul_f32_e32 v80, s54, v80
v_mul_f32_e32 v81, s54, v81
Mz 120,148,28
Cv 80,80,81
v_accvgpr_write_b32 a120, v80
Mv 120,144,24
v_mul_f32_e32 v82, s54, v82
v_mul_f32_e32 v83, s54, v83
Mz 60,136,24
Rb 172,17,18432
Cv 82,82,83
Mv 60,140,28
buffer_load_dwordx4 v14, s[8:11], s39 offen offset:2048 lds
Mz 56,132,28
v_accvgpr_write_b32 a121, v82
v_mul_f32_e32 v84, s54, v84
Mv 56,128,24
)" R"(
v_mul_f32_e32 v85, s54, v85
Cv 84,84,85
Mz 128,144,32
Rb 176,16,24576
v_accvgpr_write_b32 a122, v84
Mv 128,148,36
v_mul_f32_e32 v86, s54, v86
v_mul_f32_e32 v87, s54, v87
Mz 132,156,36
Cv 86,86,87
v_accvgpr_write_b32 a123, v86
v_mul_f32_e32 v88, s54, v88
Mv 132,152,32
buffer_load_dwordx4 v15, s[8:11], s39 offen offset:3072 lds
Mz 140,152,40
Rb 180,17,24576
v_mul_f32_e32 v89, s54, v89
Mv 140,156,44
Cv 88,88,89
v_accvgpr_write_b32 a124, v88
Mz 136,148,44
v_mul_f32_e32 v90, s54, v90
v_mul_f32_e32 v91, s54, v91
Mv 136,144,40
s_add_u32 s39, s39, 0x40000
Cv 90,90,91
Mz 144,144,48
Rb 184,16,26624
s_add_u32 m0, s37, 16384
Mv 144,148,52
buffer_load_dwordx4 v12, s[8:11], s39 offen offset:0 lds
Mz 148,156,52
v_accvgpr_write_b32 a125, v90
v_mul_f32_e32 v92, s54, v92
Mv 148,152,48
v_mul_f32_e32 v93, s54, v93
Cv 92,92,93
Mz 156,152,56
Rb 188,17,26624
v_accvgpr_write_b32 a126, v92
Mv 156,156,60
v_mul_f32_e32 v94, s54, v94
v_mul_f32_e32 v95, s54, v95
Mz 152,148,60
Cv 94,94,95
v_accvgpr_write_b32 a127, v94
Mv 152,144,56
buffer_load_dwordx4 v13, s[8:11], s39 offen offset:1024 lds
Mz 64,128,32
s_mov_b32 s85, 0x1508800
v_and_b32 v2, 7, v0
Mv 64,132,36
v_lshlrev_b32 v2, 7, v2
Mz 68,140,36
buffer_load_dword v4, v2, s[20:23], s85 offen sc0 sc1
Mv 68,136,32
Mz 72,128,40
Mv 72,132,44
buffer_load_dwordx4 v14, s[8:11], s39 offen offset:2048 lds
Mz 76,140,44
Mv 76,136,40
s_lshl_b32 s29, s91, 22
s_add_u32 s28, s89, s29
Mz 80,128,48
)" R"(
s_bfe_u32 s29, s63, 0x80000
s_lshl_b32 s29, s29, 16
Mv 80,132,52
s_add_u32 s45, s28, s29
s_bfe_u32 s29, s63, 0x80008
Mz 84,140,52
s_lshl_b32 s29, s29, 16
s_add_u32 s46, s28, s29
Mv 84,136,48
buffer_load_dwordx4 v15, s[8:11], s39 offen offset:3072 lds
Mz 88,128,56
s_bfe_u32 s29, s63, 0x80010
s_lshl_b32 s29, s29, 16
s_add_u32 s47, s28, s29
Mv 88,132,60
s_bfe_u32 s29, s63, 0x80018
s_lshl_b32 s29, s29, 16
Mz 92,140,60
s_add_u32 s48, s28, s29
s_add_u32 s50, s89, 0xd40000
Mv 92,136,56
s_waitcnt vmcnt(9) lgkmcnt(0)
s_barrier
Mz 160,160,0
Rb 128,16,32768
s_lshl2_add_u32 s28, s91, s93
Mv 160,164,4
Rb 132,17,32768
s_add_u32 s38, s38, 0xffff0080
Mz 164,172,4
Rb 136,16,34816
s_add_u32 m0, s36, 0
Mv 164,168,0
buffer_load_dwordx4 v8, s[4:7], s38 offen offset:0 lds
Mz 224,176,0
Rb 140,17,34816
s_lshl_b32 s28, s28, 7
s_add_u32 s49, s28, s50
Mv 224,180,4
Rb 144,16,40960
s_add_u32 s49, s49, 4096
Mz 228,188,4
Rb 148,17,40960
s_lshl_b32 s52, s35, 7
Mv 228,184,0
Rb 152,16,43008
buffer_load_dwordx4 v9, s[4:7], s38 offen offset:1024 lds
Mz 236,184,8
Rb 156,17,43008
Rt 0,18,32768
Mv 236,188,12
s_add_u32 s52, s52, s50
s_add_u32 s52, s52, 0xd000
s_lshl_b32 s51, s35, 17
Mz 232,180,12
Rt 2,18,34816
s_add_u32 s51, s51, s89
Mv 232,176,8
buffer_load_dwordx4 v10, s[4:7], s38 offen offset:2048 lds
Mz 172,168,8
Rt 4,18,49152
s_add_u32 s51, s51, 0x800000
Mv 172,172,12
Mz 168,164,12
Rt 6,18,51200
)" R"(
Mv 168,160,8
buffer_load_dwordx4 v11, s[4:7], s38 offen offset:3072 lds
Mz 176,160,16
Rt 8,19,32768
Mv 176,164,20
s_add_u32 s38, s38, 0x10000
Mz 180,172,20
Rt 10,19,34816
s_add_u32 m0, s36, 4096
Mv 180,168,16
buffer_load_dwordx4 v8, s[4:7], s38 offen offset:0 lds
Mz 240,176,16
Rt 12,19,49152
Mv 240,180,20
Mz 244,188,20
Rt 14,19,51200
Mv 244,184,16
buffer_load_dwordx4 v9, s[4:7], s38 offen offset:1024 lds
Mz 252,184,24
Rt 16,20,32768
Mv 252,188,28
s_cmp_lg_u32 s33, 0
s_cmov_b32 s49, 0x40000000
s_cmov_b32 s52, 0x40000000
Mz 248,180,28
Rt 18,20,34816
s_mov_b32 s53, 0x40000
Mv 248,176,24
buffer_load_dwordx4 v10, s[4:7], s38 offen offset:2048 lds
Mz 188,168,24
Rt 20,20,49152
s_mov_b32 s62, 0x4e800000
.LzH7:
s_cbranch_execz .LzH12
Mv 188,172,28
s_add_u32 s67, s89, 0xc00000
s_lshl3_add_u32 s28, s34, s35
Mz 184,164,28
Rt 22,20,51200
s_add_u32 s64, s51, 0x100000
s_lshl2_add_u32 s28, s28, s33
Mv 184,160,24
buffer_load_dwordx4 v11, s[4:7], s38 offen offset:3072 lds
Mz 192,160,32
Rt 24,21,32768
s_lshl2_add_u32 s65, s35, s33
Mv 192,164,36
s_lshl_b32 s65, s65, 7
s_add_u32 s65, s65, s50
Mz 196,172,36
Rt 26,21,34816
s_add_u32 s65, s65, 0xc000
Mv 196,168,32
s_lshl_b32 s66, s28, 10
s_add_u32 s66, s66, 0x1400000
v_mfma_f32_16x16x32_bf16 a[192:195], a[176:179], a[32:35], 0
Rt 28,21,49152
s_sub_u32 s28, 15, s35
Ma 192,180,36
s_lshl2_add_u32 s28, s28, s33
s_and_b32 s29, s34, 1
v_mfma_f32_16x16x32_bf16 a[196:199], a[188:191], a[36:39], 0
Rt 30,21,51200
s_lshl1_add_u32 s29, s28, s29
)" R"(
s_lshl_b32 s73, s29, 15
Ma 196,184,32
s_and_b32 s29, s89, 0xfe000000
s_add_u32 s73, s73, s29
v_mfma_f32_16x16x32_bf16 a[204:207], a[184:187], a[40:43], 0
Rt 32,22,32768
s_add_u32 s73, s73, 0x1002000
Ma 204,188,44
s_lshl4_add_u32 s28, s28, s34
s_lshl_b32 s68, s28, 8
v_mfma_f32_16x16x32_bf16 a[200:203], a[180:183], a[44:47], 0
Rt 34,22,34816
s_add_u32 s68, s68, 0x1510000
Ma 200,176,40
s_xor_b32 s72, s73, 0x3c6000
s_mov_b32 s55, s89
Mz 204,168,40
Rt 36,22,49152
s_mov_b32 s57, 18
Mv 204,172,44
s_mov_b32 s59, s67
s_mov_b32 s58, 0
Mz 200,164,44
Rt 38,22,51200
Mv 200,160,40
s_cmp_eq_u32 s35, 7
s_cmov_b32 s58, 2
Mz 208,160,48
Rt 40,23,32768
Mv 208,164,52
s_cmp_eq_u32 s35, 7
s_cmov_b32 s64, 0x40000000
Mz 212,172,52
Rt 42,23,34816
Mv 212,168,48
s_cmp_eq_u32 s35, 7
s_cmov_b32 s66, 0x40000000
s_add_u32 s56, s55, 0x400000
v_mfma_f32_16x16x32_bf16 a[208:211], a[176:179], a[48:51], 0
Rt 44,23,49152
Ma 208,180,52
v_mfma_f32_16x16x32_bf16 a[212:215], a[188:191], a[52:55], 0
Rt 46,23,51200
Ma 212,184,48
v_mfma_f32_16x16x32_bf16 a[220:223], a[184:187], a[56:59], 0
Rt 48,24,32768
Ma 220,188,60
v_mfma_f32_16x16x32_bf16 a[216:219], a[180:183], a[60:63], 0
Rt 50,24,34816
Ma 216,176,56
Mz 220,168,56
Rt 52,24,49152
Mv 220,172,60
Mz 216,164,60
Rt 54,24,51200
Mv 216,160,56
Mv 32,128,0
Rt 56,25,32768
Mv 32,132,4
Rt 58,25,34816
Mv 36,140,4
Rt 60,25,49152
Mv 36,136,0
Rt 62,25,51200
)" R"(
Mv 96,144,0
Mv 96,148,4
Mv 100,156,4
Mv 100,152,0
Mv 108,152,8
Mv 108,156,12
Mv 104,148,12
Mv 104,144,8
Mv 44,136,8
Mv 44,140,12
Mv 40,132,12
s_add_u32 s39, s39, 0x40000
Mv 40,128,8
s_waitcnt vmcnt(10) lgkmcnt(0)
s_barrier
Mv 48,128,16
Rb 160,16,49152
s_add_u32 m0, s37, 32768
v_xor_b32 v4, s3, v4
v_cmp_gt_u32_e32 vcc, 2, v4
s_andn2_b32 s28, 0xff, vcc_lo
s_cbranch_scc1 .Lzsp16
.Lzok16:
Mv 48,132,20
buffer_load_dwordx4 v12, s[8:11], s39 offen offset:0 lds
Mv 52,140,20
v_xor_b32 v4, s3, v4
v_and_b32 v4, 1, v4
Mv 52,136,16
v_cmp_eq_u32_e32 vcc, 1, v4
s_mov_b32 s85, vcc_lo
Mv 112,144,16
Rb 164,17,49152
s_and_b32 s28, s2, 7
Mv 112,148,20
s_lshr_b32 s28, s85, s28
s_xor_b32 s28, s28, s34
Mv 116,156,20
s_and_b32 s28, s28, 1
s_lshl_b32 s84, s28, 13
Mv 116,152,16
buffer_load_dwordx4 v13, s[8:11], s39 offen offset:1024 lds
Mv 124,152,24
Rb 168,16,51200
s_add_u32 s49, s49, s84
Mv 124,156,28
s_add_u32 s50, s50, s84
s_add_u32 s60, s50, 7168
Mv 120,148,28
s_add_u32 s52, s52, s84
s_add_u32 s65, s65, s84
Mv 120,144,24
s_lshr_b32 s28, s2, 6
s_lshl_b32 s28, s28, 14
Mv 60,136,24
Rb 172,17,51200
s_add_u32 s28, s28, s50
Mv 60,140,28
buffer_load_dwordx4 v14, s[8:11], s39 offen offset:2048 lds
Mv 56,132,28
s_bfe_u32 s29, s2, 0x20004
s_lshl2_add_u32 s29, s29, s33
Mv 56,128,24
s_lshl_b32 s29, s29, 9
s_add_u32 s28, s28, s29
)" R"(
Mv 64,128,32
Rb 176,16,57344
v_min_u32 v4, 3, v0
Mv 64,132,36
v_lshlrev_b32 v4, 7, v4
Mv 68,140,36
buffer_load_dword v4, v4, s[20:23], s28 offen sc0 sc1
Mv 68,136,32
buffer_load_dwordx4 v15, s[8:11], s39 offen offset:3072 lds
Mv 128,144,32
Rb 180,17,57344
s_lshl1_add_u32 s28, s36, s84
Mv 128,148,36
v_add_u32 v2, s28, v1
v_add_u32 v3, 0x1000, v2
Mv 132,156,36
s_lshl_b32 s28, s34, 13
s_xor_b32 s28, s28, s84
Mv 132,152,32
s_add_u32 s39, s39, 0x40000
s_and_b32 s28, s28, 0x2000
Mv 140,152,40
Rb 184,16,59392
s_add_u32 m0, s37, 49152
Mv 140,156,44
buffer_load_dwordx4 v12, s[8:11], s39 offen offset:0 lds
Mv 136,148,44
s_xor_b32 s73, s73, s28
s_xor_b32 s72, s72, s28
Mv 136,144,40
Mv 76,136,40
Rb 188,17,59392
Mv 76,140,44
Mv 72,132,44
Mv 72,128,40
buffer_load_dwordx4 v13, s[8:11], s39 offen offset:1024 lds
Mv 80,128,48
Mv 80,132,52
Mv 84,140,52
Mv 84,136,48
Mv 144,144,48
Mv 144,148,52
buffer_load_dwordx4 v14, s[8:11], s39 offen offset:2048 lds
Mv 148,156,52
Mv 148,152,48
Mv 156,152,56
Mv 156,156,60
Mv 152,148,60
Mv 152,144,56
buffer_load_dwordx4 v15, s[8:11], s39 offen offset:3072 lds
Mv 92,136,56
Mv 92,140,60
Mv 88,132,60
Mv 88,128,56
s_waitcnt vmcnt(9) lgkmcnt(0)
s_barrier
Mv 160,160,0
Rb 128,16,0
s_mov_b64 s[16:17], s[70:71]
Mv 160,164,4
Rb 132,17,0
s_add_u32 s38, s38, 0xffff0080
Mv 164,172,4
Rb 136,16,2048
s_add_u32 m0, s36, 32768
)" R"(
Mv 164,168,0
buffer_load_dwordx4 v8, s[4:7], s38 offen offset:0 lds
Mv 224,176,0
Rb 140,17,2048
s_mov_b32 s18, 0x188000
Mv 224,180,4
Rb 144,16,8192
Mv 228,188,4
Rb 148,17,8192
Mv 228,184,0
Rb 152,16,10240
buffer_load_dwordx4 v9, s[4:7], s38 offen offset:1024 lds
Mv 236,184,8
Rb 156,17,10240
Rt 0,18,0
Mv 236,188,12
s_cmp_eq_u32 s33, 0
s_cmov_b32 s18, 0x380000
Mv 232,180,12
Rt 2,18,2048
s_mul_i32 s43, s2, 3072
Mv 232,176,8
buffer_load_dwordx4 v10, s[4:7], s38 offen offset:2048 lds
Mv 172,168,8
Rt 4,18,16384
Mv 172,172,12
s_cmp_eq_u32 s33, 0
s_cselect_b32 s44, 3, 6
Mv 168,164,12
Rt 6,18,18432
s_mul_i32 s44, s44, 0x60000
Mv 168,160,8
buffer_load_dwordx4 v11, s[4:7], s38 offen offset:3072 lds
Mv 176,160,16
Rt 8,19,0
s_sub_u32 s43, s43, s44
Mv 176,164,20
s_add_u32 s38, s38, 0x10000
v_min_u32 v26, 23, v0
Mv 180,172,20
Rt 10,19,2048
s_add_u32 m0, s36, 36864
Mv 180,168,16
buffer_load_dwordx4 v8, s[4:7], s38 offen offset:0 lds
Mv 240,176,16
Rt 12,19,16384
v_lshlrev_b32 v26, 7, v26
Mv 240,180,20
v_and_b32 v5, 7, v0
v_lshlrev_b32 v5, 7, v5
Mv 244,188,20
Rt 14,19,18432
Mv 244,184,16
buffer_load_dwordx4 v9, s[4:7], s38 offen offset:1024 lds
Mv 252,184,24
Rt 16,20,0
Mv 252,188,28
Mv 248,180,28
Rt 18,20,2048
Mv 248,176,24
buffer_load_dwordx4 v10, s[4:7], s38 offen offset:2048 lds
Mv 188,168,24
Rt 20,20,16384
Mv 188,172,28
)" R"(
Mv 184,164,28
Rt 22,20,18432
v_accvgpr_write_b32 a224, -1
Mv 184,160,24
buffer_load_dwordx4 v11, s[4:7], s38 offen offset:3072 lds
Mv 192,160,32
Rt 24,21,0
v_accvgpr_write_b32 a225, -1
Mv 192,164,36
v_accvgpr_write_b32 a226, -1
v_accvgpr_write_b32 a227, -1
Mv 196,172,36
Rt 26,21,2048
s_add_u32 s88, s73, 0x4000
Mv 196,168,32
buffer_store_dwordx4 a[64:67], v2, s[20:23], s51 offen offset:0 sc0 sc1
Ma 192,176,32
Rt 28,21,16384
s_mov_b32 s40, 5
Ma 192,180,36
s_mov_b32 s94, 0
Ma 196,188,36
Rt 30,21,18432
Ma 196,184,32
Ma 204,184,40
Rt 32,22,0
Ma 204,188,44
Ma 200,180,44
Rt 34,22,2048
Ma 200,176,40
buffer_store_dwordx4 a[68:71], v2, s[20:23], s51 offen offset:1024 sc0 sc1
Mv 204,168,40
Rt 36,22,16384
Mv 204,172,44
Mv 200,164,44
Rt 38,22,18432
Mv 200,160,40
Mv 208,160,48
Rt 40,23,0
Mv 208,164,52
Mv 212,172,52
Rt 42,23,2048
Mv 212,168,48
buffer_store_dwordx4 a[72:75], v2, s[20:23], s51 offen offset:2048 sc0 sc1
Ma 208,176,48
Rt 44,23,16384
Ma 208,180,52
Ma 212,188,52
Rt 46,23,18432
Ma 212,184,48
Ma 220,184,56
Rt 48,24,0
Ma 220,188,60
Ma 216,180,60
Rt 50,24,2048
Ma 216,176,56
Mv 220,168,56
Rt 52,24,16384
Mv 220,172,60
Mv 216,164,60
Rt 54,24,18432
Mv 216,160,56
Mv 32,128,0
Rt 56,25,0
)" R"(
Mv 32,132,4
Rt 58,25,2048
Mv 36,140,4
Rt 60,25,16384
Mv 36,136,0
Rt 62,25,18432
Mv 96,144,0
Mv 96,148,4
buffer_store_dwordx4 a[76:79], v2, s[20:23], s51 offen offset:3072 sc0 sc1
Mv 100,156,4
Mv 100,152,0
Mv 108,152,8
Mv 108,156,12
Mv 104,148,12
Mv 104,144,8
buffer_store_dwordx4 a[80:83], v3, s[20:23], s51 offen offset:0 sc0 sc1
Mv 44,136,8
Mv 44,140,12
Mv 40,132,12
s_add_u32 s39, s39, 0x40000
Mv 40,128,8
s_waitcnt lgkmcnt(0)
s_barrier
Mv 48,128,16
Rb 160,16,16384
s_add_u32 m0, s37, 0
Mv 48,132,20
buffer_load_dwordx4 v12, s[8:11], s39 offen offset:0 lds
Mv 52,140,20
Mv 52,136,16
Mv 112,144,16
Rb 164,17,16384
Mv 112,148,20
Mv 116,156,20
Mv 116,152,16
buffer_load_dwordx4 v13, s[8:11], s39 offen offset:1024 lds
Mv 124,152,24
Rb 168,16,18432
Mv 124,156,28
Mv 120,148,28
Mv 120,144,24
Mv 60,136,24
Rb 172,17,18432
Mv 60,140,28
buffer_load_dwordx4 v14, s[8:11], s39 offen offset:2048 lds
Mv 56,132,28
Mv 56,128,24
Mv 64,128,32
Rb 176,16,24576
Mv 64,132,36
Mv 68,140,36
Mv 68,136,32
buffer_load_dwordx4 v15, s[8:11], s39 offen offset:3072 lds
Mv 128,144,32
Rb 180,17,24576
Mv 128,148,36
Mv 132,156,36
buffer_store_dwordx4 a[84:87], v3, s[20:23], s51 offen offset:1024 sc0 sc1
Mv 132,152,32
s_add_u32 s39, s39, 0x40000
Mv 140,152,40
Rb 184,16,26624
s_add_u32 m0, s37, 16384
Mv 140,156,44
buffer_load_dwordx4 v12, s[8:11], s39 offen offset:0 lds
)" R"(
Mv 136,148,44
Mv 136,144,40
Mv 76,136,40
Rb 188,17,26624
Mv 76,140,44
Mv 72,132,44
Mv 72,128,40
buffer_load_dwordx4 v13, s[8:11], s39 offen offset:1024 lds
Mv 80,128,48
Mv 80,132,52
Mv 84,140,52
Mv 84,136,48
Mv 144,144,48
Mv 144,148,52
buffer_load_dwordx4 v14, s[8:11], s39 offen offset:2048 lds
Mv 148,156,52
Mv 148,152,48
Mv 156,152,56
Mv 156,156,60
Mv 152,148,60
Mv 152,144,56
buffer_load_dwordx4 v15, s[8:11], s39 offen offset:3072 lds
Mv 92,136,56
Mv 92,140,60
Mv 88,132,60
Mv 88,128,56
s_waitcnt vmcnt(14) lgkmcnt(0)
s_barrier
Mv 160,160,0
Rb 128,16,32768
Mv 160,164,4
Rb 132,17,32768
s_add_u32 s38, s38, 0xffff0080
Mv 164,172,4
Rb 136,16,34816
s_add_u32 m0, s36, 0
Mv 164,168,0
buffer_load_dwordx4 v8, s[4:7], s38 offen offset:0 lds
Mv 224,176,0
Rb 140,17,34816
Mv 224,180,4
Rb 144,16,40960
Mv 228,188,4
Rb 148,17,40960
Mv 228,184,0
Rb 152,16,43008
buffer_load_dwordx4 v9, s[4:7], s38 offen offset:1024 lds
Mv 236,184,8
Rb 156,17,43008
Rt 0,18,32768
Mv 236,188,12
Mv 232,180,12
Rt 2,18,34816
Mv 232,176,8
buffer_load_dwordx4 v10, s[4:7], s38 offen offset:2048 lds
Mv 172,168,8
Rt 4,18,49152
Mv 172,172,12
Mv 168,164,12
Rt 6,18,51200
Mv 168,160,8
buffer_load_dwordx4 v11, s[4:7], s38 offen offset:3072 lds
Mv 176,160,16
Rt 8,19,32768
Mv 176,164,20
)" R"(
s_add_u32 s38, s38, 0x10000
Mv 180,172,20
Rt 10,19,34816
s_add_u32 m0, s36, 4096
Mv 180,168,16
buffer_load_dwordx4 v8, s[4:7], s38 offen offset:0 lds
Mv 240,176,16
Rt 12,19,49152
Mv 240,180,20
Mv 244,188,20
Rt 14,19,51200
Mv 244,184,16
buffer_load_dwordx4 v9, s[4:7], s38 offen offset:1024 lds
Mv 252,184,24
Rt 16,20,32768
Mv 252,188,28
Mv 248,180,28
Rt 18,20,34816
Mv 248,176,24
buffer_load_dwordx4 v10, s[4:7], s38 offen offset:2048 lds
Mv 188,168,24
Rt 20,20,49152
Mv 188,172,28
Mv 184,164,28
Rt 22,20,51200
Mv 184,160,24
buffer_load_dwordx4 v11, s[4:7], s38 offen offset:3072 lds
Mv 192,160,32
Rt 24,21,32768
Mv 192,164,36
Mv 196,172,36
Rt 26,21,34816
Mv 196,168,32
buffer_store_dwordx4 a[88:91], v3, s[20:23], s51 offen offset:2048 sc0 sc1
Ma 192,176,32
Rt 28,21,49152
Ma 192,180,36
Ma 196,188,36
Rt 30,21,51200
Ma 196,184,32
Ma 204,184,40
Rt 32,22,32768
Ma 204,188,44
buffer_store_dwordx4 a[92:95], v3, s[20:23], s51 offen offset:3072 sc0 sc1
Ma 200,180,44
Rt 34,22,34816
Ma 200,176,40
Mv 204,168,40
Rt 36,22,49152
Mv 204,172,44
Mv 200,164,44
Rt 38,22,51200
Mv 200,160,40
s_add_u32 s51, s51, 0x10000
buffer_store_dwordx4 a[96:99], v2, s[20:23], s51 offen offset:0 sc0 sc1
Mv 208,160,48
Rt 40,23,32768
Mv 208,164,52
Mv 212,172,52
Rt 42,23,34816
Mv 212,168,48
Ma 208,176,48
Rt 44,23,49152
Ma 208,180,52
)" R"(
buffer_store_dwordx4 a[100:103], v2, s[20:23], s51 offen offset:1024 sc0 sc1
Ma 212,188,52
Rt 46,23,51200
Ma 212,184,48
.LzH8:
s_cbranch_execz .LzH13
Ma 220,184,56
Rt 48,24,32768
Ma 220,188,60
s_add_u32 s43, s43, 0x60000
Ma 216,180,60
Rt 50,24,34816
Ma 216,176,56
buffer_load_dword v27, v26, s[16:19], s43 offen
Mv 220,168,56
Rt 52,24,49152
Mv 220,172,60
Mv 216,164,60
Rt 54,24,51200
Mv 216,160,56
Mv 32,128,0
Rt 56,25,32768
Mv 32,132,4
Rt 58,25,34816
Mv 36,140,4
Rt 60,25,49152
Mv 36,136,0
Rt 62,25,51200
Mv 96,144,0
Mv 96,148,4
buffer_store_dwordx4 a[104:107], v2, s[20:23], s51 offen offset:2048 sc0 sc1
Mv 100,156,4
Mv 100,152,0
Mv 108,152,8
Mv 108,156,12
Mv 104,148,12
Mv 104,144,8
buffer_store_dwordx4 a[108:111], v2, s[20:23], s51 offen offset:3072 sc0 sc1
Mv 44,136,8
Mv 44,140,12
Mv 40,132,12
s_add_u32 s39, s39, 0x40000
Mv 40,128,8
s_waitcnt lgkmcnt(0)
s_barrier
Mv 48,128,16
Rb 160,16,49152
s_add_u32 m0, s37, 32768
Mv 48,132,20
buffer_load_dwordx4 v12, s[8:11], s39 offen offset:0 lds
Mv 52,140,20
Mv 52,136,16
Mv 112,144,16
Rb 164,17,49152
Mv 112,148,20
Mv 116,156,20
Mv 116,152,16
buffer_load_dwordx4 v13, s[8:11], s39 offen offset:1024 lds
Mv 124,152,24
Rb 168,16,51200
Mv 124,156,28
Mv 120,148,28
Mv 120,144,24
Mv 60,136,24
Rb 172,17,51200
)" R"(
Mv 60,140,28
buffer_load_dwordx4 v14, s[8:11], s39 offen offset:2048 lds
Mv 56,132,28
Mv 56,128,24
Mv 64,128,32
Rb 176,16,57344
Mv 64,132,36
Mv 68,140,36
Mv 68,136,32
buffer_load_dwordx4 v15, s[8:11], s39 offen offset:3072 lds
Mv 128,144,32
Rb 180,17,57344
Mv 128,148,36
Mv 132,156,36
buffer_store_dwordx4 a[112:115], v3, s[20:23], s51 offen offset:0 sc0 sc1
Mv 132,152,32
s_add_u32 s39, s39, 0x40000
Mv 140,152,40
Rb 184,16,59392
s_add_u32 m0, s37, 49152
Mv 140,156,44
buffer_load_dwordx4 v12, s[8:11], s39 offen offset:0 lds
Mv 136,148,44
Mv 136,144,40
Mv 76,136,40
Rb 188,17,59392
Mv 76,140,44
Mv 72,132,44
Mv 72,128,40
buffer_load_dwordx4 v13, s[8:11], s39 offen offset:1024 lds
Mv 80,128,48
Mv 80,132,52
Mv 84,140,52
Mv 84,136,48
Mv 144,144,48
Mv 144,148,52
buffer_load_dwordx4 v14, s[8:11], s39 offen offset:2048 lds
Mv 148,156,52
Mv 148,152,48
Mv 156,152,56
Mv 156,156,60
Mv 152,148,60
Mv 152,144,56
buffer_load_dwordx4 v15, s[8:11], s39 offen offset:3072 lds
Mv 92,136,56
Mv 92,140,60
Mv 88,132,60
Mv 88,128,56
s_waitcnt vmcnt(16) lgkmcnt(0)
s_barrier
Mv 160,160,0
Rb 128,16,0
Mv 160,164,4
Rb 132,17,0
s_add_u32 s38, s38, 0xffff0080
Mv 164,172,4
Rb 136,16,2048
s_add_u32 m0, s36, 32768
Mv 164,168,0
buffer_load_dwordx4 v8, s[4:7], s38 offen offset:0 lds
Mv 224,176,0
Rb 140,17,2048
Mv 224,180,4
Rb 144,16,8192
Mv 228,188,4
)" R"(
Rb 148,17,8192
Mv 228,184,0
Rb 152,16,10240
buffer_load_dwordx4 v9, s[4:7], s38 offen offset:1024 lds
Mv 236,184,8
Rb 156,17,10240
Rt 0,18,0
Mv 236,188,12
Mv 232,180,12
Rt 2,18,2048
Mv 232,176,8
buffer_load_dwordx4 v10, s[4:7], s38 offen offset:2048 lds
Mv 172,168,8
Rt 4,18,16384
Mv 172,172,12
Mv 168,164,12
Rt 6,18,18432
Mv 168,160,8
buffer_load_dwordx4 v11, s[4:7], s38 offen offset:3072 lds
Mv 176,160,16
Rt 8,19,0
Mv 176,164,20
s_add_u32 s38, s38, 0x10000
Mv 180,172,20
Rt 10,19,2048
s_add_u32 m0, s36, 36864
Mv 180,168,16
buffer_load_dwordx4 v8, s[4:7], s38 offen offset:0 lds
Mv 240,176,16
Rt 12,19,16384
Mv 240,180,20
Mv 244,188,20
Rt 14,19,18432
Mv 244,184,16
buffer_load_dwordx4 v9, s[4:7], s38 offen offset:1024 lds
Mv 252,184,24
Rt 16,20,0
Mv 252,188,28
Mv 248,180,28
Rt 18,20,2048
Mv 248,176,24
buffer_load_dwordx4 v10, s[4:7], s38 offen offset:2048 lds
Mv 188,168,24
Rt 20,20,16384
Mv 188,172,28
Mv 184,164,28
Rt 22,20,18432
Mv 184,160,24
buffer_load_dwordx4 v11, s[4:7], s38 offen offset:3072 lds
Mv 192,160,32
Rt 24,21,0
Mv 192,164,36
Mv 196,172,36
Rt 26,21,2048
Mv 196,168,32
buffer_store_dwordx4 a[116:119], v3, s[20:23], s51 offen offset:1024 sc0 sc1
Ma 192,176,32
Rt 28,21,16384
Ma 192,180,36
Ma 196,188,36
Rt 30,21,18432
Ma 196,184,32
Ma 204,184,40
Rt 32,22,0
)" R"(
Ma 204,188,44
buffer_store_dwordx4 a[120:123], v3, s[20:23], s51 offen offset:2048 sc0 sc1
Ma 200,180,44
Rt 34,22,2048
Ma 200,176,40
Mv 204,168,40
Rt 36,22,16384
Mv 204,172,44
Mv 200,164,44
Rt 38,22,18432
Mv 200,160,40
buffer_store_dwordx4 a[124:127], v3, s[20:23], s51 offen offset:3072 sc0 sc1
Mv 208,160,48
Rt 40,23,0
Mv 208,164,52
Mv 212,172,52
Rt 42,23,2048
Mv 212,168,48
Ma 208,176,48
Rt 44,23,16384
Ma 208,180,52
Ma 212,188,52
Rt 46,23,18432
Ma 212,184,48
Ma 220,184,56
Rt 48,24,0
Ma 220,188,60
s_add_u32 s43, s43, 0x60000
Ma 216,180,60
Rt 50,24,2048
Ma 216,176,56
buffer_load_dword v27, v26, s[16:19], s43 offen
Mv 220,168,56
Rt 52,24,16384
Mv 220,172,60
Mv 216,164,60
Rt 54,24,18432
Mv 216,160,56
.LzPBV:
Mv 32,128,0
Rt 56,25,0
Mv 32,132,4
Rt 58,25,2048
Mv 36,140,4
Rt 60,25,16384
Mv 36,136,0
Rt 62,25,18432
Mv 96,144,0
Mv 96,148,4
Mv 100,156,4
Mv 100,152,0
Mv 108,152,8
Mv 108,156,12
Mv 104,148,12
Mv 104,144,8
Mv 44,136,8
Mv 44,140,12
Mv 40,132,12
s_add_u32 s39, s39, 0x40000
Mv 40,128,8
s_waitcnt lgkmcnt(0)
s_barrier
Mv 48,128,16
Rb 160,16,16384
s_add_u32 m0, s37, 0
)" R"(
Mv 48,132,20
buffer_load_dwordx4 v12, s[8:11], s39 offen offset:0 lds
Mv 52,140,20
Mv 52,136,16
Mv 112,144,16
Rb 164,17,16384
Mv 112,148,20
Mv 116,156,20
Mv 116,152,16
buffer_load_dwordx4 v13, s[8:11], s39 offen offset:1024 lds
Mv 124,152,24
Rb 168,16,18432
Mv 124,156,28
Mv 120,148,28
Mv 120,144,24
Mv 60,136,24
Rb 172,17,18432
Mv 60,140,28
buffer_load_dwordx4 v14, s[8:11], s39 offen offset:2048 lds
Mv 56,132,28
Mv 56,128,24
Mv 64,128,32
Rb 176,16,24576
Mv 64,132,36
Mv 68,140,36
Mv 68,136,32
buffer_load_dwordx4 v15, s[8:11], s39 offen offset:3072 lds
Mv 128,144,32
Rb 180,17,24576
Mv 128,148,36
Mv 132,156,36
Mv 132,152,32
s_add_u32 s39, s39, 0x40000
Mv 140,152,40
Rb 184,16,26624
s_add_u32 m0, s37, 16384
Mv 140,156,44
buffer_load_dwordx4 v12, s[8:11], s39 offen offset:0 lds
Mv 136,148,44
Mv 136,144,40
Mv 76,136,40
Rb 188,17,26624
Mv 76,140,44
Mv 72,132,44
Mv 72,128,40
buffer_load_dwordx4 v13, s[8:11], s39 offen offset:1024 lds
Mv 80,128,48
Mv 80,132,52
Mv 84,140,52
Mv 84,136,48
Mv 144,144,48
Mv 144,148,52
buffer_load_dwordx4 v14, s[8:11], s39 offen offset:2048 lds
Mv 148,156,52
Mv 148,152,48
Mv 156,152,56
Mv 156,156,60
Mv 152,148,60
Mv 152,144,56
buffer_load_dwordx4 v15, s[8:11], s39 offen offset:3072 lds
Mv 92,136,56
Mv 92,140,60
Mv 88,132,60
Mv 88,128,56
)" R"(
s_waitcnt vmcnt(11) lgkmcnt(0)
s_barrier
Mv 160,160,0
Rb 128,16,32768
Mv 160,164,4
Rb 132,17,32768
s_add_u32 s38, s38, 0xffff0080
Mv 164,172,4
Rb 136,16,34816
s_add_u32 m0, s36, 0
Mv 164,168,0
buffer_load_dwordx4 v8, s[4:7], s38 offen offset:0 lds
Mv 224,176,0
Rb 140,17,34816
Mv 224,180,4
Rb 144,16,40960
Mv 228,188,4
Rb 148,17,40960
Mv 228,184,0
Rb 152,16,43008
buffer_load_dwordx4 v9, s[4:7], s38 offen offset:1024 lds
Mv 236,184,8
Rb 156,17,43008
Rt 0,18,32768
Mv 236,188,12
Mv 232,180,12
Rt 2,18,34816
Mv 232,176,8
buffer_load_dwordx4 v10, s[4:7], s38 offen offset:2048 lds
Mv 172,168,8
Rt 4,18,49152
Mv 172,172,12
Mv 168,164,12
Rt 6,18,51200
Mv 168,160,8
buffer_load_dwordx4 v11, s[4:7], s38 offen offset:3072 lds
Mv 176,160,16
Rt 8,19,32768
Mv 176,164,20
s_add_u32 s38, s38, 0x10000
Mv 180,172,20
Rt 10,19,34816
s_add_u32 m0, s36, 4096
Mv 180,168,16
buffer_load_dwordx4 v8, s[4:7], s38 offen offset:0 lds
Mv 240,176,16
Rt 12,19,49152
Mv 240,180,20
Mv 244,188,20
Rt 14,19,51200
Mv 244,184,16
buffer_load_dwordx4 v9, s[4:7], s38 offen offset:1024 lds
Mv 252,184,24
Rt 16,20,32768
Mv 252,188,28
Mv 248,180,28
Rt 18,20,34816
Mv 248,176,24
buffer_load_dwordx4 v10, s[4:7], s38 offen offset:2048 lds
Mv 188,168,24
Rt 20,20,49152
Mv 188,172,28
Mv 184,164,28
Rt 22,20,51200
Mv 184,160,24
)" R"(
buffer_load_dwordx4 v11, s[4:7], s38 offen offset:3072 lds
Mv 192,160,32
Rt 24,21,32768
Mv 192,164,36
Mv 196,172,36
Rt 26,21,34816
Mv 196,168,32
Ma 192,176,32
Rt 28,21,49152
Ma 192,180,36
buffer_store_dwordx4 a[224:227], v1, s[20:23], s72 offen sc0 sc1
Ma 196,188,36
Rt 30,21,51200
s_add_u32 s72, s72, 1024
Ma 196,184,32
Ma 204,184,40
Rt 32,22,32768
Ma 204,188,44
Ma 200,180,44
Rt 34,22,34816
Ma 200,176,40
Mv 204,168,40
Rt 36,22,49152
Mv 204,172,44
Mv 200,164,44
Rt 38,22,51200
Mv 200,160,40
Mv 208,160,48
Rt 40,23,32768
Mv 208,164,52
buffer_store_dwordx4 a[224:227], v1, s[20:23], s88 offen sc0 sc1
Mv 212,172,52
Rt 42,23,34816
s_add_u32 s88, s88, 1024
Mv 212,168,48
Ma 208,176,48
Rt 44,23,49152
Ma 208,180,52
Ma 212,188,52
Rt 46,23,51200
Ma 212,184,48
Ma 220,184,56
Rt 48,24,32768
Ma 220,188,60
s_add_u32 s43, s43, 0x60000
Ma 216,180,60
Rt 50,24,34816
Ma 216,176,56
buffer_load_dword v27, v26, s[16:19], s43 offen
Mv 220,168,56
Rt 52,24,49152
Mv 220,172,60
Mv 216,164,60
Rt 54,24,51200
Mv 216,160,56
Mv 32,128,0
Rt 56,25,32768
Mv 32,132,4
Rt 58,25,34816
Mv 36,140,4
Rt 60,25,49152
Mv 36,136,0
Rt 62,25,51200
Mv 96,144,0
)" R"(
Mv 96,148,4
Mv 100,156,4
Mv 100,152,0
Mv 108,152,8
Mv 108,156,12
Mv 104,148,12
Mv 104,144,8
Mv 44,136,8
Mv 44,140,12
Mv 40,132,12
s_add_u32 s39, s39, 0x40000
Mv 40,128,8
s_waitcnt vmcnt(20) lgkmcnt(0)
s_barrier
buffer_store_dword v6, v7, s[20:23], s52 offen offset:0 sc0 sc1
s_mov_b32 s52, 0x40000000
Mv 48,128,16
Rb 160,16,49152
s_add_u32 m0, s37, 32768
Mv 48,132,20
buffer_load_dwordx4 v12, s[8:11], s39 offen offset:0 lds
Mv 52,140,20
Mv 52,136,16
Mv 112,144,16
Rb 164,17,49152
Mv 112,148,20
Mv 116,156,20
Mv 116,152,16
buffer_load_dwordx4 v13, s[8:11], s39 offen offset:1024 lds
Mv 124,152,24
Rb 168,16,51200
Mv 124,156,28
Mv 120,148,28
Mv 120,144,24
Mv 60,136,24
Rb 172,17,51200
Mv 60,140,28
buffer_load_dwordx4 v14, s[8:11], s39 offen offset:2048 lds
Mv 56,132,28
Mv 56,128,24
Mv 64,128,32
Rb 176,16,57344
Mv 64,132,36
Mv 68,140,36
Mv 68,136,32
buffer_load_dwordx4 v15, s[8:11], s39 offen offset:3072 lds
Mv 128,144,32
Rb 180,17,57344
Mv 128,148,36
Mv 132,156,36
Mv 132,152,32
s_add_u32 s39, s39, 0x40000
Mv 140,152,40
Rb 184,16,59392
s_add_u32 m0, s37, 49152
Mv 140,156,44
buffer_load_dwordx4 v12, s[8:11], s39 offen offset:0 lds
Mv 136,148,44
Mv 136,144,40
Mv 76,136,40
Rb 188,17,59392
Mv 76,140,44
Mv 72,132,44
Mv 72,128,40
buffer_load_dwordx4 v13, s[8:11], s39 offen offset:1024 lds
)" R"(
Mv 80,128,48
Mv 80,132,52
Mv 84,140,52
Mv 84,136,48
Mv 144,144,48
Mv 144,148,52
buffer_load_dwordx4 v14, s[8:11], s39 offen offset:2048 lds
Mv 148,156,52
.LzH9:
s_cbranch_execz .LzH14
Mv 148,152,48
Mv 156,152,56
Mv 156,156,60
Mv 152,148,60
Mv 152,144,56
buffer_load_dwordx4 v15, s[8:11], s39 offen offset:3072 lds
Mv 92,136,56
Mv 92,140,60
Mv 88,132,60
Mv 88,128,56
s_waitcnt vmcnt(12) lgkmcnt(0)
s_barrier
Mv 160,160,0
Rb 128,16,0
Mv 160,164,4
Rb 132,17,0
s_add_u32 s38, s38, 0xffff0080
Mv 164,172,4
Rb 136,16,2048
s_add_u32 m0, s36, 32768
Mv 164,168,0
buffer_load_dwordx4 v8, s[4:7], s38 offen offset:0 lds
Mv 224,176,0
Rb 140,17,2048
Mv 224,180,4
Rb 144,16,8192
Mv 228,188,4
Rb 148,17,8192
Mv 228,184,0
Rb 152,16,10240
buffer_load_dwordx4 v9, s[4:7], s38 offen offset:1024 lds
Mv 236,184,8
Rb 156,17,10240
Rt 0,18,0
Mv 236,188,12
Mv 232,180,12
Rt 2,18,2048
Mv 232,176,8
buffer_load_dwordx4 v10, s[4:7], s38 offen offset:2048 lds
Mv 172,168,8
Rt 4,18,16384
Mv 172,172,12
Mv 168,164,12
Rt 6,18,18432
Mv 168,160,8
buffer_load_dwordx4 v11, s[4:7], s38 offen offset:3072 lds
Mv 176,160,16
Rt 8,19,0
Mv 176,164,20
s_add_u32 s38, s38, 0x10000
Mv 180,172,20
Rt 10,19,2048
s_add_u32 m0, s36, 36864
Mv 180,168,16
buffer_load_dwordx4 v8, s[4:7], s38 offen offset:0 lds
)" R"(
Mv 240,176,16
Rt 12,19,16384
Mv 240,180,20
Mv 244,188,20
Rt 14,19,18432
Mv 244,184,16
buffer_load_dwordx4 v9, s[4:7], s38 offen offset:1024 lds
Mv 252,184,24
Rt 16,20,0
Mv 252,188,28
Mv 248,180,28
Rt 18,20,2048
Mv 248,176,24
buffer_load_dwordx4 v10, s[4:7], s38 offen offset:2048 lds
Mv 188,168,24
Rt 20,20,16384
Mv 188,172,28
Mv 184,164,28
Rt 22,20,18432
Mv 184,160,24
buffer_load_dwordx4 v11, s[4:7], s38 offen offset:3072 lds
Mv 192,160,32
Rt 24,21,0
Mv 192,164,36
Mv 196,172,36
Rt 26,21,2048
Mv 196,168,32
Ma 192,176,32
Rt 28,21,16384
Ma 192,180,36
buffer_store_dwordx4 a[224:227], v1, s[20:23], s72 offen sc0 sc1
Ma 196,188,36
Rt 30,21,18432
s_add_u32 s72, s72, 1024
Ma 196,184,32
Ma 204,184,40
Rt 32,22,0
Ma 204,188,44
Ma 200,180,44
Rt 34,22,2048
Ma 200,176,40
Mv 204,168,40
Rt 36,22,16384
Mv 204,172,44
Mv 200,164,44
Rt 38,22,18432
Mv 200,160,40
Mv 208,160,48
Rt 40,23,0
Mv 208,164,52
buffer_store_dwordx4 a[224:227], v1, s[20:23], s88 offen sc0 sc1
Mv 212,172,52
Rt 42,23,2048
s_add_u32 s88, s88, 1024
Mv 212,168,48
Ma 208,176,48
Rt 44,23,16384
Ma 208,180,52
Ma 212,188,52
Rt 46,23,18432
Ma 212,184,48
s_cmp_eq_u32 s40, 2
s_cmov_b32 s72, 0x40000000
s_cmov_b32 s88, 0x40000000
)" R"(
Ma 220,184,56
Rt 48,24,0
Ma 220,188,60
s_add_u32 s43, s43, 0x60000
Ma 216,180,60
Rt 50,24,2048
s_sub_u32 s40, s40, 1
Ma 216,176,56
buffer_load_dword v27, v26, s[16:19], s43 offen
Mv 220,168,56
Rt 52,24,16384
Mv 220,172,60
Mv 216,164,60
Rt 54,24,18432
Mv 216,160,56
s_cmp_lg_u32 s40, 0
s_cbranch_scc1 .LzPBV
.LzPBVX:
Mv 32,128,0
Rt 56,25,0
Mv 32,132,4
Rt 58,25,2048
buffer_load_dword v4, v5, s[20:23], s50 offen offset:0 sc0 sc1
Mv 36,140,4
Rt 60,25,16384
Mv 36,136,0
Rt 62,25,18432
Mv 96,144,0
Mv 96,148,4
Mv 100,156,4
Mv 100,152,0
Mv 108,152,8
Mv 108,156,12
Mv 104,148,12
Mv 104,144,8
Mv 44,136,8
Mv 44,140,12
Mv 40,132,12
Mv 40,128,8
s_waitcnt lgkmcnt(0)
s_barrier
Mv 48,128,16
Rb 160,16,16384
Mv 48,132,20
Mv 52,140,20
Mv 52,136,16
Mv 112,144,16
Rb 164,17,16384
Mv 112,148,20
Mv 116,156,20
Mv 116,152,16
Mv 124,152,24
Rb 168,16,18432
Mv 124,156,28
Mv 120,148,28
Mv 120,144,24
Mv 60,136,24
Rb 172,17,18432
Mv 60,140,28
Mv 56,132,28
Mv 56,128,24
Mv 64,128,32
Rb 176,16,24576
Mv 64,132,36
Mv 68,140,36
)" R"(
Mv 68,136,32
Mv 128,144,32
Rb 180,17,24576
Mv 128,148,36
Mv 132,156,36
Mv 132,152,32
s_waitcnt vmcnt(0)
v_cmp_eq_u32_e32 vcc, s3, v4
s_andn2_b64 s[28:29], -1, vcc
s_cbranch_scc1 .Lzsp17
.Lzok17:
Mv 140,152,40
Rb 184,16,26624
Mv 140,156,44
s_lshl3_add_u32 s29, s34, s35
s_lshl2_add_u32 s29, s29, s33
Mv 136,148,44
s_add_u32 m0, s36, 131072
s_mul_i32 s83, s29, 7168
Mv 136,144,40
buffer_load_dwordx4 v2, s[20:23], s56 offen offset:0 sc1 lds
Mv 76,136,40
Rb 188,17,26624
s_mov_b32 s82, s89
Mv 76,140,44
s_add_u32 s82, s82, 0xc00000
s_add_u32 s82, s82, s84
Mv 72,132,44
s_mov_b32 s24, s80
s_mov_b32 s25, s81
Mv 72,128,40
s_mov_b32 s26, 0x380000
s_mov_b32 s27, 0x20000
Mv 80,128,48
s_add_u32 s28, s33, 0
s_lshl2_add_u32 s28, s35, s28
Mv 80,132,52
buffer_load_dwordx4 v2, s[20:23], s56 offen offset:1024 sc1 lds
Mv 84,140,52
s_mul_i32 s29, s28, 293
s_lshr_b32 s29, s29, 11
Mv 84,136,48
s_mul_i32 s29, s29, 9
s_add_u32 s29, s29, s28
Mv 144,144,48
s_lshl_b32 s29, s29, 10
s_add_u32 s4, s29, s82
Mv 144,148,52
s_add_u32 s28, s33, 4
s_lshl3_add_u32 s28, s28, s35
Mv 148,156,52
s_mul_i32 s29, s28, 293
s_lshr_b32 s29, s29, 11
Mv 148,152,48
buffer_load_dwordx4 v2, s[20:23], s56 offen offset:2048 sc1 lds
Mv 156,152,56
s_mul_i32 s29, s29, 9
s_add_u32 s29, s29, s28
Mv 156,156,60
s_lshl_b32 s29, s29, 10
s_add_u32 s5, s29, s82
Mv 152,148,60
s_add_u32 s28, s33, 8
s_lshl3_add_u32 s28, s28, s35
)" R"(
Mv 152,144,56
s_mul_i32 s29, s28, 293
s_lshr_b32 s29, s29, 11
Mv 92,136,56
s_mul_i32 s29, s29, 9
s_add_u32 s29, s29, s28
Mv 92,140,60
buffer_load_dwordx4 v2, s[20:23], s56 offen offset:3072 sc1 lds
Mv 88,132,60
s_lshl_b32 s29, s29, 10
s_add_u32 s6, s29, s82
Mv 88,128,56
s_waitcnt lgkmcnt(0)
s_barrier
Mv 160,160,0
Rb 128,16,32768
s_add_u32 s28, s33, 12
Mv 160,164,4
Rb 132,17,32768
s_lshl3_add_u32 s28, s28, s35
Mv 164,172,4
Rb 136,16,34816
s_add_u32 m0, s36, 0
Mv 164,168,0
buffer_load_dwordx4 v2, s[20:23], s55 offen offset:0 sc1 lds
Mv 224,176,0
Rb 140,17,34816
s_mul_i32 s29, s28, 293
Mv 224,180,4
Rb 144,16,40960
s_lshr_b32 s29, s29, 11
Mv 228,188,4
Rb 148,17,40960
s_mul_i32 s29, s29, 9
Mv 228,184,0
Rb 152,16,43008
s_add_u32 s29, s29, s28
Mv 236,184,8
Rb 156,17,43008
Rt 0,18,32768
buffer_load_dwordx4 v2, s[20:23], s55 offen offset:1024 sc1 lds
Mv 236,188,12
s_lshl_b32 s29, s29, 10
s_add_u32 s7, s29, s82
Mv 232,180,12
Rt 2,18,34816
Mv 232,176,8
s_cmp_gt_u32 s33, 1
s_cmov_b32 s7, 0x40000000
Mv 172,168,8
Rt 4,18,49152
s_add_u32 s28, s33, 14
Mv 172,172,12
buffer_load_dwordx4 v2, s[20:23], s55 offen offset:2048 sc1 lds
Mv 168,164,12
Rt 6,18,51200
s_lshl3_add_u32 s28, s28, s35
Mv 168,160,8
s_mul_i32 s29, s28, 293
s_lshr_b32 s29, s29, 11
Mv 176,160,16
Rt 8,19,32768
s_mul_i32 s29, s29, 9
Mv 176,164,20
s_add_u32 s29, s29, s28
)" R"(
s_lshl_b32 s29, s29, 10
Mv 180,172,20
Rt 10,19,34816
buffer_load_dwordx4 v2, s[20:23], s55 offen offset:3072 sc1 lds
Mv 180,168,16
s_add_u32 s8, s29, s82
s_add_u32 s28, s33, 18
Mv 240,176,16
Rt 12,19,49152
s_lshl3_add_u32 s28, s28, s35
Mv 240,180,20
s_mul_i32 s29, s28, 293
s_lshr_b32 s29, s29, 11
Mv 244,188,20
Rt 14,19,51200
s_add_u32 m0, s36, 4096
Mv 244,184,16
buffer_load_dwordx4 v3, s[20:23], s55 offen offset:0 sc1 lds
Mv 252,184,24
Rt 16,20,32768
s_mul_i32 s29, s29, 9
Mv 252,188,28
s_add_u32 s29, s29, s28
s_lshl_b32 s29, s29, 10
Mv 248,180,28
Rt 18,20,34816
s_add_u32 s9, s29, s82
Mv 248,176,24
s_add_u32 s28, s33, 22
s_lshl3_add_u32 s28, s28, s35
Mv 188,168,24
Rt 20,20,49152
buffer_load_dwordx4 v3, s[20:23], s55 offen offset:1024 sc1 lds
Mv 188,172,28
s_mul_i32 s29, s28, 293
s_lshr_b32 s29, s29, 11
Mv 184,164,28
Rt 22,20,51200
s_mul_i32 s29, s29, 9
Mv 184,160,24
s_add_u32 s29, s29, s28
s_lshl_b32 s29, s29, 10
Mv 192,160,32
Rt 24,21,32768
s_add_u32 s10, s29, s82
Mv 192,164,36
buffer_load_dwordx4 v3, s[20:23], s55 offen offset:2048 sc1 lds
Mv 196,172,36
Rt 26,21,34816
s_add_u32 s28, s33, 26
Mv 196,168,32
s_lshl3_add_u32 s28, s28, s35
s_mul_i32 s29, s28, 293
Ma 192,176,32
Rt 28,21,49152
s_lshr_b32 s29, s29, 11
Ma 192,180,36
s_mul_i32 s29, s29, 9
s_add_u32 s29, s29, s28
Ma 196,188,36
Rt 30,21,51200
buffer_load_dwordx4 v3, s[20:23], s55 offen offset:3072 sc1 lds
Ma 196,184,32
s_lshl_b32 s29, s29, 10
)" R"(
s_add_u32 s11, s29, s82
Ma 204,184,40
Rt 32,22,32768
Ma 204,188,44
s_cmp_gt_u32 s33, 1
s_cmov_b32 s11, 0x40000000
Ma 200,180,44
Rt 34,22,34816
s_add_u32 m0, s36, 135168
Ma 200,176,40
buffer_load_dwordx4 v3, s[20:23], s56 offen offset:0 sc1 lds
Mv 204,168,40
Rt 36,22,49152
s_and_b32 s28, s34, 1
Mv 204,172,44
s_lshl_b32 s28, s28, 14
s_add_u32 s82, s37, s28
Mv 200,164,44
Rt 38,22,51200
s_add_u32 s82, s82, 0x10000
Mv 200,160,40
Mv 208,160,48
Rt 40,23,32768
buffer_load_dwordx4 v3, s[20:23], s56 offen offset:1024 sc1 lds
Mv 208,164,52
Mv 212,172,52
Rt 42,23,34816
Mv 212,168,48
Ma 208,176,48
Rt 44,23,49152
Ma 208,180,52
buffer_load_dwordx4 v3, s[20:23], s56 offen offset:2048 sc1 lds
Ma 212,188,52
Rt 46,23,51200
Ma 212,184,48
Ma 220,184,56
Rt 48,24,32768
Ma 220,188,60
Ma 216,180,60
Rt 50,24,34816
buffer_load_dwordx4 v3, s[20:23], s56 offen offset:3072 sc1 lds
Ma 216,176,56
Mv 220,168,56
Rt 52,24,49152
Mv 220,172,60
s_add_u32 s55, s55, 0x10000
s_add_u32 s56, s56, 0x10000
Mv 216,164,60
Rt 54,24,51200
Mv 216,160,56
Mv 32,128,0
Rt 56,25,32768
Mv 32,132,4
Rt 58,25,34816
Mv 36,140,4
Rt 60,25,49152
s_add_u32 m0, s36, 65536
Mv 36,136,0
Rt 62,25,51200
buffer_load_dwordx4 v2, s[20:23], s56 offen offset:0 sc1 lds
Mv 44,136,8
Rb 160,16,49152
Mv 44,140,12
Rb 164,17,49152
)" R"(
s_add_u32 s50, s50, 1024
Mv 40,132,12
Rb 168,16,51200
s_min_u32 s50, s50, s60
Mv 40,128,8
Rb 172,17,51200
s_waitcnt lgkmcnt(12)
Mv 48,128,16
Rb 176,16,57344
Mv 48,132,20
Rb 180,17,57344
buffer_load_dwordx4 v2, s[20:23], s56 offen offset:1024 sc1 lds
Mv 52,140,20
Rb 184,16,59392
Mv 52,136,16
Rb 188,17,59392
Mv 60,136,24
Cv 32,32,33
Cv 33,34,35
Mv 60,140,28
Cv 34,36,37
Cv 35,38,39
Mv 56,132,28
Cv 40,40,41
Cv 41,42,43
Mv 56,128,24
buffer_load_dwordx4 v2, s[20:23], s56 offen offset:2048 sc1 lds
Mv 64,128,32
Cv 42,44,45
Cv 43,46,47
Mv 64,132,36
Cv 48,48,49
Cv 49,50,51
Mv 68,140,36
Cv 50,52,53
Cv 51,54,55
Mv 68,136,32
Cv 56,56,57
Cv 57,58,59
Mv 76,136,40
Cv 58,60,61
Cv 59,62,63
Mv 76,140,44
buffer_load_dwordx4 v2, s[20:23], s56 offen offset:3072 sc1 lds
Mv 72,132,44
Mv 72,128,40
s_waitcnt lgkmcnt(0)
s_barrier
Mv 80,128,48
v_add_u32 v8, 0x10000, v1
v_add_u32 v10, 0x20000, v1
Mv 80,132,52
buffer_load_dword v4, v5, s[20:23], s50 offen offset:0 sc0 sc1
Mv 84,140,52
s_add_u32 m0, s36, 32768
s_mov_b32 s93, 2
Mv 84,136,48
buffer_load_dwordx4 v2, s[20:23], s55 offen offset:0 sc1 lds
Mv 92,136,56
s_mov_b32 s94, 0
Cv 64,64,65
Mv 92,140,60
Cv 65,66,67
Cv 66,68,69
Mv 88,132,60
)" R"(
Cv 67,70,71
Cv 72,72,73
Mv 88,128,56
Cv 73,74,75
Cv 74,76,77
Mv 96,144,0
Cv 75,78,79
Cv 80,80,81
Mv 96,148,4
Cv 81,82,83
Cv 82,84,85
Mv 100,156,4
Cv 83,86,87
Mv 100,152,0
buffer_load_dwordx4 v2, s[20:23], s55 offen offset:1024 sc1 lds
Mv 108,152,8
Cv 88,88,89
Cv 89,90,91
Mv 108,156,12
Cv 90,92,93
Cv 91,94,95
Mv 104,148,12
Mv 104,144,8
buffer_store_dwordx4 v[32:35], v2, s[20:23], s45 offen offset:0 sc0 sc1
Mv 112,144,16
Mv 112,148,20
Mv 116,156,20
Mv 116,152,16
buffer_load_dwordx4 v2, s[20:23], s55 offen offset:2048 sc1 lds
Mv 124,152,24
Mv 124,156,28
Mv 120,148,28
Mv 120,144,24
buffer_store_dwordx4 v[40:43], v2, s[20:23], s45 offen offset:1024 sc0 sc1
Mv 128,144,32
Mv 128,148,36
Mv 132,156,36
Mv 132,152,32
buffer_load_dwordx4 v2, s[20:23], s55 offen offset:3072 sc1 lds
Mv 140,152,40
Mv 140,156,44
Mv 136,148,44
Mv 136,144,40
buffer_store_dwordx4 v[48:51], v2, s[20:23], s45 offen offset:2048 sc0 sc1
Mv 144,144,48
Cv 128,128,129
Cv 129,130,131
Mv 144,148,52
Cv 130,132,133
Cv 131,134,135
Mv 148,156,52
s_add_u32 m0, s36, 36864
Cv 132,96,97
Mv 148,152,48
buffer_load_dwordx4 v3, s[20:23], s55 offen offset:0 sc1 lds
.LzH10:
s_cbranch_execz .LzH20
Mv 156,152,56
Cv 136,136,137
Cv 137,138,139
Mv 156,156,60
Cv 138,140,141
Cv 139,142,143
Mv 152,148,60
)" R"(
Cv 133,98,99
Cv 134,100,101
Mv 152,144,56
buffer_store_dwordx4 v[56:59], v2, s[20:23], s45 offen offset:3072 sc0 sc1
Mv 160,160,0
Cv 144,144,145
Cv 145,146,147
Mv 160,164,4
Cv 146,148,149
Cv 147,150,151
Mv 164,172,4
Cv 135,102,103
Cv 140,104,105
Mv 164,168,0
buffer_load_dwordx4 v3, s[20:23], s55 offen offset:1024 sc1 lds
Mv 224,176,0
Cv 152,152,153
Cv 153,154,155
Mv 224,180,4
Cv 154,156,157
Cv 155,158,159
Mv 228,188,4
Cv 141,106,107
Cv 142,108,109
Mv 228,184,0
buffer_store_dwordx4 v[64:67], v3, s[20:23], s45 offen offset:0 sc0 sc1
Mv 236,184,8
Cv 143,110,111
Cv 148,112,113
Mv 236,188,12
Cv 149,114,115
Cv 150,116,117
Mv 232,180,12
Cv 151,118,119
Cv 156,120,121
Mv 232,176,8
buffer_load_dwordx4 v3, s[20:23], s55 offen offset:2048 sc1 lds
Mv 172,168,8
Cv 157,122,123
Cv 158,124,125
Mv 172,172,12
Cv 159,126,127
Cv 160,160,161
Mv 168,164,12
Cv 161,162,163
Cv 162,164,165
Mv 168,160,8
buffer_store_dwordx4 v[72:75], v3, s[20:23], s45 offen offset:1024 sc0 sc1
Mv 176,160,16
Cv 163,166,167
v_accvgpr_write_b32 a224, v160
Mv 176,164,20
v_accvgpr_write_b32 a225, v161
v_accvgpr_write_b32 a226, v162
Mv 180,172,20
v_accvgpr_write_b32 a227, v163
Mv 180,168,16
buffer_load_dwordx4 v3, s[20:23], s55 offen offset:3072 sc1 lds
Mv 240,176,16
Cv 168,168,169
Cv 169,170,171
Mv 240,180,20
Cv 170,172,173
Cv 171,174,175
)" R"(
Mv 244,188,20
v_accvgpr_write_b32 a228, v168
v_accvgpr_write_b32 a229, v169
Mv 244,184,16
buffer_store_dwordx4 v[80:83], v3, s[20:23], s45 offen offset:2048 sc0 sc1
Mv 252,184,24
v_accvgpr_write_b32 a230, v170
v_accvgpr_write_b32 a231, v171
Mv 252,188,28
Mv 248,180,28
s_add_u32 m0, s36, 69632
Mv 248,176,24
buffer_load_dwordx4 v3, s[20:23], s56 offen offset:0 sc1 lds
Mv 188,168,24
Mv 188,172,28
Mv 184,164,28
Mv 184,160,24
buffer_store_dwordx4 v[88:91], v3, s[20:23], s45 offen offset:3072 sc0 sc1
Mv 192,160,32
Mv 192,164,36
Mv 196,172,36
Mv 196,168,32
buffer_load_dwordx4 v3, s[20:23], s56 offen offset:1024 sc1 lds
Ma 192,176,32
Ma 192,180,36
Ma 196,188,36
Ma 196,184,32
s_waitcnt vmcnt(18)
s_barrier
Ma 204,184,40
v_cmp_eq_u32_e32 vcc, s3, v4
s_andn2_b64 s[28:29], -1, vcc
s_cbranch_scc1 .Lzsp18
.Lzok18:
Ma 204,188,44
Ma 200,180,44
Ma 200,176,40
buffer_load_dwordx4 v3, s[20:23], s56 offen offset:2048 sc1 lds
Mv 204,168,40
Mv 204,172,44
Mv 200,164,44
Mv 200,160,40
Mv 208,160,48
Rb 0,1,0
Mv 208,164,52
Rb 4,1,1024
Mv 212,172,52
Rb 8,1,2048
Mv 212,168,48
buffer_load_dwordx4 v3, s[20:23], s56 offen offset:3072 sc1 lds
Rb 12,1,3072
Ma 208,176,48
Rb 16,1,8192
Ma 208,180,52
Rb 20,1,9216
Ma 212,188,52
Rb 24,1,10240
Ma 212,184,48
Rb 28,1,11264
Ma 220,184,56
Rb 32,1,4096
Ma 220,188,60
Rb 36,1,5120
Ma 216,180,60
Rb 40,1,6144
)" R"(
Ma 216,176,56
Rb 44,1,7168
Mv 220,168,56
Mv 220,172,60
Mv 216,164,60
Mv 216,160,56
s_add_u32 s56, s56, 0x10000
.LzH11:
s_cbranch_execz .LzH15
.LzFB:
s_waitcnt lgkmcnt(8)
Mz 64,0,64
Rb 48,1,12288
s_add_u32 s55, s55, 0x10000
Mv 64,4,72
s_cmp_eq_u32 s57, 2
s_cselect_b32 s55, s59, s55
s_sub_u32 s57, s57, 1
Mv 64,8,80
Rb 52,1,13312
Mv 64,12,88
buffer_store_dwordx4 v[132:135], v2, s[20:23], s46 offen offset:0 sc0 sc1
Mz 68,12,92
Rb 56,1,14336
Cv 176,176,177
Cv 177,178,179
Mv 68,8,84
Cv 178,180,181
Cv 179,182,183
v_accvgpr_write_b32 a232, v176
Mv 68,4,76
Rb 60,1,15360
s_waitcnt lgkmcnt(8)
Mv 68,0,68
v_accvgpr_write_b32 a233, v177
v_accvgpr_write_b32 a234, v178
v_accvgpr_write_b32 a235, v179
Mv 68,16,100
Rb 0,1,16384
Cv 184,184,185
Cv 185,186,187
Mv 68,20,108
Cv 186,188,189
Cv 187,190,191
Mv 68,24,116
Rb 4,1,17408
v_accvgpr_write_b32 a236, v184
v_accvgpr_write_b32 a237, v185
Mv 68,28,124
v_accvgpr_write_b32 a238, v186
v_accvgpr_write_b32 a239, v187
Cv 192,192,193
Mv 64,28,120
Rb 8,1,18432
Cv 193,194,195
Mv 64,24,112
buffer_store_dwordx4 v[140:143], v2, s[20:23], s46 offen offset:1024 sc0 sc1
Cv 194,196,197
Mv 64,20,104
Rb 12,1,19456
Cv 195,198,199
s_waitcnt lgkmcnt(8)
Mv 64,16,96
v_accvgpr_write_b32 a240, v192
)" R"(
v_accvgpr_write_b32 a241, v193
v_accvgpr_write_b32 a242, v194
Mz 72,32,64
Rb 16,1,24576
v_accvgpr_write_b32 a243, v195
Mv 72,36,72
Cv 200,200,201
Cv 201,202,203
Cv 202,204,205
Mv 72,40,80
Rb 20,1,25600
Cv 203,206,207
v_accvgpr_write_b32 a244, v200
Mv 72,44,88
v_accvgpr_write_b32 a245, v201
v_accvgpr_write_b32 a246, v202
Mz 76,44,92
Rb 24,1,26624
v_accvgpr_write_b32 a247, v203
Cv 208,208,209
Mv 76,40,84
Cv 209,210,211
Cv 210,212,213
Cv 211,214,215
Mv 76,36,76
Rb 28,1,27648
s_waitcnt lgkmcnt(8)
Mv 76,32,68
buffer_store_dwordx4 v[148:151], v2, s[20:23], s46 offen offset:2048 sc0 sc1
v_accvgpr_write_b32 a248, v208
Mv 76,48,100
Rb 32,1,20480
v_accvgpr_write_b32 a249, v209
v_accvgpr_write_b32 a250, v210
Mv 76,52,108
v_accvgpr_write_b32 a251, v211
Cv 216,216,217
Mv 76,56,116
Rb 36,1,21504
Cv 217,218,219
Cv 218,220,221
Mv 76,60,124
Cv 219,222,223
v_accvgpr_write_b32 a252, v216
v_accvgpr_write_b32 a253, v217
Mv 72,60,120
Rb 40,1,22528
v_accvgpr_write_b32 a254, v218
Mv 72,56,112
v_accvgpr_write_b32 a255, v219
Cv 224,224,225
Cv 225,226,227
Mv 72,52,104
Rb 44,1,23552
Cv 226,228,229
s_waitcnt lgkmcnt(8)
Mv 72,48,96
Cv 227,230,231
v_accvgpr_write_b32 a128, v224
v_accvgpr_write_b32 a129, v225
Mz 80,0,64
Rb 48,1,28672
v_accvgpr_write_b32 a130, v226
Mv 80,4,72
)" R"(
buffer_store_dwordx4 v[156:159], v2, s[20:23], s46 offen offset:3072 sc0 sc1
v_accvgpr_write_b32 a131, v227
Mv 80,8,80
Rb 52,1,29696
Cv 232,232,233
Cv 233,234,235
Mv 80,12,88
Cv 234,236,237
Cv 235,238,239
Mz 84,12,92
Rb 56,1,30720
v_exp_f32_e32 v32, v64
Mv 84,8,84
v_exp_f32_e32 v24, v65
v_accvgpr_write_b32 a132, v232
Mv 84,4,76
Rb 60,1,31744
s_waitcnt lgkmcnt(8)
Mv 84,0,68
v_exp_f32_e32 v33, v66
v_accvgpr_write_b32 a133, v233
Mv 84,16,100
v_exp_f32_e32 v25, v67
v_accvgpr_write_b32 a134, v234
Mv 84,20,108
v_exp_f32_e32 v34, v72
Mv 84,24,116
v_exp_f32_e32 v26, v73
v_accvgpr_write_b32 a135, v235
Mv 84,28,124
buffer_store_dwordx4 v[128:131], v3, s[20:23], s46 offen offset:0 sc0 sc1
Cv 240,240,241
Mv 80,28,120
s_add_u32 s50, s50, 1024
s_min_u32 s50, s50, s60
Mv 80,24,112
v_exp_f32_e32 v35, v74
Cv 241,242,243
Mv 80,20,104
v_exp_f32_e32 v27, v75
Cv 242,244,245
Mv 80,16,96
s_waitcnt vmcnt(8) lgkmcnt(0)
s_barrier
buffer_store_dword v6, v7, s[20:23], s49 offen offset:0 sc0 sc1
Mz 88,32,64
Rb 0,1,32768
Cv 243,246,247
Mv 88,36,72
Rb 4,1,33792
v_exp_f32_e32 v36, v68
Mv 88,40,80
Rb 8,1,34816
s_add_u32 m0, s36, 0
v_accvgpr_write_b32 a136, v240
Mv 88,44,88
Rb 12,1,35840
buffer_load_dwordx4 v2, s[20:23], s55 offen offset:0 sc1 lds
Mz 92,44,92
Rb 16,1,40960
v_exp_f32_e32 v28, v69
Mv 92,40,84
Rb 20,1,41984
v_exp_f32_e32 v37, v70
Mv 92,36,76
)" R"(
Rb 24,1,43008
v_accvgpr_write_b32 a137, v241
Mv 92,32,68
Rb 28,1,44032
v_exp_f32_e32 v29, v71
Mv 92,48,100
Rb 32,1,36864
v_exp_f32_e32 v38, v76
Mv 92,52,108
Rb 36,1,37888
v_accvgpr_write_b32 a138, v242
Mv 92,56,116
Rb 40,1,38912
v_exp_f32_e32 v39, v78
Mv 92,60,124
Rb 44,1,39936
buffer_load_dwordx4 v2, s[20:23], s55 offen offset:1024 sc1 lds
Mv 88,60,120
v_accvgpr_write_b32 a139, v243
Cv 248,248,249
Mv 88,56,112
Cv 249,250,251
Cv 250,252,253
Cv 251,254,255
Mv 88,52,104
v_accvgpr_write_b32 a140, v248
v_accvgpr_write_b32 a141, v249
s_waitcnt lgkmcnt(8)
Mv 88,48,96
v_accvgpr_write_b32 a142, v250
v_accvgpr_write_b32 a143, v251
v_mov_b32 v16, 0
Mz 96,0,64
Rb 48,1,45056
buffer_load_dword v4, v5, s[20:23], s50 offen offset:0 sc0 sc1
Mv 96,4,72
v_mov_b32 v17, 0
v_mov_b32 v18, 0
v_mov_b32 v19, 0
Mv 96,8,80
Rb 52,1,46080
v_mov_b32 v20, 0
v_mov_b32 v21, 0
Mv 96,12,88
buffer_load_dwordx4 v2, s[20:23], s55 offen offset:2048 sc1 lds
Mz 100,12,92
Rb 56,1,47104
v_exp_f32_e32 v40, v80
Mv 100,8,84
v_exp_f32_e32 v41, v82
v_mov_b32 v22, 0
Mv 100,4,76
Rb 60,1,48128
s_waitcnt lgkmcnt(8)
Mv 100,0,68
buffer_store_dwordx4 v[136:139], v3, s[20:23], s46 offen offset:1024 sc0 sc1
v_mov_b32 v23, 0
Mv 100,16,100
Rb 0,1,49152
v_exp_f32_e32 v42, v88
Mv 100,20,108
v_exp_f32_e32 v43, v90
Mv 100,24,116
Rb 4,1,50176
)" R"(
v_exp_f32_e32 v44, v84
Mv 100,28,124
buffer_load_dwordx4 v2, s[20:23], s55 offen offset:3072 sc1 lds
v_add_f32_e32 v14, v32, v24
Mv 96,28,120
Rb 8,1,51200
Cv 32,32,24
Mv 96,24,112
v_exp_f32_e32 v24, v77
v_add_f32_e32 v14, v14, v33
Mv 96,20,104
Rb 12,1,52224
v_add_f32_e32 v14, v14, v25
s_waitcnt lgkmcnt(8)
Mv 96,16,96
v_exp_f32_e32 v45, v86
Cv 33,33,25
Mz 104,32,64
Rb 16,1,57344
v_add_f32_e32 v14, v14, v34
Mv 104,36,72
v_exp_f32_e32 v25, v79
v_add_f32_e32 v14, v14, v26
Mv 104,40,80
Rb 20,1,58368
s_add_u32 m0, s36, 4096
Cv 34,34,26
Mv 104,44,88
buffer_load_dwordx4 v3, s[20:23], s55 offen offset:0 sc1 lds
Mz 108,44,92
Rb 24,1,59392
v_exp_f32_e32 v26, v81
Mv 108,40,84
v_exp_f32_e32 v46, v92
v_add_f32_e32 v14, v14, v35
Mv 108,36,76
Rb 28,1,60416
s_waitcnt lgkmcnt(8)
Mv 108,32,68
buffer_store_dwordx4 v[144:147], v3, s[20:23], s46 offen offset:2048 sc0 sc1
v_add_f32_e32 v14, v14, v27
Mv 108,48,100
Rb 32,1,53248
v_exp_f32_e32 v47, v94
Mv 108,52,108
Cv 35,35,27
v_add_f32_e32 v15, v36, v28
Mv 108,56,116
Rb 36,1,54272
v_exp_f32_e32 v27, v83
Mv 108,60,124
buffer_load_dwordx4 v3, s[20:23], s55 offen offset:1024 sc1 lds
Cv 36,36,28
Mv 104,60,120
Rb 40,1,55296
v_add_f32_e32 v15, v15, v37
Mv 104,56,112
v_exp_f32_e32 v28, v89
v_add_f32_e32 v15, v15, v29
Mv 104,52,104
Rb 44,1,56320
Cv 37,37,29
s_waitcnt lgkmcnt(8)
Mv 104,48,96
)" R"(
v_exp_f32_e32 v29, v91
v_add_f32_e32 v15, v15, v38
Mz 112,0,64
Rb 48,1,61440
v_add_f32_e32 v15, v15, v24
Mv 112,4,72
Cv 38,38,24
v_exp_f32_e32 v24, v85
Mv 112,8,80
Rb 52,1,62464
v_add_f32_e32 v15, v15, v39
v_add_f32_e32 v15, v15, v25
Mv 112,12,88
buffer_load_dwordx4 v3, s[20:23], s55 offen offset:2048 sc1 lds
Mz 116,12,92
Rb 56,1,63488
Cv 39,39,25
v_add_f32_e32 v14, v14, v40
Mv 116,8,84
v_exp_f32_e32 v25, v87
v_add_f32_e32 v14, v14, v26
Mv 116,4,76
Rb 60,1,64512
s_waitcnt lgkmcnt(8)
Mv 116,0,68
buffer_store_dwordx4 v[152:155], v3, s[20:23], s46 offen offset:3072 sc0 sc1
Cv 40,40,26
Mv 116,16,100
v_exp_f32_e32 v26, v93
v_add_f32_e32 v14, v14, v41
Mv 116,20,108
v_add_f32_e32 v14, v14, v27
Cv 41,41,27
Mv 116,24,116
v_exp_f32_e32 v27, v95
v_add_f32_e32 v14, v14, v42
Mv 116,28,124
buffer_load_dwordx4 v3, s[20:23], s55 offen offset:3072 sc1 lds
v_add_f32_e32 v14, v14, v28
Mv 112,28,120
Cv 42,42,28
v_add_f32_e32 v14, v14, v43
Mv 112,24,112
v_add_f32_e32 v14, v14, v29
Cv 43,43,29
v_add_f32_e32 v15, v15, v44
Mv 112,20,104
v_add_f32_e32 v15, v15, v24
Cv 44,44,24
v_add_f32_e32 v15, v15, v45
Mv 112,16,96
v_add_f32_e32 v15, v15, v25
s_waitcnt lgkmcnt(0)
s_barrier
Mz 120,32,64
Rb 0,10,0
Cv 45,45,25
Mv 120,36,72
Rb 4,10,16384
v_add_f32_e32 v15, v15, v46
v_add_f32_e32 v15, v15, v26
Mv 120,40,80
Rb 8,10,1024
Cv 46,46,26
v_add_f32_e32 v15, v15, v47
)" R"(
Mv 120,44,88
Rb 12,10,17408
v_add_f32_e32 v15, v15, v27
Mz 124,44,92
Rb 16,10,2048
Cv 47,47,27
v_max_f32_e32 v11, v14, v15
Mv 124,40,84
Rb 20,10,18432
v_mov_b32 v24, v14
v_mov_b32 v25, v15
Mv 124,36,76
Rb 24,10,3072
v_permlane32_swap_b32_e32 v24, v25
Mv 124,32,68
Rb 28,10,19456
v_add_f32_e32 v24, v24, v25
v_mov_b32 v25, v24
Mv 124,48,100
Rb 32,10,4096
v_permlane16_swap_b32_e32 v24, v25
v_add_f32_e32 v24, v24, v25
Mv 124,52,108
Rb 36,10,20480
s_add_u32 s55, s55, 0x10000
Mv 124,56,116
Rb 40,10,5120
v_rcp_f32_e32 v24, v24
Mv 124,60,124
Rb 44,10,21504
s_cmp_eq_u32 s57, 2
s_cselect_b32 s55, s59, s55
Mv 120,60,120
v_max_f32_e32 v11, v11, v24
v_cmp_nge_f32_e32 vcc, s62, v11
Mv 120,56,112
Mv 120,52,104
s_waitcnt lgkmcnt(8)
s_cbranch_vccnz .Lzfx6
.Lzfr6:
Mv 120,48,96
v_mov_b32 v12, v14
v_mov_b32 v13, v15
v_mfma_f32_16x16x32_bf16 v[128:131], a[0:3], v[32:35], 0
Rb 48,10,6144
Mp 128,4,40
v_mfma_f32_16x16x32_bf16 v[136:139], a[4:7], v[44:47], 0
Rb 52,10,22528
s_add_u32 m0, s36, 32768
.LzH12:
s_cbranch_execz .LzH16
Mp 136,0,36
buffer_load_dwordx4 v2, s[20:23], s55 offen offset:0 sc1 lds
v_mfma_f32_16x16x32_bf16 v[140:143], a[8:11], v[36:39], 0
Rb 56,10,7168
Mp 140,12,44
v_exp_f32_e32 v48, v96
v_mfma_f32_16x16x32_bf16 v[132:135], a[12:15], v[40:43], 0
Rb 60,10,23552
s_waitcnt lgkmcnt(8)
Mp 132,8,32
v_exp_f32_e32 v28, v97
v_mfma_f32_16x16x32_bf16 v[144:147], a[16:19], v[32:35], 0
Rb 0,10,8192
)" R"(
v_add_f32_e32 v14, v48, v28
Mp 144,20,40
v_exp_f32_e32 v49, v98
v_mfma_f32_16x16x32_bf16 v[152:155], a[20:23], v[44:47], 0
Rb 4,10,24576
Cv 48,48,28
Mp 152,16,36
buffer_load_dwordx4 v2, s[20:23], s55 offen offset:1024 sc1 lds
v_mfma_f32_16x16x32_bf16 v[156:159], a[24:27], v[36:39], 0
Rb 8,10,9216
v_exp_f32_e32 v29, v99
Mp 156,28,44
v_exp_f32_e32 v50, v104
v_mfma_f32_16x16x32_bf16 v[148:151], a[28:31], v[40:43], 0
Rb 12,10,25600
s_waitcnt lgkmcnt(8)
Mp 148,24,32
v_exp_f32_e32 v24, v105
v_mfma_f32_16x16x32_bf16 v[160:163], a[32:35], v[32:35], 0
Rb 16,10,10240
v_add_f32_e32 v14, v14, v49
Mp 160,36,40
v_exp_f32_e32 v51, v106
v_add_f32_e32 v14, v14, v29
v_mfma_f32_16x16x32_bf16 v[168:171], a[36:39], v[44:47], 0
Rb 20,10,26624
Cv 49,49,29
Mp 168,32,36
buffer_load_dwordx4 v2, s[20:23], s55 offen offset:2048 sc1 lds
v_mfma_f32_16x16x32_bf16 v[172:175], a[40:43], v[36:39], 0
Rb 24,10,11264
v_add_f32_e32 v14, v14, v50
Mp 172,44,44
v_exp_f32_e32 v25, v107
v_mfma_f32_16x16x32_bf16 v[164:167], a[44:47], v[40:43], 0
Rb 28,10,27648
s_waitcnt lgkmcnt(8)
Mp 164,40,32
v_exp_f32_e32 v52, v100
v_add_f32_e32 v14, v14, v24
v_mfma_f32_16x16x32_bf16 v[176:179], a[48:51], v[32:35], 0
Rb 32,10,12288
Cv 50,50,24
Mp 176,52,40
v_exp_f32_e32 v26, v101
v_mfma_f32_16x16x32_bf16 v[184:187], a[52:55], v[44:47], 0
Rb 36,10,28672
v_add_f32_e32 v14, v14, v51
Mp 184,48,36
buffer_load_dwordx4 v2, s[20:23], s55 offen offset:3072 sc1 lds
v_mfma_f32_16x16x32_bf16 v[188:191], a[56:59], v[36:39], 0
Rb 40,10,13312
v_exp_f32_e32 v53, v102
Mp 188,60,44
v_exp_f32_e32 v27, v103
v_mfma_f32_16x16x32_bf16 v[180:183], a[60:63], v[40:43], 0
Rb 44,10,29696
s_waitcnt lgkmcnt(8)
Mp 180,56,32
v_exp_f32_e32 v54, v108
v_mfma_f32_16x16x32_bf16 v[192:195], a[0:3], v[32:35], 0
Rb 48,10,14336
v_add_f32_e32 v14, v14, v25
Mp 192,4,40
)" R"(
v_exp_f32_e32 v28, v109
Cv 51,51,25
v_mfma_f32_16x16x32_bf16 v[200:203], a[4:7], v[44:47], 0
Rb 52,10,30720
s_add_u32 m0, s36, 36864
Mp 200,0,36
buffer_load_dwordx4 v3, s[20:23], s55 offen offset:0 sc1 lds
v_mfma_f32_16x16x32_bf16 v[204:207], a[8:11], v[36:39], 0
Rb 56,10,15360
v_add_f32_e32 v15, v52, v26
Mp 204,12,44
v_exp_f32_e32 v55, v110
v_mfma_f32_16x16x32_bf16 v[196:199], a[12:15], v[40:43], 0
Rb 60,10,31744
s_waitcnt lgkmcnt(8)
Mp 196,8,32
v_exp_f32_e32 v29, v111
Cv 52,52,26
v_mfma_f32_16x16x32_bf16 v[208:211], a[16:19], v[32:35], 0
v_add_f32_e32 v15, v15, v53
v_add_f32_e32 v15, v15, v27
Mp 208,20,40
Cv 53,53,27
v_add_f32_e32 v15, v15, v54
v_mfma_f32_16x16x32_bf16 v[216:219], a[20:23], v[44:47], 0
v_add_f32_e32 v15, v15, v28
Cv 54,54,28
Mp 216,16,36
buffer_load_dwordx4 v3, s[20:23], s55 offen offset:1024 sc1 lds
v_mfma_f32_16x16x32_bf16 v[220:223], a[24:27], v[36:39], 0
v_add_f32_e32 v15, v15, v55
v_add_f32_e32 v15, v15, v29
Cv 55,55,29
Mp 220,28,44
s_sub_u32 s57, s57, 1
v_accvgpr_read_b32 v9, a192
v_mfma_f32_16x16x32_bf16 v[212:215], a[28:31], v[40:43], 0
v_accvgpr_read_b32 v11, a193
Cv 9,9,11
Mp 212,24,32
s_waitcnt vmcnt(6) lgkmcnt(0)
s_barrier
buffer_store_dword v6, v7, s[20:23], s49 offen offset:1024 sc0 sc1
v_mfma_f32_16x16x32_bf16 v[224:227], a[32:35], v[32:35], 0
Rb 0,1,0
v_accvgpr_write_b32 a144, v9
Mp 224,36,40
Rb 4,1,1024
v_accvgpr_read_b32 v9, a194
v_accvgpr_read_b32 v11, a195
v_mfma_f32_16x16x32_bf16 v[232:235], a[36:39], v[44:47], 0
Rb 8,1,2048
Cv 9,9,11
Mp 232,32,36
Rb 12,1,3072
buffer_load_dwordx4 v3, s[20:23], s55 offen offset:2048 sc1 lds
v_mfma_f32_16x16x32_bf16 v[236:239], a[40:43], v[36:39], 0
Rb 16,1,8192
v_accvgpr_write_b32 a145, v9
Mp 236,44,44
Rb 20,1,9216
v_accvgpr_read_b32 v9, a196
v_mfma_f32_16x16x32_bf16 v[228:231], a[44:47], v[40:43], 0
Rb 24,1,10240
v_accvgpr_read_b32 v11, a197
)" R"(
Mp 228,40,32
Rb 28,1,11264
Cv 9,9,11
v_accvgpr_write_b32 a146, v9
v_mfma_f32_16x16x32_bf16 v[240:243], a[48:51], v[32:35], 0
Rb 32,1,4096
v_accvgpr_read_b32 v9, a198
Mp 240,52,40
Rb 36,1,5120
v_accvgpr_read_b32 v11, a199
v_mfma_f32_16x16x32_bf16 v[248:251], a[52:55], v[44:47], 0
Rb 40,1,6144
Cv 9,9,11
Mp 248,48,36
Rb 44,1,7168
buffer_load_dwordx4 v3, s[20:23], s55 offen offset:3072 sc1 lds
v_mfma_f32_16x16x32_bf16 v[252:255], a[56:59], v[36:39], 0
v_cmp_eq_u32_e32 vcc, s3, v4
s_andn2_b64 s[28:29], -1, vcc
s_cbranch_scc1 .Lzsp19
.Lzok19:
Mp 252,60,44
v_accvgpr_write_b32 a147, v9
v_accvgpr_read_b32 v9, a200
v_mfma_f32_16x16x32_bf16 v[244:247], a[60:63], v[40:43], 0
v_accvgpr_read_b32 v11, a201
s_waitcnt lgkmcnt(8)
Mp 244,56,32
Cv 9,9,11
v_accvgpr_write_b32 a148, v9
v_mfma_f32_16x16x32_bf16 v[64:67], a[0:3], a[64:67], v[16:19]
Rb 48,1,12288
v_add_u32 v9, 4, v0
Mv 64,4,72
v_xor_b32 v9, v9, v0
v_and_b32 v9, 32, v9
Mv 64,8,80
Rb 52,1,13312
s_add_u32 m0, s36, 98304
Mv 64,12,88
buffer_load_dwordx4 v2, s[20:23], s56 offen offset:0 sc1 lds
v_mfma_f32_16x16x32_bf16 v[68:71], a[12:15], a[92:95], v[20:23]
Rb 56,1,14336
v_lshlrev_b32 v9, 22, v9
Mv 68,8,84
v_and_or_b32 v9, v0, 32, v9
v_mul_lo_u32 v9, v9, 12
Mv 68,4,76
Rb 60,1,15360
s_waitcnt lgkmcnt(8)
Mv 68,0,68
buffer_load_dwordx4 v2, s[20:23], s56 offen offset:1024 sc1 lds
Mv 68,16,100
Rb 0,1,16384
v_add_u32 v9, v9, v1
Mv 68,20,108
v_exp_f32_e32 v56, v112
Mv 68,24,116
Rb 4,1,17408
v_exp_f32_e32 v24, v113
Mv 68,28,124
buffer_load_dwordx4 v2, s[20:23], s56 offen offset:2048 sc1 lds
Mv 64,28,120
Rb 8,1,18432
)" R"(
v_exp_f32_e32 v57, v114
Mv 64,24,112
v_exp_f32_e32 v25, v115
Mv 64,20,104
Rb 12,1,19456
v_add_f32_e32 v14, v14, v56
s_waitcnt lgkmcnt(8)
Mv 64,16,96
buffer_load_dwordx4 v2, s[20:23], s56 offen offset:3072 sc1 lds
v_add_f32_e32 v14, v14, v24
v_mfma_f32_16x16x32_bf16 v[72:75], a[32:35], a[64:67], v[16:19]
Rb 16,1,24576
Cv 56,56,24
Mv 72,36,72
v_exp_f32_e32 v58, v120
v_add_f32_e32 v14, v14, v57
Mv 72,40,80
Rb 20,1,25600
s_add_u32 m0, s36, 102400
Mv 72,44,88
buffer_load_dwordx4 v3, s[20:23], s56 offen offset:0 sc1 lds
v_add_f32_e32 v14, v14, v25
v_mfma_f32_16x16x32_bf16 v[76:79], a[44:47], a[92:95], v[20:23]
Rb 24,1,26624
Cv 57,57,25
Mv 76,40,84
v_exp_f32_e32 v26, v121
v_add_f32_e32 v14, v14, v58
Mv 76,36,76
Rb 28,1,27648
s_waitcnt lgkmcnt(8)
Mv 76,32,68
buffer_load_dwordx4 v3, s[20:23], s56 offen offset:1024 sc1 lds
v_add_f32_e32 v14, v14, v26
Mv 76,48,100
Rb 32,1,20480
v_exp_f32_e32 v59, v122
Mv 76,52,108
v_exp_f32_e32 v27, v123
Mv 76,56,116
Rb 36,1,21504
v_exp_f32_e32 v60, v116
Mv 76,60,124
buffer_load_dwordx4 v3, s[20:23], s56 offen offset:2048 sc1 lds
Mv 72,60,120
Rb 40,1,22528
v_exp_f32_e32 v28, v117
Mv 72,56,112
v_exp_f32_e32 v61, v118
Mv 72,52,104
Rb 44,1,23552
Cv 58,58,26
s_waitcnt lgkmcnt(8)
Mv 72,48,96
buffer_load_dwordx4 v3, s[20:23], s56 offen offset:3072 sc1 lds
v_add_f32_e32 v14, v14, v59
v_mfma_f32_16x16x32_bf16 v[80:83], a[0:3], a[64:67], v[16:19]
Rb 48,1,28672
s_xor_b32 m0, s82, 0
Mv 80,4,72
s_add_u32 s56, s56, 0x10000
s_add_u32 s92, s83, 448
v_add_f32_e32 v14, v14, v27
Mv 80,8,80
)" R"(
Rb 52,1,29696
Cv 59,59,27
Mv 80,12,88
buffer_load_dwordx4 v9, s[24:27], s92 offen offset:0 lds
v_add_f32_e32 v15, v15, v60
v_mfma_f32_16x16x32_bf16 v[84:87], a[12:15], a[92:95], v[20:23]
Rb 56,1,30720
s_add_u32 s92, s83, 1216
Mv 84,8,84
v_exp_f32_e32 v29, v119
v_add_f32_e32 v15, v15, v28
Mv 84,4,76
Rb 60,1,31744
s_waitcnt lgkmcnt(8)
Mv 84,0,68
buffer_load_dwordx4 v9, s[24:27], s92 offen offset:1024 lds
s_add_u32 s92, s83, 1984
Mv 84,16,100
v_exp_f32_e32 v62, v124
Cv 60,60,28
Mv 84,20,108
v_exp_f32_e32 v24, v125
Mv 84,24,116
v_exp_f32_e32 v63, v126
v_add_f32_e32 v15, v15, v61
Mv 84,28,124
buffer_load_dwordx4 v9, s[24:27], s92 offen offset:2048 lds
Mv 80,28,120
s_add_u32 s92, s83, 2752
v_exp_f32_e32 v25, v127
Mv 80,24,112
v_add_f32_e32 v15, v15, v29
Cv 61,61,29
Mv 80,20,104
buffer_load_dwordx4 v9, s[24:27], s92 offen offset:3072 lds
v_add_f32_e32 v15, v15, v62
Mv 80,16,96
s_xor_b32 m0, s82, 16384
s_waitcnt lgkmcnt(0)
s_barrier
v_mfma_f32_16x16x32_bf16 v[88:91], a[32:35], a[64:67], v[16:19]
Rb 0,8,0
s_add_u32 s92, s83, 0
Mv 88,36,72
Rb 4,8,16384
v_add_f32_e32 v15, v15, v24
Cv 62,62,24
Mv 88,40,80
Rb 8,8,1024
v_add_f32_e32 v15, v15, v63
Mv 88,44,88
Rb 12,8,17408
v_add_f32_e32 v15, v15, v25
Cv 63,63,25
v_mfma_f32_16x16x32_bf16 v[92:95], a[44:47], a[92:95], v[20:23]
Rb 16,8,2048
buffer_load_dwordx4 v9, s[24:27], s92 offen offset:0 lds
Mv 92,40,84
Rb 20,8,18432
s_add_u32 s92, s83, 768
v_max_f32_e32 v11, v14, v15
Mv 92,36,76
Rb 24,8,3072
v_cmp_nge_f32_e32 vcc, s62, v11
Mv 92,32,68
)" R"(
Rb 28,8,19456
s_add_u32 s55, s55, 0x10000
Mv 92,48,100
Rb 32,8,4096
s_cmp_eq_u32 s57, 2
s_cselect_b32 s55, s59, s55
Mv 92,52,108
Rb 36,8,20480
buffer_load_dwordx4 v9, s[24:27], s92 offen offset:1024 lds
Mv 92,56,116
Rb 40,8,5120
s_add_u32 s92, s83, 1536
Mv 92,60,124
Rb 44,8,21504
Mv 88,60,120
buffer_load_dwordx4 v9, s[24:27], s92 offen offset:2048 lds
s_add_u32 s92, s83, 2304
Mv 88,56,112
buffer_load_dwordx4 v9, s[24:27], s92 offen offset:3072 lds
Mv 88,52,104
s_waitcnt lgkmcnt(8)
s_cbranch_vccnz .Lzfx7
.Lzfr7:
Mv 88,48,96
v_add_f32_e32 v12, v12, v14
v_add_f32_e32 v13, v13, v15
Mp 128,0,48
Rb 48,8,6144
Mp 128,4,56
s_cmp_le_i32 s57, s58
s_cbranch_scc1 .Lzmk6
.Lzmr6:
Mp 136,4,60
Rb 52,8,22528
s_add_u32 m0, s36, 0
Mp 136,0,52
buffer_load_dwordx4 v2, s[20:23], s55 offen offset:0 sc1 lds
Mp 140,8,52
Rb 56,8,7168
v_exp_f32_e32 v32, v64
Mp 140,12,60
buffer_store_dwordx4 a[224:227], v2, s[20:23], s47 offen offset:0 sc0 sc1
Mp 132,12,56
Rb 60,8,23552
s_waitcnt lgkmcnt(8)
Mp 132,8,48
v_exp_f32_e32 v26, v65
Mp 144,16,48
Rb 0,8,8192
v_add_f32_e32 v14, v32, v26
Mp 144,20,56
v_exp_f32_e32 v33, v66
Cv 32,32,26
Mp 152,20,60
Rb 4,8,24576
v_add_f32_e32 v14, v14, v33
Mp 152,16,52
buffer_load_dwordx4 v2, s[20:23], s55 offen offset:1024 sc1 lds
Mp 156,24,52
Rb 8,8,9216
v_exp_f32_e32 v27, v67
Mp 156,28,60
v_exp_f32_e32 v34, v72
Mp 148,28,56
)" R"(
Rb 12,8,25600
s_waitcnt lgkmcnt(8)
Mp 148,24,48
v_exp_f32_e32 v28, v73
v_add_f32_e32 v14, v14, v27
Mp 160,32,48
Rb 16,8,10240
Cv 33,33,27
Mp 160,36,56
v_exp_f32_e32 v35, v74
v_add_f32_e32 v14, v14, v34
Mp 168,36,60
Rb 20,8,26624
v_add_f32_e32 v14, v14, v28
Mp 168,32,52
buffer_load_dwordx4 v2, s[20:23], s55 offen offset:2048 sc1 lds
Mp 172,40,52
Rb 24,8,11264
v_exp_f32_e32 v29, v75
Mp 172,44,60
buffer_store_dwordx4 a[228:231], v2, s[20:23], s47 offen offset:1024 sc0 sc1
Mp 164,44,56
Rb 28,8,27648
s_waitcnt lgkmcnt(8)
Mp 164,40,48
v_exp_f32_e32 v36, v68
Cv 34,34,28
Mp 176,48,48
Rb 32,8,12288
v_add_f32_e32 v14, v14, v35
Mp 176,52,56
v_exp_f32_e32 v24, v69
v_add_f32_e32 v14, v14, v29
Mp 184,52,60
Rb 36,8,28672
Cv 35,35,29
Mp 184,48,52
buffer_load_dwordx4 v2, s[20:23], s55 offen offset:3072 sc1 lds
Mp 188,56,52
Rb 40,8,13312
v_exp_f32_e32 v37, v70
Mp 188,60,60
v_exp_f32_e32 v25, v71
Mp 180,60,56
Rb 44,8,29696
s_waitcnt lgkmcnt(8)
Mp 180,56,48
v_exp_f32_e32 v38, v76
v_add_f32_e32 v15, v36, v24
Mp 192,0,48
Rb 48,8,14336
Cv 36,36,24
Mp 192,4,56
v_exp_f32_e32 v26, v77
v_add_f32_e32 v15, v15, v37
Mp 200,4,60
Rb 52,8,30720
s_add_u32 m0, s36, 4096
Mp 200,0,52
buffer_load_dwordx4 v3, s[20:23], s55 offen offset:0 sc1 lds
Mp 204,8,52
Rb 56,8,15360
v_exp_f32_e32 v39, v78
Mp 204,12,60
)" R"(
buffer_store_dwordx4 a[232:235], v2, s[20:23], s47 offen offset:2048 sc0 sc1
Mp 196,12,56
Rb 60,8,31744
s_waitcnt lgkmcnt(8)
Mp 196,8,48
v_exp_f32_e32 v27, v79
v_add_f32_e32 v15, v15, v25
Mp 208,16,48
Cv 37,37,25
v_add_f32_e32 v15, v15, v38
Mp 208,20,56
v_add_f32_e32 v15, v15, v26
Cv 38,38,26
v_add_f32_e32 v15, v15, v39
Mp 216,20,60
v_add_f32_e32 v15, v15, v27
Cv 39,39,27
Mp 216,16,52
buffer_load_dwordx4 v3, s[20:23], s55 offen offset:1024 sc1 lds
Mp 220,24,52
s_add_u32 s50, s50, 1024
s_min_u32 s50, s50, s60
s_sub_u32 s57, s57, 1
Mp 220,28,60
v_accvgpr_read_b32 v9, a202
v_accvgpr_read_b32 v11, a203
Mp 212,28,56
Cv 9,9,11
v_accvgpr_write_b32 a149, v9
Mp 212,24,48
v_accvgpr_read_b32 v9, a204
s_waitcnt vmcnt(25) lgkmcnt(0)
s_barrier
Mp 224,32,48
Rb 0,1,32768
v_accvgpr_read_b32 v11, a205
Mp 224,36,56
Rb 4,1,33792
Cv 9,9,11
v_accvgpr_write_b32 a150, v9
Mp 232,36,60
Rb 8,1,34816
v_accvgpr_read_b32 v9, a206
Mp 232,32,52
Rb 12,1,35840
buffer_load_dwordx4 v3, s[20:23], s55 offen offset:2048 sc1 lds
Mp 236,40,52
Rb 16,1,40960
v_accvgpr_read_b32 v11, a207
Cv 9,9,11
Mp 236,44,60
Rb 20,1,41984
buffer_store_dwordx4 a[236:239], v2, s[20:23], s47 offen offset:3072 sc0 sc1
Mp 228,44,56
Rb 24,1,43008
v_accvgpr_write_b32 a151, v9
Mp 228,40,48
Rb 28,1,44032
v_accvgpr_read_b32 v9, a208
v_accvgpr_read_b32 v11, a209
Mp 240,48,48
Rb 32,1,36864
Cv 9,9,11
Mp 240,52,56
Rb 36,1,37888
)" R"(
v_accvgpr_write_b32 a152, v9
v_accvgpr_read_b32 v9, a210
Mp 248,52,60
Rb 40,1,38912
v_accvgpr_read_b32 v11, a211
Mp 248,48,52
Rb 44,1,39936
buffer_load_dwordx4 v3, s[20:23], s55 offen offset:3072 sc1 lds
Mp 252,56,52
Cv 9,9,11
v_accvgpr_write_b32 a153, v9
v_accvgpr_read_b32 v9, a212
Mp 252,60,60
v_accvgpr_read_b32 v11, a213
Cv 9,9,11
Mp 244,60,56
v_accvgpr_write_b32 a154, v9
s_waitcnt lgkmcnt(8)
Mp 244,56,48
v_mfma_f32_16x16x32_bf16 v[96:99], a[0:3], a[64:67], v[16:19]
Rb 48,1,45056
buffer_load_dword v4, v5, s[20:23], s50 offen offset:0 sc0 sc1
Mv 96,4,72
v_exp_f32_e32 v40, v80
Mv 96,8,80
Rb 52,1,46080
s_add_u32 m0, s36, 65536
Mv 96,12,88
buffer_load_dwordx4 v2, s[20:23], s56 offen offset:0 sc1 lds
v_mfma_f32_16x16x32_bf16 v[100:103], a[12:15], a[92:95], v[20:23]
Rb 56,1,47104
v_add_f32_e32 v14, v14, v40
Mv 100,8,84
buffer_store_dwordx4 a[240:243], v3, s[20:23], s47 offen offset:0 sc0 sc1
Mv 100,4,76
Rb 60,1,48128
s_waitcnt lgkmcnt(8)
Mv 100,0,68
v_exp_f32_e32 v28, v81
Mv 100,16,100
Rb 0,1,49152
v_add_f32_e32 v14, v14, v28
Mv 100,20,108
v_exp_f32_e32 v41, v82
Mv 100,24,116
Rb 4,1,50176
Cv 40,40,28
Mv 100,28,124
buffer_load_dwordx4 v2, s[20:23], s56 offen offset:1024 sc1 lds
Mv 96,28,120
Rb 8,1,51200
v_add_f32_e32 v14, v14, v41
Mv 96,24,112
v_exp_f32_e32 v29, v83
Mv 96,20,104
Rb 12,1,52224
s_waitcnt lgkmcnt(8)
Mv 96,16,96
v_exp_f32_e32 v42, v88
v_add_f32_e32 v14, v14, v29
v_mfma_f32_16x16x32_bf16 v[104:107], a[32:35], a[64:67], v[16:19]
Rb 16,1,57344
Cv 41,41,29
.LzH13:
)" R"(
s_cbranch_execz .LzH21
Mv 104,36,72
v_exp_f32_e32 v24, v89
Mv 104,40,80
Rb 20,1,58368
v_add_f32_e32 v14, v14, v42
Mv 104,44,88
buffer_load_dwordx4 v2, s[20:23], s56 offen offset:2048 sc1 lds
v_mfma_f32_16x16x32_bf16 v[108:111], a[44:47], a[92:95], v[20:23]
Rb 24,1,59392
v_add_f32_e32 v14, v14, v24
Mv 108,40,84
buffer_store_dwordx4 a[244:247], v3, s[20:23], s47 offen offset:1024 sc0 sc1
Mv 108,36,76
Rb 28,1,60416
s_waitcnt lgkmcnt(8)
Mv 108,32,68
v_exp_f32_e32 v43, v90
Mv 108,48,100
Rb 32,1,53248
Cv 42,42,24
Mv 108,52,108
v_exp_f32_e32 v25, v91
Mv 108,56,116
Rb 36,1,54272
v_add_f32_e32 v14, v14, v43
Mv 108,60,124
buffer_load_dwordx4 v2, s[20:23], s56 offen offset:3072 sc1 lds
Mv 104,60,120
Rb 40,1,55296
v_add_f32_e32 v14, v14, v25
Mv 104,56,112
v_exp_f32_e32 v44, v84
Mv 104,52,104
Rb 44,1,56320
s_waitcnt lgkmcnt(8)
Mv 104,48,96
v_exp_f32_e32 v26, v85
Cv 43,43,25
v_mfma_f32_16x16x32_bf16 v[112:115], a[0:3], a[64:67], v[16:19]
Rb 48,1,61440
v_add_f32_e32 v15, v15, v44
Mv 112,4,72
v_exp_f32_e32 v45, v86
Mv 112,8,80
Rb 52,1,62464
s_add_u32 m0, s36, 69632
Mv 112,12,88
buffer_load_dwordx4 v3, s[20:23], s56 offen offset:0 sc1 lds
v_mfma_f32_16x16x32_bf16 v[116:119], a[12:15], a[92:95], v[20:23]
Rb 56,1,63488
v_add_f32_e32 v15, v15, v26
Mv 116,8,84
buffer_store_dwordx4 a[248:251], v3, s[20:23], s47 offen offset:2048 sc0 sc1
Mv 116,4,76
Rb 60,1,64512
s_waitcnt lgkmcnt(8)
Mv 116,0,68
v_exp_f32_e32 v27, v87
Mv 116,16,100
v_exp_f32_e32 v46, v92
Mv 116,20,108
v_exp_f32_e32 v28, v93
Mv 116,24,116
)" R"(
v_exp_f32_e32 v47, v94
Mv 116,28,124
buffer_load_dwordx4 v3, s[20:23], s56 offen offset:1024 sc1 lds
Mv 112,28,120
v_exp_f32_e32 v29, v95
Mv 112,24,112
Cv 44,44,26
v_add_f32_e32 v15, v15, v45
Mv 112,20,104
v_add_f32_e32 v15, v15, v27
Cv 45,45,27
Mv 112,16,96
v_add_f32_e32 v15, v15, v46
s_waitcnt vmcnt(30) lgkmcnt(0)
s_barrier
v_mfma_f32_16x16x32_bf16 v[120:123], a[32:35], a[64:67], v[16:19]
Rb 0,8,32768
v_add_f32_e32 v15, v15, v28
Mv 120,36,72
Rb 4,8,49152
Cv 46,46,28
Mv 120,40,80
Rb 8,8,33792
v_add_f32_e32 v15, v15, v47
Mv 120,44,88
Rb 12,8,50176
buffer_load_dwordx4 v3, s[20:23], s56 offen offset:2048 sc1 lds
v_mfma_f32_16x16x32_bf16 v[124:127], a[44:47], a[92:95], v[20:23]
Rb 16,8,34816
v_add_f32_e32 v15, v15, v29
Mv 124,40,84
Rb 20,8,51200
buffer_store_dwordx4 a[252:255], v3, s[20:23], s47 offen offset:3072 sc0 sc1
Mv 124,36,76
Rb 24,8,35840
Cv 47,47,29
Mv 124,32,68
Rb 28,8,52224
v_max_f32_e32 v11, v14, v15
Mv 124,48,100
Rb 32,8,36864
v_cmp_nge_f32_e32 vcc, s62, v11
Mv 124,52,108
Rb 36,8,53248
s_add_u32 s55, s55, 0x10000
Mv 124,56,116
Rb 40,8,37888
Mv 124,60,124
Rb 44,8,54272
buffer_load_dwordx4 v3, s[20:23], s56 offen offset:3072 sc1 lds
Mv 120,60,120
s_cmp_eq_u32 s57, 2
s_cselect_b32 s55, s59, s55
Mv 120,56,112
s_add_u32 s56, s56, 0x10000
Mv 120,52,104
s_waitcnt lgkmcnt(8)
s_cbranch_vccnz .Lzfx8
.Lzfr8:
Mv 120,48,96
v_add_f32_e32 v12, v12, v14
v_add_f32_e32 v13, v13, v15
Mp 128,0,32
Rb 48,8,38912
Mp 128,4,40
)" R"(
s_cmp_le_i32 s57, s58
s_cbranch_scc1 .Lzmk7
.Lzmr7:
Mp 136,4,44
Rb 52,8,55296
s_add_u32 m0, s36, 32768
Mp 136,0,36
buffer_load_dwordx4 v2, s[20:23], s55 offen offset:0 sc1 lds
Mp 140,8,36
Rb 56,8,39936
Mp 140,12,44
v_exp_f32_e32 v48, v96
Mp 132,12,40
Rb 60,8,56320
s_waitcnt lgkmcnt(8)
Mp 132,8,32
v_exp_f32_e32 v24, v97
Mp 144,16,32
Rb 0,8,40960
v_add_f32_e32 v14, v48, v24
Mp 144,20,40
v_exp_f32_e32 v49, v98
Mp 152,20,44
Rb 4,8,57344
Cv 48,48,24
Mp 152,16,36
buffer_load_dwordx4 v2, s[20:23], s55 offen offset:1024 sc1 lds
Mp 156,24,36
Rb 8,8,41984
v_exp_f32_e32 v25, v99
Mp 156,28,44
v_exp_f32_e32 v50, v104
Mp 148,28,40
Rb 12,8,58368
s_waitcnt lgkmcnt(8)
Mp 148,24,32
v_exp_f32_e32 v26, v105
Mp 160,32,32
Rb 16,8,43008
v_add_f32_e32 v14, v14, v49
Mp 160,36,40
v_exp_f32_e32 v51, v106
v_add_f32_e32 v14, v14, v25
Mp 168,36,44
Rb 20,8,59392
Cv 49,49,25
Mp 168,32,36
buffer_load_dwordx4 v2, s[20:23], s55 offen offset:2048 sc1 lds
Mp 172,40,36
Rb 24,8,44032
v_add_f32_e32 v14, v14, v50
Mp 172,44,44
v_exp_f32_e32 v27, v107
Mp 164,44,40
Rb 28,8,60416
s_waitcnt lgkmcnt(8)
Mp 164,40,32
v_exp_f32_e32 v52, v100
v_add_f32_e32 v14, v14, v26
Mp 176,48,32
Rb 32,8,45056
Cv 50,50,26
Mp 176,52,40
v_exp_f32_e32 v28, v101
)" R"(
Mp 184,52,44
Rb 36,8,61440
v_add_f32_e32 v14, v14, v51
Mp 184,48,36
buffer_load_dwordx4 v2, s[20:23], s55 offen offset:3072 sc1 lds
Mp 188,56,36
Rb 40,8,46080
v_exp_f32_e32 v53, v102
Mp 188,60,44
v_exp_f32_e32 v29, v103
Mp 180,60,40
Rb 44,8,62464
s_waitcnt lgkmcnt(8)
Mp 180,56,32
v_exp_f32_e32 v54, v108
Mp 192,0,32
Rb 48,8,47104
v_add_f32_e32 v14, v14, v27
Mp 192,4,40
v_exp_f32_e32 v24, v109
Cv 51,51,27
Mp 200,4,44
Rb 52,8,63488
s_add_u32 m0, s36, 36864
Mp 200,0,36
buffer_load_dwordx4 v3, s[20:23], s55 offen offset:0 sc1 lds
Mp 204,8,36
Rb 56,8,48128
v_add_f32_e32 v15, v52, v28
Mp 204,12,44
v_exp_f32_e32 v55, v110
Mp 196,12,40
Rb 60,8,64512
s_waitcnt lgkmcnt(8)
Mp 196,8,32
v_exp_f32_e32 v25, v111
Cv 52,52,28
Mp 208,16,32
v_add_f32_e32 v15, v15, v53
v_add_f32_e32 v15, v15, v29
Mp 208,20,40
Cv 53,53,29
v_add_f32_e32 v15, v15, v54
Mp 216,20,44
v_add_f32_e32 v15, v15, v24
Cv 54,54,24
Mp 216,16,36
buffer_load_dwordx4 v3, s[20:23], s55 offen offset:1024 sc1 lds
Mp 220,24,36
v_add_f32_e32 v15, v15, v55
v_add_f32_e32 v15, v15, v25
Cv 55,55,25
Mp 220,28,44
s_sub_u32 s57, s57, 1
v_accvgpr_read_b32 v9, a214
Mp 212,28,40
v_accvgpr_read_b32 v11, a215
Cv 9,9,11
Mp 212,24,32
s_waitcnt vmcnt(7) lgkmcnt(0)
s_barrier
buffer_store_dword v6, v7, s[20:23], s49 offen offset:2048 sc0 sc1
Mp 224,32,32
Rb 0,1,0
v_accvgpr_write_b32 a155, v9
)" R"(
Mp 224,36,40
Rb 4,1,1024
v_accvgpr_read_b32 v9, a216
v_accvgpr_read_b32 v11, a217
Mp 232,36,44
Rb 8,1,2048
Cv 9,9,11
Mp 232,32,36
Rb 12,1,3072
buffer_load_dwordx4 v3, s[20:23], s55 offen offset:2048 sc1 lds
Mp 236,40,36
Rb 16,1,8192
v_accvgpr_write_b32 a156, v9
Mp 236,44,44
Rb 20,1,9216
v_accvgpr_read_b32 v9, a218
Mp 228,44,40
Rb 24,1,10240
v_accvgpr_read_b32 v11, a219
Mp 228,40,32
Rb 28,1,11264
Cv 9,9,11
v_accvgpr_write_b32 a157, v9
Mp 240,48,32
Rb 32,1,4096
v_accvgpr_read_b32 v9, a220
Mp 240,52,40
Rb 36,1,5120
v_accvgpr_read_b32 v11, a221
Mp 248,52,44
Rb 40,1,6144
Cv 9,9,11
Mp 248,48,36
Rb 44,1,7168
buffer_load_dwordx4 v3, s[20:23], s55 offen offset:3072 sc1 lds
Mp 252,56,36
v_cmp_eq_u32_e32 vcc, s3, v4
s_andn2_b64 s[28:29], -1, vcc
s_cbranch_scc1 .Lzsp20
.Lzok20:
Mp 252,60,44
v_accvgpr_write_b32 a158, v9
v_accvgpr_read_b32 v9, a222
Mp 244,60,40
v_accvgpr_read_b32 v11, a223
s_waitcnt lgkmcnt(8)
Mp 244,56,32
Cv 9,9,11
v_accvgpr_write_b32 a159, v9
v_mfma_f32_16x16x32_bf16 v[64:67], a[0:3], a[64:67], v[16:19]
Rb 48,1,12288
v_lshrrev_b32 v9, 2, v0
Mv 64,4,72
v_mul_u32_u24 v9, 448, v9
s_lshr_b32 s92, s33, 1
Mv 64,8,80
Rb 52,1,13312
s_add_u32 m0, s36, 98304
Mv 64,12,88
buffer_load_dwordx4 v2, s[20:23], s56 offen offset:0 sc1 lds
v_mfma_f32_16x16x32_bf16 v[68:71], a[12:15], a[92:95], v[20:23]
Rb 56,1,14336
s_mul_i32 s92, s92, 48
Mv 68,8,84
)" R"(
buffer_store_dwordx4 a[128:131], v2, s[20:23], s48 offen offset:0 sc0 sc1
Mv 68,4,76
Rb 60,1,15360
s_waitcnt lgkmcnt(8)
Mv 68,0,68
s_lshl3_add_u32 s92, s33, s92
v_add3_u32 v9, v9, s92, v10
Mv 68,16,100
Rb 0,1,16384
Mv 68,20,108
v_exp_f32_e32 v56, v112
Mv 68,24,116
Rb 4,1,17408
Rt 160,9,0
Mv 68,28,124
buffer_load_dwordx4 v2, s[20:23], s56 offen offset:1024 sc1 lds
Mv 64,28,120
Rb 8,1,18432
v_exp_f32_e32 v26, v113
Mv 64,24,112
v_exp_f32_e32 v57, v114
Mv 64,20,104
Rb 12,1,19456
s_waitcnt lgkmcnt(9)
Mv 64,16,96
v_exp_f32_e32 v27, v115
v_add_f32_e32 v14, v14, v56
v_mfma_f32_16x16x32_bf16 v[72:75], a[32:35], a[64:67], v[16:19]
Rb 16,1,24576
v_add_f32_e32 v14, v14, v26
Mv 72,36,72
v_exp_f32_e32 v58, v120
Mv 72,40,80
Rb 20,1,25600
Cv 56,56,26
Mv 72,44,88
buffer_load_dwordx4 v2, s[20:23], s56 offen offset:2048 sc1 lds
v_add_f32_e32 v14, v14, v57
v_mfma_f32_16x16x32_bf16 v[76:79], a[44:47], a[92:95], v[20:23]
Rb 24,1,26624
Rt 162,9,8192
Mv 76,40,84
v_exp_f32_e32 v28, v121
Mv 76,36,76
Rb 28,1,27648
v_add_f32_e32 v14, v14, v27
s_waitcnt lgkmcnt(10)
Mv 76,32,68
v_exp_f32_e32 v59, v122
Mv 76,48,100
Rb 32,1,20480
Cv 57,57,27
Mv 76,52,108
v_exp_f32_e32 v29, v123
v_add_f32_e32 v14, v14, v58
Mv 76,56,116
Rb 36,1,21504
v_add_f32_e32 v14, v14, v28
Mv 76,60,124
buffer_load_dwordx4 v2, s[20:23], s56 offen offset:3072 sc1 lds
Mv 72,60,120
Rb 40,1,22528
v_exp_f32_e32 v60, v116
Mv 72,56,112
)" R"(
buffer_store_dwordx4 a[132:135], v2, s[20:23], s48 offen offset:1024 sc0 sc1
Mv 72,52,104
Rb 44,1,23552
Rt 164,9,128
s_waitcnt lgkmcnt(10)
Mv 72,48,96
v_exp_f32_e32 v24, v117
Cv 58,58,28
v_mfma_f32_16x16x32_bf16 v[80:83], a[0:3], a[64:67], v[16:19]
Rb 48,1,28672
v_add_f32_e32 v14, v14, v59
Mv 80,4,72
v_exp_f32_e32 v61, v118
Mv 80,8,80
Rb 52,1,29696
s_add_u32 m0, s36, 102400
Mv 80,12,88
buffer_load_dwordx4 v3, s[20:23], s56 offen offset:0 sc1 lds
v_add_f32_e32 v14, v14, v29
v_mfma_f32_16x16x32_bf16 v[84:87], a[12:15], a[92:95], v[20:23]
Rb 56,1,30720
Cv 59,59,29
Mv 84,8,84
v_exp_f32_e32 v25, v119
Mv 84,4,76
Rb 60,1,31744
v_add_f32_e32 v15, v15, v60
s_waitcnt lgkmcnt(9)
Mv 84,0,68
v_exp_f32_e32 v62, v124
Mv 84,16,100
Rt 166,9,8320
v_add_f32_e32 v15, v15, v24
Mv 84,20,108
buffer_store_dwordx4 a[160:163], v1, s[20:23], s4 offen sc0 sc1
Cv 60,60,24
Mv 84,24,116
v_exp_f32_e32 v26, v125
Mv 84,28,124
buffer_load_dwordx4 v3, s[20:23], s56 offen offset:1024 sc1 lds
Mv 80,28,120
v_exp_f32_e32 v63, v126
v_add_f32_e32 v15, v15, v61
Mv 80,24,112
v_exp_f32_e32 v27, v127
Mv 80,20,104
v_add_f32_e32 v15, v15, v25
Cv 61,61,25
Mv 80,16,96
v_add_f32_e32 v15, v15, v62
s_waitcnt vmcnt(18) lgkmcnt(0)
s_barrier
v_mfma_f32_16x16x32_bf16 v[88:91], a[32:35], a[64:67], v[16:19]
Rb 0,8,0
v_add_f32_e32 v15, v15, v26
Mv 88,36,72
Rb 4,8,16384
Cv 62,62,26
Mv 88,40,80
Rb 8,8,1024
Rt 168,9,256
Mv 88,44,88
Rb 12,8,17408
buffer_load_dwordx4 v3, s[20:23], s56 offen offset:2048 sc1 lds
v_mfma_f32_16x16x32_bf16 v[92:95], a[44:47], a[92:95], v[20:23]
)" R"(
Rb 16,8,2048
v_add_f32_e32 v15, v15, v63
Mv 92,40,84
Rb 20,8,18432
buffer_store_dwordx4 a[136:139], v2, s[20:23], s48 offen offset:2048 sc0 sc1
Mv 92,36,76
Rb 24,8,3072
v_add_f32_e32 v15, v15, v27
Cv 63,63,27
Mv 92,32,68
Rb 28,8,19456
v_max_f32_e32 v11, v14, v15
Mv 92,48,100
Rb 32,8,4096
v_cmp_nge_f32_e32 vcc, s62, v11
Mv 92,52,108
Rb 36,8,20480
s_add_u32 s55, s55, 0x10000
Mv 92,56,116
Rb 40,8,5120
Mv 92,60,124
Rb 44,8,21504
buffer_load_dwordx4 v3, s[20:23], s56 offen offset:3072 sc1 lds
Mv 88,60,120
Rt 170,9,8448
s_cmp_eq_u32 s57, 2
s_cselect_b32 s55, s59, s55
Mv 88,56,112
s_add_u32 s56, s56, 0x10000
buffer_store_dwordx4 a[164:167], v1, s[20:23], s5 offen sc0 sc1
Mv 88,52,104
s_waitcnt lgkmcnt(9)
s_cbranch_vccnz .Lzfx9
.Lzfr9:
Mv 88,48,96
v_add_f32_e32 v12, v12, v14
v_add_f32_e32 v13, v13, v15
Mp 128,0,48
Rb 48,8,6144
Mp 128,4,56
s_cmp_le_i32 s57, s58
s_cbranch_scc1 .Lzmk8
.Lzmr8:
Mp 136,4,60
Rb 52,8,22528
s_add_u32 m0, s36, 0
Mp 136,0,52
buffer_load_dwordx4 v2, s[20:23], s55 offen offset:0 sc1 lds
Mp 140,8,52
Rb 56,8,7168
Mp 140,12,60
v_exp_f32_e32 v32, v64
Mp 132,12,56
Rb 60,8,23552
Rt 172,9,384
s_waitcnt lgkmcnt(10)
Mp 132,8,48
v_exp_f32_e32 v28, v65
Mp 144,16,48
Rb 0,8,8192
v_add_f32_e32 v14, v32, v28
Mp 144,20,56
v_exp_f32_e32 v33, v66
Cv 32,32,28
)" R"(
Mp 152,20,60
Rb 4,8,24576
v_add_f32_e32 v14, v14, v33
Mp 152,16,52
buffer_load_dwordx4 v2, s[20:23], s55 offen offset:1024 sc1 lds
Mp 156,24,52
Rb 8,8,9216
Mp 156,28,60
buffer_store_dwordx4 a[140:143], v2, s[20:23], s48 offen offset:3072 sc0 sc1
Mp 148,28,56
Rb 12,8,25600
s_waitcnt lgkmcnt(10)
Mp 148,24,48
v_exp_f32_e32 v29, v67
Mp 160,32,48
Rb 16,8,10240
Rt 174,9,8576
Mp 160,36,56
v_exp_f32_e32 v34, v72
v_add_f32_e32 v14, v14, v29
Mp 168,36,60
Rb 20,8,26624
Cv 33,33,29
Mp 168,32,52
buffer_load_dwordx4 v2, s[20:23], s55 offen offset:2048 sc1 lds
Mp 172,40,52
Rb 24,8,11264
v_add_f32_e32 v14, v14, v34
Mp 172,44,60
v_exp_f32_e32 v24, v73
Mp 164,44,56
Rb 28,8,27648
s_waitcnt lgkmcnt(10)
Mp 164,40,48
buffer_store_dwordx4 a[168:171], v1, s[20:23], s6 offen sc0 sc1
Mp 176,48,48
Rb 32,8,12288
v_add_f32_e32 v14, v14, v24
Mp 176,52,56
v_exp_f32_e32 v35, v74
Cv 34,34,24
Mp 184,52,60
Rb 36,8,28672
Rt 176,9,16384
Mp 184,48,52
buffer_load_dwordx4 v2, s[20:23], s55 offen offset:3072 sc1 lds
Mp 188,56,52
Rb 40,8,13312
v_add_f32_e32 v14, v14, v35
Mp 188,60,60
v_exp_f32_e32 v25, v75
Mp 180,60,56
Rb 44,8,29696
s_waitcnt lgkmcnt(10)
Mp 180,56,48
v_exp_f32_e32 v36, v68
Mp 192,0,48
Rb 48,8,14336
v_add_f32_e32 v14, v14, v25
Mp 192,4,56
v_exp_f32_e32 v26, v69
Cv 35,35,25
Mp 200,4,60
Rb 52,8,30720
)" R"(
s_add_u32 m0, s36, 4096
Mp 200,0,52
buffer_load_dwordx4 v3, s[20:23], s55 offen offset:0 sc1 lds
Mp 204,8,52
Rb 56,8,15360
Rt 178,9,24576
Mp 204,12,60
buffer_store_dwordx4 a[144:147], v3, s[20:23], s48 offen offset:0 sc0 sc1
Mp 196,12,56
Rb 60,8,31744
s_waitcnt lgkmcnt(10)
Mp 196,8,48
v_exp_f32_e32 v37, v70
Mp 208,16,48
v_exp_f32_e32 v27, v71
Mp 208,20,56
v_exp_f32_e32 v38, v76
v_add_f32_e32 v15, v36, v26
.LzH14:
s_cbranch_execz .LzH22
Mp 216,20,60
v_exp_f32_e32 v28, v77
Mp 216,16,52
buffer_load_dwordx4 v3, s[20:23], s55 offen offset:1024 sc1 lds
Mp 220,24,52
s_add_u32 s50, s50, 1024
s_min_u32 s50, s50, s60
Mp 220,28,60
buffer_store_dwordx4 a[172:175], v1, s[20:23], s7 offen sc0 sc1
Mp 212,28,56
Rt 180,9,16512
Cv 36,36,26
Mp 212,24,48
s_waitcnt vmcnt(23) lgkmcnt(1)
s_barrier
Mp 224,32,48
Rb 0,1,32768
v_add_f32_e32 v15, v15, v37
Mp 224,36,56
Rb 4,1,33792
v_exp_f32_e32 v39, v78
Mp 232,36,60
Rb 8,1,34816
v_add_f32_e32 v15, v15, v27
Mp 232,32,52
Rb 12,1,35840
buffer_load_dwordx4 v3, s[20:23], s55 offen offset:2048 sc1 lds
Mp 236,40,52
Rb 16,1,40960
Cv 37,37,27
Mp 236,44,60
Rb 20,1,41984
v_add_f32_e32 v15, v15, v38
Mp 228,44,56
Rb 24,1,43008
v_add_f32_e32 v15, v15, v28
Mp 228,40,48
Rb 28,1,44032
Cv 38,38,28
Mp 240,48,48
Rb 32,1,36864
Rt 182,9,24704
Mp 240,52,56
Rb 36,1,37888
v_exp_f32_e32 v29, v79
)" R"(
Mp 248,52,60
Rb 40,1,38912
v_add_f32_e32 v15, v15, v39
Mp 248,48,52
Rb 44,1,39936
buffer_load_dwordx4 v3, s[20:23], s55 offen offset:3072 sc1 lds
Mp 252,56,52
v_add_f32_e32 v15, v15, v29
Cv 39,39,29
Mp 252,60,60
buffer_store_dwordx4 a[148:151], v3, s[20:23], s48 offen offset:1024 sc0 sc1
Mp 244,60,56
s_sub_u32 s57, s57, 1
s_waitcnt lgkmcnt(9)
Mp 244,56,48
v_mfma_f32_16x16x32_bf16 v[96:99], a[0:3], a[64:67], v[16:19]
Rb 48,1,45056
buffer_load_dword v4, v5, s[20:23], s50 offen offset:0 sc0 sc1
Mv 96,4,72
v_exp_f32_e32 v40, v80
Mv 96,8,80
Rb 52,1,46080
s_add_u32 m0, s36, 65536
Rt 184,9,16640
Mv 96,12,88
buffer_load_dwordx4 v2, s[20:23], s56 offen offset:0 sc1 lds
v_mfma_f32_16x16x32_bf16 v[100:103], a[12:15], a[92:95], v[20:23]
Rb 56,1,47104
v_add_f32_e32 v14, v14, v40
Mv 100,8,84
v_exp_f32_e32 v24, v81
Mv 100,4,76
Rb 60,1,48128
s_waitcnt lgkmcnt(10)
Mv 100,0,68
buffer_store_dwordx4 a[176:179], v1, s[20:23], s8 offen sc0 sc1
v_add_f32_e32 v14, v14, v24
Mv 100,16,100
Rb 0,1,49152
Cv 40,40,24
Mv 100,20,108
v_exp_f32_e32 v41, v82
Mv 100,24,116
Rb 4,1,50176
v_add_f32_e32 v14, v14, v41
Mv 100,28,124
buffer_load_dwordx4 v2, s[20:23], s56 offen offset:1024 sc1 lds
Mv 96,28,120
Rb 8,1,51200
Rt 186,9,24832
Mv 96,24,112
v_exp_f32_e32 v25, v83
Mv 96,20,104
Rb 12,1,52224
s_waitcnt lgkmcnt(10)
Mv 96,16,96
v_exp_f32_e32 v42, v88
v_add_f32_e32 v14, v14, v25
v_mfma_f32_16x16x32_bf16 v[104:107], a[32:35], a[64:67], v[16:19]
Rb 16,1,57344
Cv 41,41,25
Mv 104,36,72
v_exp_f32_e32 v26, v89
Mv 104,40,80
)" R"(
Rb 20,1,58368
v_add_f32_e32 v14, v14, v42
Mv 104,44,88
buffer_load_dwordx4 v2, s[20:23], s56 offen offset:2048 sc1 lds
v_mfma_f32_16x16x32_bf16 v[108:111], a[44:47], a[92:95], v[20:23]
Rb 24,1,59392
v_add_f32_e32 v14, v14, v26
Mv 108,40,84
buffer_store_dwordx4 a[152:155], v3, s[20:23], s48 offen offset:2048 sc0 sc1
Mv 108,36,76
Rb 28,1,60416
Rt 188,9,16768
s_waitcnt lgkmcnt(10)
Mv 108,32,68
v_exp_f32_e32 v43, v90
Cv 42,42,26
Mv 108,48,100
Rb 32,1,53248
v_add_f32_e32 v14, v14, v43
Mv 108,52,108
v_exp_f32_e32 v27, v91
Mv 108,56,116
Rb 36,1,54272
v_add_f32_e32 v14, v14, v27
Mv 108,60,124
buffer_load_dwordx4 v2, s[20:23], s56 offen offset:3072 sc1 lds
Mv 104,60,120
Rb 40,1,55296
Cv 43,43,27
Mv 104,56,112
buffer_store_dwordx4 a[180:183], v1, s[20:23], s9 offen sc0 sc1
Mv 104,52,104
Rb 44,1,56320
s_waitcnt lgkmcnt(9)
Mv 104,48,96
v_exp_f32_e32 v44, v84
s_add_u32 s55, s55, 0x10000
v_mfma_f32_16x16x32_bf16 v[112:115], a[0:3], a[64:67], v[16:19]
Rb 48,1,61440
Rt 190,9,24960
Mv 112,4,72
v_exp_f32_e32 v28, v85
Mv 112,8,80
Rb 52,1,62464
s_add_u32 m0, s36, 69632
Mv 112,12,88
buffer_load_dwordx4 v3, s[20:23], s56 offen offset:0 sc1 lds
v_mfma_f32_16x16x32_bf16 v[116:119], a[12:15], a[92:95], v[20:23]
Rb 56,1,63488
v_add_f32_e32 v15, v15, v44
Mv 116,8,84
v_exp_f32_e32 v45, v86
Mv 116,4,76
Rb 60,1,64512
s_waitcnt lgkmcnt(10)
Mv 116,0,68
v_exp_f32_e32 v29, v87
v_add_f32_e32 v15, v15, v28
Mv 116,16,100
v_exp_f32_e32 v46, v92
Mv 116,20,108
v_exp_f32_e32 v24, v93
Mv 116,24,116
v_exp_f32_e32 v47, v94
)" R"(
Mv 116,28,124
buffer_load_dwordx4 v3, s[20:23], s56 offen offset:1024 sc1 lds
Mv 112,28,120
v_exp_f32_e32 v25, v95
Mv 112,24,112
buffer_store_dwordx4 a[156:159], v3, s[20:23], s48 offen offset:3072 sc0 sc1
Mv 112,20,104
s_cmp_eq_u32 s57, 2
s_cselect_b32 s55, s59, s55
Mv 112,16,96
Cv 44,44,28
s_waitcnt vmcnt(25) lgkmcnt(0)
s_barrier
v_mfma_f32_16x16x32_bf16 v[120:123], a[32:35], a[64:67], v[16:19]
Rb 0,8,32768
v_add_f32_e32 v15, v15, v45
Mv 120,36,72
Rb 4,8,49152
v_add_f32_e32 v15, v15, v29
Mv 120,40,80
Rb 8,8,33792
Cv 45,45,29
Mv 120,44,88
Rb 12,8,50176
buffer_load_dwordx4 v3, s[20:23], s56 offen offset:2048 sc1 lds
v_mfma_f32_16x16x32_bf16 v[124:127], a[44:47], a[92:95], v[20:23]
Rb 16,8,34816
v_add_f32_e32 v15, v15, v46
Mv 124,40,84
Rb 20,8,51200
v_add_f32_e32 v15, v15, v24
Mv 124,36,76
Rb 24,8,35840
Cv 46,46,24
Mv 124,32,68
Rb 28,8,52224
buffer_store_dwordx4 a[184:187], v1, s[20:23], s10 offen sc0 sc1
Mv 124,48,100
Rb 32,8,36864
v_add_f32_e32 v15, v15, v47
Mv 124,52,108
Rb 36,8,53248
v_add_f32_e32 v15, v15, v25
Mv 124,56,116
Rb 40,8,37888
Cv 47,47,25
Mv 124,60,124
Rb 44,8,54272
buffer_load_dwordx4 v3, s[20:23], s56 offen offset:3072 sc1 lds
Mv 120,60,120
v_max_f32_e32 v11, v14, v15
v_cmp_nge_f32_e32 vcc, s62, v11
Mv 120,56,112
s_add_u32 s56, s56, 0x10000
Mv 120,52,104
s_waitcnt lgkmcnt(8)
s_cbranch_vccnz .Lzfx10
.Lzfr10:
Mv 120,48,96
v_add_f32_e32 v12, v12, v14
v_add_f32_e32 v13, v13, v15
Mp 128,0,32
Rb 48,8,38912
Mp 128,4,40
s_cmp_le_i32 s57, s58
)" R"(
s_cbranch_scc1 .Lzmk9
.Lzmr9:
Mp 136,4,44
Rb 52,8,55296
s_add_u32 m0, s36, 32768
Mp 136,0,36
buffer_load_dwordx4 v2, s[20:23], s55 offen offset:0 sc1 lds
Mp 140,8,36
Rb 56,8,39936
Mp 140,12,44
buffer_store_dwordx4 a[188:191], v1, s[20:23], s11 offen sc0 sc1
Mp 132,12,40
Rb 60,8,56320
s_waitcnt lgkmcnt(8)
Mp 132,8,32
v_exp_f32_e32 v48, v96
Mp 144,16,32
Rb 0,8,40960
Mp 144,20,40
v_exp_f32_e32 v26, v97
Mp 152,20,44
Rb 4,8,57344
v_add_f32_e32 v14, v48, v26
Mp 152,16,36
buffer_load_dwordx4 v2, s[20:23], s55 offen offset:1024 sc1 lds
Mp 156,24,36
Rb 8,8,41984
Cv 48,48,26
Mp 156,28,44
v_exp_f32_e32 v49, v98
Mp 148,28,40
Rb 12,8,58368
s_waitcnt lgkmcnt(8)
Mp 148,24,32
v_exp_f32_e32 v27, v99
Mp 160,32,32
Rb 16,8,43008
v_add_f32_e32 v14, v14, v49
Mp 160,36,40
v_exp_f32_e32 v50, v104
Mp 168,36,44
Rb 20,8,59392
v_add_f32_e32 v14, v14, v27
Mp 168,32,36
buffer_load_dwordx4 v2, s[20:23], s55 offen offset:2048 sc1 lds
Mp 172,40,36
Rb 24,8,44032
Cv 49,49,27
Mp 172,44,44
v_exp_f32_e32 v28, v105
Mp 164,44,40
Rb 28,8,60416
s_waitcnt lgkmcnt(8)
Mp 164,40,32
v_exp_f32_e32 v51, v106
Mp 176,48,32
Rb 32,8,45056
v_add_f32_e32 v14, v14, v50
Mp 176,52,40
v_exp_f32_e32 v29, v107
Mp 184,52,44
Rb 36,8,61440
v_add_f32_e32 v14, v14, v28
Mp 184,48,36
)" R"(
buffer_load_dwordx4 v2, s[20:23], s55 offen offset:3072 sc1 lds
Mp 188,56,36
Rb 40,8,46080
Cv 50,50,28
Mp 188,60,44
v_exp_f32_e32 v52, v100
Mp 180,60,40
Rb 44,8,62464
s_waitcnt lgkmcnt(8)
Mp 180,56,32
v_exp_f32_e32 v24, v101
Mp 192,0,32
Rb 48,8,47104
v_add_f32_e32 v14, v14, v51
Mp 192,4,40
v_exp_f32_e32 v53, v102
Mp 200,4,44
Rb 52,8,63488
s_add_u32 m0, s36, 36864
Mp 200,0,36
buffer_load_dwordx4 v3, s[20:23], s55 offen offset:0 sc1 lds
Mp 204,8,36
Rb 56,8,48128
v_add_f32_e32 v14, v14, v29
Mp 204,12,44
v_exp_f32_e32 v25, v103
Mp 196,12,40
Rb 60,8,64512
s_waitcnt lgkmcnt(8)
Mp 196,8,32
v_exp_f32_e32 v54, v108
Mp 208,16,32
v_exp_f32_e32 v26, v109
Mp 208,20,40
v_exp_f32_e32 v55, v110
Mp 216,20,44
v_exp_f32_e32 v27, v111
Mp 216,16,36
buffer_load_dwordx4 v3, s[20:23], s55 offen offset:1024 sc1 lds
Mp 220,24,36
Cv 51,51,29
v_add_f32_e32 v15, v52, v24
Mp 220,28,44
Cv 52,52,24
v_add_f32_e32 v15, v15, v53
Mp 212,28,40
v_add_f32_e32 v15, v15, v25
Cv 53,53,25
Mp 212,24,32
s_waitcnt vmcnt(5) lgkmcnt(0)
s_barrier
buffer_store_dword v6, v7, s[20:23], s49 offen offset:3072 sc0 sc1
Mp 224,32,32
Rb 0,1,0
v_add_f32_e32 v15, v15, v54
Mp 224,36,40
Rb 4,1,1024
v_add_f32_e32 v15, v15, v26
Mp 232,36,44
Rb 8,1,2048
Cv 54,54,26
Mp 232,32,36
Rb 12,1,3072
buffer_load_dwordx4 v3, s[20:23], s55 offen offset:2048 sc1 lds
Mp 236,40,36
)" R"(
Rb 16,1,8192
v_add_f32_e32 v15, v15, v55
Mp 236,44,44
Rb 20,1,9216
v_add_f32_e32 v15, v15, v27
Mp 228,44,40
Rb 24,1,10240
Cv 55,55,27
Mp 228,40,32
Rb 28,1,11264
s_sub_u32 s57, s57, 1
Mp 240,48,32
Rb 32,1,4096
Mp 240,52,40
Rb 36,1,5120
Mp 248,52,44
Rb 40,1,6144
Mp 248,48,36
Rb 44,1,7168
buffer_load_dwordx4 v3, s[20:23], s55 offen offset:3072 sc1 lds
Mp 252,56,36
v_cmp_eq_u32_e32 vcc, s3, v4
s_andn2_b64 s[28:29], -1, vcc
s_cbranch_scc1 .Lzsp21
.Lzok21:
Mp 252,60,44
Mp 244,60,40
s_waitcnt lgkmcnt(8)
Mp 244,56,32
.LzH15:
s_cbranch_execz .LzHR
.LzL:
v_mfma_f32_16x16x32_bf16 v[64:67], a[0:3], a[64:67], v[16:19]
Rb 48,1,12288
Mv 64,4,72
v_exp_f32_e32 v56, v112
Mv 64,8,80
Rb 52,1,13312
s_add_u32 m0, s36, 98304
Mv 64,12,88
buffer_load_dwordx4 v2, s[20:23], s56 offen offset:0 sc1 lds
v_mfma_f32_16x16x32_bf16 v[68:71], a[12:15], a[92:95], v[20:23]
Rb 56,1,14336
v_add_f32_e32 v14, v14, v56
Mv 68,8,84
v_exp_f32_e32 v24, v113
Mv 68,4,76
Rb 60,1,15360
s_waitcnt lgkmcnt(8)
Mv 68,0,68
v_exp_f32_e32 v57, v114
Mv 68,16,100
Rb 0,1,16384
v_add_f32_e32 v14, v14, v24
Mv 68,20,108
v_exp_f32_e32 v25, v115
Mv 68,24,116
Rb 4,1,17408
Cv 56,56,24
Mv 68,28,124
buffer_load_dwordx4 v2, s[20:23], s56 offen offset:1024 sc1 lds
Mv 64,28,120
Rb 8,1,18432
v_add_f32_e32 v14, v14, v57
)" R"(
Mv 64,24,112
v_exp_f32_e32 v58, v120
Mv 64,20,104
Rb 12,1,19456
s_waitcnt lgkmcnt(8)
Mv 64,16,96
v_exp_f32_e32 v26, v121
v_mfma_f32_16x16x32_bf16 v[72:75], a[32:35], a[64:67], v[16:19]
Rb 16,1,24576
v_add_f32_e32 v14, v14, v25
Mv 72,36,72
v_exp_f32_e32 v59, v122
Mv 72,40,80
Rb 20,1,25600
Cv 57,57,25
Mv 72,44,88
buffer_load_dwordx4 v2, s[20:23], s56 offen offset:2048 sc1 lds
v_mfma_f32_16x16x32_bf16 v[76:79], a[44:47], a[92:95], v[20:23]
Rb 24,1,26624
v_add_f32_e32 v14, v14, v58
Mv 76,40,84
v_exp_f32_e32 v27, v123
Mv 76,36,76
Rb 28,1,27648
s_waitcnt lgkmcnt(8)
Mv 76,32,68
v_exp_f32_e32 v60, v116
Mv 76,48,100
Rb 32,1,20480
v_add_f32_e32 v14, v14, v26
Mv 76,52,108
v_exp_f32_e32 v28, v117
Mv 76,56,116
Rb 36,1,21504
Cv 58,58,26
Mv 76,60,124
buffer_load_dwordx4 v2, s[20:23], s56 offen offset:3072 sc1 lds
Mv 72,60,120
Rb 40,1,22528
v_add_f32_e32 v14, v14, v59
Mv 72,56,112
v_exp_f32_e32 v61, v118
Mv 72,52,104
Rb 44,1,23552
s_waitcnt lgkmcnt(8)
Mv 72,48,96
v_exp_f32_e32 v29, v119
v_mfma_f32_16x16x32_bf16 v[80:83], a[0:3], a[64:67], v[16:19]
Rb 48,1,28672
v_add_f32_e32 v14, v14, v27
Mv 80,4,72
v_exp_f32_e32 v62, v124
Mv 80,8,80
Rb 52,1,29696
s_add_u32 m0, s36, 102400
Mv 80,12,88
buffer_load_dwordx4 v3, s[20:23], s56 offen offset:0 sc1 lds
v_mfma_f32_16x16x32_bf16 v[84:87], a[12:15], a[92:95], v[20:23]
Rb 56,1,30720
Cv 59,59,27
Mv 84,8,84
v_exp_f32_e32 v24, v125
Mv 84,4,76
Rb 60,1,31744
)" R"(
s_waitcnt lgkmcnt(8)
Mv 84,0,68
v_exp_f32_e32 v63, v126
Mv 84,16,100
v_exp_f32_e32 v25, v127
Mv 84,20,108
v_add_f32_e32 v15, v15, v60
v_add_f32_e32 v15, v15, v28
Mv 84,24,116
Cv 60,60,28
v_add_f32_e32 v15, v15, v61
Mv 84,28,124
buffer_load_dwordx4 v3, s[20:23], s56 offen offset:1024 sc1 lds
Mv 80,28,120
v_add_f32_e32 v15, v15, v29
Cv 61,61,29
Mv 80,24,112
v_add_f32_e32 v15, v15, v62
v_add_f32_e32 v15, v15, v24
Mv 80,20,104
Cv 62,62,24
v_add_f32_e32 v15, v15, v63
Mv 80,16,96
s_waitcnt vmcnt(14) lgkmcnt(0)
s_barrier
v_mfma_f32_16x16x32_bf16 v[88:91], a[32:35], a[64:67], v[16:19]
Rb 0,8,0
v_add_f32_e32 v15, v15, v25
Mv 88,36,72
Rb 4,8,16384
Cv 63,63,25
Mv 88,40,80
Rb 8,8,1024
v_max_f32_e32 v11, v14, v15
Mv 88,44,88
Rb 12,8,17408
buffer_load_dwordx4 v3, s[20:23], s56 offen offset:2048 sc1 lds
v_mfma_f32_16x16x32_bf16 v[92:95], a[44:47], a[92:95], v[20:23]
Rb 16,8,2048
v_cmp_nge_f32_e32 vcc, s62, v11
Mv 92,40,84
Rb 20,8,18432
s_add_u32 s55, s55, 0x10000
Mv 92,36,76
Rb 24,8,3072
Mv 92,32,68
Rb 28,8,19456
Mv 92,48,100
Rb 32,8,4096
Mv 92,52,108
Rb 36,8,20480
Mv 92,56,116
Rb 40,8,5120
Mv 92,60,124
Rb 44,8,21504
buffer_load_dwordx4 v3, s[20:23], s56 offen offset:3072 sc1 lds
Mv 88,60,120
s_cmp_eq_u32 s57, 2
s_cselect_b32 s55, s59, s55
Mv 88,56,112
s_add_u32 s56, s56, 0x10000
Mv 88,52,104
s_waitcnt lgkmcnt(8)
s_cbranch_vccnz .Lzfx1
.Lzfr1:
)" R"(
Mv 88,48,96
v_add_f32_e32 v12, v12, v14
v_add_f32_e32 v13, v13, v15
Mp 128,0,48
Rb 48,8,6144
Mp 128,4,56
s_cmp_le_i32 s57, s58
s_cbranch_scc1 .Lzmk2
.Lzmr2:
Mp 136,4,60
Rb 52,8,22528
s_add_u32 m0, s36, 0
Mp 136,0,52
buffer_load_dwordx4 v2, s[20:23], s55 offen offset:0 sc1 lds
Mp 140,8,52
Rb 56,8,7168
Mp 140,12,60
v_exp_f32_e32 v32, v64
Mp 132,12,56
Rb 60,8,23552
s_waitcnt lgkmcnt(8)
Mp 132,8,48
v_exp_f32_e32 v26, v65
Mp 144,16,48
Rb 0,8,8192
v_add_f32_e32 v14, v32, v26
Mp 144,20,56
v_exp_f32_e32 v33, v66
Mp 152,20,60
Rb 4,8,24576
Cv 32,32,26
Mp 152,16,52
buffer_load_dwordx4 v2, s[20:23], s55 offen offset:1024 sc1 lds
Mp 156,24,52
Rb 8,8,9216
v_add_f32_e32 v14, v14, v33
Mp 156,28,60
v_exp_f32_e32 v27, v67
Mp 148,28,56
Rb 12,8,25600
s_waitcnt lgkmcnt(8)
.LzH16:
s_cbranch_execz .LzHR
Mp 148,24,48
v_exp_f32_e32 v34, v72
Mp 160,32,48
Rb 16,8,10240
v_add_f32_e32 v14, v14, v27
Mp 160,36,56
v_exp_f32_e32 v28, v73
Mp 168,36,60
Rb 20,8,26624
Cv 33,33,27
Mp 168,32,52
buffer_load_dwordx4 v2, s[20:23], s55 offen offset:2048 sc1 lds
Mp 172,40,52
Rb 24,8,11264
v_add_f32_e32 v14, v14, v34
Mp 172,44,60
v_exp_f32_e32 v35, v74
Mp 164,44,56
Rb 28,8,27648
s_waitcnt lgkmcnt(8)
Mp 164,40,48
)" R"(
v_exp_f32_e32 v29, v75
Mp 176,48,48
Rb 32,8,12288
v_add_f32_e32 v14, v14, v28
Mp 176,52,56
v_exp_f32_e32 v36, v68
Mp 184,52,60
Rb 36,8,28672
Cv 34,34,28
Mp 184,48,52
buffer_load_dwordx4 v2, s[20:23], s55 offen offset:3072 sc1 lds
Mp 188,56,52
Rb 40,8,13312
v_add_f32_e32 v14, v14, v35
Mp 188,60,60
v_exp_f32_e32 v24, v69
Mp 180,60,56
Rb 44,8,29696
s_waitcnt lgkmcnt(8)
Mp 180,56,48
v_exp_f32_e32 v37, v70
Mp 192,0,48
Rb 48,8,14336
v_add_f32_e32 v14, v14, v29
Mp 192,4,56
v_exp_f32_e32 v25, v71
Mp 200,4,60
Rb 52,8,30720
s_add_u32 m0, s36, 4096
Mp 200,0,52
buffer_load_dwordx4 v3, s[20:23], s55 offen offset:0 sc1 lds
Mp 204,8,52
Rb 56,8,15360
Cv 35,35,29
Mp 204,12,60
v_exp_f32_e32 v38, v76
Mp 196,12,56
Rb 60,8,31744
s_waitcnt lgkmcnt(8)
Mp 196,8,48
v_exp_f32_e32 v26, v77
Mp 208,16,48
v_exp_f32_e32 v39, v78
Mp 208,20,56
v_exp_f32_e32 v27, v79
Mp 216,20,60
v_add_f32_e32 v15, v36, v24
Cv 36,36,24
Mp 216,16,52
buffer_load_dwordx4 v3, s[20:23], s55 offen offset:1024 sc1 lds
Mp 220,24,52
s_add_u32 s50, s50, 1024
s_min_u32 s50, s50, s60
Mp 220,28,60
v_add_f32_e32 v15, v15, v37
v_add_f32_e32 v15, v15, v25
Mp 212,28,56
Cv 37,37,25
v_add_f32_e32 v15, v15, v38
Mp 212,24,48
s_waitcnt vmcnt(14) lgkmcnt(0)
s_barrier
Mp 224,32,48
Rb 0,1,32768
v_add_f32_e32 v15, v15, v26
)" R"(
Mp 224,36,56
Rb 4,1,33792
Cv 38,38,26
Mp 232,36,60
Rb 8,1,34816
v_add_f32_e32 v15, v15, v39
Mp 232,32,52
Rb 12,1,35840
buffer_load_dwordx4 v3, s[20:23], s55 offen offset:2048 sc1 lds
Mp 236,40,52
Rb 16,1,40960
v_add_f32_e32 v15, v15, v27
Mp 236,44,60
Rb 20,1,41984
Cv 39,39,27
Mp 228,44,56
Rb 24,1,43008
s_sub_u32 s57, s57, 1
Mp 228,40,48
Rb 28,1,44032
Mp 240,48,48
Rb 32,1,36864
Mp 240,52,56
Rb 36,1,37888
Mp 248,52,60
Rb 40,1,38912
Mp 248,48,52
Rb 44,1,39936
buffer_load_dwordx4 v3, s[20:23], s55 offen offset:3072 sc1 lds
Mp 252,56,52
Mp 252,60,60
Mp 244,60,56
s_waitcnt lgkmcnt(8)
Mp 244,56,48
v_mfma_f32_16x16x32_bf16 v[96:99], a[0:3], a[64:67], v[16:19]
Rb 48,1,45056
buffer_load_dword v4, v5, s[20:23], s50 offen offset:0 sc0 sc1
Mv 96,4,72
v_exp_f32_e32 v40, v80
Mv 96,8,80
Rb 52,1,46080
s_add_u32 m0, s36, 65536
Mv 96,12,88
buffer_load_dwordx4 v2, s[20:23], s56 offen offset:0 sc1 lds
v_mfma_f32_16x16x32_bf16 v[100:103], a[12:15], a[92:95], v[20:23]
Rb 56,1,47104
v_add_f32_e32 v14, v14, v40
Mv 100,8,84
v_exp_f32_e32 v28, v81
Mv 100,4,76
Rb 60,1,48128
s_waitcnt lgkmcnt(8)
Mv 100,0,68
v_exp_f32_e32 v41, v82
Mv 100,16,100
Rb 0,1,49152
v_add_f32_e32 v14, v14, v28
Mv 100,20,108
v_exp_f32_e32 v29, v83
Mv 100,24,116
Rb 4,1,50176
Cv 40,40,28
Mv 100,28,124
buffer_load_dwordx4 v2, s[20:23], s56 offen offset:1024 sc1 lds
)" R"(
Mv 96,28,120
Rb 8,1,51200
v_add_f32_e32 v14, v14, v41
Mv 96,24,112
v_exp_f32_e32 v42, v88
Mv 96,20,104
Rb 12,1,52224
s_waitcnt lgkmcnt(8)
Mv 96,16,96
v_exp_f32_e32 v24, v89
v_mfma_f32_16x16x32_bf16 v[104:107], a[32:35], a[64:67], v[16:19]
Rb 16,1,57344
v_add_f32_e32 v14, v14, v29
Mv 104,36,72
v_exp_f32_e32 v43, v90
Mv 104,40,80
Rb 20,1,58368
Cv 41,41,29
Mv 104,44,88
buffer_load_dwordx4 v2, s[20:23], s56 offen offset:2048 sc1 lds
v_mfma_f32_16x16x32_bf16 v[108:111], a[44:47], a[92:95], v[20:23]
Rb 24,1,59392
v_add_f32_e32 v14, v14, v42
Mv 108,40,84
v_exp_f32_e32 v25, v91
Mv 108,36,76
Rb 28,1,60416
s_waitcnt lgkmcnt(8)
Mv 108,32,68
v_exp_f32_e32 v44, v84
Mv 108,48,100
Rb 32,1,53248
v_add_f32_e32 v14, v14, v24
Mv 108,52,108
v_exp_f32_e32 v26, v85
Mv 108,56,116
Rb 36,1,54272
Cv 42,42,24
Mv 108,60,124
buffer_load_dwordx4 v2, s[20:23], s56 offen offset:3072 sc1 lds
Mv 104,60,120
Rb 40,1,55296
v_add_f32_e32 v14, v14, v43
Mv 104,56,112
v_exp_f32_e32 v45, v86
Mv 104,52,104
Rb 44,1,56320
s_waitcnt lgkmcnt(8)
Mv 104,48,96
v_exp_f32_e32 v27, v87
v_mfma_f32_16x16x32_bf16 v[112:115], a[0:3], a[64:67], v[16:19]
Rb 48,1,61440
v_add_f32_e32 v14, v14, v25
Mv 112,4,72
v_exp_f32_e32 v46, v92
Mv 112,8,80
Rb 52,1,62464
s_add_u32 m0, s36, 69632
Mv 112,12,88
buffer_load_dwordx4 v3, s[20:23], s56 offen offset:0 sc1 lds
v_mfma_f32_16x16x32_bf16 v[116:119], a[12:15], a[92:95], v[20:23]
Rb 56,1,63488
Cv 43,43,25
Mv 116,8,84
)" R"(
v_exp_f32_e32 v28, v93
Mv 116,4,76
Rb 60,1,64512
s_waitcnt lgkmcnt(8)
Mv 116,0,68
v_exp_f32_e32 v47, v94
Mv 116,16,100
v_exp_f32_e32 v29, v95
Mv 116,20,108
v_add_f32_e32 v15, v15, v44
v_add_f32_e32 v15, v15, v26
Mv 116,24,116
Cv 44,44,26
v_add_f32_e32 v15, v15, v45
Mv 116,28,124
buffer_load_dwordx4 v3, s[20:23], s56 offen offset:1024 sc1 lds
Mv 112,28,120
v_add_f32_e32 v15, v15, v27
Cv 45,45,27
Mv 112,24,112
v_add_f32_e32 v15, v15, v46
v_add_f32_e32 v15, v15, v28
Mv 112,20,104
Cv 46,46,28
v_add_f32_e32 v15, v15, v47
Mv 112,16,96
s_waitcnt vmcnt(15) lgkmcnt(0)
s_barrier
v_mfma_f32_16x16x32_bf16 v[120:123], a[32:35], a[64:67], v[16:19]
Rb 0,8,32768
v_add_f32_e32 v15, v15, v29
Mv 120,36,72
Rb 4,8,49152
Cv 47,47,29
Mv 120,40,80
Rb 8,8,33792
v_max_f32_e32 v11, v14, v15
Mv 120,44,88
Rb 12,8,50176
buffer_load_dwordx4 v3, s[20:23], s56 offen offset:2048 sc1 lds
v_mfma_f32_16x16x32_bf16 v[124:127], a[44:47], a[92:95], v[20:23]
Rb 16,8,34816
v_cmp_nge_f32_e32 vcc, s62, v11
Mv 124,40,84
Rb 20,8,51200
s_add_u32 s55, s55, 0x10000
Mv 124,36,76
Rb 24,8,35840
Mv 124,32,68
Rb 28,8,52224
Mv 124,48,100
Rb 32,8,36864
Mv 124,52,108
Rb 36,8,53248
Mv 124,56,116
Rb 40,8,37888
Mv 124,60,124
Rb 44,8,54272
buffer_load_dwordx4 v3, s[20:23], s56 offen offset:3072 sc1 lds
Mv 120,60,120
s_cmp_eq_u32 s57, 2
s_cselect_b32 s55, s59, s55
Mv 120,56,112
s_add_u32 s56, s56, 0x10000
Mv 120,52,104
)" R"(
s_waitcnt lgkmcnt(8)
s_cbranch_vccnz .Lzfx2
.Lzfr2:
Mv 120,48,96
v_add_f32_e32 v12, v12, v14
v_add_f32_e32 v13, v13, v15
Mp 128,0,32
Rb 48,8,38912
Mp 128,4,40
s_cmp_le_i32 s57, s58
s_cbranch_scc1 .Lzmk3
.Lzmr3:
Mp 136,4,44
Rb 52,8,55296
s_add_u32 m0, s36, 32768
Mp 136,0,36
buffer_load_dwordx4 v2, s[20:23], s55 offen offset:0 sc1 lds
Mp 140,8,36
Rb 56,8,39936
Mp 140,12,44
v_exp_f32_e32 v48, v96
Mp 132,12,40
Rb 60,8,56320
s_waitcnt lgkmcnt(8)
Mp 132,8,32
v_exp_f32_e32 v24, v97
Mp 144,16,32
Rb 0,8,40960
v_add_f32_e32 v14, v48, v24
Mp 144,20,40
v_exp_f32_e32 v49, v98
Mp 152,20,44
Rb 4,8,57344
Cv 48,48,24
Mp 152,16,36
buffer_load_dwordx4 v2, s[20:23], s55 offen offset:1024 sc1 lds
Mp 156,24,36
Rb 8,8,41984
v_add_f32_e32 v14, v14, v49
Mp 156,28,44
v_exp_f32_e32 v25, v99
Mp 148,28,40
Rb 12,8,58368
s_waitcnt lgkmcnt(8)
Mp 148,24,32
v_exp_f32_e32 v50, v104
Mp 160,32,32
Rb 16,8,43008
v_add_f32_e32 v14, v14, v25
Mp 160,36,40
v_exp_f32_e32 v26, v105
Mp 168,36,44
Rb 20,8,59392
Cv 49,49,25
Mp 168,32,36
buffer_load_dwordx4 v2, s[20:23], s55 offen offset:2048 sc1 lds
Mp 172,40,36
Rb 24,8,44032
v_add_f32_e32 v14, v14, v50
Mp 172,44,44
v_exp_f32_e32 v51, v106
Mp 164,44,40
Rb 28,8,60416
s_waitcnt lgkmcnt(8)
)" R"(
Mp 164,40,32
v_exp_f32_e32 v27, v107
Mp 176,48,32
Rb 32,8,45056
v_add_f32_e32 v14, v14, v26
Mp 176,52,40
v_exp_f32_e32 v52, v100
Mp 184,52,44
Rb 36,8,61440
Cv 50,50,26
Mp 184,48,36
buffer_load_dwordx4 v2, s[20:23], s55 offen offset:3072 sc1 lds
Mp 188,56,36
Rb 40,8,46080
v_add_f32_e32 v14, v14, v51
Mp 188,60,44
v_exp_f32_e32 v28, v101
Mp 180,60,40
Rb 44,8,62464
s_waitcnt lgkmcnt(8)
Mp 180,56,32
v_exp_f32_e32 v53, v102
Mp 192,0,32
Rb 48,8,47104
v_add_f32_e32 v14, v14, v27
Mp 192,4,40
v_exp_f32_e32 v29, v103
Mp 200,4,44
Rb 52,8,63488
s_add_u32 m0, s36, 36864
Mp 200,0,36
buffer_load_dwordx4 v3, s[20:23], s55 offen offset:0 sc1 lds
Mp 204,8,36
Rb 56,8,48128
Cv 51,51,27
Mp 204,12,44
v_exp_f32_e32 v54, v108
Mp 196,12,40
Rb 60,8,64512
s_waitcnt lgkmcnt(8)
Mp 196,8,32
v_exp_f32_e32 v24, v109
Mp 208,16,32
v_exp_f32_e32 v55, v110
Mp 208,20,40
v_exp_f32_e32 v25, v111
Mp 216,20,44
v_add_f32_e32 v15, v52, v28
Cv 52,52,28
Mp 216,16,36
buffer_load_dwordx4 v3, s[20:23], s55 offen offset:1024 sc1 lds
Mp 220,24,36
v_add_f32_e32 v15, v15, v53
v_add_f32_e32 v15, v15, v29
Mp 220,28,44
Cv 53,53,29
v_add_f32_e32 v15, v15, v54
Mp 212,28,40
v_add_f32_e32 v15, v15, v24
Cv 54,54,24
Mp 212,24,32
s_waitcnt vmcnt(14) lgkmcnt(0)
s_barrier
Mp 224,32,32
Rb 0,1,0
)" R"(
v_add_f32_e32 v15, v15, v55
Mp 224,36,40
Rb 4,1,1024
v_add_f32_e32 v15, v15, v25
Mp 232,36,44
Rb 8,1,2048
Cv 55,55,25
Mp 232,32,36
Rb 12,1,3072
buffer_load_dwordx4 v3, s[20:23], s55 offen offset:2048 sc1 lds
Mp 236,40,36
Rb 16,1,8192
s_sub_u32 s57, s57, 1
Mp 236,44,44
Rb 20,1,9216
Mp 228,44,40
Rb 24,1,10240
Mp 228,40,32
Rb 28,1,11264
Mp 240,48,32
Rb 32,1,4096
Mp 240,52,40
Rb 36,1,5120
Mp 248,52,44
Rb 40,1,6144
Mp 248,48,36
Rb 44,1,7168
buffer_load_dwordx4 v3, s[20:23], s55 offen offset:3072 sc1 lds
Mp 252,56,36
v_cmp_eq_u32_e32 vcc, s3, v4
s_andn2_b64 s[28:29], -1, vcc
s_cbranch_scc1 .Lzsp9
.Lzok9:
Mp 252,60,44
Mp 244,60,40
s_waitcnt lgkmcnt(8)
Mp 244,56,32
s_cmp_gt_i32 s57, s93
s_cbranch_scc1 .LzL
s_cmp_eq_u32 s94, 1
s_cbranch_scc1 .LzW
s_cmp_eq_u32 s94, 2
s_cbranch_scc1 .LzDA
.LzEB:
v_mfma_f32_16x16x32_bf16 v[64:67], a[0:3], a[64:67], v[16:19]
Rb 48,1,12288
s_mov_b32 s83, 0x1508000
buffer_load_dword v4, v7, s[20:23], s83 offen offset:4 sc0 sc1
Mv 64,4,72
s_cmp_lg_u32 s35, 7
s_cselect_b64 s[60:61], 1, 0
Mv 64,8,80
Rb 52,1,13312
s_add_u32 m0, s36, 98304
Mv 64,12,88
buffer_load_dwordx4 v2, s[20:23], s56 offen offset:0 sc1 lds
v_mfma_f32_16x16x32_bf16 v[68:71], a[12:15], a[92:95], v[20:23]
Rb 56,1,14336
Mv 68,8,84
v_exp_f32_e32 v56, v112
Mv 68,4,76
Rb 60,1,15360
s_waitcnt lgkmcnt(8)
Mv 68,0,68
)" R"(
v_exp_f32_e32 v28, v113
Mv 68,16,100
Rb 0,1,16384
v_add_f32_e32 v14, v14, v56
Mv 68,20,108
v_exp_f32_e32 v57, v114
Mv 68,24,116
Rb 4,1,17408
v_add_f32_e32 v14, v14, v28
Mv 68,28,124
buffer_load_dwordx4 v2, s[20:23], s56 offen offset:1024 sc1 lds
Mv 64,28,120
Rb 8,1,18432
Cv 56,56,28
Mv 64,24,112
v_exp_f32_e32 v29, v115
Mv 64,20,104
Rb 12,1,19456
s_waitcnt lgkmcnt(8)
Mv 64,16,96
v_exp_f32_e32 v58, v120
v_mfma_f32_16x16x32_bf16 v[72:75], a[32:35], a[64:67], v[16:19]
Rb 16,1,24576
v_add_f32_e32 v14, v14, v57
Mv 72,36,72
v_exp_f32_e32 v24, v121
Mv 72,40,80
Rb 20,1,25600
v_add_f32_e32 v14, v14, v29
Mv 72,44,88
buffer_load_dwordx4 v2, s[20:23], s56 offen offset:2048 sc1 lds
v_mfma_f32_16x16x32_bf16 v[76:79], a[44:47], a[92:95], v[20:23]
Rb 24,1,26624
Cv 57,57,29
Mv 76,40,84
v_exp_f32_e32 v59, v122
Mv 76,36,76
Rb 28,1,27648
s_waitcnt lgkmcnt(8)
Mv 76,32,68
v_exp_f32_e32 v25, v123
Mv 76,48,100
Rb 32,1,20480
v_add_f32_e32 v14, v14, v58
Mv 76,52,108
v_exp_f32_e32 v60, v116
Mv 76,56,116
Rb 36,1,21504
v_add_f32_e32 v14, v14, v24
Mv 76,60,124
buffer_load_dwordx4 v2, s[20:23], s56 offen offset:3072 sc1 lds
Mv 72,60,120
Rb 40,1,22528
Cv 58,58,24
Mv 72,56,112
v_exp_f32_e32 v26, v117
Mv 72,52,104
Rb 44,1,23552
s_waitcnt lgkmcnt(8)
Mv 72,48,96
v_exp_f32_e32 v61, v118
v_mfma_f32_16x16x32_bf16 v[80:83], a[0:3], a[64:67], v[16:19]
Rb 48,1,28672
v_add_f32_e32 v14, v14, v59
)" R"(
Mv 80,4,72
v_exp_f32_e32 v27, v119
Mv 80,8,80
Rb 52,1,29696
s_add_u32 m0, s36, 102400
Mv 80,12,88
buffer_load_dwordx4 v3, s[20:23], s56 offen offset:0 sc1 lds
v_mfma_f32_16x16x32_bf16 v[84:87], a[12:15], a[92:95], v[20:23]
Rb 56,1,30720
v_add_f32_e32 v14, v14, v25
Mv 84,8,84
v_exp_f32_e32 v62, v124
Mv 84,4,76
Rb 60,1,31744
s_waitcnt lgkmcnt(8)
Mv 84,0,68
v_exp_f32_e32 v28, v125
Mv 84,16,100
v_exp_f32_e32 v63, v126
Mv 84,20,108
v_exp_f32_e32 v29, v127
Mv 84,24,116
Cv 59,59,25
v_add_f32_e32 v15, v15, v60
Mv 84,28,124
buffer_load_dwordx4 v3, s[20:23], s56 offen offset:1024 sc1 lds
Mv 80,28,120
v_add_f32_e32 v15, v15, v26
Cv 60,60,26
Mv 80,24,112
v_add_f32_e32 v15, v15, v61
v_add_f32_e32 v15, v15, v27
Mv 80,20,104
Cv 61,61,27
v_add_f32_e32 v15, v15, v62
Mv 80,16,96
s_waitcnt vmcnt(15) lgkmcnt(0)
s_barrier
v_mfma_f32_16x16x32_bf16 v[88:91], a[32:35], a[64:67], v[16:19]
Rb 0,8,0
v_add_f32_e32 v15, v15, v28
Mv 88,36,72
Rb 4,8,16384
Cv 62,62,28
Mv 88,40,80
Rb 8,8,1024
v_add_f32_e32 v15, v15, v63
Mv 88,44,88
Rb 12,8,17408
buffer_load_dwordx4 v3, s[20:23], s56 offen offset:2048 sc1 lds
v_mfma_f32_16x16x32_bf16 v[92:95], a[44:47], a[92:95], v[20:23]
Rb 16,8,2048
v_add_f32_e32 v15, v15, v29
Mv 92,40,84
Rb 20,8,18432
Cv 63,63,29
Mv 92,36,76
Rb 24,8,3072
v_max_f32_e32 v11, v14, v15
Mv 92,32,68
Rb 28,8,19456
v_cmp_nge_f32_e32 vcc, s62, v11
Mv 92,48,100
Rb 32,8,4096
s_add_u32 s55, s55, 0x10000
)" R"(
.LzH17:
s_cbranch_execz .LzHR
Mv 92,52,108
Rb 36,8,20480
Mv 92,56,116
Rb 40,8,5120
Mv 92,60,124
Rb 44,8,21504
buffer_load_dwordx4 v3, s[20:23], s56 offen offset:3072 sc1 lds
Mv 88,60,120
s_cmp_eq_u32 s57, 2
s_cselect_b32 s55, s59, s55
Mv 88,56,112
s_add_u32 s56, s56, 0x10000
Mv 88,52,104
s_waitcnt lgkmcnt(8)
s_cbranch_vccnz .Lzfx11
.Lzfr11:
Mv 88,48,96
s_waitcnt vmcnt(8)
v_cmp_eq_u32_e32 vcc, s3, v4
s_andn2_b64 s[28:29], 1, vcc
s_cbranch_scc1 .Lzsp22
.Lzok22:
Mp 128,0,48
Rb 48,8,6144
buffer_load_dword v9, v7, s[20:23], s83 offen sc0 sc1
Mp 128,4,56
v_add_f32_e32 v12, v12, v14
s_cmp_le_i32 s57, s58
s_cbranch_scc1 .Lzmk10
.Lzmr10:
Mp 136,4,60
Rb 52,8,22528
s_add_u32 m0, s36, 0
v_add_f32_e32 v13, v13, v15
Mp 136,0,52
buffer_load_dwordx4 v2, s[20:23], s55 offen offset:0 sc1 lds
Mp 140,8,52
Rb 56,8,7168
Mp 140,12,60
v_exp_f32_e32 v32, v64
Mp 132,12,56
Rb 60,8,23552
s_waitcnt lgkmcnt(8)
Mp 132,8,48
v_exp_f32_e32 v24, v65
Mp 144,16,48
Rb 0,8,8192
v_add_f32_e32 v14, v32, v24
Mp 144,20,56
v_exp_f32_e32 v33, v66
Cv 32,32,24
Mp 152,20,60
Rb 4,8,24576
v_add_f32_e32 v14, v14, v33
Mp 152,16,52
buffer_load_dwordx4 v2, s[20:23], s55 offen offset:1024 sc1 lds
Mp 156,24,52
Rb 8,8,9216
Mp 156,28,60
v_exp_f32_e32 v25, v67
Mp 148,28,56
Rb 12,8,25600
)" R"(
s_waitcnt lgkmcnt(8)
Mp 148,24,48
v_exp_f32_e32 v34, v72
Mp 160,32,48
Rb 16,8,10240
v_add_f32_e32 v14, v14, v25
Mp 160,36,56
v_exp_f32_e32 v26, v73
Cv 33,33,25
Mp 168,36,60
Rb 20,8,26624
v_add_f32_e32 v14, v14, v34
Mp 168,32,52
buffer_load_dwordx4 v2, s[20:23], s55 offen offset:2048 sc1 lds
Mp 172,40,52
Rb 24,8,11264
v_add_f32_e32 v14, v14, v26
Mp 172,44,60
v_exp_f32_e32 v35, v74
Mp 164,44,56
Rb 28,8,27648
s_waitcnt lgkmcnt(8)
Mp 164,40,48
v_exp_f32_e32 v27, v75
Mp 176,48,48
Rb 32,8,12288
Cv 34,34,26
Mp 176,52,56
v_exp_f32_e32 v36, v68
v_add_f32_e32 v14, v14, v35
Mp 184,52,60
Rb 36,8,28672
v_add_f32_e32 v14, v14, v27
Mp 184,48,52
buffer_load_dwordx4 v2, s[20:23], s55 offen offset:3072 sc1 lds
Mp 188,56,52
Rb 40,8,13312
Cv 35,35,27
Mp 188,60,60
v_exp_f32_e32 v28, v69
Mp 180,60,56
Rb 44,8,29696
s_waitcnt lgkmcnt(8)
Mp 180,56,48
v_exp_f32_e32 v37, v70
Mp 192,0,48
Rb 48,8,14336
v_add_f32_e32 v15, v36, v28
Mp 192,4,56
v_exp_f32_e32 v29, v71
Cv 36,36,28
Mp 200,4,60
Rb 52,8,30720
s_add_u32 m0, s36, 4096
Mp 200,0,52
buffer_load_dwordx4 v3, s[20:23], s55 offen offset:0 sc1 lds
Mp 204,8,52
Rb 56,8,15360
v_add_f32_e32 v15, v15, v37
Mp 204,12,60
v_exp_f32_e32 v38, v76
Mp 196,12,56
Rb 60,8,31744
s_waitcnt lgkmcnt(8)
)" R"(
Mp 196,8,48
v_exp_f32_e32 v24, v77
Mp 208,16,48
v_exp_f32_e32 v39, v78
Mp 208,20,56
v_exp_f32_e32 v25, v79
v_add_f32_e32 v15, v15, v29
Mp 216,20,60
Cv 37,37,29
v_add_f32_e32 v15, v15, v38
Mp 216,16,52
buffer_load_dwordx4 v3, s[20:23], s55 offen offset:1024 sc1 lds
Mp 220,24,52
v_add_f32_e32 v15, v15, v24
Cv 38,38,24
Mp 220,28,60
v_add_f32_e32 v15, v15, v39
v_add_f32_e32 v15, v15, v25
Mp 212,28,56
Cv 39,39,25
s_sub_u32 s57, s57, 1
Mp 212,24,48
s_waitcnt lgkmcnt(0)
s_barrier
Mp 224,32,48
Rb 0,1,32768
v_bfe_u32 v11, v0, 2, 2
Mp 224,36,56
Rb 4,1,33792
v_lshrrev_b32 v4, 4, v0
v_xor_b32 v11, v11, v4
Mp 232,36,60
Rb 8,1,34816
v_bfe_u32 v4, v0, 1, 1
Mp 232,32,52
Rb 12,1,35840
buffer_load_dwordx4 v3, s[20:23], s55 offen offset:2048 sc1 lds
Mp 236,40,52
Rb 16,1,40960
v_lshl_or_b32 v11, v11, 1, v4
Mp 236,44,60
Rb 20,1,41984
v_and_b32 v4, 1, v0
Mp 228,44,56
Rb 24,1,43008
v_mul_u32_u24 v4, 0xffff, v4
Mp 228,40,48
Rb 28,1,44032
v_add_u32 v4, 1, v4
Mp 240,48,48
Rb 32,1,36864
v_mul_u32_u24 v4, 0x3f80, v4
Mp 240,52,56
Rb 36,1,37888
v_cmp_eq_u32_e32 vcc, 0, v11
v_cmp_eq_u32_e64 s[28:29], 1, v11
Mp 248,52,60
Rb 40,1,38912
v_accvgpr_write_b32 a192, 0
Mp 248,48,52
Rb 44,1,39936
buffer_load_dwordx4 v3, s[20:23], s55 offen offset:3072 sc1 lds
Mp 252,56,52
v_accvgpr_write_b32 a193, 0
v_accvgpr_write_b32 a196, 0
)" R"(
Mp 252,60,60
v_accvgpr_write_b32 a197, 0
v_cndmask_b32_e32 v11, 0, v4, vcc
Mp 244,60,56
v_cndmask_b32_e64 v4, 0, v4, s[28:29]
s_waitcnt lgkmcnt(8)
Mp 244,56,48
v_accvgpr_write_b32 a194, v11
v_accvgpr_write_b32 a195, v4
v_mfma_f32_16x16x32_bf16 v[96:99], a[0:3], a[64:67], v[16:19]
Rb 48,1,45056
Mv 96,4,72
buffer_load_dword v4, v7, s[20:23], s65 offen offset:0 sc0 sc1
Mv 96,8,80
Rb 52,1,46080
s_add_u32 m0, s36, 65536
Mv 96,12,88
buffer_load_dwordx4 v2, s[20:23], s56 offen offset:0 sc1 lds
v_mfma_f32_16x16x32_bf16 v[100:103], a[12:15], a[92:95], v[20:23]
Rb 56,1,47104
Mv 100,8,84
v_exp_f32_e32 v40, v80
Mv 100,4,76
Rb 60,1,48128
s_waitcnt lgkmcnt(8)
Mv 100,0,68
v_exp_f32_e32 v26, v81
v_add_f32_e32 v14, v14, v40
Mv 100,16,100
Rb 0,1,49152
v_add_f32_e32 v14, v14, v26
Mv 100,20,108
v_exp_f32_e32 v41, v82
Mv 100,24,116
Rb 4,1,50176
Cv 40,40,26
Mv 100,28,124
buffer_load_dwordx4 v2, s[20:23], s56 offen offset:1024 sc1 lds
Mv 96,28,120
Rb 8,1,51200
v_add_f32_e32 v14, v14, v41
Mv 96,24,112
v_exp_f32_e32 v27, v83
Mv 96,20,104
Rb 12,1,52224
s_waitcnt lgkmcnt(8)
Mv 96,16,96
v_exp_f32_e32 v42, v88
v_add_f32_e32 v14, v14, v27
v_mfma_f32_16x16x32_bf16 v[104:107], a[32:35], a[64:67], v[16:19]
Rb 16,1,57344
Cv 41,41,27
Mv 104,36,72
v_exp_f32_e32 v28, v89
Mv 104,40,80
Rb 20,1,58368
v_add_f32_e32 v14, v14, v42
Mv 104,44,88
buffer_load_dwordx4 v2, s[20:23], s56 offen offset:2048 sc1 lds
v_mfma_f32_16x16x32_bf16 v[108:111], a[44:47], a[92:95], v[20:23]
Rb 24,1,59392
v_add_f32_e32 v14, v14, v28
Mv 108,40,84
v_exp_f32_e32 v43, v90
)" R"(
Mv 108,36,76
Rb 28,1,60416
s_waitcnt lgkmcnt(8)
Mv 108,32,68
v_exp_f32_e32 v29, v91
Cv 42,42,28
Mv 108,48,100
Rb 32,1,53248
v_add_f32_e32 v14, v14, v43
Mv 108,52,108
v_exp_f32_e32 v44, v84
Mv 108,56,116
Rb 36,1,54272
v_add_f32_e32 v14, v14, v29
Mv 108,60,124
buffer_load_dwordx4 v2, s[20:23], s56 offen offset:3072 sc1 lds
Mv 104,60,120
Rb 40,1,55296
Cv 43,43,29
Mv 104,56,112
v_exp_f32_e32 v24, v85
Mv 104,52,104
Rb 44,1,56320
s_waitcnt lgkmcnt(8)
Mv 104,48,96
v_exp_f32_e32 v45, v86
v_add_f32_e32 v15, v15, v44
v_mfma_f32_16x16x32_bf16 v[112:115], a[0:3], a[64:67], v[16:19]
Rb 48,1,61440
v_add_f32_e32 v15, v15, v24
Mv 112,4,72
v_exp_f32_e32 v25, v87
Mv 112,8,80
Rb 52,1,62464
s_add_u32 m0, s36, 69632
Mv 112,12,88
buffer_load_dwordx4 v3, s[20:23], s56 offen offset:0 sc1 lds
v_mfma_f32_16x16x32_bf16 v[116:119], a[12:15], a[92:95], v[20:23]
Rb 56,1,63488
Cv 44,44,24
Mv 116,8,84
v_exp_f32_e32 v46, v92
Mv 116,4,76
Rb 60,1,64512
s_waitcnt lgkmcnt(8)
Mv 116,0,68
v_exp_f32_e32 v26, v93
v_add_f32_e32 v15, v15, v45
Mv 116,16,100
v_exp_f32_e32 v47, v94
Mv 116,20,108
v_exp_f32_e32 v27, v95
Mv 116,24,116
v_add_f32_e32 v15, v15, v25
Cv 45,45,25
Mv 116,28,124
buffer_load_dwordx4 v3, s[20:23], s56 offen offset:1024 sc1 lds
Mv 112,28,120
v_add_f32_e32 v15, v15, v46
v_add_f32_e32 v15, v15, v26
Mv 112,24,112
Cv 46,46,26
v_add_f32_e32 v15, v15, v47
Mv 112,20,104
)" R"(
v_add_f32_e32 v15, v15, v27
Cv 47,47,27
Mv 112,16,96
v_max_f32_e32 v11, v14, v15
s_waitcnt vmcnt(6) lgkmcnt(0)
s_barrier
v_mfma_f32_16x16x32_bf16 v[120:123], a[32:35], a[64:67], v[16:19]
Rb 0,8,32768
v_cmp_nge_f32_e32 vcc, s62, v11
Mv 120,36,72
Rb 4,8,49152
s_add_u32 s55, s55, 0x10000
Mv 120,40,80
Rb 8,8,33792
Mv 120,44,88
Rb 12,8,50176
buffer_load_dwordx4 v3, s[20:23], s56 offen offset:2048 sc1 lds
v_mfma_f32_16x16x32_bf16 v[124:127], a[44:47], a[92:95], v[20:23]
Rb 16,8,34816
Mv 124,40,84
Rb 20,8,51200
Mv 124,36,76
Rb 24,8,35840
Mv 124,32,68
Rb 28,8,52224
s_cmp_eq_u32 s57, 2
s_cselect_b32 s55, s59, s55
Mv 124,48,100
Rb 32,8,36864
Mv 124,52,108
Rb 36,8,53248
Mv 124,56,116
Rb 40,8,37888
Mv 124,60,124
Rb 44,8,54272
buffer_load_dwordx4 v3, s[20:23], s56 offen offset:3072 sc1 lds
Mv 120,60,120
v_readfirstlane_b32 s63, v9
Mv 120,56,112
s_add_u32 s56, s56, 0x10000
Mv 120,52,104
s_waitcnt lgkmcnt(8)
s_cbranch_vccnz .Lzfx12
.Lzfr12:
Mv 120,48,96
v_cmp_eq_u32_e32 vcc, s3, v4
s_andn2_b64 s[28:29], s[60:61], vcc
s_cbranch_scc1 .Lzsp23
.Lzok23:
Mp 128,0,32
Rb 48,8,38912
v_add_f32_e32 v12, v12, v14
Mp 128,4,40
v_add_f32_e32 v13, v13, v15
s_cmp_le_i32 s57, s58
s_cbranch_scc1 .Lzmk11
.Lzmr11:
Mp 136,4,44
Rb 52,8,55296
s_add_u32 m0, s36, 32768
buffer_load_dwordx4 v[64:67], v1, s[20:23], s66 offen sc0 sc1
Mp 136,0,36
buffer_load_dwordx4 v2, s[20:23], s55 offen offset:0 sc1 lds
Mp 140,8,36
Rb 56,8,39936
)" R"(
Mp 140,12,44
buffer_load_dwordx4 a[128:131], v2, s[20:23], s64 offen offset:0 sc0 sc1
Mp 132,12,40
Rb 60,8,56320
s_waitcnt lgkmcnt(8)
Mp 132,8,32
buffer_load_dwordx4 a[132:135], v2, s[20:23], s64 offen offset:1024 sc0 sc1
Mp 144,16,32
Rb 0,8,40960
Mp 144,20,40
buffer_load_dwordx4 a[136:139], v2, s[20:23], s64 offen offset:2048 sc0 sc1
Mp 152,20,44
Rb 4,8,57344
v_exp_f32_e32 v48, v96
Mp 152,16,36
buffer_load_dwordx4 v2, s[20:23], s55 offen offset:1024 sc1 lds
Mp 156,24,36
Rb 8,8,41984
v_exp_f32_e32 v28, v97
Mp 156,28,44
buffer_load_dwordx4 a[140:143], v2, s[20:23], s64 offen offset:3072 sc0 sc1
Mp 148,28,40
Rb 12,8,58368
v_add_f32_e32 v14, v48, v28
s_waitcnt lgkmcnt(8)
Mp 148,24,32
buffer_load_dwordx4 a[144:147], v3, s[20:23], s64 offen offset:0 sc0 sc1
Mp 160,32,32
Rb 16,8,43008
v_exp_f32_e32 v49, v98
Mp 160,36,40
buffer_load_dwordx4 a[148:151], v3, s[20:23], s64 offen offset:1024 sc0 sc1
Cv 48,48,28
Mp 168,36,44
Rb 20,8,59392
v_add_f32_e32 v14, v14, v49
Mp 168,32,36
buffer_load_dwordx4 v2, s[20:23], s55 offen offset:2048 sc1 lds
Mp 172,40,36
Rb 24,8,44032
Mp 172,44,44
buffer_load_dwordx4 a[152:155], v3, s[20:23], s64 offen offset:2048 sc0 sc1
Mp 164,44,40
Rb 28,8,60416
s_waitcnt lgkmcnt(8)
Mp 164,40,32
buffer_load_dwordx4 a[156:159], v3, s[20:23], s64 offen offset:3072 sc0 sc1
Mp 176,48,32
Rb 32,8,45056
Mp 176,52,40
s_add_u32 s64, s64, 0x10000
buffer_load_dwordx4 a[160:163], v2, s[20:23], s64 offen offset:0 sc0 sc1
Mp 184,52,44
Rb 36,8,61440
v_exp_f32_e32 v29, v99
Mp 184,48,36
buffer_load_dwordx4 v2, s[20:23], s55 offen offset:3072 sc1 lds
Mp 188,56,36
Rb 40,8,46080
v_exp_f32_e32 v50, v104
Mp 188,60,44
buffer_load_dwordx4 a[164:167], v2, s[20:23], s64 offen offset:1024 sc0 sc1
Mp 180,60,40
Rb 44,8,62464
)" R"(
v_add_f32_e32 v14, v14, v29
s_waitcnt lgkmcnt(8)
Mp 180,56,32
buffer_load_dwordx4 a[168:171], v2, s[20:23], s64 offen offset:2048 sc0 sc1
Mp 192,0,32
Rb 48,8,47104
v_exp_f32_e32 v24, v105
Mp 192,4,40
buffer_load_dwordx4 a[172:175], v2, s[20:23], s64 offen offset:3072 sc0 sc1
Cv 49,49,29
Mp 200,4,44
Rb 52,8,63488
s_add_u32 m0, s36, 36864
Mp 200,0,36
buffer_load_dwordx4 v3, s[20:23], s55 offen offset:0 sc1 lds
v_add_f32_e32 v14, v14, v50
Mp 204,8,36
Rb 56,8,48128
v_add_f32_e32 v14, v14, v24
Mp 204,12,44
buffer_load_dwordx4 a[176:179], v3, s[20:23], s64 offen offset:0 sc0 sc1
Cv 50,50,24
Mp 196,12,40
Rb 60,8,64512
s_waitcnt lgkmcnt(8)
Mp 196,8,32
buffer_load_dwordx4 a[180:183], v3, s[20:23], s64 offen offset:1024 sc0 sc1
Mp 208,16,32
v_exp_f32_e32 v51, v106
Mp 208,20,40
buffer_load_dwordx4 a[184:187], v3, s[20:23], s64 offen offset:2048 sc0 sc1
v_add_f32_e32 v14, v14, v51
Mp 216,20,44
v_exp_f32_e32 v25, v107
s_sub_u32 s57, s57, 1
Mp 216,16,36
buffer_load_dwordx4 v3, s[20:23], s55 offen offset:1024 sc1 lds
Mp 220,24,36
v_exp_f32_e32 v52, v100
v_add_f32_e32 v14, v14, v25
Mp 220,28,44
buffer_load_dwordx4 a[188:191], v3, s[20:23], s64 offen offset:3072 sc0 sc1
Mp 212,28,40
v_exp_f32_e32 v26, v101
Cv 51,51,25
Mp 212,24,32
s_waitcnt vmcnt(23) lgkmcnt(0)
s_barrier
Mp 224,32,32
Rb 0,8,0
v_exp_f32_e32 v53, v102
Mp 224,36,40
Rb 4,8,16384
v_exp_f32_e32 v27, v103
Mp 232,36,44
Rb 8,8,1024
v_add_f32_e32 v15, v52, v26
Mp 232,32,36
Rb 12,8,17408
buffer_load_dwordx4 v3, s[20:23], s55 offen offset:2048 sc1 lds
Mp 236,40,36
Rb 16,8,2048
Cv 52,52,26
Mp 236,44,44
Rb 20,8,18432
)" R"(
v_exp_f32_e32 v54, v108
Mp 228,44,40
Rb 24,8,3072
v_add_f32_e32 v15, v15, v53
Mp 228,40,32
Rb 28,8,19456
v_exp_f32_e32 v28, v109
Mp 240,48,32
Rb 32,8,4096
v_add_f32_e32 v15, v15, v27
Mp 240,52,40
Rb 36,8,20480
v_exp_f32_e32 v55, v110
Mp 248,52,44
Rb 40,8,5120
v_exp_f32_e32 v29, v111
Mp 248,48,36
Rb 44,8,21504
buffer_load_dwordx4 v3, s[20:23], s55 offen offset:3072 sc1 lds
Mp 252,56,36
Cv 53,53,27
v_add_f32_e32 v15, v15, v54
v_add_f32_e32 v15, v15, v28
Mp 252,60,44
Cv 54,54,28
v_add_f32_e32 v15, v15, v55
Mp 244,60,40
v_add_f32_e32 v15, v15, v29
Cv 55,55,29
Mp 244,56,32
s_waitcnt vmcnt(18)
v_cmp_neq_f32_e32 vcc, v16, v66
v_cmp_neq_f32_e64 s[28:29], v20, v67
s_or_b64 vcc, vcc, s[28:29]
s_cbranch_scc1 .LzMRG
.LzMRR:
Mv 128,194,128
v_exp_f32_e32 v56, v112
Mv 136,194,132
v_exp_f32_e32 v26, v113
v_add_f32_e32 v12, v12, v64
Mv 132,192,128
s_add_u32 m0, s36, 131072
s_add_u32 s40, s67, 0x20000
v_add_f32_e32 v13, v13, v65
Mv 140,192,132
buffer_load_dwordx4 v2, s[20:23], s40 offen offset:0 sc1 lds
Mv 144,194,136
v_exp_f32_e32 v57, v114
v_add_f32_e32 v14, v14, v56
Mv 152,194,140
v_exp_f32_e32 v27, v115
v_add_f32_e32 v14, v14, v26
Mv 148,192,136
Cv 56,56,26
s_waitcnt vmcnt(14)
Mv 156,192,140
buffer_load_dwordx4 v2, s[20:23], s40 offen offset:1024 sc1 lds
v_add_f32_e32 v14, v14, v57
Mv 160,194,144
v_exp_f32_e32 v58, v120
v_add_f32_e32 v14, v14, v27
Mv 168,194,148
v_exp_f32_e32 v28, v121
)" R"(
Mv 164,192,144
v_exp_f32_e32 v59, v122
Cv 57,57,27
Mv 172,192,148
buffer_load_dwordx4 v2, s[20:23], s40 offen offset:2048 sc1 lds
v_add_f32_e32 v14, v14, v58
Mv 176,194,152
v_exp_f32_e32 v29, v123
Mv 184,194,156
v_exp_f32_e32 v60, v116
v_add_f32_e32 v14, v14, v28
Mv 180,192,152
v_exp_f32_e32 v24, v117
s_waitcnt vmcnt(11)
Mv 188,192,156
buffer_load_dwordx4 v2, s[20:23], s40 offen offset:3072 sc1 lds
Cv 58,58,28
Mv 192,194,160
v_exp_f32_e32 v61, v118
Mv 200,194,164
v_exp_f32_e32 v25, v119
v_add_f32_e32 v14, v14, v59
Mv 196,192,160
s_add_u32 m0, s36, 135168
v_exp_f32_e32 v62, v124
Mv 204,192,164
buffer_load_dwordx4 v3, s[20:23], s40 offen offset:0 sc1 lds
Mv 208,194,168
v_exp_f32_e32 v26, v125
v_add_f32_e32 v14, v14, v29
.LzH18:
s_cbranch_execz .LzHR
Mv 216,194,172
v_exp_f32_e32 v63, v126
Cv 59,59,29
Mv 212,192,168
v_add_f32_e32 v15, v15, v60
s_waitcnt vmcnt(7)
Mv 220,192,172
buffer_load_dwordx4 v3, s[20:23], s40 offen offset:1024 sc1 lds
v_add_f32_e32 v15, v15, v24
Mv 224,194,176
v_exp_f32_e32 v27, v127
Cv 60,60,24
Mv 232,194,180
v_add_f32_e32 v15, v15, v61
v_add_f32_e32 v15, v15, v25
Mv 228,192,176
Cv 61,61,25
v_add_f32_e32 v15, v15, v62
v_add_f32_e32 v15, v15, v26
Mv 236,192,180
buffer_load_dwordx4 v3, s[20:23], s40 offen offset:2048 sc1 lds
Cv 62,62,26
Mv 240,194,184
v_add_f32_e32 v15, v15, v63
v_add_f32_e32 v15, v15, v27
Mv 248,194,188
s_add_u32 s40, s67, 0x30000
Cv 63,63,27
v_max_f32_e32 v11, v14, v15
Mv 244,192,184
s_add_u32 m0, s36, 98304
v_cmp_nge_f32_e32 vcc, s62, v11
)" R"(
s_waitcnt lgkmcnt(8)
Mv 252,192,188
buffer_load_dwordx4 v2, s[20:23], s40 offen offset:0 sc1 lds
s_cbranch_vccnz .Lzfx13
.Lzfr13:
Mp 128,0,48
Rb 48,8,6144
v_add_f32_e32 v12, v12, v14
Mp 128,4,56
v_add_f32_e32 v13, v13, v15
v_mov_b32 v24, v12
v_mov_b32 v25, v13
Mp 136,4,60
Rb 52,8,22528
v_permlane32_swap_b32_e32 v12, v24
v_add_f32_e32 v12, v12, v24
Mp 136,0,52
buffer_load_dwordx4 v2, s[20:23], s40 offen offset:1024 sc1 lds
v_mov_b32 v24, v12
Mp 140,8,52
Rb 56,8,7168
v_permlane16_swap_b32_e32 v12, v24
Mp 140,12,60
v_add_f32_e32 v12, v12, v24
v_rcp_f32_e32 v12, v12
Mp 132,12,56
Rb 60,8,23552
v_permlane32_swap_b32_e32 v13, v25
s_waitcnt lgkmcnt(8)
Mp 132,8,48
buffer_load_dwordx4 v2, s[20:23], s40 offen offset:2048 sc1 lds
v_add_f32_e32 v13, v13, v25
Mp 144,16,48
Rb 0,8,8192
v_mov_b32 v25, v13
Mp 144,20,56
s_nop 0
v_permlane16_swap_b32_e32 v13, v25
v_add_f32_e32 v13, v13, v25
Mp 152,20,60
Rb 4,8,24576
v_rcp_f32_e32 v13, v13
Mp 152,16,52
buffer_load_dwordx4 v2, s[20:23], s40 offen offset:3072 sc1 lds
v_mul_f32_e32 v128, v12, v128
Mp 156,24,52
Rb 8,8,9216
v_mul_f32_e32 v129, v12, v129
Mp 156,28,60
s_add_u32 m0, s36, 102400
Cv 128,128,129
v_mul_f32_e32 v130, v12, v130
Mp 148,28,56
Rb 12,8,25600
v_mul_f32_e32 v131, v12, v131
s_waitcnt lgkmcnt(8)
Mp 148,24,48
buffer_load_dwordx4 v3, s[20:23], s40 offen offset:0 sc1 lds
Cv 129,130,131
Mp 160,32,48
Rb 16,8,10240
v_mul_f32_e32 v132, v12, v132
Mp 160,36,56
v_mul_f32_e32 v133, v12, v133
)" R"(
Cv 130,132,133
v_mul_f32_e32 v134, v12, v134
Mp 168,36,60
Rb 20,8,26624
v_mul_f32_e32 v135, v12, v135
Cv 131,134,135
Mp 168,32,52
buffer_load_dwordx4 v3, s[20:23], s40 offen offset:1024 sc1 lds
v_mul_f32_e32 v136, v13, v136
Mp 172,40,52
Rb 24,8,11264
v_mul_f32_e32 v137, v13, v137
Mp 172,44,60
Cv 136,136,137
v_mul_f32_e32 v138, v13, v138
v_mul_f32_e32 v139, v13, v139
Mp 164,44,56
Rb 28,8,27648
Cv 137,138,139
s_waitcnt lgkmcnt(8)
Mp 164,40,48
buffer_load_dwordx4 v3, s[20:23], s40 offen offset:2048 sc1 lds
v_mul_f32_e32 v140, v13, v140
Mp 176,48,48
Rb 32,8,12288
v_mul_f32_e32 v141, v13, v141
Mp 176,52,56
Cv 138,140,141
v_mul_f32_e32 v142, v13, v142
v_mul_f32_e32 v143, v13, v143
Mp 184,52,60
Rb 36,8,28672
Cv 139,142,143
v_mul_f32_e32 v144, v12, v144
Mp 184,48,52
v_mul_f32_e32 v145, v12, v145
Cv 144,144,145
v_mul_f32_e32 v146, v12, v146
Mp 188,56,52
Rb 40,8,13312
v_mul_f32_e32 v147, v12, v147
Mp 188,60,60
Cv 145,146,147
v_mul_f32_e32 v148, v12, v148
v_mul_f32_e32 v149, v12, v149
Mp 180,60,56
Rb 44,8,29696
Cv 146,148,149
s_waitcnt lgkmcnt(8)
Mp 180,56,48
v_mul_f32_e32 v150, v12, v150
v_mul_f32_e32 v151, v12, v151
Cv 147,150,151
Mp 192,0,48
Rb 48,8,14336
v_mul_f32_e32 v152, v13, v152
Mp 192,4,56
v_mul_f32_e32 v153, v13, v153
Cv 152,152,153
v_mul_f32_e32 v154, v13, v154
Mp 200,4,60
Rb 52,8,30720
v_mul_f32_e32 v155, v13, v155
Cv 153,154,155
)" R"(
Mp 200,0,52
v_mul_f32_e32 v156, v13, v156
v_mul_f32_e32 v157, v13, v157
Cv 154,156,157
Mp 204,8,52
Rb 56,8,15360
v_mul_f32_e32 v158, v13, v158
Mp 204,12,60
v_mul_f32_e32 v159, v13, v159
Cv 155,158,159
v_mul_f32_e32 v160, v12, v160
Mp 196,12,56
Rb 60,8,31744
v_mul_f32_e32 v161, v12, v161
s_waitcnt lgkmcnt(8)
Mp 196,8,48
Cv 160,160,161
v_mul_f32_e32 v162, v12, v162
v_mul_f32_e32 v163, v12, v163
Mp 208,16,48
Rb 0,1,0
Cv 161,162,163
Mp 208,20,56
v_mul_f32_e32 v164, v12, v164
v_mul_f32_e32 v165, v12, v165
Cv 162,164,165
Mp 216,20,60
Rb 4,1,1024
v_mul_f32_e32 v166, v12, v166
v_mul_f32_e32 v167, v12, v167
Mp 216,16,52
Cv 163,166,167
v_mul_f32_e32 v168, v13, v168
v_mul_f32_e32 v169, v13, v169
Mp 220,24,52
Rb 8,1,2048
Cv 168,168,169
Mp 220,28,60
v_mul_f32_e32 v170, v13, v170
v_mul_f32_e32 v171, v13, v171
Cv 169,170,171
Mp 212,28,56
Rb 12,1,3072
s_waitcnt vmcnt(14) lgkmcnt(4)
s_barrier
Mp 212,24,48
s_add_u32 s40, s67, 0x40000
v_mul_f32_e32 v172, v13, v172
v_mul_f32_e32 v173, v13, v173
Mp 224,32,48
Rb 16,1,4096
s_add_u32 m0, s36, 65536
Mp 224,36,56
buffer_load_dwordx4 v2, s[20:23], s40 offen offset:0 sc1 lds
Cv 170,172,173
Mp 232,36,60
Rb 20,1,5120
v_mul_f32_e32 v174, v13, v174
v_mul_f32_e32 v175, v13, v175
Mp 232,32,52
Cv 171,174,175
v_mul_f32_e32 v176, v12, v176
v_mul_f32_e32 v177, v12, v177
Mp 236,40,52
Rb 24,1,6144
)" R"(
Cv 176,176,177
s_lshl_b32 s64, s35, 17
Mp 236,44,60
v_mul_f32_e32 v178, v12, v178
v_mul_f32_e32 v179, v12, v179
Cv 177,178,179
Mp 228,44,56
Rb 28,1,8192
v_mul_f32_e32 v180, v12, v180
Mp 228,40,48
buffer_load_dwordx4 v2, s[20:23], s40 offen offset:1024 sc1 lds
v_mul_f32_e32 v181, v12, v181
Mp 240,48,48
Rb 32,1,9216
Cv 178,180,181
s_add_u32 s64, s64, s89
Mp 240,52,56
v_mul_f32_e32 v182, v12, v182
v_mul_f32_e32 v183, v12, v183
Cv 179,182,183
Mp 248,52,60
Rb 36,1,10240
v_mul_f32_e32 v184, v13, v184
v_mul_f32_e32 v185, v13, v185
Mp 248,48,52
Cv 184,184,185
v_mul_f32_e32 v186, v13, v186
v_mul_f32_e32 v187, v13, v187
Mp 252,56,52
Rb 40,1,11264
Cv 185,186,187
s_add_u32 s64, s64, 0x900000
Mp 252,60,60
buffer_load_dwordx4 v2, s[20:23], s40 offen offset:2048 sc1 lds
v_mul_f32_e32 v188, v13, v188
Mp 244,60,56
Rb 44,1,12288
v_mul_f32_e32 v189, v13, v189
s_mov_b32 s55, 0x1508c00
s_waitcnt lgkmcnt(8)
Mp 244,56,48
Cv 186,188,189
v_mul_f32_e32 v190, v13, v190
v_mul_f32_e32 v191, v13, v191
v_mfma_f32_16x16x32_bf16 v[32:35], a[0:3], v[128:131], 0
Rb 48,1,13312
Cv 187,190,191
s_xor_b32 s66, s64, 0x202000
v_mfma_f32_16x16x32_bf16 v[40:43], a[0:3], v[136:139], 0
v_mul_f32_e32 v192, v12, v192
v_mul_f32_e32 v193, v12, v193
Cv 192,192,193
v_mfma_f32_16x16x32_bf16 v[44:47], a[4:7], v[136:139], 0
Rb 52,1,14336
v_mul_f32_e32 v194, v12, v194
v_mul_f32_e32 v195, v12, v195
v_mfma_f32_16x16x32_bf16 v[36:39], a[4:7], v[128:131], 0
buffer_load_dwordx4 v2, s[20:23], s40 offen offset:3072 sc1 lds
Cv 193,194,195
v_mfma_f32_16x16x32_bf16 v[48:51], a[8:11], v[128:131], 0
Rb 56,1,16384
v_mul_f32_e32 v196, v12, v196
s_sub_u32 s66, s66, s84
v_mfma_f32_16x16x32_bf16 v[56:59], a[8:11], v[136:139], 0
)" R"(
v_mul_f32_e32 v197, v12, v197
Cv 194,196,197
v_mul_f32_e32 v198, v12, v198
v_mfma_f32_16x16x32_bf16 v[60:63], a[12:15], v[136:139], 0
Rb 60,1,17408
v_mul_f32_e32 v199, v12, v199
s_cmp_lg_u32 s33, 0
s_cmov_b32 s55, 0x40000000
s_waitcnt lgkmcnt(8)
v_mfma_f32_16x16x32_bf16 v[52:55], a[12:15], v[128:131], 0
Cv 195,198,199
v_mul_f32_e32 v200, v13, v200
v_mul_f32_e32 v201, v13, v201
v_mov_b32 v16, 1
Mp 32,16,144
Rb 0,1,18432
s_add_u32 m0, s36, 69632
s_sub_u32 s66, s66, s84
v_add_u32 v9, s36, v8
Mp 40,16,152
buffer_load_dwordx4 v3, s[20:23], s40 offen offset:0 sc1 lds
Cv 200,200,201
Mp 44,20,152
Rb 4,1,19456
v_mul_f32_e32 v202, v13, v202
v_mul_f32_e32 v203, v13, v203
Mp 36,20,144
Cv 201,202,203
v_mul_f32_e32 v204, v13, v204
v_mul_f32_e32 v205, v13, v205
Mp 48,24,144
Rb 8,1,20480
Cv 202,204,205
s_lshl2_add_u32 s65, s35, s33
Mp 56,24,152
v_mul_f32_e32 v206, v13, v206
v_mul_f32_e32 v207, v13, v207
Cv 203,206,207
Mp 60,28,152
Rb 12,1,21504
v_mul_f32_e32 v208, v12, v208
s_waitcnt lgkmcnt(8)
Mp 52,28,144
buffer_load_dwordx4 v3, s[20:23], s40 offen offset:1024 sc1 lds
v_mul_f32_e32 v209, v12, v209
Mp 32,32,160
Rb 16,1,22528
Cv 208,208,209
s_lshl_b32 s65, s65, 8
Mp 40,32,168
v_mul_f32_e32 v210, v12, v210
v_mul_f32_e32 v211, v12, v211
Cv 209,210,211
Mp 44,36,168
Rb 20,1,24576
v_mul_f32_e32 v212, v12, v212
v_mul_f32_e32 v213, v12, v213
Mp 36,36,160
Cv 210,212,213
v_mul_f32_e32 v214, v12, v214
v_mul_f32_e32 v215, v12, v215
Mp 48,40,160
Rb 24,1,25600
Cv 211,214,215
)" R"(
s_add_u32 s65, s65, s50
Mp 56,40,168
buffer_load_dwordx4 v3, s[20:23], s40 offen offset:2048 sc1 lds
v_mul_f32_e32 v216, v13, v216
Mp 60,44,168
Rb 28,1,26624
v_mul_f32_e32 v217, v13, v217
s_waitcnt lgkmcnt(8)
Mp 52,44,160
Cv 216,216,217
v_mul_f32_e32 v218, v13, v218
v_mul_f32_e32 v219, v13, v219
Mp 32,48,176
Rb 32,1,27648
Cv 217,218,219
s_add_u32 s65, s65, 0x6400
Mp 40,48,184
v_mul_f32_e32 v220, v13, v220
v_mul_f32_e32 v221, v13, v221
Cv 218,220,221
Mp 44,52,184
Rb 36,1,28672
v_mul_f32_e32 v222, v13, v222
v_mul_f32_e32 v223, v13, v223
Mp 36,52,176
Cv 219,222,223
v_mul_f32_e32 v224, v12, v224
v_mul_f32_e32 v225, v12, v225
Mp 48,56,176
Rb 40,1,29696
Cv 224,224,225
s_xor_b32 s52, s65, 0x202000
Mp 56,56,184
v_mul_f32_e32 v226, v12, v226
v_mul_f32_e32 v227, v12, v227
Cv 225,226,227
Mp 60,60,184
Rb 44,1,30720
v_mul_f32_e32 v228, v12, v228
s_waitcnt lgkmcnt(8)
Mp 52,60,176
v_mul_f32_e32 v229, v12, v229
Cv 226,228,229
v_mul_f32_e32 v230, v12, v230
Mp 32,0,192
Rb 48,1,32768
v_mul_f32_e32 v231, v12, v231
buffer_atomic_add v16, v7, s[20:23], s55 offen sc0 sc1
Mp 40,0,200
Cv 227,230,231
v_mul_f32_e32 v232, v13, v232
v_mul_f32_e32 v233, v13, v233
Mp 44,4,200
Rb 52,1,33792
Cv 232,232,233
v_mul_f32_e32 v234, v13, v234
Mp 36,4,192
v_mul_f32_e32 v235, v13, v235
Cv 233,234,235
v_mul_f32_e32 v236, v13, v236
Mp 48,8,192
Rb 56,1,34816
v_mul_f32_e32 v237, v13, v237
Mp 56,8,200
)" R"(
Cv 234,236,237
v_mul_f32_e32 v238, v13, v238
v_mul_f32_e32 v239, v13, v239
Mp 60,12,200
Rb 60,1,35840
s_waitcnt vmcnt(15) lgkmcnt(4)
s_barrier
Mp 52,12,192
s_add_u32 s40, s67, 0x50000
Cv 235,238,239
v_mul_f32_e32 v240, v12, v240
Mp 32,16,208
Rb 0,1,36864
s_add_u32 m0, s36, 0
Mp 40,16,216
buffer_load_dwordx4 v2, s[20:23], s40 offen offset:0 sc1 lds
v_mul_f32_e32 v241, v12, v241
Mp 44,20,216
Rb 4,1,37888
Cv 240,240,241
v_mul_f32_e32 v242, v12, v242
Mp 36,20,208
v_mul_f32_e32 v243, v12, v243
Cv 241,242,243
v_mul_f32_e32 v244, v12, v244
Mp 48,24,208
Rb 8,1,38912
v_mul_f32_e32 v245, v12, v245
Mp 56,24,216
Cv 242,244,245
v_mul_f32_e32 v246, v12, v246
v_mul_f32_e32 v247, v12, v247
Mp 60,28,216
Rb 12,1,40960
Cv 243,246,247
Mp 52,28,208
buffer_load_dwordx4 v2, s[20:23], s40 offen offset:1024 sc1 lds
v_mul_f32_e32 v248, v13, v248
Mp 32,32,224
Rb 16,1,41984
v_mul_f32_e32 v249, v13, v249
Mp 40,32,232
Cv 248,248,249
v_mul_f32_e32 v250, v13, v250
v_mul_f32_e32 v251, v13, v251
Mp 44,36,232
Rb 20,1,43008
Cv 249,250,251
v_mul_f32_e32 v252, v13, v252
Mp 36,36,224
v_mul_f32_e32 v253, v13, v253
Cv 250,252,253
v_mul_f32_e32 v254, v13, v254
Mp 48,40,224
Rb 24,1,44032
v_mul_f32_e32 v255, v13, v255
Mp 56,40,232
buffer_load_dwordx4 v2, s[20:23], s40 offen offset:2048 sc1 lds
Cv 251,254,255
Mp 60,44,232
Rb 28,1,45056
s_waitcnt lgkmcnt(8)
Mp 52,44,224
Mp 32,48,240
Rb 32,1,46080
)" R"(
Mp 40,48,248
Mp 44,52,248
Rb 36,1,47104
Mp 36,52,240
buffer_load_dwordx4 v2, s[20:23], s40 offen offset:3072 sc1 lds
Mp 48,56,240
Rb 40,1,49152
Mp 56,56,248
Mp 60,60,248
Rb 44,1,50176
s_waitcnt lgkmcnt(8)
Mp 52,60,240
v_mfma_f32_16x16x32_bf16 v[64:67], a[0:3], v[128:131], 0
Rb 48,1,51200
s_add_u32 m0, s36, 4096
Mp 64,4,144
buffer_load_dwordx4 v3, s[20:23], s40 offen offset:0 sc1 lds
Mp 64,8,160
Rb 52,1,52224
Mp 64,12,176
Cv 32,32,33
Cv 33,34,35
v_mfma_f32_16x16x32_bf16 v[72:75], a[12:15], v[184:187], 0
Rb 56,1,53248
Cv 34,36,37
Mp 72,8,168
Cv 35,38,39
Mp 72,4,152
Rb 60,1,54272
s_waitcnt lgkmcnt(8)
Mp 72,0,136
buffer_load_dwordx4 v3, s[20:23], s40 offen offset:1024 sc1 lds
Mp 72,16,200
Rb 0,1,55296
Mp 72,20,216
buffer_store_dwordx4 v[32:35], v2, s[20:23], s64 offen offset:0 sc0 sc1
Mp 72,24,232
Rb 4,1,57344
Mp 72,28,248
Mp 64,28,240
Rb 8,1,58368
Mp 64,24,224
buffer_load_dwordx4 v3, s[20:23], s40 offen offset:2048 sc1 lds
Mp 64,20,208
Rb 12,1,59392
s_waitcnt lgkmcnt(8)
Mp 64,16,192
Cv 40,40,41
Cv 41,42,43
v_mfma_f32_16x16x32_bf16 v[68:71], a[32:35], v[128:131], 0
Rb 16,1,60416
Cv 42,44,45
Mp 68,36,144
Cv 43,46,47
Mp 68,40,160
Rb 20,1,61440
Mp 68,44,176
buffer_store_dwordx4 v[40:43], v2, s[20:23], s64 offen offset:1024 sc0 sc1
v_mfma_f32_16x16x32_bf16 v[76:79], a[44:47], v[184:187], 0
Rb 24,1,62464
Mp 76,40,168
Mp 76,36,152
Rb 28,1,63488
s_waitcnt lgkmcnt(8)
)" R"(
Mp 76,32,136
Mp 76,48,200
Rb 32,10,0
Mp 76,52,216
Cv 48,48,49
Cv 49,50,51
Mp 76,56,232
Rb 36,10,1024
Cv 50,52,53
Mp 76,60,248
Cv 51,54,55
Mp 68,60,240
Rb 40,10,2048
Mp 68,56,224
buffer_store_dwordx4 v[48:51], v2, s[20:23], s64 offen offset:2048 sc0 sc1
Mp 68,52,208
Rb 44,10,3072
s_waitcnt vmcnt(18) lgkmcnt(4)
s_barrier
Mp 68,48,192
s_add_u32 s40, s67, 0x60000
v_mfma_f32_16x16x32_bf16 v[80:83], a[0:3], v[128:131], 0
Rb 48,10,4096
s_add_u32 m0, s36, 32768
Mp 80,4,144
buffer_load_dwordx4 v2, s[20:23], s40 offen offset:0 sc1 lds
Mp 80,8,160
Rb 52,10,5120
Mp 80,12,176
Cv 56,56,57
Cv 57,58,59
v_mfma_f32_16x16x32_bf16 v[88:91], a[12:15], v[184:187], 0
Rb 56,10,6144
Cv 58,60,61
Mp 88,8,168
Cv 59,62,63
Mp 88,4,152
Rb 60,10,8192
Mp 88,0,136
buffer_load_dwordx4 v2, s[20:23], s40 offen offset:1024 sc1 lds
Mp 88,16,200
Rb 0,10,9216
Mp 88,20,216
buffer_store_dwordx4 v[56:59], v2, s[20:23], s64 offen offset:3072 sc0 sc1
Mp 88,24,232
Rb 4,10,10240
Mp 88,28,248
Mp 80,28,240
Rb 8,10,11264
Mp 80,24,224
buffer_load_dwordx4 v2, s[20:23], s40 offen offset:2048 sc1 lds
Mp 80,20,208
Rb 12,10,12288
s_waitcnt lgkmcnt(8)
Mp 80,16,192
Cv 64,64,65
Cv 65,66,67
v_mfma_f32_16x16x32_bf16 v[84:87], a[32:35], v[128:131], 0
Rb 16,10,13312
Cv 66,68,69
Mp 84,36,144
Cv 67,70,71
Mp 84,40,160
Rb 20,10,14336
.LzH19:
)" R"(
s_cbranch_execz .LzHR
Mp 84,44,176
buffer_load_dwordx4 v2, s[20:23], s40 offen offset:3072 sc1 lds
v_mfma_f32_16x16x32_bf16 v[92:95], a[44:47], v[184:187], 0
Rb 24,10,16384
Mp 92,40,168
buffer_store_dwordx4 v[64:67], v3, s[20:23], s64 offen offset:0 sc0 sc1
Mp 92,36,152
Rb 28,10,17408
s_waitcnt lgkmcnt(8)
Mp 92,32,136
Mp 92,48,200
Rb 32,10,18432
s_add_u32 m0, s36, 36864
Mp 92,52,216
buffer_load_dwordx4 v3, s[20:23], s40 offen offset:0 sc1 lds
Mp 92,56,232
Rb 36,10,19456
Mp 92,60,248
Cv 72,72,73
Cv 73,74,75
Mp 84,60,240
Rb 40,10,20480
Cv 74,76,77
Mp 84,56,224
Cv 75,78,79
Mp 84,52,208
Rb 44,10,21504
s_waitcnt lgkmcnt(8)
Mp 84,48,192
buffer_load_dwordx4 v3, s[20:23], s40 offen offset:1024 sc1 lds
v_mfma_f32_16x16x32_bf16 v[96:99], a[0:3], v[128:131], 0
Rb 48,10,22528
Mp 96,4,144
buffer_store_dwordx4 v[72:75], v3, s[20:23], s64 offen offset:1024 sc0 sc1
Mp 96,8,160
Rb 52,10,24576
Mp 96,12,176
v_mfma_f32_16x16x32_bf16 v[104:107], a[12:15], v[184:187], 0
Rb 56,10,25600
Mp 104,8,168
buffer_load_dwordx4 v3, s[20:23], s40 offen offset:2048 sc1 lds
Mp 104,4,152
Rb 60,10,26624
s_waitcnt lgkmcnt(8)
Mp 104,0,136
Cv 80,80,81
Cv 81,82,83
Mp 104,16,200
Rb 0,10,27648
Cv 82,84,85
Mp 104,20,216
Cv 83,86,87
Mp 104,24,232
Rb 4,10,28672
Mp 104,28,248
buffer_store_dwordx4 v[80:83], v3, s[20:23], s64 offen offset:2048 sc0 sc1
Mp 96,28,240
Rb 8,10,29696
Mp 96,24,224
Mp 96,20,208
Rb 12,10,30720
s_waitcnt lgkmcnt(8)
Mp 96,16,192
)" R"(
v_mfma_f32_16x16x32_bf16 v[100:103], a[32:35], v[128:131], 0
Rb 16,8,32768
Mp 100,36,144
Cv 88,88,89
Cv 89,90,91
Mp 100,40,160
Rb 20,8,33792
Cv 90,92,93
Mp 100,44,176
Cv 91,94,95
v_mfma_f32_16x16x32_bf16 v[108:111], a[44:47], v[184:187], 0
Rb 24,8,34816
Mp 108,40,168
buffer_store_dwordx4 v[88:91], v3, s[20:23], s64 offen offset:3072 sc0 sc1
Mp 108,36,152
Rb 28,8,35840
s_waitcnt vmcnt(23) lgkmcnt(4)
s_barrier
Mp 108,32,136
s_add_u32 s40, s67, 0x70000
Mp 108,48,200
Rb 32,8,36864
s_add_u32 m0, s36, 131072
Mp 108,52,216
buffer_load_dwordx4 v2, s[20:23], s40 offen offset:0 sc1 lds
Mp 108,56,232
Rb 36,8,37888
s_mov_b32 s8, s74
Mp 108,60,248
s_mov_b32 s9, s75
s_mov_b32 s10, 0x1c0000
Mp 100,60,240
Rb 40,8,38912
s_mov_b32 s11, 0x20000
Mp 100,56,224
v_and_b32 v174, 1, v0
v_mul_u32_u24 v174, 0xffff, v174
Mp 100,52,208
Rb 44,8,40960
Mp 100,48,192
buffer_load_dwordx4 v2, s[20:23], s40 offen offset:1024 sc1 lds
v_mfma_f32_16x16x32_bf16 v[112:115], a[0:3], v[128:131], 0
Rb 48,8,41984
v_add_u32 v174, 1, v174
Mp 112,4,144
v_mul_u32_u24 v174, 0x3f80, v174
v_bfe_u32 v173, v0, 1, 3
Mp 112,8,160
Rb 52,8,43008
v_cmp_eq_u32_e32 vcc, 0, v173
Mp 112,12,176
Cv 96,96,97
Cv 97,98,99
v_mfma_f32_16x16x32_bf16 v[120:123], a[12:15], v[184:187], 0
Rb 56,8,44032
Cv 98,100,101
Mp 120,8,168
buffer_load_dwordx4 v2, s[20:23], s40 offen offset:2048 sc1 lds
Mp 120,4,152
Rb 60,8,45056
s_waitcnt lgkmcnt(8)
Mp 120,0,136
Cv 99,102,103
v_cndmask_b32_e32 v132, 0, v174, vcc
v_cmp_eq_u32_e32 vcc, 1, v173
)" R"(
Mp 120,16,200
Rb 0,8,46080
v_cndmask_b32_e32 v133, 0, v174, vcc
Mp 120,20,216
s_add_u32 s64, s64, 0x10000
buffer_store_dwordx4 v[96:99], v2, s[20:23], s64 offen offset:0 sc0 sc1
Mp 120,24,232
Rb 4,8,47104
v_cmp_eq_u32_e32 vcc, 2, v173
Mp 120,28,248
buffer_load_dwordx4 v2, s[20:23], s40 offen offset:3072 sc1 lds
Mp 112,28,240
Rb 8,8,49152
v_cndmask_b32_e32 v134, 0, v174, vcc
Mp 112,24,224
v_cmp_eq_u32_e32 vcc, 3, v173
v_and_b32 v167, 0xf0, v1
Mp 112,20,208
Rb 12,8,50176
s_waitcnt lgkmcnt(8)
Mp 112,16,192
Cv 104,104,105
Cv 105,106,107
v_mfma_f32_16x16x32_bf16 v[116:119], a[32:35], v[128:131], 0
Rb 16,8,51200
s_add_u32 m0, s36, 135168
Mp 116,36,144
buffer_load_dwordx4 v3, s[20:23], s40 offen offset:0 sc1 lds
Mp 116,40,160
Rb 20,8,52224
Cv 106,108,109
Mp 116,44,176
Cv 107,110,111
v_cndmask_b32_e32 v135, 0, v174, vcc
v_mfma_f32_16x16x32_bf16 v[124:127], a[44:47], v[184:187], 0
Rb 24,8,53248
v_cmp_eq_u32_e32 vcc, 4, v173
Mp 124,40,168
buffer_store_dwordx4 v[104:107], v2, s[20:23], s64 offen offset:1024 sc0 sc1
Mp 124,36,152
Rb 28,8,54272
s_waitcnt lgkmcnt(8)
Mp 124,32,136
buffer_load_dwordx4 v3, s[20:23], s40 offen offset:1024 sc1 lds
v_cndmask_b32_e32 v140, 0, v174, vcc
Mp 124,48,200
Rb 32,8,55296
v_cmp_eq_u32_e32 vcc, 5, v173
Mp 124,52,216
s_nop 0
v_cndmask_b32_e32 v141, 0, v174, vcc
v_cmp_eq_u32_e32 vcc, 6, v173
Mp 124,56,232
Rb 36,8,57344
v_cndmask_b32_e32 v142, 0, v174, vcc
Mp 124,60,248
v_cmp_eq_u32_e32 vcc, 7, v173
v_lshrrev_b32 v173, 4, v0
Mp 116,60,240
Rb 40,8,58368
v_cndmask_b32_e32 v143, 0, v174, vcc
Mp 116,56,224
buffer_load_dwordx4 v3, s[20:23], s40 offen offset:2048 sc1 lds
Mp 116,52,208
)" R"(
Rb 44,8,59392
s_waitcnt vmcnt(9) lgkmcnt(8)
buffer_store_dword v6, v7, s[20:23], s65 offen offset:0 sc0 sc1
Mp 116,48,192
v_lshl_or_b32 v164, v173, 25, v167
v_mul_u32_u24 v166, 56, v167
v_mfma_f32_16x16x32_bf16 v[32:35], a[0:3], v[128:131], 0
Rb 48,8,60416
v_lshl_add_u32 v166, v173, 3, v166
Mp 32,4,144
v_lshrrev_b32_e64 v173, v173, s85
v_bfe_u32 v174, v173, 4, 1
Mp 32,8,160
Rb 52,8,61440
v_lshl_or_b32 v183, v174, 13, v164
Mp 32,12,176
Cv 112,112,113
Cv 113,114,115
v_mfma_f32_16x16x32_bf16 v[40:43], a[12:15], v[184:187], 0
Rb 56,8,62464
Cv 114,116,117
Mp 40,8,168
Cv 115,118,119
v_and_b32 v174, 1, v173
Mp 40,4,152
Rb 60,8,63488
s_waitcnt lgkmcnt(8)
Mp 40,0,136
buffer_store_dwordx4 v[112:115], v2, s[20:23], s64 offen offset:2048 sc0 sc1
ds_write_b32 v9, v16 offset:39936
Mp 40,16,200
Rb 0,8,0
v_lshl_or_b32 v164, v174, 13, v164
Mp 40,20,216
v_and_b32 v174, 7, v0
v_and_b32 v165, 0x80, v1
Mp 40,24,232
Rb 4,8,1024
v_lshl_or_b32 v165, v174, 8, v165
Mp 40,28,248
v_lshlrev_b32 v165, 1, v165
s_mov_b32 s93, 0x1111
Mp 32,28,240
Rb 8,8,2048
v_lshrrev_b32 v173, 2, v0
Mp 32,24,224
Cv 120,120,121
Cv 121,122,123
Mp 32,20,208
Rb 12,8,3072
s_waitcnt lgkmcnt(3)
s_barrier
Mp 32,16,192
Cv 122,124,125
Cv 123,126,127
ds_read_b32 v16, v8 offset:39936
v_mfma_f32_16x16x32_bf16 v[36:39], a[32:35], v[128:131], 0
Rb 16,8,4096
v_mul_u32_u24 v173, 832, v173
Mp 36,36,144
buffer_store_dwordx4 v[120:123], v2, s[20:23], s64 offen offset:3072 sc0 sc1
Mp 36,40,160
Rb 20,8,5120
v_add_u32 v173, v173, v1
Mp 36,44,176
)" R"(
v_lshrrev_b32 v174, 1, v0
v_and_b32 v174, 24, v174
v_mfma_f32_16x16x32_bf16 v[44:47], a[44:47], v[184:187], 0
Rb 24,8,6144
v_lshl_add_u32 v174, v167, 2, v174
Mp 44,40,168
s_add_u32 s59, s36, 98304
v_add_u32 v174, s59, v174
Mp 44,36,152
Rb 28,8,8192
s_waitcnt lgkmcnt(4)
Mp 44,32,136
v_readfirstlane_b32 s82, v16
Mp 44,48,200
Rb 32,8,9216
s_lshl2_add_u32 s56, s82, s33
Mp 44,52,216
s_mul_i32 s88, s56, 4682
s_lshr_b32 s58, s88, 16
Mp 44,56,232
Rb 36,8,10240
s_mul_i32 s57, s58, 14
Mp 44,60,248
s_sub_u32 s57, s56, s57
s_lshl_b32 s40, s58, 12
Mp 36,60,240
Rb 40,8,11264
Mp 36,56,224
s_cmp_gt_u32 s57, 6
s_cselect_b32 s88, 128, 0
Mp 36,52,208
Rb 44,8,12288
Mp 36,48,192
buffer_load_dword v4, v7, s[20:23], s52 offen offset:0 sc0 sc1
s_add_u32 s40, s40, s88
v_mfma_f32_16x16x32_bf16 v[48:51], a[0:3], v[128:131], 0
Rb 48,8,13312
s_add_u32 s40, s40, 0x1510000
Mp 48,4,144
s_lshl_b32 s55, s58, 6
s_add_u32 s55, s55, s57
Mp 48,8,160
Rb 52,8,14336
Mp 48,12,176
Cv 32,32,33
Cv 33,34,35
v_mfma_f32_16x16x32_bf16 v[56:59], a[12:15], v[184:187], 0
Rb 56,8,16384
Cv 34,36,37
Mp 56,8,168
Cv 35,38,39
Mp 56,4,152
Rb 60,8,17408
s_waitcnt lgkmcnt(8)
Mp 56,0,136
buffer_store_dwordx4 v[32:35], v3, s[20:23], s64 offen offset:0 sc0 sc1
Mp 56,16,200
Rb 0,8,18432
Mp 56,20,216
s_cmp_gt_u32 s57, 6
s_cselect_b32 s88, 9, 0
Mp 56,24,232
Rb 4,8,19456
s_add_u32 s55, s55, s88
)" R"(
Mp 56,28,248
s_and_b32 s88, s58, 32
s_lshl_b32 s88, s88, 8
Mp 48,28,240
Rb 8,8,20480
v_xor_b32 v164, s88, v164
Mp 48,24,224
Cv 40,40,41
Cv 41,42,43
Mp 48,20,208
Rb 12,8,21504
s_waitcnt lgkmcnt(8)
Mp 48,16,192
Cv 42,44,45
Cv 43,46,47
v_xor_b32 v183, s88, v183
v_mfma_f32_16x16x32_bf16 v[52:55], a[32:35], v[128:131], 0
Rb 16,8,22528
s_lshl_b32 s55, s55, 10
Mp 52,36,144
buffer_store_dwordx4 v[40:43], v3, s[20:23], s64 offen offset:1024 sc0 sc1
Mp 52,40,160
Rb 20,8,24576
s_add_u32 s55, s55, 0x1000000
Mp 52,44,176
s_and_b32 s88, s57, 1
s_lshl1_add_u32 s58, s58, s88
v_mfma_f32_16x16x32_bf16 v[60:63], a[44:47], v[184:187], 0
Rb 24,8,25600
s_lshl_b32 s88, s58, 8
Mp 60,40,168
v_add_u32 v167, s88, v167
s_mul_i32 s59, s58, 14336
Mp 60,36,152
Rb 28,8,26624
s_waitcnt lgkmcnt(8)
Mp 60,32,136
s_lshr_b32 s88, s57, 1
s_lshl_b32 s88, s88, 6
s_add_u32 s88, s88, s59
Mp 60,48,200
Rb 32,8,27648
v_add_u32 v166, s88, v166
Mp 60,52,216
s_mov_b32 s57, 0
s_add_u32 m0, s36, 98304
Mp 60,56,232
Rb 36,8,28672
Mp 60,60,248
buffer_load_dwordx4 v173, s[12:15], s88 offen offset:0 lds
Mp 52,60,240
Rb 40,8,29696
s_add_u32 m0, s36, 98880
Mp 52,56,224
buffer_load_dwordx4 v173, s[12:15], s88 offen offset:448 lds
Mp 52,52,208
Rb 44,8,30720
s_waitcnt lgkmcnt(8)
Mp 52,48,192
s_mov_b32 s88, 0x1500008
buffer_load_dword v167, v167, s[20:23], s88 offen sc0 sc1
v_mfma_f32_16x16x32_bf16 v[64:67], a[0:3], v[128:131], 0
Rb 48,1,0
Mp 64,4,144
)" R"(
Mp 64,8,160
Rb 52,1,1024
Mp 64,12,176
v_mfma_f32_16x16x32_bf16 v[72:75], a[12:15], v[184:187], 0
Rb 56,1,2048
Mp 72,8,168
Mp 72,4,152
Rb 60,1,3072
s_waitcnt vmcnt(5) lgkmcnt(8)
s_barrier
v_cmp_eq_u32_e32 vcc, s3, v4
s_andn2_b64 s[28:29], 1, vcc
s_cbranch_scc1 .Lzsp24
.Lzok24:
Mp 72,0,136
buffer_load_dwordx4 a[64:67], v2, s[20:23], s66 offen offset:0 sc0 sc1
Mp 72,16,200
Rb 0,1,4096
Mp 72,20,216
Mp 72,24,232
Rb 4,1,5120
Mp 72,28,248
buffer_load_dwordx4 a[68:71], v2, s[20:23], s66 offen offset:1024 sc0 sc1
Mp 64,28,240
Rb 8,1,6144
Mp 64,24,224
Mp 64,20,208
Rb 12,1,8192
s_waitcnt lgkmcnt(8)
Mp 64,16,192
buffer_load_dwordx4 a[72:75], v2, s[20:23], s66 offen offset:2048 sc0 sc1
v_mfma_f32_16x16x32_bf16 v[68:71], a[32:35], v[128:131], 0
Rb 16,1,9216
Mp 68,36,144
Mp 68,40,160
Rb 20,1,10240
Mp 68,44,176
buffer_load_dwordx4 a[76:79], v2, s[20:23], s66 offen offset:3072 sc0 sc1
v_mfma_f32_16x16x32_bf16 v[76:79], a[44:47], v[184:187], 0
Rb 24,1,11264
Mp 76,40,168
Mp 76,36,152
Rb 28,1,12288
s_waitcnt vmcnt(7) lgkmcnt(8)
buffer_store_dword v6, v7, s[20:23], s65 offen offset:128 sc0 sc1
Mp 76,32,136
buffer_load_dwordx4 a[80:83], v3, s[20:23], s66 offen offset:0 sc0 sc1
Mp 76,48,200
Rb 32,1,13312
Mp 76,52,216
Mp 76,56,232
Rb 36,1,14336
Mp 76,60,248
buffer_load_dwordx4 a[84:87], v3, s[20:23], s66 offen offset:1024 sc0 sc1
Mp 68,60,240
Rb 40,1,16384
Mp 68,56,224
Mp 68,52,208
Rb 44,1,17408
s_waitcnt lgkmcnt(8)
Mp 68,48,192
buffer_load_dwordx4 a[88:91], v3, s[20:23], s66 offen offset:2048 sc0 sc1
v_mfma_f32_16x16x32_bf16 v[80:83], a[0:3], v[128:131], 0
Rb 48,1,18432
Mp 80,4,144
)" R"(
Mp 80,8,160
Rb 52,1,19456
Mp 80,12,176
buffer_load_dwordx4 a[92:95], v3, s[20:23], s66 offen offset:3072 sc0 sc1
v_mfma_f32_16x16x32_bf16 v[88:91], a[12:15], v[184:187], 0
Rb 56,1,20480
Mp 88,8,168
Mp 88,4,152
Rb 60,1,21504
s_waitcnt lgkmcnt(8)
Mp 88,0,136
Mp 88,16,200
Rb 0,1,22528
Mp 88,20,216
Mp 88,24,232
Rb 4,1,24576
Mp 88,28,248
Mp 80,28,240
Rb 8,1,25600
Mp 80,24,224
Mp 80,20,208
Rb 12,1,26624
s_waitcnt lgkmcnt(8)
Mp 80,16,192
v_mfma_f32_16x16x32_bf16 v[84:87], a[32:35], v[128:131], 0
Rb 16,1,27648
Mp 84,36,144
Mp 84,40,160
Rb 20,1,28672
Mp 84,44,176
v_mfma_f32_16x16x32_bf16 v[92:95], a[44:47], v[184:187], 0
Rb 24,1,29696
Mp 92,40,168
Mp 92,36,152
Rb 28,1,30720
s_waitcnt lgkmcnt(8)
Mp 92,32,136
buffer_load_dword v4, v7, s[20:23], s52 offen offset:128 sc0 sc1
Mp 92,48,200
Rb 32,1,32768
Mp 92,52,216
Mp 92,56,232
Rb 36,1,33792
Mp 92,60,248
Mp 84,60,240
Rb 40,1,34816
Mp 84,56,224
Mp 84,52,208
Rb 44,1,35840
s_waitcnt vmcnt(6)
s_barrier
Mp 84,48,192
Mv 48,194,64
Mv 56,194,68
Mv 52,192,64
Mv 60,192,68
Mv 64,194,72
Mv 72,194,76
Mv 68,192,72
s_waitcnt vmcnt(3) lgkmcnt(9)
Mv 76,192,76
Cv 48,48,49
Cv 49,50,51
Mv 80,194,80
Cv 50,52,53
)" R"(
Cv 51,54,55
Mv 88,194,84
s_add_u32 s29, s73, 0x0
buffer_store_dwordx4 v[48:51], v1, s[20:23], s29 offen sc0 sc1
Mv 84,192,80
Mv 92,192,84
v_mfma_f32_16x16x32_bf16 v[96:99], a[0:3], v[128:131], 0
Rb 48,1,36864
Mp 96,4,144
Mp 96,8,160
Rb 52,1,37888
s_waitcnt lgkmcnt(7)
Mp 96,12,176
Cv 56,56,57
Cv 57,58,59
ds_read_b64 v[188:189], v174 offset:0
v_mfma_f32_16x16x32_bf16 v[104:107], a[12:15], v[184:187], 0
Rb 56,1,38912
Cv 58,60,61
Mp 104,8,168
Cv 59,62,63
ds_read_b64 v[190:191], v174 offset:32
Mp 104,4,152
Rb 60,1,40960
Mp 104,0,136
s_add_u32 s29, s73, 0x400
buffer_store_dwordx4 v[56:59], v1, s[20:23], s29 offen sc0 sc1
ds_read_b64 v[196:197], v174 offset:1024
Mp 104,16,200
Rb 0,1,41984
Mp 104,20,216
ds_read_b64 v[198:199], v174 offset:1056
Mp 104,24,232
Rb 4,1,43008
s_waitcnt lgkmcnt(11)
Mp 104,28,248
Mp 96,28,240
Rb 8,1,44032
Mp 96,24,224
Cv 64,64,65
Cv 65,66,67
Mp 96,20,208
Rb 12,1,45056
Cv 66,68,69
.LzH20:
s_cbranch_execz .LzHR
Mp 96,16,192
Cv 67,70,71
v_mfma_f32_16x16x32_bf16 v[100:103], a[32:35], v[128:131], 0
Rb 16,1,46080
Mp 100,36,144
s_add_u32 s29, s73, 0x800
buffer_store_dwordx4 v[64:67], v1, s[20:23], s29 offen sc0 sc1
Mp 100,40,160
Rb 20,1,47104
s_waitcnt vmcnt(3) lgkmcnt(10)
v_cmp_eq_u32_e32 vcc, s3, v4
s_andn2_b64 s[28:29], 1, vcc
s_cbranch_scc1 .Lzsp25
.Lzok25:
Mp 100,44,176
s_add_u32 s66, s66, 0x10000
buffer_load_dwordx4 a[96:99], v2, s[20:23], s66 offen offset:0 sc0 sc1
v_mfma_f32_16x16x32_bf16 v[108:111], a[44:47], v[184:187], 0
)" R"(
Rb 24,1,49152
v_sub_f32_e32 v172, 1.0, v167
Mp 108,40,168
Cv 172,172,172
v_lshlrev_b32 v172, 16, v172
Mp 108,36,152
Rb 28,1,50176
v_lshlrev_b32 v204, 16, v188
Mp 108,32,136
buffer_load_dwordx4 a[100:103], v2, s[20:23], s66 offen offset:1024 sc0 sc1
Mp 108,48,200
Rb 32,1,51200
Cv 72,72,73
Mp 108,52,216
Cv 73,74,75
Cv 74,76,77
Mp 108,56,232
Rb 36,1,52224
s_waitcnt lgkmcnt(12)
Mp 108,60,248
buffer_load_dwordx4 a[104:107], v2, s[20:23], s66 offen offset:2048 sc0 sc1
Mp 100,60,240
Rb 40,1,53248
Cv 75,78,79
Mp 100,56,224
s_add_u32 s29, s73, 0xc00
buffer_store_dwordx4 v[72:75], v1, s[20:23], s29 offen sc0 sc1
Mp 100,52,208
Rb 44,1,54272
v_and_b32 v205, 0xffff0000, v188
Mp 100,48,192
buffer_load_dwordx4 a[108:111], v2, s[20:23], s66 offen offset:3072 sc0 sc1
Mv 96,194,88
v_mul_f32_e32 v204, v172, v204
v_mul_f32_e32 v205, v172, v205
Mv 104,194,92
Cv 205,204,205
v_lshlrev_b32 v204, 16, v205
Mv 100,192,88
v_and_b32 v205, 0xffff0000, v205
s_waitcnt lgkmcnt(8)
Mv 108,192,92
buffer_load_dwordx4 a[112:115], v3, s[20:23], s66 offen offset:0 sc0 sc1
v_mfma_f32_16x16x32_bf16 v[112:115], a[0:3], v[128:131], 0
Rb 48,1,55296
Cv 80,80,81
Mp 112,4,144
Cv 81,82,83
Cv 82,84,85
Mp 112,8,160
Rb 52,1,57344
Cv 83,86,87
Mp 112,12,176
buffer_load_dwordx4 a[116:119], v3, s[20:23], s66 offen offset:1024 sc0 sc1
v_mfma_f32_16x16x32_bf16 v[120:123], a[12:15], v[184:187], 0
Rb 56,1,58368
v_lshlrev_b32 v206, 16, v189
Mp 120,8,168
s_add_u32 s29, s73, 0x1000
buffer_store_dwordx4 v[80:83], v1, s[20:23], s29 offen sc0 sc1
Mp 120,4,152
Rb 60,1,59392
s_waitcnt lgkmcnt(8)
Mp 120,0,136
)" R"(
v_and_b32 v207, 0xffff0000, v189
v_mul_f32_e32 v206, v172, v206
Mp 120,16,200
Rb 0,1,60416
v_mul_f32_e32 v207, v172, v207
Mp 120,20,216
Cv 207,206,207
v_lshlrev_b32 v206, 16, v207
Mp 120,24,232
Rb 4,1,61440
v_and_b32 v207, 0xffff0000, v207
Mp 120,28,248
Cv 88,88,89
Cv 89,90,91
Mp 112,28,240
Rb 8,1,62464
Cv 90,92,93
Mp 112,24,224
Cv 91,94,95
v_lshlrev_b32 v212, 16, v190
Mp 112,20,208
Rb 12,1,63488
s_waitcnt lgkmcnt(8)
Mp 112,16,192
s_add_u32 s29, s73, 0x1400
buffer_store_dwordx4 v[88:91], v1, s[20:23], s29 offen sc0 sc1
v_mfma_f32_16x16x32_bf16 v[116:119], a[32:35], v[128:131], 0
Rb 16,10,0
v_and_b32 v213, 0xffff0000, v190
Mp 116,36,144
v_mul_f32_e32 v212, v172, v212
v_mul_f32_e32 v213, v172, v213
Mp 116,40,160
Rb 20,10,1024
Cv 213,212,213
Mp 116,44,176
v_lshlrev_b32 v212, 16, v213
v_and_b32 v213, 0xffff0000, v213
v_mfma_f32_16x16x32_bf16 v[124:127], a[44:47], v[184:187], 0
Rb 24,10,2048
v_lshlrev_b32 v214, 16, v191
Mp 124,40,168
Cv 96,96,97
Cv 97,98,99
Mp 124,36,152
Rb 28,10,3072
s_waitcnt lgkmcnt(8)
Mp 124,32,136
Cv 98,100,101
Cv 99,102,103
Mp 124,48,200
Rb 32,10,4096
v_and_b32 v215, 0xffff0000, v191
Mp 124,52,216
s_add_u32 s29, s73, 0x1800
buffer_store_dwordx4 v[96:99], v1, s[20:23], s29 offen sc0 sc1
Mp 124,56,232
Rb 36,10,5120
v_mul_f32_e32 v214, v172, v214
Mp 124,60,248
v_mul_f32_e32 v215, v172, v215
Cv 215,214,215
Mp 116,60,240
Rb 40,10,6144
)" R"(
v_lshlrev_b32 v214, 16, v215
Mp 116,56,224
v_and_b32 v215, 0xffff0000, v215
v_lshlrev_b32 v220, 16, v196
Mp 116,52,208
Rb 44,10,8192
s_waitcnt vmcnt(8) lgkmcnt(9)
Mp 116,48,192
Cv 104,104,105
Cv 105,106,107
Mv 112,194,96
Cv 106,108,109
Cv 107,110,111
Mv 120,194,100
s_add_u32 s29, s73, 0x4000
buffer_store_dwordx4 v[104:107], v1, s[20:23], s29 offen sc0 sc1
Mv 116,192,96
v_and_b32 v221, 0xffff0000, v196
v_mul_f32_e32 v220, v172, v220
Mv 124,192,100
v_mul_f32_e32 v221, v172, v221
Cv 221,220,221
v_mfma_f32_16x16x32_bf16 v[32:35], a[0:3], v[128:131], 0
Rb 48,10,9216
v_lshlrev_b32 v220, 16, v221
Mp 32,4,144
v_and_b32 v221, 0xffff0000, v221
v_lshlrev_b32 v222, 16, v197
Mp 32,8,160
Rb 52,10,10240
s_waitcnt lgkmcnt(7)
Mp 32,12,176
Cv 112,112,113
Cv 113,114,115
v_mfma_f32_16x16x32_bf16 v[40:43], a[12:15], v[184:187], 0
Rb 56,10,11264
Cv 114,116,117
Mp 40,8,168
Cv 115,118,119
v_and_b32 v223, 0xffff0000, v197
Mp 40,4,152
Rb 60,10,12288
v_mul_f32_e32 v222, v172, v222
Mp 40,0,136
s_add_u32 s29, s73, 0x4400
buffer_store_dwordx4 v[112:115], v1, s[20:23], s29 offen sc0 sc1
Mp 40,16,200
Rb 0,10,13312
v_mul_f32_e32 v223, v172, v223
Mp 40,20,216
Cv 223,222,223
v_lshlrev_b32 v222, 16, v223
Mp 40,24,232
Rb 4,10,14336
s_waitcnt lgkmcnt(7)
Mp 40,28,248
v_and_b32 v223, 0xffff0000, v223
v_lshlrev_b32 v228, 16, v198
Mp 32,28,240
Rb 8,10,16384
v_and_b32 v229, 0xffff0000, v198
Mp 32,24,224
Cv 120,120,121
Cv 121,122,123
)" R"(
Mp 32,20,208
Rb 12,10,17408
Cv 122,124,125
Mp 32,16,192
Cv 123,126,127
v_mul_f32_e32 v228, v172, v228
v_mfma_f32_16x16x32_bf16 v[36:39], a[32:35], v[128:131], 0
Rb 16,10,18432
v_mul_f32_e32 v229, v172, v229
Mp 36,36,144
s_add_u32 s29, s73, 0x4800
buffer_store_dwordx4 v[120:123], v1, s[20:23], s29 offen sc0 sc1
Mp 36,40,160
Rb 20,10,19456
s_waitcnt lgkmcnt(7)
Mp 36,44,176
Cv 229,228,229
v_lshlrev_b32 v228, 16, v229
v_mfma_f32_16x16x32_bf16 v[44:47], a[44:47], v[184:187], 0
Rb 24,10,20480
v_and_b32 v229, 0xffff0000, v229
Mp 44,40,168
v_lshlrev_b32 v230, 16, v199
v_and_b32 v231, 0xffff0000, v199
Mp 44,36,152
Rb 28,10,21504
v_mul_f32_e32 v230, v172, v230
Mp 44,32,136
v_mul_f32_e32 v231, v172, v231
Cv 231,230,231
Mp 44,48,200
Rb 32,10,22528
v_lshlrev_b32 v230, 16, v231
Mp 44,52,216
v_and_b32 v231, 0xffff0000, v231
Mp 44,56,232
Rb 36,10,24576
s_waitcnt vmcnt(3) lgkmcnt(10)
buffer_store_dword v6, v7, s[20:23], s68 offen offset:0 sc0 sc1
Mp 44,60,248
Mp 36,60,240
Rb 40,10,25600
Mp 36,56,224
Mp 36,52,208
Rb 44,10,26624
Mp 36,48,192
Mv 32,194,104
Mv 40,194,108
Mv 36,192,104
s_waitcnt lgkmcnt(8)
Mv 44,192,108
v_mfma_f32_16x16x32_bf16 v[48:51], a[0:3], v[128:131], 0
Rb 48,10,27648
Mp 48,4,144
Mp 48,8,160
Rb 52,10,28672
Mp 48,12,176
Cv 32,32,33
Cv 33,34,35
v_mfma_f32_16x16x32_bf16 v[56:59], a[12:15], v[184:187], 0
Rb 56,10,29696
Cv 34,36,37
Mp 56,8,168
Cv 35,38,39
)" R"(
Mp 56,4,152
Rb 60,10,30720
s_waitcnt lgkmcnt(8)
Mp 56,0,136
s_add_u32 s29, s73, 0x4c00
buffer_store_dwordx4 v[32:35], v1, s[20:23], s29 offen sc0 sc1
Mp 56,16,200
Mp 56,20,216
Mp 56,24,232
Mp 56,28,248
Mp 48,28,240
Mp 48,24,224
Cv 40,40,41
Cv 41,42,43
buffer_store_dword v6, v7, s[20:23], s68 offen offset:128 sc0 sc1
Mp 48,20,208
Cv 42,44,45
s_waitcnt lgkmcnt(4)
Mp 48,16,192
Cv 43,46,47
v_mfma_f32_16x16x32_bf16 v[52:55], a[32:35], v[128:131], 0
s_add_u32 s29, s73, 0x5000
buffer_store_dwordx4 v[40:43], v1, s[20:23], s29 offen sc0 sc1
Mp 52,36,144
buffer_load_dword v4, v165, s[20:23], s40 offen sc0 sc1
Mp 52,40,160
Mp 52,44,176
v_mfma_f32_16x16x32_bf16 v[60:63], a[44:47], v[184:187], 0
Mp 60,40,168
Mp 60,36,152
s_waitcnt lgkmcnt(0)
Mp 60,32,136
Mp 60,48,200
Mp 60,52,216
Mp 60,56,232
Mp 60,60,248
Mp 52,60,240
Mp 52,56,224
Mp 52,52,208
Mp 52,48,192
Mv 48,194,112
Mv 56,194,116
Mv 52,192,112
Mv 60,192,116
s_nop 7
Cv 48,48,49
Cv 49,50,51
Cv 50,52,53
Cv 51,54,55
s_add_u32 s29, s73, 0x5400
buffer_store_dwordx4 v[48:51], v1, s[20:23], s29 offen sc0 sc1
Cv 56,56,57
Cv 57,58,59
Cv 58,60,61
Cv 59,62,63
s_add_u32 s29, s73, 0x5800
buffer_store_dwordx4 v[56:59], v1, s[20:23], s29 offen sc0 sc1
.LzH21:
s_cbranch_execz .LzHR
.LzEND:
s_cmp_gt_u32 s82, 223
s_cbranch_scc1 .LzFIN
s_cmp_lt_u32 s82, 14
s_cbranch_scc1 .LzBB
)" R"(
.LzBBR:
s_waitcnt vmcnt(2)
.LzS0:
v_cmp_ne_u32_e32 vcc, s3, v4
s_lshr_b32 s28, vcc_lo, 1
s_or_b32 s28, s28, vcc_lo
s_lshr_b32 s29, s28, 2
s_or_b32 s28, s28, s29
s_andn2_b32 s28, s93, s28
s_sub_u32 s53, s53, 1
s_cselect_b32 s28, s93, s28
s_cmov_b32 s53, 0
s_xor_b32 s93, s93, s28
s_or_b32 s57, s57, s28
s_cbranch_scc1 .LzS2
s_sleep 4
s_branch .LzSk3
.LzS2:
s_bitcmp0_b32 s57, 0
s_cbranch_scc1 .LzSk0
s_add_u32 s29, s55, 0x0
buffer_load_dwordx4 a[128:131], v164, s[20:23], s29 offen offset:0 sc0 sc1
buffer_load_dwordx4 a[132:135], v164, s[20:23], s29 offen offset:256 sc0 sc1
buffer_load_dwordx4 a[136:139], v164, s[20:23], s29 offen offset:512 sc0 sc1
buffer_load_dwordx4 a[140:143], v164, s[20:23], s29 offen offset:768 sc0 sc1
.LzSk0:
s_bitcmp0_b32 s57, 4
s_cbranch_scc1 .LzSk1
s_add_u32 s29, s55, 0x8000000
buffer_load_dwordx4 a[144:147], v183, s[20:23], s29 offen offset:0 sc0 sc1
buffer_load_dwordx4 a[148:151], v183, s[20:23], s29 offen offset:256 sc0 sc1
buffer_load_dwordx4 a[152:155], v183, s[20:23], s29 offen offset:512 sc0 sc1
buffer_load_dwordx4 a[156:159], v183, s[20:23], s29 offen offset:768 sc0 sc1
.LzSk1:
s_bitcmp0_b32 s57, 8
s_cbranch_scc1 .LzSk2
s_add_u32 s29, s55, 0x8000
buffer_load_dwordx4 a[160:163], v164, s[20:23], s29 offen offset:0 sc0 sc1
buffer_load_dwordx4 a[164:167], v164, s[20:23], s29 offen offset:256 sc0 sc1
buffer_load_dwordx4 a[168:171], v164, s[20:23], s29 offen offset:512 sc0 sc1
buffer_load_dwordx4 a[172:175], v164, s[20:23], s29 offen offset:768 sc0 sc1
.LzSk2:
s_bitcmp0_b32 s57, 12
s_cbranch_scc1 .LzSk3
s_add_u32 s29, s55, 0x8008000
buffer_load_dwordx4 a[176:179], v183, s[20:23], s29 offen offset:0 sc0 sc1
buffer_load_dwordx4 a[180:183], v183, s[20:23], s29 offen offset:256 sc0 sc1
buffer_load_dwordx4 a[184:187], v183, s[20:23], s29 offen offset:512 sc0 sc1
buffer_load_dwordx4 a[188:191], v183, s[20:23], s29 offen offset:768 sc0 sc1
.LzSk3:
s_cmp_eq_u32 s93, 0
s_cbranch_scc1 .LzS1
buffer_load_dword v4, v165, s[20:23], s40 offen sc0 sc1
.LzS1:
s_waitcnt vmcnt(0)
s_bitcmp0_b32 s57, 0
s_cbranch_scc1 .LzSv0
v_accvgpr_read_b32 v173, a131
v_accvgpr_read_b32 v174, a135
v_accvgpr_read_b32 v175, a139
v_accvgpr_read_b32 v180, a143
v_max3_u32 v173, v173, v174, v175
v_max_u32 v173, v173, v180
v_cmp_eq_u32_e32 vcc, -1, v173
)" R"(
s_cbranch_vccnz .LzSv0
s_bitset0_b32 s57, 0
.LzSv0:
s_bitcmp0_b32 s57, 4
s_cbranch_scc1 .LzSv1
v_accvgpr_read_b32 v173, a147
v_accvgpr_read_b32 v174, a151
v_accvgpr_read_b32 v175, a155
v_accvgpr_read_b32 v180, a159
v_max3_u32 v173, v173, v174, v175
v_max_u32 v173, v173, v180
v_cmp_eq_u32_e32 vcc, -1, v173
s_cbranch_vccnz .LzSv1
s_bitset0_b32 s57, 4
.LzSv1:
s_bitcmp0_b32 s57, 8
s_cbranch_scc1 .LzSv2
v_accvgpr_read_b32 v173, a163
v_accvgpr_read_b32 v174, a167
v_accvgpr_read_b32 v175, a171
v_accvgpr_read_b32 v180, a175
v_max3_u32 v173, v173, v174, v175
v_max_u32 v173, v173, v180
v_cmp_eq_u32_e32 vcc, -1, v173
s_cbranch_vccnz .LzSv2
s_bitset0_b32 s57, 8
.LzSv2:
s_bitcmp0_b32 s57, 12
s_cbranch_scc1 .LzSv3
v_accvgpr_read_b32 v173, a179
v_accvgpr_read_b32 v174, a183
v_accvgpr_read_b32 v175, a187
v_accvgpr_read_b32 v180, a191
v_max3_u32 v173, v173, v174, v175
v_max_u32 v173, v173, v180
v_cmp_eq_u32_e32 vcc, -1, v173
s_cbranch_vccnz .LzSv3
s_bitset0_b32 s57, 12
.LzSv3:
s_cmp_eq_u32 s53, 0
s_cmov_b32 s57, 0
s_or_b32 s28, s57, s93
s_cbranch_scc1 .LzS0
s_bfe_u32 s28, s63, 0x70004
s_cmp_eq_u32 s28, s58
s_cbranch_scc1 .LzRT
s_bfe_u32 s28, s63, 0x70014
s_cmp_eq_u32 s28, s58
s_cbranch_scc1 .LzRT
.LzRT3:
v_mfma_f32_16x16x32_bf16 v[80:83], v[132:135], a[128:131], 0
v_mfma_f32_16x16x32_bf16 v[88:91], v[132:135], a[160:163], 0
v_mfma_f32_16x16x32_bf16 v[84:87], v[132:135], a[136:139], 0
v_mfma_f32_16x16x32_bf16 v[92:95], v[132:135], a[168:171], 0
v_mfma_f32_16x16x32_bf16 v[80:83], v[140:143], a[132:135], v[80:83]
v_mfma_f32_16x16x32_bf16 v[88:91], v[140:143], a[164:167], v[88:91]
v_mfma_f32_16x16x32_bf16 v[84:87], v[140:143], a[140:143], v[84:87]
v_mfma_f32_16x16x32_bf16 v[92:95], v[140:143], a[172:175], v[92:95]
v_mfma_f32_16x16x32_bf16 v[80:83], v[132:135], a[144:147], v[80:83]
v_mfma_f32_16x16x32_bf16 v[88:91], v[132:135], a[176:179], v[88:91]
v_mfma_f32_16x16x32_bf16 v[84:87], v[132:135], a[152:155], v[84:87]
v_mfma_f32_16x16x32_bf16 v[92:95], v[132:135], a[184:187], v[92:95]
v_mfma_f32_16x16x32_bf16 v[80:83], v[140:143], a[148:151], v[80:83]
v_mfma_f32_16x16x32_bf16 v[88:91], v[140:143], a[180:183], v[88:91]
)" R"(
v_mfma_f32_16x16x32_bf16 v[84:87], v[140:143], a[156:159], v[84:87]
v_mfma_f32_16x16x32_bf16 v[92:95], v[140:143], a[188:191], v[92:95]
v_lshrrev_b32 v173, 4, v0
v_and_b32 v173, 1, v173
v_mad_u32_u24 v166, v173, 24, v166
s_nop 1
Cv 81,80,81
v_lshlrev_b32 v80, 16, v81
v_and_b32 v81, 0xffff0000, v81
Cv 83,82,83
v_lshlrev_b32 v82, 16, v83
v_and_b32 v83, 0xffff0000, v83
Cv 89,88,89
v_lshlrev_b32 v88, 16, v89
v_and_b32 v89, 0xffff0000, v89
Cv 91,90,91
v_lshlrev_b32 v90, 16, v91
v_and_b32 v91, 0xffff0000, v91
Cv 85,84,85
v_lshlrev_b32 v84, 16, v85
v_and_b32 v85, 0xffff0000, v85
Cv 87,86,87
v_lshlrev_b32 v86, 16, v87
v_and_b32 v87, 0xffff0000, v87
Cv 93,92,93
v_lshlrev_b32 v92, 16, v93
v_and_b32 v93, 0xffff0000, v93
Cv 95,94,95
v_lshlrev_b32 v94, 16, v95
v_and_b32 v95, 0xffff0000, v95
v_mul_f32_e32 v80, v167, v80
v_mul_f32_e32 v81, v167, v81
v_mul_f32_e32 v82, v167, v82
v_mul_f32_e32 v83, v167, v83
v_mul_f32_e32 v88, v167, v88
v_mul_f32_e32 v89, v167, v89
v_mul_f32_e32 v90, v167, v90
v_mul_f32_e32 v91, v167, v91
v_mul_f32_e32 v84, v167, v84
v_mul_f32_e32 v85, v167, v85
v_mul_f32_e32 v86, v167, v86
v_mul_f32_e32 v87, v167, v87
v_mul_f32_e32 v92, v167, v92
v_mul_f32_e32 v93, v167, v93
v_mul_f32_e32 v94, v167, v94
v_mul_f32_e32 v95, v167, v95
Cv 81,80,81
v_lshlrev_b32 v80, 16, v81
v_and_b32 v81, 0xffff0000, v81
Cv 83,82,83
v_lshlrev_b32 v82, 16, v83
v_and_b32 v83, 0xffff0000, v83
Cv 89,88,89
v_lshlrev_b32 v88, 16, v89
v_and_b32 v89, 0xffff0000, v89
Cv 91,90,91
v_lshlrev_b32 v90, 16, v91
v_and_b32 v91, 0xffff0000, v91
Cv 85,84,85
v_lshlrev_b32 v84, 16, v85
v_and_b32 v85, 0xffff0000, v85
Cv 87,86,87
v_lshlrev_b32 v86, 16, v87
v_and_b32 v87, 0xffff0000, v87
)" R"(
Cv 93,92,93
v_lshlrev_b32 v92, 16, v93
v_and_b32 v93, 0xffff0000, v93
Cv 95,94,95
v_lshlrev_b32 v94, 16, v95
v_and_b32 v95, 0xffff0000, v95
v_add_f32_e32 v80, v80, v204
v_add_f32_e32 v81, v81, v205
v_add_f32_e32 v82, v82, v206
v_add_f32_e32 v83, v83, v207
v_add_f32_e32 v88, v88, v220
v_add_f32_e32 v89, v89, v221
v_add_f32_e32 v90, v90, v222
v_add_f32_e32 v91, v91, v223
v_add_f32_e32 v84, v84, v212
v_add_f32_e32 v85, v85, v213
v_add_f32_e32 v86, v86, v214
v_add_f32_e32 v87, v87, v215
v_add_f32_e32 v92, v92, v228
v_add_f32_e32 v93, v93, v229
v_add_f32_e32 v94, v94, v230
v_add_f32_e32 v95, v95, v231
Cv 64,80,81
Cv 65,82,83
Cv 66,84,85
Cv 67,86,87
Cv 68,88,89
Cv 69,90,91
Cv 70,92,93
Cv 71,94,95
v_permlane16_swap_b32_e32 v64, v66
v_permlane16_swap_b32_e32 v65, v67
buffer_store_dwordx4 v[64:67], v166, s[8:11], 0 offen offset:0 sc0 sc1
v_permlane16_swap_b32_e32 v68, v70
v_permlane16_swap_b32_e32 v69, v71
buffer_store_dwordx4 v[68:71], v166, s[8:11], 0 offen offset:448 sc0 sc1
s_or_b32 s28, s82, s33
s_cbranch_scc1 .LzFIN
s_mov_b32 s28, 0x1508400
buffer_store_dword v6, v7, s[20:23], s28 offen offset:0 sc0 sc1
.LzFIN:
s_endpgm
.LzRT:
s_and_b32 s28, s63, 0x7ff
s_mul_i32 s28, s28, 896
v_subrev_u32 v174, s28, v166
s_bfe_u32 s28, s63, 0xb0010
s_mul_i32 s28, s28, 896
v_subrev_u32 v175, s28, v166
s_movk_i32 s28, 896
v_cmp_gt_u32_e64 s[24:25], s28, v175
v_cmp_gt_u32_e32 vcc, s28, v174
v_add_u32 v175, s28, v175
s_or_b64 s[24:25], s[24:25], vcc
v_cndmask_b32_e32 v174, v175, v174, vcc
v_lshrrev_b32 v174, 5, v174
v_lshlrev_b32 v174, 7, v174
v_and_or_b32 v174, v0, 48, v174
s_mov_b64 exec, s[24:25]
s_mov_b32 s29, 0x1509000
.LzRT1:
buffer_load_dwordx4 v[64:67], v174, s[20:23], s29 offen offset:0 sc0 sc1
buffer_load_dwordx4 v[68:71], v174, s[20:23], s29 offen offset:128 sc0 sc1
buffer_load_dwordx4 v[72:75], v174, s[20:23], s29 offen offset:1792 sc0 sc1
)" R"(
buffer_load_dwordx4 v[76:79], v174, s[20:23], s29 offen offset:1920 sc0 sc1
s_waitcnt vmcnt(0)
v_xor_b32 v175, s3, v65
v_xor_b32 v173, s3, v67
v_or_b32 v175, v175, v173
v_xor_b32 v173, s3, v69
v_or_b32 v175, v175, v173
v_xor_b32 v173, s3, v71
v_or_b32 v175, v175, v173
v_xor_b32 v173, s3, v73
v_or_b32 v175, v175, v173
v_xor_b32 v173, s3, v75
v_or_b32 v175, v175, v173
v_xor_b32 v173, s3, v77
v_or_b32 v175, v175, v173
v_xor_b32 v173, s3, v79
v_or_b32 v175, v175, v173
v_cmp_ne_u32_e32 vcc, 0, v175
s_cbranch_vccz .LzRT2
s_sleep 2
s_sub_u32 s53, s53, 1
s_cbranch_scc0 .LzRT1
s_mov_b32 s53, 0
.LzRT2:
v_mov_b32 v188, v64
v_mov_b32 v189, v66
v_mov_b32 v190, v68
v_mov_b32 v191, v70
v_mov_b32 v196, v72
v_mov_b32 v197, v74
v_mov_b32 v198, v76
v_mov_b32 v199, v78
v_sub_f32_e32 v172, 1.0, v167
Cv 172,172,172
v_lshlrev_b32 v172, 16, v172
v_lshlrev_b32 v204, 16, v188
v_and_b32 v205, 0xffff0000, v188
v_mul_f32_e32 v204, v172, v204
v_mul_f32_e32 v205, v172, v205
Cv 205,204,205
v_lshlrev_b32 v204, 16, v205
v_and_b32 v205, 0xffff0000, v205
v_lshlrev_b32 v206, 16, v189
v_and_b32 v207, 0xffff0000, v189
v_mul_f32_e32 v206, v172, v206
v_mul_f32_e32 v207, v172, v207
Cv 207,206,207
v_lshlrev_b32 v206, 16, v207
v_and_b32 v207, 0xffff0000, v207
.LzH22:
s_cbranch_execz .LzHR
v_lshlrev_b32 v212, 16, v190
v_and_b32 v213, 0xffff0000, v190
v_mul_f32_e32 v212, v172, v212
v_mul_f32_e32 v213, v172, v213
Cv 213,212,213
v_lshlrev_b32 v212, 16, v213
v_and_b32 v213, 0xffff0000, v213
v_lshlrev_b32 v214, 16, v191
v_and_b32 v215, 0xffff0000, v191
v_mul_f32_e32 v214, v172, v214
v_mul_f32_e32 v215, v172, v215
Cv 215,214,215
v_lshlrev_b32 v214, 16, v215
)" R"(
v_and_b32 v215, 0xffff0000, v215
v_lshlrev_b32 v220, 16, v196
v_and_b32 v221, 0xffff0000, v196
v_mul_f32_e32 v220, v172, v220
v_mul_f32_e32 v221, v172, v221
Cv 221,220,221
v_lshlrev_b32 v220, 16, v221
v_and_b32 v221, 0xffff0000, v221
v_lshlrev_b32 v222, 16, v197
v_and_b32 v223, 0xffff0000, v197
v_mul_f32_e32 v222, v172, v222
v_mul_f32_e32 v223, v172, v223
Cv 223,222,223
v_lshlrev_b32 v222, 16, v223
v_and_b32 v223, 0xffff0000, v223
v_lshlrev_b32 v228, 16, v198
v_and_b32 v229, 0xffff0000, v198
v_mul_f32_e32 v228, v172, v228
v_mul_f32_e32 v229, v172, v229
Cv 229,228,229
v_lshlrev_b32 v228, 16, v229
v_and_b32 v229, 0xffff0000, v229
v_lshlrev_b32 v230, 16, v199
v_and_b32 v231, 0xffff0000, v199
v_mul_f32_e32 v230, v172, v230
v_mul_f32_e32 v231, v172, v231
Cv 231,230,231
v_lshlrev_b32 v230, 16, v231
v_and_b32 v231, 0xffff0000, v231
s_mov_b64 exec, -1
s_branch .LzRT3
.LzH23:
s_cbranch_execz .LzH39
.LzMASK0:
s_lshl1_add_u32 s28, s57, s33
s_mov_b32 s29, 0xff800000
s_cmp_gt_u32 s28, 5
s_cbranch_scc1 .LzMASKE0
s_cmp_eq_u32 s28, 5
s_cbranch_scc1 .LzMASKB0
v_mov_b32 v72, s29
v_mov_b32 v73, s29
v_mov_b32 v74, s29
v_mov_b32 v75, s29
v_mov_b32 v80, s29
v_mov_b32 v81, s29
v_mov_b32 v82, s29
v_mov_b32 v83, s29
v_mov_b32 v84, s29
v_mov_b32 v85, s29
v_mov_b32 v86, s29
v_mov_b32 v87, s29
v_mov_b32 v88, s29
v_mov_b32 v89, s29
v_mov_b32 v90, s29
v_mov_b32 v91, s29
v_mov_b32 v92, s29
v_mov_b32 v93, s29
v_mov_b32 v94, s29
v_mov_b32 v95, s29
s_cmp_eq_u32 s28, 4
s_cbranch_scc1 .LzMASKA0
v_mov_b32 v64, s29
v_mov_b32 v65, s29
)" R"(
v_mov_b32 v66, s29
v_mov_b32 v67, s29
v_mov_b32 v68, s29
v_mov_b32 v69, s29
v_mov_b32 v70, s29
v_mov_b32 v71, s29
v_mov_b32 v76, s29
v_mov_b32 v77, s29
v_mov_b32 v78, s29
v_mov_b32 v79, s29
.LzMASKE0:
s_setpc_b64 s[30:31]
.LzMASKA0:
s_mov_b32 exec_lo, 0xf0000
s_mov_b32 exec_hi, 0xfff00ff
v_mov_b32 v64, s29
v_mov_b32 v76, s29
s_mov_b32 exec_lo, 0x1f0001
s_mov_b32 exec_hi, 0x1fff01ff
v_mov_b32 v65, s29
v_mov_b32 v77, s29
s_mov_b32 exec_lo, 0x3f0003
s_mov_b32 exec_hi, 0x3fff03ff
v_mov_b32 v66, s29
v_mov_b32 v78, s29
s_mov_b32 exec_lo, 0x7f0007
s_mov_b32 exec_hi, 0x7fff07ff
v_mov_b32 v67, s29
v_mov_b32 v79, s29
s_mov_b64 exec, -1
s_setpc_b64 s[30:31]
.LzMASKB0:
s_nop 1
v_mov_b32 v88, s29
v_mov_b32 v89, s29
v_mov_b32 v90, s29
v_mov_b32 v91, s29
s_mov_b32 exec_lo, 0xf0000
s_mov_b32 exec_hi, 0xfff00ff
v_mov_b32 v80, s29
v_mov_b32 v92, s29
s_mov_b32 exec_lo, 0x1f0001
s_mov_b32 exec_hi, 0x1fff01ff
v_mov_b32 v81, s29
v_mov_b32 v93, s29
s_mov_b32 exec_lo, 0x3f0003
s_mov_b32 exec_hi, 0x3fff03ff
v_mov_b32 v82, s29
v_mov_b32 v94, s29
s_mov_b32 exec_lo, 0x7f0007
s_mov_b32 exec_hi, 0x7fff07ff
v_mov_b32 v83, s29
v_mov_b32 v95, s29
s_mov_b64 exec, -1
s_setpc_b64 s[30:31]
.LzFIX0:
s_nop 7
v_max_f32_e32 v28, v64, v65
v_max3_f32 v28, v28, v66, v67
v_max3_f32 v28, v28, v72, v73
v_max3_f32 v28, v28, v74, v75
v_max3_f32 v28, v28, v80, v81
v_max3_f32 v28, v28, v82, v83
v_max3_f32 v28, v28, v88, v89
)" R"(
v_max3_f32 v28, v28, v90, v91
v_mov_b32 v24, v28
s_nop 1
v_permlane32_swap_b32_e32 v28, v24
v_max_f32_e32 v28, v28, v24
v_mov_b32 v24, v28
s_nop 1
v_permlane16_swap_b32_e32 v28, v24
v_max_f32_e32 v28, v28, v24
v_max_f32_e32 v29, v68, v69
v_max3_f32 v29, v29, v70, v71
v_max3_f32 v29, v29, v76, v77
v_max3_f32 v29, v29, v78, v79
v_max3_f32 v29, v29, v84, v85
v_max3_f32 v29, v29, v86, v87
v_max3_f32 v29, v29, v92, v93
v_max3_f32 v29, v29, v94, v95
v_mov_b32 v24, v29
s_nop 1
v_permlane32_swap_b32_e32 v29, v24
v_max_f32_e32 v29, v29, v24
v_mov_b32 v24, v29
s_nop 1
v_permlane16_swap_b32_e32 v29, v24
v_max_f32_e32 v29, v29, v24
s_bitcmp1_b32 s88, 0
s_cbranch_scc1 .LzFIXF0
v_max_f32_e32 v28, 0, v28
v_sub_f32_e32 v24, 0, v28
v_exp_f32_e32 v24, v24
s_nop 0
v_mul_f32_e32 v12, v24, v12
v_mul_f32_e32 v128, v24, v128
v_mul_f32_e32 v129, v24, v129
v_mul_f32_e32 v130, v24, v130
v_mul_f32_e32 v131, v24, v131
v_mul_f32_e32 v132, v24, v132
v_mul_f32_e32 v133, v24, v133
v_mul_f32_e32 v134, v24, v134
v_mul_f32_e32 v135, v24, v135
v_mul_f32_e32 v144, v24, v144
v_mul_f32_e32 v145, v24, v145
v_mul_f32_e32 v146, v24, v146
v_mul_f32_e32 v147, v24, v147
v_mul_f32_e32 v148, v24, v148
v_mul_f32_e32 v149, v24, v149
v_mul_f32_e32 v150, v24, v150
v_mul_f32_e32 v151, v24, v151
v_mul_f32_e32 v160, v24, v160
v_mul_f32_e32 v161, v24, v161
v_mul_f32_e32 v162, v24, v162
v_mul_f32_e32 v163, v24, v163
v_mul_f32_e32 v164, v24, v164
v_mul_f32_e32 v165, v24, v165
v_mul_f32_e32 v166, v24, v166
v_mul_f32_e32 v167, v24, v167
v_mul_f32_e32 v176, v24, v176
v_mul_f32_e32 v177, v24, v177
v_mul_f32_e32 v178, v24, v178
v_mul_f32_e32 v179, v24, v179
v_mul_f32_e32 v180, v24, v180
v_mul_f32_e32 v181, v24, v181
v_mul_f32_e32 v182, v24, v182
v_mul_f32_e32 v183, v24, v183
)" R"(
v_mul_f32_e32 v192, v24, v192
v_mul_f32_e32 v193, v24, v193
v_mul_f32_e32 v194, v24, v194
v_mul_f32_e32 v195, v24, v195
v_mul_f32_e32 v196, v24, v196
v_mul_f32_e32 v197, v24, v197
v_mul_f32_e32 v198, v24, v198
v_mul_f32_e32 v199, v24, v199
v_mul_f32_e32 v208, v24, v208
v_mul_f32_e32 v209, v24, v209
v_mul_f32_e32 v210, v24, v210
v_mul_f32_e32 v211, v24, v211
v_mul_f32_e32 v212, v24, v212
v_mul_f32_e32 v213, v24, v213
v_mul_f32_e32 v214, v24, v214
v_mul_f32_e32 v215, v24, v215
v_mul_f32_e32 v224, v24, v224
v_mul_f32_e32 v225, v24, v225
v_mul_f32_e32 v226, v24, v226
v_mul_f32_e32 v227, v24, v227
v_mul_f32_e32 v228, v24, v228
v_mul_f32_e32 v229, v24, v229
v_mul_f32_e32 v230, v24, v230
v_mul_f32_e32 v231, v24, v231
v_mul_f32_e32 v240, v24, v240
v_mul_f32_e32 v241, v24, v241
v_mul_f32_e32 v242, v24, v242
v_mul_f32_e32 v243, v24, v243
v_mul_f32_e32 v244, v24, v244
v_mul_f32_e32 v245, v24, v245
v_mul_f32_e32 v246, v24, v246
v_mul_f32_e32 v247, v24, v247
v_max_f32_e32 v29, 0, v29
v_sub_f32_e32 v24, 0, v29
v_exp_f32_e32 v24, v24
s_nop 0
v_mul_f32_e32 v13, v24, v13
v_mul_f32_e32 v136, v24, v136
v_mul_f32_e32 v137, v24, v137
v_mul_f32_e32 v138, v24, v138
v_mul_f32_e32 v139, v24, v139
v_mul_f32_e32 v140, v24, v140
v_mul_f32_e32 v141, v24, v141
v_mul_f32_e32 v142, v24, v142
v_mul_f32_e32 v143, v24, v143
v_mul_f32_e32 v152, v24, v152
v_mul_f32_e32 v153, v24, v153
v_mul_f32_e32 v154, v24, v154
v_mul_f32_e32 v155, v24, v155
v_mul_f32_e32 v156, v24, v156
v_mul_f32_e32 v157, v24, v157
v_mul_f32_e32 v158, v24, v158
v_mul_f32_e32 v159, v24, v159
v_mul_f32_e32 v168, v24, v168
v_mul_f32_e32 v169, v24, v169
v_mul_f32_e32 v170, v24, v170
v_mul_f32_e32 v171, v24, v171
v_mul_f32_e32 v172, v24, v172
v_mul_f32_e32 v173, v24, v173
v_mul_f32_e32 v174, v24, v174
v_mul_f32_e32 v175, v24, v175
v_mul_f32_e32 v184, v24, v184
v_mul_f32_e32 v185, v24, v185
v_mul_f32_e32 v186, v24, v186
)" R"(
v_mul_f32_e32 v187, v24, v187
v_mul_f32_e32 v188, v24, v188
v_mul_f32_e32 v189, v24, v189
v_mul_f32_e32 v190, v24, v190
v_mul_f32_e32 v191, v24, v191
v_mul_f32_e32 v200, v24, v200
v_mul_f32_e32 v201, v24, v201
v_mul_f32_e32 v202, v24, v202
v_mul_f32_e32 v203, v24, v203
v_mul_f32_e32 v204, v24, v204
v_mul_f32_e32 v205, v24, v205
v_mul_f32_e32 v206, v24, v206
v_mul_f32_e32 v207, v24, v207
v_mul_f32_e32 v216, v24, v216
v_mul_f32_e32 v217, v24, v217
v_mul_f32_e32 v218, v24, v218
v_mul_f32_e32 v219, v24, v219
v_mul_f32_e32 v220, v24, v220
v_mul_f32_e32 v221, v24, v221
v_mul_f32_e32 v222, v24, v222
v_mul_f32_e32 v223, v24, v223
v_mul_f32_e32 v232, v24, v232
v_mul_f32_e32 v233, v24, v233
v_mul_f32_e32 v234, v24, v234
v_mul_f32_e32 v235, v24, v235
v_mul_f32_e32 v236, v24, v236
v_mul_f32_e32 v237, v24, v237
v_mul_f32_e32 v238, v24, v238
v_mul_f32_e32 v239, v24, v239
v_mul_f32_e32 v248, v24, v248
v_mul_f32_e32 v249, v24, v249
v_mul_f32_e32 v250, v24, v250
v_mul_f32_e32 v251, v24, v251
v_mul_f32_e32 v252, v24, v252
v_mul_f32_e32 v253, v24, v253
v_mul_f32_e32 v254, v24, v254
v_mul_f32_e32 v255, v24, v255
s_branch .LzFIXG0
.LzFIXF0:
v_cmp_lt_f32_e32 vcc, 0xff7fffff, v28
s_nop 1
v_cndmask_b32_e32 v28, 0, v28, vcc
v_cmp_lt_f32_e32 vcc, 0xff7fffff, v29
s_nop 1
v_cndmask_b32_e32 v29, 0, v29, vcc
.LzFIXG0:
v_sub_f32_e32 v16, v16, v28
v_sub_f32_e32 v17, v17, v28
v_sub_f32_e32 v18, v18, v28
v_sub_f32_e32 v19, v19, v28
v_sub_f32_e32 v64, v64, v28
v_sub_f32_e32 v65, v65, v28
v_sub_f32_e32 v66, v66, v28
v_sub_f32_e32 v67, v67, v28
v_sub_f32_e32 v72, v72, v28
v_sub_f32_e32 v73, v73, v28
v_sub_f32_e32 v74, v74, v28
v_sub_f32_e32 v75, v75, v28
v_sub_f32_e32 v80, v80, v28
v_sub_f32_e32 v81, v81, v28
v_sub_f32_e32 v82, v82, v28
v_sub_f32_e32 v83, v83, v28
v_sub_f32_e32 v88, v88, v28
v_sub_f32_e32 v89, v89, v28
)" R"(
v_sub_f32_e32 v90, v90, v28
v_sub_f32_e32 v91, v91, v28
v_sub_f32_e32 v20, v20, v29
v_sub_f32_e32 v21, v21, v29
v_sub_f32_e32 v22, v22, v29
v_sub_f32_e32 v23, v23, v29
v_sub_f32_e32 v68, v68, v29
v_sub_f32_e32 v69, v69, v29
v_sub_f32_e32 v70, v70, v29
v_sub_f32_e32 v71, v71, v29
v_sub_f32_e32 v76, v76, v29
v_sub_f32_e32 v77, v77, v29
v_sub_f32_e32 v78, v78, v29
v_sub_f32_e32 v79, v79, v29
v_sub_f32_e32 v84, v84, v29
v_sub_f32_e32 v85, v85, v29
v_sub_f32_e32 v86, v86, v29
v_sub_f32_e32 v87, v87, v29
v_sub_f32_e32 v92, v92, v29
v_sub_f32_e32 v93, v93, v29
v_sub_f32_e32 v94, v94, v29
v_sub_f32_e32 v95, v95, v29
s_bitcmp0_b32 s88, 1
s_cbranch_scc1 .LzFIXH0
v_sub_f32_e32 v96, v96, v28
v_sub_f32_e32 v97, v97, v28
v_sub_f32_e32 v98, v98, v28
v_sub_f32_e32 v99, v99, v28
v_sub_f32_e32 v104, v104, v28
v_sub_f32_e32 v105, v105, v28
v_sub_f32_e32 v106, v106, v28
v_sub_f32_e32 v107, v107, v28
v_sub_f32_e32 v112, v112, v28
v_sub_f32_e32 v113, v113, v28
v_sub_f32_e32 v114, v114, v28
v_sub_f32_e32 v115, v115, v28
v_sub_f32_e32 v120, v120, v28
v_sub_f32_e32 v121, v121, v28
v_sub_f32_e32 v122, v122, v28
v_sub_f32_e32 v123, v123, v28
v_sub_f32_e32 v100, v100, v29
v_sub_f32_e32 v101, v101, v29
v_sub_f32_e32 v102, v102, v29
v_sub_f32_e32 v103, v103, v29
v_sub_f32_e32 v108, v108, v29
v_sub_f32_e32 v109, v109, v29
v_sub_f32_e32 v110, v110, v29
v_sub_f32_e32 v111, v111, v29
v_sub_f32_e32 v116, v116, v29
v_sub_f32_e32 v117, v117, v29
v_sub_f32_e32 v118, v118, v29
v_sub_f32_e32 v119, v119, v29
v_sub_f32_e32 v124, v124, v29
v_sub_f32_e32 v125, v125, v29
v_sub_f32_e32 v126, v126, v29
v_sub_f32_e32 v127, v127, v29
.LzFIXH0:
v_exp_f32_e32 v32, v64
v_exp_f32_e32 v28, v65
s_nop 0
v_add_f32_e32 v14, v32, v28
Cv 32,32,28
v_exp_f32_e32 v33, v66
v_exp_f32_e32 v29, v67
)" R"(
v_add_f32_e32 v14, v14, v33
v_add_f32_e32 v14, v14, v29
Cv 33,33,29
v_exp_f32_e32 v34, v72
v_exp_f32_e32 v24, v73
v_add_f32_e32 v14, v14, v34
v_add_f32_e32 v14, v14, v24
Cv 34,34,24
v_exp_f32_e32 v35, v74
v_exp_f32_e32 v25, v75
v_add_f32_e32 v14, v14, v35
v_add_f32_e32 v14, v14, v25
Cv 35,35,25
v_exp_f32_e32 v36, v68
v_exp_f32_e32 v26, v69
s_nop 0
v_add_f32_e32 v15, v36, v26
Cv 36,36,26
v_exp_f32_e32 v37, v70
v_exp_f32_e32 v27, v71
v_add_f32_e32 v15, v15, v37
v_add_f32_e32 v15, v15, v27
Cv 37,37,27
v_exp_f32_e32 v38, v76
v_exp_f32_e32 v28, v77
v_add_f32_e32 v15, v15, v38
v_add_f32_e32 v15, v15, v28
Cv 38,38,28
v_exp_f32_e32 v39, v78
v_exp_f32_e32 v29, v79
v_add_f32_e32 v15, v15, v39
v_add_f32_e32 v15, v15, v29
Cv 39,39,29
v_exp_f32_e32 v40, v80
v_exp_f32_e32 v24, v81
v_add_f32_e32 v14, v14, v40
v_add_f32_e32 v14, v14, v24
Cv 40,40,24
v_exp_f32_e32 v41, v82
v_exp_f32_e32 v25, v83
v_add_f32_e32 v14, v14, v41
v_add_f32_e32 v14, v14, v25
Cv 41,41,25
v_exp_f32_e32 v42, v88
v_exp_f32_e32 v26, v89
v_add_f32_e32 v14, v14, v42
v_add_f32_e32 v14, v14, v26
Cv 42,42,26
v_exp_f32_e32 v43, v90
v_exp_f32_e32 v27, v91
v_add_f32_e32 v14, v14, v43
v_add_f32_e32 v14, v14, v27
Cv 43,43,27
v_exp_f32_e32 v44, v84
v_exp_f32_e32 v28, v85
v_add_f32_e32 v15, v15, v44
v_add_f32_e32 v15, v15, v28
Cv 44,44,28
v_exp_f32_e32 v45, v86
v_exp_f32_e32 v29, v87
v_add_f32_e32 v15, v15, v45
v_add_f32_e32 v15, v15, v29
Cv 45,45,29
v_exp_f32_e32 v46, v92
)" R"(
v_exp_f32_e32 v24, v93
v_add_f32_e32 v15, v15, v46
v_add_f32_e32 v15, v15, v24
Cv 46,46,24
v_exp_f32_e32 v47, v94
v_exp_f32_e32 v25, v95
v_add_f32_e32 v15, v15, v47
v_add_f32_e32 v15, v15, v25
Cv 47,47,25
s_nop 1
s_setpc_b64 s[30:31]
.LzH24:
s_cbranch_execz .LzH40
.LzMASK1:
s_lshl1_add_u32 s28, s57, s33
s_mov_b32 s29, 0xff800000
s_cmp_gt_u32 s28, 5
s_cbranch_scc1 .LzMASKE1
s_cmp_eq_u32 s28, 5
s_cbranch_scc1 .LzMASKB1
v_mov_b32 v104, s29
v_mov_b32 v105, s29
v_mov_b32 v106, s29
v_mov_b32 v107, s29
v_mov_b32 v112, s29
v_mov_b32 v113, s29
v_mov_b32 v114, s29
v_mov_b32 v115, s29
v_mov_b32 v116, s29
v_mov_b32 v117, s29
v_mov_b32 v118, s29
v_mov_b32 v119, s29
v_mov_b32 v120, s29
v_mov_b32 v121, s29
v_mov_b32 v122, s29
v_mov_b32 v123, s29
v_mov_b32 v124, s29
v_mov_b32 v125, s29
v_mov_b32 v126, s29
v_mov_b32 v127, s29
s_cmp_eq_u32 s28, 4
s_cbranch_scc1 .LzMASKA1
v_mov_b32 v96, s29
v_mov_b32 v97, s29
v_mov_b32 v98, s29
v_mov_b32 v99, s29
v_mov_b32 v100, s29
v_mov_b32 v101, s29
v_mov_b32 v102, s29
v_mov_b32 v103, s29
v_mov_b32 v108, s29
v_mov_b32 v109, s29
v_mov_b32 v110, s29
v_mov_b32 v111, s29
.LzMASKE1:
s_setpc_b64 s[30:31]
.LzMASKA1:
s_mov_b32 exec_lo, 0xf0000
s_mov_b32 exec_hi, 0xfff00ff
v_mov_b32 v96, s29
v_mov_b32 v108, s29
s_mov_b32 exec_lo, 0x1f0001
s_mov_b32 exec_hi, 0x1fff01ff
v_mov_b32 v97, s29
)" R"(
v_mov_b32 v109, s29
s_mov_b32 exec_lo, 0x3f0003
s_mov_b32 exec_hi, 0x3fff03ff
v_mov_b32 v98, s29
v_mov_b32 v110, s29
s_mov_b32 exec_lo, 0x7f0007
s_mov_b32 exec_hi, 0x7fff07ff
v_mov_b32 v99, s29
v_mov_b32 v111, s29
s_mov_b64 exec, -1
s_setpc_b64 s[30:31]
.LzMASKB1:
s_nop 1
v_mov_b32 v120, s29
v_mov_b32 v121, s29
v_mov_b32 v122, s29
v_mov_b32 v123, s29
s_mov_b32 exec_lo, 0xf0000
s_mov_b32 exec_hi, 0xfff00ff
v_mov_b32 v112, s29
v_mov_b32 v124, s29
s_mov_b32 exec_lo, 0x1f0001
s_mov_b32 exec_hi, 0x1fff01ff
v_mov_b32 v113, s29
v_mov_b32 v125, s29
s_mov_b32 exec_lo, 0x3f0003
s_mov_b32 exec_hi, 0x3fff03ff
v_mov_b32 v114, s29
v_mov_b32 v126, s29
s_mov_b32 exec_lo, 0x7f0007
s_mov_b32 exec_hi, 0x7fff07ff
v_mov_b32 v115, s29
v_mov_b32 v127, s29
s_mov_b64 exec, -1
s_setpc_b64 s[30:31]
.LzFIX1:
s_nop 7
v_max_f32_e32 v28, v96, v97
v_max3_f32 v28, v28, v98, v99
v_max3_f32 v28, v28, v104, v105
v_max3_f32 v28, v28, v106, v107
v_max3_f32 v28, v28, v112, v113
v_max3_f32 v28, v28, v114, v115
v_max3_f32 v28, v28, v120, v121
v_max3_f32 v28, v28, v122, v123
v_mov_b32 v24, v28
s_nop 1
v_permlane32_swap_b32_e32 v28, v24
v_max_f32_e32 v28, v28, v24
v_mov_b32 v24, v28
s_nop 1
v_permlane16_swap_b32_e32 v28, v24
v_max_f32_e32 v28, v28, v24
v_max_f32_e32 v29, v100, v101
v_max3_f32 v29, v29, v102, v103
v_max3_f32 v29, v29, v108, v109
v_max3_f32 v29, v29, v110, v111
v_max3_f32 v29, v29, v116, v117
v_max3_f32 v29, v29, v118, v119
v_max3_f32 v29, v29, v124, v125
v_max3_f32 v29, v29, v126, v127
v_mov_b32 v24, v29
s_nop 1
v_permlane32_swap_b32_e32 v29, v24
)" R"(
v_max_f32_e32 v29, v29, v24
v_mov_b32 v24, v29
s_nop 1
v_permlane16_swap_b32_e32 v29, v24
v_max_f32_e32 v29, v29, v24
s_bitcmp1_b32 s88, 0
s_cbranch_scc1 .LzFIXF1
v_max_f32_e32 v28, 0, v28
v_sub_f32_e32 v24, 0, v28
v_exp_f32_e32 v24, v24
s_nop 0
v_mul_f32_e32 v12, v24, v12
v_mul_f32_e32 v128, v24, v128
v_mul_f32_e32 v129, v24, v129
v_mul_f32_e32 v130, v24, v130
v_mul_f32_e32 v131, v24, v131
v_mul_f32_e32 v132, v24, v132
v_mul_f32_e32 v133, v24, v133
v_mul_f32_e32 v134, v24, v134
v_mul_f32_e32 v135, v24, v135
v_mul_f32_e32 v144, v24, v144
v_mul_f32_e32 v145, v24, v145
v_mul_f32_e32 v146, v24, v146
v_mul_f32_e32 v147, v24, v147
v_mul_f32_e32 v148, v24, v148
v_mul_f32_e32 v149, v24, v149
v_mul_f32_e32 v150, v24, v150
v_mul_f32_e32 v151, v24, v151
v_mul_f32_e32 v160, v24, v160
v_mul_f32_e32 v161, v24, v161
v_mul_f32_e32 v162, v24, v162
v_mul_f32_e32 v163, v24, v163
v_mul_f32_e32 v164, v24, v164
v_mul_f32_e32 v165, v24, v165
v_mul_f32_e32 v166, v24, v166
v_mul_f32_e32 v167, v24, v167
v_mul_f32_e32 v176, v24, v176
v_mul_f32_e32 v177, v24, v177
v_mul_f32_e32 v178, v24, v178
v_mul_f32_e32 v179, v24, v179
v_mul_f32_e32 v180, v24, v180
v_mul_f32_e32 v181, v24, v181
v_mul_f32_e32 v182, v24, v182
v_mul_f32_e32 v183, v24, v183
v_mul_f32_e32 v192, v24, v192
v_mul_f32_e32 v193, v24, v193
v_mul_f32_e32 v194, v24, v194
v_mul_f32_e32 v195, v24, v195
v_mul_f32_e32 v196, v24, v196
v_mul_f32_e32 v197, v24, v197
v_mul_f32_e32 v198, v24, v198
v_mul_f32_e32 v199, v24, v199
v_mul_f32_e32 v208, v24, v208
v_mul_f32_e32 v209, v24, v209
v_mul_f32_e32 v210, v24, v210
v_mul_f32_e32 v211, v24, v211
v_mul_f32_e32 v212, v24, v212
v_mul_f32_e32 v213, v24, v213
v_mul_f32_e32 v214, v24, v214
v_mul_f32_e32 v215, v24, v215
v_mul_f32_e32 v224, v24, v224
v_mul_f32_e32 v225, v24, v225
v_mul_f32_e32 v226, v24, v226
v_mul_f32_e32 v227, v24, v227
)" R"(
v_mul_f32_e32 v228, v24, v228
v_mul_f32_e32 v229, v24, v229
v_mul_f32_e32 v230, v24, v230
v_mul_f32_e32 v231, v24, v231
v_mul_f32_e32 v240, v24, v240
v_mul_f32_e32 v241, v24, v241
v_mul_f32_e32 v242, v24, v242
v_mul_f32_e32 v243, v24, v243
v_mul_f32_e32 v244, v24, v244
v_mul_f32_e32 v245, v24, v245
v_mul_f32_e32 v246, v24, v246
v_mul_f32_e32 v247, v24, v247
v_max_f32_e32 v29, 0, v29
v_sub_f32_e32 v24, 0, v29
v_exp_f32_e32 v24, v24
s_nop 0
v_mul_f32_e32 v13, v24, v13
v_mul_f32_e32 v136, v24, v136
v_mul_f32_e32 v137, v24, v137
v_mul_f32_e32 v138, v24, v138
v_mul_f32_e32 v139, v24, v139
v_mul_f32_e32 v140, v24, v140
v_mul_f32_e32 v141, v24, v141
v_mul_f32_e32 v142, v24, v142
v_mul_f32_e32 v143, v24, v143
v_mul_f32_e32 v152, v24, v152
v_mul_f32_e32 v153, v24, v153
v_mul_f32_e32 v154, v24, v154
v_mul_f32_e32 v155, v24, v155
v_mul_f32_e32 v156, v24, v156
v_mul_f32_e32 v157, v24, v157
v_mul_f32_e32 v158, v24, v158
v_mul_f32_e32 v159, v24, v159
v_mul_f32_e32 v168, v24, v168
v_mul_f32_e32 v169, v24, v169
v_mul_f32_e32 v170, v24, v170
v_mul_f32_e32 v171, v24, v171
v_mul_f32_e32 v172, v24, v172
v_mul_f32_e32 v173, v24, v173
v_mul_f32_e32 v174, v24, v174
v_mul_f32_e32 v175, v24, v175
v_mul_f32_e32 v184, v24, v184
v_mul_f32_e32 v185, v24, v185
v_mul_f32_e32 v186, v24, v186
v_mul_f32_e32 v187, v24, v187
v_mul_f32_e32 v188, v24, v188
v_mul_f32_e32 v189, v24, v189
v_mul_f32_e32 v190, v24, v190
v_mul_f32_e32 v191, v24, v191
v_mul_f32_e32 v200, v24, v200
v_mul_f32_e32 v201, v24, v201
v_mul_f32_e32 v202, v24, v202
v_mul_f32_e32 v203, v24, v203
v_mul_f32_e32 v204, v24, v204
v_mul_f32_e32 v205, v24, v205
v_mul_f32_e32 v206, v24, v206
v_mul_f32_e32 v207, v24, v207
v_mul_f32_e32 v216, v24, v216
v_mul_f32_e32 v217, v24, v217
v_mul_f32_e32 v218, v24, v218
v_mul_f32_e32 v219, v24, v219
v_mul_f32_e32 v220, v24, v220
v_mul_f32_e32 v221, v24, v221
v_mul_f32_e32 v222, v24, v222
)" R"(
v_mul_f32_e32 v223, v24, v223
v_mul_f32_e32 v232, v24, v232
v_mul_f32_e32 v233, v24, v233
v_mul_f32_e32 v234, v24, v234
v_mul_f32_e32 v235, v24, v235
v_mul_f32_e32 v236, v24, v236
v_mul_f32_e32 v237, v24, v237
v_mul_f32_e32 v238, v24, v238
v_mul_f32_e32 v239, v24, v239
v_mul_f32_e32 v248, v24, v248
v_mul_f32_e32 v249, v24, v249
v_mul_f32_e32 v250, v24, v250
v_mul_f32_e32 v251, v24, v251
v_mul_f32_e32 v252, v24, v252
v_mul_f32_e32 v253, v24, v253
v_mul_f32_e32 v254, v24, v254
v_mul_f32_e32 v255, v24, v255
s_branch .LzFIXG1
.LzFIXF1:
v_cmp_lt_f32_e32 vcc, 0xff7fffff, v28
s_nop 1
v_cndmask_b32_e32 v28, 0, v28, vcc
v_cmp_lt_f32_e32 vcc, 0xff7fffff, v29
s_nop 1
v_cndmask_b32_e32 v29, 0, v29, vcc
.LzFIXG1:
v_sub_f32_e32 v16, v16, v28
v_sub_f32_e32 v17, v17, v28
v_sub_f32_e32 v18, v18, v28
v_sub_f32_e32 v19, v19, v28
v_sub_f32_e32 v96, v96, v28
v_sub_f32_e32 v97, v97, v28
v_sub_f32_e32 v98, v98, v28
v_sub_f32_e32 v99, v99, v28
v_sub_f32_e32 v104, v104, v28
v_sub_f32_e32 v105, v105, v28
v_sub_f32_e32 v106, v106, v28
v_sub_f32_e32 v107, v107, v28
v_sub_f32_e32 v112, v112, v28
v_sub_f32_e32 v113, v113, v28
v_sub_f32_e32 v114, v114, v28
v_sub_f32_e32 v115, v115, v28
v_sub_f32_e32 v120, v120, v28
v_sub_f32_e32 v121, v121, v28
v_sub_f32_e32 v122, v122, v28
v_sub_f32_e32 v123, v123, v28
v_sub_f32_e32 v20, v20, v29
v_sub_f32_e32 v21, v21, v29
v_sub_f32_e32 v22, v22, v29
v_sub_f32_e32 v23, v23, v29
v_sub_f32_e32 v100, v100, v29
v_sub_f32_e32 v101, v101, v29
v_sub_f32_e32 v102, v102, v29
v_sub_f32_e32 v103, v103, v29
v_sub_f32_e32 v108, v108, v29
v_sub_f32_e32 v109, v109, v29
v_sub_f32_e32 v110, v110, v29
v_sub_f32_e32 v111, v111, v29
v_sub_f32_e32 v116, v116, v29
v_sub_f32_e32 v117, v117, v29
v_sub_f32_e32 v118, v118, v29
v_sub_f32_e32 v119, v119, v29
v_sub_f32_e32 v124, v124, v29
v_sub_f32_e32 v125, v125, v29
)" R"(
v_sub_f32_e32 v126, v126, v29
v_sub_f32_e32 v127, v127, v29
s_bitcmp0_b32 s88, 1
s_cbranch_scc1 .LzFIXH1
v_sub_f32_e32 v64, v64, v28
v_sub_f32_e32 v65, v65, v28
v_sub_f32_e32 v66, v66, v28
v_sub_f32_e32 v67, v67, v28
v_sub_f32_e32 v72, v72, v28
v_sub_f32_e32 v73, v73, v28
v_sub_f32_e32 v74, v74, v28
v_sub_f32_e32 v75, v75, v28
v_sub_f32_e32 v80, v80, v28
v_sub_f32_e32 v81, v81, v28
v_sub_f32_e32 v82, v82, v28
v_sub_f32_e32 v83, v83, v28
v_sub_f32_e32 v88, v88, v28
v_sub_f32_e32 v89, v89, v28
v_sub_f32_e32 v90, v90, v28
v_sub_f32_e32 v91, v91, v28
v_sub_f32_e32 v68, v68, v29
v_sub_f32_e32 v69, v69, v29
v_sub_f32_e32 v70, v70, v29
v_sub_f32_e32 v71, v71, v29
v_sub_f32_e32 v76, v76, v29
v_sub_f32_e32 v77, v77, v29
v_sub_f32_e32 v78, v78, v29
v_sub_f32_e32 v79, v79, v29
v_sub_f32_e32 v84, v84, v29
v_sub_f32_e32 v85, v85, v29
v_sub_f32_e32 v86, v86, v29
v_sub_f32_e32 v87, v87, v29
v_sub_f32_e32 v92, v92, v29
v_sub_f32_e32 v93, v93, v29
v_sub_f32_e32 v94, v94, v29
v_sub_f32_e32 v95, v95, v29
.LzFIXH1:
v_exp_f32_e32 v48, v96
v_exp_f32_e32 v26, v97
s_nop 0
v_add_f32_e32 v14, v48, v26
Cv 48,48,26
v_exp_f32_e32 v49, v98
v_exp_f32_e32 v27, v99
v_add_f32_e32 v14, v14, v49
v_add_f32_e32 v14, v14, v27
Cv 49,49,27
v_exp_f32_e32 v50, v104
v_exp_f32_e32 v28, v105
v_add_f32_e32 v14, v14, v50
v_add_f32_e32 v14, v14, v28
Cv 50,50,28
v_exp_f32_e32 v51, v106
v_exp_f32_e32 v29, v107
v_add_f32_e32 v14, v14, v51
v_add_f32_e32 v14, v14, v29
Cv 51,51,29
v_exp_f32_e32 v52, v100
v_exp_f32_e32 v24, v101
s_nop 0
v_add_f32_e32 v15, v52, v24
Cv 52,52,24
v_exp_f32_e32 v53, v102
v_exp_f32_e32 v25, v103
)" R"(
v_add_f32_e32 v15, v15, v53
v_add_f32_e32 v15, v15, v25
Cv 53,53,25
v_exp_f32_e32 v54, v108
v_exp_f32_e32 v26, v109
v_add_f32_e32 v15, v15, v54
v_add_f32_e32 v15, v15, v26
Cv 54,54,26
v_exp_f32_e32 v55, v110
v_exp_f32_e32 v27, v111
v_add_f32_e32 v15, v15, v55
v_add_f32_e32 v15, v15, v27
Cv 55,55,27
v_exp_f32_e32 v56, v112
v_exp_f32_e32 v28, v113
v_add_f32_e32 v14, v14, v56
v_add_f32_e32 v14, v14, v28
Cv 56,56,28
v_exp_f32_e32 v57, v114
v_exp_f32_e32 v29, v115
v_add_f32_e32 v14, v14, v57
v_add_f32_e32 v14, v14, v29
Cv 57,57,29
v_exp_f32_e32 v58, v120
v_exp_f32_e32 v24, v121
v_add_f32_e32 v14, v14, v58
v_add_f32_e32 v14, v14, v24
Cv 58,58,24
v_exp_f32_e32 v59, v122
v_exp_f32_e32 v25, v123
v_add_f32_e32 v14, v14, v59
v_add_f32_e32 v14, v14, v25
Cv 59,59,25
v_exp_f32_e32 v60, v116
v_exp_f32_e32 v26, v117
v_add_f32_e32 v15, v15, v60
v_add_f32_e32 v15, v15, v26
Cv 60,60,26
v_exp_f32_e32 v61, v118
v_exp_f32_e32 v27, v119
v_add_f32_e32 v15, v15, v61
v_add_f32_e32 v15, v15, v27
Cv 61,61,27
v_exp_f32_e32 v62, v124
v_exp_f32_e32 v28, v125
v_add_f32_e32 v15, v15, v62
v_add_f32_e32 v15, v15, v28
Cv 62,62,28
v_exp_f32_e32 v63, v126
v_exp_f32_e32 v29, v127
v_add_f32_e32 v15, v15, v63
v_add_f32_e32 v15, v15, v29
Cv 63,63,29
s_nop 1
s_setpc_b64 s[30:31]
.Lzsp1:
s_sub_u32 s53, s53, 1
s_cbranch_scc1 .Lzsq1
s_sleep 1
buffer_load_dword v4, v2, s[20:23], s85 offen sc0 sc1
s_waitcnt vmcnt(0)
v_xor_b32 v4, s3, v4
v_cmp_gt_u32_e32 vcc, 2, v4
s_andn2_b32 s28, 0xff, vcc_lo
)" R"(
s_cbranch_scc1 .Lzsp1
s_branch .Lzok1
.Lzsq1:
s_mov_b32 s53, 0
s_branch .Lzok1
.Lzsp2:
s_sub_u32 s53, s53, 1
s_cbranch_scc1 .Lzsq2
s_sleep 1
buffer_load_dword v4, v7, s[20:23], s52 offen offset:0 sc0 sc1
s_waitcnt vmcnt(0)
v_cmp_eq_u32_e32 vcc, s3, v4
s_andn2_b64 s[28:29], 1, vcc
s_cbranch_scc1 .Lzsp2
s_branch .Lzok2
.Lzsq2:
s_mov_b32 s53, 0
s_branch .Lzok2
.Lzsp3:
s_sub_u32 s53, s53, 1
s_cbranch_scc1 .Lzsq3
s_sleep 1
buffer_load_dword v4, v2, s[20:23], s85 offen sc0 sc1
s_waitcnt vmcnt(0)
v_xor_b32 v4, s3, v4
v_cmp_gt_u32_e32 vcc, 2, v4
s_andn2_b32 s28, 0xff, vcc_lo
s_cbranch_scc1 .Lzsp3
s_branch .Lzok3
.Lzsq3:
s_mov_b32 s53, 0
s_branch .Lzok3
.Lzsp4:
s_sub_u32 s53, s53, 1
s_cbranch_scc1 .Lzsq4
s_sleep 1
buffer_load_dword v4, v7, s[20:23], s52 offen offset:0 sc0 sc1
s_waitcnt vmcnt(0)
v_cmp_eq_u32_e32 vcc, s3, v4
s_andn2_b64 s[28:29], 1, vcc
s_cbranch_scc1 .Lzsp4
s_branch .Lzok4
.Lzsq4:
s_mov_b32 s53, 0
s_branch .Lzok4
.Lzsp5:
s_sub_u32 s53, s53, 1
s_cbranch_scc1 .Lzsq5
s_sleep 1
buffer_load_dword v4, v5, s[20:23], s50 offen offset:0 sc0 sc1
s_waitcnt vmcnt(0)
v_cmp_eq_u32_e32 vcc, s3, v4
s_andn2_b64 s[28:29], -1, vcc
s_cbranch_scc1 .Lzsp5
s_branch .Lzok5
.Lzsq5:
s_mov_b32 s53, 0
s_branch .Lzok5
.Lzsp6:
s_sub_u32 s53, s53, 1
s_cbranch_scc1 .Lzsq6
s_sleep 1
buffer_load_dword v4, v5, s[20:23], s50 offen offset:0 sc0 sc1
s_waitcnt vmcnt(0)
)" R"(
v_cmp_eq_u32_e32 vcc, s3, v4
s_andn2_b64 s[28:29], -1, vcc
s_cbranch_scc1 .Lzsp6
s_branch .Lzok6
.Lzsq6:
s_mov_b32 s53, 0
s_branch .Lzok6
.Lzsp7:
s_sub_u32 s53, s53, 1
s_cbranch_scc1 .Lzsq7
s_sleep 1
buffer_load_dwordx2 v[24:25], v7, s[20:23], s83 offen sc0 sc1
s_waitcnt vmcnt(0)
v_cmp_eq_u32_e32 vcc, s3, v25
s_andn2_b64 s[28:29], 1, vcc
s_cbranch_scc1 .Lzsp7
s_branch .Lzok7
.Lzsq7:
s_mov_b32 s53, 0
s_branch .Lzok7
.LzT2S:
s_cmp_lg_u32 s33, 0
s_cbranch_scc1 .LzT2E
.LzT2:
s_mov_b32 s28, 0x1500000
buffer_load_dwordx2 v[32:33], v1, s[20:23], s28 offen offset:0 sc0 sc1
buffer_load_dwordx2 v[34:35], v1, s[20:23], s28 offen offset:1024 sc0 sc1
buffer_load_dwordx2 v[36:37], v1, s[20:23], s28 offen offset:2048 sc0 sc1
buffer_load_dwordx2 v[38:39], v1, s[20:23], s28 offen offset:3072 sc0 sc1
s_mov_b32 s28, 0x1501000
buffer_load_dwordx2 v[40:41], v1, s[20:23], s28 offen offset:0 sc0 sc1
buffer_load_dwordx2 v[42:43], v1, s[20:23], s28 offen offset:1024 sc0 sc1
buffer_load_dwordx2 v[44:45], v1, s[20:23], s28 offen offset:2048 sc0 sc1
buffer_load_dwordx2 v[46:47], v1, s[20:23], s28 offen offset:3072 sc0 sc1
s_mov_b32 s28, 0x1502000
buffer_load_dwordx2 v[48:49], v1, s[20:23], s28 offen offset:0 sc0 sc1
buffer_load_dwordx2 v[50:51], v1, s[20:23], s28 offen offset:1024 sc0 sc1
buffer_load_dwordx2 v[52:53], v1, s[20:23], s28 offen offset:2048 sc0 sc1
buffer_load_dwordx2 v[54:55], v1, s[20:23], s28 offen offset:3072 sc0 sc1
s_mov_b32 s28, 0x1503000
buffer_load_dwordx2 v[56:57], v1, s[20:23], s28 offen offset:0 sc0 sc1
buffer_load_dwordx2 v[58:59], v1, s[20:23], s28 offen offset:1024 sc0 sc1
buffer_load_dwordx2 v[60:61], v1, s[20:23], s28 offen offset:2048 sc0 sc1
buffer_load_dwordx2 v[62:63], v1, s[20:23], s28 offen offset:3072 sc0 sc1
s_mov_b32 s28, 0x1504000
buffer_load_dwordx2 v[64:65], v1, s[20:23], s28 offen offset:0 sc0 sc1
buffer_load_dwordx2 v[66:67], v1, s[20:23], s28 offen offset:1024 sc0 sc1
buffer_load_dwordx2 v[68:69], v1, s[20:23], s28 offen offset:2048 sc0 sc1
buffer_load_dwordx2 v[70:71], v1, s[20:23], s28 offen offset:3072 sc0 sc1
s_mov_b32 s28, 0x1505000
buffer_load_dwordx2 v[72:73], v1, s[20:23], s28 offen offset:0 sc0 sc1
buffer_load_dwordx2 v[74:75], v1, s[20:23], s28 offen offset:1024 sc0 sc1
buffer_load_dwordx2 v[76:77], v1, s[20:23], s28 offen offset:2048 sc0 sc1
buffer_load_dwordx2 v[78:79], v1, s[20:23], s28 offen offset:3072 sc0 sc1
s_mov_b32 s28, 0x1506000
buffer_load_dwordx2 v[80:81], v1, s[20:23], s28 offen offset:0 sc0 sc1
buffer_load_dwordx2 v[82:83], v1, s[20:23], s28 offen offset:1024 sc0 sc1
buffer_load_dwordx2 v[84:85], v1, s[20:23], s28 offen offset:2048 sc0 sc1
buffer_load_dwordx2 v[86:87], v1, s[20:23], s28 offen offset:3072 sc0 sc1
s_mov_b32 s28, 0x1507000
buffer_load_dwordx2 v[88:89], v1, s[20:23], s28 offen offset:0 sc0 sc1
buffer_load_dwordx2 v[90:91], v1, s[20:23], s28 offen offset:1024 sc0 sc1
buffer_load_dwordx2 v[92:93], v1, s[20:23], s28 offen offset:2048 sc0 sc1
buffer_load_dwordx2 v[94:95], v1, s[20:23], s28 offen offset:3072 sc0 sc1
)" R"(
v_mov_b32 v26, 0
v_mov_b32 v27, -1
s_mov_b32 s28, 0x1508c00
buffer_store_dword v26, v7, s[20:23], s28 offen sc0 sc1
buffer_store_dword v26, v7, s[20:23], s28 offen offset:128 sc0 sc1
s_waitcnt vmcnt(0)
v_max3_u32 v26, v26, v33, v35
v_min3_u32 v27, v27, v33, v35
v_max3_u32 v26, v26, v37, v39
v_min3_u32 v27, v27, v37, v39
v_max3_u32 v26, v26, v41, v43
v_min3_u32 v27, v27, v41, v43
v_max3_u32 v26, v26, v45, v47
v_min3_u32 v27, v27, v45, v47
v_max3_u32 v26, v26, v49, v51
v_min3_u32 v27, v27, v49, v51
v_max3_u32 v26, v26, v53, v55
v_min3_u32 v27, v27, v53, v55
v_max3_u32 v26, v26, v57, v59
v_min3_u32 v27, v27, v57, v59
v_max3_u32 v26, v26, v61, v63
v_min3_u32 v27, v27, v61, v63
v_max3_u32 v26, v26, v65, v67
v_min3_u32 v27, v27, v65, v67
v_max3_u32 v26, v26, v69, v71
v_min3_u32 v27, v27, v69, v71
v_max3_u32 v26, v26, v73, v75
v_min3_u32 v27, v27, v73, v75
v_max3_u32 v26, v26, v77, v79
v_min3_u32 v27, v27, v77, v79
v_max3_u32 v26, v26, v81, v83
v_min3_u32 v27, v27, v81, v83
v_max3_u32 v26, v26, v85, v87
v_min3_u32 v27, v27, v85, v87
v_max3_u32 v26, v26, v89, v91
v_min3_u32 v27, v27, v89, v91
v_max3_u32 v26, v26, v93, v95
v_min3_u32 v27, v27, v93, v95
v_cmp_ne_u32_e32 vcc, s3, v26
v_cmp_ne_u32_e64 s[28:29], s3, v27
s_or_b64 vcc, vcc, s[28:29]
s_cbranch_scc0 .LzT2G
s_sub_u32 s53, s53, 1
s_cbranch_scc0 .LzT2
s_mov_b32 s53, 0
.LzT2G:
v_max_u32 v26, v32, v34
v_min_u32 v27, v32, v34
v_med3_u32 v27, v26, v27, v36
v_max_u32 v26, v26, v36
v_med3_u32 v27, v26, v27, v38
v_max_u32 v26, v26, v38
v_med3_u32 v27, v26, v27, v40
v_max_u32 v26, v26, v40
v_med3_u32 v27, v26, v27, v42
v_max_u32 v26, v26, v42
v_med3_u32 v27, v26, v27, v44
v_max_u32 v26, v26, v44
v_med3_u32 v27, v26, v27, v46
v_max_u32 v26, v26, v46
v_med3_u32 v27, v26, v27, v48
v_max_u32 v26, v26, v48
v_med3_u32 v27, v26, v27, v50
v_max_u32 v26, v26, v50
)" R"(
v_med3_u32 v27, v26, v27, v52
v_max_u32 v26, v26, v52
v_med3_u32 v27, v26, v27, v54
v_max_u32 v26, v26, v54
v_med3_u32 v27, v26, v27, v56
v_max_u32 v26, v26, v56
v_med3_u32 v27, v26, v27, v58
v_max_u32 v26, v26, v58
v_med3_u32 v27, v26, v27, v60
v_max_u32 v26, v26, v60
v_med3_u32 v27, v26, v27, v62
v_max_u32 v26, v26, v62
v_med3_u32 v27, v26, v27, v64
v_max_u32 v26, v26, v64
v_med3_u32 v27, v26, v27, v66
v_max_u32 v26, v26, v66
v_med3_u32 v27, v26, v27, v68
v_max_u32 v26, v26, v68
v_med3_u32 v27, v26, v27, v70
v_max_u32 v26, v26, v70
v_med3_u32 v27, v26, v27, v72
v_max_u32 v26, v26, v72
v_med3_u32 v27, v26, v27, v74
v_max_u32 v26, v26, v74
v_med3_u32 v27, v26, v27, v76
v_max_u32 v26, v26, v76
v_med3_u32 v27, v26, v27, v78
v_max_u32 v26, v26, v78
v_med3_u32 v27, v26, v27, v80
v_max_u32 v26, v26, v80
v_med3_u32 v27, v26, v27, v82
v_max_u32 v26, v26, v82
v_med3_u32 v27, v26, v27, v84
v_max_u32 v26, v26, v84
v_med3_u32 v27, v26, v27, v86
v_max_u32 v26, v26, v86
v_med3_u32 v27, v26, v27, v88
v_max_u32 v26, v26, v88
v_med3_u32 v27, v26, v27, v90
v_max_u32 v26, v26, v90
v_med3_u32 v27, v26, v27, v92
v_max_u32 v26, v26, v92
v_med3_u32 v27, v26, v27, v94
v_max_u32 v26, v26, v94
v_mov_b32 v24, v26
v_mov_b32 v25, v27
s_nop 1
v_permlane32_swap_b32_e32 v26, v24
v_permlane32_swap_b32_e32 v27, v25
v_max_u32 v27, v27, v25
v_med3_u32 v27, v26, v27, v24
v_max_u32 v26, v26, v24
v_mov_b32 v24, v26
v_mov_b32 v25, v27
s_nop 1
v_permlane16_swap_b32_e32 v26, v24
v_permlane16_swap_b32_e32 v27, v25
v_max_u32 v27, v27, v25
v_med3_u32 v27, v26, v27, v24
v_max_u32 v26, v26, v24
s_nop 1
v_mov_b32_dpp v24, v26 row_ror:8 row_mask:0xf bank_mask:0xf
v_mov_b32_dpp v25, v27 row_ror:8 row_mask:0xf bank_mask:0xf
v_max_u32 v27, v27, v25
)" R"(
v_med3_u32 v27, v26, v27, v24
v_max_u32 v26, v26, v24
s_nop 1
v_mov_b32_dpp v24, v26 row_ror:4 row_mask:0xf bank_mask:0xf
v_mov_b32_dpp v25, v27 row_ror:4 row_mask:0xf bank_mask:0xf
v_max_u32 v27, v27, v25
v_med3_u32 v27, v26, v27, v24
v_max_u32 v26, v26, v24
s_nop 1
v_mov_b32_dpp v24, v26 row_ror:2 row_mask:0xf bank_mask:0xf
v_mov_b32_dpp v25, v27 row_ror:2 row_mask:0xf bank_mask:0xf
v_max_u32 v27, v27, v25
v_med3_u32 v27, v26, v27, v24
v_max_u32 v26, v26, v24
s_nop 1
v_mov_b32_dpp v24, v26 row_ror:1 row_mask:0xf bank_mask:0xf
v_mov_b32_dpp v25, v27 row_ror:1 row_mask:0xf bank_mask:0xf
v_max_u32 v27, v27, v25
v_med3_u32 v27, v26, v27, v24
v_max_u32 v26, v26, v24
v_and_b32 v26, 0xffff, v26
v_lshl_or_b32 v26, v27, 16, v26
v_not_b32 v24, v26
v_mov_b32 v25, s3
s_mov_b32 s28, 0x1508000
buffer_store_dwordx2 v[24:25], v7, s[20:23], s28 offen sc0 sc1
s_branch .LzT2E
.LzA7:
s_waitcnt vmcnt(0)
s_nop 7
v_mul_f32_e32 v96, s54, v96
v_mul_f32_e32 v97, s54, v97
Cv 96,96,97
v_accvgpr_write_b32 a112, v96
v_mul_f32_e32 v98, s54, v98
v_mul_f32_e32 v99, s54, v99
Cv 98,98,99
v_accvgpr_write_b32 a113, v98
v_mul_f32_e32 v100, s54, v100
v_mul_f32_e32 v101, s54, v101
Cv 100,100,101
v_accvgpr_write_b32 a114, v100
v_mul_f32_e32 v102, s54, v102
v_mul_f32_e32 v103, s54, v103
Cv 102,102,103
v_accvgpr_write_b32 a115, v102
v_mul_f32_e32 v104, s54, v104
v_mul_f32_e32 v105, s54, v105
Cv 104,104,105
v_accvgpr_write_b32 a116, v104
v_mul_f32_e32 v106, s54, v106
v_mul_f32_e32 v107, s54, v107
Cv 106,106,107
v_accvgpr_write_b32 a117, v106
v_mul_f32_e32 v108, s54, v108
v_mul_f32_e32 v109, s54, v109
Cv 108,108,109
v_accvgpr_write_b32 a118, v108
v_mul_f32_e32 v110, s54, v110
v_mul_f32_e32 v111, s54, v111
Cv 110,110,111
v_accvgpr_write_b32 a119, v110
v_mul_f32_e32 v112, s54, v112
v_mul_f32_e32 v113, s54, v113
)" R"(
Cv 112,112,113
v_accvgpr_write_b32 a120, v112
v_mul_f32_e32 v114, s54, v114
v_mul_f32_e32 v115, s54, v115
Cv 114,114,115
v_accvgpr_write_b32 a121, v114
v_mul_f32_e32 v116, s54, v116
v_mul_f32_e32 v117, s54, v117
Cv 116,116,117
v_accvgpr_write_b32 a122, v116
v_mul_f32_e32 v118, s54, v118
v_mul_f32_e32 v119, s54, v119
Cv 118,118,119
v_accvgpr_write_b32 a123, v118
v_mul_f32_e32 v120, s54, v120
v_mul_f32_e32 v121, s54, v121
Cv 120,120,121
v_accvgpr_write_b32 a124, v120
v_mul_f32_e32 v122, s54, v122
v_mul_f32_e32 v123, s54, v123
Cv 122,122,123
v_accvgpr_write_b32 a125, v122
v_mul_f32_e32 v124, s54, v124
v_mul_f32_e32 v125, s54, v125
Cv 124,124,125
v_accvgpr_write_b32 a126, v124
v_mul_f32_e32 v126, s54, v126
v_mul_f32_e32 v127, s54, v127
Cv 126,126,127
v_accvgpr_write_b32 a127, v126
v_accvgpr_mov_b32 a64, a224
v_accvgpr_mov_b32 a65, a225
v_accvgpr_mov_b32 a66, a226
v_accvgpr_mov_b32 a67, a227
v_accvgpr_mov_b32 a68, a228
v_accvgpr_mov_b32 a69, a229
v_accvgpr_mov_b32 a70, a230
v_accvgpr_mov_b32 a71, a231
v_accvgpr_mov_b32 a72, a232
v_accvgpr_mov_b32 a73, a233
v_accvgpr_mov_b32 a74, a234
v_accvgpr_mov_b32 a75, a235
v_accvgpr_mov_b32 a76, a236
v_accvgpr_mov_b32 a77, a237
v_accvgpr_mov_b32 a78, a238
v_accvgpr_mov_b32 a79, a239
v_accvgpr_mov_b32 a80, a240
v_accvgpr_mov_b32 a81, a241
v_accvgpr_mov_b32 a82, a242
v_accvgpr_mov_b32 a83, a243
v_accvgpr_mov_b32 a84, a244
v_accvgpr_mov_b32 a85, a245
v_accvgpr_mov_b32 a86, a246
v_accvgpr_mov_b32 a87, a247
v_accvgpr_mov_b32 a88, a248
v_accvgpr_mov_b32 a89, a249
v_accvgpr_mov_b32 a90, a250
v_accvgpr_mov_b32 a91, a251
v_accvgpr_mov_b32 a92, a252
v_accvgpr_mov_b32 a93, a253
v_accvgpr_mov_b32 a94, a254
v_accvgpr_mov_b32 a95, a255
v_accvgpr_mov_b32 a96, a192
v_accvgpr_mov_b32 a97, a193
)" R"(
v_accvgpr_mov_b32 a98, a194
v_accvgpr_mov_b32 a99, a195
v_accvgpr_mov_b32 a100, a196
v_accvgpr_mov_b32 a101, a197
v_accvgpr_mov_b32 a102, a198
v_accvgpr_mov_b32 a103, a199
v_accvgpr_mov_b32 a104, a200
v_accvgpr_mov_b32 a105, a201
v_accvgpr_mov_b32 a106, a202
v_accvgpr_mov_b32 a107, a203
v_accvgpr_mov_b32 a108, a204
v_accvgpr_mov_b32 a109, a205
v_accvgpr_mov_b32 a110, a206
v_accvgpr_mov_b32 a111, a207
s_mov_b32 s94, 2
s_nop 1
s_branch .LzA7R
.Lzsp8:
s_sub_u32 s53, s53, 1
s_cbranch_scc1 .Lzsq8
s_sleep 1
buffer_load_dword v4, v5, s[20:23], s50 offen offset:0 sc0 sc1
s_waitcnt vmcnt(0)
v_cmp_eq_u32_e32 vcc, s3, v4
s_andn2_b64 s[28:29], -1, vcc
s_cbranch_scc1 .Lzsp8
s_branch .Lzok8
.Lzsq8:
s_mov_b32 s53, 0
s_branch .Lzok8
.Lzmk0:
s_call_b64 s[30:31], .LzMASK0
s_branch .Lzmr0
.Lzfx0:
s_mov_b32 s88, 3
s_call_b64 s[30:31], .LzFIX0
s_branch .Lzfr0
.Lzmk1:
s_call_b64 s[30:31], .LzMASK1
s_branch .Lzmr1
.Lzsp9:
s_sub_u32 s53, s53, 1
s_cbranch_scc1 .Lzsq9
s_sleep 1
buffer_load_dword v4, v5, s[20:23], s50 offen offset:0 sc0 sc1
s_waitcnt vmcnt(0)
v_cmp_eq_u32_e32 vcc, s3, v4
s_andn2_b64 s[28:29], -1, vcc
s_cbranch_scc1 .Lzsp9
s_branch .Lzok9
.Lzsq9:
s_mov_b32 s53, 0
s_branch .Lzok9
.Lzfx1:
s_mov_b32 s88, 2
s_call_b64 s[30:31], .LzFIX1
s_branch .Lzfr1
.Lzmk2:
s_call_b64 s[30:31], .LzMASK0
s_branch .Lzmr2
.Lzfx2:
s_mov_b32 s88, 2
s_call_b64 s[30:31], .LzFIX0
s_branch .Lzfr2
)" R"(
.Lzmk3:
s_call_b64 s[30:31], .LzMASK1
s_branch .Lzmr3
.Lzsp10:
s_sub_u32 s53, s53, 1
s_cbranch_scc1 .Lzsq10
s_sleep 1
buffer_load_dword v4, v5, s[20:23], s50 offen offset:0 sc0 sc1
s_waitcnt vmcnt(0)
v_cmp_eq_u32_e32 vcc, s3, v4
s_andn2_b64 s[28:29], -1, vcc
s_cbranch_scc1 .Lzsp10
s_branch .Lzok10
.Lzsq10:
s_mov_b32 s53, 0
s_branch .Lzok10
.Lzfx3:
s_mov_b32 s88, 0
s_call_b64 s[30:31], .LzFIX1
s_branch .Lzfr3
.Lzmk4:
s_call_b64 s[30:31], .LzMASK0
s_branch .Lzmr4
.Lzfx4:
s_mov_b32 s88, 3
s_call_b64 s[30:31], .LzFIX0
s_branch .Lzfr4
.Lzmk5:
s_call_b64 s[30:31], .LzMASK1
s_branch .Lzmr5
.Lzfx5:
s_mov_b32 s88, 0
s_call_b64 s[30:31], .LzFIX1
s_branch .Lzfr5
.Lzsp11:
s_sub_u32 s53, s53, 1
s_cbranch_scc1 .Lzsq11
s_sleep 1
buffer_load_dword v4, v7, s[20:23], s52 offen offset:0 sc0 sc1
s_waitcnt vmcnt(0)
v_cmp_eq_u32_e32 vcc, s3, v4
s_andn2_b64 s[28:29], 1, vcc
s_cbranch_scc1 .Lzsp11
s_branch .Lzok11
.Lzsq11:
s_mov_b32 s53, 0
s_branch .Lzok11
.Lzsp12:
s_sub_u32 s53, s53, 1
s_cbranch_scc1 .Lzsq12
s_sleep 1
buffer_load_dword v4, v7, s[20:23], s52 offen offset:128 sc0 sc1
s_waitcnt vmcnt(0)
v_cmp_eq_u32_e32 vcc, s3, v4
s_andn2_b64 s[28:29], 1, vcc
s_cbranch_scc1 .Lzsp12
s_branch .Lzok12
.Lzsq12:
s_mov_b32 s53, 0
s_branch .Lzok12
.Lzsp13:
s_sub_u32 s53, s53, 1
s_cbranch_scc1 .Lzsq13
s_sleep 1
)" R"(
buffer_load_dword v4, v2, s[20:23], s85 offen sc0 sc1
s_waitcnt vmcnt(0)
v_xor_b32 v4, s3, v4
v_cmp_gt_u32_e32 vcc, 2, v4
s_andn2_b32 s28, 0xff, vcc_lo
s_cbranch_scc1 .Lzsp13
s_branch .Lzok13
.Lzsq13:
s_mov_b32 s53, 0
s_branch .Lzok13
.Lzsp14:
s_sub_u32 s53, s53, 1
s_cbranch_scc1 .Lzsq14
s_sleep 1
buffer_load_dword v4, v5, s[20:23], s50 offen offset:0 sc0 sc1
s_waitcnt vmcnt(0)
v_cmp_eq_u32_e32 vcc, s3, v4
s_andn2_b64 s[28:29], -1, vcc
s_cbranch_scc1 .Lzsp14
s_branch .Lzok14
.Lzsq14:
s_mov_b32 s53, 0
s_branch .Lzok14
.Lzsp15:
s_sub_u32 s53, s53, 1
s_cbranch_scc1 .Lzsq15
s_sleep 1
buffer_load_dword v4, v5, s[20:23], s50 offen offset:0 sc0 sc1
s_waitcnt vmcnt(0)
v_cmp_eq_u32_e32 vcc, s3, v4
s_andn2_b64 s[28:29], -1, vcc
s_cbranch_scc1 .Lzsp15
s_branch .Lzok15
.Lzsq15:
s_mov_b32 s53, 0
s_branch .Lzok15
.Lzsp16:
s_sub_u32 s53, s53, 1
s_cbranch_scc1 .Lzsq16
s_sleep 1
buffer_load_dword v4, v2, s[20:23], s85 offen sc0 sc1
s_waitcnt vmcnt(0)
v_xor_b32 v4, s3, v4
v_cmp_gt_u32_e32 vcc, 2, v4
s_andn2_b32 s28, 0xff, vcc_lo
s_cbranch_scc1 .Lzsp16
s_branch .Lzok16
.Lzsq16:
s_mov_b32 s53, 0
s_branch .Lzok16
.Lzsp17:
s_sub_u32 s53, s53, 1
s_cbranch_scc1 .Lzsq17
s_sleep 1
buffer_load_dword v4, v5, s[20:23], s50 offen offset:0 sc0 sc1
s_waitcnt vmcnt(0)
v_cmp_eq_u32_e32 vcc, s3, v4
s_andn2_b64 s[28:29], -1, vcc
s_cbranch_scc1 .Lzsp17
s_branch .Lzok17
.Lzsq17:
s_mov_b32 s53, 0
s_branch .Lzok17
.Lzsp18:
)" R"(
s_sub_u32 s53, s53, 1
s_cbranch_scc1 .Lzsq18
s_sleep 1
buffer_load_dword v4, v5, s[20:23], s50 offen offset:0 sc0 sc1
s_waitcnt vmcnt(0)
v_cmp_eq_u32_e32 vcc, s3, v4
s_andn2_b64 s[28:29], -1, vcc
s_cbranch_scc1 .Lzsp18
s_branch .Lzok18
.Lzsq18:
s_mov_b32 s53, 0
s_branch .Lzok18
.Lzsp19:
s_sub_u32 s53, s53, 1
s_cbranch_scc1 .Lzsq19
s_sleep 1
buffer_load_dword v4, v5, s[20:23], s50 offen offset:0 sc0 sc1
s_waitcnt vmcnt(0)
v_cmp_eq_u32_e32 vcc, s3, v4
s_andn2_b64 s[28:29], -1, vcc
s_cbranch_scc1 .Lzsp19
s_branch .Lzok19
.Lzsq19:
s_mov_b32 s53, 0
s_branch .Lzok19
.Lzfx6:
s_mov_b32 s88, 3
s_call_b64 s[30:31], .LzFIX0
s_branch .Lzfr6
.Lzsp20:
s_sub_u32 s53, s53, 1
s_cbranch_scc1 .Lzsq20
s_sleep 1
buffer_load_dword v4, v5, s[20:23], s50 offen offset:0 sc0 sc1
s_waitcnt vmcnt(0)
v_cmp_eq_u32_e32 vcc, s3, v4
s_andn2_b64 s[28:29], -1, vcc
s_cbranch_scc1 .Lzsp20
s_branch .Lzok20
.Lzsq20:
s_mov_b32 s53, 0
s_branch .Lzok20
.Lzfx7:
s_mov_b32 s88, 2
s_call_b64 s[30:31], .LzFIX1
s_branch .Lzfr7
.Lzmk6:
s_call_b64 s[30:31], .LzMASK0
s_branch .Lzmr6
.Lzfx8:
s_mov_b32 s88, 2
s_call_b64 s[30:31], .LzFIX0
s_branch .Lzfr8
.Lzmk7:
s_call_b64 s[30:31], .LzMASK1
s_branch .Lzmr7
.Lzsp21:
s_sub_u32 s53, s53, 1
s_cbranch_scc1 .Lzsq21
s_sleep 1
buffer_load_dword v4, v5, s[20:23], s50 offen offset:0 sc0 sc1
s_waitcnt vmcnt(0)
v_cmp_eq_u32_e32 vcc, s3, v4
s_andn2_b64 s[28:29], -1, vcc
)" R"(
s_cbranch_scc1 .Lzsp21
s_branch .Lzok21
.Lzsq21:
s_mov_b32 s53, 0
s_branch .Lzok21
.Lzfx9:
s_mov_b32 s88, 2
s_call_b64 s[30:31], .LzFIX1
s_branch .Lzfr9
.Lzmk8:
s_call_b64 s[30:31], .LzMASK0
s_branch .Lzmr8
.Lzfx10:
s_mov_b32 s88, 2
s_call_b64 s[30:31], .LzFIX0
s_branch .Lzfr10
.Lzmk9:
s_call_b64 s[30:31], .LzMASK1
s_branch .Lzmr9
.Lzsp22:
s_sub_u32 s53, s53, 1
s_cbranch_scc1 .Lzsq22
s_sleep 1
buffer_load_dword v4, v7, s[20:23], s83 offen offset:4 sc0 sc1
s_waitcnt vmcnt(0)
v_cmp_eq_u32_e32 vcc, s3, v4
s_andn2_b64 s[28:29], 1, vcc
s_cbranch_scc1 .Lzsp22
s_branch .Lzok22
.Lzsq22:
s_mov_b32 s53, 0
s_branch .Lzok22
.Lzsp23:
s_sub_u32 s53, s53, 1
s_cbranch_scc1 .Lzsq23
s_sleep 1
buffer_load_dword v4, v7, s[20:23], s65 offen offset:0 sc0 sc1
s_waitcnt vmcnt(0)
v_cmp_eq_u32_e32 vcc, s3, v4
s_andn2_b64 s[28:29], s[60:61], vcc
s_cbranch_scc1 .Lzsp23
s_branch .Lzok23
.Lzsq23:
s_mov_b32 s53, 0
s_branch .Lzok23
.Lzfx11:
s_mov_b32 s88, 2
s_call_b64 s[30:31], .LzFIX1
s_branch .Lzfr11
.Lzmk10:
s_call_b64 s[30:31], .LzMASK0
s_branch .Lzmr10
.Lzfx12:
s_mov_b32 s88, 2
s_call_b64 s[30:31], .LzFIX0
s_branch .Lzfr12
.Lzmk11:
s_call_b64 s[30:31], .LzMASK1
s_branch .Lzmr11
.Lzfx13:
s_mov_b32 s88, 0
s_call_b64 s[30:31], .LzFIX1
s_branch .Lzfr13
.LzMRG:
)" R"(
s_waitcnt vmcnt(0)
s_nop 7
v_mov_b32 v25, v64
v_mov_b32 v24, v25
s_nop 1
v_permlane32_swap_b32_e32 v25, v24
v_add_f32_e32 v25, v25, v24
v_mov_b32 v24, v25
s_nop 1
v_permlane16_swap_b32_e32 v25, v24
v_add_f32_e32 v25, v25, v24
v_cmp_lt_f32_e32 vcc, 0, v25
s_nop 1
v_cndmask_b32_e32 v66, v16, v66, vcc
v_sub_f32_e32 v28, v16, v66
v_min_f32_e32 v26, 0, v28
v_exp_f32_e32 v26, v26
v_max_f32_e32 v28, 0, v28
v_sub_f32_e32 v24, 0, v28
v_exp_f32_e32 v24, v24
v_mul_f32_e32 v64, v26, v64
v_mul_f32_e32 v12, v24, v12
v_mul_f32_e32 v128, v24, v128
v_mul_f32_e32 v129, v24, v129
v_mul_f32_e32 v130, v24, v130
v_mul_f32_e32 v131, v24, v131
v_mul_f32_e32 v132, v24, v132
v_mul_f32_e32 v133, v24, v133
v_mul_f32_e32 v134, v24, v134
v_mul_f32_e32 v135, v24, v135
v_mul_f32_e32 v144, v24, v144
v_mul_f32_e32 v145, v24, v145
v_mul_f32_e32 v146, v24, v146
v_mul_f32_e32 v147, v24, v147
v_mul_f32_e32 v148, v24, v148
v_mul_f32_e32 v149, v24, v149
v_mul_f32_e32 v150, v24, v150
v_mul_f32_e32 v151, v24, v151
v_mul_f32_e32 v160, v24, v160
v_mul_f32_e32 v161, v24, v161
v_mul_f32_e32 v162, v24, v162
v_mul_f32_e32 v163, v24, v163
v_mul_f32_e32 v164, v24, v164
v_mul_f32_e32 v165, v24, v165
v_mul_f32_e32 v166, v24, v166
v_mul_f32_e32 v167, v24, v167
v_mul_f32_e32 v176, v24, v176
v_mul_f32_e32 v177, v24, v177
v_mul_f32_e32 v178, v24, v178
v_mul_f32_e32 v179, v24, v179
v_mul_f32_e32 v180, v24, v180
v_mul_f32_e32 v181, v24, v181
v_mul_f32_e32 v182, v24, v182
v_mul_f32_e32 v183, v24, v183
v_mul_f32_e32 v192, v24, v192
v_mul_f32_e32 v193, v24, v193
v_mul_f32_e32 v194, v24, v194
v_mul_f32_e32 v195, v24, v195
v_mul_f32_e32 v196, v24, v196
v_mul_f32_e32 v197, v24, v197
v_mul_f32_e32 v198, v24, v198
v_mul_f32_e32 v199, v24, v199
v_mul_f32_e32 v208, v24, v208
v_mul_f32_e32 v209, v24, v209
)" R"(
v_mul_f32_e32 v210, v24, v210
v_mul_f32_e32 v211, v24, v211
v_mul_f32_e32 v212, v24, v212
v_mul_f32_e32 v213, v24, v213
v_mul_f32_e32 v214, v24, v214
v_mul_f32_e32 v215, v24, v215
v_mul_f32_e32 v224, v24, v224
v_mul_f32_e32 v225, v24, v225
v_mul_f32_e32 v226, v24, v226
v_mul_f32_e32 v227, v24, v227
v_mul_f32_e32 v228, v24, v228
v_mul_f32_e32 v229, v24, v229
v_mul_f32_e32 v230, v24, v230
v_mul_f32_e32 v231, v24, v231
v_mul_f32_e32 v240, v24, v240
v_mul_f32_e32 v241, v24, v241
v_mul_f32_e32 v242, v24, v242
v_mul_f32_e32 v243, v24, v243
v_mul_f32_e32 v244, v24, v244
v_mul_f32_e32 v245, v24, v245
v_mul_f32_e32 v246, v24, v246
v_mul_f32_e32 v247, v24, v247
v_sub_f32_e32 v16, v16, v28
v_sub_f32_e32 v17, v17, v28
v_sub_f32_e32 v18, v18, v28
v_sub_f32_e32 v19, v19, v28
v_sub_f32_e32 v96, v96, v28
v_sub_f32_e32 v97, v97, v28
v_sub_f32_e32 v98, v98, v28
v_sub_f32_e32 v99, v99, v28
v_sub_f32_e32 v104, v104, v28
v_sub_f32_e32 v105, v105, v28
v_sub_f32_e32 v106, v106, v28
v_sub_f32_e32 v107, v107, v28
v_sub_f32_e32 v112, v112, v28
v_sub_f32_e32 v113, v113, v28
v_sub_f32_e32 v114, v114, v28
v_sub_f32_e32 v115, v115, v28
v_sub_f32_e32 v120, v120, v28
v_sub_f32_e32 v121, v121, v28
v_sub_f32_e32 v122, v122, v28
v_sub_f32_e32 v123, v123, v28
v_accvgpr_read_b32 v25, a128
v_lshlrev_b32 v24, 16, v25
v_and_b32 v25, 0xffff0000, v25
v_mul_f32_e32 v24, v26, v24
v_mul_f32_e32 v25, v26, v25
Cv 24,24,25
v_accvgpr_write_b32 a128, v24
v_accvgpr_read_b32 v25, a129
v_lshlrev_b32 v24, 16, v25
v_and_b32 v25, 0xffff0000, v25
v_mul_f32_e32 v24, v26, v24
v_mul_f32_e32 v25, v26, v25
Cv 24,24,25
v_accvgpr_write_b32 a129, v24
v_accvgpr_read_b32 v25, a130
v_lshlrev_b32 v24, 16, v25
v_and_b32 v25, 0xffff0000, v25
v_mul_f32_e32 v24, v26, v24
v_mul_f32_e32 v25, v26, v25
Cv 24,24,25
v_accvgpr_write_b32 a130, v24
v_accvgpr_read_b32 v25, a131
)" R"(
v_lshlrev_b32 v24, 16, v25
v_and_b32 v25, 0xffff0000, v25
v_mul_f32_e32 v24, v26, v24
v_mul_f32_e32 v25, v26, v25
Cv 24,24,25
v_accvgpr_write_b32 a131, v24
v_accvgpr_read_b32 v25, a136
v_lshlrev_b32 v24, 16, v25
v_and_b32 v25, 0xffff0000, v25
v_mul_f32_e32 v24, v26, v24
v_mul_f32_e32 v25, v26, v25
Cv 24,24,25
v_accvgpr_write_b32 a136, v24
v_accvgpr_read_b32 v25, a137
v_lshlrev_b32 v24, 16, v25
v_and_b32 v25, 0xffff0000, v25
v_mul_f32_e32 v24, v26, v24
v_mul_f32_e32 v25, v26, v25
Cv 24,24,25
v_accvgpr_write_b32 a137, v24
v_accvgpr_read_b32 v25, a138
v_lshlrev_b32 v24, 16, v25
v_and_b32 v25, 0xffff0000, v25
v_mul_f32_e32 v24, v26, v24
v_mul_f32_e32 v25, v26, v25
Cv 24,24,25
v_accvgpr_write_b32 a138, v24
v_accvgpr_read_b32 v25, a139
v_lshlrev_b32 v24, 16, v25
v_and_b32 v25, 0xffff0000, v25
v_mul_f32_e32 v24, v26, v24
v_mul_f32_e32 v25, v26, v25
Cv 24,24,25
v_accvgpr_write_b32 a139, v24
v_accvgpr_read_b32 v25, a144
v_lshlrev_b32 v24, 16, v25
v_and_b32 v25, 0xffff0000, v25
v_mul_f32_e32 v24, v26, v24
v_mul_f32_e32 v25, v26, v25
Cv 24,24,25
v_accvgpr_write_b32 a144, v24
v_accvgpr_read_b32 v25, a145
v_lshlrev_b32 v24, 16, v25
v_and_b32 v25, 0xffff0000, v25
v_mul_f32_e32 v24, v26, v24
v_mul_f32_e32 v25, v26, v25
Cv 24,24,25
v_accvgpr_write_b32 a145, v24
v_accvgpr_read_b32 v25, a146
v_lshlrev_b32 v24, 16, v25
v_and_b32 v25, 0xffff0000, v25
v_mul_f32_e32 v24, v26, v24
v_mul_f32_e32 v25, v26, v25
Cv 24,24,25
v_accvgpr_write_b32 a146, v24
v_accvgpr_read_b32 v25, a147
v_lshlrev_b32 v24, 16, v25
v_and_b32 v25, 0xffff0000, v25
v_mul_f32_e32 v24, v26, v24
v_mul_f32_e32 v25, v26, v25
Cv 24,24,25
v_accvgpr_write_b32 a147, v24
v_accvgpr_read_b32 v25, a152
v_lshlrev_b32 v24, 16, v25
)" R"(
v_and_b32 v25, 0xffff0000, v25
v_mul_f32_e32 v24, v26, v24
v_mul_f32_e32 v25, v26, v25
Cv 24,24,25
v_accvgpr_write_b32 a152, v24
v_accvgpr_read_b32 v25, a153
v_lshlrev_b32 v24, 16, v25
v_and_b32 v25, 0xffff0000, v25
v_mul_f32_e32 v24, v26, v24
v_mul_f32_e32 v25, v26, v25
Cv 24,24,25
v_accvgpr_write_b32 a153, v24
v_accvgpr_read_b32 v25, a154
v_lshlrev_b32 v24, 16, v25
v_and_b32 v25, 0xffff0000, v25
v_mul_f32_e32 v24, v26, v24
v_mul_f32_e32 v25, v26, v25
Cv 24,24,25
v_accvgpr_write_b32 a154, v24
v_accvgpr_read_b32 v25, a155
v_lshlrev_b32 v24, 16, v25
v_and_b32 v25, 0xffff0000, v25
v_mul_f32_e32 v24, v26, v24
v_mul_f32_e32 v25, v26, v25
Cv 24,24,25
v_accvgpr_write_b32 a155, v24
v_accvgpr_read_b32 v25, a160
v_lshlrev_b32 v24, 16, v25
v_and_b32 v25, 0xffff0000, v25
v_mul_f32_e32 v24, v26, v24
v_mul_f32_e32 v25, v26, v25
Cv 24,24,25
v_accvgpr_write_b32 a160, v24
v_accvgpr_read_b32 v25, a161
v_lshlrev_b32 v24, 16, v25
v_and_b32 v25, 0xffff0000, v25
v_mul_f32_e32 v24, v26, v24
v_mul_f32_e32 v25, v26, v25
Cv 24,24,25
v_accvgpr_write_b32 a161, v24
v_accvgpr_read_b32 v25, a162
v_lshlrev_b32 v24, 16, v25
v_and_b32 v25, 0xffff0000, v25
v_mul_f32_e32 v24, v26, v24
v_mul_f32_e32 v25, v26, v25
Cv 24,24,25
v_accvgpr_write_b32 a162, v24
v_accvgpr_read_b32 v25, a163
v_lshlrev_b32 v24, 16, v25
v_and_b32 v25, 0xffff0000, v25
v_mul_f32_e32 v24, v26, v24
v_mul_f32_e32 v25, v26, v25
Cv 24,24,25
v_accvgpr_write_b32 a163, v24
v_accvgpr_read_b32 v25, a168
v_lshlrev_b32 v24, 16, v25
v_and_b32 v25, 0xffff0000, v25
v_mul_f32_e32 v24, v26, v24
v_mul_f32_e32 v25, v26, v25
Cv 24,24,25
v_accvgpr_write_b32 a168, v24
v_accvgpr_read_b32 v25, a169
v_lshlrev_b32 v24, 16, v25
v_and_b32 v25, 0xffff0000, v25
)" R"(
v_mul_f32_e32 v24, v26, v24
v_mul_f32_e32 v25, v26, v25
Cv 24,24,25
v_accvgpr_write_b32 a169, v24
v_accvgpr_read_b32 v25, a170
v_lshlrev_b32 v24, 16, v25
v_and_b32 v25, 0xffff0000, v25
v_mul_f32_e32 v24, v26, v24
v_mul_f32_e32 v25, v26, v25
Cv 24,24,25
v_accvgpr_write_b32 a170, v24
v_accvgpr_read_b32 v25, a171
v_lshlrev_b32 v24, 16, v25
v_and_b32 v25, 0xffff0000, v25
v_mul_f32_e32 v24, v26, v24
v_mul_f32_e32 v25, v26, v25
Cv 24,24,25
v_accvgpr_write_b32 a171, v24
v_accvgpr_read_b32 v25, a176
v_lshlrev_b32 v24, 16, v25
v_and_b32 v25, 0xffff0000, v25
v_mul_f32_e32 v24, v26, v24
v_mul_f32_e32 v25, v26, v25
Cv 24,24,25
v_accvgpr_write_b32 a176, v24
v_accvgpr_read_b32 v25, a177
v_lshlrev_b32 v24, 16, v25
v_and_b32 v25, 0xffff0000, v25
v_mul_f32_e32 v24, v26, v24
v_mul_f32_e32 v25, v26, v25
Cv 24,24,25
v_accvgpr_write_b32 a177, v24
v_accvgpr_read_b32 v25, a178
v_lshlrev_b32 v24, 16, v25
v_and_b32 v25, 0xffff0000, v25
v_mul_f32_e32 v24, v26, v24
v_mul_f32_e32 v25, v26, v25
Cv 24,24,25
v_accvgpr_write_b32 a178, v24
v_accvgpr_read_b32 v25, a179
v_lshlrev_b32 v24, 16, v25
v_and_b32 v25, 0xffff0000, v25
v_mul_f32_e32 v24, v26, v24
v_mul_f32_e32 v25, v26, v25
Cv 24,24,25
v_accvgpr_write_b32 a179, v24
v_accvgpr_read_b32 v25, a184
v_lshlrev_b32 v24, 16, v25
v_and_b32 v25, 0xffff0000, v25
v_mul_f32_e32 v24, v26, v24
v_mul_f32_e32 v25, v26, v25
Cv 24,24,25
v_accvgpr_write_b32 a184, v24
v_accvgpr_read_b32 v25, a185
v_lshlrev_b32 v24, 16, v25
v_and_b32 v25, 0xffff0000, v25
v_mul_f32_e32 v24, v26, v24
v_mul_f32_e32 v25, v26, v25
Cv 24,24,25
v_accvgpr_write_b32 a185, v24
v_accvgpr_read_b32 v25, a186
v_lshlrev_b32 v24, 16, v25
v_and_b32 v25, 0xffff0000, v25
v_mul_f32_e32 v24, v26, v24
)" R"(
v_mul_f32_e32 v25, v26, v25
Cv 24,24,25
v_accvgpr_write_b32 a186, v24
v_accvgpr_read_b32 v25, a187
v_lshlrev_b32 v24, 16, v25
v_and_b32 v25, 0xffff0000, v25
v_mul_f32_e32 v24, v26, v24
v_mul_f32_e32 v25, v26, v25
Cv 24,24,25
v_accvgpr_write_b32 a187, v24
v_mov_b32 v25, v65
v_mov_b32 v24, v25
s_nop 1
v_permlane32_swap_b32_e32 v25, v24
v_add_f32_e32 v25, v25, v24
v_mov_b32 v24, v25
s_nop 1
v_permlane16_swap_b32_e32 v25, v24
v_add_f32_e32 v25, v25, v24
v_cmp_lt_f32_e32 vcc, 0, v25
s_nop 1
v_cndmask_b32_e32 v67, v20, v67, vcc
v_sub_f32_e32 v29, v20, v67
v_min_f32_e32 v27, 0, v29
v_exp_f32_e32 v27, v27
v_max_f32_e32 v29, 0, v29
v_sub_f32_e32 v24, 0, v29
v_exp_f32_e32 v24, v24
v_mul_f32_e32 v65, v27, v65
v_mul_f32_e32 v13, v24, v13
v_mul_f32_e32 v136, v24, v136
v_mul_f32_e32 v137, v24, v137
v_mul_f32_e32 v138, v24, v138
v_mul_f32_e32 v139, v24, v139
v_mul_f32_e32 v140, v24, v140
v_mul_f32_e32 v141, v24, v141
v_mul_f32_e32 v142, v24, v142
v_mul_f32_e32 v143, v24, v143
v_mul_f32_e32 v152, v24, v152
v_mul_f32_e32 v153, v24, v153
v_mul_f32_e32 v154, v24, v154
v_mul_f32_e32 v155, v24, v155
v_mul_f32_e32 v156, v24, v156
v_mul_f32_e32 v157, v24, v157
v_mul_f32_e32 v158, v24, v158
v_mul_f32_e32 v159, v24, v159
v_mul_f32_e32 v168, v24, v168
v_mul_f32_e32 v169, v24, v169
v_mul_f32_e32 v170, v24, v170
v_mul_f32_e32 v171, v24, v171
v_mul_f32_e32 v172, v24, v172
v_mul_f32_e32 v173, v24, v173
v_mul_f32_e32 v174, v24, v174
v_mul_f32_e32 v175, v24, v175
v_mul_f32_e32 v184, v24, v184
v_mul_f32_e32 v185, v24, v185
v_mul_f32_e32 v186, v24, v186
v_mul_f32_e32 v187, v24, v187
v_mul_f32_e32 v188, v24, v188
v_mul_f32_e32 v189, v24, v189
v_mul_f32_e32 v190, v24, v190
v_mul_f32_e32 v191, v24, v191
v_mul_f32_e32 v200, v24, v200
v_mul_f32_e32 v201, v24, v201
)" R"(
v_mul_f32_e32 v202, v24, v202
v_mul_f32_e32 v203, v24, v203
v_mul_f32_e32 v204, v24, v204
v_mul_f32_e32 v205, v24, v205
v_mul_f32_e32 v206, v24, v206
v_mul_f32_e32 v207, v24, v207
v_mul_f32_e32 v216, v24, v216
v_mul_f32_e32 v217, v24, v217
v_mul_f32_e32 v218, v24, v218
v_mul_f32_e32 v219, v24, v219
v_mul_f32_e32 v220, v24, v220
v_mul_f32_e32 v221, v24, v221
v_mul_f32_e32 v222, v24, v222
v_mul_f32_e32 v223, v24, v223
v_mul_f32_e32 v232, v24, v232
v_mul_f32_e32 v233, v24, v233
v_mul_f32_e32 v234, v24, v234
v_mul_f32_e32 v235, v24, v235
v_mul_f32_e32 v236, v24, v236
v_mul_f32_e32 v237, v24, v237
v_mul_f32_e32 v238, v24, v238
v_mul_f32_e32 v239, v24, v239
v_mul_f32_e32 v248, v24, v248
v_mul_f32_e32 v249, v24, v249
v_mul_f32_e32 v250, v24, v250
v_mul_f32_e32 v251, v24, v251
v_mul_f32_e32 v252, v24, v252
v_mul_f32_e32 v253, v24, v253
v_mul_f32_e32 v254, v24, v254
v_mul_f32_e32 v255, v24, v255
v_sub_f32_e32 v20, v20, v29
v_sub_f32_e32 v21, v21, v29
v_sub_f32_e32 v22, v22, v29
v_sub_f32_e32 v23, v23, v29
v_sub_f32_e32 v100, v100, v29
v_sub_f32_e32 v101, v101, v29
v_sub_f32_e32 v102, v102, v29
v_sub_f32_e32 v103, v103, v29
v_sub_f32_e32 v108, v108, v29
v_sub_f32_e32 v109, v109, v29
v_sub_f32_e32 v110, v110, v29
v_sub_f32_e32 v111, v111, v29
v_sub_f32_e32 v116, v116, v29
v_sub_f32_e32 v117, v117, v29
v_sub_f32_e32 v118, v118, v29
v_sub_f32_e32 v119, v119, v29
v_sub_f32_e32 v124, v124, v29
v_sub_f32_e32 v125, v125, v29
v_sub_f32_e32 v126, v126, v29
v_sub_f32_e32 v127, v127, v29
v_accvgpr_read_b32 v25, a132
v_lshlrev_b32 v24, 16, v25
v_and_b32 v25, 0xffff0000, v25
v_mul_f32_e32 v24, v27, v24
v_mul_f32_e32 v25, v27, v25
Cv 24,24,25
v_accvgpr_write_b32 a132, v24
v_accvgpr_read_b32 v25, a133
v_lshlrev_b32 v24, 16, v25
v_and_b32 v25, 0xffff0000, v25
v_mul_f32_e32 v24, v27, v24
v_mul_f32_e32 v25, v27, v25
Cv 24,24,25
v_accvgpr_write_b32 a133, v24
)" R"(
v_accvgpr_read_b32 v25, a134
v_lshlrev_b32 v24, 16, v25
v_and_b32 v25, 0xffff0000, v25
v_mul_f32_e32 v24, v27, v24
v_mul_f32_e32 v25, v27, v25
Cv 24,24,25
v_accvgpr_write_b32 a134, v24
v_accvgpr_read_b32 v25, a135
v_lshlrev_b32 v24, 16, v25
v_and_b32 v25, 0xffff0000, v25
v_mul_f32_e32 v24, v27, v24
v_mul_f32_e32 v25, v27, v25
Cv 24,24,25
v_accvgpr_write_b32 a135, v24
v_accvgpr_read_b32 v25, a140
v_lshlrev_b32 v24, 16, v25
v_and_b32 v25, 0xffff0000, v25
v_mul_f32_e32 v24, v27, v24
v_mul_f32_e32 v25, v27, v25
Cv 24,24,25
v_accvgpr_write_b32 a140, v24
v_accvgpr_read_b32 v25, a141
v_lshlrev_b32 v24, 16, v25
v_and_b32 v25, 0xffff0000, v25
v_mul_f32_e32 v24, v27, v24
v_mul_f32_e32 v25, v27, v25
Cv 24,24,25
v_accvgpr_write_b32 a141, v24
v_accvgpr_read_b32 v25, a142
v_lshlrev_b32 v24, 16, v25
v_and_b32 v25, 0xffff0000, v25
v_mul_f32_e32 v24, v27, v24
v_mul_f32_e32 v25, v27, v25
Cv 24,24,25
v_accvgpr_write_b32 a142, v24
v_accvgpr_read_b32 v25, a143
v_lshlrev_b32 v24, 16, v25
v_and_b32 v25, 0xffff0000, v25
v_mul_f32_e32 v24, v27, v24
v_mul_f32_e32 v25, v27, v25
Cv 24,24,25
v_accvgpr_write_b32 a143, v24
v_accvgpr_read_b32 v25, a148
v_lshlrev_b32 v24, 16, v25
v_and_b32 v25, 0xffff0000, v25
v_mul_f32_e32 v24, v27, v24
v_mul_f32_e32 v25, v27, v25
Cv 24,24,25
v_accvgpr_write_b32 a148, v24
v_accvgpr_read_b32 v25, a149
v_lshlrev_b32 v24, 16, v25
v_and_b32 v25, 0xffff0000, v25
v_mul_f32_e32 v24, v27, v24
v_mul_f32_e32 v25, v27, v25
Cv 24,24,25
v_accvgpr_write_b32 a149, v24
v_accvgpr_read_b32 v25, a150
v_lshlrev_b32 v24, 16, v25
v_and_b32 v25, 0xffff0000, v25
v_mul_f32_e32 v24, v27, v24
v_mul_f32_e32 v25, v27, v25
Cv 24,24,25
v_accvgpr_write_b32 a150, v24
v_accvgpr_read_b32 v25, a151
)" R"(
v_lshlrev_b32 v24, 16, v25
v_and_b32 v25, 0xffff0000, v25
v_mul_f32_e32 v24, v27, v24
v_mul_f32_e32 v25, v27, v25
Cv 24,24,25
v_accvgpr_write_b32 a151, v24
v_accvgpr_read_b32 v25, a156
v_lshlrev_b32 v24, 16, v25
v_and_b32 v25, 0xffff0000, v25
v_mul_f32_e32 v24, v27, v24
v_mul_f32_e32 v25, v27, v25
Cv 24,24,25
v_accvgpr_write_b32 a156, v24
v_accvgpr_read_b32 v25, a157
v_lshlrev_b32 v24, 16, v25
v_and_b32 v25, 0xffff0000, v25
v_mul_f32_e32 v24, v27, v24
v_mul_f32_e32 v25, v27, v25
Cv 24,24,25
v_accvgpr_write_b32 a157, v24
v_accvgpr_read_b32 v25, a158
v_lshlrev_b32 v24, 16, v25
v_and_b32 v25, 0xffff0000, v25
v_mul_f32_e32 v24, v27, v24
v_mul_f32_e32 v25, v27, v25
Cv 24,24,25
v_accvgpr_write_b32 a158, v24
v_accvgpr_read_b32 v25, a159
v_lshlrev_b32 v24, 16, v25
v_and_b32 v25, 0xffff0000, v25
v_mul_f32_e32 v24, v27, v24
v_mul_f32_e32 v25, v27, v25
Cv 24,24,25
v_accvgpr_write_b32 a159, v24
v_accvgpr_read_b32 v25, a164
v_lshlrev_b32 v24, 16, v25
v_and_b32 v25, 0xffff0000, v25
v_mul_f32_e32 v24, v27, v24
v_mul_f32_e32 v25, v27, v25
Cv 24,24,25
v_accvgpr_write_b32 a164, v24
v_accvgpr_read_b32 v25, a165
v_lshlrev_b32 v24, 16, v25
v_and_b32 v25, 0xffff0000, v25
v_mul_f32_e32 v24, v27, v24
v_mul_f32_e32 v25, v27, v25
Cv 24,24,25
v_accvgpr_write_b32 a165, v24
v_accvgpr_read_b32 v25, a166
v_lshlrev_b32 v24, 16, v25
v_and_b32 v25, 0xffff0000, v25
v_mul_f32_e32 v24, v27, v24
v_mul_f32_e32 v25, v27, v25
Cv 24,24,25
v_accvgpr_write_b32 a166, v24
v_accvgpr_read_b32 v25, a167
v_lshlrev_b32 v24, 16, v25
v_and_b32 v25, 0xffff0000, v25
v_mul_f32_e32 v24, v27, v24
v_mul_f32_e32 v25, v27, v25
Cv 24,24,25
v_accvgpr_write_b32 a167, v24
v_accvgpr_read_b32 v25, a172
v_lshlrev_b32 v24, 16, v25
)" R"(
v_and_b32 v25, 0xffff0000, v25
v_mul_f32_e32 v24, v27, v24
v_mul_f32_e32 v25, v27, v25
Cv 24,24,25
v_accvgpr_write_b32 a172, v24
v_accvgpr_read_b32 v25, a173
v_lshlrev_b32 v24, 16, v25
v_and_b32 v25, 0xffff0000, v25
v_mul_f32_e32 v24, v27, v24
v_mul_f32_e32 v25, v27, v25
Cv 24,24,25
v_accvgpr_write_b32 a173, v24
v_accvgpr_read_b32 v25, a174
v_lshlrev_b32 v24, 16, v25
v_and_b32 v25, 0xffff0000, v25
v_mul_f32_e32 v24, v27, v24
v_mul_f32_e32 v25, v27, v25
Cv 24,24,25
v_accvgpr_write_b32 a174, v24
v_accvgpr_read_b32 v25, a175
v_lshlrev_b32 v24, 16, v25
v_and_b32 v25, 0xffff0000, v25
v_mul_f32_e32 v24, v27, v24
v_mul_f32_e32 v25, v27, v25
Cv 24,24,25
v_accvgpr_write_b32 a175, v24
v_accvgpr_read_b32 v25, a180
v_lshlrev_b32 v24, 16, v25
v_and_b32 v25, 0xffff0000, v25
v_mul_f32_e32 v24, v27, v24
v_mul_f32_e32 v25, v27, v25
Cv 24,24,25
v_accvgpr_write_b32 a180, v24
v_accvgpr_read_b32 v25, a181
v_lshlrev_b32 v24, 16, v25
v_and_b32 v25, 0xffff0000, v25
v_mul_f32_e32 v24, v27, v24
v_mul_f32_e32 v25, v27, v25
Cv 24,24,25
v_accvgpr_write_b32 a181, v24
v_accvgpr_read_b32 v25, a182
v_lshlrev_b32 v24, 16, v25
v_and_b32 v25, 0xffff0000, v25
v_mul_f32_e32 v24, v27, v24
v_mul_f32_e32 v25, v27, v25
Cv 24,24,25
v_accvgpr_write_b32 a182, v24
v_accvgpr_read_b32 v25, a183
v_lshlrev_b32 v24, 16, v25
v_and_b32 v25, 0xffff0000, v25
v_mul_f32_e32 v24, v27, v24
v_mul_f32_e32 v25, v27, v25
Cv 24,24,25
v_accvgpr_write_b32 a183, v24
v_accvgpr_read_b32 v25, a188
v_lshlrev_b32 v24, 16, v25
v_and_b32 v25, 0xffff0000, v25
v_mul_f32_e32 v24, v27, v24
v_mul_f32_e32 v25, v27, v25
Cv 24,24,25
v_accvgpr_write_b32 a188, v24
v_accvgpr_read_b32 v25, a189
v_lshlrev_b32 v24, 16, v25
v_and_b32 v25, 0xffff0000, v25
)" R"(
v_mul_f32_e32 v24, v27, v24
v_mul_f32_e32 v25, v27, v25
Cv 24,24,25
v_accvgpr_write_b32 a189, v24
v_accvgpr_read_b32 v25, a190
v_lshlrev_b32 v24, 16, v25
v_and_b32 v25, 0xffff0000, v25
v_mul_f32_e32 v24, v27, v24
v_mul_f32_e32 v25, v27, v25
Cv 24,24,25
v_accvgpr_write_b32 a190, v24
v_accvgpr_read_b32 v25, a191
v_lshlrev_b32 v24, 16, v25
v_and_b32 v25, 0xffff0000, v25
v_mul_f32_e32 v24, v27, v24
v_mul_f32_e32 v25, v27, v25
Cv 24,24,25
v_accvgpr_write_b32 a191, v24
v_exp_f32_e32 v48, v96
v_exp_f32_e32 v24, v97
s_nop 0
v_add_f32_e32 v14, v48, v24
Cv 48,48,24
v_exp_f32_e32 v49, v98
v_exp_f32_e32 v25, v99
v_add_f32_e32 v14, v14, v49
v_add_f32_e32 v14, v14, v25
Cv 49,49,25
v_exp_f32_e32 v50, v104
v_exp_f32_e32 v26, v105
v_add_f32_e32 v14, v14, v50
v_add_f32_e32 v14, v14, v26
Cv 50,50,26
v_exp_f32_e32 v51, v106
v_exp_f32_e32 v27, v107
v_add_f32_e32 v14, v14, v51
v_add_f32_e32 v14, v14, v27
Cv 51,51,27
v_exp_f32_e32 v52, v100
v_exp_f32_e32 v28, v101
s_nop 0
v_add_f32_e32 v15, v52, v28
Cv 52,52,28
v_exp_f32_e32 v53, v102
v_exp_f32_e32 v29, v103
v_add_f32_e32 v15, v15, v53
v_add_f32_e32 v15, v15, v29
Cv 53,53,29
v_exp_f32_e32 v54, v108
v_exp_f32_e32 v24, v109
v_add_f32_e32 v15, v15, v54
v_add_f32_e32 v15, v15, v24
Cv 54,54,24
v_exp_f32_e32 v55, v110
v_exp_f32_e32 v25, v111
v_add_f32_e32 v15, v15, v55
v_add_f32_e32 v15, v15, v25
Cv 55,55,25
s_nop 1
s_branch .LzMRR
.Lzsp24:
s_sub_u32 s53, s53, 1
s_cbranch_scc1 .Lzsq24
s_sleep 1
)" R"(
buffer_load_dword v4, v7, s[20:23], s52 offen offset:0 sc0 sc1
s_waitcnt vmcnt(0)
v_cmp_eq_u32_e32 vcc, s3, v4
s_andn2_b64 s[28:29], 1, vcc
s_cbranch_scc1 .Lzsp24
s_branch .Lzok24
.Lzsq24:
s_mov_b32 s53, 0
s_branch .Lzok24
.Lzsp25:
s_sub_u32 s53, s53, 1
s_cbranch_scc1 .Lzsq25
s_sleep 1
buffer_load_dword v4, v7, s[20:23], s52 offen offset:128 sc0 sc1
s_waitcnt vmcnt(0)
v_cmp_eq_u32_e32 vcc, s3, v4
s_andn2_b64 s[28:29], 1, vcc
s_cbranch_scc1 .Lzsp25
s_branch .Lzok25
.Lzsq25:
s_mov_b32 s53, 0
s_branch .Lzok25
.LzBB:
v_lshlrev_b32 v95, 7, v0
s_mov_b32 s28, 0x150c000
s_add_u32 s29, s28, 6144
.LzBB1:
buffer_load_dword v92, v95, s[20:23], s28 offen sc0 sc1
buffer_load_dword v93, v95, s[20:23], s29 offen sc0 sc1
s_waitcnt vmcnt(0)
v_cmp_ne_u32_e32 vcc, s3, v92
v_cmp_ne_u32_e64 s[30:31], s3, v93
s_or_b64 vcc, vcc, s[30:31]
s_cbranch_scc0 .LzBB2
s_sub_u32 s53, s53, 1
s_cbranch_scc0 .LzBB1
s_mov_b32 s53, 0
.LzBB2:
s_mul_i32 s28, s56, 7168
s_add_u32 s28, s28, 0x1558000
s_add_u32 s29, s28, 4096
buffer_load_dwordx4 v[64:67], v1, s[20:23], s28 offen offset:0 sc0 sc1
buffer_load_dwordx4 v[68:71], v1, s[20:23], s28 offen offset:1024 sc0 sc1
buffer_load_dwordx4 v[72:75], v1, s[20:23], s28 offen offset:2048 sc0 sc1
buffer_load_dwordx4 v[76:79], v1, s[20:23], s28 offen offset:3072 sc0 sc1
buffer_load_dwordx4 v[80:83], v1, s[20:23], s29 offen offset:0 sc0 sc1
buffer_load_dwordx4 v[84:87], v1, s[20:23], s29 offen offset:1024 sc0 sc1
buffer_load_dwordx4 v[88:91], v1, s[20:23], s29 offen offset:2048 sc0 sc1
s_cmp_gt_u32 s56, 27
s_cselect_b32 s28, 16, 0
s_cselect_b32 s29, 28, 0
s_lshr_b32 s28, s63, s28
s_and_b32 s28, s28, 0x7ff
s_sub_u32 s29, s56, s29
s_lshl_b32 s29, s29, 5
s_mul_i32 s83, s28, 896
s_add_u32 s29, s29, s83
v_and_b32 v95, 3, v0
v_lshlrev_b32 v95, 3, v95
buffer_load_dwordx2 v[92:93], v95, s[12:15], s29 offen
s_lshl4_add_u32 s28, s28, 0x150000c
v_mov_b32 v94, 0
buffer_load_dword v94, v94, s[20:23], s28 offen sc0 sc1
s_waitcnt vmcnt(0)
)" R"(
v_add_f32_e32 v64, v64, v68
v_add_f32_e32 v65, v65, v69
v_add_f32_e32 v66, v66, v70
v_add_f32_e32 v67, v67, v71
v_add_f32_e32 v72, v72, v76
v_add_f32_e32 v73, v73, v77
v_add_f32_e32 v74, v74, v78
v_add_f32_e32 v75, v75, v79
v_add_f32_e32 v80, v80, v84
v_add_f32_e32 v81, v81, v85
v_add_f32_e32 v82, v82, v86
v_add_f32_e32 v83, v83, v87
v_add_f32_e32 v64, v64, v72
v_add_f32_e32 v65, v65, v73
v_add_f32_e32 v66, v66, v74
v_add_f32_e32 v67, v67, v75
v_add_f32_e32 v80, v80, v88
v_add_f32_e32 v81, v81, v89
v_add_f32_e32 v82, v82, v90
v_add_f32_e32 v83, v83, v91
v_add_f32_e32 v64, v64, v80
v_add_f32_e32 v65, v65, v81
v_add_f32_e32 v66, v66, v82
v_add_f32_e32 v67, v67, v83
v_mov_b32 v68, v64
s_nop 1
v_permlane32_swap_b32_e32 v64, v68
v_add_f32_e32 v64, v64, v68
v_mov_b32 v68, v64
s_nop 1
v_permlane16_swap_b32_e32 v64, v68
v_add_f32_e32 v64, v64, v68
v_mov_b32 v68, v65
s_nop 1
v_permlane32_swap_b32_e32 v65, v68
v_add_f32_e32 v65, v65, v68
v_mov_b32 v68, v65
s_nop 1
v_permlane16_swap_b32_e32 v65, v68
v_add_f32_e32 v65, v65, v68
v_mov_b32 v68, v66
s_nop 1
v_permlane32_swap_b32_e32 v66, v68
v_add_f32_e32 v66, v66, v68
v_mov_b32 v68, v66
s_nop 1
v_permlane16_swap_b32_e32 v66, v68
v_add_f32_e32 v66, v66, v68
v_mov_b32 v68, v67
s_nop 1
v_permlane32_swap_b32_e32 v67, v68
v_add_f32_e32 v67, v67, v68
v_mov_b32 v68, v67
s_nop 1
v_permlane16_swap_b32_e32 v67, v68
v_add_f32_e32 v67, v67, v68
v_add_f32_dpp v64, v64, v64 row_ror:8 row_mask:0xf bank_mask:0xf
v_add_f32_dpp v65, v65, v65 row_ror:8 row_mask:0xf bank_mask:0xf
v_add_f32_dpp v66, v66, v66 row_ror:8 row_mask:0xf bank_mask:0xf
v_add_f32_dpp v67, v67, v67 row_ror:8 row_mask:0xf bank_mask:0xf
v_add_f32_dpp v64, v64, v64 row_ror:4 row_mask:0xf bank_mask:0xf
v_add_f32_dpp v65, v65, v65 row_ror:4 row_mask:0xf bank_mask:0xf
v_add_f32_dpp v66, v66, v66 row_ror:4 row_mask:0xf bank_mask:0xf
v_add_f32_dpp v67, v67, v67 row_ror:4 row_mask:0xf bank_mask:0xf
)" R"(
v_mul_f32_e32 v94, 0xbfb8aa3b, v94
v_exp_f32_e32 v94, v94
Cv 65,64,65
v_lshlrev_b32 v64, 16, v65
v_and_b32 v65, 0xffff0000, v65
v_add_f32_e32 v94, 1.0, v94
v_rcp_f32_e32 v94, v94
Cv 67,66,67
v_lshlrev_b32 v66, 16, v67
v_and_b32 v67, 0xffff0000, v67
Cv 94,94,94
v_lshlrev_b32 v94, 16, v94
v_mul_f32_e32 v64, v94, v64
v_mul_f32_e32 v65, v94, v65
v_mul_f32_e32 v66, v94, v66
v_mul_f32_e32 v67, v94, v67
Cv 65,64,65
v_lshlrev_b32 v64, 16, v65
v_and_b32 v65, 0xffff0000, v65
Cv 67,66,67
v_lshlrev_b32 v66, 16, v67
v_and_b32 v67, 0xffff0000, v67
v_lshlrev_b32 v68, 16, v92
v_and_b32 v69, 0xffff0000, v92
v_lshlrev_b32 v70, 16, v93
v_and_b32 v71, 0xffff0000, v93
v_add_f32_e32 v64, v64, v68
v_add_f32_e32 v65, v65, v69
v_add_f32_e32 v66, v66, v70
v_add_f32_e32 v67, v67, v71
Cv 64,64,65
Cv 66,66,67
v_mov_b32 v65, s3
v_mov_b32 v67, s3
v_lshrrev_b32 v95, 2, v0
v_min_u32 v95, 1, v95
v_lshl_or_b32 v95, v95, 30, v1
s_lshl_b32 s28, s56, 7
s_add_u32 s28, s28, 0x1509000
buffer_store_dwordx4 v[64:67], v95, s[20:23], s28 offen sc0 sc1
buffer_load_dword v4, v165, s[20:23], s40 offen sc0 sc1
s_waitcnt vmcnt(0)
s_branch .LzBBR
.LzPRB:
s_mov_b32 s24, s89
s_mul_i32 s25, s33, 2304
s_add_u32 s24, s24, s25
s_add_u32 s24, s24, 0xd00000
s_and_b32 s25, s36, 0x2000
s_xor_b32 s24, s24, s25
s_xor_b32 s25, s24, 0x2000
buffer_store_dword v0, v7, s[20:23], s24 offen sc0 sc1
buffer_store_dword v0, v7, s[20:23], s25 offen sc0 sc1
s_mov_b32 s26, -1
s_mov_b32 s27, -1
s_waitcnt vmcnt(0)
s_memtime s[28:29]
s_waitcnt lgkmcnt(0)
buffer_store_dword v0, v7, s[20:23], s24 offen sc0 sc1
s_waitcnt vmcnt(0)
s_memtime s[30:31]
s_waitcnt lgkmcnt(0)
s_sub_u32 s30, s30, s28
s_min_u32 s26, s26, s30
)" R"(
s_memtime s[28:29]
s_waitcnt lgkmcnt(0)
buffer_store_dword v0, v7, s[20:23], s25 offen sc0 sc1
s_waitcnt vmcnt(0)
s_memtime s[30:31]
s_waitcnt lgkmcnt(0)
s_sub_u32 s30, s30, s28
s_min_u32 s27, s27, s30
s_memtime s[28:29]
s_waitcnt lgkmcnt(0)
buffer_store_dword v0, v7, s[20:23], s24 offen sc0 sc1
s_waitcnt vmcnt(0)
s_memtime s[30:31]
s_waitcnt lgkmcnt(0)
s_sub_u32 s30, s30, s28
s_min_u32 s26, s26, s30
s_memtime s[28:29]
s_waitcnt lgkmcnt(0)
buffer_store_dword v0, v7, s[20:23], s25 offen sc0 sc1
s_waitcnt vmcnt(0)
s_memtime s[30:31]
s_waitcnt lgkmcnt(0)
s_sub_u32 s30, s30, s28
s_min_u32 s27, s27, s30
s_bitcmp1_b32 s33, 0
s_cselect_b32 s28, s27, s26
s_cselect_b32 s29, s26, s27
v_mov_b32 v32, s28
v_mov_b32 v33, s29
s_lshl_b32 s24, s33, 3
v_mov_b32 v34, s24
v_mov_b32 v35, 0
ds_write_b64 v34, v[32:33]
s_waitcnt lgkmcnt(0)
s_barrier
ds_read_b128 v[36:39], v35
ds_read_b128 v[40:43], v35 offset:16
s_mul_i32 s24, s3, 1664525
s_add_u32 s24, s24, 1013904223
s_and_b32 s24, s24, -2
s_lshl_b32 s25, s34, 6
s_add_u32 s25, s25, 0x1508800
s_cmp_lg_u32 s33, 0
s_cmov_b32 s25, 0x40000000
s_waitcnt lgkmcnt(0)
v_min3_u32 v32, v36, v38, v40
v_min3_u32 v33, v37, v39, v41
v_min_u32 v32, v32, v42
v_min_u32 v33, v33, v43
v_cmp_gt_u32_e32 vcc, v32, v33
v_mov_b32 v34, s24
s_nop 0
v_addc_co_u32_e32 v34, vcc, 0, v34, vcc
buffer_store_dword v34, v7, s[20:23], s25 offen sc0 sc1
s_barrier
s_branch .LzPRR
.LzHOP:
s_lshr_b32 s82, s2, 7
s_lshl1_add_u32 s82, s82, s91
s_and_b32 s83, s34, 1
s_lshl1_add_u32 s82, s82, s83
s_lshl2_add_u32 s82, s82, s33
s_lshl2_add_u32 s82, s82, 16
s_getpc_b64 vcc
)" R"(
s_add_u32 vcc_lo, vcc_lo, s82
s_addc_u32 vcc_hi, vcc_hi, 0
s_mov_b64 exec, 0
s_setpc_b64 vcc
s_branch .LzH23
s_branch .LzH28
s_branch .LzH27
s_branch .LzH26
s_branch .LzH25
s_branch .LzH34
s_branch .LzH33
s_branch .LzH24
s_branch .LzH23
s_branch .LzH32
s_branch .LzH31
s_branch .LzH30
s_branch .LzH29
s_branch .LzH34
s_branch .LzH33
s_branch .LzH24
s_branch .LzH4
s_branch .LzH3
s_branch .LzH2
s_branch .LzH11
s_branch .LzH10
s_branch .LzH5
s_branch .LzH1
s_branch .LzH0
s_branch .LzH9
s_branch .LzH8
s_branch .LzH7
s_branch .LzH6
s_branch .LzH10
s_branch .LzH5
s_branch .LzH1
s_branch .LzH0
.LzHR:
s_mov_b64 exec, -1
s_setpc_b64 s[30:31]
.LzA0:
s_mov_b32 s63, 0x6040200
s_mov_b32 s28, 0x7050312
s_bitcmp1_b32 s2, 5
s_cmov_b32 s63, 0x18161401
s_cmov_b32 s28, 0x19171513
s_bitcmp1_b32 s2, 6
s_cmov_b32 s63, s28
s_lshl_b32 s28, s33, 3
s_lshr_b32 s28, s63, s28
s_and_b32 s28, s28, 255
s_lshl_b32 s38, s28, 17
s_add_u32 s38, s38, 0xff80
s_lshl_b32 s39, s33, 16
s_lshl_b32 s28, s34, 9
s_add_u32 s39, s39, s28
s_sub_u32 s39, s39, 0x40000
s_lshl_b32 s37, s33, 12
s_add_u32 s37, s37, 0x10000
v_lshrrev_b32 v28, 4, v0
v_and_b32 v29, 7, v0
v_xor_b32 v28, v28, v29
v_lshlrev_b32 v28, 4, v28
v_lshrrev_b32 v29, 3, v0
v_lshl_or_b32 v8, v29, 11, v28
)" R"(
v_xor_b32 v9, 64, v8
v_add_u32 v9, 15360, v9
v_add_u32 v10, 30720, v8
v_add_u32 v11, 30720, v9
s_and_b32 s28, s33, 1
s_lshl_b32 s28, s28, 7
v_lshrrev_b32 v29, 5, v0
v_mul_u32_u24 v29, 0x2220, v29
v_xor_b32 v29, v29, v1
v_xor_b32 v12, s28, v29
v_xor_b32 v13, 64, v12
v_add_u32 v13, 15360, v13
v_add_u32 v14, 30720, v12
v_add_u32 v15, 30720, v13
v_min_u32 v7, 1, v0
v_lshlrev_b32 v7, 30, v7
s_waitcnt lgkmcnt(0)
s_mov_b32 s28, 0x1508400
s_load_dword s3, s[74:75], s28
s_mov_b32 s20, s74
s_mov_b32 s21, s75
s_mov_b32 s22, 0xf400000
s_mov_b32 s23, 0x20000
s_mov_b32 s4, s66
s_mov_b32 s5, s67
s_mov_b32 s6, 0x400000
s_mov_b32 s7, 0x20000
s_cmp_eq_u32 s91, 1
s_cselect_b32 s8, s72, s70
s_cselect_b32 s9, s73, s71
s_mov_b32 s10, 0x800000
s_mov_b32 s11, 0x20000
s_lshr_b32 s28, s2, 3
s_cmp_eq_u32 s28, 14
s_cbranch_scc1 .LzPRB
.LzPRR:
s_add_u32 m0, s37, 0
s_add_u32 s39, s39, 0x40000
buffer_load_dwordx4 v12, s[8:11], s39 offen offset:0 lds
buffer_load_dwordx4 v13, s[8:11], s39 offen offset:1024 lds
buffer_load_dwordx4 v14, s[8:11], s39 offen offset:2048 lds
buffer_load_dwordx4 v15, s[8:11], s39 offen offset:3072 lds
s_add_u32 m0, s36, 0
s_add_u32 s38, s38, 0xffff0080
buffer_load_dwordx4 v8, s[4:7], s38 offen offset:0 lds
buffer_load_dwordx4 v9, s[4:7], s38 offen offset:1024 lds
buffer_load_dwordx4 v10, s[4:7], s38 offen offset:2048 lds
buffer_load_dwordx4 v11, s[4:7], s38 offen offset:3072 lds
s_add_u32 m0, s36, 4096
s_add_u32 s38, s38, 0x10000
buffer_load_dwordx4 v8, s[4:7], s38 offen offset:0 lds
buffer_load_dwordx4 v9, s[4:7], s38 offen offset:1024 lds
buffer_load_dwordx4 v10, s[4:7], s38 offen offset:2048 lds
buffer_load_dwordx4 v11, s[4:7], s38 offen offset:3072 lds
s_add_u32 m0, s37, 16384
s_add_u32 s39, s39, 0x40000
buffer_load_dwordx4 v12, s[8:11], s39 offen offset:0 lds
buffer_load_dwordx4 v13, s[8:11], s39 offen offset:1024 lds
buffer_load_dwordx4 v14, s[8:11], s39 offen offset:2048 lds
buffer_load_dwordx4 v15, s[8:11], s39 offen offset:3072 lds
s_mov_b32 s12, s64
s_mov_b32 s13, s65
s_mov_b32 s14, 0x1c0000
s_mov_b32 s15, 0x20000
)" R"(
s_and_b32 s94, s2, 127
s_lshl2_add_u32 s94, s94, s33
s_lshl_b32 s94, s94, 2
s_mul_i32 s29, s94, 896
v_mov_b32 v120, 0x40000000
v_cmp_gt_u32_e32 vcc, 56, v0
s_nop 1
v_cndmask_b32_e32 v120, v120, v1, vcc
buffer_load_dwordx4 v[96:99], v120, s[12:15], s29 offen offset:0
buffer_load_dwordx4 v[100:103], v120, s[12:15], s29 offen offset:896
buffer_load_dwordx4 v[104:107], v120, s[12:15], s29 offen offset:1792
buffer_load_dwordx4 v[108:111], v120, s[12:15], s29 offen offset:2688
s_mov_b32 s24, s76
s_mov_b32 s25, s77
s_mov_b32 s26, 0x380
s_mov_b32 s27, 0x20000
buffer_load_dwordx4 v[112:115], v120, s[24:27], 0 offen
s_mov_b32 s24, s78
s_mov_b32 s25, s79
s_mov_b32 s26, 0x380
s_mov_b32 s27, 0x20000
buffer_load_dwordx4 v[116:119], v120, s[24:27], 0 offen
s_add_u32 m0, s36, 32768
s_add_u32 s38, s38, 0xffff0080
buffer_load_dwordx4 v8, s[4:7], s38 offen offset:0 lds
buffer_load_dwordx4 v9, s[4:7], s38 offen offset:1024 lds
buffer_load_dwordx4 v10, s[4:7], s38 offen offset:2048 lds
buffer_load_dwordx4 v11, s[4:7], s38 offen offset:3072 lds
s_add_u32 m0, s36, 36864
s_add_u32 s38, s38, 0x10000
buffer_load_dwordx4 v8, s[4:7], s38 offen offset:0 lds
buffer_load_dwordx4 v9, s[4:7], s38 offen offset:1024 lds
buffer_load_dwordx4 v10, s[4:7], s38 offen offset:2048 lds
buffer_load_dwordx4 v11, s[4:7], s38 offen offset:3072 lds
s_mov_b32 s16, s68
s_mov_b32 s17, s69
s_mov_b32 s18, 0x380000
s_mov_b32 s19, 0x20000
s_load_dwordx2 s[70:71], s[0:1], 88
s_load_dwordx2 s[74:75], s[0:1], 96
v_bfe_u32 v28, v0, 1, 3
v_lshrrev_b32 v29, 4, v0
v_xor_b32 v28, v28, v29
v_lshlrev_b32 v28, 4, v28
v_and_b32 v29, 15, v0
v_lshl_or_b32 v28, v29, 7, v28
s_lshr_b32 s28, s33, 1
s_lshl_b32 s28, s28, 12
v_or_b32 v16, s28, v28
v_xor_b32 v17, 64, v16
v_bfe_u32 v28, v0, 2, 3
v_lshlrev_b32 v28, 5, v28
v_and_b32 v29, 3, v0
v_lshl_or_b32 v28, v29, 3, v28
v_bfe_u32 v29, v0, 2, 2
v_lshl_or_b32 v28, v29, 9, v28
v_lshrrev_b32 v29, 4, v0
v_lshl_or_b32 v28, v29, 12, v28
s_and_b32 s28, s33, 1
s_lshl_b32 s28, s28, 8
s_bitset1_b32 s28, 16
v_or_b32 v18, s28, v28
v_xor_b32 v19, 32, v18
v_xor_b32 v20, 64, v18
)" R"(
v_xor_b32 v21, 96, v18
v_xor_b32 v22, 128, v18
v_xor_b32 v23, 160, v18
v_xor_b32 v24, 192, v18
v_xor_b32 v25, 224, v18
s_load_dwordx4 s[76:79], s[0:1], 72
s_waitcnt vmcnt(14) lgkmcnt(0)
s_barrier
Rt 0,18,0
Rt 2,18,2048
Rt 4,18,16384
Rt 6,18,18432
Rb 64,16,0
Rb 68,17,0
Rb 72,16,2048
Rb 76,17,2048
Rb 80,16,8192
Rb 84,17,8192
Rb 88,16,10240
Rb 92,17,10240
Rt 8,19,0
Rt 10,19,2048
Rt 12,19,16384
Rt 14,19,18432
Rt 16,20,0
Rt 18,20,2048
Rt 20,20,16384
Rt 22,20,18432
Rt 24,21,0
Rt 26,21,2048
Rt 28,21,16384
Rt 30,21,18432
Rt 32,22,0
Rt 34,22,2048
Rt 36,22,16384
Rt 38,22,18432
Rt 40,23,0
Rt 42,23,2048
Rt 44,23,16384
Rt 46,23,18432
Rt 48,24,0
Rt 50,24,2048
Rt 52,24,16384
Rt 54,24,18432
s_add_u32 m0, s37, 32768
s_add_u32 s39, s39, 0x40000
buffer_load_dwordx4 v12, s[8:11], s39 offen offset:0 lds
buffer_load_dwordx4 v13, s[8:11], s39 offen offset:1024 lds
buffer_load_dwordx4 v14, s[8:11], s39 offen offset:2048 lds
buffer_load_dwordx4 v15, s[8:11], s39 offen offset:3072 lds
s_add_u32 m0, s37, 49152
s_add_u32 s39, s39, 0x40000
buffer_load_dwordx4 v12, s[8:11], s39 offen offset:0 lds
buffer_load_dwordx4 v13, s[8:11], s39 offen offset:1024 lds
buffer_load_dwordx4 v14, s[8:11], s39 offen offset:2048 lds
buffer_load_dwordx4 v15, s[8:11], s39 offen offset:3072 lds
s_lshl2_add_u32 s40, s2, s33
s_mul_i32 s40, s40, 7168
v_min_u32 v2, 55, v0
v_lshlrev_b32 v2, 7, v2
buffer_load_dword v6, v2, s[16:19], s40 offen
s_mul_i32 s3, s3, 1664525
s_add_u32 s3, s3, 1013904223
s_waitcnt vmcnt(17)
v_dot2_f32_bf16 v120, v96, v112, 0
)" R"(
v_dot2_f32_bf16 v121, v100, v112, 0
v_dot2_f32_bf16 v122, v104, v112, 0
v_dot2_f32_bf16 v123, v108, v112, 0
v_dot2_f32_bf16 v124, v96, v116, 0
v_dot2_f32_bf16 v125, v100, v116, 0
v_dot2_f32_bf16 v126, v104, v116, 0
v_dot2_f32_bf16 v127, v108, v116, 0
v_dot2_f32_bf16 v120, v97, v113, v120
v_dot2_f32_bf16 v121, v101, v113, v121
v_dot2_f32_bf16 v122, v105, v113, v122
v_dot2_f32_bf16 v123, v109, v113, v123
v_dot2_f32_bf16 v124, v97, v117, v124
v_dot2_f32_bf16 v125, v101, v117, v125
v_dot2_f32_bf16 v126, v105, v117, v126
v_dot2_f32_bf16 v127, v109, v117, v127
v_dot2_f32_bf16 v120, v98, v114, v120
v_dot2_f32_bf16 v121, v102, v114, v121
v_dot2_f32_bf16 v122, v106, v114, v122
v_dot2_f32_bf16 v123, v110, v114, v123
v_dot2_f32_bf16 v124, v98, v118, v124
v_dot2_f32_bf16 v125, v102, v118, v125
v_dot2_f32_bf16 v126, v106, v118, v126
v_dot2_f32_bf16 v127, v110, v118, v127
v_dot2_f32_bf16 v120, v99, v115, v120
v_dot2_f32_bf16 v121, v103, v115, v121
v_dot2_f32_bf16 v122, v107, v115, v122
v_dot2_f32_bf16 v123, v111, v115, v123
v_dot2_f32_bf16 v124, v99, v119, v124
v_dot2_f32_bf16 v125, v103, v119, v125
v_dot2_f32_bf16 v126, v107, v119, v126
v_dot2_f32_bf16 v127, v111, v119, v127
v_permlane32_swap_b32_e32 v120, v121
v_add_f32_e32 v120, v120, v121
v_permlane32_swap_b32_e32 v122, v123
v_add_f32_e32 v122, v122, v123
v_permlane32_swap_b32_e32 v124, v125
v_add_f32_e32 v124, v124, v125
v_permlane32_swap_b32_e32 v126, v127
v_add_f32_e32 v126, v126, v127
v_permlane16_swap_b32_e32 v120, v122
v_add_f32_e32 v120, v120, v122
v_permlane16_swap_b32_e32 v124, v126
v_add_f32_e32 v124, v124, v126
v_add_f32_dpp v120, v120, v120 row_ror:8 row_mask:0xf bank_mask:0xf
s_nop 0
v_add_f32_dpp v124, v124, v124 row_ror:8 row_mask:0xf bank_mask:0xf
v_add_f32_dpp v120, v120, v120 row_ror:4 row_mask:0xf bank_mask:0xf
s_nop 0
v_add_f32_dpp v124, v124, v124 row_ror:4 row_mask:0xf bank_mask:0xf
v_add_f32_dpp v120, v120, v120 row_ror:2 row_mask:0xf bank_mask:0xf
s_nop 0
v_add_f32_dpp v124, v124, v124 row_ror:2 row_mask:0xf bank_mask:0xf
v_add_f32_dpp v120, v120, v120 row_ror:1 row_mask:0xf bank_mask:0xf
s_nop 0
v_add_f32_dpp v124, v124, v124 row_ror:1 row_mask:0xf bank_mask:0xf
Cv 120,120,120
v_lshlrev_b32 v120, 16, v120
Cv 124,124,124
v_lshlrev_b32 v124, 16, v124
v_mul_f32_e32 v120, 0xbfb8aa3b, v120
v_exp_f32_e32 v120, v120
s_nop 0
v_add_f32_e32 v120, 1.0, v120
v_rcp_f32_e32 v120, v120
)" R"(
s_nop 0
Cv 120,120,120
v_lshlrev_b32 v120, 16, v120
v_lshrrev_b32 v121, 5, v0
v_bfe_u32 v122, v0, 4, 1
v_lshl_or_b32 v121, v122, 1, v121
v_add_u32 v121, s94, v121
v_add_f32_e32 v125, 0, v124
v_ashrrev_i32 v126, 31, v125
v_or_b32 v126, 0x80000000, v126
v_xor_b32 v125, v125, v126
v_and_b32 v125, 0xffff0000, v125
v_sub_u32 v126, 0xffff, v121
v_or_b32 v116, v125, v126
v_mov_b32 v117, s3
v_mov_b32 v118, v120
v_mov_b32 v119, v124
v_and_b32 v122, 15, v0
v_min_u32 v122, 1, v122
v_lshlrev_b32 v122, 30, v122
v_lshl_or_b32 v122, v121, 4, v122
s_mov_b32 s94, 0x1500000
buffer_store_dwordx4 v[116:119], v122, s[20:23], s94 offen sc0 sc1
s_cmp_eq_u32 s91, 1
s_barrier
s_cbranch_scc1 .LzAV
s_branch .LzAK
.LzAK:
Mz 32,0,64
Rt 56,25,0
Mv 32,4,68
Rt 58,25,2048
s_lshl1_add_u32 s40, s34, s91
Mz 64,4,76
Rt 60,25,16384
s_lshl_b32 s40, s40, 12
Mv 64,0,72
Rt 62,25,18432
s_and_b32 s29, s38, 0xfffe0000
Mz 96,0,80
s_add_u32 s40, s40, s29
v_min_u32 v3, 31, v0
Mv 96,4,84
v_lshlrev_b32 v3, 7, v3
Mz 128,4,92
Mv 128,0,88
Mz 132,8,88
Mv 132,12,92
Mz 100,12,84
Mv 100,8,80
Mz 68,8,72
Mv 68,12,76
Mz 36,12,68
s_add_u32 s39, s39, 0x40000
Mv 36,8,64
s_waitcnt lgkmcnt(0)
s_barrier
Mz 40,16,64
Rb 96,16,16384
s_add_u32 m0, s37, 0
Mv 40,20,68
buffer_load_dwordx4 v12, s[8:11], s39 offen offset:0 lds
Mz 72,20,76
Mv 72,16,72
Mz 104,16,80
)" R"(
Rb 100,17,16384
Mv 104,20,84
Mz 136,20,92
Mv 136,16,88
buffer_load_dwordx4 v13, s[8:11], s39 offen offset:1024 lds
Mz 140,24,88
Rb 104,16,18432
Mv 140,28,92
Mz 108,28,84
Mv 108,24,80
Mz 76,24,72
Rb 108,17,18432
Mv 76,28,76
buffer_load_dwordx4 v14, s[8:11], s39 offen offset:2048 lds
Mz 44,28,68
Mv 44,24,64
Mz 48,32,64
Rb 112,16,24576
Mv 48,36,68
buffer_load_dword v6, v3, s[4:7], s40 offen
Mz 80,36,76
Mv 80,32,72
buffer_load_dwordx4 v15, s[8:11], s39 offen offset:3072 lds
Mz 112,32,80
Rb 116,17,24576
s_mov_b32 s28, s35
Mv 112,36,84
s_lshl2_add_u32 s28, s28, s33
s_mul_i32 s43, s28, 28672
Mz 144,36,92
v_and_b32 v26, 0x70, v1
v_and_b32 v4, 48, v0
Mv 144,32,88
s_add_u32 s39, s39, 0x40000
v_xor_b32 v26, v26, v4
Mz 148,40,88
Rb 120,16,26624
s_add_u32 m0, s37, 16384
Mv 148,44,92
buffer_load_dwordx4 v12, s[8:11], s39 offen offset:0 lds
Mz 116,44,84
v_lshrrev_b32 v4, 3, v0
v_mul_u32_u24 v4, 896, v4
Mv 116,40,80
v_add_u32 v26, v26, v4
v_xor_b32 v27, 64, v26
Mz 84,40,72
Rb 124,17,26624
v_add_u32 v27, 6144, v27
Mv 84,44,76
v_bfe_u32 v28, v0, 1, 3
v_lshrrev_b32 v4, 4, v0
Mz 52,44,68
v_xor_b32 v28, v28, v4
v_lshlrev_b32 v28, 4, v28
Mv 52,40,64
buffer_load_dwordx4 v13, s[8:11], s39 offen offset:1024 lds
Mz 56,48,64
v_and_b32 v4, 15, v0
v_lshl_or_b32 v28, v4, 7, v28
Mv 56,52,68
s_add_u32 s28, s36, 0x18000
v_add_u32 v28, s28, v28
Mz 88,52,76
)" R"(
v_xor_b32 v29, 64, v28
Mv 88,48,72
Mz 120,48,80
Mv 120,52,84
buffer_load_dwordx4 v14, s[8:11], s39 offen offset:2048 lds
Mz 152,52,92
Mv 152,48,88
Mz 156,56,88
Mv 156,60,92
Mz 124,60,84
Mv 124,56,80
buffer_load_dwordx4 v15, s[8:11], s39 offen offset:3072 lds
Mz 92,56,72
Mv 92,60,76
Mz 60,60,68
Mv 60,56,64
s_call_b64 s[30:31], .LzHOP
s_waitcnt vmcnt(11) lgkmcnt(0)
s_barrier
Mz 160,0,96
Rb 64,16,32768
s_bfe_u32 s88, s2, 0x20005
Mv 160,4,100
Rb 68,17,32768
s_add_u32 s38, s38, 0xffff0080
Mz 192,4,108
Rb 72,16,34816
s_add_u32 m0, s36, 0
Mv 192,0,104
buffer_load_dwordx4 v8, s[4:7], s38 offen offset:0 lds
Mz 224,0,112
Rb 76,17,34816
s_and_b32 s28, s2, 15
Mv 224,4,116
Rb 80,16,40960
s_lshl4_add_u32 s88, s88, s28
v_mfma_f32_16x16x32_bf16 a[224:227], a[4:7], a[124:127], 0
Rb 84,17,40960
s_lshl2_add_u32 s88, s88, s33
Ma 224,0,120
Rb 88,16,43008
buffer_load_dwordx4 v9, s[4:7], s38 offen offset:1024 lds
v_mfma_f32_16x16x32_bf16 a[228:231], a[8:11], a[120:123], 0
Rb 92,17,43008
Rt 0,18,32768
Ma 228,12,124
s_mul_i32 s88, s88, 4096
s_add_u32 s88, s88, 0x100000
Mz 228,12,116
Rt 2,18,34816
v_min_u32 v5, 31, v0
Mv 228,8,112
buffer_load_dwordx4 v10, s[4:7], s38 offen offset:2048 lds
Mz 196,8,104
Rt 4,18,49152
v_lshlrev_b32 v5, 7, v5
Mv 196,12,108
s_mov_b32 s90, 0x100000
s_mov_b64 s[24:25], s[8:9]
Mz 164,12,100
Rt 6,18,51200
s_mov_b64 s[26:27], s[10:11]
Mv 164,8,96
buffer_load_dwordx4 v11, s[4:7], s38 offen offset:3072 lds
Mz 168,16,96
)" R"(
Rt 8,19,32768
Mv 168,20,100
s_add_u32 s38, s38, 0x10000
Mz 200,20,108
Rt 10,19,34816
s_add_u32 m0, s36, 4096
Mv 200,16,104
buffer_load_dwordx4 v8, s[4:7], s38 offen offset:0 lds
Mz 232,16,112
Rt 12,19,49152
Mv 232,20,116
v_mfma_f32_16x16x32_bf16 a[232:235], a[20:23], a[124:127], 0
Rt 14,19,51200
Ma 232,16,120
buffer_load_dwordx4 v9, s[4:7], s38 offen offset:1024 lds
v_mfma_f32_16x16x32_bf16 a[236:239], a[24:27], a[120:123], 0
Rt 16,20,32768
Ma 236,28,124
Mz 236,28,116
Rt 18,20,34816
Mv 236,24,112
buffer_load_dwordx4 v10, s[4:7], s38 offen offset:2048 lds
Mz 204,24,104
Rt 20,20,49152
Mv 204,28,108
Mz 172,28,100
Rt 22,20,51200
Mv 172,24,96
buffer_load_dwordx4 v11, s[4:7], s38 offen offset:3072 lds
Mz 176,32,96
Rt 24,21,32768
Mv 176,36,100
Mz 208,36,108
Rt 26,21,34816
Mv 208,32,104
Mz 240,32,112
Rt 28,21,49152
Mv 240,36,116
v_mfma_f32_16x16x32_bf16 a[240:243], a[36:39], a[124:127], 0
Rt 30,21,51200
Ma 240,32,120
v_mfma_f32_16x16x32_bf16 a[244:247], a[40:43], a[120:123], 0
Rt 32,22,32768
Ma 244,44,124
Mz 244,44,116
Rt 34,22,34816
Mv 244,40,112
Mz 212,40,104
Rt 36,22,49152
Mv 212,44,108
Mz 180,44,100
Rt 38,22,51200
Mv 180,40,96
Mz 184,48,96
Rt 40,23,32768
Mv 184,52,100
Mz 216,52,108
Rt 42,23,34816
Mv 216,48,104
Mz 248,48,112
Rt 44,23,49152
Mv 248,52,116
v_mfma_f32_16x16x32_bf16 a[248:251], a[52:55], a[124:127], 0
Rt 46,23,51200
)" R"(
Ma 248,48,120
v_mfma_f32_16x16x32_bf16 a[252:255], a[56:59], a[120:123], 0
Rt 48,24,32768
Ma 252,60,124
s_add_u32 s88, s88, s90
Mz 252,60,116
Rt 50,24,34816
Mv 252,56,112
buffer_load_dword v6, v5, s[24:27], s88 offen
Mz 220,56,104
Rt 52,24,49152
Mv 220,60,108
.LzH25:
s_cbranch_execz .LzH35
Mz 188,60,100
Rt 54,24,51200
Mv 188,56,96
Mv 32,0,64
Rt 56,25,32768
s_lshl_b32 s29, s91, 22
Mv 32,4,68
Rt 58,25,34816
s_add_u32 s28, s89, s29
Mv 64,4,76
Rt 60,25,49152
s_bfe_u32 s29, s63, 0x80000
Mv 64,0,72
Rt 62,25,51200
s_lshl_b32 s29, s29, 16
Mv 96,0,80
s_add_u32 s45, s28, s29
s_bfe_u32 s29, s63, 0x80008
Mv 96,4,84
s_lshl_b32 s29, s29, 16
s_add_u32 s46, s28, s29
Mv 128,4,92
s_bfe_u32 s29, s63, 0x80010
s_lshl_b32 s29, s29, 16
Mv 128,0,88
s_add_u32 s47, s28, s29
Mv 132,8,88
s_bfe_u32 s29, s63, 0x80018
s_lshl_b32 s29, s29, 16
Mv 132,12,92
s_add_u32 s48, s28, s29
s_add_u32 s50, s89, 0xd40000
Mv 100,12,84
s_lshl2_add_u32 s28, s91, s93
s_lshl_b32 s28, s28, 7
Mv 100,8,80
s_add_u32 s49, s28, s50
s_lshl_b32 s52, s35, 7
Mv 68,8,72
s_add_u32 s52, s52, s50
s_add_u32 s52, s52, 0xd000
Mv 68,12,76
s_lshl_b32 s51, s35, 17
s_add_u32 s51, s51, s89
Mv 36,12,68
s_add_u32 s39, s39, 0x40000
s_add_u32 s51, s51, 0x800000
Mv 36,8,64
s_waitcnt lgkmcnt(0)
s_barrier
Mv 40,16,64
)" R"(
Rb 96,16,49152
s_add_u32 m0, s37, 32768
Mv 40,20,68
buffer_load_dwordx4 v12, s[8:11], s39 offen offset:0 lds
Mv 72,20,76
s_cmp_lg_u32 s33, 0
s_cmov_b32 s49, 0x40000000
Mv 72,16,72
s_mov_b32 s53, 0x40000
s_mov_b32 s54, 0x3db8aa3b
Mv 104,16,80
Rb 100,17,49152
s_mul_i32 s41, s33, 7168
Mv 104,20,84
s_mul_i32 s42, s33, 0x70000
s_lshl_b32 s28, s34, 9
Mv 136,20,92
s_add_u32 s42, s42, s28
s_mov_b32 s62, 0x4e800000
Mv 136,16,88
buffer_load_dwordx4 v13, s[8:11], s39 offen offset:1024 lds
Mv 140,24,88
Rb 104,16,51200
s_add_u32 s67, s89, 0xc00000
Mv 140,28,92
s_lshl3_add_u32 s28, s34, s35
s_add_u32 s64, s51, 0x100000
Mv 108,28,84
s_lshl2_add_u32 s28, s28, s33
s_lshl2_add_u32 s65, s35, s33
Mv 108,24,80
s_lshl_b32 s65, s65, 7
s_add_u32 s65, s65, s50
Mv 76,24,72
Rb 108,17,51200
s_add_u32 s65, s65, 0xc000
Mv 76,28,76
buffer_load_dwordx4 v14, s[8:11], s39 offen offset:2048 lds
Mv 44,28,68
s_lshl_b32 s66, s28, 10
s_add_u32 s66, s66, 0x1400000
Mv 44,24,64
s_mov_b32 s28, s35
s_lshl2_add_u32 s28, s28, s33
Mv 48,32,64
Rb 112,16,57344
s_and_b32 s29, s34, 1
Mv 48,36,68
s_lshl1_add_u32 s29, s28, s29
s_lshl_b32 s73, s29, 15
Mv 80,36,76
s_and_b32 s29, s89, 0xfe000000
s_add_u32 s73, s73, s29
Mv 80,32,72
buffer_load_dwordx4 v15, s[8:11], s39 offen offset:3072 lds
Mv 112,32,80
Rb 116,17,57344
s_add_u32 s73, s73, 0x1000000
Mv 112,36,84
s_lshl4_add_u32 s28, s28, s34
s_lshl_b32 s68, s28, 8
Mv 144,36,92
s_add_u32 s68, s68, 0x1510000
s_add_u32 s55, s89, 0x120000
)" R"(
Mv 144,32,88
s_add_u32 s39, s39, 0x40000
s_lshl_b32 s28, s35, 1
Mv 148,40,88
Rb 120,16,59392
s_add_u32 m0, s37, 49152
Mv 148,44,92
buffer_load_dwordx4 v12, s[8:11], s39 offen offset:0 lds
Mv 116,44,84
s_sub_u32 s57, 14, s28
s_mov_b32 s59, s89
Mv 116,40,80
s_mov_b32 s58, 2
Mv 84,40,72
Rb 124,17,59392
Mv 84,44,76
s_cmp_eq_u32 s35, 7
s_cmov_b32 s55, s89
Mv 52,44,68
s_cmp_eq_u32 s35, 7
s_cmov_b32 s57, 16
Mv 52,40,64
buffer_load_dwordx4 v13, s[8:11], s39 offen offset:1024 lds
Mv 56,48,64
s_cmp_eq_u32 s35, 7
s_cmov_b32 s59, s67
Mv 56,52,68
s_add_u32 s56, s55, 0x400000
Mv 88,52,76
Mv 88,48,72
Mv 120,48,80
Mv 120,52,84
buffer_load_dwordx4 v14, s[8:11], s39 offen offset:2048 lds
Mv 152,52,92
Mv 152,48,88
Mv 156,56,88
Mv 156,60,92
Mv 124,60,84
Mv 124,56,80
buffer_load_dwordx4 v15, s[8:11], s39 offen offset:3072 lds
Mv 92,56,72
Mv 92,60,76
Mv 60,60,68
Mv 60,56,64
s_waitcnt vmcnt(9) lgkmcnt(0)
s_barrier
Mv 160,0,96
Rb 64,16,0
Mv 160,4,100
Rb 68,17,0
s_add_u32 s38, s38, 0xffff0080
Mv 192,4,108
Rb 72,16,2048
s_add_u32 m0, s36, 32768
Mv 192,0,104
buffer_load_dwordx4 v8, s[4:7], s38 offen offset:0 lds
Mv 224,0,112
Rb 76,17,2048
Mv 224,4,116
Rb 80,16,8192
Ma 224,4,124
Rb 84,17,8192
Ma 224,0,120
Rb 88,16,10240
buffer_load_dwordx4 v9, s[4:7], s38 offen offset:1024 lds
)" R"(
Ma 228,8,120
Rb 92,17,10240
Rt 0,18,0
Ma 228,12,124
s_lshl2_add_u32 s28, s2, s33
s_lshl_b32 s28, s28, 11
Mv 228,12,116
Rt 2,18,2048
s_add_u32 s28, s28, 0x1500000
Mv 228,8,112
buffer_load_dwordx4 v10, s[4:7], s38 offen offset:2048 lds
Mv 196,8,104
Rt 4,18,16384
v_min_u32 v3, 15, v0
Mv 196,12,108
v_lshlrev_b32 v3, 7, v3
Mv 164,12,100
Rt 6,18,18432
Mv 164,8,96
buffer_load_dwordx4 v11, s[4:7], s38 offen offset:3072 lds
Mv 168,16,96
Rt 8,19,0
Mv 168,20,100
s_add_u32 s38, s38, 0x10000
Mv 200,20,108
Rt 10,19,2048
s_add_u32 m0, s36, 36864
Mv 200,16,104
buffer_load_dwordx4 v8, s[4:7], s38 offen offset:0 lds
Mv 232,16,112
Rt 12,19,16384
Mv 232,20,116
Ma 232,20,124
Rt 14,19,18432
Ma 232,16,120
buffer_load_dwordx4 v9, s[4:7], s38 offen offset:1024 lds
Ma 236,24,120
Rt 16,20,0
Ma 236,28,124
Mv 236,28,116
Rt 18,20,2048
Mv 236,24,112
buffer_load_dwordx4 v10, s[4:7], s38 offen offset:2048 lds
Mv 204,24,104
Rt 20,20,16384
Mv 204,28,108
Mv 172,28,100
Rt 22,20,18432
Mv 172,24,96
buffer_load_dwordx4 v11, s[4:7], s38 offen offset:3072 lds
Mv 176,32,96
Rt 24,21,0
Mv 176,36,100
s_add_u32 s44, s43, 0
Mv 208,36,108
Rt 26,21,2048
s_add_u32 m0, s36, 131072
Mv 208,32,104
buffer_load_dwordx4 v26, s[12:15], s44 offen offset:0 lds
Mv 240,32,112
Rt 28,21,16384
Mv 240,36,116
Ma 240,36,124
Rt 30,21,18432
)" R"(
buffer_load_dwordx4 v27, s[12:15], s44 offen offset:1024 lds
Ma 240,32,120
Ma 244,40,120
Rt 32,22,0
Ma 244,44,124
s_add_u32 s44, s43, 12288
buffer_load_dwordx4 v26, s[12:15], s44 offen offset:2048 lds
Mv 244,44,116
Rt 34,22,2048
Mv 244,40,112
Mv 212,40,104
Rt 36,22,16384
buffer_load_dwordx4 v27, s[12:15], s44 offen offset:3072 lds
Mv 212,44,108
s_add_u32 s44, s43, 128
Mv 180,44,100
Rt 38,22,18432
s_add_u32 m0, s36, 135168
Mv 180,40,96
buffer_load_dwordx4 v26, s[12:15], s44 offen offset:0 lds
Mv 184,48,96
Rt 40,23,0
Mv 184,52,100
Mv 216,52,108
Rt 42,23,2048
buffer_load_dwordx4 v27, s[12:15], s44 offen offset:1024 lds
Mv 216,48,104
Mv 248,48,112
Rt 44,23,16384
Mv 248,52,116
s_add_u32 s44, s43, 12416
buffer_load_dwordx4 v26, s[12:15], s44 offen offset:2048 lds
Ma 248,52,124
Rt 46,23,18432
Ma 248,48,120
Ma 252,56,120
Rt 48,24,0
buffer_load_dwordx4 v27, s[12:15], s44 offen offset:3072 lds
Ma 252,60,124
s_add_u32 s88, s88, s90
s_mov_b32 s40, 5
Mv 252,60,116
Rt 50,24,2048
Mv 252,56,112
buffer_load_dword v6, v5, s[24:27], s88 offen
Mv 220,56,104
Rt 52,24,16384
Mv 220,60,108
buffer_load_dword v6, v3, s[20:23], s28 offen sc0 sc1
Mv 188,60,100
Rt 54,24,18432
Mv 188,56,96
.LzPAK:
Mv 32,0,64
Rt 56,25,0
Mv 32,4,68
Rt 58,25,2048
Mv 64,4,76
Rt 60,25,16384
Mv 64,0,72
Rt 62,25,18432
Mv 96,0,80
Mv 96,4,84
Mv 128,4,92
)" R"(
Mv 128,0,88
Mv 132,8,88
Mv 132,12,92
Mv 100,12,84
Mv 100,8,80
Mv 68,8,72
Mv 68,12,76
Mv 36,12,68
s_add_u32 s39, s39, 0x40000
Mv 36,8,64
s_waitcnt lgkmcnt(0)
s_barrier
Mv 40,16,64
Rb 96,16,16384
s_add_u32 m0, s37, 0
Mv 40,20,68
buffer_load_dwordx4 v12, s[8:11], s39 offen offset:0 lds
Mv 72,20,76
Mv 72,16,72
Mv 104,16,80
Rb 100,17,16384
Mv 104,20,84
Mv 136,20,92
Mv 136,16,88
buffer_load_dwordx4 v13, s[8:11], s39 offen offset:1024 lds
Mv 140,24,88
Rb 104,16,18432
Mv 140,28,92
Mv 108,28,84
Mv 108,24,80
Mv 76,24,72
Rb 108,17,18432
Mv 76,28,76
buffer_load_dwordx4 v14, s[8:11], s39 offen offset:2048 lds
Mv 44,28,68
Mv 44,24,64
Mv 48,32,64
Rb 112,16,24576
Mv 48,36,68
Mv 80,36,76
Mv 80,32,72
buffer_load_dwordx4 v15, s[8:11], s39 offen offset:3072 lds
Mv 112,32,80
Rb 116,17,24576
Mv 112,36,84
Mv 144,36,92
Mv 144,32,88
s_add_u32 s39, s39, 0x40000
Mv 148,40,88
Rb 120,16,26624
s_add_u32 m0, s37, 16384
Mv 148,44,92
buffer_load_dwordx4 v12, s[8:11], s39 offen offset:0 lds
Mv 116,44,84
Mv 116,40,80
Mv 84,40,72
Rb 124,17,26624
Mv 84,44,76
Mv 52,44,68
Mv 52,40,64
buffer_load_dwordx4 v13, s[8:11], s39 offen offset:1024 lds
Mv 56,48,64
Mv 56,52,68
Mv 88,52,76
Mv 88,48,72
)" R"(
Mv 120,48,80
Mv 120,52,84
buffer_load_dwordx4 v14, s[8:11], s39 offen offset:2048 lds
Mv 152,52,92
Mv 152,48,88
Mv 156,56,88
Mv 156,60,92
Mv 124,60,84
Mv 124,56,80
buffer_load_dwordx4 v15, s[8:11], s39 offen offset:3072 lds
Mv 92,56,72
Mv 92,60,76
Mv 60,60,68
Mv 60,56,64
s_waitcnt vmcnt(9) lgkmcnt(0)
s_barrier
Mv 160,0,96
Rb 64,16,32768
Mv 160,4,100
Rb 68,17,32768
s_add_u32 s38, s38, 0xffff0080
Mv 192,4,108
Rb 72,16,34816
s_add_u32 m0, s36, 0
Mv 192,0,104
buffer_load_dwordx4 v8, s[4:7], s38 offen offset:0 lds
Mv 224,0,112
Rb 76,17,34816
Mv 224,4,116
Rb 80,16,40960
Ma 224,4,124
Rb 84,17,40960
Ma 224,0,120
Rb 88,16,43008
buffer_load_dwordx4 v9, s[4:7], s38 offen offset:1024 lds
Ma 228,8,120
Rb 92,17,43008
Rt 0,18,32768
Ma 228,12,124
Mv 228,12,116
Rt 2,18,34816
Mv 228,8,112
buffer_load_dwordx4 v10, s[4:7], s38 offen offset:2048 lds
Mv 196,8,104
Rt 4,18,49152
Mv 196,12,108
Mv 164,12,100
Rt 6,18,51200
Mv 164,8,96
buffer_load_dwordx4 v11, s[4:7], s38 offen offset:3072 lds
Mv 168,16,96
Rt 8,19,32768
Mv 168,20,100
s_add_u32 s38, s38, 0x10000
Mv 200,20,108
Rt 10,19,34816
s_add_u32 m0, s36, 4096
Mv 200,16,104
buffer_load_dwordx4 v8, s[4:7], s38 offen offset:0 lds
Mv 232,16,112
Rt 12,19,49152
Mv 232,20,116
Ma 232,20,124
Rt 14,19,51200
Ma 232,16,120
)" R"(
buffer_load_dwordx4 v9, s[4:7], s38 offen offset:1024 lds
Ma 236,24,120
Rt 16,20,32768
Ma 236,28,124
Mv 236,28,116
Rt 18,20,34816
Mv 236,24,112
buffer_load_dwordx4 v10, s[4:7], s38 offen offset:2048 lds
Mv 204,24,104
Rt 20,20,49152
Mv 204,28,108
Mv 172,28,100
Rt 22,20,51200
Mv 172,24,96
buffer_load_dwordx4 v11, s[4:7], s38 offen offset:3072 lds
Mv 176,32,96
Rt 24,21,32768
Mv 176,36,100
Mv 208,36,108
Rt 26,21,34816
Mv 208,32,104
Mv 240,32,112
Rt 28,21,49152
Mv 240,36,116
Ma 240,36,124
Rt 30,21,51200
Ma 240,32,120
Ma 244,40,120
Rt 32,22,32768
Ma 244,44,124
Mv 244,44,116
Rt 34,22,34816
Mv 244,40,112
Mv 212,40,104
Rt 36,22,49152
Mv 212,44,108
Mv 180,44,100
Rt 38,22,51200
Mv 180,40,96
Mv 184,48,96
Rt 40,23,32768
Mv 184,52,100
Mv 216,52,108
Rt 42,23,34816
Mv 216,48,104
Mv 248,48,112
Rt 44,23,49152
Mv 248,52,116
Ma 248,52,124
Rt 46,23,51200
Ma 248,48,120
Ma 252,56,120
Rt 48,24,32768
Ma 252,60,124
s_add_u32 s88, s88, s90
Mv 252,60,116
Rt 50,24,34816
Mv 252,56,112
buffer_load_dword v6, v5, s[24:27], s88 offen
Mv 220,56,104
Rt 52,24,49152
Mv 220,60,108
Mv 188,60,100
Rt 54,24,51200
)" R"(
Mv 188,56,96
Mv 32,0,64
Rt 56,25,32768
Mv 32,4,68
Rt 58,25,34816
Mv 64,4,76
Rt 60,25,49152
Mv 64,0,72
Rt 62,25,51200
Mv 96,0,80
Mv 96,4,84
Mv 128,4,92
Mv 128,0,88
Mv 132,8,88
Mv 132,12,92
Mv 100,12,84
Mv 100,8,80
Mv 68,8,72
Mv 68,12,76
Mv 36,12,68
s_add_u32 s39, s39, 0x40000
Mv 36,8,64
s_waitcnt lgkmcnt(0)
s_barrier
Mv 40,16,64
Rb 96,16,49152
s_add_u32 m0, s37, 32768
Mv 40,20,68
buffer_load_dwordx4 v12, s[8:11], s39 offen offset:0 lds
Mv 72,20,76
Mv 72,16,72
Mv 104,16,80
Rb 100,17,49152
Mv 104,20,84
Mv 136,20,92
Mv 136,16,88
buffer_load_dwordx4 v13, s[8:11], s39 offen offset:1024 lds
Mv 140,24,88
Rb 104,16,51200
Mv 140,28,92
Mv 108,28,84
Mv 108,24,80
Mv 76,24,72
Rb 108,17,51200
.LzH26:
s_cbranch_execz .LzH36
Mv 76,28,76
buffer_load_dwordx4 v14, s[8:11], s39 offen offset:2048 lds
Mv 44,28,68
Mv 44,24,64
Mv 48,32,64
Rb 112,16,57344
Mv 48,36,68
Mv 80,36,76
Mv 80,32,72
buffer_load_dwordx4 v15, s[8:11], s39 offen offset:3072 lds
Mv 112,32,80
Rb 116,17,57344
Mv 112,36,84
Mv 144,36,92
Mv 144,32,88
s_add_u32 s39, s39, 0x40000
Mv 148,40,88
Rb 120,16,59392
s_add_u32 m0, s37, 49152
)" R"(
Mv 148,44,92
buffer_load_dwordx4 v12, s[8:11], s39 offen offset:0 lds
Mv 116,44,84
Mv 116,40,80
Mv 84,40,72
Rb 124,17,59392
Mv 84,44,76
Mv 52,44,68
Mv 52,40,64
buffer_load_dwordx4 v13, s[8:11], s39 offen offset:1024 lds
Mv 56,48,64
Mv 56,52,68
Mv 88,52,76
Mv 88,48,72
Mv 120,48,80
Mv 120,52,84
buffer_load_dwordx4 v14, s[8:11], s39 offen offset:2048 lds
Mv 152,52,92
Mv 152,48,88
Mv 156,56,88
Mv 156,60,92
Mv 124,60,84
Mv 124,56,80
buffer_load_dwordx4 v15, s[8:11], s39 offen offset:3072 lds
Mv 92,56,72
Mv 92,60,76
Mv 60,60,68
Mv 60,56,64
s_waitcnt vmcnt(9) lgkmcnt(0)
s_barrier
Mv 160,0,96
Rb 64,16,0
Mv 160,4,100
Rb 68,17,0
s_add_u32 s38, s38, 0xffff0080
Mv 192,4,108
Rb 72,16,2048
s_add_u32 m0, s36, 32768
Mv 192,0,104
buffer_load_dwordx4 v8, s[4:7], s38 offen offset:0 lds
Mv 224,0,112
Rb 76,17,2048
Mv 224,4,116
Rb 80,16,8192
Ma 224,4,124
Rb 84,17,8192
Ma 224,0,120
Rb 88,16,10240
buffer_load_dwordx4 v9, s[4:7], s38 offen offset:1024 lds
Ma 228,8,120
Rb 92,17,10240
Rt 0,18,0
Ma 228,12,124
Mv 228,12,116
Rt 2,18,2048
Mv 228,8,112
buffer_load_dwordx4 v10, s[4:7], s38 offen offset:2048 lds
Mv 196,8,104
Rt 4,18,16384
Mv 196,12,108
Mv 164,12,100
Rt 6,18,18432
Mv 164,8,96
buffer_load_dwordx4 v11, s[4:7], s38 offen offset:3072 lds
Mv 168,16,96
)" R"(
Rt 8,19,0
Mv 168,20,100
s_add_u32 s38, s38, 0x10000
Mv 200,20,108
Rt 10,19,2048
s_add_u32 m0, s36, 36864
Mv 200,16,104
buffer_load_dwordx4 v8, s[4:7], s38 offen offset:0 lds
Mv 232,16,112
Rt 12,19,16384
Mv 232,20,116
Ma 232,20,124
Rt 14,19,18432
Ma 232,16,120
buffer_load_dwordx4 v9, s[4:7], s38 offen offset:1024 lds
Ma 236,24,120
Rt 16,20,0
Ma 236,28,124
Mv 236,28,116
Rt 18,20,2048
Mv 236,24,112
buffer_load_dwordx4 v10, s[4:7], s38 offen offset:2048 lds
Mv 204,24,104
Rt 20,20,16384
Mv 204,28,108
Mv 172,28,100
Rt 22,20,18432
Mv 172,24,96
buffer_load_dwordx4 v11, s[4:7], s38 offen offset:3072 lds
Mv 176,32,96
Rt 24,21,0
Mv 176,36,100
Mv 208,36,108
Rt 26,21,2048
Mv 208,32,104
Mv 240,32,112
Rt 28,21,16384
Mv 240,36,116
Ma 240,36,124
Rt 30,21,18432
Ma 240,32,120
Ma 244,40,120
Rt 32,22,0
Ma 244,44,124
Mv 244,44,116
Rt 34,22,2048
Mv 244,40,112
Mv 212,40,104
Rt 36,22,16384
Mv 212,44,108
Mv 180,44,100
Rt 38,22,18432
Mv 180,40,96
Mv 184,48,96
Rt 40,23,0
Mv 184,52,100
Mv 216,52,108
Rt 42,23,2048
Mv 216,48,104
Mv 248,48,112
Rt 44,23,16384
Mv 248,52,116
Ma 248,52,124
Rt 46,23,18432
)" R"(
Ma 248,48,120
Ma 252,56,120
Rt 48,24,0
Ma 252,60,124
s_add_u32 s88, s88, s90
Mv 252,60,116
Rt 50,24,2048
s_sub_u32 s40, s40, 1
Mv 252,56,112
buffer_load_dword v6, v5, s[24:27], s88 offen
Mv 220,56,104
Rt 52,24,16384
Mv 220,60,108
Mv 188,60,100
Rt 54,24,18432
Mv 188,56,96
s_cmp_lg_u32 s40, 0
s_cbranch_scc1 .LzPAK
.LzPAKX:
Mv 32,0,64
Rt 56,25,0
Mv 32,4,68
Rt 58,25,2048
Mv 64,4,76
Rt 60,25,16384
Mv 64,0,72
Rt 62,25,18432
Mv 96,0,80
Rb 192,28,32768
Mv 96,4,84
Rb 196,28,34816
Mv 128,4,92
Rb 200,29,32768
Mv 128,0,88
Mv 132,8,88
Rb 204,29,34816
Mv 132,12,92
Mv 100,12,84
Mv 100,8,80
Mv 68,8,72
Mv 68,12,76
Mv 36,12,68
s_add_u32 s39, s39, 0x40000
Mv 36,8,64
s_waitcnt lgkmcnt(0)
s_barrier
Mv 40,16,64
Rb 96,16,16384
s_add_u32 m0, s37, 0
Mv 40,20,68
buffer_load_dwordx4 v12, s[8:11], s39 offen offset:0 lds
Mv 72,20,76
Mv 72,16,72
Mv 104,16,80
Rb 100,17,16384
Mv 104,20,84
buffer_load_dwordx4 v13, s[8:11], s39 offen offset:1024 lds
Mv 136,20,92
Mv 136,16,88
Mv 140,24,88
Rb 104,16,18432
Mv 140,28,92
buffer_load_dwordx4 v14, s[8:11], s39 offen offset:2048 lds
Mv 108,28,84
Mv 108,24,80
)" R"(
Mv 76,24,72
Rb 108,17,18432
Mv 76,28,76
buffer_load_dwordx4 v15, s[8:11], s39 offen offset:3072 lds
Mv 44,28,68
Mv 44,24,64
s_add_u32 s39, s39, 0x40000
Mv 48,32,64
Rb 112,16,24576
s_add_u32 m0, s37, 16384
Mv 48,36,68
buffer_load_dwordx4 v12, s[8:11], s39 offen offset:0 lds
Mv 80,36,76
Mv 80,32,72
Mv 112,32,80
Rb 116,17,24576
Mv 112,36,84
buffer_load_dwordx4 v13, s[8:11], s39 offen offset:1024 lds
Mv 144,36,92
Mv 144,32,88
Mv 148,40,88
Rb 120,16,26624
Mv 148,44,92
buffer_load_dwordx4 v14, s[8:11], s39 offen offset:2048 lds
Mv 116,44,84
Mv 116,40,80
Mv 84,40,72
Rb 124,17,26624
Mv 84,44,76
buffer_load_dwordx4 v15, s[8:11], s39 offen offset:3072 lds
Mv 52,44,68
Mv 52,40,64
s_add_u32 s44, s43, 256
Mv 56,48,64
Rb 208,28,36864
s_add_u32 m0, s36, 131072
Mv 56,52,68
buffer_load_dwordx4 v26, s[12:15], s44 offen offset:0 lds
Mv 88,52,76
Rb 212,28,38912
Mv 88,48,72
Mv 120,48,80
Rb 216,29,36864
Mv 120,52,84
buffer_load_dwordx4 v27, s[12:15], s44 offen offset:1024 lds
Mv 152,52,92
Rb 220,29,38912
Mv 152,48,88
Mv 156,56,88
Mv 156,60,92
s_add_u32 s44, s43, 12544
buffer_load_dwordx4 v26, s[12:15], s44 offen offset:2048 lds
Mv 124,60,84
Mv 124,56,80
Mv 92,56,72
Mv 92,60,76
buffer_load_dwordx4 v27, s[12:15], s44 offen offset:3072 lds
Mv 60,60,68
Mv 60,56,64
s_waitcnt vmcnt(13) lgkmcnt(0)
s_barrier
Mv 160,0,96
Rb 64,16,32768
Mv 160,4,100
Rb 68,17,32768
)" R"(
s_add_u32 s38, s38, 0xffff0080
Mv 192,4,108
Rb 72,16,34816
s_add_u32 m0, s36, 0
Mv 192,0,104
buffer_load_dwordx4 v8, s[4:7], s38 offen offset:0 lds
Mv 224,0,112
Rb 76,17,34816
Mv 224,4,116
Rb 80,16,40960
Ma 224,4,124
Rb 84,17,40960
Ma 224,0,120
Rb 88,16,43008
buffer_load_dwordx4 v9, s[4:7], s38 offen offset:1024 lds
Ma 228,8,120
Rb 92,17,43008
Rt 0,18,32768
Ma 228,12,124
Mv 228,12,116
Rt 2,18,34816
Mv 228,8,112
buffer_load_dwordx4 v10, s[4:7], s38 offen offset:2048 lds
Mv 196,8,104
Rt 4,18,49152
Mv 196,12,108
Mv 164,12,100
Rt 6,18,51200
Mv 164,8,96
buffer_load_dwordx4 v11, s[4:7], s38 offen offset:3072 lds
Mv 168,16,96
Rt 8,19,32768
Mv 168,20,100
s_add_u32 s38, s38, 0x10000
Mv 200,20,108
Rt 10,19,34816
s_add_u32 m0, s36, 4096
Mv 200,16,104
buffer_load_dwordx4 v8, s[4:7], s38 offen offset:0 lds
Mv 232,16,112
Rt 12,19,49152
Mv 232,20,116
Ma 232,20,124
Rt 14,19,51200
Ma 232,16,120
buffer_load_dwordx4 v9, s[4:7], s38 offen offset:1024 lds
Ma 236,24,120
Rt 16,20,32768
Ma 236,28,124
Mv 236,28,116
Rt 18,20,34816
Mv 236,24,112
buffer_load_dwordx4 v10, s[4:7], s38 offen offset:2048 lds
Mv 204,24,104
Rt 20,20,49152
Mv 204,28,108
Mv 172,28,100
Rt 22,20,51200
Mv 172,24,96
buffer_load_dwordx4 v11, s[4:7], s38 offen offset:3072 lds
Mv 176,32,96
Rt 24,21,32768
Mv 176,36,100
s_add_u32 s44, s43, 384
)" R"(
Mv 208,36,108
Rt 26,21,34816
s_add_u32 m0, s36, 135168
Mv 208,32,104
buffer_load_dwordx4 v26, s[12:15], s44 offen offset:0 lds
Mv 240,32,112
Rt 28,21,49152
Mv 240,36,116
Ma 240,36,124
Rt 30,21,51200
Ma 240,32,120
buffer_load_dwordx4 v27, s[12:15], s44 offen offset:1024 lds
Ma 244,40,120
Rt 32,22,32768
Ma 244,44,124
Mv 244,44,116
Rt 34,22,34816
Mv 244,40,112
s_add_u32 s44, s43, 12672
buffer_load_dwordx4 v26, s[12:15], s44 offen offset:2048 lds
Mv 212,40,104
Rt 36,22,49152
Mv 212,44,108
Mv 180,44,100
Rt 38,22,51200
Mv 180,40,96
buffer_load_dwordx4 v27, s[12:15], s44 offen offset:3072 lds
Mv 184,48,96
Rt 40,23,32768
Mv 184,52,100
s_mov_b32 s85, 0x1508800
v_and_b32 v2, 7, v0
Mv 216,52,108
Rt 42,23,34816
v_lshlrev_b32 v2, 7, v2
Mv 216,48,104
buffer_load_dword v4, v2, s[20:23], s85 offen sc0 sc1
Mv 248,48,112
Rt 44,23,49152
Mv 248,52,116
Ma 248,52,124
Rt 46,23,51200
Ma 248,48,120
Ma 252,56,120
Rt 48,24,32768
Ma 252,60,124
Mv 252,60,116
Rt 50,24,34816
Mv 252,56,112
Mv 220,56,104
Rt 52,24,49152
Mv 220,60,108
Mv 188,60,100
Rt 54,24,51200
Mv 188,56,96
Mv 32,0,64
Rt 56,25,32768
Mv 32,4,68
Rt 58,25,34816
Mv 64,4,76
Rt 60,25,49152
Mv 64,0,72
Rt 62,25,51200
s_waitcnt vmcnt(13)
)" R"(
Mv 96,0,80
Rb 128,28,32768
Mv 96,4,84
Rb 132,28,34816
Mv 128,4,92
Rb 136,29,32768
Mv 128,0,88
Mv 132,8,88
Rb 140,29,34816
Mv 132,12,92
Mv 100,12,84
Mv 100,8,80
Mv 68,8,72
Mv 68,12,76
Mv 36,12,68
s_add_u32 s39, s39, 0x40000
Mv 36,8,64
s_waitcnt lgkmcnt(0)
s_barrier
Mv 40,16,64
Rb 96,16,49152
s_add_u32 m0, s37, 32768
Mv 40,20,68
buffer_load_dwordx4 v12, s[8:11], s39 offen offset:0 lds
Mv 72,20,76
Mv 72,16,72
Mv 104,16,80
Rb 100,17,49152
Mv 104,20,84
buffer_load_dwordx4 v13, s[8:11], s39 offen offset:1024 lds
Mv 136,20,92
Mv 136,16,88
Mv 140,24,88
Rb 104,16,51200
Mv 140,28,92
buffer_load_dwordx4 v14, s[8:11], s39 offen offset:2048 lds
Mv 108,28,84
Mv 108,24,80
Mv 76,24,72
Rb 108,17,51200
Mv 76,28,76
buffer_load_dwordx4 v15, s[8:11], s39 offen offset:3072 lds
Mv 44,28,68
Mv 44,24,64
s_add_u32 s39, s39, 0x40000
Mv 48,32,64
Rb 112,16,57344
s_add_u32 m0, s37, 49152
Mv 48,36,68
buffer_load_dwordx4 v12, s[8:11], s39 offen offset:0 lds
Mv 80,36,76
Mv 80,32,72
Mv 112,32,80
Rb 116,17,57344
Mv 112,36,84
buffer_load_dwordx4 v13, s[8:11], s39 offen offset:1024 lds
Mv 144,36,92
Mv 144,32,88
Mv 148,40,88
Rb 120,16,59392
Mv 148,44,92
buffer_load_dwordx4 v14, s[8:11], s39 offen offset:2048 lds
Mv 116,44,84
Mv 116,40,80
Mv 84,40,72
)" R"(
Rb 124,17,59392
Mv 84,44,76
buffer_load_dwordx4 v15, s[8:11], s39 offen offset:3072 lds
Mv 52,44,68
Mv 52,40,64
s_add_u32 s44, s43, 512
s_waitcnt vmcnt(9)
Mv 56,48,64
Rb 144,28,36864
s_add_u32 m0, s36, 131072
Mv 56,52,68
buffer_load_dwordx4 v26, s[12:15], s44 offen offset:0 lds
Mv 88,52,76
Rb 148,28,38912
Mv 88,48,72
Mv 120,48,80
Rb 152,29,36864
Mv 120,52,84
buffer_load_dwordx4 v27, s[12:15], s44 offen offset:1024 lds
Mv 152,52,92
Rb 156,29,38912
Mv 152,48,88
Mv 156,56,88
Mv 156,60,92
s_add_u32 s44, s43, 12800
buffer_load_dwordx4 v26, s[12:15], s44 offen offset:2048 lds
Mv 124,60,84
Mv 124,56,80
Mv 92,56,72
Mv 92,60,76
buffer_load_dwordx4 v27, s[12:15], s44 offen offset:3072 lds
Mv 60,60,68
Mv 60,56,64
s_waitcnt vmcnt(12) lgkmcnt(0)
s_barrier
Mv 160,0,96
Rb 64,16,0
v_xor_b32 v4, s3, v4
v_cmp_gt_u32_e32 vcc, 2, v4
s_andn2_b32 s28, 0xff, vcc_lo
s_cbranch_scc1 .Lzsp1
.Lzok1:
Mv 160,4,100
Rb 68,17,0
s_add_u32 s38, s38, 0xffff0080
Mv 192,4,108
Rb 72,16,2048
s_add_u32 m0, s36, 32768
Mv 192,0,104
buffer_load_dwordx4 v8, s[4:7], s38 offen offset:0 lds
Mv 224,0,112
Rb 76,17,2048
v_xor_b32 v4, s3, v4
.LzH27:
s_cbranch_execz .LzH37
Mv 224,4,116
Rb 80,16,8192
v_and_b32 v4, 1, v4
Ma 224,4,124
Rb 84,17,8192
Ma 224,0,120
Rb 88,16,10240
buffer_load_dwordx4 v9, s[4:7], s38 offen offset:1024 lds
Ma 228,8,120
Rb 92,17,10240
)" R"(
Rt 0,18,0
Ma 228,12,124
v_cmp_eq_u32_e32 vcc, 1, v4
s_mov_b32 s85, vcc_lo
Mv 228,12,116
Rt 2,18,2048
s_and_b32 s28, s2, 7
Mv 228,8,112
buffer_load_dwordx4 v10, s[4:7], s38 offen offset:2048 lds
Mv 196,8,104
Rt 4,18,16384
s_lshr_b32 s28, s85, s28
Mv 196,12,108
s_xor_b32 s28, s28, s34
s_and_b32 s28, s28, 1
Mv 164,12,100
Rt 6,18,18432
s_lshl_b32 s84, s28, 13
Mv 164,8,96
buffer_load_dwordx4 v11, s[4:7], s38 offen offset:3072 lds
Mv 168,16,96
Rt 8,19,0
s_add_u32 s49, s49, s84
Mv 168,20,100
s_add_u32 s38, s38, 0x10000
s_add_u32 s50, s50, s84
Mv 200,20,108
Rt 10,19,2048
s_add_u32 m0, s36, 36864
Mv 200,16,104
buffer_load_dwordx4 v8, s[4:7], s38 offen offset:0 lds
Mv 232,16,112
Rt 12,19,16384
s_add_u32 s60, s50, 7168
Mv 232,20,116
s_add_u32 s52, s52, s84
s_add_u32 s65, s65, s84
Ma 232,20,124
Rt 14,19,18432
s_lshr_b32 s28, s2, 6
Ma 232,16,120
buffer_load_dwordx4 v9, s[4:7], s38 offen offset:1024 lds
Ma 236,24,120
Rt 16,20,0
s_lshl_b32 s28, s28, 14
Ma 236,28,124
s_add_u32 s28, s28, s50
s_bfe_u32 s29, s2, 0x20004
Mv 236,28,116
Rt 18,20,2048
s_lshl2_add_u32 s29, s29, s33
Mv 236,24,112
buffer_load_dwordx4 v10, s[4:7], s38 offen offset:2048 lds
Mv 204,24,104
Rt 20,20,16384
s_lshl_b32 s29, s29, 9
Mv 204,28,108
s_add_u32 s28, s28, s29
v_min_u32 v4, 3, v0
Mv 172,28,100
Rt 22,20,18432
v_lshlrev_b32 v4, 7, v4
Mv 172,24,96
buffer_load_dwordx4 v11, s[4:7], s38 offen offset:3072 lds
)" R"(
Mv 176,32,96
Rt 24,21,0
Mv 176,36,100
s_add_u32 s44, s43, 640
Mv 208,36,108
Rt 26,21,2048
s_add_u32 m0, s36, 135168
Mv 208,32,104
buffer_load_dwordx4 v26, s[12:15], s44 offen offset:0 lds
Mv 240,32,112
Rt 28,21,16384
v_lshrrev_b32 v10, 3, v0
Mv 240,36,116
buffer_load_dword v4, v4, s[20:23], s28 offen sc0 sc1
Ma 240,36,124
Rt 30,21,18432
s_lshl1_add_u32 s28, s36, s84
Ma 240,32,120
buffer_load_dwordx4 v27, s[12:15], s44 offen offset:1024 lds
Ma 244,40,120
Rt 32,22,0
v_add_u32 v2, s28, v1
Ma 244,44,124
v_add_u32 v3, 0x1000, v2
s_lshl_b32 s28, s34, 13
Mv 244,44,116
Rt 34,22,2048
s_xor_b32 s28, s28, s84
Mv 244,40,112
s_add_u32 s44, s43, 12928
buffer_load_dwordx4 v26, s[12:15], s44 offen offset:2048 lds
Mv 212,40,104
Rt 36,22,16384
s_and_b32 s28, s28, 0x2000
Mv 212,44,108
s_xor_b32 s73, s73, s28
v_and_b32 v11, 0x70, v1
Mv 180,44,100
Rt 38,22,18432
v_lshl_or_b32 v10, v10, 13, v11
Mv 180,40,96
buffer_load_dwordx4 v27, s[12:15], s44 offen offset:3072 lds
Mv 184,48,96
Rt 40,23,0
v_lshlrev_b32 v11, 1, v0
Mv 184,52,100
v_and_b32 v11, 32, v11
v_xor_b32 v10, v10, v11
Mv 216,52,108
Rt 42,23,2048
s_and_b32 s28, s33, 1
Mv 216,48,104
s_lshl_b32 s28, s28, 6
v_xor_b32 v8, s28, v10
Mv 248,48,112
Rt 44,23,16384
v_xor_b32 v9, 64, v8
Mv 248,52,116
v_add_u32 v9, 0xfc00, v9
v_add_u32 v10, 0x1f800, v8
Ma 248,52,124
Rt 46,23,18432
v_add_u32 v11, 0x1f800, v9
Ma 248,48,120
)" R"(
Ma 252,56,120
Rt 48,24,0
Ma 252,60,124
Mv 252,60,116
Rt 50,24,2048
Mv 252,56,112
Mv 220,56,104
Rt 52,24,16384
Mv 220,60,108
Mv 188,60,100
Rt 54,24,18432
Mv 188,56,96
Mv 32,0,64
Rt 56,25,0
Mv 32,4,68
Rt 58,25,2048
v_and_b32 v5, 7, v0
Mv 64,4,76
Rt 60,25,16384
v_lshlrev_b32 v5, 7, v5
Mv 64,0,72
Rt 62,25,18432
s_waitcnt vmcnt(13)
Mv 96,0,80
Rb 160,28,32768
v_mov_b32 v6, s3
Mv 96,4,84
Rb 164,28,34816
Mv 128,4,92
Rb 168,29,32768
Mv 128,0,88
Mv 132,8,88
Rb 172,29,34816
Mv 132,12,92
Mv 100,12,84
Mv 100,8,80
Mv 68,8,72
Mv 68,12,76
Mv 36,12,68
Mv 36,8,64
s_waitcnt lgkmcnt(4)
s_barrier
Mv 40,16,64
Rb 96,16,16384
Mv 40,20,68
Mv 72,20,76
s_add_u32 m0, s41, 65536
s_add_u32 s92, s42, 0x80
Mv 72,16,72
buffer_load_dwordx4 v8, s[16:19], s92 offen offset:0 lds
Mv 104,16,80
Rb 100,17,16384
Mv 104,20,84
Mv 136,20,92
Mv 136,16,88
buffer_load_dwordx4 v9, s[16:19], s92 offen offset:1024 lds
Mv 140,24,88
Rb 104,16,18432
Mv 140,28,92
Mv 108,28,84
Mv 108,24,80
buffer_load_dwordx4 v10, s[16:19], s92 offen offset:2048 lds
Mv 76,24,72
Rb 108,17,18432
Mv 76,28,76
)" R"(
Mv 44,28,68
Mv 44,24,64
buffer_load_dwordx4 v11, s[16:19], s92 offen offset:3072 lds
Mv 48,32,64
Rb 112,16,24576
Mv 48,36,68
Mv 80,36,76
s_add_u32 m0, s41, 69632
s_add_u32 s92, s42, 0x40080
Mv 80,32,72
buffer_load_dwordx4 v8, s[16:19], s92 offen offset:0 lds
Mv 112,32,80
Rb 116,17,24576
Mv 112,36,84
Mv 144,36,92
Mv 144,32,88
buffer_load_dwordx4 v9, s[16:19], s92 offen offset:1024 lds
Mv 148,40,88
Rb 120,16,26624
Mv 148,44,92
Mv 116,44,84
Mv 116,40,80
buffer_load_dwordx4 v10, s[16:19], s92 offen offset:2048 lds
Mv 84,40,72
Rb 124,17,26624
Mv 84,44,76
Mv 52,44,68
Mv 52,40,64
s_waitcnt vmcnt(7)
Mv 56,48,64
Rb 176,28,36864
Mv 56,52,68
Mv 88,52,76
Rb 180,28,38912
Mv 88,48,72
Mv 120,48,80
Rb 184,29,36864
Mv 120,52,84
Mv 152,52,92
Rb 188,29,38912
Mv 152,48,88
Mv 156,56,88
Mv 156,60,92
buffer_load_dword v4, v7, s[20:23], s52 offen offset:0 sc0 sc1
Mv 124,60,84
Mv 124,56,80
Mv 92,56,72
Mv 92,60,76
Mv 60,60,68
Mv 60,56,64
s_waitcnt lgkmcnt(0)
s_barrier
Mv 160,0,96
Rb 64,16,32768
Mv 160,4,100
Rb 68,17,32768
s_add_u32 s92, s42, 0x0
Mv 192,4,108
Rb 72,16,34816
s_add_u32 m0, s41, 131072
Mv 192,0,104
buffer_load_dwordx4 v8, s[16:19], s92 offen offset:0 lds
Mv 224,0,112
Rb 76,17,34816
Mv 224,4,116
)" R"(
Rb 80,16,40960
Ma 224,4,124
Rb 84,17,40960
Ma 224,0,120
Rb 88,16,43008
buffer_load_dwordx4 v9, s[16:19], s92 offen offset:1024 lds
Ma 228,8,120
Rb 92,17,43008
Rt 0,18,32768
Ma 228,12,124
Mv 228,12,116
Rt 2,18,34816
Mv 228,8,112
buffer_load_dwordx4 v10, s[16:19], s92 offen offset:2048 lds
Mv 196,8,104
Rt 4,18,49152
Mv 196,12,108
Mv 164,12,100
Rt 6,18,51200
Mv 164,8,96
buffer_load_dwordx4 v11, s[16:19], s92 offen offset:3072 lds
Mv 168,16,96
Rt 8,19,32768
Mv 168,20,100
s_add_u32 s92, s42, 0x40000
Mv 200,20,108
Rt 10,19,34816
s_add_u32 m0, s41, 135168
Mv 200,16,104
buffer_load_dwordx4 v8, s[16:19], s92 offen offset:0 lds
Mv 232,16,112
Rt 12,19,49152
Mv 232,20,116
Ma 232,20,124
Rt 14,19,51200
Ma 232,16,120
buffer_load_dwordx4 v9, s[16:19], s92 offen offset:1024 lds
Ma 236,24,120
Rt 16,20,32768
Ma 236,28,124
Mv 236,28,116
Rt 18,20,34816
Mv 236,24,112
buffer_load_dwordx4 v10, s[16:19], s92 offen offset:2048 lds
Mv 204,24,104
Rt 20,20,49152
Mv 204,28,108
s_add_u32 s92, s42, 0x1c0000
Mv 172,28,100
Rt 22,20,51200
s_add_u32 m0, s41, 0
Mv 172,24,96
buffer_load_dwordx4 v8, s[16:19], s92 offen offset:0 lds
Mv 176,32,96
Rt 24,21,32768
Mv 176,36,100
Mv 208,36,108
Rt 26,21,34816
Mv 208,32,104
buffer_load_dwordx4 v9, s[16:19], s92 offen offset:1024 lds
Mv 240,32,112
Rt 28,21,49152
Mv 240,36,116
Ma 240,36,124
)" R"(
Rt 30,21,51200
Ma 240,32,120
buffer_load_dwordx4 v10, s[16:19], s92 offen offset:2048 lds
Ma 244,40,120
Rt 32,22,32768
Ma 244,44,124
Mv 244,44,116
Rt 34,22,34816
Mv 244,40,112
buffer_load_dwordx4 v11, s[16:19], s92 offen offset:3072 lds
Mv 212,40,104
Rt 36,22,49152
Mv 212,44,108
s_add_u32 s92, s42, 0x200000
Mv 180,44,100
Rt 38,22,51200
s_add_u32 m0, s41, 4096
Mv 180,40,96
buffer_load_dwordx4 v8, s[16:19], s92 offen offset:0 lds
Mv 184,48,96
Rt 40,23,32768
Mv 184,52,100
Mv 216,52,108
Rt 42,23,34816
Mv 216,48,104
buffer_load_dwordx4 v9, s[16:19], s92 offen offset:1024 lds
Mv 248,48,112
Rt 44,23,49152
Mv 248,52,116
Ma 248,52,124
Rt 46,23,51200
Ma 248,48,120
buffer_load_dwordx4 v10, s[16:19], s92 offen offset:2048 lds
Ma 252,56,120
Rt 48,24,32768
Ma 252,60,124
Mv 252,60,116
Rt 50,24,34816
Mv 252,56,112
Mv 220,56,104
Rt 52,24,49152
Mv 220,60,108
Mv 188,60,100
Rt 54,24,51200
s_waitcnt vmcnt(14)
v_cmp_eq_u32_e32 vcc, s3, v4
s_andn2_b64 s[28:29], 1, vcc
s_cbranch_scc1 .Lzsp2
.Lzok2:
Mv 188,56,96
Mv 32,0,64
Rt 56,25,32768
Mv 32,4,68
Rt 58,25,34816
Mv 64,4,76
Rt 60,25,49152
Mv 64,0,72
Rt 62,25,51200
Mv 68,8,72
Rb 96,16,49152
Mv 68,12,76
Rb 100,17,49152
Mv 36,12,68
Rb 104,16,51200
)" R"(
Mv 36,8,64
Rb 108,17,51200
s_waitcnt lgkmcnt(12)
Mv 40,16,64
Rb 112,16,57344
Mv 40,20,68
Rb 116,17,57344
Mv 72,20,76
Rb 120,16,59392
Mv 72,16,72
Rb 124,17,59392
Mv 76,24,72
Cv 32,32,33
Cv 33,34,35
Mv 76,28,76
v_bfe_u32 v12, v0, 3, 2
v_lshlrev_b32 v12, 5, v12
Mv 44,28,68
v_and_b32 v4, 3, v0
v_lshl_or_b32 v12, v4, 3, v12
Mv 44,24,64
v_bfe_u32 v4, v0, 2, 2
v_lshl_or_b32 v12, v4, 7, v12
Mv 48,32,64
v_lshrrev_b32 v4, 4, v0
v_lshl_or_b32 v12, v4, 10, v12
Mv 48,36,68
v_xor_b32 v13, 32, v12
v_xor_b32 v14, 64, v12
Mv 80,36,76
v_xor_b32 v15, 96, v12
v_add_u32 v20, 0x20000, v12
Mv 80,32,72
v_add_u32 v21, 0x20000, v13
v_add_u32 v22, 0x20000, v14
Mv 84,40,72
v_add_u32 v23, 0x20000, v15
v_add_u32 v16, 0x10000, v12
Mv 84,44,76
v_add_u32 v17, 0x10000, v13
v_add_u32 v18, 0x10000, v14
Mv 52,44,68
v_add_u32 v19, 0x10000, v15
Cv 34,36,37
Mv 52,40,64
s_waitcnt lgkmcnt(0)
s_barrier
Mv 56,48,64
Cv 35,38,39
Cv 40,40,41
Mv 56,52,68
Cv 41,42,43
Cv 42,44,45
Mv 88,52,76
s_add_u32 m0, s36, 98304
s_add_u32 s44, s43, 768
Mv 88,48,72
buffer_load_dwordx4 v26, s[12:15], s44 offen offset:0 lds
Mv 92,56,72
Cv 43,46,47
Cv 48,48,49
Mv 92,60,76
Cv 49,50,51
Cv 50,52,53
Mv 60,60,68
)" R"(
Cv 51,54,55
Cv 64,64,65
Mv 60,56,64
Cv 65,66,67
Cv 66,68,69
Mv 96,0,80
Cv 67,70,71
Cv 72,72,73
Mv 96,4,84
Cv 73,74,75
Cv 74,76,77
Mv 128,4,92
Cv 75,78,79
Cv 80,80,81
Mv 128,0,88
buffer_load_dwordx4 v27, s[12:15], s44 offen offset:1024 lds
Mv 132,8,88
Cv 56,56,57
Cv 57,58,59
Mv 132,12,92
Cv 58,60,61
Cv 59,62,63
Mv 100,12,84
Cv 81,82,83
Cv 82,84,85
Mv 100,8,80
buffer_store_dwordx4 v[32:35], v2, s[20:23], s45 offen offset:0 sc0 sc1
Mv 104,16,80
Cv 83,86,87
Cv 88,88,89
Mv 104,20,84
Cv 89,90,91
Cv 90,92,93
Mv 136,20,92
Cv 91,94,95
Cv 128,128,129
Mv 136,16,88
s_add_u32 s44, s43, 13056
buffer_load_dwordx4 v26, s[12:15], s44 offen offset:2048 lds
Mv 140,24,88
Cv 96,96,97
Cv 97,98,99
Mv 140,28,92
Cv 98,100,101
Cv 99,102,103
Mv 108,28,84
Cv 129,130,131
Cv 130,132,133
Mv 108,24,80
buffer_store_dwordx4 v[40:43], v2, s[20:23], s45 offen offset:1024 sc0 sc1
Mv 112,32,80
Cv 131,134,135
Mv 112,36,84
Cv 136,136,137
Cv 137,138,139
Mv 144,36,92
Cv 138,140,141
Cv 139,142,143
Mv 144,32,88
buffer_load_dwordx4 v27, s[12:15], s44 offen offset:3072 lds
Mv 148,40,88
Cv 104,104,105
Cv 105,106,107
Mv 148,44,92
)" R"(
Cv 106,108,109
Cv 107,110,111
Mv 116,44,84
Mv 116,40,80
buffer_store_dwordx4 v[48:51], v2, s[20:23], s45 offen offset:2048 sc0 sc1
Mv 120,48,80
Mv 120,52,84
Cv 144,144,145
Cv 145,146,147
Mv 152,52,92
s_add_u32 m0, s41, 32768
s_add_u32 s92, s42, 0x1c0080
Mv 152,48,88
buffer_load_dwordx4 v8, s[16:19], s92 offen offset:0 lds
Mv 156,56,88
Cv 112,112,113
Cv 113,114,115
.LzH28:
s_cbranch_execz .LzH38
Mv 156,60,92
Cv 114,116,117
Cv 115,118,119
Mv 124,60,84
Cv 146,148,149
Cv 147,150,151
Mv 124,56,80
buffer_store_dwordx4 v[56:59], v2, s[20:23], s45 offen offset:3072 sc0 sc1
Mv 160,0,96
Mv 160,4,100
Cv 152,152,153
Cv 153,154,155
Mv 192,4,108
Cv 154,156,157
Cv 155,158,159
Mv 192,0,104
buffer_load_dwordx4 v9, s[16:19], s92 offen offset:1024 lds
Mv 224,0,112
Cv 120,120,121
Cv 121,122,123
Mv 224,4,116
Cv 122,124,125
Cv 123,126,127
Ma 224,4,124
Ma 224,0,120
buffer_store_dwordx4 v[64:67], v3, s[20:23], s45 offen offset:0 sc0 sc1
Ma 228,8,120
Ma 228,12,124
Mv 228,12,116
Mv 228,8,112
buffer_load_dwordx4 v10, s[16:19], s92 offen offset:2048 lds
Mv 196,8,104
Mv 196,12,108
Mv 164,12,100
Mv 164,8,96
buffer_store_dwordx4 v[72:75], v3, s[20:23], s45 offen offset:1024 sc0 sc1
Mv 168,16,96
Mv 168,20,100
Mv 200,20,108
Mv 200,16,104
buffer_load_dwordx4 v11, s[16:19], s92 offen offset:3072 lds
Mv 232,16,112
Mv 232,20,116
Ma 232,20,124
Ma 232,16,120
)" R"(
buffer_store_dwordx4 v[80:83], v3, s[20:23], s45 offen offset:2048 sc0 sc1
Ma 236,24,120
Ma 236,28,124
Mv 236,28,116
s_add_u32 m0, s41, 36864
s_add_u32 s92, s42, 0x200080
Mv 236,24,112
buffer_load_dwordx4 v8, s[16:19], s92 offen offset:0 lds
Mv 204,24,104
Mv 204,28,108
Mv 172,28,100
Mv 172,24,96
buffer_store_dwordx4 v[88:91], v3, s[20:23], s45 offen offset:3072 sc0 sc1
Mv 176,32,96
Mv 176,36,100
Mv 208,36,108
Mv 208,32,104
buffer_load_dwordx4 v9, s[16:19], s92 offen offset:1024 lds
Mv 240,32,112
Mv 240,36,116
Ma 240,36,124
Ma 240,32,120
s_waitcnt vmcnt(18)
s_barrier
Ma 244,40,120
Ma 244,44,124
Mv 244,44,116
Mv 244,40,112
buffer_load_dwordx4 v10, s[16:19], s92 offen offset:2048 lds
Mv 212,40,104
Mv 212,44,108
Mv 180,44,100
Mv 180,40,96
Mv 184,48,96
Rt 16,20,0
Mv 184,52,100
Rt 18,20,512
Mv 216,52,108
Rt 20,20,4096
Mv 216,48,104
Rt 22,20,4608
Mv 248,48,112
Rt 24,21,0
Mv 248,52,116
Rt 26,21,512
Ma 248,52,124
Rt 28,21,4096
Ma 248,48,120
Rt 30,21,4608
Ma 252,56,120
Rt 32,22,0
Ma 252,60,124
Rt 34,22,512
Mv 252,60,116
Rt 36,22,4096
Mv 252,56,112
Rt 38,22,4608
Mv 220,56,104
Rt 40,23,0
Mv 220,60,108
Rt 42,23,512
Mv 188,60,100
Rt 44,23,4096
Mv 188,56,96
Rt 46,23,4608
)" R"(
s_waitcnt vmcnt(13) lgkmcnt(8)
s_branch .LzQA
.LzH29:
s_cbranch_execz .LzH35
.LzAV:
Mz 32,64,0
Rt 56,25,0
Mv 32,68,4
Rt 58,25,2048
s_lshl1_add_u32 s40, s34, s91
Mz 36,76,4
Rt 60,25,16384
s_lshl_b32 s40, s40, 12
Mv 36,72,0
Rt 62,25,18432
s_and_b32 s29, s38, 0xfffe0000
Mz 96,80,0
s_add_u32 s40, s40, s29
v_min_u32 v3, 31, v0
Mv 96,84,4
v_lshlrev_b32 v3, 7, v3
Mz 100,92,4
Mv 100,88,0
Mz 108,88,8
Mv 108,92,12
Mz 104,84,12
Mv 104,80,8
Mz 44,72,8
Mv 44,76,12
Mz 40,68,12
s_add_u32 s39, s39, 0x40000
Mv 40,64,8
s_waitcnt lgkmcnt(0)
s_barrier
Mz 48,64,16
Rb 96,16,16384
s_add_u32 m0, s37, 0
Mv 48,68,20
buffer_load_dwordx4 v12, s[8:11], s39 offen offset:0 lds
Mz 52,76,20
Mv 52,72,16
Mz 112,80,16
Rb 100,17,16384
Mv 112,84,20
Mz 116,92,20
Mv 116,88,16
buffer_load_dwordx4 v13, s[8:11], s39 offen offset:1024 lds
Mz 124,88,24
Rb 104,16,18432
Mv 124,92,28
Mz 120,84,28
Mv 120,80,24
Mz 60,72,24
Rb 108,17,18432
Mv 60,76,28
buffer_load_dwordx4 v14, s[8:11], s39 offen offset:2048 lds
Mz 56,68,28
Mv 56,64,24
Mz 64,64,32
Rb 112,16,24576
Mv 64,68,36
buffer_load_dword v6, v3, s[4:7], s40 offen
Mz 68,76,36
Mv 68,72,32
buffer_load_dwordx4 v15, s[8:11], s39 offen offset:3072 lds
)" R"(
Mz 128,80,32
Rb 116,17,24576
s_mov_b32 s28, s35
Mv 128,84,36
s_lshl2_add_u32 s28, s28, s33
s_mul_i32 s43, s28, 28672
Mz 132,92,36
v_and_b32 v26, 0x70, v1
v_and_b32 v4, 48, v0
Mv 132,88,32
s_add_u32 s39, s39, 0x40000
v_xor_b32 v26, v26, v4
Mz 140,88,40
Rb 120,16,26624
s_add_u32 m0, s37, 16384
Mv 140,92,44
buffer_load_dwordx4 v12, s[8:11], s39 offen offset:0 lds
Mz 136,84,44
v_lshrrev_b32 v4, 3, v0
v_mul_u32_u24 v4, 896, v4
Mv 136,80,40
v_add_u32 v26, v26, v4
v_xor_b32 v27, 64, v26
Mz 76,72,40
Rb 124,17,26624
v_add_u32 v27, 6144, v27
Mv 76,76,44
v_bfe_u32 v28, v0, 1, 3
v_lshrrev_b32 v4, 4, v0
Mz 72,68,44
v_xor_b32 v28, v28, v4
v_lshlrev_b32 v28, 4, v28
Mv 72,64,40
buffer_load_dwordx4 v13, s[8:11], s39 offen offset:1024 lds
Mz 80,64,48
v_and_b32 v4, 15, v0
v_lshl_or_b32 v28, v4, 7, v28
Mv 80,68,52
s_add_u32 s28, s36, 0x18000
v_add_u32 v28, s28, v28
Mz 84,76,52
v_xor_b32 v29, 64, v28
Mv 84,72,48
Mz 144,80,48
Mv 144,84,52
buffer_load_dwordx4 v14, s[8:11], s39 offen offset:2048 lds
Mz 148,92,52
Mv 148,88,48
Mz 156,88,56
Mv 156,92,60
Mz 152,84,60
Mv 152,80,56
buffer_load_dwordx4 v15, s[8:11], s39 offen offset:3072 lds
Mz 92,72,56
Mv 92,76,60
Mz 88,68,60
Mv 88,64,56
s_call_b64 s[30:31], .LzHOP
s_waitcnt vmcnt(11) lgkmcnt(0)
s_barrier
Mz 160,96,0
Rb 64,16,32768
s_bfe_u32 s88, s2, 0x20005
Mv 160,100,4
Rb 68,17,32768
)" R"(
s_add_u32 s38, s38, 0xffff0080
Mz 164,108,4
Rb 72,16,34816
s_add_u32 m0, s36, 0
Mv 164,104,0
buffer_load_dwordx4 v8, s[4:7], s38 offen offset:0 lds
Mz 224,112,0
Rb 76,17,34816
s_and_b32 s28, s2, 15
Mv 224,116,4
Rb 80,16,40960
s_lshl4_add_u32 s88, s88, s28
Mz 228,124,4
Rb 84,17,40960
s_lshl2_add_u32 s88, s88, s33
Mv 228,120,0
Rb 88,16,43008
buffer_load_dwordx4 v9, s[4:7], s38 offen offset:1024 lds
Mz 236,120,8
Rb 92,17,43008
Rt 0,18,32768
Mv 236,124,12
s_mul_i32 s88, s88, 4096
s_add_u32 s88, s88, 0x100000
Mz 232,116,12
Rt 2,18,34816
v_min_u32 v5, 31, v0
Mv 232,112,8
buffer_load_dwordx4 v10, s[4:7], s38 offen offset:2048 lds
Mz 172,104,8
Rt 4,18,49152
v_lshlrev_b32 v5, 7, v5
Mv 172,108,12
s_mov_b32 s90, 0x100000
s_mov_b64 s[24:25], s[8:9]
Mz 168,100,12
Rt 6,18,51200
s_mov_b64 s[26:27], s[10:11]
Mv 168,96,8
buffer_load_dwordx4 v11, s[4:7], s38 offen offset:3072 lds
Mz 176,96,16
Rt 8,19,32768
Mv 176,100,20
s_add_u32 s38, s38, 0x10000
Mz 180,108,20
Rt 10,19,34816
s_add_u32 m0, s36, 4096
Mv 180,104,16
buffer_load_dwordx4 v8, s[4:7], s38 offen offset:0 lds
Mz 240,112,16
Rt 12,19,49152
Mv 240,116,20
Mz 244,124,20
Rt 14,19,51200
Mv 244,120,16
buffer_load_dwordx4 v9, s[4:7], s38 offen offset:1024 lds
Mz 252,120,24
Rt 16,20,32768
Mv 252,124,28
Mz 248,116,28
Rt 18,20,34816
Mv 248,112,24
buffer_load_dwordx4 v10, s[4:7], s38 offen offset:2048 lds
Mz 188,104,24
)" R"(
Rt 20,20,49152
Mv 188,108,28
Mz 184,100,28
Rt 22,20,51200
Mv 184,96,24
buffer_load_dwordx4 v11, s[4:7], s38 offen offset:3072 lds
Mz 192,96,32
Rt 24,21,32768
Mv 192,100,36
Mz 196,108,36
Rt 26,21,34816
Mv 196,104,32
v_mfma_f32_16x16x32_bf16 a[224:227], a[112:115], a[32:35], 0
Rt 28,21,49152
Ma 224,116,36
v_mfma_f32_16x16x32_bf16 a[228:231], a[124:127], a[36:39], 0
Rt 30,21,51200
Ma 228,120,32
v_mfma_f32_16x16x32_bf16 a[236:239], a[120:123], a[40:43], 0
Rt 32,22,32768
Ma 236,124,44
v_mfma_f32_16x16x32_bf16 a[232:235], a[116:119], a[44:47], 0
Rt 34,22,34816
Ma 232,112,40
Mz 204,104,40
Rt 36,22,49152
Mv 204,108,44
Mz 200,100,44
Rt 38,22,51200
Mv 200,96,40
Mz 208,96,48
Rt 40,23,32768
Mv 208,100,52
Mz 212,108,52
Rt 42,23,34816
Mv 212,104,48
v_mfma_f32_16x16x32_bf16 a[240:243], a[112:115], a[48:51], 0
Rt 44,23,49152
Ma 240,116,52
v_mfma_f32_16x16x32_bf16 a[244:247], a[124:127], a[52:55], 0
Rt 46,23,51200
Ma 244,120,48
v_mfma_f32_16x16x32_bf16 a[252:255], a[120:123], a[56:59], 0
Rt 48,24,32768
Ma 252,124,60
s_add_u32 s88, s88, s90
v_mfma_f32_16x16x32_bf16 a[248:251], a[116:119], a[60:63], 0
Rt 50,24,34816
Ma 248,112,56
buffer_load_dword v6, v5, s[24:27], s88 offen
Mz 220,104,56
Rt 52,24,49152
Mv 220,108,60
Mz 216,100,60
Rt 54,24,51200
Mv 216,96,56
Mv 32,64,0
Rt 56,25,32768
s_lshl_b32 s29, s91, 22
Mv 32,68,4
Rt 58,25,34816
s_add_u32 s28, s89, s29
Mv 36,76,4
Rt 60,25,49152
)" R"(
s_bfe_u32 s29, s63, 0x80000
Mv 36,72,0
Rt 62,25,51200
s_lshl_b32 s29, s29, 16
Mv 96,80,0
s_add_u32 s45, s28, s29
s_bfe_u32 s29, s63, 0x80008
Mv 96,84,4
s_lshl_b32 s29, s29, 16
s_add_u32 s46, s28, s29
Mv 100,92,4
s_bfe_u32 s29, s63, 0x80010
s_lshl_b32 s29, s29, 16
Mv 100,88,0
s_add_u32 s47, s28, s29
Mv 108,88,8
s_bfe_u32 s29, s63, 0x80018
s_lshl_b32 s29, s29, 16
Mv 108,92,12
s_add_u32 s48, s28, s29
s_add_u32 s50, s89, 0xd40000
Mv 104,84,12
s_lshl2_add_u32 s28, s91, s93
s_lshl_b32 s28, s28, 7
Mv 104,80,8
s_add_u32 s49, s28, s50
s_lshl_b32 s52, s35, 7
Mv 44,72,8
s_add_u32 s52, s52, s50
s_add_u32 s52, s52, 0xd000
Mv 44,76,12
s_lshl_b32 s51, s35, 17
s_add_u32 s51, s51, s89
Mv 40,68,12
s_add_u32 s39, s39, 0x40000
s_add_u32 s51, s51, 0x800000
Mv 40,64,8
s_waitcnt lgkmcnt(0)
s_barrier
Mv 48,64,16
Rb 96,16,49152
s_add_u32 m0, s37, 32768
Mv 48,68,20
buffer_load_dwordx4 v12, s[8:11], s39 offen offset:0 lds
Mv 52,76,20
s_cmp_lg_u32 s33, 0
s_cmov_b32 s49, 0x40000000
Mv 52,72,16
s_mov_b32 s53, 0x40000
s_mov_b32 s54, 0x3db8aa3b
Mv 112,80,16
Rb 100,17,49152
s_mul_i32 s41, s33, 7168
Mv 112,84,20
s_mul_i32 s42, s33, 0x70000
s_lshl_b32 s28, s34, 9
Mv 116,92,20
s_add_u32 s42, s42, s28
s_mov_b32 s62, 0x4e800000
Mv 116,88,16
buffer_load_dwordx4 v13, s[8:11], s39 offen offset:1024 lds
Mv 124,88,24
Rb 104,16,51200
s_add_u32 s67, s89, 0xc00000
Mv 124,92,28
)" R"(
s_lshl3_add_u32 s28, s34, s35
s_add_u32 s64, s51, 0x100000
Mv 120,84,28
s_lshl2_add_u32 s28, s28, s33
s_lshl2_add_u32 s65, s35, s33
Mv 120,80,24
s_lshl_b32 s65, s65, 7
s_add_u32 s65, s65, s50
Mv 60,72,24
Rb 108,17,51200
s_add_u32 s65, s65, 0xc000
Mv 60,76,28
buffer_load_dwordx4 v14, s[8:11], s39 offen offset:2048 lds
Mv 56,68,28
s_lshl_b32 s66, s28, 10
s_add_u32 s66, s66, 0x1400000
Mv 56,64,24
s_mov_b32 s28, s35
s_lshl2_add_u32 s28, s28, s33
Mv 64,64,32
Rb 112,16,57344
s_and_b32 s29, s34, 1
Mv 64,68,36
s_lshl1_add_u32 s29, s28, s29
s_lshl_b32 s73, s29, 15
Mv 68,76,36
s_and_b32 s29, s89, 0xfe000000
s_add_u32 s73, s73, s29
Mv 68,72,32
buffer_load_dwordx4 v15, s[8:11], s39 offen offset:3072 lds
Mv 128,80,32
Rb 116,17,57344
s_add_u32 s73, s73, 0x1000000
Mv 128,84,36
s_lshl4_add_u32 s28, s28, s34
s_lshl_b32 s68, s28, 8
Mv 132,92,36
s_add_u32 s68, s68, 0x1510000
s_add_u32 s55, s89, 0x120000
Mv 132,88,32
s_add_u32 s39, s39, 0x40000
s_lshl_b32 s28, s35, 1
Mv 140,88,40
Rb 120,16,59392
s_add_u32 m0, s37, 49152
Mv 140,92,44
buffer_load_dwordx4 v12, s[8:11], s39 offen offset:0 lds
Mv 136,84,44
s_sub_u32 s57, 14, s28
s_mov_b32 s59, s89
Mv 136,80,40
s_mov_b32 s58, 2
Mv 76,72,40
Rb 124,17,59392
Mv 76,76,44
s_cmp_eq_u32 s35, 7
s_cmov_b32 s55, s89
Mv 72,68,44
s_cmp_eq_u32 s35, 7
s_cmov_b32 s57, 16
Mv 72,64,40
buffer_load_dwordx4 v13, s[8:11], s39 offen offset:1024 lds
Mv 80,64,48
s_cmp_eq_u32 s35, 7
)" R"(
s_cmov_b32 s59, s67
Mv 80,68,52
s_add_u32 s56, s55, 0x400000
Mv 84,76,52
Mv 84,72,48
Mv 144,80,48
Mv 144,84,52
buffer_load_dwordx4 v14, s[8:11], s39 offen offset:2048 lds
Mv 148,92,52
Mv 148,88,48
Mv 156,88,56
Mv 156,92,60
Mv 152,84,60
Mv 152,80,56
buffer_load_dwordx4 v15, s[8:11], s39 offen offset:3072 lds
Mv 92,72,56
Mv 92,76,60
Mv 88,68,60
Mv 88,64,56
s_waitcnt vmcnt(9) lgkmcnt(0)
s_barrier
Mv 160,96,0
Rb 64,16,0
Mv 160,100,4
Rb 68,17,0
s_add_u32 s38, s38, 0xffff0080
Mv 164,108,4
Rb 72,16,2048
s_add_u32 m0, s36, 32768
Mv 164,104,0
buffer_load_dwordx4 v8, s[4:7], s38 offen offset:0 lds
Mv 224,112,0
Rb 76,17,2048
Mv 224,116,4
Rb 80,16,8192
Mv 228,124,4
Rb 84,17,8192
Mv 228,120,0
Rb 88,16,10240
buffer_load_dwordx4 v9, s[4:7], s38 offen offset:1024 lds
Mv 236,120,8
Rb 92,17,10240
Rt 0,18,0
Mv 236,124,12
s_lshl2_add_u32 s28, s2, s33
s_lshl_b32 s28, s28, 11
Mv 232,116,12
Rt 2,18,2048
s_add_u32 s28, s28, 0x1500000
Mv 232,112,8
buffer_load_dwordx4 v10, s[4:7], s38 offen offset:2048 lds
Mv 172,104,8
Rt 4,18,16384
v_min_u32 v3, 15, v0
Mv 172,108,12
v_lshlrev_b32 v3, 7, v3
Mv 168,100,12
Rt 6,18,18432
Mv 168,96,8
buffer_load_dwordx4 v11, s[4:7], s38 offen offset:3072 lds
Mv 176,96,16
Rt 8,19,0
.LzH30:
s_cbranch_execz .LzH36
Mv 176,100,20
)" R"(
s_add_u32 s38, s38, 0x10000
Mv 180,108,20
Rt 10,19,2048
s_add_u32 m0, s36, 36864
Mv 180,104,16
buffer_load_dwordx4 v8, s[4:7], s38 offen offset:0 lds
Mv 240,112,16
Rt 12,19,16384
Mv 240,116,20
Mv 244,124,20
Rt 14,19,18432
Mv 244,120,16
buffer_load_dwordx4 v9, s[4:7], s38 offen offset:1024 lds
Mv 252,120,24
Rt 16,20,0
Mv 252,124,28
Mv 248,116,28
Rt 18,20,2048
Mv 248,112,24
buffer_load_dwordx4 v10, s[4:7], s38 offen offset:2048 lds
Mv 188,104,24
Rt 20,20,16384
Mv 188,108,28
Mv 184,100,28
Rt 22,20,18432
Mv 184,96,24
buffer_load_dwordx4 v11, s[4:7], s38 offen offset:3072 lds
Mv 192,96,32
Rt 24,21,0
Mv 192,100,36
s_add_u32 s44, s43, 0
Mv 196,108,36
Rt 26,21,2048
s_add_u32 m0, s36, 131072
Mv 196,104,32
buffer_load_dwordx4 v26, s[12:15], s44 offen offset:0 lds
Ma 224,112,32
Rt 28,21,16384
Ma 224,116,36
Ma 228,124,36
Rt 30,21,18432
buffer_load_dwordx4 v27, s[12:15], s44 offen offset:1024 lds
Ma 228,120,32
Ma 236,120,40
Rt 32,22,0
Ma 236,124,44
s_add_u32 s44, s43, 12288
buffer_load_dwordx4 v26, s[12:15], s44 offen offset:2048 lds
Ma 232,116,44
Rt 34,22,2048
Ma 232,112,40
Mv 204,104,40
Rt 36,22,16384
buffer_load_dwordx4 v27, s[12:15], s44 offen offset:3072 lds
Mv 204,108,44
s_add_u32 s44, s43, 128
Mv 200,100,44
Rt 38,22,18432
s_add_u32 m0, s36, 135168
Mv 200,96,40
buffer_load_dwordx4 v26, s[12:15], s44 offen offset:0 lds
Mv 208,96,48
Rt 40,23,0
Mv 208,100,52
)" R"(
Mv 212,108,52
Rt 42,23,2048
buffer_load_dwordx4 v27, s[12:15], s44 offen offset:1024 lds
Mv 212,104,48
Ma 240,112,48
Rt 44,23,16384
Ma 240,116,52
s_add_u32 s44, s43, 12416
buffer_load_dwordx4 v26, s[12:15], s44 offen offset:2048 lds
Ma 244,124,52
Rt 46,23,18432
Ma 244,120,48
Ma 252,120,56
Rt 48,24,0
buffer_load_dwordx4 v27, s[12:15], s44 offen offset:3072 lds
Ma 252,124,60
s_add_u32 s88, s88, s90
s_mov_b32 s40, 5
Ma 248,116,60
Rt 50,24,2048
Ma 248,112,56
buffer_load_dword v6, v5, s[24:27], s88 offen
Mv 220,104,56
Rt 52,24,16384
Mv 220,108,60
buffer_load_dword v6, v3, s[20:23], s28 offen sc0 sc1
Mv 216,100,60
Rt 54,24,18432
Mv 216,96,56
.LzPAV:
Mv 32,64,0
Rt 56,25,0
Mv 32,68,4
Rt 58,25,2048
Mv 36,76,4
Rt 60,25,16384
Mv 36,72,0
Rt 62,25,18432
Mv 96,80,0
Mv 96,84,4
Mv 100,92,4
Mv 100,88,0
Mv 108,88,8
Mv 108,92,12
Mv 104,84,12
Mv 104,80,8
Mv 44,72,8
Mv 44,76,12
Mv 40,68,12
s_add_u32 s39, s39, 0x40000
Mv 40,64,8
s_waitcnt lgkmcnt(0)
s_barrier
Mv 48,64,16
Rb 96,16,16384
s_add_u32 m0, s37, 0
Mv 48,68,20
buffer_load_dwordx4 v12, s[8:11], s39 offen offset:0 lds
Mv 52,76,20
Mv 52,72,16
Mv 112,80,16
Rb 100,17,16384
Mv 112,84,20
Mv 116,92,20
Mv 116,88,16
)" R"(
buffer_load_dwordx4 v13, s[8:11], s39 offen offset:1024 lds
Mv 124,88,24
Rb 104,16,18432
Mv 124,92,28
Mv 120,84,28
Mv 120,80,24
Mv 60,72,24
Rb 108,17,18432
Mv 60,76,28
buffer_load_dwordx4 v14, s[8:11], s39 offen offset:2048 lds
Mv 56,68,28
Mv 56,64,24
Mv 64,64,32
Rb 112,16,24576
Mv 64,68,36
Mv 68,76,36
Mv 68,72,32
buffer_load_dwordx4 v15, s[8:11], s39 offen offset:3072 lds
Mv 128,80,32
Rb 116,17,24576
Mv 128,84,36
Mv 132,92,36
Mv 132,88,32
s_add_u32 s39, s39, 0x40000
Mv 140,88,40
Rb 120,16,26624
s_add_u32 m0, s37, 16384
Mv 140,92,44
buffer_load_dwordx4 v12, s[8:11], s39 offen offset:0 lds
Mv 136,84,44
Mv 136,80,40
Mv 76,72,40
Rb 124,17,26624
Mv 76,76,44
Mv 72,68,44
Mv 72,64,40
buffer_load_dwordx4 v13, s[8:11], s39 offen offset:1024 lds
Mv 80,64,48
Mv 80,68,52
Mv 84,76,52
Mv 84,72,48
Mv 144,80,48
Mv 144,84,52
buffer_load_dwordx4 v14, s[8:11], s39 offen offset:2048 lds
Mv 148,92,52
Mv 148,88,48
Mv 156,88,56
Mv 156,92,60
Mv 152,84,60
Mv 152,80,56
buffer_load_dwordx4 v15, s[8:11], s39 offen offset:3072 lds
Mv 92,72,56
Mv 92,76,60
Mv 88,68,60
Mv 88,64,56
s_waitcnt vmcnt(9) lgkmcnt(0)
s_barrier
Mv 160,96,0
Rb 64,16,32768
Mv 160,100,4
Rb 68,17,32768
s_add_u32 s38, s38, 0xffff0080
Mv 164,108,4
Rb 72,16,34816
s_add_u32 m0, s36, 0
)" R"(
Mv 164,104,0
buffer_load_dwordx4 v8, s[4:7], s38 offen offset:0 lds
Mv 224,112,0
Rb 76,17,34816
Mv 224,116,4
Rb 80,16,40960
Mv 228,124,4
Rb 84,17,40960
Mv 228,120,0
Rb 88,16,43008
buffer_load_dwordx4 v9, s[4:7], s38 offen offset:1024 lds
Mv 236,120,8
Rb 92,17,43008
Rt 0,18,32768
Mv 236,124,12
Mv 232,116,12
Rt 2,18,34816
Mv 232,112,8
buffer_load_dwordx4 v10, s[4:7], s38 offen offset:2048 lds
Mv 172,104,8
Rt 4,18,49152
Mv 172,108,12
Mv 168,100,12
Rt 6,18,51200
Mv 168,96,8
buffer_load_dwordx4 v11, s[4:7], s38 offen offset:3072 lds
Mv 176,96,16
Rt 8,19,32768
Mv 176,100,20
s_add_u32 s38, s38, 0x10000
Mv 180,108,20
Rt 10,19,34816
s_add_u32 m0, s36, 4096
Mv 180,104,16
buffer_load_dwordx4 v8, s[4:7], s38 offen offset:0 lds
Mv 240,112,16
Rt 12,19,49152
Mv 240,116,20
Mv 244,124,20
Rt 14,19,51200
Mv 244,120,16
buffer_load_dwordx4 v9, s[4:7], s38 offen offset:1024 lds
Mv 252,120,24
Rt 16,20,32768
Mv 252,124,28
Mv 248,116,28
Rt 18,20,34816
Mv 248,112,24
buffer_load_dwordx4 v10, s[4:7], s38 offen offset:2048 lds
Mv 188,104,24
Rt 20,20,49152
Mv 188,108,28
Mv 184,100,28
Rt 22,20,51200
Mv 184,96,24
buffer_load_dwordx4 v11, s[4:7], s38 offen offset:3072 lds
Mv 192,96,32
Rt 24,21,32768
Mv 192,100,36
Mv 196,108,36
Rt 26,21,34816
Mv 196,104,32
Ma 224,112,32
Rt 28,21,49152
)" R"(
Ma 224,116,36
Ma 228,124,36
Rt 30,21,51200
Ma 228,120,32
Ma 236,120,40
Rt 32,22,32768
Ma 236,124,44
Ma 232,116,44
Rt 34,22,34816
Ma 232,112,40
Mv 204,104,40
Rt 36,22,49152
Mv 204,108,44
Mv 200,100,44
Rt 38,22,51200
Mv 200,96,40
Mv 208,96,48
Rt 40,23,32768
Mv 208,100,52
Mv 212,108,52
Rt 42,23,34816
Mv 212,104,48
Ma 240,112,48
Rt 44,23,49152
Ma 240,116,52
Ma 244,124,52
Rt 46,23,51200
Ma 244,120,48
Ma 252,120,56
Rt 48,24,32768
Ma 252,124,60
s_add_u32 s88, s88, s90
Ma 248,116,60
Rt 50,24,34816
Ma 248,112,56
buffer_load_dword v6, v5, s[24:27], s88 offen
Mv 220,104,56
Rt 52,24,49152
Mv 220,108,60
Mv 216,100,60
Rt 54,24,51200
Mv 216,96,56
Mv 32,64,0
Rt 56,25,32768
Mv 32,68,4
Rt 58,25,34816
Mv 36,76,4
Rt 60,25,49152
Mv 36,72,0
Rt 62,25,51200
Mv 96,80,0
Mv 96,84,4
Mv 100,92,4
Mv 100,88,0
Mv 108,88,8
Mv 108,92,12
Mv 104,84,12
Mv 104,80,8
Mv 44,72,8
Mv 44,76,12
Mv 40,68,12
s_add_u32 s39, s39, 0x40000
Mv 40,64,8
s_waitcnt lgkmcnt(0)
s_barrier
)" R"(
Mv 48,64,16
Rb 96,16,49152
s_add_u32 m0, s37, 32768
Mv 48,68,20
buffer_load_dwordx4 v12, s[8:11], s39 offen offset:0 lds
Mv 52,76,20
Mv 52,72,16
Mv 112,80,16
Rb 100,17,49152
Mv 112,84,20
Mv 116,92,20
Mv 116,88,16
buffer_load_dwordx4 v13, s[8:11], s39 offen offset:1024 lds
Mv 124,88,24
Rb 104,16,51200
Mv 124,92,28
Mv 120,84,28
Mv 120,80,24
Mv 60,72,24
Rb 108,17,51200
Mv 60,76,28
buffer_load_dwordx4 v14, s[8:11], s39 offen offset:2048 lds
Mv 56,68,28
Mv 56,64,24
Mv 64,64,32
Rb 112,16,57344
Mv 64,68,36
Mv 68,76,36
Mv 68,72,32
buffer_load_dwordx4 v15, s[8:11], s39 offen offset:3072 lds
Mv 128,80,32
Rb 116,17,57344
Mv 128,84,36
Mv 132,92,36
Mv 132,88,32
s_add_u32 s39, s39, 0x40000
Mv 140,88,40
Rb 120,16,59392
s_add_u32 m0, s37, 49152
Mv 140,92,44
buffer_load_dwordx4 v12, s[8:11], s39 offen offset:0 lds
Mv 136,84,44
Mv 136,80,40
Mv 76,72,40
Rb 124,17,59392
Mv 76,76,44
Mv 72,68,44
Mv 72,64,40
buffer_load_dwordx4 v13, s[8:11], s39 offen offset:1024 lds
Mv 80,64,48
Mv 80,68,52
Mv 84,76,52
Mv 84,72,48
Mv 144,80,48
Mv 144,84,52
buffer_load_dwordx4 v14, s[8:11], s39 offen offset:2048 lds
Mv 148,92,52
Mv 148,88,48
Mv 156,88,56
Mv 156,92,60
Mv 152,84,60
Mv 152,80,56
buffer_load_dwordx4 v15, s[8:11], s39 offen offset:3072 lds
Mv 92,72,56
)" R"(
Mv 92,76,60
Mv 88,68,60
Mv 88,64,56
s_waitcnt vmcnt(9) lgkmcnt(0)
s_barrier
Mv 160,96,0
Rb 64,16,0
Mv 160,100,4
Rb 68,17,0
s_add_u32 s38, s38, 0xffff0080
Mv 164,108,4
Rb 72,16,2048
s_add_u32 m0, s36, 32768
Mv 164,104,0
buffer_load_dwordx4 v8, s[4:7], s38 offen offset:0 lds
Mv 224,112,0
Rb 76,17,2048
Mv 224,116,4
Rb 80,16,8192
Mv 228,124,4
Rb 84,17,8192
Mv 228,120,0
Rb 88,16,10240
buffer_load_dwordx4 v9, s[4:7], s38 offen offset:1024 lds
Mv 236,120,8
Rb 92,17,10240
Rt 0,18,0
Mv 236,124,12
Mv 232,116,12
Rt 2,18,2048
Mv 232,112,8
buffer_load_dwordx4 v10, s[4:7], s38 offen offset:2048 lds
Mv 172,104,8
Rt 4,18,16384
Mv 172,108,12
Mv 168,100,12
Rt 6,18,18432
Mv 168,96,8
buffer_load_dwordx4 v11, s[4:7], s38 offen offset:3072 lds
Mv 176,96,16
Rt 8,19,0
Mv 176,100,20
s_add_u32 s38, s38, 0x10000
Mv 180,108,20
Rt 10,19,2048
s_add_u32 m0, s36, 36864
Mv 180,104,16
buffer_load_dwordx4 v8, s[4:7], s38 offen offset:0 lds
Mv 240,112,16
Rt 12,19,16384
Mv 240,116,20
Mv 244,124,20
Rt 14,19,18432
Mv 244,120,16
buffer_load_dwordx4 v9, s[4:7], s38 offen offset:1024 lds
Mv 252,120,24
Rt 16,20,0
Mv 252,124,28
Mv 248,116,28
Rt 18,20,2048
Mv 248,112,24
buffer_load_dwordx4 v10, s[4:7], s38 offen offset:2048 lds
Mv 188,104,24
Rt 20,20,16384
Mv 188,108,28
)" R"(
Mv 184,100,28
Rt 22,20,18432
Mv 184,96,24
buffer_load_dwordx4 v11, s[4:7], s38 offen offset:3072 lds
Mv 192,96,32
Rt 24,21,0
Mv 192,100,36
Mv 196,108,36
Rt 26,21,2048
Mv 196,104,32
Ma 224,112,32
Rt 28,21,16384
Ma 224,116,36
Ma 228,124,36
Rt 30,21,18432
Ma 228,120,32
Ma 236,120,40
Rt 32,22,0
Ma 236,124,44
Ma 232,116,44
Rt 34,22,2048
Ma 232,112,40
Mv 204,104,40
Rt 36,22,16384
Mv 204,108,44
Mv 200,100,44
Rt 38,22,18432
Mv 200,96,40
Mv 208,96,48
Rt 40,23,0
Mv 208,100,52
Mv 212,108,52
Rt 42,23,2048
Mv 212,104,48
Ma 240,112,48
Rt 44,23,16384
Ma 240,116,52
Ma 244,124,52
Rt 46,23,18432
Ma 244,120,48
Ma 252,120,56
Rt 48,24,0
Ma 252,124,60
s_add_u32 s88, s88, s90
Ma 248,116,60
Rt 50,24,2048
s_sub_u32 s40, s40, 1
Ma 248,112,56
buffer_load_dword v6, v5, s[24:27], s88 offen
Mv 220,104,56
Rt 52,24,16384
Mv 220,108,60
Mv 216,100,60
Rt 54,24,18432
Mv 216,96,56
s_cmp_lg_u32 s40, 0
s_cbranch_scc1 .LzPAV
.LzPAVX:
Mv 32,64,0
Rt 56,25,0
Mv 32,68,4
Rt 58,25,2048
Mv 36,76,4
Rt 60,25,16384
)" R"(
Mv 36,72,0
Rt 62,25,18432
Mv 96,80,0
Rb 192,28,32768
.LzH31:
s_cbranch_execz .LzH37
Mv 96,84,4
Rb 196,28,34816
Mv 100,92,4
Rb 200,29,32768
Mv 100,88,0
Mv 108,88,8
Rb 204,29,34816
Mv 108,92,12
Mv 104,84,12
Mv 104,80,8
Mv 44,72,8
Mv 44,76,12
Mv 40,68,12
s_add_u32 s39, s39, 0x40000
Mv 40,64,8
s_waitcnt lgkmcnt(0)
s_barrier
Mv 48,64,16
Rb 96,16,16384
s_add_u32 m0, s37, 0
Mv 48,68,20
buffer_load_dwordx4 v12, s[8:11], s39 offen offset:0 lds
Mv 52,76,20
Mv 52,72,16
Mv 112,80,16
Rb 100,17,16384
Mv 112,84,20
buffer_load_dwordx4 v13, s[8:11], s39 offen offset:1024 lds
Mv 116,92,20
Mv 116,88,16
Mv 124,88,24
Rb 104,16,18432
Mv 124,92,28
buffer_load_dwordx4 v14, s[8:11], s39 offen offset:2048 lds
Mv 120,84,28
Mv 120,80,24
Mv 60,72,24
Rb 108,17,18432
Mv 60,76,28
buffer_load_dwordx4 v15, s[8:11], s39 offen offset:3072 lds
Mv 56,68,28
Mv 56,64,24
s_add_u32 s39, s39, 0x40000
Mv 64,64,32
Rb 112,16,24576
s_add_u32 m0, s37, 16384
Mv 64,68,36
buffer_load_dwordx4 v12, s[8:11], s39 offen offset:0 lds
Mv 68,76,36
Mv 68,72,32
Mv 128,80,32
Rb 116,17,24576
Mv 128,84,36
buffer_load_dwordx4 v13, s[8:11], s39 offen offset:1024 lds
Mv 132,92,36
Mv 132,88,32
Mv 140,88,40
Rb 120,16,26624
Mv 140,92,44
)" R"(
buffer_load_dwordx4 v14, s[8:11], s39 offen offset:2048 lds
Mv 136,84,44
Mv 136,80,40
Mv 76,72,40
Rb 124,17,26624
Mv 76,76,44
buffer_load_dwordx4 v15, s[8:11], s39 offen offset:3072 lds
Mv 72,68,44
Mv 72,64,40
s_add_u32 s44, s43, 256
Mv 80,64,48
Rb 208,28,36864
s_add_u32 m0, s36, 131072
Mv 80,68,52
buffer_load_dwordx4 v26, s[12:15], s44 offen offset:0 lds
Mv 84,76,52
Rb 212,28,38912
Mv 84,72,48
Mv 144,80,48
Rb 216,29,36864
Mv 144,84,52
buffer_load_dwordx4 v27, s[12:15], s44 offen offset:1024 lds
Mv 148,92,52
Rb 220,29,38912
Mv 148,88,48
Mv 156,88,56
Mv 156,92,60
s_add_u32 s44, s43, 12544
buffer_load_dwordx4 v26, s[12:15], s44 offen offset:2048 lds
Mv 152,84,60
Mv 152,80,56
Mv 92,72,56
Mv 92,76,60
buffer_load_dwordx4 v27, s[12:15], s44 offen offset:3072 lds
Mv 88,68,60
Mv 88,64,56
s_waitcnt vmcnt(13) lgkmcnt(0)
s_barrier
Mv 160,96,0
Rb 64,16,32768
Mv 160,100,4
Rb 68,17,32768
s_add_u32 s38, s38, 0xffff0080
Mv 164,108,4
Rb 72,16,34816
s_add_u32 m0, s36, 0
Mv 164,104,0
buffer_load_dwordx4 v8, s[4:7], s38 offen offset:0 lds
Mv 224,112,0
Rb 76,17,34816
Mv 224,116,4
Rb 80,16,40960
Mv 228,124,4
Rb 84,17,40960
Mv 228,120,0
Rb 88,16,43008
buffer_load_dwordx4 v9, s[4:7], s38 offen offset:1024 lds
Mv 236,120,8
Rb 92,17,43008
Rt 0,18,32768
Mv 236,124,12
Mv 232,116,12
Rt 2,18,34816
Mv 232,112,8
buffer_load_dwordx4 v10, s[4:7], s38 offen offset:2048 lds
)" R"(
Mv 172,104,8
Rt 4,18,49152
Mv 172,108,12
Mv 168,100,12
Rt 6,18,51200
Mv 168,96,8
buffer_load_dwordx4 v11, s[4:7], s38 offen offset:3072 lds
Mv 176,96,16
Rt 8,19,32768
Mv 176,100,20
s_add_u32 s38, s38, 0x10000
Mv 180,108,20
Rt 10,19,34816
s_add_u32 m0, s36, 4096
Mv 180,104,16
buffer_load_dwordx4 v8, s[4:7], s38 offen offset:0 lds
Mv 240,112,16
Rt 12,19,49152
Mv 240,116,20
Mv 244,124,20
Rt 14,19,51200
Mv 244,120,16
buffer_load_dwordx4 v9, s[4:7], s38 offen offset:1024 lds
Mv 252,120,24
Rt 16,20,32768
Mv 252,124,28
Mv 248,116,28
Rt 18,20,34816
Mv 248,112,24
buffer_load_dwordx4 v10, s[4:7], s38 offen offset:2048 lds
Mv 188,104,24
Rt 20,20,49152
Mv 188,108,28
Mv 184,100,28
Rt 22,20,51200
Mv 184,96,24
buffer_load_dwordx4 v11, s[4:7], s38 offen offset:3072 lds
Mv 192,96,32
Rt 24,21,32768
Mv 192,100,36
s_add_u32 s44, s43, 384
Mv 196,108,36
Rt 26,21,34816
s_add_u32 m0, s36, 135168
Mv 196,104,32
buffer_load_dwordx4 v26, s[12:15], s44 offen offset:0 lds
Ma 224,112,32
Rt 28,21,49152
Ma 224,116,36
Ma 228,124,36
Rt 30,21,51200
Ma 228,120,32
buffer_load_dwordx4 v27, s[12:15], s44 offen offset:1024 lds
Ma 236,120,40
Rt 32,22,32768
Ma 236,124,44
Ma 232,116,44
Rt 34,22,34816
Ma 232,112,40
s_add_u32 s44, s43, 12672
buffer_load_dwordx4 v26, s[12:15], s44 offen offset:2048 lds
Mv 204,104,40
Rt 36,22,49152
Mv 204,108,44
)" R"(
Mv 200,100,44
Rt 38,22,51200
Mv 200,96,40
buffer_load_dwordx4 v27, s[12:15], s44 offen offset:3072 lds
Mv 208,96,48
Rt 40,23,32768
Mv 208,100,52
s_mov_b32 s85, 0x1508800
v_and_b32 v2, 7, v0
Mv 212,108,52
Rt 42,23,34816
v_lshlrev_b32 v2, 7, v2
Mv 212,104,48
buffer_load_dword v4, v2, s[20:23], s85 offen sc0 sc1
Ma 240,112,48
Rt 44,23,49152
Ma 240,116,52
Ma 244,124,52
Rt 46,23,51200
Ma 244,120,48
Ma 252,120,56
Rt 48,24,32768
Ma 252,124,60
Ma 248,116,60
Rt 50,24,34816
Ma 248,112,56
Mv 220,104,56
Rt 52,24,49152
Mv 220,108,60
Mv 216,100,60
Rt 54,24,51200
Mv 216,96,56
Mv 32,64,0
Rt 56,25,32768
Mv 32,68,4
Rt 58,25,34816
Mv 36,76,4
Rt 60,25,49152
Mv 36,72,0
Rt 62,25,51200
s_waitcnt vmcnt(13)
Mv 96,80,0
Rb 128,28,32768
Mv 96,84,4
Rb 132,28,34816
Mv 100,92,4
Rb 136,29,32768
Mv 100,88,0
Mv 108,88,8
Rb 140,29,34816
Mv 108,92,12
Mv 104,84,12
Mv 104,80,8
Mv 44,72,8
Mv 44,76,12
Mv 40,68,12
s_add_u32 s39, s39, 0x40000
Mv 40,64,8
s_waitcnt lgkmcnt(0)
s_barrier
Mv 48,64,16
Rb 96,16,49152
s_add_u32 m0, s37, 32768
Mv 48,68,20
buffer_load_dwordx4 v12, s[8:11], s39 offen offset:0 lds
)" R"(
Mv 52,76,20
Mv 52,72,16
Mv 112,80,16
Rb 100,17,49152
Mv 112,84,20
buffer_load_dwordx4 v13, s[8:11], s39 offen offset:1024 lds
Mv 116,92,20
Mv 116,88,16
Mv 124,88,24
Rb 104,16,51200
Mv 124,92,28
buffer_load_dwordx4 v14, s[8:11], s39 offen offset:2048 lds
Mv 120,84,28
Mv 120,80,24
Mv 60,72,24
Rb 108,17,51200
Mv 60,76,28
buffer_load_dwordx4 v15, s[8:11], s39 offen offset:3072 lds
Mv 56,68,28
Mv 56,64,24
s_add_u32 s39, s39, 0x40000
Mv 64,64,32
Rb 112,16,57344
s_add_u32 m0, s37, 49152
Mv 64,68,36
buffer_load_dwordx4 v12, s[8:11], s39 offen offset:0 lds
Mv 68,76,36
Mv 68,72,32
Mv 128,80,32
Rb 116,17,57344
Mv 128,84,36
buffer_load_dwordx4 v13, s[8:11], s39 offen offset:1024 lds
Mv 132,92,36
Mv 132,88,32
Mv 140,88,40
Rb 120,16,59392
Mv 140,92,44
buffer_load_dwordx4 v14, s[8:11], s39 offen offset:2048 lds
Mv 136,84,44
Mv 136,80,40
Mv 76,72,40
Rb 124,17,59392
Mv 76,76,44
buffer_load_dwordx4 v15, s[8:11], s39 offen offset:3072 lds
Mv 72,68,44
Mv 72,64,40
s_add_u32 s44, s43, 512
s_waitcnt vmcnt(9)
Mv 80,64,48
Rb 144,28,36864
s_add_u32 m0, s36, 131072
Mv 80,68,52
buffer_load_dwordx4 v26, s[12:15], s44 offen offset:0 lds
Mv 84,76,52
Rb 148,28,38912
Mv 84,72,48
Mv 144,80,48
Rb 152,29,36864
Mv 144,84,52
buffer_load_dwordx4 v27, s[12:15], s44 offen offset:1024 lds
Mv 148,92,52
Rb 156,29,38912
Mv 148,88,48
Mv 156,88,56
)" R"(
Mv 156,92,60
s_add_u32 s44, s43, 12800
buffer_load_dwordx4 v26, s[12:15], s44 offen offset:2048 lds
Mv 152,84,60
Mv 152,80,56
Mv 92,72,56
Mv 92,76,60
buffer_load_dwordx4 v27, s[12:15], s44 offen offset:3072 lds
Mv 88,68,60
Mv 88,64,56
s_waitcnt vmcnt(12) lgkmcnt(0)
s_barrier
Mv 160,96,0
Rb 64,16,0
v_xor_b32 v4, s3, v4
v_cmp_gt_u32_e32 vcc, 2, v4
s_andn2_b32 s28, 0xff, vcc_lo
s_cbranch_scc1 .Lzsp3
.Lzok3:
Mv 160,100,4
Rb 68,17,0
s_add_u32 s38, s38, 0xffff0080
Mv 164,108,4
Rb 72,16,2048
s_add_u32 m0, s36, 32768
Mv 164,104,0
buffer_load_dwordx4 v8, s[4:7], s38 offen offset:0 lds
Mv 224,112,0
Rb 76,17,2048
v_xor_b32 v4, s3, v4
Mv 224,116,4
Rb 80,16,8192
v_and_b32 v4, 1, v4
Mv 228,124,4
Rb 84,17,8192
Mv 228,120,0
Rb 88,16,10240
buffer_load_dwordx4 v9, s[4:7], s38 offen offset:1024 lds
Mv 236,120,8
Rb 92,17,10240
Rt 0,18,0
Mv 236,124,12
v_cmp_eq_u32_e32 vcc, 1, v4
s_mov_b32 s85, vcc_lo
Mv 232,116,12
Rt 2,18,2048
s_and_b32 s28, s2, 7
Mv 232,112,8
buffer_load_dwordx4 v10, s[4:7], s38 offen offset:2048 lds
Mv 172,104,8
Rt 4,18,16384
s_lshr_b32 s28, s85, s28
Mv 172,108,12
s_xor_b32 s28, s28, s34
s_and_b32 s28, s28, 1
Mv 168,100,12
Rt 6,18,18432
s_lshl_b32 s84, s28, 13
Mv 168,96,8
buffer_load_dwordx4 v11, s[4:7], s38 offen offset:3072 lds
Mv 176,96,16
Rt 8,19,0
s_add_u32 s49, s49, s84
Mv 176,100,20
s_add_u32 s38, s38, 0x10000
)" R"(
s_add_u32 s50, s50, s84
Mv 180,108,20
Rt 10,19,2048
s_add_u32 m0, s36, 36864
Mv 180,104,16
buffer_load_dwordx4 v8, s[4:7], s38 offen offset:0 lds
Mv 240,112,16
Rt 12,19,16384
s_add_u32 s60, s50, 7168
Mv 240,116,20
s_add_u32 s52, s52, s84
s_add_u32 s65, s65, s84
Mv 244,124,20
Rt 14,19,18432
s_lshr_b32 s28, s2, 6
Mv 244,120,16
buffer_load_dwordx4 v9, s[4:7], s38 offen offset:1024 lds
Mv 252,120,24
Rt 16,20,0
s_lshl_b32 s28, s28, 14
Mv 252,124,28
s_add_u32 s28, s28, s50
s_bfe_u32 s29, s2, 0x20004
Mv 248,116,28
Rt 18,20,2048
s_lshl2_add_u32 s29, s29, s33
Mv 248,112,24
buffer_load_dwordx4 v10, s[4:7], s38 offen offset:2048 lds
Mv 188,104,24
Rt 20,20,16384
s_lshl_b32 s29, s29, 9
Mv 188,108,28
s_add_u32 s28, s28, s29
v_min_u32 v4, 3, v0
Mv 184,100,28
Rt 22,20,18432
v_lshlrev_b32 v4, 7, v4
Mv 184,96,24
buffer_load_dwordx4 v11, s[4:7], s38 offen offset:3072 lds
Mv 192,96,32
Rt 24,21,0
Mv 192,100,36
s_add_u32 s44, s43, 640
Mv 196,108,36
Rt 26,21,2048
s_add_u32 m0, s36, 135168
Mv 196,104,32
buffer_load_dwordx4 v26, s[12:15], s44 offen offset:0 lds
Ma 224,112,32
Rt 28,21,16384
v_lshrrev_b32 v10, 3, v0
Ma 224,116,36
buffer_load_dword v4, v4, s[20:23], s28 offen sc0 sc1
Ma 228,124,36
Rt 30,21,18432
s_lshl1_add_u32 s28, s36, s84
Ma 228,120,32
buffer_load_dwordx4 v27, s[12:15], s44 offen offset:1024 lds
Ma 236,120,40
Rt 32,22,0
v_add_u32 v2, s28, v1
Ma 236,124,44
v_add_u32 v3, 0x1000, v2
s_lshl_b32 s28, s34, 13
)" R"(
Ma 232,116,44
Rt 34,22,2048
s_xor_b32 s28, s28, s84
Ma 232,112,40
s_add_u32 s44, s43, 12928
buffer_load_dwordx4 v26, s[12:15], s44 offen offset:2048 lds
Mv 204,104,40
Rt 36,22,16384
s_and_b32 s28, s28, 0x2000
Mv 204,108,44
s_xor_b32 s73, s73, s28
v_and_b32 v11, 0x70, v1
Mv 200,100,44
Rt 38,22,18432
v_lshl_or_b32 v10, v10, 13, v11
Mv 200,96,40
buffer_load_dwordx4 v27, s[12:15], s44 offen offset:3072 lds
Mv 208,96,48
Rt 40,23,0
v_lshlrev_b32 v11, 1, v0
Mv 208,100,52
v_and_b32 v11, 32, v11
v_xor_b32 v10, v10, v11
Mv 212,108,52
Rt 42,23,2048
s_and_b32 s28, s33, 1
Mv 212,104,48
s_lshl_b32 s28, s28, 6
v_xor_b32 v8, s28, v10
Ma 240,112,48
Rt 44,23,16384
v_xor_b32 v9, 64, v8
Ma 240,116,52
v_add_u32 v9, 0xfc00, v9
v_add_u32 v10, 0x1f800, v8
Ma 244,124,52
Rt 46,23,18432
v_add_u32 v11, 0x1f800, v9
Ma 244,120,48
Ma 252,120,56
Rt 48,24,0
Ma 252,124,60
Ma 248,116,60
Rt 50,24,2048
Ma 248,112,56
Mv 220,104,56
Rt 52,24,16384
Mv 220,108,60
Mv 216,100,60
Rt 54,24,18432
Mv 216,96,56
Mv 32,64,0
Rt 56,25,0
Mv 32,68,4
Rt 58,25,2048
v_and_b32 v5, 7, v0
Mv 36,76,4
Rt 60,25,16384
v_lshlrev_b32 v5, 7, v5
Mv 36,72,0
Rt 62,25,18432
s_waitcnt vmcnt(13)
Mv 96,80,0
Rb 160,28,32768
)" R"(
v_mov_b32 v6, s3
Mv 96,84,4
Rb 164,28,34816
Mv 100,92,4
Rb 168,29,32768
Mv 100,88,0
Mv 108,88,8
Rb 172,29,34816
Mv 108,92,12
Mv 104,84,12
Mv 104,80,8
Mv 44,72,8
Mv 44,76,12
Mv 40,68,12
Mv 40,64,8
s_waitcnt lgkmcnt(4)
s_barrier
Mv 48,64,16
Rb 96,16,16384
Mv 48,68,20
Mv 52,76,20
s_add_u32 m0, s41, 65536
s_add_u32 s92, s42, 0x80
Mv 52,72,16
buffer_load_dwordx4 v8, s[16:19], s92 offen offset:0 lds
Mv 112,80,16
Rb 100,17,16384
Mv 112,84,20
Mv 116,92,20
Mv 116,88,16
buffer_load_dwordx4 v9, s[16:19], s92 offen offset:1024 lds
.LzH32:
s_cbranch_execz .LzH38
Mv 124,88,24
Rb 104,16,18432
Mv 124,92,28
Mv 120,84,28
Mv 120,80,24
buffer_load_dwordx4 v10, s[16:19], s92 offen offset:2048 lds
Mv 60,72,24
Rb 108,17,18432
Mv 60,76,28
Mv 56,68,28
Mv 56,64,24
buffer_load_dwordx4 v11, s[16:19], s92 offen offset:3072 lds
Mv 64,64,32
Rb 112,16,24576
Mv 64,68,36
Mv 68,76,36
s_add_u32 m0, s41, 69632
s_add_u32 s92, s42, 0x40080
Mv 68,72,32
buffer_load_dwordx4 v8, s[16:19], s92 offen offset:0 lds
Mv 128,80,32
Rb 116,17,24576
Mv 128,84,36
Mv 132,92,36
Mv 132,88,32
buffer_load_dwordx4 v9, s[16:19], s92 offen offset:1024 lds
Mv 140,88,40
Rb 120,16,26624
Mv 140,92,44
Mv 136,84,44
Mv 136,80,40
buffer_load_dwordx4 v10, s[16:19], s92 offen offset:2048 lds
)" R"(
Mv 76,72,40
Rb 124,17,26624
Mv 76,76,44
Mv 72,68,44
Mv 72,64,40
s_waitcnt vmcnt(7)
Mv 80,64,48
Rb 176,28,36864
Mv 80,68,52
Mv 84,76,52
Rb 180,28,38912
Mv 84,72,48
Mv 144,80,48
Rb 184,29,36864
Mv 144,84,52
Mv 148,92,52
Rb 188,29,38912
Mv 148,88,48
Mv 156,88,56
Mv 156,92,60
buffer_load_dword v4, v7, s[20:23], s52 offen offset:0 sc0 sc1
Mv 152,84,60
Mv 152,80,56
Mv 92,72,56
Mv 92,76,60
Mv 88,68,60
Mv 88,64,56
s_waitcnt lgkmcnt(0)
s_barrier
Mv 160,96,0
Rb 64,16,32768
Mv 160,100,4
Rb 68,17,32768
s_add_u32 s92, s42, 0x0
Mv 164,108,4
Rb 72,16,34816
s_add_u32 m0, s41, 131072
Mv 164,104,0
buffer_load_dwordx4 v8, s[16:19], s92 offen offset:0 lds
Mv 224,112,0
Rb 76,17,34816
Mv 224,116,4
Rb 80,16,40960
Mv 228,124,4
Rb 84,17,40960
Mv 228,120,0
Rb 88,16,43008
buffer_load_dwordx4 v9, s[16:19], s92 offen offset:1024 lds
Mv 236,120,8
Rb 92,17,43008
Rt 0,18,32768
Mv 236,124,12
Mv 232,116,12
Rt 2,18,34816
Mv 232,112,8
buffer_load_dwordx4 v10, s[16:19], s92 offen offset:2048 lds
Mv 172,104,8
Rt 4,18,49152
Mv 172,108,12
Mv 168,100,12
Rt 6,18,51200
Mv 168,96,8
buffer_load_dwordx4 v11, s[16:19], s92 offen offset:3072 lds
Mv 176,96,16
Rt 8,19,32768
)" R"(
Mv 176,100,20
s_add_u32 s92, s42, 0x40000
Mv 180,108,20
Rt 10,19,34816
s_add_u32 m0, s41, 135168
Mv 180,104,16
buffer_load_dwordx4 v8, s[16:19], s92 offen offset:0 lds
Mv 240,112,16
Rt 12,19,49152
Mv 240,116,20
Mv 244,124,20
Rt 14,19,51200
Mv 244,120,16
buffer_load_dwordx4 v9, s[16:19], s92 offen offset:1024 lds
Mv 252,120,24
Rt 16,20,32768
Mv 252,124,28
Mv 248,116,28
Rt 18,20,34816
Mv 248,112,24
buffer_load_dwordx4 v10, s[16:19], s92 offen offset:2048 lds
Mv 188,104,24
Rt 20,20,49152
Mv 188,108,28
s_add_u32 s92, s42, 0x1c0000
Mv 184,100,28
Rt 22,20,51200
s_add_u32 m0, s41, 0
Mv 184,96,24
buffer_load_dwordx4 v8, s[16:19], s92 offen offset:0 lds
Mv 192,96,32
Rt 24,21,32768
Mv 192,100,36
Mv 196,108,36
Rt 26,21,34816
Mv 196,104,32
buffer_load_dwordx4 v9, s[16:19], s92 offen offset:1024 lds
Ma 224,112,32
Rt 28,21,49152
Ma 224,116,36
Ma 228,124,36
Rt 30,21,51200
Ma 228,120,32
buffer_load_dwordx4 v10, s[16:19], s92 offen offset:2048 lds
Ma 236,120,40
Rt 32,22,32768
Ma 236,124,44
Ma 232,116,44
Rt 34,22,34816
Ma 232,112,40
buffer_load_dwordx4 v11, s[16:19], s92 offen offset:3072 lds
Mv 204,104,40
Rt 36,22,49152
Mv 204,108,44
s_add_u32 s92, s42, 0x200000
Mv 200,100,44
Rt 38,22,51200
s_add_u32 m0, s41, 4096
Mv 200,96,40
buffer_load_dwordx4 v8, s[16:19], s92 offen offset:0 lds
Mv 208,96,48
Rt 40,23,32768
Mv 208,100,52
Mv 212,108,52
)" R"(
Rt 42,23,34816
Mv 212,104,48
buffer_load_dwordx4 v9, s[16:19], s92 offen offset:1024 lds
Ma 240,112,48
Rt 44,23,49152
Ma 240,116,52
Ma 244,124,52
Rt 46,23,51200
Ma 244,120,48
buffer_load_dwordx4 v10, s[16:19], s92 offen offset:2048 lds
Ma 252,120,56
Rt 48,24,32768
Ma 252,124,60
Ma 248,116,60
Rt 50,24,34816
Ma 248,112,56
Mv 220,104,56
Rt 52,24,49152
Mv 220,108,60
Mv 216,100,60
Rt 54,24,51200
s_waitcnt vmcnt(14)
v_cmp_eq_u32_e32 vcc, s3, v4
s_andn2_b64 s[28:29], 1, vcc
s_cbranch_scc1 .Lzsp4
.Lzok4:
Mv 216,96,56
Mv 32,64,0
Rt 56,25,32768
Mv 32,68,4
Rt 58,25,34816
Mv 36,76,4
Rt 60,25,49152
Mv 36,72,0
Rt 62,25,51200
Mv 44,72,8
Rb 96,16,49152
Mv 44,76,12
Rb 100,17,49152
Mv 40,68,12
Rb 104,16,51200
Mv 40,64,8
Rb 108,17,51200
s_waitcnt lgkmcnt(12)
Mv 48,64,16
Rb 112,16,57344
Mv 48,68,20
Rb 116,17,57344
Mv 52,76,20
Rb 120,16,59392
Mv 52,72,16
Rb 124,17,59392
Mv 60,72,24
Cv 32,32,33
Cv 33,34,35
Mv 60,76,28
v_bfe_u32 v12, v0, 3, 2
v_lshlrev_b32 v12, 5, v12
Mv 56,68,28
v_and_b32 v4, 3, v0
v_lshl_or_b32 v12, v4, 3, v12
Mv 56,64,24
v_bfe_u32 v4, v0, 2, 2
v_lshl_or_b32 v12, v4, 7, v12
)" R"(
Mv 64,64,32
v_lshrrev_b32 v4, 4, v0
v_lshl_or_b32 v12, v4, 10, v12
Mv 64,68,36
v_xor_b32 v13, 32, v12
v_xor_b32 v14, 64, v12
Mv 68,76,36
v_xor_b32 v15, 96, v12
v_add_u32 v20, 0x20000, v12
Mv 68,72,32
v_add_u32 v21, 0x20000, v13
v_add_u32 v22, 0x20000, v14
Mv 76,72,40
v_add_u32 v23, 0x20000, v15
v_add_u32 v16, 0x10000, v12
Mv 76,76,44
v_add_u32 v17, 0x10000, v13
v_add_u32 v18, 0x10000, v14
Mv 72,68,44
v_add_u32 v19, 0x10000, v15
Cv 34,36,37
Mv 72,64,40
s_waitcnt lgkmcnt(0)
s_barrier
Mv 80,64,48
Cv 35,38,39
Cv 40,40,41
Mv 80,68,52
Cv 41,42,43
Cv 42,44,45
Mv 84,76,52
s_add_u32 m0, s36, 98304
s_add_u32 s44, s43, 768
Mv 84,72,48
buffer_load_dwordx4 v26, s[12:15], s44 offen offset:0 lds
Mv 92,72,56
Cv 43,46,47
Cv 48,48,49
Mv 92,76,60
Cv 49,50,51
Cv 50,52,53
Mv 88,68,60
Cv 51,54,55
Cv 56,56,57
Mv 88,64,56
Cv 57,58,59
Cv 58,60,61
Mv 96,80,0
Cv 59,62,63
Cv 64,64,65
Mv 96,84,4
Cv 65,66,67
Cv 66,68,69
Mv 100,92,4
Cv 67,70,71
Cv 72,72,73
Mv 100,88,0
buffer_load_dwordx4 v27, s[12:15], s44 offen offset:1024 lds
Mv 108,88,8
Cv 73,74,75
Cv 74,76,77
Mv 108,92,12
Cv 75,78,79
Cv 80,80,81
Mv 104,84,12
)" R"(
Cv 81,82,83
Cv 82,84,85
Mv 104,80,8
buffer_store_dwordx4 v[32:35], v2, s[20:23], s45 offen offset:0 sc0 sc1
Mv 112,80,16
Cv 83,86,87
Cv 88,88,89
Mv 112,84,20
Cv 89,90,91
Cv 90,92,93
Mv 116,92,20
Cv 91,94,95
Cv 96,96,97
Mv 116,88,16
s_add_u32 s44, s43, 13056
buffer_load_dwordx4 v26, s[12:15], s44 offen offset:2048 lds
Mv 124,88,24
Cv 97,98,99
Cv 98,100,101
Mv 124,92,28
Cv 99,102,103
Cv 104,104,105
Mv 120,84,28
Cv 105,106,107
Cv 106,108,109
Mv 120,80,24
buffer_store_dwordx4 v[40:43], v2, s[20:23], s45 offen offset:1024 sc0 sc1
Mv 128,80,32
Cv 107,110,111
Cv 112,112,113
Mv 128,84,36
Cv 113,114,115
Cv 114,116,117
Mv 132,92,36
Cv 115,118,119
Mv 132,88,32
buffer_load_dwordx4 v27, s[12:15], s44 offen offset:3072 lds
Mv 140,88,40
Cv 120,120,121
Cv 121,122,123
Mv 140,92,44
Cv 122,124,125
Cv 123,126,127
Mv 136,84,44
Mv 136,80,40
buffer_store_dwordx4 v[48:51], v2, s[20:23], s45 offen offset:2048 sc0 sc1
Mv 144,80,48
Cv 128,128,129
Cv 129,130,131
Mv 144,84,52
Cv 130,132,133
Cv 131,134,135
Mv 148,92,52
s_add_u32 m0, s41, 32768
s_add_u32 s92, s42, 0x1c0080
Mv 148,88,48
buffer_load_dwordx4 v8, s[16:19], s92 offen offset:0 lds
Mv 156,88,56
Cv 136,136,137
Cv 137,138,139
Mv 156,92,60
Cv 138,140,141
Cv 139,142,143
Mv 152,84,60
)" R"(
Mv 152,80,56
buffer_store_dwordx4 v[56:59], v2, s[20:23], s45 offen offset:3072 sc0 sc1
Mv 160,96,0
Cv 144,144,145
Cv 145,146,147
Mv 160,100,4
Cv 146,148,149
Cv 147,150,151
Mv 164,108,4
Mv 164,104,0
buffer_load_dwordx4 v9, s[16:19], s92 offen offset:1024 lds
Mv 224,112,0
Cv 152,152,153
Cv 153,154,155
Mv 224,116,4
Cv 154,156,157
Cv 155,158,159
Mv 228,124,4
Mv 228,120,0
buffer_store_dwordx4 v[64:67], v3, s[20:23], s45 offen offset:0 sc0 sc1
Mv 236,120,8
Mv 236,124,12
Mv 232,116,12
Mv 232,112,8
buffer_load_dwordx4 v10, s[16:19], s92 offen offset:2048 lds
Mv 172,104,8
Mv 172,108,12
Mv 168,100,12
Mv 168,96,8
buffer_store_dwordx4 v[72:75], v3, s[20:23], s45 offen offset:1024 sc0 sc1
Mv 176,96,16
Mv 176,100,20
Mv 180,108,20
Mv 180,104,16
buffer_load_dwordx4 v11, s[16:19], s92 offen offset:3072 lds
Mv 240,112,16
Mv 240,116,20
Mv 244,124,20
Mv 244,120,16
buffer_store_dwordx4 v[80:83], v3, s[20:23], s45 offen offset:2048 sc0 sc1
Mv 252,120,24
Mv 252,124,28
Mv 248,116,28
s_add_u32 m0, s41, 36864
s_add_u32 s92, s42, 0x200080
Mv 248,112,24
buffer_load_dwordx4 v8, s[16:19], s92 offen offset:0 lds
Mv 188,104,24
Mv 188,108,28
Mv 184,100,28
Mv 184,96,24
buffer_store_dwordx4 v[88:91], v3, s[20:23], s45 offen offset:3072 sc0 sc1
Mv 192,96,32
Mv 192,100,36
Mv 196,108,36
Mv 196,104,32
buffer_load_dwordx4 v9, s[16:19], s92 offen offset:1024 lds
Ma 224,112,32
Ma 224,116,36
Ma 228,124,36
Ma 228,120,32
s_waitcnt vmcnt(18)
s_barrier
Ma 236,120,40
Ma 236,124,44
)" R"(
Ma 232,116,44
Ma 232,112,40
buffer_load_dwordx4 v10, s[16:19], s92 offen offset:2048 lds
Mv 204,104,40
Mv 204,108,44
Mv 200,100,44
Mv 200,96,40
Mv 208,96,48
Rt 16,20,0
Mv 208,100,52
Rt 18,20,512
Mv 212,108,52
Rt 20,20,4096
Mv 212,104,48
Rt 22,20,4608
Ma 240,112,48
Rt 24,21,0
Ma 240,116,52
Rt 26,21,512
Ma 244,124,52
Rt 28,21,4096
Ma 244,120,48
Rt 30,21,4608
Ma 252,120,56
Rt 32,22,0
Ma 252,124,60
Rt 34,22,512
Ma 248,116,60
Rt 36,22,4096
Ma 248,112,56
Rt 38,22,4608
Mv 220,104,56
Rt 40,23,0
Mv 220,108,60
Rt 42,23,512
Mv 216,100,60
Rt 44,23,4096
Mv 216,96,56
Rt 46,23,4608
s_waitcnt vmcnt(13) lgkmcnt(8)
.LzH33:
s_cbranch_execz .LzH41
.LzQA:
s_cmp_eq_u32 s2, 112
s_cbranch_scc1 .LzT2S
.LzT2E:
Mz 32,16,192
Rt 0,20,8192
Mv 32,20,200
Rt 2,20,8704
Rb 48,28,0
Mz 40,20,204
Rt 4,20,12288
buffer_store_dwordx4 v[96:99], v2, s[20:23], s46 offen offset:0 sc0 sc1
Mv 40,16,196
Rt 6,20,12800
Rb 52,28,2048
Mz 44,24,196
Rt 8,21,8192
Cv 224,224,225
Mv 44,28,204
Rt 10,21,8704
Rb 56,29,0
buffer_load_dwordx4 a[64:67], v2, s[20:23], s51 offen offset:0 sc0 sc1
)" R"(
Mz 36,28,200
Rt 12,21,12288
Cv 225,226,227
Mv 36,24,192
Rt 14,21,12800
Rb 60,29,2048
s_waitcnt lgkmcnt(12)
Mz 48,32,192
Rt 16,22,8192
Cv 226,228,229
Mv 48,36,200
Rt 18,22,8704
Cv 227,230,231
Mz 56,36,204
Rt 20,22,12288
buffer_load_dwordx4 a[68:71], v2, s[20:23], s51 offen offset:1024 sc0 sc1
Mv 56,32,196
Rt 22,22,12800
Cv 232,232,233
Mz 60,40,196
Rt 24,23,8192
buffer_store_dwordx4 v[104:107], v2, s[20:23], s46 offen offset:1024 sc0 sc1
Mv 60,44,204
Rt 26,23,8704
Cv 233,234,235
Mz 52,44,200
Rt 28,23,12288
Cv 234,236,237
Mv 52,40,192
Rt 30,23,12800
buffer_load_dwordx4 a[72:75], v2, s[20:23], s51 offen offset:2048 sc0 sc1
s_waitcnt lgkmcnt(9)
Mv 32,0,208
Rt 32,20,16384
Cv 235,238,239
Mv 32,4,216
Rt 34,20,16896
Cv 240,240,241
Mv 40,4,220
Rt 36,20,20480
Cv 241,242,243
Cv 242,244,245
Mv 40,0,212
Rt 38,20,20992
Cv 243,246,247
.LzH34:
s_cbranch_execz .LzH42
Mv 44,8,212
Rt 40,20,24576
buffer_load_dwordx4 a[76:79], v2, s[20:23], s51 offen offset:3072 sc0 sc1
Mv 44,12,220
Rt 42,20,25088
Cv 248,248,249
Mv 36,12,216
Rt 44,21,16384
buffer_store_dwordx4 v[112:115], v2, s[20:23], s46 offen offset:2048 sc0 sc1
Mv 36,8,208
Rt 46,21,16896
s_waitcnt lgkmcnt(8)
Mv 48,16,208
Rt 0,21,20480
Cv 249,250,251
Mv 48,20,216
Rt 2,21,20992
)" R"(
buffer_load_dwordx4 a[80:83], v3, s[20:23], s51 offen offset:0 sc0 sc1
Mv 56,20,220
Rt 4,21,24576
Cv 250,252,253
Cv 251,254,255
Mv 56,16,212
Rt 6,21,25088
v_accvgpr_read_b32 v228, a224
Mv 60,24,212
Rt 8,22,16384
v_accvgpr_read_b32 v24, a225
Mv 60,28,220
Rt 10,22,16896
Cv 228,228,24
Mv 52,28,216
Rt 12,22,20480
buffer_load_dwordx4 a[84:87], v3, s[20:23], s51 offen offset:1024 sc0 sc1
Mv 52,24,208
Rt 14,22,20992
s_waitcnt lgkmcnt(6)
Mv 32,32,128
Rt 16,22,24576
buffer_store_dwordx4 v[120:123], v2, s[20:23], s46 offen offset:3072 sc0 sc1
Mv 32,36,136
Rt 18,22,25088
v_accvgpr_read_b32 v229, a226
Mv 32,40,144
Rt 20,23,16384
v_accvgpr_read_b32 v25, a227
Cv 229,229,25
Mv 40,40,148
Rt 22,23,16896
buffer_load_dwordx4 a[88:91], v3, s[20:23], s51 offen offset:2048 sc0 sc1
Mv 40,36,140
Rt 24,23,20480
v_accvgpr_read_b32 v230, a228
Mv 40,32,132
Rt 26,23,20992
v_accvgpr_read_b32 v24, a229
Mv 44,44,132
Rt 28,23,24576
Cv 230,230,24
Mv 44,0,140
Rt 30,23,25088
s_waitcnt lgkmcnt(6)
Mv 44,4,148
Rt 32,12,0
buffer_load_dwordx4 a[92:95], v3, s[20:23], s51 offen offset:3072 sc0 sc1
Mv 36,4,144
Rt 34,12,512
v_accvgpr_read_b32 v231, a230
Mv 36,0,136
Rt 36,12,4096
buffer_store_dwordx4 v[128:131], v3, s[20:23], s46 offen offset:0 sc0 sc1
Mv 36,44,128
Rt 38,12,4608
v_accvgpr_read_b32 v25, a231
Mv 48,8,128
Rt 40,13,0
Cv 231,231,25
Mv 48,12,136
Rt 42,13,512
v_accvgpr_read_b32 v236, a232
Mv 48,16,144
)" R"(
Rt 44,13,4096
v_accvgpr_read_b32 v24, a233
Mv 56,16,148
Rt 46,13,4608
s_waitcnt vmcnt(15) lgkmcnt(8)
s_barrier
buffer_store_dword v6, v7, s[20:23], s49 offen offset:0 sc0 sc1
Mv 56,12,140
Rt 0,14,0
Cv 236,236,24
Mv 56,8,132
Rt 2,14,512
s_add_u32 s92, s42, 0x100
Mv 60,20,132
Rt 4,14,4096
s_add_u32 m0, s41, 98304
v_accvgpr_read_b32 v237, a234
Mv 60,24,140
Rt 6,14,4608
buffer_load_dwordx4 v8, s[16:19], s92 offen offset:0 lds
Mv 60,28,148
Rt 8,15,0
v_accvgpr_read_b32 v25, a235
Mv 52,28,144
Rt 10,15,512
Cv 237,237,25
Mv 52,24,136
Rt 12,15,4096
v_accvgpr_read_b32 v238, a236
Mv 52,20,128
Rt 14,15,4608
buffer_load_dwordx4 v9, s[16:19], s92 offen offset:1024 lds
s_waitcnt lgkmcnt(8)
Mv 32,32,152
Rt 16,12,8192
v_accvgpr_read_b32 v24, a237
Mv 32,36,160
Rt 18,12,8704
buffer_store_dwordx4 v[136:139], v3, s[20:23], s46 offen offset:1024 sc0 sc1
Mv 40,36,164
Rt 20,12,12288
Cv 238,238,24
v_accvgpr_read_b32 v239, a238
Mv 40,32,156
Rt 22,12,12800
buffer_load_dwordx4 v10, s[16:19], s92 offen offset:2048 lds
Mv 44,40,156
Rt 24,13,8192
v_accvgpr_read_b32 v25, a239
Mv 44,44,164
Rt 26,13,8704
Cv 239,239,25
Mv 36,44,160
Rt 28,13,12288
v_accvgpr_read_b32 v244, a240
Mv 36,40,152
Rt 30,13,12800
buffer_load_dwordx4 v11, s[16:19], s92 offen offset:3072 lds
s_waitcnt lgkmcnt(8)
Mv 48,0,152
Rt 32,14,8192
v_accvgpr_read_b32 v24, a241
Mv 48,4,160
Rt 34,14,8704
s_add_u32 s92, s42, 0x40100
)" R"(
Mv 56,4,164
Rt 36,14,12288
s_add_u32 m0, s41, 102400
Cv 244,244,24
Mv 56,0,156
Rt 38,14,12800
buffer_load_dwordx4 v8, s[16:19], s92 offen offset:0 lds
Mv 60,8,156
Rt 40,15,8192
v_accvgpr_read_b32 v245, a242
Mv 60,12,164
Rt 42,15,8704
v_accvgpr_read_b32 v25, a243
Mv 52,12,160
Rt 44,15,12288
Cv 245,245,25
Mv 52,8,152
Rt 46,15,12800
buffer_load_dwordx4 v9, s[16:19], s92 offen offset:1024 lds
s_waitcnt lgkmcnt(8)
Mv 32,16,168
Rt 0,12,16384
v_accvgpr_read_b32 v246, a244
Mv 32,20,176
Rt 2,12,16896
buffer_store_dwordx4 v[144:147], v3, s[20:23], s46 offen offset:2048 sc0 sc1
Mv 40,20,180
Rt 4,12,20480
v_accvgpr_read_b32 v24, a245
Cv 246,246,24
Mv 40,16,172
Rt 6,12,20992
buffer_load_dwordx4 v10, s[16:19], s92 offen offset:2048 lds
Mv 44,24,172
Rt 8,12,24576
v_accvgpr_read_b32 v247, a246
Mv 44,28,180
Rt 10,12,25088
s_add_u32 s92, s42, 0x1c0100
Mv 36,28,176
Rt 12,13,16384
s_add_u32 m0, s41, 131072
Mv 36,24,168
Rt 14,13,16896
buffer_load_dwordx4 v8, s[16:19], s92 offen offset:0 lds
s_waitcnt lgkmcnt(8)
Mv 48,32,168
Rt 16,13,20480
v_accvgpr_read_b32 v25, a247
Mv 48,36,176
Rt 18,13,20992
Cv 247,247,25
Mv 56,36,180
Rt 20,13,24576
v_accvgpr_read_b32 v252, a248
v_accvgpr_read_b32 v24, a249
Mv 56,32,172
Rt 22,13,25088
buffer_load_dwordx4 v9, s[16:19], s92 offen offset:1024 lds
Mv 60,40,172
Rt 24,14,16384
Cv 252,252,24
Mv 60,44,180
Rt 26,14,16896
)" R"(
v_accvgpr_read_b32 v253, a250
Mv 52,44,176
Rt 28,14,20480
v_accvgpr_read_b32 v25, a251
Mv 52,40,168
Rt 30,14,20992
buffer_load_dwordx4 v10, s[16:19], s92 offen offset:2048 lds
s_waitcnt lgkmcnt(6)
Mv 32,0,184
Rt 32,14,24576
Cv 253,253,25
Mv 32,4,48
Rt 34,14,25088
buffer_store_dwordx4 v[152:155], v3, s[20:23], s46 offen offset:3072 sc0 sc1
Mv 32,8,56
Rt 36,15,16384
v_accvgpr_read_b32 v254, a252
v_accvgpr_read_b32 v24, a253
Mv 40,8,60
Rt 38,15,16896
buffer_load_dwordx4 v11, s[16:19], s92 offen offset:3072 lds
Mv 40,4,52
Rt 40,15,20480
Cv 254,254,24
Mv 40,0,188
Rt 42,15,20992
s_add_u32 s92, s42, 0x200100
Mv 44,12,188
Rt 44,15,24576
s_add_u32 m0, s41, 135168
Mv 44,16,52
Rt 46,15,25088
buffer_load_dwordx4 v8, s[16:19], s92 offen offset:0 lds
s_waitcnt lgkmcnt(6)
Mv 44,20,60
Rt 0,16,0
v_accvgpr_read_b32 v255, a254
Mv 36,20,56
Rt 2,16,512
v_accvgpr_read_b32 v25, a255
Mv 36,16,48
Rt 4,16,4096
Cv 255,255,25
Cv 160,160,161
Mv 36,12,184
Rt 6,16,4608
buffer_load_dwordx4 v9, s[16:19], s92 offen offset:1024 lds
Mv 48,24,184
Rt 8,17,0
Cv 161,162,163
Mv 48,28,48
Rt 10,17,512
Cv 162,164,165
Mv 48,32,56
Rt 12,17,4096
Cv 163,166,167
Mv 56,32,60
Rt 14,17,4608
buffer_load_dwordx4 v10, s[16:19], s92 offen offset:2048 lds
s_waitcnt vmcnt(31) lgkmcnt(8)
s_barrier
Mv 56,28,52
Rt 16,18,0
Cv 168,168,169
Mv 56,24,188
)" R"(
Rt 18,18,512
buffer_store_dwordx4 v[160:163], v2, s[20:23], s47 offen offset:0 sc0 sc1
Mv 60,36,188
Rt 20,18,4096
Cv 169,170,171
Cv 170,172,173
Mv 60,40,52
Rt 22,18,4608
s_add_u32 s92, s42, 0x180
Mv 60,44,60
Rt 24,19,0
s_add_u32 m0, s41, 0
Mv 52,44,56
Rt 26,19,512
buffer_load_dwordx4 v8, s[16:19], s92 offen offset:0 lds
Mv 52,40,48
Rt 28,19,4096
Cv 171,174,175
Mv 52,36,184
Rt 30,19,4608
s_waitcnt lgkmcnt(8)
Mz 64,0,192
Rt 32,16,8192
Cv 176,176,177
Mv 64,4,200
Rt 34,16,8704
s_add_u32 s51, s51, 0x10000
buffer_load_dwordx4 a[96:99], v2, s[20:23], s51 offen offset:0 sc0 sc1
Mz 72,4,204
Rt 36,16,12288
Cv 177,178,179
Cv 178,180,181
Mv 72,0,196
Rt 38,16,12800
Cv 179,182,183
Mz 76,8,196
Rt 40,17,8192
Cv 184,184,185
Mv 76,12,204
Rt 42,17,8704
buffer_load_dwordx4 v9, s[16:19], s92 offen offset:1024 lds
Mz 68,12,200
Rt 44,17,12288
Cv 185,186,187
v_mul_f32_e32 v32, s54, v32
Mv 68,8,192
Rt 46,17,12800
s_waitcnt lgkmcnt(8)
Mz 80,16,192
Rt 0,18,8192
Cv 186,188,189
Mv 80,20,200
Rt 2,18,8704
buffer_store_dwordx4 v[168:171], v2, s[20:23], s47 offen offset:1024 sc0 sc1
Mz 88,20,204
Rt 4,18,12288
Cv 187,190,191
Cv 192,192,193
Mv 88,16,196
Rt 6,18,12800
Cv 193,194,195
v_mul_f32_e32 v33, s54, v33
Mz 92,24,196
Rt 8,19,8192
)" R"(
Cv 194,196,197
Mv 92,28,204
Rt 10,19,8704
buffer_load_dwordx4 v10, s[16:19], s92 offen offset:2048 lds
Mz 84,28,200
Rt 12,19,12288
Cv 195,198,199
Cv 32,32,33
Mv 84,24,192
Rt 14,19,12800
s_waitcnt lgkmcnt(8)
Mv 64,32,208
Rt 16,16,16384
Cv 200,200,201
Mv 64,36,216
Rt 18,16,16896
buffer_load_dwordx4 a[100:103], v2, s[20:23], s51 offen offset:1024 sc0 sc1
Mv 72,36,220
Rt 20,16,20480
Cv 201,202,203
Cv 202,204,205
Mv 72,32,212
Rt 22,16,20992
Cv 203,206,207
v_accvgpr_write_b32 a224, v32
Mv 76,40,212
Rt 24,16,24576
Cv 208,208,209
Mv 76,44,220
Rt 26,16,25088
buffer_load_dwordx4 v11, s[16:19], s92 offen offset:3072 lds
Mv 68,44,216
Rt 28,17,16384
Cv 209,210,211
v_mul_f32_e32 v34, s54, v34
Mv 68,40,208
Rt 30,17,16896
s_waitcnt lgkmcnt(8)
Mv 80,0,208
Rt 32,17,20480
Cv 210,212,213
Mv 80,4,216
Rt 34,17,20992
buffer_store_dwordx4 v[176:179], v2, s[20:23], s47 offen offset:2048 sc0 sc1
Mv 88,4,220
Rt 36,17,24576
Cv 211,214,215
Cv 216,216,217
Mv 88,0,212
Rt 38,17,25088
s_add_u32 s92, s42, 0x40180
v_mul_f32_e32 v35, s54, v35
Mv 92,8,212
Rt 40,18,16384
s_add_u32 m0, s41, 4096
Mv 92,12,220
Rt 42,18,16896
buffer_load_dwordx4 v8, s[16:19], s92 offen offset:0 lds
Mv 84,12,216
Rt 44,18,20480
Cv 217,218,219
Cv 34,34,35
Mv 84,8,208
Rt 46,18,20992
)" R"(
s_waitcnt lgkmcnt(6)
Mv 64,16,128
Rt 0,18,24576
Cv 218,220,221
Mv 64,20,136
Rt 2,18,25088
buffer_load_dwordx4 a[104:107], v2, s[20:23], s51 offen offset:2048 sc0 sc1
Mv 64,24,144
Rt 4,19,16384
Cv 219,222,223
Mv 72,24,148
Rt 6,19,16896
v_accvgpr_write_b32 a225, v34
v_mul_f32_e32 v36, s54, v36
Mv 72,20,140
Rt 8,19,20480
v_mul_f32_e32 v37, s54, v37
Mv 72,16,132
Rt 10,19,20992
buffer_load_dwordx4 v9, s[16:19], s92 offen offset:1024 lds
Mv 76,28,132
Rt 12,19,24576
Cv 36,36,37
v_accvgpr_write_b32 a226, v36
Mv 76,32,140
Rt 14,19,25088
s_waitcnt lgkmcnt(6)
Mv 76,36,148
Rt 16,12,32768
buffer_store_dwordx4 v[184:187], v2, s[20:23], s47 offen offset:3072 sc0 sc1
Mv 68,36,144
Rt 18,12,33280
v_mul_f32_e32 v38, s54, v38
v_mul_f32_e32 v39, s54, v39
Mv 68,32,136
Rt 20,12,36864
Cv 38,38,39
Mv 68,28,128
Rt 22,12,37376
buffer_load_dwordx4 a[108:111], v2, s[20:23], s51 offen offset:3072 sc0 sc1
Mv 80,40,128
Rt 24,13,32768
v_accvgpr_write_b32 a227, v38
Mv 80,44,136
Rt 26,13,33280
buffer_load_dwordx4 v10, s[16:19], s92 offen offset:2048 lds
Mv 80,0,144
Rt 28,13,36864
v_mul_f32_e32 v40, s54, v40
v_mul_f32_e32 v41, s54, v41
Mv 88,0,148
Rt 30,13,37376
s_waitcnt vmcnt(19) lgkmcnt(8)
s_barrier
buffer_store_dword v6, v7, s[20:23], s49 offen offset:1024 sc0 sc1
Mv 88,44,140
Rt 32,14,32768
buffer_load_dword v4, v5, s[20:23], s50 offen offset:0 sc0 sc1
Mv 88,40,132
Rt 34,14,33280
Cv 40,40,41
v_accvgpr_write_b32 a228, v40
Mv 92,4,132
Rt 36,14,36864
v_mul_f32_e32 v42, s54, v42
)" R"(
Mv 92,8,140
Rt 38,14,37376
s_add_u32 s92, s42, 0x1c0180
v_mul_f32_e32 v43, s54, v43
Mv 92,12,148
Rt 40,15,32768
s_add_u32 m0, s41, 65536
Mv 84,12,144
Rt 42,15,33280
buffer_load_dwordx4 v8, s[16:19], s92 offen offset:0 lds
Mv 84,8,136
Rt 44,15,36864
Cv 42,42,43
v_accvgpr_write_b32 a229, v42
Mv 84,4,128
Rt 46,15,37376
s_waitcnt lgkmcnt(8)
Mv 64,16,152
Rt 0,12,40960
v_mul_f32_e32 v44, s54, v44
Mv 64,20,160
Rt 2,12,41472
buffer_load_dwordx4 a[112:115], v3, s[20:23], s51 offen offset:0 sc0 sc1
Mv 72,20,164
Rt 4,12,45056
v_mul_f32_e32 v45, s54, v45
Mv 72,16,156
Rt 6,12,45568
Cv 44,44,45
v_accvgpr_write_b32 a230, v44
Mv 76,24,156
Rt 8,13,40960
v_mul_f32_e32 v46, s54, v46
Mv 76,28,164
Rt 10,13,41472
buffer_load_dwordx4 v9, s[16:19], s92 offen offset:1024 lds
Mv 68,28,160
Rt 12,13,45056
v_mul_f32_e32 v47, s54, v47
Cv 46,46,47
Mv 68,24,152
Rt 14,13,45568
s_waitcnt lgkmcnt(8)
Mv 80,32,152
Rt 16,14,40960
v_accvgpr_write_b32 a231, v46
Mv 80,36,160
Rt 18,14,41472
buffer_store_dwordx4 v[192:195], v3, s[20:23], s47 offen offset:0 sc0 sc1
Mv 88,36,164
Rt 20,14,45056
v_mul_f32_e32 v48, s54, v48
Mv 88,32,156
Rt 22,14,45568
v_mul_f32_e32 v49, s54, v49
Cv 48,48,49
Mv 92,40,156
Rt 24,15,40960
v_accvgpr_write_b32 a232, v48
Mv 92,44,164
Rt 26,15,41472
buffer_load_dwordx4 v10, s[16:19], s92 offen offset:2048 lds
Mv 84,44,160
Rt 28,15,45056
)" R"(
v_mul_f32_e32 v50, s54, v50
v_mul_f32_e32 v51, s54, v51
.LzH35:
s_cbranch_execz .LzH43
Mv 84,40,152
Rt 30,15,45568
s_waitcnt lgkmcnt(8)
Mv 64,0,168
Rt 32,12,49152
Cv 50,50,51
Mv 64,4,176
Rt 34,12,49664
buffer_load_dwordx4 a[116:119], v3, s[20:23], s51 offen offset:1024 sc0 sc1
Mv 72,4,180
Rt 36,12,53248
v_accvgpr_write_b32 a233, v50
Mv 72,0,172
Rt 38,12,53760
v_mul_f32_e32 v52, s54, v52
v_mul_f32_e32 v53, s54, v53
Mv 76,8,172
Rt 40,12,57344
Cv 52,52,53
Mv 76,12,180
Rt 42,12,57856
buffer_load_dwordx4 v11, s[16:19], s92 offen offset:3072 lds
Mv 68,12,176
Rt 44,13,49152
v_accvgpr_write_b32 a234, v52
v_mul_f32_e32 v54, s54, v54
Mv 68,8,168
Rt 46,13,49664
s_waitcnt lgkmcnt(8)
Mv 80,16,168
Rt 0,13,53248
v_mul_f32_e32 v55, s54, v55
Mv 80,20,176
Rt 2,13,53760
buffer_store_dwordx4 v[200:203], v3, s[20:23], s47 offen offset:1024 sc0 sc1
Mv 88,20,180
Rt 4,13,57344
Cv 54,54,55
Mv 88,16,172
Rt 6,13,57856
s_add_u32 s92, s42, 0x200180
v_accvgpr_write_b32 a235, v54
Mv 92,24,172
Rt 8,14,49152
s_add_u32 m0, s41, 69632
Mv 92,28,180
Rt 10,14,49664
buffer_load_dwordx4 v8, s[16:19], s92 offen offset:0 lds
Mv 84,28,176
Rt 12,14,53248
v_mul_f32_e32 v56, s54, v56
v_mul_f32_e32 v57, s54, v57
Mv 84,24,168
Rt 14,14,53760
s_waitcnt lgkmcnt(6)
Mv 64,32,184
Rt 16,14,57344
Cv 56,56,57
Mv 64,36,48
Rt 18,14,57856
)" R"(
buffer_load_dwordx4 a[120:123], v3, s[20:23], s51 offen offset:2048 sc0 sc1
Mv 64,40,56
Rt 20,15,49152
v_accvgpr_write_b32 a236, v56
Mv 72,40,60
Rt 22,15,49664
v_mul_f32_e32 v58, s54, v58
v_mul_f32_e32 v59, s54, v59
Mv 72,36,52
Rt 24,15,53248
Cv 58,58,59
Mv 72,32,188
Rt 26,15,53760
buffer_load_dwordx4 v9, s[16:19], s92 offen offset:1024 lds
Mv 76,44,188
Rt 28,15,57344
v_accvgpr_write_b32 a237, v58
v_mul_f32_e32 v60, s54, v60
Mv 76,0,52
Rt 30,15,57856
s_waitcnt lgkmcnt(6)
Mv 76,4,60
Rt 32,16,32768
buffer_store_dwordx4 v[208:211], v3, s[20:23], s47 offen offset:2048 sc0 sc1
Mv 68,4,56
Rt 34,16,33280
v_mul_f32_e32 v61, s54, v61
Cv 60,60,61
Mv 68,0,48
Rt 36,16,36864
v_accvgpr_write_b32 a238, v60
Mv 68,44,184
Rt 38,16,37376
buffer_load_dwordx4 a[124:127], v3, s[20:23], s51 offen offset:3072 sc0 sc1
Mv 80,8,184
Rt 40,17,32768
v_mul_f32_e32 v62, s54, v62
Mv 80,12,48
Rt 42,17,33280
buffer_load_dwordx4 v10, s[16:19], s92 offen offset:2048 lds
Mv 80,16,56
Rt 44,17,36864
v_mul_f32_e32 v63, s54, v63
Cv 62,62,63
Mv 88,16,60
Rt 46,17,37376
s_waitcnt vmcnt(14) lgkmcnt(8)
s_barrier
Mv 88,12,52
Rt 0,18,32768
v_cmp_eq_u32_e32 vcc, s3, v4
s_andn2_b64 s[28:29], -1, vcc
s_cbranch_scc1 .Lzsp5
.Lzok5:
Mv 88,8,188
Rt 2,18,33280
s_add_u32 s40, s55, 0x10000
v_accvgpr_write_b32 a239, v62
Mv 92,20,188
Rt 4,18,36864
Mv 92,24,52
Rt 6,18,37376
Mv 92,28,60
Rt 8,19,32768
s_add_u32 m0, s36, 32768
)" R"(
Mv 84,28,56
Rt 10,19,33280
buffer_load_dwordx4 v2, s[20:23], s40 offen offset:0 sc1 lds
Mv 84,24,48
Rt 12,19,36864
Mv 84,20,184
Rt 14,19,37376
s_waitcnt lgkmcnt(8)
Mz 32,32,192
Rt 16,16,40960
buffer_store_dwordx4 v[216:219], v3, s[20:23], s47 offen offset:3072 sc0 sc1
Mv 32,36,200
Rt 18,16,41472
Mz 40,36,204
Rt 20,16,45056
Mv 40,32,196
Rt 22,16,45568
buffer_load_dwordx4 v2, s[20:23], s40 offen offset:1024 sc1 lds
Mz 44,40,196
Rt 24,17,40960
v_mul_f32_e32 v64, s54, v64
Mv 44,44,204
Rt 26,17,41472
v_mul_f32_e32 v65, s54, v65
Mz 36,44,200
Rt 28,17,45056
Cv 64,64,65
Mv 36,40,192
Rt 30,17,45568
s_waitcnt lgkmcnt(8)
Mz 48,0,192
Rt 32,18,40960
v_accvgpr_write_b32 a240, v64
Mv 48,4,200
Rt 34,18,41472
buffer_load_dwordx4 v2, s[20:23], s40 offen offset:2048 sc1 lds
Mz 56,4,204
Rt 36,18,45056
v_mul_f32_e32 v66, s54, v66
Mv 56,0,196
Rt 38,18,45568
v_mul_f32_e32 v67, s54, v67
Mz 60,8,196
Rt 40,19,40960
buffer_store_dwordx4 v[224:227], v2, s[20:23], s48 offen offset:0 sc0 sc1
Mv 60,12,204
Rt 42,19,41472
Cv 66,66,67
Mz 52,12,200
Rt 44,19,45056
v_accvgpr_write_b32 a241, v66
Mv 52,8,192
Rt 46,19,45568
buffer_load_dwordx4 v2, s[20:23], s40 offen offset:3072 sc1 lds
s_waitcnt lgkmcnt(8)
Mv 32,16,208
Rt 0,16,49152
v_mul_f32_e32 v68, s54, v68
Mv 32,20,216
Rt 2,16,49664
v_mul_f32_e32 v69, s54, v69
Mv 40,20,220
Rt 4,16,53248
Cv 68,68,69
)" R"(
Mv 40,16,212
Rt 6,16,53760
v_accvgpr_write_b32 a242, v68
Mv 44,24,212
Rt 8,16,57344
s_add_u32 m0, s36, 36864
Mv 44,28,220
Rt 10,16,57856
buffer_load_dwordx4 v3, s[20:23], s40 offen offset:0 sc1 lds
Mv 36,28,216
Rt 12,17,49152
v_mul_f32_e32 v70, s54, v70
Mv 36,24,208
Rt 14,17,49664
s_waitcnt lgkmcnt(8)
Mv 48,32,208
Rt 16,17,53248
buffer_store_dwordx4 v[232:235], v2, s[20:23], s48 offen offset:1024 sc0 sc1
Mv 48,36,216
Rt 18,17,53760
v_mul_f32_e32 v71, s54, v71
Mv 56,36,220
Rt 20,17,57344
Cv 70,70,71
Mv 56,32,212
Rt 22,17,57856
buffer_load_dwordx4 v3, s[20:23], s40 offen offset:1024 sc1 lds
Mv 60,40,212
Rt 24,18,49152
v_accvgpr_write_b32 a243, v70
Mv 60,44,220
Rt 26,18,49664
v_mul_f32_e32 v72, s54, v72
Mv 52,44,216
Rt 28,18,53248
v_mul_f32_e32 v73, s54, v73
Mv 52,40,208
Rt 30,18,53760
s_waitcnt lgkmcnt(6)
Mv 32,0,128
Rt 32,18,57344
Cv 72,72,73
Mv 32,4,136
Rt 34,18,57856
buffer_load_dwordx4 v3, s[20:23], s40 offen offset:2048 sc1 lds
Mv 32,8,144
Rt 36,19,49152
v_accvgpr_write_b32 a244, v72
Mv 40,8,148
Rt 38,19,49664
v_mul_f32_e32 v74, s54, v74
Mv 40,4,140
Rt 40,19,53248
buffer_store_dwordx4 v[240:243], v2, s[20:23], s48 offen offset:2048 sc0 sc1
Mv 40,0,132
Rt 42,19,53760
v_mul_f32_e32 v75, s54, v75
Mv 44,12,132
Rt 44,19,57344
Cv 74,74,75
Mv 44,16,140
Rt 46,19,57856
buffer_load_dwordx4 v3, s[20:23], s40 offen offset:3072 sc1 lds
s_waitcnt lgkmcnt(6)
)" R"(
Mv 44,20,148
Rt 0,20,0
v_accvgpr_write_b32 a245, v74
Mv 36,20,144
Rt 2,20,512
v_mul_f32_e32 v76, s54, v76
Mv 36,16,136
Rt 4,20,4096
v_mul_f32_e32 v77, s54, v77
Mv 36,12,128
Rt 6,20,4608
Cv 76,76,77
Mv 48,24,128
Rt 8,21,0
v_accvgpr_write_b32 a246, v76
Mv 48,28,136
Rt 10,21,512
v_mul_f32_e32 v78, s54, v78
Mv 48,32,144
Rt 12,21,4096
v_mul_f32_e32 v79, s54, v79
Mv 56,32,148
Rt 14,21,4608
s_waitcnt vmcnt(10) lgkmcnt(8)
s_barrier
buffer_store_dword v6, v7, s[20:23], s49 offen offset:2048 sc0 sc1
Mv 56,28,140
Rt 16,22,0
s_mov_b32 s83, 0x1508000
buffer_load_dwordx2 v[24:25], v7, s[20:23], s83 offen sc0 sc1
Mv 56,24,132
Rt 18,22,512
Cv 78,78,79
Mv 60,36,132
Rt 20,22,4096
s_add_u32 m0, s36, 98304
Mv 60,40,140
Rt 22,22,4608
buffer_load_dwordx4 v2, s[20:23], s55 offen offset:0 sc1 lds
Mv 60,44,148
Rt 24,23,0
v_accvgpr_write_b32 a247, v78
Mv 52,44,144
Rt 26,23,512
v_mul_f32_e32 v80, s54, v80
Mv 52,40,136
Rt 28,23,4096
buffer_store_dwordx4 v[248:251], v2, s[20:23], s48 offen offset:3072 sc0 sc1
Mv 52,36,128
Rt 30,23,4608
s_waitcnt lgkmcnt(8)
Mv 32,0,152
Rt 32,20,8192
v_mul_f32_e32 v81, s54, v81
Mv 32,4,160
Rt 34,20,8704
buffer_load_dwordx4 v2, s[20:23], s55 offen offset:1024 sc1 lds
Mv 40,4,164
Rt 36,20,12288
Cv 80,80,81
Mv 40,0,156
Rt 38,20,12800
v_accvgpr_write_b32 a248, v80
Mv 44,8,156
Rt 40,21,8192
)" R"(
v_mul_f32_e32 v82, s54, v82
Mv 44,12,164
Rt 42,21,8704
v_mul_f32_e32 v83, s54, v83
Mv 36,12,160
Rt 44,21,12288
Cv 82,82,83
Mv 36,8,152
Rt 46,21,12800
buffer_load_dwordx4 v2, s[20:23], s55 offen offset:2048 sc1 lds
s_waitcnt lgkmcnt(8)
Mv 48,16,152
Rt 0,22,8192
v_accvgpr_write_b32 a249, v82
Mv 48,20,160
Rt 2,22,8704
v_mul_f32_e32 v84, s54, v84
Mv 56,20,164
Rt 4,22,12288
buffer_store_dwordx4 v[228:231], v3, s[20:23], s48 offen offset:0 sc0 sc1
Mv 56,16,156
Rt 6,22,12800
v_mul_f32_e32 v85, s54, v85
Mv 60,24,156
Rt 8,23,8192
Cv 84,84,85
Mv 60,28,164
Rt 10,23,8704
buffer_load_dwordx4 v2, s[20:23], s55 offen offset:3072 sc1 lds
Mv 52,28,160
Rt 12,23,12288
v_accvgpr_write_b32 a250, v84
Mv 52,24,152
Rt 14,23,12800
s_waitcnt lgkmcnt(8)
Mv 32,32,168
Rt 16,20,16384
v_mul_f32_e32 v86, s54, v86
Mv 32,36,176
Rt 18,20,16896
v_mul_f32_e32 v87, s54, v87
Mv 40,36,180
Rt 20,20,20480
s_add_u32 m0, s36, 102400
Mv 40,32,172
Rt 22,20,20992
buffer_load_dwordx4 v3, s[20:23], s55 offen offset:0 sc1 lds
Mv 44,40,172
Rt 24,20,24576
Cv 86,86,87
Mv 44,44,180
Rt 26,20,25088
v_accvgpr_write_b32 a251, v86
Mv 36,44,176
Rt 28,21,16384
buffer_store_dwordx4 v[236:239], v3, s[20:23], s48 offen offset:1024 sc0 sc1
Mv 36,40,168
Rt 30,21,16896
s_waitcnt lgkmcnt(8)
Mv 48,0,168
Rt 32,21,20480
v_mul_f32_e32 v88, s54, v88
Mv 48,4,176
Rt 34,21,20992
)" R"(
buffer_load_dwordx4 v3, s[20:23], s55 offen offset:1024 sc1 lds
Mv 56,4,180
Rt 36,21,24576
v_mul_f32_e32 v89, s54, v89
Mv 56,0,172
Rt 38,21,25088
Cv 88,88,89
Mv 60,8,172
Rt 40,22,16384
s_add_u32 s50, s50, 1024
Mv 60,12,180
Rt 42,22,16896
s_min_u32 s50, s50, s60
Mv 52,12,176
Rt 44,22,20480
v_accvgpr_write_b32 a252, v88
Mv 52,8,168
Rt 46,22,20992
buffer_load_dwordx4 v3, s[20:23], s55 offen offset:2048 sc1 lds
s_waitcnt lgkmcnt(6)
Mv 32,16,184
Rt 0,22,24576
v_mul_f32_e32 v90, s54, v90
Mv 32,20,48
Rt 2,22,25088
v_mul_f32_e32 v91, s54, v91
Mv 32,24,56
Rt 4,23,16384
buffer_store_dwordx4 v[244:247], v3, s[20:23], s48 offen offset:2048 sc0 sc1
Mv 40,24,60
Rt 6,23,16896
Cv 90,90,91
Mv 40,20,52
Rt 8,23,20480
v_accvgpr_write_b32 a253, v90
Mv 40,16,188
Rt 10,23,20992
buffer_load_dwordx4 v3, s[20:23], s55 offen offset:3072 sc1 lds
Mv 44,28,188
Rt 12,23,24576
v_mul_f32_e32 v92, s54, v92
Mv 44,32,52
Rt 14,23,25088
s_waitcnt lgkmcnt(6)
Mv 44,36,60
Rt 16,12,0
v_mul_f32_e32 v93, s54, v93
Mv 36,36,56
Rt 18,12,512
Cv 92,92,93
Mv 36,32,48
Rt 20,12,4096
v_accvgpr_write_b32 a254, v92
Mv 36,28,184
Rt 22,12,4608
v_mul_f32_e32 v94, s54, v94
Mv 48,40,184
Rt 24,13,0
buffer_store_dwordx4 v[252:255], v3, s[20:23], s48 offen offset:3072 sc0 sc1
Mv 48,44,48
Rt 26,13,512
v_mul_f32_e32 v95, s54, v95
Mv 48,0,56
Rt 28,13,4096
)" R"(
Cv 94,94,95
Mv 56,0,60
Rt 30,13,4608
s_waitcnt vmcnt(13) lgkmcnt(8)
s_barrier
v_cmp_eq_u32_e32 vcc, s3, v25
s_andn2_b64 s[28:29], 1, vcc
s_cbranch_scc1 .Lzsp7
.Lzok7:
Mv 56,44,52
Rt 32,14,0
buffer_load_dword v4, v5, s[20:23], s50 offen offset:0 sc0 sc1
Mv 56,40,188
Rt 34,14,512
v_accvgpr_write_b32 a255, v94
Mv 60,4,188
Rt 36,14,4096
s_add_u32 m0, s36, 131072
Mv 60,8,52
Rt 38,14,4608
buffer_load_dwordx4 v2, s[20:23], s56 offen offset:0 sc1 lds
Mv 60,12,60
Rt 40,15,0
Mv 52,12,56
Rt 42,15,512
Mv 52,8,48
Rt 44,15,4096
Mv 52,4,184
Rt 46,15,4608
s_waitcnt lgkmcnt(8)
Mz 96,16,192
Rt 0,12,8192
Mv 96,20,200
Rt 2,12,8704
buffer_load_dwordx4 v2, s[20:23], s56 offen offset:1024 sc1 lds
Mz 104,20,204
Rt 4,12,12288
Mv 104,16,196
Rt 6,12,12800
Mz 108,24,196
Rt 8,13,8192
v_mul_f32_e32 v32, s54, v32
Mv 108,28,204
Rt 10,13,8704
v_mul_f32_e32 v33, s54, v33
Mz 100,28,200
Rt 12,13,12288
Cv 32,32,33
Mv 100,24,192
Rt 14,13,12800
buffer_load_dwordx4 v2, s[20:23], s56 offen offset:2048 sc1 lds
s_waitcnt lgkmcnt(8)
Mz 112,32,192
Rt 16,14,8192
v_mul_f32_e32 v34, s54, v34
Mv 112,36,200
Rt 18,14,8704
v_mul_f32_e32 v35, s54, v35
Mz 120,36,204
Rt 20,14,12288
Cv 34,34,35
Mv 120,32,196
Rt 22,14,12800
v_mul_f32_e32 v36, s54, v36
v_mul_f32_e32 v37, s54, v37
)" R"(
Mz 124,40,196
Rt 24,15,8192
Cv 36,36,37
Mv 124,44,204
Rt 26,15,8704
buffer_load_dwordx4 v2, s[20:23], s56 offen offset:3072 sc1 lds
Mz 116,44,200
Rt 28,15,12288
v_mul_f32_e32 v38, s54, v38
Mv 116,40,192
Rt 30,15,12800
s_waitcnt lgkmcnt(8)
Mv 96,0,208
Rt 32,12,16384
v_mul_f32_e32 v39, s54, v39
Mv 96,4,216
Rt 34,12,16896
v_accvgpr_write_b32 a192, v32
Mv 104,4,220
Rt 36,12,20480
s_add_u32 m0, s36, 135168
Mv 104,0,212
Rt 38,12,20992
buffer_load_dwordx4 v3, s[20:23], s56 offen offset:0 sc1 lds
Mv 108,8,212
Rt 40,12,24576
v_accvgpr_write_b32 a193, v34
Mv 108,12,220
Rt 42,12,25088
v_accvgpr_write_b32 a194, v36
Mv 100,12,216
Rt 44,13,16384
Cv 38,38,39
Mv 100,8,208
Rt 46,13,16896
s_waitcnt lgkmcnt(8)
Mv 112,16,208
Rt 0,13,20480
v_accvgpr_write_b32 a195, v38
Mv 112,20,216
Rt 2,13,20992
buffer_load_dwordx4 v3, s[20:23], s56 offen offset:1024 sc1 lds
Mv 120,20,220
Rt 4,13,24576
v_mul_f32_e32 v40, s54, v40
.LzH36:
s_cbranch_execz .LzH15
Mv 120,16,212
Rt 6,13,25088
v_mul_f32_e32 v41, s54, v41
Cv 40,40,41
Mv 124,24,212
Rt 8,14,16384
v_accvgpr_write_b32 a196, v40
Mv 124,28,220
Rt 10,14,16896
v_mul_f32_e32 v42, s54, v42
Mv 116,28,216
Rt 12,14,20480
v_mul_f32_e32 v43, s54, v43
Mv 116,24,208
Rt 14,14,20992
buffer_load_dwordx4 v3, s[20:23], s56 offen offset:2048 sc1 lds
s_waitcnt lgkmcnt(6)
)" R"(
Mv 96,32,128
Rt 16,14,24576
Cv 42,42,43
Mv 96,36,136
Rt 18,14,25088
v_accvgpr_write_b32 a197, v42
Mv 96,40,144
Rt 20,15,16384
v_mul_f32_e32 v44, s54, v44
Mv 104,40,148
Rt 22,15,16896
v_mul_f32_e32 v45, s54, v45
Cv 44,44,45
Mv 104,36,140
Rt 24,15,20480
v_accvgpr_write_b32 a198, v44
Mv 104,32,132
Rt 26,15,20992
buffer_load_dwordx4 v3, s[20:23], s56 offen offset:3072 sc1 lds
Mv 108,44,132
Rt 28,15,24576
v_mul_f32_e32 v46, s54, v46
Mv 108,0,140
Rt 30,15,25088
s_waitcnt lgkmcnt(6)
Mv 108,4,148
Rt 32,16,0
v_readfirstlane_b32 s63, v24
Mv 100,4,144
Rt 34,16,512
v_and_b32 v24, 16, v1
Mv 100,0,136
Rt 36,16,4096
v_lshrrev_b32_e64 v24, v24, s63
Mv 100,44,128
Rt 38,16,4608
v_and_b32 v24, 0x7ff, v24
v_mul_u32_u24 v24, 896, v24
Mv 112,8,128
Rt 40,17,0
v_and_b32 v25, 14, v0
Mv 112,12,136
Rt 42,17,512
v_min_u32 v25, 1, v25
Mv 112,16,144
Rt 44,17,4096
v_lshlrev_b32 v25, 30, v25
v_and_or_b32 v25, v0, 48, v25
Mv 120,16,148
Rt 46,17,4608
s_waitcnt vmcnt(8) lgkmcnt(8)
s_barrier
buffer_store_dword v6, v7, s[20:23], s49 offen offset:3072 sc0 sc1
Mv 120,12,140
Rt 0,18,0
v_cmp_eq_u32_e32 vcc, s3, v4
s_andn2_b64 s[28:29], -1, vcc
s_cbranch_scc1 .Lzsp6
.Lzok6:
Mv 120,8,132
Rt 2,18,512
v_add_u32 v24, v24, v25
v_add_u32 v8, 0x10000, v1
Mv 124,20,132
Rt 4,18,4096
)" R"(
s_and_b32 s83, s33, 1
v_add_u32 v10, 0x20000, v1
Mv 124,24,140
Rt 6,18,4608
s_mul_i32 s82, s83, 448
v_mul_f32_e32 v47, s54, v47
s_add_u32 s56, s56, 0x10000
Mv 124,28,148
Rt 8,19,0
Cv 46,46,47
s_add_u32 s55, s55, 0x10000
Mv 116,28,144
Rt 10,19,512
v_accvgpr_write_b32 a199, v46
s_add_u32 s55, s55, 0x10000
Mv 116,24,136
Rt 12,19,4096
v_mul_f32_e32 v48, s54, v48
s_cmp_eq_u32 s57, 2
s_cselect_b32 s55, s59, s55
Mv 116,20,128
Rt 14,19,4608
s_mov_b32 s93, 0
s_waitcnt lgkmcnt(8)
Mv 96,32,152
Rt 16,16,8192
v_mul_f32_e32 v49, s54, v49
s_mov_b32 s94, 1
Mv 96,36,160
Rt 18,16,8704
Cv 48,48,49
Mv 104,36,164
Rt 20,16,12288
v_accvgpr_write_b32 a200, v48
Mv 104,32,156
Rt 22,16,12800
v_mul_f32_e32 v50, s54, v50
v_mul_f32_e32 v51, s54, v51
Mv 108,40,156
Rt 24,17,8192
Cv 50,50,51
Mv 108,44,164
Rt 26,17,8704
v_accvgpr_write_b32 a201, v50
Mv 100,44,160
Rt 28,17,12288
v_mul_f32_e32 v52, s54, v52
Mv 100,40,152
Rt 30,17,12800
s_waitcnt lgkmcnt(8)
Mv 112,0,152
Rt 32,18,8192
v_mul_f32_e32 v53, s54, v53
Mv 112,4,160
Rt 34,18,8704
Cv 52,52,53
Mv 120,4,164
Rt 36,18,12288
v_accvgpr_write_b32 a202, v52
Mv 120,0,156
Rt 38,18,12800
buffer_load_dwordx4 a[128:131], v24, s[12:15], s82 offen offset:0
Mv 124,8,156
Rt 40,19,8192
)" R"(
v_mul_f32_e32 v54, s54, v54
Mv 124,12,164
Rt 42,19,8704
v_mul_f32_e32 v55, s54, v55
Mv 116,12,160
Rt 44,19,12288
Cv 54,54,55
Mv 116,8,152
Rt 46,19,12800
s_waitcnt lgkmcnt(8)
Mv 96,16,168
Rt 0,16,16384
buffer_load_dwordx4 a[132:135], v24, s[12:15], s82 offen offset:64
Mv 96,20,176
Rt 2,16,16896
v_accvgpr_write_b32 a203, v54
Mv 104,20,180
Rt 4,16,20480
v_mul_f32_e32 v56, s54, v56
Mv 104,16,172
Rt 6,16,20992
v_mul_f32_e32 v57, s54, v57
Cv 56,56,57
Mv 108,24,172
Rt 8,16,24576
v_accvgpr_write_b32 a204, v56
Mv 108,28,180
Rt 10,16,25088
buffer_load_dwordx4 a[136:139], v24, s[12:15], s82 offen offset:128
Mv 100,28,176
Rt 12,17,16384
v_mul_f32_e32 v58, s54, v58
Mv 100,24,168
Rt 14,17,16896
s_waitcnt lgkmcnt(8)
Mv 112,32,168
Rt 16,17,20480
buffer_load_dwordx4 a[140:143], v24, s[12:15], s82 offen offset:192
Mv 112,36,176
Rt 18,17,20992
v_mul_f32_e32 v59, s54, v59
Mv 120,36,180
Rt 20,17,24576
Cv 58,58,59
Mv 120,32,172
Rt 22,17,25088
v_accvgpr_write_b32 a205, v58
v_mul_f32_e32 v60, s54, v60
Mv 124,40,172
Rt 24,18,16384
v_mul_f32_e32 v61, s54, v61
Mv 124,44,180
Rt 26,18,16896
buffer_load_dwordx4 a[144:147], v24, s[12:15], s82 offen offset:256
Mv 116,44,176
Rt 28,18,20480
Cv 60,60,61
Mv 116,40,168
Rt 30,18,20992
s_waitcnt lgkmcnt(6)
s_barrier
Mv 96,0,184
Rt 32,18,24576
buffer_load_dwordx4 a[148:151], v24, s[12:15], s82 offen offset:320
Mv 96,4,48
)" R"(
Rt 34,18,25088
v_accvgpr_write_b32 a206, v60
Mv 96,8,56
Rt 36,19,16384
v_mul_f32_e32 v62, s54, v62
Mv 104,8,60
Rt 38,19,16896
v_mul_f32_e32 v63, s54, v63
Cv 62,62,63
Mv 104,4,52
Rt 40,19,20480
v_accvgpr_write_b32 a207, v62
Mv 104,0,188
Rt 42,19,20992
buffer_load_dwordx4 a[152:155], v24, s[12:15], s82 offen offset:384
Mv 108,12,188
Rt 44,19,24576
Mv 108,16,52
Rt 46,19,25088
s_waitcnt lgkmcnt(6)
Mv 108,20,60
Rb 0,8,32768
Mv 100,20,56
Rb 4,8,33792
Mv 100,16,48
Rb 8,8,34816
Mv 100,12,184
Rb 12,8,35840
Mv 112,24,184
Rb 16,8,40960
Mv 112,28,48
Rb 20,8,41984
Mv 112,32,56
Mv 120,32,60
s_waitcnt lgkmcnt(6)
Mv 120,28,52
Mv 120,24,188
Rb 24,8,43008
Mv 124,36,188
Rb 28,8,44032
Mv 124,40,52
Rb 32,8,36864
Mv 124,44,60
Mv 116,44,56
Mv 116,40,48
Mv 116,36,184
Rb 36,8,37888
Rb 40,8,38912
Rb 44,8,39936
s_cmp_eq_u32 s35, 7
s_cbranch_scc1 .LzA7
.LzA7R:
s_waitcnt lgkmcnt(8)
Mz 64,0,64
Rb 48,8,45056
v_mul_f32_e32 v96, s54, v96
Mv 64,4,72
v_mul_f32_e32 v97, s54, v97
Cv 96,96,97
Mv 64,8,80
Rb 52,8,46080
s_add_u32 m0, s36, 0
Mv 64,12,88
buffer_load_dwordx4 v2, s[20:23], s55 offen offset:0 sc1 lds
)" R"(
Mz 68,12,92
Rb 56,8,47104
v_accvgpr_write_b32 a208, v96
Mv 68,8,84
v_mul_f32_e32 v98, s54, v98
v_mul_f32_e32 v99, s54, v99
Mv 68,4,76
Rb 60,8,48128
s_waitcnt lgkmcnt(8)
Mv 68,0,68
Cv 98,98,99
v_accvgpr_write_b32 a209, v98
v_mul_f32_e32 v100, s54, v100
Mv 68,16,100
Rb 0,8,49152
v_mul_f32_e32 v101, s54, v101
Mv 68,20,108
Cv 100,100,101
v_accvgpr_write_b32 a210, v100
Mv 68,24,116
Rb 4,8,50176
v_mul_f32_e32 v102, s54, v102
Mv 68,28,124
buffer_load_dwordx4 v2, s[20:23], s55 offen offset:1024 sc1 lds
Mv 64,28,120
Rb 8,8,51200
v_mul_f32_e32 v103, s54, v103
Mv 64,24,112
Cv 102,102,103
v_accvgpr_write_b32 a211, v102
Mv 64,20,104
Rb 12,8,52224
s_waitcnt lgkmcnt(8)
Mv 64,16,96
v_mul_f32_e32 v104, s54, v104
v_mul_f32_e32 v105, s54, v105
Cv 104,104,105
Mz 72,32,64
Rb 16,8,57344
v_accvgpr_write_b32 a212, v104
Mv 72,36,72
v_mul_f32_e32 v106, s54, v106
v_mul_f32_e32 v107, s54, v107
Mv 72,40,80
Rb 20,8,58368
Cv 106,106,107
Mv 72,44,88
buffer_load_dwordx4 v2, s[20:23], s55 offen offset:2048 sc1 lds
Mz 76,44,92
Rb 24,8,59392
v_accvgpr_write_b32 a213, v106
Mv 76,40,84
v_mul_f32_e32 v108, s54, v108
v_mul_f32_e32 v109, s54, v109
Mv 76,36,76
Rb 28,8,60416
s_waitcnt lgkmcnt(8)
Mv 76,32,68
Cv 108,108,109
v_accvgpr_write_b32 a214, v108
v_mul_f32_e32 v110, s54, v110
Mv 76,48,100
Rb 32,8,53248
v_mul_f32_e32 v111, s54, v111
)" R"(
Mv 76,52,108
Cv 110,110,111
v_accvgpr_write_b32 a215, v110
Mv 76,56,116
Rb 36,8,54272
v_mul_f32_e32 v112, s54, v112
Mv 76,60,124
buffer_load_dwordx4 v2, s[20:23], s55 offen offset:3072 sc1 lds
Mv 72,60,120
Rb 40,8,55296
v_mul_f32_e32 v113, s54, v113
Mv 72,56,112
Cv 112,112,113
v_accvgpr_write_b32 a216, v112
Mv 72,52,104
Rb 44,8,56320
s_waitcnt lgkmcnt(8)
Mv 72,48,96
v_mul_f32_e32 v114, s54, v114
v_mul_f32_e32 v115, s54, v115
Cv 114,114,115
Mz 80,0,64
Rb 48,8,61440
v_accvgpr_write_b32 a217, v114
Mv 80,4,72
v_mul_f32_e32 v116, s54, v116
v_mul_f32_e32 v117, s54, v117
Mv 80,8,80
Rb 52,8,62464
s_add_u32 m0, s36, 4096
Mv 80,12,88
buffer_load_dwordx4 v3, s[20:23], s55 offen offset:0 sc1 lds
Mz 84,12,92
Rb 56,8,63488
Cv 116,116,117
Mv 84,8,84
v_accvgpr_write_b32 a218, v116
v_mul_f32_e32 v118, s54, v118
Mv 84,4,76
Rb 60,8,64512
s_waitcnt lgkmcnt(8)
Mv 84,0,68
v_mul_f32_e32 v119, s54, v119
Cv 118,118,119
v_accvgpr_write_b32 a219, v118
Mv 84,16,100
v_mul_f32_e32 v120, s54, v120
v_mul_f32_e32 v121, s54, v121
Mv 84,20,108
Cv 120,120,121
v_accvgpr_write_b32 a220, v120
Mv 84,24,116
v_mul_f32_e32 v122, s54, v122
v_mul_f32_e32 v123, s54, v123
Mv 84,28,124
buffer_load_dwordx4 v3, s[20:23], s55 offen offset:1024 sc1 lds
Mv 80,28,120
s_add_u32 s50, s50, 1024
s_min_u32 s50, s50, s60
Mv 80,24,112
Cv 122,122,123
v_accvgpr_write_b32 a221, v122
Mv 80,20,104
v_mul_f32_e32 v124, s54, v124
)" R"(
v_mul_f32_e32 v125, s54, v125
Mv 80,16,96
Cv 124,124,125
s_waitcnt lgkmcnt(0)
s_barrier
Mz 88,32,64
Rb 0,1,32768
v_accvgpr_write_b32 a222, v124
Mv 88,36,72
Rb 4,1,33792
v_mul_f32_e32 v126, s54, v126
Mv 88,40,80
Rb 8,1,34816
v_mul_f32_e32 v127, s54, v127
Mv 88,44,88
Rb 12,1,35840
buffer_load_dwordx4 v3, s[20:23], s55 offen offset:2048 sc1 lds
Mz 92,44,92
Rb 16,1,40960
Cv 126,126,127
Mv 92,40,84
Rb 20,1,41984
v_accvgpr_write_b32 a223, v126
Mv 92,36,76
Rb 24,1,43008
v_mov_b32 v16, 0
Mv 92,32,68
Rb 28,1,44032
v_mov_b32 v17, 0
v_mov_b32 v18, 0
Mv 92,48,100
Rb 32,1,36864
v_mov_b32 v19, 0
Mv 92,52,108
Rb 36,1,37888
v_mov_b32 v20, 0
Mv 92,56,116
Rb 40,1,38912
v_mov_b32 v21, 0
Mv 92,60,124
Rb 44,1,39936
buffer_load_dwordx4 v3, s[20:23], s55 offen offset:3072 sc1 lds
Mv 88,60,120
v_mov_b32 v22, 0
v_mov_b32 v23, 0
Mv 88,56,112
Mv 88,52,104
s_waitcnt lgkmcnt(8)
Mv 88,48,96
Mz 96,0,64
Rb 48,1,45056
buffer_load_dword v4, v5, s[20:23], s50 offen offset:0 sc0 sc1
Mv 96,4,72
s_cmp_le_i32 s57, s58
s_cbranch_scc1 .Lzmk0
.Lzmr0:
Mv 96,8,80
Rb 52,1,46080
s_add_u32 m0, s36, 65536
s_sub_u32 s57, s57, 1
Mv 96,12,88
buffer_load_dwordx4 v2, s[20:23], s56 offen offset:0 sc1 lds
s_add_u32 s55, s55, 0x10000
Mz 100,12,92
Rb 56,1,47104
)" R"(
s_cmp_eq_u32 s57, 2
s_cselect_b32 s55, s59, s55
Mv 100,8,84
v_exp_f32_e32 v32, v64
Mv 100,4,76
Rb 60,1,48128
s_waitcnt lgkmcnt(8)
Mv 100,0,68
v_exp_f32_e32 v24, v65
Mv 100,16,100
Rb 0,1,49152
v_exp_f32_e32 v33, v66
Mv 100,20,108
v_exp_f32_e32 v25, v67
v_exp_f32_e32 v34, v72
Mv 100,24,116
Rb 4,1,50176
v_exp_f32_e32 v26, v73
Mv 100,28,124
buffer_load_dwordx4 v2, s[20:23], s56 offen offset:1024 sc1 lds
v_add_f32_e32 v14, v32, v24
Mv 96,28,120
Rb 8,1,51200
v_exp_f32_e32 v35, v74
Mv 96,24,112
v_exp_f32_e32 v27, v75
Cv 32,32,24
Mv 96,20,104
Rb 12,1,52224
v_add_f32_e32 v14, v14, v33
s_waitcnt lgkmcnt(8)
Mv 96,16,96
v_exp_f32_e32 v36, v68
v_add_f32_e32 v14, v14, v25
Mz 104,32,64
Rb 16,1,57344
v_exp_f32_e32 v28, v69
Mv 104,36,72
v_exp_f32_e32 v37, v70
v_exp_f32_e32 v29, v71
Mv 104,40,80
Rb 20,1,58368
v_exp_f32_e32 v38, v76
Mv 104,44,88
buffer_load_dwordx4 v2, s[20:23], s56 offen offset:2048 sc1 lds
Cv 33,33,25
Mz 108,44,92
Rb 24,1,59392
v_exp_f32_e32 v24, v77
Mv 108,40,84
v_exp_f32_e32 v39, v78
v_add_f32_e32 v14, v14, v34
Mv 108,36,76
Rb 28,1,60416
v_add_f32_e32 v14, v14, v26
s_waitcnt lgkmcnt(8)
Mv 108,32,68
v_exp_f32_e32 v25, v79
Cv 34,34,26
Mv 108,48,100
Rb 32,1,53248
v_exp_f32_e32 v40, v80
Mv 108,52,108
v_exp_f32_e32 v26, v81
)" R"(
v_add_f32_e32 v14, v14, v35
v_add_f32_e32 v14, v14, v27
Mv 108,56,116
Rb 36,1,54272
Cv 35,35,27
v_add_f32_e32 v15, v36, v28
Mv 108,60,124
buffer_load_dwordx4 v2, s[20:23], s56 offen offset:3072 sc1 lds
Cv 36,36,28
Mv 104,60,120
Rb 40,1,55296
v_exp_f32_e32 v41, v82
Mv 104,56,112
v_exp_f32_e32 v27, v83
v_add_f32_e32 v15, v15, v37
Mv 104,52,104
Rb 44,1,56320
v_add_f32_e32 v15, v15, v29
s_waitcnt lgkmcnt(8)
Mv 104,48,96
v_exp_f32_e32 v42, v88
Cv 37,37,29
Mz 112,0,64
Rb 48,1,61440
v_exp_f32_e32 v28, v89
Mv 112,4,72
v_exp_f32_e32 v43, v90
v_add_f32_e32 v15, v15, v38
v_add_f32_e32 v15, v15, v24
Mv 112,8,80
Rb 52,1,62464
s_add_u32 m0, s36, 69632
Cv 38,38,24
Mv 112,12,88
buffer_load_dwordx4 v3, s[20:23], s56 offen offset:0 sc1 lds
v_add_f32_e32 v15, v15, v39
Mz 116,12,92
Rb 56,1,63488
v_exp_f32_e32 v29, v91
Mv 116,8,84
v_exp_f32_e32 v44, v84
v_add_f32_e32 v15, v15, v25
Mv 116,4,76
Rb 60,1,64512
Cv 39,39,25
s_waitcnt lgkmcnt(8)
Mv 116,0,68
v_exp_f32_e32 v24, v85
v_add_f32_e32 v14, v14, v40
Mv 116,16,100
v_exp_f32_e32 v45, v86
v_add_f32_e32 v14, v14, v26
Mv 116,20,108
v_exp_f32_e32 v25, v87
Cv 40,40,26
v_add_f32_e32 v14, v14, v41
Mv 116,24,116
v_exp_f32_e32 v46, v92
v_add_f32_e32 v14, v14, v27
Mv 116,28,124
buffer_load_dwordx4 v3, s[20:23], s56 offen offset:1024 sc1 lds
Cv 41,41,27
.LzH37:
s_cbranch_execz .LzH16
)" R"(
Mv 112,28,120
v_exp_f32_e32 v26, v93
v_add_f32_e32 v14, v14, v42
Mv 112,24,112
v_exp_f32_e32 v47, v94
v_add_f32_e32 v14, v14, v28
Mv 112,20,104
v_exp_f32_e32 v27, v95
Cv 42,42,28
Mv 112,16,96
v_add_f32_e32 v14, v14, v43
s_waitcnt vmcnt(23) lgkmcnt(0)
s_barrier
Mz 120,32,64
Rb 0,10,0
v_add_f32_e32 v14, v14, v29
Cv 43,43,29
Mv 120,36,72
Rb 4,10,16384
v_add_f32_e32 v15, v15, v44
v_add_f32_e32 v15, v15, v24
Cv 44,44,24
Mv 120,40,80
Rb 8,10,1024
v_add_f32_e32 v15, v15, v45
v_add_f32_e32 v15, v15, v25
Mv 120,44,88
Rb 12,10,17408
buffer_load_dwordx4 v3, s[20:23], s56 offen offset:2048 sc1 lds
Mz 124,44,92
Rb 16,10,2048
Cv 45,45,25
v_add_f32_e32 v15, v15, v46
Mv 124,40,84
Rb 20,10,18432
v_add_f32_e32 v15, v15, v26
Cv 46,46,26
Mv 124,36,76
Rb 24,10,3072
v_add_f32_e32 v15, v15, v47
v_add_f32_e32 v15, v15, v27
Mv 124,32,68
Rb 28,10,19456
Cv 47,47,27
v_max_f32_e32 v11, v14, v15
Mv 124,48,100
Rb 32,10,4096
v_mov_b32 v24, v14
v_mov_b32 v25, v15
Mv 124,52,108
Rb 36,10,20480
v_permlane32_swap_b32_e32 v24, v25
v_add_f32_e32 v24, v24, v25
v_mov_b32 v25, v24
Mv 124,56,116
Rb 40,10,5120
v_permlane16_swap_b32_e32 v24, v25
v_add_f32_e32 v24, v24, v25
Mv 124,60,124
Rb 44,10,21504
buffer_load_dwordx4 v3, s[20:23], s56 offen offset:3072 sc1 lds
Mv 120,60,120
v_rcp_f32_e32 v24, v24
Mv 120,56,112
s_add_u32 s56, s56, 0x10000
)" R"(
v_max_f32_e32 v11, v11, v24
v_cmp_nge_f32_e32 vcc, s62, v11
Mv 120,52,104
s_waitcnt lgkmcnt(8)
s_cbranch_vccnz .Lzfx0
.Lzfr0:
Mv 120,48,96
v_mov_b32 v12, v14
v_mov_b32 v13, v15
v_mfma_f32_16x16x32_bf16 v[128:131], a[0:3], v[32:35], 0
Rb 48,10,6144
Mp 128,4,40
s_cmp_le_i32 s57, s58
s_cbranch_scc1 .Lzmk1
.Lzmr1:
v_mfma_f32_16x16x32_bf16 v[136:139], a[4:7], v[44:47], 0
Rb 52,10,22528
s_add_u32 m0, s36, 32768
Mp 136,0,36
buffer_load_dwordx4 v2, s[20:23], s55 offen offset:0 sc1 lds
v_mfma_f32_16x16x32_bf16 v[140:143], a[8:11], v[36:39], 0
Rb 56,10,7168
s_and_b32 s92, s2, 7
Mp 140,12,44
s_mul_i32 s92, s92, 14
s_lshr_b32 s82, s2, 3
v_mfma_f32_16x16x32_bf16 v[132:135], a[12:15], v[40:43], 0
Rb 60,10,23552
s_waitcnt lgkmcnt(8)
Mp 132,8,32
buffer_load_dwordx4 v2, s[20:23], s55 offen offset:1024 sc1 lds
v_mfma_f32_16x16x32_bf16 v[144:147], a[16:19], v[32:35], 0
Rb 0,10,8192
s_add_u32 s82, s82, s92
Mp 144,20,40
v_exp_f32_e32 v48, v96
v_mfma_f32_16x16x32_bf16 v[152:155], a[20:23], v[44:47], 0
Rb 4,10,24576
Mp 152,16,36
buffer_load_dwordx4 v2, s[20:23], s55 offen offset:2048 sc1 lds
v_mfma_f32_16x16x32_bf16 v[156:159], a[24:27], v[36:39], 0
Rb 8,10,9216
v_exp_f32_e32 v28, v97
Mp 156,28,44
v_exp_f32_e32 v49, v98
v_mfma_f32_16x16x32_bf16 v[148:151], a[28:31], v[40:43], 0
Rb 12,10,25600
s_waitcnt lgkmcnt(8)
Mp 148,24,32
buffer_load_dwordx4 v2, s[20:23], s55 offen offset:3072 sc1 lds
v_add_f32_e32 v14, v48, v28
v_mfma_f32_16x16x32_bf16 v[160:163], a[32:35], v[32:35], 0
Rb 16,10,10240
Cv 48,48,28
Mp 160,36,40
v_exp_f32_e32 v29, v99
v_add_f32_e32 v14, v14, v49
v_mfma_f32_16x16x32_bf16 v[168:171], a[36:39], v[44:47], 0
Rb 20,10,26624
s_add_u32 m0, s36, 36864
Mp 168,32,36
buffer_load_dwordx4 v3, s[20:23], s55 offen offset:0 sc1 lds
v_mfma_f32_16x16x32_bf16 v[172:175], a[40:43], v[36:39], 0
Rb 24,10,11264
)" R"(
v_exp_f32_e32 v50, v104
Mp 172,44,44
s_cmp_gt_u32 s33, 1
s_cselect_b32 s24, s78, s76
s_cselect_b32 s25, s79, s77
v_mfma_f32_16x16x32_bf16 v[164:167], a[44:47], v[40:43], 0
Rb 28,10,27648
s_waitcnt lgkmcnt(8)
Mp 164,40,32
buffer_load_dwordx4 v3, s[20:23], s55 offen offset:1024 sc1 lds
v_add_f32_e32 v14, v14, v29
v_mfma_f32_16x16x32_bf16 v[176:179], a[48:51], v[32:35], 0
Rb 32,10,12288
s_mov_b32 s27, 0x20000
Mp 176,52,40
s_cmp_lt_u32 s35, 7
s_cselect_b32 s26, 0x188000, 0
Cv 49,49,29
v_mfma_f32_16x16x32_bf16 v[184:187], a[52:55], v[44:47], 0
Rb 36,10,28672
s_mul_i32 s83, s83, 802816
Mp 184,48,36
buffer_load_dwordx4 v3, s[20:23], s55 offen offset:2048 sc1 lds
v_mfma_f32_16x16x32_bf16 v[188:191], a[56:59], v[36:39], 0
Rb 40,10,13312
s_lshl_b32 s92, s82, 5
v_add_f32_e32 v14, v14, v50
Mp 188,60,44
s_add_u32 s83, s83, s92
v_and_b32 v9, 62, v0
v_mul_u32_u24 v9, 0x6f0, v9
v_mfma_f32_16x16x32_bf16 v[180:183], a[60:63], v[40:43], 0
Rb 44,10,29696
s_waitcnt lgkmcnt(8)
Mp 180,56,32
buffer_load_dwordx4 v3, s[20:23], s55 offen offset:3072 sc1 lds
v_mfma_f32_16x16x32_bf16 v[192:195], a[0:3], v[32:35], 0
Rb 48,10,14336
v_add_u32 v9, v9, v1
Mp 192,4,40
v_exp_f32_e32 v24, v105
v_mfma_f32_16x16x32_bf16 v[200:203], a[4:7], v[44:47], 0
Rb 52,10,30720
v_add_f32_e32 v14, v14, v24
Mp 200,0,36
v_exp_f32_e32 v51, v106
v_mfma_f32_16x16x32_bf16 v[204:207], a[8:11], v[36:39], 0
Rb 56,10,15360
v_exp_f32_e32 v25, v107
Mp 204,12,44
v_exp_f32_e32 v52, v100
v_mfma_f32_16x16x32_bf16 v[196:199], a[12:15], v[40:43], 0
Rb 60,10,31744
s_waitcnt lgkmcnt(8)
Mp 196,8,32
v_exp_f32_e32 v26, v101
Cv 50,50,24
v_mfma_f32_16x16x32_bf16 v[208:211], a[16:19], v[32:35], 0
v_exp_f32_e32 v53, v102
Mp 208,20,40
v_exp_f32_e32 v27, v103
v_add_f32_e32 v14, v14, v51
v_mfma_f32_16x16x32_bf16 v[216:219], a[20:23], v[44:47], 0
v_exp_f32_e32 v54, v108
)" R"(
Mp 216,16,36
v_exp_f32_e32 v28, v109
v_mfma_f32_16x16x32_bf16 v[220:223], a[24:27], v[36:39], 0
v_exp_f32_e32 v55, v110
v_add_f32_e32 v14, v14, v25
Mp 220,28,44
v_exp_f32_e32 v29, v111
v_mfma_f32_16x16x32_bf16 v[212:215], a[28:31], v[40:43], 0
Cv 51,51,25
v_add_f32_e32 v15, v52, v26
Mp 212,24,32
Cv 52,52,26
s_waitcnt vmcnt(16) lgkmcnt(0)
s_barrier
v_mfma_f32_16x16x32_bf16 v[224:227], a[32:35], v[32:35], 0
Rb 0,1,0
s_add_u32 m0, s36, 131072
s_nop 0
buffer_load_dwordx4 v9, s[24:27], s83 offen offset:0 lds
Mp 224,36,40
Rb 4,1,1024
v_add_f32_e32 v15, v15, v53
v_add_f32_e32 v15, v15, v27
v_mfma_f32_16x16x32_bf16 v[232:235], a[36:39], v[44:47], 0
Rb 8,1,2048
s_add_u32 s83, s83, 0x1bc00
buffer_load_dwordx4 v9, s[24:27], s83 offen offset:1024 lds
Mp 232,32,36
Rb 12,1,3072
Cv 53,53,27
v_mfma_f32_16x16x32_bf16 v[236:239], a[40:43], v[36:39], 0
Rb 16,1,8192
s_add_u32 s83, s83, 0x1bc00
buffer_load_dwordx4 v9, s[24:27], s83 offen offset:2048 lds
Mp 236,44,44
Rb 20,1,9216
v_add_f32_e32 v15, v15, v54
v_mfma_f32_16x16x32_bf16 v[228:231], a[44:47], v[40:43], 0
Rb 24,1,10240
s_add_u32 s83, s83, 0x1bc00
buffer_load_dwordx4 v9, s[24:27], s83 offen offset:3072 lds
Mp 228,40,32
Rb 28,1,11264
v_add_f32_e32 v15, v15, v28
Cv 54,54,28
v_mfma_f32_16x16x32_bf16 v[240:243], a[48:51], v[32:35], 0
Rb 32,1,4096
s_add_u32 m0, s36, 135168
s_add_u32 s83, s83, 0x1cc00
buffer_load_dwordx4 v9, s[24:27], s83 offen offset:0 lds
Mp 240,52,40
Rb 36,1,5120
v_add_f32_e32 v15, v15, v55
v_add_f32_e32 v15, v15, v29
v_mfma_f32_16x16x32_bf16 v[248:251], a[52:55], v[44:47], 0
Rb 40,1,6144
s_add_u32 s83, s83, 0x1bc00
buffer_load_dwordx4 v9, s[24:27], s83 offen offset:1024 lds
Mp 248,48,36
Rb 44,1,7168
Cv 55,55,29
v_mfma_f32_16x16x32_bf16 v[252:255], a[56:59], v[36:39], 0
s_add_u32 s83, s83, 0x1bc00
buffer_load_dwordx4 v9, s[24:27], s83 offen offset:2048 lds
v_cmp_eq_u32_e32 vcc, s3, v4
)" R"(
s_andn2_b64 s[28:29], -1, vcc
s_cbranch_scc1 .Lzsp8
.Lzok8:
Mp 252,60,44
s_sub_u32 s57, s57, 1
v_mfma_f32_16x16x32_bf16 v[244:247], a[60:63], v[40:43], 0
s_waitcnt lgkmcnt(8)
Mp 244,56,32
s_cmp_gt_i32 s57, s93
s_cbranch_scc1 .LzL
.LzH38:
s_cbranch_execz .LzH21
.LzW:
s_lshl1_add_u32 s57, s35, 2
s_mov_b32 s59, s67
s_add_u32 s56, s89, 0x400000
s_mov_b32 s94, 2
Mz 64,0,224
Rb 48,1,12288
v_lshrrev_b32 v9, 4, v0
Mv 64,4,232
v_lshlrev_b32 v9, 4, v9
v_add_lshl_u32 v9, v9, v0, 3
v_accvgpr_mov_b32 a64, a224
Mv 64,8,240
Rb 52,1,13312
s_add_u32 m0, s36, 98304
v_accvgpr_mov_b32 a65, a225
Mv 64,12,248
buffer_load_dwordx4 v2, s[20:23], s56 offen offset:0 sc1 lds
v_accvgpr_mov_b32 a66, a226
Mz 68,12,252
Rb 56,1,14336
s_add_u32 s92, s36, 131072
v_accvgpr_mov_b32 a67, a227
Mv 68,8,244
v_add_u32 v9, s92, v9
v_accvgpr_mov_b32 a68, a228
s_waitcnt vmcnt(1)
Mv 68,4,236
Rb 60,1,15360
v_accvgpr_mov_b32 a69, a229
s_waitcnt lgkmcnt(8)
Mv 68,0,228
buffer_load_dwordx4 v2, s[20:23], s56 offen offset:1024 sc1 lds
Rt 156,9,0
Mv 68,16,196
Rb 0,1,16384
Rt 158,9,128
v_accvgpr_mov_b32 a70, a230
Mv 68,20,204
Rt 160,9,1024
v_exp_f32_e32 v56, v112
Mv 68,24,212
Rb 4,1,17408
Rt 162,9,1152
v_add_f32_e32 v14, v14, v56
Mv 68,28,220
buffer_load_dwordx4 v2, s[20:23], s56 offen offset:2048 sc1 lds
Rt 164,9,2048
Mv 64,28,216
Rb 8,1,18432
Rt 166,9,2176
v_accvgpr_mov_b32 a71, a231
)" R"(
Mv 64,24,208
Rt 168,9,3072
v_exp_f32_e32 v26, v113
Mv 64,20,200
Rb 12,1,19456
Rt 170,9,3200
Mv 64,16,192
buffer_load_dwordx4 v2, s[20:23], s56 offen offset:3072 sc1 lds
Rt 172,9,4096
v_add_f32_e32 v14, v14, v26
Mz 72,32,224
Rb 16,1,24576
Rt 174,9,4224
Cv 56,56,26
Mv 72,36,232
Rt 176,9,5120
v_exp_f32_e32 v57, v114
Mv 72,40,240
Rb 20,1,25600
s_add_u32 m0, s36, 102400
Rt 178,9,5248
Mv 72,44,248
buffer_load_dwordx4 v3, s[20:23], s56 offen offset:0 sc1 lds
Rt 180,9,6144
Mz 76,44,252
Rb 24,1,26624
Rt 182,9,6272
v_add_f32_e32 v14, v14, v57
Mv 76,40,244
v_exp_f32_e32 v27, v115
v_accvgpr_mov_b32 a72, a232
Mv 76,36,236
Rb 28,1,27648
v_add_f32_e32 v14, v14, v27
Mv 76,32,228
buffer_load_dwordx4 v3, s[20:23], s56 offen offset:1024 sc1 lds
Cv 57,57,27
Mv 76,48,196
Rb 32,1,20480
v_and_b32 v9, 14, v0
v_accvgpr_mov_b32 a73, a233
Mv 76,52,204
v_min_u32 v9, 1, v9
v_lshlrev_b32 v9, 8, v9
v_accvgpr_mov_b32 a74, a234
Mv 76,56,212
Rb 36,1,21504
v_and_or_b32 v9, v0, 48, v9
v_accvgpr_mov_b32 a75, a235
Mv 76,60,220
buffer_load_dwordx4 v3, s[20:23], s56 offen offset:2048 sc1 lds
v_accvgpr_mov_b32 a76, a236
Mv 72,60,216
Rb 40,1,22528
v_lshlrev_b32 v9, 1, v9
v_accvgpr_mov_b32 a77, a237
Mv 72,56,208
v_and_or_b32 v9, v1, 16, v9
s_lshl_b32 s92, s33, 7
v_accvgpr_mov_b32 a78, a238
Mv 72,52,200
Rb 44,1,23552
v_accvgpr_mov_b32 a79, a239
s_waitcnt lgkmcnt(5)
)" R"(
Mv 72,48,192
v_mfma_f32_16x16x32_bf16 a[184:187], a[156:159], a[128:131], 0
buffer_load_dwordx4 v3, s[20:23], s56 offen offset:3072 sc1 lds
v_exp_f32_e32 v58, v120
Mz 80,0,224
Ma 184,160,132
Rb 48,1,28672
s_add_u32 s92, s92, 162816
v_add_f32_e32 v14, v14, v58
Mv 80,4,232
Ma 184,164,136
s_add_u32 s56, s56, 0x10000
v_add_u32 v9, s92, v9
v_accvgpr_mov_b32 a80, a240
Mv 80,8,240
Ma 184,168,140
Rb 52,1,29696
v_exp_f32_e32 v28, v121
Mv 80,12,248
Ma 184,172,144
v_exp_f32_e32 v59, v122
v_add_f32_e32 v14, v14, v28
Mz 84,12,252
Ma 184,176,148
Rb 56,1,30720
s_mov_b32 s24, s70
s_mov_b32 s25, s71
Mv 84,8,244
Ma 184,180,152
s_mul_i32 s83, s82, 14336
s_lshl_b32 s92, s33, 12
Cv 58,58,28
Mv 84,4,236
Rb 60,1,31744
v_add_f32_e32 v14, v14, v59
s_waitcnt lgkmcnt(8)
Mv 84,0,228
s_add_u32 s83, s83, s92
s_add_u32 m0, s92, 131072
v_accvgpr_mov_b32 a81, a241
Mv 84,16,196
v_exp_f32_e32 v29, v123
v_accvgpr_mov_b32 a82, a242
Mv 84,20,204
v_exp_f32_e32 v60, v116
v_add_f32_e32 v14, v14, v29
Mv 84,24,212
ds_write_b128 v9, a[184:187]
v_and_b32 v9, 0x70, v9
Cv 59,59,29
Mv 84,28,220
v_add_u32 v9, 0x27c00, v9
v_exp_f32_e32 v24, v117
Mv 80,28,216
v_exp_f32_e32 v61, v118
v_add_f32_e32 v15, v15, v60
Mv 80,24,208
v_exp_f32_e32 v25, v119
v_add_f32_e32 v15, v15, v24
Mv 80,20,200
v_exp_f32_e32 v62, v124
Cv 60,60,24
Mv 80,16,192
v_exp_f32_e32 v26, v125
)" R"(
s_waitcnt lgkmcnt(0)
s_barrier
Mz 88,32,224
Rb 0,8,0
v_exp_f32_e32 v63, v126
Mv 88,36,232
Rb 4,8,16384
v_exp_f32_e32 v27, v127
Mv 88,40,240
Rb 8,8,1024
v_add_f32_e32 v15, v15, v61
v_add_f32_e32 v15, v15, v25
Mv 88,44,248
Rb 12,8,17408
Cv 61,61,25
v_add_f32_e32 v15, v15, v62
Mz 92,44,252
Rb 16,8,2048
buffer_load_dwordx4 v1, s[24:27], s83 offen offset:0 lds
Mv 92,40,244
Rb 20,8,18432
v_add_f32_e32 v15, v15, v26
Cv 62,62,26
Mv 92,36,236
Rb 24,8,3072
v_add_f32_e32 v15, v15, v63
v_add_f32_e32 v15, v15, v27
Mv 92,32,228
Rb 28,8,19456
Cv 63,63,27
v_max_f32_e32 v11, v14, v15
Mv 92,48,196
Rb 32,8,4096
v_cmp_nge_f32_e32 vcc, s62, v11
s_add_u32 s55, s55, 0x10000
Mv 92,52,204
Rb 36,8,20480
buffer_load_dwordx4 v1, s[24:27], s83 offen offset:1024 lds
Mv 92,56,212
Rb 40,8,5120
s_cmp_eq_u32 s57, 2
s_cselect_b32 s55, s59, s55
Mv 92,60,220
Rb 44,8,21504
v_accvgpr_mov_b32 a83, a243
v_accvgpr_mov_b32 a84, a244
Mv 88,60,216
buffer_load_dwordx4 v1, s[24:27], s83 offen offset:2048 lds
v_accvgpr_mov_b32 a85, a245
Mv 88,56,208
buffer_load_dwordx4 v1, s[24:27], s83 offen offset:3072 lds
v_accvgpr_mov_b32 a86, a246
Mv 88,52,200
v_accvgpr_mov_b32 a87, a247
s_waitcnt lgkmcnt(8)
s_cbranch_vccnz .Lzfx3
.Lzfr3:
Mv 88,48,192
v_add_f32_e32 v12, v12, v14
v_add_f32_e32 v13, v13, v15
v_mov_b32 v14, v16
Mp 128,0,48
Rb 48,8,6144
v_mov_b32 v15, v20
Mp 128,4,56
)" R"(
buffer_store_dwordx4 v[12:15], v1, s[20:23], s66 offen sc0 sc1
s_cmp_le_i32 s57, s58
s_cbranch_scc1 .Lzmk4
.Lzmr4:
Mp 136,4,60
Rb 52,8,22528
s_add_u32 m0, s36, 0
v_accvgpr_mov_b32 a88, a248
Mp 136,0,52
buffer_load_dwordx4 v2, s[20:23], s55 offen offset:0 sc1 lds
v_accvgpr_mov_b32 a89, a249
Mp 140,8,52
Rb 56,8,7168
v_exp_f32_e32 v32, v64
Mp 140,12,60
v_exp_f32_e32 v28, v65
v_accvgpr_mov_b32 a90, a250
Mp 132,12,56
Rb 60,8,23552
v_exp_f32_e32 v33, v66
s_waitcnt lgkmcnt(8)
Mp 132,8,48
v_exp_f32_e32 v29, v67
v_add_f32_e32 v14, v32, v28
Mp 144,16,48
Rb 0,8,8192
v_exp_f32_e32 v34, v72
Mp 144,20,56
v_exp_f32_e32 v24, v73
v_exp_f32_e32 v35, v74
Mp 152,20,60
Rb 4,8,24576
v_exp_f32_e32 v25, v75
Mp 152,16,52
buffer_load_dwordx4 v2, s[20:23], s55 offen offset:1024 sc1 lds
Cv 32,32,28
Mp 156,24,52
Rb 8,8,9216
Cv 128,128,129
Cv 129,130,131
v_add_f32_e32 v14, v14, v33
Mp 156,28,60
Cv 130,132,133
Cv 131,134,135
v_add_f32_e32 v14, v14, v29
Mp 148,28,56
Rb 12,8,25600
Cv 136,136,137
s_waitcnt lgkmcnt(8)
Mp 148,24,48
Cv 137,138,139
Cv 138,140,141
Cv 139,142,143
Cv 33,33,29
Mp 160,32,48
Rb 16,8,10240
v_exp_f32_e32 v36, v68
Mp 160,36,56
v_exp_f32_e32 v26, v69
v_add_f32_e32 v14, v14, v34
Mp 168,36,60
Rb 20,8,26624
v_exp_f32_e32 v37, v70
v_add_f32_e32 v14, v14, v24
)" R"(
Mp 168,32,52
buffer_load_dwordx4 v2, s[20:23], s55 offen offset:2048 sc1 lds
Cv 144,144,145
Mp 172,40,52
Rb 24,8,11264
Cv 145,146,147
Cv 34,34,24
Mp 172,44,60
buffer_store_dwordx4 v[128:131], v2, s[20:23], s64 offen offset:0 sc0 sc1
Cv 146,148,149
Mp 164,44,56
Rb 28,8,27648
v_exp_f32_e32 v27, v71
s_waitcnt lgkmcnt(8)
Mp 164,40,48
Cv 147,150,151
Cv 152,152,153
Cv 153,154,155
Mp 176,48,48
Rb 32,8,12288
Cv 154,156,157
Cv 155,158,159
Mp 176,52,56
v_exp_f32_e32 v38, v76
v_exp_f32_e32 v28, v77
Mp 184,52,60
Rb 36,8,28672
v_exp_f32_e32 v39, v78
Mp 184,48,52
buffer_load_dwordx4 v2, s[20:23], s55 offen offset:3072 sc1 lds
v_add_f32_e32 v14, v14, v35
Mp 188,56,52
Rb 40,8,13312
Cv 160,160,161
Cv 161,162,163
v_add_f32_e32 v14, v14, v25
Mp 188,60,60
buffer_store_dwordx4 v[136:139], v2, s[20:23], s64 offen offset:1024 sc0 sc1
Cv 35,35,25
Mp 180,60,56
Rb 44,8,29696
Cv 162,164,165
s_waitcnt lgkmcnt(8)
Mp 180,56,48
Cv 163,166,167
Cv 168,168,169
Cv 169,170,171
v_add_f32_e32 v15, v36, v26
.LzH39:
s_cbranch_execz .LzH22
Mp 192,0,48
Rb 48,8,14336
Cv 170,172,173
Cv 36,36,26
Mp 192,4,56
buffer_store_dwordx4 v[144:147], v2, s[20:23], s64 offen offset:2048 sc0 sc1
Cv 171,174,175
Mp 200,4,60
Rb 52,8,30720
s_add_u32 m0, s36, 4096
v_exp_f32_e32 v29, v79
Mp 200,0,52
buffer_load_dwordx4 v3, s[20:23], s55 offen offset:0 sc1 lds
Cv 176,176,177
)" R"(
Mp 204,8,52
Rb 56,8,15360
Cv 177,178,179
v_add_f32_e32 v15, v15, v37
Mp 204,12,60
Cv 178,180,181
Cv 179,182,183
Cv 184,184,185
Mp 196,12,56
Rb 60,8,31744
v_add_f32_e32 v15, v15, v27
Cv 37,37,27
s_waitcnt lgkmcnt(8)
Mp 196,8,48
buffer_store_dwordx4 v[152:155], v2, s[20:23], s64 offen offset:3072 sc0 sc1
Cv 185,186,187
Mp 208,16,48
ds_read_b128 v[132:135], v9 offset:0
Cv 186,188,189
Cv 187,190,191
Mp 208,20,56
ds_read_b128 v[140:143], v9 offset:128
v_add_f32_e32 v15, v15, v38
v_add_f32_e32 v15, v15, v28
Cv 38,38,28
Mp 216,20,60
ds_read_b128 v[148:151], v9 offset:256
v_add_f32_e32 v15, v15, v39
v_add_f32_e32 v15, v15, v29
Mp 216,16,52
buffer_load_dwordx4 v3, s[20:23], s55 offen offset:1024 sc1 lds
ds_read_b128 v[156:159], v9 offset:384
Mp 220,24,52
s_add_u32 s50, s50, 1024
s_min_u32 s50, s50, s60
Cv 192,192,193
Cv 39,39,29
Mp 220,28,60
Cv 193,194,195
Cv 194,196,197
s_sub_u32 s57, s57, 1
Mp 212,28,56
buffer_store_dwordx4 v[160:163], v3, s[20:23], s64 offen offset:0 sc0 sc1
Cv 195,198,199
Mp 212,24,48
Cv 200,200,201
v_mov_b32 v16, 0
s_waitcnt lgkmcnt(0)
s_barrier
Mp 224,32,48
Rb 0,1,32768
v_add_f32_e32 v132, v132, v140
v_mov_b32 v17, 0
Mp 224,36,56
Rb 4,1,33792
v_add_f32_e32 v133, v133, v141
Cv 201,202,203
Mp 232,36,60
Rb 8,1,34816
v_add_f32_e32 v134, v134, v142
v_mov_b32 v18, 0
v_mov_b32 v19, 0
Mp 232,32,52
Rb 12,1,35840
buffer_load_dwordx4 v3, s[20:23], s55 offen offset:2048 sc1 lds
)" R"(
Mp 236,40,52
Rb 16,1,40960
v_add_f32_e32 v135, v135, v143
buffer_store_dwordx4 v[168:171], v3, s[20:23], s64 offen offset:1024 sc0 sc1
Mp 236,44,60
Rb 20,1,41984
v_add_f32_e32 v148, v148, v156
Cv 202,204,205
Mp 228,44,56
Rb 24,1,43008
v_add_f32_e32 v149, v149, v157
v_mov_b32 v20, 0
v_mov_b32 v21, 0
Mp 228,40,48
Rb 28,1,44032
v_add_f32_e32 v150, v150, v158
Cv 203,206,207
Mp 240,48,48
Rb 32,1,36864
v_add_f32_e32 v151, v151, v159
Cv 208,208,209
Mp 240,52,56
Rb 36,1,37888
Cv 132,132,132
v_mov_b32 v22, 0
v_mov_b32 v23, 0
Mp 248,52,60
Rb 40,1,38912
v_lshlrev_b32 v132, 16, v132
buffer_store_dwordx4 v[176:179], v3, s[20:23], s64 offen offset:2048 sc0 sc1
Mp 248,48,52
Rb 44,1,39936
buffer_load_dwordx4 v3, s[20:23], s55 offen offset:3072 sc1 lds
Mp 252,56,52
Cv 148,148,148
v_lshlrev_b32 v148, 16, v148
Cv 209,210,211
Mp 252,60,60
Cv 133,133,133
v_lshlrev_b32 v133, 16, v133
v_accvgpr_mov_b32 a91, a251
Mp 244,60,56
Cv 149,149,149
Cv 210,212,213
s_waitcnt lgkmcnt(8)
Mp 244,56,48
v_lshlrev_b32 v149, 16, v149
Cv 134,134,134
v_lshlrev_b32 v134, 16, v134
Mz 96,0,224
Rb 48,1,45056
buffer_load_dword v4, v5, s[20:23], s50 offen offset:0 sc0 sc1
buffer_store_dwordx4 v[184:187], v3, s[20:23], s64 offen offset:3072 sc0 sc1
Mv 96,4,232
Cv 150,150,150
v_lshlrev_b32 v150, 16, v150
Cv 211,214,215
Mv 96,8,240
Rb 52,1,46080
s_add_u32 m0, s36, 65536
v_exp_f32_e32 v40, v80
Mv 96,12,248
buffer_load_dwordx4 v2, s[20:23], s56 offen offset:0 sc1 lds
Cv 216,216,217
)" R"(
v_add_f32_e32 v14, v14, v40
Mz 100,12,252
Rb 56,1,47104
Cv 135,135,135
v_accvgpr_mov_b32 a92, a252
Mv 100,8,244
v_lshlrev_b32 v135, 16, v135
Cv 151,151,151
Cv 217,218,219
Mv 100,4,236
Rb 60,1,48128
s_add_u32 s64, s64, 0x10000
buffer_store_dwordx4 v[192:195], v2, s[20:23], s64 offen offset:0 sc0 sc1
s_waitcnt lgkmcnt(8)
Mv 100,0,228
v_lshlrev_b32 v151, 16, v151
v_mul_f32_e32 v140, 0xbfb8aa3b, v132
Cv 218,220,221
Mv 100,16,196
Rb 0,1,49152
v_mul_f32_e32 v141, 0xbfb8aa3b, v133
Cv 219,222,223
Mv 100,20,204
v_exp_f32_e32 v140, v140
v_exp_f32_e32 v24, v81
Mv 100,24,212
Rb 4,1,50176
v_mul_f32_e32 v142, 0xbfb8aa3b, v134
Cv 224,224,225
v_add_f32_e32 v14, v14, v24
Mv 100,28,220
buffer_load_dwordx4 v2, s[20:23], s56 offen offset:1024 sc1 lds
v_exp_f32_e32 v41, v82
Mv 96,28,216
Rb 8,1,51200
v_mul_f32_e32 v143, 0xbfb8aa3b, v135
buffer_store_dwordx4 v[200:203], v2, s[20:23], s64 offen offset:1024 sc0 sc1
Mv 96,24,208
v_exp_f32_e32 v141, v141
v_exp_f32_e32 v25, v83
Mv 96,20,200
Rb 12,1,52224
Cv 225,226,227
Cv 40,40,24
s_waitcnt lgkmcnt(8)
Mv 96,16,192
v_exp_f32_e32 v142, v142
v_add_f32_e32 v140, 1.0, v140
v_add_f32_e32 v14, v14, v41
Mz 104,32,224
Rb 16,1,57344
v_add_f32_e32 v141, 1.0, v141
v_add_f32_e32 v14, v14, v25
Mv 104,36,232
v_exp_f32_e32 v143, v143
Cv 226,228,229
Cv 41,41,25
Mv 104,40,240
Rb 20,1,58368
v_add_f32_e32 v142, 1.0, v142
buffer_store_dwordx4 v[208:211], v2, s[20:23], s64 offen offset:2048 sc0 sc1
Mv 104,44,248
buffer_load_dwordx4 v2, s[20:23], s56 offen offset:2048 sc1 lds
Cv 227,230,231
)" R"(
Mz 108,44,252
Rb 24,1,59392
v_add_f32_e32 v143, 1.0, v143
v_accvgpr_mov_b32 a93, a253
Mv 108,40,244
v_rcp_f32_e32 v140, v140
Cv 232,232,233
Mv 108,36,236
Rb 28,1,60416
v_exp_f32_e32 v42, v88
s_waitcnt lgkmcnt(8)
Mv 108,32,228
v_rcp_f32_e32 v141, v141
Cv 233,234,235
v_add_f32_e32 v14, v14, v42
Mv 108,48,196
Rb 32,1,53248
v_mul_f32_e32 v132, v132, v140
buffer_store_dwordx4 v[216:219], v2, s[20:23], s64 offen offset:3072 sc0 sc1
Mv 108,52,204
v_rcp_f32_e32 v142, v142
v_exp_f32_e32 v26, v89
Mv 108,56,212
Rb 36,1,54272
v_mul_f32_e32 v133, v133, v141
Cv 234,236,237
v_add_f32_e32 v14, v14, v26
Mv 108,60,220
buffer_load_dwordx4 v2, s[20:23], s56 offen offset:3072 sc1 lds
v_exp_f32_e32 v43, v90
Mv 104,60,216
Rb 40,1,55296
v_mul_f32_e32 v134, v134, v142
Cv 235,238,239
Mv 104,56,208
v_rcp_f32_e32 v143, v143
v_exp_f32_e32 v27, v91
Mv 104,52,200
Rb 44,1,56320
buffer_store_dwordx4 v[224:227], v3, s[20:23], s64 offen offset:0 sc0 sc1
s_waitcnt lgkmcnt(8)
Mv 104,48,192
v_mul_f32_e32 v135, v135, v143
Cv 132,132,132
v_lshlrev_b32 v132, 16, v132
Cv 42,42,26
Mz 112,0,224
Rb 48,1,61440
Cv 133,133,133
v_add_f32_e32 v14, v14, v43
Mv 112,4,232
v_lshlrev_b32 v133, 16, v133
Cv 134,134,134
Cv 240,240,241
v_add_f32_e32 v14, v14, v27
Mv 112,8,240
Rb 52,1,62464
s_add_u32 m0, s36, 69632
v_exp_f32_e32 v44, v84
Mv 112,12,248
buffer_load_dwordx4 v3, s[20:23], s56 offen offset:0 sc1 lds
Cv 241,242,243
Cv 43,43,27
Mz 116,12,252
)" R"(
Rb 56,1,63488
v_lshlrev_b32 v134, 16, v134
buffer_store_dwordx4 v[232:235], v3, s[20:23], s64 offen offset:1024 sc0 sc1
Mv 116,8,244
Cv 135,135,135
v_lshlrev_b32 v135, 16, v135
Cv 242,244,245
v_add_f32_e32 v15, v15, v44
Mv 116,4,236
Rb 60,1,64512
v_exp_f32_e32 v28, v85
s_waitcnt lgkmcnt(8)
Mv 116,0,228
v_mul_f32_e32 v132, v132, v148
v_mul_f32_e32 v133, v133, v149
Cv 243,246,247
v_add_f32_e32 v15, v15, v28
Mv 116,16,196
v_mul_f32_e32 v134, v134, v150
v_mul_f32_e32 v135, v135, v151
Cv 248,248,249
Mv 116,20,204
Cv 140,132,133
Cv 141,134,135
v_exp_f32_e32 v45, v86
Mv 116,24,212
v_accvgpr_write_b32 a188, v140
v_accvgpr_write_b32 a189, v141
buffer_store_dwordx4 v[240:243], v3, s[20:23], s64 offen offset:2048 sc0 sc1
Mv 116,28,220
buffer_load_dwordx4 v3, s[20:23], s56 offen offset:1024 sc1 lds
v_exp_f32_e32 v29, v87
Mv 112,28,216
v_lshrrev_b32 v156, 2, v0
v_mul_u32_u24 v156, 864, v156
Cv 249,250,251
Mv 112,24,208
v_lshl_add_u32 v156, v0, 3, v156
s_mul_i32 s92, s33, 224
v_exp_f32_e32 v46, v92
Mv 112,20,200
s_add_u32 s92, s92, 131072
v_add_u32 v156, s92, v156
Cv 250,252,253
Cv 44,44,28
Mv 112,16,192
Cv 251,254,255
v_add_f32_e32 v15, v15, v45
s_waitcnt vmcnt(31) lgkmcnt(0)
s_barrier
Mz 120,32,224
Rb 0,8,32768
Rt 156,156,0
buffer_store_dwordx4 v[248:251], v3, s[20:23], s64 offen offset:3072 sc0 sc1
Mv 120,36,232
Rb 4,8,49152
Rt 158,156,32
v_exp_f32_e32 v24, v93
Mv 120,40,240
Rb 8,8,33792
Rt 160,156,64
v_exp_f32_e32 v47, v94
Mv 120,44,248
Rb 12,8,50176
buffer_load_dwordx4 v3, s[20:23], s56 offen offset:2048 sc1 lds
)" R"(
Rt 162,156,96
Mz 124,44,252
Rb 16,8,34816
Rt 164,156,128
v_add_f32_e32 v15, v15, v29
Mv 124,40,244
Rb 20,8,51200
Rt 166,156,160
v_exp_f32_e32 v25, v95
Mv 124,36,236
Rb 24,8,35840
Rt 168,156,192
Cv 45,45,29
v_add_f32_e32 v15, v15, v46
Mv 124,32,228
Rb 28,8,52224
v_add_f32_e32 v15, v15, v24
Cv 46,46,24
v_add_f32_e32 v15, v15, v47
Mv 124,48,196
Rb 32,8,36864
v_add_f32_e32 v15, v15, v25
Cv 47,47,25
Mv 124,52,204
Rb 36,8,53248
v_max_f32_e32 v11, v14, v15
v_mov_b32 v24, v14
v_mov_b32 v25, v15
Mv 124,56,212
Rb 40,8,37888
v_permlane32_swap_b32_e32 v24, v25
v_add_f32_e32 v24, v24, v25
v_mov_b32 v25, v24
Mv 124,60,220
Rb 44,8,54272
buffer_load_dwordx4 v3, s[20:23], s56 offen offset:3072 sc1 lds
v_permlane16_swap_b32_e32 v24, v25
Mv 120,60,216
v_add_f32_e32 v24, v24, v25
v_rcp_f32_e32 v24, v24
Mv 120,56,208
s_add_u32 s56, s56, 0x10000
v_max_f32_e32 v11, v11, v24
v_cmp_nge_f32_e32 vcc, s62, v11
s_add_u32 s55, s55, 0x10000
Mv 120,52,200
s_cmp_eq_u32 s57, 2
s_cselect_b32 s55, s59, s55
s_waitcnt lgkmcnt(12)
s_cbranch_vccnz .Lzfx4
.Lzfr4:
Mv 120,48,192
v_mov_b32 v12, v14
v_mov_b32 v13, v15
v_accvgpr_mov_b32 a94, a254
v_mfma_f32_16x16x32_bf16 v[128:131], a[0:3], v[32:35], 0
Rb 48,8,38912
v_accvgpr_mov_b32 a95, a255
Mp 128,4,40
v_accvgpr_mov_b32 a96, a192
s_cmp_le_i32 s57, s58
s_cbranch_scc1 .Lzmk5
.Lzmr5:
v_mfma_f32_16x16x32_bf16 v[136:139], a[4:7], v[44:47], 0
)" R"(
Rb 52,8,55296
s_add_u32 m0, s36, 32768
v_accvgpr_mov_b32 a97, a193
Mp 136,0,36
buffer_load_dwordx4 v2, s[20:23], s55 offen offset:0 sc1 lds
v_accvgpr_mov_b32 a98, a194
v_mfma_f32_16x16x32_bf16 v[140:143], a[8:11], v[36:39], 0
Rb 56,8,39936
v_and_b32 v11, 14, v0
v_accvgpr_mov_b32 a99, a195
Mp 140,12,44
v_min_u32 v11, 1, v11
v_lshlrev_b32 v11, 13, v11
v_accvgpr_mov_b32 a100, a196
v_mfma_f32_16x16x32_bf16 v[132:135], a[12:15], v[40:43], 0
Rb 60,8,56320
v_accvgpr_mov_b32 a101, a197
s_waitcnt lgkmcnt(8)
Mp 132,8,32
v_mfma_f32_16x16x16_bf16 a[128:131], a[156:157], a[188:189], 0
v_and_or_b32 v11, v0, 1, v11
v_mul_u32_u24 v11, 0x31000, v11
v_accvgpr_mov_b32 a102, a198
v_mfma_f32_16x16x32_bf16 v[144:147], a[16:19], v[32:35], 0
v_mfma_f32_16x16x16_bf16 a[132:135], a[158:159], a[188:189], 0
Rb 0,8,40960
v_and_or_b32 v11, v0, 48, v11
v_accvgpr_mov_b32 a103, a199
Mp 144,20,40
v_mfma_f32_16x16x16_bf16 a[136:139], a[160:161], a[188:189], 0
s_mul_i32 s83, s33, 50176
s_lshl_b32 s92, s82, 6
v_accvgpr_mov_b32 a104, a200
v_mfma_f32_16x16x32_bf16 v[152:155], a[20:23], v[44:47], 0
v_mfma_f32_16x16x16_bf16 a[140:143], a[162:163], a[188:189], 0
Rb 4,8,57344
s_add_u32 s83, s83, s92
v_accvgpr_mov_b32 a105, a201
Mp 152,16,36
v_mfma_f32_16x16x16_bf16 a[144:147], a[164:165], a[188:189], 0
buffer_load_dwordx4 v2, s[20:23], s55 offen offset:1024 sc1 lds
v_accvgpr_mov_b32 a106, a202
v_mfma_f32_16x16x32_bf16 v[156:159], a[24:27], v[36:39], 0
v_mfma_f32_16x16x16_bf16 a[148:151], a[166:167], a[188:189], 0
Rb 8,8,41984
s_add_u32 s83, s83, 0x1558000
v_accvgpr_mov_b32 a107, a203
Mp 156,28,44
v_mfma_f32_16x16x16_bf16 a[152:155], a[168:169], a[188:189], 0
v_exp_f32_e32 v48, v96
v_accvgpr_mov_b32 a108, a204
v_mfma_f32_16x16x32_bf16 v[148:151], a[28:31], v[40:43], 0
Rb 12,8,58368
v_accvgpr_mov_b32 a109, a205
s_waitcnt lgkmcnt(8)
Mp 148,24,32
v_exp_f32_e32 v26, v97
v_accvgpr_mov_b32 a110, a206
v_mfma_f32_16x16x32_bf16 v[160:163], a[32:35], v[32:35], 0
Rb 16,8,43008
v_add_f32_e32 v14, v48, v26
Mp 160,36,40
v_exp_f32_e32 v49, v98
Cv 48,48,26
)" R"(
v_mfma_f32_16x16x32_bf16 v[168:171], a[36:39], v[44:47], 0
Rb 20,8,59392
v_add_f32_e32 v14, v14, v49
v_accvgpr_mov_b32 a111, a207
Mp 168,32,36
buffer_load_dwordx4 v2, s[20:23], s55 offen offset:2048 sc1 lds
v_accvgpr_mov_b32 a112, a208
v_mfma_f32_16x16x32_bf16 v[172:175], a[40:43], v[36:39], 0
Rb 24,8,44032
v_exp_f32_e32 v27, v99
Mp 172,44,44
buffer_store_dwordx4 a[128:131], v11, s[20:23], s83 offen sc0 sc1
v_accvgpr_mov_b32 a113, a209
v_mfma_f32_16x16x32_bf16 v[164:167], a[44:47], v[40:43], 0
Rb 28,8,60416
v_accvgpr_mov_b32 a114, a210
s_waitcnt lgkmcnt(8)
Mp 164,40,32
s_add_u32 s83, s83, 7168
v_exp_f32_e32 v50, v104
v_mfma_f32_16x16x32_bf16 v[176:179], a[48:51], v[32:35], 0
Rb 32,8,45056
v_add_f32_e32 v14, v14, v27
v_accvgpr_mov_b32 a115, a211
Mp 176,52,40
v_exp_f32_e32 v28, v105
Cv 49,49,27
v_mfma_f32_16x16x32_bf16 v[184:187], a[52:55], v[44:47], 0
Rb 36,8,61440
v_add_f32_e32 v14, v14, v50
v_accvgpr_mov_b32 a116, a212
Mp 184,48,36
buffer_load_dwordx4 v2, s[20:23], s55 offen offset:3072 sc1 lds
v_accvgpr_mov_b32 a117, a213
v_mfma_f32_16x16x32_bf16 v[188:191], a[56:59], v[36:39], 0
Rb 40,8,46080
v_exp_f32_e32 v51, v106
Mp 188,60,44
buffer_store_dwordx4 a[132:135], v11, s[20:23], s83 offen sc0 sc1
v_accvgpr_mov_b32 a118, a214
v_mfma_f32_16x16x32_bf16 v[180:183], a[60:63], v[40:43], 0
Rb 44,8,62464
v_accvgpr_mov_b32 a119, a215
s_waitcnt lgkmcnt(8)
Mp 180,56,32
s_add_u32 s83, s83, 7168
v_exp_f32_e32 v29, v107
v_mfma_f32_16x16x32_bf16 v[192:195], a[0:3], v[32:35], 0
Rb 48,8,47104
v_add_f32_e32 v14, v14, v28
Mp 192,4,40
buffer_store_dwordx4 a[136:139], v11, s[20:23], s83 offen sc0 sc1
s_add_u32 s83, s83, 7168
v_mfma_f32_16x16x32_bf16 v[200:203], a[4:7], v[44:47], 0
Rb 52,8,63488
s_add_u32 m0, s36, 36864
v_accvgpr_mov_b32 a120, a216
Mp 200,0,36
buffer_load_dwordx4 v3, s[20:23], s55 offen offset:0 sc1 lds
v_accvgpr_mov_b32 a121, a217
v_mfma_f32_16x16x32_bf16 v[204:207], a[8:11], v[36:39], 0
Rb 56,8,48128
v_exp_f32_e32 v52, v100
Mp 204,12,44
)" R"(
v_exp_f32_e32 v24, v101
v_accvgpr_mov_b32 a122, a218
v_mfma_f32_16x16x32_bf16 v[196:199], a[12:15], v[40:43], 0
Rb 60,8,64512
v_accvgpr_mov_b32 a123, a219
s_waitcnt lgkmcnt(8)
Mp 196,8,32
buffer_store_dwordx4 a[140:143], v11, s[20:23], s83 offen sc0 sc1
Cv 50,50,28
v_mfma_f32_16x16x32_bf16 v[208:211], a[16:19], v[32:35], 0
s_add_u32 s83, s83, 7168
v_add_f32_e32 v14, v14, v51
v_accvgpr_mov_b32 a124, a220
Mp 208,20,40
v_exp_f32_e32 v53, v102
v_add_f32_e32 v14, v14, v29
v_mfma_f32_16x16x32_bf16 v[216:219], a[20:23], v[44:47], 0
v_exp_f32_e32 v25, v103
v_accvgpr_mov_b32 a125, a221
Mp 216,16,36
buffer_load_dwordx4 v3, s[20:23], s55 offen offset:1024 sc1 lds
v_accvgpr_mov_b32 a126, a222
v_mfma_f32_16x16x32_bf16 v[220:223], a[24:27], v[36:39], 0
v_exp_f32_e32 v54, v108
Cv 51,51,29
Mp 220,28,44
buffer_store_dwordx4 a[144:147], v11, s[20:23], s83 offen sc0 sc1
v_accvgpr_mov_b32 a127, a223
v_mfma_f32_16x16x32_bf16 v[212:215], a[28:31], v[40:43], 0
s_add_u32 s83, s83, 7168
v_add_f32_e32 v15, v52, v24
Mp 212,24,32
Cv 52,52,24
s_waitcnt vmcnt(13) lgkmcnt(0)
s_barrier
buffer_store_dword v6, v7, s[20:23], s65 offen offset:0 sc0 sc1
v_mfma_f32_16x16x32_bf16 v[224:227], a[32:35], v[32:35], 0
Rb 0,1,0
v_add_f32_e32 v15, v15, v53
Mp 224,36,40
Rb 4,1,1024
v_exp_f32_e32 v26, v109
v_mfma_f32_16x16x32_bf16 v[232:235], a[36:39], v[44:47], 0
Rb 8,1,2048
v_add_f32_e32 v15, v15, v25
Mp 232,32,36
Rb 12,1,3072
buffer_load_dwordx4 v3, s[20:23], s55 offen offset:2048 sc1 lds
v_mfma_f32_16x16x32_bf16 v[236:239], a[40:43], v[36:39], 0
Rb 16,1,8192
v_exp_f32_e32 v55, v110
Mp 236,44,44
Rb 20,1,9216
Cv 53,53,25
v_mfma_f32_16x16x32_bf16 v[228:231], a[44:47], v[40:43], 0
Rb 24,1,10240
buffer_store_dwordx4 a[148:151], v11, s[20:23], s83 offen sc0 sc1
Mp 228,40,32
Rb 28,1,11264
s_add_u32 s83, s83, 7168
v_add_f32_e32 v15, v15, v54
v_mfma_f32_16x16x32_bf16 v[240:243], a[48:51], v[32:35], 0
Rb 32,1,4096
v_add_f32_e32 v15, v15, v26
Mp 240,52,40
)" R"(
Rb 36,1,5120
v_exp_f32_e32 v27, v111
v_mfma_f32_16x16x32_bf16 v[248:251], a[52:55], v[44:47], 0
Rb 40,1,6144
Cv 54,54,26
Mp 248,48,36
Rb 44,1,7168
buffer_load_dwordx4 v3, s[20:23], s55 offen offset:3072 sc1 lds
v_mfma_f32_16x16x32_bf16 v[252:255], a[56:59], v[36:39], 0
v_cmp_eq_u32_e32 vcc, s3, v4
s_andn2_b64 s[28:29], -1, vcc
s_cbranch_scc1 .Lzsp10
.Lzok10:
Mp 252,60,44
buffer_store_dwordx4 a[152:155], v11, s[20:23], s83 offen sc0 sc1
v_mfma_f32_16x16x32_bf16 v[244:247], a[60:63], v[40:43], 0
v_add_f32_e32 v15, v15, v55
s_waitcnt lgkmcnt(8)
Mp 244,56,32
v_add_f32_e32 v15, v15, v27
Cv 55,55,27
.LzH40:
s_cbranch_execz .LzHR
s_sub_u32 s57, s57, 1
s_cmp_gt_i32 s57, s93
s_cbranch_scc1 .LzL
.LzH41:
s_cbranch_execz .LzHR
.LzDA:
s_waitcnt vmcnt(8) lgkmcnt(0)
s_barrier
Rb 0,8,0
Rb 4,8,16384
Rb 8,8,1024
Rb 12,8,17408
Rb 16,8,2048
Rb 20,8,18432
Rb 24,8,3072
Rb 28,8,19456
Rb 32,8,4096
Rb 36,8,20480
Rb 40,8,5120
Rb 44,8,21504
v_exp_f32_e32 v56, v112
v_exp_f32_e32 v28, v113
v_add_f32_e32 v14, v14, v56
v_add_f32_e32 v14, v14, v28
Cv 56,56,28
v_exp_f32_e32 v57, v114
v_exp_f32_e32 v29, v115
v_add_f32_e32 v14, v14, v57
v_add_f32_e32 v14, v14, v29
Cv 57,57,29
v_exp_f32_e32 v58, v120
v_exp_f32_e32 v24, v121
v_add_f32_e32 v14, v14, v58
v_add_f32_e32 v14, v14, v24
Cv 58,58,24
v_exp_f32_e32 v59, v122
v_exp_f32_e32 v25, v123
v_add_f32_e32 v14, v14, v59
v_add_f32_e32 v14, v14, v25
Cv 59,59,25
v_exp_f32_e32 v60, v116
v_exp_f32_e32 v26, v117
)" R"(
v_add_f32_e32 v15, v15, v60
v_add_f32_e32 v15, v15, v26
Cv 60,60,26
v_exp_f32_e32 v61, v118
v_exp_f32_e32 v27, v119
v_add_f32_e32 v15, v15, v61
v_add_f32_e32 v15, v15, v27
Cv 61,61,27
v_exp_f32_e32 v62, v124
v_exp_f32_e32 v28, v125
v_add_f32_e32 v15, v15, v62
v_add_f32_e32 v15, v15, v28
Cv 62,62,28
v_exp_f32_e32 v63, v126
v_exp_f32_e32 v29, v127
v_add_f32_e32 v15, v15, v63
v_add_f32_e32 v15, v15, v29
Cv 63,63,29
v_max_f32_e32 v11, v14, v15
v_cmp_nge_f32_e32 vcc, s62, v11
s_cbranch_vccnz .Lzfx5
.Lzfr5:
s_waitcnt lgkmcnt(8)
Mp 128,0,48
Rb 48,8,6144
s_add_u32 m0, s36, 131072
s_add_u32 s40, s67, 0x20000
s_lshl_b32 s82, s82, 7
Mp 128,4,56
buffer_load_dwordx4 v2, s[20:23], s40 offen offset:0 sc1 lds
v_add_f32_e32 v12, v12, v14
s_add_u32 s82, s82, 0x150c000
Mp 136,4,60
Rb 52,8,22528
v_add_f32_e32 v13, v13, v15
v_mov_b32 v24, v12
Mp 136,0,52
buffer_load_dwordx4 v2, s[20:23], s40 offen offset:1024 sc1 lds
v_permlane32_swap_b32_e32 v12, v24
Mp 140,8,52
Rb 56,8,7168
v_add_f32_e32 v12, v12, v24
v_mov_b32 v24, v12
Mp 140,12,60
s_nop 0
v_permlane16_swap_b32_e32 v12, v24
v_add_f32_e32 v12, v12, v24
v_mov_b32 v25, v13
Mp 132,12,56
Rb 60,8,23552
v_permlane32_swap_b32_e32 v13, v25
s_waitcnt lgkmcnt(8)
Mp 132,8,48
buffer_load_dwordx4 v2, s[20:23], s40 offen offset:2048 sc1 lds
v_add_f32_e32 v13, v13, v25
Mp 144,16,48
Rb 0,8,8192
v_rcp_f32_e32 v12, v12
Mp 144,20,56
buffer_load_dwordx4 v2, s[20:23], s40 offen offset:3072 sc1 lds
v_mov_b32 v25, v13
Mp 152,20,60
Rb 4,8,24576
v_permlane16_swap_b32_e32 v13, v25
)" R"(
v_add_f32_e32 v13, v13, v25
Mp 152,16,52
v_rcp_f32_e32 v13, v13
v_mul_f32_e32 v128, v12, v128
Mp 156,24,52
Rb 8,8,9216
s_add_u32 m0, s36, 135168
v_mul_f32_e32 v129, v12, v129
Mp 156,28,60
buffer_load_dwordx4 v3, s[20:23], s40 offen offset:0 sc1 lds
Cv 128,128,129
Mp 148,28,56
Rb 12,8,25600
v_mul_f32_e32 v130, v12, v130
s_waitcnt lgkmcnt(8)
Mp 148,24,48
buffer_load_dwordx4 v3, s[20:23], s40 offen offset:1024 sc1 lds
v_mul_f32_e32 v131, v12, v131
Mp 160,32,48
Rb 16,8,10240
Cv 129,130,131
s_cmp_lg_u32 s33, 0
s_cmov_b32 s82, 0x40000000
Mp 160,36,56
v_mul_f32_e32 v132, v12, v132
v_mul_f32_e32 v133, v12, v133
Cv 130,132,133
Mp 168,36,60
Rb 20,8,26624
v_mul_f32_e32 v134, v12, v134
v_mul_f32_e32 v135, v12, v135
Mp 168,32,52
buffer_load_dwordx4 v3, s[20:23], s40 offen offset:2048 sc1 lds
Cv 131,134,135
Mp 172,40,52
Rb 24,8,11264
s_add_u32 m0, s36, 98304
s_add_u32 s40, s67, 0x30000
Mp 172,44,60
buffer_load_dwordx4 v2, s[20:23], s40 offen offset:0 sc1 lds
v_mul_f32_e32 v136, v13, v136
Mp 164,44,56
Rb 28,8,27648
v_mul_f32_e32 v137, v13, v137
s_waitcnt lgkmcnt(8)
Mp 164,40,48
Cv 136,136,137
v_mul_f32_e32 v138, v13, v138
v_mul_f32_e32 v139, v13, v139
Mp 176,48,48
Rb 32,8,12288
Cv 137,138,139
v_mul_f32_e32 v140, v13, v140
Mp 176,52,56
buffer_load_dwordx4 v2, s[20:23], s40 offen offset:1024 sc1 lds
v_mul_f32_e32 v141, v13, v141
Mp 184,52,60
Rb 36,8,28672
Cv 138,140,141
v_mul_f32_e32 v142, v13, v142
Mp 184,48,52
buffer_load_dwordx4 v2, s[20:23], s40 offen offset:2048 sc1 lds
v_mul_f32_e32 v143, v13, v143
Mp 188,56,52
)" R"(
Rb 40,8,13312
Cv 139,142,143
v_mul_f32_e32 v144, v12, v144
Mp 188,60,60
v_mul_f32_e32 v145, v12, v145
Cv 144,144,145
v_mul_f32_e32 v146, v12, v146
Mp 180,60,56
Rb 44,8,29696
v_mul_f32_e32 v147, v12, v147
s_waitcnt lgkmcnt(8)
Mp 180,56,48
buffer_load_dwordx4 v2, s[20:23], s40 offen offset:3072 sc1 lds
Cv 145,146,147
Mp 192,0,48
Rb 48,8,14336
s_add_u32 m0, s36, 102400
s_cmp_eq_u32 s35, 7
s_cmov_b32 s82, 0x40000000
Mp 192,4,56
buffer_load_dwordx4 v3, s[20:23], s40 offen offset:0 sc1 lds
v_mul_f32_e32 v148, v12, v148
Mp 200,4,60
Rb 52,8,30720
v_mul_f32_e32 v149, v12, v149
Cv 146,148,149
Mp 200,0,52
v_mul_f32_e32 v150, v12, v150
v_mul_f32_e32 v151, v12, v151
Cv 147,150,151
Mp 204,8,52
Rb 56,8,15360
v_mul_f32_e32 v152, v13, v152
v_mul_f32_e32 v153, v13, v153
Mp 204,12,60
buffer_load_dwordx4 v3, s[20:23], s40 offen offset:1024 sc1 lds
Cv 152,152,153
Mp 196,12,56
Rb 60,8,31744
v_mul_f32_e32 v154, v13, v154
s_waitcnt lgkmcnt(8)
Mp 196,8,48
buffer_load_dwordx4 v3, s[20:23], s40 offen offset:2048 sc1 lds
v_mul_f32_e32 v155, v13, v155
Mp 208,16,48
Rb 0,1,0
Cv 153,154,155
v_mul_f32_e32 v156, v13, v156
Mp 208,20,56
v_mul_f32_e32 v157, v13, v157
Cv 154,156,157
v_mul_f32_e32 v158, v13, v158
Mp 216,20,60
Rb 4,1,1024
v_mul_f32_e32 v159, v13, v159
Cv 155,158,159
Mp 216,16,52
v_mul_f32_e32 v160, v12, v160
v_mul_f32_e32 v161, v12, v161
Cv 160,160,161
Mp 220,24,52
Rb 8,1,2048
v_mul_f32_e32 v162, v12, v162
v_mul_f32_e32 v163, v12, v163
)" R"(
Mp 220,28,60
Cv 161,162,163
v_mul_f32_e32 v164, v12, v164
v_mul_f32_e32 v165, v12, v165
Mp 212,28,56
Rb 12,1,3072
s_waitcnt vmcnt(14) lgkmcnt(4)
s_barrier
Mp 212,24,48
s_add_u32 s40, s67, 0x40000
Cv 162,164,165
v_mul_f32_e32 v166, v12, v166
Mp 224,32,48
Rb 16,1,4096
s_add_u32 m0, s36, 65536
Mp 224,36,56
buffer_load_dwordx4 v2, s[20:23], s40 offen offset:0 sc1 lds
v_mul_f32_e32 v167, v12, v167
Mp 232,36,60
Rb 20,1,5120
Cv 163,166,167
v_mul_f32_e32 v168, v13, v168
Mp 232,32,52
v_mul_f32_e32 v169, v13, v169
Cv 168,168,169
v_mul_f32_e32 v170, v13, v170
Mp 236,40,52
Rb 24,1,6144
v_mul_f32_e32 v171, v13, v171
Cv 169,170,171
Mp 236,44,60
v_mul_f32_e32 v172, v13, v172
v_mul_f32_e32 v173, v13, v173
Cv 170,172,173
Mp 228,44,56
Rb 28,1,8192
v_mul_f32_e32 v174, v13, v174
Mp 228,40,48
buffer_load_dwordx4 v2, s[20:23], s40 offen offset:1024 sc1 lds
v_mul_f32_e32 v175, v13, v175
s_lshl_b32 s64, s35, 17
Mp 240,48,48
Rb 32,1,9216
Cv 171,174,175
v_mul_f32_e32 v176, v12, v176
Mp 240,52,56
v_mul_f32_e32 v177, v12, v177
Cv 176,176,177
v_mul_f32_e32 v178, v12, v178
Mp 248,52,60
Rb 36,1,10240
v_mul_f32_e32 v179, v12, v179
Cv 177,178,179
Mp 248,48,52
v_mul_f32_e32 v180, v12, v180
v_mul_f32_e32 v181, v12, v181
Cv 178,180,181
Mp 252,56,52
Rb 40,1,11264
v_mul_f32_e32 v182, v12, v182
v_mul_f32_e32 v183, v12, v183
s_add_u32 s64, s64, s89
Mp 252,60,60
buffer_load_dwordx4 v2, s[20:23], s40 offen offset:2048 sc1 lds
Cv 179,182,183
)" R"(
Mp 244,60,56
Rb 44,1,12288
v_mul_f32_e32 v184, v13, v184
s_mov_b32 s55, 0x1508c00
s_waitcnt lgkmcnt(8)
Mp 244,56,48
v_mul_f32_e32 v185, v13, v185
Cv 184,184,185
v_mul_f32_e32 v186, v13, v186
v_mfma_f32_16x16x32_bf16 v[32:35], a[0:3], v[128:131], 0
Rb 48,1,13312
v_mul_f32_e32 v187, v13, v187
s_add_u32 s64, s64, 0x800000
v_mfma_f32_16x16x32_bf16 v[40:43], a[0:3], v[136:139], 0
Cv 185,186,187
v_mul_f32_e32 v188, v13, v188
v_mul_f32_e32 v189, v13, v189
s_xor_b32 s66, s64, 0x202000
v_mfma_f32_16x16x32_bf16 v[44:47], a[4:7], v[136:139], 0
Rb 52,1,14336
Cv 186,188,189
v_mul_f32_e32 v190, v13, v190
v_mfma_f32_16x16x32_bf16 v[36:39], a[4:7], v[128:131], 0
buffer_load_dwordx4 v2, s[20:23], s40 offen offset:3072 sc1 lds
v_mul_f32_e32 v191, v13, v191
v_mfma_f32_16x16x32_bf16 v[48:51], a[8:11], v[128:131], 0
Rb 56,1,16384
Cv 187,190,191
v_mul_f32_e32 v192, v12, v192
s_cmp_lg_u32 s33, 0
s_cmov_b32 s55, 0x40000000
v_mfma_f32_16x16x32_bf16 v[56:59], a[8:11], v[136:139], 0
v_mul_f32_e32 v193, v12, v193
Cv 192,192,193
v_mul_f32_e32 v194, v12, v194
v_mov_b32 v16, 1
v_mfma_f32_16x16x32_bf16 v[60:63], a[12:15], v[136:139], 0
Rb 60,1,17408
v_mul_f32_e32 v195, v12, v195
v_add_u32 v9, s36, v8
s_waitcnt lgkmcnt(8)
v_mfma_f32_16x16x32_bf16 v[52:55], a[12:15], v[128:131], 0
Cv 193,194,195
v_mul_f32_e32 v196, v12, v196
v_mul_f32_e32 v197, v12, v197
s_sub_u32 s66, s66, s84
Mp 32,16,144
Rb 0,1,18432
s_add_u32 m0, s36, 69632
Cv 194,196,197
Mp 40,16,152
buffer_load_dwordx4 v3, s[20:23], s40 offen offset:0 sc1 lds
v_mul_f32_e32 v198, v12, v198
Mp 44,20,152
Rb 4,1,19456
v_mul_f32_e32 v199, v12, v199
Cv 195,198,199
Mp 36,20,144
v_mul_f32_e32 v200, v13, v200
v_mul_f32_e32 v201, v13, v201
Cv 200,200,201
Mp 48,24,144
Rb 8,1,20480
v_mul_f32_e32 v202, v13, v202
)" R"(
v_mul_f32_e32 v203, v13, v203
s_sub_u32 s66, s66, s84
Mp 56,24,152
Cv 201,202,203
v_mul_f32_e32 v204, v13, v204
v_mul_f32_e32 v205, v13, v205
Mp 60,28,152
Rb 12,1,21504
Cv 202,204,205
s_waitcnt lgkmcnt(8)
Mp 52,28,144
buffer_load_dwordx4 v3, s[20:23], s40 offen offset:1024 sc1 lds
v_mul_f32_e32 v206, v13, v206
Mp 32,32,160
Rb 16,1,22528
v_mul_f32_e32 v207, v13, v207
s_lshl2_add_u32 s65, s35, s33
Mp 40,32,168
Cv 203,206,207
v_mul_f32_e32 v208, v12, v208
v_mul_f32_e32 v209, v12, v209
s_lshl_b32 s65, s65, 8
Mp 44,36,168
Rb 20,1,24576
Cv 208,208,209
v_mul_f32_e32 v210, v12, v210
Mp 36,36,160
v_mul_f32_e32 v211, v12, v211
Cv 209,210,211
v_mul_f32_e32 v212, v12, v212
Mp 48,40,160
Rb 24,1,25600
v_mul_f32_e32 v213, v12, v213
Cv 210,212,213
Mp 56,40,168
buffer_load_dwordx4 v3, s[20:23], s40 offen offset:2048 sc1 lds
v_mul_f32_e32 v214, v12, v214
Mp 60,44,168
Rb 28,1,26624
v_mul_f32_e32 v215, v12, v215
s_waitcnt lgkmcnt(8)
Mp 52,44,160
Cv 211,214,215
v_mul_f32_e32 v216, v13, v216
v_mul_f32_e32 v217, v13, v217
s_add_u32 s65, s65, s50
Mp 32,48,176
Rb 32,1,27648
Cv 216,216,217
v_mul_f32_e32 v218, v13, v218
Mp 40,48,184
v_mul_f32_e32 v219, v13, v219
Cv 217,218,219
v_mul_f32_e32 v220, v13, v220
Mp 44,52,184
Rb 36,1,28672
v_mul_f32_e32 v221, v13, v221
Cv 218,220,221
Mp 36,52,176
v_mul_f32_e32 v222, v13, v222
v_mul_f32_e32 v223, v13, v223
Cv 219,222,223
Mp 48,56,176
Rb 40,1,29696
)" R"(
v_mul_f32_e32 v224, v12, v224
v_mul_f32_e32 v225, v12, v225
s_add_u32 s65, s65, 0x2400
Mp 56,56,184
Cv 224,224,225
v_mul_f32_e32 v226, v12, v226
v_mul_f32_e32 v227, v12, v227
Mp 60,60,184
Rb 44,1,30720
Cv 225,226,227
buffer_atomic_add v16, v7, s[20:23], s55 offen sc0 sc1
s_waitcnt lgkmcnt(8)
Mp 52,60,176
v_mul_f32_e32 v228, v12, v228
v_mul_f32_e32 v229, v12, v229
Cv 226,228,229
Mp 32,0,192
Rb 48,1,32768
v_mul_f32_e32 v230, v12, v230
s_xor_b32 s52, s65, 0x202000
Mp 40,0,200
v_mul_f32_e32 v231, v12, v231
Cv 227,230,231
v_mul_f32_e32 v232, v13, v232
Mp 44,4,200
Rb 52,1,33792
v_mul_f32_e32 v233, v13, v233
Cv 232,232,233
Mp 36,4,192
v_mul_f32_e32 v234, v13, v234
v_mul_f32_e32 v235, v13, v235
Cv 233,234,235
Mp 48,8,192
Rb 56,1,34816
v_mul_f32_e32 v236, v13, v236
v_mul_f32_e32 v237, v13, v237
Mp 56,8,200
Cv 234,236,237
v_mul_f32_e32 v238, v13, v238
v_mul_f32_e32 v239, v13, v239
Mp 60,12,200
Rb 60,1,35840
s_waitcnt vmcnt(15) lgkmcnt(4)
s_barrier
buffer_store_dword v6, v7, s[20:23], s82 offen offset:0 sc0 sc1
Mp 52,12,192
s_add_u32 s40, s67, 0x50000
Cv 235,238,239
v_mul_f32_e32 v240, v12, v240
Mp 32,16,208
Rb 0,1,36864
s_add_u32 m0, s36, 0
v_mul_f32_e32 v241, v12, v241
Mp 40,16,216
buffer_load_dwordx4 v2, s[20:23], s40 offen offset:0 sc1 lds
Cv 240,240,241
Mp 44,20,216
Rb 4,1,37888
v_mul_f32_e32 v242, v12, v242
v_mul_f32_e32 v243, v12, v243
Mp 36,20,208
Cv 241,242,243
v_mul_f32_e32 v244, v12, v244
v_mul_f32_e32 v245, v12, v245
Mp 48,24,208
)" R"(
Rb 8,1,38912
Cv 242,244,245
v_mul_f32_e32 v246, v12, v246
Mp 56,24,216
v_mul_f32_e32 v247, v12, v247
Cv 243,246,247
v_mul_f32_e32 v248, v13, v248
Mp 60,28,216
Rb 12,1,40960
v_mul_f32_e32 v249, v13, v249
Mp 52,28,208
buffer_load_dwordx4 v2, s[20:23], s40 offen offset:1024 sc1 lds
Cv 248,248,249
Mp 32,32,224
Rb 16,1,41984
v_mul_f32_e32 v250, v13, v250
Mp 40,32,232
v_mul_f32_e32 v251, v13, v251
Cv 249,250,251
v_mul_f32_e32 v252, v13, v252
Mp 44,36,232
Rb 20,1,43008
v_mul_f32_e32 v253, v13, v253
Cv 250,252,253
Mp 36,36,224
v_mul_f32_e32 v254, v13, v254
v_mul_f32_e32 v255, v13, v255
Cv 251,254,255
Mp 48,40,224
Rb 24,1,44032
Mp 56,40,232
buffer_load_dwordx4 v2, s[20:23], s40 offen offset:2048 sc1 lds
Mp 60,44,232
Rb 28,1,45056
s_waitcnt lgkmcnt(8)
Mp 52,44,224
Mp 32,48,240
Rb 32,1,46080
Mp 40,48,248
Mp 44,52,248
Rb 36,1,47104
Mp 36,52,240
buffer_load_dwordx4 v2, s[20:23], s40 offen offset:3072 sc1 lds
Mp 48,56,240
Rb 40,1,49152
Mp 56,56,248
Mp 60,60,248
Rb 44,1,50176
s_waitcnt lgkmcnt(8)
Mp 52,60,240
v_mfma_f32_16x16x32_bf16 v[64:67], a[0:3], v[128:131], 0
Rb 48,1,51200
s_add_u32 m0, s36, 4096
Mp 64,4,144
buffer_load_dwordx4 v3, s[20:23], s40 offen offset:0 sc1 lds
Mp 64,8,160
Rb 52,1,52224
Mp 64,12,176
Cv 32,32,33
Cv 33,34,35
v_mfma_f32_16x16x32_bf16 v[72:75], a[12:15], v[184:187], 0
Rb 56,1,53248
Cv 34,36,37
Mp 72,8,168
)" R"(
Cv 35,38,39
Mp 72,4,152
Rb 60,1,54272
s_waitcnt lgkmcnt(8)
Mp 72,0,136
buffer_load_dwordx4 v3, s[20:23], s40 offen offset:1024 sc1 lds
Mp 72,16,200
Rb 0,1,55296
Mp 72,20,216
buffer_store_dwordx4 v[32:35], v2, s[20:23], s64 offen offset:0 sc0 sc1
Mp 72,24,232
Rb 4,1,57344
Mp 72,28,248
Mp 64,28,240
Rb 8,1,58368
Mp 64,24,224
buffer_load_dwordx4 v3, s[20:23], s40 offen offset:2048 sc1 lds
Mp 64,20,208
Rb 12,1,59392
s_waitcnt lgkmcnt(8)
Mp 64,16,192
Cv 40,40,41
Cv 41,42,43
v_mfma_f32_16x16x32_bf16 v[68:71], a[32:35], v[128:131], 0
Rb 16,1,60416
Cv 42,44,45
Mp 68,36,144
Cv 43,46,47
Mp 68,40,160
Rb 20,1,61440
Mp 68,44,176
buffer_store_dwordx4 v[40:43], v2, s[20:23], s64 offen offset:1024 sc0 sc1
v_mfma_f32_16x16x32_bf16 v[76:79], a[44:47], v[184:187], 0
Rb 24,1,62464
Mp 76,40,168
Mp 76,36,152
Rb 28,1,63488
s_waitcnt lgkmcnt(8)
Mp 76,32,136
Mp 76,48,200
Rb 32,10,0
Mp 76,52,216
Cv 48,48,49
Cv 49,50,51
Mp 76,56,232
Rb 36,10,1024
Cv 50,52,53
Mp 76,60,248
Cv 51,54,55
Mp 68,60,240
Rb 40,10,2048
Mp 68,56,224
buffer_store_dwordx4 v[48:51], v2, s[20:23], s64 offen offset:2048 sc0 sc1
Mp 68,52,208
Rb 44,10,3072
s_waitcnt vmcnt(19) lgkmcnt(4)
s_barrier
Mp 68,48,192
s_add_u32 s40, s67, 0x60000
v_mfma_f32_16x16x32_bf16 v[80:83], a[0:3], v[128:131], 0
Rb 48,10,4096
s_add_u32 m0, s36, 32768
Mp 80,4,144
buffer_load_dwordx4 v2, s[20:23], s40 offen offset:0 sc1 lds
Mp 80,8,160
)" R"(
Rb 52,10,5120
Mp 80,12,176
Cv 56,56,57
Cv 57,58,59
Cv 58,60,61
v_mfma_f32_16x16x32_bf16 v[88:91], a[12:15], v[184:187], 0
Rb 56,10,6144
v_bfe_u32 v11, v0, 2, 2
Cv 59,62,63
.LzH42:
s_cbranch_execz .LzHR
Mp 88,8,168
v_lshrrev_b32 v4, 4, v0
v_xor_b32 v11, v11, v4
Mp 88,4,152
Rb 60,10,8192
Mp 88,0,136
buffer_load_dwordx4 v2, s[20:23], s40 offen offset:1024 sc1 lds
Mp 88,16,200
Rb 0,10,9216
v_bfe_u32 v4, v0, 1, 1
Mp 88,20,216
v_lshl_or_b32 v11, v11, 1, v4
v_and_b32 v4, 1, v0
Mp 88,24,232
Rb 4,10,10240
v_mul_u32_u24 v4, 0xffff, v4
buffer_store_dwordx4 v[56:59], v2, s[20:23], s64 offen offset:3072 sc0 sc1
Mp 88,28,248
v_add_u32 v4, 1, v4
v_mul_u32_u24 v4, 0x3f80, v4
Mp 80,28,240
Rb 8,10,11264
v_cmp_eq_u32_e32 vcc, 0, v11
Mp 80,24,224
buffer_load_dwordx4 v2, s[20:23], s40 offen offset:2048 sc1 lds
Mp 80,20,208
Rb 12,10,12288
s_waitcnt lgkmcnt(8)
Mp 80,16,192
v_cmp_eq_u32_e64 s[28:29], 1, v11
v_accvgpr_write_b32 a192, 0
v_mfma_f32_16x16x32_bf16 v[84:87], a[32:35], v[128:131], 0
Rb 16,10,13312
v_accvgpr_write_b32 a193, 0
Mp 84,36,144
v_accvgpr_write_b32 a196, 0
v_accvgpr_write_b32 a197, 0
Cv 64,64,65
Mp 84,40,160
Rb 20,10,14336
v_cndmask_b32_e32 v11, 0, v4, vcc
Mp 84,44,176
buffer_load_dwordx4 v2, s[20:23], s40 offen offset:3072 sc1 lds
Cv 65,66,67
v_mfma_f32_16x16x32_bf16 v[92:95], a[44:47], v[184:187], 0
Rb 24,10,16384
v_cndmask_b32_e64 v4, 0, v4, s[28:29]
Mp 92,40,168
v_accvgpr_write_b32 a194, v11
v_accvgpr_write_b32 a195, v4
Cv 66,68,69
Mp 92,36,152
Rb 28,10,17408
)" R"(
s_waitcnt lgkmcnt(8)
Mp 92,32,136
Cv 67,70,71
buffer_store_dwordx4 v[64:67], v3, s[20:23], s64 offen offset:0 sc0 sc1
Mp 92,48,200
Rb 32,10,18432
s_add_u32 m0, s36, 36864
Mp 92,52,216
buffer_load_dwordx4 v3, s[20:23], s40 offen offset:0 sc1 lds
Mp 92,56,232
Rb 36,10,19456
Mp 92,60,248
Mp 84,60,240
Rb 40,10,20480
Mp 84,56,224
Cv 72,72,73
Cv 73,74,75
Mp 84,52,208
Rb 44,10,21504
s_waitcnt lgkmcnt(8)
Mp 84,48,192
buffer_load_dwordx4 v3, s[20:23], s40 offen offset:1024 sc1 lds
v_mfma_f32_16x16x32_bf16 v[96:99], a[0:3], v[128:131], 0
Rb 48,10,22528
Cv 74,76,77
Mp 96,4,144
Cv 75,78,79
Mp 96,8,160
Rb 52,10,24576
Mp 96,12,176
buffer_store_dwordx4 v[72:75], v3, s[20:23], s64 offen offset:1024 sc0 sc1
v_mfma_f32_16x16x32_bf16 v[104:107], a[12:15], v[184:187], 0
Rb 56,10,25600
Mp 104,8,168
buffer_load_dwordx4 v3, s[20:23], s40 offen offset:2048 sc1 lds
Mp 104,4,152
Rb 60,10,26624
s_waitcnt lgkmcnt(8)
Mp 104,0,136
Mp 104,16,200
Rb 0,10,27648
Mp 104,20,216
Cv 80,80,81
Cv 81,82,83
Mp 104,24,232
Rb 4,10,28672
Cv 82,84,85
Mp 104,28,248
Cv 83,86,87
Mp 96,28,240
Rb 8,10,29696
Mp 96,24,224
buffer_store_dwordx4 v[80:83], v3, s[20:23], s64 offen offset:2048 sc0 sc1
Mp 96,20,208
Rb 12,10,30720
s_waitcnt lgkmcnt(8)
Mp 96,16,192
v_mfma_f32_16x16x32_bf16 v[100:103], a[32:35], v[128:131], 0
Rb 16,8,32768
Mp 100,36,144
Mp 100,40,160
Rb 20,8,33792
Mp 100,44,176
Cv 88,88,89
)" R"(
Cv 89,90,91
v_mfma_f32_16x16x32_bf16 v[108:111], a[44:47], v[184:187], 0
Rb 24,8,34816
Cv 90,92,93
Mp 108,40,168
Cv 91,94,95
Mp 108,36,152
Rb 28,8,35840
s_waitcnt vmcnt(23) lgkmcnt(4)
s_barrier
Mp 108,32,136
s_add_u32 s40, s67, 0x70000
Mp 108,48,200
Rb 32,8,36864
s_add_u32 m0, s36, 131072
Mp 108,52,216
buffer_load_dwordx4 v2, s[20:23], s40 offen offset:0 sc1 lds
Mp 108,56,232
Rb 36,8,37888
buffer_store_dwordx4 v[88:91], v3, s[20:23], s64 offen offset:3072 sc0 sc1
Mp 108,60,248
s_mov_b32 s8, s74
s_mov_b32 s9, s75
Mp 100,60,240
Rb 40,8,38912
s_mov_b32 s10, 0x1c0000
Mp 100,56,224
s_mov_b32 s11, 0x20000
v_and_b32 v174, 1, v0
Mp 100,52,208
Rb 44,8,40960
Mp 100,48,192
buffer_load_dwordx4 v2, s[20:23], s40 offen offset:1024 sc1 lds
v_mfma_f32_16x16x32_bf16 v[112:115], a[0:3], v[128:131], 0
Rb 48,8,41984
v_mul_u32_u24 v174, 0xffff, v174
Mp 112,4,144
v_add_u32 v174, 1, v174
v_mul_u32_u24 v174, 0x3f80, v174
Mp 112,8,160
Rb 52,8,43008
v_bfe_u32 v173, v0, 1, 3
Mp 112,12,176
Cv 96,96,97
Cv 97,98,99
v_mfma_f32_16x16x32_bf16 v[120:123], a[12:15], v[184:187], 0
Rb 56,8,44032
Cv 98,100,101
Mp 120,8,168
buffer_load_dwordx4 v2, s[20:23], s40 offen offset:2048 sc1 lds
Mp 120,4,152
Rb 60,8,45056
s_waitcnt lgkmcnt(8)
Mp 120,0,136
Cv 99,102,103
v_cmp_eq_u32_e32 vcc, 0, v173
v_and_b32 v167, 0xf0, v1
Mp 120,16,200
Rb 0,8,46080
v_cndmask_b32_e32 v132, 0, v174, vcc
Mp 120,20,216
s_add_u32 s64, s64, 0x10000
buffer_store_dwordx4 v[96:99], v2, s[20:23], s64 offen offset:0 sc0 sc1
Mp 120,24,232
Rb 4,8,47104
)" R"(
v_cmp_eq_u32_e32 vcc, 1, v173
Mp 120,28,248
buffer_load_dwordx4 v2, s[20:23], s40 offen offset:3072 sc1 lds
Mp 112,28,240
Rb 8,8,49152
v_cndmask_b32_e32 v133, 0, v174, vcc
Mp 112,24,224
v_cmp_eq_u32_e32 vcc, 2, v173
v_mul_u32_u24 v166, 56, v167
Mp 112,20,208
Rb 12,8,50176
s_waitcnt lgkmcnt(8)
Mp 112,16,192
Cv 104,104,105
Cv 105,106,107
v_mfma_f32_16x16x32_bf16 v[116:119], a[32:35], v[128:131], 0
Rb 16,8,51200
s_add_u32 m0, s36, 135168
Mp 116,36,144
buffer_load_dwordx4 v3, s[20:23], s40 offen offset:0 sc1 lds
Mp 116,40,160
Rb 20,8,52224
Cv 106,108,109
Mp 116,44,176
Cv 107,110,111
v_cndmask_b32_e32 v134, 0, v174, vcc
v_mfma_f32_16x16x32_bf16 v[124:127], a[44:47], v[184:187], 0
Rb 24,8,53248
v_cmp_eq_u32_e32 vcc, 3, v173
Mp 124,40,168
buffer_store_dwordx4 v[104:107], v2, s[20:23], s64 offen offset:1024 sc0 sc1
Mp 124,36,152
Rb 28,8,54272
s_waitcnt lgkmcnt(8)
Mp 124,32,136
buffer_load_dwordx4 v3, s[20:23], s40 offen offset:1024 sc1 lds
v_cndmask_b32_e32 v135, 0, v174, vcc
Mp 124,48,200
Rb 32,8,55296
v_cmp_eq_u32_e32 vcc, 4, v173
Mp 124,52,216
s_nop 0
v_cndmask_b32_e32 v140, 0, v174, vcc
v_cmp_eq_u32_e32 vcc, 5, v173
Mp 124,56,232
Rb 36,8,57344
v_cndmask_b32_e32 v141, 0, v174, vcc
Mp 124,60,248
v_cmp_eq_u32_e32 vcc, 6, v173
v_and_b32 v165, 0x80, v1
Mp 116,60,240
Rb 40,8,58368
v_cndmask_b32_e32 v142, 0, v174, vcc
Mp 116,56,224
buffer_load_dwordx4 v3, s[20:23], s40 offen offset:2048 sc1 lds
Mp 116,52,208
Rb 44,8,59392
s_waitcnt vmcnt(8) lgkmcnt(8)
buffer_store_dword v6, v7, s[20:23], s65 offen offset:0 sc0 sc1
Mp 116,48,192
v_cmp_eq_u32_e32 vcc, 7, v173
v_lshrrev_b32 v173, 4, v0
v_mfma_f32_16x16x32_bf16 v[32:35], a[0:3], v[128:131], 0
Rb 48,8,60416
)" R"(
v_cndmask_b32_e32 v143, 0, v174, vcc
Mp 32,4,144
v_lshl_or_b32 v164, v173, 25, v167
v_lshl_add_u32 v166, v173, 3, v166
Mp 32,8,160
Rb 52,8,61440
v_lshrrev_b32_e64 v173, v173, s85
Mp 32,12,176
Cv 112,112,113
Cv 113,114,115
v_mfma_f32_16x16x32_bf16 v[40:43], a[12:15], v[184:187], 0
Rb 56,8,62464
Cv 114,116,117
Mp 40,8,168
Cv 115,118,119
v_bfe_u32 v174, v173, 4, 1
Mp 40,4,152
Rb 60,8,63488
s_waitcnt lgkmcnt(8)
Mp 40,0,136
buffer_store_dwordx4 v[112:115], v2, s[20:23], s64 offen offset:2048 sc0 sc1
ds_write_b32 v9, v16 offset:39936
Mp 40,16,200
Rb 0,8,0
v_lshl_or_b32 v183, v174, 13, v164
Mp 40,20,216
v_and_b32 v174, 1, v173
v_lshl_or_b32 v164, v174, 13, v164
Mp 40,24,232
Rb 4,8,1024
v_and_b32 v174, 7, v0
Mp 40,28,248
v_lshl_or_b32 v165, v174, 8, v165
v_lshlrev_b32 v165, 1, v165
Mp 32,28,240
Rb 8,8,2048
s_mov_b32 s93, 0x1111
Mp 32,24,224
Cv 120,120,121
Cv 121,122,123
Mp 32,20,208
Rb 12,8,3072
s_waitcnt lgkmcnt(3)
s_barrier
Mp 32,16,192
Cv 122,124,125
Cv 123,126,127
ds_read_b32 v16, v8 offset:39936
v_mfma_f32_16x16x32_bf16 v[36:39], a[32:35], v[128:131], 0
Rb 16,8,4096
v_lshrrev_b32 v173, 2, v0
Mp 36,36,144
buffer_store_dwordx4 v[120:123], v2, s[20:23], s64 offen offset:3072 sc0 sc1
Mp 36,40,160
Rb 20,8,5120
v_mul_u32_u24 v173, 832, v173
Mp 36,44,176
v_add_u32 v173, v173, v1
v_lshrrev_b32 v174, 1, v0
v_mfma_f32_16x16x32_bf16 v[44:47], a[44:47], v[184:187], 0
Rb 24,8,6144
v_and_b32 v174, 24, v174
Mp 44,40,168
v_lshl_add_u32 v174, v167, 2, v174
s_add_u32 s59, s36, 98304
)" R"(
Mp 44,36,152
Rb 28,8,8192
s_waitcnt lgkmcnt(4)
Mp 44,32,136
v_readfirstlane_b32 s82, v16
v_add_u32 v174, s59, v174
Mp 44,48,200
Rb 32,8,9216
s_lshl2_add_u32 s56, s82, s33
Mp 44,52,216
s_mul_i32 s88, s56, 4682
s_lshr_b32 s58, s88, 16
Mp 44,56,232
Rb 36,8,10240
s_mul_i32 s57, s58, 14
Mp 44,60,248
s_sub_u32 s57, s56, s57
s_lshl_b32 s40, s58, 12
Mp 36,60,240
Rb 40,8,11264
Mp 36,56,224
s_cmp_gt_u32 s57, 6
s_cselect_b32 s88, 128, 0
Mp 36,52,208
Rb 44,8,12288
Mp 36,48,192
buffer_load_dword v4, v7, s[20:23], s52 offen offset:0 sc0 sc1
s_add_u32 s40, s40, s88
v_mfma_f32_16x16x32_bf16 v[48:51], a[0:3], v[128:131], 0
Rb 48,8,13312
s_add_u32 s40, s40, 0x1510000
Mp 48,4,144
s_lshl_b32 s55, s58, 6
s_add_u32 s55, s55, s57
Mp 48,8,160
Rb 52,8,14336
Mp 48,12,176
Cv 32,32,33
Cv 33,34,35
v_mfma_f32_16x16x32_bf16 v[56:59], a[12:15], v[184:187], 0
Rb 56,8,16384
Cv 34,36,37
Mp 56,8,168
Cv 35,38,39
Mp 56,4,152
Rb 60,8,17408
s_waitcnt lgkmcnt(8)
Mp 56,0,136
buffer_store_dwordx4 v[32:35], v3, s[20:23], s64 offen offset:0 sc0 sc1
Mp 56,16,200
Rb 0,8,18432
Mp 56,20,216
s_cmp_gt_u32 s57, 6
s_cselect_b32 s88, 9, 0
Mp 56,24,232
Rb 4,8,19456
s_add_u32 s55, s55, s88
Mp 56,28,248
s_and_b32 s88, s58, 32
s_lshl_b32 s88, s88, 8
Mp 48,28,240
Rb 8,8,20480
v_xor_b32 v164, s88, v164
Mp 48,24,224
)" R"(
Cv 40,40,41
Cv 41,42,43
Mp 48,20,208
Rb 12,8,21504
s_waitcnt lgkmcnt(8)
Mp 48,16,192
Cv 42,44,45
Cv 43,46,47
v_xor_b32 v183, s88, v183
v_mfma_f32_16x16x32_bf16 v[52:55], a[32:35], v[128:131], 0
Rb 16,8,22528
s_lshl_b32 s55, s55, 10
Mp 52,36,144
buffer_store_dwordx4 v[40:43], v3, s[20:23], s64 offen offset:1024 sc0 sc1
Mp 52,40,160
Rb 20,8,24576
s_add_u32 s55, s55, 0x1000000
Mp 52,44,176
s_and_b32 s88, s57, 1
s_lshl1_add_u32 s58, s58, s88
v_mfma_f32_16x16x32_bf16 v[60:63], a[44:47], v[184:187], 0
Rb 24,8,25600
s_lshl_b32 s88, s58, 8
Mp 60,40,168
v_add_u32 v167, s88, v167
s_mul_i32 s59, s58, 14336
Mp 60,36,152
Rb 28,8,26624
s_waitcnt lgkmcnt(8)
Mp 60,32,136
s_lshr_b32 s88, s57, 1
s_lshl_b32 s88, s88, 6
s_add_u32 s88, s88, s59
Mp 60,48,200
Rb 32,8,27648
v_add_u32 v166, s88, v166
Mp 60,52,216
s_mov_b32 s57, 0
s_add_u32 m0, s36, 98304
Mp 60,56,232
Rb 36,8,28672
Mp 60,60,248
buffer_load_dwordx4 v173, s[12:15], s88 offen offset:0 lds
Mp 52,60,240
Rb 40,8,29696
s_add_u32 m0, s36, 98880
Mp 52,56,224
buffer_load_dwordx4 v173, s[12:15], s88 offen offset:448 lds
Mp 52,52,208
Rb 44,8,30720
s_waitcnt lgkmcnt(8)
Mp 52,48,192
s_mov_b32 s88, 0x1500008
buffer_load_dword v167, v167, s[20:23], s88 offen sc0 sc1
v_mfma_f32_16x16x32_bf16 v[64:67], a[0:3], v[128:131], 0
Rb 48,1,0
Mp 64,4,144
Mp 64,8,160
Rb 52,1,1024
Mp 64,12,176
v_mfma_f32_16x16x32_bf16 v[72:75], a[12:15], v[184:187], 0
Rb 56,1,2048
Mp 72,8,168
Mp 72,4,152
)" R"(
Rb 60,1,3072
s_waitcnt vmcnt(5) lgkmcnt(8)
s_barrier
v_cmp_eq_u32_e32 vcc, s3, v4
s_andn2_b64 s[28:29], 1, vcc
s_cbranch_scc1 .Lzsp11
.Lzok11:
Mp 72,0,136
buffer_load_dwordx4 a[64:67], v2, s[20:23], s66 offen offset:0 sc0 sc1
Mp 72,16,200
Rb 0,1,4096
Mp 72,20,216
Mp 72,24,232
Rb 4,1,5120
Mp 72,28,248
buffer_load_dwordx4 a[68:71], v2, s[20:23], s66 offen offset:1024 sc0 sc1
Mp 64,28,240
Rb 8,1,6144
Mp 64,24,224
Mp 64,20,208
Rb 12,1,8192
s_waitcnt lgkmcnt(8)
Mp 64,16,192
buffer_load_dwordx4 a[72:75], v2, s[20:23], s66 offen offset:2048 sc0 sc1
v_mfma_f32_16x16x32_bf16 v[68:71], a[32:35], v[128:131], 0
Rb 16,1,9216
Mp 68,36,144
Mp 68,40,160
Rb 20,1,10240
Mp 68,44,176
buffer_load_dwordx4 a[76:79], v2, s[20:23], s66 offen offset:3072 sc0 sc1
v_mfma_f32_16x16x32_bf16 v[76:79], a[44:47], v[184:187], 0
Rb 24,1,11264
Mp 76,40,168
Mp 76,36,152
Rb 28,1,12288
s_waitcnt vmcnt(7) lgkmcnt(8)
buffer_store_dword v6, v7, s[20:23], s65 offen offset:128 sc0 sc1
Mp 76,32,136
buffer_load_dwordx4 a[80:83], v3, s[20:23], s66 offen offset:0 sc0 sc1
Mp 76,48,200
Rb 32,1,13312
Mp 76,52,216
Mp 76,56,232
Rb 36,1,14336
Mp 76,60,248
buffer_load_dwordx4 a[84:87], v3, s[20:23], s66 offen offset:1024 sc0 sc1
Mp 68,60,240
Rb 40,1,16384
Mp 68,56,224
Mp 68,52,208
Rb 44,1,17408
s_waitcnt lgkmcnt(8)
Mp 68,48,192
buffer_load_dwordx4 a[88:91], v3, s[20:23], s66 offen offset:2048 sc0 sc1
v_mfma_f32_16x16x32_bf16 v[80:83], a[0:3], v[128:131], 0
Rb 48,1,18432
Mp 80,4,144
Mp 80,8,160
Rb 52,1,19456
Mp 80,12,176
buffer_load_dwordx4 a[92:95], v3, s[20:23], s66 offen offset:3072 sc0 sc1
v_mfma_f32_16x16x32_bf16 v[88:91], a[12:15], v[184:187], 0
Rb 56,1,20480
Mp 88,8,168
)" R"(
Mp 88,4,152
Rb 60,1,21504
s_waitcnt lgkmcnt(8)
Mp 88,0,136
Mp 88,16,200
Rb 0,1,22528
Mp 88,20,216
Mp 88,24,232
Rb 4,1,24576
Mp 88,28,248
Mp 80,28,240
Rb 8,1,25600
Mp 80,24,224
Mp 80,20,208
Rb 12,1,26624
s_waitcnt lgkmcnt(8)
Mp 80,16,192
v_mfma_f32_16x16x32_bf16 v[84:87], a[32:35], v[128:131], 0
Rb 16,1,27648
Mp 84,36,144
Mp 84,40,160
Rb 20,1,28672
Mp 84,44,176
v_mfma_f32_16x16x32_bf16 v[92:95], a[44:47], v[184:187], 0
Rb 24,1,29696
Mp 92,40,168
Mp 92,36,152
Rb 28,1,30720
s_waitcnt lgkmcnt(8)
Mp 92,32,136
buffer_load_dword v4, v7, s[20:23], s52 offen offset:128 sc0 sc1
Mp 92,48,200
Rb 32,1,32768
Mp 92,52,216
Mp 92,56,232
Rb 36,1,33792
Mp 92,60,248
Mp 84,60,240
Rb 40,1,34816
Mp 84,56,224
Mp 84,52,208
Rb 44,1,35840
s_waitcnt vmcnt(6)
s_barrier
Mp 84,48,192
Mv 48,194,64
Mv 56,194,68
Mv 52,192,64
Mv 60,192,68
Mv 64,194,72
Mv 72,194,76
Mv 68,192,72
s_waitcnt vmcnt(3) lgkmcnt(9)
Mv 76,192,76
Cv 48,48,49
Cv 49,50,51
.LzH43:
s_cbranch_execz .LzHR
Mv 80,194,80
Cv 50,52,53
Cv 51,54,55
Mv 88,194,84
s_add_u32 s29, s73, 0x0
buffer_store_dwordx4 v[48:51], v1, s[20:23], s29 offen sc0 sc1
Mv 84,192,80
)" R"(
Mv 92,192,84
v_mfma_f32_16x16x32_bf16 v[96:99], a[0:3], v[128:131], 0
Rb 48,1,36864
Mp 96,4,144
Mp 96,8,160
Rb 52,1,37888
s_waitcnt lgkmcnt(7)
Mp 96,12,176
Cv 56,56,57
Cv 57,58,59
ds_read_b64 v[188:189], v174 offset:0
v_mfma_f32_16x16x32_bf16 v[104:107], a[12:15], v[184:187], 0
Rb 56,1,38912
Cv 58,60,61
Mp 104,8,168
Cv 59,62,63
ds_read_b64 v[190:191], v174 offset:32
Mp 104,4,152
Rb 60,1,40960
Mp 104,0,136
s_add_u32 s29, s73, 0x400
buffer_store_dwordx4 v[56:59], v1, s[20:23], s29 offen sc0 sc1
ds_read_b64 v[196:197], v174 offset:1024
Mp 104,16,200
Rb 0,1,41984
Mp 104,20,216
ds_read_b64 v[198:199], v174 offset:1056
Mp 104,24,232
Rb 4,1,43008
s_waitcnt lgkmcnt(11)
Mp 104,28,248
Mp 96,28,240
Rb 8,1,44032
Mp 96,24,224
Cv 64,64,65
Cv 65,66,67
Mp 96,20,208
Rb 12,1,45056
Cv 66,68,69
Mp 96,16,192
Cv 67,70,71
v_mfma_f32_16x16x32_bf16 v[100:103], a[32:35], v[128:131], 0
Rb 16,1,46080
Mp 100,36,144
s_add_u32 s29, s73, 0x800
buffer_store_dwordx4 v[64:67], v1, s[20:23], s29 offen sc0 sc1
Mp 100,40,160
Rb 20,1,47104
s_waitcnt vmcnt(3) lgkmcnt(10)
v_cmp_eq_u32_e32 vcc, s3, v4
s_andn2_b64 s[28:29], 1, vcc
s_cbranch_scc1 .Lzsp12
.Lzok12:
Mp 100,44,176
s_add_u32 s66, s66, 0x10000
buffer_load_dwordx4 a[96:99], v2, s[20:23], s66 offen offset:0 sc0 sc1
v_mfma_f32_16x16x32_bf16 v[108:111], a[44:47], v[184:187], 0
Rb 24,1,49152
v_sub_f32_e32 v172, 1.0, v167
Mp 108,40,168
Cv 172,172,172
v_lshlrev_b32 v172, 16, v172
Mp 108,36,152
Rb 28,1,50176
)" R"(
v_lshlrev_b32 v204, 16, v188
Mp 108,32,136
buffer_load_dwordx4 a[100:103], v2, s[20:23], s66 offen offset:1024 sc0 sc1
Mp 108,48,200
Rb 32,1,51200
Cv 72,72,73
Mp 108,52,216
Cv 73,74,75
Cv 74,76,77
Mp 108,56,232
Rb 36,1,52224
s_waitcnt lgkmcnt(12)
Mp 108,60,248
buffer_load_dwordx4 a[104:107], v2, s[20:23], s66 offen offset:2048 sc0 sc1
Mp 100,60,240
Rb 40,1,53248
Cv 75,78,79
Mp 100,56,224
s_add_u32 s29, s73, 0xc00
buffer_store_dwordx4 v[72:75], v1, s[20:23], s29 offen sc0 sc1
Mp 100,52,208
Rb 44,1,54272
v_and_b32 v205, 0xffff0000, v188
Mp 100,48,192
buffer_load_dwordx4 a[108:111], v2, s[20:23], s66 offen offset:3072 sc0 sc1
Mv 96,194,88
v_mul_f32_e32 v204, v172, v204
v_mul_f32_e32 v205, v172, v205
Mv 104,194,92
Cv 205,204,205
v_lshlrev_b32 v204, 16, v205
Mv 100,192,88
v_and_b32 v205, 0xffff0000, v205
s_waitcnt lgkmcnt(8)
Mv 108,192,92
buffer_load_dwordx4 a[112:115], v3, s[20:23], s66 offen offset:0 sc0 sc1
v_mfma_f32_16x16x32_bf16 v[112:115], a[0:3], v[128:131], 0
Rb 48,1,55296
Cv 80,80,81
Mp 112,4,144
Cv 81,82,83
Cv 82,84,85
Mp 112,8,160
Rb 52,1,57344
Cv 83,86,87
Mp 112,12,176
buffer_load_dwordx4 a[116:119], v3, s[20:23], s66 offen offset:1024 sc0 sc1
v_mfma_f32_16x16x32_bf16 v[120:123], a[12:15], v[184:187], 0
Rb 56,1,58368
v_lshlrev_b32 v206, 16, v189
Mp 120,8,168
s_add_u32 s29, s73, 0x1000
buffer_store_dwordx4 v[80:83], v1, s[20:23], s29 offen sc0 sc1
Mp 120,4,152
Rb 60,1,59392
s_waitcnt lgkmcnt(8)
Mp 120,0,136
v_and_b32 v207, 0xffff0000, v189
v_mul_f32_e32 v206, v172, v206
Mp 120,16,200
Rb 0,1,60416
v_mul_f32_e32 v207, v172, v207
Mp 120,20,216
Cv 207,206,207
)" R"(
v_lshlrev_b32 v206, 16, v207
Mp 120,24,232
Rb 4,1,61440
v_and_b32 v207, 0xffff0000, v207
Mp 120,28,248
Cv 88,88,89
Cv 89,90,91
Mp 112,28,240
Rb 8,1,62464
Cv 90,92,93
Mp 112,24,224
Cv 91,94,95
v_lshlrev_b32 v212, 16, v190
Mp 112,20,208
Rb 12,1,63488
s_waitcnt lgkmcnt(8)
Mp 112,16,192
s_add_u32 s29, s73, 0x1400
buffer_store_dwordx4 v[88:91], v1, s[20:23], s29 offen sc0 sc1
v_mfma_f32_16x16x32_bf16 v[116:119], a[32:35], v[128:131], 0
Rb 16,10,0
v_and_b32 v213, 0xffff0000, v190
Mp 116,36,144
v_mul_f32_e32 v212, v172, v212
v_mul_f32_e32 v213, v172, v213
Mp 116,40,160
Rb 20,10,1024
Cv 213,212,213
Mp 116,44,176
v_lshlrev_b32 v212, 16, v213
v_and_b32 v213, 0xffff0000, v213
v_mfma_f32_16x16x32_bf16 v[124:127], a[44:47], v[184:187], 0
Rb 24,10,2048
v_lshlrev_b32 v214, 16, v191
Mp 124,40,168
Cv 96,96,97
Cv 97,98,99
Mp 124,36,152
Rb 28,10,3072
s_waitcnt lgkmcnt(8)
Mp 124,32,136
Cv 98,100,101
Cv 99,102,103
Mp 124,48,200
Rb 32,10,4096
v_and_b32 v215, 0xffff0000, v191
Mp 124,52,216
s_add_u32 s29, s73, 0x1800
buffer_store_dwordx4 v[96:99], v1, s[20:23], s29 offen sc0 sc1
Mp 124,56,232
Rb 36,10,5120
v_mul_f32_e32 v214, v172, v214
Mp 124,60,248
v_mul_f32_e32 v215, v172, v215
Cv 215,214,215
Mp 116,60,240
Rb 40,10,6144
v_lshlrev_b32 v214, 16, v215
Mp 116,56,224
v_and_b32 v215, 0xffff0000, v215
v_lshlrev_b32 v220, 16, v196
Mp 116,52,208
Rb 44,10,8192
s_waitcnt vmcnt(8) lgkmcnt(9)
)" R"(
Mp 116,48,192
Cv 104,104,105
Cv 105,106,107
Mv 112,194,96
Cv 106,108,109
Cv 107,110,111
Mv 120,194,100
s_add_u32 s29, s73, 0x4000
buffer_store_dwordx4 v[104:107], v1, s[20:23], s29 offen sc0 sc1
Mv 116,192,96
v_and_b32 v221, 0xffff0000, v196
v_mul_f32_e32 v220, v172, v220
Mv 124,192,100
v_mul_f32_e32 v221, v172, v221
Cv 221,220,221
v_mfma_f32_16x16x32_bf16 v[32:35], a[0:3], v[128:131], 0
Rb 48,10,9216
v_lshlrev_b32 v220, 16, v221
Mp 32,4,144
v_and_b32 v221, 0xffff0000, v221
v_lshlrev_b32 v222, 16, v197
Mp 32,8,160
Rb 52,10,10240
s_waitcnt lgkmcnt(7)
Mp 32,12,176
Cv 112,112,113
Cv 113,114,115
v_mfma_f32_16x16x32_bf16 v[40:43], a[12:15], v[184:187], 0
Rb 56,10,11264
Cv 114,116,117
Mp 40,8,168
Cv 115,118,119
v_and_b32 v223, 0xffff0000, v197
Mp 40,4,152
Rb 60,10,12288
v_mul_f32_e32 v222, v172, v222
Mp 40,0,136
s_add_u32 s29, s73, 0x4400
buffer_store_dwordx4 v[112:115], v1, s[20:23], s29 offen sc0 sc1
Mp 40,16,200
Rb 0,10,13312
v_mul_f32_e32 v223, v172, v223
Mp 40,20,216
Cv 223,222,223
v_lshlrev_b32 v222, 16, v223
Mp 40,24,232
Rb 4,10,14336
s_waitcnt lgkmcnt(7)
Mp 40,28,248
v_and_b32 v223, 0xffff0000, v223
v_lshlrev_b32 v228, 16, v198
Mp 32,28,240
Rb 8,10,16384
v_and_b32 v229, 0xffff0000, v198
Mp 32,24,224
Cv 120,120,121
Cv 121,122,123
Mp 32,20,208
Rb 12,10,17408
Cv 122,124,125
Mp 32,16,192
Cv 123,126,127
v_mul_f32_e32 v228, v172, v228
v_mfma_f32_16x16x32_bf16 v[36:39], a[32:35], v[128:131], 0
)" R"(
Rb 16,10,18432
v_mul_f32_e32 v229, v172, v229
Mp 36,36,144
s_add_u32 s29, s73, 0x4800
buffer_store_dwordx4 v[120:123], v1, s[20:23], s29 offen sc0 sc1
Mp 36,40,160
Rb 20,10,19456
s_waitcnt lgkmcnt(7)
Mp 36,44,176
Cv 229,228,229
v_lshlrev_b32 v228, 16, v229
v_mfma_f32_16x16x32_bf16 v[44:47], a[44:47], v[184:187], 0
Rb 24,10,20480
v_and_b32 v229, 0xffff0000, v229
Mp 44,40,168
v_lshlrev_b32 v230, 16, v199
v_and_b32 v231, 0xffff0000, v199
Mp 44,36,152
Rb 28,10,21504
v_mul_f32_e32 v230, v172, v230
Mp 44,32,136
v_mul_f32_e32 v231, v172, v231
Cv 231,230,231
Mp 44,48,200
Rb 32,10,22528
v_lshlrev_b32 v230, 16, v231
Mp 44,52,216
v_and_b32 v231, 0xffff0000, v231
Mp 44,56,232
Rb 36,10,24576
s_waitcnt vmcnt(3) lgkmcnt(10)
buffer_store_dword v6, v7, s[20:23], s68 offen offset:0 sc0 sc1
Mp 44,60,248
Mp 36,60,240
Rb 40,10,25600
Mp 36,56,224
Mp 36,52,208
Rb 44,10,26624
Mp 36,48,192
Mv 32,194,104
Mv 40,194,108
Mv 36,192,104
s_waitcnt lgkmcnt(8)
Mv 44,192,108
v_mfma_f32_16x16x32_bf16 v[48:51], a[0:3], v[128:131], 0
Rb 48,10,27648
Mp 48,4,144
Mp 48,8,160
Rb 52,10,28672
Mp 48,12,176
Cv 32,32,33
Cv 33,34,35
v_mfma_f32_16x16x32_bf16 v[56:59], a[12:15], v[184:187], 0
Rb 56,10,29696
Cv 34,36,37
Mp 56,8,168
Cv 35,38,39
Mp 56,4,152
Rb 60,10,30720
s_waitcnt lgkmcnt(8)
Mp 56,0,136
s_add_u32 s29, s73, 0x4c00
buffer_store_dwordx4 v[32:35], v1, s[20:23], s29 offen sc0 sc1
Mp 56,16,200
)" R"(
Mp 56,20,216
Mp 56,24,232
Mp 56,28,248
Mp 48,28,240
Mp 48,24,224
Cv 40,40,41
Cv 41,42,43
buffer_store_dword v6, v7, s[20:23], s68 offen offset:128 sc0 sc1
Mp 48,20,208
Cv 42,44,45
s_waitcnt lgkmcnt(4)
Mp 48,16,192
Cv 43,46,47
v_mfma_f32_16x16x32_bf16 v[52:55], a[32:35], v[128:131], 0
s_add_u32 s29, s73, 0x5000
buffer_store_dwordx4 v[40:43], v1, s[20:23], s29 offen sc0 sc1
Mp 52,36,144
buffer_load_dword v4, v165, s[20:23], s40 offen sc0 sc1
Mp 52,40,160
Mp 52,44,176
v_mfma_f32_16x16x32_bf16 v[60:63], a[44:47], v[184:187], 0
Mp 60,40,168
Mp 60,36,152
s_waitcnt lgkmcnt(0)
Mp 60,32,136
Mp 60,48,200
Mp 60,52,216
Mp 60,56,232
Mp 60,60,248
Mp 52,60,240
Mp 52,56,224
Mp 52,52,208
Mp 52,48,192
Mv 48,194,112
Mv 56,194,116
Mv 52,192,112
Mv 60,192,116
s_nop 7
Cv 48,48,49
Cv 49,50,51
Cv 50,52,53
Cv 51,54,55
s_add_u32 s29, s73, 0x5400
buffer_store_dwordx4 v[48:51], v1, s[20:23], s29 offen sc0 sc1
Cv 56,56,57
Cv 57,58,59
Cv 58,60,61
Cv 59,62,63
s_add_u32 s29, s73, 0x5800
buffer_store_dwordx4 v[56:59], v1, s[20:23], s29 offen sc0 sc1
s_branch .LzEND
.LzCEND:
)" : "+{s[0:1]}"(ka), "+{s2}"(bid), "+{v0}"(tid) :
   : "memory", "scc", "vcc", "v1", "v2", "v3", "v4", "v5", "v6", "v7", "v8", "v9", R256("v"), V10("a"), R256("a"),
     "s3", "s4", "s5", "s6", "s7", "s8", "s9", V10("s1"), V10("s2"), "s30", "s31", "s33", "s34", "s35", "s36", "s37", "s38", "s39",
     V10("s4"), V10("s5"), V10("s6"), V10("s7"), V10("s8"), V10("s9"));
}

extern "C" void kernel_launch(void* const* d_in, const int* in_sizes, int n_in, void* d_out, int out_size, void* d_ws, size_t ws_size, hipStream_t stream) {
  (void)hipFuncSetAttribute((const void*)k_main, hipFuncAttributeMaxDynamicSharedMemorySize, 163840);
  k_main<<<256, 256, 163840, stream>>>((const u16*)d_in[0], (const u16*)d_in[1], (const u16*)d_in[2], (const u16*)d_in[3], (const u16*)d_in[4],
      (char*)d_ws, (const u16*)d_in[11], (const u16*)d_in[7], (const u16*)d_in[5], (const u16*)d_in[8], (const u16*)d_in[9], (const u16*)d_in[10], (u16*)d_out);
}
```
